```python
import torch

def _softmax(x, axis=-1):
    return torch.softmax(x, dim=axis)
def _gelu(x):
    return torch.nn.functional.gelu(x, approximate="tanh")
def _silu(x):
    return torch.nn.functional.silu(x)
def _sigmoid(x):
    return torch.sigmoid(x)
def _layernorm(x, axis=-1, eps=1e-5):
    m = x.mean(dim=axis, keepdim=True)
    v = x.var(dim=axis, keepdim=True, unbiased=False)
    return (x - m) / torch.sqrt(v + eps)
def _scatter_add(tgt, idx, src, axis=0):
    return tgt.clone().index_add_(axis, idx, src)
def _segment_sum(src, seg, n):
    out = torch.zeros((n,) + src.shape[1:], dtype=src.dtype)
    return out.index_add_(0, seg, src)
def _pad(x, pw):
    flat = tuple(v for pair in reversed(pw) for v in pair)
    return torch.nn.functional.pad(x, flat)

kernel_name = 'concat_heads__mha_block_cached__mha_block_cached__near_ridge'

def setup_inputs():

    _w = (lambda a: (lambda t: t.to(torch.bfloat16) if t.is_floating_point() else t)(torch.as_tensor(a)))
    def _A_setup():
        B, Sq, D = 16, 224, 640
        H, Sk, Dh = 32, 1024, 128
        return {
            "x": _w((lambda *s: torch.randn(*s, dtype=torch.bfloat16) * 0.8)(B, Sq, D)),
            "k_cache": _w((lambda *s: torch.randn(*s, dtype=torch.bfloat16) * 0.8)(B, H, Sk, Dh)),
            "v_cache": _w((lambda *s: torch.randn(*s, dtype=torch.bfloat16) * 0.05)(B, H, Sk, Dh)),
            "wq": _w((lambda *s: torch.randn(*s, dtype=torch.bfloat16) * 0.8)(D, H * Dh) * (D ** -0.5)),
            "wo": _w((lambda *s: torch.randn(*s, dtype=torch.bfloat16) * 0.05)(H * Dh, D) * ((H * Dh) ** -0.5)),
        }
    _A_in = _A_setup()

    def _B_setup():
        B, Sq, D = 16, 224, 640
        H, Sk, Dh = 32, 1024, 128
        return {
            "x": _w((lambda *s: torch.randn(*s, dtype=torch.bfloat16) * 0.8)(B, Sq, D)),
            "k_cache": _w((lambda *s: torch.randn(*s, dtype=torch.bfloat16) * 0.8)(B, H, Sk, Dh)),
            "v_cache": _w((lambda *s: torch.randn(*s, dtype=torch.bfloat16) * 0.05)(B, H, Sk, Dh)),
            "wq": _w((lambda *s: torch.randn(*s, dtype=torch.bfloat16) * 0.8)(D, H * Dh) * (D ** -0.5)),
            "wo": _w((lambda *s: torch.randn(*s, dtype=torch.bfloat16) * 0.05)(H * Dh, D) * ((H * Dh) ** -0.5)),
        }
    _B_in = _B_setup()

    return {
        "A_x": _A_in["x"],
        "A_k_cache": _A_in["k_cache"],
        "A_v_cache": _A_in["v_cache"],
        "A_wq": _A_in["wq"],
        "A_wo": _A_in["wo"],
        "B_x": _B_in["x"],
        "B_k_cache": _B_in["k_cache"],
        "B_v_cache": _B_in["v_cache"],
        "B_wq": _B_in["wq"],
        "B_wo": _B_in["wo"],
    }

def reference(A_x, A_k_cache, A_v_cache, A_wq, A_wo, B_x, B_k_cache, B_v_cache, B_wq, B_wo):
    def _A_ref(x, k_cache, v_cache, wq, wo):
        B, Sq, D = x.shape
        H, Dh = 32, 128
        q = (lambda x, *d: x.permute(*d))((x @ wq).reshape(B, Sq, H, Dh), 0, 2, 1, 3)
        scores = torch.einsum("bhqd,bhkd->bhqk", q, k_cache) * (Dh ** -0.5)
        p = _softmax(scores, axis=-1)
        o = torch.einsum("bhqk,bhkd->bhqd", p, v_cache)
        return (lambda x, *d: x.permute(*d))(o, 0, 2, 1, 3).reshape(B, Sq, H * Dh) @ wo

    def _B_ref(x, k_cache, v_cache, wq, wo):
        B, Sq, D = x.shape
        H, Dh = 32, 128
        q = (lambda x, *d: x.permute(*d))((x @ wq).reshape(B, Sq, H, Dh), 0, 2, 1, 3)
        scores = torch.einsum("bhqd,bhkd->bhqk", q, k_cache) * (Dh ** -0.5)
        p = _softmax(scores, axis=-1)
        o = torch.einsum("bhqk,bhkd->bhqd", p, v_cache)
        return (lambda x, *d: x.permute(*d))(o, 0, 2, 1, 3).reshape(B, Sq, H * Dh) @ wo

    a = _A_ref(A_x, A_k_cache, A_v_cache, A_wq, A_wo)
    b = _B_ref(B_x, B_k_cache, B_v_cache, B_wq, B_wo)
    return torch.concatenate([a, b], axis=-1)
```

```cpp
#include <hip/hip_runtime.h>
#include <hip/hip_bf16.h>
#include <stdint.h>

constexpr int NBLK = 2, BATCH = 16, SQ = 224, DM = 640, NH = 32, SK = 1024, DH = 128, HD = NH * DH;
constexpr int MROWS = BATCH * SQ;


__global__ __launch_bounds__(256, 1)
void attnq_asm(const unsigned short* __restrict__ X0, const unsigned short* __restrict__ X1, const unsigned short* __restrict__ W0, const unsigned short* __restrict__ W1,
               const unsigned short* __restrict__ K0, const unsigned short* __restrict__ K1,
               const unsigned short* __restrict__ V0, const unsigned short* __restrict__ V1, unsigned short* __restrict__ O, unsigned long long* dbg,
               const unsigned short* __restrict__ WO0, const unsigned short* __restrict__ WO1, unsigned* __restrict__ flags) {
  extern __shared__ __attribute__((aligned(16))) char lds[];
  const unsigned c = blockIdx.x;
  const unsigned xcd = c & 7u, idx = c >> 3;
  const unsigned bbi = idx >> 3, hg = idx & 7u;
  const unsigned bb = xcd * 4u + bbi;
  const unsigned bk = bb >> 4, b = bb & 15u;
  const unsigned wid = __builtin_amdgcn_readfirstlane(threadIdx.x >> 6);
  const unsigned head0 = hg * 4u + (wid >> 1);
  const unsigned long long kp = (unsigned long long)(bk ? K1 : K0) + ((unsigned long long)b * 32u + head0) * 262144ull - 1024ull;
  const unsigned long long vp = (unsigned long long)(bk ? V1 : V0) + ((unsigned long long)b * 32u + head0) * 262144ull - 1024ull;
  const unsigned long long xp = (unsigned long long)(bk ? X1 : X0) + (unsigned long long)b * (224u * 1280u);
  const unsigned long long wp = (unsigned long long)(bk ? W1 : W0);
  const unsigned long long op = (unsigned long long)O + (unsigned long long)bb * 1835008ull;
  const unsigned ldsb = (unsigned)(size_t)lds;
  const unsigned long long wop = (unsigned long long)(c >= 128u ? WO1 : WO0) + (unsigned long long)(c & 127u) * 40960ull;
  if (c < 128u && threadIdx.x < 32u) flags[c * 32u + threadIdx.x] = 0u;
  asm volatile(
".macro Qta d,v,o\nds_read_b64_tr_b16 a[\\d:\\d+1], v\\v offset:\\o\n.endm\n.macro Qdv d,v,o\nds_read_b128 v[\\d:\\d+3], v\\v offset:\\o\n.endm\n.macro Qda d,v,o\nds_read_b128 a[\\d:\\d+3], v\\v offset:\\o\n"
".endm\n.macro Qzvav d,a,b\nv_mfma_f32_16x16x32_bf16 v[\\d:\\d+3], a[\\a:\\a+3], v[\\b:\\b+3], 0\n.endm\n.macro Qzaav d,a,b\nv_mfma_f32_16x16x32_bf16 a[\\d:\\d+3], a[\\a:\\a+3], v[\\b:\\b+3], 0\n.endm\n.macro Qmvaa d,a,b\n"
"v_mfma_f32_16x16x32_bf16 v[\\d:\\d+3], a[\\a:\\a+3], a[\\b:\\b+3], v[\\d:\\d+3]\n.endm\n.macro Qmaaa d,a,b\nv_mfma_f32_16x16x32_bf16 a[\\d:\\d+3], a[\\a:\\a+3], a[\\b:\\b+3], a[\\d:\\d+3]\n.endm\n.macro Qmvav d,a,b\nv_mfma_f32_16x16x32_bf16 v[\\d:\\d+3], a[\\a:\\a+3], v[\\b:\\b+3], v[\\d:\\d+3]\n.endm\n"
".macro Qmaav d,a,b\nv_mfma_f32_16x16x32_bf16 a[\\d:\\d+3], a[\\a:\\a+3], v[\\b:\\b+3], a[\\d:\\d+3]\n.endm\n.macro Qk d,a,b\nv_cvt_pk_bf16_f32 v\\d, v\\a, v\\b\n.endm\n.macro Qzvaa d,a,b\nv_mfma_f32_16x16x32_bf16 v[\\d:\\d+3], a[\\a:\\a+3], a[\\b:\\b+3], 0\n"
".endm\n.macro Qa d,a,b\nv_add_f32 v\\d, v\\a, v\\b\n.endm\n.macro Qe d\nv_exp_f32 v\\d, v\\d\n.endm\n.macro Qcvaa d,a,b,c\n"
"v_mfma_f32_16x16x32_bf16 v[\\d:\\d+3], a[\\a:\\a+3], a[\\b:\\b+3], v[\\c:\\c+3]\n.endm\ns_mov_b32 s52, %6\ns_and_b32 s53, %7, 0xffff\ns_mov_b32 s54, 5242880\ns_mov_b32 s55, 0x20000\ns_mov_b32 s48, %4\ns_and_b32 s49, %5, 0xffff\n"
"s_mov_b32 s50, 286720\ns_mov_b32 s51, 0x20000\ns_mov_b32 s70, %11\ns_cmp_eq_u32 s70, 0\ns_cselect_b64 s[24:25], -1, 0\ns_cmp_eq_u32 s70, 1\ns_cselect_b64 s[26:27], -1, 0\ns_cmp_eq_u32 s70, 2\n"
"s_cselect_b64 s[28:29], -1, 0\ns_cmp_eq_u32 s70, 3\ns_cselect_b64 s[30:31], -1, 0\ns_and_b32 s92, s70, 1\ns_lshr_b32 s93, s70, 1\nv_mbcnt_lo_u32_b32 v66, -1, 0\nv_mbcnt_hi_u32_b32 v66, -1, v66\nv_lshrrev_b32 v68, 4, v66\n"
"s_lshl_b32 s68, s92, 17\nv_and_b32 v70, 3, v66\nv_lshlrev_b32 v70, 4, v70\nv_lshl_add_u32 v70, v68, 6, v70\nv_bfe_u32 v67, v66, 2, 2\nv_lshl_add_u32 v70, v67, 13, v70\nv_add_u32 v62, s68, v70\ns_lshl_b32 s87, %12, 8\n"
"s_mul_i32 s62, s93, 8224\ns_mul_i32 s68, s92, 4112\ns_add_u32 s62, s62, s68\ns_add_u32 s62, s62, %10\ns_add_u32 s62, s62, 33024\ns_lshl_b32 s69, s92, 1\nv_lshrrev_b32 v69, 5, v66\nv_and_b32 v70, 3, v66\n"
"v_xor_b32 v70, s69, v70\nv_lshlrev_b32 v70, 4, v70\nv_lshl_add_u32 v70, v69, 6, v70\nv_bfe_u32 v67, v66, 2, 3\nv_mul_u32_u24 v64, 1280, v67\nv_add_u32 v64, v64, v70\ns_mul_i32 s86, s93, 143360\ns_mul_i32 s68, s92, 10240\n"
"s_add_u32 s86, s86, s68\ns_mul_i32 s63, s92, 14336\ns_mul_i32 s68, s93, 7168\ns_add_u32 s63, s63, s68\ns_add_u32 s63, s63, %10\ns_add_u32 s63, s63, 99072\ns_nop 1\ns_add_u32 m0, s62, 0\n"
"s_add_u32 s69, s87, 0\ns_nop 0\nbuffer_load_dwordx4 v62, s[52:55], s69 offen lds\ns_add_u32 s69, s87, 31744\ns_nop 0\nbuffer_load_dwordx4 v62, s[52:55], s69 offen offset:1024 lds\ns_add_u32 s69, s87, 63480\ns_nop 0\n"
"buffer_load_dwordx4 v62, s[52:55], s69 offen offset:2056 lds\ns_add_u32 s69, s87, 95224\ns_nop 0\nbuffer_load_dwordx4 v62, s[52:55], s69 offen offset:3080 lds\ns_add_u32 m0, s63, 33024\ns_add_u32 s88, s86, 0\ns_nop 0\nbuffer_load_dwordx4 v64, s[48:51], s88 offen lds\n"
"s_add_u32 s88, s86, 19456\ns_nop 0\nbuffer_load_dwordx4 v64, s[48:51], s88 offen offset:1024 lds\ns_add_u32 s88, s86, 38912\ns_nop 0\nbuffer_load_dwordx4 v64, s[48:51], s88 offen offset:2048 lds\ns_add_u32 s88, s86, 58368\ns_nop 0\n"
"buffer_load_dwordx4 v64, s[48:51], s88 offen offset:3072 lds\ns_add_u32 m0, s63, 37120\ns_add_u32 s88, s86, 81920\ns_nop 0\nbuffer_load_dwordx4 v64, s[48:51], s88 offen lds\ns_add_u32 s88, s86, 101376\ns_nop 0\nbuffer_load_dwordx4 v64, s[48:51], s88 offen offset:1024 lds\n"
"s_add_u32 s88, s86, 120832\ns_nop 0\nbuffer_load_dwordx4 v64, s[48:51], s88 offen offset:2048 lds\ns_add_u32 m0, s62, 16448\ns_add_u32 s69, s87, 262144\ns_nop 0\nbuffer_load_dwordx4 v62, s[52:55], s69 offen lds\ns_add_u32 s69, s87, 293888\n"
"s_nop 0\nbuffer_load_dwordx4 v62, s[52:55], s69 offen offset:1024 lds\ns_add_u32 s69, s87, 325624\ns_nop 0\nbuffer_load_dwordx4 v62, s[52:55], s69 offen offset:2056 lds\ns_add_u32 s69, s87, 357368\ns_nop 0\nbuffer_load_dwordx4 v62, s[52:55], s69 offen offset:3080 lds\n"
"s_add_u32 m0, s63, 0\ns_add_u32 s88, s86, 128\ns_nop 0\nbuffer_load_dwordx4 v64, s[48:51], s88 offen lds\ns_add_u32 s88, s86, 19584\ns_nop 0\nbuffer_load_dwordx4 v64, s[48:51], s88 offen offset:1024 lds\ns_add_u32 s88, s86, 39040\n"
"s_nop 0\nbuffer_load_dwordx4 v64, s[48:51], s88 offen offset:2048 lds\ns_add_u32 s88, s86, 58496\ns_nop 0\nbuffer_load_dwordx4 v64, s[48:51], s88 offen offset:3072 lds\ns_add_u32 m0, s63, 4096\ns_add_u32 s88, s86, 82048\ns_nop 0\n"
"buffer_load_dwordx4 v64, s[48:51], s88 offen lds\ns_add_u32 s88, s86, 101504\ns_nop 0\nbuffer_load_dwordx4 v64, s[48:51], s88 offen offset:1024 lds\ns_add_u32 s88, s86, 120960\ns_nop 0\nbuffer_load_dwordx4 v64, s[48:51], s88 offen offset:2048 lds\ns_add_u32 m0, s62, 33024\n"
"s_add_u32 s69, s87, 524288\ns_nop 0\nbuffer_load_dwordx4 v62, s[52:55], s69 offen lds\ns_add_u32 s69, s87, 556032\ns_nop 0\nbuffer_load_dwordx4 v62, s[52:55], s69 offen offset:1024 lds\ns_add_u32 s69, s87, 587768\ns_nop 0\n"
"buffer_load_dwordx4 v62, s[52:55], s69 offen offset:2056 lds\ns_add_u32 s69, s87, 619512\ns_nop 0\nbuffer_load_dwordx4 v62, s[52:55], s69 offen offset:3080 lds\ns_add_u32 m0, s62, 49472\ns_add_u32 s69, s87, 786432\ns_nop 0\nbuffer_load_dwordx4 v62, s[52:55], s69 offen lds\n"
"s_add_u32 s69, s87, 818176\ns_nop 0\nbuffer_load_dwordx4 v62, s[52:55], s69 offen offset:1024 lds\ns_add_u32 s69, s87, 849912\ns_nop 0\nbuffer_load_dwordx4 v62, s[52:55], s69 offen offset:2056 lds\ns_add_u32 s69, s87, 881656\ns_nop 0\n"
"buffer_load_dwordx4 v62, s[52:55], s69 offen offset:3080 lds\ns_mov_b32 s40, %0\ns_and_b32 s41, %1, 0xffff\ns_mov_b32 s42, 787456\ns_mov_b32 s43, 0x20000\ns_mov_b32 s44, %2\ns_and_b32 s45, %3, 0xffff\ns_mov_b32 s46, 787456\n"
"s_mov_b32 s47, 0x20000\ns_mov_b32 s56, %8\ns_and_b32 s57, %9, 0xffff\ns_mov_b32 s58, 0\ns_mov_b32 s59, 0x20000\ns_mov_b32 s60, 0\ns_mov_b32 s67, 0\ns_mov_b32 s72, 0x3e0293ee\n"
"s_lshl_b32 s68, s92, 12\ns_add_u32 s88, s68, 1024\nv_lshl_add_u32 v70, v67, 8, v70\nv_add_u32 v56, s88, v70\nv_subrev_u32 v57, 896, v56\nv_and_b32 v70, 1, v66\nv_lshlrev_b32 v70, 4, v70\nv_lshl_add_u32 v70, v68, 5, v70\n"
"v_bfe_u32 v67, v66, 1, 3\nv_lshl_add_u32 v70, v67, 8, v70\nv_add_u32 v58, s88, v70\nv_subrev_u32 v59, 896, v58\ns_lshl_b32 s69, s93, 14\ns_add_u32 s69, s69, %10\ns_add_u32 s61, s69, s68\ns_nop 1\n"
"s_add_u32 m0, s61, 0\ns_nop 0\nbuffer_load_dwordx4 v56, s[40:43], s60 offen nt lds\nbuffer_load_dwordx4 v57, s[40:43], s60 offen offset:1024 nt lds\nbuffer_load_dwordx4 v56, s[40:43], s60 offen offset:2048 nt lds\nbuffer_load_dwordx4 v57, s[40:43], s60 offen offset:3072 nt lds\ns_add_u32 m0, s61, 8192\ns_nop 0\n"
"buffer_load_dwordx4 v58, s[44:47], s60 offen nt lds\nbuffer_load_dwordx4 v59, s[44:47], s60 offen offset:1024 nt lds\nbuffer_load_dwordx4 v58, s[44:47], s60 offen offset:2048 nt lds\nbuffer_load_dwordx4 v59, s[44:47], s60 offen offset:3072 nt lds\ns_add_u32 s60, s60, 8192\nv_bfe_u32 v69, v66, 3, 1\nv_lshlrev_b32 v70, 1, v69\nv_xor_b32 v70, v70, v68\n"
"v_and_b32 v67, 7, v66\nv_lshl_add_u32 v70, v67, 2, v70\nv_lshlrev_b32 v70, 4, v70\nv_lshl_add_u32 v67, v69, 12, v70\nv_add_u32 v52, s69, v67\nv_add_u32 v53, 66048, v52\ns_mul_i32 s68, s92, 7168\ns_add_u32 s68, s68, %10\n"
"s_add_u32 s68, s68, 99072\nv_mul_u32_u24 v67, 14336, v69\nv_add3_u32 v63, v67, v70, s68\nv_lshrrev_b32 v69, 5, v66\nv_and_b32 v70, 31, v66\nv_lshlrev_b32 v70, 3, v70\nv_lshl_add_u32 v70, v69, 12, v70\nv_add_u32 v70, 8192, v70\n"
"v_add_u32 v54, s69, v70\nv_add_u32 v55, 66048, v54\ns_mul_i32 s68, s93, 8224\ns_add_u32 s68, s68, %10\ns_add_u32 s68, s68, 33024\nv_mul_u32_u24 v69, 2056, v68\nv_and_b32 v70, 15, v66\nv_lshlrev_b32 v70, 4, v70\n"
"v_add3_u32 v61, v69, v70, s68\nv_lshlrev_b32 v60, 4, v66\ns_mul_i32 s71, %12, 57344\ns_mul_i32 s68, s92, 7168\ns_add_u32 s71, s71, s68\n.Lround:\ns_waitcnt vmcnt(15)\ns_barrier\n"
"Qta 112,61,0\nQta 114,61,1024\nQdv 24,63,33024\nQta 116,61,8\nQta 118,61,1032\nQdv 28,63,34048\nQta 120,61,256\nQta 122,61,1280\n"
"Qdv 32,63,35072\nQta 124,61,264\nQta 126,61,1288\nQdv 36,63,36096\nQta 128,61,512\nQta 130,61,1536\nQdv 40,63,37120\nQta 132,61,520\n"
"Qta 134,61,1544\nQdv 44,63,38144\nQta 136,61,768\nQta 138,61,1792\nQdv 48,63,39168\nQta 140,61,776\nQta 142,61,1800\nQda 176,63,33536\n"
"Qda 180,63,34560\nQda 184,63,35584\nQda 188,63,36608\nQda 192,63,37632\nQda 196,63,38656\nQda 200,63,39680\ns_waitcnt lgkmcnt(7)\nQzvav 80,112,24\n"
"Qzvav 84,116,24\ns_add_u32 s68, s71, 0\nQzvav 88,120,24\nbuffer_store_dwordx4 a[0:3], v60, s[56:59], s68 offen offset:0 sc1\nQzvav 92,124,24\nQzvav 96,128,24\ns_add_u32 s68, s71, 14336\nQzvav 100,132,24\n"
"buffer_store_dwordx4 a[4:7], v60, s[56:59], s68 offen offset:0 sc1\nQzvav 104,136,24\nQzvav 108,140,24\ns_waitcnt lgkmcnt(0)\nQzvav 112,112,28\ns_waitcnt vmcnt(24)\ns_barrier\nQzvav 116,116,28\n"
"Qta 144,61,16448\ns_add_u32 m0, s62, 0\ns_add_u32 s69, s87, 1048576\nQzvav 120,120,28\nbuffer_load_dwordx4 v62, s[52:55], s69 offen lds\nQzvav 124,124,28\nQta 146,61,17472\nQzvav 128,128,28\n"
"Qzvav 132,132,28\nQta 148,61,16456\ns_add_u32 s69, s87, 1080320\nQzvav 136,136,28\nbuffer_load_dwordx4 v62, s[52:55], s69 offen offset:1024 lds\nQzvav 140,140,28\nQta 150,61,17480\nQzvav 144,112,32\n"
"Qzvav 148,116,32\nQta 152,61,16704\ns_add_u32 s69, s87, 1112056\nQzvav 152,120,32\nbuffer_load_dwordx4 v62, s[52:55], s69 offen offset:2056 lds\nQzvav 156,124,32\nQta 154,61,17728\nQzvav 160,128,32\n"
"Qzvav 164,132,32\nQta 156,61,16712\ns_add_u32 s69, s87, 1143800\nQzvav 168,136,32\nbuffer_load_dwordx4 v62, s[52:55], s69 offen offset:3080 lds\nQzvav 172,140,32\nQta 158,61,17736\nQzvav 176,112,36\n"
"Qzvav 180,116,36\nQta 160,61,16960\ns_add_u32 m0, s63, 33024\ns_add_u32 s88, s86, 256\nQzvav 184,120,36\nbuffer_load_dwordx4 v64, s[48:51], s88 offen lds\nQzvav 188,124,36\nQta 162,61,17984\n"
"Qzvav 192,128,36\nQzvav 196,132,36\nQta 164,61,16968\ns_add_u32 s88, s86, 19712\nQzvav 200,136,36\nbuffer_load_dwordx4 v64, s[48:51], s88 offen offset:1024 lds\nQzvav 204,140,36\nQta 166,61,17992\n"
"Qzvav 208,112,40\nQzvav 212,116,40\nQta 168,61,17216\ns_add_u32 s88, s86, 39168\nQzvav 216,120,40\nbuffer_load_dwordx4 v64, s[48:51], s88 offen offset:2048 lds\nQzvav 220,124,40\nQta 170,61,18240\n"
"Qzvav 224,128,40\nQzvav 228,132,40\nQta 172,61,17224\ns_add_u32 s88, s86, 58624\nQzvav 232,136,40\nbuffer_load_dwordx4 v64, s[48:51], s88 offen offset:3072 lds\nQzvav 236,140,40\nQta 174,61,18248\n"
"Qzvav 240,112,44\nQzvav 244,116,44\ns_add_u32 m0, s63, 37120\ns_add_u32 s88, s86, 82176\nQzvav 248,120,44\nbuffer_load_dwordx4 v64, s[48:51], s88 offen lds\nQzvav 252,124,44\nQzaav 208,128,44\n"
"Qzaav 212,132,44\ns_add_u32 s88, s86, 101632\nQzaav 216,136,44\nbuffer_load_dwordx4 v64, s[48:51], s88 offen offset:1024 lds\nQzaav 220,140,44\nQzaav 224,112,48\nQzaav 228,116,48\ns_add_u32 s88, s86, 121088\n"
"Qzaav 232,120,48\nbuffer_load_dwordx4 v64, s[48:51], s88 offen offset:2048 lds\nQzaav 236,124,48\nQzaav 240,128,48\nQzaav 244,132,48\nQzaav 248,136,48\nQzaav 252,140,48\ns_waitcnt lgkmcnt(0)\n"
"Qmvaa 80,144,176\nQmvaa 84,148,176\ns_add_u32 s68, s71, 28672\nQmvaa 88,152,176\nbuffer_store_dwordx4 a[8:11], v60, s[56:59], s68 offen offset:0 sc1\nQmvaa 92,156,176\nQmvaa 96,160,176\ns_add_u32 s68, s71, 43008\n"
"Qmvaa 100,164,176\nbuffer_store_dwordx4 a[12:15], v60, s[56:59], s68 offen offset:0 sc1\nQmvaa 104,168,176\nQmvaa 108,172,176\ns_waitcnt lgkmcnt(0)\nQmvaa 112,144,180\ns_waitcnt vmcnt(15)\ns_barrier\n"
"Qmvaa 116,148,180\nQda 176,63,512\ns_add_u32 m0, s62, 16448\ns_add_u32 s69, s87, 1310720\nQmvaa 120,152,180\nbuffer_load_dwordx4 v62, s[52:55], s69 offen lds\nQmvaa 124,156,180\nQta 112,61,33024\n"
"Qmvaa 128,160,180\nQta 114,61,34048\nQmvaa 132,164,180\nQta 116,61,33032\ns_add_u32 s69, s87, 1342464\nQmvaa 136,168,180\nbuffer_load_dwordx4 v62, s[52:55], s69 offen offset:1024 lds\nQmvaa 140,172,180\n"
"Qda 180,63,1536\nQmvaa 144,144,184\nQta 118,61,34056\nQmvaa 148,148,184\nQdv 24,63,0\ns_add_u32 s69, s87, 1374200\nQmvaa 152,152,184\nbuffer_load_dwordx4 v62, s[52:55], s69 offen offset:2056 lds\n"
"Qmvaa 156,156,184\nQta 120,61,33280\nQmvaa 160,160,184\nQta 122,61,34304\nQmvaa 164,164,184\nQta 124,61,33288\ns_add_u32 s69, s87, 1405944\nQmvaa 168,168,184\n"
"buffer_load_dwordx4 v62, s[52:55], s69 offen offset:3080 lds\nQmvaa 172,172,184\nQda 184,63,2560\nQmvaa 176,144,188\nQta 126,61,34312\nQmvaa 180,148,188\nQta 128,61,33536\nQmvaa 184,152,188\n"
"Qta 130,61,34560\nQmvaa 188,156,188\nQta 132,61,33544\nQmvaa 192,160,188\nQta 134,61,34568\nQmvaa 196,164,188\nQta 136,61,33792\nQmvaa 200,168,188\n"
"Qta 138,61,34816\nQmvaa 204,172,188\nQda 188,63,3584\nQmvaa 208,144,192\nQta 140,61,33800\nQmvaa 212,148,192\nQta 142,61,34824\nQmvaa 216,152,192\n"
"Qdv 28,63,1024\nQmvaa 220,156,192\nQdv 32,63,2048\nQmvaa 224,160,192\nQdv 36,63,3072\nQmvaa 228,164,192\nQdv 40,63,4096\nQmvaa 232,168,192\n"
"Qdv 44,63,5120\nQmvaa 236,172,192\nQda 192,63,4608\nQmvaa 240,144,196\nQdv 48,63,6144\nQmvaa 244,148,196\nQmvaa 248,152,196\nQmvaa 252,156,196\n"
"Qmaaa 208,160,196\nQmaaa 212,164,196\nQmaaa 216,168,196\nQmaaa 220,172,196\nQda 196,63,5632\nQmaaa 224,144,200\nQmaaa 228,148,200\nQmaaa 232,152,200\n"
"Qmaaa 236,156,200\nQmaaa 240,160,200\nQmaaa 244,164,200\nQmaaa 248,168,200\nQmaaa 252,172,200\nQda 200,63,6656\ns_waitcnt lgkmcnt(2)\nQmvav 80,112,24\n"
"Qmvav 84,116,24\ns_add_u32 s68, s71, 0\nQmvav 88,120,24\nbuffer_store_dwordx4 a[16:19], v60, s[56:59], s68 offen offset:1024 sc1\nQmvav 92,124,24\nQmvav 96,128,24\ns_add_u32 s68, s71, 14336\nQmvav 100,132,24\n"
"buffer_store_dwordx4 a[20:23], v60, s[56:59], s68 offen offset:1024 sc1\nQmvav 104,136,24\nQmvav 108,140,24\ns_waitcnt lgkmcnt(0)\nQmvav 112,112,28\ns_waitcnt vmcnt(28)\ns_barrier\nQmvav 116,116,28\n"
"Qta 144,61,49472\ns_add_u32 m0, s62, 33024\ns_add_u32 s69, s87, 1572864\nQmvav 120,120,28\nbuffer_load_dwordx4 v62, s[52:55], s69 offen lds\nQmvav 124,124,28\nQta 146,61,50496\nQmvav 128,128,28\n"
"Qmvav 132,132,28\nQta 148,61,49480\ns_add_u32 s69, s87, 1604608\nQmvav 136,136,28\nbuffer_load_dwordx4 v62, s[52:55], s69 offen offset:1024 lds\nQmvav 140,140,28\nQta 150,61,50504\nQmvav 144,112,32\n"
"Qmvav 148,116,32\nQta 152,61,49728\ns_add_u32 s69, s87, 1636344\nQmvav 152,120,32\nbuffer_load_dwordx4 v62, s[52:55], s69 offen offset:2056 lds\nQmvav 156,124,32\nQta 154,61,50752\nQmvav 160,128,32\n"
"Qmvav 164,132,32\nQta 156,61,49736\ns_add_u32 s69, s87, 1668088\nQmvav 168,136,32\nbuffer_load_dwordx4 v62, s[52:55], s69 offen offset:3080 lds\nQmvav 172,140,32\nQta 158,61,50760\nQmvav 176,112,36\n"
"Qmvav 180,116,36\nQta 160,61,49984\ns_add_u32 m0, s63, 0\ns_add_u32 s88, s86, 384\nQmvav 184,120,36\nbuffer_load_dwordx4 v64, s[48:51], s88 offen lds\nQmvav 188,124,36\nQta 162,61,51008\n"
"Qmvav 192,128,36\nQmvav 196,132,36\nQta 164,61,49992\ns_add_u32 s88, s86, 19840\nQmvav 200,136,36\nbuffer_load_dwordx4 v64, s[48:51], s88 offen offset:1024 lds\nQmvav 204,140,36\nQta 166,61,51016\n"
"Qmvav 208,112,40\nQmvav 212,116,40\nQta 168,61,50240\ns_add_u32 s88, s86, 39296\nQmvav 216,120,40\nbuffer_load_dwordx4 v64, s[48:51], s88 offen offset:2048 lds\nQmvav 220,124,40\nQta 170,61,51264\n"
"Qmvav 224,128,40\nQmvav 228,132,40\nQta 172,61,50248\ns_add_u32 s88, s86, 58752\nQmvav 232,136,40\nbuffer_load_dwordx4 v64, s[48:51], s88 offen offset:3072 lds\nQmvav 236,140,40\nQta 174,61,51272\n"
"Qmvav 240,112,44\nQmvav 244,116,44\ns_add_u32 m0, s63, 4096\ns_add_u32 s88, s86, 82304\nQmvav 248,120,44\nbuffer_load_dwordx4 v64, s[48:51], s88 offen lds\nQmvav 252,124,44\nQmaav 208,128,44\n"
"Qmaav 212,132,44\ns_add_u32 s88, s86, 101760\nQmaav 216,136,44\nbuffer_load_dwordx4 v64, s[48:51], s88 offen offset:1024 lds\nQmaav 220,140,44\nQmaav 224,112,48\nQmaav 228,116,48\ns_add_u32 s88, s86, 121216\n"
"Qmaav 232,120,48\nbuffer_load_dwordx4 v64, s[48:51], s88 offen offset:2048 lds\nQmaav 236,124,48\nQmaav 240,128,48\nQmaav 244,132,48\nQmaav 248,136,48\nQmaav 252,140,48\ns_waitcnt lgkmcnt(0)\n"
"Qmvaa 80,144,176\nQmvaa 84,148,176\ns_add_u32 s68, s71, 28672\nQmvaa 88,152,176\nbuffer_store_dwordx4 a[24:27], v60, s[56:59], s68 offen offset:1024 sc1\nQmvaa 92,156,176\nQmvaa 96,160,176\ns_add_u32 s68, s71, 43008\n"
"Qmvaa 100,164,176\nbuffer_store_dwordx4 a[28:31], v60, s[56:59], s68 offen offset:1024 sc1\nQmvaa 104,168,176\nQmvaa 108,172,176\ns_waitcnt lgkmcnt(0)\nQmvaa 112,144,180\ns_waitcnt vmcnt(21)\ns_barrier\n"
"Qmvaa 116,148,180\nQda 176,63,33536\ns_add_u32 m0, s62, 49472\ns_add_u32 s69, s87, 1835008\nQmvaa 120,152,180\nbuffer_load_dwordx4 v62, s[52:55], s69 offen lds\nQmvaa 124,156,180\nQta 112,61,0\n"
"Qmvaa 128,160,180\nQta 114,61,1024\nQmvaa 132,164,180\nQta 116,61,8\ns_add_u32 s69, s87, 1866752\nQmvaa 136,168,180\nbuffer_load_dwordx4 v62, s[52:55], s69 offen offset:1024 lds\nQmvaa 140,172,180\n"
"Qda 180,63,34560\nQmvaa 144,144,184\nQta 118,61,1032\nQmvaa 148,148,184\nQdv 24,63,33024\ns_add_u32 s69, s87, 1898488\nQmvaa 152,152,184\nbuffer_load_dwordx4 v62, s[52:55], s69 offen offset:2056 lds\n"
"Qmvaa 156,156,184\nQta 120,61,256\nQmvaa 160,160,184\nQta 122,61,1280\nQmvaa 164,164,184\nQta 124,61,264\ns_add_u32 s69, s87, 1930232\nQmvaa 168,168,184\n"
"buffer_load_dwordx4 v62, s[52:55], s69 offen offset:3080 lds\nQmvaa 172,172,184\nQda 184,63,35584\nQmvaa 176,144,188\nQta 126,61,1288\nQmvaa 180,148,188\nQta 128,61,512\nQmvaa 184,152,188\n"
"Qta 130,61,1536\nQmvaa 188,156,188\nQta 132,61,520\nQmvaa 192,160,188\nQta 134,61,1544\nQmvaa 196,164,188\nQta 136,61,768\nQmvaa 200,168,188\n"
"Qta 138,61,1792\nQmvaa 204,172,188\nQda 188,63,36608\nQmvaa 208,144,192\nQta 140,61,776\nQmvaa 212,148,192\nQta 142,61,1800\nQmvaa 216,152,192\n"
"Qdv 28,63,34048\nQmvaa 220,156,192\nQdv 32,63,35072\nQmvaa 224,160,192\nQdv 36,63,36096\nQmvaa 228,164,192\nQdv 40,63,37120\nQmvaa 232,168,192\n"
"Qdv 44,63,38144\nQmvaa 236,172,192\nQda 192,63,37632\nQmvaa 240,144,196\nQdv 48,63,39168\nQmvaa 244,148,196\nQmvaa 248,152,196\nQmvaa 252,156,196\n"
"Qmaaa 208,160,196\nQmaaa 212,164,196\nQmaaa 216,168,196\nQmaaa 220,172,196\nQda 196,63,38656\nQmaaa 224,144,200\nQmaaa 228,148,200\nQmaaa 232,152,200\n"
"Qmaaa 236,156,200\nQmaaa 240,160,200\nQmaaa 244,164,200\nQmaaa 248,168,200\nQmaaa 252,172,200\nQda 200,63,39680\ns_waitcnt lgkmcnt(2)\nQmvav 80,112,24\n"
"Qmvav 84,116,24\ns_add_u32 s68, s71, 0\nQmvav 88,120,24\nbuffer_store_dwordx4 a[32:35], v60, s[56:59], s68 offen offset:2048 sc1\nQmvav 92,124,24\nQmvav 96,128,24\ns_add_u32 s68, s71, 14336\nQmvav 100,132,24\n"
"buffer_store_dwordx4 a[36:39], v60, s[56:59], s68 offen offset:2048 sc1\nQmvav 104,136,24\nQmvav 108,140,24\ns_waitcnt lgkmcnt(0)\nQmvav 112,112,28\ns_waitcnt vmcnt(21)\ns_barrier\nQmvav 116,116,28\n"
"Qta 144,61,16448\ns_add_u32 m0, s62, 0\ns_add_u32 s69, s87, 2097152\nQmvav 120,120,28\nbuffer_load_dwordx4 v62, s[52:55], s69 offen lds\nQmvav 124,124,28\nQta 146,61,17472\nQmvav 128,128,28\n"
"Qmvav 132,132,28\nQta 148,61,16456\ns_add_u32 s69, s87, 2128896\nQmvav 136,136,28\nbuffer_load_dwordx4 v62, s[52:55], s69 offen offset:1024 lds\nQmvav 140,140,28\nQta 150,61,17480\nQmvav 144,112,32\n"
"Qmvav 148,116,32\nQta 152,61,16704\ns_add_u32 s69, s87, 2160632\nQmvav 152,120,32\nbuffer_load_dwordx4 v62, s[52:55], s69 offen offset:2056 lds\nQmvav 156,124,32\nQta 154,61,17728\nQmvav 160,128,32\n"
"Qmvav 164,132,32\nQta 156,61,16712\ns_add_u32 s69, s87, 2192376\nQmvav 168,136,32\nbuffer_load_dwordx4 v62, s[52:55], s69 offen offset:3080 lds\nQmvav 172,140,32\nQta 158,61,17736\nQmvav 176,112,36\n"
"Qmvav 180,116,36\nQta 160,61,16960\ns_add_u32 m0, s63, 33024\ns_add_u32 s88, s86, 512\nQmvav 184,120,36\nbuffer_load_dwordx4 v64, s[48:51], s88 offen lds\nQmvav 188,124,36\nQta 162,61,17984\n"
"Qmvav 192,128,36\nQmvav 196,132,36\nQta 164,61,16968\ns_add_u32 s88, s86, 19968\nQmvav 200,136,36\nbuffer_load_dwordx4 v64, s[48:51], s88 offen offset:1024 lds\nQmvav 204,140,36\nQta 166,61,17992\n"
"Qmvav 208,112,40\nQmvav 212,116,40\nQta 168,61,17216\ns_add_u32 s88, s86, 39424\nQmvav 216,120,40\nbuffer_load_dwordx4 v64, s[48:51], s88 offen offset:2048 lds\nQmvav 220,124,40\nQta 170,61,18240\n"
"Qmvav 224,128,40\nQmvav 228,132,40\nQta 172,61,17224\ns_add_u32 s88, s86, 58880\nQmvav 232,136,40\nbuffer_load_dwordx4 v64, s[48:51], s88 offen offset:3072 lds\nQmvav 236,140,40\nQta 174,61,18248\n"
"Qmvav 240,112,44\nQmvav 244,116,44\ns_add_u32 m0, s63, 37120\ns_add_u32 s88, s86, 82432\nQmvav 248,120,44\nbuffer_load_dwordx4 v64, s[48:51], s88 offen lds\nQmvav 252,124,44\nQmaav 208,128,44\n"
"Qmaav 212,132,44\ns_add_u32 s88, s86, 101888\nQmaav 216,136,44\nbuffer_load_dwordx4 v64, s[48:51], s88 offen offset:1024 lds\nQmaav 220,140,44\nQmaav 224,112,48\nQmaav 228,116,48\ns_add_u32 s88, s86, 121344\n"
"Qmaav 232,120,48\nbuffer_load_dwordx4 v64, s[48:51], s88 offen offset:2048 lds\nQmaav 236,124,48\nQmaav 240,128,48\nQmaav 244,132,48\nQmaav 248,136,48\nQmaav 252,140,48\ns_waitcnt lgkmcnt(0)\n"
"Qmvaa 80,144,176\nQmvaa 84,148,176\ns_add_u32 s68, s71, 28672\nQmvaa 88,152,176\nbuffer_store_dwordx4 a[40:43], v60, s[56:59], s68 offen offset:2048 sc1\nQmvaa 92,156,176\nQmvaa 96,160,176\ns_add_u32 s68, s71, 43008\n"
"Qmvaa 100,164,176\nbuffer_store_dwordx4 a[44:47], v60, s[56:59], s68 offen offset:2048 sc1\nQmvaa 104,168,176\nQmvaa 108,172,176\ns_waitcnt lgkmcnt(0)\nQmvaa 112,144,180\ns_waitcnt vmcnt(21)\ns_barrier\n"
"Qmvaa 116,148,180\nQda 176,63,512\ns_add_u32 m0, s62, 16448\ns_add_u32 s69, s87, 2359296\nQmvaa 120,152,180\nbuffer_load_dwordx4 v62, s[52:55], s69 offen lds\nQmvaa 124,156,180\nQta 112,61,33024\n"
"Qmvaa 128,160,180\nQta 114,61,34048\nQmvaa 132,164,180\nQta 116,61,33032\ns_add_u32 s69, s87, 2391040\nQmvaa 136,168,180\nbuffer_load_dwordx4 v62, s[52:55], s69 offen offset:1024 lds\nQmvaa 140,172,180\n"
"Qda 180,63,1536\nQmvaa 144,144,184\nQta 118,61,34056\nQmvaa 148,148,184\nQdv 24,63,0\ns_add_u32 s69, s87, 2422776\nQmvaa 152,152,184\nbuffer_load_dwordx4 v62, s[52:55], s69 offen offset:2056 lds\n"
"Qmvaa 156,156,184\nQta 120,61,33280\nQmvaa 160,160,184\nQta 122,61,34304\nQmvaa 164,164,184\nQta 124,61,33288\ns_add_u32 s69, s87, 2454520\nQmvaa 168,168,184\n"
"buffer_load_dwordx4 v62, s[52:55], s69 offen offset:3080 lds\nQmvaa 172,172,184\nQda 184,63,2560\nQmvaa 176,144,188\nQta 126,61,34312\nQmvaa 180,148,188\nQta 128,61,33536\nQmvaa 184,152,188\n"
"Qta 130,61,34560\nQmvaa 188,156,188\nQta 132,61,33544\nQmvaa 192,160,188\nQta 134,61,34568\nQmvaa 196,164,188\nQta 136,61,33792\nQmvaa 200,168,188\n"
"Qta 138,61,34816\nQmvaa 204,172,188\nQda 188,63,3584\nQmvaa 208,144,192\nQta 140,61,33800\nQmvaa 212,148,192\nQta 142,61,34824\nQmvaa 216,152,192\n"
"Qdv 28,63,1024\nQmvaa 220,156,192\nQdv 32,63,2048\nQmvaa 224,160,192\nQdv 36,63,3072\nQmvaa 228,164,192\nQdv 40,63,4096\nQmvaa 232,168,192\n"
"Qdv 44,63,5120\nQmvaa 236,172,192\nQda 192,63,4608\nQmvaa 240,144,196\nQdv 48,63,6144\nQmvaa 244,148,196\nQmvaa 248,152,196\nQmvaa 252,156,196\n"
"Qmaaa 208,160,196\nQmaaa 212,164,196\nQmaaa 216,168,196\nQmaaa 220,172,196\nQda 196,63,5632\nQmaaa 224,144,200\nQmaaa 228,148,200\nQmaaa 232,152,200\n"
"Qmaaa 236,156,200\nQmaaa 240,160,200\nQmaaa 244,164,200\nQmaaa 248,168,200\nQmaaa 252,172,200\nQda 200,63,6656\ns_waitcnt lgkmcnt(2)\nQmvav 80,112,24\n"
"Qmvav 84,116,24\ns_add_u32 s68, s71, 0\nQmvav 88,120,24\nbuffer_store_dwordx4 a[48:51], v60, s[56:59], s68 offen offset:3072 sc1\nQmvav 92,124,24\nQmvav 96,128,24\ns_add_u32 s68, s71, 14336\nQmvav 100,132,24\n"
"buffer_store_dwordx4 a[52:55], v60, s[56:59], s68 offen offset:3072 sc1\nQmvav 104,136,24\nQmvav 108,140,24\ns_waitcnt lgkmcnt(0)\nQmvav 112,112,28\ns_waitcnt vmcnt(21)\ns_barrier\nQmvav 116,116,28\n"
"Qta 144,61,49472\ns_add_u32 m0, s62, 33024\ns_add_u32 s69, s87, 2621440\nQmvav 120,120,28\nbuffer_load_dwordx4 v62, s[52:55], s69 offen lds\nQmvav 124,124,28\nQta 146,61,50496\nQmvav 128,128,28\n"
"Qmvav 132,132,28\nQta 148,61,49480\ns_add_u32 s69, s87, 2653184\nQmvav 136,136,28\nbuffer_load_dwordx4 v62, s[52:55], s69 offen offset:1024 lds\nQmvav 140,140,28\nQta 150,61,50504\nQmvav 144,112,32\n"
"Qmvav 148,116,32\nQta 152,61,49728\ns_add_u32 s69, s87, 2684920\nQmvav 152,120,32\nbuffer_load_dwordx4 v62, s[52:55], s69 offen offset:2056 lds\nQmvav 156,124,32\nQta 154,61,50752\nQmvav 160,128,32\n"
"Qmvav 164,132,32\nQta 156,61,49736\ns_add_u32 s69, s87, 2716664\nQmvav 168,136,32\nbuffer_load_dwordx4 v62, s[52:55], s69 offen offset:3080 lds\nQmvav 172,140,32\nQta 158,61,50760\nQmvav 176,112,36\n"
"Qmvav 180,116,36\nQta 160,61,49984\ns_add_u32 m0, s63, 0\ns_add_u32 s88, s86, 640\nQmvav 184,120,36\nbuffer_load_dwordx4 v64, s[48:51], s88 offen lds\nQmvav 188,124,36\nQta 162,61,51008\n"
"Qmvav 192,128,36\nQmvav 196,132,36\nQta 164,61,49992\ns_add_u32 s88, s86, 20096\nQmvav 200,136,36\nbuffer_load_dwordx4 v64, s[48:51], s88 offen offset:1024 lds\nQmvav 204,140,36\nQta 166,61,51016\n"
"Qmvav 208,112,40\nQmvav 212,116,40\nQta 168,61,50240\ns_add_u32 s88, s86, 39552\nQmvav 216,120,40\nbuffer_load_dwordx4 v64, s[48:51], s88 offen offset:2048 lds\nQmvav 220,124,40\nQta 170,61,51264\n"
"Qmvav 224,128,40\nQmvav 228,132,40\nQta 172,61,50248\ns_add_u32 s88, s86, 59008\nQmvav 232,136,40\nbuffer_load_dwordx4 v64, s[48:51], s88 offen offset:3072 lds\nQmvav 236,140,40\nQta 174,61,51272\n"
"Qmvav 240,112,44\nQmvav 244,116,44\ns_add_u32 m0, s63, 4096\ns_add_u32 s88, s86, 82560\nQmvav 248,120,44\nbuffer_load_dwordx4 v64, s[48:51], s88 offen lds\nQmvav 252,124,44\nQmaav 208,128,44\n"
"Qmaav 212,132,44\ns_add_u32 s88, s86, 102016\nQmaav 216,136,44\nbuffer_load_dwordx4 v64, s[48:51], s88 offen offset:1024 lds\nQmaav 220,140,44\nQmaav 224,112,48\nQmaav 228,116,48\ns_add_u32 s88, s86, 121472\n"
"Qmaav 232,120,48\nbuffer_load_dwordx4 v64, s[48:51], s88 offen offset:2048 lds\nQmaav 236,124,48\nQmaav 240,128,48\nQmaav 244,132,48\nQmaav 248,136,48\nQmaav 252,140,48\ns_waitcnt lgkmcnt(0)\n"
"Qmvaa 80,144,176\nQmvaa 84,148,176\ns_add_u32 s68, s71, 28672\nQmvaa 88,152,176\nbuffer_store_dwordx4 a[56:59], v60, s[56:59], s68 offen offset:3072 sc1\nQmvaa 92,156,176\nQmvaa 96,160,176\ns_add_u32 s68, s71, 43008\n"
"Qmvaa 100,164,176\nbuffer_store_dwordx4 a[60:63], v60, s[56:59], s68 offen offset:3072 sc1\nQmvaa 104,168,176\nQmvaa 108,172,176\ns_waitcnt lgkmcnt(0)\nQmvaa 112,144,180\ns_waitcnt vmcnt(21)\ns_barrier\n"
"Qmvaa 116,148,180\nQda 176,63,33536\ns_add_u32 m0, s62, 49472\ns_add_u32 s69, s87, 2883584\nQmvaa 120,152,180\nbuffer_load_dwordx4 v62, s[52:55], s69 offen lds\nQmvaa 124,156,180\nQta 112,61,0\n"
"Qmvaa 128,160,180\nQta 114,61,1024\nQmvaa 132,164,180\nQta 116,61,8\ns_add_u32 s69, s87, 2915328\nQmvaa 136,168,180\nbuffer_load_dwordx4 v62, s[52:55], s69 offen offset:1024 lds\nQmvaa 140,172,180\n"
"Qda 180,63,34560\nQmvaa 144,144,184\nQta 118,61,1032\nQmvaa 148,148,184\nQdv 24,63,33024\ns_add_u32 s69, s87, 2947064\nQmvaa 152,152,184\nbuffer_load_dwordx4 v62, s[52:55], s69 offen offset:2056 lds\n"
"Qmvaa 156,156,184\nQta 120,61,256\nQmvaa 160,160,184\nQta 122,61,1280\nQmvaa 164,164,184\nQta 124,61,264\ns_add_u32 s69, s87, 2978808\nQmvaa 168,168,184\n"
"buffer_load_dwordx4 v62, s[52:55], s69 offen offset:3080 lds\nQmvaa 172,172,184\nQda 184,63,35584\nQmvaa 176,144,188\nQta 126,61,1288\nQmvaa 180,148,188\nQta 128,61,512\nQmvaa 184,152,188\n"
"Qta 130,61,1536\nQmvaa 188,156,188\nQta 132,61,520\nQmvaa 192,160,188\nQta 134,61,1544\nQmvaa 196,164,188\nQta 136,61,768\nQmvaa 200,168,188\n"
"Qta 138,61,1792\nQmvaa 204,172,188\nQda 188,63,36608\nQmvaa 208,144,192\nQta 140,61,776\nQmvaa 212,148,192\nQta 142,61,1800\nQmvaa 216,152,192\n"
"Qdv 28,63,34048\nQmvaa 220,156,192\nQdv 32,63,35072\nQmvaa 224,160,192\nQdv 36,63,36096\nQmvaa 228,164,192\nQdv 40,63,37120\nQmvaa 232,168,192\n"
"Qdv 44,63,38144\nQmvaa 236,172,192\nQda 192,63,37632\nQmvaa 240,144,196\nQdv 48,63,39168\nQmvaa 244,148,196\nQmvaa 248,152,196\nQmvaa 252,156,196\n"
"Qmaaa 208,160,196\nQmaaa 212,164,196\nQmaaa 216,168,196\nQmaaa 220,172,196\nQda 196,63,38656\nQmaaa 224,144,200\nQmaaa 228,148,200\nQmaaa 232,152,200\n"
"Qmaaa 236,156,200\nQmaaa 240,160,200\nQmaaa 244,164,200\nQmaaa 248,168,200\nQmaaa 252,172,200\nQda 200,63,39680\ns_waitcnt lgkmcnt(2)\nQmvav 80,112,24\n"
"Qmvav 84,116,24\ns_add_u32 s68, s71, 4096\nQmvav 88,120,24\nbuffer_store_dwordx4 a[64:67], v60, s[56:59], s68 offen offset:0 sc1\nQmvav 92,124,24\nQmvav 96,128,24\ns_add_u32 s68, s71, 18432\nQmvav 100,132,24\n"
"buffer_store_dwordx4 a[68:71], v60, s[56:59], s68 offen offset:0 sc1\nQmvav 104,136,24\nQmvav 108,140,24\ns_waitcnt lgkmcnt(0)\nQmvav 112,112,28\ns_waitcnt vmcnt(21)\ns_barrier\nQmvav 116,116,28\n"
"Qta 144,61,16448\ns_add_u32 m0, s62, 0\ns_add_u32 s69, s87, 3145728\nQmvav 120,120,28\nbuffer_load_dwordx4 v62, s[52:55], s69 offen lds\nQmvav 124,124,28\nQta 146,61,17472\nQmvav 128,128,28\n"
"Qmvav 132,132,28\nQta 148,61,16456\ns_add_u32 s69, s87, 3177472\nQmvav 136,136,28\nbuffer_load_dwordx4 v62, s[52:55], s69 offen offset:1024 lds\nQmvav 140,140,28\nQta 150,61,17480\nQmvav 144,112,32\n"
"Qmvav 148,116,32\nQta 152,61,16704\ns_add_u32 s69, s87, 3209208\nQmvav 152,120,32\nbuffer_load_dwordx4 v62, s[52:55], s69 offen offset:2056 lds\nQmvav 156,124,32\nQta 154,61,17728\nQmvav 160,128,32\n"
"Qmvav 164,132,32\nQta 156,61,16712\ns_add_u32 s69, s87, 3240952\nQmvav 168,136,32\nbuffer_load_dwordx4 v62, s[52:55], s69 offen offset:3080 lds\nQmvav 172,140,32\nQta 158,61,17736\nQmvav 176,112,36\n"
"Qmvav 180,116,36\nQta 160,61,16960\ns_add_u32 m0, s63, 33024\ns_add_u32 s88, s86, 768\nQmvav 184,120,36\nbuffer_load_dwordx4 v64, s[48:51], s88 offen lds\nQmvav 188,124,36\nQta 162,61,17984\n"
"Qmvav 192,128,36\nQmvav 196,132,36\nQta 164,61,16968\ns_add_u32 s88, s86, 20224\nQmvav 200,136,36\nbuffer_load_dwordx4 v64, s[48:51], s88 offen offset:1024 lds\nQmvav 204,140,36\nQta 166,61,17992\n"
"Qmvav 208,112,40\nQmvav 212,116,40\nQta 168,61,17216\ns_add_u32 s88, s86, 39680\nQmvav 216,120,40\nbuffer_load_dwordx4 v64, s[48:51], s88 offen offset:2048 lds\nQmvav 220,124,40\nQta 170,61,18240\n"
"Qmvav 224,128,40\nQmvav 228,132,40\nQta 172,61,17224\ns_add_u32 s88, s86, 59136\nQmvav 232,136,40\nbuffer_load_dwordx4 v64, s[48:51], s88 offen offset:3072 lds\nQmvav 236,140,40\nQta 174,61,18248\n"
"Qmvav 240,112,44\nQmvav 244,116,44\ns_add_u32 m0, s63, 37120\ns_add_u32 s88, s86, 82688\nQmvav 248,120,44\nbuffer_load_dwordx4 v64, s[48:51], s88 offen lds\nQmvav 252,124,44\nQmaav 208,128,44\n"
"Qmaav 212,132,44\ns_add_u32 s88, s86, 102144\nQmaav 216,136,44\nbuffer_load_dwordx4 v64, s[48:51], s88 offen offset:1024 lds\nQmaav 220,140,44\nQmaav 224,112,48\nQmaav 228,116,48\ns_add_u32 s88, s86, 121600\n"
"Qmaav 232,120,48\nbuffer_load_dwordx4 v64, s[48:51], s88 offen offset:2048 lds\nQmaav 236,124,48\nQmaav 240,128,48\nQmaav 244,132,48\nQmaav 248,136,48\nQmaav 252,140,48\ns_waitcnt lgkmcnt(0)\n"
"Qmvaa 80,144,176\nQmvaa 84,148,176\ns_add_u32 s68, s71, 32768\nQmvaa 88,152,176\nbuffer_store_dwordx4 a[72:75], v60, s[56:59], s68 offen offset:0 sc1\nQmvaa 92,156,176\nQmvaa 96,160,176\ns_add_u32 s68, s71, 47104\n"
"Qmvaa 100,164,176\nbuffer_store_dwordx4 a[76:79], v60, s[56:59], s68 offen offset:0 sc1\nQmvaa 104,168,176\nQmvaa 108,172,176\ns_waitcnt lgkmcnt(0)\nQmvaa 112,144,180\ns_waitcnt vmcnt(21)\ns_barrier\n"
"Qmvaa 116,148,180\nQda 176,63,512\ns_add_u32 m0, s62, 16448\ns_add_u32 s69, s87, 3407872\nQmvaa 120,152,180\nbuffer_load_dwordx4 v62, s[52:55], s69 offen lds\nQmvaa 124,156,180\nQta 112,61,33024\n"
"Qmvaa 128,160,180\nQta 114,61,34048\nQmvaa 132,164,180\nQta 116,61,33032\ns_add_u32 s69, s87, 3439616\nQmvaa 136,168,180\nbuffer_load_dwordx4 v62, s[52:55], s69 offen offset:1024 lds\nQmvaa 140,172,180\n"
"Qda 180,63,1536\nQmvaa 144,144,184\nQta 118,61,34056\nQmvaa 148,148,184\nQdv 24,63,0\ns_add_u32 s69, s87, 3471352\nQmvaa 152,152,184\nbuffer_load_dwordx4 v62, s[52:55], s69 offen offset:2056 lds\n"
"Qmvaa 156,156,184\nQta 120,61,33280\nQmvaa 160,160,184\nQta 122,61,34304\nQmvaa 164,164,184\nQta 124,61,33288\ns_add_u32 s69, s87, 3503096\nQmvaa 168,168,184\n"
"buffer_load_dwordx4 v62, s[52:55], s69 offen offset:3080 lds\nQmvaa 172,172,184\nQda 184,63,2560\nQmvaa 176,144,188\nQta 126,61,34312\nQmvaa 180,148,188\nQta 128,61,33536\nQmvaa 184,152,188\n"
"Qta 130,61,34560\nQmvaa 188,156,188\nQta 132,61,33544\nQmvaa 192,160,188\nQta 134,61,34568\nQmvaa 196,164,188\nQta 136,61,33792\nQmvaa 200,168,188\n"
"Qta 138,61,34816\nQmvaa 204,172,188\nQda 188,63,3584\nQmvaa 208,144,192\nQta 140,61,33800\nQmvaa 212,148,192\nQta 142,61,34824\nQmvaa 216,152,192\n"
"Qdv 28,63,1024\nQmvaa 220,156,192\nQdv 32,63,2048\nQmvaa 224,160,192\nQdv 36,63,3072\nQmvaa 228,164,192\nQdv 40,63,4096\nQmvaa 232,168,192\n"
"Qdv 44,63,5120\nQmvaa 236,172,192\nQda 192,63,4608\nQmvaa 240,144,196\nQdv 48,63,6144\nQmvaa 244,148,196\nQmvaa 248,152,196\nQmvaa 252,156,196\n"
"Qmaaa 208,160,196\nQmaaa 212,164,196\nQmaaa 216,168,196\nQmaaa 220,172,196\nQda 196,63,5632\nQmaaa 224,144,200\nQmaaa 228,148,200\nQmaaa 232,152,200\n"
"Qmaaa 236,156,200\nQmaaa 240,160,200\nQmaaa 244,164,200\nQmaaa 248,168,200\nQmaaa 252,172,200\nQda 200,63,6656\ns_waitcnt lgkmcnt(2)\nQmvav 80,112,24\n"
"Qmvav 84,116,24\ns_add_u32 s68, s71, 4096\nQmvav 88,120,24\nbuffer_store_dwordx4 a[80:83], v60, s[56:59], s68 offen offset:1024 sc1\nQmvav 92,124,24\nQmvav 96,128,24\ns_add_u32 s68, s71, 18432\nQmvav 100,132,24\n"
"buffer_store_dwordx4 a[84:87], v60, s[56:59], s68 offen offset:1024 sc1\nQmvav 104,136,24\nQmvav 108,140,24\ns_waitcnt lgkmcnt(0)\nQmvav 112,112,28\ns_waitcnt vmcnt(21)\ns_barrier\nQmvav 116,116,28\n"
"Qta 144,61,49472\ns_add_u32 m0, s62, 33024\ns_add_u32 s69, s87, 3670016\nQmvav 120,120,28\nbuffer_load_dwordx4 v62, s[52:55], s69 offen lds\nQmvav 124,124,28\nQta 146,61,50496\nQmvav 128,128,28\n"
"Qmvav 132,132,28\nQta 148,61,49480\ns_add_u32 s69, s87, 3701760\nQmvav 136,136,28\nbuffer_load_dwordx4 v62, s[52:55], s69 offen offset:1024 lds\nQmvav 140,140,28\nQta 150,61,50504\nQmvav 144,112,32\n"
"Qmvav 148,116,32\nQta 152,61,49728\ns_add_u32 s69, s87, 3733496\nQmvav 152,120,32\nbuffer_load_dwordx4 v62, s[52:55], s69 offen offset:2056 lds\nQmvav 156,124,32\nQta 154,61,50752\nQmvav 160,128,32\n"
"Qmvav 164,132,32\nQta 156,61,49736\ns_add_u32 s69, s87, 3765240\nQmvav 168,136,32\nbuffer_load_dwordx4 v62, s[52:55], s69 offen offset:3080 lds\nQmvav 172,140,32\nQta 158,61,50760\nQmvav 176,112,36\n"
"Qmvav 180,116,36\nQta 160,61,49984\ns_add_u32 m0, s63, 0\ns_add_u32 s88, s86, 896\nQmvav 184,120,36\nbuffer_load_dwordx4 v64, s[48:51], s88 offen lds\nQmvav 188,124,36\nQta 162,61,51008\n"
"Qmvav 192,128,36\nQmvav 196,132,36\nQta 164,61,49992\ns_add_u32 s88, s86, 20352\nQmvav 200,136,36\nbuffer_load_dwordx4 v64, s[48:51], s88 offen offset:1024 lds\nQmvav 204,140,36\nQta 166,61,51016\n"
"Qmvav 208,112,40\nQmvav 212,116,40\nQta 168,61,50240\ns_add_u32 s88, s86, 39808\nQmvav 216,120,40\nbuffer_load_dwordx4 v64, s[48:51], s88 offen offset:2048 lds\nQmvav 220,124,40\nQta 170,61,51264\n"
"Qmvav 224,128,40\nQmvav 228,132,40\nQta 172,61,50248\ns_add_u32 s88, s86, 59264\nQmvav 232,136,40\nbuffer_load_dwordx4 v64, s[48:51], s88 offen offset:3072 lds\nQmvav 236,140,40\nQta 174,61,51272\n"
"Qmvav 240,112,44\nQmvav 244,116,44\ns_add_u32 m0, s63, 4096\ns_add_u32 s88, s86, 82816\nQmvav 248,120,44\nbuffer_load_dwordx4 v64, s[48:51], s88 offen lds\nQmvav 252,124,44\nQmaav 208,128,44\n"
"Qmaav 212,132,44\ns_add_u32 s88, s86, 102272\nQmaav 216,136,44\nbuffer_load_dwordx4 v64, s[48:51], s88 offen offset:1024 lds\nQmaav 220,140,44\nQmaav 224,112,48\nQmaav 228,116,48\ns_add_u32 s88, s86, 121728\n"
"Qmaav 232,120,48\nbuffer_load_dwordx4 v64, s[48:51], s88 offen offset:2048 lds\nQmaav 236,124,48\nQmaav 240,128,48\nQmaav 244,132,48\nQmaav 248,136,48\nQmaav 252,140,48\ns_waitcnt lgkmcnt(0)\n"
"Qmvaa 80,144,176\nQmvaa 84,148,176\ns_add_u32 s68, s71, 32768\nQmvaa 88,152,176\nbuffer_store_dwordx4 a[88:91], v60, s[56:59], s68 offen offset:1024 sc1\nQmvaa 92,156,176\nQmvaa 96,160,176\ns_add_u32 s68, s71, 47104\n"
"Qmvaa 100,164,176\nbuffer_store_dwordx4 a[92:95], v60, s[56:59], s68 offen offset:1024 sc1\nQmvaa 104,168,176\nQmvaa 108,172,176\ns_waitcnt lgkmcnt(0)\nQmvaa 112,144,180\ns_waitcnt vmcnt(21)\ns_barrier\n"
"Qmvaa 116,148,180\nQda 176,63,33536\ns_add_u32 m0, s62, 49472\ns_add_u32 s69, s87, 3932160\nQmvaa 120,152,180\nbuffer_load_dwordx4 v62, s[52:55], s69 offen lds\nQmvaa 124,156,180\nQta 112,61,0\n"
"Qmvaa 128,160,180\nQta 114,61,1024\nQmvaa 132,164,180\nQta 116,61,8\ns_add_u32 s69, s87, 3963904\nQmvaa 136,168,180\nbuffer_load_dwordx4 v62, s[52:55], s69 offen offset:1024 lds\nQmvaa 140,172,180\n"
"Qda 180,63,34560\nQmvaa 144,144,184\nQta 118,61,1032\nQmvaa 148,148,184\nQdv 24,63,33024\ns_add_u32 s69, s87, 3995640\nQmvaa 152,152,184\nbuffer_load_dwordx4 v62, s[52:55], s69 offen offset:2056 lds\n"
"Qmvaa 156,156,184\nQta 120,61,256\nQmvaa 160,160,184\nQta 122,61,1280\nQmvaa 164,164,184\nQta 124,61,264\ns_add_u32 s69, s87, 4027384\nQmvaa 168,168,184\n"
"buffer_load_dwordx4 v62, s[52:55], s69 offen offset:3080 lds\nQmvaa 172,172,184\nQda 184,63,35584\nQmvaa 176,144,188\nQta 126,61,1288\nQmvaa 180,148,188\nQta 128,61,512\nQmvaa 184,152,188\n"
"Qta 130,61,1536\nQmvaa 188,156,188\nQta 132,61,520\nQmvaa 192,160,188\nQta 134,61,1544\nQmvaa 196,164,188\nQta 136,61,768\nQmvaa 200,168,188\n"
"Qta 138,61,1792\nQmvaa 204,172,188\nQda 188,63,36608\nQmvaa 208,144,192\nQta 140,61,776\nQmvaa 212,148,192\nQta 142,61,1800\nQmvaa 216,152,192\n"
"Qdv 28,63,34048\nQmvaa 220,156,192\nQdv 32,63,35072\nQmvaa 224,160,192\nQdv 36,63,36096\nQmvaa 228,164,192\nQdv 40,63,37120\nQmvaa 232,168,192\n"
"Qdv 44,63,38144\nQmvaa 236,172,192\nQda 192,63,37632\nQmvaa 240,144,196\nQdv 48,63,39168\nQmvaa 244,148,196\nQmvaa 248,152,196\nQmvaa 252,156,196\n"
"Qmaaa 208,160,196\nQmaaa 212,164,196\nQmaaa 216,168,196\nQmaaa 220,172,196\nQda 196,63,38656\nQmaaa 224,144,200\nQmaaa 228,148,200\nQmaaa 232,152,200\n"
"Qmaaa 236,156,200\nQmaaa 240,160,200\nQmaaa 244,164,200\nQmaaa 248,168,200\nQmaaa 252,172,200\nQda 200,63,39680\ns_waitcnt lgkmcnt(2)\nQmvav 80,112,24\n"
"Qmvav 84,116,24\ns_add_u32 s68, s71, 4096\nQmvav 88,120,24\nbuffer_store_dwordx4 a[96:99], v60, s[56:59], s68 offen offset:2048 sc1\nQmvav 92,124,24\nQmvav 96,128,24\ns_add_u32 s68, s71, 18432\nQmvav 100,132,24\n"
"buffer_store_dwordx4 a[100:103], v60, s[56:59], s68 offen offset:2048 sc1\nQmvav 104,136,24\nQmvav 108,140,24\ns_waitcnt lgkmcnt(0)\nQmvav 112,112,28\ns_waitcnt vmcnt(21)\ns_barrier\nQmvav 116,116,28\n"
"Qta 144,61,16448\ns_add_u32 m0, s62, 0\ns_add_u32 s69, s87, 4194304\nQmvav 120,120,28\nbuffer_load_dwordx4 v62, s[52:55], s69 offen lds\nQmvav 124,124,28\nQta 146,61,17472\nQmvav 128,128,28\n"
"Qmvav 132,132,28\nQta 148,61,16456\ns_add_u32 s69, s87, 4226048\nQmvav 136,136,28\nbuffer_load_dwordx4 v62, s[52:55], s69 offen offset:1024 lds\nQmvav 140,140,28\nQta 150,61,17480\nQmvav 144,112,32\n"
"Qmvav 148,116,32\nQta 152,61,16704\ns_add_u32 s69, s87, 4257784\nQmvav 152,120,32\nbuffer_load_dwordx4 v62, s[52:55], s69 offen offset:2056 lds\nQmvav 156,124,32\nQta 154,61,17728\nQmvav 160,128,32\n"
"Qmvav 164,132,32\nQta 156,61,16712\ns_add_u32 s69, s87, 4289528\nQmvav 168,136,32\nbuffer_load_dwordx4 v62, s[52:55], s69 offen offset:3080 lds\nQmvav 172,140,32\nQta 158,61,17736\nQmvav 176,112,36\n"
"Qmvav 180,116,36\nQta 160,61,16960\ns_add_u32 m0, s63, 33024\ns_add_u32 s88, s86, 1024\nQmvav 184,120,36\nbuffer_load_dwordx4 v64, s[48:51], s88 offen lds\nQmvav 188,124,36\nQta 162,61,17984\n"
"Qmvav 192,128,36\nQmvav 196,132,36\nQta 164,61,16968\ns_add_u32 s88, s86, 20480\nQmvav 200,136,36\nbuffer_load_dwordx4 v64, s[48:51], s88 offen offset:1024 lds\nQmvav 204,140,36\nQta 166,61,17992\n"
"Qmvav 208,112,40\nQmvav 212,116,40\nQta 168,61,17216\ns_add_u32 s88, s86, 39936\nQmvav 216,120,40\nbuffer_load_dwordx4 v64, s[48:51], s88 offen offset:2048 lds\nQmvav 220,124,40\nQta 170,61,18240\n"
"Qmvav 224,128,40\nQmvav 228,132,40\nQta 172,61,17224\ns_add_u32 s88, s86, 59392\nQmvav 232,136,40\nbuffer_load_dwordx4 v64, s[48:51], s88 offen offset:3072 lds\nQmvav 236,140,40\nQta 174,61,18248\n"
"Qmvav 240,112,44\nQmvav 244,116,44\ns_add_u32 m0, s63, 37120\ns_add_u32 s88, s86, 82944\nQmvav 248,120,44\nbuffer_load_dwordx4 v64, s[48:51], s88 offen lds\nQmvav 252,124,44\nQmaav 208,128,44\n"
"Qmaav 212,132,44\ns_add_u32 s88, s86, 102400\nQmaav 216,136,44\nbuffer_load_dwordx4 v64, s[48:51], s88 offen offset:1024 lds\nQmaav 220,140,44\nQmaav 224,112,48\nQmaav 228,116,48\ns_add_u32 s88, s86, 121856\n"
"Qmaav 232,120,48\nbuffer_load_dwordx4 v64, s[48:51], s88 offen offset:2048 lds\nQmaav 236,124,48\nQmaav 240,128,48\nQmaav 244,132,48\nQmaav 248,136,48\nQmaav 252,140,48\ns_waitcnt lgkmcnt(0)\n"
"Qmvaa 80,144,176\nQmvaa 84,148,176\ns_add_u32 s68, s71, 32768\nQmvaa 88,152,176\nbuffer_store_dwordx4 a[104:107], v60, s[56:59], s68 offen offset:2048 sc1\nQmvaa 92,156,176\nQmvaa 96,160,176\ns_add_u32 s68, s71, 47104\n"
"Qmvaa 100,164,176\nbuffer_store_dwordx4 a[108:111], v60, s[56:59], s68 offen offset:2048 sc1\nQmvaa 104,168,176\nQmvaa 108,172,176\ns_waitcnt lgkmcnt(0)\nQmvaa 112,144,180\ns_waitcnt vmcnt(21)\ns_barrier\n"
"Qmvaa 116,148,180\nQda 176,63,512\ns_add_u32 m0, s62, 16448\ns_add_u32 s69, s87, 4456448\nQmvaa 120,152,180\nbuffer_load_dwordx4 v62, s[52:55], s69 offen lds\nQmvaa 124,156,180\nQta 112,61,33024\n"
"Qmvaa 128,160,180\nQta 114,61,34048\nQmvaa 132,164,180\nQta 116,61,33032\ns_add_u32 s69, s87, 4488192\nQmvaa 136,168,180\nbuffer_load_dwordx4 v62, s[52:55], s69 offen offset:1024 lds\nQmvaa 140,172,180\n"
"Qda 180,63,1536\nQmvaa 144,144,184\nQta 118,61,34056\nQmvaa 148,148,184\nQdv 24,63,0\ns_add_u32 s69, s87, 4519928\nQmvaa 152,152,184\nbuffer_load_dwordx4 v62, s[52:55], s69 offen offset:2056 lds\n"
"Qmvaa 156,156,184\nQta 120,61,33280\nQmvaa 160,160,184\nQta 122,61,34304\nQmvaa 164,164,184\nQta 124,61,33288\ns_add_u32 s69, s87, 4551672\nQmvaa 168,168,184\n"
"buffer_load_dwordx4 v62, s[52:55], s69 offen offset:3080 lds\nQmvaa 172,172,184\nQda 184,63,2560\nQmvaa 176,144,188\nQta 126,61,34312\nQmvaa 180,148,188\nQta 128,61,33536\nQmvaa 184,152,188\n"
"Qta 130,61,34560\nQmvaa 188,156,188\nQta 132,61,33544\nQmvaa 192,160,188\nQta 134,61,34568\nQmvaa 196,164,188\nQta 136,61,33792\nQmvaa 200,168,188\n"
"Qta 138,61,34816\nQmvaa 204,172,188\nQda 188,63,3584\nQmvaa 208,144,192\nQta 140,61,33800\nQmvaa 212,148,192\nQta 142,61,34824\nQmvaa 216,152,192\n"
"Qdv 28,63,1024\nQmvaa 220,156,192\nQdv 32,63,2048\nQmvaa 224,160,192\nQdv 36,63,3072\nQmvaa 228,164,192\nQdv 40,63,4096\nQmvaa 232,168,192\n"
"Qdv 44,63,5120\nQmvaa 236,172,192\nQda 192,63,4608\nQmvaa 240,144,196\nQdv 48,63,6144\nQmvaa 244,148,196\nQmvaa 248,152,196\nQmvaa 252,156,196\n"
"Qmaaa 208,160,196\nQmaaa 212,164,196\nQmaaa 216,168,196\nQmaaa 220,172,196\nQda 196,63,5632\nQmaaa 224,144,200\nQmaaa 228,148,200\nQmaaa 232,152,200\n"
"Qmaaa 236,156,200\nQmaaa 240,160,200\nQmaaa 244,164,200\nQmaaa 248,168,200\nQmaaa 252,172,200\nQda 200,63,6656\ns_waitcnt lgkmcnt(2)\nQmvav 80,112,24\n"
"Qmvav 84,116,24\nQmvav 88,120,24\nQmvav 92,124,24\nQmvav 96,128,24\nQmvav 100,132,24\nQmvav 104,136,24\nQmvav 108,140,24\ns_waitcnt lgkmcnt(0)\n"
"Qmvav 112,112,28\ns_waitcnt vmcnt(19)\ns_barrier\nQmvav 116,116,28\nQta 144,61,49472\ns_add_u32 m0, s62, 33024\ns_add_u32 s69, s87, 4718592\nQmvav 120,120,28\n"
"buffer_load_dwordx4 v62, s[52:55], s69 offen lds\nQmvav 124,124,28\nQta 146,61,50496\nQmvav 128,128,28\nQmvav 132,132,28\nQta 148,61,49480\ns_add_u32 s69, s87, 4750336\nQmvav 136,136,28\n"
"buffer_load_dwordx4 v62, s[52:55], s69 offen offset:1024 lds\nQmvav 140,140,28\nQta 150,61,50504\nQmvav 144,112,32\nQmvav 148,116,32\nQta 152,61,49728\ns_add_u32 s69, s87, 4782072\nQmvav 152,120,32\n"
"buffer_load_dwordx4 v62, s[52:55], s69 offen offset:2056 lds\nQmvav 156,124,32\nQta 154,61,50752\nQmvav 160,128,32\nQmvav 164,132,32\nQta 156,61,49736\ns_add_u32 s69, s87, 4813816\nQmvav 168,136,32\n"
"buffer_load_dwordx4 v62, s[52:55], s69 offen offset:3080 lds\nQmvav 172,140,32\nQta 158,61,50760\nQmvav 176,112,36\nQmvav 180,116,36\nQta 160,61,49984\ns_add_u32 m0, s63, 0\ns_add_u32 s88, s86, 1152\n"
"Qmvav 184,120,36\nbuffer_load_dwordx4 v64, s[48:51], s88 offen lds\nQmvav 188,124,36\nQta 162,61,51008\nQmvav 192,128,36\nQmvav 196,132,36\nQta 164,61,49992\ns_add_u32 s88, s86, 20608\n"
"Qmvav 200,136,36\nbuffer_load_dwordx4 v64, s[48:51], s88 offen offset:1024 lds\nQmvav 204,140,36\nQta 166,61,51016\nQmvav 208,112,40\nQmvav 212,116,40\nQta 168,61,50240\ns_add_u32 s88, s86, 40064\n"
"Qmvav 216,120,40\nbuffer_load_dwordx4 v64, s[48:51], s88 offen offset:2048 lds\nQmvav 220,124,40\nQta 170,61,51264\nQmvav 224,128,40\nQmvav 228,132,40\nQta 172,61,50248\ns_add_u32 s88, s86, 59520\n"
"Qmvav 232,136,40\nbuffer_load_dwordx4 v64, s[48:51], s88 offen offset:3072 lds\nQmvav 236,140,40\nQta 174,61,51272\nQmvav 240,112,44\nQmvav 244,116,44\ns_add_u32 m0, s63, 4096\ns_add_u32 s88, s86, 83072\n"
"Qmvav 248,120,44\nbuffer_load_dwordx4 v64, s[48:51], s88 offen lds\nQmvav 252,124,44\nQmaav 208,128,44\nQmaav 212,132,44\ns_add_u32 s88, s86, 102528\nQmaav 216,136,44\nbuffer_load_dwordx4 v64, s[48:51], s88 offen offset:1024 lds\n"
"Qmaav 220,140,44\nQmaav 224,112,48\nQmaav 228,116,48\ns_add_u32 s88, s86, 121984\nQmaav 232,120,48\nbuffer_load_dwordx4 v64, s[48:51], s88 offen offset:2048 lds\nQmaav 236,124,48\nQmaav 240,128,48\n"
"Qmaav 244,132,48\nQmaav 248,136,48\nQmaav 252,140,48\ns_waitcnt lgkmcnt(0)\nQmvaa 80,144,176\nQmvaa 84,148,176\nQmvaa 88,152,176\nQmvaa 92,156,176\n"
"Qmvaa 96,160,176\nQmvaa 100,164,176\nQmvaa 104,168,176\nQmvaa 108,172,176\ns_waitcnt lgkmcnt(0)\nQmvaa 112,144,180\ns_waitcnt vmcnt(17)\ns_barrier\n"
"Qmvaa 116,148,180\nQda 176,63,33536\ns_add_u32 m0, s62, 49472\ns_add_u32 s69, s87, 4980736\nQmvaa 120,152,180\nbuffer_load_dwordx4 v62, s[52:55], s69 offen lds\nQmvaa 124,156,180\nQta 112,61,0\n"
"Qmvaa 128,160,180\nQta 114,61,1024\nQmvaa 132,164,180\nQta 116,61,8\ns_add_u32 s69, s87, 5012480\nQmvaa 136,168,180\nbuffer_load_dwordx4 v62, s[52:55], s69 offen offset:1024 lds\nQmvaa 140,172,180\n"
"Qda 180,63,34560\nQmvaa 144,144,184\nQta 118,61,1032\nQmvaa 148,148,184\nQdv 24,63,33024\ns_add_u32 s69, s87, 5044216\nQmvaa 152,152,184\nbuffer_load_dwordx4 v62, s[52:55], s69 offen offset:2056 lds\n"
"Qmvaa 156,156,184\nQta 120,61,256\nQmvaa 160,160,184\nQta 122,61,1280\nQmvaa 164,164,184\nQta 124,61,264\ns_add_u32 s69, s87, 5075960\nQmvaa 168,168,184\n"
"buffer_load_dwordx4 v62, s[52:55], s69 offen offset:3080 lds\nQmvaa 172,172,184\nQda 184,63,35584\nQmvaa 176,144,188\nQta 126,61,1288\nQmvaa 180,148,188\nQta 128,61,512\nQmvaa 184,152,188\n"
"Qta 130,61,1536\nQmvaa 188,156,188\nQta 132,61,520\nQmvaa 192,160,188\nQta 134,61,1544\nQmvaa 196,164,188\nQta 136,61,768\nQmvaa 200,168,188\n"
"Qta 138,61,1792\nQmvaa 204,172,188\nQda 188,63,36608\nQmvaa 208,144,192\nQta 140,61,776\nQmvaa 212,148,192\nQta 142,61,1800\nQmvaa 216,152,192\n"
"Qdv 28,63,34048\nQmvaa 220,156,192\nQdv 32,63,35072\nQmvaa 224,160,192\nQdv 36,63,36096\nQmvaa 228,164,192\nQdv 40,63,37120\nQmvaa 232,168,192\n"
"Qdv 44,63,38144\nQmvaa 236,172,192\nQda 192,63,37632\nQmvaa 240,144,196\nQdv 48,63,39168\nQmvaa 244,148,196\nQmvaa 248,152,196\nQmvaa 252,156,196\n"
"Qmaaa 208,160,196\nQmaaa 212,164,196\nQmaaa 216,168,196\nQmaaa 220,172,196\nQda 196,63,38656\nQmaaa 224,144,200\nQmaaa 228,148,200\nQmaaa 232,152,200\n"
"Qmaaa 236,156,200\nQmaaa 240,160,200\nQmaaa 244,164,200\nQmaaa 248,168,200\nQmaaa 252,172,200\nQda 200,63,39680\ns_waitcnt lgkmcnt(2)\nQmvav 80,112,24\n"
"Qmvav 84,116,24\nQmvav 88,120,24\nQmvav 92,124,24\nQmvav 96,128,24\nQmvav 100,132,24\nQmvav 104,136,24\nQmvav 108,140,24\ns_waitcnt lgkmcnt(0)\n"
"Qmvav 112,112,28\ns_waitcnt vmcnt(15)\ns_barrier\nQmvav 116,116,28\nQta 144,61,16448\nQmvav 120,120,28\nQmvav 124,124,28\nQta 146,61,17472\n"
"Qmvav 128,128,28\nQmvav 132,132,28\nQta 148,61,16456\nQmvav 136,136,28\nQmvav 140,140,28\nQta 150,61,17480\nQmvav 144,112,32\nQmvav 148,116,32\n"
"Qta 152,61,16704\nQmvav 152,120,32\nQmvav 156,124,32\nQta 154,61,17728\nQmvav 160,128,32\nQmvav 164,132,32\nQta 156,61,16712\nQmvav 168,136,32\n"
"Qmvav 172,140,32\nQta 158,61,17736\nQmvav 176,112,36\nQmvav 180,116,36\nQta 160,61,16960\nQmvav 184,120,36\nQmvav 188,124,36\nQta 162,61,17984\n"
"Qmvav 192,128,36\nQmvav 196,132,36\nQta 164,61,16968\nQmvav 200,136,36\nQmvav 204,140,36\nQta 166,61,17992\nQmvav 208,112,40\nQmvav 212,116,40\n"
"Qta 168,61,17216\nQmvav 216,120,40\nQmvav 220,124,40\nQta 170,61,18240\nQmvav 224,128,40\nQmvav 228,132,40\nQta 172,61,17224\nQmvav 232,136,40\n"
"Qmvav 236,140,40\nQta 174,61,18248\nQmvav 240,112,44\nQmvav 244,116,44\nQmvav 248,120,44\nQmvav 252,124,44\nQmaav 208,128,44\nQmaav 212,132,44\n"
"Qmaav 216,136,44\nQmaav 220,140,44\nQmaav 224,112,48\nQmaav 228,116,48\nQmaav 232,120,48\nQmaav 236,124,48\nQmaav 240,128,48\nQmaav 244,132,48\n"
"Qmaav 248,136,48\nQmaav 252,140,48\ns_waitcnt lgkmcnt(0)\nQmvaa 80,144,176\nQmvaa 84,148,176\nQmvaa 88,152,176\nQmvaa 92,156,176\nQmvaa 96,160,176\n"
"Qmvaa 100,164,176\nQmvaa 104,168,176\nQmvaa 108,172,176\ns_waitcnt lgkmcnt(0)\nQmvaa 112,144,180\ns_waitcnt vmcnt(4)\ns_barrier\nQmvaa 116,148,180\n"
"Qda 176,63,512\ns_add_u32 m0, s61, 33024\nQmvaa 120,152,180\nbuffer_load_dwordx4 v56, s[40:43], s60 offen nt lds\nQmvaa 124,156,180\nQta 112,61,33024\nQmvaa 128,160,180\nQta 114,61,34048\n"
"Qmvaa 132,164,180\nQta 116,61,33032\nQmvaa 136,168,180\nbuffer_load_dwordx4 v57, s[40:43], s60 offen offset:1024 nt lds\nQmvaa 140,172,180\nQda 180,63,1536\nQmvaa 144,144,184\nQta 118,61,34056\n"
"Qmvaa 148,148,184\nQdv 24,63,0\nQmvaa 152,152,184\nbuffer_load_dwordx4 v56, s[40:43], s60 offen offset:2048 nt lds\nQmvaa 156,156,184\nQta 120,61,33280\nQmvaa 160,160,184\nQta 122,61,34304\n"
"Qmvaa 164,164,184\nQta 124,61,33288\nQmvaa 168,168,184\nbuffer_load_dwordx4 v57, s[40:43], s60 offen offset:3072 nt lds\nQmvaa 172,172,184\nQda 184,63,2560\nQmvaa 176,144,188\nQta 126,61,34312\n"
"Qmvaa 180,148,188\nQta 128,61,33536\ns_add_u32 m0, s61, 41216\nQmvaa 184,152,188\nbuffer_load_dwordx4 v58, s[44:47], s60 offen nt lds\nQmvaa 188,156,188\nQta 130,61,34560\nQmvaa 192,160,188\n"
"Qta 132,61,33544\nQmvaa 196,164,188\nQta 134,61,34568\nQmvaa 200,168,188\nbuffer_load_dwordx4 v59, s[44:47], s60 offen offset:1024 nt lds\nQmvaa 204,172,188\nQda 188,63,3584\nQmvaa 208,144,192\n"
"Qta 136,61,33792\nQmvaa 212,148,192\nQta 138,61,34816\nQmvaa 216,152,192\nbuffer_load_dwordx4 v58, s[44:47], s60 offen offset:2048 nt lds\nQmvaa 220,156,192\nQta 140,61,33800\nQmvaa 224,160,192\n"
"Qta 142,61,34824\nQmvaa 228,164,192\nQdv 28,63,1024\nQmvaa 232,168,192\nbuffer_load_dwordx4 v59, s[44:47], s60 offen offset:3072 nt lds\ns_add_u32 s60, s60, 8192\ns_add_u32 s88, s60, 8192\nQmvaa 236,172,192\n"
"Qda 192,63,4608\nQmvaa 240,144,196\nQdv 32,63,2048\nQmvaa 244,148,196\nQdv 36,63,3072\nQmvaa 248,152,196\nQdv 40,63,4096\nQmvaa 252,156,196\n"
"Qdv 44,63,5120\nQmaaa 208,160,196\nQdv 48,63,6144\nQmaaa 212,164,196\nQmaaa 216,168,196\nQmaaa 220,172,196\nQda 196,63,5632\nQmaaa 224,144,200\n"
"Qmaaa 228,148,200\nQmaaa 232,152,200\nQmaaa 236,156,200\nQmaaa 240,160,200\nQmaaa 244,164,200\nQmaaa 248,168,200\nQmaaa 252,172,200\nQda 200,63,6656\n"
"s_waitcnt lgkmcnt(2)\nQmvav 80,112,24\nQmvav 84,116,24\nQmvav 88,120,24\nQmvav 92,124,24\nQmvav 96,128,24\nQmvav 100,132,24\nQmvav 104,136,24\n"
"Qmvav 108,140,24\ns_waitcnt lgkmcnt(0)\nQmvav 112,112,28\ns_waitcnt vmcnt(8)\ns_barrier\nQmvav 116,116,28\nQta 144,61,49472\ns_add_u32 m0, s61, 99072\n"
"Qmvav 120,120,28\nbuffer_load_dwordx4 v56, s[40:43], s88 offen nt lds\nQmvav 124,124,28\nQta 146,61,50496\nQmvav 128,128,28\nQmvav 132,132,28\nQta 148,61,49480\nQmvav 136,136,28\n"
"buffer_load_dwordx4 v57, s[40:43], s88 offen offset:1024 nt lds\nQmvav 140,140,28\nQta 150,61,50504\nQmvav 144,112,32\nQmvav 148,116,32\nQta 152,61,49728\nQmvav 152,120,32\nbuffer_load_dwordx4 v56, s[40:43], s88 offen offset:2048 nt lds\n"
"Qmvav 156,124,32\nQta 154,61,50752\nQmvav 160,128,32\nQmvav 164,132,32\nQta 156,61,49736\nQmvav 168,136,32\nbuffer_load_dwordx4 v57, s[40:43], s88 offen offset:3072 nt lds\nQmvav 172,140,32\n"
"Qta 158,61,50760\nQmvav 176,112,36\nQmvav 180,116,36\nQta 160,61,49984\ns_add_u32 m0, s61, 107264\nQmvav 184,120,36\nbuffer_load_dwordx4 v58, s[44:47], s88 offen nt lds\nQmvav 188,124,36\n"
"Qta 162,61,51008\nQmvav 192,128,36\nQmvav 196,132,36\nQta 164,61,49992\nQmvav 200,136,36\nbuffer_load_dwordx4 v59, s[44:47], s88 offen offset:1024 nt lds\nQmvav 204,140,36\nQta 166,61,51016\n"
"Qmvav 208,112,40\nQmvav 212,116,40\nQta 168,61,50240\nQmvav 216,120,40\nbuffer_load_dwordx4 v58, s[44:47], s88 offen offset:2048 nt lds\nQmvav 220,124,40\nQta 170,61,51264\nQmvav 224,128,40\n"
"Qmvav 228,132,40\nQta 172,61,50248\nQmvav 232,136,40\nbuffer_load_dwordx4 v59, s[44:47], s88 offen offset:3072 nt lds\nQmvav 236,140,40\nQta 174,61,51272\nQmvav 240,112,44\nQmvav 244,116,44\n"
"Qmvav 248,120,44\nQmvav 252,124,44\nQmaav 208,128,44\nQmaav 212,132,44\nQmaav 216,136,44\nQmaav 220,140,44\nQmaav 224,112,48\nQmaav 228,116,48\n"
"Qmaav 232,120,48\nQmaav 236,124,48\nQmaav 240,128,48\nQmaav 244,132,48\nQmaav 248,136,48\nQmaav 252,140,48\ns_waitcnt lgkmcnt(0)\nQmvaa 80,144,176\n"
"Qmvaa 84,148,176\nQmvaa 88,152,176\nQmvaa 92,156,176\nQmvaa 96,160,176\nQmvaa 100,164,176\nQmvaa 104,168,176\nQmvaa 108,172,176\ns_waitcnt lgkmcnt(0)\n"
"Qmvaa 112,144,180\ns_waitcnt vmcnt(63)\ns_barrier\nQmvaa 116,148,180\nQda 112,52,0\ns_add_u32 m0, s61, 66048\nQmvaa 120,152,180\nbuffer_load_dwordx4 v56, s[40:43], s60 offen nt lds\n"
"Qmvaa 124,156,180\nQda 116,52,512\nQmvaa 128,160,180\nQmvaa 132,164,180\nQda 120,52,1024\nQmvaa 136,168,180\nbuffer_load_dwordx4 v57, s[40:43], s60 offen offset:1024 nt lds\nQmvaa 140,172,180\n"
"Qda 124,52,1536\nQmvaa 144,144,184\nQmvaa 148,148,184\nQda 128,52,2048\nQmvaa 152,152,184\nbuffer_load_dwordx4 v56, s[40:43], s60 offen offset:2048 nt lds\nQmvaa 156,156,184\nQda 132,52,2560\n"
"Qmvaa 160,160,184\nQmvaa 164,164,184\nQda 136,52,3072\nQmvaa 168,168,184\nbuffer_load_dwordx4 v57, s[40:43], s60 offen offset:3072 nt lds\nQmvaa 172,172,184\nQda 140,52,3584\nQmvaa 176,144,188\n"
"Qmvaa 180,148,188\ns_add_u32 m0, s61, 74240\nQmvaa 184,152,188\nbuffer_load_dwordx4 v58, s[44:47], s60 offen nt lds\nQmvaa 188,156,188\nQmvaa 192,160,188\nQmvaa 196,164,188\nQmvaa 200,168,188\n"
"buffer_load_dwordx4 v59, s[44:47], s60 offen offset:1024 nt lds\nQmvaa 204,172,188\nQmvaa 208,144,192\nQmvaa 212,148,192\nQmvaa 216,152,192\nbuffer_load_dwordx4 v58, s[44:47], s60 offen offset:2048 nt lds\nQmvaa 220,156,192\nQmvaa 224,160,192\n"
"Qmvaa 228,164,192\nQmvaa 232,168,192\nbuffer_load_dwordx4 v59, s[44:47], s60 offen offset:3072 nt lds\ns_add_u32 s60, s60, 16384\nQmvaa 236,172,192\nQmvaa 240,144,196\nQmvaa 244,148,196\nQmvaa 248,152,196\n"
"Qmvaa 252,156,196\nQmaaa 208,160,196\nQmaaa 212,164,196\nQmaaa 216,168,196\nQmaaa 220,172,196\nQmaaa 224,144,200\nQmaaa 228,148,200\nQmaaa 232,152,200\n"
"Qmaaa 236,156,200\nQmaaa 240,160,200\nQmaaa 244,164,200\nQmaaa 248,168,200\nQmaaa 252,172,200\nQta 144,54,0\nQta 146,54,2048\nQta 148,54,256\n"
"Qta 150,54,2304\nQta 152,54,512\nQta 154,54,2560\nQta 156,54,768\nQta 158,54,2816\nQta 160,54,1024\nQta 162,54,3072\nQta 164,54,1280\n"
"Qta 166,54,3328\nQta 168,54,1536\nQta 170,54,3584\nQta 172,54,1792\nQta 174,54,3840\nv_pk_mul_f32 v[80:81], v[80:81], s[72:73] op_sel_hi:[1,0]\nv_pk_mul_f32 v[82:83], v[82:83], s[72:73] op_sel_hi:[1,0]\nv_pk_mul_f32 v[84:85], v[84:85], s[72:73] op_sel_hi:[1,0]\n"
"v_pk_mul_f32 v[86:87], v[86:87], s[72:73] op_sel_hi:[1,0]\nQk 80,80,81\nQk 81,82,83\nQk 82,84,85\nQk 83,86,87\nv_accvgpr_write_b32 a0, v80\nv_accvgpr_write_b32 a1, v81\nv_accvgpr_write_b32 a2, v82\n"
"v_accvgpr_write_b32 a3, v83\nv_pk_mul_f32 v[88:89], v[88:89], s[72:73] op_sel_hi:[1,0]\nv_pk_mul_f32 v[90:91], v[90:91], s[72:73] op_sel_hi:[1,0]\nv_pk_mul_f32 v[92:93], v[92:93], s[72:73] op_sel_hi:[1,0]\nv_pk_mul_f32 v[94:95], v[94:95], s[72:73] op_sel_hi:[1,0]\nQk 88,88,89\nQk 89,90,91\nQk 90,92,93\n"
"Qk 91,94,95\nv_accvgpr_write_b32 a4, v88\nv_accvgpr_write_b32 a5, v89\nv_accvgpr_write_b32 a6, v90\nv_accvgpr_write_b32 a7, v91\nv_pk_mul_f32 v[96:97], v[96:97], s[72:73] op_sel_hi:[1,0]\nv_pk_mul_f32 v[98:99], v[98:99], s[72:73] op_sel_hi:[1,0]\nv_pk_mul_f32 v[100:101], v[100:101], s[72:73] op_sel_hi:[1,0]\n"
"v_pk_mul_f32 v[102:103], v[102:103], s[72:73] op_sel_hi:[1,0]\nQk 96,96,97\nQk 97,98,99\nQk 98,100,101\nQk 99,102,103\nv_accvgpr_write_b32 a8, v96\nv_accvgpr_write_b32 a9, v97\nv_accvgpr_write_b32 a10, v98\n"
"v_accvgpr_write_b32 a11, v99\nv_pk_mul_f32 v[104:105], v[104:105], s[72:73] op_sel_hi:[1,0]\nv_pk_mul_f32 v[106:107], v[106:107], s[72:73] op_sel_hi:[1,0]\nv_pk_mul_f32 v[108:109], v[108:109], s[72:73] op_sel_hi:[1,0]\nv_pk_mul_f32 v[110:111], v[110:111], s[72:73] op_sel_hi:[1,0]\nQk 104,104,105\nQk 105,106,107\nQk 106,108,109\n"
"Qk 107,110,111\nv_accvgpr_write_b32 a12, v104\nv_accvgpr_write_b32 a13, v105\nv_accvgpr_write_b32 a14, v106\nv_accvgpr_write_b32 a15, v107\nv_pk_mul_f32 v[112:113], v[112:113], s[72:73] op_sel_hi:[1,0]\nv_pk_mul_f32 v[114:115], v[114:115], s[72:73] op_sel_hi:[1,0]\nv_pk_mul_f32 v[116:117], v[116:117], s[72:73] op_sel_hi:[1,0]\n"
"v_pk_mul_f32 v[118:119], v[118:119], s[72:73] op_sel_hi:[1,0]\nQk 112,112,113\nQk 113,114,115\nQk 114,116,117\nQk 115,118,119\nv_accvgpr_write_b32 a16, v112\nv_accvgpr_write_b32 a17, v113\nv_accvgpr_write_b32 a18, v114\n"
"v_accvgpr_write_b32 a19, v115\nv_pk_mul_f32 v[120:121], v[120:121], s[72:73] op_sel_hi:[1,0]\nv_pk_mul_f32 v[122:123], v[122:123], s[72:73] op_sel_hi:[1,0]\nv_pk_mul_f32 v[124:125], v[124:125], s[72:73] op_sel_hi:[1,0]\nv_pk_mul_f32 v[126:127], v[126:127], s[72:73] op_sel_hi:[1,0]\nQk 120,120,121\nQk 121,122,123\nQk 122,124,125\n"
"Qk 123,126,127\nv_accvgpr_write_b32 a20, v120\nv_accvgpr_write_b32 a21, v121\nv_accvgpr_write_b32 a22, v122\nv_accvgpr_write_b32 a23, v123\nv_pk_mul_f32 v[128:129], v[128:129], s[72:73] op_sel_hi:[1,0]\nv_pk_mul_f32 v[130:131], v[130:131], s[72:73] op_sel_hi:[1,0]\nv_pk_mul_f32 v[132:133], v[132:133], s[72:73] op_sel_hi:[1,0]\n"
"v_pk_mul_f32 v[134:135], v[134:135], s[72:73] op_sel_hi:[1,0]\nQk 128,128,129\nQk 129,130,131\nQk 130,132,133\nQk 131,134,135\nv_accvgpr_write_b32 a24, v128\nv_accvgpr_write_b32 a25, v129\nv_accvgpr_write_b32 a26, v130\n"
"v_accvgpr_write_b32 a27, v131\nv_pk_mul_f32 v[136:137], v[136:137], s[72:73] op_sel_hi:[1,0]\nv_pk_mul_f32 v[138:139], v[138:139], s[72:73] op_sel_hi:[1,0]\nv_pk_mul_f32 v[140:141], v[140:141], s[72:73] op_sel_hi:[1,0]\nv_pk_mul_f32 v[142:143], v[142:143], s[72:73] op_sel_hi:[1,0]\nQk 136,136,137\nQk 137,138,139\nQk 138,140,141\n"
"Qk 139,142,143\nv_accvgpr_write_b32 a28, v136\nv_accvgpr_write_b32 a29, v137\nv_accvgpr_write_b32 a30, v138\nv_accvgpr_write_b32 a31, v139\nv_pk_mul_f32 v[144:145], v[144:145], s[72:73] op_sel_hi:[1,0]\nv_pk_mul_f32 v[146:147], v[146:147], s[72:73] op_sel_hi:[1,0]\nv_pk_mul_f32 v[148:149], v[148:149], s[72:73] op_sel_hi:[1,0]\n"
"v_pk_mul_f32 v[150:151], v[150:151], s[72:73] op_sel_hi:[1,0]\nQk 144,144,145\nQk 145,146,147\nQk 146,148,149\nQk 147,150,151\nv_accvgpr_write_b32 a32, v144\nv_accvgpr_write_b32 a33, v145\nv_accvgpr_write_b32 a34, v146\n"
"v_accvgpr_write_b32 a35, v147\nv_pk_mul_f32 v[152:153], v[152:153], s[72:73] op_sel_hi:[1,0]\nv_pk_mul_f32 v[154:155], v[154:155], s[72:73] op_sel_hi:[1,0]\nv_pk_mul_f32 v[156:157], v[156:157], s[72:73] op_sel_hi:[1,0]\nv_pk_mul_f32 v[158:159], v[158:159], s[72:73] op_sel_hi:[1,0]\nQk 152,152,153\nQk 153,154,155\nQk 154,156,157\n"
"Qk 155,158,159\nv_accvgpr_write_b32 a36, v152\nv_accvgpr_write_b32 a37, v153\nv_accvgpr_write_b32 a38, v154\nv_accvgpr_write_b32 a39, v155\nv_pk_mul_f32 v[160:161], v[160:161], s[72:73] op_sel_hi:[1,0]\nv_pk_mul_f32 v[162:163], v[162:163], s[72:73] op_sel_hi:[1,0]\nv_pk_mul_f32 v[164:165], v[164:165], s[72:73] op_sel_hi:[1,0]\n"
"v_pk_mul_f32 v[166:167], v[166:167], s[72:73] op_sel_hi:[1,0]\nQk 160,160,161\nQk 161,162,163\nQk 162,164,165\nQk 163,166,167\nv_accvgpr_write_b32 a40, v160\nv_accvgpr_write_b32 a41, v161\nv_accvgpr_write_b32 a42, v162\n"
"v_accvgpr_write_b32 a43, v163\nv_pk_mul_f32 v[168:169], v[168:169], s[72:73] op_sel_hi:[1,0]\nv_pk_mul_f32 v[170:171], v[170:171], s[72:73] op_sel_hi:[1,0]\nv_pk_mul_f32 v[172:173], v[172:173], s[72:73] op_sel_hi:[1,0]\nv_pk_mul_f32 v[174:175], v[174:175], s[72:73] op_sel_hi:[1,0]\nQk 168,168,169\nQk 169,170,171\nQk 170,172,173\n"
"Qk 171,174,175\nv_accvgpr_write_b32 a44, v168\nv_accvgpr_write_b32 a45, v169\nv_accvgpr_write_b32 a46, v170\nv_accvgpr_write_b32 a47, v171\nv_pk_mul_f32 v[176:177], v[176:177], s[72:73] op_sel_hi:[1,0]\nv_pk_mul_f32 v[178:179], v[178:179], s[72:73] op_sel_hi:[1,0]\nv_pk_mul_f32 v[180:181], v[180:181], s[72:73] op_sel_hi:[1,0]\n"
"v_pk_mul_f32 v[182:183], v[182:183], s[72:73] op_sel_hi:[1,0]\nQk 176,176,177\nQk 177,178,179\nQk 178,180,181\nQk 179,182,183\nv_accvgpr_write_b32 a48, v176\nv_accvgpr_write_b32 a49, v177\nv_accvgpr_write_b32 a50, v178\n"
"v_accvgpr_write_b32 a51, v179\nv_pk_mul_f32 v[184:185], v[184:185], s[72:73] op_sel_hi:[1,0]\nv_pk_mul_f32 v[186:187], v[186:187], s[72:73] op_sel_hi:[1,0]\nv_pk_mul_f32 v[188:189], v[188:189], s[72:73] op_sel_hi:[1,0]\nv_pk_mul_f32 v[190:191], v[190:191], s[72:73] op_sel_hi:[1,0]\nQk 184,184,185\nQk 185,186,187\nQk 186,188,189\n"
"Qk 187,190,191\nv_accvgpr_write_b32 a52, v184\nv_accvgpr_write_b32 a53, v185\nv_accvgpr_write_b32 a54, v186\nv_accvgpr_write_b32 a55, v187\nv_pk_mul_f32 v[192:193], v[192:193], s[72:73] op_sel_hi:[1,0]\nv_pk_mul_f32 v[194:195], v[194:195], s[72:73] op_sel_hi:[1,0]\nv_pk_mul_f32 v[196:197], v[196:197], s[72:73] op_sel_hi:[1,0]\n"
"v_pk_mul_f32 v[198:199], v[198:199], s[72:73] op_sel_hi:[1,0]\nQk 192,192,193\nQk 193,194,195\nQk 194,196,197\nQk 195,198,199\nv_accvgpr_write_b32 a56, v192\nv_accvgpr_write_b32 a57, v193\nv_accvgpr_write_b32 a58, v194\n"
"v_accvgpr_write_b32 a59, v195\nv_pk_mul_f32 v[200:201], v[200:201], s[72:73] op_sel_hi:[1,0]\nv_pk_mul_f32 v[202:203], v[202:203], s[72:73] op_sel_hi:[1,0]\nv_pk_mul_f32 v[204:205], v[204:205], s[72:73] op_sel_hi:[1,0]\nv_pk_mul_f32 v[206:207], v[206:207], s[72:73] op_sel_hi:[1,0]\nQk 200,200,201\nQk 201,202,203\nQk 202,204,205\n"
"Qk 203,206,207\nv_accvgpr_write_b32 a60, v200\nv_accvgpr_write_b32 a61, v201\nv_accvgpr_write_b32 a62, v202\nv_accvgpr_write_b32 a63, v203\nv_pk_mul_f32 v[208:209], v[208:209], s[72:73] op_sel_hi:[1,0]\nv_pk_mul_f32 v[210:211], v[210:211], s[72:73] op_sel_hi:[1,0]\nv_pk_mul_f32 v[212:213], v[212:213], s[72:73] op_sel_hi:[1,0]\n"
"v_pk_mul_f32 v[214:215], v[214:215], s[72:73] op_sel_hi:[1,0]\nQk 208,208,209\nQk 209,210,211\nQk 210,212,213\nQk 211,214,215\nv_accvgpr_write_b32 a64, v208\nv_accvgpr_write_b32 a65, v209\nv_accvgpr_write_b32 a66, v210\n"
"v_accvgpr_write_b32 a67, v211\nv_pk_mul_f32 v[216:217], v[216:217], s[72:73] op_sel_hi:[1,0]\nv_pk_mul_f32 v[218:219], v[218:219], s[72:73] op_sel_hi:[1,0]\nv_pk_mul_f32 v[220:221], v[220:221], s[72:73] op_sel_hi:[1,0]\nv_pk_mul_f32 v[222:223], v[222:223], s[72:73] op_sel_hi:[1,0]\nQk 216,216,217\nQk 217,218,219\nQk 218,220,221\n"
"Qk 219,222,223\nv_accvgpr_write_b32 a68, v216\nv_accvgpr_write_b32 a69, v217\nv_accvgpr_write_b32 a70, v218\nv_accvgpr_write_b32 a71, v219\nv_pk_mul_f32 v[224:225], v[224:225], s[72:73] op_sel_hi:[1,0]\nv_pk_mul_f32 v[226:227], v[226:227], s[72:73] op_sel_hi:[1,0]\nv_pk_mul_f32 v[228:229], v[228:229], s[72:73] op_sel_hi:[1,0]\n"
"v_pk_mul_f32 v[230:231], v[230:231], s[72:73] op_sel_hi:[1,0]\nQk 224,224,225\nQk 225,226,227\nQk 226,228,229\nQk 227,230,231\nv_accvgpr_write_b32 a72, v224\nv_accvgpr_write_b32 a73, v225\nv_accvgpr_write_b32 a74, v226\n"
"v_accvgpr_write_b32 a75, v227\nv_pk_mul_f32 v[232:233], v[232:233], s[72:73] op_sel_hi:[1,0]\nv_pk_mul_f32 v[234:235], v[234:235], s[72:73] op_sel_hi:[1,0]\nv_pk_mul_f32 v[236:237], v[236:237], s[72:73] op_sel_hi:[1,0]\nv_pk_mul_f32 v[238:239], v[238:239], s[72:73] op_sel_hi:[1,0]\nQk 232,232,233\nQk 233,234,235\nQk 234,236,237\n"
"Qk 235,238,239\nv_accvgpr_write_b32 a76, v232\nv_accvgpr_write_b32 a77, v233\nv_accvgpr_write_b32 a78, v234\nv_accvgpr_write_b32 a79, v235\nv_pk_mul_f32 v[240:241], v[240:241], s[72:73] op_sel_hi:[1,0]\nv_pk_mul_f32 v[242:243], v[242:243], s[72:73] op_sel_hi:[1,0]\nv_pk_mul_f32 v[244:245], v[244:245], s[72:73] op_sel_hi:[1,0]\n"
"v_pk_mul_f32 v[246:247], v[246:247], s[72:73] op_sel_hi:[1,0]\nQk 240,240,241\nQk 241,242,243\nQk 242,244,245\nQk 243,246,247\nv_accvgpr_write_b32 a80, v240\nv_accvgpr_write_b32 a81, v241\nv_accvgpr_write_b32 a82, v242\n"
"v_accvgpr_write_b32 a83, v243\nv_pk_mul_f32 v[248:249], v[248:249], s[72:73] op_sel_hi:[1,0]\nv_pk_mul_f32 v[250:251], v[250:251], s[72:73] op_sel_hi:[1,0]\nv_pk_mul_f32 v[252:253], v[252:253], s[72:73] op_sel_hi:[1,0]\nv_pk_mul_f32 v[254:255], v[254:255], s[72:73] op_sel_hi:[1,0]\nQk 248,248,249\nQk 249,250,251\nQk 250,252,253\n"
"Qk 251,254,255\nv_accvgpr_write_b32 a84, v248\nv_accvgpr_write_b32 a85, v249\nv_accvgpr_write_b32 a86, v250\nv_accvgpr_write_b32 a87, v251\nv_accvgpr_read_b32 v0, a208\nv_accvgpr_read_b32 v1, a209\nv_accvgpr_read_b32 v2, a210\n"
"v_accvgpr_read_b32 v3, a211\nv_accvgpr_read_b32 v4, a212\nv_accvgpr_read_b32 v5, a213\nv_accvgpr_read_b32 v6, a214\nv_accvgpr_read_b32 v7, a215\nv_pk_mul_f32 v[0:1], v[0:1], s[72:73] op_sel_hi:[1,0]\nv_pk_mul_f32 v[2:3], v[2:3], s[72:73] op_sel_hi:[1,0]\nv_pk_mul_f32 v[4:5], v[4:5], s[72:73] op_sel_hi:[1,0]\n"
"v_pk_mul_f32 v[6:7], v[6:7], s[72:73] op_sel_hi:[1,0]\nQk 0,0,1\nQk 1,2,3\nQk 2,4,5\nQk 3,6,7\nv_accvgpr_write_b32 a88, v0\nv_accvgpr_write_b32 a89, v1\nv_accvgpr_write_b32 a90, v2\n"
"v_accvgpr_write_b32 a91, v3\nv_accvgpr_read_b32 v8, a216\nv_accvgpr_read_b32 v9, a217\nv_accvgpr_read_b32 v10, a218\nv_accvgpr_read_b32 v11, a219\nv_accvgpr_read_b32 v12, a220\nv_accvgpr_read_b32 v13, a221\nv_accvgpr_read_b32 v14, a222\n"
"v_accvgpr_read_b32 v15, a223\nv_pk_mul_f32 v[8:9], v[8:9], s[72:73] op_sel_hi:[1,0]\nv_pk_mul_f32 v[10:11], v[10:11], s[72:73] op_sel_hi:[1,0]\nv_pk_mul_f32 v[12:13], v[12:13], s[72:73] op_sel_hi:[1,0]\nv_pk_mul_f32 v[14:15], v[14:15], s[72:73] op_sel_hi:[1,0]\nQk 8,8,9\nQk 9,10,11\nQk 10,12,13\n"
"Qk 11,14,15\nv_accvgpr_write_b32 a92, v8\nv_accvgpr_write_b32 a93, v9\nv_accvgpr_write_b32 a94, v10\nv_accvgpr_write_b32 a95, v11\nv_accvgpr_read_b32 v0, a224\nv_accvgpr_read_b32 v1, a225\nv_accvgpr_read_b32 v2, a226\n"
"v_accvgpr_read_b32 v3, a227\nv_accvgpr_read_b32 v4, a228\nv_accvgpr_read_b32 v5, a229\nv_accvgpr_read_b32 v6, a230\nv_accvgpr_read_b32 v7, a231\nv_pk_mul_f32 v[0:1], v[0:1], s[72:73] op_sel_hi:[1,0]\nv_pk_mul_f32 v[2:3], v[2:3], s[72:73] op_sel_hi:[1,0]\nv_pk_mul_f32 v[4:5], v[4:5], s[72:73] op_sel_hi:[1,0]\n"
"v_pk_mul_f32 v[6:7], v[6:7], s[72:73] op_sel_hi:[1,0]\nQk 0,0,1\nQk 1,2,3\nQk 2,4,5\nQk 3,6,7\nv_accvgpr_write_b32 a96, v0\nv_accvgpr_write_b32 a97, v1\nv_accvgpr_write_b32 a98, v2\n"
"v_accvgpr_write_b32 a99, v3\nv_accvgpr_read_b32 v8, a232\nv_accvgpr_read_b32 v9, a233\nv_accvgpr_read_b32 v10, a234\nv_accvgpr_read_b32 v11, a235\nv_accvgpr_read_b32 v12, a236\nv_accvgpr_read_b32 v13, a237\nv_accvgpr_read_b32 v14, a238\n"
"v_accvgpr_read_b32 v15, a239\nv_pk_mul_f32 v[8:9], v[8:9], s[72:73] op_sel_hi:[1,0]\nv_pk_mul_f32 v[10:11], v[10:11], s[72:73] op_sel_hi:[1,0]\nv_pk_mul_f32 v[12:13], v[12:13], s[72:73] op_sel_hi:[1,0]\nv_pk_mul_f32 v[14:15], v[14:15], s[72:73] op_sel_hi:[1,0]\nQk 8,8,9\nQk 9,10,11\nQk 10,12,13\n"
"Qk 11,14,15\nv_accvgpr_write_b32 a100, v8\nv_accvgpr_write_b32 a101, v9\nv_accvgpr_write_b32 a102, v10\nv_accvgpr_write_b32 a103, v11\nv_accvgpr_read_b32 v0, a240\nv_accvgpr_read_b32 v1, a241\nv_accvgpr_read_b32 v2, a242\n"
"v_accvgpr_read_b32 v3, a243\nv_accvgpr_read_b32 v4, a244\nv_accvgpr_read_b32 v5, a245\nv_accvgpr_read_b32 v6, a246\nv_accvgpr_read_b32 v7, a247\nv_pk_mul_f32 v[0:1], v[0:1], s[72:73] op_sel_hi:[1,0]\nv_pk_mul_f32 v[2:3], v[2:3], s[72:73] op_sel_hi:[1,0]\nv_pk_mul_f32 v[4:5], v[4:5], s[72:73] op_sel_hi:[1,0]\n"
"v_pk_mul_f32 v[6:7], v[6:7], s[72:73] op_sel_hi:[1,0]\nQk 0,0,1\nQk 1,2,3\nQk 2,4,5\nQk 3,6,7\nv_accvgpr_write_b32 a104, v0\nv_accvgpr_write_b32 a105, v1\nv_accvgpr_write_b32 a106, v2\n"
"v_accvgpr_write_b32 a107, v3\nv_accvgpr_read_b32 v8, a248\nv_accvgpr_read_b32 v9, a249\nv_accvgpr_read_b32 v10, a250\nv_accvgpr_read_b32 v11, a251\nv_accvgpr_read_b32 v12, a252\nv_accvgpr_read_b32 v13, a253\nv_accvgpr_read_b32 v14, a254\n"
"v_accvgpr_read_b32 v15, a255\nv_pk_mul_f32 v[8:9], v[8:9], s[72:73] op_sel_hi:[1,0]\nv_pk_mul_f32 v[10:11], v[10:11], s[72:73] op_sel_hi:[1,0]\nv_pk_mul_f32 v[12:13], v[12:13], s[72:73] op_sel_hi:[1,0]\nv_pk_mul_f32 v[14:15], v[14:15], s[72:73] op_sel_hi:[1,0]\nQk 8,8,9\nQk 9,10,11\nQk 10,12,13\n"
"Qk 11,14,15\nv_accvgpr_write_b32 a108, v8\nv_accvgpr_write_b32 a109, v9\nv_accvgpr_write_b32 a110, v10\nv_accvgpr_write_b32 a111, v11\nv_mov_b32 v66, 0x3f803f80\nv_accvgpr_write_b32 a204, v66\nv_accvgpr_write_b32 a205, v66\n"
"v_accvgpr_write_b32 a206, v66\nv_accvgpr_write_b32 a207, v66\ns_add_u32 s87, s87, 512\ns_waitcnt lgkmcnt(15)\nQzvaa 0,112,0\nQmvaa 0,116,4\nQmvaa 0,120,8\nQmvaa 0,124,12\n"
"Qzvaa 4,128,0\nQmvaa 4,132,4\nQmvaa 4,136,8\nQmvaa 4,140,12\ns_nop 3\nv_max3_f32 v66, v0, v1, v2\ns_nop 2\nv_max3_f32 v66, v66, v3, v4\n"
"v_max3_f32 v66, v66, v5, v6\nv_max_f32 v66, v66, v7\nQzvaa 8,112,16\nv_mov_b32 v67, v66\nQmvaa 8,116,20\ns_nop 0\nv_permlane32_swap_b32 v66, v67\nv_max_f32 v66, v66, v67\n"
"Qmvaa 8,120,24\nv_mov_b32 v67, v66\nQmvaa 8,124,28\ns_nop 0\nv_permlane16_swap_b32 v66, v67\nv_max_f32 v66, v66, v67\nQzvaa 12,128,16\nv_sub_f32 v24, 0xc1800000, v66\n"
"Qa 0,0,24\nQmvaa 12,132,20\nQa 1,1,24\nQa 2,2,24\nQmvaa 12,136,24\nQa 3,3,24\nQa 4,4,24\nQmvaa 12,140,28\n"
"Qe 0\nQe 1\nQe 2\nQe 3\nQk 16,0,1\nQk 17,2,3\nQa 5,5,24\nQa 6,6,24\n"
"Qa 7,7,24\nv_mov_b32 v25, v24\nQe 4\nQe 5\nQe 6\nQzvaa 0,112,32\nQe 7\nQmvaa 0,116,36\n"
"Qk 18,4,5\nQk 19,6,7\nQmvaa 0,120,40\nv_max3_f32 v68, v8, v9, v10\nv_max3_f32 v68, v68, v11, v12\nQmvaa 0,124,44\nv_max3_f32 v68, v68, v13, v14\nv_max_f32 v68, v68, v15\n"
"Qzvaa 4,128,32\nv_mov_b32 v69, v68\nv_mov_b32 v26, v24\nQmvaa 4,132,36\nv_permlane32_swap_b32 v68, v69\nv_max_f32 v68, v68, v69\nQmvaa 4,136,40\nv_mov_b32 v69, v68\n"
"v_mov_b32 v27, v24\nQmvaa 4,140,44\ns_waitcnt lgkmcnt(0)\nv_permlane16_swap_b32 v68, v69\nQzvav 80,144,16\nv_max_f32 v68, v68, v69\nv_sub_f32 v28, 0xc1800000, v68\nQzvav 84,148,16\n"
"Qa 8,8,28\nQa 9,9,28\nQzvav 88,152,16\nQa 10,10,28\nQa 11,11,28\nQzvav 92,156,16\nQe 8\nQzvav 96,160,16\n"
"Qe 9\nQzvav 100,164,16\nQe 10\nQzvav 104,168,16\nQe 11\nQzvav 108,172,16\nQk 20,8,9\nQk 21,10,11\n"
"Qzaav 176,204,16\nQa 12,12,28\nQa 13,13,28\nQzvaa 8,112,48\nQa 14,14,28\nQa 15,15,28\nQmvaa 8,116,52\nQe 12\n"
"Qmvaa 8,120,56\nQe 13\nQe 14\nQe 15\nQmvaa 8,124,60\nQk 22,12,13\nQk 23,14,15\nQzvaa 12,128,48\n"
"v_max3_f32 v66, v0, v1, v2\nv_max3_f32 v66, v66, v3, v4\nQmvaa 12,132,52\nv_max3_f32 v66, v66, v5, v6\nv_max_f32 v66, v66, v7\nQmvaa 12,136,56\nv_mov_b32 v67, v66\nv_mov_b32 v29, v28\n"
"Qmvaa 12,140,60\nv_permlane32_swap_b32 v66, v67\nv_max_f32 v66, v66, v67\nQzvav 112,144,20\nv_mov_b32 v67, v66\nv_mov_b32 v30, v28\nQzvav 116,148,20\nv_permlane16_swap_b32 v66, v67\n"
"v_max_f32 v66, v66, v67\nQzvav 120,152,20\nv_sub_f32 v32, 0xc1800000, v66\nQa 0,0,32\nQzvav 124,156,20\nQa 1,1,32\nQa 2,2,32\nQzvav 128,160,20\n"
"Qa 3,3,32\nQa 4,4,32\nQzvav 132,164,20\nQe 0\nQzvav 136,168,20\nQe 1\nQzvav 140,172,20\nQe 2\n"
"Qe 3\nQzaav 180,204,20\nQk 16,0,1\nQk 17,2,3\nQzvaa 0,112,64\nQa 5,5,32\nQa 6,6,32\nQmvaa 0,116,68\n"
"Qa 7,7,32\nv_max3_f32 v68, v8, v9, v10\nQmvaa 0,120,72\nQe 4\nQe 5\nQe 6\nQe 7\nQmvaa 0,124,76\n"
"Qk 18,4,5\nQk 19,6,7\nQzvaa 4,128,64\nv_max3_f32 v68, v68, v11, v12\nv_max3_f32 v68, v68, v13, v14\nQmvaa 4,132,68\nv_max_f32 v68, v68, v15\nv_mov_b32 v69, v68\n"
"Qmvaa 4,136,72\ns_nop 0\nv_permlane32_swap_b32 v68, v69\nv_max_f32 v68, v68, v69\nQmvaa 4,140,76\nv_mov_b32 v69, v68\nv_mov_b32 v31, v28\nQzvav 144,144,16\n"
"v_permlane16_swap_b32 v68, v69\nv_max_f32 v68, v68, v69\nQzvav 148,148,16\nv_sub_f32 v36, 0xc1800000, v68\nQa 8,8,36\nQzvav 152,152,16\nQa 9,9,36\nQa 10,10,36\n"
"Qzvav 156,156,16\nQa 11,11,36\nQa 12,12,36\nQzvav 160,160,16\nQe 8\nQzvav 164,164,16\nQe 9\nQzvav 168,168,16\n"
"Qe 10\nQzvav 172,172,16\nQe 11\nQzaav 184,204,16\nQk 20,8,9\nQk 21,10,11\nQzvaa 8,112,80\nQa 13,13,36\n"
"Qa 14,14,36\nQmvaa 8,116,84\nQa 15,15,36\nv_max3_f32 v66, v0, v1, v2\nQmvaa 8,120,88\nQe 12\nQe 13\nQe 14\n"
"Qe 15\nQmvaa 8,124,92\nQk 22,12,13\nQk 23,14,15\nQzvaa 12,128,80\nv_max3_f32 v66, v66, v3, v4\nv_max3_f32 v66, v66, v5, v6\nQmvaa 12,132,84\n"
"v_max_f32 v66, v66, v7\nv_mov_b32 v67, v66\nQmvaa 12,136,88\ns_nop 0\nv_permlane32_swap_b32 v66, v67\nv_max_f32 v66, v66, v67\nQmvaa 12,140,92\nv_mov_b32 v67, v66\n"
"v_mov_b32 v33, v32\nQzvav 176,144,20\nv_permlane16_swap_b32 v66, v67\nv_max_f32 v66, v66, v67\nQzvav 180,148,20\ns_waitcnt vmcnt(16)\ns_barrier\nQzvav 184,152,20\n"
"v_sub_f32 v40, 0xc1800000, v66\nQa 0,0,40\nQzvav 188,156,20\nQa 1,1,40\nQa 2,2,40\nQzvav 192,160,20\nQa 3,3,40\nQa 4,4,40\n"
"Qzvav 196,164,20\nQe 0\nQzvav 200,168,20\nQe 1\nQzvav 204,172,20\nQe 2\nQe 3\nQzaav 188,204,20\n"
"Qk 16,0,1\nQk 17,2,3\nQzvaa 0,112,96\nQda 112,52,33024\nQa 5,5,40\nQmvaa 0,116,100\nQda 116,52,33536\nQa 6,6,40\n"
"Qmvaa 0,120,104\nQda 120,52,34048\nQa 7,7,40\nQe 4\nQe 5\nQe 6\nQe 7\nQmvaa 0,124,108\n"
"Qda 124,52,34560\nQk 18,4,5\nQk 19,6,7\nQzvaa 4,128,96\nQda 128,52,35072\nv_max3_f32 v68, v8, v9, v10\nQmvaa 4,132,100\nQda 132,52,35584\n"
"v_max3_f32 v68, v68, v11, v12\nQmvaa 4,136,104\nQda 136,52,36096\nv_max3_f32 v68, v68, v13, v14\nQmvaa 4,140,108\nQda 140,52,36608\nv_max_f32 v68, v68, v15\nQzvav 208,144,16\n"
"v_mov_b32 v69, v68\nv_mov_b32 v34, v32\nQzvav 212,148,16\ns_add_u32 m0, s61, 0\nv_permlane32_swap_b32 v68, v69\nQzvav 216,152,16\nbuffer_load_dwordx4 v56, s[40:43], s60 offen nt lds\nQzvav 220,156,16\n"
"v_max_f32 v68, v68, v69\nv_mov_b32 v69, v68\nQzvav 224,160,16\ns_nop 0\nv_permlane16_swap_b32 v68, v69\nv_max_f32 v68, v68, v69\nQzvav 228,164,16\nv_sub_f32 v44, 0xc1800000, v68\n"
"Qa 8,8,44\nQzvav 232,168,16\nbuffer_load_dwordx4 v57, s[40:43], s60 offen offset:1024 nt lds\nQzvav 236,172,16\nQa 9,9,44\nQa 10,10,44\nQa 11,11,44\nQe 8\n"
"Qe 9\nQe 10\nQe 11\nQzaav 192,204,16\ns_waitcnt lgkmcnt(0)\nQk 20,8,9\nQk 21,10,11\nQcvaa 8,112,0,24\n"
"Qa 12,12,44\nQa 13,13,44\nQmvaa 8,116,4\nbuffer_load_dwordx4 v56, s[40:43], s60 offen offset:2048 nt lds\nQmvaa 8,120,8\nQa 14,14,44\nQa 15,15,44\nQe 12\n"
"Qe 13\nQe 14\nQe 15\nQmvaa 8,124,12\nQk 22,12,13\nQk 23,14,15\nQcvaa 12,128,0,24\nv_max3_f32 v66, v0, v1, v2\n"
"v_max3_f32 v66, v66, v3, v4\nQmvaa 12,132,4\nbuffer_load_dwordx4 v57, s[40:43], s60 offen offset:3072 nt lds\nQmvaa 12,136,8\nv_max3_f32 v66, v66, v5, v6\nv_max_f32 v66, v66, v7\nQmvaa 12,140,12\nv_mov_b32 v67, v66\n"
"v_mov_b32 v35, v32\nQzvav 240,144,20\ns_add_u32 m0, s61, 8192\nv_permlane32_swap_b32 v66, v67\nQzvav 244,148,20\nbuffer_load_dwordx4 v58, s[44:47], s60 offen nt lds\nQzvav 248,152,20\nv_max_f32 v66, v66, v67\n"
"v_mov_b32 v67, v66\nQzvav 252,156,20\nQe 8\nQzaav 208,160,20\nQe 9\nQzaav 212,164,20\nbuffer_load_dwordx4 v59, s[44:47], s60 offen offset:1024 nt lds\nQe 10\n"
"Qzaav 216,168,20\nQe 11\nQzaav 220,172,20\nQe 12\nv_permlane16_swap_b32 v66, v67\nv_max_f32 v66, v66, v67\nv_sub_f32 v48, 0xc1800000, v66\nQa 0,0,48\n"
"Qa 1,1,48\nQa 2,2,48\nQa 3,3,48\nQe 0\nQe 1\nQe 2\nQe 3\nQzaav 196,204,20\n"
"Qk 20,8,9\nQk 21,10,11\nQk 16,0,1\nQk 17,2,3\nQcvaa 0,112,16,28\nbuffer_load_dwordx4 v58, s[44:47], s60 offen offset:2048 nt lds\nQe 13\nQmvaa 0,116,20\n"
"Qe 14\nQmvaa 0,120,24\nQe 15\nQa 4,4,48\nQa 5,5,48\nQa 6,6,48\nQa 7,7,48\nQe 4\n"
"Qe 5\nQe 6\nQe 7\nQmvaa 0,124,28\nQk 22,12,13\nQk 23,14,15\nQk 18,4,5\nQk 19,6,7\n"
"Qcvaa 4,128,16,28\nbuffer_load_dwordx4 v59, s[44:47], s60 offen offset:3072 nt lds\nQmvaa 4,132,20\nv_mov_b32 v37, v36\nv_mov_b32 v38, v36\nQmvaa 4,136,24\ns_add_u32 s60, s60, 8192\nv_mov_b32 v39, v36\n"
"Qmvaa 4,140,28\nv_mov_b32 v41, v40\nv_mov_b32 v42, v40\nQzaav 224,144,16\nQta 144,54,33024\nQta 146,54,35072\nQzaav 228,148,16\nQta 148,54,33280\n"
"Qta 150,54,35328\nv_mov_b32 v43, v40\nv_mov_b32 v45, v44\nQzaav 232,152,16\nQta 152,54,33536\nQta 154,54,35584\nv_mov_b32 v46, v44\nQzaav 236,156,16\n"
"Qta 156,54,33792\nQta 158,54,35840\nv_mov_b32 v47, v44\nv_mov_b32 v49, v48\nQzaav 240,160,16\nQta 160,54,34048\nQta 162,54,36096\nv_mov_b32 v50, v48\n"
"Qzaav 244,164,16\nQta 164,54,34304\nQta 166,54,36352\nv_mov_b32 v51, v48\nQzaav 248,168,16\nQta 168,54,34560\nQta 170,54,36608\nQzaav 252,172,16\n"
"Qta 172,54,34816\nQta 174,54,36864\nQzaav 200,204,16\nQe 0\ns_mov_b32 s66, 0\n.Lloop:\nQcvaa 8,112,32,32\nQe 1\n"
"Qmvaa 8,116,36\nQe 2\nQmvaa 8,120,40\nQe 3\nQmvaa 8,124,44\nQk 16,0,1\nQk 17,2,3\nQcvaa 12,128,32,32\n"
"Qe 4\nQmvaa 12,132,36\nQe 5\nQmvaa 12,136,40\nQe 6\nQmvaa 12,140,44\ns_waitcnt lgkmcnt(0)\nQk 18,4,5\n"
"Qmvav 80,144,20\nQe 7\nQmvav 84,148,20\ns_waitcnt vmcnt(8)\ns_barrier\nQmvav 88,152,20\nQk 19,6,7\nQmvav 92,156,20\n"
"s_add_u32 m0, s61, 33024\nQmvav 96,160,20\nbuffer_load_dwordx4 v56, s[40:43], s60 offen nt lds\nQmvav 100,164,20\nQe 8\nQmvav 104,168,20\nQe 9\nQmvav 108,172,20\n"
"Qe 10\nQmaav 176,204,20\nQe 11\nQcvaa 0,112,48,36\nQk 20,8,9\nQk 21,10,11\nQmvaa 0,116,52\nQe 12\n"
"Qmvaa 0,120,56\nbuffer_load_dwordx4 v57, s[40:43], s60 offen offset:1024 nt lds\nQmvaa 0,124,60\nQe 13\nQcvaa 4,128,48,36\nQe 14\nQmvaa 4,132,52\nQe 15\n"
"Qmvaa 4,136,56\nQk 22,12,13\nQk 23,14,15\nQmvaa 4,140,60\nQmvav 112,144,16\nQmvav 116,148,16\nbuffer_load_dwordx4 v56, s[40:43], s60 offen offset:2048 nt lds\nQmvav 120,152,16\n"
"Qmvav 124,156,16\nQe 0\nQmvav 128,160,16\nQe 1\nQmvav 132,164,16\nQe 2\nQmvav 136,168,16\nQe 3\n"
"Qmvav 140,172,16\nQe 4\nQmaav 180,204,16\nbuffer_load_dwordx4 v57, s[40:43], s60 offen offset:3072 nt lds\nQcvaa 8,112,64,40\nQk 16,0,1\nQk 17,2,3\nQmvaa 8,116,68\n"
"Qe 5\nQmvaa 8,120,72\nQe 6\nQmvaa 8,124,76\nQe 7\nQcvaa 12,128,64,40\nQk 18,4,5\nQk 19,6,7\n"
"Qmvaa 12,132,68\ns_add_u32 m0, s61, 41216\nQmvaa 12,136,72\nbuffer_load_dwordx4 v58, s[44:47], s60 offen nt lds\nQmvaa 12,140,76\nQmvav 144,144,20\nQmvav 148,148,20\nQmvav 152,152,20\n"
"Qmvav 156,156,20\nQe 8\nQmvav 160,160,20\nQe 9\nQmvav 164,164,20\nbuffer_load_dwordx4 v59, s[44:47], s60 offen offset:1024 nt lds\nQmvav 168,168,20\nQe 10\n"
"Qmvav 172,172,20\nQe 11\nQmaav 184,204,20\nQk 20,8,9\nQk 21,10,11\nQcvaa 0,112,80,44\nQe 12\nQmvaa 0,116,84\n"
"Qe 13\nQmvaa 0,120,88\nQe 14\nQmvaa 0,124,92\nbuffer_load_dwordx4 v58, s[44:47], s60 offen offset:2048 nt lds\nQcvaa 4,128,80,44\nQe 15\nQmvaa 4,132,84\n"
"Qk 22,12,13\nQk 23,14,15\nQmvaa 4,136,88\nQmvaa 4,140,92\nQmvav 176,144,16\nQmvav 180,148,16\nQmvav 184,152,16\nbuffer_load_dwordx4 v59, s[44:47], s60 offen offset:3072 nt lds\n"
"Qmvav 188,156,16\nQe 0\nQmvav 192,160,16\nQe 1\nQmvav 196,164,16\nQe 2\nQmvav 200,168,16\nQe 3\n"
"Qmvav 204,172,16\ns_add_u32 s60, s60, 8192\nQmaav 188,204,16\nQk 16,0,1\nQk 17,2,3\nQcvaa 8,112,96,48\nQda 112,53,0\nQmvaa 8,116,100\n"
"Qda 116,53,512\nQmvaa 8,120,104\nQda 120,53,1024\nQmvaa 8,124,108\nQda 124,53,1536\nQcvaa 12,128,96,48\nQda 128,53,2048\nQmvaa 12,132,100\n"
"Qda 132,53,2560\nQmvaa 12,136,104\nQda 136,53,3072\nQmvaa 12,140,108\nQda 140,53,3584\nQmvav 208,144,20\nQe 4\nQmvav 212,148,20\n"
"Qe 5\nQmvav 216,152,20\nQe 6\nQmvav 220,156,20\nQe 7\nQmvav 224,160,20\nQk 18,4,5\nQk 19,6,7\n"
"Qmvav 228,164,20\nQe 8\nQmvav 232,168,20\nQe 9\nQmvav 236,172,20\nQe 10\nQmaav 192,204,20\ns_waitcnt lgkmcnt(0)\n"
"Qk 20,8,9\nQcvaa 0,112,0,24\nQe 11\nQmvaa 0,116,4\nQk 21,10,11\nQmvaa 0,120,8\nQe 12\nQmvaa 0,124,12\n"
"Qe 13\nQcvaa 4,128,0,24\nQe 14\nQmvaa 4,132,4\nQe 15\nQmvaa 4,136,8\nQk 22,12,13\nQk 23,14,15\n"
"Qmvaa 4,140,12\nQmvav 240,144,16\nQmvav 244,148,16\nQmvav 248,152,16\nQmvav 252,156,16\nQe 0\nQmaav 208,160,16\nQe 1\n"
"Qmaav 212,164,16\nQe 2\nQmaav 216,168,16\nQe 3\nQmaav 220,172,16\nQe 4\nQmaav 196,204,16\nQk 16,0,1\n"
"Qk 17,2,3\nQcvaa 8,112,16,28\nQe 5\nQmvaa 8,116,20\nQe 6\nQmvaa 8,120,24\nQe 7\nQmvaa 8,124,28\n"
"Qk 18,4,5\nQk 19,6,7\nQcvaa 12,128,16,28\nQmvaa 12,132,20\nQmvaa 12,136,24\nQmvaa 12,140,28\nQmaav 224,144,20\nQta 144,55,0\n"
"Qta 146,55,2048\nQmaav 228,148,20\nQta 148,55,256\nQta 150,55,2304\nQmaav 232,152,20\nQta 152,55,512\nQta 154,55,2560\nQmaav 236,156,20\n"
"Qta 156,55,768\nQta 158,55,2816\nQmaav 240,160,20\nQta 160,55,1024\nQta 162,55,3072\nQmaav 244,164,20\nQta 164,55,1280\nQta 166,55,3328\n"
"Qmaav 248,168,20\nQta 168,55,1536\nQta 170,55,3584\nQmaav 252,172,20\nQta 172,55,1792\nQta 174,55,3840\nQmaav 200,204,20\nQe 8\n"
"Qcvaa 0,112,32,32\nQe 9\nQmvaa 0,116,36\nQe 10\nQmvaa 0,120,40\nQe 11\nQmvaa 0,124,44\nQk 20,8,9\n"
"Qk 21,10,11\nQcvaa 4,128,32,32\nQe 12\nQmvaa 4,132,36\nQe 13\nQmvaa 4,136,40\nQe 14\nQmvaa 4,140,44\n"
"s_waitcnt lgkmcnt(0)\nQk 22,12,13\nQmvav 80,144,16\nQe 15\nQmvav 84,148,16\ns_waitcnt vmcnt(16)\ns_barrier\nQmvav 88,152,16\n"
"Qk 23,14,15\nQmvav 92,156,16\ns_add_u32 m0, s61, 66048\nQmvav 96,160,16\nbuffer_load_dwordx4 v56, s[40:43], s60 offen nt lds\nQmvav 100,164,16\nQe 0\nQmvav 104,168,16\n"
"Qe 1\nQmvav 108,172,16\nQe 2\nQmaav 176,204,16\nQe 3\nQcvaa 8,112,48,36\nQk 16,0,1\nQk 17,2,3\n"
"Qmvaa 8,116,52\nQe 4\nQmvaa 8,120,56\nbuffer_load_dwordx4 v57, s[40:43], s60 offen offset:1024 nt lds\nQmvaa 8,124,60\nQe 5\nQcvaa 12,128,48,36\nQe 6\n"
"Qmvaa 12,132,52\nQe 7\nQmvaa 12,136,56\nQk 18,4,5\nQk 19,6,7\nQmvaa 12,140,60\nQmvav 112,144,20\nQmvav 116,148,20\n"
"buffer_load_dwordx4 v56, s[40:43], s60 offen offset:2048 nt lds\nQmvav 120,152,20\nQmvav 124,156,20\nQe 8\nQmvav 128,160,20\nQe 9\nQmvav 132,164,20\nQe 10\n"
"Qmvav 136,168,20\nQe 11\nQmvav 140,172,20\nQe 12\nQmaav 180,204,20\nbuffer_load_dwordx4 v57, s[40:43], s60 offen offset:3072 nt lds\nQcvaa 0,112,64,40\nQk 20,8,9\n"
"Qk 21,10,11\nQmvaa 0,116,68\nQe 13\nQmvaa 0,120,72\nQe 14\nQmvaa 0,124,76\nQe 15\nQcvaa 4,128,64,40\n"
"Qk 22,12,13\nQk 23,14,15\nQmvaa 4,132,68\ns_add_u32 m0, s61, 74240\nQmvaa 4,136,72\nbuffer_load_dwordx4 v58, s[44:47], s60 offen nt lds\nQmvaa 4,140,76\nQmvav 144,144,16\n"
"Qmvav 148,148,16\nQmvav 152,152,16\nQmvav 156,156,16\nQe 0\nQmvav 160,160,16\nQe 1\nQmvav 164,164,16\nbuffer_load_dwordx4 v59, s[44:47], s60 offen offset:1024 nt lds\n"
"Qmvav 168,168,16\nQe 2\nQmvav 172,172,16\nQe 3\nQmaav 184,204,16\nQk 16,0,1\nQk 17,2,3\nQcvaa 8,112,80,44\n"
"Qe 4\nQmvaa 8,116,84\nQe 5\nQmvaa 8,120,88\nQe 6\nQmvaa 8,124,92\nbuffer_load_dwordx4 v58, s[44:47], s60 offen offset:2048 nt lds\nQcvaa 12,128,80,44\n"
"Qe 7\nQmvaa 12,132,84\nQk 18,4,5\nQk 19,6,7\nQmvaa 12,136,88\nQmvaa 12,140,92\nQmvav 176,144,20\nQmvav 180,148,20\n"
"Qmvav 184,152,20\nbuffer_load_dwordx4 v59, s[44:47], s60 offen offset:3072 nt lds\nQmvav 188,156,20\nQe 8\nQmvav 192,160,20\nQe 9\nQmvav 196,164,20\nQe 10\n"
"Qmvav 200,168,20\nQe 11\nQmvav 204,172,20\ns_add_u32 s60, s60, 8192\nQmaav 188,204,20\nQk 20,8,9\nQk 21,10,11\nQcvaa 0,112,96,48\n"
"Qda 112,53,33024\nQmvaa 0,116,100\nQda 116,53,33536\nQmvaa 0,120,104\nQda 120,53,34048\nQmvaa 0,124,108\nQda 124,53,34560\nQcvaa 4,128,96,48\n"
"Qda 128,53,35072\nQmvaa 4,132,100\nQda 132,53,35584\nQmvaa 4,136,104\nQda 136,53,36096\nQmvaa 4,140,108\nQda 140,53,36608\nQmvav 208,144,16\n"
"Qe 12\nQmvav 212,148,16\nQe 13\nQmvav 216,152,16\nQe 14\nQmvav 220,156,16\nQe 15\nQmvav 224,160,16\n"
"Qk 22,12,13\nQk 23,14,15\nQmvav 228,164,16\nQe 0\nQmvav 232,168,16\nQe 1\nQmvav 236,172,16\nQe 2\n"
"Qmaav 192,204,16\ns_waitcnt lgkmcnt(0)\nQk 16,0,1\nQcvaa 8,112,0,24\nQe 3\nQmvaa 8,116,4\nQk 17,2,3\nQmvaa 8,120,8\n"
"Qe 4\nQmvaa 8,124,12\nQe 5\nQcvaa 12,128,0,24\nQe 6\nQmvaa 12,132,4\nQe 7\nQmvaa 12,136,8\n"
"Qk 18,4,5\nQk 19,6,7\nQmvaa 12,140,12\nQmvav 240,144,20\nQmvav 244,148,20\nQmvav 248,152,20\nQmvav 252,156,20\nQe 8\n"
"Qmaav 208,160,20\nQe 9\nQmaav 212,164,20\nQe 10\nQmaav 216,168,20\nQe 11\nQmaav 220,172,20\nQe 12\n"
"Qmaav 196,204,20\nQk 20,8,9\nQk 21,10,11\nQcvaa 0,112,16,28\nQe 13\nQmvaa 0,116,20\nQe 14\nQmvaa 0,120,24\n"
"Qe 15\nQmvaa 0,124,28\nQk 22,12,13\nQk 23,14,15\nQcvaa 4,128,16,28\nQmvaa 4,132,20\nQmvaa 4,136,24\nQmvaa 4,140,28\n"
"Qmaav 224,144,16\nQta 144,55,33024\nQta 146,55,35072\nQmaav 228,148,16\nQta 148,55,33280\nQta 150,55,35328\nQmaav 232,152,16\nQta 152,55,33536\n"
"Qta 154,55,35584\nQmaav 236,156,16\nQta 156,55,33792\nQta 158,55,35840\nQmaav 240,160,16\nQta 160,55,34048\nQta 162,55,36096\nQmaav 244,164,16\n"
"Qta 164,55,34304\nQta 166,55,36352\nQmaav 248,168,16\nQta 168,55,34560\nQta 170,55,36608\nQmaav 252,172,16\nQta 172,55,34816\nQta 174,55,36864\n"
"Qmaav 200,204,16\nQe 0\nQcvaa 8,112,32,32\nQe 1\nQmvaa 8,116,36\nQe 2\nQmvaa 8,120,40\nQe 3\n"
"Qmvaa 8,124,44\nQk 16,0,1\nQk 17,2,3\nQcvaa 12,128,32,32\nQe 4\nQmvaa 12,132,36\nQe 5\nQmvaa 12,136,40\n"
"Qe 6\nQmvaa 12,140,44\ns_waitcnt lgkmcnt(0)\nQk 18,4,5\nQmvav 80,144,20\nQe 7\nQmvav 84,148,20\ns_waitcnt vmcnt(16)\n"
"s_barrier\nQmvav 88,152,20\nQk 19,6,7\nQmvav 92,156,20\ns_add_u32 m0, s61, 99072\nQmvav 96,160,20\nbuffer_load_dwordx4 v56, s[40:43], s60 offen nt lds\nQmvav 100,164,20\n"
"Qe 8\nQmvav 104,168,20\nQe 9\nQmvav 108,172,20\nQe 10\nQmaav 176,204,20\nQe 11\nQcvaa 0,112,48,36\n"
"Qk 20,8,9\nQk 21,10,11\nQmvaa 0,116,52\nQe 12\nQmvaa 0,120,56\nbuffer_load_dwordx4 v57, s[40:43], s60 offen offset:1024 nt lds\nQmvaa 0,124,60\nQe 13\n"
"Qcvaa 4,128,48,36\nQe 14\nQmvaa 4,132,52\nQe 15\nQmvaa 4,136,56\nQk 22,12,13\nQk 23,14,15\nQmvaa 4,140,60\n"
"Qmvav 112,144,16\nQmvav 116,148,16\nbuffer_load_dwordx4 v56, s[40:43], s60 offen offset:2048 nt lds\nQmvav 120,152,16\nQmvav 124,156,16\nQe 0\nQmvav 128,160,16\nQe 1\n"
"Qmvav 132,164,16\nQe 2\nQmvav 136,168,16\nQe 3\nQmvav 140,172,16\nQe 4\nQmaav 180,204,16\nbuffer_load_dwordx4 v57, s[40:43], s60 offen offset:3072 nt lds\n"
"Qcvaa 8,112,64,40\nQk 16,0,1\nQk 17,2,3\nQmvaa 8,116,68\nQe 5\nQmvaa 8,120,72\nQe 6\nQmvaa 8,124,76\n"
"Qe 7\nQcvaa 12,128,64,40\nQk 18,4,5\nQk 19,6,7\nQmvaa 12,132,68\ns_add_u32 m0, s61, 107264\nQmvaa 12,136,72\nbuffer_load_dwordx4 v58, s[44:47], s60 offen nt lds\n"
"Qmvaa 12,140,76\nQmvav 144,144,20\nQmvav 148,148,20\nQmvav 152,152,20\nQmvav 156,156,20\nQe 8\nQmvav 160,160,20\nQe 9\n"
"Qmvav 164,164,20\nbuffer_load_dwordx4 v59, s[44:47], s60 offen offset:1024 nt lds\nQmvav 168,168,20\nQe 10\nQmvav 172,172,20\nQe 11\nQmaav 184,204,20\nQk 20,8,9\n"
"Qk 21,10,11\nQcvaa 0,112,80,44\nQe 12\nQmvaa 0,116,84\nQe 13\nQmvaa 0,120,88\nQe 14\nQmvaa 0,124,92\n"
"buffer_load_dwordx4 v58, s[44:47], s60 offen offset:2048 nt lds\nQcvaa 4,128,80,44\nQe 15\nQmvaa 4,132,84\nQk 22,12,13\nQk 23,14,15\nQmvaa 4,136,88\nQmvaa 4,140,92\n"
"Qmvav 176,144,16\nQmvav 180,148,16\nQmvav 184,152,16\nbuffer_load_dwordx4 v59, s[44:47], s60 offen offset:3072 nt lds\nQmvav 188,156,16\nQe 0\nQmvav 192,160,16\nQe 1\n"
"Qmvav 196,164,16\nQe 2\nQmvav 200,168,16\nQe 3\nQmvav 204,172,16\ns_add_u32 s60, s60, 8192\ns_and_b32 s68, s60, 0x40000\ns_add_u32 s60, s60, s68\n"
"Qmaav 188,204,16\nQk 16,0,1\nQk 17,2,3\nQcvaa 8,112,96,48\nQda 112,52,0\nQmvaa 8,116,100\nQda 116,52,512\nQmvaa 8,120,104\n"
"Qda 120,52,1024\nQmvaa 8,124,108\nQda 124,52,1536\nQcvaa 12,128,96,48\nQda 128,52,2048\nQmvaa 12,132,100\nQda 132,52,2560\nQmvaa 12,136,104\n"
"Qda 136,52,3072\nQmvaa 12,140,108\nQda 140,52,3584\nQmvav 208,144,20\nQe 4\nQmvav 212,148,20\nQe 5\nQmvav 216,152,20\n"
"Qe 6\nQmvav 220,156,20\nQe 7\nQmvav 224,160,20\nQk 18,4,5\nQk 19,6,7\nQmvav 228,164,20\nQe 8\n"
"Qmvav 232,168,20\nQe 9\nQmvav 236,172,20\nQe 10\nQmaav 192,204,20\ns_waitcnt lgkmcnt(0)\nQk 20,8,9\nQcvaa 0,112,0,24\n"
"Qe 11\nQmvaa 0,116,4\nQk 21,10,11\nQmvaa 0,120,8\nQe 12\nQmvaa 0,124,12\nQe 13\nQcvaa 4,128,0,24\n"
"Qe 14\nQmvaa 4,132,4\nQe 15\nQmvaa 4,136,8\nQk 22,12,13\nQk 23,14,15\nQmvaa 4,140,12\nQmvav 240,144,16\n"
"Qmvav 244,148,16\nQmvav 248,152,16\nQmvav 252,156,16\nQe 0\nQmaav 208,160,16\nQe 1\nQmaav 212,164,16\nQe 2\n"
"Qmaav 216,168,16\nQe 3\nQmaav 220,172,16\nQe 4\nQmaav 196,204,16\nQk 16,0,1\nQk 17,2,3\nQcvaa 8,112,16,28\n"
"Qe 5\nQmvaa 8,116,20\nQe 6\nQmvaa 8,120,24\nQe 7\nQmvaa 8,124,28\nQk 18,4,5\nQk 19,6,7\n"
"Qcvaa 12,128,16,28\nQmvaa 12,132,20\nQmvaa 12,136,24\nQmvaa 12,140,28\nQmaav 224,144,20\nQta 144,54,0\nQta 146,54,2048\nQmaav 228,148,20\n"
"Qta 148,54,256\nQta 150,54,2304\nQmaav 232,152,20\nQta 152,54,512\nQta 154,54,2560\nQmaav 236,156,20\nQta 156,54,768\nQta 158,54,2816\n"
"Qmaav 240,160,20\nQta 160,54,1024\nQta 162,54,3072\nQmaav 244,164,20\nQta 164,54,1280\nQta 166,54,3328\nQmaav 248,168,20\nQta 168,54,1536\n"
"Qta 170,54,3584\nQmaav 252,172,20\nQta 172,54,1792\nQta 174,54,3840\nQmaav 200,204,20\nQe 8\nQcvaa 0,112,32,32\nQe 9\n"
"Qmvaa 0,116,36\nQe 10\nQmvaa 0,120,40\nQe 11\nQmvaa 0,124,44\nQk 20,8,9\nQk 21,10,11\nQcvaa 4,128,32,32\n"
"Qe 12\nQmvaa 4,132,36\nQe 13\nQmvaa 4,136,40\nQe 14\nQmvaa 4,140,44\ns_waitcnt lgkmcnt(0)\nQk 22,12,13\n"
"Qmvav 80,144,16\nQe 15\nQmvav 84,148,16\ns_waitcnt vmcnt(16)\ns_barrier\nQmvav 88,152,16\nQk 23,14,15\nQmvav 92,156,16\n"
"s_add_u32 m0, s61, 0\nQmvav 96,160,16\nbuffer_load_dwordx4 v56, s[40:43], s60 offen nt lds\nQmvav 100,164,16\nQe 0\nQmvav 104,168,16\nQe 1\nQmvav 108,172,16\n"
"Qe 2\nQmaav 176,204,16\nQe 3\nQcvaa 8,112,48,36\nQk 16,0,1\nQk 17,2,3\nQmvaa 8,116,52\nQe 4\n"
"Qmvaa 8,120,56\nbuffer_load_dwordx4 v57, s[40:43], s60 offen offset:1024 nt lds\nQmvaa 8,124,60\nQe 5\nQcvaa 12,128,48,36\nQe 6\nQmvaa 12,132,52\nQe 7\n"
"Qmvaa 12,136,56\nQk 18,4,5\nQk 19,6,7\nQmvaa 12,140,60\nQmvav 112,144,20\nQmvav 116,148,20\nbuffer_load_dwordx4 v56, s[40:43], s60 offen offset:2048 nt lds\nQmvav 120,152,20\n"
"Qmvav 124,156,20\nQe 8\nQmvav 128,160,20\nQe 9\nQmvav 132,164,20\nQe 10\nQmvav 136,168,20\nQe 11\n"
"Qmvav 140,172,20\nQe 12\nQmaav 180,204,20\nbuffer_load_dwordx4 v57, s[40:43], s60 offen offset:3072 nt lds\nQcvaa 0,112,64,40\nQk 20,8,9\nQk 21,10,11\nQmvaa 0,116,68\n"
"Qe 13\nQmvaa 0,120,72\nQe 14\nQmvaa 0,124,76\nQe 15\nQcvaa 4,128,64,40\nQk 22,12,13\nQk 23,14,15\n"
"Qmvaa 4,132,68\ns_add_u32 m0, s61, 8192\nQmvaa 4,136,72\nbuffer_load_dwordx4 v58, s[44:47], s60 offen nt lds\nQmvaa 4,140,76\nQmvav 144,144,16\nQmvav 148,148,16\nQmvav 152,152,16\n"
"Qmvav 156,156,16\nQe 0\nQmvav 160,160,16\nQe 1\nQmvav 164,164,16\nbuffer_load_dwordx4 v59, s[44:47], s60 offen offset:1024 nt lds\nQmvav 168,168,16\nQe 2\n"
"Qmvav 172,172,16\nQe 3\nQmaav 184,204,16\nQk 16,0,1\nQk 17,2,3\nQcvaa 8,112,80,44\nQe 4\nQmvaa 8,116,84\n"
"Qe 5\nQmvaa 8,120,88\nQe 6\nQmvaa 8,124,92\nbuffer_load_dwordx4 v58, s[44:47], s60 offen offset:2048 nt lds\nQcvaa 12,128,80,44\nQe 7\nQmvaa 12,132,84\n"
"Qk 18,4,5\nQk 19,6,7\nQmvaa 12,136,88\nQmvaa 12,140,92\nQmvav 176,144,20\nQmvav 180,148,20\nQmvav 184,152,20\nbuffer_load_dwordx4 v59, s[44:47], s60 offen offset:3072 nt lds\n"
"Qmvav 188,156,20\nQe 8\nQmvav 192,160,20\nQe 9\nQmvav 196,164,20\nQe 10\nQmvav 200,168,20\nQe 11\n"
"Qmvav 204,172,20\ns_add_u32 s60, s60, 8192\nQmaav 188,204,20\nQk 20,8,9\nQk 21,10,11\nQcvaa 0,112,96,48\nQda 112,52,33024\nQmvaa 0,116,100\n"
"Qda 116,52,33536\nQmvaa 0,120,104\nQda 120,52,34048\nQmvaa 0,124,108\nQda 124,52,34560\nQcvaa 4,128,96,48\nQda 128,52,35072\nQmvaa 4,132,100\n"
"Qda 132,52,35584\nQmvaa 4,136,104\nQda 136,52,36096\nQmvaa 4,140,108\nQda 140,52,36608\nQmvav 208,144,16\nQe 12\nQmvav 212,148,16\n"
"Qe 13\nQmvav 216,152,16\nQe 14\nQmvav 220,156,16\nQe 15\nQmvav 224,160,16\nQk 22,12,13\nQk 23,14,15\n"
"Qmvav 228,164,16\nQe 0\nQmvav 232,168,16\nQe 1\nQmvav 236,172,16\nQe 2\nQmaav 192,204,16\ns_waitcnt lgkmcnt(0)\n"
"Qk 16,0,1\nQcvaa 8,112,0,24\nQe 3\nQmvaa 8,116,4\nQk 17,2,3\nQmvaa 8,120,8\nQe 4\nQmvaa 8,124,12\n"
"Qe 5\nQcvaa 12,128,0,24\nQe 6\nQmvaa 12,132,4\nQe 7\nQmvaa 12,136,8\nQk 18,4,5\nQk 19,6,7\n"
"Qmvaa 12,140,12\nQmvav 240,144,20\nQmvav 244,148,20\nQmvav 248,152,20\nQmvav 252,156,20\nQe 8\nQmaav 208,160,20\nQe 9\n"
"Qmaav 212,164,20\nQe 10\nQmaav 216,168,20\nQe 11\nQmaav 220,172,20\nQe 12\nQmaav 196,204,20\nQk 20,8,9\n"
"Qk 21,10,11\nQcvaa 0,112,16,28\nQe 13\nQmvaa 0,116,20\nQe 14\nQmvaa 0,120,24\nQe 15\nQmvaa 0,124,28\n"
"Qk 22,12,13\nQk 23,14,15\nQcvaa 4,128,16,28\nQmvaa 4,132,20\nQmvaa 4,136,24\nQmvaa 4,140,28\nQmaav 224,144,16\nQta 144,54,33024\n"
"Qta 146,54,35072\nQmaav 228,148,16\nQta 148,54,33280\nQta 150,54,35328\nQmaav 232,152,16\nQta 152,54,33536\nQta 154,54,35584\nQmaav 236,156,16\n"
"Qta 156,54,33792\nQta 158,54,35840\nQmaav 240,160,16\nQta 160,54,34048\nQta 162,54,36096\nQmaav 244,164,16\nQta 164,54,34304\nQta 166,54,36352\n"
"Qmaav 248,168,16\nQta 168,54,34560\nQta 170,54,36608\nQmaav 252,172,16\nQta 172,54,34816\nQta 174,54,36864\nQmaav 200,204,16\nQe 0\n"
"s_add_u32 s66, s66, 1\ns_cmp_lt_u32 s66, 7\ns_cbranch_scc1 .Lloop\ns_cmp_eq_u32 s67, 0\ns_cbranch_scc1 .Lnowo\ns_mov_b32 s52, %15\ns_and_b32 s53, %16, 0xffff\ns_mov_b32 s54, 40960\n"
"s_mul_i32 s68, s70, 10240\ns_add_u32 m0, %10, 132096\ns_nop 0\nbuffer_load_dwordx4 v60, s[52:55], s68 offen lds\ns_add_u32 s68, s68, 1024\nbuffer_load_dwordx4 v60, s[52:55], s68 offen lds\ns_add_u32 s68, s68, 1024\nbuffer_load_dwordx4 v60, s[52:55], s68 offen lds\n"
"s_add_u32 s68, s68, 1024\nbuffer_load_dwordx4 v60, s[52:55], s68 offen lds\ns_add_u32 s68, s68, 1024\nbuffer_load_dwordx4 v60, s[52:55], s68 offen lds\ns_add_u32 s68, s68, 1024\nbuffer_load_dwordx4 v60, s[52:55], s68 offen lds\ns_add_u32 s68, s68, 1024\nbuffer_load_dwordx4 v60, s[52:55], s68 offen lds\n"
"s_add_u32 s68, s68, 1024\nbuffer_load_dwordx4 v60, s[52:55], s68 offen lds\ns_add_u32 s68, s68, 1024\nbuffer_load_dwordx4 v60, s[52:55], s68 offen lds\ns_add_u32 s68, s68, 1024\nbuffer_load_dwordx4 v60, s[52:55], s68 offen lds\ns_add_u32 s68, s68, 1024\ns_mov_b32 s54, 0\n"
"s_mov_b32 s50, 0\ns_add_u32 s71, s71, 114688\n.Lnowo:\nQcvaa 8,112,32,32\nQe 1\nQmvaa 8,116,36\nQe 2\nQmvaa 8,120,40\n"
"Qe 3\nQmvaa 8,124,44\nQk 16,0,1\nQk 17,2,3\nQcvaa 12,128,32,32\nQe 4\nQmvaa 12,132,36\nQe 5\n"
"Qmvaa 12,136,40\nQe 6\nQmvaa 12,140,44\ns_waitcnt lgkmcnt(0)\nQk 18,4,5\nQmvav 80,144,20\nQe 7\nQmvav 84,148,20\n"
"s_waitcnt vmcnt(16)\ns_barrier\nQmvav 88,152,20\nQk 19,6,7\nQmvav 92,156,20\ns_add_u32 m0, s62, 0\ns_add_u32 s69, s87, 0\nQmvav 96,160,20\n"
"buffer_load_dwordx4 v62, s[52:55], s69 offen lds\nQmvav 100,164,20\nQe 8\nQmvav 104,168,20\nQe 9\nQmvav 108,172,20\nQe 10\nQmaav 176,204,20\n"
"Qe 11\nQcvaa 0,112,48,36\nQk 20,8,9\nQk 21,10,11\nQmvaa 0,116,52\ns_add_u32 s69, s87, 31744\nQe 12\nQmvaa 0,120,56\n"
"buffer_load_dwordx4 v62, s[52:55], s69 offen offset:1024 lds\nQmvaa 0,124,60\nQe 13\nQcvaa 4,128,48,36\nQe 14\nQmvaa 4,132,52\nQe 15\nQmvaa 4,136,56\n"
"Qk 22,12,13\nQk 23,14,15\nQmvaa 4,140,60\nQmvav 112,144,16\ns_add_u32 s69, s87, 63480\nQmvav 116,148,16\nbuffer_load_dwordx4 v62, s[52:55], s69 offen offset:2056 lds\nQmvav 120,152,16\n"
"Qmvav 124,156,16\nQe 0\nQmvav 128,160,16\nQe 1\nQmvav 132,164,16\nQe 2\nQmvav 136,168,16\nQe 3\n"
"Qmvav 140,172,16\ns_add_u32 s69, s87, 95224\nQe 4\nQmaav 180,204,16\nbuffer_load_dwordx4 v62, s[52:55], s69 offen offset:3080 lds\nQcvaa 8,112,64,40\nQk 16,0,1\nQk 17,2,3\n"
"Qmvaa 8,116,68\nQe 5\nQmvaa 8,120,72\nQe 6\nQmvaa 8,124,76\nQe 7\nQcvaa 12,128,64,40\nQk 18,4,5\n"
"Qk 19,6,7\nQmvaa 12,132,68\ns_add_u32 m0, s62, 16448\ns_add_u32 s69, s87, 262144\nQmvaa 12,136,72\nbuffer_load_dwordx4 v62, s[52:55], s69 offen lds\nQmvaa 12,140,76\nQmvav 144,144,20\n"
"Qmvav 148,148,20\nQmvav 152,152,20\nQmvav 156,156,20\nQe 8\nQmvav 160,160,20\ns_add_u32 s69, s87, 293888\nQe 9\nQmvav 164,164,20\n"
"buffer_load_dwordx4 v62, s[52:55], s69 offen offset:1024 lds\nQmvav 168,168,20\nQe 10\nQmvav 172,172,20\nQe 11\nQmaav 184,204,20\nQk 20,8,9\nQk 21,10,11\n"
"Qcvaa 0,112,80,44\nQe 12\nQmvaa 0,116,84\nQe 13\nQmvaa 0,120,88\ns_add_u32 s69, s87, 325624\nQe 14\nQmvaa 0,124,92\n"
"buffer_load_dwordx4 v62, s[52:55], s69 offen offset:2056 lds\nQcvaa 4,128,80,44\nQe 15\nQmvaa 4,132,84\nQk 22,12,13\nQk 23,14,15\nQmvaa 4,136,88\nQmvaa 4,140,92\n"
"Qmvav 176,144,16\nQmvav 180,148,16\ns_add_u32 s69, s87, 357368\nQmvav 184,152,16\nbuffer_load_dwordx4 v62, s[52:55], s69 offen offset:3080 lds\nQmvav 188,156,16\nQe 0\nQmvav 192,160,16\n"
"Qe 1\nQmvav 196,164,16\nQe 2\nQmvav 200,168,16\nQe 3\nQmvav 204,172,16\nQmaav 188,204,16\nQk 16,0,1\n"
"Qk 17,2,3\nQcvaa 8,112,96,48\nQda 112,53,0\nQmvaa 8,116,100\nQda 116,53,512\nQmvaa 8,120,104\nQda 120,53,1024\nQmvaa 8,124,108\n"
"Qda 124,53,1536\nQcvaa 12,128,96,48\nQda 128,53,2048\nQmvaa 12,132,100\nQda 132,53,2560\nQmvaa 12,136,104\nQda 136,53,3072\nQmvaa 12,140,108\n"
"Qda 140,53,3584\nQmvav 208,144,20\nQe 4\nQmvav 212,148,20\ns_add_u32 m0, s63, 33024\ns_add_u32 s88, s86, 0\nQe 5\nQmvav 216,152,20\n"
"buffer_load_dwordx4 v64, s[48:51], s88 offen lds\nQe 6\nQmvav 220,156,20\nQe 7\nQmvav 224,160,20\nQk 18,4,5\nQk 19,6,7\nQmvav 228,164,20\n"
"s_add_u32 s88, s86, 19456\nQe 8\nQmvav 232,168,20\nbuffer_load_dwordx4 v64, s[48:51], s88 offen offset:1024 lds\nQe 9\nQmvav 236,172,20\nQe 10\nQmaav 192,204,20\n"
"s_waitcnt lgkmcnt(0)\nQk 20,8,9\nQcvaa 0,112,0,24\ns_add_u32 s88, s86, 38912\nQe 11\nQmvaa 0,116,4\nbuffer_load_dwordx4 v64, s[48:51], s88 offen offset:2048 lds\nQk 21,10,11\n"
"Qmvaa 0,120,8\nQe 12\nQmvaa 0,124,12\nQe 13\nQcvaa 4,128,0,24\ns_add_u32 s88, s86, 58368\nQe 14\nQmvaa 4,132,4\n"
"buffer_load_dwordx4 v64, s[48:51], s88 offen offset:3072 lds\nQe 15\nQmvaa 4,136,8\nQk 22,12,13\nQk 23,14,15\nQmvaa 4,140,12\nQmvav 240,144,16\ns_add_u32 m0, s63, 37120\n"
"s_add_u32 s88, s86, 81920\nQmvav 244,148,16\nbuffer_load_dwordx4 v64, s[48:51], s88 offen lds\nQmvav 248,152,16\nQmvav 252,156,16\nQe 0\nQmaav 208,160,16\ns_add_u32 s88, s86, 101376\n"
"Qe 1\nQmaav 212,164,16\nbuffer_load_dwordx4 v64, s[48:51], s88 offen offset:1024 lds\nQe 2\nQmaav 216,168,16\nQe 3\nQmaav 220,172,16\nQe 4\n"
"Qmaav 196,204,16\ns_add_u32 s88, s86, 120832\nQk 16,0,1\nQk 17,2,3\nQcvaa 8,112,16,28\nbuffer_load_dwordx4 v64, s[48:51], s88 offen offset:2048 lds\nQe 5\nQmvaa 8,116,20\n"
"Qe 6\nQmvaa 8,120,24\nQe 7\nQmvaa 8,124,28\nQk 18,4,5\nQk 19,6,7\nQcvaa 12,128,16,28\nQmvaa 12,132,20\n"
"Qmvaa 12,136,24\nQmvaa 12,140,28\nQmaav 224,144,20\nQta 144,55,0\nQta 146,55,2048\nQmaav 228,148,20\nQta 148,55,256\nQta 150,55,2304\n"
"Qmaav 232,152,20\nQta 152,55,512\nQta 154,55,2560\nQmaav 236,156,20\nQta 156,55,768\nQta 158,55,2816\nQmaav 240,160,20\nQta 160,55,1024\n"
"Qta 162,55,3072\nQmaav 244,164,20\nQta 164,55,1280\nQta 166,55,3328\nQmaav 248,168,20\nQta 168,55,1536\nQta 170,55,3584\nQmaav 252,172,20\n"
"Qta 172,55,1792\nQta 174,55,3840\nQmaav 200,204,20\nQe 8\nQcvaa 0,112,32,32\nQe 9\nQmvaa 0,116,36\nQe 10\n"
"Qmvaa 0,120,40\nQe 11\nQmvaa 0,124,44\nQk 20,8,9\nQk 21,10,11\nQcvaa 4,128,32,32\nQe 12\nQmvaa 4,132,36\n"
"Qe 13\nQmvaa 4,136,40\nQe 14\nQmvaa 4,140,44\ns_waitcnt lgkmcnt(0)\nQk 22,12,13\nQmvav 80,144,16\nQe 15\n"
"Qmvav 84,148,16\ns_waitcnt vmcnt(23)\ns_barrier\nQmvav 88,152,16\nQk 23,14,15\nQmvav 92,156,16\ns_add_u32 m0, s62, 33024\ns_add_u32 s69, s87, 524288\n"
"Qmvav 96,160,16\nbuffer_load_dwordx4 v62, s[52:55], s69 offen lds\nQmvav 100,164,16\nQe 0\nQmvav 104,168,16\nQe 1\nQmvav 108,172,16\nQe 2\n"
"Qmaav 176,204,16\nQe 3\nQcvaa 8,112,48,36\nQk 16,0,1\nQk 17,2,3\nQmvaa 8,116,52\ns_add_u32 s69, s87, 556032\nQe 4\n"
"Qmvaa 8,120,56\nbuffer_load_dwordx4 v62, s[52:55], s69 offen offset:1024 lds\nQmvaa 8,124,60\nQe 5\nQcvaa 12,128,48,36\nQe 6\nQmvaa 12,132,52\nQe 7\n"
"Qmvaa 12,136,56\nQk 18,4,5\nQk 19,6,7\nQmvaa 12,140,60\nQmvav 112,144,20\ns_add_u32 s69, s87, 587768\nQmvav 116,148,20\nbuffer_load_dwordx4 v62, s[52:55], s69 offen offset:2056 lds\n"
"Qmvav 120,152,20\nQmvav 124,156,20\nQe 8\nQmvav 128,160,20\nQe 9\nQmvav 132,164,20\nQe 10\nQmvav 136,168,20\n"
"Qe 11\nQmvav 140,172,20\ns_add_u32 s69, s87, 619512\nQe 12\nQmaav 180,204,20\nbuffer_load_dwordx4 v62, s[52:55], s69 offen offset:3080 lds\nQcvaa 0,112,64,40\nQk 20,8,9\n"
"Qk 21,10,11\nQmvaa 0,116,68\nQe 13\nQmvaa 0,120,72\nQe 14\nQmvaa 0,124,76\nQe 15\nQcvaa 4,128,64,40\n"
"Qk 22,12,13\nQk 23,14,15\nQmvaa 4,132,68\ns_add_u32 m0, s62, 49472\ns_add_u32 s69, s87, 786432\nQmvaa 4,136,72\nbuffer_load_dwordx4 v62, s[52:55], s69 offen lds\nQmvaa 4,140,76\n"
"Qmvav 144,144,16\nQmvav 148,148,16\nQmvav 152,152,16\nQmvav 156,156,16\nQe 0\nQmvav 160,160,16\ns_add_u32 s69, s87, 818176\nQe 1\n"
"Qmvav 164,164,16\nbuffer_load_dwordx4 v62, s[52:55], s69 offen offset:1024 lds\nQmvav 168,168,16\nQe 2\nQmvav 172,172,16\nQe 3\nQmaav 184,204,16\nQk 16,0,1\n"
"Qk 17,2,3\nQcvaa 8,112,80,44\nQe 4\nQmvaa 8,116,84\nQe 5\nQmvaa 8,120,88\ns_add_u32 s69, s87, 849912\nQe 6\n"
"Qmvaa 8,124,92\nbuffer_load_dwordx4 v62, s[52:55], s69 offen offset:2056 lds\nQcvaa 12,128,80,44\nQe 7\nQmvaa 12,132,84\nQk 18,4,5\nQk 19,6,7\nQmvaa 12,136,88\n"
"Qmvaa 12,140,92\nQmvav 176,144,20\nQmvav 180,148,20\ns_add_u32 s69, s87, 881656\nQmvav 184,152,20\nbuffer_load_dwordx4 v62, s[52:55], s69 offen offset:3080 lds\nQmvav 188,156,20\nQe 8\n"
"Qmvav 192,160,20\nQe 9\nQmvav 196,164,20\nQe 10\nQmvav 200,168,20\nQe 11\nQmvav 204,172,20\nQmaav 188,204,20\n"
"Qk 20,8,9\nQk 21,10,11\nQcvaa 0,112,96,48\nQda 112,53,33024\nQmvaa 0,116,100\nQda 116,53,33536\nQmvaa 0,120,104\nQda 120,53,34048\n"
"Qmvaa 0,124,108\nQda 124,53,34560\nQcvaa 4,128,96,48\nQda 128,53,35072\nQmvaa 4,132,100\nQda 132,53,35584\nQmvaa 4,136,104\nQda 136,53,36096\n"
"Qmvaa 4,140,108\nQda 140,53,36608\nQmvav 208,144,16\nQe 12\nQmvav 212,148,16\nQe 13\nQmvav 216,152,16\nQe 14\n"
"Qmvav 220,156,16\nQe 15\nQmvav 224,160,16\nQk 22,12,13\nQk 23,14,15\nQmvav 228,164,16\nQe 0\nQmvav 232,168,16\n"
"Qe 1\nQmvav 236,172,16\nQe 2\nQmaav 192,204,16\ns_waitcnt lgkmcnt(0)\nQk 16,0,1\nQcvaa 8,112,0,24\nQe 3\n"
"Qmvaa 8,116,4\nQk 17,2,3\nQmvaa 8,120,8\nQe 4\nQmvaa 8,124,12\nQe 5\nQcvaa 12,128,0,24\nQe 6\n"
"Qmvaa 12,132,4\nQe 7\nQmvaa 12,136,8\nQk 18,4,5\nQk 19,6,7\nQmvaa 12,140,12\nQmvav 240,144,20\nQmvav 244,148,20\n"
"Qmvav 248,152,20\nQmvav 252,156,20\nQe 8\nQmaav 208,160,20\nQe 9\nQmaav 212,164,20\nQe 10\nQmaav 216,168,20\n"
"Qe 11\nQmaav 220,172,20\nQe 12\nQmaav 196,204,20\nQk 20,8,9\nQk 21,10,11\nQcvaa 0,112,16,28\nQe 13\n"
"Qmvaa 0,116,20\nQe 14\nQmvaa 0,120,24\nQe 15\nQmvaa 0,124,28\nQk 22,12,13\nQk 23,14,15\nQcvaa 4,128,16,28\n"
"Qmvaa 4,132,20\nQmvaa 4,136,24\nQmvaa 4,140,28\nQmaav 224,144,16\nQta 144,55,33024\nQta 146,55,35072\nQmaav 228,148,16\nQta 148,55,33280\n"
"Qta 150,55,35328\nQmaav 232,152,16\nQta 152,55,33536\nQta 154,55,35584\nQmaav 236,156,16\nQta 156,55,33792\nQta 158,55,35840\nQmaav 240,160,16\n"
"Qta 160,55,34048\nQta 162,55,36096\nQmaav 244,164,16\nQta 164,55,34304\nQta 166,55,36352\nQmaav 248,168,16\nQta 168,55,34560\nQta 170,55,36608\n"
"Qmaav 252,172,16\nQta 172,55,34816\nQta 174,55,36864\nQmaav 200,204,16\nQe 0\nQcvaa 8,112,32,32\nQe 1\nQmvaa 8,116,36\n"
"Qe 2\nQmvaa 8,120,40\nQe 3\nQmvaa 8,124,44\nQk 16,0,1\nQk 17,2,3\nQcvaa 12,128,32,32\nQe 4\n"
"Qmvaa 12,132,36\nQe 5\nQmvaa 12,136,40\nQe 6\nQmvaa 12,140,44\ns_waitcnt lgkmcnt(0)\nQk 18,4,5\nQmvav 80,144,20\n"
"Qe 7\nQmvav 84,148,20\ns_barrier\nQmvav 88,152,20\nQk 19,6,7\nQmvav 92,156,20\ns_add_u32 m0, s63, 0\ns_add_u32 s88, s86, 128\n"
"Qmvav 96,160,20\nbuffer_load_dwordx4 v64, s[48:51], s88 offen lds\nQmvav 100,164,20\nQe 8\nQmvav 104,168,20\nQe 9\nQmvav 108,172,20\ns_add_u32 s88, s86, 19584\n"
"Qe 10\nQmaav 176,204,20\nbuffer_load_dwordx4 v64, s[48:51], s88 offen offset:1024 lds\nQe 11\nQcvaa 0,112,48,36\nQk 20,8,9\nQk 21,10,11\nQmvaa 0,116,52\n"
"Qe 12\nQmvaa 0,120,56\ns_add_u32 s88, s86, 39040\nQmvaa 0,124,60\nbuffer_load_dwordx4 v64, s[48:51], s88 offen offset:2048 lds\nQe 13\nQcvaa 4,128,48,36\nQe 14\n"
"Qmvaa 4,132,52\nQe 15\nQmvaa 4,136,56\ns_add_u32 s88, s86, 58496\nQk 22,12,13\nQk 23,14,15\nQmvaa 4,140,60\nbuffer_load_dwordx4 v64, s[48:51], s88 offen offset:3072 lds\n"
"Qmvav 112,144,16\nQmvav 116,148,16\nQmvav 120,152,16\ns_add_u32 m0, s63, 4096\ns_add_u32 s88, s86, 82048\nQmvav 124,156,16\nbuffer_load_dwordx4 v64, s[48:51], s88 offen lds\nQe 0\n"
"Qmvav 128,160,16\nQe 1\nQmvav 132,164,16\nQe 2\nQmvav 136,168,16\ns_add_u32 s88, s86, 101504\nQe 3\nQmvav 140,172,16\n"
"buffer_load_dwordx4 v64, s[48:51], s88 offen offset:1024 lds\nQe 4\nQmaav 180,204,16\nQcvaa 8,112,64,40\nQk 16,0,1\nQk 17,2,3\nQmvaa 8,116,68\ns_add_u32 s88, s86, 120960\n"
"Qe 5\nQmvaa 8,120,72\nbuffer_load_dwordx4 v64, s[48:51], s88 offen offset:2048 lds\nQe 6\nQmvaa 8,124,76\nQe 7\nQcvaa 12,128,64,40\nQk 18,4,5\n"
"Qk 19,6,7\nQmvaa 12,132,68\nQmvaa 12,136,72\nQmvaa 12,140,76\nQmvav 144,144,20\nQmvav 148,148,20\nQmvav 152,152,20\nQmvav 156,156,20\n"
"Qe 8\nQmvav 160,160,20\nQe 9\nQmvav 164,164,20\nQmvav 168,168,20\nQe 10\nQmvav 172,172,20\nQe 11\n"
"Qmaav 184,204,20\nQk 20,8,9\nQk 21,10,11\nQcvaa 0,112,80,44\nQe 12\nQmvaa 0,116,84\nQe 13\nQmvaa 0,120,88\n"
"Qe 14\nQmvaa 0,124,92\nQcvaa 4,128,80,44\nQe 15\nQmvaa 4,132,84\nQk 22,12,13\nQk 23,14,15\nQmvaa 4,136,88\n"
"Qmvaa 4,140,92\nQmvav 176,144,16\nQmvav 180,148,16\nQmvav 184,152,16\nQmvav 188,156,16\nQe 0\nQmvav 192,160,16\nQe 1\n"
"Qmvav 196,164,16\nQe 2\nQmvav 200,168,16\nQe 3\nQmvav 204,172,16\nQmaav 188,204,16\nQk 16,0,1\nQk 17,2,3\n"
"Qcvaa 8,112,96,48\nQmvaa 8,116,100\nQmvaa 8,120,104\nQmvaa 8,124,108\nQcvaa 12,128,96,48\nQmvaa 12,132,100\nQmvaa 12,136,104\nQmvaa 12,140,108\n"
"Qmvav 208,144,20\nQe 4\nQmvav 212,148,20\nQe 5\nQmvav 216,152,20\nQe 6\nQmvav 220,156,20\nQe 7\n"
"Qmvav 224,160,20\nQk 18,4,5\nQk 19,6,7\nQmvav 228,164,20\nQe 8\nQmvav 232,168,20\nQe 9\nQmvav 236,172,20\n"
"Qe 10\nQmaav 192,204,20\nQk 20,8,9\nQe 11\ns_nop 0\nQk 21,10,11\nQe 12\nQe 13\n"
"Qe 14\nQe 15\nQk 22,12,13\nQk 23,14,15\nQmvav 240,144,16\nQmvav 244,148,16\nQmvav 248,152,16\nQmvav 252,156,16\n"
"Qmaav 208,160,16\nQmaav 212,164,16\nQmaav 216,168,16\nQmaav 220,172,16\nQmaav 196,204,16\nQmaav 224,144,20\nQmaav 228,148,20\nQmaav 232,152,20\n"
"Qmaav 236,156,20\nQmaav 240,160,20\nQmaav 244,164,20\nQmaav 248,168,20\nQmaav 252,172,20\nQmaav 200,204,20\ns_nop 7\nv_accvgpr_read_b32 v24, a176\n"
"v_accvgpr_read_b32 v28, a180\nv_accvgpr_read_b32 v32, a184\nv_accvgpr_read_b32 v36, a188\nv_accvgpr_read_b32 v40, a192\nv_accvgpr_read_b32 v44, a196\nv_accvgpr_read_b32 v48, a200\nv_cmp_nge_f32 vcc, 0x44800000, v24\ns_mov_b64 s[74:75], vcc\n"
"v_cmp_nge_f32 vcc, 0x44800000, v28\ns_or_b64 s[74:75], s[74:75], vcc\nv_cmp_nge_f32 vcc, 0x44800000, v32\ns_or_b64 s[74:75], s[74:75], vcc\nv_cmp_nge_f32 vcc, 0x44800000, v36\ns_or_b64 s[74:75], s[74:75], vcc\nv_cmp_nge_f32 vcc, 0x44800000, v40\ns_or_b64 s[74:75], s[74:75], vcc\n"
"v_cmp_nge_f32 vcc, 0x44800000, v44\ns_or_b64 s[74:75], s[74:75], vcc\nv_cmp_nge_f32 vcc, 0x44800000, v48\ns_or_b64 s[74:75], s[74:75], vcc\ns_cmp_lg_u64 s[74:75], 0\ns_cbranch_scc1 .Lfb\n.Lfbret:\nv_rcp_f32 v25, v24\n"
"v_rcp_f32 v24, v24\nv_rcp_f32 v29, v28\nv_rcp_f32 v28, v28\nv_rcp_f32 v33, v32\nv_rcp_f32 v32, v32\nv_rcp_f32 v37, v36\nv_rcp_f32 v36, v36\nv_rcp_f32 v41, v40\n"
"v_rcp_f32 v40, v40\nv_rcp_f32 v45, v44\nv_rcp_f32 v44, v44\nv_rcp_f32 v49, v48\nv_rcp_f32 v48, v48\nv_pk_mul_f32 v[80:81], v[80:81], v[24:25]\nv_pk_mul_f32 v[82:83], v[82:83], v[24:25]\nv_pk_mul_f32 v[84:85], v[84:85], v[24:25]\n"
"v_pk_mul_f32 v[86:87], v[86:87], v[24:25]\nQk 80,80,81\nQk 81,82,83\nQk 82,84,85\nQk 83,86,87\nv_accvgpr_write_b32 a0, v80\nv_accvgpr_write_b32 a1, v81\nv_accvgpr_write_b32 a2, v82\n"
"v_accvgpr_write_b32 a3, v83\nv_pk_mul_f32 v[88:89], v[88:89], v[24:25]\nv_pk_mul_f32 v[90:91], v[90:91], v[24:25]\nv_pk_mul_f32 v[92:93], v[92:93], v[24:25]\nv_pk_mul_f32 v[94:95], v[94:95], v[24:25]\nQk 88,88,89\nQk 89,90,91\nQk 90,92,93\n"
"Qk 91,94,95\nv_accvgpr_write_b32 a4, v88\nv_accvgpr_write_b32 a5, v89\nv_accvgpr_write_b32 a6, v90\nv_accvgpr_write_b32 a7, v91\nv_pk_mul_f32 v[96:97], v[96:97], v[24:25]\nv_pk_mul_f32 v[98:99], v[98:99], v[24:25]\nv_pk_mul_f32 v[100:101], v[100:101], v[24:25]\n"
"v_pk_mul_f32 v[102:103], v[102:103], v[24:25]\nQk 96,96,97\nQk 97,98,99\nQk 98,100,101\nQk 99,102,103\nv_accvgpr_write_b32 a8, v96\nv_accvgpr_write_b32 a9, v97\nv_accvgpr_write_b32 a10, v98\n"
"v_accvgpr_write_b32 a11, v99\nv_pk_mul_f32 v[104:105], v[104:105], v[24:25]\nv_pk_mul_f32 v[106:107], v[106:107], v[24:25]\nv_pk_mul_f32 v[108:109], v[108:109], v[24:25]\nv_pk_mul_f32 v[110:111], v[110:111], v[24:25]\nQk 104,104,105\nQk 105,106,107\nQk 106,108,109\n"
"Qk 107,110,111\nv_accvgpr_write_b32 a12, v104\nv_accvgpr_write_b32 a13, v105\nv_accvgpr_write_b32 a14, v106\nv_accvgpr_write_b32 a15, v107\nv_pk_mul_f32 v[112:113], v[112:113], v[28:29]\nv_pk_mul_f32 v[114:115], v[114:115], v[28:29]\nv_pk_mul_f32 v[116:117], v[116:117], v[28:29]\n"
"v_pk_mul_f32 v[118:119], v[118:119], v[28:29]\nQk 112,112,113\nQk 113,114,115\nQk 114,116,117\nQk 115,118,119\nv_accvgpr_write_b32 a16, v112\nv_accvgpr_write_b32 a17, v113\nv_accvgpr_write_b32 a18, v114\n"
"v_accvgpr_write_b32 a19, v115\nv_pk_mul_f32 v[120:121], v[120:121], v[28:29]\nv_pk_mul_f32 v[122:123], v[122:123], v[28:29]\nv_pk_mul_f32 v[124:125], v[124:125], v[28:29]\nv_pk_mul_f32 v[126:127], v[126:127], v[28:29]\nQk 120,120,121\nQk 121,122,123\nQk 122,124,125\n"
"Qk 123,126,127\nv_accvgpr_write_b32 a20, v120\nv_accvgpr_write_b32 a21, v121\nv_accvgpr_write_b32 a22, v122\nv_accvgpr_write_b32 a23, v123\nv_pk_mul_f32 v[128:129], v[128:129], v[28:29]\nv_pk_mul_f32 v[130:131], v[130:131], v[28:29]\nv_pk_mul_f32 v[132:133], v[132:133], v[28:29]\n"
"v_pk_mul_f32 v[134:135], v[134:135], v[28:29]\nQk 128,128,129\nQk 129,130,131\nQk 130,132,133\nQk 131,134,135\nv_accvgpr_write_b32 a24, v128\nv_accvgpr_write_b32 a25, v129\nv_accvgpr_write_b32 a26, v130\n"
"v_accvgpr_write_b32 a27, v131\nv_pk_mul_f32 v[136:137], v[136:137], v[28:29]\nv_pk_mul_f32 v[138:139], v[138:139], v[28:29]\nv_pk_mul_f32 v[140:141], v[140:141], v[28:29]\nv_pk_mul_f32 v[142:143], v[142:143], v[28:29]\nQk 136,136,137\nQk 137,138,139\nQk 138,140,141\n"
"Qk 139,142,143\nv_accvgpr_write_b32 a28, v136\nv_accvgpr_write_b32 a29, v137\nv_accvgpr_write_b32 a30, v138\nv_accvgpr_write_b32 a31, v139\nv_pk_mul_f32 v[144:145], v[144:145], v[32:33]\nv_pk_mul_f32 v[146:147], v[146:147], v[32:33]\nv_pk_mul_f32 v[148:149], v[148:149], v[32:33]\n"
"v_pk_mul_f32 v[150:151], v[150:151], v[32:33]\nQk 144,144,145\nQk 145,146,147\nQk 146,148,149\nQk 147,150,151\nv_accvgpr_write_b32 a32, v144\nv_accvgpr_write_b32 a33, v145\nv_accvgpr_write_b32 a34, v146\n"
"v_accvgpr_write_b32 a35, v147\nv_pk_mul_f32 v[152:153], v[152:153], v[32:33]\nv_pk_mul_f32 v[154:155], v[154:155], v[32:33]\nv_pk_mul_f32 v[156:157], v[156:157], v[32:33]\nv_pk_mul_f32 v[158:159], v[158:159], v[32:33]\nQk 152,152,153\nQk 153,154,155\nQk 154,156,157\n"
"Qk 155,158,159\nv_accvgpr_write_b32 a36, v152\nv_accvgpr_write_b32 a37, v153\nv_accvgpr_write_b32 a38, v154\nv_accvgpr_write_b32 a39, v155\nv_pk_mul_f32 v[160:161], v[160:161], v[32:33]\nv_pk_mul_f32 v[162:163], v[162:163], v[32:33]\nv_pk_mul_f32 v[164:165], v[164:165], v[32:33]\n"
"v_pk_mul_f32 v[166:167], v[166:167], v[32:33]\nQk 160,160,161\nQk 161,162,163\nQk 162,164,165\nQk 163,166,167\nv_accvgpr_write_b32 a40, v160\nv_accvgpr_write_b32 a41, v161\nv_accvgpr_write_b32 a42, v162\n"
"v_accvgpr_write_b32 a43, v163\nv_pk_mul_f32 v[168:169], v[168:169], v[32:33]\nv_pk_mul_f32 v[170:171], v[170:171], v[32:33]\nv_pk_mul_f32 v[172:173], v[172:173], v[32:33]\nv_pk_mul_f32 v[174:175], v[174:175], v[32:33]\nQk 168,168,169\nQk 169,170,171\nQk 170,172,173\n"
"Qk 171,174,175\nv_accvgpr_write_b32 a44, v168\nv_accvgpr_write_b32 a45, v169\nv_accvgpr_write_b32 a46, v170\nv_accvgpr_write_b32 a47, v171\nv_pk_mul_f32 v[176:177], v[176:177], v[36:37]\nv_pk_mul_f32 v[178:179], v[178:179], v[36:37]\nv_pk_mul_f32 v[180:181], v[180:181], v[36:37]\n"
"v_pk_mul_f32 v[182:183], v[182:183], v[36:37]\nQk 176,176,177\nQk 177,178,179\nQk 178,180,181\nQk 179,182,183\nv_accvgpr_write_b32 a48, v176\nv_accvgpr_write_b32 a49, v177\nv_accvgpr_write_b32 a50, v178\n"
"v_accvgpr_write_b32 a51, v179\nv_pk_mul_f32 v[184:185], v[184:185], v[36:37]\nv_pk_mul_f32 v[186:187], v[186:187], v[36:37]\nv_pk_mul_f32 v[188:189], v[188:189], v[36:37]\nv_pk_mul_f32 v[190:191], v[190:191], v[36:37]\nQk 184,184,185\nQk 185,186,187\nQk 186,188,189\n"
"Qk 187,190,191\nv_accvgpr_write_b32 a52, v184\nv_accvgpr_write_b32 a53, v185\nv_accvgpr_write_b32 a54, v186\nv_accvgpr_write_b32 a55, v187\nv_pk_mul_f32 v[192:193], v[192:193], v[36:37]\nv_pk_mul_f32 v[194:195], v[194:195], v[36:37]\nv_pk_mul_f32 v[196:197], v[196:197], v[36:37]\n"
"v_pk_mul_f32 v[198:199], v[198:199], v[36:37]\nQk 192,192,193\nQk 193,194,195\nQk 194,196,197\nQk 195,198,199\nv_accvgpr_write_b32 a56, v192\nv_accvgpr_write_b32 a57, v193\nv_accvgpr_write_b32 a58, v194\n"
"v_accvgpr_write_b32 a59, v195\nv_pk_mul_f32 v[200:201], v[200:201], v[36:37]\nv_pk_mul_f32 v[202:203], v[202:203], v[36:37]\nv_pk_mul_f32 v[204:205], v[204:205], v[36:37]\nv_pk_mul_f32 v[206:207], v[206:207], v[36:37]\nQk 200,200,201\nQk 201,202,203\nQk 202,204,205\n"
"Qk 203,206,207\nv_accvgpr_write_b32 a60, v200\nv_accvgpr_write_b32 a61, v201\nv_accvgpr_write_b32 a62, v202\nv_accvgpr_write_b32 a63, v203\nv_pk_mul_f32 v[208:209], v[208:209], v[40:41]\nv_pk_mul_f32 v[210:211], v[210:211], v[40:41]\nv_pk_mul_f32 v[212:213], v[212:213], v[40:41]\n"
"v_pk_mul_f32 v[214:215], v[214:215], v[40:41]\nQk 208,208,209\nQk 209,210,211\nQk 210,212,213\nQk 211,214,215\nv_accvgpr_write_b32 a64, v208\nv_accvgpr_write_b32 a65, v209\nv_accvgpr_write_b32 a66, v210\n"
"v_accvgpr_write_b32 a67, v211\nv_pk_mul_f32 v[216:217], v[216:217], v[40:41]\nv_pk_mul_f32 v[218:219], v[218:219], v[40:41]\nv_pk_mul_f32 v[220:221], v[220:221], v[40:41]\nv_pk_mul_f32 v[222:223], v[222:223], v[40:41]\nQk 216,216,217\nQk 217,218,219\nQk 218,220,221\n"
"Qk 219,222,223\nv_accvgpr_write_b32 a68, v216\nv_accvgpr_write_b32 a69, v217\nv_accvgpr_write_b32 a70, v218\nv_accvgpr_write_b32 a71, v219\nv_pk_mul_f32 v[224:225], v[224:225], v[40:41]\nv_pk_mul_f32 v[226:227], v[226:227], v[40:41]\nv_pk_mul_f32 v[228:229], v[228:229], v[40:41]\n"
"v_pk_mul_f32 v[230:231], v[230:231], v[40:41]\nQk 224,224,225\nQk 225,226,227\nQk 226,228,229\nQk 227,230,231\nv_accvgpr_write_b32 a72, v224\nv_accvgpr_write_b32 a73, v225\nv_accvgpr_write_b32 a74, v226\n"
"v_accvgpr_write_b32 a75, v227\nv_pk_mul_f32 v[232:233], v[232:233], v[40:41]\nv_pk_mul_f32 v[234:235], v[234:235], v[40:41]\nv_pk_mul_f32 v[236:237], v[236:237], v[40:41]\nv_pk_mul_f32 v[238:239], v[238:239], v[40:41]\nQk 232,232,233\nQk 233,234,235\nQk 234,236,237\n"
"Qk 235,238,239\nv_accvgpr_write_b32 a76, v232\nv_accvgpr_write_b32 a77, v233\nv_accvgpr_write_b32 a78, v234\nv_accvgpr_write_b32 a79, v235\nv_pk_mul_f32 v[240:241], v[240:241], v[44:45]\nv_pk_mul_f32 v[242:243], v[242:243], v[44:45]\nv_pk_mul_f32 v[244:245], v[244:245], v[44:45]\n"
"v_pk_mul_f32 v[246:247], v[246:247], v[44:45]\nQk 240,240,241\nQk 241,242,243\nQk 242,244,245\nQk 243,246,247\nv_accvgpr_write_b32 a80, v240\nv_accvgpr_write_b32 a81, v241\nv_accvgpr_write_b32 a82, v242\n"
"v_accvgpr_write_b32 a83, v243\nv_pk_mul_f32 v[248:249], v[248:249], v[44:45]\nv_pk_mul_f32 v[250:251], v[250:251], v[44:45]\nv_pk_mul_f32 v[252:253], v[252:253], v[44:45]\nv_pk_mul_f32 v[254:255], v[254:255], v[44:45]\nQk 248,248,249\nQk 249,250,251\nQk 250,252,253\n"
"Qk 251,254,255\nv_accvgpr_write_b32 a84, v248\nv_accvgpr_write_b32 a85, v249\nv_accvgpr_write_b32 a86, v250\nv_accvgpr_write_b32 a87, v251\nv_accvgpr_read_b32 v0, a208\nv_accvgpr_read_b32 v1, a209\nv_accvgpr_read_b32 v2, a210\n"
"v_accvgpr_read_b32 v3, a211\nv_accvgpr_read_b32 v4, a212\nv_accvgpr_read_b32 v5, a213\nv_accvgpr_read_b32 v6, a214\nv_accvgpr_read_b32 v7, a215\nv_pk_mul_f32 v[0:1], v[0:1], v[44:45]\nv_pk_mul_f32 v[2:3], v[2:3], v[44:45]\nv_pk_mul_f32 v[4:5], v[4:5], v[44:45]\n"
"v_pk_mul_f32 v[6:7], v[6:7], v[44:45]\nQk 0,0,1\nQk 1,2,3\nQk 2,4,5\nQk 3,6,7\nv_accvgpr_write_b32 a88, v0\nv_accvgpr_write_b32 a89, v1\nv_accvgpr_write_b32 a90, v2\n"
"v_accvgpr_write_b32 a91, v3\nv_accvgpr_read_b32 v8, a216\nv_accvgpr_read_b32 v9, a217\nv_accvgpr_read_b32 v10, a218\nv_accvgpr_read_b32 v11, a219\nv_accvgpr_read_b32 v12, a220\nv_accvgpr_read_b32 v13, a221\nv_accvgpr_read_b32 v14, a222\n"
"v_accvgpr_read_b32 v15, a223\nv_pk_mul_f32 v[8:9], v[8:9], v[44:45]\nv_pk_mul_f32 v[10:11], v[10:11], v[44:45]\nv_pk_mul_f32 v[12:13], v[12:13], v[44:45]\nv_pk_mul_f32 v[14:15], v[14:15], v[44:45]\nQk 8,8,9\nQk 9,10,11\nQk 10,12,13\n"
"Qk 11,14,15\nv_accvgpr_write_b32 a92, v8\nv_accvgpr_write_b32 a93, v9\nv_accvgpr_write_b32 a94, v10\nv_accvgpr_write_b32 a95, v11\nv_accvgpr_read_b32 v0, a224\nv_accvgpr_read_b32 v1, a225\nv_accvgpr_read_b32 v2, a226\n"
"v_accvgpr_read_b32 v3, a227\nv_accvgpr_read_b32 v4, a228\nv_accvgpr_read_b32 v5, a229\nv_accvgpr_read_b32 v6, a230\nv_accvgpr_read_b32 v7, a231\nv_pk_mul_f32 v[0:1], v[0:1], v[48:49]\nv_pk_mul_f32 v[2:3], v[2:3], v[48:49]\nv_pk_mul_f32 v[4:5], v[4:5], v[48:49]\n"
"v_pk_mul_f32 v[6:7], v[6:7], v[48:49]\nQk 0,0,1\nQk 1,2,3\nQk 2,4,5\nQk 3,6,7\nv_accvgpr_write_b32 a96, v0\nv_accvgpr_write_b32 a97, v1\nv_accvgpr_write_b32 a98, v2\n"
"v_accvgpr_write_b32 a99, v3\nv_accvgpr_read_b32 v8, a232\nv_accvgpr_read_b32 v9, a233\nv_accvgpr_read_b32 v10, a234\nv_accvgpr_read_b32 v11, a235\nv_accvgpr_read_b32 v12, a236\nv_accvgpr_read_b32 v13, a237\nv_accvgpr_read_b32 v14, a238\n"
"v_accvgpr_read_b32 v15, a239\nv_pk_mul_f32 v[8:9], v[8:9], v[48:49]\nv_pk_mul_f32 v[10:11], v[10:11], v[48:49]\nv_pk_mul_f32 v[12:13], v[12:13], v[48:49]\nv_pk_mul_f32 v[14:15], v[14:15], v[48:49]\nQk 8,8,9\nQk 9,10,11\nQk 10,12,13\n"
"Qk 11,14,15\nv_accvgpr_write_b32 a100, v8\nv_accvgpr_write_b32 a101, v9\nv_accvgpr_write_b32 a102, v10\nv_accvgpr_write_b32 a103, v11\nv_accvgpr_read_b32 v0, a240\nv_accvgpr_read_b32 v1, a241\nv_accvgpr_read_b32 v2, a242\n"
"v_accvgpr_read_b32 v3, a243\nv_accvgpr_read_b32 v4, a244\nv_accvgpr_read_b32 v5, a245\nv_accvgpr_read_b32 v6, a246\nv_accvgpr_read_b32 v7, a247\nv_pk_mul_f32 v[0:1], v[0:1], v[48:49]\nv_pk_mul_f32 v[2:3], v[2:3], v[48:49]\nv_pk_mul_f32 v[4:5], v[4:5], v[48:49]\n"
"v_pk_mul_f32 v[6:7], v[6:7], v[48:49]\nQk 0,0,1\nQk 1,2,3\nQk 2,4,5\nQk 3,6,7\nv_accvgpr_write_b32 a104, v0\nv_accvgpr_write_b32 a105, v1\nv_accvgpr_write_b32 a106, v2\n"
"v_accvgpr_write_b32 a107, v3\nv_accvgpr_read_b32 v8, a248\nv_accvgpr_read_b32 v9, a249\nv_accvgpr_read_b32 v10, a250\nv_accvgpr_read_b32 v11, a251\nv_accvgpr_read_b32 v12, a252\nv_accvgpr_read_b32 v13, a253\nv_accvgpr_read_b32 v14, a254\n"
"v_accvgpr_read_b32 v15, a255\nv_pk_mul_f32 v[8:9], v[8:9], v[48:49]\nv_pk_mul_f32 v[10:11], v[10:11], v[48:49]\nv_pk_mul_f32 v[12:13], v[12:13], v[48:49]\nv_pk_mul_f32 v[14:15], v[14:15], v[48:49]\nQk 8,8,9\nQk 9,10,11\nQk 10,12,13\n"
"Qk 11,14,15\nv_accvgpr_write_b32 a108, v8\nv_accvgpr_write_b32 a109, v9\nv_accvgpr_write_b32 a110, v10\nv_accvgpr_write_b32 a111, v11\ns_mov_b32 s58, 1835008\ns_add_u32 s67, s67, 1\ns_cmp_lt_u32 s67, 2\n"
"s_cbranch_scc1 .Lround\ns_add_u32 s68, s71, 0\nbuffer_store_dwordx4 a[0:3], v60, s[56:59], s68 offen offset:0 sc1\ns_add_u32 s68, s71, 14336\nbuffer_store_dwordx4 a[4:7], v60, s[56:59], s68 offen offset:0 sc1\ns_add_u32 s68, s71, 28672\nbuffer_store_dwordx4 a[8:11], v60, s[56:59], s68 offen offset:0 sc1\ns_add_u32 s68, s71, 43008\n"
"buffer_store_dwordx4 a[12:15], v60, s[56:59], s68 offen offset:0 sc1\ns_add_u32 s68, s71, 0\nbuffer_store_dwordx4 a[16:19], v60, s[56:59], s68 offen offset:1024 sc1\ns_add_u32 s68, s71, 14336\nbuffer_store_dwordx4 a[20:23], v60, s[56:59], s68 offen offset:1024 sc1\ns_add_u32 s68, s71, 28672\nbuffer_store_dwordx4 a[24:27], v60, s[56:59], s68 offen offset:1024 sc1\ns_add_u32 s68, s71, 43008\n"
"buffer_store_dwordx4 a[28:31], v60, s[56:59], s68 offen offset:1024 sc1\ns_add_u32 s68, s71, 0\nbuffer_store_dwordx4 a[32:35], v60, s[56:59], s68 offen offset:2048 sc1\ns_add_u32 s68, s71, 14336\nbuffer_store_dwordx4 a[36:39], v60, s[56:59], s68 offen offset:2048 sc1\ns_add_u32 s68, s71, 28672\nbuffer_store_dwordx4 a[40:43], v60, s[56:59], s68 offen offset:2048 sc1\ns_add_u32 s68, s71, 43008\n"
"buffer_store_dwordx4 a[44:47], v60, s[56:59], s68 offen offset:2048 sc1\ns_add_u32 s68, s71, 0\nbuffer_store_dwordx4 a[48:51], v60, s[56:59], s68 offen offset:3072 sc1\ns_add_u32 s68, s71, 14336\nbuffer_store_dwordx4 a[52:55], v60, s[56:59], s68 offen offset:3072 sc1\ns_add_u32 s68, s71, 28672\nbuffer_store_dwordx4 a[56:59], v60, s[56:59], s68 offen offset:3072 sc1\ns_add_u32 s68, s71, 43008\n"
"buffer_store_dwordx4 a[60:63], v60, s[56:59], s68 offen offset:3072 sc1\ns_add_u32 s68, s71, 4096\nbuffer_store_dwordx4 a[64:67], v60, s[56:59], s68 offen offset:0 sc1\ns_add_u32 s68, s71, 18432\nbuffer_store_dwordx4 a[68:71], v60, s[56:59], s68 offen offset:0 sc1\ns_add_u32 s68, s71, 32768\nbuffer_store_dwordx4 a[72:75], v60, s[56:59], s68 offen offset:0 sc1\ns_add_u32 s68, s71, 47104\n"
"buffer_store_dwordx4 a[76:79], v60, s[56:59], s68 offen offset:0 sc1\ns_add_u32 s68, s71, 4096\nbuffer_store_dwordx4 a[80:83], v60, s[56:59], s68 offen offset:1024 sc1\ns_add_u32 s68, s71, 18432\nbuffer_store_dwordx4 a[84:87], v60, s[56:59], s68 offen offset:1024 sc1\ns_add_u32 s68, s71, 32768\nbuffer_store_dwordx4 a[88:91], v60, s[56:59], s68 offen offset:1024 sc1\ns_add_u32 s68, s71, 47104\n"
"buffer_store_dwordx4 a[92:95], v60, s[56:59], s68 offen offset:1024 sc1\ns_add_u32 s68, s71, 4096\nbuffer_store_dwordx4 a[96:99], v60, s[56:59], s68 offen offset:2048 sc1\ns_add_u32 s68, s71, 18432\nbuffer_store_dwordx4 a[100:103], v60, s[56:59], s68 offen offset:2048 sc1\ns_add_u32 s68, s71, 32768\nbuffer_store_dwordx4 a[104:107], v60, s[56:59], s68 offen offset:2048 sc1\ns_add_u32 s68, s71, 47104\n"
"buffer_store_dwordx4 a[108:111], v60, s[56:59], s68 offen offset:2048 sc1\ns_branch .Lend\n.Lfb:\ns_waitcnt vmcnt(0) lgkmcnt(0)\nv_lshrrev_b32 v69, 4, v60\nv_and_b32 v70, 15, v69\nv_lshrrev_b32 v69, 4, v69\nv_lshlrev_b32 v66, 8, v70\n"
"v_lshl_add_u32 v66, v69, 4, v66\nv_add_u32 v66, 1024, v66\nv_lshlrev_b32 v67, 10, v69\nv_lshl_add_u32 v67, v70, 1, v67\nv_add_u32 v67, 1024, v67\nv_mov_b32 v24, 0xff800000\nv_mov_b32 v28, 0xff800000\nv_mov_b32 v32, 0xff800000\n"
"v_mov_b32 v36, 0xff800000\nv_mov_b32 v40, 0xff800000\nv_mov_b32 v44, 0xff800000\nv_mov_b32 v48, 0xff800000\ns_lshl_b32 s89, s67, 19\ns_mov_b32 s66, 0\n.Lfb1:\ns_add_u32 s68, s89, 4096\n"
"buffer_load_dwordx4 a[112:115], v66, s[40:43], s89 offen offset:0\nbuffer_load_dwordx4 a[116:119], v66, s[40:43], s89 offen offset:64\nbuffer_load_dwordx4 a[120:123], v66, s[40:43], s89 offen offset:128\nbuffer_load_dwordx4 a[124:127], v66, s[40:43], s89 offen offset:192\nbuffer_load_dwordx4 a[128:131], v66, s[40:43], s68 offen offset:0\nbuffer_load_dwordx4 a[132:135], v66, s[40:43], s68 offen offset:64\nbuffer_load_dwordx4 a[136:139], v66, s[40:43], s68 offen offset:128\nbuffer_load_dwordx4 a[140:143], v66, s[40:43], s68 offen offset:192\n"
"s_waitcnt vmcnt(0)\nQzvaa 0,112,0\nQmvaa 0,116,4\nQmvaa 0,120,8\nQmvaa 0,124,12\nQzvaa 4,128,0\nQmvaa 4,132,4\nQmvaa 4,136,8\n"
"Qmvaa 4,140,12\ns_nop 7\nv_max3_f32 v24, v24, v0, v1\nv_max3_f32 v24, v24, v2, v3\nv_max3_f32 v24, v24, v4, v5\nv_max3_f32 v24, v24, v6, v7\nQzvaa 0,112,16\nQmvaa 0,116,20\n"
"Qmvaa 0,120,24\nQmvaa 0,124,28\nQzvaa 4,128,16\nQmvaa 4,132,20\nQmvaa 4,136,24\nQmvaa 4,140,28\ns_nop 7\nv_max3_f32 v28, v28, v0, v1\n"
"v_max3_f32 v28, v28, v2, v3\nv_max3_f32 v28, v28, v4, v5\nv_max3_f32 v28, v28, v6, v7\nQzvaa 0,112,32\nQmvaa 0,116,36\nQmvaa 0,120,40\nQmvaa 0,124,44\nQzvaa 4,128,32\n"
"Qmvaa 4,132,36\nQmvaa 4,136,40\nQmvaa 4,140,44\ns_nop 7\nv_max3_f32 v32, v32, v0, v1\nv_max3_f32 v32, v32, v2, v3\nv_max3_f32 v32, v32, v4, v5\nv_max3_f32 v32, v32, v6, v7\n"
"Qzvaa 0,112,48\nQmvaa 0,116,52\nQmvaa 0,120,56\nQmvaa 0,124,60\nQzvaa 4,128,48\nQmvaa 4,132,52\nQmvaa 4,136,56\nQmvaa 4,140,60\n"
"s_nop 7\nv_max3_f32 v36, v36, v0, v1\nv_max3_f32 v36, v36, v2, v3\nv_max3_f32 v36, v36, v4, v5\nv_max3_f32 v36, v36, v6, v7\nQzvaa 0,112,64\nQmvaa 0,116,68\nQmvaa 0,120,72\n"
"Qmvaa 0,124,76\nQzvaa 4,128,64\nQmvaa 4,132,68\nQmvaa 4,136,72\nQmvaa 4,140,76\ns_nop 7\nv_max3_f32 v40, v40, v0, v1\nv_max3_f32 v40, v40, v2, v3\n"
"v_max3_f32 v40, v40, v4, v5\nv_max3_f32 v40, v40, v6, v7\nQzvaa 0,112,80\nQmvaa 0,116,84\nQmvaa 0,120,88\nQmvaa 0,124,92\nQzvaa 4,128,80\nQmvaa 4,132,84\n"
"Qmvaa 4,136,88\nQmvaa 4,140,92\ns_nop 7\nv_max3_f32 v44, v44, v0, v1\nv_max3_f32 v44, v44, v2, v3\nv_max3_f32 v44, v44, v4, v5\nv_max3_f32 v44, v44, v6, v7\nQzvaa 0,112,96\n"
"Qmvaa 0,116,100\nQmvaa 0,120,104\nQmvaa 0,124,108\nQzvaa 4,128,96\nQmvaa 4,132,100\nQmvaa 4,136,104\nQmvaa 4,140,108\ns_nop 7\n"
"v_max3_f32 v48, v48, v0, v1\nv_max3_f32 v48, v48, v2, v3\nv_max3_f32 v48, v48, v4, v5\nv_max3_f32 v48, v48, v6, v7\ns_add_u32 s89, s89, 8192\ns_add_u32 s66, s66, 1\ns_cmp_lt_u32 s66, 32\ns_cbranch_scc1 .Lfb1\n"
"v_mov_b32 v25, v24\ns_nop 1\nv_permlane32_swap_b32 v24, v25\nv_max_f32 v24, v24, v25\nv_mov_b32 v25, v24\ns_nop 1\nv_permlane16_swap_b32 v24, v25\nv_max_f32 v24, v24, v25\n"
"v_sub_f32 v24, 0, v24\nv_mov_b32 v25, v24\nv_mov_b32 v26, v24\nv_mov_b32 v27, v24\nv_mov_b32 v29, v28\ns_nop 1\nv_permlane32_swap_b32 v28, v29\nv_max_f32 v28, v28, v29\n"
"v_mov_b32 v29, v28\ns_nop 1\nv_permlane16_swap_b32 v28, v29\nv_max_f32 v28, v28, v29\nv_sub_f32 v28, 0, v28\nv_mov_b32 v29, v28\nv_mov_b32 v30, v28\nv_mov_b32 v31, v28\n"
"v_mov_b32 v33, v32\ns_nop 1\nv_permlane32_swap_b32 v32, v33\nv_max_f32 v32, v32, v33\nv_mov_b32 v33, v32\ns_nop 1\nv_permlane16_swap_b32 v32, v33\nv_max_f32 v32, v32, v33\n"
"v_sub_f32 v32, 0, v32\nv_mov_b32 v33, v32\nv_mov_b32 v34, v32\nv_mov_b32 v35, v32\nv_mov_b32 v37, v36\ns_nop 1\nv_permlane32_swap_b32 v36, v37\nv_max_f32 v36, v36, v37\n"
"v_mov_b32 v37, v36\ns_nop 1\nv_permlane16_swap_b32 v36, v37\nv_max_f32 v36, v36, v37\nv_sub_f32 v36, 0, v36\nv_mov_b32 v37, v36\nv_mov_b32 v38, v36\nv_mov_b32 v39, v36\n"
"v_mov_b32 v41, v40\ns_nop 1\nv_permlane32_swap_b32 v40, v41\nv_max_f32 v40, v40, v41\nv_mov_b32 v41, v40\ns_nop 1\nv_permlane16_swap_b32 v40, v41\nv_max_f32 v40, v40, v41\n"
"v_sub_f32 v40, 0, v40\nv_mov_b32 v41, v40\nv_mov_b32 v42, v40\nv_mov_b32 v43, v40\nv_mov_b32 v45, v44\ns_nop 1\nv_permlane32_swap_b32 v44, v45\nv_max_f32 v44, v44, v45\n"
"v_mov_b32 v45, v44\ns_nop 1\nv_permlane16_swap_b32 v44, v45\nv_max_f32 v44, v44, v45\nv_sub_f32 v44, 0, v44\nv_mov_b32 v45, v44\nv_mov_b32 v46, v44\nv_mov_b32 v47, v44\n"
"v_mov_b32 v49, v48\ns_nop 1\nv_permlane32_swap_b32 v48, v49\nv_max_f32 v48, v48, v49\nv_mov_b32 v49, v48\ns_nop 1\nv_permlane16_swap_b32 v48, v49\nv_max_f32 v48, v48, v49\n"
"v_sub_f32 v48, 0, v48\nv_mov_b32 v49, v48\nv_mov_b32 v50, v48\nv_mov_b32 v51, v48\nv_mov_b32 v69, 0\nv_mov_b32 v80, 0\nv_mov_b32 v81, 0\nv_mov_b32 v82, 0\n"
"v_mov_b32 v83, 0\nv_mov_b32 v84, 0\nv_mov_b32 v85, 0\nv_mov_b32 v86, 0\nv_mov_b32 v87, 0\nv_mov_b32 v88, 0\nv_mov_b32 v89, 0\nv_mov_b32 v90, 0\n"
"v_mov_b32 v91, 0\nv_mov_b32 v92, 0\nv_mov_b32 v93, 0\nv_mov_b32 v94, 0\nv_mov_b32 v95, 0\nv_mov_b32 v96, 0\nv_mov_b32 v97, 0\nv_mov_b32 v98, 0\n"
"v_mov_b32 v99, 0\nv_mov_b32 v100, 0\nv_mov_b32 v101, 0\nv_mov_b32 v102, 0\nv_mov_b32 v103, 0\nv_mov_b32 v104, 0\nv_mov_b32 v105, 0\nv_mov_b32 v106, 0\n"
"v_mov_b32 v107, 0\nv_mov_b32 v108, 0\nv_mov_b32 v109, 0\nv_mov_b32 v110, 0\nv_mov_b32 v111, 0\nv_accvgpr_write_b32 a176, v69\nv_accvgpr_write_b32 a177, v69\nv_accvgpr_write_b32 a178, v69\n"
"v_accvgpr_write_b32 a179, v69\nv_mov_b32 v112, 0\nv_mov_b32 v113, 0\nv_mov_b32 v114, 0\nv_mov_b32 v115, 0\nv_mov_b32 v116, 0\nv_mov_b32 v117, 0\nv_mov_b32 v118, 0\n"
"v_mov_b32 v119, 0\nv_mov_b32 v120, 0\nv_mov_b32 v121, 0\nv_mov_b32 v122, 0\nv_mov_b32 v123, 0\nv_mov_b32 v124, 0\nv_mov_b32 v125, 0\nv_mov_b32 v126, 0\n"
"v_mov_b32 v127, 0\nv_mov_b32 v128, 0\nv_mov_b32 v129, 0\nv_mov_b32 v130, 0\nv_mov_b32 v131, 0\nv_mov_b32 v132, 0\nv_mov_b32 v133, 0\nv_mov_b32 v134, 0\n"
"v_mov_b32 v135, 0\nv_mov_b32 v136, 0\nv_mov_b32 v137, 0\nv_mov_b32 v138, 0\nv_mov_b32 v139, 0\nv_mov_b32 v140, 0\nv_mov_b32 v141, 0\nv_mov_b32 v142, 0\n"
"v_mov_b32 v143, 0\nv_accvgpr_write_b32 a180, v69\nv_accvgpr_write_b32 a181, v69\nv_accvgpr_write_b32 a182, v69\nv_accvgpr_write_b32 a183, v69\nv_mov_b32 v144, 0\nv_mov_b32 v145, 0\nv_mov_b32 v146, 0\n"
"v_mov_b32 v147, 0\nv_mov_b32 v148, 0\nv_mov_b32 v149, 0\nv_mov_b32 v150, 0\nv_mov_b32 v151, 0\nv_mov_b32 v152, 0\nv_mov_b32 v153, 0\nv_mov_b32 v154, 0\n"
"v_mov_b32 v155, 0\nv_mov_b32 v156, 0\nv_mov_b32 v157, 0\nv_mov_b32 v158, 0\nv_mov_b32 v159, 0\nv_mov_b32 v160, 0\nv_mov_b32 v161, 0\nv_mov_b32 v162, 0\n"
"v_mov_b32 v163, 0\nv_mov_b32 v164, 0\nv_mov_b32 v165, 0\nv_mov_b32 v166, 0\nv_mov_b32 v167, 0\nv_mov_b32 v168, 0\nv_mov_b32 v169, 0\nv_mov_b32 v170, 0\n"
"v_mov_b32 v171, 0\nv_mov_b32 v172, 0\nv_mov_b32 v173, 0\nv_mov_b32 v174, 0\nv_mov_b32 v175, 0\nv_accvgpr_write_b32 a184, v69\nv_accvgpr_write_b32 a185, v69\nv_accvgpr_write_b32 a186, v69\n"
"v_accvgpr_write_b32 a187, v69\nv_mov_b32 v176, 0\nv_mov_b32 v177, 0\nv_mov_b32 v178, 0\nv_mov_b32 v179, 0\nv_mov_b32 v180, 0\nv_mov_b32 v181, 0\nv_mov_b32 v182, 0\n"
"v_mov_b32 v183, 0\nv_mov_b32 v184, 0\nv_mov_b32 v185, 0\nv_mov_b32 v186, 0\nv_mov_b32 v187, 0\nv_mov_b32 v188, 0\nv_mov_b32 v189, 0\nv_mov_b32 v190, 0\n"
"v_mov_b32 v191, 0\nv_mov_b32 v192, 0\nv_mov_b32 v193, 0\nv_mov_b32 v194, 0\nv_mov_b32 v195, 0\nv_mov_b32 v196, 0\nv_mov_b32 v197, 0\nv_mov_b32 v198, 0\n"
"v_mov_b32 v199, 0\nv_mov_b32 v200, 0\nv_mov_b32 v201, 0\nv_mov_b32 v202, 0\nv_mov_b32 v203, 0\nv_mov_b32 v204, 0\nv_mov_b32 v205, 0\nv_mov_b32 v206, 0\n"
"v_mov_b32 v207, 0\nv_accvgpr_write_b32 a188, v69\nv_accvgpr_write_b32 a189, v69\nv_accvgpr_write_b32 a190, v69\nv_accvgpr_write_b32 a191, v69\nv_mov_b32 v208, 0\nv_mov_b32 v209, 0\nv_mov_b32 v210, 0\n"
"v_mov_b32 v211, 0\nv_mov_b32 v212, 0\nv_mov_b32 v213, 0\nv_mov_b32 v214, 0\nv_mov_b32 v215, 0\nv_mov_b32 v216, 0\nv_mov_b32 v217, 0\nv_mov_b32 v218, 0\n"
"v_mov_b32 v219, 0\nv_mov_b32 v220, 0\nv_mov_b32 v221, 0\nv_mov_b32 v222, 0\nv_mov_b32 v223, 0\nv_mov_b32 v224, 0\nv_mov_b32 v225, 0\nv_mov_b32 v226, 0\n"
"v_mov_b32 v227, 0\nv_mov_b32 v228, 0\nv_mov_b32 v229, 0\nv_mov_b32 v230, 0\nv_mov_b32 v231, 0\nv_mov_b32 v232, 0\nv_mov_b32 v233, 0\nv_mov_b32 v234, 0\n"
"v_mov_b32 v235, 0\nv_mov_b32 v236, 0\nv_mov_b32 v237, 0\nv_mov_b32 v238, 0\nv_mov_b32 v239, 0\nv_accvgpr_write_b32 a192, v69\nv_accvgpr_write_b32 a193, v69\nv_accvgpr_write_b32 a194, v69\n"
"v_accvgpr_write_b32 a195, v69\nv_mov_b32 v240, 0\nv_mov_b32 v241, 0\nv_mov_b32 v242, 0\nv_mov_b32 v243, 0\nv_mov_b32 v244, 0\nv_mov_b32 v245, 0\nv_mov_b32 v246, 0\n"
"v_mov_b32 v247, 0\nv_mov_b32 v248, 0\nv_mov_b32 v249, 0\nv_mov_b32 v250, 0\nv_mov_b32 v251, 0\nv_mov_b32 v252, 0\nv_mov_b32 v253, 0\nv_mov_b32 v254, 0\n"
"v_mov_b32 v255, 0\nv_accvgpr_write_b32 a208, v69\nv_accvgpr_write_b32 a209, v69\nv_accvgpr_write_b32 a210, v69\nv_accvgpr_write_b32 a211, v69\nv_accvgpr_write_b32 a212, v69\nv_accvgpr_write_b32 a213, v69\nv_accvgpr_write_b32 a214, v69\n"
"v_accvgpr_write_b32 a215, v69\nv_accvgpr_write_b32 a216, v69\nv_accvgpr_write_b32 a217, v69\nv_accvgpr_write_b32 a218, v69\nv_accvgpr_write_b32 a219, v69\nv_accvgpr_write_b32 a220, v69\nv_accvgpr_write_b32 a221, v69\nv_accvgpr_write_b32 a222, v69\n"
"v_accvgpr_write_b32 a223, v69\nv_accvgpr_write_b32 a196, v69\nv_accvgpr_write_b32 a197, v69\nv_accvgpr_write_b32 a198, v69\nv_accvgpr_write_b32 a199, v69\nv_accvgpr_write_b32 a224, v69\nv_accvgpr_write_b32 a225, v69\nv_accvgpr_write_b32 a226, v69\n"
"v_accvgpr_write_b32 a227, v69\nv_accvgpr_write_b32 a228, v69\nv_accvgpr_write_b32 a229, v69\nv_accvgpr_write_b32 a230, v69\nv_accvgpr_write_b32 a231, v69\nv_accvgpr_write_b32 a232, v69\nv_accvgpr_write_b32 a233, v69\nv_accvgpr_write_b32 a234, v69\n"
"v_accvgpr_write_b32 a235, v69\nv_accvgpr_write_b32 a236, v69\nv_accvgpr_write_b32 a237, v69\nv_accvgpr_write_b32 a238, v69\nv_accvgpr_write_b32 a239, v69\nv_accvgpr_write_b32 a240, v69\nv_accvgpr_write_b32 a241, v69\nv_accvgpr_write_b32 a242, v69\n"
"v_accvgpr_write_b32 a243, v69\nv_accvgpr_write_b32 a244, v69\nv_accvgpr_write_b32 a245, v69\nv_accvgpr_write_b32 a246, v69\nv_accvgpr_write_b32 a247, v69\nv_accvgpr_write_b32 a248, v69\nv_accvgpr_write_b32 a249, v69\nv_accvgpr_write_b32 a250, v69\n"
"v_accvgpr_write_b32 a251, v69\nv_accvgpr_write_b32 a252, v69\nv_accvgpr_write_b32 a253, v69\nv_accvgpr_write_b32 a254, v69\nv_accvgpr_write_b32 a255, v69\nv_accvgpr_write_b32 a200, v69\nv_accvgpr_write_b32 a201, v69\nv_accvgpr_write_b32 a202, v69\n"
"v_accvgpr_write_b32 a203, v69\ns_lshl_b32 s89, s67, 19\ns_mov_b32 s66, 0\n.Lfb2:\ns_add_u32 s68, s89, 4096\nbuffer_load_dwordx4 a[112:115], v66, s[40:43], s89 offen offset:0\nbuffer_load_dwordx4 a[116:119], v66, s[40:43], s89 offen offset:64\nbuffer_load_dwordx4 a[120:123], v66, s[40:43], s89 offen offset:128\n"
"buffer_load_dwordx4 a[124:127], v66, s[40:43], s89 offen offset:192\nbuffer_load_dwordx4 a[128:131], v66, s[40:43], s68 offen offset:0\nbuffer_load_dwordx4 a[132:135], v66, s[40:43], s68 offen offset:64\nbuffer_load_dwordx4 a[136:139], v66, s[40:43], s68 offen offset:128\nbuffer_load_dwordx4 a[140:143], v66, s[40:43], s68 offen offset:192\nbuffer_load_ushort v8, v67, s[44:47], s89 offen offset:0\nbuffer_load_ushort v9, v67, s[44:47], s89 offen offset:256\nbuffer_load_ushort v10, v67, s[44:47], s89 offen offset:512\n"
"buffer_load_ushort v11, v67, s[44:47], s89 offen offset:768\nbuffer_load_ushort v12, v67, s[44:47], s68 offen offset:0\nbuffer_load_ushort v13, v67, s[44:47], s68 offen offset:256\nbuffer_load_ushort v14, v67, s[44:47], s68 offen offset:512\nbuffer_load_ushort v15, v67, s[44:47], s68 offen offset:768\ns_waitcnt vmcnt(0)\nv_lshl_or_b32 v8, v9, 16, v8\nv_accvgpr_write_b32 a144, v8\n"
"v_lshl_or_b32 v10, v11, 16, v10\nv_accvgpr_write_b32 a145, v10\nv_lshl_or_b32 v12, v13, 16, v12\nv_accvgpr_write_b32 a146, v12\nv_lshl_or_b32 v14, v15, 16, v14\nv_accvgpr_write_b32 a147, v14\nbuffer_load_ushort v8, v67, s[44:47], s89 offen offset:32\nbuffer_load_ushort v9, v67, s[44:47], s89 offen offset:288\n"
"buffer_load_ushort v10, v67, s[44:47], s89 offen offset:544\nbuffer_load_ushort v11, v67, s[44:47], s89 offen offset:800\nbuffer_load_ushort v12, v67, s[44:47], s68 offen offset:32\nbuffer_load_ushort v13, v67, s[44:47], s68 offen offset:288\nbuffer_load_ushort v14, v67, s[44:47], s68 offen offset:544\nbuffer_load_ushort v15, v67, s[44:47], s68 offen offset:800\ns_waitcnt vmcnt(0)\nv_lshl_or_b32 v8, v9, 16, v8\n"
"v_accvgpr_write_b32 a148, v8\nv_lshl_or_b32 v10, v11, 16, v10\nv_accvgpr_write_b32 a149, v10\nv_lshl_or_b32 v12, v13, 16, v12\nv_accvgpr_write_b32 a150, v12\nv_lshl_or_b32 v14, v15, 16, v14\nv_accvgpr_write_b32 a151, v14\nbuffer_load_ushort v8, v67, s[44:47], s89 offen offset:64\n"
"buffer_load_ushort v9, v67, s[44:47], s89 offen offset:320\nbuffer_load_ushort v10, v67, s[44:47], s89 offen offset:576\nbuffer_load_ushort v11, v67, s[44:47], s89 offen offset:832\nbuffer_load_ushort v12, v67, s[44:47], s68 offen offset:64\nbuffer_load_ushort v13, v67, s[44:47], s68 offen offset:320\nbuffer_load_ushort v14, v67, s[44:47], s68 offen offset:576\nbuffer_load_ushort v15, v67, s[44:47], s68 offen offset:832\ns_waitcnt vmcnt(0)\n"
"v_lshl_or_b32 v8, v9, 16, v8\nv_accvgpr_write_b32 a152, v8\nv_lshl_or_b32 v10, v11, 16, v10\nv_accvgpr_write_b32 a153, v10\nv_lshl_or_b32 v12, v13, 16, v12\nv_accvgpr_write_b32 a154, v12\nv_lshl_or_b32 v14, v15, 16, v14\nv_accvgpr_write_b32 a155, v14\n"
"buffer_load_ushort v8, v67, s[44:47], s89 offen offset:96\nbuffer_load_ushort v9, v67, s[44:47], s89 offen offset:352\nbuffer_load_ushort v10, v67, s[44:47], s89 offen offset:608\nbuffer_load_ushort v11, v67, s[44:47], s89 offen offset:864\nbuffer_load_ushort v12, v67, s[44:47], s68 offen offset:96\nbuffer_load_ushort v13, v67, s[44:47], s68 offen offset:352\nbuffer_load_ushort v14, v67, s[44:47], s68 offen offset:608\nbuffer_load_ushort v15, v67, s[44:47], s68 offen offset:864\n"
"s_waitcnt vmcnt(0)\nv_lshl_or_b32 v8, v9, 16, v8\nv_accvgpr_write_b32 a156, v8\nv_lshl_or_b32 v10, v11, 16, v10\nv_accvgpr_write_b32 a157, v10\nv_lshl_or_b32 v12, v13, 16, v12\nv_accvgpr_write_b32 a158, v12\nv_lshl_or_b32 v14, v15, 16, v14\n"
"v_accvgpr_write_b32 a159, v14\nbuffer_load_ushort v8, v67, s[44:47], s89 offen offset:128\nbuffer_load_ushort v9, v67, s[44:47], s89 offen offset:384\nbuffer_load_ushort v10, v67, s[44:47], s89 offen offset:640\nbuffer_load_ushort v11, v67, s[44:47], s89 offen offset:896\nbuffer_load_ushort v12, v67, s[44:47], s68 offen offset:128\nbuffer_load_ushort v13, v67, s[44:47], s68 offen offset:384\nbuffer_load_ushort v14, v67, s[44:47], s68 offen offset:640\n"
"buffer_load_ushort v15, v67, s[44:47], s68 offen offset:896\ns_waitcnt vmcnt(0)\nv_lshl_or_b32 v8, v9, 16, v8\nv_accvgpr_write_b32 a160, v8\nv_lshl_or_b32 v10, v11, 16, v10\nv_accvgpr_write_b32 a161, v10\nv_lshl_or_b32 v12, v13, 16, v12\nv_accvgpr_write_b32 a162, v12\n"
"v_lshl_or_b32 v14, v15, 16, v14\nv_accvgpr_write_b32 a163, v14\nbuffer_load_ushort v8, v67, s[44:47], s89 offen offset:160\nbuffer_load_ushort v9, v67, s[44:47], s89 offen offset:416\nbuffer_load_ushort v10, v67, s[44:47], s89 offen offset:672\nbuffer_load_ushort v11, v67, s[44:47], s89 offen offset:928\nbuffer_load_ushort v12, v67, s[44:47], s68 offen offset:160\nbuffer_load_ushort v13, v67, s[44:47], s68 offen offset:416\n"
"buffer_load_ushort v14, v67, s[44:47], s68 offen offset:672\nbuffer_load_ushort v15, v67, s[44:47], s68 offen offset:928\ns_waitcnt vmcnt(0)\nv_lshl_or_b32 v8, v9, 16, v8\nv_accvgpr_write_b32 a164, v8\nv_lshl_or_b32 v10, v11, 16, v10\nv_accvgpr_write_b32 a165, v10\nv_lshl_or_b32 v12, v13, 16, v12\n"
"v_accvgpr_write_b32 a166, v12\nv_lshl_or_b32 v14, v15, 16, v14\nv_accvgpr_write_b32 a167, v14\nbuffer_load_ushort v8, v67, s[44:47], s89 offen offset:192\nbuffer_load_ushort v9, v67, s[44:47], s89 offen offset:448\nbuffer_load_ushort v10, v67, s[44:47], s89 offen offset:704\nbuffer_load_ushort v11, v67, s[44:47], s89 offen offset:960\nbuffer_load_ushort v12, v67, s[44:47], s68 offen offset:192\n"
"buffer_load_ushort v13, v67, s[44:47], s68 offen offset:448\nbuffer_load_ushort v14, v67, s[44:47], s68 offen offset:704\nbuffer_load_ushort v15, v67, s[44:47], s68 offen offset:960\ns_waitcnt vmcnt(0)\nv_lshl_or_b32 v8, v9, 16, v8\nv_accvgpr_write_b32 a168, v8\nv_lshl_or_b32 v10, v11, 16, v10\nv_accvgpr_write_b32 a169, v10\n"
"v_lshl_or_b32 v12, v13, 16, v12\nv_accvgpr_write_b32 a170, v12\nv_lshl_or_b32 v14, v15, 16, v14\nv_accvgpr_write_b32 a171, v14\nbuffer_load_ushort v8, v67, s[44:47], s89 offen offset:224\nbuffer_load_ushort v9, v67, s[44:47], s89 offen offset:480\nbuffer_load_ushort v10, v67, s[44:47], s89 offen offset:736\nbuffer_load_ushort v11, v67, s[44:47], s89 offen offset:992\n"
"buffer_load_ushort v12, v67, s[44:47], s68 offen offset:224\nbuffer_load_ushort v13, v67, s[44:47], s68 offen offset:480\nbuffer_load_ushort v14, v67, s[44:47], s68 offen offset:736\nbuffer_load_ushort v15, v67, s[44:47], s68 offen offset:992\ns_waitcnt vmcnt(0)\nv_lshl_or_b32 v8, v9, 16, v8\nv_accvgpr_write_b32 a172, v8\nv_lshl_or_b32 v10, v11, 16, v10\n"
"v_accvgpr_write_b32 a173, v10\nv_lshl_or_b32 v12, v13, 16, v12\nv_accvgpr_write_b32 a174, v12\nv_lshl_or_b32 v14, v15, 16, v14\nv_accvgpr_write_b32 a175, v14\nQcvaa 0,112,0,24\nQmvaa 0,116,4\nQmvaa 0,120,8\n"
"Qmvaa 0,124,12\nQcvaa 4,128,0,24\nQmvaa 4,132,4\nQmvaa 4,136,8\nQmvaa 4,140,12\ns_nop 7\nQe 0\nQe 1\n"
"Qe 2\nQe 3\nQe 4\nQe 5\nQe 6\nQe 7\ns_nop 0\nQk 16,0,1\n"
"Qk 17,2,3\nQk 18,4,5\nQk 19,6,7\ns_nop 1\nQmvav 80,144,16\nQmvav 84,148,16\nQmvav 88,152,16\nQmvav 92,156,16\n"
"Qmvav 96,160,16\nQmvav 100,164,16\nQmvav 104,168,16\nQmvav 108,172,16\nQmaav 176,204,16\nQcvaa 0,112,16,28\nQmvaa 0,116,20\nQmvaa 0,120,24\n"
"Qmvaa 0,124,28\nQcvaa 4,128,16,28\nQmvaa 4,132,20\nQmvaa 4,136,24\nQmvaa 4,140,28\ns_nop 7\nQe 0\nQe 1\n"
"Qe 2\nQe 3\nQe 4\nQe 5\nQe 6\nQe 7\ns_nop 0\nQk 16,0,1\n"
"Qk 17,2,3\nQk 18,4,5\nQk 19,6,7\ns_nop 1\nQmvav 112,144,16\nQmvav 116,148,16\nQmvav 120,152,16\nQmvav 124,156,16\n"
"Qmvav 128,160,16\nQmvav 132,164,16\nQmvav 136,168,16\nQmvav 140,172,16\nQmaav 180,204,16\nQcvaa 0,112,32,32\nQmvaa 0,116,36\nQmvaa 0,120,40\n"
"Qmvaa 0,124,44\nQcvaa 4,128,32,32\nQmvaa 4,132,36\nQmvaa 4,136,40\nQmvaa 4,140,44\ns_nop 7\nQe 0\nQe 1\n"
"Qe 2\nQe 3\nQe 4\nQe 5\nQe 6\nQe 7\ns_nop 0\nQk 16,0,1\n"
"Qk 17,2,3\nQk 18,4,5\nQk 19,6,7\ns_nop 1\nQmvav 144,144,16\nQmvav 148,148,16\nQmvav 152,152,16\nQmvav 156,156,16\n"
"Qmvav 160,160,16\nQmvav 164,164,16\nQmvav 168,168,16\nQmvav 172,172,16\nQmaav 184,204,16\nQcvaa 0,112,48,36\nQmvaa 0,116,52\nQmvaa 0,120,56\n"
"Qmvaa 0,124,60\nQcvaa 4,128,48,36\nQmvaa 4,132,52\nQmvaa 4,136,56\nQmvaa 4,140,60\ns_nop 7\nQe 0\nQe 1\n"
"Qe 2\nQe 3\nQe 4\nQe 5\nQe 6\nQe 7\ns_nop 0\nQk 16,0,1\n"
"Qk 17,2,3\nQk 18,4,5\nQk 19,6,7\ns_nop 1\nQmvav 176,144,16\nQmvav 180,148,16\nQmvav 184,152,16\nQmvav 188,156,16\n"
"Qmvav 192,160,16\nQmvav 196,164,16\nQmvav 200,168,16\nQmvav 204,172,16\nQmaav 188,204,16\nQcvaa 0,112,64,40\nQmvaa 0,116,68\nQmvaa 0,120,72\n"
"Qmvaa 0,124,76\nQcvaa 4,128,64,40\nQmvaa 4,132,68\nQmvaa 4,136,72\nQmvaa 4,140,76\ns_nop 7\nQe 0\nQe 1\n"
"Qe 2\nQe 3\nQe 4\nQe 5\nQe 6\nQe 7\ns_nop 0\nQk 16,0,1\n"
"Qk 17,2,3\nQk 18,4,5\nQk 19,6,7\ns_nop 1\nQmvav 208,144,16\nQmvav 212,148,16\nQmvav 216,152,16\nQmvav 220,156,16\n"
"Qmvav 224,160,16\nQmvav 228,164,16\nQmvav 232,168,16\nQmvav 236,172,16\nQmaav 192,204,16\nQcvaa 0,112,80,44\nQmvaa 0,116,84\nQmvaa 0,120,88\n"
"Qmvaa 0,124,92\nQcvaa 4,128,80,44\nQmvaa 4,132,84\nQmvaa 4,136,88\nQmvaa 4,140,92\ns_nop 7\nQe 0\nQe 1\n"
"Qe 2\nQe 3\nQe 4\nQe 5\nQe 6\nQe 7\ns_nop 0\nQk 16,0,1\n"
"Qk 17,2,3\nQk 18,4,5\nQk 19,6,7\ns_nop 1\nQmvav 240,144,16\nQmvav 244,148,16\nQmvav 248,152,16\nQmvav 252,156,16\n"
"Qmaav 208,160,16\nQmaav 212,164,16\nQmaav 216,168,16\nQmaav 220,172,16\nQmaav 196,204,16\nQcvaa 0,112,96,48\nQmvaa 0,116,100\nQmvaa 0,120,104\n"
"Qmvaa 0,124,108\nQcvaa 4,128,96,48\nQmvaa 4,132,100\nQmvaa 4,136,104\nQmvaa 4,140,108\ns_nop 7\nQe 0\nQe 1\n"
"Qe 2\nQe 3\nQe 4\nQe 5\nQe 6\nQe 7\ns_nop 0\nQk 16,0,1\n"
"Qk 17,2,3\nQk 18,4,5\nQk 19,6,7\ns_nop 1\nQmaav 224,144,16\nQmaav 228,148,16\nQmaav 232,152,16\nQmaav 236,156,16\n"
"Qmaav 240,160,16\nQmaav 244,164,16\nQmaav 248,168,16\nQmaav 252,172,16\nQmaav 200,204,16\ns_add_u32 s89, s89, 8192\ns_add_u32 s66, s66, 1\ns_cmp_lt_u32 s66, 32\n"
"s_cbranch_scc1 .Lfb2\ns_nop 7\nv_accvgpr_read_b32 v24, a176\nv_accvgpr_read_b32 v28, a180\nv_accvgpr_read_b32 v32, a184\nv_accvgpr_read_b32 v36, a188\nv_accvgpr_read_b32 v40, a192\nv_accvgpr_read_b32 v44, a196\n"
"v_accvgpr_read_b32 v48, a200\ns_branch .Lfbret\n.Lend:\ns_waitcnt vmcnt(0) lgkmcnt(0)\n"
  :: "s"((unsigned)kp), "s"((unsigned)(kp >> 32)), "s"((unsigned)vp), "s"((unsigned)(vp >> 32)), "s"((unsigned)xp), "s"((unsigned)(xp >> 32)),
     "s"((unsigned)wp), "s"((unsigned)(wp >> 32)), "s"((unsigned)op), "s"((unsigned)(op >> 32)), "s"(ldsb), "s"(wid), "s"(head0),
     "s"((unsigned)(unsigned long long)(dbg + (c * 4u + wid) * 32u)), "s"((unsigned)((unsigned long long)(dbg + (c * 4u + wid) * 32u) >> 32)),
     "s"((unsigned)wop), "s"((unsigned)(wop >> 32))
  : "v0", "v1", "v2", "v3", "v4", "v5", "v6", "v7", "v8", "v9", "v10", "v11", "v12", "v13", "v14", "v15", "v16", "v17", "v18", "v19", "v20", "v21", "v22", "v23", "v24", "v25", "v26", "v27", "v28", "v29", "v30", "v31", "v32", "v33", "v34", "v35", "v36", "v37", "v38", "v39", "v40", "v41", "v42", "v43", "v44", "v45", "v46", "v47", "v48", "v49", "v50", "v51", "v52", "v53", "v54", "v55", "v56", "v57", "v58", "v59", "v60", "v61", "v62", "v63", "v64", "v65", "v66", "v67", "v68", "v69", "v70", "v71", "v72", "v73", "v74", "v75", "v76", "v77", "v78", "v79", "v80", "v81", "v82", "v83", "v84", "v85", "v86", "v87", "v88", "v89", "v90", "v91", "v92", "v93", "v94", "v95", "v96", "v97", "v98", "v99", "v100", "v101", "v102", "v103", "v104", "v105", "v106", "v107", "v108", "v109", "v110", "v111", "v112", "v113", "v114", "v115", "v116", "v117", "v118", "v119", "v120", "v121", "v122", "v123", "v124", "v125", "v126", "v127", "v128", "v129", "v130", "v131", "v132", "v133", "v134", "v135", "v136", "v137", "v138", "v139", "v140", "v141", "v142", "v143", "v144", "v145", "v146", "v147", "v148", "v149", "v150", "v151", "v152", "v153", "v154", "v155", "v156", "v157", "v158", "v159", "v160", "v161", "v162", "v163", "v164", "v165", "v166", "v167", "v168", "v169", "v170", "v171", "v172", "v173", "v174", "v175", "v176", "v177", "v178", "v179", "v180", "v181", "v182", "v183", "v184", "v185", "v186", "v187", "v188", "v189", "v190", "v191", "v192", "v193", "v194", "v195", "v196", "v197", "v198", "v199", "v200", "v201", "v202", "v203", "v204", "v205", "v206", "v207", "v208", "v209", "v210", "v211", "v212", "v213", "v214", "v215", "v216", "v217", "v218", "v219", "v220", "v221", "v222", "v223", "v224", "v225", "v226", "v227", "v228", "v229", "v230", "v231", "v232", "v233", "v234", "v235", "v236", "v237", "v238", "v239", "v240", "v241", "v242", "v243", "v244", "v245", "v246", "v247", "v248", "v249", "v250", "v251", "v252", "v253", "v254", "v255", "a0", "a1", "a2", "a3", "a4", "a5", "a6", "a7", "a8", "a9", "a10", "a11", "a12", "a13", "a14", "a15", "a16", "a17", "a18", "a19", "a20", "a21", "a22", "a23", "a24", "a25", "a26", "a27", "a28", "a29", "a30", "a31", "a32", "a33", "a34", "a35", "a36", "a37", "a38", "a39", "a40", "a41", "a42", "a43", "a44", "a45", "a46", "a47", "a48", "a49", "a50", "a51", "a52", "a53", "a54", "a55", "a56", "a57", "a58", "a59", "a60", "a61", "a62", "a63", "a64", "a65", "a66", "a67", "a68", "a69", "a70", "a71", "a72", "a73", "a74", "a75", "a76", "a77", "a78", "a79", "a80", "a81", "a82", "a83", "a84", "a85", "a86", "a87", "a88", "a89", "a90", "a91", "a92", "a93", "a94", "a95", "a96", "a97", "a98", "a99", "a100", "a101", "a102", "a103", "a104", "a105", "a106", "a107", "a108", "a109", "a110", "a111", "a112", "a113", "a114", "a115", "a116", "a117", "a118", "a119", "a120", "a121", "a122", "a123", "a124", "a125", "a126", "a127", "a128", "a129", "a130", "a131", "a132", "a133", "a134", "a135", "a136", "a137", "a138", "a139", "a140", "a141", "a142", "a143", "a144", "a145", "a146", "a147", "a148", "a149", "a150", "a151", "a152", "a153", "a154", "a155", "a156", "a157", "a158", "a159", "a160", "a161", "a162", "a163", "a164", "a165", "a166", "a167", "a168", "a169", "a170", "a171", "a172", "a173", "a174", "a175", "a176", "a177", "a178", "a179", "a180", "a181", "a182", "a183", "a184", "a185", "a186", "a187", "a188", "a189", "a190", "a191", "a192", "a193", "a194", "a195", "a196", "a197", "a198", "a199", "a200", "a201", "a202", "a203", "a204", "a205", "a206", "a207", "a208", "a209", "a210", "a211", "a212", "a213", "a214", "a215", "a216", "a217", "a218", "a219", "a220", "a221", "a222", "a223", "a224", "a225", "a226", "a227", "a228", "a229", "a230", "a231", "a232", "a233", "a234", "a235", "a236", "a237", "a238", "a239", "a240", "a241", "a242", "a243", "a244", "a245", "a246", "a247", "a248", "a249", "a250", "a251", "a252", "a253", "a254", "a255", "s24", "s25", "s26", "s27", "s28", "s29", "s30", "s31", "s40", "s41", "s42", "s43", "s44", "s45", "s46", "s47", "s48", "s49", "s50", "s51", "s52", "s53", "s54", "s55", "s56", "s57", "s58", "s59", "s60", "s61", "s62", "s63", "s64", "s65", "s66", "s67", "s68", "s69", "s70", "s71", "s72", "s73", "s74", "s75", "s76", "s77", "s78", "s79", "s80", "s81", "s82", "s83", "s84", "s85", "s86", "s87", "s88", "s89", "s90", "s91", "s92", "s93", "s94", "s95", "s96", "s97", "s98", "vcc", "scc", "memory");
}

__global__ __launch_bounds__(256, 1)
void oproj_asm(const unsigned short* __restrict__ O, const unsigned short* __restrict__ W0, const unsigned short* __restrict__ W1,
               float* __restrict__ P, unsigned* __restrict__ cnt, unsigned short* __restrict__ out, unsigned long long* dbg) {
  extern __shared__ __attribute__((aligned(16))) char lds[];
  const unsigned c = blockIdx.x;
  const unsigned xcd = c & 7u, j = c >> 3;
  const unsigned bb = xcd * 4u + (j >> 3), nt = j & 3u, sk = (j >> 2) & 1u;
  const unsigned bk = bb >> 4, b = bb & 15u;
  const unsigned tile = bb * 4u + nt;
  const unsigned long long ap = (unsigned long long)O + (unsigned long long)bb * 1835008ull + (unsigned long long)sk * (64ull * 14336ull);
  const unsigned long long bp = (unsigned long long)(bk ? W1 : W0) + (unsigned long long)sk * (2048ull * 1280ull) + nt * 320u;
  const unsigned long long pp = (unsigned long long)P + (unsigned long long)tile * (2ull * 81920ull);
  const unsigned long long op = (unsigned long long)out + (unsigned long long)b * (224ull * 2560ull) + bk * 1280u + nt * 320u;
  const unsigned long long cp = (unsigned long long)(cnt + tile * 32u);
  const unsigned wid = __builtin_amdgcn_readfirstlane(threadIdx.x >> 6);
  const unsigned ldsb = (unsigned)(size_t)lds;
  asm volatile(
".macro Pta d,v,o\nds_read_b64_tr_b16 a[\\d:\\d+1], v\\v offset:\\o\n.endm\n.macro Pda d,v,o\nds_read_b128 a[\\d:\\d+3], v\\v offset:\\o\n.endm\n.macro Pzvaa d,a,b\nv_mfma_f32_16x16x32_bf16 v[\\d:\\d+3], a[\\a:\\a+3], a[\\b:\\b+3], 0\n"
".endm\n.macro Pmvaa d,a,b\nv_mfma_f32_16x16x32_bf16 v[\\d:\\d+3], a[\\a:\\a+3], a[\\b:\\b+3], v[\\d:\\d+3]\n.endm\n.macro Pk d,a,b\nv_cvt_pk_bf16_f32 v\\d, v\\a, v\\b\n.endm\n.macro Pa d,a,b\n"
"v_add_f32 v\\d, v\\a, v\\b\n.endm\ns_mov_b32 s62, %9\ns_mov_b32 s65, %14\ns_cmp_lt_u32 s62, 3\ns_cselect_b32 s40, %0, %2\ns_cselect_b32 s41, %1, %3\ns_and_b32 s41, s41, 0xffff\n"
"s_cmp_lt_u32 s62, 3\ns_mov_b32 s63, 917504\ns_mov_b32 s64, 2621440\ns_cselect_b32 s42, s63, s64\ns_mov_b32 s43, 0x20000\ns_mov_b32 s63, 14336\ns_mov_b32 s64, 40960\ns_cselect_b32 s58, s63, s64\n"
"s_mov_b32 s56, 0\ns_cmp_lt_u32 s62, 2\ns_cselect_b32 s44, %0, %2\ns_cselect_b32 s45, %1, %3\ns_and_b32 s45, s45, 0xffff\ns_cmp_lt_u32 s62, 2\ns_mov_b32 s63, 917504\ns_mov_b32 s64, 2621440\n"
"s_cselect_b32 s46, s63, s64\ns_mov_b32 s47, 0x20000\ns_mov_b32 s63, 14336\ns_mov_b32 s64, 40960\ns_cselect_b32 s59, s63, s64\ns_mov_b32 s57, 0\ns_mov_b32 s48, %4\ns_and_b32 s49, %5, 0xffff\n"
"s_mov_b32 s50, 163840\ns_mov_b32 s51, 0x20000\ns_mov_b32 s52, %6\ns_and_b32 s53, %7, 0xffff\ns_mov_b32 s54, 573440\ns_mov_b32 s55, 0x20000\ns_mov_b32 s66, %10\ns_mov_b32 s67, %11\n"
"s_mul_i32 s60, s62, 6144\ns_add_u32 s60, s60, %8\nv_mbcnt_lo_u32_b32 v152, -1, 0\nv_mbcnt_hi_u32_b32 v152, -1, v152\nv_and_b32 v153, 15, v152\nv_lshrrev_b32 v154, 4, v152\ns_mul_i32 s63, s62, 6\ns_add_u32 s63, s63, 0\n"
"s_lshl_b32 s64, s63, 10\nv_lshlrev_b32 v155, 4, v152\nv_add_u32 v155, s64, v155\ns_sub_u32 s64, s63, 14\ns_lshl_b32 s64, s64, 2\nv_add_u32 v156, s64, v154\nv_mul_u32_u24 v157, 205, v156\nv_lshrrev_b32 v157, 11, v157\n"
"v_mul_u32_u24 v142, 10, v157\nv_sub_u32 v156, v156, v142\nv_lshlrev_b32 v156, 5, v156\nv_bfe_u32 v142, v152, 1, 3\nv_lshl_add_u32 v157, v157, 3, v142\nv_mul_u32_u24 v157, 1280, v157\nv_add_u32 v156, v156, v157\nv_and_b32 v157, 1, v152\n"
"v_lshl_add_u32 v156, v157, 4, v156\ns_cmp_lt_u32 s63, 14\ns_cselect_b64 vcc, -1, 0\nv_cndmask_b32 v142, v156, v155, vcc\ns_mul_i32 s63, s62, 6\ns_add_u32 s63, s63, 1\ns_lshl_b32 s64, s63, 10\nv_lshlrev_b32 v155, 4, v152\n"
"v_add_u32 v155, s64, v155\ns_sub_u32 s64, s63, 14\ns_lshl_b32 s64, s64, 2\nv_add_u32 v156, s64, v154\nv_mul_u32_u24 v157, 205, v156\nv_lshrrev_b32 v157, 11, v157\nv_mul_u32_u24 v143, 10, v157\nv_sub_u32 v156, v156, v143\n"
"v_lshlrev_b32 v156, 5, v156\nv_bfe_u32 v143, v152, 1, 3\nv_lshl_add_u32 v157, v157, 3, v143\nv_mul_u32_u24 v157, 1280, v157\nv_add_u32 v156, v156, v157\nv_and_b32 v157, 1, v152\nv_lshl_add_u32 v156, v157, 4, v156\ns_cmp_lt_u32 s63, 14\n"
"s_cselect_b64 vcc, -1, 0\nv_cndmask_b32 v143, v156, v155, vcc\ns_mul_i32 s63, s62, 6\ns_add_u32 s63, s63, 2\ns_lshl_b32 s64, s63, 10\nv_lshlrev_b32 v155, 4, v152\nv_add_u32 v155, s64, v155\ns_sub_u32 s64, s63, 14\n"
"s_lshl_b32 s64, s64, 2\nv_add_u32 v156, s64, v154\nv_mul_u32_u24 v157, 205, v156\nv_lshrrev_b32 v157, 11, v157\nv_mul_u32_u24 v144, 10, v157\nv_sub_u32 v156, v156, v144\nv_lshlrev_b32 v156, 5, v156\nv_bfe_u32 v144, v152, 1, 3\n"
"v_lshl_add_u32 v157, v157, 3, v144\nv_mul_u32_u24 v157, 1280, v157\nv_add_u32 v156, v156, v157\nv_and_b32 v157, 1, v152\nv_lshl_add_u32 v156, v157, 4, v156\ns_cmp_lt_u32 s63, 14\ns_cselect_b64 vcc, -1, 0\nv_cndmask_b32 v144, v156, v155, vcc\n"
"s_mul_i32 s63, s62, 6\ns_add_u32 s63, s63, 3\ns_lshl_b32 s64, s63, 10\nv_lshlrev_b32 v155, 4, v152\nv_add_u32 v155, s64, v155\ns_sub_u32 s64, s63, 14\ns_lshl_b32 s64, s64, 2\nv_add_u32 v156, s64, v154\n"
"v_mul_u32_u24 v157, 205, v156\nv_lshrrev_b32 v157, 11, v157\nv_mul_u32_u24 v145, 10, v157\nv_sub_u32 v156, v156, v145\nv_lshlrev_b32 v156, 5, v156\nv_bfe_u32 v145, v152, 1, 3\nv_lshl_add_u32 v157, v157, 3, v145\nv_mul_u32_u24 v157, 1280, v157\n"
"v_add_u32 v156, v156, v157\nv_and_b32 v157, 1, v152\nv_lshl_add_u32 v156, v157, 4, v156\ns_cmp_lt_u32 s63, 14\ns_cselect_b64 vcc, -1, 0\nv_cndmask_b32 v145, v156, v155, vcc\ns_mul_i32 s63, s62, 6\ns_add_u32 s63, s63, 4\n"
"s_lshl_b32 s64, s63, 10\nv_lshlrev_b32 v155, 4, v152\nv_add_u32 v155, s64, v155\ns_sub_u32 s64, s63, 14\ns_lshl_b32 s64, s64, 2\nv_add_u32 v156, s64, v154\nv_mul_u32_u24 v157, 205, v156\nv_lshrrev_b32 v157, 11, v157\n"
"v_mul_u32_u24 v146, 10, v157\nv_sub_u32 v156, v156, v146\nv_lshlrev_b32 v156, 5, v156\nv_bfe_u32 v146, v152, 1, 3\nv_lshl_add_u32 v157, v157, 3, v146\nv_mul_u32_u24 v157, 1280, v157\nv_add_u32 v156, v156, v157\nv_and_b32 v157, 1, v152\n"
"v_lshl_add_u32 v156, v157, 4, v156\ns_cmp_lt_u32 s63, 14\ns_cselect_b64 vcc, -1, 0\nv_cndmask_b32 v146, v156, v155, vcc\ns_mul_i32 s63, s62, 6\ns_add_u32 s63, s63, 5\ns_lshl_b32 s64, s63, 10\nv_lshlrev_b32 v155, 4, v152\n"
"v_add_u32 v155, s64, v155\ns_sub_u32 s64, s63, 14\ns_lshl_b32 s64, s64, 2\nv_add_u32 v156, s64, v154\nv_mul_u32_u24 v157, 205, v156\nv_lshrrev_b32 v157, 11, v157\nv_mul_u32_u24 v147, 10, v157\nv_sub_u32 v156, v156, v147\n"
"v_lshlrev_b32 v156, 5, v156\nv_bfe_u32 v147, v152, 1, 3\nv_lshl_add_u32 v157, v157, 3, v147\nv_mul_u32_u24 v157, 1280, v157\nv_add_u32 v156, v156, v157\nv_and_b32 v157, 1, v152\nv_lshl_add_u32 v156, v157, 4, v156\ns_cmp_lt_u32 s63, 14\n"
"s_cselect_b64 vcc, -1, 0\nv_cndmask_b32 v147, v156, v155, vcc\ns_nop 3\ns_add_u32 m0, s60, 0\ns_nop 0\nbuffer_load_dwordx4 v142, s[40:43], s56 offen lds\ns_add_u32 m0, s60, 1024\ns_nop 0\n"
"buffer_load_dwordx4 v143, s[40:43], s56 offen lds\ns_add_u32 m0, s60, 2048\ns_nop 0\nbuffer_load_dwordx4 v144, s[44:47], s57 offen lds\ns_add_u32 m0, s60, 3072\ns_nop 0\nbuffer_load_dwordx4 v145, s[44:47], s57 offen lds\ns_add_u32 m0, s60, 4096\n"
"s_nop 0\nbuffer_load_dwordx4 v146, s[44:47], s57 offen lds\ns_add_u32 m0, s60, 5120\ns_nop 0\nbuffer_load_dwordx4 v147, s[44:47], s57 offen lds\ns_add_u32 s56, s56, s58\ns_add_u32 s57, s57, s59\ns_add_u32 m0, s60, 24576\n"
"s_nop 0\nbuffer_load_dwordx4 v142, s[40:43], s56 offen lds\ns_add_u32 m0, s60, 25600\ns_nop 0\nbuffer_load_dwordx4 v143, s[40:43], s56 offen lds\ns_add_u32 m0, s60, 26624\ns_nop 0\nbuffer_load_dwordx4 v144, s[44:47], s57 offen lds\n"
"s_add_u32 m0, s60, 27648\ns_nop 0\nbuffer_load_dwordx4 v145, s[44:47], s57 offen lds\ns_add_u32 m0, s60, 28672\ns_nop 0\nbuffer_load_dwordx4 v146, s[44:47], s57 offen lds\ns_add_u32 m0, s60, 29696\ns_nop 0\n"
"buffer_load_dwordx4 v147, s[44:47], s57 offen lds\ns_add_u32 s56, s56, s58\ns_add_u32 s57, s57, s59\ns_add_u32 m0, s60, 49152\ns_nop 0\nbuffer_load_dwordx4 v142, s[40:43], s56 offen lds\ns_add_u32 m0, s60, 50176\ns_nop 0\n"
"buffer_load_dwordx4 v143, s[40:43], s56 offen lds\ns_add_u32 m0, s60, 51200\ns_nop 0\nbuffer_load_dwordx4 v144, s[44:47], s57 offen lds\ns_add_u32 m0, s60, 52224\ns_nop 0\nbuffer_load_dwordx4 v145, s[44:47], s57 offen lds\ns_add_u32 m0, s60, 53248\n"
"s_nop 0\nbuffer_load_dwordx4 v146, s[44:47], s57 offen lds\ns_add_u32 m0, s60, 54272\ns_nop 0\nbuffer_load_dwordx4 v147, s[44:47], s57 offen lds\ns_add_u32 s56, s56, s58\ns_add_u32 s57, s57, s59\ns_add_u32 m0, s60, 73728\n"
"s_nop 0\nbuffer_load_dwordx4 v142, s[40:43], s56 offen lds\ns_add_u32 m0, s60, 74752\ns_nop 0\nbuffer_load_dwordx4 v143, s[40:43], s56 offen lds\ns_add_u32 m0, s60, 75776\ns_nop 0\nbuffer_load_dwordx4 v144, s[44:47], s57 offen lds\n"
"s_add_u32 m0, s60, 76800\ns_nop 0\nbuffer_load_dwordx4 v145, s[44:47], s57 offen lds\ns_add_u32 m0, s60, 77824\ns_nop 0\nbuffer_load_dwordx4 v146, s[44:47], s57 offen lds\ns_add_u32 m0, s60, 78848\ns_nop 0\n"
"buffer_load_dwordx4 v147, s[44:47], s57 offen lds\ns_add_u32 s56, s56, s58\ns_add_u32 s57, s57, s59\ns_add_u32 m0, s60, 98304\ns_nop 0\nbuffer_load_dwordx4 v142, s[40:43], s56 offen lds\ns_add_u32 m0, s60, 99328\ns_nop 0\n"
"buffer_load_dwordx4 v143, s[40:43], s56 offen lds\ns_add_u32 m0, s60, 100352\ns_nop 0\nbuffer_load_dwordx4 v144, s[44:47], s57 offen lds\ns_add_u32 m0, s60, 101376\ns_nop 0\nbuffer_load_dwordx4 v145, s[44:47], s57 offen lds\ns_add_u32 m0, s60, 102400\n"
"s_nop 0\nbuffer_load_dwordx4 v146, s[44:47], s57 offen lds\ns_add_u32 m0, s60, 103424\ns_nop 0\nbuffer_load_dwordx4 v147, s[44:47], s57 offen lds\ns_add_u32 s56, s56, s58\ns_add_u32 s57, s57, s59\ns_add_u32 m0, s60, 122880\n"
"s_nop 0\nbuffer_load_dwordx4 v142, s[40:43], s56 offen lds\ns_add_u32 m0, s60, 123904\ns_nop 0\nbuffer_load_dwordx4 v143, s[40:43], s56 offen lds\ns_add_u32 m0, s60, 124928\ns_nop 0\nbuffer_load_dwordx4 v144, s[44:47], s57 offen lds\n"
"s_add_u32 m0, s60, 125952\ns_nop 0\nbuffer_load_dwordx4 v145, s[44:47], s57 offen lds\ns_add_u32 m0, s60, 126976\ns_nop 0\nbuffer_load_dwordx4 v146, s[44:47], s57 offen lds\ns_add_u32 m0, s60, 128000\ns_nop 0\n"
"buffer_load_dwordx4 v147, s[44:47], s57 offen lds\ns_add_u32 s56, s56, s58\ns_add_u32 s57, s57, s59\ns_and_b32 s63, s62, 1\ns_mul_i32 s63, s63, 7168\ns_add_u32 s63, s63, %8\nv_lshlrev_b32 v155, 4, v152\nv_add_u32 v140, s63, v155\n"
"s_lshr_b32 s63, s62, 1\ns_mul_i32 s63, s63, 1280\ns_add_u32 s63, s63, %8\ns_add_u32 s63, s63, 14336\nv_lshrrev_b32 v155, 5, v152\nv_mul_u32_u24 v155, 2560, v155\nv_bfe_u32 v156, v152, 2, 3\nv_lshl_add_u32 v155, v156, 5, v155\n"
"v_and_b32 v156, 3, v152\nv_lshl_add_u32 v155, v156, 3, v155\nv_add_u32 v141, s63, v155\nv_add_u32 v150, 49152, v140\nv_add_u32 v151, 49152, v141\nv_add_u32 v252, 98304, v140\nv_add_u32 v253, 98304, v141\ns_mul_i32 s63, s62, 20480\n"
"v_lshlrev_b32 v155, 4, v152\nv_add_u32 v149, s63, v155\ns_and_b32 s63, s62, 1\ns_mul_i32 s63, s63, 286720\ns_lshr_b32 s64, s62, 1\ns_mul_i32 s64, s64, 160\ns_add_u32 s63, s63, s64\nv_mul_u32_u24 v155, 2560, v153\n"
"v_lshl_add_u32 v155, v154, 3, v155\nv_add_u32 v148, s63, v155\ns_lshr_b32 s64, s62, 1\ns_mul_i32 s64, s64, 160\ns_add_u32 s64, s64, %8\nv_mul_u32_u24 v155, 336, v153\nv_lshl_add_u32 v155, v154, 3, v155\nv_add_u32 v254, s64, v155\n"
"s_lshl_b32 s64, s62, 6\nv_add_u32 v255, s64, v152\ns_mul_i32 s70, s65, 286720\ns_sub_u32 s71, 286720, s70\ns_waitcnt vmcnt(30)\ns_barrier\nPta 64,141,0\nPta 66,141,5120\n"
"Pta 68,141,256\nPta 70,141,5376\nPta 72,141,512\nPta 74,141,5632\nPta 76,141,768\nPta 78,141,5888\nPta 80,141,1024\nPta 82,141,6144\n"
"Pda 0,140,0\nPda 4,140,1024\nPda 8,140,2048\nPda 12,140,3072\nPda 16,140,4096\nPda 20,140,5120\nPda 24,140,6144\ns_waitcnt lgkmcnt(0)\n"
"Pzvaa 0,64,0\ns_waitcnt vmcnt(24)\ns_barrier\nPzvaa 4,68,0\nPta 84,141,24576\nPzvaa 8,72,0\nPta 86,141,29696\ns_add_u32 m0, s60, 0\n"
"Pzvaa 12,76,0\nbuffer_load_dwordx4 v142, s[40:43], s56 offen lds\nPzvaa 16,80,0\nPta 88,141,24832\nPzvaa 20,64,4\nPta 90,141,29952\nPzvaa 24,68,4\nPzvaa 28,72,4\n"
"Pta 92,141,25088\ns_add_u32 m0, s60, 1024\nPzvaa 32,76,4\nPta 94,141,30208\nbuffer_load_dwordx4 v143, s[40:43], s56 offen lds\nPzvaa 36,80,4\nPzvaa 40,64,8\nPta 96,141,25344\n"
"Pzvaa 44,68,8\nPta 98,141,30464\nPzvaa 48,72,8\ns_add_u32 m0, s60, 2048\nPzvaa 52,76,8\nPta 100,141,25600\nbuffer_load_dwordx4 v144, s[44:47], s57 offen lds\nPzvaa 56,80,8\n"
"Pta 102,141,30720\nPzvaa 60,64,12\nPzvaa 64,68,12\nPda 28,140,24576\nPzvaa 68,72,12\nPda 32,140,25600\ns_add_u32 m0, s60, 3072\nPzvaa 72,76,12\n"
"buffer_load_dwordx4 v145, s[44:47], s57 offen lds\nPzvaa 76,80,12\nPda 36,140,26624\nPzvaa 80,64,16\nPda 40,140,27648\nPzvaa 84,68,16\nPzvaa 88,72,16\nPda 44,140,28672\n"
"s_add_u32 m0, s60, 4096\nPzvaa 92,76,16\nPda 48,140,29696\nbuffer_load_dwordx4 v146, s[44:47], s57 offen lds\nPzvaa 96,80,16\nPzvaa 100,64,20\nPda 52,140,30720\nPzvaa 104,68,20\n"
"Pzvaa 108,72,20\ns_add_u32 m0, s60, 5120\nPzvaa 112,76,20\nbuffer_load_dwordx4 v147, s[44:47], s57 offen lds\nPzvaa 116,80,20\nPzvaa 120,64,24\ns_add_u32 s56, s56, s58\ns_add_u32 s57, s57, s59\n"
"Pzvaa 124,68,24\nPzvaa 128,72,24\nPzvaa 132,76,24\nPzvaa 136,80,24\ns_mov_b32 s61, 0\n.Lo_loop:\ns_waitcnt lgkmcnt(0)\nPmvaa 0,84,28\n"
"s_waitcnt vmcnt(24)\ns_barrier\nPmvaa 4,88,28\nPta 64,151,0\nPmvaa 8,92,28\nPta 66,151,5120\ns_add_u32 m0, s60, 24576\nPmvaa 12,96,28\n"
"buffer_load_dwordx4 v142, s[40:43], s56 offen lds\nPmvaa 16,100,28\nPta 68,151,256\nPmvaa 20,84,32\nPta 70,151,5376\nPmvaa 24,88,32\nPmvaa 28,92,32\nPta 72,151,512\n"
"s_add_u32 m0, s60, 25600\nPmvaa 32,96,32\nPta 74,151,5632\nbuffer_load_dwordx4 v143, s[40:43], s56 offen lds\nPmvaa 36,100,32\nPmvaa 40,84,36\nPta 76,151,768\nPmvaa 44,88,36\n"
"Pta 78,151,5888\nPmvaa 48,92,36\ns_add_u32 m0, s60, 26624\nPmvaa 52,96,36\nPta 80,151,1024\nbuffer_load_dwordx4 v144, s[44:47], s57 offen lds\nPmvaa 56,100,36\nPta 82,151,6144\n"
"Pmvaa 60,84,40\nPmvaa 64,88,40\nPda 0,150,0\nPmvaa 68,92,40\nPda 4,150,1024\ns_add_u32 m0, s60, 27648\nPmvaa 72,96,40\nbuffer_load_dwordx4 v145, s[44:47], s57 offen lds\n"
"Pmvaa 76,100,40\nPda 8,150,2048\nPmvaa 80,84,44\nPda 12,150,3072\nPmvaa 84,88,44\nPmvaa 88,92,44\nPda 16,150,4096\ns_add_u32 m0, s60, 28672\n"
"Pmvaa 92,96,44\nPda 20,150,5120\nbuffer_load_dwordx4 v146, s[44:47], s57 offen lds\nPmvaa 96,100,44\nPmvaa 100,84,48\nPda 24,150,6144\nPmvaa 104,88,48\nPmvaa 108,92,48\n"
"s_add_u32 m0, s60, 29696\nPmvaa 112,96,48\nbuffer_load_dwordx4 v147, s[44:47], s57 offen lds\nPmvaa 116,100,48\nPmvaa 120,84,52\ns_add_u32 s56, s56, s58\ns_add_u32 s57, s57, s59\nPmvaa 124,88,52\n"
"Pmvaa 128,92,52\nPmvaa 132,96,52\nPmvaa 136,100,52\ns_waitcnt lgkmcnt(0)\nPmvaa 0,64,0\ns_waitcnt vmcnt(24)\ns_barrier\nPmvaa 4,68,0\n"
"Pta 84,151,24576\nPmvaa 8,72,0\nPta 86,151,29696\ns_add_u32 m0, s60, 49152\nPmvaa 12,76,0\nbuffer_load_dwordx4 v142, s[40:43], s56 offen lds\nPmvaa 16,80,0\nPta 88,151,24832\n"
"Pmvaa 20,64,4\nPta 90,151,29952\nPmvaa 24,68,4\nPmvaa 28,72,4\nPta 92,151,25088\ns_add_u32 m0, s60, 50176\nPmvaa 32,76,4\nPta 94,151,30208\n"
"buffer_load_dwordx4 v143, s[40:43], s56 offen lds\nPmvaa 36,80,4\nPmvaa 40,64,8\nPta 96,151,25344\nPmvaa 44,68,8\nPta 98,151,30464\nPmvaa 48,72,8\ns_add_u32 m0, s60, 51200\n"
"Pmvaa 52,76,8\nPta 100,151,25600\nbuffer_load_dwordx4 v144, s[44:47], s57 offen lds\nPmvaa 56,80,8\nPta 102,151,30720\nPmvaa 60,64,12\nPmvaa 64,68,12\nPda 28,150,24576\n"
"Pmvaa 68,72,12\nPda 32,150,25600\ns_add_u32 m0, s60, 52224\nPmvaa 72,76,12\nbuffer_load_dwordx4 v145, s[44:47], s57 offen lds\nPmvaa 76,80,12\nPda 36,150,26624\nPmvaa 80,64,16\n"
"Pda 40,150,27648\nPmvaa 84,68,16\nPmvaa 88,72,16\nPda 44,150,28672\ns_add_u32 m0, s60, 53248\nPmvaa 92,76,16\nPda 48,150,29696\nbuffer_load_dwordx4 v146, s[44:47], s57 offen lds\n"
"Pmvaa 96,80,16\nPmvaa 100,64,20\nPda 52,150,30720\nPmvaa 104,68,20\nPmvaa 108,72,20\ns_add_u32 m0, s60, 54272\nPmvaa 112,76,20\nbuffer_load_dwordx4 v147, s[44:47], s57 offen lds\n"
"Pmvaa 116,80,20\nPmvaa 120,64,24\ns_add_u32 s56, s56, s58\ns_add_u32 s57, s57, s59\nPmvaa 124,68,24\nPmvaa 128,72,24\nPmvaa 132,76,24\nPmvaa 136,80,24\n"
"s_waitcnt lgkmcnt(0)\nPmvaa 0,84,28\ns_waitcnt vmcnt(24)\ns_barrier\nPmvaa 4,88,28\nPta 64,253,0\nPmvaa 8,92,28\nPta 66,253,5120\n"
"s_add_u32 m0, s60, 73728\nPmvaa 12,96,28\nbuffer_load_dwordx4 v142, s[40:43], s56 offen lds\nPmvaa 16,100,28\nPta 68,253,256\nPmvaa 20,84,32\nPta 70,253,5376\nPmvaa 24,88,32\n"
"Pmvaa 28,92,32\nPta 72,253,512\ns_add_u32 m0, s60, 74752\nPmvaa 32,96,32\nPta 74,253,5632\nbuffer_load_dwordx4 v143, s[40:43], s56 offen lds\nPmvaa 36,100,32\nPmvaa 40,84,36\n"
"Pta 76,253,768\nPmvaa 44,88,36\nPta 78,253,5888\nPmvaa 48,92,36\ns_add_u32 m0, s60, 75776\nPmvaa 52,96,36\nPta 80,253,1024\nbuffer_load_dwordx4 v144, s[44:47], s57 offen lds\n"
"Pmvaa 56,100,36\nPta 82,253,6144\nPmvaa 60,84,40\nPmvaa 64,88,40\nPda 0,252,0\nPmvaa 68,92,40\nPda 4,252,1024\ns_add_u32 m0, s60, 76800\n"
"Pmvaa 72,96,40\nbuffer_load_dwordx4 v145, s[44:47], s57 offen lds\nPmvaa 76,100,40\nPda 8,252,2048\nPmvaa 80,84,44\nPda 12,252,3072\nPmvaa 84,88,44\nPmvaa 88,92,44\n"
"Pda 16,252,4096\ns_add_u32 m0, s60, 77824\nPmvaa 92,96,44\nPda 20,252,5120\nbuffer_load_dwordx4 v146, s[44:47], s57 offen lds\nPmvaa 96,100,44\nPmvaa 100,84,48\nPda 24,252,6144\n"
"Pmvaa 104,88,48\nPmvaa 108,92,48\ns_add_u32 m0, s60, 78848\nPmvaa 112,96,48\nbuffer_load_dwordx4 v147, s[44:47], s57 offen lds\nPmvaa 116,100,48\nPmvaa 120,84,52\ns_add_u32 s56, s56, s58\n"
"s_add_u32 s57, s57, s59\nPmvaa 124,88,52\nPmvaa 128,92,52\nPmvaa 132,96,52\nPmvaa 136,100,52\ns_cmp_eq_u32 s61, 10\ns_cbranch_scc1 .Lo_done\ns_waitcnt lgkmcnt(0)\n"
"Pmvaa 0,64,0\ns_waitcnt vmcnt(24)\ns_barrier\nPmvaa 4,68,0\nPta 84,253,24576\nPmvaa 8,72,0\nPta 86,253,29696\ns_add_u32 m0, s60, 98304\n"
"Pmvaa 12,76,0\nbuffer_load_dwordx4 v142, s[40:43], s56 offen lds\nPmvaa 16,80,0\nPta 88,253,24832\nPmvaa 20,64,4\nPta 90,253,29952\nPmvaa 24,68,4\nPmvaa 28,72,4\n"
"Pta 92,253,25088\ns_add_u32 m0, s60, 99328\nPmvaa 32,76,4\nPta 94,253,30208\nbuffer_load_dwordx4 v143, s[40:43], s56 offen lds\nPmvaa 36,80,4\nPmvaa 40,64,8\nPta 96,253,25344\n"
"Pmvaa 44,68,8\nPta 98,253,30464\nPmvaa 48,72,8\ns_add_u32 m0, s60, 100352\nPmvaa 52,76,8\nPta 100,253,25600\nbuffer_load_dwordx4 v144, s[44:47], s57 offen lds\nPmvaa 56,80,8\n"
"Pta 102,253,30720\nPmvaa 60,64,12\nPmvaa 64,68,12\nPda 28,252,24576\nPmvaa 68,72,12\nPda 32,252,25600\ns_add_u32 m0, s60, 101376\nPmvaa 72,76,12\n"
"buffer_load_dwordx4 v145, s[44:47], s57 offen lds\nPmvaa 76,80,12\nPda 36,252,26624\nPmvaa 80,64,16\nPda 40,252,27648\nPmvaa 84,68,16\nPmvaa 88,72,16\nPda 44,252,28672\n"
"s_add_u32 m0, s60, 102400\nPmvaa 92,76,16\nPda 48,252,29696\nbuffer_load_dwordx4 v146, s[44:47], s57 offen lds\nPmvaa 96,80,16\nPmvaa 100,64,20\nPda 52,252,30720\nPmvaa 104,68,20\n"
"Pmvaa 108,72,20\ns_add_u32 m0, s60, 103424\nPmvaa 112,76,20\nbuffer_load_dwordx4 v147, s[44:47], s57 offen lds\nPmvaa 116,80,20\nPmvaa 120,64,24\ns_add_u32 s56, s56, s58\ns_add_u32 s57, s57, s59\n"
"Pmvaa 124,68,24\nPmvaa 128,72,24\nPmvaa 132,76,24\nPmvaa 136,80,24\ns_waitcnt lgkmcnt(0)\nPmvaa 0,84,28\ns_waitcnt vmcnt(24)\ns_barrier\n"
"Pmvaa 4,88,28\nPta 64,141,0\nPmvaa 8,92,28\nPta 66,141,5120\ns_add_u32 m0, s60, 122880\nPmvaa 12,96,28\nbuffer_load_dwordx4 v142, s[40:43], s56 offen lds\nPmvaa 16,100,28\n"
"Pta 68,141,256\nPmvaa 20,84,32\nPta 70,141,5376\nPmvaa 24,88,32\nPmvaa 28,92,32\nPta 72,141,512\ns_add_u32 m0, s60, 123904\nPmvaa 32,96,32\n"
"Pta 74,141,5632\nbuffer_load_dwordx4 v143, s[40:43], s56 offen lds\nPmvaa 36,100,32\nPmvaa 40,84,36\nPta 76,141,768\nPmvaa 44,88,36\nPta 78,141,5888\nPmvaa 48,92,36\n"
"s_add_u32 m0, s60, 124928\nPmvaa 52,96,36\nPta 80,141,1024\nbuffer_load_dwordx4 v144, s[44:47], s57 offen lds\nPmvaa 56,100,36\nPta 82,141,6144\nPmvaa 60,84,40\nPmvaa 64,88,40\n"
"Pda 0,140,0\nPmvaa 68,92,40\nPda 4,140,1024\ns_add_u32 m0, s60, 125952\nPmvaa 72,96,40\nbuffer_load_dwordx4 v145, s[44:47], s57 offen lds\nPmvaa 76,100,40\nPda 8,140,2048\n"
"Pmvaa 80,84,44\nPda 12,140,3072\nPmvaa 84,88,44\nPmvaa 88,92,44\nPda 16,140,4096\ns_add_u32 m0, s60, 126976\nPmvaa 92,96,44\nPda 20,140,5120\n"
"buffer_load_dwordx4 v146, s[44:47], s57 offen lds\nPmvaa 96,100,44\nPmvaa 100,84,48\nPda 24,140,6144\nPmvaa 104,88,48\nPmvaa 108,92,48\ns_add_u32 m0, s60, 128000\nPmvaa 112,96,48\n"
"buffer_load_dwordx4 v147, s[44:47], s57 offen lds\nPmvaa 116,100,48\nPmvaa 120,84,52\ns_add_u32 s56, s56, s58\ns_add_u32 s57, s57, s59\nPmvaa 124,88,52\nPmvaa 128,92,52\nPmvaa 132,96,52\n"
"Pmvaa 136,100,52\ns_waitcnt lgkmcnt(0)\nPmvaa 0,64,0\ns_waitcnt vmcnt(24)\ns_barrier\nPmvaa 4,68,0\nPta 84,141,24576\nPmvaa 8,72,0\n"
"Pta 86,141,29696\ns_add_u32 m0, s60, 0\nPmvaa 12,76,0\nbuffer_load_dwordx4 v142, s[40:43], s56 offen lds\nPmvaa 16,80,0\nPta 88,141,24832\nPmvaa 20,64,4\nPta 90,141,29952\n"
"Pmvaa 24,68,4\nPmvaa 28,72,4\nPta 92,141,25088\ns_add_u32 m0, s60, 1024\nPmvaa 32,76,4\nPta 94,141,30208\nbuffer_load_dwordx4 v143, s[40:43], s56 offen lds\nPmvaa 36,80,4\n"
"Pmvaa 40,64,8\nPta 96,141,25344\nPmvaa 44,68,8\nPta 98,141,30464\nPmvaa 48,72,8\ns_add_u32 m0, s60, 2048\nPmvaa 52,76,8\nPta 100,141,25600\n"
"buffer_load_dwordx4 v144, s[44:47], s57 offen lds\nPmvaa 56,80,8\nPta 102,141,30720\nPmvaa 60,64,12\nPmvaa 64,68,12\nPda 28,140,24576\nPmvaa 68,72,12\nPda 32,140,25600\n"
"s_add_u32 m0, s60, 3072\nPmvaa 72,76,12\nbuffer_load_dwordx4 v145, s[44:47], s57 offen lds\nPmvaa 76,80,12\nPda 36,140,26624\nPmvaa 80,64,16\nPda 40,140,27648\nPmvaa 84,68,16\n"
"Pmvaa 88,72,16\nPda 44,140,28672\ns_add_u32 m0, s60, 4096\nPmvaa 92,76,16\nPda 48,140,29696\nbuffer_load_dwordx4 v146, s[44:47], s57 offen lds\nPmvaa 96,80,16\nPmvaa 100,64,20\n"
"Pda 52,140,30720\nPmvaa 104,68,20\nPmvaa 108,72,20\ns_add_u32 m0, s60, 5120\nPmvaa 112,76,20\nbuffer_load_dwordx4 v147, s[44:47], s57 offen lds\nPmvaa 116,80,20\nPmvaa 120,64,24\n"
"s_add_u32 s56, s56, s58\ns_add_u32 s57, s57, s59\nPmvaa 124,68,24\nPmvaa 128,72,24\nPmvaa 132,76,24\nPmvaa 136,80,24\ns_add_u32 s61, s61, 1\ns_branch .Lo_loop\n"
".Lo_done:\ns_nop 7\ns_nop 7\ns_mul_i32 s68, s65, 81920\ns_sub_u32 s69, 81920, s68\ns_lshl_b32 s64, s65, 6\nv_mov_b32 v152, s64\nv_xor_b32 v154, 64, v152\n"
"v_mov_b32 v153, 1\ns_and_b32 s63, s62, 1\ns_xor_b32 s63, s63, s65\ns_cmp_eq_u32 s63, 1\ns_cbranch_scc1 .Lo_r1\n.Lo_r0:\nPk 80,80,81\nPk 81,82,83\n"
"Pk 82,84,85\nPk 83,86,87\ns_add_u32 s63, s68, 0\nbuffer_store_dwordx4 v[80:83], v149, s[48:51], s63 offen offset:0 sc1\nPk 88,88,89\nPk 89,90,91\nPk 90,92,93\nPk 91,94,95\n"
"buffer_store_dwordx4 v[88:91], v149, s[48:51], s63 offen offset:1024 sc1\nPk 96,96,97\nPk 97,98,99\nPk 98,100,101\nPk 99,102,103\nbuffer_store_dwordx4 v[96:99], v149, s[48:51], s63 offen offset:2048 sc1\nPk 104,104,105\nPk 105,106,107\n"
"Pk 106,108,109\nPk 107,110,111\nbuffer_store_dwordx4 v[104:107], v149, s[48:51], s63 offen offset:3072 sc1\nPk 112,112,113\nPk 113,114,115\nPk 114,116,117\nPk 115,118,119\ns_add_u32 s63, s68, 4096\n"
"buffer_store_dwordx4 v[112:115], v149, s[48:51], s63 offen offset:0 sc1\nPk 120,120,121\nPk 121,122,123\nPk 122,124,125\nPk 123,126,127\nbuffer_store_dwordx4 v[120:123], v149, s[48:51], s63 offen offset:1024 sc1\nPk 128,128,129\nPk 129,130,131\n"
"Pk 130,132,133\nPk 131,134,135\nbuffer_store_dwordx4 v[128:131], v149, s[48:51], s63 offen offset:2048 sc1\nPk 136,136,137\nPk 137,138,139\nbuffer_store_dwordx4 v[136:139], v149, s[48:51], s63 offen offset:3072 sc1\nv_add_u32 v156, 0, v255\nv_mul_u32_u24 v157, 3277, v156\n"
"v_lshrrev_b32 v157, 16, v157\nv_mul_u32_u24 v158, 20, v157\nv_sub_u32 v158, v156, v158\nv_lshlrev_b32 v158, 4, v158\nv_mul_u32_u24 v159, 336, v157\nv_add3_u32 v212, v159, v158, %8\nv_cmp_gt_u32 vcc, 64, v157\nv_mov_b32 v159, s71\n"
"v_mov_b32 v156, s70\ns_nop 0\nv_cndmask_b32 v159, v159, v156, vcc\nv_mul_u32_u24 v157, 2560, v157\nv_add3_u32 v222, v157, v158, v159\nv_add_u32 v156, 256, v255\nv_mul_u32_u24 v157, 3277, v156\nv_lshrrev_b32 v157, 16, v157\n"
"v_mul_u32_u24 v158, 20, v157\nv_sub_u32 v158, v156, v158\nv_lshlrev_b32 v158, 4, v158\nv_mul_u32_u24 v159, 336, v157\nv_add3_u32 v213, v159, v158, %8\nv_cmp_gt_u32 vcc, 64, v157\nv_mov_b32 v159, s71\nv_mov_b32 v156, s70\n"
"s_nop 0\nv_cndmask_b32 v159, v159, v156, vcc\nv_mul_u32_u24 v157, 2560, v157\nv_add3_u32 v223, v157, v158, v159\nv_add_u32 v156, 512, v255\nv_mul_u32_u24 v157, 3277, v156\nv_lshrrev_b32 v157, 16, v157\nv_mul_u32_u24 v158, 20, v157\n"
"v_sub_u32 v158, v156, v158\nv_lshlrev_b32 v158, 4, v158\nv_mul_u32_u24 v159, 336, v157\nv_add3_u32 v214, v159, v158, %8\nv_cmp_gt_u32 vcc, 64, v157\nv_mov_b32 v159, s71\nv_mov_b32 v156, s70\ns_nop 0\n"
"v_cndmask_b32 v159, v159, v156, vcc\nv_mul_u32_u24 v157, 2560, v157\nv_add3_u32 v224, v157, v158, v159\nv_add_u32 v156, 768, v255\nv_mul_u32_u24 v157, 3277, v156\nv_lshrrev_b32 v157, 16, v157\nv_mul_u32_u24 v158, 20, v157\nv_sub_u32 v158, v156, v158\n"
"v_lshlrev_b32 v158, 4, v158\nv_mul_u32_u24 v159, 336, v157\nv_add3_u32 v215, v159, v158, %8\nv_cmp_gt_u32 vcc, 64, v157\nv_mov_b32 v159, s71\nv_mov_b32 v156, s70\ns_nop 0\nv_cndmask_b32 v159, v159, v156, vcc\n"
"v_mul_u32_u24 v157, 2560, v157\nv_add3_u32 v225, v157, v158, v159\nv_add_u32 v156, 1024, v255\nv_mul_u32_u24 v157, 3277, v156\nv_lshrrev_b32 v157, 16, v157\nv_mul_u32_u24 v158, 20, v157\nv_sub_u32 v158, v156, v158\nv_lshlrev_b32 v158, 4, v158\n"
"v_mul_u32_u24 v159, 336, v157\nv_add3_u32 v216, v159, v158, %8\nv_cmp_gt_u32 vcc, 64, v157\nv_mov_b32 v159, s71\nv_mov_b32 v156, s70\ns_nop 0\nv_cndmask_b32 v159, v159, v156, vcc\nv_mul_u32_u24 v157, 2560, v157\n"
"v_add3_u32 v226, v157, v158, v159\nv_add_u32 v156, 1280, v255\nv_mul_u32_u24 v157, 3277, v156\nv_lshrrev_b32 v157, 16, v157\nv_mul_u32_u24 v158, 20, v157\nv_sub_u32 v158, v156, v158\nv_lshlrev_b32 v158, 4, v158\nv_mul_u32_u24 v159, 336, v157\n"
"v_add3_u32 v217, v159, v158, %8\nv_cmp_gt_u32 vcc, 64, v157\nv_mov_b32 v159, s71\nv_mov_b32 v156, s70\ns_nop 0\nv_cndmask_b32 v159, v159, v156, vcc\nv_mul_u32_u24 v157, 2560, v157\nv_add3_u32 v227, v157, v158, v159\n"
"v_add_u32 v156, 1536, v255\nv_mul_u32_u24 v157, 3277, v156\nv_lshrrev_b32 v157, 16, v157\nv_mul_u32_u24 v158, 20, v157\nv_sub_u32 v158, v156, v158\nv_lshlrev_b32 v158, 4, v158\nv_mul_u32_u24 v159, 336, v157\nv_add3_u32 v218, v159, v158, %8\n"
"v_cmp_gt_u32 vcc, 64, v157\nv_mov_b32 v159, s71\nv_mov_b32 v156, s70\ns_nop 0\nv_cndmask_b32 v159, v159, v156, vcc\nv_mul_u32_u24 v157, 2560, v157\nv_add3_u32 v228, v157, v158, v159\nv_add_u32 v156, 1792, v255\n"
"v_mul_u32_u24 v157, 3277, v156\nv_lshrrev_b32 v157, 16, v157\nv_mul_u32_u24 v158, 20, v157\nv_sub_u32 v158, v156, v158\nv_lshlrev_b32 v158, 4, v158\nv_mul_u32_u24 v159, 336, v157\nv_add3_u32 v219, v159, v158, %8\nv_cmp_gt_u32 vcc, 64, v157\n"
"v_mov_b32 v159, s71\nv_mov_b32 v156, s70\ns_nop 0\nv_cndmask_b32 v159, v159, v156, vcc\nv_mul_u32_u24 v157, 2560, v157\nv_add3_u32 v229, v157, v158, v159\nv_add_u32 v156, 2048, v255\nv_mul_u32_u24 v157, 3277, v156\n"
"v_lshrrev_b32 v157, 16, v157\nv_mul_u32_u24 v158, 20, v157\nv_sub_u32 v158, v156, v158\nv_lshlrev_b32 v158, 4, v158\nv_mul_u32_u24 v159, 336, v157\nv_add3_u32 v220, v159, v158, %8\nv_cmp_gt_u32 vcc, 64, v157\nv_mov_b32 v159, s71\n"
"v_mov_b32 v156, s70\ns_nop 0\nv_cndmask_b32 v159, v159, v156, vcc\nv_mul_u32_u24 v157, 2560, v157\nv_add3_u32 v230, v157, v158, v159\nv_cmp_lt_u32 vcc, 191, v255\nv_mov_b32 v159, 0x7f000000\ns_nop 0\n"
"v_cndmask_b32 v230, v230, v159, vcc\ns_waitcnt vmcnt(0) lgkmcnt(0)\ns_barrier\ns_cmp_lg_u32 s62, 0\ns_cbranch_scc1 .Lo_r0_np\ns_mov_b64 exec, 1\nglobal_store_dword v152, v153, s[66:67] sc1\ns_mov_b64 exec, -1\n"
"s_mov_b32 s64, 0\n.Lo_r0_poll:\nglobal_load_dword v155, v154, s[66:67] sc1\ns_waitcnt vmcnt(0)\ns_nop 0\nv_readfirstlane_b32 s63, v155\ns_nop 3\ns_cmp_eq_u32 s63, 1\n"
"s_cbranch_scc1 .Lo_r0_np\ns_sleep 1\ns_add_u32 s64, s64, 1\ns_cmp_lt_u32 s64, 0x400000\ns_cbranch_scc1 .Lo_r0_poll\n.Lo_r0_np:\ns_barrier\ns_add_u32 s63, s69, 0\n"
"buffer_load_dwordx4 v[160:163], v149, s[48:51], s63 offen offset:0 sc1\nbuffer_load_dwordx4 v[164:167], v149, s[48:51], s63 offen offset:1024 sc1\nbuffer_load_dwordx4 v[168:171], v149, s[48:51], s63 offen offset:2048 sc1\nbuffer_load_dwordx4 v[172:175], v149, s[48:51], s63 offen offset:3072 sc1\ns_add_u32 s63, s69, 4096\nbuffer_load_dwordx4 v[176:179], v149, s[48:51], s63 offen offset:0 sc1\nbuffer_load_dwordx4 v[180:183], v149, s[48:51], s63 offen offset:1024 sc1\nbuffer_load_dwordx4 v[184:187], v149, s[48:51], s63 offen offset:2048 sc1\n"
"buffer_load_dwordx4 v[188:191], v149, s[48:51], s63 offen offset:3072 sc1\ns_add_u32 s63, s69, 8192\nbuffer_load_dwordx4 v[192:195], v149, s[48:51], s63 offen offset:0 sc1\nbuffer_load_dwordx4 v[196:199], v149, s[48:51], s63 offen offset:1024 sc1\ns_waitcnt vmcnt(9)\nv_lshlrev_b32 v156, 16, v160\nv_and_b32 v157, 0xffff0000, v160\nPa 0,0,156\n"
"Pa 1,1,157\nv_lshlrev_b32 v156, 16, v161\nv_and_b32 v157, 0xffff0000, v161\nPa 2,2,156\nPa 3,3,157\nPk 0,0,1\nPk 1,2,3\nds_write_b64 v254, v[0:1] offset:0\n"
"v_lshlrev_b32 v156, 16, v162\nv_and_b32 v157, 0xffff0000, v162\nPa 4,4,156\nPa 5,5,157\nv_lshlrev_b32 v156, 16, v163\nv_and_b32 v157, 0xffff0000, v163\nPa 6,6,156\nPa 7,7,157\n"
"Pk 4,4,5\nPk 5,6,7\nds_write_b64 v254, v[4:5] offset:32\ns_waitcnt vmcnt(8)\nv_lshlrev_b32 v156, 16, v164\nv_and_b32 v157, 0xffff0000, v164\nPa 8,8,156\nPa 9,9,157\n"
"v_lshlrev_b32 v156, 16, v165\nv_and_b32 v157, 0xffff0000, v165\nPa 10,10,156\nPa 11,11,157\nPk 8,8,9\nPk 9,10,11\nds_write_b64 v254, v[8:9] offset:64\nv_lshlrev_b32 v156, 16, v166\n"
"v_and_b32 v157, 0xffff0000, v166\nPa 12,12,156\nPa 13,13,157\nv_lshlrev_b32 v156, 16, v167\nv_and_b32 v157, 0xffff0000, v167\nPa 14,14,156\nPa 15,15,157\nPk 12,12,13\n"
"Pk 13,14,15\nds_write_b64 v254, v[12:13] offset:96\ns_waitcnt vmcnt(7)\nv_lshlrev_b32 v156, 16, v168\nv_and_b32 v157, 0xffff0000, v168\nPa 16,16,156\nPa 17,17,157\nv_lshlrev_b32 v156, 16, v169\n"
"v_and_b32 v157, 0xffff0000, v169\nPa 18,18,156\nPa 19,19,157\nPk 16,16,17\nPk 17,18,19\nds_write_b64 v254, v[16:17] offset:128\nv_lshlrev_b32 v156, 16, v170\nv_and_b32 v157, 0xffff0000, v170\n"
"Pa 20,20,156\nPa 21,21,157\nv_lshlrev_b32 v156, 16, v171\nv_and_b32 v157, 0xffff0000, v171\nPa 22,22,156\nPa 23,23,157\nPk 20,20,21\nPk 21,22,23\n"
"ds_write_b64 v254, v[20:21] offset:5376\ns_waitcnt vmcnt(6)\nv_lshlrev_b32 v156, 16, v172\nv_and_b32 v157, 0xffff0000, v172\nPa 24,24,156\nPa 25,25,157\nv_lshlrev_b32 v156, 16, v173\nv_and_b32 v157, 0xffff0000, v173\n"
"Pa 26,26,156\nPa 27,27,157\nPk 24,24,25\nPk 25,26,27\nds_write_b64 v254, v[24:25] offset:5408\nv_lshlrev_b32 v156, 16, v174\nv_and_b32 v157, 0xffff0000, v174\nPa 28,28,156\n"
"Pa 29,29,157\nv_lshlrev_b32 v156, 16, v175\nv_and_b32 v157, 0xffff0000, v175\nPa 30,30,156\nPa 31,31,157\nPk 28,28,29\nPk 29,30,31\nds_write_b64 v254, v[28:29] offset:5440\n"
"s_waitcnt vmcnt(5)\nv_lshlrev_b32 v156, 16, v176\nv_and_b32 v157, 0xffff0000, v176\nPa 32,32,156\nPa 33,33,157\nv_lshlrev_b32 v156, 16, v177\nv_and_b32 v157, 0xffff0000, v177\nPa 34,34,156\n"
"Pa 35,35,157\nPk 32,32,33\nPk 33,34,35\nds_write_b64 v254, v[32:33] offset:5472\nv_lshlrev_b32 v156, 16, v178\nv_and_b32 v157, 0xffff0000, v178\nPa 36,36,156\nPa 37,37,157\n"
"v_lshlrev_b32 v156, 16, v179\nv_and_b32 v157, 0xffff0000, v179\nPa 38,38,156\nPa 39,39,157\nPk 36,36,37\nPk 37,38,39\nds_write_b64 v254, v[36:37] offset:5504\ns_waitcnt vmcnt(4)\n"
"v_lshlrev_b32 v156, 16, v180\nv_and_b32 v157, 0xffff0000, v180\nPa 40,40,156\nPa 41,41,157\nv_lshlrev_b32 v156, 16, v181\nv_and_b32 v157, 0xffff0000, v181\nPa 42,42,156\nPa 43,43,157\n"
"Pk 40,40,41\nPk 41,42,43\nds_write_b64 v254, v[40:41] offset:10752\nv_lshlrev_b32 v156, 16, v182\nv_and_b32 v157, 0xffff0000, v182\nPa 44,44,156\nPa 45,45,157\nv_lshlrev_b32 v156, 16, v183\n"
"v_and_b32 v157, 0xffff0000, v183\nPa 46,46,156\nPa 47,47,157\nPk 44,44,45\nPk 45,46,47\nds_write_b64 v254, v[44:45] offset:10784\ns_waitcnt vmcnt(3)\nv_lshlrev_b32 v156, 16, v184\n"
"v_and_b32 v157, 0xffff0000, v184\nPa 48,48,156\nPa 49,49,157\nv_lshlrev_b32 v156, 16, v185\nv_and_b32 v157, 0xffff0000, v185\nPa 50,50,156\nPa 51,51,157\nPk 48,48,49\n"
"Pk 49,50,51\nds_write_b64 v254, v[48:49] offset:10816\nv_lshlrev_b32 v156, 16, v186\nv_and_b32 v157, 0xffff0000, v186\nPa 52,52,156\nPa 53,53,157\nv_lshlrev_b32 v156, 16, v187\nv_and_b32 v157, 0xffff0000, v187\n"
"Pa 54,54,156\nPa 55,55,157\nPk 52,52,53\nPk 53,54,55\nds_write_b64 v254, v[52:53] offset:10848\ns_waitcnt vmcnt(2)\nv_lshlrev_b32 v156, 16, v188\nv_and_b32 v157, 0xffff0000, v188\n"
"Pa 56,56,156\nPa 57,57,157\nv_lshlrev_b32 v156, 16, v189\nv_and_b32 v157, 0xffff0000, v189\nPa 58,58,156\nPa 59,59,157\nPk 56,56,57\nPk 57,58,59\n"
"ds_write_b64 v254, v[56:57] offset:10880\nv_lshlrev_b32 v156, 16, v190\nv_and_b32 v157, 0xffff0000, v190\nPa 60,60,156\nPa 61,61,157\nv_lshlrev_b32 v156, 16, v191\nv_and_b32 v157, 0xffff0000, v191\nPa 62,62,156\n"
"Pa 63,63,157\nPk 60,60,61\nPk 61,62,63\nds_write_b64 v254, v[60:61] offset:16128\ns_waitcnt vmcnt(1)\nv_lshlrev_b32 v156, 16, v192\nv_and_b32 v157, 0xffff0000, v192\nPa 64,64,156\n"
"Pa 65,65,157\nv_lshlrev_b32 v156, 16, v193\nv_and_b32 v157, 0xffff0000, v193\nPa 66,66,156\nPa 67,67,157\nPk 64,64,65\nPk 65,66,67\nds_write_b64 v254, v[64:65] offset:16160\n"
"v_lshlrev_b32 v156, 16, v194\nv_and_b32 v157, 0xffff0000, v194\nPa 68,68,156\nPa 69,69,157\nv_lshlrev_b32 v156, 16, v195\nv_and_b32 v157, 0xffff0000, v195\nPa 70,70,156\nPa 71,71,157\n"
"Pk 68,68,69\nPk 69,70,71\nds_write_b64 v254, v[68:69] offset:16192\ns_waitcnt vmcnt(0)\nv_lshlrev_b32 v156, 16, v196\nv_and_b32 v157, 0xffff0000, v196\nPa 72,72,156\nPa 73,73,157\n"
"v_lshlrev_b32 v156, 16, v197\nv_and_b32 v157, 0xffff0000, v197\nPa 74,74,156\nPa 75,75,157\nPk 72,72,73\nPk 73,74,75\nds_write_b64 v254, v[72:73] offset:16224\nv_lshlrev_b32 v156, 16, v198\n"
"v_and_b32 v157, 0xffff0000, v198\nPa 76,76,156\nPa 77,77,157\nv_lshlrev_b32 v156, 16, v199\nv_and_b32 v157, 0xffff0000, v199\nPa 78,78,156\nPa 79,79,157\nPk 76,76,77\n"
"Pk 77,78,79\nds_write_b64 v254, v[76:77] offset:16256\ns_branch .Lo_fin\n.Lo_r1:\nPk 0,0,1\nPk 1,2,3\nPk 2,4,5\nPk 3,6,7\n"
"s_add_u32 s63, s68, 0\nbuffer_store_dwordx4 v[0:3], v149, s[48:51], s63 offen offset:0 sc1\nPk 8,8,9\nPk 9,10,11\nPk 10,12,13\nPk 11,14,15\nbuffer_store_dwordx4 v[8:11], v149, s[48:51], s63 offen offset:1024 sc1\nPk 16,16,17\n"
"Pk 17,18,19\nPk 18,20,21\nPk 19,22,23\nbuffer_store_dwordx4 v[16:19], v149, s[48:51], s63 offen offset:2048 sc1\nPk 24,24,25\nPk 25,26,27\nPk 26,28,29\nPk 27,30,31\n"
"buffer_store_dwordx4 v[24:27], v149, s[48:51], s63 offen offset:3072 sc1\nPk 32,32,33\nPk 33,34,35\nPk 34,36,37\nPk 35,38,39\ns_add_u32 s63, s68, 4096\nbuffer_store_dwordx4 v[32:35], v149, s[48:51], s63 offen offset:0 sc1\nPk 40,40,41\n"
"Pk 41,42,43\nPk 42,44,45\nPk 43,46,47\nbuffer_store_dwordx4 v[40:43], v149, s[48:51], s63 offen offset:1024 sc1\nPk 48,48,49\nPk 49,50,51\nPk 50,52,53\nPk 51,54,55\n"
"buffer_store_dwordx4 v[48:51], v149, s[48:51], s63 offen offset:2048 sc1\nPk 56,56,57\nPk 57,58,59\nPk 58,60,61\nPk 59,62,63\nbuffer_store_dwordx4 v[56:59], v149, s[48:51], s63 offen offset:3072 sc1\nPk 64,64,65\nPk 65,66,67\n"
"Pk 66,68,69\nPk 67,70,71\ns_add_u32 s63, s68, 8192\nbuffer_store_dwordx4 v[64:67], v149, s[48:51], s63 offen offset:0 sc1\nPk 72,72,73\nPk 73,74,75\nPk 74,76,77\nPk 75,78,79\n"
"buffer_store_dwordx4 v[72:75], v149, s[48:51], s63 offen offset:1024 sc1\nv_add_u32 v156, 0, v255\nv_mul_u32_u24 v157, 3277, v156\nv_lshrrev_b32 v157, 16, v157\nv_mul_u32_u24 v158, 20, v157\nv_sub_u32 v158, v156, v158\nv_lshlrev_b32 v158, 4, v158\nv_mul_u32_u24 v159, 336, v157\n"
"v_add3_u32 v212, v159, v158, %8\nv_cmp_gt_u32 vcc, 64, v157\nv_mov_b32 v159, s71\nv_mov_b32 v156, s70\ns_nop 0\nv_cndmask_b32 v159, v159, v156, vcc\nv_mul_u32_u24 v157, 2560, v157\nv_add3_u32 v222, v157, v158, v159\n"
"v_add_u32 v156, 256, v255\nv_mul_u32_u24 v157, 3277, v156\nv_lshrrev_b32 v157, 16, v157\nv_mul_u32_u24 v158, 20, v157\nv_sub_u32 v158, v156, v158\nv_lshlrev_b32 v158, 4, v158\nv_mul_u32_u24 v159, 336, v157\nv_add3_u32 v213, v159, v158, %8\n"
"v_cmp_gt_u32 vcc, 64, v157\nv_mov_b32 v159, s71\nv_mov_b32 v156, s70\ns_nop 0\nv_cndmask_b32 v159, v159, v156, vcc\nv_mul_u32_u24 v157, 2560, v157\nv_add3_u32 v223, v157, v158, v159\nv_add_u32 v156, 512, v255\n"
"v_mul_u32_u24 v157, 3277, v156\nv_lshrrev_b32 v157, 16, v157\nv_mul_u32_u24 v158, 20, v157\nv_sub_u32 v158, v156, v158\nv_lshlrev_b32 v158, 4, v158\nv_mul_u32_u24 v159, 336, v157\nv_add3_u32 v214, v159, v158, %8\nv_cmp_gt_u32 vcc, 64, v157\n"
"v_mov_b32 v159, s71\nv_mov_b32 v156, s70\ns_nop 0\nv_cndmask_b32 v159, v159, v156, vcc\nv_mul_u32_u24 v157, 2560, v157\nv_add3_u32 v224, v157, v158, v159\nv_add_u32 v156, 768, v255\nv_mul_u32_u24 v157, 3277, v156\n"
"v_lshrrev_b32 v157, 16, v157\nv_mul_u32_u24 v158, 20, v157\nv_sub_u32 v158, v156, v158\nv_lshlrev_b32 v158, 4, v158\nv_mul_u32_u24 v159, 336, v157\nv_add3_u32 v215, v159, v158, %8\nv_cmp_gt_u32 vcc, 64, v157\nv_mov_b32 v159, s71\n"
"v_mov_b32 v156, s70\ns_nop 0\nv_cndmask_b32 v159, v159, v156, vcc\nv_mul_u32_u24 v157, 2560, v157\nv_add3_u32 v225, v157, v158, v159\nv_add_u32 v156, 1024, v255\nv_mul_u32_u24 v157, 3277, v156\nv_lshrrev_b32 v157, 16, v157\n"
"v_mul_u32_u24 v158, 20, v157\nv_sub_u32 v158, v156, v158\nv_lshlrev_b32 v158, 4, v158\nv_mul_u32_u24 v159, 336, v157\nv_add3_u32 v216, v159, v158, %8\nv_cmp_gt_u32 vcc, 64, v157\nv_mov_b32 v159, s71\nv_mov_b32 v156, s70\n"
"s_nop 0\nv_cndmask_b32 v159, v159, v156, vcc\nv_mul_u32_u24 v157, 2560, v157\nv_add3_u32 v226, v157, v158, v159\nv_add_u32 v156, 1280, v255\nv_mul_u32_u24 v157, 3277, v156\nv_lshrrev_b32 v157, 16, v157\nv_mul_u32_u24 v158, 20, v157\n"
"v_sub_u32 v158, v156, v158\nv_lshlrev_b32 v158, 4, v158\nv_mul_u32_u24 v159, 336, v157\nv_add3_u32 v217, v159, v158, %8\nv_cmp_gt_u32 vcc, 64, v157\nv_mov_b32 v159, s71\nv_mov_b32 v156, s70\ns_nop 0\n"
"v_cndmask_b32 v159, v159, v156, vcc\nv_mul_u32_u24 v157, 2560, v157\nv_add3_u32 v227, v157, v158, v159\nv_add_u32 v156, 1536, v255\nv_mul_u32_u24 v157, 3277, v156\nv_lshrrev_b32 v157, 16, v157\nv_mul_u32_u24 v158, 20, v157\nv_sub_u32 v158, v156, v158\n"
"v_lshlrev_b32 v158, 4, v158\nv_mul_u32_u24 v159, 336, v157\nv_add3_u32 v218, v159, v158, %8\nv_cmp_gt_u32 vcc, 64, v157\nv_mov_b32 v159, s71\nv_mov_b32 v156, s70\ns_nop 0\nv_cndmask_b32 v159, v159, v156, vcc\n"
"v_mul_u32_u24 v157, 2560, v157\nv_add3_u32 v228, v157, v158, v159\nv_add_u32 v156, 1792, v255\nv_mul_u32_u24 v157, 3277, v156\nv_lshrrev_b32 v157, 16, v157\nv_mul_u32_u24 v158, 20, v157\nv_sub_u32 v158, v156, v158\nv_lshlrev_b32 v158, 4, v158\n"
"v_mul_u32_u24 v159, 336, v157\nv_add3_u32 v219, v159, v158, %8\nv_cmp_gt_u32 vcc, 64, v157\nv_mov_b32 v159, s71\nv_mov_b32 v156, s70\ns_nop 0\nv_cndmask_b32 v159, v159, v156, vcc\nv_mul_u32_u24 v157, 2560, v157\n"
"v_add3_u32 v229, v157, v158, v159\nv_add_u32 v156, 2048, v255\nv_mul_u32_u24 v157, 3277, v156\nv_lshrrev_b32 v157, 16, v157\nv_mul_u32_u24 v158, 20, v157\nv_sub_u32 v158, v156, v158\nv_lshlrev_b32 v158, 4, v158\nv_mul_u32_u24 v159, 336, v157\n"
"v_add3_u32 v220, v159, v158, %8\nv_cmp_gt_u32 vcc, 64, v157\nv_mov_b32 v159, s71\nv_mov_b32 v156, s70\ns_nop 0\nv_cndmask_b32 v159, v159, v156, vcc\nv_mul_u32_u24 v157, 2560, v157\nv_add3_u32 v230, v157, v158, v159\n"
"v_cmp_lt_u32 vcc, 191, v255\nv_mov_b32 v159, 0x7f000000\ns_nop 0\nv_cndmask_b32 v230, v230, v159, vcc\ns_waitcnt vmcnt(0) lgkmcnt(0)\ns_barrier\ns_cmp_lg_u32 s62, 0\ns_cbranch_scc1 .Lo_r1_np\n"
"s_mov_b64 exec, 1\nglobal_store_dword v152, v153, s[66:67] sc1\ns_mov_b64 exec, -1\ns_mov_b32 s64, 0\n.Lo_r1_poll:\nglobal_load_dword v155, v154, s[66:67] sc1\ns_waitcnt vmcnt(0)\ns_nop 0\n"
"v_readfirstlane_b32 s63, v155\ns_nop 3\ns_cmp_eq_u32 s63, 1\ns_cbranch_scc1 .Lo_r1_np\ns_sleep 1\ns_add_u32 s64, s64, 1\ns_cmp_lt_u32 s64, 0x400000\ns_cbranch_scc1 .Lo_r1_poll\n"
".Lo_r1_np:\ns_barrier\ns_add_u32 s63, s69, 0\nbuffer_load_dwordx4 v[160:163], v149, s[48:51], s63 offen offset:0 sc1\nbuffer_load_dwordx4 v[164:167], v149, s[48:51], s63 offen offset:1024 sc1\nbuffer_load_dwordx4 v[168:171], v149, s[48:51], s63 offen offset:2048 sc1\nbuffer_load_dwordx4 v[172:175], v149, s[48:51], s63 offen offset:3072 sc1\ns_add_u32 s63, s69, 4096\n"
"buffer_load_dwordx4 v[176:179], v149, s[48:51], s63 offen offset:0 sc1\nbuffer_load_dwordx4 v[180:183], v149, s[48:51], s63 offen offset:1024 sc1\nbuffer_load_dwordx4 v[184:187], v149, s[48:51], s63 offen offset:2048 sc1\nbuffer_load_dwordx4 v[188:191], v149, s[48:51], s63 offen offset:3072 sc1\ns_waitcnt vmcnt(7)\nv_lshlrev_b32 v156, 16, v160\nv_and_b32 v157, 0xffff0000, v160\nPa 80,80,156\n"
"Pa 81,81,157\nv_lshlrev_b32 v156, 16, v161\nv_and_b32 v157, 0xffff0000, v161\nPa 82,82,156\nPa 83,83,157\nPk 80,80,81\nPk 81,82,83\nds_write_b64 v254, v[80:81] offset:21504\n"
"v_lshlrev_b32 v156, 16, v162\nv_and_b32 v157, 0xffff0000, v162\nPa 84,84,156\nPa 85,85,157\nv_lshlrev_b32 v156, 16, v163\nv_and_b32 v157, 0xffff0000, v163\nPa 86,86,156\nPa 87,87,157\n"
"Pk 84,84,85\nPk 85,86,87\nds_write_b64 v254, v[84:85] offset:21536\ns_waitcnt vmcnt(6)\nv_lshlrev_b32 v156, 16, v164\nv_and_b32 v157, 0xffff0000, v164\nPa 88,88,156\nPa 89,89,157\n"
"v_lshlrev_b32 v156, 16, v165\nv_and_b32 v157, 0xffff0000, v165\nPa 90,90,156\nPa 91,91,157\nPk 88,88,89\nPk 89,90,91\nds_write_b64 v254, v[88:89] offset:21568\nv_lshlrev_b32 v156, 16, v166\n"
"v_and_b32 v157, 0xffff0000, v166\nPa 92,92,156\nPa 93,93,157\nv_lshlrev_b32 v156, 16, v167\nv_and_b32 v157, 0xffff0000, v167\nPa 94,94,156\nPa 95,95,157\nPk 92,92,93\n"
"Pk 93,94,95\nds_write_b64 v254, v[92:93] offset:21600\ns_waitcnt vmcnt(5)\nv_lshlrev_b32 v156, 16, v168\nv_and_b32 v157, 0xffff0000, v168\nPa 96,96,156\nPa 97,97,157\nv_lshlrev_b32 v156, 16, v169\n"
"v_and_b32 v157, 0xffff0000, v169\nPa 98,98,156\nPa 99,99,157\nPk 96,96,97\nPk 97,98,99\nds_write_b64 v254, v[96:97] offset:21632\nv_lshlrev_b32 v156, 16, v170\nv_and_b32 v157, 0xffff0000, v170\n"
"Pa 100,100,156\nPa 101,101,157\nv_lshlrev_b32 v156, 16, v171\nv_and_b32 v157, 0xffff0000, v171\nPa 102,102,156\nPa 103,103,157\nPk 100,100,101\nPk 101,102,103\n"
"ds_write_b64 v254, v[100:101] offset:26880\ns_waitcnt vmcnt(4)\nv_lshlrev_b32 v156, 16, v172\nv_and_b32 v157, 0xffff0000, v172\nPa 104,104,156\nPa 105,105,157\nv_lshlrev_b32 v156, 16, v173\nv_and_b32 v157, 0xffff0000, v173\n"
"Pa 106,106,156\nPa 107,107,157\nPk 104,104,105\nPk 105,106,107\nds_write_b64 v254, v[104:105] offset:26912\nv_lshlrev_b32 v156, 16, v174\nv_and_b32 v157, 0xffff0000, v174\nPa 108,108,156\n"
"Pa 109,109,157\nv_lshlrev_b32 v156, 16, v175\nv_and_b32 v157, 0xffff0000, v175\nPa 110,110,156\nPa 111,111,157\nPk 108,108,109\nPk 109,110,111\nds_write_b64 v254, v[108:109] offset:26944\n"
"s_waitcnt vmcnt(3)\nv_lshlrev_b32 v156, 16, v176\nv_and_b32 v157, 0xffff0000, v176\nPa 112,112,156\nPa 113,113,157\nv_lshlrev_b32 v156, 16, v177\nv_and_b32 v157, 0xffff0000, v177\nPa 114,114,156\n"
"Pa 115,115,157\nPk 112,112,113\nPk 113,114,115\nds_write_b64 v254, v[112:113] offset:26976\nv_lshlrev_b32 v156, 16, v178\nv_and_b32 v157, 0xffff0000, v178\nPa 116,116,156\nPa 117,117,157\n"
"v_lshlrev_b32 v156, 16, v179\nv_and_b32 v157, 0xffff0000, v179\nPa 118,118,156\nPa 119,119,157\nPk 116,116,117\nPk 117,118,119\nds_write_b64 v254, v[116:117] offset:27008\ns_waitcnt vmcnt(2)\n"
"v_lshlrev_b32 v156, 16, v180\nv_and_b32 v157, 0xffff0000, v180\nPa 120,120,156\nPa 121,121,157\nv_lshlrev_b32 v156, 16, v181\nv_and_b32 v157, 0xffff0000, v181\nPa 122,122,156\nPa 123,123,157\n"
"Pk 120,120,121\nPk 121,122,123\nds_write_b64 v254, v[120:121] offset:32256\nv_lshlrev_b32 v156, 16, v182\nv_and_b32 v157, 0xffff0000, v182\nPa 124,124,156\nPa 125,125,157\nv_lshlrev_b32 v156, 16, v183\n"
"v_and_b32 v157, 0xffff0000, v183\nPa 126,126,156\nPa 127,127,157\nPk 124,124,125\nPk 125,126,127\nds_write_b64 v254, v[124:125] offset:32288\ns_waitcnt vmcnt(1)\nv_lshlrev_b32 v156, 16, v184\n"
"v_and_b32 v157, 0xffff0000, v184\nPa 128,128,156\nPa 129,129,157\nv_lshlrev_b32 v156, 16, v185\nv_and_b32 v157, 0xffff0000, v185\nPa 130,130,156\nPa 131,131,157\nPk 128,128,129\n"
"Pk 129,130,131\nds_write_b64 v254, v[128:129] offset:32320\nv_lshlrev_b32 v156, 16, v186\nv_and_b32 v157, 0xffff0000, v186\nPa 132,132,156\nPa 133,133,157\nv_lshlrev_b32 v156, 16, v187\nv_and_b32 v157, 0xffff0000, v187\n"
"Pa 134,134,156\nPa 135,135,157\nPk 132,132,133\nPk 133,134,135\nds_write_b64 v254, v[132:133] offset:32352\ns_waitcnt vmcnt(0)\nv_lshlrev_b32 v156, 16, v188\nv_and_b32 v157, 0xffff0000, v188\n"
"Pa 136,136,156\nPa 137,137,157\nv_lshlrev_b32 v156, 16, v189\nv_and_b32 v157, 0xffff0000, v189\nPa 138,138,156\nPa 139,139,157\nPk 136,136,137\nPk 137,138,139\n"
"ds_write_b64 v254, v[136:137] offset:32384\ns_branch .Lo_fin\n.Lo_fin:\ns_waitcnt lgkmcnt(0)\ns_barrier\nds_read_b128 v[0:3], v212\nds_read_b128 v[4:7], v213\nds_read_b128 v[8:11], v214\n"
"ds_read_b128 v[12:15], v215\nds_read_b128 v[16:19], v216\nds_read_b128 v[20:23], v217\nds_read_b128 v[24:27], v218\nds_read_b128 v[28:31], v219\nds_read_b128 v[32:35], v220\ns_waitcnt lgkmcnt(8)\nbuffer_store_dwordx4 v[0:3], v222, s[52:55], 0 offen sc1\n"
"s_waitcnt lgkmcnt(7)\nbuffer_store_dwordx4 v[4:7], v223, s[52:55], 0 offen sc1\ns_waitcnt lgkmcnt(6)\nbuffer_store_dwordx4 v[8:11], v224, s[52:55], 0 offen sc1\ns_waitcnt lgkmcnt(5)\nbuffer_store_dwordx4 v[12:15], v225, s[52:55], 0 offen sc1\ns_waitcnt lgkmcnt(4)\nbuffer_store_dwordx4 v[16:19], v226, s[52:55], 0 offen sc1\n"
"s_waitcnt lgkmcnt(3)\nbuffer_store_dwordx4 v[20:23], v227, s[52:55], 0 offen sc1\ns_waitcnt lgkmcnt(2)\nbuffer_store_dwordx4 v[24:27], v228, s[52:55], 0 offen sc1\ns_waitcnt lgkmcnt(1)\nbuffer_store_dwordx4 v[28:31], v229, s[52:55], 0 offen sc1\ns_waitcnt lgkmcnt(0)\nbuffer_store_dwordx4 v[32:35], v230, s[52:55], 0 offen sc1\n"
".Lo_end:\ns_waitcnt vmcnt(0) lgkmcnt(0)\n"
  :: "s"((unsigned)ap), "s"((unsigned)(ap >> 32)), "s"((unsigned)bp), "s"((unsigned)(bp >> 32)), "s"((unsigned)pp), "s"((unsigned)(pp >> 32)),
     "s"((unsigned)op), "s"((unsigned)(op >> 32)), "s"(ldsb), "s"(wid), "s"((unsigned)cp), "s"((unsigned)(cp >> 32)),
     "s"((unsigned)(unsigned long long)(dbg + (c * 4u + wid) * 4u)), "s"((unsigned)((unsigned long long)(dbg + (c * 4u + wid) * 4u) >> 32)), "s"(sk)
  : "v0", "v1", "v2", "v3", "v4", "v5", "v6", "v7", "v8", "v9", "v10", "v11", "v12", "v13", "v14", "v15", "v16", "v17", "v18", "v19", "v20", "v21", "v22", "v23", "v24", "v25", "v26", "v27", "v28", "v29", "v30", "v31", "v32", "v33", "v34", "v35", "v36", "v37", "v38", "v39", "v40", "v41", "v42", "v43", "v44", "v45", "v46", "v47", "v48", "v49", "v50", "v51", "v52", "v53", "v54", "v55", "v56", "v57", "v58", "v59", "v60", "v61", "v62", "v63", "v64", "v65", "v66", "v67", "v68", "v69", "v70", "v71", "v72", "v73", "v74", "v75", "v76", "v77", "v78", "v79", "v80", "v81", "v82", "v83", "v84", "v85", "v86", "v87", "v88", "v89", "v90", "v91", "v92", "v93", "v94", "v95", "v96", "v97", "v98", "v99", "v100", "v101", "v102", "v103", "v104", "v105", "v106", "v107", "v108", "v109", "v110", "v111", "v112", "v113", "v114", "v115", "v116", "v117", "v118", "v119", "v120", "v121", "v122", "v123", "v124", "v125", "v126", "v127", "v128", "v129", "v130", "v131", "v132", "v133", "v134", "v135", "v136", "v137", "v138", "v139", "v140", "v141", "v142", "v143", "v144", "v145", "v146", "v147", "v148", "v149", "v150", "v151", "v152", "v153", "v154", "v155", "v156", "v157", "v158", "v159", "v160", "v161", "v162", "v163", "v164", "v165", "v166", "v167", "v168", "v169", "v170", "v171", "v172", "v173", "v174", "v175", "v176", "v177", "v178", "v179", "v180", "v181", "v182", "v183", "v184", "v185", "v186", "v187", "v188", "v189", "v190", "v191", "v192", "v193", "v194", "v195", "v196", "v197", "v198", "v199", "v200", "v201", "v202", "v203", "v204", "v205", "v206", "v207", "v208", "v209", "v210", "v211", "v212", "v213", "v214", "v215", "v216", "v217", "v218", "v219", "v220", "v221", "v222", "v223", "v224", "v225", "v226", "v227", "v228", "v229", "v230", "v231", "v232", "v233", "v234", "v235", "v236", "v237", "v238", "v239", "v240", "v241", "v242", "v243", "v244", "v245", "v246", "v247", "v248", "v249", "v250", "v251", "v252", "v253", "v254", "v255", "a0", "a1", "a2", "a3", "a4", "a5", "a6", "a7", "a8", "a9", "a10", "a11", "a12", "a13", "a14", "a15", "a16", "a17", "a18", "a19", "a20", "a21", "a22", "a23", "a24", "a25", "a26", "a27", "a28", "a29", "a30", "a31", "a32", "a33", "a34", "a35", "a36", "a37", "a38", "a39", "a40", "a41", "a42", "a43", "a44", "a45", "a46", "a47", "a48", "a49", "a50", "a51", "a52", "a53", "a54", "a55", "a56", "a57", "a58", "a59", "a60", "a61", "a62", "a63", "a64", "a65", "a66", "a67", "a68", "a69", "a70", "a71", "a72", "a73", "a74", "a75", "a76", "a77", "a78", "a79", "a80", "a81", "a82", "a83", "a84", "a85", "a86", "a87", "a88", "a89", "a90", "a91", "a92", "a93", "a94", "a95", "a96", "a97", "a98", "a99", "a100", "a101", "a102", "a103", "a104", "a105", "a106", "a107", "a108", "a109", "a110", "a111", "a112", "a113", "a114", "a115", "a116", "a117", "a118", "a119", "a120", "a121", "a122", "a123", "a124", "a125", "a126", "a127", "a128", "a129", "a130", "a131", "a132", "a133", "a134", "a135", "a136", "a137", "a138", "a139", "a140", "a141", "a142", "a143", "a144", "a145", "a146", "a147", "a148", "a149", "a150", "a151", "a152", "a153", "a154", "a155", "a156", "a157", "a158", "a159", "a160", "a161", "a162", "a163", "a164", "a165", "a166", "a167", "a168", "a169", "a170", "a171", "a172", "a173", "a174", "a175", "a176", "a177", "a178", "a179", "a180", "a181", "a182", "a183", "a184", "a185", "a186", "a187", "a188", "a189", "a190", "a191", "a192", "a193", "a194", "a195", "a196", "a197", "a198", "a199", "a200", "a201", "a202", "a203", "a204", "a205", "a206", "a207", "a208", "a209", "a210", "a211", "a212", "a213", "a214", "a215", "a216", "a217", "a218", "a219", "a220", "a221", "a222", "a223", "a224", "a225", "a226", "a227", "a228", "a229", "a230", "a231", "a232", "a233", "a234", "a235", "a236", "a237", "a238", "a239", "a240", "a241", "a242", "a243", "a244", "a245", "a246", "a247", "a248", "a249", "a250", "a251", "a252", "a253", "a254", "a255", "s40", "s41", "s42", "s43", "s44", "s45", "s46", "s47", "s48", "s49", "s50", "s51", "s52", "s53", "s54", "s55", "s56", "s57", "s58", "s59", "s60", "s61", "s62", "s63", "s64", "s65", "s66", "s67", "s68", "s69", "s70", "s71", "s72", "s73", "s74", "s75", "s76", "s77", "s78", "s79", "s80", "s81", "s82", "s83", "s84", "s85", "s86", "s87", "vcc", "scc", "memory");
}

extern "C" void kernel_launch(void* const* d_in, const int* in_sizes, int n_in,
                              void* d_out, int out_size, void* d_ws, size_t ws_size,
                              hipStream_t stream) {
  typedef unsigned short u16;
  const u16* xA = (const u16*)d_in[0]; const u16* kA = (const u16*)d_in[1]; const u16* vA = (const u16*)d_in[2];
  const u16* wqA = (const u16*)d_in[3]; const u16* woA = (const u16*)d_in[4];
  const u16* xB = (const u16*)d_in[5]; const u16* kB = (const u16*)d_in[6]; const u16* vB = (const u16*)d_in[7];
  const u16* wqB = (const u16*)d_in[8]; const u16* woB = (const u16*)d_in[9];
  u16* out = (u16*)d_out;
  u16* O = (u16*)d_ws;
  float* P = (float*)(O + (size_t)NBLK * MROWS * HD);
  unsigned* flags = (unsigned*)((char*)P + (size_t)128 * 2 * 81920);
  (void)hipFuncSetAttribute((const void*)attnq_asm, hipFuncAttributeMaxDynamicSharedMemorySize, 163840);
  (void)hipFuncSetAttribute((const void*)oproj_asm, hipFuncAttributeMaxDynamicSharedMemorySize, 147456);
  hipLaunchKernelGGL(attnq_asm, dim3(256), dim3(256), 163840, stream, xA, xB, wqA, wqB, kA, kB, vA, vB, O, (unsigned long long*)P, woA, woB, flags);
  hipLaunchKernelGGL(oproj_asm, dim3(256), dim3(256), 147456, stream, (const u16*)O, woA, woB, P, flags, out, (unsigned long long*)P);
}
```

```cpp
#include <hip/hip_runtime.h>
#include <stdint.h>

__global__ __launch_bounds__(256, 1)
void mha2_asm(const unsigned short* __restrict__ X0, const unsigned short* __restrict__ X1, const unsigned short* __restrict__ W0, const unsigned short* __restrict__ W1,
              const unsigned short* __restrict__ K0, const unsigned short* __restrict__ K1,
              const unsigned short* __restrict__ V0, const unsigned short* __restrict__ V1,
              const unsigned short* __restrict__ WO0, const unsigned short* __restrict__ WO1, char* ws, unsigned short* out) {
  extern __shared__ __attribute__((aligned(16))) char lds[];
  const unsigned c = blockIdx.x;
  const unsigned xcd = c & 7u, idx = c >> 3;
  const unsigned bbi = idx >> 3, hg = idx & 7u;
  const unsigned bb = xcd * 4u + bbi;
  const unsigned bk = bb >> 4, b = bb & 15u;
  const unsigned wid = __builtin_amdgcn_readfirstlane(threadIdx.x >> 6);
  const unsigned head0 = (hg >> 2) * 16u + (hg & 3u) * 2u + (wid >> 1);
  const unsigned long long kp = (unsigned long long)(bk ? K1 : K0) + ((unsigned long long)b * 32u + head0) * 262144ull - 1024ull;
  const unsigned long long vp = (unsigned long long)(bk ? V1 : V0) + ((unsigned long long)b * 32u + head0) * 262144ull - 1024ull;
  const unsigned long long xp = (unsigned long long)(bk ? X1 : X0) + (unsigned long long)b * (224u * 1280u);
  const unsigned long long wp = (unsigned long long)(bk ? W1 : W0);
  const unsigned long long op = (unsigned long long)ws + (unsigned long long)bb * 2295808ull;
  const unsigned ldsb = (unsigned)(size_t)lds;
  const unsigned long long wop = (unsigned long long)(bk ? WO1 : WO0) - 4096ull;
  const unsigned long long yp = (unsigned long long)out + (unsigned long long)b * (224ull * 2560ull) + bk * 1280u + (hg & 3u) * 320u;
  const unsigned long long dp = (unsigned long long)ws + (100ull << 20) + (c * 4u + wid) * 256u;
  asm volatile(
".macro Qta d,v,o\nds_read_b64_tr_b16 a[\\d:\\d+1], v\\v offset:\\o\n.endm\n.macro Qdv d,v,o\nds_read_b128 v[\\d:\\d+3], v\\v offset:\\o\n.endm\n.macro Qda d,v,o\nds_read_b128 a[\\d:\\d+3], v\\v offset:\\o\n"
".endm\n.macro Qzvav d,a,b\nv_mfma_f32_16x16x32_bf16 v[\\d:\\d+3], a[\\a:\\a+3], v[\\b:\\b+3], 0\n.endm\n.macro Qzaav d,a,b\nv_mfma_f32_16x16x32_bf16 a[\\d:\\d+3], a[\\a:\\a+3], v[\\b:\\b+3], 0\n.endm\n.macro Qmvaa d,a,b\n"
"v_mfma_f32_16x16x32_bf16 v[\\d:\\d+3], a[\\a:\\a+3], a[\\b:\\b+3], v[\\d:\\d+3]\n.endm\n.macro Qmaaa d,a,b\nv_mfma_f32_16x16x32_bf16 a[\\d:\\d+3], a[\\a:\\a+3], a[\\b:\\b+3], a[\\d:\\d+3]\n.endm\n.macro Qmvav d,a,b\nv_mfma_f32_16x16x32_bf16 v[\\d:\\d+3], a[\\a:\\a+3], v[\\b:\\b+3], v[\\d:\\d+3]\n.endm\n"
".macro Qmaav d,a,b\nv_mfma_f32_16x16x32_bf16 a[\\d:\\d+3], a[\\a:\\a+3], v[\\b:\\b+3], a[\\d:\\d+3]\n.endm\n.macro Qk d,a,b\nv_cvt_pk_bf16_f32 v\\d, v\\a, v\\b\n.endm\n.macro Qzvaa d,a,b\nv_mfma_f32_16x16x32_bf16 v[\\d:\\d+3], a[\\a:\\a+3], a[\\b:\\b+3], 0\n"
".endm\n.macro Qa d,a,b\nv_add_f32 v\\d, v\\a, v\\b\n.endm\n.macro Qe d\nv_exp_f32 v\\d, v\\d\n.endm\n.macro Qcvaa d,a,b,c\n"
"v_mfma_f32_16x16x32_bf16 v[\\d:\\d+3], a[\\a:\\a+3], a[\\b:\\b+3], v[\\c:\\c+3]\n.endm\n.macro Qtv d,v,o\nds_read_b64_tr_b16 v[\\d:\\d+1], v\\v offset:\\o\n.endm\n.macro Qzava d,a,b\nv_mfma_f32_16x16x32_bf16 a[\\d:\\d+3], v[\\a:\\a+3], a[\\b:\\b+3], 0\n.endm\n"
".macro Qmava d,a,b\nv_mfma_f32_16x16x32_bf16 a[\\d:\\d+3], v[\\a:\\a+3], a[\\b:\\b+3], a[\\d:\\d+3]\n.endm\n.macro Qmvva d,a,b\nv_mfma_f32_16x16x32_bf16 v[\\d:\\d+3], v[\\a:\\a+3], a[\\b:\\b+3], v[\\d:\\d+3]\n.endm\ns_mov_b32 s52, %6\ns_and_b32 s53, %7, 0xffff\n"
"s_mov_b32 s54, 5242880\ns_mov_b32 s55, 0x20000\ns_mov_b32 s48, %4\ns_and_b32 s49, %5, 0xffff\ns_mov_b32 s50, 286720\ns_mov_b32 s51, 0x20000\ns_mov_b32 s70, %11\ns_mov_b32 s24, %8\n"
"s_and_b32 s25, %9, 0xffff\ns_mov_b32 s26, 2295808\ns_mov_b32 s27, 0x20000\ns_and_b32 s92, s70, 1\ns_lshr_b32 s93, s70, 1\nv_mbcnt_lo_u32_b32 v0, -1, 0\nv_mbcnt_hi_u32_b32 v0, -1, v0\nv_and_b32 v4, 7, v0\n"
"v_lshlrev_b32 v4, 7, v4\nv_add_u32 v76, 2293760, v4\nbuffer_load_dword v65, v76, s[24:27], 0 offen sc1\nv_lshrrev_b32 v2, 4, v0\ns_lshl_b32 s68, s92, 17\nv_and_b32 v4, 3, v0\nv_lshlrev_b32 v4, 4, v4\nv_lshl_add_u32 v4, v2, 6, v4\n"
"v_bfe_u32 v1, v0, 2, 2\nv_lshl_add_u32 v4, v1, 13, v4\nv_add_u32 v62, s68, v4\ns_lshl_b32 s87, %12, 8\ns_mul_i32 s62, s93, 8224\ns_mul_i32 s68, s92, 4112\ns_add_u32 s62, s62, s68\ns_add_u32 s62, s62, %10\n"
"s_add_u32 s62, s62, 33024\ns_lshl_b32 s69, s92, 1\nv_lshrrev_b32 v3, 5, v0\nv_and_b32 v4, 3, v0\nv_xor_b32 v4, s69, v4\nv_lshlrev_b32 v4, 4, v4\nv_lshl_add_u32 v4, v3, 6, v4\nv_bfe_u32 v1, v0, 2, 3\n"
"v_mul_u32_u24 v64, 1280, v1\nv_add_u32 v64, v64, v4\ns_mul_i32 s86, s93, 143360\ns_mul_i32 s68, s92, 10240\ns_add_u32 s86, s86, s68\ns_mul_i32 s63, s92, 14336\ns_mul_i32 s68, s93, 7168\ns_add_u32 s63, s63, s68\n"
"s_add_u32 s63, s63, %10\ns_add_u32 s63, s63, 99072\ns_nop 1\ns_add_u32 m0, s62, 0\ns_add_u32 s69, s87, 0\ns_nop 0\nbuffer_load_dwordx4 v62, s[52:55], s69 offen lds\ns_add_u32 s69, s87, 31744\n"
"s_nop 0\nbuffer_load_dwordx4 v62, s[52:55], s69 offen offset:1024 lds\ns_add_u32 s69, s87, 63480\ns_nop 0\nbuffer_load_dwordx4 v62, s[52:55], s69 offen offset:2056 lds\ns_add_u32 s69, s87, 95224\ns_nop 0\nbuffer_load_dwordx4 v62, s[52:55], s69 offen offset:3080 lds\n"
"s_add_u32 m0, s63, 33024\ns_add_u32 s88, s86, 0\ns_nop 0\nbuffer_load_dwordx4 v64, s[48:51], s88 offen lds\ns_add_u32 s88, s86, 19456\ns_nop 0\nbuffer_load_dwordx4 v64, s[48:51], s88 offen offset:1024 lds\ns_add_u32 s88, s86, 38912\n"
"s_nop 0\nbuffer_load_dwordx4 v64, s[48:51], s88 offen offset:2048 lds\ns_add_u32 s88, s86, 58368\ns_nop 0\nbuffer_load_dwordx4 v64, s[48:51], s88 offen offset:3072 lds\ns_add_u32 m0, s63, 37120\ns_add_u32 s88, s86, 81920\ns_nop 0\n"
"buffer_load_dwordx4 v64, s[48:51], s88 offen lds\ns_add_u32 s88, s86, 101376\ns_nop 0\nbuffer_load_dwordx4 v64, s[48:51], s88 offen offset:1024 lds\ns_add_u32 s88, s86, 120832\ns_nop 0\nbuffer_load_dwordx4 v64, s[48:51], s88 offen offset:2048 lds\ns_add_u32 m0, s62, 16448\n"
"s_add_u32 s69, s87, 262144\ns_nop 0\nbuffer_load_dwordx4 v62, s[52:55], s69 offen lds\ns_add_u32 s69, s87, 293888\ns_nop 0\nbuffer_load_dwordx4 v62, s[52:55], s69 offen offset:1024 lds\ns_add_u32 s69, s87, 325624\ns_nop 0\n"
"buffer_load_dwordx4 v62, s[52:55], s69 offen offset:2056 lds\ns_add_u32 s69, s87, 357368\ns_nop 0\nbuffer_load_dwordx4 v62, s[52:55], s69 offen offset:3080 lds\ns_add_u32 m0, s63, 0\ns_add_u32 s88, s86, 128\ns_nop 0\nbuffer_load_dwordx4 v64, s[48:51], s88 offen lds\n"
"s_add_u32 s88, s86, 19584\ns_nop 0\nbuffer_load_dwordx4 v64, s[48:51], s88 offen offset:1024 lds\ns_add_u32 s88, s86, 39040\ns_nop 0\nbuffer_load_dwordx4 v64, s[48:51], s88 offen offset:2048 lds\ns_add_u32 s88, s86, 58496\ns_nop 0\n"
"buffer_load_dwordx4 v64, s[48:51], s88 offen offset:3072 lds\ns_add_u32 m0, s63, 4096\ns_add_u32 s88, s86, 82048\ns_nop 0\nbuffer_load_dwordx4 v64, s[48:51], s88 offen lds\ns_add_u32 s88, s86, 101504\ns_nop 0\nbuffer_load_dwordx4 v64, s[48:51], s88 offen offset:1024 lds\n"
"s_add_u32 s88, s86, 120960\ns_nop 0\nbuffer_load_dwordx4 v64, s[48:51], s88 offen offset:2048 lds\ns_add_u32 m0, s62, 33024\ns_add_u32 s69, s87, 524288\ns_nop 0\nbuffer_load_dwordx4 v62, s[52:55], s69 offen lds\ns_add_u32 s69, s87, 556032\n"
"s_nop 0\nbuffer_load_dwordx4 v62, s[52:55], s69 offen offset:1024 lds\ns_add_u32 s69, s87, 587768\ns_nop 0\nbuffer_load_dwordx4 v62, s[52:55], s69 offen offset:2056 lds\ns_add_u32 s69, s87, 619512\ns_nop 0\nbuffer_load_dwordx4 v62, s[52:55], s69 offen offset:3080 lds\n"
"s_add_u32 m0, s62, 49472\ns_add_u32 s69, s87, 786432\ns_nop 0\nbuffer_load_dwordx4 v62, s[52:55], s69 offen lds\ns_add_u32 s69, s87, 818176\ns_nop 0\nbuffer_load_dwordx4 v62, s[52:55], s69 offen offset:1024 lds\ns_add_u32 s69, s87, 849912\n"
"s_nop 0\nbuffer_load_dwordx4 v62, s[52:55], s69 offen offset:2056 lds\ns_add_u32 s69, s87, 881656\ns_nop 0\nbuffer_load_dwordx4 v62, s[52:55], s69 offen offset:3080 lds\ns_mov_b32 s40, %0\ns_and_b32 s41, %1, 0xffff\ns_mov_b32 s42, 2360320\n"
"s_mov_b32 s43, 0x20000\ns_mov_b32 s44, %2\ns_and_b32 s45, %3, 0xffff\ns_mov_b32 s46, 2360320\ns_mov_b32 s47, 0x20000\ns_mov_b32 s56, %8\ns_and_b32 s57, %9, 0xffff\ns_mov_b32 s58, 0\n"
"s_mov_b32 s59, 0x20000\ns_mov_b32 s60, 0\ns_mov_b32 s67, 0\ns_mov_b32 s72, 0x3e0293ee\ns_lshl_b32 s68, s92, 12\ns_add_u32 s88, s68, 1024\nv_lshl_add_u32 v4, v1, 8, v4\nv_add_u32 v56, s88, v4\n"
"v_subrev_u32 v57, 896, v56\nv_and_b32 v4, 1, v0\nv_lshlrev_b32 v4, 4, v4\nv_lshl_add_u32 v4, v2, 5, v4\nv_bfe_u32 v1, v0, 1, 3\nv_lshl_add_u32 v4, v1, 8, v4\nv_add_u32 v58, s88, v4\nv_subrev_u32 v59, 896, v58\n"
"s_lshl_b32 s69, s93, 14\ns_add_u32 s69, s69, %10\ns_add_u32 s61, s69, s68\ns_nop 1\ns_add_u32 m0, s61, 0\ns_nop 0\nbuffer_load_dwordx4 v56, s[40:43], s60 offen nt lds\nbuffer_load_dwordx4 v57, s[40:43], s60 offen offset:1024 nt lds\n"
"buffer_load_dwordx4 v56, s[40:43], s60 offen offset:2048 nt lds\nbuffer_load_dwordx4 v57, s[40:43], s60 offen offset:3072 nt lds\ns_add_u32 m0, s61, 8192\ns_nop 0\nbuffer_load_dwordx4 v58, s[44:47], s60 offen nt lds\nbuffer_load_dwordx4 v59, s[44:47], s60 offen offset:1024 nt lds\nbuffer_load_dwordx4 v58, s[44:47], s60 offen offset:2048 nt lds\nbuffer_load_dwordx4 v59, s[44:47], s60 offen offset:3072 nt lds\n"
"s_add_u32 s60, s60, 8192\nv_bfe_u32 v3, v0, 3, 1\nv_lshlrev_b32 v4, 1, v3\nv_xor_b32 v4, v4, v2\nv_and_b32 v1, 7, v0\nv_lshl_add_u32 v4, v1, 2, v4\nv_lshlrev_b32 v4, 4, v4\nv_lshl_add_u32 v1, v3, 12, v4\n"
"v_add_u32 v52, s69, v1\nv_add_u32 v53, 66048, v52\ns_mul_i32 s68, s92, 7168\ns_add_u32 s68, s68, %10\ns_add_u32 s68, s68, 99072\nv_mul_u32_u24 v1, 14336, v3\nv_add3_u32 v63, v1, v4, s68\nv_lshrrev_b32 v3, 5, v0\n"
"v_and_b32 v4, 31, v0\nv_lshlrev_b32 v4, 3, v4\nv_lshl_add_u32 v4, v3, 12, v4\nv_add_u32 v4, 8192, v4\nv_add_u32 v54, s69, v4\nv_add_u32 v55, 66048, v54\ns_mul_i32 s68, s93, 8224\ns_add_u32 s68, s68, %10\n"
"s_add_u32 s68, s68, 33024\nv_mul_u32_u24 v3, 2056, v2\nv_and_b32 v4, 15, v0\nv_lshlrev_b32 v4, 4, v4\nv_add3_u32 v61, v3, v4, s68\nv_lshlrev_b32 v60, 4, v0\ns_mul_i32 s68, s70, 6\ns_add_u32 s68, s68, 0\n"
"s_sub_u32 s69, s68, 0\ns_lshl_b32 s69, s69, 10\nv_add_u32 v3, s69, v60\ns_sub_u32 s69, s68, 14\ns_lshl_b32 s69, s69, 2\ns_and_b32 s69, s69, 63\nv_lshrrev_b32 v4, 8, v60\nv_add_u32 v4, s69, v4\n"
"v_mul_u32_u24 v5, 205, v4\nv_lshrrev_b32 v5, 11, v5\nv_mul_u32_u24 v70, 10, v5\nv_sub_u32 v4, v4, v70\nv_lshlrev_b32 v4, 5, v4\nv_bfe_u32 v70, v60, 5, 3\nv_lshl_add_u32 v5, v5, 3, v70\nv_mul_u32_u24 v5, 1280, v5\n"
"v_and_b32 v70, 16, v60\nv_add3_u32 v4, v4, v5, v70\nv_add_u32 v4, 4096, v4\ns_cmp_lt_u32 s68, 14\ns_cselect_b64 vcc, -1, 0\nv_cndmask_b32 v70, v4, v3, vcc\ns_mul_i32 s68, s70, 6\ns_add_u32 s68, s68, 1\n"
"s_sub_u32 s69, s68, 1\ns_lshl_b32 s69, s69, 10\nv_add_u32 v3, s69, v60\ns_sub_u32 s69, s68, 14\ns_lshl_b32 s69, s69, 2\ns_and_b32 s69, s69, 63\nv_lshrrev_b32 v4, 8, v60\nv_add_u32 v4, s69, v4\n"
"v_mul_u32_u24 v5, 205, v4\nv_lshrrev_b32 v5, 11, v5\nv_mul_u32_u24 v71, 10, v5\nv_sub_u32 v4, v4, v71\nv_lshlrev_b32 v4, 5, v4\nv_bfe_u32 v71, v60, 5, 3\nv_lshl_add_u32 v5, v5, 3, v71\nv_mul_u32_u24 v5, 1280, v5\n"
"v_and_b32 v71, 16, v60\nv_add3_u32 v4, v4, v5, v71\nv_add_u32 v4, 3072, v4\ns_cmp_lt_u32 s68, 14\ns_cselect_b64 vcc, -1, 0\nv_cndmask_b32 v71, v4, v3, vcc\ns_mul_i32 s68, s70, 6\ns_add_u32 s68, s68, 2\n"
"s_sub_u32 s69, s68, 2\ns_lshl_b32 s69, s69, 10\nv_add_u32 v3, s69, v60\ns_sub_u32 s69, s68, 14\ns_lshl_b32 s69, s69, 2\ns_and_b32 s69, s69, 63\nv_lshrrev_b32 v4, 8, v60\nv_add_u32 v4, s69, v4\n"
"v_mul_u32_u24 v5, 205, v4\nv_lshrrev_b32 v5, 11, v5\nv_mul_u32_u24 v72, 10, v5\nv_sub_u32 v4, v4, v72\nv_lshlrev_b32 v4, 5, v4\nv_bfe_u32 v72, v60, 5, 3\nv_lshl_add_u32 v5, v5, 3, v72\nv_mul_u32_u24 v5, 1280, v5\n"
"v_and_b32 v72, 16, v60\nv_add3_u32 v4, v4, v5, v72\nv_add_u32 v4, 2048, v4\ns_cmp_lt_u32 s68, 14\ns_cselect_b64 vcc, -1, 0\nv_cndmask_b32 v72, v4, v3, vcc\ns_lshl_b32 s68, s70, 2\ns_add_u32 s69, %10, 131840\n"
"s_add_u32 s68, s68, s69\nv_mov_b32 v73, s68\nv_and_b32 v3, 3, v0\nv_lshl_add_u32 v74, v3, 2, s69\nv_mov_b32 v75, 0\nds_write_b32 v73, v75\ns_waitcnt lgkmcnt(0)\ns_mov_b32 s68, 40960\n"
"s_cmp_lt_u32 s70, 3\ns_cselect_b32 s31, 14336, s68\ns_mov_b32 s68, 40960\ns_cmp_lt_u32 s70, 2\ns_cselect_b32 s90, 14336, s68\ns_mul_i32 s91, s70, 6144\ns_add_u32 s91, s91, %10\ns_cmp_eq_u32 s70, 0\n"
"s_cselect_b32 s68, %19, 64\nv_mov_b32 v3, 0x7ffff000\nv_cmp_eq_u32 vcc, s68, v0\ns_nop 1\nv_cndmask_b32 v77, v3, v76, vcc\ns_and_b32 s68, %19, 4\ns_lshl_b32 s28, 15, s68\ns_mov_b32 s29, 0\n"
"s_and_b32 s69, %19, 3\ns_lshl_b32 s69, s69, 2\ns_lshr_b32 s68, s68, 1\ns_add_u32 s69, s69, s68\ns_add_u32 s69, s69, s93\ns_mul_i32 s98, s69, 28672\ns_mul_i32 s68, s92, 7168\ns_add_u32 s98, s98, s68\n"
"s_add_u32 s98, s98, 1835008\ns_mov_b32 s71, s98\ns_add_u32 s58, s98, 17408\nv_mov_b32 v3, -1\nv_accvgpr_write_b32 a0, v3\nv_accvgpr_write_b32 a1, v3\nv_accvgpr_write_b32 a2, v3\nv_accvgpr_write_b32 a3, v3\n"
"v_accvgpr_write_b32 a16, v3\nv_accvgpr_write_b32 a17, v3\nv_accvgpr_write_b32 a18, v3\nv_accvgpr_write_b32 a19, v3\nv_accvgpr_write_b32 a32, v3\nv_accvgpr_write_b32 a33, v3\nv_accvgpr_write_b32 a34, v3\nv_accvgpr_write_b32 a35, v3\n"
"v_accvgpr_write_b32 a48, v3\nv_accvgpr_write_b32 a49, v3\nv_accvgpr_write_b32 a50, v3\nv_accvgpr_write_b32 a51, v3\nv_accvgpr_write_b32 a64, v3\nv_accvgpr_write_b32 a65, v3\nv_accvgpr_write_b32 a66, v3\nv_accvgpr_write_b32 a67, v3\n"
"v_accvgpr_write_b32 a80, v3\nv_accvgpr_write_b32 a81, v3\nv_accvgpr_write_b32 a82, v3\nv_accvgpr_write_b32 a83, v3\nv_accvgpr_write_b32 a96, v3\nv_accvgpr_write_b32 a97, v3\nv_accvgpr_write_b32 a98, v3\nv_accvgpr_write_b32 a99, v3\n"
"v_accvgpr_write_b32 a4, v3\nv_accvgpr_write_b32 a5, v3\nv_accvgpr_write_b32 a6, v3\nv_accvgpr_write_b32 a7, v3\nv_accvgpr_write_b32 a20, v3\nv_accvgpr_write_b32 a21, v3\nv_accvgpr_write_b32 a22, v3\nv_accvgpr_write_b32 a23, v3\n"
"v_accvgpr_write_b32 a36, v3\nv_accvgpr_write_b32 a37, v3\nv_accvgpr_write_b32 a38, v3\nv_accvgpr_write_b32 a39, v3\n.Lround:\ns_waitcnt vmcnt(15)\ns_barrier\nQta 112,61,0\n"
"Qta 114,61,1024\nQdv 24,63,33024\nQta 116,61,8\nQta 118,61,1032\nQdv 28,63,34048\nQta 120,61,256\nQta 122,61,1280\nQdv 32,63,35072\n"
"Qta 124,61,264\nQta 126,61,1288\nQdv 36,63,36096\nQta 128,61,512\nQta 130,61,1536\nQdv 40,63,37120\nQta 132,61,520\nQta 134,61,1544\n"
"Qdv 44,63,38144\nQta 136,61,768\nQta 138,61,1792\nQdv 48,63,39168\nQta 140,61,776\nQta 142,61,1800\nQda 176,63,33536\nQda 180,63,34560\n"
"Qda 184,63,35584\nQda 188,63,36608\nQda 192,63,37632\nQda 196,63,38656\nQda 200,63,39680\ns_waitcnt lgkmcnt(7)\nQzvav 80,112,24\nQzvav 84,116,24\n"
"s_add_u32 s68, s71, 0\nQzvav 88,120,24\nbuffer_store_dwordx4 a[0:3], v60, s[56:59], s68 offen offset:0 sc1\nQzvav 92,124,24\nQzvav 96,128,24\ns_add_u32 s68, s71, 14336\nQzvav 100,132,24\nbuffer_store_dwordx4 a[4:7], v60, s[56:59], s68 offen offset:0 sc1\n"
"Qzvav 104,136,24\nQzvav 108,140,24\ns_waitcnt lgkmcnt(0)\nQzvav 112,112,28\ns_waitcnt vmcnt(24)\ns_barrier\nQzvav 116,116,28\nQta 144,61,16448\n"
"s_add_u32 m0, s62, 0\ns_add_u32 s69, s87, 1048576\nQzvav 120,120,28\nbuffer_load_dwordx4 v62, s[52:55], s69 offen lds\nQzvav 124,124,28\nQta 146,61,17472\nQzvav 128,128,28\nQzvav 132,132,28\n"
"Qta 148,61,16456\ns_add_u32 s69, s87, 1080320\nQzvav 136,136,28\nbuffer_load_dwordx4 v62, s[52:55], s69 offen offset:1024 lds\nQzvav 140,140,28\nQta 150,61,17480\nQzvav 144,112,32\nQzvav 148,116,32\n"
"Qta 152,61,16704\ns_add_u32 s69, s87, 1112056\nQzvav 152,120,32\nbuffer_load_dwordx4 v62, s[52:55], s69 offen offset:2056 lds\nQzvav 156,124,32\nQta 154,61,17728\nQzvav 160,128,32\nQzvav 164,132,32\n"
"Qta 156,61,16712\ns_add_u32 s69, s87, 1143800\nQzvav 168,136,32\nbuffer_load_dwordx4 v62, s[52:55], s69 offen offset:3080 lds\nQzvav 172,140,32\nQta 158,61,17736\nQzvav 176,112,36\nQzvav 180,116,36\n"
"Qta 160,61,16960\ns_add_u32 m0, s63, 33024\ns_add_u32 s88, s86, 256\nQzvav 184,120,36\nbuffer_load_dwordx4 v64, s[48:51], s88 offen lds\nQzvav 188,124,36\nQta 162,61,17984\nQzvav 192,128,36\n"
"Qzvav 196,132,36\nQta 164,61,16968\ns_add_u32 s88, s86, 19712\nQzvav 200,136,36\nbuffer_load_dwordx4 v64, s[48:51], s88 offen offset:1024 lds\nQzvav 204,140,36\nQta 166,61,17992\nQzvav 208,112,40\n"
"Qzvav 212,116,40\nQta 168,61,17216\ns_add_u32 s88, s86, 39168\nQzvav 216,120,40\nbuffer_load_dwordx4 v64, s[48:51], s88 offen offset:2048 lds\nQzvav 220,124,40\nQta 170,61,18240\nQzvav 224,128,40\n"
"Qzvav 228,132,40\nQta 172,61,17224\ns_add_u32 s88, s86, 58624\nQzvav 232,136,40\nbuffer_load_dwordx4 v64, s[48:51], s88 offen offset:3072 lds\nQzvav 236,140,40\nQta 174,61,18248\nQzvav 240,112,44\n"
"Qzvav 244,116,44\ns_add_u32 m0, s63, 37120\ns_add_u32 s88, s86, 82176\nQzvav 248,120,44\nbuffer_load_dwordx4 v64, s[48:51], s88 offen lds\nQzvav 252,124,44\nQzaav 208,128,44\nQzaav 212,132,44\n"
"s_add_u32 s88, s86, 101632\nQzaav 216,136,44\nbuffer_load_dwordx4 v64, s[48:51], s88 offen offset:1024 lds\nQzaav 220,140,44\nQzaav 224,112,48\nQzaav 228,116,48\ns_add_u32 s88, s86, 121088\nQzaav 232,120,48\n"
"buffer_load_dwordx4 v64, s[48:51], s88 offen offset:2048 lds\nQzaav 236,124,48\nQzaav 240,128,48\nQzaav 244,132,48\nQzaav 248,136,48\nQzaav 252,140,48\ns_waitcnt lgkmcnt(0)\nQmvaa 80,144,176\n"
"Qmvaa 84,148,176\ns_add_u32 s68, s71, 28672\nQmvaa 88,152,176\nbuffer_store_dwordx4 a[8:11], v60, s[56:59], s68 offen offset:0 sc1\nQmvaa 92,156,176\nQmvaa 96,160,176\ns_add_u32 s68, s71, 43008\nQmvaa 100,164,176\n"
"buffer_store_dwordx4 a[12:15], v60, s[56:59], s68 offen offset:0 sc1\nQmvaa 104,168,176\nQmvaa 108,172,176\ns_waitcnt lgkmcnt(0)\nQmvaa 112,144,180\ns_waitcnt vmcnt(15)\ns_barrier\nQmvaa 116,148,180\n"
"Qda 176,63,512\ns_add_u32 m0, s62, 16448\ns_add_u32 s69, s87, 1310720\nQmvaa 120,152,180\nbuffer_load_dwordx4 v62, s[52:55], s69 offen lds\nQmvaa 124,156,180\nQta 112,61,33024\nQmvaa 128,160,180\n"
"Qta 114,61,34048\nQmvaa 132,164,180\nQta 116,61,33032\ns_add_u32 s69, s87, 1342464\nQmvaa 136,168,180\nbuffer_load_dwordx4 v62, s[52:55], s69 offen offset:1024 lds\nQmvaa 140,172,180\nQda 180,63,1536\n"
"Qmvaa 144,144,184\nQta 118,61,34056\nQmvaa 148,148,184\nQdv 24,63,0\ns_add_u32 s69, s87, 1374200\nQmvaa 152,152,184\nbuffer_load_dwordx4 v62, s[52:55], s69 offen offset:2056 lds\nQmvaa 156,156,184\n"
"Qta 120,61,33280\nQmvaa 160,160,184\nQta 122,61,34304\nQmvaa 164,164,184\nQta 124,61,33288\ns_add_u32 s69, s87, 1405944\nQmvaa 168,168,184\nbuffer_load_dwordx4 v62, s[52:55], s69 offen offset:3080 lds\n"
"Qmvaa 172,172,184\nQda 184,63,2560\nQmvaa 176,144,188\nQta 126,61,34312\nQmvaa 180,148,188\nQta 128,61,33536\nQmvaa 184,152,188\nQta 130,61,34560\n"
"Qmvaa 188,156,188\nQta 132,61,33544\nQmvaa 192,160,188\nQta 134,61,34568\nQmvaa 196,164,188\nQta 136,61,33792\nQmvaa 200,168,188\nQta 138,61,34816\n"
"Qmvaa 204,172,188\nQda 188,63,3584\nQmvaa 208,144,192\nQta 140,61,33800\nQmvaa 212,148,192\nQta 142,61,34824\nQmvaa 216,152,192\nQdv 28,63,1024\n"
"Qmvaa 220,156,192\nQdv 32,63,2048\nQmvaa 224,160,192\nQdv 36,63,3072\nQmvaa 228,164,192\nQdv 40,63,4096\nQmvaa 232,168,192\nQdv 44,63,5120\n"
"Qmvaa 236,172,192\nQda 192,63,4608\nQmvaa 240,144,196\nQdv 48,63,6144\nQmvaa 244,148,196\nQmvaa 248,152,196\nQmvaa 252,156,196\nQmaaa 208,160,196\n"
"Qmaaa 212,164,196\nQmaaa 216,168,196\nQmaaa 220,172,196\nQda 196,63,5632\nQmaaa 224,144,200\nQmaaa 228,148,200\nQmaaa 232,152,200\nQmaaa 236,156,200\n"
"Qmaaa 240,160,200\nQmaaa 244,164,200\nQmaaa 248,168,200\nQmaaa 252,172,200\nQda 200,63,6656\ns_waitcnt lgkmcnt(9)\nQmvav 80,112,24\nQmvav 84,116,24\n"
"s_add_u32 s68, s71, 0\nQmvav 88,120,24\nbuffer_store_dwordx4 a[16:19], v60, s[56:59], s68 offen offset:1024 sc1\nQmvav 92,124,24\nQmvav 96,128,24\ns_add_u32 s68, s71, 14336\nQmvav 100,132,24\nbuffer_store_dwordx4 a[20:23], v60, s[56:59], s68 offen offset:1024 sc1\n"
"Qmvav 104,136,24\nQmvav 108,140,24\ns_waitcnt lgkmcnt(0)\nQmvav 112,112,28\ns_waitcnt vmcnt(28)\ns_barrier\nQmvav 116,116,28\nQta 144,61,49472\n"
"s_add_u32 m0, s62, 33024\ns_add_u32 s69, s87, 1572864\nQmvav 120,120,28\nbuffer_load_dwordx4 v62, s[52:55], s69 offen lds\nQmvav 124,124,28\nQta 146,61,50496\nQmvav 128,128,28\nQmvav 132,132,28\n"
"Qta 148,61,49480\ns_add_u32 s69, s87, 1604608\nQmvav 136,136,28\nbuffer_load_dwordx4 v62, s[52:55], s69 offen offset:1024 lds\nQmvav 140,140,28\nQta 150,61,50504\nQmvav 144,112,32\nQmvav 148,116,32\n"
"Qta 152,61,49728\ns_add_u32 s69, s87, 1636344\nQmvav 152,120,32\nbuffer_load_dwordx4 v62, s[52:55], s69 offen offset:2056 lds\nQmvav 156,124,32\nQta 154,61,50752\nQmvav 160,128,32\nQmvav 164,132,32\n"
"Qta 156,61,49736\ns_add_u32 s69, s87, 1668088\nQmvav 168,136,32\nbuffer_load_dwordx4 v62, s[52:55], s69 offen offset:3080 lds\nQmvav 172,140,32\nQta 158,61,50760\nQmvav 176,112,36\nQmvav 180,116,36\n"
"Qta 160,61,49984\ns_add_u32 m0, s63, 0\ns_add_u32 s88, s86, 384\nQmvav 184,120,36\nbuffer_load_dwordx4 v64, s[48:51], s88 offen lds\nQmvav 188,124,36\nQta 162,61,51008\nQmvav 192,128,36\n"
"Qmvav 196,132,36\nQta 164,61,49992\ns_add_u32 s88, s86, 19840\nQmvav 200,136,36\nbuffer_load_dwordx4 v64, s[48:51], s88 offen offset:1024 lds\nQmvav 204,140,36\nQta 166,61,51016\nQmvav 208,112,40\n"
"Qmvav 212,116,40\nQta 168,61,50240\ns_add_u32 s88, s86, 39296\nQmvav 216,120,40\nbuffer_load_dwordx4 v64, s[48:51], s88 offen offset:2048 lds\nQmvav 220,124,40\nQta 170,61,51264\nQmvav 224,128,40\n"
"Qmvav 228,132,40\nQta 172,61,50248\ns_add_u32 s88, s86, 58752\nQmvav 232,136,40\nbuffer_load_dwordx4 v64, s[48:51], s88 offen offset:3072 lds\nQmvav 236,140,40\nQta 174,61,51272\nQmvav 240,112,44\n"
"Qmvav 244,116,44\ns_add_u32 m0, s63, 4096\ns_add_u32 s88, s86, 82304\nQmvav 248,120,44\nbuffer_load_dwordx4 v64, s[48:51], s88 offen lds\nQmvav 252,124,44\nQmaav 208,128,44\nQmaav 212,132,44\n"
"s_add_u32 s88, s86, 101760\nQmaav 216,136,44\nbuffer_load_dwordx4 v64, s[48:51], s88 offen offset:1024 lds\nQmaav 220,140,44\nQmaav 224,112,48\nQmaav 228,116,48\ns_add_u32 s88, s86, 121216\nQmaav 232,120,48\n"
"buffer_load_dwordx4 v64, s[48:51], s88 offen offset:2048 lds\nQmaav 236,124,48\nQmaav 240,128,48\nQmaav 244,132,48\nQmaav 248,136,48\nQmaav 252,140,48\ns_waitcnt lgkmcnt(0)\nQmvaa 80,144,176\n"
"Qmvaa 84,148,176\ns_add_u32 s68, s71, 28672\nQmvaa 88,152,176\nbuffer_store_dwordx4 a[24:27], v60, s[56:59], s68 offen offset:1024 sc1\nQmvaa 92,156,176\nQmvaa 96,160,176\ns_add_u32 s68, s71, 43008\nQmvaa 100,164,176\n"
"buffer_store_dwordx4 a[28:31], v60, s[56:59], s68 offen offset:1024 sc1\nQmvaa 104,168,176\nQmvaa 108,172,176\ns_waitcnt lgkmcnt(0)\nQmvaa 112,144,180\ns_waitcnt vmcnt(21)\ns_barrier\nQmvaa 116,148,180\n"
"Qda 176,63,33536\ns_add_u32 m0, s62, 49472\ns_add_u32 s69, s87, 1835008\nQmvaa 120,152,180\nbuffer_load_dwordx4 v62, s[52:55], s69 offen lds\nQmvaa 124,156,180\nQta 112,61,0\nQmvaa 128,160,180\n"
"Qta 114,61,1024\nQmvaa 132,164,180\nQta 116,61,8\ns_add_u32 s69, s87, 1866752\nQmvaa 136,168,180\nbuffer_load_dwordx4 v62, s[52:55], s69 offen offset:1024 lds\nQmvaa 140,172,180\nQda 180,63,34560\n"
"Qmvaa 144,144,184\nQta 118,61,1032\nQmvaa 148,148,184\nQdv 24,63,33024\ns_add_u32 s69, s87, 1898488\nQmvaa 152,152,184\nbuffer_load_dwordx4 v62, s[52:55], s69 offen offset:2056 lds\nQmvaa 156,156,184\n"
"Qta 120,61,256\nQmvaa 160,160,184\nQta 122,61,1280\nQmvaa 164,164,184\nQta 124,61,264\ns_add_u32 s69, s87, 1930232\nQmvaa 168,168,184\nbuffer_load_dwordx4 v62, s[52:55], s69 offen offset:3080 lds\n"
"Qmvaa 172,172,184\nQda 184,63,35584\nQmvaa 176,144,188\nQta 126,61,1288\nQmvaa 180,148,188\nQta 128,61,512\nQmvaa 184,152,188\nQta 130,61,1536\n"
"Qmvaa 188,156,188\nQta 132,61,520\nQmvaa 192,160,188\nQta 134,61,1544\nQmvaa 196,164,188\nQta 136,61,768\nQmvaa 200,168,188\nQta 138,61,1792\n"
"Qmvaa 204,172,188\nQda 188,63,36608\nQmvaa 208,144,192\nQta 140,61,776\nQmvaa 212,148,192\nQta 142,61,1800\nQmvaa 216,152,192\nQdv 28,63,34048\n"
"Qmvaa 220,156,192\nQdv 32,63,35072\nQmvaa 224,160,192\nQdv 36,63,36096\nQmvaa 228,164,192\nQdv 40,63,37120\nQmvaa 232,168,192\nQdv 44,63,38144\n"
"Qmvaa 236,172,192\nQda 192,63,37632\nQmvaa 240,144,196\nQdv 48,63,39168\nQmvaa 244,148,196\nQmvaa 248,152,196\nQmvaa 252,156,196\nQmaaa 208,160,196\n"
"Qmaaa 212,164,196\nQmaaa 216,168,196\nQmaaa 220,172,196\nQda 196,63,38656\nQmaaa 224,144,200\nQmaaa 228,148,200\nQmaaa 232,152,200\nQmaaa 236,156,200\n"
"Qmaaa 240,160,200\nQmaaa 244,164,200\nQmaaa 248,168,200\nQmaaa 252,172,200\nQda 200,63,39680\ns_waitcnt lgkmcnt(9)\nQmvav 80,112,24\nQmvav 84,116,24\n"
"s_add_u32 s68, s71, 0\nQmvav 88,120,24\nbuffer_store_dwordx4 a[32:35], v60, s[56:59], s68 offen offset:2048 sc1\nQmvav 92,124,24\nQmvav 96,128,24\ns_add_u32 s68, s71, 14336\nQmvav 100,132,24\nbuffer_store_dwordx4 a[36:39], v60, s[56:59], s68 offen offset:2048 sc1\n"
"Qmvav 104,136,24\nQmvav 108,140,24\ns_waitcnt lgkmcnt(0)\nQmvav 112,112,28\ns_waitcnt vmcnt(21)\ns_barrier\nQmvav 116,116,28\nQta 144,61,16448\n"
"s_add_u32 m0, s62, 0\ns_add_u32 s69, s87, 2097152\nQmvav 120,120,28\nbuffer_load_dwordx4 v62, s[52:55], s69 offen lds\nQmvav 124,124,28\nQta 146,61,17472\nQmvav 128,128,28\nQmvav 132,132,28\n"
"Qta 148,61,16456\ns_add_u32 s69, s87, 2128896\nQmvav 136,136,28\nbuffer_load_dwordx4 v62, s[52:55], s69 offen offset:1024 lds\nQmvav 140,140,28\nQta 150,61,17480\nQmvav 144,112,32\nQmvav 148,116,32\n"
"Qta 152,61,16704\ns_add_u32 s69, s87, 2160632\nQmvav 152,120,32\nbuffer_load_dwordx4 v62, s[52:55], s69 offen offset:2056 lds\nQmvav 156,124,32\nQta 154,61,17728\nQmvav 160,128,32\nQmvav 164,132,32\n"
"Qta 156,61,16712\ns_add_u32 s69, s87, 2192376\nQmvav 168,136,32\nbuffer_load_dwordx4 v62, s[52:55], s69 offen offset:3080 lds\nQmvav 172,140,32\nQta 158,61,17736\nQmvav 176,112,36\nQmvav 180,116,36\n"
"Qta 160,61,16960\ns_add_u32 m0, s63, 33024\ns_add_u32 s88, s86, 512\nQmvav 184,120,36\nbuffer_load_dwordx4 v64, s[48:51], s88 offen lds\nQmvav 188,124,36\nQta 162,61,17984\nQmvav 192,128,36\n"
"Qmvav 196,132,36\nQta 164,61,16968\ns_add_u32 s88, s86, 19968\nQmvav 200,136,36\nbuffer_load_dwordx4 v64, s[48:51], s88 offen offset:1024 lds\nQmvav 204,140,36\nQta 166,61,17992\nQmvav 208,112,40\n"
"Qmvav 212,116,40\nQta 168,61,17216\ns_add_u32 s88, s86, 39424\nQmvav 216,120,40\nbuffer_load_dwordx4 v64, s[48:51], s88 offen offset:2048 lds\nQmvav 220,124,40\nQta 170,61,18240\nQmvav 224,128,40\n"
"Qmvav 228,132,40\nQta 172,61,17224\ns_add_u32 s88, s86, 58880\nQmvav 232,136,40\nbuffer_load_dwordx4 v64, s[48:51], s88 offen offset:3072 lds\nQmvav 236,140,40\nQta 174,61,18248\nQmvav 240,112,44\n"
"Qmvav 244,116,44\ns_add_u32 m0, s63, 37120\ns_add_u32 s88, s86, 82432\nQmvav 248,120,44\nbuffer_load_dwordx4 v64, s[48:51], s88 offen lds\nQmvav 252,124,44\nQmaav 208,128,44\nQmaav 212,132,44\n"
"s_add_u32 s88, s86, 101888\nQmaav 216,136,44\nbuffer_load_dwordx4 v64, s[48:51], s88 offen offset:1024 lds\nQmaav 220,140,44\nQmaav 224,112,48\nQmaav 228,116,48\ns_add_u32 s88, s86, 121344\nQmaav 232,120,48\n"
"buffer_load_dwordx4 v64, s[48:51], s88 offen offset:2048 lds\nQmaav 236,124,48\nQmaav 240,128,48\nQmaav 244,132,48\nQmaav 248,136,48\nQmaav 252,140,48\ns_waitcnt lgkmcnt(0)\nQmvaa 80,144,176\n"
"Qmvaa 84,148,176\ns_add_u32 s68, s71, 28672\nQmvaa 88,152,176\nbuffer_store_dwordx4 a[40:43], v60, s[56:59], s68 offen offset:2048 sc1\nQmvaa 92,156,176\nQmvaa 96,160,176\ns_add_u32 s68, s71, 43008\nQmvaa 100,164,176\n"
"buffer_store_dwordx4 a[44:47], v60, s[56:59], s68 offen offset:2048 sc1\nQmvaa 104,168,176\nQmvaa 108,172,176\ns_waitcnt lgkmcnt(0)\nQmvaa 112,144,180\ns_waitcnt vmcnt(21)\ns_barrier\nQmvaa 116,148,180\n"
"Qda 176,63,512\ns_add_u32 m0, s62, 16448\ns_add_u32 s69, s87, 2359296\nQmvaa 120,152,180\nbuffer_load_dwordx4 v62, s[52:55], s69 offen lds\nQmvaa 124,156,180\nQta 112,61,33024\nQmvaa 128,160,180\n"
"Qta 114,61,34048\nQmvaa 132,164,180\nQta 116,61,33032\ns_add_u32 s69, s87, 2391040\nQmvaa 136,168,180\nbuffer_load_dwordx4 v62, s[52:55], s69 offen offset:1024 lds\nQmvaa 140,172,180\nQda 180,63,1536\n"
"Qmvaa 144,144,184\nQta 118,61,34056\nQmvaa 148,148,184\nQdv 24,63,0\ns_add_u32 s69, s87, 2422776\nQmvaa 152,152,184\nbuffer_load_dwordx4 v62, s[52:55], s69 offen offset:2056 lds\nQmvaa 156,156,184\n"
"Qta 120,61,33280\nQmvaa 160,160,184\nQta 122,61,34304\nQmvaa 164,164,184\nQta 124,61,33288\ns_add_u32 s69, s87, 2454520\nQmvaa 168,168,184\nbuffer_load_dwordx4 v62, s[52:55], s69 offen offset:3080 lds\n"
"Qmvaa 172,172,184\nQda 184,63,2560\nQmvaa 176,144,188\nQta 126,61,34312\nQmvaa 180,148,188\nQta 128,61,33536\nQmvaa 184,152,188\nQta 130,61,34560\n"
"Qmvaa 188,156,188\nQta 132,61,33544\nQmvaa 192,160,188\nQta 134,61,34568\nQmvaa 196,164,188\nQta 136,61,33792\nQmvaa 200,168,188\nQta 138,61,34816\n"
"Qmvaa 204,172,188\nQda 188,63,3584\nQmvaa 208,144,192\nQta 140,61,33800\nQmvaa 212,148,192\nQta 142,61,34824\nQmvaa 216,152,192\nQdv 28,63,1024\n"
"Qmvaa 220,156,192\nQdv 32,63,2048\nQmvaa 224,160,192\nQdv 36,63,3072\nQmvaa 228,164,192\nQdv 40,63,4096\nQmvaa 232,168,192\nQdv 44,63,5120\n"
"Qmvaa 236,172,192\nQda 192,63,4608\nQmvaa 240,144,196\nQdv 48,63,6144\nQmvaa 244,148,196\nQmvaa 248,152,196\nQmvaa 252,156,196\nQmaaa 208,160,196\n"
"Qmaaa 212,164,196\nQmaaa 216,168,196\nQmaaa 220,172,196\nQda 196,63,5632\nQmaaa 224,144,200\nQmaaa 228,148,200\nQmaaa 232,152,200\nQmaaa 236,156,200\n"
"Qmaaa 240,160,200\nQmaaa 244,164,200\nQmaaa 248,168,200\nQmaaa 252,172,200\nQda 200,63,6656\ns_waitcnt lgkmcnt(9)\nQmvav 80,112,24\nQmvav 84,116,24\n"
"s_add_u32 s68, s71, 0\nQmvav 88,120,24\nbuffer_store_dwordx4 a[48:51], v60, s[56:59], s68 offen offset:3072 sc1\nQmvav 92,124,24\nQmvav 96,128,24\ns_add_u32 s68, s71, 14336\nQmvav 100,132,24\nbuffer_store_dwordx4 a[52:55], v60, s[56:59], s68 offen offset:3072 sc1\n"
"Qmvav 104,136,24\nQmvav 108,140,24\ns_waitcnt lgkmcnt(0)\nQmvav 112,112,28\ns_waitcnt vmcnt(21)\ns_barrier\nQmvav 116,116,28\nQta 144,61,49472\n"
"s_add_u32 m0, s62, 33024\ns_add_u32 s69, s87, 2621440\nQmvav 120,120,28\nbuffer_load_dwordx4 v62, s[52:55], s69 offen lds\nQmvav 124,124,28\nQta 146,61,50496\nQmvav 128,128,28\nQmvav 132,132,28\n"
"Qta 148,61,49480\ns_add_u32 s69, s87, 2653184\nQmvav 136,136,28\nbuffer_load_dwordx4 v62, s[52:55], s69 offen offset:1024 lds\nQmvav 140,140,28\nQta 150,61,50504\nQmvav 144,112,32\nQmvav 148,116,32\n"
"Qta 152,61,49728\ns_add_u32 s69, s87, 2684920\nQmvav 152,120,32\nbuffer_load_dwordx4 v62, s[52:55], s69 offen offset:2056 lds\nQmvav 156,124,32\nQta 154,61,50752\nQmvav 160,128,32\nQmvav 164,132,32\n"
"Qta 156,61,49736\ns_add_u32 s69, s87, 2716664\nQmvav 168,136,32\nbuffer_load_dwordx4 v62, s[52:55], s69 offen offset:3080 lds\nQmvav 172,140,32\nQta 158,61,50760\nQmvav 176,112,36\nQmvav 180,116,36\n"
"Qta 160,61,49984\ns_add_u32 m0, s63, 0\ns_add_u32 s88, s86, 640\nQmvav 184,120,36\nbuffer_load_dwordx4 v64, s[48:51], s88 offen lds\nQmvav 188,124,36\nQta 162,61,51008\nQmvav 192,128,36\n"
"Qmvav 196,132,36\nQta 164,61,49992\ns_add_u32 s88, s86, 20096\nQmvav 200,136,36\nbuffer_load_dwordx4 v64, s[48:51], s88 offen offset:1024 lds\nQmvav 204,140,36\nQta 166,61,51016\nQmvav 208,112,40\n"
"Qmvav 212,116,40\nQta 168,61,50240\ns_add_u32 s88, s86, 39552\nQmvav 216,120,40\nbuffer_load_dwordx4 v64, s[48:51], s88 offen offset:2048 lds\nQmvav 220,124,40\nQta 170,61,51264\nQmvav 224,128,40\n"
"Qmvav 228,132,40\nQta 172,61,50248\ns_add_u32 s88, s86, 59008\nQmvav 232,136,40\nbuffer_load_dwordx4 v64, s[48:51], s88 offen offset:3072 lds\nQmvav 236,140,40\nQta 174,61,51272\nQmvav 240,112,44\n"
"Qmvav 244,116,44\ns_add_u32 m0, s63, 4096\ns_add_u32 s88, s86, 82560\nQmvav 248,120,44\nbuffer_load_dwordx4 v64, s[48:51], s88 offen lds\nQmvav 252,124,44\nQmaav 208,128,44\nQmaav 212,132,44\n"
"s_add_u32 s88, s86, 102016\nQmaav 216,136,44\nbuffer_load_dwordx4 v64, s[48:51], s88 offen offset:1024 lds\nQmaav 220,140,44\nQmaav 224,112,48\nQmaav 228,116,48\ns_add_u32 s88, s86, 121472\nQmaav 232,120,48\n"
"buffer_load_dwordx4 v64, s[48:51], s88 offen offset:2048 lds\nQmaav 236,124,48\nQmaav 240,128,48\nQmaav 244,132,48\nQmaav 248,136,48\nQmaav 252,140,48\ns_waitcnt lgkmcnt(0)\nQmvaa 80,144,176\n"
"Qmvaa 84,148,176\ns_add_u32 s68, s71, 28672\nQmvaa 88,152,176\nbuffer_store_dwordx4 a[56:59], v60, s[56:59], s68 offen offset:3072 sc1\nQmvaa 92,156,176\nQmvaa 96,160,176\ns_add_u32 s68, s71, 43008\nQmvaa 100,164,176\n"
"buffer_store_dwordx4 a[60:63], v60, s[56:59], s68 offen offset:3072 sc1\nQmvaa 104,168,176\nQmvaa 108,172,176\ns_waitcnt lgkmcnt(0)\nQmvaa 112,144,180\ns_waitcnt vmcnt(21)\ns_barrier\nQmvaa 116,148,180\n"
"Qda 176,63,33536\ns_add_u32 m0, s62, 49472\ns_add_u32 s69, s87, 2883584\nQmvaa 120,152,180\nbuffer_load_dwordx4 v62, s[52:55], s69 offen lds\nQmvaa 124,156,180\nQta 112,61,0\nQmvaa 128,160,180\n"
"Qta 114,61,1024\nQmvaa 132,164,180\nQta 116,61,8\ns_add_u32 s69, s87, 2915328\nQmvaa 136,168,180\nbuffer_load_dwordx4 v62, s[52:55], s69 offen offset:1024 lds\nQmvaa 140,172,180\nQda 180,63,34560\n"
"Qmvaa 144,144,184\nQta 118,61,1032\nQmvaa 148,148,184\nQdv 24,63,33024\ns_add_u32 s69, s87, 2947064\nQmvaa 152,152,184\nbuffer_load_dwordx4 v62, s[52:55], s69 offen offset:2056 lds\nQmvaa 156,156,184\n"
"Qta 120,61,256\nQmvaa 160,160,184\nQta 122,61,1280\nQmvaa 164,164,184\nQta 124,61,264\ns_add_u32 s69, s87, 2978808\nQmvaa 168,168,184\nbuffer_load_dwordx4 v62, s[52:55], s69 offen offset:3080 lds\n"
"Qmvaa 172,172,184\nQda 184,63,35584\nQmvaa 176,144,188\nQta 126,61,1288\nQmvaa 180,148,188\nQta 128,61,512\nQmvaa 184,152,188\nQta 130,61,1536\n"
"Qmvaa 188,156,188\nQta 132,61,520\nQmvaa 192,160,188\nQta 134,61,1544\nQmvaa 196,164,188\nQta 136,61,768\nQmvaa 200,168,188\nQta 138,61,1792\n"
"Qmvaa 204,172,188\nQda 188,63,36608\nQmvaa 208,144,192\nQta 140,61,776\nQmvaa 212,148,192\nQta 142,61,1800\nQmvaa 216,152,192\nQdv 28,63,34048\n"
"Qmvaa 220,156,192\nQdv 32,63,35072\nQmvaa 224,160,192\nQdv 36,63,36096\nQmvaa 228,164,192\nQdv 40,63,37120\nQmvaa 232,168,192\nQdv 44,63,38144\n"
"Qmvaa 236,172,192\nQda 192,63,37632\nQmvaa 240,144,196\nQdv 48,63,39168\nQmvaa 244,148,196\nQmvaa 248,152,196\nQmvaa 252,156,196\nQmaaa 208,160,196\n"
"Qmaaa 212,164,196\nQmaaa 216,168,196\nQmaaa 220,172,196\nQda 196,63,38656\nQmaaa 224,144,200\nQmaaa 228,148,200\nQmaaa 232,152,200\nQmaaa 236,156,200\n"
"Qmaaa 240,160,200\nQmaaa 244,164,200\nQmaaa 248,168,200\nQmaaa 252,172,200\nQda 200,63,39680\ns_waitcnt lgkmcnt(9)\nQmvav 80,112,24\nQmvav 84,116,24\n"
"s_add_u32 s68, s71, 4096\nQmvav 88,120,24\nbuffer_store_dwordx4 a[64:67], v60, s[56:59], s68 offen offset:0 sc1\nQmvav 92,124,24\nQmvav 96,128,24\ns_add_u32 s68, s71, 18432\nQmvav 100,132,24\nbuffer_store_dwordx4 a[68:71], v60, s[56:59], s68 offen offset:0 sc1\n"
"Qmvav 104,136,24\nQmvav 108,140,24\ns_waitcnt lgkmcnt(0)\nQmvav 112,112,28\ns_waitcnt vmcnt(21)\ns_barrier\nQmvav 116,116,28\nQta 144,61,16448\n"
"s_add_u32 m0, s62, 0\ns_add_u32 s69, s87, 3145728\nQmvav 120,120,28\nbuffer_load_dwordx4 v62, s[52:55], s69 offen lds\nQmvav 124,124,28\nQta 146,61,17472\nQmvav 128,128,28\nQmvav 132,132,28\n"
"Qta 148,61,16456\ns_add_u32 s69, s87, 3177472\nQmvav 136,136,28\nbuffer_load_dwordx4 v62, s[52:55], s69 offen offset:1024 lds\nQmvav 140,140,28\nQta 150,61,17480\nQmvav 144,112,32\nQmvav 148,116,32\n"
"Qta 152,61,16704\ns_add_u32 s69, s87, 3209208\nQmvav 152,120,32\nbuffer_load_dwordx4 v62, s[52:55], s69 offen offset:2056 lds\nQmvav 156,124,32\nQta 154,61,17728\nQmvav 160,128,32\nQmvav 164,132,32\n"
"Qta 156,61,16712\ns_add_u32 s69, s87, 3240952\nQmvav 168,136,32\nbuffer_load_dwordx4 v62, s[52:55], s69 offen offset:3080 lds\nQmvav 172,140,32\nQta 158,61,17736\nQmvav 176,112,36\nQmvav 180,116,36\n"
"Qta 160,61,16960\ns_add_u32 m0, s63, 33024\ns_add_u32 s88, s86, 768\nQmvav 184,120,36\nbuffer_load_dwordx4 v64, s[48:51], s88 offen lds\nQmvav 188,124,36\nQta 162,61,17984\nQmvav 192,128,36\n"
"Qmvav 196,132,36\nQta 164,61,16968\ns_add_u32 s88, s86, 20224\nQmvav 200,136,36\nbuffer_load_dwordx4 v64, s[48:51], s88 offen offset:1024 lds\nQmvav 204,140,36\nQta 166,61,17992\nQmvav 208,112,40\n"
"Qmvav 212,116,40\nQta 168,61,17216\ns_add_u32 s88, s86, 39680\nQmvav 216,120,40\nbuffer_load_dwordx4 v64, s[48:51], s88 offen offset:2048 lds\nQmvav 220,124,40\nQta 170,61,18240\nQmvav 224,128,40\n"
"Qmvav 228,132,40\nQta 172,61,17224\ns_add_u32 s88, s86, 59136\nQmvav 232,136,40\nbuffer_load_dwordx4 v64, s[48:51], s88 offen offset:3072 lds\nQmvav 236,140,40\nQta 174,61,18248\nQmvav 240,112,44\n"
"Qmvav 244,116,44\ns_add_u32 m0, s63, 37120\ns_add_u32 s88, s86, 82688\nQmvav 248,120,44\nbuffer_load_dwordx4 v64, s[48:51], s88 offen lds\nQmvav 252,124,44\nQmaav 208,128,44\nQmaav 212,132,44\n"
"s_add_u32 s88, s86, 102144\nQmaav 216,136,44\nbuffer_load_dwordx4 v64, s[48:51], s88 offen offset:1024 lds\nQmaav 220,140,44\nQmaav 224,112,48\nQmaav 228,116,48\ns_add_u32 s88, s86, 121600\nQmaav 232,120,48\n"
"buffer_load_dwordx4 v64, s[48:51], s88 offen offset:2048 lds\nQmaav 236,124,48\nQmaav 240,128,48\nQmaav 244,132,48\nQmaav 248,136,48\nQmaav 252,140,48\ns_waitcnt lgkmcnt(0)\nQmvaa 80,144,176\n"
"Qmvaa 84,148,176\ns_add_u32 s68, s71, 32768\nQmvaa 88,152,176\nbuffer_store_dwordx4 a[72:75], v60, s[56:59], s68 offen offset:0 sc1\nQmvaa 92,156,176\nQmvaa 96,160,176\ns_add_u32 s68, s71, 47104\nQmvaa 100,164,176\n"
"buffer_store_dwordx4 a[76:79], v60, s[56:59], s68 offen offset:0 sc1\nQmvaa 104,168,176\nQmvaa 108,172,176\ns_waitcnt lgkmcnt(0)\nQmvaa 112,144,180\ns_waitcnt vmcnt(21)\ns_barrier\nQmvaa 116,148,180\n"
"Qda 176,63,512\ns_add_u32 m0, s62, 16448\ns_add_u32 s69, s87, 3407872\nQmvaa 120,152,180\nbuffer_load_dwordx4 v62, s[52:55], s69 offen lds\nQmvaa 124,156,180\nQta 112,61,33024\nQmvaa 128,160,180\n"
"Qta 114,61,34048\nQmvaa 132,164,180\nQta 116,61,33032\ns_add_u32 s69, s87, 3439616\nQmvaa 136,168,180\nbuffer_load_dwordx4 v62, s[52:55], s69 offen offset:1024 lds\nQmvaa 140,172,180\nQda 180,63,1536\n"
"Qmvaa 144,144,184\nQta 118,61,34056\nQmvaa 148,148,184\nQdv 24,63,0\ns_add_u32 s69, s87, 3471352\nQmvaa 152,152,184\nbuffer_load_dwordx4 v62, s[52:55], s69 offen offset:2056 lds\nQmvaa 156,156,184\n"
"Qta 120,61,33280\nQmvaa 160,160,184\nQta 122,61,34304\nQmvaa 164,164,184\nQta 124,61,33288\ns_add_u32 s69, s87, 3503096\nQmvaa 168,168,184\nbuffer_load_dwordx4 v62, s[52:55], s69 offen offset:3080 lds\n"
"Qmvaa 172,172,184\nQda 184,63,2560\nQmvaa 176,144,188\nQta 126,61,34312\nQmvaa 180,148,188\nQta 128,61,33536\nQmvaa 184,152,188\nQta 130,61,34560\n"
"Qmvaa 188,156,188\nQta 132,61,33544\nQmvaa 192,160,188\nQta 134,61,34568\nQmvaa 196,164,188\nQta 136,61,33792\nQmvaa 200,168,188\nQta 138,61,34816\n"
"Qmvaa 204,172,188\nQda 188,63,3584\nQmvaa 208,144,192\nQta 140,61,33800\nQmvaa 212,148,192\nQta 142,61,34824\nQmvaa 216,152,192\nQdv 28,63,1024\n"
"Qmvaa 220,156,192\nQdv 32,63,2048\nQmvaa 224,160,192\nQdv 36,63,3072\nQmvaa 228,164,192\nQdv 40,63,4096\nQmvaa 232,168,192\nQdv 44,63,5120\n"
"Qmvaa 236,172,192\nQda 192,63,4608\nQmvaa 240,144,196\nQdv 48,63,6144\nQmvaa 244,148,196\nQmvaa 248,152,196\nQmvaa 252,156,196\nQmaaa 208,160,196\n"
"Qmaaa 212,164,196\nQmaaa 216,168,196\nQmaaa 220,172,196\nQda 196,63,5632\nQmaaa 224,144,200\nQmaaa 228,148,200\nQmaaa 232,152,200\nQmaaa 236,156,200\n"
"Qmaaa 240,160,200\nQmaaa 244,164,200\nQmaaa 248,168,200\nQmaaa 252,172,200\nQda 200,63,6656\ns_waitcnt lgkmcnt(9)\nQmvav 80,112,24\nQmvav 84,116,24\n"
"s_add_u32 s68, s71, 4096\nQmvav 88,120,24\nbuffer_store_dwordx4 a[80:83], v60, s[56:59], s68 offen offset:1024 sc1\nQmvav 92,124,24\nQmvav 96,128,24\ns_add_u32 s68, s71, 18432\nQmvav 100,132,24\nbuffer_store_dwordx4 a[84:87], v60, s[56:59], s68 offen offset:1024 sc1\n"
"Qmvav 104,136,24\nQmvav 108,140,24\ns_waitcnt lgkmcnt(0)\nQmvav 112,112,28\ns_waitcnt vmcnt(21)\ns_barrier\nQmvav 116,116,28\nQta 144,61,49472\n"
"s_add_u32 m0, s62, 33024\ns_add_u32 s69, s87, 3670016\nQmvav 120,120,28\nbuffer_load_dwordx4 v62, s[52:55], s69 offen lds\nQmvav 124,124,28\nQta 146,61,50496\nQmvav 128,128,28\nQmvav 132,132,28\n"
"Qta 148,61,49480\ns_add_u32 s69, s87, 3701760\nQmvav 136,136,28\nbuffer_load_dwordx4 v62, s[52:55], s69 offen offset:1024 lds\nQmvav 140,140,28\nQta 150,61,50504\nQmvav 144,112,32\nQmvav 148,116,32\n"
"Qta 152,61,49728\ns_add_u32 s69, s87, 3733496\nQmvav 152,120,32\nbuffer_load_dwordx4 v62, s[52:55], s69 offen offset:2056 lds\nQmvav 156,124,32\nQta 154,61,50752\nQmvav 160,128,32\nQmvav 164,132,32\n"
"Qta 156,61,49736\ns_add_u32 s69, s87, 3765240\nQmvav 168,136,32\nbuffer_load_dwordx4 v62, s[52:55], s69 offen offset:3080 lds\nQmvav 172,140,32\nQta 158,61,50760\nQmvav 176,112,36\nQmvav 180,116,36\n"
"Qta 160,61,49984\ns_add_u32 m0, s63, 0\ns_add_u32 s88, s86, 896\nQmvav 184,120,36\nbuffer_load_dwordx4 v64, s[48:51], s88 offen lds\nQmvav 188,124,36\nQta 162,61,51008\nQmvav 192,128,36\n"
"Qmvav 196,132,36\nQta 164,61,49992\ns_add_u32 s88, s86, 20352\nQmvav 200,136,36\nbuffer_load_dwordx4 v64, s[48:51], s88 offen offset:1024 lds\nQmvav 204,140,36\nQta 166,61,51016\nQmvav 208,112,40\n"
"Qmvav 212,116,40\nQta 168,61,50240\ns_add_u32 s88, s86, 39808\nQmvav 216,120,40\nbuffer_load_dwordx4 v64, s[48:51], s88 offen offset:2048 lds\nQmvav 220,124,40\nQta 170,61,51264\nQmvav 224,128,40\n"
"Qmvav 228,132,40\nQta 172,61,50248\ns_add_u32 s88, s86, 59264\nQmvav 232,136,40\nbuffer_load_dwordx4 v64, s[48:51], s88 offen offset:3072 lds\nQmvav 236,140,40\nQta 174,61,51272\nQmvav 240,112,44\n"
"Qmvav 244,116,44\ns_add_u32 m0, s63, 4096\ns_add_u32 s88, s86, 82816\nQmvav 248,120,44\nbuffer_load_dwordx4 v64, s[48:51], s88 offen lds\nQmvav 252,124,44\nQmaav 208,128,44\nQmaav 212,132,44\n"
"s_add_u32 s88, s86, 102272\nQmaav 216,136,44\nbuffer_load_dwordx4 v64, s[48:51], s88 offen offset:1024 lds\nQmaav 220,140,44\nQmaav 224,112,48\nQmaav 228,116,48\ns_add_u32 s88, s86, 121728\nQmaav 232,120,48\n"
"buffer_load_dwordx4 v64, s[48:51], s88 offen offset:2048 lds\nQmaav 236,124,48\nQmaav 240,128,48\nQmaav 244,132,48\nQmaav 248,136,48\nQmaav 252,140,48\ns_waitcnt lgkmcnt(0)\nQmvaa 80,144,176\n"
"Qmvaa 84,148,176\ns_add_u32 s68, s71, 32768\nQmvaa 88,152,176\nbuffer_store_dwordx4 a[88:91], v60, s[56:59], s68 offen offset:1024 sc1\nQmvaa 92,156,176\nQmvaa 96,160,176\ns_add_u32 s68, s71, 47104\nQmvaa 100,164,176\n"
"buffer_store_dwordx4 a[92:95], v60, s[56:59], s68 offen offset:1024 sc1\nQmvaa 104,168,176\nQmvaa 108,172,176\ns_waitcnt lgkmcnt(0)\nQmvaa 112,144,180\ns_waitcnt vmcnt(21)\ns_barrier\nQmvaa 116,148,180\n"
"Qda 176,63,33536\ns_add_u32 m0, s62, 49472\ns_add_u32 s69, s87, 3932160\nQmvaa 120,152,180\nbuffer_load_dwordx4 v62, s[52:55], s69 offen lds\nQmvaa 124,156,180\nQta 112,61,0\nQmvaa 128,160,180\n"
"Qta 114,61,1024\nQmvaa 132,164,180\nQta 116,61,8\ns_add_u32 s69, s87, 3963904\nQmvaa 136,168,180\nbuffer_load_dwordx4 v62, s[52:55], s69 offen offset:1024 lds\nQmvaa 140,172,180\nQda 180,63,34560\n"
"Qmvaa 144,144,184\nQta 118,61,1032\nQmvaa 148,148,184\nQdv 24,63,33024\ns_add_u32 s69, s87, 3995640\nQmvaa 152,152,184\nbuffer_load_dwordx4 v62, s[52:55], s69 offen offset:2056 lds\nQmvaa 156,156,184\n"
"Qta 120,61,256\nQmvaa 160,160,184\nQta 122,61,1280\nQmvaa 164,164,184\nQta 124,61,264\ns_add_u32 s69, s87, 4027384\nQmvaa 168,168,184\nbuffer_load_dwordx4 v62, s[52:55], s69 offen offset:3080 lds\n"
"Qmvaa 172,172,184\nQda 184,63,35584\nQmvaa 176,144,188\nQta 126,61,1288\nQmvaa 180,148,188\nQta 128,61,512\nQmvaa 184,152,188\nQta 130,61,1536\n"
"Qmvaa 188,156,188\nQta 132,61,520\nQmvaa 192,160,188\nQta 134,61,1544\nQmvaa 196,164,188\nQta 136,61,768\nQmvaa 200,168,188\nQta 138,61,1792\n"
"Qmvaa 204,172,188\nQda 188,63,36608\nQmvaa 208,144,192\nQta 140,61,776\nQmvaa 212,148,192\nQta 142,61,1800\nQmvaa 216,152,192\nQdv 28,63,34048\n"
"Qmvaa 220,156,192\nQdv 32,63,35072\nQmvaa 224,160,192\nQdv 36,63,36096\nQmvaa 228,164,192\nQdv 40,63,37120\nQmvaa 232,168,192\nQdv 44,63,38144\n"
"Qmvaa 236,172,192\nQda 192,63,37632\nQmvaa 240,144,196\nQdv 48,63,39168\nQmvaa 244,148,196\nQmvaa 248,152,196\nQmvaa 252,156,196\nQmaaa 208,160,196\n"
"Qmaaa 212,164,196\nQmaaa 216,168,196\nQmaaa 220,172,196\nQda 196,63,38656\nQmaaa 224,144,200\nQmaaa 228,148,200\nQmaaa 232,152,200\nQmaaa 236,156,200\n"
"Qmaaa 240,160,200\nQmaaa 244,164,200\nQmaaa 248,168,200\nQmaaa 252,172,200\nQda 200,63,39680\ns_waitcnt lgkmcnt(9)\nQmvav 80,112,24\nQmvav 84,116,24\n"
"s_add_u32 s68, s71, 4096\nQmvav 88,120,24\nbuffer_store_dwordx4 a[96:99], v60, s[56:59], s68 offen offset:2048 sc1\nQmvav 92,124,24\nQmvav 96,128,24\ns_add_u32 s68, s71, 18432\nQmvav 100,132,24\nbuffer_store_dwordx4 a[100:103], v60, s[56:59], s68 offen offset:2048 sc1\n"
"Qmvav 104,136,24\nQmvav 108,140,24\ns_waitcnt lgkmcnt(0)\nQmvav 112,112,28\ns_waitcnt vmcnt(21)\ns_barrier\nQmvav 116,116,28\nQta 144,61,16448\n"
"s_add_u32 m0, s62, 0\ns_add_u32 s69, s87, 4194304\nQmvav 120,120,28\nbuffer_load_dwordx4 v62, s[52:55], s69 offen lds\nQmvav 124,124,28\nQta 146,61,17472\nQmvav 128,128,28\nQmvav 132,132,28\n"
"Qta 148,61,16456\ns_add_u32 s69, s87, 4226048\nQmvav 136,136,28\nbuffer_load_dwordx4 v62, s[52:55], s69 offen offset:1024 lds\nQmvav 140,140,28\nQta 150,61,17480\nQmvav 144,112,32\nQmvav 148,116,32\n"
"Qta 152,61,16704\ns_add_u32 s69, s87, 4257784\nQmvav 152,120,32\nbuffer_load_dwordx4 v62, s[52:55], s69 offen offset:2056 lds\nQmvav 156,124,32\nQta 154,61,17728\nQmvav 160,128,32\nQmvav 164,132,32\n"
"Qta 156,61,16712\ns_add_u32 s69, s87, 4289528\nQmvav 168,136,32\nbuffer_load_dwordx4 v62, s[52:55], s69 offen offset:3080 lds\nQmvav 172,140,32\nQta 158,61,17736\nQmvav 176,112,36\nQmvav 180,116,36\n"
"Qta 160,61,16960\ns_add_u32 m0, s63, 33024\ns_add_u32 s88, s86, 1024\nQmvav 184,120,36\nbuffer_load_dwordx4 v64, s[48:51], s88 offen lds\nQmvav 188,124,36\nQta 162,61,17984\nQmvav 192,128,36\n"
"Qmvav 196,132,36\nQta 164,61,16968\ns_add_u32 s88, s86, 20480\nQmvav 200,136,36\nbuffer_load_dwordx4 v64, s[48:51], s88 offen offset:1024 lds\nQmvav 204,140,36\nQta 166,61,17992\nQmvav 208,112,40\n"
"Qmvav 212,116,40\nQta 168,61,17216\ns_add_u32 s88, s86, 39936\nQmvav 216,120,40\nbuffer_load_dwordx4 v64, s[48:51], s88 offen offset:2048 lds\nQmvav 220,124,40\nQta 170,61,18240\nQmvav 224,128,40\n"
"Qmvav 228,132,40\nQta 172,61,17224\ns_add_u32 s88, s86, 59392\nQmvav 232,136,40\nbuffer_load_dwordx4 v64, s[48:51], s88 offen offset:3072 lds\nQmvav 236,140,40\nQta 174,61,18248\nQmvav 240,112,44\n"
"Qmvav 244,116,44\ns_add_u32 m0, s63, 37120\ns_add_u32 s88, s86, 82944\nQmvav 248,120,44\nbuffer_load_dwordx4 v64, s[48:51], s88 offen lds\nQmvav 252,124,44\nQmaav 208,128,44\nQmaav 212,132,44\n"
"s_add_u32 s88, s86, 102400\nQmaav 216,136,44\nbuffer_load_dwordx4 v64, s[48:51], s88 offen offset:1024 lds\nQmaav 220,140,44\nQmaav 224,112,48\nQmaav 228,116,48\ns_add_u32 s88, s86, 121856\nQmaav 232,120,48\n"
"buffer_load_dwordx4 v64, s[48:51], s88 offen offset:2048 lds\nQmaav 236,124,48\nQmaav 240,128,48\nQmaav 244,132,48\nQmaav 248,136,48\nQmaav 252,140,48\ns_waitcnt lgkmcnt(0)\nQmvaa 80,144,176\n"
"Qmvaa 84,148,176\ns_add_u32 s68, s71, 32768\nQmvaa 88,152,176\nbuffer_store_dwordx4 a[104:107], v60, s[56:59], s68 offen offset:2048 sc1\nQmvaa 92,156,176\nQmvaa 96,160,176\ns_add_u32 s68, s71, 47104\nQmvaa 100,164,176\n"
"buffer_store_dwordx4 a[108:111], v60, s[56:59], s68 offen offset:2048 sc1\nQmvaa 104,168,176\nQmvaa 108,172,176\ns_waitcnt lgkmcnt(0)\nQmvaa 112,144,180\ns_waitcnt vmcnt(21)\ns_barrier\nQmvaa 116,148,180\n"
"Qda 176,63,512\ns_add_u32 m0, s62, 16448\ns_add_u32 s69, s87, 4456448\nQmvaa 120,152,180\nbuffer_load_dwordx4 v62, s[52:55], s69 offen lds\nQmvaa 124,156,180\nQta 112,61,33024\nQmvaa 128,160,180\n"
"Qta 114,61,34048\nQmvaa 132,164,180\nQta 116,61,33032\ns_add_u32 s69, s87, 4488192\nQmvaa 136,168,180\nbuffer_load_dwordx4 v62, s[52:55], s69 offen offset:1024 lds\nQmvaa 140,172,180\nQda 180,63,1536\n"
"Qmvaa 144,144,184\nQta 118,61,34056\nQmvaa 148,148,184\nQdv 24,63,0\ns_add_u32 s69, s87, 4519928\nQmvaa 152,152,184\nbuffer_load_dwordx4 v62, s[52:55], s69 offen offset:2056 lds\nQmvaa 156,156,184\n"
"Qta 120,61,33280\nQmvaa 160,160,184\nQta 122,61,34304\nQmvaa 164,164,184\nQta 124,61,33288\ns_add_u32 s69, s87, 4551672\nQmvaa 168,168,184\nbuffer_load_dwordx4 v62, s[52:55], s69 offen offset:3080 lds\n"
"Qmvaa 172,172,184\nQda 184,63,2560\nQmvaa 176,144,188\nQta 126,61,34312\nQmvaa 180,148,188\nQta 128,61,33536\nQmvaa 184,152,188\nQta 130,61,34560\n"
"Qmvaa 188,156,188\nQta 132,61,33544\nQmvaa 192,160,188\nQta 134,61,34568\nQmvaa 196,164,188\nQta 136,61,33792\nQmvaa 200,168,188\nQta 138,61,34816\n"
"Qmvaa 204,172,188\nQda 188,63,3584\nQmvaa 208,144,192\nQta 140,61,33800\nQmvaa 212,148,192\nQta 142,61,34824\nQmvaa 216,152,192\nQdv 28,63,1024\n"
"Qmvaa 220,156,192\nQdv 32,63,2048\nQmvaa 224,160,192\nQdv 36,63,3072\nQmvaa 228,164,192\nQdv 40,63,4096\nQmvaa 232,168,192\nQdv 44,63,5120\n"
"Qmvaa 236,172,192\nQda 192,63,4608\nQmvaa 240,144,196\nQdv 48,63,6144\nQmvaa 244,148,196\nQmvaa 248,152,196\nQmvaa 252,156,196\nQmaaa 208,160,196\n"
"Qmaaa 212,164,196\nQmaaa 216,168,196\nQmaaa 220,172,196\nQda 196,63,5632\nQmaaa 224,144,200\nQmaaa 228,148,200\nQmaaa 232,152,200\nQmaaa 236,156,200\n"
"Qmaaa 240,160,200\nQmaaa 244,164,200\nQmaaa 248,168,200\nQmaaa 252,172,200\nQda 200,63,6656\ns_waitcnt lgkmcnt(9)\nQmvav 80,112,24\nQmvav 84,116,24\n"
"Qmvav 88,120,24\nQmvav 92,124,24\nQmvav 96,128,24\nQmvav 100,132,24\nQmvav 104,136,24\nQmvav 108,140,24\ns_waitcnt lgkmcnt(0)\nQmvav 112,112,28\n"
"s_waitcnt vmcnt(19)\ns_barrier\nQmvav 116,116,28\nQta 144,61,49472\ns_add_u32 m0, s62, 33024\ns_add_u32 s69, s87, 4718592\nQmvav 120,120,28\nbuffer_load_dwordx4 v62, s[52:55], s69 offen lds\n"
"Qmvav 124,124,28\nQta 146,61,50496\nQmvav 128,128,28\nQmvav 132,132,28\nQta 148,61,49480\ns_add_u32 s69, s87, 4750336\nQmvav 136,136,28\nbuffer_load_dwordx4 v62, s[52:55], s69 offen offset:1024 lds\n"
"Qmvav 140,140,28\nQta 150,61,50504\nQmvav 144,112,32\nQmvav 148,116,32\nQta 152,61,49728\ns_add_u32 s69, s87, 4782072\nQmvav 152,120,32\nbuffer_load_dwordx4 v62, s[52:55], s69 offen offset:2056 lds\n"
"Qmvav 156,124,32\nQta 154,61,50752\nQmvav 160,128,32\nQmvav 164,132,32\nQta 156,61,49736\ns_add_u32 s69, s87, 4813816\nQmvav 168,136,32\nbuffer_load_dwordx4 v62, s[52:55], s69 offen offset:3080 lds\n"
"Qmvav 172,140,32\nQta 158,61,50760\nQmvav 176,112,36\nQmvav 180,116,36\nQta 160,61,49984\ns_add_u32 m0, s63, 0\ns_add_u32 s88, s86, 1152\nQmvav 184,120,36\n"
"buffer_load_dwordx4 v64, s[48:51], s88 offen lds\nQmvav 188,124,36\nQta 162,61,51008\nQmvav 192,128,36\nQmvav 196,132,36\nQta 164,61,49992\ns_add_u32 s88, s86, 20608\nQmvav 200,136,36\n"
"buffer_load_dwordx4 v64, s[48:51], s88 offen offset:1024 lds\nQmvav 204,140,36\nQta 166,61,51016\nQmvav 208,112,40\nQmvav 212,116,40\nQta 168,61,50240\ns_add_u32 s88, s86, 40064\nQmvav 216,120,40\n"
"buffer_load_dwordx4 v64, s[48:51], s88 offen offset:2048 lds\nQmvav 220,124,40\nQta 170,61,51264\nQmvav 224,128,40\nQmvav 228,132,40\nQta 172,61,50248\ns_add_u32 s88, s86, 59520\nQmvav 232,136,40\n"
"buffer_load_dwordx4 v64, s[48:51], s88 offen offset:3072 lds\nQmvav 236,140,40\nQta 174,61,51272\nQmvav 240,112,44\nQmvav 244,116,44\ns_add_u32 m0, s63, 4096\ns_add_u32 s88, s86, 83072\nQmvav 248,120,44\n"
"buffer_load_dwordx4 v64, s[48:51], s88 offen lds\nQmvav 252,124,44\nQmaav 208,128,44\nQmaav 212,132,44\ns_add_u32 s88, s86, 102528\nQmaav 216,136,44\nbuffer_load_dwordx4 v64, s[48:51], s88 offen offset:1024 lds\nQmaav 220,140,44\n"
"Qmaav 224,112,48\nQmaav 228,116,48\ns_add_u32 s88, s86, 121984\nQmaav 232,120,48\nbuffer_load_dwordx4 v64, s[48:51], s88 offen offset:2048 lds\nQmaav 236,124,48\nQmaav 240,128,48\nQmaav 244,132,48\n"
"Qmaav 248,136,48\nQmaav 252,140,48\ns_waitcnt lgkmcnt(0)\nQmvaa 80,144,176\nQmvaa 84,148,176\nQmvaa 88,152,176\nQmvaa 92,156,176\nQmvaa 96,160,176\n"
"Qmvaa 100,164,176\nQmvaa 104,168,176\nQmvaa 108,172,176\ns_waitcnt lgkmcnt(0)\nQmvaa 112,144,180\ns_waitcnt vmcnt(17)\ns_barrier\nQmvaa 116,148,180\n"
"Qda 176,63,33536\ns_add_u32 m0, s62, 49472\ns_add_u32 s69, s87, 4980736\nQmvaa 120,152,180\nbuffer_load_dwordx4 v62, s[52:55], s69 offen lds\nQmvaa 124,156,180\nQta 112,61,0\nQmvaa 128,160,180\n"
"Qta 114,61,1024\nQmvaa 132,164,180\nQta 116,61,8\ns_add_u32 s69, s87, 5012480\nQmvaa 136,168,180\nbuffer_load_dwordx4 v62, s[52:55], s69 offen offset:1024 lds\nQmvaa 140,172,180\nQda 180,63,34560\n"
"Qmvaa 144,144,184\nQta 118,61,1032\nQmvaa 148,148,184\nQdv 24,63,33024\ns_add_u32 s69, s87, 5044216\nQmvaa 152,152,184\nbuffer_load_dwordx4 v62, s[52:55], s69 offen offset:2056 lds\nQmvaa 156,156,184\n"
"Qta 120,61,256\nQmvaa 160,160,184\nQta 122,61,1280\nQmvaa 164,164,184\nQta 124,61,264\ns_add_u32 s69, s87, 5075960\nQmvaa 168,168,184\nbuffer_load_dwordx4 v62, s[52:55], s69 offen offset:3080 lds\n"
"Qmvaa 172,172,184\nQda 184,63,35584\nQmvaa 176,144,188\nQta 126,61,1288\nQmvaa 180,148,188\nQta 128,61,512\nQmvaa 184,152,188\nQta 130,61,1536\n"
"Qmvaa 188,156,188\nQta 132,61,520\nQmvaa 192,160,188\nQta 134,61,1544\nQmvaa 196,164,188\nQta 136,61,768\nQmvaa 200,168,188\nQta 138,61,1792\n"
"Qmvaa 204,172,188\nQda 188,63,36608\nQmvaa 208,144,192\nQta 140,61,776\nQmvaa 212,148,192\nQta 142,61,1800\nQmvaa 216,152,192\nQdv 28,63,34048\n"
"Qmvaa 220,156,192\nQdv 32,63,35072\nQmvaa 224,160,192\nQdv 36,63,36096\nQmvaa 228,164,192\nQdv 40,63,37120\nQmvaa 232,168,192\nQdv 44,63,38144\n"
"Qmvaa 236,172,192\nQda 192,63,37632\nQmvaa 240,144,196\nQdv 48,63,39168\nQmvaa 244,148,196\nQmvaa 248,152,196\nQmvaa 252,156,196\nQmaaa 208,160,196\n"
"Qmaaa 212,164,196\nQmaaa 216,168,196\nQmaaa 220,172,196\nQda 196,63,38656\nQmaaa 224,144,200\nQmaaa 228,148,200\nQmaaa 232,152,200\nQmaaa 236,156,200\n"
"Qmaaa 240,160,200\nQmaaa 244,164,200\nQmaaa 248,168,200\nQmaaa 252,172,200\nQda 200,63,39680\ns_waitcnt lgkmcnt(9)\nQmvav 80,112,24\nQmvav 84,116,24\n"
"Qmvav 88,120,24\nQmvav 92,124,24\nQmvav 96,128,24\nQmvav 100,132,24\nQmvav 104,136,24\nQmvav 108,140,24\ns_waitcnt lgkmcnt(0)\nQmvav 112,112,28\n"
"s_waitcnt vmcnt(15)\ns_barrier\nQmvav 116,116,28\nQta 144,61,16448\nQmvav 120,120,28\nQmvav 124,124,28\nQta 146,61,17472\nQmvav 128,128,28\n"
"Qmvav 132,132,28\nQta 148,61,16456\nQmvav 136,136,28\nQmvav 140,140,28\nQta 150,61,17480\nQmvav 144,112,32\nQmvav 148,116,32\nQta 152,61,16704\n"
"Qmvav 152,120,32\nQmvav 156,124,32\nQta 154,61,17728\nQmvav 160,128,32\nQmvav 164,132,32\nQta 156,61,16712\nQmvav 168,136,32\nQmvav 172,140,32\n"
"Qta 158,61,17736\nQmvav 176,112,36\nQmvav 180,116,36\nQta 160,61,16960\nQmvav 184,120,36\nQmvav 188,124,36\nQta 162,61,17984\nQmvav 192,128,36\n"
"Qmvav 196,132,36\nQta 164,61,16968\nQmvav 200,136,36\nQmvav 204,140,36\nQta 166,61,17992\nQmvav 208,112,40\nQmvav 212,116,40\nQta 168,61,17216\n"
"Qmvav 216,120,40\nQmvav 220,124,40\nQta 170,61,18240\nQmvav 224,128,40\nQmvav 228,132,40\nQta 172,61,17224\nQmvav 232,136,40\nQmvav 236,140,40\n"
"Qta 174,61,18248\nQmvav 240,112,44\nQmvav 244,116,44\nQmvav 248,120,44\nQmvav 252,124,44\nQmaav 208,128,44\nQmaav 212,132,44\nQmaav 216,136,44\n"
"Qmaav 220,140,44\nQmaav 224,112,48\nQmaav 228,116,48\nQmaav 232,120,48\nQmaav 236,124,48\nQmaav 240,128,48\nQmaav 244,132,48\nQmaav 248,136,48\n"
"Qmaav 252,140,48\ns_waitcnt lgkmcnt(0)\nQmvaa 80,144,176\nQmvaa 84,148,176\nQmvaa 88,152,176\nQmvaa 92,156,176\nQmvaa 96,160,176\nQmvaa 100,164,176\n"
"Qmvaa 104,168,176\nQmvaa 108,172,176\ns_waitcnt lgkmcnt(0)\nQmvaa 112,144,180\ns_waitcnt vmcnt(4)\ns_barrier\nQmvaa 116,148,180\nQda 176,63,512\n"
"s_add_u32 m0, s61, 33024\nQmvaa 120,152,180\nbuffer_load_dwordx4 v56, s[40:43], s60 offen nt lds\nQmvaa 124,156,180\nQta 112,61,33024\nQmvaa 128,160,180\nQta 114,61,34048\nQmvaa 132,164,180\n"
"Qta 116,61,33032\nQmvaa 136,168,180\nbuffer_load_dwordx4 v57, s[40:43], s60 offen offset:1024 nt lds\nQmvaa 140,172,180\nQda 180,63,1536\nQmvaa 144,144,184\nQta 118,61,34056\nQmvaa 148,148,184\n"
"Qdv 24,63,0\nQmvaa 152,152,184\nbuffer_load_dwordx4 v56, s[40:43], s60 offen offset:2048 nt lds\nQmvaa 156,156,184\nQta 120,61,33280\nQmvaa 160,160,184\nQta 122,61,34304\nQmvaa 164,164,184\n"
"Qta 124,61,33288\nQmvaa 168,168,184\nbuffer_load_dwordx4 v57, s[40:43], s60 offen offset:3072 nt lds\nQmvaa 172,172,184\nQda 184,63,2560\nQmvaa 176,144,188\nQta 126,61,34312\nQmvaa 180,148,188\n"
"Qta 128,61,33536\ns_add_u32 m0, s61, 41216\nQmvaa 184,152,188\nbuffer_load_dwordx4 v58, s[44:47], s60 offen nt lds\nQmvaa 188,156,188\nQta 130,61,34560\nQmvaa 192,160,188\nQta 132,61,33544\n"
"Qmvaa 196,164,188\nQta 134,61,34568\nQmvaa 200,168,188\nbuffer_load_dwordx4 v59, s[44:47], s60 offen offset:1024 nt lds\nQmvaa 204,172,188\nQda 188,63,3584\nQmvaa 208,144,192\nQta 136,61,33792\n"
"Qmvaa 212,148,192\nQta 138,61,34816\nQmvaa 216,152,192\nbuffer_load_dwordx4 v58, s[44:47], s60 offen offset:2048 nt lds\nQmvaa 220,156,192\nQta 140,61,33800\nQmvaa 224,160,192\nQta 142,61,34824\n"
"Qmvaa 228,164,192\nQdv 28,63,1024\nQmvaa 232,168,192\nbuffer_load_dwordx4 v59, s[44:47], s60 offen offset:3072 nt lds\ns_add_u32 s60, s60, 8192\ns_add_u32 s88, s60, 8192\nQmvaa 236,172,192\nQda 192,63,4608\n"
"Qmvaa 240,144,196\nQdv 32,63,2048\nQmvaa 244,148,196\nQdv 36,63,3072\nQmvaa 248,152,196\nQdv 40,63,4096\nQmvaa 252,156,196\nQdv 44,63,5120\n"
"Qmaaa 208,160,196\nQdv 48,63,6144\nQmaaa 212,164,196\nQmaaa 216,168,196\nQmaaa 220,172,196\nQda 196,63,5632\nQmaaa 224,144,200\nQmaaa 228,148,200\n"
"Qmaaa 232,152,200\nQmaaa 236,156,200\nQmaaa 240,160,200\nQmaaa 244,164,200\nQmaaa 248,168,200\nQmaaa 252,172,200\nQda 200,63,6656\ns_waitcnt lgkmcnt(9)\n"
"Qmvav 80,112,24\nQmvav 84,116,24\nQmvav 88,120,24\nQmvav 92,124,24\nQmvav 96,128,24\nQmvav 100,132,24\nQmvav 104,136,24\nQmvav 108,140,24\n"
"s_waitcnt lgkmcnt(0)\nQmvav 112,112,28\ns_waitcnt vmcnt(8)\ns_barrier\nQmvav 116,116,28\nQta 144,61,49472\ns_add_u32 m0, s61, 99072\nQmvav 120,120,28\n"
"buffer_load_dwordx4 v56, s[40:43], s88 offen nt lds\nQmvav 124,124,28\nQta 146,61,50496\nQmvav 128,128,28\nQmvav 132,132,28\nQta 148,61,49480\nQmvav 136,136,28\nbuffer_load_dwordx4 v57, s[40:43], s88 offen offset:1024 nt lds\n"
"Qmvav 140,140,28\nQta 150,61,50504\nQmvav 144,112,32\nQmvav 148,116,32\nQta 152,61,49728\nQmvav 152,120,32\nbuffer_load_dwordx4 v56, s[40:43], s88 offen offset:2048 nt lds\nQmvav 156,124,32\n"
"Qta 154,61,50752\nQmvav 160,128,32\nQmvav 164,132,32\nQta 156,61,49736\nQmvav 168,136,32\nbuffer_load_dwordx4 v57, s[40:43], s88 offen offset:3072 nt lds\nQmvav 172,140,32\nQta 158,61,50760\n"
"Qmvav 176,112,36\nQmvav 180,116,36\nQta 160,61,49984\ns_add_u32 m0, s61, 107264\nQmvav 184,120,36\nbuffer_load_dwordx4 v58, s[44:47], s88 offen nt lds\nQmvav 188,124,36\nQta 162,61,51008\n"
"Qmvav 192,128,36\nQmvav 196,132,36\nQta 164,61,49992\nQmvav 200,136,36\nbuffer_load_dwordx4 v59, s[44:47], s88 offen offset:1024 nt lds\nQmvav 204,140,36\nQta 166,61,51016\nQmvav 208,112,40\n"
"Qmvav 212,116,40\nQta 168,61,50240\nQmvav 216,120,40\nbuffer_load_dwordx4 v58, s[44:47], s88 offen offset:2048 nt lds\nQmvav 220,124,40\nQta 170,61,51264\nQmvav 224,128,40\nQmvav 228,132,40\n"
"Qta 172,61,50248\nQmvav 232,136,40\nbuffer_load_dwordx4 v59, s[44:47], s88 offen offset:3072 nt lds\nQmvav 236,140,40\nQta 174,61,51272\nQmvav 240,112,44\nQmvav 244,116,44\nQmvav 248,120,44\n"
"Qmvav 252,124,44\nQmaav 208,128,44\nQmaav 212,132,44\nQmaav 216,136,44\nQmaav 220,140,44\nQmaav 224,112,48\nQmaav 228,116,48\nQmaav 232,120,48\n"
"Qmaav 236,124,48\nQmaav 240,128,48\nQmaav 244,132,48\nQmaav 248,136,48\nQmaav 252,140,48\ns_waitcnt lgkmcnt(0)\nQmvaa 80,144,176\nQmvaa 84,148,176\n"
"Qmvaa 88,152,176\nQmvaa 92,156,176\nQmvaa 96,160,176\nQmvaa 100,164,176\nQmvaa 104,168,176\nQmvaa 108,172,176\ns_waitcnt lgkmcnt(0)\nQmvaa 112,144,180\n"
"s_waitcnt vmcnt(63)\ns_barrier\nQmvaa 116,148,180\nQda 112,52,0\ns_add_u32 m0, s61, 66048\nQmvaa 120,152,180\nbuffer_load_dwordx4 v56, s[40:43], s60 offen nt lds\nQmvaa 124,156,180\n"
"Qda 116,52,512\nQmvaa 128,160,180\nQmvaa 132,164,180\nQda 120,52,1024\nQmvaa 136,168,180\nbuffer_load_dwordx4 v57, s[40:43], s60 offen offset:1024 nt lds\nQmvaa 140,172,180\nQda 124,52,1536\n"
"Qmvaa 144,144,184\nQmvaa 148,148,184\nQda 128,52,2048\nQmvaa 152,152,184\nbuffer_load_dwordx4 v56, s[40:43], s60 offen offset:2048 nt lds\nQmvaa 156,156,184\nQda 132,52,2560\nQmvaa 160,160,184\n"
"Qmvaa 164,164,184\nQda 136,52,3072\nQmvaa 168,168,184\nbuffer_load_dwordx4 v57, s[40:43], s60 offen offset:3072 nt lds\nQmvaa 172,172,184\nQda 140,52,3584\nQmvaa 176,144,188\nQmvaa 180,148,188\n"
"s_add_u32 m0, s61, 74240\nQmvaa 184,152,188\nbuffer_load_dwordx4 v58, s[44:47], s60 offen nt lds\nQmvaa 188,156,188\nQmvaa 192,160,188\nQmvaa 196,164,188\nQmvaa 200,168,188\nbuffer_load_dwordx4 v59, s[44:47], s60 offen offset:1024 nt lds\n"
"Qmvaa 204,172,188\nQmvaa 208,144,192\nQmvaa 212,148,192\nQmvaa 216,152,192\nbuffer_load_dwordx4 v58, s[44:47], s60 offen offset:2048 nt lds\nQmvaa 220,156,192\nQmvaa 224,160,192\nQmvaa 228,164,192\n"
"Qmvaa 232,168,192\nbuffer_load_dwordx4 v59, s[44:47], s60 offen offset:3072 nt lds\ns_add_u32 s60, s60, 16384\nQmvaa 236,172,192\nQmvaa 240,144,196\nQmvaa 244,148,196\nQmvaa 248,152,196\nQmvaa 252,156,196\n"
"Qmaaa 208,160,196\nQmaaa 212,164,196\nQmaaa 216,168,196\nQmaaa 220,172,196\nQmaaa 224,144,200\nQmaaa 228,148,200\nQmaaa 232,152,200\nQmaaa 236,156,200\n"
"Qmaaa 240,160,200\nQmaaa 244,164,200\nQmaaa 248,168,200\nQmaaa 252,172,200\nQta 144,54,0\nQta 146,54,2048\nQta 148,54,256\nQta 150,54,2304\n"
"Qta 152,54,512\nQta 154,54,2560\nQta 156,54,768\nQta 158,54,2816\nQta 160,54,1024\nQta 162,54,3072\nQta 164,54,1280\nQta 166,54,3328\n"
"Qta 168,54,1536\nQta 170,54,3584\nQta 172,54,1792\nQta 174,54,3840\nv_pk_mul_f32 v[80:81], v[80:81], s[72:73] op_sel_hi:[1,0]\nv_pk_mul_f32 v[82:83], v[82:83], s[72:73] op_sel_hi:[1,0]\nv_pk_mul_f32 v[84:85], v[84:85], s[72:73] op_sel_hi:[1,0]\nv_pk_mul_f32 v[86:87], v[86:87], s[72:73] op_sel_hi:[1,0]\n"
"Qk 80,80,81\nQk 81,82,83\nQk 82,84,85\nQk 83,86,87\nv_accvgpr_write_b32 a0, v80\nv_accvgpr_write_b32 a1, v81\nv_accvgpr_write_b32 a2, v82\nv_accvgpr_write_b32 a3, v83\n"
"v_pk_mul_f32 v[88:89], v[88:89], s[72:73] op_sel_hi:[1,0]\nv_pk_mul_f32 v[90:91], v[90:91], s[72:73] op_sel_hi:[1,0]\nv_pk_mul_f32 v[92:93], v[92:93], s[72:73] op_sel_hi:[1,0]\nv_pk_mul_f32 v[94:95], v[94:95], s[72:73] op_sel_hi:[1,0]\nQk 88,88,89\nQk 89,90,91\nQk 90,92,93\nQk 91,94,95\n"
"v_accvgpr_write_b32 a4, v88\nv_accvgpr_write_b32 a5, v89\nv_accvgpr_write_b32 a6, v90\nv_accvgpr_write_b32 a7, v91\nv_pk_mul_f32 v[96:97], v[96:97], s[72:73] op_sel_hi:[1,0]\nv_pk_mul_f32 v[98:99], v[98:99], s[72:73] op_sel_hi:[1,0]\nv_pk_mul_f32 v[100:101], v[100:101], s[72:73] op_sel_hi:[1,0]\nv_pk_mul_f32 v[102:103], v[102:103], s[72:73] op_sel_hi:[1,0]\n"
"Qk 96,96,97\nQk 97,98,99\nQk 98,100,101\nQk 99,102,103\nv_accvgpr_write_b32 a8, v96\nv_accvgpr_write_b32 a9, v97\nv_accvgpr_write_b32 a10, v98\nv_accvgpr_write_b32 a11, v99\n"
"v_pk_mul_f32 v[104:105], v[104:105], s[72:73] op_sel_hi:[1,0]\nv_pk_mul_f32 v[106:107], v[106:107], s[72:73] op_sel_hi:[1,0]\nv_pk_mul_f32 v[108:109], v[108:109], s[72:73] op_sel_hi:[1,0]\nv_pk_mul_f32 v[110:111], v[110:111], s[72:73] op_sel_hi:[1,0]\nQk 104,104,105\nQk 105,106,107\nQk 106,108,109\nQk 107,110,111\n"
"v_accvgpr_write_b32 a12, v104\nv_accvgpr_write_b32 a13, v105\nv_accvgpr_write_b32 a14, v106\nv_accvgpr_write_b32 a15, v107\nv_pk_mul_f32 v[112:113], v[112:113], s[72:73] op_sel_hi:[1,0]\nv_pk_mul_f32 v[114:115], v[114:115], s[72:73] op_sel_hi:[1,0]\nv_pk_mul_f32 v[116:117], v[116:117], s[72:73] op_sel_hi:[1,0]\nv_pk_mul_f32 v[118:119], v[118:119], s[72:73] op_sel_hi:[1,0]\n"
"Qk 112,112,113\nQk 113,114,115\nQk 114,116,117\nQk 115,118,119\nv_accvgpr_write_b32 a16, v112\nv_accvgpr_write_b32 a17, v113\nv_accvgpr_write_b32 a18, v114\nv_accvgpr_write_b32 a19, v115\n"
"v_pk_mul_f32 v[120:121], v[120:121], s[72:73] op_sel_hi:[1,0]\nv_pk_mul_f32 v[122:123], v[122:123], s[72:73] op_sel_hi:[1,0]\nv_pk_mul_f32 v[124:125], v[124:125], s[72:73] op_sel_hi:[1,0]\nv_pk_mul_f32 v[126:127], v[126:127], s[72:73] op_sel_hi:[1,0]\nQk 120,120,121\nQk 121,122,123\nQk 122,124,125\nQk 123,126,127\n"
"v_accvgpr_write_b32 a20, v120\nv_accvgpr_write_b32 a21, v121\nv_accvgpr_write_b32 a22, v122\nv_accvgpr_write_b32 a23, v123\nv_pk_mul_f32 v[128:129], v[128:129], s[72:73] op_sel_hi:[1,0]\nv_pk_mul_f32 v[130:131], v[130:131], s[72:73] op_sel_hi:[1,0]\nv_pk_mul_f32 v[132:133], v[132:133], s[72:73] op_sel_hi:[1,0]\nv_pk_mul_f32 v[134:135], v[134:135], s[72:73] op_sel_hi:[1,0]\n"
"Qk 128,128,129\nQk 129,130,131\nQk 130,132,133\nQk 131,134,135\nv_accvgpr_write_b32 a24, v128\nv_accvgpr_write_b32 a25, v129\nv_accvgpr_write_b32 a26, v130\nv_accvgpr_write_b32 a27, v131\n"
"v_pk_mul_f32 v[136:137], v[136:137], s[72:73] op_sel_hi:[1,0]\nv_pk_mul_f32 v[138:139], v[138:139], s[72:73] op_sel_hi:[1,0]\nv_pk_mul_f32 v[140:141], v[140:141], s[72:73] op_sel_hi:[1,0]\nv_pk_mul_f32 v[142:143], v[142:143], s[72:73] op_sel_hi:[1,0]\nQk 136,136,137\nQk 137,138,139\nQk 138,140,141\nQk 139,142,143\n"
"v_accvgpr_write_b32 a28, v136\nv_accvgpr_write_b32 a29, v137\nv_accvgpr_write_b32 a30, v138\nv_accvgpr_write_b32 a31, v139\nv_pk_mul_f32 v[144:145], v[144:145], s[72:73] op_sel_hi:[1,0]\nv_pk_mul_f32 v[146:147], v[146:147], s[72:73] op_sel_hi:[1,0]\nv_pk_mul_f32 v[148:149], v[148:149], s[72:73] op_sel_hi:[1,0]\nv_pk_mul_f32 v[150:151], v[150:151], s[72:73] op_sel_hi:[1,0]\n"
"Qk 144,144,145\nQk 145,146,147\nQk 146,148,149\nQk 147,150,151\nv_accvgpr_write_b32 a32, v144\nv_accvgpr_write_b32 a33, v145\nv_accvgpr_write_b32 a34, v146\nv_accvgpr_write_b32 a35, v147\n"
"v_pk_mul_f32 v[152:153], v[152:153], s[72:73] op_sel_hi:[1,0]\nv_pk_mul_f32 v[154:155], v[154:155], s[72:73] op_sel_hi:[1,0]\nv_pk_mul_f32 v[156:157], v[156:157], s[72:73] op_sel_hi:[1,0]\nv_pk_mul_f32 v[158:159], v[158:159], s[72:73] op_sel_hi:[1,0]\nQk 152,152,153\nQk 153,154,155\nQk 154,156,157\nQk 155,158,159\n"
"v_accvgpr_write_b32 a36, v152\nv_accvgpr_write_b32 a37, v153\nv_accvgpr_write_b32 a38, v154\nv_accvgpr_write_b32 a39, v155\nv_pk_mul_f32 v[160:161], v[160:161], s[72:73] op_sel_hi:[1,0]\nv_pk_mul_f32 v[162:163], v[162:163], s[72:73] op_sel_hi:[1,0]\nv_pk_mul_f32 v[164:165], v[164:165], s[72:73] op_sel_hi:[1,0]\nv_pk_mul_f32 v[166:167], v[166:167], s[72:73] op_sel_hi:[1,0]\n"
"Qk 160,160,161\nQk 161,162,163\nQk 162,164,165\nQk 163,166,167\nv_accvgpr_write_b32 a40, v160\nv_accvgpr_write_b32 a41, v161\nv_accvgpr_write_b32 a42, v162\nv_accvgpr_write_b32 a43, v163\n"
"v_pk_mul_f32 v[168:169], v[168:169], s[72:73] op_sel_hi:[1,0]\nv_pk_mul_f32 v[170:171], v[170:171], s[72:73] op_sel_hi:[1,0]\nv_pk_mul_f32 v[172:173], v[172:173], s[72:73] op_sel_hi:[1,0]\nv_pk_mul_f32 v[174:175], v[174:175], s[72:73] op_sel_hi:[1,0]\nQk 168,168,169\nQk 169,170,171\nQk 170,172,173\nQk 171,174,175\n"
"v_accvgpr_write_b32 a44, v168\nv_accvgpr_write_b32 a45, v169\nv_accvgpr_write_b32 a46, v170\nv_accvgpr_write_b32 a47, v171\nv_pk_mul_f32 v[176:177], v[176:177], s[72:73] op_sel_hi:[1,0]\nv_pk_mul_f32 v[178:179], v[178:179], s[72:73] op_sel_hi:[1,0]\nv_pk_mul_f32 v[180:181], v[180:181], s[72:73] op_sel_hi:[1,0]\nv_pk_mul_f32 v[182:183], v[182:183], s[72:73] op_sel_hi:[1,0]\n"
"Qk 176,176,177\nQk 177,178,179\nQk 178,180,181\nQk 179,182,183\nv_accvgpr_write_b32 a48, v176\nv_accvgpr_write_b32 a49, v177\nv_accvgpr_write_b32 a50, v178\nv_accvgpr_write_b32 a51, v179\n"
"v_pk_mul_f32 v[184:185], v[184:185], s[72:73] op_sel_hi:[1,0]\nv_pk_mul_f32 v[186:187], v[186:187], s[72:73] op_sel_hi:[1,0]\nv_pk_mul_f32 v[188:189], v[188:189], s[72:73] op_sel_hi:[1,0]\nv_pk_mul_f32 v[190:191], v[190:191], s[72:73] op_sel_hi:[1,0]\nQk 184,184,185\nQk 185,186,187\nQk 186,188,189\nQk 187,190,191\n"
"v_accvgpr_write_b32 a52, v184\nv_accvgpr_write_b32 a53, v185\nv_accvgpr_write_b32 a54, v186\nv_accvgpr_write_b32 a55, v187\nv_pk_mul_f32 v[192:193], v[192:193], s[72:73] op_sel_hi:[1,0]\nv_pk_mul_f32 v[194:195], v[194:195], s[72:73] op_sel_hi:[1,0]\nv_pk_mul_f32 v[196:197], v[196:197], s[72:73] op_sel_hi:[1,0]\nv_pk_mul_f32 v[198:199], v[198:199], s[72:73] op_sel_hi:[1,0]\n"
"Qk 192,192,193\nQk 193,194,195\nQk 194,196,197\nQk 195,198,199\nv_accvgpr_write_b32 a56, v192\nv_accvgpr_write_b32 a57, v193\nv_accvgpr_write_b32 a58, v194\nv_accvgpr_write_b32 a59, v195\n"
"v_pk_mul_f32 v[200:201], v[200:201], s[72:73] op_sel_hi:[1,0]\nv_pk_mul_f32 v[202:203], v[202:203], s[72:73] op_sel_hi:[1,0]\nv_pk_mul_f32 v[204:205], v[204:205], s[72:73] op_sel_hi:[1,0]\nv_pk_mul_f32 v[206:207], v[206:207], s[72:73] op_sel_hi:[1,0]\nQk 200,200,201\nQk 201,202,203\nQk 202,204,205\nQk 203,206,207\n"
"v_accvgpr_write_b32 a60, v200\nv_accvgpr_write_b32 a61, v201\nv_accvgpr_write_b32 a62, v202\nv_accvgpr_write_b32 a63, v203\nv_pk_mul_f32 v[208:209], v[208:209], s[72:73] op_sel_hi:[1,0]\nv_pk_mul_f32 v[210:211], v[210:211], s[72:73] op_sel_hi:[1,0]\nv_pk_mul_f32 v[212:213], v[212:213], s[72:73] op_sel_hi:[1,0]\nv_pk_mul_f32 v[214:215], v[214:215], s[72:73] op_sel_hi:[1,0]\n"
"Qk 208,208,209\nQk 209,210,211\nQk 210,212,213\nQk 211,214,215\nv_accvgpr_write_b32 a64, v208\nv_accvgpr_write_b32 a65, v209\nv_accvgpr_write_b32 a66, v210\nv_accvgpr_write_b32 a67, v211\n"
"v_pk_mul_f32 v[216:217], v[216:217], s[72:73] op_sel_hi:[1,0]\nv_pk_mul_f32 v[218:219], v[218:219], s[72:73] op_sel_hi:[1,0]\nv_pk_mul_f32 v[220:221], v[220:221], s[72:73] op_sel_hi:[1,0]\nv_pk_mul_f32 v[222:223], v[222:223], s[72:73] op_sel_hi:[1,0]\nQk 216,216,217\nQk 217,218,219\nQk 218,220,221\nQk 219,222,223\n"
"v_accvgpr_write_b32 a68, v216\nv_accvgpr_write_b32 a69, v217\nv_accvgpr_write_b32 a70, v218\nv_accvgpr_write_b32 a71, v219\nv_pk_mul_f32 v[224:225], v[224:225], s[72:73] op_sel_hi:[1,0]\nv_pk_mul_f32 v[226:227], v[226:227], s[72:73] op_sel_hi:[1,0]\nv_pk_mul_f32 v[228:229], v[228:229], s[72:73] op_sel_hi:[1,0]\nv_pk_mul_f32 v[230:231], v[230:231], s[72:73] op_sel_hi:[1,0]\n"
"Qk 224,224,225\nQk 225,226,227\nQk 226,228,229\nQk 227,230,231\nv_accvgpr_write_b32 a72, v224\nv_accvgpr_write_b32 a73, v225\nv_accvgpr_write_b32 a74, v226\nv_accvgpr_write_b32 a75, v227\n"
"v_pk_mul_f32 v[232:233], v[232:233], s[72:73] op_sel_hi:[1,0]\nv_pk_mul_f32 v[234:235], v[234:235], s[72:73] op_sel_hi:[1,0]\nv_pk_mul_f32 v[236:237], v[236:237], s[72:73] op_sel_hi:[1,0]\nv_pk_mul_f32 v[238:239], v[238:239], s[72:73] op_sel_hi:[1,0]\nQk 232,232,233\nQk 233,234,235\nQk 234,236,237\nQk 235,238,239\n"
"v_accvgpr_write_b32 a76, v232\nv_accvgpr_write_b32 a77, v233\nv_accvgpr_write_b32 a78, v234\nv_accvgpr_write_b32 a79, v235\nv_pk_mul_f32 v[240:241], v[240:241], s[72:73] op_sel_hi:[1,0]\nv_pk_mul_f32 v[242:243], v[242:243], s[72:73] op_sel_hi:[1,0]\nv_pk_mul_f32 v[244:245], v[244:245], s[72:73] op_sel_hi:[1,0]\nv_pk_mul_f32 v[246:247], v[246:247], s[72:73] op_sel_hi:[1,0]\n"
"Qk 240,240,241\nQk 241,242,243\nQk 242,244,245\nQk 243,246,247\nv_accvgpr_write_b32 a80, v240\nv_accvgpr_write_b32 a81, v241\nv_accvgpr_write_b32 a82, v242\nv_accvgpr_write_b32 a83, v243\n"
"v_pk_mul_f32 v[248:249], v[248:249], s[72:73] op_sel_hi:[1,0]\nv_pk_mul_f32 v[250:251], v[250:251], s[72:73] op_sel_hi:[1,0]\nv_pk_mul_f32 v[252:253], v[252:253], s[72:73] op_sel_hi:[1,0]\nv_pk_mul_f32 v[254:255], v[254:255], s[72:73] op_sel_hi:[1,0]\nQk 248,248,249\nQk 249,250,251\nQk 250,252,253\nQk 251,254,255\n"
"v_accvgpr_write_b32 a84, v248\nv_accvgpr_write_b32 a85, v249\nv_accvgpr_write_b32 a86, v250\nv_accvgpr_write_b32 a87, v251\nv_accvgpr_read_b32 v0, a208\nv_accvgpr_read_b32 v1, a209\nv_accvgpr_read_b32 v2, a210\nv_accvgpr_read_b32 v3, a211\n"
"v_accvgpr_read_b32 v4, a212\nv_accvgpr_read_b32 v5, a213\nv_accvgpr_read_b32 v6, a214\nv_accvgpr_read_b32 v7, a215\nv_pk_mul_f32 v[0:1], v[0:1], s[72:73] op_sel_hi:[1,0]\nv_pk_mul_f32 v[2:3], v[2:3], s[72:73] op_sel_hi:[1,0]\nv_pk_mul_f32 v[4:5], v[4:5], s[72:73] op_sel_hi:[1,0]\nv_pk_mul_f32 v[6:7], v[6:7], s[72:73] op_sel_hi:[1,0]\n"
"Qk 0,0,1\nQk 1,2,3\nQk 2,4,5\nQk 3,6,7\nv_accvgpr_write_b32 a88, v0\nv_accvgpr_write_b32 a89, v1\nv_accvgpr_write_b32 a90, v2\nv_accvgpr_write_b32 a91, v3\n"
"v_accvgpr_read_b32 v8, a216\nv_accvgpr_read_b32 v9, a217\nv_accvgpr_read_b32 v10, a218\nv_accvgpr_read_b32 v11, a219\nv_accvgpr_read_b32 v12, a220\nv_accvgpr_read_b32 v13, a221\nv_accvgpr_read_b32 v14, a222\nv_accvgpr_read_b32 v15, a223\n"
"v_pk_mul_f32 v[8:9], v[8:9], s[72:73] op_sel_hi:[1,0]\nv_pk_mul_f32 v[10:11], v[10:11], s[72:73] op_sel_hi:[1,0]\nv_pk_mul_f32 v[12:13], v[12:13], s[72:73] op_sel_hi:[1,0]\nv_pk_mul_f32 v[14:15], v[14:15], s[72:73] op_sel_hi:[1,0]\nQk 8,8,9\nQk 9,10,11\nQk 10,12,13\nQk 11,14,15\n"
"v_accvgpr_write_b32 a92, v8\nv_accvgpr_write_b32 a93, v9\nv_accvgpr_write_b32 a94, v10\nv_accvgpr_write_b32 a95, v11\nv_accvgpr_read_b32 v0, a224\nv_accvgpr_read_b32 v1, a225\nv_accvgpr_read_b32 v2, a226\nv_accvgpr_read_b32 v3, a227\n"
"v_accvgpr_read_b32 v4, a228\nv_accvgpr_read_b32 v5, a229\nv_accvgpr_read_b32 v6, a230\nv_accvgpr_read_b32 v7, a231\nv_pk_mul_f32 v[0:1], v[0:1], s[72:73] op_sel_hi:[1,0]\nv_pk_mul_f32 v[2:3], v[2:3], s[72:73] op_sel_hi:[1,0]\nv_pk_mul_f32 v[4:5], v[4:5], s[72:73] op_sel_hi:[1,0]\nv_pk_mul_f32 v[6:7], v[6:7], s[72:73] op_sel_hi:[1,0]\n"
"Qk 0,0,1\nQk 1,2,3\nQk 2,4,5\nQk 3,6,7\nv_accvgpr_write_b32 a96, v0\nv_accvgpr_write_b32 a97, v1\nv_accvgpr_write_b32 a98, v2\nv_accvgpr_write_b32 a99, v3\n"
"v_accvgpr_read_b32 v8, a232\nv_accvgpr_read_b32 v9, a233\nv_accvgpr_read_b32 v10, a234\nv_accvgpr_read_b32 v11, a235\nv_accvgpr_read_b32 v12, a236\nv_accvgpr_read_b32 v13, a237\nv_accvgpr_read_b32 v14, a238\nv_accvgpr_read_b32 v15, a239\n"
"v_pk_mul_f32 v[8:9], v[8:9], s[72:73] op_sel_hi:[1,0]\nv_pk_mul_f32 v[10:11], v[10:11], s[72:73] op_sel_hi:[1,0]\nv_pk_mul_f32 v[12:13], v[12:13], s[72:73] op_sel_hi:[1,0]\nv_pk_mul_f32 v[14:15], v[14:15], s[72:73] op_sel_hi:[1,0]\nQk 8,8,9\nQk 9,10,11\nQk 10,12,13\nQk 11,14,15\n"
"v_accvgpr_write_b32 a100, v8\nv_accvgpr_write_b32 a101, v9\nv_accvgpr_write_b32 a102, v10\nv_accvgpr_write_b32 a103, v11\nv_accvgpr_read_b32 v0, a240\nv_accvgpr_read_b32 v1, a241\nv_accvgpr_read_b32 v2, a242\nv_accvgpr_read_b32 v3, a243\n"
"v_accvgpr_read_b32 v4, a244\nv_accvgpr_read_b32 v5, a245\nv_accvgpr_read_b32 v6, a246\nv_accvgpr_read_b32 v7, a247\nv_pk_mul_f32 v[0:1], v[0:1], s[72:73] op_sel_hi:[1,0]\nv_pk_mul_f32 v[2:3], v[2:3], s[72:73] op_sel_hi:[1,0]\nv_pk_mul_f32 v[4:5], v[4:5], s[72:73] op_sel_hi:[1,0]\nv_pk_mul_f32 v[6:7], v[6:7], s[72:73] op_sel_hi:[1,0]\n"
"Qk 0,0,1\nQk 1,2,3\nQk 2,4,5\nQk 3,6,7\nv_accvgpr_write_b32 a104, v0\nv_accvgpr_write_b32 a105, v1\nv_accvgpr_write_b32 a106, v2\nv_accvgpr_write_b32 a107, v3\n"
"v_accvgpr_read_b32 v8, a248\nv_accvgpr_read_b32 v9, a249\nv_accvgpr_read_b32 v10, a250\nv_accvgpr_read_b32 v11, a251\nv_accvgpr_read_b32 v12, a252\nv_accvgpr_read_b32 v13, a253\nv_accvgpr_read_b32 v14, a254\nv_accvgpr_read_b32 v15, a255\n"
"v_pk_mul_f32 v[8:9], v[8:9], s[72:73] op_sel_hi:[1,0]\nv_pk_mul_f32 v[10:11], v[10:11], s[72:73] op_sel_hi:[1,0]\nv_pk_mul_f32 v[12:13], v[12:13], s[72:73] op_sel_hi:[1,0]\nv_pk_mul_f32 v[14:15], v[14:15], s[72:73] op_sel_hi:[1,0]\nQk 8,8,9\nQk 9,10,11\nQk 10,12,13\nQk 11,14,15\n"
"v_accvgpr_write_b32 a108, v8\nv_accvgpr_write_b32 a109, v9\nv_accvgpr_write_b32 a110, v10\nv_accvgpr_write_b32 a111, v11\nv_mov_b32 v66, 0x3f803f80\nv_accvgpr_write_b32 a204, v66\nv_accvgpr_write_b32 a205, v66\nv_accvgpr_write_b32 a206, v66\n"
"v_accvgpr_write_b32 a207, v66\ns_add_u32 s87, s87, 2048\ns_waitcnt lgkmcnt(15)\nQzvaa 0,112,0\nQmvaa 0,116,4\nQmvaa 0,120,8\nQmvaa 0,124,12\nQzvaa 4,128,0\n"
"Qmvaa 4,132,4\nQmvaa 4,136,8\nQmvaa 4,140,12\ns_nop 3\nv_max3_f32 v66, v0, v1, v2\ns_nop 2\nv_max3_f32 v66, v66, v3, v4\nv_max3_f32 v66, v66, v5, v6\n"
"v_max_f32 v66, v66, v7\nQzvaa 8,112,16\nv_mov_b32 v67, v66\nQmvaa 8,116,20\ns_nop 0\nv_permlane32_swap_b32 v66, v67\nv_max_f32 v66, v66, v67\nQmvaa 8,120,24\n"
"v_mov_b32 v67, v66\nQmvaa 8,124,28\ns_nop 0\nv_permlane16_swap_b32 v66, v67\nv_max_f32 v66, v66, v67\nQzvaa 12,128,16\nv_sub_f32 v24, 0xc1800000, v66\nQa 0,0,24\n"
"Qmvaa 12,132,20\nQa 1,1,24\nQa 2,2,24\nQmvaa 12,136,24\nQa 3,3,24\nQa 4,4,24\nQmvaa 12,140,28\nQe 0\n"
"Qe 1\nQe 2\nQe 3\nQk 16,0,1\nQk 17,2,3\nQa 5,5,24\nQa 6,6,24\nQa 7,7,24\n"
"v_mov_b32 v25, v24\nQe 4\nQe 5\nQe 6\nQzvaa 0,112,32\nQe 7\nQmvaa 0,116,36\nQk 18,4,5\n"
"Qk 19,6,7\nQmvaa 0,120,40\nv_max3_f32 v68, v8, v9, v10\nv_max3_f32 v68, v68, v11, v12\nQmvaa 0,124,44\nv_max3_f32 v68, v68, v13, v14\nv_max_f32 v68, v68, v15\nQzvaa 4,128,32\n"
"v_mov_b32 v69, v68\nv_mov_b32 v26, v24\nQmvaa 4,132,36\nv_permlane32_swap_b32 v68, v69\nv_max_f32 v68, v68, v69\nQmvaa 4,136,40\nv_mov_b32 v69, v68\nv_mov_b32 v27, v24\n"
"Qmvaa 4,140,44\ns_waitcnt lgkmcnt(0)\nv_permlane16_swap_b32 v68, v69\nQzvav 80,144,16\nv_max_f32 v68, v68, v69\nv_sub_f32 v28, 0xc1800000, v68\nQzvav 84,148,16\nQa 8,8,28\n"
"Qa 9,9,28\nQzvav 88,152,16\nQa 10,10,28\nQa 11,11,28\nQzvav 92,156,16\nQe 8\nQzvav 96,160,16\nQe 9\n"
"Qzvav 100,164,16\nQe 10\nQzvav 104,168,16\nQe 11\nQzvav 108,172,16\nQk 20,8,9\nQk 21,10,11\nQzaav 176,204,16\n"
"Qa 12,12,28\nQa 13,13,28\nQzvaa 8,112,48\nQa 14,14,28\nQa 15,15,28\nQmvaa 8,116,52\nQe 12\nQmvaa 8,120,56\n"
"Qe 13\nQe 14\nQe 15\nQmvaa 8,124,60\nQk 22,12,13\nQk 23,14,15\nQzvaa 12,128,48\nv_max3_f32 v66, v0, v1, v2\n"
"v_max3_f32 v66, v66, v3, v4\nQmvaa 12,132,52\nv_max3_f32 v66, v66, v5, v6\nv_max_f32 v66, v66, v7\nQmvaa 12,136,56\nv_mov_b32 v67, v66\nv_mov_b32 v29, v28\nQmvaa 12,140,60\n"
"v_permlane32_swap_b32 v66, v67\nv_max_f32 v66, v66, v67\nQzvav 112,144,20\nv_mov_b32 v67, v66\nv_mov_b32 v30, v28\nQzvav 116,148,20\nv_permlane16_swap_b32 v66, v67\nv_max_f32 v66, v66, v67\n"
"Qzvav 120,152,20\nv_sub_f32 v32, 0xc1800000, v66\nQa 0,0,32\nQzvav 124,156,20\nQa 1,1,32\nQa 2,2,32\nQzvav 128,160,20\nQa 3,3,32\n"
"Qa 4,4,32\nQzvav 132,164,20\nQe 0\nQzvav 136,168,20\nQe 1\nQzvav 140,172,20\nQe 2\nQe 3\n"
"Qzaav 180,204,20\nQk 16,0,1\nQk 17,2,3\nQzvaa 0,112,64\nQa 5,5,32\nQa 6,6,32\nQmvaa 0,116,68\nQa 7,7,32\n"
"v_max3_f32 v68, v8, v9, v10\nQmvaa 0,120,72\nQe 4\nQe 5\nQe 6\nQe 7\nQmvaa 0,124,76\nQk 18,4,5\n"
"Qk 19,6,7\nQzvaa 4,128,64\nv_max3_f32 v68, v68, v11, v12\nv_max3_f32 v68, v68, v13, v14\nQmvaa 4,132,68\nv_max_f32 v68, v68, v15\nv_mov_b32 v69, v68\nQmvaa 4,136,72\n"
"s_nop 0\nv_permlane32_swap_b32 v68, v69\nv_max_f32 v68, v68, v69\nQmvaa 4,140,76\nv_mov_b32 v69, v68\nv_mov_b32 v31, v28\nQzvav 144,144,16\nv_permlane16_swap_b32 v68, v69\n"
"v_max_f32 v68, v68, v69\nQzvav 148,148,16\nv_sub_f32 v36, 0xc1800000, v68\nQa 8,8,36\nQzvav 152,152,16\nQa 9,9,36\nQa 10,10,36\nQzvav 156,156,16\n"
"Qa 11,11,36\nQa 12,12,36\nQzvav 160,160,16\nQe 8\nQzvav 164,164,16\nQe 9\nQzvav 168,168,16\nQe 10\n"
"Qzvav 172,172,16\nQe 11\nQzaav 184,204,16\nQk 20,8,9\nQk 21,10,11\nQzvaa 8,112,80\nQa 13,13,36\nQa 14,14,36\n"
"Qmvaa 8,116,84\nQa 15,15,36\nv_max3_f32 v66, v0, v1, v2\nQmvaa 8,120,88\nQe 12\nQe 13\nQe 14\nQe 15\n"
"Qmvaa 8,124,92\nQk 22,12,13\nQk 23,14,15\nQzvaa 12,128,80\nv_max3_f32 v66, v66, v3, v4\nv_max3_f32 v66, v66, v5, v6\nQmvaa 12,132,84\nv_max_f32 v66, v66, v7\n"
"v_mov_b32 v67, v66\nQmvaa 12,136,88\ns_nop 0\nv_permlane32_swap_b32 v66, v67\nv_max_f32 v66, v66, v67\nQmvaa 12,140,92\nv_mov_b32 v67, v66\nv_mov_b32 v33, v32\n"
"Qzvav 176,144,20\nv_permlane16_swap_b32 v66, v67\nv_max_f32 v66, v66, v67\nQzvav 180,148,20\ns_waitcnt vmcnt(16)\ns_barrier\nQzvav 184,152,20\nv_sub_f32 v40, 0xc1800000, v66\n"
"Qa 0,0,40\nQzvav 188,156,20\nQa 1,1,40\nQa 2,2,40\nQzvav 192,160,20\nQa 3,3,40\nQa 4,4,40\nQzvav 196,164,20\n"
"Qe 0\nQzvav 200,168,20\nQe 1\nQzvav 204,172,20\nQe 2\nQe 3\nQzaav 188,204,20\nQk 16,0,1\n"
"Qk 17,2,3\nQzvaa 0,112,96\nQda 112,52,33024\nQa 5,5,40\nQmvaa 0,116,100\nQda 116,52,33536\nQa 6,6,40\nQmvaa 0,120,104\n"
"Qda 120,52,34048\nQa 7,7,40\nQe 4\nQe 5\nQe 6\nQe 7\nQmvaa 0,124,108\nQda 124,52,34560\n"
"Qk 18,4,5\nQk 19,6,7\nQzvaa 4,128,96\nQda 128,52,35072\nv_max3_f32 v68, v8, v9, v10\nQmvaa 4,132,100\nQda 132,52,35584\nv_max3_f32 v68, v68, v11, v12\n"
"Qmvaa 4,136,104\nQda 136,52,36096\nv_max3_f32 v68, v68, v13, v14\nQmvaa 4,140,108\nQda 140,52,36608\nv_max_f32 v68, v68, v15\nQzvav 208,144,16\nv_mov_b32 v69, v68\n"
"v_mov_b32 v34, v32\nQzvav 212,148,16\ns_add_u32 m0, s61, 0\nv_permlane32_swap_b32 v68, v69\nQzvav 216,152,16\nbuffer_load_dwordx4 v56, s[40:43], s60 offen nt lds\nQzvav 220,156,16\nv_max_f32 v68, v68, v69\n"
"v_mov_b32 v69, v68\nQzvav 224,160,16\ns_nop 0\nv_permlane16_swap_b32 v68, v69\nv_max_f32 v68, v68, v69\nQzvav 228,164,16\nv_sub_f32 v44, 0xc1800000, v68\nQa 8,8,44\n"
"Qzvav 232,168,16\nbuffer_load_dwordx4 v57, s[40:43], s60 offen offset:1024 nt lds\nQzvav 236,172,16\nQa 9,9,44\nQa 10,10,44\nQa 11,11,44\nQe 8\nQe 9\n"
"Qe 10\nQe 11\nQzaav 192,204,16\ns_waitcnt lgkmcnt(0)\nQk 20,8,9\nQk 21,10,11\nQcvaa 8,112,0,24\nQa 12,12,44\n"
"Qa 13,13,44\nQmvaa 8,116,4\nbuffer_load_dwordx4 v56, s[40:43], s60 offen offset:2048 nt lds\nQmvaa 8,120,8\nQa 14,14,44\nQa 15,15,44\nQe 12\nQe 13\n"
"Qe 14\nQe 15\nQmvaa 8,124,12\nQk 22,12,13\nQk 23,14,15\nQcvaa 12,128,0,24\nv_max3_f32 v66, v0, v1, v2\nv_max3_f32 v66, v66, v3, v4\n"
"Qmvaa 12,132,4\nbuffer_load_dwordx4 v57, s[40:43], s60 offen offset:3072 nt lds\nQmvaa 12,136,8\nv_max3_f32 v66, v66, v5, v6\nv_max_f32 v66, v66, v7\nQmvaa 12,140,12\nv_mov_b32 v67, v66\nv_mov_b32 v35, v32\n"
"Qzvav 240,144,20\ns_add_u32 m0, s61, 8192\nv_permlane32_swap_b32 v66, v67\nQzvav 244,148,20\nbuffer_load_dwordx4 v58, s[44:47], s60 offen nt lds\nQzvav 248,152,20\nv_max_f32 v66, v66, v67\nv_mov_b32 v67, v66\n"
"Qzvav 252,156,20\nQe 8\nQzaav 208,160,20\nQe 9\nQzaav 212,164,20\nbuffer_load_dwordx4 v59, s[44:47], s60 offen offset:1024 nt lds\nQe 10\nQzaav 216,168,20\n"
"Qe 11\nQzaav 220,172,20\nQe 12\nv_permlane16_swap_b32 v66, v67\nv_max_f32 v66, v66, v67\nv_sub_f32 v48, 0xc1800000, v66\nQa 0,0,48\nQa 1,1,48\n"
"Qa 2,2,48\nQa 3,3,48\nQe 0\nQe 1\nQe 2\nQe 3\nQzaav 196,204,20\nQk 20,8,9\n"
"Qk 21,10,11\nQk 16,0,1\nQk 17,2,3\nQcvaa 0,112,16,28\nbuffer_load_dwordx4 v58, s[44:47], s60 offen offset:2048 nt lds\nQe 13\nQmvaa 0,116,20\nQe 14\n"
"Qmvaa 0,120,24\nQe 15\nQa 4,4,48\nQa 5,5,48\nQa 6,6,48\nQa 7,7,48\nQe 4\nQe 5\n"
"Qe 6\nQe 7\nQmvaa 0,124,28\nQk 22,12,13\nQk 23,14,15\nQk 18,4,5\nQk 19,6,7\nQcvaa 4,128,16,28\n"
"buffer_load_dwordx4 v59, s[44:47], s60 offen offset:3072 nt lds\nQmvaa 4,132,20\nv_mov_b32 v37, v36\nv_mov_b32 v38, v36\nQmvaa 4,136,24\ns_add_u32 s60, s60, 8192\nv_mov_b32 v39, v36\nQmvaa 4,140,28\n"
"v_mov_b32 v41, v40\nv_mov_b32 v42, v40\nQzaav 224,144,16\nQta 144,54,33024\nQta 146,54,35072\nQzaav 228,148,16\nQta 148,54,33280\nQta 150,54,35328\n"
"v_mov_b32 v43, v40\nv_mov_b32 v45, v44\nQzaav 232,152,16\nQta 152,54,33536\nQta 154,54,35584\nv_mov_b32 v46, v44\nQzaav 236,156,16\nQta 156,54,33792\n"
"Qta 158,54,35840\nv_mov_b32 v47, v44\nv_mov_b32 v49, v48\nQzaav 240,160,16\nQta 160,54,34048\nQta 162,54,36096\nv_mov_b32 v50, v48\nQzaav 244,164,16\n"
"Qta 164,54,34304\nQta 166,54,36352\nv_mov_b32 v51, v48\nQzaav 248,168,16\nQta 168,54,34560\nQta 170,54,36608\nQzaav 252,172,16\nQta 172,54,34816\n"
"Qta 174,54,36864\nQzaav 200,204,16\nQe 0\ns_lshl_b32 s66, s67, 3\n.Lloop:\ns_bitcmp1_b32 6914, s66\ns_cbranch_scc1 .Lhook\n.Lhookret:\n"
"Qcvaa 8,112,32,32\nv_add_u32 v75, 1, v75\nQmvaa 8,116,36\ns_waitcnt vmcnt(8)\nds_write_b32 v73, v75\nQmvaa 8,120,40\nQe 1\nQmvaa 8,124,44\n"
"Qe 2\nQcvaa 12,128,32,32\nQe 3\nQmvaa 12,132,36\nQk 16,0,1\nQk 17,2,3\nQmvaa 12,136,40\nQe 4\n"
"Qmvaa 12,140,44\ns_waitcnt lgkmcnt(1)\nQmvav 80,144,20\nQe 5\nQmvav 84,148,20\nQe 6\nQmvav 88,152,20\nQe 7\n"
"Qmvav 92,156,20\nQk 18,4,5\nQk 19,6,7\nQmvav 96,160,20\nQe 8\nQmvav 100,164,20\nQe 9\nQmvav 104,168,20\n"
"Qe 10\nQmvav 108,172,20\nQe 11\nQmaav 176,204,20\nQk 20,8,9\nQk 21,10,11\nQcvaa 0,112,48,36\nQe 12\n"
"Qmvaa 0,116,52\nds_read_b32 v69, v74\nQmvaa 0,120,56\nQe 13\nQmvaa 0,124,60\nQe 14\nQcvaa 4,128,48,36\nQe 15\n"
"Qmvaa 4,132,52\nQk 22,12,13\nQk 23,14,15\nQmvaa 4,136,56\ns_waitcnt lgkmcnt(0)\nv_cmp_gt_u32 vcc, v75, v69\nQmvaa 4,140,60\ns_cbranch_vccnz .Lsa1\n"
".Lra1:\nQmvav 112,144,16\ns_add_u32 m0, s61, 33024\nQmvav 116,148,16\nbuffer_load_dwordx4 v56, s[40:43], s60 offen nt lds\nQmvav 120,152,16\nQmvav 124,156,16\nQe 0\n"
"Qmvav 128,160,16\nQe 1\nQmvav 132,164,16\nQe 2\nQmvav 136,168,16\nbuffer_load_dwordx4 v57, s[40:43], s60 offen offset:1024 nt lds\nQmvav 140,172,16\nQe 3\n"
"Qmaav 180,204,16\nQk 16,0,1\nQk 17,2,3\nQcvaa 8,112,64,40\nQe 4\nQmvaa 8,116,68\nQe 5\nQmvaa 8,120,72\n"
"buffer_load_dwordx4 v56, s[40:43], s60 offen offset:2048 nt lds\nQmvaa 8,124,76\nQe 6\nQcvaa 12,128,64,40\nQe 7\nQmvaa 12,132,68\nQk 18,4,5\nQk 19,6,7\n"
"Qmvaa 12,136,72\nQmvaa 12,140,76\nbuffer_load_dwordx4 v57, s[40:43], s60 offen offset:3072 nt lds\nQmvav 144,144,20\nQmvav 148,148,20\nQmvav 152,152,20\nQmvav 156,156,20\ns_add_u32 m0, s61, 41216\n"
"Qmvav 160,160,20\nbuffer_load_dwordx4 v58, s[44:47], s60 offen nt lds\nQmvav 164,164,20\nQe 8\nQmvav 168,168,20\nQe 9\nQmvav 172,172,20\nQe 10\n"
"Qmaav 184,204,20\nQe 11\nQcvaa 0,112,80,44\nbuffer_load_dwordx4 v59, s[44:47], s60 offen offset:1024 nt lds\nQmvaa 0,116,84\nQk 20,8,9\nQk 21,10,11\nQmvaa 0,120,88\n"
"Qe 12\nQmvaa 0,124,92\nQe 13\nQcvaa 4,128,80,44\nQe 14\nQmvaa 4,132,84\nbuffer_load_dwordx4 v58, s[44:47], s60 offen offset:2048 nt lds\nQmvaa 4,136,88\n"
"Qe 15\nQmvaa 4,140,92\nQk 22,12,13\nQk 23,14,15\nQmvav 176,144,16\nQmvav 180,148,16\nQmvav 184,152,16\nbuffer_load_dwordx4 v59, s[44:47], s60 offen offset:3072 nt lds\n"
"Qmvav 188,156,16\nQe 0\nQmvav 192,160,16\nQe 1\nQmvav 196,164,16\ns_add_u32 s60, s60, 8192\nQmvav 200,168,16\nQe 2\n"
"Qmvav 204,172,16\nQe 3\nQmaav 188,204,16\nQk 16,0,1\nQk 17,2,3\nQcvaa 8,112,96,48\nQda 112,53,0\nQmvaa 8,116,100\n"
"Qda 116,53,512\nQmvaa 8,120,104\nQda 120,53,1024\nQmvaa 8,124,108\nQda 124,53,1536\nQcvaa 12,128,96,48\nQda 128,53,2048\nQmvaa 12,132,100\n"
"Qda 132,53,2560\nQmvaa 12,136,104\nQda 136,53,3072\nQmvaa 12,140,108\nQda 140,53,3584\nQmvav 208,144,20\nQe 4\nQmvav 212,148,20\n"
"Qe 5\nQmvav 216,152,20\nQe 6\nQmvav 220,156,20\nQe 7\nQmvav 224,160,20\nQk 18,4,5\nQk 19,6,7\n"
"Qmvav 228,164,20\nQe 8\nQmvav 232,168,20\nQe 9\nQmvav 236,172,20\nQe 10\nQmaav 192,204,20\ns_waitcnt lgkmcnt(0)\n"
"Qk 20,8,9\nQcvaa 0,112,0,24\nQe 11\nQmvaa 0,116,4\nQk 21,10,11\nQmvaa 0,120,8\nQe 12\nQmvaa 0,124,12\n"
"Qe 13\nQcvaa 4,128,0,24\nQe 14\nQmvaa 4,132,4\nQe 15\nQmvaa 4,136,8\nQk 22,12,13\nQk 23,14,15\n"
"Qmvaa 4,140,12\nQmvav 240,144,16\nQmvav 244,148,16\nQmvav 248,152,16\nQmvav 252,156,16\nQe 0\nQmaav 208,160,16\nQe 1\n"
"Qmaav 212,164,16\nQe 2\nQmaav 216,168,16\nQe 3\nQmaav 220,172,16\nQe 4\nQmaav 196,204,16\nQk 16,0,1\n"
"Qk 17,2,3\nQcvaa 8,112,16,28\nQe 5\nQmvaa 8,116,20\nQe 6\nQmvaa 8,120,24\nQe 7\nQmvaa 8,124,28\n"
"Qk 18,4,5\nQk 19,6,7\nQcvaa 12,128,16,28\nQmvaa 12,132,20\nQmvaa 12,136,24\nQmvaa 12,140,28\nQmaav 224,144,20\nQta 144,55,0\n"
"Qta 146,55,2048\nQmaav 228,148,20\nQta 148,55,256\nQta 150,55,2304\nQmaav 232,152,20\nQta 152,55,512\nQta 154,55,2560\nQmaav 236,156,20\n"
"Qta 156,55,768\nQta 158,55,2816\nQmaav 240,160,20\nQta 160,55,1024\nQta 162,55,3072\nQmaav 244,164,20\nQta 164,55,1280\nQta 166,55,3328\n"
"Qmaav 248,168,20\nQta 168,55,1536\nQta 170,55,3584\nQmaav 252,172,20\nQta 172,55,1792\nQta 174,55,3840\nQmaav 200,204,20\nQe 8\n"
"Qcvaa 0,112,32,32\nv_add_u32 v75, 1, v75\nQmvaa 0,116,36\ns_waitcnt vmcnt(16)\nds_write_b32 v73, v75\nQmvaa 0,120,40\nQe 9\nQmvaa 0,124,44\n"
"Qe 10\nQcvaa 4,128,32,32\nQe 11\nQmvaa 4,132,36\nQk 20,8,9\nQk 21,10,11\nQmvaa 4,136,40\nQe 12\n"
"Qmvaa 4,140,44\ns_waitcnt lgkmcnt(1)\nQmvav 80,144,16\nQe 13\nQmvav 84,148,16\nQe 14\nQmvav 88,152,16\nQe 15\n"
"Qmvav 92,156,16\nQk 22,12,13\nQk 23,14,15\nQmvav 96,160,16\nQe 0\nQmvav 100,164,16\nQe 1\nQmvav 104,168,16\n"
"Qe 2\nQmvav 108,172,16\nQe 3\nQmaav 176,204,16\nQk 16,0,1\nQk 17,2,3\nQcvaa 8,112,48,36\nQe 4\n"
"Qmvaa 8,116,52\nds_read_b32 v69, v74\nQmvaa 8,120,56\nQe 5\nQmvaa 8,124,60\nQe 6\nQcvaa 12,128,48,36\nQe 7\n"
"Qmvaa 12,132,52\nQk 18,4,5\nQk 19,6,7\nQmvaa 12,136,56\ns_waitcnt lgkmcnt(0)\nv_cmp_gt_u32 vcc, v75, v69\nQmvaa 12,140,60\ns_cbranch_vccnz .Lsa2\n"
".Lra2:\nQmvav 112,144,20\ns_add_u32 m0, s61, 66048\nQmvav 116,148,20\nbuffer_load_dwordx4 v56, s[40:43], s60 offen nt lds\nQmvav 120,152,20\nQmvav 124,156,20\nQe 8\n"
"Qmvav 128,160,20\nQe 9\nQmvav 132,164,20\nQe 10\nQmvav 136,168,20\nbuffer_load_dwordx4 v57, s[40:43], s60 offen offset:1024 nt lds\nQmvav 140,172,20\nQe 11\n"
"Qmaav 180,204,20\nQk 20,8,9\nQk 21,10,11\nQcvaa 0,112,64,40\nQe 12\nQmvaa 0,116,68\nQe 13\nQmvaa 0,120,72\n"
"buffer_load_dwordx4 v56, s[40:43], s60 offen offset:2048 nt lds\nQmvaa 0,124,76\nQe 14\nQcvaa 4,128,64,40\nQe 15\nQmvaa 4,132,68\nQk 22,12,13\nQk 23,14,15\n"
"Qmvaa 4,136,72\nQmvaa 4,140,76\nbuffer_load_dwordx4 v57, s[40:43], s60 offen offset:3072 nt lds\nQmvav 144,144,16\nQmvav 148,148,16\nQmvav 152,152,16\nQmvav 156,156,16\ns_add_u32 m0, s61, 74240\n"
"Qmvav 160,160,16\nbuffer_load_dwordx4 v58, s[44:47], s60 offen nt lds\nQmvav 164,164,16\nQe 0\nQmvav 168,168,16\nQe 1\nQmvav 172,172,16\nQe 2\n"
"Qmaav 184,204,16\nQe 3\nQcvaa 8,112,80,44\nbuffer_load_dwordx4 v59, s[44:47], s60 offen offset:1024 nt lds\nQmvaa 8,116,84\nQk 16,0,1\nQk 17,2,3\nQmvaa 8,120,88\n"
"Qe 4\nQmvaa 8,124,92\nQe 5\nQcvaa 12,128,80,44\nQe 6\nQmvaa 12,132,84\nbuffer_load_dwordx4 v58, s[44:47], s60 offen offset:2048 nt lds\nQmvaa 12,136,88\n"
"Qe 7\nQmvaa 12,140,92\nQk 18,4,5\nQk 19,6,7\nQmvav 176,144,20\nQmvav 180,148,20\nQmvav 184,152,20\nbuffer_load_dwordx4 v59, s[44:47], s60 offen offset:3072 nt lds\n"
"Qmvav 188,156,20\nQe 8\nQmvav 192,160,20\nQe 9\nQmvav 196,164,20\ns_add_u32 s60, s60, 8192\nQmvav 200,168,20\nQe 10\n"
"Qmvav 204,172,20\nQe 11\nQmaav 188,204,20\nQk 20,8,9\nQk 21,10,11\nQcvaa 0,112,96,48\nQda 112,53,33024\nQmvaa 0,116,100\n"
"Qda 116,53,33536\nQmvaa 0,120,104\nQda 120,53,34048\nQmvaa 0,124,108\nQda 124,53,34560\nQcvaa 4,128,96,48\nQda 128,53,35072\nQmvaa 4,132,100\n"
"Qda 132,53,35584\nQmvaa 4,136,104\nQda 136,53,36096\nQmvaa 4,140,108\nQda 140,53,36608\nQmvav 208,144,16\nQe 12\nQmvav 212,148,16\n"
"Qe 13\nQmvav 216,152,16\nQe 14\nQmvav 220,156,16\nQe 15\nQmvav 224,160,16\nQk 22,12,13\nQk 23,14,15\n"
"Qmvav 228,164,16\nQe 0\nQmvav 232,168,16\nQe 1\nQmvav 236,172,16\nQe 2\nQmaav 192,204,16\ns_waitcnt lgkmcnt(0)\n"
"Qk 16,0,1\nQcvaa 8,112,0,24\nQe 3\nQmvaa 8,116,4\nQk 17,2,3\nQmvaa 8,120,8\nQe 4\nQmvaa 8,124,12\n"
"Qe 5\nQcvaa 12,128,0,24\nQe 6\nQmvaa 12,132,4\nQe 7\nQmvaa 12,136,8\nQk 18,4,5\nQk 19,6,7\n"
"Qmvaa 12,140,12\nQmvav 240,144,20\nQmvav 244,148,20\nQmvav 248,152,20\nQmvav 252,156,20\nQe 8\nQmaav 208,160,20\nQe 9\n"
"Qmaav 212,164,20\nQe 10\nQmaav 216,168,20\nQe 11\nQmaav 220,172,20\nQe 12\nQmaav 196,204,20\nQk 20,8,9\n"
"Qk 21,10,11\nQcvaa 0,112,16,28\nQe 13\nQmvaa 0,116,20\nQe 14\nQmvaa 0,120,24\nQe 15\nQmvaa 0,124,28\n"
"Qk 22,12,13\nQk 23,14,15\nQcvaa 4,128,16,28\nQmvaa 4,132,20\nQmvaa 4,136,24\nQmvaa 4,140,28\nQmaav 224,144,16\nQta 144,55,33024\n"
"Qta 146,55,35072\nQmaav 228,148,16\nQta 148,55,33280\nQta 150,55,35328\nQmaav 232,152,16\nQta 152,55,33536\nQta 154,55,35584\nQmaav 236,156,16\n"
"Qta 156,55,33792\nQta 158,55,35840\nQmaav 240,160,16\nQta 160,55,34048\nQta 162,55,36096\nQmaav 244,164,16\nQta 164,55,34304\nQta 166,55,36352\n"
"Qmaav 248,168,16\nQta 168,55,34560\nQta 170,55,36608\nQmaav 252,172,16\nQta 172,55,34816\nQta 174,55,36864\nQmaav 200,204,16\nQe 0\n"
"Qcvaa 8,112,32,32\nv_add_u32 v75, 1, v75\nQmvaa 8,116,36\ns_waitcnt vmcnt(16)\nds_write_b32 v73, v75\nQmvaa 8,120,40\nQe 1\nQmvaa 8,124,44\n"
"Qe 2\nQcvaa 12,128,32,32\nQe 3\nQmvaa 12,132,36\nQk 16,0,1\nQk 17,2,3\nQmvaa 12,136,40\nQe 4\n"
"Qmvaa 12,140,44\ns_waitcnt lgkmcnt(1)\nQmvav 80,144,20\nQe 5\nQmvav 84,148,20\nQe 6\nQmvav 88,152,20\nQe 7\n"
"Qmvav 92,156,20\nQk 18,4,5\nQk 19,6,7\nQmvav 96,160,20\nQe 8\nQmvav 100,164,20\nQe 9\nQmvav 104,168,20\n"
"Qe 10\nQmvav 108,172,20\nQe 11\nQmaav 176,204,20\nQk 20,8,9\nQk 21,10,11\nQcvaa 0,112,48,36\nQe 12\n"
"Qmvaa 0,116,52\nds_read_b32 v69, v74\nQmvaa 0,120,56\nQe 13\nQmvaa 0,124,60\nQe 14\nQcvaa 4,128,48,36\nQe 15\n"
"Qmvaa 4,132,52\nQk 22,12,13\nQk 23,14,15\nQmvaa 4,136,56\ns_waitcnt lgkmcnt(0)\nv_cmp_gt_u32 vcc, v75, v69\nQmvaa 4,140,60\ns_cbranch_vccnz .Lsa3\n"
".Lra3:\nQmvav 112,144,16\ns_add_u32 m0, s61, 99072\nQmvav 116,148,16\nbuffer_load_dwordx4 v56, s[40:43], s60 offen nt lds\nQmvav 120,152,16\nQmvav 124,156,16\nQe 0\n"
"Qmvav 128,160,16\nQe 1\nQmvav 132,164,16\nQe 2\nQmvav 136,168,16\nbuffer_load_dwordx4 v57, s[40:43], s60 offen offset:1024 nt lds\nQmvav 140,172,16\nQe 3\n"
"Qmaav 180,204,16\nQk 16,0,1\nQk 17,2,3\nQcvaa 8,112,64,40\nQe 4\nQmvaa 8,116,68\nQe 5\nQmvaa 8,120,72\n"
"buffer_load_dwordx4 v56, s[40:43], s60 offen offset:2048 nt lds\nQmvaa 8,124,76\nQe 6\nQcvaa 12,128,64,40\nQe 7\nQmvaa 12,132,68\nQk 18,4,5\nQk 19,6,7\n"
"Qmvaa 12,136,72\nQmvaa 12,140,76\nbuffer_load_dwordx4 v57, s[40:43], s60 offen offset:3072 nt lds\nQmvav 144,144,20\nQmvav 148,148,20\nQmvav 152,152,20\nQmvav 156,156,20\ns_add_u32 m0, s61, 107264\n"
"Qmvav 160,160,20\nbuffer_load_dwordx4 v58, s[44:47], s60 offen nt lds\nQmvav 164,164,20\nQe 8\nQmvav 168,168,20\nQe 9\nQmvav 172,172,20\nQe 10\n"
"Qmaav 184,204,20\nQe 11\nQcvaa 0,112,80,44\nbuffer_load_dwordx4 v59, s[44:47], s60 offen offset:1024 nt lds\nQmvaa 0,116,84\nQk 20,8,9\nQk 21,10,11\nQmvaa 0,120,88\n"
"Qe 12\nQmvaa 0,124,92\nQe 13\nQcvaa 4,128,80,44\nQe 14\nQmvaa 4,132,84\nbuffer_load_dwordx4 v58, s[44:47], s60 offen offset:2048 nt lds\nQmvaa 4,136,88\n"
"Qe 15\nQmvaa 4,140,92\nQk 22,12,13\nQk 23,14,15\nQmvav 176,144,16\nQmvav 180,148,16\nQmvav 184,152,16\nbuffer_load_dwordx4 v59, s[44:47], s60 offen offset:3072 nt lds\n"
"Qmvav 188,156,16\nQe 0\nQmvav 192,160,16\nQe 1\nQmvav 196,164,16\ns_add_u32 s60, s60, 8192\ns_and_b32 s68, s60, 0x40000\ns_mul_i32 s68, s68, 7\n"
"s_add_u32 s60, s60, s68\nQmvav 200,168,16\nQe 2\nQmvav 204,172,16\nQe 3\nQmaav 188,204,16\nQk 16,0,1\nQk 17,2,3\n"
"Qcvaa 8,112,96,48\nQda 112,52,0\nQmvaa 8,116,100\nQda 116,52,512\nQmvaa 8,120,104\nQda 120,52,1024\nQmvaa 8,124,108\nQda 124,52,1536\n"
"Qcvaa 12,128,96,48\nQda 128,52,2048\nQmvaa 12,132,100\nQda 132,52,2560\nQmvaa 12,136,104\nQda 136,52,3072\nQmvaa 12,140,108\nQda 140,52,3584\n"
"Qmvav 208,144,20\nQe 4\nQmvav 212,148,20\nQe 5\nQmvav 216,152,20\nQe 6\nQmvav 220,156,20\nQe 7\n"
"Qmvav 224,160,20\nQk 18,4,5\nQk 19,6,7\nQmvav 228,164,20\nQe 8\nQmvav 232,168,20\nQe 9\nQmvav 236,172,20\n"
"Qe 10\nQmaav 192,204,20\ns_waitcnt lgkmcnt(0)\nQk 20,8,9\nQcvaa 0,112,0,24\nQe 11\nQmvaa 0,116,4\nQk 21,10,11\n"
"Qmvaa 0,120,8\nQe 12\nQmvaa 0,124,12\nQe 13\nQcvaa 4,128,0,24\nQe 14\nQmvaa 4,132,4\nQe 15\n"
"Qmvaa 4,136,8\nQk 22,12,13\nQk 23,14,15\nQmvaa 4,140,12\nQmvav 240,144,16\nQmvav 244,148,16\nQmvav 248,152,16\nQmvav 252,156,16\n"
"Qe 0\nQmaav 208,160,16\nQe 1\nQmaav 212,164,16\nQe 2\nQmaav 216,168,16\nQe 3\nQmaav 220,172,16\n"
"Qe 4\nQmaav 196,204,16\nQk 16,0,1\nQk 17,2,3\nQcvaa 8,112,16,28\nQe 5\nQmvaa 8,116,20\nQe 6\n"
"Qmvaa 8,120,24\nQe 7\nQmvaa 8,124,28\nQk 18,4,5\nQk 19,6,7\nQcvaa 12,128,16,28\nQmvaa 12,132,20\nQmvaa 12,136,24\n"
"Qmvaa 12,140,28\nQmaav 224,144,20\nQta 144,54,0\nQta 146,54,2048\nQmaav 228,148,20\nQta 148,54,256\nQta 150,54,2304\nQmaav 232,152,20\n"
"Qta 152,54,512\nQta 154,54,2560\nQmaav 236,156,20\nQta 156,54,768\nQta 158,54,2816\nQmaav 240,160,20\nQta 160,54,1024\nQta 162,54,3072\n"
"Qmaav 244,164,20\nQta 164,54,1280\nQta 166,54,3328\nQmaav 248,168,20\nQta 168,54,1536\nQta 170,54,3584\nQmaav 252,172,20\nQta 172,54,1792\n"
"Qta 174,54,3840\nQmaav 200,204,20\nQe 8\nQcvaa 0,112,32,32\nv_add_u32 v75, 1, v75\nQmvaa 0,116,36\ns_waitcnt vmcnt(16)\nds_write_b32 v73, v75\n"
"Qmvaa 0,120,40\nQe 9\nQmvaa 0,124,44\nQe 10\nQcvaa 4,128,32,32\nQe 11\nQmvaa 4,132,36\nQk 20,8,9\n"
"Qk 21,10,11\nQmvaa 4,136,40\nQe 12\nQmvaa 4,140,44\ns_waitcnt lgkmcnt(1)\nQmvav 80,144,16\nQe 13\nQmvav 84,148,16\n"
"Qe 14\nQmvav 88,152,16\nQe 15\nQmvav 92,156,16\nQk 22,12,13\nQk 23,14,15\nQmvav 96,160,16\nQe 0\n"
"Qmvav 100,164,16\nQe 1\nQmvav 104,168,16\nQe 2\nQmvav 108,172,16\nQe 3\nQmaav 176,204,16\nQk 16,0,1\n"
"Qk 17,2,3\nQcvaa 8,112,48,36\nQe 4\nQmvaa 8,116,52\nds_read_b32 v69, v74\nQmvaa 8,120,56\nQe 5\nQmvaa 8,124,60\n"
"Qe 6\nQcvaa 12,128,48,36\nQe 7\nQmvaa 12,132,52\nQk 18,4,5\nQk 19,6,7\nQmvaa 12,136,56\ns_waitcnt lgkmcnt(0)\n"
"v_cmp_gt_u32 vcc, v75, v69\nQmvaa 12,140,60\ns_cbranch_vccnz .Lsa4\n.Lra4:\nQmvav 112,144,20\ns_add_u32 m0, s61, 0\nQmvav 116,148,20\nbuffer_load_dwordx4 v56, s[40:43], s60 offen nt lds\n"
"Qmvav 120,152,20\nQmvav 124,156,20\nQe 8\nQmvav 128,160,20\nQe 9\nQmvav 132,164,20\nQe 10\nQmvav 136,168,20\n"
"buffer_load_dwordx4 v57, s[40:43], s60 offen offset:1024 nt lds\nQmvav 140,172,20\nQe 11\nQmaav 180,204,20\nQk 20,8,9\nQk 21,10,11\nQcvaa 0,112,64,40\nQe 12\n"
"Qmvaa 0,116,68\nQe 13\nQmvaa 0,120,72\nbuffer_load_dwordx4 v56, s[40:43], s60 offen offset:2048 nt lds\nQmvaa 0,124,76\nQe 14\nQcvaa 4,128,64,40\nQe 15\n"
"Qmvaa 4,132,68\nQk 22,12,13\nQk 23,14,15\nQmvaa 4,136,72\nQmvaa 4,140,76\nbuffer_load_dwordx4 v57, s[40:43], s60 offen offset:3072 nt lds\nQmvav 144,144,16\nQmvav 148,148,16\n"
"Qmvav 152,152,16\nQmvav 156,156,16\ns_add_u32 m0, s61, 8192\nQmvav 160,160,16\nbuffer_load_dwordx4 v58, s[44:47], s60 offen nt lds\nQmvav 164,164,16\nQe 0\nQmvav 168,168,16\n"
"Qe 1\nQmvav 172,172,16\nQe 2\nQmaav 184,204,16\nQe 3\nQcvaa 8,112,80,44\nbuffer_load_dwordx4 v59, s[44:47], s60 offen offset:1024 nt lds\nQmvaa 8,116,84\n"
"Qk 16,0,1\nQk 17,2,3\nQmvaa 8,120,88\nQe 4\nQmvaa 8,124,92\nQe 5\nQcvaa 12,128,80,44\nQe 6\n"
"Qmvaa 12,132,84\nbuffer_load_dwordx4 v58, s[44:47], s60 offen offset:2048 nt lds\nQmvaa 12,136,88\nQe 7\nQmvaa 12,140,92\nQk 18,4,5\nQk 19,6,7\nQmvav 176,144,20\n"
"Qmvav 180,148,20\nQmvav 184,152,20\nbuffer_load_dwordx4 v59, s[44:47], s60 offen offset:3072 nt lds\nQmvav 188,156,20\nQe 8\nQmvav 192,160,20\nQe 9\nQmvav 196,164,20\n"
"s_add_u32 s60, s60, 8192\nQmvav 200,168,20\nQe 10\nQmvav 204,172,20\nQe 11\nQmaav 188,204,20\nQk 20,8,9\nQk 21,10,11\n"
"Qcvaa 0,112,96,48\nQda 112,52,33024\nQmvaa 0,116,100\nQda 116,52,33536\nQmvaa 0,120,104\nQda 120,52,34048\nQmvaa 0,124,108\nQda 124,52,34560\n"
"Qcvaa 4,128,96,48\nQda 128,52,35072\nQmvaa 4,132,100\nQda 132,52,35584\nQmvaa 4,136,104\nQda 136,52,36096\nQmvaa 4,140,108\nQda 140,52,36608\n"
"Qmvav 208,144,16\nQe 12\nQmvav 212,148,16\nQe 13\nQmvav 216,152,16\nQe 14\nQmvav 220,156,16\nQe 15\n"
"Qmvav 224,160,16\nQk 22,12,13\nQk 23,14,15\nQmvav 228,164,16\nQe 0\nQmvav 232,168,16\nQe 1\nQmvav 236,172,16\n"
"Qe 2\nQmaav 192,204,16\ns_waitcnt lgkmcnt(0)\nQk 16,0,1\nQcvaa 8,112,0,24\nQe 3\nQmvaa 8,116,4\nQk 17,2,3\n"
"Qmvaa 8,120,8\nQe 4\nQmvaa 8,124,12\nQe 5\nQcvaa 12,128,0,24\nQe 6\nQmvaa 12,132,4\nQe 7\n"
"Qmvaa 12,136,8\nQk 18,4,5\nQk 19,6,7\nQmvaa 12,140,12\nQmvav 240,144,20\nQmvav 244,148,20\nQmvav 248,152,20\nQmvav 252,156,20\n"
"Qe 8\nQmaav 208,160,20\nQe 9\nQmaav 212,164,20\nQe 10\nQmaav 216,168,20\nQe 11\nQmaav 220,172,20\n"
"Qe 12\nQmaav 196,204,20\nQk 20,8,9\nQk 21,10,11\nQcvaa 0,112,16,28\nQe 13\nQmvaa 0,116,20\nQe 14\n"
"Qmvaa 0,120,24\nQe 15\nQmvaa 0,124,28\nQk 22,12,13\nQk 23,14,15\nQcvaa 4,128,16,28\nQmvaa 4,132,20\nQmvaa 4,136,24\n"
"Qmvaa 4,140,28\nQmaav 224,144,16\nQta 144,54,33024\nQta 146,54,35072\nQmaav 228,148,16\nQta 148,54,33280\nQta 150,54,35328\nQmaav 232,152,16\n"
"Qta 152,54,33536\nQta 154,54,35584\nQmaav 236,156,16\nQta 156,54,33792\nQta 158,54,35840\nQmaav 240,160,16\nQta 160,54,34048\nQta 162,54,36096\n"
"Qmaav 244,164,16\nQta 164,54,34304\nQta 166,54,36352\nQmaav 248,168,16\nQta 168,54,34560\nQta 170,54,36608\nQmaav 252,172,16\nQta 172,54,34816\n"
"Qta 174,54,36864\nQmaav 200,204,16\nQe 0\ns_add_u32 s66, s66, 1\ns_bitcmp1_b32 0x7f7f, s66\ns_cbranch_scc1 .Lloop\ns_cmp_lg_u32 s67, 0\ns_cbranch_scc1 .Ltail1\n"
"Qcvaa 8,112,32,32\nv_add_u32 v75, 1, v75\nQmvaa 8,116,36\ns_waitcnt vmcnt(16)\nds_write_b32 v73, v75\nQmvaa 8,120,40\nQe 1\nQmvaa 8,124,44\n"
"Qe 2\nQcvaa 12,128,32,32\nQe 3\nQmvaa 12,132,36\nQk 16,0,1\nQk 17,2,3\nQmvaa 12,136,40\nQe 4\n"
"Qmvaa 12,140,44\ns_waitcnt lgkmcnt(1)\nQmvav 80,144,20\nQe 5\nQmvav 84,148,20\nQe 6\nQmvav 88,152,20\nQe 7\n"
"Qmvav 92,156,20\nQk 18,4,5\nQk 19,6,7\nQmvav 96,160,20\nQe 8\nQmvav 100,164,20\nQe 9\nQmvav 104,168,20\n"
"Qe 10\nQmvav 108,172,20\nQe 11\nQmaav 176,204,20\nQk 20,8,9\nQk 21,10,11\nQcvaa 0,112,48,36\nQe 12\n"
"Qmvaa 0,116,52\nds_read_b32 v69, v74\nQmvaa 0,120,56\nQe 13\nQmvaa 0,124,60\nQe 14\nQcvaa 4,128,48,36\nQe 15\n"
"Qmvaa 4,132,52\nQk 22,12,13\nQk 23,14,15\nQmvaa 4,136,56\ns_waitcnt lgkmcnt(0)\nv_cmp_gt_u32 vcc, v75, v69\nQmvaa 4,140,60\ns_cbranch_vccnz .Lsa29\n"
".Lra29:\nQmvav 112,144,16\ns_add_u32 m0, s62, 0\ns_add_u32 s69, s87, 0\nQmvav 116,148,16\nbuffer_load_dwordx4 v62, s[52:55], s69 offen lds\nQmvav 120,152,16\nQmvav 124,156,16\n"
"Qe 0\nQmvav 128,160,16\nQe 1\nQmvav 132,164,16\ns_add_u32 s69, s87, 31744\nQe 2\nQmvav 136,168,16\nbuffer_load_dwordx4 v62, s[52:55], s69 offen offset:1024 lds\n"
"Qmvav 140,172,16\nQe 3\nQmaav 180,204,16\nQk 16,0,1\nQk 17,2,3\nQcvaa 8,112,64,40\nQe 4\nQmvaa 8,116,68\n"
"s_add_u32 s69, s87, 63480\nQe 5\nQmvaa 8,120,72\nbuffer_load_dwordx4 v62, s[52:55], s69 offen offset:2056 lds\nQmvaa 8,124,76\nQe 6\nQcvaa 12,128,64,40\nQe 7\n"
"Qmvaa 12,132,68\nQk 18,4,5\nQk 19,6,7\nQmvaa 12,136,72\ns_add_u32 s69, s87, 95224\nQmvaa 12,140,76\nbuffer_load_dwordx4 v62, s[52:55], s69 offen offset:3080 lds\nQmvav 144,144,20\n"
"Qmvav 148,148,20\nQmvav 152,152,20\nQmvav 156,156,20\ns_add_u32 m0, s62, 16448\ns_add_u32 s69, s87, 262144\nQmvav 160,160,20\nbuffer_load_dwordx4 v62, s[52:55], s69 offen lds\nQmvav 164,164,20\n"
"Qe 8\nQmvav 168,168,20\nQe 9\nQmvav 172,172,20\nQe 10\nQmaav 184,204,20\ns_add_u32 s69, s87, 293888\nQe 11\n"
"Qcvaa 0,112,80,44\nbuffer_load_dwordx4 v62, s[52:55], s69 offen offset:1024 lds\nQmvaa 0,116,84\nQk 20,8,9\nQk 21,10,11\nQmvaa 0,120,88\nQe 12\nQmvaa 0,124,92\n"
"Qe 13\nQcvaa 4,128,80,44\ns_add_u32 s69, s87, 325624\nQe 14\nQmvaa 4,132,84\nbuffer_load_dwordx4 v62, s[52:55], s69 offen offset:2056 lds\nQmvaa 4,136,88\nQe 15\n"
"Qmvaa 4,140,92\nQk 22,12,13\nQk 23,14,15\nQmvav 176,144,16\nQmvav 180,148,16\ns_add_u32 s69, s87, 357368\nQmvav 184,152,16\nbuffer_load_dwordx4 v62, s[52:55], s69 offen offset:3080 lds\n"
"Qmvav 188,156,16\nQe 0\nQmvav 192,160,16\nQe 1\nQmvav 196,164,16\nQmvav 200,168,16\nQe 2\nQmvav 204,172,16\n"
"Qe 3\nQmaav 188,204,16\nQk 16,0,1\nQk 17,2,3\nQcvaa 8,112,96,48\nQda 112,53,0\nQmvaa 8,116,100\nQda 116,53,512\n"
"Qmvaa 8,120,104\nQda 120,53,1024\nQmvaa 8,124,108\nQda 124,53,1536\nQcvaa 12,128,96,48\nQda 128,53,2048\nQmvaa 12,132,100\nQda 132,53,2560\n"
"Qmvaa 12,136,104\nQda 136,53,3072\nQmvaa 12,140,108\nQda 140,53,3584\nQmvav 208,144,20\nQe 4\nQmvav 212,148,20\ns_add_u32 m0, s63, 33024\n"
"s_add_u32 s88, s86, 0\nQe 5\nQmvav 216,152,20\nbuffer_load_dwordx4 v64, s[48:51], s88 offen lds\nQe 6\nQmvav 220,156,20\nQe 7\nQmvav 224,160,20\n"
"Qk 18,4,5\nQk 19,6,7\nQmvav 228,164,20\ns_add_u32 s88, s86, 19456\nQe 8\nQmvav 232,168,20\nbuffer_load_dwordx4 v64, s[48:51], s88 offen offset:1024 lds\nQe 9\n"
"Qmvav 236,172,20\nQe 10\nQmaav 192,204,20\ns_waitcnt lgkmcnt(0)\nQk 20,8,9\nQcvaa 0,112,0,24\ns_add_u32 s88, s86, 38912\nQe 11\n"
"Qmvaa 0,116,4\nbuffer_load_dwordx4 v64, s[48:51], s88 offen offset:2048 lds\nQk 21,10,11\nQmvaa 0,120,8\nQe 12\nQmvaa 0,124,12\nQe 13\nQcvaa 4,128,0,24\n"
"s_add_u32 s88, s86, 58368\nQe 14\nQmvaa 4,132,4\nbuffer_load_dwordx4 v64, s[48:51], s88 offen offset:3072 lds\nQe 15\nQmvaa 4,136,8\nQk 22,12,13\nQk 23,14,15\n"
"Qmvaa 4,140,12\nQmvav 240,144,16\ns_add_u32 m0, s63, 37120\ns_add_u32 s88, s86, 81920\nQmvav 244,148,16\nbuffer_load_dwordx4 v64, s[48:51], s88 offen lds\nQmvav 248,152,16\nQmvav 252,156,16\n"
"Qe 0\nQmaav 208,160,16\ns_add_u32 s88, s86, 101376\nQe 1\nQmaav 212,164,16\nbuffer_load_dwordx4 v64, s[48:51], s88 offen offset:1024 lds\nQe 2\nQmaav 216,168,16\n"
"Qe 3\nQmaav 220,172,16\nQe 4\nQmaav 196,204,16\ns_add_u32 s88, s86, 120832\nQk 16,0,1\nQk 17,2,3\nQcvaa 8,112,16,28\n"
"buffer_load_dwordx4 v64, s[48:51], s88 offen offset:2048 lds\nQe 5\nQmvaa 8,116,20\nQe 6\nQmvaa 8,120,24\nQe 7\nQmvaa 8,124,28\nQk 18,4,5\n"
"Qk 19,6,7\nQcvaa 12,128,16,28\nQmvaa 12,132,20\nQmvaa 12,136,24\nQmvaa 12,140,28\nQmaav 224,144,20\nQta 144,55,0\nQta 146,55,2048\n"
"Qmaav 228,148,20\nQta 148,55,256\nQta 150,55,2304\nQmaav 232,152,20\nQta 152,55,512\nQta 154,55,2560\nQmaav 236,156,20\nQta 156,55,768\n"
"Qta 158,55,2816\nQmaav 240,160,20\nQta 160,55,1024\nQta 162,55,3072\nQmaav 244,164,20\nQta 164,55,1280\nQta 166,55,3328\nQmaav 248,168,20\n"
"Qta 168,55,1536\nQta 170,55,3584\nQmaav 252,172,20\nQta 172,55,1792\nQta 174,55,3840\nQmaav 200,204,20\nQe 8\nQcvaa 0,112,32,32\n"
"v_add_u32 v75, 1, v75\nQmvaa 0,116,36\ns_waitcnt vmcnt(23)\nds_write_b32 v73, v75\nQmvaa 0,120,40\nQe 9\nQmvaa 0,124,44\nQe 10\n"
"Qcvaa 4,128,32,32\nQe 11\nQmvaa 4,132,36\nQk 20,8,9\nQk 21,10,11\nQmvaa 4,136,40\nQe 12\nQmvaa 4,140,44\n"
"s_waitcnt lgkmcnt(1)\nQmvav 80,144,16\nQe 13\nQmvav 84,148,16\nQe 14\nQmvav 88,152,16\nQe 15\nQmvav 92,156,16\n"
"Qk 22,12,13\nQk 23,14,15\nQmvav 96,160,16\nQe 0\nQmvav 100,164,16\nQe 1\nQmvav 104,168,16\nQe 2\n"
"Qmvav 108,172,16\nQe 3\nQmaav 176,204,16\nQk 16,0,1\nQk 17,2,3\nQcvaa 8,112,48,36\nQe 4\nQmvaa 8,116,52\n"
"ds_read_b32 v69, v74\nQmvaa 8,120,56\nQe 5\nQmvaa 8,124,60\nQe 6\nQcvaa 12,128,48,36\nQe 7\nQmvaa 12,132,52\n"
"Qk 18,4,5\nQk 19,6,7\nQmvaa 12,136,56\ns_waitcnt lgkmcnt(0)\nv_cmp_gt_u32 vcc, v75, v69\nQmvaa 12,140,60\ns_cbranch_vccnz .Lsa30\n.Lra30:\n"
"Qmvav 112,144,20\ns_add_u32 m0, s62, 33024\ns_add_u32 s69, s87, 524288\nQmvav 116,148,20\nbuffer_load_dwordx4 v62, s[52:55], s69 offen lds\nQmvav 120,152,20\nQmvav 124,156,20\nQe 8\n"
"Qmvav 128,160,20\nQe 9\nQmvav 132,164,20\ns_add_u32 s69, s87, 556032\nQe 10\nQmvav 136,168,20\nbuffer_load_dwordx4 v62, s[52:55], s69 offen offset:1024 lds\nQmvav 140,172,20\n"
"Qe 11\nQmaav 180,204,20\nQk 20,8,9\nQk 21,10,11\nQcvaa 0,112,64,40\nQe 12\nQmvaa 0,116,68\ns_add_u32 s69, s87, 587768\n"
"Qe 13\nQmvaa 0,120,72\nbuffer_load_dwordx4 v62, s[52:55], s69 offen offset:2056 lds\nQmvaa 0,124,76\nQe 14\nQcvaa 4,128,64,40\nQe 15\nQmvaa 4,132,68\n"
"Qk 22,12,13\nQk 23,14,15\nQmvaa 4,136,72\ns_add_u32 s69, s87, 619512\nQmvaa 4,140,76\nbuffer_load_dwordx4 v62, s[52:55], s69 offen offset:3080 lds\nQmvav 144,144,16\nQmvav 148,148,16\n"
"Qmvav 152,152,16\nQmvav 156,156,16\ns_add_u32 m0, s62, 49472\ns_add_u32 s69, s87, 786432\nQmvav 160,160,16\nbuffer_load_dwordx4 v62, s[52:55], s69 offen lds\nQmvav 164,164,16\nQe 0\n"
"Qmvav 168,168,16\nQe 1\nQmvav 172,172,16\nQe 2\nQmaav 184,204,16\ns_add_u32 s69, s87, 818176\nQe 3\nQcvaa 8,112,80,44\n"
"buffer_load_dwordx4 v62, s[52:55], s69 offen offset:1024 lds\nQmvaa 8,116,84\nQk 16,0,1\nQk 17,2,3\nQmvaa 8,120,88\nQe 4\nQmvaa 8,124,92\nQe 5\n"
"Qcvaa 12,128,80,44\ns_add_u32 s69, s87, 849912\nQe 6\nQmvaa 12,132,84\nbuffer_load_dwordx4 v62, s[52:55], s69 offen offset:2056 lds\nQmvaa 12,136,88\nQe 7\nQmvaa 12,140,92\n"
"Qk 18,4,5\nQk 19,6,7\nQmvav 176,144,20\nQmvav 180,148,20\ns_add_u32 s69, s87, 881656\nQmvav 184,152,20\nbuffer_load_dwordx4 v62, s[52:55], s69 offen offset:3080 lds\nQmvav 188,156,20\n"
"Qe 8\nQmvav 192,160,20\nQe 9\nQmvav 196,164,20\nQmvav 200,168,20\nQe 10\nQmvav 204,172,20\nQe 11\n"
"Qmaav 188,204,20\nQk 20,8,9\nQk 21,10,11\nQcvaa 0,112,96,48\nQda 112,53,33024\nQmvaa 0,116,100\nQda 116,53,33536\nQmvaa 0,120,104\n"
"Qda 120,53,34048\nQmvaa 0,124,108\nQda 124,53,34560\nQcvaa 4,128,96,48\nQda 128,53,35072\nQmvaa 4,132,100\nQda 132,53,35584\nQmvaa 4,136,104\n"
"Qda 136,53,36096\nQmvaa 4,140,108\nQda 140,53,36608\nQmvav 208,144,16\nQe 12\nQmvav 212,148,16\nQe 13\nQmvav 216,152,16\n"
"Qe 14\nQmvav 220,156,16\nQe 15\nQmvav 224,160,16\nQk 22,12,13\nQk 23,14,15\nQmvav 228,164,16\nQe 0\n"
"Qmvav 232,168,16\nQe 1\nQmvav 236,172,16\nQe 2\nQmaav 192,204,16\ns_waitcnt lgkmcnt(0)\nQk 16,0,1\nQcvaa 8,112,0,24\n"
"Qe 3\nQmvaa 8,116,4\nQk 17,2,3\nQmvaa 8,120,8\nQe 4\nQmvaa 8,124,12\nQe 5\nQcvaa 12,128,0,24\n"
"Qe 6\nQmvaa 12,132,4\nQe 7\nQmvaa 12,136,8\nQk 18,4,5\nQk 19,6,7\nQmvaa 12,140,12\nQmvav 240,144,20\n"
"Qmvav 244,148,20\nQmvav 248,152,20\nQmvav 252,156,20\nQe 8\nQmaav 208,160,20\nQe 9\nQmaav 212,164,20\nQe 10\n"
"Qmaav 216,168,20\nQe 11\nQmaav 220,172,20\nQe 12\nQmaav 196,204,20\nQk 20,8,9\nQk 21,10,11\nQcvaa 0,112,16,28\n"
"Qe 13\nQmvaa 0,116,20\nQe 14\nQmvaa 0,120,24\nQe 15\nQmvaa 0,124,28\nQk 22,12,13\nQk 23,14,15\n"
"Qcvaa 4,128,16,28\nQmvaa 4,132,20\nQmvaa 4,136,24\nQmvaa 4,140,28\nQmaav 224,144,16\nQta 144,55,33024\nQta 146,55,35072\nQmaav 228,148,16\n"
"Qta 148,55,33280\nQta 150,55,35328\nQmaav 232,152,16\nQta 152,55,33536\nQta 154,55,35584\nQmaav 236,156,16\nQta 156,55,33792\nQta 158,55,35840\n"
"Qmaav 240,160,16\nQta 160,55,34048\nQta 162,55,36096\nQmaav 244,164,16\nQta 164,55,34304\nQta 166,55,36352\nQmaav 248,168,16\nQta 168,55,34560\n"
"Qta 170,55,36608\nQmaav 252,172,16\nQta 172,55,34816\nQta 174,55,36864\nQmaav 200,204,16\nQe 0\nQcvaa 8,112,32,32\nv_add_u32 v75, 1, v75\n"
"Qmvaa 8,116,36\nds_write_b32 v73, v75\nQmvaa 8,120,40\nQe 1\nQmvaa 8,124,44\nQe 2\nQcvaa 12,128,32,32\nQe 3\n"
"Qmvaa 12,132,36\nQk 16,0,1\nQk 17,2,3\nQmvaa 12,136,40\nQe 4\nQmvaa 12,140,44\ns_waitcnt lgkmcnt(1)\nQmvav 80,144,20\n"
"Qe 5\nQmvav 84,148,20\nQe 6\nQmvav 88,152,20\nQe 7\nQmvav 92,156,20\nQk 18,4,5\nQk 19,6,7\n"
"Qmvav 96,160,20\nQe 8\nQmvav 100,164,20\nQe 9\nQmvav 104,168,20\nQe 10\nQmvav 108,172,20\nQe 11\n"
"Qmaav 176,204,20\nQk 20,8,9\nQk 21,10,11\nQcvaa 0,112,48,36\nQe 12\nQmvaa 0,116,52\nds_read_b32 v69, v74\nQmvaa 0,120,56\n"
"Qe 13\nQmvaa 0,124,60\nQe 14\nQcvaa 4,128,48,36\nQe 15\nQmvaa 4,132,52\nQk 22,12,13\nQk 23,14,15\n"
"Qmvaa 4,136,56\ns_waitcnt lgkmcnt(0)\nv_cmp_gt_u32 vcc, v75, v69\nQmvaa 4,140,60\ns_cbranch_vccnz .Lsa31\n.Lra31:\nQmvav 112,144,16\ns_add_u32 m0, s63, 0\n"
"s_add_u32 s88, s86, 128\nQmvav 116,148,16\nbuffer_load_dwordx4 v64, s[48:51], s88 offen lds\nQmvav 120,152,16\nQmvav 124,156,16\nQe 0\nQmvav 128,160,16\ns_add_u32 s88, s86, 19584\n"
"Qe 1\nQmvav 132,164,16\nbuffer_load_dwordx4 v64, s[48:51], s88 offen offset:1024 lds\nQe 2\nQmvav 136,168,16\nQmvav 140,172,16\nQe 3\nQmaav 180,204,16\n"
"s_add_u32 s88, s86, 39040\nQk 16,0,1\nQk 17,2,3\nQcvaa 8,112,64,40\nbuffer_load_dwordx4 v64, s[48:51], s88 offen offset:2048 lds\nQe 4\nQmvaa 8,116,68\nQe 5\n"
"Qmvaa 8,120,72\nQmvaa 8,124,76\ns_add_u32 s88, s86, 58496\nQe 6\nQcvaa 12,128,64,40\nbuffer_load_dwordx4 v64, s[48:51], s88 offen offset:3072 lds\nQe 7\nQmvaa 12,132,68\n"
"Qk 18,4,5\nQk 19,6,7\nQmvaa 12,136,72\nQmvaa 12,140,76\ns_add_u32 m0, s63, 4096\ns_add_u32 s88, s86, 82048\nQmvav 144,144,20\nbuffer_load_dwordx4 v64, s[48:51], s88 offen lds\n"
"Qmvav 148,148,20\nQmvav 152,152,20\nQmvav 156,156,20\ns_add_u32 s88, s86, 101504\nQmvav 160,160,20\nbuffer_load_dwordx4 v64, s[48:51], s88 offen offset:1024 lds\nQmvav 164,164,20\nQe 8\n"
"Qmvav 168,168,20\nQe 9\nQmvav 172,172,20\ns_add_u32 s88, s86, 120960\nQe 10\nQmaav 184,204,20\nbuffer_load_dwordx4 v64, s[48:51], s88 offen offset:2048 lds\nQe 11\n"
"Qcvaa 0,112,80,44\nQmvaa 0,116,84\nQk 20,8,9\nQk 21,10,11\nQmvaa 0,120,88\nQe 12\nQmvaa 0,124,92\nQe 13\n"
"Qcvaa 4,128,80,44\nQe 14\nQmvaa 4,132,84\nQmvaa 4,136,88\nQe 15\nQmvaa 4,140,92\nQk 22,12,13\nQk 23,14,15\n"
"Qmvav 176,144,16\nQmvav 180,148,16\nQmvav 184,152,16\nQmvav 188,156,16\nQe 0\nQmvav 192,160,16\nQe 1\nQmvav 196,164,16\n"
"Qmvav 200,168,16\nQe 2\nQmvav 204,172,16\nQe 3\nQmaav 188,204,16\nQk 16,0,1\nQk 17,2,3\nQcvaa 8,112,96,48\n"
"Qmvaa 8,116,100\nQmvaa 8,120,104\nQmvaa 8,124,108\nQcvaa 12,128,96,48\nQmvaa 12,132,100\nQmvaa 12,136,104\nQmvaa 12,140,108\nQmvav 208,144,20\n"
"Qe 4\nQmvav 212,148,20\nQe 5\nQmvav 216,152,20\nQe 6\nQmvav 220,156,20\nQe 7\nQmvav 224,160,20\n"
"Qk 18,4,5\nQk 19,6,7\nQmvav 228,164,20\nQe 8\nQmvav 232,168,20\nQe 9\nQmvav 236,172,20\nQe 10\n"
"Qmaav 192,204,20\nQk 20,8,9\nQe 11\ns_nop 0\nQk 21,10,11\nQe 12\nQe 13\nQe 14\n"
"Qe 15\nQk 22,12,13\nQk 23,14,15\nQmvav 240,144,16\nQmvav 244,148,16\nQmvav 248,152,16\nQmvav 252,156,16\nQmaav 208,160,16\n"
"Qmaav 212,164,16\nQmaav 216,168,16\nQmaav 220,172,16\nQmaav 196,204,16\nQmaav 224,144,20\nQmaav 228,148,20\nQmaav 232,152,20\nQmaav 236,156,20\n"
"Qmaav 240,160,20\nQmaav 244,164,20\nQmaav 248,168,20\nQmaav 252,172,20\nQmaav 200,204,20\ns_nop 7\nv_accvgpr_read_b32 v24, a176\nv_accvgpr_read_b32 v28, a180\n"
"v_accvgpr_read_b32 v32, a184\nv_accvgpr_read_b32 v36, a188\nv_accvgpr_read_b32 v40, a192\nv_accvgpr_read_b32 v44, a196\nv_accvgpr_read_b32 v48, a200\nv_cmp_nge_f32 vcc, 0x44800000, v24\ns_mov_b64 s[74:75], vcc\nv_cmp_nge_f32 vcc, 0x44800000, v28\n"
"s_or_b64 s[74:75], s[74:75], vcc\nv_cmp_nge_f32 vcc, 0x44800000, v32\ns_or_b64 s[74:75], s[74:75], vcc\nv_cmp_nge_f32 vcc, 0x44800000, v36\ns_or_b64 s[74:75], s[74:75], vcc\nv_cmp_nge_f32 vcc, 0x44800000, v40\ns_or_b64 s[74:75], s[74:75], vcc\nv_cmp_nge_f32 vcc, 0x44800000, v44\n"
"s_or_b64 s[74:75], s[74:75], vcc\nv_cmp_nge_f32 vcc, 0x44800000, v48\ns_or_b64 s[74:75], s[74:75], vcc\ns_cmp_lg_u64 s[74:75], 0\ns_cbranch_scc1 .Lfb\n.Lfbret0:\nv_rcp_f32 v25, v24\nv_rcp_f32 v24, v24\n"
"v_rcp_f32 v29, v28\nv_rcp_f32 v28, v28\nv_rcp_f32 v33, v32\nv_rcp_f32 v32, v32\nv_rcp_f32 v37, v36\nv_rcp_f32 v36, v36\nv_rcp_f32 v41, v40\nv_rcp_f32 v40, v40\n"
"v_rcp_f32 v45, v44\nv_rcp_f32 v44, v44\nv_rcp_f32 v49, v48\nv_rcp_f32 v48, v48\nv_pk_mul_f32 v[80:81], v[80:81], v[24:25]\nv_pk_mul_f32 v[82:83], v[82:83], v[24:25]\nv_pk_mul_f32 v[84:85], v[84:85], v[24:25]\nv_pk_mul_f32 v[86:87], v[86:87], v[24:25]\n"
"Qk 80,80,81\nQk 81,82,83\nQk 82,84,85\nQk 83,86,87\nv_accvgpr_write_b32 a0, v80\nv_accvgpr_write_b32 a1, v81\nv_accvgpr_write_b32 a2, v82\nv_accvgpr_write_b32 a3, v83\n"
"v_pk_mul_f32 v[88:89], v[88:89], v[24:25]\nv_pk_mul_f32 v[90:91], v[90:91], v[24:25]\nv_pk_mul_f32 v[92:93], v[92:93], v[24:25]\nv_pk_mul_f32 v[94:95], v[94:95], v[24:25]\nQk 88,88,89\nQk 89,90,91\nQk 90,92,93\nQk 91,94,95\n"
"v_accvgpr_write_b32 a4, v88\nv_accvgpr_write_b32 a5, v89\nv_accvgpr_write_b32 a6, v90\nv_accvgpr_write_b32 a7, v91\nv_pk_mul_f32 v[96:97], v[96:97], v[24:25]\nv_pk_mul_f32 v[98:99], v[98:99], v[24:25]\nv_pk_mul_f32 v[100:101], v[100:101], v[24:25]\nv_pk_mul_f32 v[102:103], v[102:103], v[24:25]\n"
"Qk 96,96,97\nQk 97,98,99\nQk 98,100,101\nQk 99,102,103\nv_accvgpr_write_b32 a8, v96\nv_accvgpr_write_b32 a9, v97\nv_accvgpr_write_b32 a10, v98\nv_accvgpr_write_b32 a11, v99\n"
"v_pk_mul_f32 v[104:105], v[104:105], v[24:25]\nv_pk_mul_f32 v[106:107], v[106:107], v[24:25]\nv_pk_mul_f32 v[108:109], v[108:109], v[24:25]\nv_pk_mul_f32 v[110:111], v[110:111], v[24:25]\nQk 104,104,105\nQk 105,106,107\nQk 106,108,109\nQk 107,110,111\n"
"v_accvgpr_write_b32 a12, v104\nv_accvgpr_write_b32 a13, v105\nv_accvgpr_write_b32 a14, v106\nv_accvgpr_write_b32 a15, v107\nv_pk_mul_f32 v[112:113], v[112:113], v[28:29]\nv_pk_mul_f32 v[114:115], v[114:115], v[28:29]\nv_pk_mul_f32 v[116:117], v[116:117], v[28:29]\nv_pk_mul_f32 v[118:119], v[118:119], v[28:29]\n"
"Qk 112,112,113\nQk 113,114,115\nQk 114,116,117\nQk 115,118,119\nv_accvgpr_write_b32 a16, v112\nv_accvgpr_write_b32 a17, v113\nv_accvgpr_write_b32 a18, v114\nv_accvgpr_write_b32 a19, v115\n"
"v_pk_mul_f32 v[120:121], v[120:121], v[28:29]\nv_pk_mul_f32 v[122:123], v[122:123], v[28:29]\nv_pk_mul_f32 v[124:125], v[124:125], v[28:29]\nv_pk_mul_f32 v[126:127], v[126:127], v[28:29]\nQk 120,120,121\nQk 121,122,123\nQk 122,124,125\nQk 123,126,127\n"
"v_accvgpr_write_b32 a20, v120\nv_accvgpr_write_b32 a21, v121\nv_accvgpr_write_b32 a22, v122\nv_accvgpr_write_b32 a23, v123\nv_pk_mul_f32 v[128:129], v[128:129], v[28:29]\nv_pk_mul_f32 v[130:131], v[130:131], v[28:29]\nv_pk_mul_f32 v[132:133], v[132:133], v[28:29]\nv_pk_mul_f32 v[134:135], v[134:135], v[28:29]\n"
"Qk 128,128,129\nQk 129,130,131\nQk 130,132,133\nQk 131,134,135\nv_accvgpr_write_b32 a24, v128\nv_accvgpr_write_b32 a25, v129\nv_accvgpr_write_b32 a26, v130\nv_accvgpr_write_b32 a27, v131\n"
"v_pk_mul_f32 v[136:137], v[136:137], v[28:29]\nv_pk_mul_f32 v[138:139], v[138:139], v[28:29]\nv_pk_mul_f32 v[140:141], v[140:141], v[28:29]\nv_pk_mul_f32 v[142:143], v[142:143], v[28:29]\nQk 136,136,137\nQk 137,138,139\nQk 138,140,141\nQk 139,142,143\n"
"v_accvgpr_write_b32 a28, v136\nv_accvgpr_write_b32 a29, v137\nv_accvgpr_write_b32 a30, v138\nv_accvgpr_write_b32 a31, v139\nv_pk_mul_f32 v[144:145], v[144:145], v[32:33]\nv_pk_mul_f32 v[146:147], v[146:147], v[32:33]\nv_pk_mul_f32 v[148:149], v[148:149], v[32:33]\nv_pk_mul_f32 v[150:151], v[150:151], v[32:33]\n"
"Qk 144,144,145\nQk 145,146,147\nQk 146,148,149\nQk 147,150,151\nv_accvgpr_write_b32 a32, v144\nv_accvgpr_write_b32 a33, v145\nv_accvgpr_write_b32 a34, v146\nv_accvgpr_write_b32 a35, v147\n"
"v_pk_mul_f32 v[152:153], v[152:153], v[32:33]\nv_pk_mul_f32 v[154:155], v[154:155], v[32:33]\nv_pk_mul_f32 v[156:157], v[156:157], v[32:33]\nv_pk_mul_f32 v[158:159], v[158:159], v[32:33]\nQk 152,152,153\nQk 153,154,155\nQk 154,156,157\nQk 155,158,159\n"
"v_accvgpr_write_b32 a36, v152\nv_accvgpr_write_b32 a37, v153\nv_accvgpr_write_b32 a38, v154\nv_accvgpr_write_b32 a39, v155\nv_pk_mul_f32 v[160:161], v[160:161], v[32:33]\nv_pk_mul_f32 v[162:163], v[162:163], v[32:33]\nv_pk_mul_f32 v[164:165], v[164:165], v[32:33]\nv_pk_mul_f32 v[166:167], v[166:167], v[32:33]\n"
"Qk 160,160,161\nQk 161,162,163\nQk 162,164,165\nQk 163,166,167\nv_accvgpr_write_b32 a40, v160\nv_accvgpr_write_b32 a41, v161\nv_accvgpr_write_b32 a42, v162\nv_accvgpr_write_b32 a43, v163\n"
"v_pk_mul_f32 v[168:169], v[168:169], v[32:33]\nv_pk_mul_f32 v[170:171], v[170:171], v[32:33]\nv_pk_mul_f32 v[172:173], v[172:173], v[32:33]\nv_pk_mul_f32 v[174:175], v[174:175], v[32:33]\nQk 168,168,169\nQk 169,170,171\nQk 170,172,173\nQk 171,174,175\n"
"v_accvgpr_write_b32 a44, v168\nv_accvgpr_write_b32 a45, v169\nv_accvgpr_write_b32 a46, v170\nv_accvgpr_write_b32 a47, v171\nv_pk_mul_f32 v[176:177], v[176:177], v[36:37]\nv_pk_mul_f32 v[178:179], v[178:179], v[36:37]\nv_pk_mul_f32 v[180:181], v[180:181], v[36:37]\nv_pk_mul_f32 v[182:183], v[182:183], v[36:37]\n"
"Qk 176,176,177\nQk 177,178,179\nQk 178,180,181\nQk 179,182,183\nv_accvgpr_write_b32 a48, v176\nv_accvgpr_write_b32 a49, v177\nv_accvgpr_write_b32 a50, v178\nv_accvgpr_write_b32 a51, v179\n"
"v_pk_mul_f32 v[184:185], v[184:185], v[36:37]\nv_pk_mul_f32 v[186:187], v[186:187], v[36:37]\nv_pk_mul_f32 v[188:189], v[188:189], v[36:37]\nv_pk_mul_f32 v[190:191], v[190:191], v[36:37]\nQk 184,184,185\nQk 185,186,187\nQk 186,188,189\nQk 187,190,191\n"
"v_accvgpr_write_b32 a52, v184\nv_accvgpr_write_b32 a53, v185\nv_accvgpr_write_b32 a54, v186\nv_accvgpr_write_b32 a55, v187\nv_pk_mul_f32 v[192:193], v[192:193], v[36:37]\nv_pk_mul_f32 v[194:195], v[194:195], v[36:37]\nv_pk_mul_f32 v[196:197], v[196:197], v[36:37]\nv_pk_mul_f32 v[198:199], v[198:199], v[36:37]\n"
"Qk 192,192,193\nQk 193,194,195\nQk 194,196,197\nQk 195,198,199\nv_accvgpr_write_b32 a56, v192\nv_accvgpr_write_b32 a57, v193\nv_accvgpr_write_b32 a58, v194\nv_accvgpr_write_b32 a59, v195\n"
"v_pk_mul_f32 v[200:201], v[200:201], v[36:37]\nv_pk_mul_f32 v[202:203], v[202:203], v[36:37]\nv_pk_mul_f32 v[204:205], v[204:205], v[36:37]\nv_pk_mul_f32 v[206:207], v[206:207], v[36:37]\nQk 200,200,201\nQk 201,202,203\nQk 202,204,205\nQk 203,206,207\n"
"v_accvgpr_write_b32 a60, v200\nv_accvgpr_write_b32 a61, v201\nv_accvgpr_write_b32 a62, v202\nv_accvgpr_write_b32 a63, v203\nv_pk_mul_f32 v[208:209], v[208:209], v[40:41]\nv_pk_mul_f32 v[210:211], v[210:211], v[40:41]\nv_pk_mul_f32 v[212:213], v[212:213], v[40:41]\nv_pk_mul_f32 v[214:215], v[214:215], v[40:41]\n"
"Qk 208,208,209\nQk 209,210,211\nQk 210,212,213\nQk 211,214,215\nv_accvgpr_write_b32 a64, v208\nv_accvgpr_write_b32 a65, v209\nv_accvgpr_write_b32 a66, v210\nv_accvgpr_write_b32 a67, v211\n"
"v_pk_mul_f32 v[216:217], v[216:217], v[40:41]\nv_pk_mul_f32 v[218:219], v[218:219], v[40:41]\nv_pk_mul_f32 v[220:221], v[220:221], v[40:41]\nv_pk_mul_f32 v[222:223], v[222:223], v[40:41]\nQk 216,216,217\nQk 217,218,219\nQk 218,220,221\nQk 219,222,223\n"
"v_accvgpr_write_b32 a68, v216\nv_accvgpr_write_b32 a69, v217\nv_accvgpr_write_b32 a70, v218\nv_accvgpr_write_b32 a71, v219\nv_pk_mul_f32 v[224:225], v[224:225], v[40:41]\nv_pk_mul_f32 v[226:227], v[226:227], v[40:41]\nv_pk_mul_f32 v[228:229], v[228:229], v[40:41]\nv_pk_mul_f32 v[230:231], v[230:231], v[40:41]\n"
"Qk 224,224,225\nQk 225,226,227\nQk 226,228,229\nQk 227,230,231\nv_accvgpr_write_b32 a72, v224\nv_accvgpr_write_b32 a73, v225\nv_accvgpr_write_b32 a74, v226\nv_accvgpr_write_b32 a75, v227\n"
"v_pk_mul_f32 v[232:233], v[232:233], v[40:41]\nv_pk_mul_f32 v[234:235], v[234:235], v[40:41]\nv_pk_mul_f32 v[236:237], v[236:237], v[40:41]\nv_pk_mul_f32 v[238:239], v[238:239], v[40:41]\nQk 232,232,233\nQk 233,234,235\nQk 234,236,237\nQk 235,238,239\n"
"v_accvgpr_write_b32 a76, v232\nv_accvgpr_write_b32 a77, v233\nv_accvgpr_write_b32 a78, v234\nv_accvgpr_write_b32 a79, v235\nv_pk_mul_f32 v[240:241], v[240:241], v[44:45]\nv_pk_mul_f32 v[242:243], v[242:243], v[44:45]\nv_pk_mul_f32 v[244:245], v[244:245], v[44:45]\nv_pk_mul_f32 v[246:247], v[246:247], v[44:45]\n"
"Qk 240,240,241\nQk 241,242,243\nQk 242,244,245\nQk 243,246,247\nv_accvgpr_write_b32 a80, v240\nv_accvgpr_write_b32 a81, v241\nv_accvgpr_write_b32 a82, v242\nv_accvgpr_write_b32 a83, v243\n"
"v_pk_mul_f32 v[248:249], v[248:249], v[44:45]\nv_pk_mul_f32 v[250:251], v[250:251], v[44:45]\nv_pk_mul_f32 v[252:253], v[252:253], v[44:45]\nv_pk_mul_f32 v[254:255], v[254:255], v[44:45]\nQk 248,248,249\nQk 249,250,251\nQk 250,252,253\nQk 251,254,255\n"
"v_accvgpr_write_b32 a84, v248\nv_accvgpr_write_b32 a85, v249\nv_accvgpr_write_b32 a86, v250\nv_accvgpr_write_b32 a87, v251\nv_accvgpr_read_b32 v0, a208\nv_accvgpr_read_b32 v1, a209\nv_accvgpr_read_b32 v2, a210\nv_accvgpr_read_b32 v3, a211\n"
"v_accvgpr_read_b32 v4, a212\nv_accvgpr_read_b32 v5, a213\nv_accvgpr_read_b32 v6, a214\nv_accvgpr_read_b32 v7, a215\nv_pk_mul_f32 v[0:1], v[0:1], v[44:45]\nv_pk_mul_f32 v[2:3], v[2:3], v[44:45]\nv_pk_mul_f32 v[4:5], v[4:5], v[44:45]\nv_pk_mul_f32 v[6:7], v[6:7], v[44:45]\n"
"Qk 0,0,1\nQk 1,2,3\nQk 2,4,5\nQk 3,6,7\nv_accvgpr_write_b32 a88, v0\nv_accvgpr_write_b32 a89, v1\nv_accvgpr_write_b32 a90, v2\nv_accvgpr_write_b32 a91, v3\n"
"v_accvgpr_read_b32 v8, a216\nv_accvgpr_read_b32 v9, a217\nv_accvgpr_read_b32 v10, a218\nv_accvgpr_read_b32 v11, a219\nv_accvgpr_read_b32 v12, a220\nv_accvgpr_read_b32 v13, a221\nv_accvgpr_read_b32 v14, a222\nv_accvgpr_read_b32 v15, a223\n"
"v_pk_mul_f32 v[8:9], v[8:9], v[44:45]\nv_pk_mul_f32 v[10:11], v[10:11], v[44:45]\nv_pk_mul_f32 v[12:13], v[12:13], v[44:45]\nv_pk_mul_f32 v[14:15], v[14:15], v[44:45]\nQk 8,8,9\nQk 9,10,11\nQk 10,12,13\nQk 11,14,15\n"
"v_accvgpr_write_b32 a92, v8\nv_accvgpr_write_b32 a93, v9\nv_accvgpr_write_b32 a94, v10\nv_accvgpr_write_b32 a95, v11\nv_accvgpr_read_b32 v0, a224\nv_accvgpr_read_b32 v1, a225\nv_accvgpr_read_b32 v2, a226\nv_accvgpr_read_b32 v3, a227\n"
"v_accvgpr_read_b32 v4, a228\nv_accvgpr_read_b32 v5, a229\nv_accvgpr_read_b32 v6, a230\nv_accvgpr_read_b32 v7, a231\nv_pk_mul_f32 v[0:1], v[0:1], v[48:49]\nv_pk_mul_f32 v[2:3], v[2:3], v[48:49]\nv_pk_mul_f32 v[4:5], v[4:5], v[48:49]\nv_pk_mul_f32 v[6:7], v[6:7], v[48:49]\n"
"Qk 0,0,1\nQk 1,2,3\nQk 2,4,5\nQk 3,6,7\nv_accvgpr_write_b32 a96, v0\nv_accvgpr_write_b32 a97, v1\nv_accvgpr_write_b32 a98, v2\nv_accvgpr_write_b32 a99, v3\n"
"v_accvgpr_read_b32 v8, a232\nv_accvgpr_read_b32 v9, a233\nv_accvgpr_read_b32 v10, a234\nv_accvgpr_read_b32 v11, a235\nv_accvgpr_read_b32 v12, a236\nv_accvgpr_read_b32 v13, a237\nv_accvgpr_read_b32 v14, a238\nv_accvgpr_read_b32 v15, a239\n"
"v_pk_mul_f32 v[8:9], v[8:9], v[48:49]\nv_pk_mul_f32 v[10:11], v[10:11], v[48:49]\nv_pk_mul_f32 v[12:13], v[12:13], v[48:49]\nv_pk_mul_f32 v[14:15], v[14:15], v[48:49]\nQk 8,8,9\nQk 9,10,11\nQk 10,12,13\nQk 11,14,15\n"
"v_accvgpr_write_b32 a100, v8\nv_accvgpr_write_b32 a101, v9\nv_accvgpr_write_b32 a102, v10\nv_accvgpr_write_b32 a103, v11\nv_accvgpr_read_b32 v0, a240\nv_accvgpr_read_b32 v1, a241\nv_accvgpr_read_b32 v2, a242\nv_accvgpr_read_b32 v3, a243\n"
"v_accvgpr_read_b32 v4, a244\nv_accvgpr_read_b32 v5, a245\nv_accvgpr_read_b32 v6, a246\nv_accvgpr_read_b32 v7, a247\nv_pk_mul_f32 v[0:1], v[0:1], v[48:49]\nv_pk_mul_f32 v[2:3], v[2:3], v[48:49]\nv_pk_mul_f32 v[4:5], v[4:5], v[48:49]\nv_pk_mul_f32 v[6:7], v[6:7], v[48:49]\n"
"Qk 0,0,1\nQk 1,2,3\nQk 2,4,5\nQk 3,6,7\nv_accvgpr_write_b32 a104, v0\nv_accvgpr_write_b32 a105, v1\nv_accvgpr_write_b32 a106, v2\nv_accvgpr_write_b32 a107, v3\n"
"v_accvgpr_read_b32 v8, a248\nv_accvgpr_read_b32 v9, a249\nv_accvgpr_read_b32 v10, a250\nv_accvgpr_read_b32 v11, a251\nv_accvgpr_read_b32 v12, a252\nv_accvgpr_read_b32 v13, a253\nv_accvgpr_read_b32 v14, a254\nv_accvgpr_read_b32 v15, a255\n"
"v_pk_mul_f32 v[8:9], v[8:9], v[48:49]\nv_pk_mul_f32 v[10:11], v[10:11], v[48:49]\nv_pk_mul_f32 v[12:13], v[12:13], v[48:49]\nv_pk_mul_f32 v[14:15], v[14:15], v[48:49]\nQk 8,8,9\nQk 9,10,11\nQk 10,12,13\nQk 11,14,15\n"
"v_accvgpr_write_b32 a108, v8\nv_accvgpr_write_b32 a109, v9\nv_accvgpr_write_b32 a110, v10\nv_accvgpr_write_b32 a111, v11\ns_mov_b32 s58, 1835008\ns_mov_b32 s67, 1\ns_mul_i32 s71, %12, 57344\ns_mul_i32 s68, s92, 7168\n"
"s_add_u32 s71, s71, s68\ns_branch .Lround\n.Lhook:\ns_cmp_eq_u32 s66, 1\ns_cbranch_scc1 .Lh1\ns_cmp_eq_u32 s66, 8\ns_cbranch_scc1 .Lh8\ns_cmp_eq_u32 s66, 9\n"
"s_cbranch_scc1 .Lh9\ns_cmp_eq_u32 s66, 11\ns_cbranch_scc1 .Lh11\ns_cmp_eq_u32 s66, 12\ns_cbranch_scc1 .Lh12\n.Lh1:\ns_cmp_eq_u32 s70, 0\ns_cselect_b32 s68, 128, 0\n"
"v_mov_b32 v66, 0x7ffff000\nv_cmp_gt_u32 vcc, s68, v60\nv_not_b32 v67, v65\ns_lshl_b32 s68, %19, 2\nv_cndmask_b32 v66, v66, v76, vcc\nv_readlane_b32 s30, v67, %19\nbuffer_store_dword v67, v66, s[24:27], s68 offen offset:1024 sc1\ns_branch .Lhookret\n"
".Lh8:\nv_bfe_u32 v66, v60, 4, 3\ns_lshl_b32 s68, %19, 7\nv_lshlrev_b32 v66, 2, v66\ns_add_u32 s68, s68, 2293760\nbuffer_load_dword v78, v66, s[24:27], s68 offen offset:1024 sc1\ns_branch .Lhookret\n.Lh9:\n"
"s_waitcnt vmcnt(32)\ns_mov_b32 s97, 0\n.Lack_l:\nv_cmp_ne_u32 vcc, s30, v78\ns_cmp_eq_u64 vcc, 0\ns_cbranch_scc1 .Lack_ok\ns_sleep 1\nv_bfe_u32 v66, v60, 4, 3\n"
"s_lshl_b32 s68, %19, 7\nv_lshlrev_b32 v66, 2, v66\ns_add_u32 s68, s68, 2293760\nbuffer_load_dword v78, v66, s[24:27], s68 offen offset:1024 sc1\ns_waitcnt vmcnt(0)\ns_add_u32 s97, s97, 1\ns_cmp_lt_u32 s97, 1048576\ns_cbranch_scc1 .Lack_l\n"
".Lack_ok:\nv_add_u32 v66, 1, v65\nbuffer_store_dword v66, v77, s[24:27], 0 offen sc1\ns_branch .Lhookret\n.Lh11:\ns_mov_b32 s52, %15\ns_and_b32 s53, %16, 0xffff\ns_mov_b32 s54, 5246976\n"
"s_mul_i32 s68, s70, 10240\ns_mul_i32 s69, %20, 40960\ns_add_u32 s68, s68, s69\ns_add_u32 s68, s68, 4096\ns_add_u32 m0, %10, 132096\ns_nop 0\nbuffer_load_dwordx4 v60, s[52:55], s68 offen lds\ns_add_u32 s68, s68, 1024\n"
"buffer_load_dwordx4 v60, s[52:55], s68 offen lds\ns_add_u32 s68, s68, 1024\nbuffer_load_dwordx4 v60, s[52:55], s68 offen lds\ns_add_u32 s68, s68, 1024\nbuffer_load_dwordx4 v60, s[52:55], s68 offen lds\ns_add_u32 s68, s68, 1024\nbuffer_load_dwordx4 v60, s[52:55], s68 offen lds\ns_add_u32 s68, s68, 1024\n"
"buffer_load_dwordx4 v60, s[52:55], s68 offen lds\ns_add_u32 s68, s68, 1024\nbuffer_load_dwordx4 v60, s[52:55], s68 offen lds\ns_add_u32 s68, s68, 1024\nbuffer_load_dwordx4 v60, s[52:55], s68 offen lds\ns_add_u32 s68, s68, 1024\nbuffer_load_dwordx4 v60, s[52:55], s68 offen lds\ns_add_u32 s68, s68, 1024\n"
"buffer_load_dwordx4 v60, s[52:55], s68 offen lds\ns_add_u32 s68, s68, 1024\nbuffer_load_dword v78, v76, s[24:27], 0 offen sc1\ns_branch .Lhookret\n.Lh12:\ns_waitcnt vmcnt(32)\nv_sub_u32 v66, v78, v65\nv_cmp_gt_u32 vcc, 1, v66\n"
"s_and_b64 vcc, vcc, s[28:29]\ns_cbranch_scc1 .Lf1_w\n.Lf1_r:\ns_cmp_lg_u32 s70, 3\ns_cbranch_scc1 .Lf1_n\nbuffer_inv sc1\n.Lf1_n:\ns_lshr_b32 s88, %19, 2\n"
"s_mul_i32 s68, s88, 917504\ns_add_u32 s48, %8, s68\ns_addc_u32 s49, %9, 0\ns_mul_i32 s68, s88, 2621440\ns_and_b32 s69, %19, 3\ns_mul_i32 s69, s69, 320\ns_add_u32 s68, s68, s69\ns_sub_u32 s88, 5246976, s68\n"
"s_add_u32 s68, %15, s68\ns_addc_u32 s69, %16, 0\ns_cmp_lt_u32 s70, 2\ns_cselect_b32 s52, s48, s68\ns_cselect_b32 s53, s49, s69\ns_cselect_b32 s54, 917504, s88\ns_cmp_lt_u32 s70, 3\ns_cselect_b32 s48, s48, s68\n"
"s_cselect_b32 s49, s49, s69\ns_cselect_b32 s50, 917504, s88\ns_and_b32 s49, s49, 0xffff\ns_and_b32 s53, s53, 0xffff\ns_add_u32 s71, s71, 458752\ns_mov_b32 s62, s68\ns_and_b32 s63, s69, 0xffff\ns_mov_b32 s86, s88\n"
"s_mul_i32 s68, s70, 6\ns_add_u32 s68, s68, 3\ns_sub_u32 s69, s68, 3\ns_lshl_b32 s69, s69, 10\nv_add_u32 v66, s69, v60\ns_sub_u32 s69, s68, 14\ns_lshl_b32 s69, s69, 2\ns_and_b32 s69, s69, 63\n"
"v_lshrrev_b32 v67, 8, v60\nv_add_u32 v67, s69, v67\nv_mul_u32_u24 v68, 205, v67\nv_lshrrev_b32 v68, 11, v68\nv_mul_u32_u24 v61, 10, v68\nv_sub_u32 v67, v67, v61\nv_lshlrev_b32 v67, 5, v67\nv_bfe_u32 v61, v60, 5, 3\n"
"v_lshl_add_u32 v68, v68, 3, v61\nv_mul_u32_u24 v68, 1280, v68\nv_and_b32 v61, 16, v60\nv_add3_u32 v67, v67, v68, v61\nv_add_u32 v67, 1024, v67\ns_cmp_lt_u32 s68, 14\ns_cselect_b64 vcc, -1, 0\nv_cndmask_b32 v61, v67, v66, vcc\n"
"s_mul_i32 s68, s70, 6\ns_add_u32 s68, s68, 4\ns_sub_u32 s69, s68, 0\ns_lshl_b32 s69, s69, 10\nv_add_u32 v66, s69, v60\ns_sub_u32 s69, s68, 14\ns_lshl_b32 s69, s69, 2\ns_and_b32 s69, s69, 63\n"
"v_lshrrev_b32 v67, 8, v60\nv_add_u32 v67, s69, v67\nv_mul_u32_u24 v68, 205, v67\nv_lshrrev_b32 v68, 11, v68\nv_mul_u32_u24 v62, 10, v68\nv_sub_u32 v67, v67, v62\nv_lshlrev_b32 v67, 5, v67\nv_bfe_u32 v62, v60, 5, 3\n"
"v_lshl_add_u32 v68, v68, 3, v62\nv_mul_u32_u24 v68, 1280, v68\nv_and_b32 v62, 16, v60\nv_add3_u32 v67, v67, v68, v62\nv_add_u32 v67, 4096, v67\ns_cmp_lt_u32 s68, 14\ns_cselect_b64 vcc, -1, 0\nv_cndmask_b32 v62, v67, v66, vcc\n"
"s_mul_i32 s68, s70, 6\ns_add_u32 s68, s68, 5\ns_sub_u32 s69, s68, 1\ns_lshl_b32 s69, s69, 10\nv_add_u32 v66, s69, v60\ns_sub_u32 s69, s68, 14\ns_lshl_b32 s69, s69, 2\ns_and_b32 s69, s69, 63\n"
"v_lshrrev_b32 v67, 8, v60\nv_add_u32 v67, s69, v67\nv_mul_u32_u24 v68, 205, v67\nv_lshrrev_b32 v68, 11, v68\nv_mul_u32_u24 v63, 10, v68\nv_sub_u32 v67, v67, v63\nv_lshlrev_b32 v67, 5, v67\nv_bfe_u32 v63, v60, 5, 3\n"
"v_lshl_add_u32 v68, v68, 3, v63\nv_mul_u32_u24 v68, 1280, v68\nv_and_b32 v63, 16, v60\nv_add3_u32 v67, v67, v68, v63\nv_add_u32 v67, 3072, v67\ns_cmp_lt_u32 s68, 14\ns_cselect_b64 vcc, -1, 0\nv_cndmask_b32 v63, v67, v66, vcc\n"
"s_branch .Lhookret\n.Lf1_w:\ns_mov_b32 s97, 0\n.Lf1_l:\ns_sleep 1\nbuffer_load_dword v78, v76, s[24:27], 0 offen sc1\ns_waitcnt vmcnt(0)\ns_add_u32 s97, s97, 1\n"
"s_cmp_gt_u32 s97, 1048576\ns_cbranch_scc1 .Lf1_r\nv_sub_u32 v66, v78, v65\nv_cmp_gt_u32 vcc, 1, v66\ns_and_b64 vcc, vcc, s[28:29]\ns_cbranch_scc1 .Lf1_l\ns_branch .Lf1_r\n.Lfb:\n"
"s_waitcnt vmcnt(0) lgkmcnt(0)\nv_lshrrev_b32 v68, 4, v60\nv_and_b32 v69, 15, v68\nv_lshrrev_b32 v68, 4, v68\nv_lshlrev_b32 v66, 8, v69\nv_lshl_add_u32 v66, v68, 4, v66\nv_add_u32 v66, 1024, v66\nv_lshlrev_b32 v67, 10, v68\n"
"v_lshl_add_u32 v67, v69, 1, v67\nv_add_u32 v67, 1024, v67\nv_mov_b32 v24, 0xff800000\nv_mov_b32 v28, 0xff800000\nv_mov_b32 v32, 0xff800000\nv_mov_b32 v36, 0xff800000\nv_mov_b32 v40, 0xff800000\nv_mov_b32 v44, 0xff800000\n"
"v_mov_b32 v48, 0xff800000\ns_lshl_b32 s89, s67, 21\ns_mov_b32 s66, 0\n.Lfb1:\ns_add_u32 s68, s89, 4096\nbuffer_load_dwordx4 a[112:115], v66, s[40:43], s89 offen offset:0\nbuffer_load_dwordx4 a[116:119], v66, s[40:43], s89 offen offset:64\nbuffer_load_dwordx4 a[120:123], v66, s[40:43], s89 offen offset:128\n"
"buffer_load_dwordx4 a[124:127], v66, s[40:43], s89 offen offset:192\nbuffer_load_dwordx4 a[128:131], v66, s[40:43], s68 offen offset:0\nbuffer_load_dwordx4 a[132:135], v66, s[40:43], s68 offen offset:64\nbuffer_load_dwordx4 a[136:139], v66, s[40:43], s68 offen offset:128\nbuffer_load_dwordx4 a[140:143], v66, s[40:43], s68 offen offset:192\ns_waitcnt vmcnt(0)\nQzvaa 0,112,0\nQmvaa 0,116,4\n"
"Qmvaa 0,120,8\nQmvaa 0,124,12\nQzvaa 4,128,0\nQmvaa 4,132,4\nQmvaa 4,136,8\nQmvaa 4,140,12\ns_nop 7\nv_max3_f32 v24, v24, v0, v1\n"
"v_max3_f32 v24, v24, v2, v3\nv_max3_f32 v24, v24, v4, v5\nv_max3_f32 v24, v24, v6, v7\nQzvaa 0,112,16\nQmvaa 0,116,20\nQmvaa 0,120,24\nQmvaa 0,124,28\nQzvaa 4,128,16\n"
"Qmvaa 4,132,20\nQmvaa 4,136,24\nQmvaa 4,140,28\ns_nop 7\nv_max3_f32 v28, v28, v0, v1\nv_max3_f32 v28, v28, v2, v3\nv_max3_f32 v28, v28, v4, v5\nv_max3_f32 v28, v28, v6, v7\n"
"Qzvaa 0,112,32\nQmvaa 0,116,36\nQmvaa 0,120,40\nQmvaa 0,124,44\nQzvaa 4,128,32\nQmvaa 4,132,36\nQmvaa 4,136,40\nQmvaa 4,140,44\n"
"s_nop 7\nv_max3_f32 v32, v32, v0, v1\nv_max3_f32 v32, v32, v2, v3\nv_max3_f32 v32, v32, v4, v5\nv_max3_f32 v32, v32, v6, v7\nQzvaa 0,112,48\nQmvaa 0,116,52\nQmvaa 0,120,56\n"
"Qmvaa 0,124,60\nQzvaa 4,128,48\nQmvaa 4,132,52\nQmvaa 4,136,56\nQmvaa 4,140,60\ns_nop 7\nv_max3_f32 v36, v36, v0, v1\nv_max3_f32 v36, v36, v2, v3\n"
"v_max3_f32 v36, v36, v4, v5\nv_max3_f32 v36, v36, v6, v7\nQzvaa 0,112,64\nQmvaa 0,116,68\nQmvaa 0,120,72\nQmvaa 0,124,76\nQzvaa 4,128,64\nQmvaa 4,132,68\n"
"Qmvaa 4,136,72\nQmvaa 4,140,76\ns_nop 7\nv_max3_f32 v40, v40, v0, v1\nv_max3_f32 v40, v40, v2, v3\nv_max3_f32 v40, v40, v4, v5\nv_max3_f32 v40, v40, v6, v7\nQzvaa 0,112,80\n"
"Qmvaa 0,116,84\nQmvaa 0,120,88\nQmvaa 0,124,92\nQzvaa 4,128,80\nQmvaa 4,132,84\nQmvaa 4,136,88\nQmvaa 4,140,92\ns_nop 7\n"
"v_max3_f32 v44, v44, v0, v1\nv_max3_f32 v44, v44, v2, v3\nv_max3_f32 v44, v44, v4, v5\nv_max3_f32 v44, v44, v6, v7\nQzvaa 0,112,96\nQmvaa 0,116,100\nQmvaa 0,120,104\nQmvaa 0,124,108\n"
"Qzvaa 4,128,96\nQmvaa 4,132,100\nQmvaa 4,136,104\nQmvaa 4,140,108\ns_nop 7\nv_max3_f32 v48, v48, v0, v1\nv_max3_f32 v48, v48, v2, v3\nv_max3_f32 v48, v48, v4, v5\n"
"v_max3_f32 v48, v48, v6, v7\ns_add_u32 s89, s89, 8192\ns_add_u32 s66, s66, 1\ns_cmp_lt_u32 s66, 32\ns_cbranch_scc1 .Lfb1\nv_mov_b32 v25, v24\ns_nop 1\nv_permlane32_swap_b32 v24, v25\n"
"v_max_f32 v24, v24, v25\nv_mov_b32 v25, v24\ns_nop 1\nv_permlane16_swap_b32 v24, v25\nv_max_f32 v24, v24, v25\nv_sub_f32 v24, 0, v24\nv_mov_b32 v25, v24\nv_mov_b32 v26, v24\n"
"v_mov_b32 v27, v24\nv_mov_b32 v29, v28\ns_nop 1\nv_permlane32_swap_b32 v28, v29\nv_max_f32 v28, v28, v29\nv_mov_b32 v29, v28\ns_nop 1\nv_permlane16_swap_b32 v28, v29\n"
"v_max_f32 v28, v28, v29\nv_sub_f32 v28, 0, v28\nv_mov_b32 v29, v28\nv_mov_b32 v30, v28\nv_mov_b32 v31, v28\nv_mov_b32 v33, v32\ns_nop 1\nv_permlane32_swap_b32 v32, v33\n"
"v_max_f32 v32, v32, v33\nv_mov_b32 v33, v32\ns_nop 1\nv_permlane16_swap_b32 v32, v33\nv_max_f32 v32, v32, v33\nv_sub_f32 v32, 0, v32\nv_mov_b32 v33, v32\nv_mov_b32 v34, v32\n"
"v_mov_b32 v35, v32\nv_mov_b32 v37, v36\ns_nop 1\nv_permlane32_swap_b32 v36, v37\nv_max_f32 v36, v36, v37\nv_mov_b32 v37, v36\ns_nop 1\nv_permlane16_swap_b32 v36, v37\n"
"v_max_f32 v36, v36, v37\nv_sub_f32 v36, 0, v36\nv_mov_b32 v37, v36\nv_mov_b32 v38, v36\nv_mov_b32 v39, v36\nv_mov_b32 v41, v40\ns_nop 1\nv_permlane32_swap_b32 v40, v41\n"
"v_max_f32 v40, v40, v41\nv_mov_b32 v41, v40\ns_nop 1\nv_permlane16_swap_b32 v40, v41\nv_max_f32 v40, v40, v41\nv_sub_f32 v40, 0, v40\nv_mov_b32 v41, v40\nv_mov_b32 v42, v40\n"
"v_mov_b32 v43, v40\nv_mov_b32 v45, v44\ns_nop 1\nv_permlane32_swap_b32 v44, v45\nv_max_f32 v44, v44, v45\nv_mov_b32 v45, v44\ns_nop 1\nv_permlane16_swap_b32 v44, v45\n"
"v_max_f32 v44, v44, v45\nv_sub_f32 v44, 0, v44\nv_mov_b32 v45, v44\nv_mov_b32 v46, v44\nv_mov_b32 v47, v44\nv_mov_b32 v49, v48\ns_nop 1\nv_permlane32_swap_b32 v48, v49\n"
"v_max_f32 v48, v48, v49\nv_mov_b32 v49, v48\ns_nop 1\nv_permlane16_swap_b32 v48, v49\nv_max_f32 v48, v48, v49\nv_sub_f32 v48, 0, v48\nv_mov_b32 v49, v48\nv_mov_b32 v50, v48\n"
"v_mov_b32 v51, v48\nv_mov_b32 v68, 0\nv_mov_b32 v80, 0\nv_mov_b32 v81, 0\nv_mov_b32 v82, 0\nv_mov_b32 v83, 0\nv_mov_b32 v84, 0\nv_mov_b32 v85, 0\n"
"v_mov_b32 v86, 0\nv_mov_b32 v87, 0\nv_mov_b32 v88, 0\nv_mov_b32 v89, 0\nv_mov_b32 v90, 0\nv_mov_b32 v91, 0\nv_mov_b32 v92, 0\nv_mov_b32 v93, 0\n"
"v_mov_b32 v94, 0\nv_mov_b32 v95, 0\nv_mov_b32 v96, 0\nv_mov_b32 v97, 0\nv_mov_b32 v98, 0\nv_mov_b32 v99, 0\nv_mov_b32 v100, 0\nv_mov_b32 v101, 0\n"
"v_mov_b32 v102, 0\nv_mov_b32 v103, 0\nv_mov_b32 v104, 0\nv_mov_b32 v105, 0\nv_mov_b32 v106, 0\nv_mov_b32 v107, 0\nv_mov_b32 v108, 0\nv_mov_b32 v109, 0\n"
"v_mov_b32 v110, 0\nv_mov_b32 v111, 0\nv_accvgpr_write_b32 a176, v68\nv_accvgpr_write_b32 a177, v68\nv_accvgpr_write_b32 a178, v68\nv_accvgpr_write_b32 a179, v68\nv_mov_b32 v112, 0\nv_mov_b32 v113, 0\n"
"v_mov_b32 v114, 0\nv_mov_b32 v115, 0\nv_mov_b32 v116, 0\nv_mov_b32 v117, 0\nv_mov_b32 v118, 0\nv_mov_b32 v119, 0\nv_mov_b32 v120, 0\nv_mov_b32 v121, 0\n"
"v_mov_b32 v122, 0\nv_mov_b32 v123, 0\nv_mov_b32 v124, 0\nv_mov_b32 v125, 0\nv_mov_b32 v126, 0\nv_mov_b32 v127, 0\nv_mov_b32 v128, 0\nv_mov_b32 v129, 0\n"
"v_mov_b32 v130, 0\nv_mov_b32 v131, 0\nv_mov_b32 v132, 0\nv_mov_b32 v133, 0\nv_mov_b32 v134, 0\nv_mov_b32 v135, 0\nv_mov_b32 v136, 0\nv_mov_b32 v137, 0\n"
"v_mov_b32 v138, 0\nv_mov_b32 v139, 0\nv_mov_b32 v140, 0\nv_mov_b32 v141, 0\nv_mov_b32 v142, 0\nv_mov_b32 v143, 0\nv_accvgpr_write_b32 a180, v68\nv_accvgpr_write_b32 a181, v68\n"
"v_accvgpr_write_b32 a182, v68\nv_accvgpr_write_b32 a183, v68\nv_mov_b32 v144, 0\nv_mov_b32 v145, 0\nv_mov_b32 v146, 0\nv_mov_b32 v147, 0\nv_mov_b32 v148, 0\nv_mov_b32 v149, 0\n"
"v_mov_b32 v150, 0\nv_mov_b32 v151, 0\nv_mov_b32 v152, 0\nv_mov_b32 v153, 0\nv_mov_b32 v154, 0\nv_mov_b32 v155, 0\nv_mov_b32 v156, 0\nv_mov_b32 v157, 0\n"
"v_mov_b32 v158, 0\nv_mov_b32 v159, 0\nv_mov_b32 v160, 0\nv_mov_b32 v161, 0\nv_mov_b32 v162, 0\nv_mov_b32 v163, 0\nv_mov_b32 v164, 0\nv_mov_b32 v165, 0\n"
"v_mov_b32 v166, 0\nv_mov_b32 v167, 0\nv_mov_b32 v168, 0\nv_mov_b32 v169, 0\nv_mov_b32 v170, 0\nv_mov_b32 v171, 0\nv_mov_b32 v172, 0\nv_mov_b32 v173, 0\n"
"v_mov_b32 v174, 0\nv_mov_b32 v175, 0\nv_accvgpr_write_b32 a184, v68\nv_accvgpr_write_b32 a185, v68\nv_accvgpr_write_b32 a186, v68\nv_accvgpr_write_b32 a187, v68\nv_mov_b32 v176, 0\nv_mov_b32 v177, 0\n"
"v_mov_b32 v178, 0\nv_mov_b32 v179, 0\nv_mov_b32 v180, 0\nv_mov_b32 v181, 0\nv_mov_b32 v182, 0\nv_mov_b32 v183, 0\nv_mov_b32 v184, 0\nv_mov_b32 v185, 0\n"
"v_mov_b32 v186, 0\nv_mov_b32 v187, 0\nv_mov_b32 v188, 0\nv_mov_b32 v189, 0\nv_mov_b32 v190, 0\nv_mov_b32 v191, 0\nv_mov_b32 v192, 0\nv_mov_b32 v193, 0\n"
"v_mov_b32 v194, 0\nv_mov_b32 v195, 0\nv_mov_b32 v196, 0\nv_mov_b32 v197, 0\nv_mov_b32 v198, 0\nv_mov_b32 v199, 0\nv_mov_b32 v200, 0\nv_mov_b32 v201, 0\n"
"v_mov_b32 v202, 0\nv_mov_b32 v203, 0\nv_mov_b32 v204, 0\nv_mov_b32 v205, 0\nv_mov_b32 v206, 0\nv_mov_b32 v207, 0\nv_accvgpr_write_b32 a188, v68\nv_accvgpr_write_b32 a189, v68\n"
"v_accvgpr_write_b32 a190, v68\nv_accvgpr_write_b32 a191, v68\nv_mov_b32 v208, 0\nv_mov_b32 v209, 0\nv_mov_b32 v210, 0\nv_mov_b32 v211, 0\nv_mov_b32 v212, 0\nv_mov_b32 v213, 0\n"
"v_mov_b32 v214, 0\nv_mov_b32 v215, 0\nv_mov_b32 v216, 0\nv_mov_b32 v217, 0\nv_mov_b32 v218, 0\nv_mov_b32 v219, 0\nv_mov_b32 v220, 0\nv_mov_b32 v221, 0\n"
"v_mov_b32 v222, 0\nv_mov_b32 v223, 0\nv_mov_b32 v224, 0\nv_mov_b32 v225, 0\nv_mov_b32 v226, 0\nv_mov_b32 v227, 0\nv_mov_b32 v228, 0\nv_mov_b32 v229, 0\n"
"v_mov_b32 v230, 0\nv_mov_b32 v231, 0\nv_mov_b32 v232, 0\nv_mov_b32 v233, 0\nv_mov_b32 v234, 0\nv_mov_b32 v235, 0\nv_mov_b32 v236, 0\nv_mov_b32 v237, 0\n"
"v_mov_b32 v238, 0\nv_mov_b32 v239, 0\nv_accvgpr_write_b32 a192, v68\nv_accvgpr_write_b32 a193, v68\nv_accvgpr_write_b32 a194, v68\nv_accvgpr_write_b32 a195, v68\nv_mov_b32 v240, 0\nv_mov_b32 v241, 0\n"
"v_mov_b32 v242, 0\nv_mov_b32 v243, 0\nv_mov_b32 v244, 0\nv_mov_b32 v245, 0\nv_mov_b32 v246, 0\nv_mov_b32 v247, 0\nv_mov_b32 v248, 0\nv_mov_b32 v249, 0\n"
"v_mov_b32 v250, 0\nv_mov_b32 v251, 0\nv_mov_b32 v252, 0\nv_mov_b32 v253, 0\nv_mov_b32 v254, 0\nv_mov_b32 v255, 0\nv_accvgpr_write_b32 a208, v68\nv_accvgpr_write_b32 a209, v68\n"
"v_accvgpr_write_b32 a210, v68\nv_accvgpr_write_b32 a211, v68\nv_accvgpr_write_b32 a212, v68\nv_accvgpr_write_b32 a213, v68\nv_accvgpr_write_b32 a214, v68\nv_accvgpr_write_b32 a215, v68\nv_accvgpr_write_b32 a216, v68\nv_accvgpr_write_b32 a217, v68\n"
"v_accvgpr_write_b32 a218, v68\nv_accvgpr_write_b32 a219, v68\nv_accvgpr_write_b32 a220, v68\nv_accvgpr_write_b32 a221, v68\nv_accvgpr_write_b32 a222, v68\nv_accvgpr_write_b32 a223, v68\nv_accvgpr_write_b32 a196, v68\nv_accvgpr_write_b32 a197, v68\n"
"v_accvgpr_write_b32 a198, v68\nv_accvgpr_write_b32 a199, v68\nv_accvgpr_write_b32 a224, v68\nv_accvgpr_write_b32 a225, v68\nv_accvgpr_write_b32 a226, v68\nv_accvgpr_write_b32 a227, v68\nv_accvgpr_write_b32 a228, v68\nv_accvgpr_write_b32 a229, v68\n"
"v_accvgpr_write_b32 a230, v68\nv_accvgpr_write_b32 a231, v68\nv_accvgpr_write_b32 a232, v68\nv_accvgpr_write_b32 a233, v68\nv_accvgpr_write_b32 a234, v68\nv_accvgpr_write_b32 a235, v68\nv_accvgpr_write_b32 a236, v68\nv_accvgpr_write_b32 a237, v68\n"
"v_accvgpr_write_b32 a238, v68\nv_accvgpr_write_b32 a239, v68\nv_accvgpr_write_b32 a240, v68\nv_accvgpr_write_b32 a241, v68\nv_accvgpr_write_b32 a242, v68\nv_accvgpr_write_b32 a243, v68\nv_accvgpr_write_b32 a244, v68\nv_accvgpr_write_b32 a245, v68\n"
"v_accvgpr_write_b32 a246, v68\nv_accvgpr_write_b32 a247, v68\nv_accvgpr_write_b32 a248, v68\nv_accvgpr_write_b32 a249, v68\nv_accvgpr_write_b32 a250, v68\nv_accvgpr_write_b32 a251, v68\nv_accvgpr_write_b32 a252, v68\nv_accvgpr_write_b32 a253, v68\n"
"v_accvgpr_write_b32 a254, v68\nv_accvgpr_write_b32 a255, v68\nv_accvgpr_write_b32 a200, v68\nv_accvgpr_write_b32 a201, v68\nv_accvgpr_write_b32 a202, v68\nv_accvgpr_write_b32 a203, v68\ns_lshl_b32 s89, s67, 21\ns_mov_b32 s66, 0\n"
".Lfb2:\ns_add_u32 s68, s89, 4096\nbuffer_load_dwordx4 a[112:115], v66, s[40:43], s89 offen offset:0\nbuffer_load_dwordx4 a[116:119], v66, s[40:43], s89 offen offset:64\nbuffer_load_dwordx4 a[120:123], v66, s[40:43], s89 offen offset:128\nbuffer_load_dwordx4 a[124:127], v66, s[40:43], s89 offen offset:192\nbuffer_load_dwordx4 a[128:131], v66, s[40:43], s68 offen offset:0\nbuffer_load_dwordx4 a[132:135], v66, s[40:43], s68 offen offset:64\n"
"buffer_load_dwordx4 a[136:139], v66, s[40:43], s68 offen offset:128\nbuffer_load_dwordx4 a[140:143], v66, s[40:43], s68 offen offset:192\nbuffer_load_ushort v8, v67, s[44:47], s89 offen offset:0\nbuffer_load_ushort v9, v67, s[44:47], s89 offen offset:256\nbuffer_load_ushort v10, v67, s[44:47], s89 offen offset:512\nbuffer_load_ushort v11, v67, s[44:47], s89 offen offset:768\nbuffer_load_ushort v12, v67, s[44:47], s68 offen offset:0\nbuffer_load_ushort v13, v67, s[44:47], s68 offen offset:256\n"
"buffer_load_ushort v14, v67, s[44:47], s68 offen offset:512\nbuffer_load_ushort v15, v67, s[44:47], s68 offen offset:768\ns_waitcnt vmcnt(0)\nv_lshl_or_b32 v8, v9, 16, v8\nv_accvgpr_write_b32 a144, v8\nv_lshl_or_b32 v10, v11, 16, v10\nv_accvgpr_write_b32 a145, v10\nv_lshl_or_b32 v12, v13, 16, v12\n"
"v_accvgpr_write_b32 a146, v12\nv_lshl_or_b32 v14, v15, 16, v14\nv_accvgpr_write_b32 a147, v14\nbuffer_load_ushort v8, v67, s[44:47], s89 offen offset:32\nbuffer_load_ushort v9, v67, s[44:47], s89 offen offset:288\nbuffer_load_ushort v10, v67, s[44:47], s89 offen offset:544\nbuffer_load_ushort v11, v67, s[44:47], s89 offen offset:800\nbuffer_load_ushort v12, v67, s[44:47], s68 offen offset:32\n"
"buffer_load_ushort v13, v67, s[44:47], s68 offen offset:288\nbuffer_load_ushort v14, v67, s[44:47], s68 offen offset:544\nbuffer_load_ushort v15, v67, s[44:47], s68 offen offset:800\ns_waitcnt vmcnt(0)\nv_lshl_or_b32 v8, v9, 16, v8\nv_accvgpr_write_b32 a148, v8\nv_lshl_or_b32 v10, v11, 16, v10\nv_accvgpr_write_b32 a149, v10\n"
"v_lshl_or_b32 v12, v13, 16, v12\nv_accvgpr_write_b32 a150, v12\nv_lshl_or_b32 v14, v15, 16, v14\nv_accvgpr_write_b32 a151, v14\nbuffer_load_ushort v8, v67, s[44:47], s89 offen offset:64\nbuffer_load_ushort v9, v67, s[44:47], s89 offen offset:320\nbuffer_load_ushort v10, v67, s[44:47], s89 offen offset:576\nbuffer_load_ushort v11, v67, s[44:47], s89 offen offset:832\n"
"buffer_load_ushort v12, v67, s[44:47], s68 offen offset:64\nbuffer_load_ushort v13, v67, s[44:47], s68 offen offset:320\nbuffer_load_ushort v14, v67, s[44:47], s68 offen offset:576\nbuffer_load_ushort v15, v67, s[44:47], s68 offen offset:832\ns_waitcnt vmcnt(0)\nv_lshl_or_b32 v8, v9, 16, v8\nv_accvgpr_write_b32 a152, v8\nv_lshl_or_b32 v10, v11, 16, v10\n"
"v_accvgpr_write_b32 a153, v10\nv_lshl_or_b32 v12, v13, 16, v12\nv_accvgpr_write_b32 a154, v12\nv_lshl_or_b32 v14, v15, 16, v14\nv_accvgpr_write_b32 a155, v14\nbuffer_load_ushort v8, v67, s[44:47], s89 offen offset:96\nbuffer_load_ushort v9, v67, s[44:47], s89 offen offset:352\nbuffer_load_ushort v10, v67, s[44:47], s89 offen offset:608\n"
"buffer_load_ushort v11, v67, s[44:47], s89 offen offset:864\nbuffer_load_ushort v12, v67, s[44:47], s68 offen offset:96\nbuffer_load_ushort v13, v67, s[44:47], s68 offen offset:352\nbuffer_load_ushort v14, v67, s[44:47], s68 offen offset:608\nbuffer_load_ushort v15, v67, s[44:47], s68 offen offset:864\ns_waitcnt vmcnt(0)\nv_lshl_or_b32 v8, v9, 16, v8\nv_accvgpr_write_b32 a156, v8\n"
"v_lshl_or_b32 v10, v11, 16, v10\nv_accvgpr_write_b32 a157, v10\nv_lshl_or_b32 v12, v13, 16, v12\nv_accvgpr_write_b32 a158, v12\nv_lshl_or_b32 v14, v15, 16, v14\nv_accvgpr_write_b32 a159, v14\nbuffer_load_ushort v8, v67, s[44:47], s89 offen offset:128\nbuffer_load_ushort v9, v67, s[44:47], s89 offen offset:384\n"
"buffer_load_ushort v10, v67, s[44:47], s89 offen offset:640\nbuffer_load_ushort v11, v67, s[44:47], s89 offen offset:896\nbuffer_load_ushort v12, v67, s[44:47], s68 offen offset:128\nbuffer_load_ushort v13, v67, s[44:47], s68 offen offset:384\nbuffer_load_ushort v14, v67, s[44:47], s68 offen offset:640\nbuffer_load_ushort v15, v67, s[44:47], s68 offen offset:896\ns_waitcnt vmcnt(0)\nv_lshl_or_b32 v8, v9, 16, v8\n"
"v_accvgpr_write_b32 a160, v8\nv_lshl_or_b32 v10, v11, 16, v10\nv_accvgpr_write_b32 a161, v10\nv_lshl_or_b32 v12, v13, 16, v12\nv_accvgpr_write_b32 a162, v12\nv_lshl_or_b32 v14, v15, 16, v14\nv_accvgpr_write_b32 a163, v14\nbuffer_load_ushort v8, v67, s[44:47], s89 offen offset:160\n"
"buffer_load_ushort v9, v67, s[44:47], s89 offen offset:416\nbuffer_load_ushort v10, v67, s[44:47], s89 offen offset:672\nbuffer_load_ushort v11, v67, s[44:47], s89 offen offset:928\nbuffer_load_ushort v12, v67, s[44:47], s68 offen offset:160\nbuffer_load_ushort v13, v67, s[44:47], s68 offen offset:416\nbuffer_load_ushort v14, v67, s[44:47], s68 offen offset:672\nbuffer_load_ushort v15, v67, s[44:47], s68 offen offset:928\ns_waitcnt vmcnt(0)\n"
"v_lshl_or_b32 v8, v9, 16, v8\nv_accvgpr_write_b32 a164, v8\nv_lshl_or_b32 v10, v11, 16, v10\nv_accvgpr_write_b32 a165, v10\nv_lshl_or_b32 v12, v13, 16, v12\nv_accvgpr_write_b32 a166, v12\nv_lshl_or_b32 v14, v15, 16, v14\nv_accvgpr_write_b32 a167, v14\n"
"buffer_load_ushort v8, v67, s[44:47], s89 offen offset:192\nbuffer_load_ushort v9, v67, s[44:47], s89 offen offset:448\nbuffer_load_ushort v10, v67, s[44:47], s89 offen offset:704\nbuffer_load_ushort v11, v67, s[44:47], s89 offen offset:960\nbuffer_load_ushort v12, v67, s[44:47], s68 offen offset:192\nbuffer_load_ushort v13, v67, s[44:47], s68 offen offset:448\nbuffer_load_ushort v14, v67, s[44:47], s68 offen offset:704\nbuffer_load_ushort v15, v67, s[44:47], s68 offen offset:960\n"
"s_waitcnt vmcnt(0)\nv_lshl_or_b32 v8, v9, 16, v8\nv_accvgpr_write_b32 a168, v8\nv_lshl_or_b32 v10, v11, 16, v10\nv_accvgpr_write_b32 a169, v10\nv_lshl_or_b32 v12, v13, 16, v12\nv_accvgpr_write_b32 a170, v12\nv_lshl_or_b32 v14, v15, 16, v14\n"
"v_accvgpr_write_b32 a171, v14\nbuffer_load_ushort v8, v67, s[44:47], s89 offen offset:224\nbuffer_load_ushort v9, v67, s[44:47], s89 offen offset:480\nbuffer_load_ushort v10, v67, s[44:47], s89 offen offset:736\nbuffer_load_ushort v11, v67, s[44:47], s89 offen offset:992\nbuffer_load_ushort v12, v67, s[44:47], s68 offen offset:224\nbuffer_load_ushort v13, v67, s[44:47], s68 offen offset:480\nbuffer_load_ushort v14, v67, s[44:47], s68 offen offset:736\n"
"buffer_load_ushort v15, v67, s[44:47], s68 offen offset:992\ns_waitcnt vmcnt(0)\nv_lshl_or_b32 v8, v9, 16, v8\nv_accvgpr_write_b32 a172, v8\nv_lshl_or_b32 v10, v11, 16, v10\nv_accvgpr_write_b32 a173, v10\nv_lshl_or_b32 v12, v13, 16, v12\nv_accvgpr_write_b32 a174, v12\n"
"v_lshl_or_b32 v14, v15, 16, v14\nv_accvgpr_write_b32 a175, v14\nQcvaa 0,112,0,24\nQmvaa 0,116,4\nQmvaa 0,120,8\nQmvaa 0,124,12\nQcvaa 4,128,0,24\nQmvaa 4,132,4\n"
"Qmvaa 4,136,8\nQmvaa 4,140,12\ns_nop 7\nQe 0\nQe 1\nQe 2\nQe 3\nQe 4\n"
"Qe 5\nQe 6\nQe 7\ns_nop 0\nQk 16,0,1\nQk 17,2,3\nQk 18,4,5\nQk 19,6,7\n"
"s_nop 1\nQmvav 80,144,16\nQmvav 84,148,16\nQmvav 88,152,16\nQmvav 92,156,16\nQmvav 96,160,16\nQmvav 100,164,16\nQmvav 104,168,16\n"
"Qmvav 108,172,16\nQmaav 176,204,16\nQcvaa 0,112,16,28\nQmvaa 0,116,20\nQmvaa 0,120,24\nQmvaa 0,124,28\nQcvaa 4,128,16,28\nQmvaa 4,132,20\n"
"Qmvaa 4,136,24\nQmvaa 4,140,28\ns_nop 7\nQe 0\nQe 1\nQe 2\nQe 3\nQe 4\n"
"Qe 5\nQe 6\nQe 7\ns_nop 0\nQk 16,0,1\nQk 17,2,3\nQk 18,4,5\nQk 19,6,7\n"
"s_nop 1\nQmvav 112,144,16\nQmvav 116,148,16\nQmvav 120,152,16\nQmvav 124,156,16\nQmvav 128,160,16\nQmvav 132,164,16\nQmvav 136,168,16\n"
"Qmvav 140,172,16\nQmaav 180,204,16\nQcvaa 0,112,32,32\nQmvaa 0,116,36\nQmvaa 0,120,40\nQmvaa 0,124,44\nQcvaa 4,128,32,32\nQmvaa 4,132,36\n"
"Qmvaa 4,136,40\nQmvaa 4,140,44\ns_nop 7\nQe 0\nQe 1\nQe 2\nQe 3\nQe 4\n"
"Qe 5\nQe 6\nQe 7\ns_nop 0\nQk 16,0,1\nQk 17,2,3\nQk 18,4,5\nQk 19,6,7\n"
"s_nop 1\nQmvav 144,144,16\nQmvav 148,148,16\nQmvav 152,152,16\nQmvav 156,156,16\nQmvav 160,160,16\nQmvav 164,164,16\nQmvav 168,168,16\n"
"Qmvav 172,172,16\nQmaav 184,204,16\nQcvaa 0,112,48,36\nQmvaa 0,116,52\nQmvaa 0,120,56\nQmvaa 0,124,60\nQcvaa 4,128,48,36\nQmvaa 4,132,52\n"
"Qmvaa 4,136,56\nQmvaa 4,140,60\ns_nop 7\nQe 0\nQe 1\nQe 2\nQe 3\nQe 4\n"
"Qe 5\nQe 6\nQe 7\ns_nop 0\nQk 16,0,1\nQk 17,2,3\nQk 18,4,5\nQk 19,6,7\n"
"s_nop 1\nQmvav 176,144,16\nQmvav 180,148,16\nQmvav 184,152,16\nQmvav 188,156,16\nQmvav 192,160,16\nQmvav 196,164,16\nQmvav 200,168,16\n"
"Qmvav 204,172,16\nQmaav 188,204,16\nQcvaa 0,112,64,40\nQmvaa 0,116,68\nQmvaa 0,120,72\nQmvaa 0,124,76\nQcvaa 4,128,64,40\nQmvaa 4,132,68\n"
"Qmvaa 4,136,72\nQmvaa 4,140,76\ns_nop 7\nQe 0\nQe 1\nQe 2\nQe 3\nQe 4\n"
"Qe 5\nQe 6\nQe 7\ns_nop 0\nQk 16,0,1\nQk 17,2,3\nQk 18,4,5\nQk 19,6,7\n"
"s_nop 1\nQmvav 208,144,16\nQmvav 212,148,16\nQmvav 216,152,16\nQmvav 220,156,16\nQmvav 224,160,16\nQmvav 228,164,16\nQmvav 232,168,16\n"
"Qmvav 236,172,16\nQmaav 192,204,16\nQcvaa 0,112,80,44\nQmvaa 0,116,84\nQmvaa 0,120,88\nQmvaa 0,124,92\nQcvaa 4,128,80,44\nQmvaa 4,132,84\n"
"Qmvaa 4,136,88\nQmvaa 4,140,92\ns_nop 7\nQe 0\nQe 1\nQe 2\nQe 3\nQe 4\n"
"Qe 5\nQe 6\nQe 7\ns_nop 0\nQk 16,0,1\nQk 17,2,3\nQk 18,4,5\nQk 19,6,7\n"
"s_nop 1\nQmvav 240,144,16\nQmvav 244,148,16\nQmvav 248,152,16\nQmvav 252,156,16\nQmaav 208,160,16\nQmaav 212,164,16\nQmaav 216,168,16\n"
"Qmaav 220,172,16\nQmaav 196,204,16\nQcvaa 0,112,96,48\nQmvaa 0,116,100\nQmvaa 0,120,104\nQmvaa 0,124,108\nQcvaa 4,128,96,48\nQmvaa 4,132,100\n"
"Qmvaa 4,136,104\nQmvaa 4,140,108\ns_nop 7\nQe 0\nQe 1\nQe 2\nQe 3\nQe 4\n"
"Qe 5\nQe 6\nQe 7\ns_nop 0\nQk 16,0,1\nQk 17,2,3\nQk 18,4,5\nQk 19,6,7\n"
"s_nop 1\nQmaav 224,144,16\nQmaav 228,148,16\nQmaav 232,152,16\nQmaav 236,156,16\nQmaav 240,160,16\nQmaav 244,164,16\nQmaav 248,168,16\n"
"Qmaav 252,172,16\nQmaav 200,204,16\ns_add_u32 s89, s89, 8192\ns_add_u32 s66, s66, 1\ns_cmp_lt_u32 s66, 32\ns_cbranch_scc1 .Lfb2\ns_nop 7\nv_accvgpr_read_b32 v24, a176\n"
"v_accvgpr_read_b32 v28, a180\nv_accvgpr_read_b32 v32, a184\nv_accvgpr_read_b32 v36, a188\nv_accvgpr_read_b32 v40, a192\nv_accvgpr_read_b32 v44, a196\nv_accvgpr_read_b32 v48, a200\ns_cmp_eq_u32 s67, 0\ns_cbranch_scc1 .Lfbret0\n"
"s_branch .Lfbret1\n.Lf2_w:\ns_mov_b32 s97, 0\n.Lf2_l:\ns_sleep 1\nbuffer_load_dword v78, v76, s[24:27], 0 offen sc1\ns_waitcnt vmcnt(0)\ns_add_u32 s97, s97, 1\n"
"s_cmp_gt_u32 s97, 1048576\ns_cbranch_scc1 .Lf2_r\nv_sub_u32 v66, v78, v65\nv_cmp_gt_u32 vcc, 2, v66\ns_and_b64 vcc, vcc, s[28:29]\ns_cbranch_scc1 .Lf2_l\ns_branch .Lf2_r\n.Lf3_w:\n"
"s_mov_b32 s97, 0\n.Lf3_l:\ns_sleep 1\nbuffer_load_dword v78, v76, s[24:27], 0 offen sc1\ns_waitcnt vmcnt(0)\ns_add_u32 s97, s97, 1\ns_cmp_gt_u32 s97, 1048576\ns_cbranch_scc1 .Lf3_r\n"
"v_sub_u32 v66, v78, v65\nv_cmp_gt_u32 vcc, 3, v66\ns_and_b64 vcc, vcc, s[28:29]\ns_cbranch_scc1 .Lf3_l\ns_branch .Lf3_r\n.Lf4_w:\ns_mov_b32 s97, 0\n.Lf4_l:\n"
"s_sleep 1\nbuffer_load_dword v78, v76, s[24:27], 0 offen sc1\ns_waitcnt vmcnt(0)\ns_add_u32 s97, s97, 1\ns_cmp_gt_u32 s97, 1048576\ns_cbranch_scc1 .Lf4_r\nv_sub_u32 v66, v78, v65\nv_cmp_gt_u32 vcc, 4, v66\n"
"s_and_b64 vcc, vcc, s[28:29]\ns_cbranch_scc1 .Lf4_l\ns_branch .Lf4_r\n.Lf5_w:\ns_mov_b32 s97, 0\n.Lf5_l:\ns_sleep 1\nbuffer_load_dword v78, v76, s[24:27], 0 offen sc1\n"
"s_waitcnt vmcnt(0)\ns_add_u32 s97, s97, 1\ns_cmp_gt_u32 s97, 1048576\ns_cbranch_scc1 .Lf5_r\nv_sub_u32 v66, v78, v65\nv_cmp_gt_u32 vcc, 5, v66\ns_and_b64 vcc, vcc, s[28:29]\ns_cbranch_scc1 .Lf5_l\n"
"s_branch .Lf5_r\n.Lsa1:\nds_read_b32 v69, v74\ns_waitcnt lgkmcnt(0)\nv_cmp_gt_u32 vcc, v75, v69\ns_cbranch_vccnz .Lsa1\ns_branch .Lra1\n.Lsa2:\n"
"ds_read_b32 v69, v74\ns_waitcnt lgkmcnt(0)\nv_cmp_gt_u32 vcc, v75, v69\ns_cbranch_vccnz .Lsa2\ns_branch .Lra2\n.Lsa3:\nds_read_b32 v69, v74\ns_waitcnt lgkmcnt(0)\n"
"v_cmp_gt_u32 vcc, v75, v69\ns_cbranch_vccnz .Lsa3\ns_branch .Lra3\n.Lsa4:\nds_read_b32 v69, v74\ns_waitcnt lgkmcnt(0)\nv_cmp_gt_u32 vcc, v75, v69\ns_cbranch_vccnz .Lsa4\n"
"s_branch .Lra4\n.Lsa29:\nds_read_b32 v69, v74\ns_waitcnt lgkmcnt(0)\nv_cmp_gt_u32 vcc, v75, v69\ns_cbranch_vccnz .Lsa29\ns_branch .Lra29\n.Lsa30:\n"
"ds_read_b32 v69, v74\ns_waitcnt lgkmcnt(0)\nv_cmp_gt_u32 vcc, v75, v69\ns_cbranch_vccnz .Lsa30\ns_branch .Lra30\n.Lsa31:\nds_read_b32 v69, v74\ns_waitcnt lgkmcnt(0)\n"
"v_cmp_gt_u32 vcc, v75, v69\ns_cbranch_vccnz .Lsa31\ns_branch .Lra31\n.Lsa29x:\nds_read_b32 v69, v74\ns_waitcnt lgkmcnt(0)\nv_cmp_gt_u32 vcc, v75, v69\ns_cbranch_vccnz .Lsa29x\n"
"s_branch .Lra29x\n.Lsa30x:\nds_read_b32 v69, v74\ns_waitcnt lgkmcnt(0)\nv_cmp_gt_u32 vcc, v75, v69\ns_cbranch_vccnz .Lsa30x\ns_branch .Lra30x\n.Lsa31x:\n"
"ds_read_b32 v69, v74\ns_waitcnt lgkmcnt(0)\nv_cmp_gt_u32 vcc, v75, v69\ns_cbranch_vccnz .Lsa31x\ns_branch .Lra31x\n.Lso0:\nds_read_b32 v69, v74\ns_waitcnt lgkmcnt(0)\n"
"v_cmp_gt_u32 vcc, v75, v69\ns_cbranch_vccnz .Lso0\ns_branch .Lro0\n.Lso1:\nds_read_b32 v69, v74\ns_waitcnt lgkmcnt(0)\nv_cmp_gt_u32 vcc, v75, v69\ns_cbranch_vccnz .Lso1\n"
"s_branch .Lro1\n.Lso2:\nds_read_b32 v69, v74\ns_waitcnt lgkmcnt(0)\nv_cmp_gt_u32 vcc, v75, v69\ns_cbranch_vccnz .Lso2\ns_branch .Lro2\n.Lso3:\n"
"ds_read_b32 v69, v74\ns_waitcnt lgkmcnt(0)\nv_cmp_gt_u32 vcc, v75, v69\ns_cbranch_vccnz .Lso3\ns_branch .Lro3\n.Lso4:\nds_read_b32 v69, v74\ns_waitcnt lgkmcnt(0)\n"
"v_cmp_gt_u32 vcc, v75, v69\ns_cbranch_vccnz .Lso4\ns_branch .Lro4\n.Lso5:\nds_read_b32 v69, v74\ns_waitcnt lgkmcnt(0)\nv_cmp_gt_u32 vcc, v75, v69\ns_cbranch_vccnz .Lso5\n"
"s_branch .Lro5\n.Lso6:\nds_read_b32 v69, v74\ns_waitcnt lgkmcnt(0)\nv_cmp_gt_u32 vcc, v75, v69\ns_cbranch_vccnz .Lso6\ns_branch .Lro6\n.Lso7:\n"
"ds_read_b32 v69, v74\ns_waitcnt lgkmcnt(0)\nv_cmp_gt_u32 vcc, v75, v69\ns_cbranch_vccnz .Lso7\ns_branch .Lro7\n.Lso8:\nds_read_b32 v69, v74\ns_waitcnt lgkmcnt(0)\n"
"v_cmp_gt_u32 vcc, v75, v69\ns_cbranch_vccnz .Lso8\ns_branch .Lro8\n.Lso9:\nds_read_b32 v69, v74\ns_waitcnt lgkmcnt(0)\nv_cmp_gt_u32 vcc, v75, v69\ns_cbranch_vccnz .Lso9\n"
"s_branch .Lro9\n.Lso10:\nds_read_b32 v69, v74\ns_waitcnt lgkmcnt(0)\nv_cmp_gt_u32 vcc, v75, v69\ns_cbranch_vccnz .Lso10\ns_branch .Lro10\n.Lso11:\n"
"ds_read_b32 v69, v74\ns_waitcnt lgkmcnt(0)\nv_cmp_gt_u32 vcc, v75, v69\ns_cbranch_vccnz .Lso11\ns_branch .Lro11\n.Lso12:\nds_read_b32 v69, v74\ns_waitcnt lgkmcnt(0)\n"
"v_cmp_gt_u32 vcc, v75, v69\ns_cbranch_vccnz .Lso12\ns_branch .Lro12\n.Lso13:\nds_read_b32 v69, v74\ns_waitcnt lgkmcnt(0)\nv_cmp_gt_u32 vcc, v75, v69\ns_cbranch_vccnz .Lso13\n"
"s_branch .Lro13\n.Lso14:\nds_read_b32 v69, v74\ns_waitcnt lgkmcnt(0)\nv_cmp_gt_u32 vcc, v75, v69\ns_cbranch_vccnz .Lso14\ns_branch .Lro14\n.Lso15:\n"
"ds_read_b32 v69, v74\ns_waitcnt lgkmcnt(0)\nv_cmp_gt_u32 vcc, v75, v69\ns_cbranch_vccnz .Lso15\ns_branch .Lro15\n.Lso16:\nds_read_b32 v69, v74\ns_waitcnt lgkmcnt(0)\n"
"v_cmp_gt_u32 vcc, v75, v69\ns_cbranch_vccnz .Lso16\ns_branch .Lro16\n.Lso17:\nds_read_b32 v69, v74\ns_waitcnt lgkmcnt(0)\nv_cmp_gt_u32 vcc, v75, v69\ns_cbranch_vccnz .Lso17\n"
"s_branch .Lro17\n.Lso18:\nds_read_b32 v69, v74\ns_waitcnt lgkmcnt(0)\nv_cmp_gt_u32 vcc, v75, v69\ns_cbranch_vccnz .Lso18\ns_branch .Lro18\n.Lso19:\n"
"ds_read_b32 v69, v74\ns_waitcnt lgkmcnt(0)\nv_cmp_gt_u32 vcc, v75, v69\ns_cbranch_vccnz .Lso19\ns_branch .Lro19\n.Lso20:\nds_read_b32 v69, v74\ns_waitcnt lgkmcnt(0)\n"
"v_cmp_gt_u32 vcc, v75, v69\ns_cbranch_vccnz .Lso20\ns_branch .Lro20\n.Lso21:\nds_read_b32 v69, v74\ns_waitcnt lgkmcnt(0)\nv_cmp_gt_u32 vcc, v75, v69\ns_cbranch_vccnz .Lso21\n"
"s_branch .Lro21\n.Lso22:\nds_read_b32 v69, v74\ns_waitcnt lgkmcnt(0)\nv_cmp_gt_u32 vcc, v75, v69\ns_cbranch_vccnz .Lso22\ns_branch .Lro22\n.Lso23:\n"
"ds_read_b32 v69, v74\ns_waitcnt lgkmcnt(0)\nv_cmp_gt_u32 vcc, v75, v69\ns_cbranch_vccnz .Lso23\ns_branch .Lro23\n.Lso24:\nds_read_b32 v69, v74\ns_waitcnt lgkmcnt(0)\n"
"v_cmp_gt_u32 vcc, v75, v69\ns_cbranch_vccnz .Lso24\ns_branch .Lro24\n.Lso25:\nds_read_b32 v69, v74\ns_waitcnt lgkmcnt(0)\nv_cmp_gt_u32 vcc, v75, v69\ns_cbranch_vccnz .Lso25\n"
"s_branch .Lro25\n.Lso26:\nds_read_b32 v69, v74\ns_waitcnt lgkmcnt(0)\nv_cmp_gt_u32 vcc, v75, v69\ns_cbranch_vccnz .Lso26\ns_branch .Lro26\n.Lso27:\n"
"ds_read_b32 v69, v74\ns_waitcnt lgkmcnt(0)\nv_cmp_gt_u32 vcc, v75, v69\ns_cbranch_vccnz .Lso27\ns_branch .Lro27\n.Lso28:\nds_read_b32 v69, v74\ns_waitcnt lgkmcnt(0)\n"
"v_cmp_gt_u32 vcc, v75, v69\ns_cbranch_vccnz .Lso28\ns_branch .Lro28\n.Lso29:\nds_read_b32 v69, v74\ns_waitcnt lgkmcnt(0)\nv_cmp_gt_u32 vcc, v75, v69\ns_cbranch_vccnz .Lso29\n"
"s_branch .Lro29\n.Lso30:\nds_read_b32 v69, v74\ns_waitcnt lgkmcnt(0)\nv_cmp_gt_u32 vcc, v75, v69\ns_cbranch_vccnz .Lso30\ns_branch .Lro30\n.Lso31:\n"
"ds_read_b32 v69, v74\ns_waitcnt lgkmcnt(0)\nv_cmp_gt_u32 vcc, v75, v69\ns_cbranch_vccnz .Lso31\ns_branch .Lro31\n.Lso32:\nds_read_b32 v69, v74\ns_waitcnt lgkmcnt(0)\n"
"v_cmp_gt_u32 vcc, v75, v69\ns_cbranch_vccnz .Lso32\ns_branch .Lro32\n.Lso33:\nds_read_b32 v69, v74\ns_waitcnt lgkmcnt(0)\nv_cmp_gt_u32 vcc, v75, v69\ns_cbranch_vccnz .Lso33\n"
"s_branch .Lro33\n.Lso34:\nds_read_b32 v69, v74\ns_waitcnt lgkmcnt(0)\nv_cmp_gt_u32 vcc, v75, v69\ns_cbranch_vccnz .Lso34\ns_branch .Lro34\n.Lso35:\n"
"ds_read_b32 v69, v74\ns_waitcnt lgkmcnt(0)\nv_cmp_gt_u32 vcc, v75, v69\ns_cbranch_vccnz .Lso35\ns_branch .Lro35\n.Lso36:\nds_read_b32 v69, v74\ns_waitcnt lgkmcnt(0)\n"
"v_cmp_gt_u32 vcc, v75, v69\ns_cbranch_vccnz .Lso36\ns_branch .Lro36\n.Lso37:\nds_read_b32 v69, v74\ns_waitcnt lgkmcnt(0)\nv_cmp_gt_u32 vcc, v75, v69\ns_cbranch_vccnz .Lso37\n"
"s_branch .Lro37\n.Lso38:\nds_read_b32 v69, v74\ns_waitcnt lgkmcnt(0)\nv_cmp_gt_u32 vcc, v75, v69\ns_cbranch_vccnz .Lso38\ns_branch .Lro38\n.Lso39:\n"
"ds_read_b32 v69, v74\ns_waitcnt lgkmcnt(0)\nv_cmp_gt_u32 vcc, v75, v69\ns_cbranch_vccnz .Lso39\ns_branch .Lro39\n.Lso40:\nds_read_b32 v69, v74\ns_waitcnt lgkmcnt(0)\n"
"v_cmp_gt_u32 vcc, v75, v69\ns_cbranch_vccnz .Lso40\ns_branch .Lro40\n.Lso41:\nds_read_b32 v69, v74\ns_waitcnt lgkmcnt(0)\nv_cmp_gt_u32 vcc, v75, v69\ns_cbranch_vccnz .Lso41\n"
"s_branch .Lro41\n.Lso42:\nds_read_b32 v69, v74\ns_waitcnt lgkmcnt(0)\nv_cmp_gt_u32 vcc, v75, v69\ns_cbranch_vccnz .Lso42\ns_branch .Lro42\n.Lso43:\n"
"ds_read_b32 v69, v74\ns_waitcnt lgkmcnt(0)\nv_cmp_gt_u32 vcc, v75, v69\ns_cbranch_vccnz .Lso43\ns_branch .Lro43\n.Lso44:\nds_read_b32 v69, v74\ns_waitcnt lgkmcnt(0)\n"
"v_cmp_gt_u32 vcc, v75, v69\ns_cbranch_vccnz .Lso44\ns_branch .Lro44\n.Lso45:\nds_read_b32 v69, v74\ns_waitcnt lgkmcnt(0)\nv_cmp_gt_u32 vcc, v75, v69\ns_cbranch_vccnz .Lso45\n"
"s_branch .Lro45\n.Lso46:\nds_read_b32 v69, v74\ns_waitcnt lgkmcnt(0)\nv_cmp_gt_u32 vcc, v75, v69\ns_cbranch_vccnz .Lso46\ns_branch .Lro46\n.Lso47:\n"
"ds_read_b32 v69, v74\ns_waitcnt lgkmcnt(0)\nv_cmp_gt_u32 vcc, v75, v69\ns_cbranch_vccnz .Lso47\ns_branch .Lro47\n.Lso48:\nds_read_b32 v69, v74\ns_waitcnt lgkmcnt(0)\n"
"v_cmp_gt_u32 vcc, v75, v69\ns_cbranch_vccnz .Lso48\ns_branch .Lro48\n.Lso49r0:\nds_read_b32 v69, v74\ns_waitcnt lgkmcnt(0)\nv_cmp_gt_u32 vcc, v75, v69\ns_cbranch_vccnz .Lso49r0\n"
"s_branch .Lro49r0\n.Lso50r0:\nds_read_b32 v69, v74\ns_waitcnt lgkmcnt(0)\nv_cmp_gt_u32 vcc, v75, v69\ns_cbranch_vccnz .Lso50r0\ns_branch .Lro50r0\n.Lso51r0:\n"
"ds_read_b32 v69, v74\ns_waitcnt lgkmcnt(0)\nv_cmp_gt_u32 vcc, v75, v69\ns_cbranch_vccnz .Lso51r0\ns_branch .Lro51r0\n.Lso52r0:\nds_read_b32 v69, v74\ns_waitcnt lgkmcnt(0)\n"
"v_cmp_gt_u32 vcc, v75, v69\ns_cbranch_vccnz .Lso52r0\ns_branch .Lro52r0\n.Lso53r0:\nds_read_b32 v69, v74\ns_waitcnt lgkmcnt(0)\nv_cmp_gt_u32 vcc, v75, v69\ns_cbranch_vccnz .Lso53r0\n"
"s_branch .Lro53r0\n.Lso54r0:\nds_read_b32 v69, v74\ns_waitcnt lgkmcnt(0)\nv_cmp_gt_u32 vcc, v75, v69\ns_cbranch_vccnz .Lso54r0\ns_branch .Lro54r0\n.Lso55r0:\n"
"ds_read_b32 v69, v74\ns_waitcnt lgkmcnt(0)\nv_cmp_gt_u32 vcc, v75, v69\ns_cbranch_vccnz .Lso55r0\ns_branch .Lro55r0\n.Lso56r0:\nds_read_b32 v69, v74\ns_waitcnt lgkmcnt(0)\n"
"v_cmp_gt_u32 vcc, v75, v69\ns_cbranch_vccnz .Lso56r0\ns_branch .Lro56r0\n.Lso57r0:\nds_read_b32 v69, v74\ns_waitcnt lgkmcnt(0)\nv_cmp_gt_u32 vcc, v75, v69\ns_cbranch_vccnz .Lso57r0\n"
"s_branch .Lro57r0\n.Lso58r0:\nds_read_b32 v69, v74\ns_waitcnt lgkmcnt(0)\nv_cmp_gt_u32 vcc, v75, v69\ns_cbranch_vccnz .Lso58r0\ns_branch .Lro58r0\n.Lso59r0:\n"
"ds_read_b32 v69, v74\ns_waitcnt lgkmcnt(0)\nv_cmp_gt_u32 vcc, v75, v69\ns_cbranch_vccnz .Lso59r0\ns_branch .Lro59r0\n.Lso60r0:\nds_read_b32 v69, v74\ns_waitcnt lgkmcnt(0)\n"
"v_cmp_gt_u32 vcc, v75, v69\ns_cbranch_vccnz .Lso60r0\ns_branch .Lro60r0\n.Lso61r0:\nds_read_b32 v69, v74\ns_waitcnt lgkmcnt(0)\nv_cmp_gt_u32 vcc, v75, v69\ns_cbranch_vccnz .Lso61r0\n"
"s_branch .Lro61r0\n.Lso62r0:\nds_read_b32 v69, v74\ns_waitcnt lgkmcnt(0)\nv_cmp_gt_u32 vcc, v75, v69\ns_cbranch_vccnz .Lso62r0\ns_branch .Lro62r0\n.Lso63r0:\n"
"ds_read_b32 v69, v74\ns_waitcnt lgkmcnt(0)\nv_cmp_gt_u32 vcc, v75, v69\ns_cbranch_vccnz .Lso63r0\ns_branch .Lro63r0\n.Lso49r1:\nds_read_b32 v69, v74\ns_waitcnt lgkmcnt(0)\n"
"v_cmp_gt_u32 vcc, v75, v69\ns_cbranch_vccnz .Lso49r1\ns_branch .Lro49r1\n.Lso50r1:\nds_read_b32 v69, v74\ns_waitcnt lgkmcnt(0)\nv_cmp_gt_u32 vcc, v75, v69\ns_cbranch_vccnz .Lso50r1\n"
"s_branch .Lro50r1\n.Lso51r1:\nds_read_b32 v69, v74\ns_waitcnt lgkmcnt(0)\nv_cmp_gt_u32 vcc, v75, v69\ns_cbranch_vccnz .Lso51r1\ns_branch .Lro51r1\n.Lso52r1:\n"
"ds_read_b32 v69, v74\ns_waitcnt lgkmcnt(0)\nv_cmp_gt_u32 vcc, v75, v69\ns_cbranch_vccnz .Lso52r1\ns_branch .Lro52r1\n.Lso53r1:\nds_read_b32 v69, v74\ns_waitcnt lgkmcnt(0)\n"
"v_cmp_gt_u32 vcc, v75, v69\ns_cbranch_vccnz .Lso53r1\ns_branch .Lro53r1\n.Lso54r1:\nds_read_b32 v69, v74\ns_waitcnt lgkmcnt(0)\nv_cmp_gt_u32 vcc, v75, v69\ns_cbranch_vccnz .Lso54r1\n"
"s_branch .Lro54r1\n.Lso55r1:\nds_read_b32 v69, v74\ns_waitcnt lgkmcnt(0)\nv_cmp_gt_u32 vcc, v75, v69\ns_cbranch_vccnz .Lso55r1\ns_branch .Lro55r1\n.Lso56r1:\n"
"ds_read_b32 v69, v74\ns_waitcnt lgkmcnt(0)\nv_cmp_gt_u32 vcc, v75, v69\ns_cbranch_vccnz .Lso56r1\ns_branch .Lro56r1\n.Lso57r1:\nds_read_b32 v69, v74\ns_waitcnt lgkmcnt(0)\n"
"v_cmp_gt_u32 vcc, v75, v69\ns_cbranch_vccnz .Lso57r1\ns_branch .Lro57r1\n.Lso58r1:\nds_read_b32 v69, v74\ns_waitcnt lgkmcnt(0)\nv_cmp_gt_u32 vcc, v75, v69\ns_cbranch_vccnz .Lso58r1\n"
"s_branch .Lro58r1\n.Lso59r1:\nds_read_b32 v69, v74\ns_waitcnt lgkmcnt(0)\nv_cmp_gt_u32 vcc, v75, v69\ns_cbranch_vccnz .Lso59r1\ns_branch .Lro59r1\n.Lso60r1:\n"
"ds_read_b32 v69, v74\ns_waitcnt lgkmcnt(0)\nv_cmp_gt_u32 vcc, v75, v69\ns_cbranch_vccnz .Lso60r1\ns_branch .Lro60r1\n.Lso61r1:\nds_read_b32 v69, v74\ns_waitcnt lgkmcnt(0)\n"
"v_cmp_gt_u32 vcc, v75, v69\ns_cbranch_vccnz .Lso61r1\ns_branch .Lro61r1\n.Lso62r1:\nds_read_b32 v69, v74\ns_waitcnt lgkmcnt(0)\nv_cmp_gt_u32 vcc, v75, v69\ns_cbranch_vccnz .Lso62r1\n"
"s_branch .Lro62r1\n.Lso63r1:\nds_read_b32 v69, v74\ns_waitcnt lgkmcnt(0)\nv_cmp_gt_u32 vcc, v75, v69\ns_cbranch_vccnz .Lso63r1\ns_branch .Lro63r1\n.Ltail1:\n"
"Qcvaa 8,112,32,32\nv_add_u32 v75, 1, v75\nQmvaa 8,116,36\ns_waitcnt vmcnt(16)\nds_write_b32 v73, v75\nQmvaa 8,120,40\nQe 1\nQmvaa 8,124,44\n"
"Qe 2\nQcvaa 12,128,32,32\nQe 3\nQmvaa 12,132,36\nQk 16,0,1\nQk 17,2,3\nQmvaa 12,136,40\nQe 4\n"
"Qmvaa 12,140,44\ns_waitcnt lgkmcnt(1)\nQmvav 80,144,20\nQe 5\nQmvav 84,148,20\nQe 6\nQmvav 88,152,20\nQe 7\n"
"Qmvav 92,156,20\nQk 18,4,5\nQk 19,6,7\nQmvav 96,160,20\nQe 8\nQmvav 100,164,20\nQe 9\nQmvav 104,168,20\n"
"Qe 10\nQmvav 108,172,20\nQe 11\nQmaav 176,204,20\nQk 20,8,9\nQk 21,10,11\nQcvaa 0,112,48,36\nQe 12\n"
"Qmvaa 0,116,52\nds_read_b32 v69, v74\nQmvaa 0,120,56\nQe 13\nQmvaa 0,124,60\nQe 14\nQcvaa 4,128,48,36\nQe 15\n"
"Qmvaa 4,132,52\nQk 22,12,13\nQk 23,14,15\nQmvaa 4,136,56\ns_waitcnt lgkmcnt(0)\nv_cmp_gt_u32 vcc, v75, v69\nQmvaa 4,140,60\ns_cbranch_vccnz .Lsa29x\n"
".Lra29x:\nQmvav 112,144,16\ns_add_u32 m0, s91, 132096\ns_mul_i32 s94, s31, 0\nQmvav 116,148,16\nbuffer_load_dwordx4 v70, s[48:51], s94 offen lds\nQmvav 120,152,16\nQmvav 124,156,16\n"
"Qe 0\nQmvav 128,160,16\nQe 1\nQmvav 132,164,16\nQe 2\nQmvav 136,168,16\nbuffer_load_dwordx4 v71, s[48:51], s94 offen offset:1024 lds\nQmvav 140,172,16\n"
"Qe 3\nQmaav 180,204,16\nQk 16,0,1\nQk 17,2,3\nQcvaa 8,112,64,40\nQe 4\nQmvaa 8,116,68\ns_mul_i32 s95, s90, 0\n"
"Qe 5\nQmvaa 8,120,72\nbuffer_load_dwordx4 v72, s[52:55], s95 offen offset:2048 lds\nQmvaa 8,124,76\nQe 6\nQcvaa 12,128,64,40\nQe 7\nQmvaa 12,132,68\n"
"Qk 18,4,5\nQk 19,6,7\nQmvaa 12,136,72\nQmvaa 12,140,76\nbuffer_load_dwordx4 v61, s[52:55], s95 offen offset:3072 lds\nQmvav 144,144,20\nQmvav 148,148,20\nQmvav 152,152,20\n"
"Qmvav 156,156,20\ns_add_u32 m0, s91, 136192\nQmvav 160,160,20\nbuffer_load_dwordx4 v62, s[52:55], s95 offen lds\nQmvav 164,164,20\nQe 8\nQmvav 168,168,20\nQe 9\n"
"Qmvav 172,172,20\nQe 10\nQmaav 184,204,20\nQe 11\nQcvaa 0,112,80,44\nbuffer_load_dwordx4 v63, s[52:55], s95 offen offset:1024 lds\nQmvaa 0,116,84\nQk 20,8,9\n"
"Qk 21,10,11\nQmvaa 0,120,88\nQe 12\nQmvaa 0,124,92\nQe 13\nQcvaa 4,128,80,44\nQe 14\nQmvaa 4,132,84\n"
"Qmvaa 4,136,88\nQe 15\nQmvaa 4,140,92\nQk 22,12,13\nQk 23,14,15\nQmvav 176,144,16\nQmvav 180,148,16\nQmvav 184,152,16\n"
"Qmvav 188,156,16\nQe 0\nQmvav 192,160,16\nQe 1\nQmvav 196,164,16\nQmvav 200,168,16\nQe 2\nQmvav 204,172,16\n"
"Qe 3\nQmaav 188,204,16\nQk 16,0,1\nQk 17,2,3\nQcvaa 8,112,96,48\nQda 112,53,0\nQmvaa 8,116,100\nQda 116,53,512\n"
"Qmvaa 8,120,104\nQda 120,53,1024\nQmvaa 8,124,108\nQda 124,53,1536\nQcvaa 12,128,96,48\nQda 128,53,2048\nQmvaa 12,132,100\nQda 132,53,2560\n"
"Qmvaa 12,136,104\nQda 136,53,3072\nQmvaa 12,140,108\nQda 140,53,3584\nQmvav 208,144,20\nQe 4\nQmvav 212,148,20\nQe 5\n"
"Qmvav 216,152,20\nQe 6\nQmvav 220,156,20\nQe 7\nQmvav 224,160,20\nQk 18,4,5\nQk 19,6,7\nQmvav 228,164,20\n"
"Qe 8\nQmvav 232,168,20\nQe 9\nQmvav 236,172,20\nQe 10\nQmaav 192,204,20\ns_waitcnt lgkmcnt(0)\nQk 20,8,9\n"
"Qcvaa 0,112,0,24\nQe 11\nQmvaa 0,116,4\nQk 21,10,11\nQmvaa 0,120,8\nQe 12\nQmvaa 0,124,12\nQe 13\n"
"Qcvaa 4,128,0,24\nQe 14\nQmvaa 4,132,4\nQe 15\nQmvaa 4,136,8\nQk 22,12,13\nQk 23,14,15\nQmvaa 4,140,12\n"
"Qmvav 240,144,16\nQmvav 244,148,16\nQmvav 248,152,16\nQmvav 252,156,16\nQe 0\nQmaav 208,160,16\nQe 1\nQmaav 212,164,16\n"
"Qe 2\nQmaav 216,168,16\nQe 3\nQmaav 220,172,16\nQe 4\nQmaav 196,204,16\nQk 16,0,1\nQk 17,2,3\n"
"Qcvaa 8,112,16,28\nQe 5\nQmvaa 8,116,20\nQe 6\nQmvaa 8,120,24\nQe 7\nQmvaa 8,124,28\nQk 18,4,5\n"
"Qk 19,6,7\nQcvaa 12,128,16,28\nQmvaa 12,132,20\nQmvaa 12,136,24\nQmvaa 12,140,28\nQmaav 224,144,20\nQta 144,55,0\nQta 146,55,2048\n"
"Qmaav 228,148,20\nQta 148,55,256\nQta 150,55,2304\nQmaav 232,152,20\nQta 152,55,512\nQta 154,55,2560\nQmaav 236,156,20\nQta 156,55,768\n"
"Qta 158,55,2816\nQmaav 240,160,20\nQta 160,55,1024\nQta 162,55,3072\nQmaav 244,164,20\nQta 164,55,1280\nQta 166,55,3328\nQmaav 248,168,20\n"
"Qta 168,55,1536\nQta 170,55,3584\nQmaav 252,172,20\nQta 172,55,1792\nQta 174,55,3840\nQmaav 200,204,20\nQe 8\nQcvaa 0,112,32,32\n"
"v_add_u32 v75, 1, v75\nQmvaa 0,116,36\ns_waitcnt vmcnt(6)\nds_write_b32 v73, v75\nQmvaa 0,120,40\nQe 9\nQmvaa 0,124,44\nQe 10\n"
"Qcvaa 4,128,32,32\nQe 11\nQmvaa 4,132,36\nQk 20,8,9\nQk 21,10,11\nQmvaa 4,136,40\nQe 12\nQmvaa 4,140,44\n"
"s_waitcnt lgkmcnt(1)\nQmvav 80,144,16\nQe 13\nQmvav 84,148,16\nQe 14\nQmvav 88,152,16\nQe 15\nQmvav 92,156,16\n"
"Qk 22,12,13\nQk 23,14,15\nQmvav 96,160,16\nQe 0\nQmvav 100,164,16\nQe 1\nQmvav 104,168,16\nQe 2\n"
"Qmvav 108,172,16\nQe 3\nQmaav 176,204,16\nQk 16,0,1\nQk 17,2,3\nQcvaa 8,112,48,36\nQe 4\nQmvaa 8,116,52\n"
"ds_read_b32 v69, v74\nQmvaa 8,120,56\nQe 5\nQmvaa 8,124,60\nQe 6\nQcvaa 12,128,48,36\nQe 7\nQmvaa 12,132,52\n"
"Qk 18,4,5\nQk 19,6,7\nQmvaa 12,136,56\ns_waitcnt lgkmcnt(0)\nv_cmp_gt_u32 vcc, v75, v69\nQmvaa 12,140,60\ns_cbranch_vccnz .Lsa30x\n.Lra30x:\n"
"Qmvav 112,144,20\ns_add_u32 m0, s91, 0\ns_mul_i32 s94, s31, 1\nQmvav 116,148,20\nbuffer_load_dwordx4 v70, s[48:51], s94 offen lds\nQmvav 120,152,20\nQmvav 124,156,20\nQe 8\n"
"Qmvav 128,160,20\nQe 9\nQmvav 132,164,20\nQe 10\nQmvav 136,168,20\nbuffer_load_dwordx4 v71, s[48:51], s94 offen offset:1024 lds\nQmvav 140,172,20\nQe 11\n"
"Qmaav 180,204,20\nQk 20,8,9\nQk 21,10,11\nQcvaa 0,112,64,40\nQe 12\nQmvaa 0,116,68\ns_mul_i32 s95, s90, 1\nQe 13\n"
"Qmvaa 0,120,72\nbuffer_load_dwordx4 v72, s[52:55], s95 offen offset:2048 lds\nQmvaa 0,124,76\nQe 14\nQcvaa 4,128,64,40\nQe 15\nQmvaa 4,132,68\nQk 22,12,13\n"
"Qk 23,14,15\nQmvaa 4,136,72\nQmvaa 4,140,76\nbuffer_load_dwordx4 v61, s[52:55], s95 offen offset:3072 lds\nQmvav 144,144,16\nQmvav 148,148,16\nQmvav 152,152,16\nQmvav 156,156,16\n"
"s_add_u32 m0, s91, 4096\nQmvav 160,160,16\nbuffer_load_dwordx4 v62, s[52:55], s95 offen lds\nQmvav 164,164,16\nQe 0\nQmvav 168,168,16\nQe 1\nQmvav 172,172,16\n"
"Qe 2\nQmaav 184,204,16\nQe 3\nQcvaa 8,112,80,44\nbuffer_load_dwordx4 v63, s[52:55], s95 offen offset:1024 lds\nQmvaa 8,116,84\nQk 16,0,1\nQk 17,2,3\n"
"Qmvaa 8,120,88\nQe 4\nQmvaa 8,124,92\nQe 5\nQcvaa 12,128,80,44\ns_add_u32 m0, s91, 24576\ns_mul_i32 s94, s31, 2\nQe 6\n"
"Qmvaa 12,132,84\nbuffer_load_dwordx4 v70, s[48:51], s94 offen lds\nQmvaa 12,136,88\nQe 7\nQmvaa 12,140,92\nQk 18,4,5\nQk 19,6,7\nQmvav 176,144,20\n"
"Qmvav 180,148,20\nQmvav 184,152,20\nbuffer_load_dwordx4 v71, s[48:51], s94 offen offset:1024 lds\nQmvav 188,156,20\nQe 8\nQmvav 192,160,20\nQe 9\nQmvav 196,164,20\n"
"Qmvav 200,168,20\ns_mul_i32 s95, s90, 2\nQe 10\nQmvav 204,172,20\nbuffer_load_dwordx4 v72, s[52:55], s95 offen offset:2048 lds\nQe 11\nQmaav 188,204,20\nQk 20,8,9\n"
"Qk 21,10,11\nQcvaa 0,112,96,48\nQda 112,53,33024\nQmvaa 0,116,100\nQda 116,53,33536\nQmvaa 0,120,104\nQda 120,53,34048\nQmvaa 0,124,108\n"
"Qda 124,53,34560\nbuffer_load_dwordx4 v61, s[52:55], s95 offen offset:3072 lds\nQcvaa 4,128,96,48\nQda 128,53,35072\nQmvaa 4,132,100\nQda 132,53,35584\nQmvaa 4,136,104\nQda 136,53,36096\n"
"Qmvaa 4,140,108\nQda 140,53,36608\ns_add_u32 m0, s91, 28672\nQmvav 208,144,16\nbuffer_load_dwordx4 v62, s[52:55], s95 offen lds\nQe 12\nQmvav 212,148,16\nQe 13\n"
"Qmvav 216,152,16\nQe 14\nQmvav 220,156,16\nQe 15\nQmvav 224,160,16\nQk 22,12,13\nQk 23,14,15\nQmvav 228,164,16\n"
"buffer_load_dwordx4 v63, s[52:55], s95 offen offset:1024 lds\nQe 0\nQmvav 232,168,16\nQe 1\nQmvav 236,172,16\nQe 2\nQmaav 192,204,16\ns_waitcnt lgkmcnt(0)\n"
"Qk 16,0,1\nQcvaa 8,112,0,24\ns_add_u32 m0, s91, 49152\ns_mul_i32 s94, s31, 3\nQe 3\nQmvaa 8,116,4\nbuffer_load_dwordx4 v70, s[48:51], s94 offen lds\nQk 17,2,3\n"
"Qmvaa 8,120,8\nQe 4\nQmvaa 8,124,12\nQe 5\nQcvaa 12,128,0,24\nQe 6\nQmvaa 12,132,4\nQe 7\n"
"Qmvaa 12,136,8\nbuffer_load_dwordx4 v71, s[48:51], s94 offen offset:1024 lds\nQk 18,4,5\nQk 19,6,7\nQmvaa 12,140,12\nQmvav 240,144,20\nQmvav 244,148,20\nQmvav 248,152,20\n"
"s_mul_i32 s95, s90, 3\nQmvav 252,156,20\nbuffer_load_dwordx4 v72, s[52:55], s95 offen offset:2048 lds\nQe 8\nQmaav 208,160,20\nQe 9\nQmaav 212,164,20\nQe 10\n"
"Qmaav 216,168,20\nQe 11\nQmaav 220,172,20\nQe 12\nQmaav 196,204,20\nbuffer_load_dwordx4 v61, s[52:55], s95 offen offset:3072 lds\nQk 20,8,9\nQk 21,10,11\n"
"Qcvaa 0,112,16,28\nQe 13\nQmvaa 0,116,20\nQe 14\nQmvaa 0,120,24\nQe 15\nQmvaa 0,124,28\ns_add_u32 m0, s91, 53248\n"
"Qk 22,12,13\nQk 23,14,15\nQcvaa 4,128,16,28\nbuffer_load_dwordx4 v62, s[52:55], s95 offen lds\nQmvaa 4,132,20\nQmvaa 4,136,24\nQmvaa 4,140,28\nQmaav 224,144,16\n"
"Qta 144,55,33024\nQta 146,55,35072\nQmaav 228,148,16\nQta 148,55,33280\nQta 150,55,35328\nbuffer_load_dwordx4 v63, s[52:55], s95 offen offset:1024 lds\nQmaav 232,152,16\nQta 152,55,33536\n"
"Qta 154,55,35584\nQmaav 236,156,16\nQta 156,55,33792\nQta 158,55,35840\nQmaav 240,160,16\nQta 160,55,34048\nQta 162,55,36096\nQmaav 244,164,16\n"
"Qta 164,55,34304\nQta 166,55,36352\nQmaav 248,168,16\nQta 168,55,34560\nQta 170,55,36608\nQmaav 252,172,16\nQta 172,55,34816\nQta 174,55,36864\n"
"Qmaav 200,204,16\nQe 0\nQcvaa 8,112,32,32\nv_add_u32 v75, 1, v75\nQmvaa 8,116,36\nds_write_b32 v73, v75\nQmvaa 8,120,40\nQe 1\n"
"Qmvaa 8,124,44\nQe 2\nQcvaa 12,128,32,32\nQe 3\nQmvaa 12,132,36\nQk 16,0,1\nQk 17,2,3\nQmvaa 12,136,40\n"
"Qe 4\nQmvaa 12,140,44\ns_waitcnt lgkmcnt(1)\nQmvav 80,144,20\nQe 5\nQmvav 84,148,20\nQe 6\nQmvav 88,152,20\n"
"Qe 7\nQmvav 92,156,20\nQk 18,4,5\nQk 19,6,7\nQmvav 96,160,20\nQe 8\nQmvav 100,164,20\nQe 9\n"
"Qmvav 104,168,20\nQe 10\nQmvav 108,172,20\nQe 11\nQmaav 176,204,20\nQk 20,8,9\nQk 21,10,11\nQcvaa 0,112,48,36\n"
"Qe 12\nQmvaa 0,116,52\nds_read_b32 v69, v74\nQmvaa 0,120,56\nQe 13\nQmvaa 0,124,60\nQe 14\nQcvaa 4,128,48,36\n"
"Qe 15\nQmvaa 4,132,52\nQk 22,12,13\nQk 23,14,15\nQmvaa 4,136,56\ns_waitcnt lgkmcnt(0)\nv_cmp_gt_u32 vcc, v75, v69\nQmvaa 4,140,60\n"
"s_cbranch_vccnz .Lsa31x\n.Lra31x:\nQmvav 112,144,16\ns_add_u32 m0, s91, 73728\ns_mul_i32 s94, s31, 4\nQmvav 116,148,16\nbuffer_load_dwordx4 v70, s[48:51], s94 offen lds\nQmvav 120,152,16\n"
"Qmvav 124,156,16\nQe 0\nQmvav 128,160,16\nQe 1\nQmvav 132,164,16\nQe 2\nQmvav 136,168,16\nbuffer_load_dwordx4 v71, s[48:51], s94 offen offset:1024 lds\n"
"Qmvav 140,172,16\nQe 3\nQmaav 180,204,16\nQk 16,0,1\nQk 17,2,3\nQcvaa 8,112,64,40\nQe 4\nQmvaa 8,116,68\n"
"s_mul_i32 s95, s90, 4\nQe 5\nQmvaa 8,120,72\nbuffer_load_dwordx4 v72, s[52:55], s95 offen offset:2048 lds\nQmvaa 8,124,76\nQe 6\nQcvaa 12,128,64,40\nQe 7\n"
"Qmvaa 12,132,68\nQk 18,4,5\nQk 19,6,7\nQmvaa 12,136,72\nQmvaa 12,140,76\nbuffer_load_dwordx4 v61, s[52:55], s95 offen offset:3072 lds\nQmvav 144,144,20\nQmvav 148,148,20\n"
"Qmvav 152,152,20\nQmvav 156,156,20\ns_add_u32 m0, s91, 77824\nQmvav 160,160,20\nbuffer_load_dwordx4 v62, s[52:55], s95 offen lds\nQmvav 164,164,20\nQe 8\nQmvav 168,168,20\n"
"Qe 9\nQmvav 172,172,20\nQe 10\nQmaav 184,204,20\nQe 11\nQcvaa 0,112,80,44\nbuffer_load_dwordx4 v63, s[52:55], s95 offen offset:1024 lds\nQmvaa 0,116,84\n"
"Qk 20,8,9\nQk 21,10,11\nQmvaa 0,120,88\nQe 12\nQmvaa 0,124,92\nQe 13\nQcvaa 4,128,80,44\ns_add_u32 m0, s91, 98304\n"
"s_mul_i32 s94, s31, 5\nQe 14\nQmvaa 4,132,84\nbuffer_load_dwordx4 v70, s[48:51], s94 offen lds\nQmvaa 4,136,88\nQe 15\nQmvaa 4,140,92\nQk 22,12,13\n"
"Qk 23,14,15\nQmvav 176,144,16\nQmvav 180,148,16\nQmvav 184,152,16\nbuffer_load_dwordx4 v71, s[48:51], s94 offen offset:1024 lds\nQmvav 188,156,16\nQe 0\nQmvav 192,160,16\n"
"Qe 1\nQmvav 196,164,16\nQmvav 200,168,16\ns_mul_i32 s95, s90, 5\nQe 2\nQmvav 204,172,16\nbuffer_load_dwordx4 v72, s[52:55], s95 offen offset:2048 lds\nQe 3\n"
"Qmaav 188,204,16\nQk 16,0,1\nQk 17,2,3\nQcvaa 8,112,96,48\nQmvaa 8,116,100\nQmvaa 8,120,104\nQmvaa 8,124,108\nbuffer_load_dwordx4 v61, s[52:55], s95 offen offset:3072 lds\n"
"Qcvaa 12,128,96,48\nQmvaa 12,132,100\nQmvaa 12,136,104\nQmvaa 12,140,108\ns_add_u32 m0, s91, 102400\nQmvav 208,144,20\nbuffer_load_dwordx4 v62, s[52:55], s95 offen lds\nQe 4\n"
"Qmvav 212,148,20\nQe 5\nQmvav 216,152,20\nQe 6\nQmvav 220,156,20\nQe 7\nQmvav 224,160,20\nQk 18,4,5\n"
"Qk 19,6,7\nQmvav 228,164,20\nbuffer_load_dwordx4 v63, s[52:55], s95 offen offset:1024 lds\nQe 8\nQmvav 232,168,20\nQe 9\nQmvav 236,172,20\nQe 10\n"
"Qmaav 192,204,20\nQk 20,8,9\nQe 11\ns_nop 0\nQk 21,10,11\nQe 12\nQe 13\nQe 14\n"
"Qe 15\nQk 22,12,13\nQk 23,14,15\nQmvav 240,144,16\nQmvav 244,148,16\nQmvav 248,152,16\nQmvav 252,156,16\nQmaav 208,160,16\n"
"Qmaav 212,164,16\nQmaav 216,168,16\nQmaav 220,172,16\nQmaav 196,204,16\nQmaav 224,144,20\nQmaav 228,148,20\nQmaav 232,152,20\nQmaav 236,156,20\n"
"Qmaav 240,160,20\nQmaav 244,164,20\nQmaav 248,168,20\nQmaav 252,172,20\nQmaav 200,204,20\ns_nop 7\nv_accvgpr_read_b32 v24, a176\nv_accvgpr_read_b32 v28, a180\n"
"v_accvgpr_read_b32 v32, a184\nv_accvgpr_read_b32 v36, a188\nv_accvgpr_read_b32 v40, a192\nv_accvgpr_read_b32 v44, a196\nv_accvgpr_read_b32 v48, a200\nv_cmp_nge_f32 vcc, 0x44800000, v24\ns_mov_b64 s[74:75], vcc\nv_cmp_nge_f32 vcc, 0x44800000, v28\n"
"s_or_b64 s[74:75], s[74:75], vcc\nv_cmp_nge_f32 vcc, 0x44800000, v32\ns_or_b64 s[74:75], s[74:75], vcc\nv_cmp_nge_f32 vcc, 0x44800000, v36\ns_or_b64 s[74:75], s[74:75], vcc\nv_cmp_nge_f32 vcc, 0x44800000, v40\ns_or_b64 s[74:75], s[74:75], vcc\nv_cmp_nge_f32 vcc, 0x44800000, v44\n"
"s_or_b64 s[74:75], s[74:75], vcc\nv_cmp_nge_f32 vcc, 0x44800000, v48\ns_or_b64 s[74:75], s[74:75], vcc\ns_cmp_lg_u64 s[74:75], 0\ns_cbranch_scc1 .Lfb\n.Lfbret1:\nv_rcp_f32 v52, v24\nv_rcp_f32 v53, v28\n"
"v_rcp_f32 v54, v32\nv_rcp_f32 v55, v36\nv_rcp_f32 v56, v40\nv_rcp_f32 v57, v44\nv_rcp_f32 v58, v48\ns_mul_i32 s68, s92, 7168\ns_add_u32 s68, s68, %10\nv_add_u32 v48, s68, v60\n"
"v_add_u32 v49, 73728, v48\ns_mul_i32 s68, s93, 1280\ns_add_u32 s68, s68, %10\nv_lshrrev_b32 v66, 9, v60\nv_mul_u32_u24 v66, 2560, v66\nv_bfe_u32 v67, v60, 1, 8\nv_add3_u32 v64, v66, v67, s68\nv_add_u32 v59, 57344, v64\n"
"v_add_u32 v50, 114688, v64\ns_waitcnt vmcnt(24)\ns_barrier\nQtv 0,50,31744\nQtv 2,50,36864\nQtv 4,50,32000\nQtv 6,50,37120\nQtv 8,50,32256\n"
"Qtv 10,50,37376\nQtv 12,50,32512\nQtv 14,50,37632\nQtv 16,50,32768\nQtv 18,50,37888\nQda 112,49,58368\nQda 116,49,59392\nQda 120,49,60416\n"
"Qda 124,49,61440\nQda 128,49,62464\nQda 132,49,63488\nQda 136,49,64512\nv_add_u32 v75, 1, v75\ns_waitcnt vmcnt(18)\nds_write_b32 v73, v75\nv_mul_f32 v80, v52, v80\n"
"v_mul_f32 v81, v52, v81\nv_mul_f32 v82, v52, v82\nv_mul_f32 v83, v52, v83\nv_mul_f32 v84, v52, v84\nv_mul_f32 v85, v52, v85\nv_mul_f32 v86, v52, v86\nv_mul_f32 v87, v52, v87\nQk 80,80,81\n"
"Qk 81,82,83\nQk 82,84,85\nQk 83,86,87\nv_mul_f32 v112, v53, v112\nv_mul_f32 v113, v53, v113\nv_mul_f32 v114, v53, v114\nv_mul_f32 v115, v53, v115\nv_mul_f32 v116, v53, v116\n"
"v_mul_f32 v117, v53, v117\nv_mul_f32 v118, v53, v118\nv_mul_f32 v119, v53, v119\nQk 112,112,113\nQk 113,114,115\nQk 114,116,117\nQk 115,118,119\ns_waitcnt lgkmcnt(5)\n"
"Qzava 0,0,112\nQtv 20,64,14336\nv_mul_f32 v144, v54, v144\nQzava 4,4,112\nQtv 22,64,19456\nv_mul_f32 v145, v54, v145\nQzava 8,8,112\nQtv 24,64,14592\n"
"v_mul_f32 v146, v54, v146\nQzava 12,12,112\nv_mul_f32 v147, v54, v147\nv_mul_f32 v148, v54, v148\nQzava 16,16,112\nQtv 26,64,19712\nv_mul_f32 v149, v54, v149\nQzava 20,0,116\n"
"Qtv 28,64,14848\nv_mul_f32 v150, v54, v150\nQzava 24,4,116\nv_mul_f32 v151, v54, v151\nQk 144,144,145\nQzava 28,8,116\nds_read_b32 v69, v74\nQk 145,146,147\n"
"Qzava 32,12,116\nQtv 30,64,19968\nQk 146,148,149\nQzava 36,16,116\nQtv 32,64,15104\nQk 147,150,151\nQzava 40,0,120\nv_mul_f32 v176, v55, v176\n"
"v_mul_f32 v177, v55, v177\nQzava 44,4,120\ns_waitcnt lgkmcnt(2)\nv_cmp_gt_u32 vcc, v75, v69\nQzava 48,8,120\ns_cbranch_vccnz .Lso0\n.Lro0:\nv_mul_f32 v178, v55, v178\n"
"Qzava 52,12,120\nQtv 34,64,20224\ns_add_u32 m0, s91, 132096\ns_mul_i32 s94, s31, 6\nQzava 56,16,120\nbuffer_load_dwordx4 v70, s[48:51], s94 offen lds\nQzava 60,0,124\nQtv 36,64,15360\n"
"v_mul_f32 v179, v55, v179\nQzava 64,4,124\nv_mul_f32 v180, v55, v180\nv_mul_f32 v181, v55, v181\nQzava 68,8,124\nQtv 38,64,20480\nv_mul_f32 v182, v55, v182\nQzava 72,12,124\n"
"buffer_load_dwordx4 v71, s[48:51], s94 offen offset:1024 lds\nQzava 76,16,124\nQda 140,48,0\nv_mul_f32 v183, v55, v183\nQzava 80,0,128\nQk 176,176,177\nQk 177,178,179\nQzava 84,4,128\n"
"Qda 144,48,1024\ns_mul_i32 s95, s90, 6\nQzava 88,8,128\nbuffer_load_dwordx4 v72, s[52:55], s95 offen offset:2048 lds\nQzava 92,12,128\nQda 148,48,2048\nQk 178,180,181\nQzava 96,16,128\n"
"Qk 179,182,183\nv_mul_f32 v208, v56, v208\nQzava 100,0,132\nQda 152,48,3072\nv_mul_f32 v209, v56, v209\nQzava 104,4,132\nbuffer_load_dwordx4 v61, s[52:55], s95 offen offset:3072 lds\nQzava 108,8,132\n"
"Qda 156,48,4096\nv_mul_f32 v210, v56, v210\nQzava 176,12,132\nv_mul_f32 v211, v56, v211\nv_mul_f32 v212, v56, v212\nQzava 180,16,132\nQda 160,48,5120\ns_add_u32 m0, s91, 136192\n"
"Qzava 184,0,136\nbuffer_load_dwordx4 v62, s[52:55], s95 offen lds\nQzava 188,4,136\nQda 164,48,6144\nv_mul_f32 v213, v56, v213\nQzava 192,8,136\nv_mul_f32 v214, v56, v214\nv_mul_f32 v215, v56, v215\n"
"Qzava 196,12,136\nbuffer_load_dwordx4 v63, s[52:55], s95 offen offset:1024 lds\nv_add_u32 v75, 1, v75\nQzava 200,16,136\ns_waitcnt vmcnt(18)\nds_write_b32 v73, v75\ns_waitcnt lgkmcnt(5)\nQmava 0,20,140\n"
"Qtv 0,64,38912\ns_add_u32 s88, s71, 0\nQmava 4,24,140\nbuffer_store_dwordx4 v[80:83], v60, s[56:59], s88 offen offset:0 sc1\nQmava 8,28,140\nQtv 2,64,44032\nQk 208,208,209\nQmava 12,32,140\n"
"Qtv 4,64,39168\nQk 209,210,211\nQmava 16,36,140\nQk 210,212,213\nQk 211,214,215\nQmava 20,20,144\nQtv 6,64,44288\nv_mul_f32 v240, v57, v240\n"
"Qmava 24,24,144\nQtv 8,64,39424\nv_mul_f32 v241, v57, v241\nQmava 28,28,144\nds_read_b32 v69, v74\nv_mul_f32 v242, v57, v242\nQmava 32,32,144\nv_mul_f32 v243, v57, v243\n"
"v_mul_f32 v244, v57, v244\nQmava 36,36,144\nQtv 10,64,44544\nv_mul_f32 v245, v57, v245\nQmava 40,20,148\nQtv 12,64,39680\nv_mul_f32 v246, v57, v246\nQmava 44,24,148\n"
"s_waitcnt lgkmcnt(2)\nv_cmp_gt_u32 vcc, v75, v69\nQmava 48,28,148\ns_cbranch_vccnz .Lso1\n.Lro1:\nv_mul_f32 v247, v57, v247\nQmava 52,32,148\nQtv 14,64,44800\n"
"s_add_u32 m0, s91, 0\ns_mul_i32 s94, s31, 7\nQmava 56,36,148\nbuffer_load_dwordx4 v70, s[48:51], s94 offen lds\nQmava 60,20,152\nQk 240,240,241\nQk 241,242,243\nQmava 64,24,152\n"
"Qtv 16,64,39936\nQk 242,244,245\nQmava 68,28,152\nQtv 18,64,45056\nQk 243,246,247\nQmava 72,32,152\nbuffer_load_dwordx4 v71, s[48:51], s94 offen offset:1024 lds\nQmava 76,36,152\n"
"v_accvgpr_read_b32 v40, a224\nv_accvgpr_read_b32 v41, a225\nQmava 80,20,156\nQda 112,48,24576\nv_accvgpr_read_b32 v42, a226\nQmava 84,24,156\nQda 116,48,25600\ns_mul_i32 s95, s90, 7\n"
"Qmava 88,28,156\nbuffer_load_dwordx4 v72, s[52:55], s95 offen offset:2048 lds\nQmava 92,32,156\nQda 120,48,26624\nv_accvgpr_read_b32 v43, a227\nQmava 96,36,156\nv_accvgpr_read_b32 v44, a228\nv_accvgpr_read_b32 v45, a229\n"
"Qmava 100,20,160\nQda 124,48,27648\nv_accvgpr_read_b32 v46, a230\nQmava 104,24,160\nbuffer_load_dwordx4 v61, s[52:55], s95 offen offset:3072 lds\nQmava 108,28,160\nQda 128,48,28672\nv_accvgpr_read_b32 v47, a231\n"
"Qmava 176,32,160\nv_mul_f32 v40, v58, v40\nv_mul_f32 v41, v58, v41\nQmava 180,36,160\nQda 132,48,29696\ns_add_u32 m0, s91, 4096\nQmava 184,20,164\nbuffer_load_dwordx4 v62, s[52:55], s95 offen lds\n"
"Qmava 188,24,164\nQda 136,48,30720\nv_mul_f32 v42, v58, v42\nQmava 192,28,164\nv_mul_f32 v43, v58, v43\nv_mul_f32 v44, v58, v44\nQmava 196,32,164\nbuffer_load_dwordx4 v63, s[52:55], s95 offen offset:1024 lds\n"
"v_add_u32 v75, 1, v75\nQmava 200,36,164\ns_waitcnt vmcnt(19)\nds_write_b32 v73, v75\ns_waitcnt lgkmcnt(5)\nQmava 0,0,112\nQtv 20,59,6144\nv_mul_f32 v45, v58, v45\n"
"Qmava 4,4,112\nbuffer_store_dwordx4 v[112:115], v60, s[56:59], s88 offen offset:1024 sc1\nQmava 8,8,112\nQtv 22,59,11264\nv_mul_f32 v46, v58, v46\nQmava 12,12,112\nQtv 24,59,6400\nv_mul_f32 v47, v58, v47\n"
"Qmava 16,16,112\nQk 84,40,41\nQk 85,42,43\nQmava 20,0,116\nQtv 26,59,11520\nQk 86,44,45\nQmava 24,4,116\nQtv 28,59,6656\n"
"Qk 87,46,47\nQmava 28,8,116\nds_read_b32 v69, v74\nv_mul_f32 v88, v52, v88\nQmava 32,12,116\nv_mul_f32 v89, v52, v89\nv_mul_f32 v90, v52, v90\nQmava 36,16,116\n"
"Qtv 30,59,11776\nv_mul_f32 v91, v52, v91\nQmava 40,0,120\nQtv 32,59,6912\nv_mul_f32 v92, v52, v92\nQmava 44,4,120\ns_waitcnt lgkmcnt(2)\nv_cmp_gt_u32 vcc, v75, v69\n"
"Qmava 48,8,120\ns_cbranch_vccnz .Lso2\n.Lro2:\nv_mul_f32 v93, v52, v93\nQmava 52,12,120\nQtv 34,59,12032\ns_add_u32 m0, s91, 24576\ns_mul_i32 s94, s31, 8\n"
"Qmava 56,16,120\nbuffer_load_dwordx4 v70, s[48:51], s94 offen lds\nQmava 60,0,124\nv_mul_f32 v94, v52, v94\nv_mul_f32 v95, v52, v95\nQmava 64,4,124\nQtv 36,59,7168\nQk 88,88,89\n"
"Qmava 68,8,124\nQtv 38,59,12288\nQk 89,90,91\nQmava 72,12,124\nbuffer_load_dwordx4 v71, s[48:51], s94 offen offset:1024 lds\nQmava 76,16,124\nQk 90,92,93\nQk 91,94,95\n"
"Qmava 80,0,128\nQda 140,48,49152\nv_mul_f32 v120, v53, v120\nQmava 84,4,128\nQda 144,48,50176\ns_mul_i32 s95, s90, 8\nQmava 88,8,128\nbuffer_load_dwordx4 v72, s[52:55], s95 offen offset:2048 lds\n"
"Qmava 92,12,128\nQda 148,48,51200\nv_mul_f32 v121, v53, v121\nQmava 96,16,128\nv_mul_f32 v122, v53, v122\nv_mul_f32 v123, v53, v123\nQmava 100,0,132\nQda 152,48,52224\n"
"v_mul_f32 v124, v53, v124\nQmava 104,4,132\nbuffer_load_dwordx4 v61, s[52:55], s95 offen offset:3072 lds\nQmava 108,8,132\nQda 156,48,53248\nv_mul_f32 v125, v53, v125\nQmava 176,12,132\nv_mul_f32 v126, v53, v126\n"
"v_mul_f32 v127, v53, v127\nQmava 180,16,132\nQda 160,48,54272\ns_add_u32 m0, s91, 28672\nQmava 184,0,136\nbuffer_load_dwordx4 v62, s[52:55], s95 offen lds\nQmava 188,4,136\nQda 164,48,55296\n"
"Qk 120,120,121\nQmava 192,8,136\nQk 121,122,123\nQk 122,124,125\nQmava 196,12,136\nbuffer_load_dwordx4 v63, s[52:55], s95 offen offset:1024 lds\nv_add_u32 v75, 1, v75\nQmava 200,16,136\n"
"s_waitcnt vmcnt(20)\nds_write_b32 v73, v75\ns_waitcnt lgkmcnt(5)\nQmava 0,20,140\nQtv 0,59,30720\nQk 123,126,127\nQmava 4,24,140\nbuffer_store_dwordx4 v[144:147], v60, s[56:59], s88 offen offset:2048 sc1\n"
"Qmava 8,28,140\nQtv 2,59,35840\nv_mul_f32 v152, v54, v152\nQmava 12,32,140\nQtv 4,59,30976\nv_mul_f32 v153, v54, v153\nQmava 16,36,140\nv_mul_f32 v154, v54, v154\n"
"v_mul_f32 v155, v54, v155\nQmava 20,20,144\nQtv 6,59,36096\nv_mul_f32 v156, v54, v156\nQmava 24,24,144\nQtv 8,59,31232\nv_mul_f32 v157, v54, v157\nQmava 28,28,144\n"
"ds_read_b32 v69, v74\nv_mul_f32 v158, v54, v158\nQmava 32,32,144\nv_mul_f32 v159, v54, v159\nQk 152,152,153\nQmava 36,36,144\nQtv 10,59,36352\nQk 153,154,155\n"
"Qmava 40,20,148\nQtv 12,59,31488\nQk 154,156,157\nQmava 44,24,148\ns_waitcnt lgkmcnt(2)\nv_cmp_gt_u32 vcc, v75, v69\nQmava 48,28,148\ns_cbranch_vccnz .Lso3\n"
".Lro3:\nQk 155,158,159\nQmava 52,32,148\nQtv 14,59,36608\ns_add_u32 m0, s91, 49152\ns_mul_i32 s94, s31, 9\nQmava 56,36,148\nbuffer_load_dwordx4 v70, s[48:51], s94 offen lds\n"
"Qmava 60,20,152\nv_mul_f32 v184, v55, v184\nv_mul_f32 v185, v55, v185\nQmava 64,24,152\nQtv 16,59,31744\nv_mul_f32 v186, v55, v186\nQmava 68,28,152\nQtv 18,59,36864\n"
"v_mul_f32 v187, v55, v187\nQmava 72,32,152\nbuffer_load_dwordx4 v71, s[48:51], s94 offen offset:1024 lds\nQmava 76,36,152\nv_mul_f32 v188, v55, v188\nv_mul_f32 v189, v55, v189\nQmava 80,20,156\nQda 112,49,0\n"
"v_mul_f32 v190, v55, v190\nQmava 84,24,156\nQda 116,49,1024\ns_mul_i32 s95, s90, 9\nQmava 88,28,156\nbuffer_load_dwordx4 v72, s[52:55], s95 offen offset:2048 lds\nQmava 92,32,156\nQda 120,49,2048\n"
"v_mul_f32 v191, v55, v191\nQmava 96,36,156\nQk 184,184,185\nQk 185,186,187\nQmava 100,20,160\nQda 124,49,3072\nQk 186,188,189\nQmava 104,24,160\n"
"buffer_load_dwordx4 v61, s[52:55], s95 offen offset:3072 lds\nQmava 108,28,160\nQda 128,49,4096\nQk 187,190,191\nQmava 176,32,160\nv_mul_f32 v216, v56, v216\nv_mul_f32 v217, v56, v217\nQmava 180,36,160\n"
"Qda 132,49,5120\ns_add_u32 m0, s91, 53248\nQmava 184,20,164\nbuffer_load_dwordx4 v62, s[52:55], s95 offen lds\nQmava 188,24,164\nQda 136,49,6144\nv_mul_f32 v218, v56, v218\nQmava 192,28,164\n"
"v_mul_f32 v219, v56, v219\nv_mul_f32 v220, v56, v220\nQmava 196,32,164\nbuffer_load_dwordx4 v63, s[52:55], s95 offen offset:1024 lds\nv_add_u32 v75, 1, v75\nQmava 200,36,164\ns_waitcnt vmcnt(21)\nds_write_b32 v73, v75\n"
"s_waitcnt lgkmcnt(5)\nQmava 0,0,112\nQtv 20,59,55296\nv_mul_f32 v221, v56, v221\nQmava 4,4,112\nbuffer_store_dwordx4 v[176:179], v60, s[56:59], s88 offen offset:3072 sc1\nQmava 8,8,112\nQtv 22,59,60416\n"
"v_mul_f32 v222, v56, v222\nQmava 12,12,112\nQtv 24,59,55552\nv_mul_f32 v223, v56, v223\nQmava 16,16,112\nQk 216,216,217\nQk 217,218,219\nQmava 20,0,116\n"
"Qtv 26,59,60672\nQk 218,220,221\nQmava 24,4,116\nQtv 28,59,55808\nQk 219,222,223\nQmava 28,8,116\nds_read_b32 v69, v74\nv_mul_f32 v248, v57, v248\n"
"Qmava 32,12,116\nv_mul_f32 v249, v57, v249\nv_mul_f32 v250, v57, v250\nQmava 36,16,116\nQtv 30,59,60928\nv_mul_f32 v251, v57, v251\nQmava 40,0,120\nQtv 32,59,56064\n"
"v_mul_f32 v252, v57, v252\nQmava 44,4,120\ns_waitcnt lgkmcnt(2)\nv_cmp_gt_u32 vcc, v75, v69\nQmava 48,8,120\ns_cbranch_vccnz .Lso4\n.Lro4:\nv_mul_f32 v253, v57, v253\n"
"Qmava 52,12,120\nQtv 34,59,61184\ns_add_u32 m0, s91, 73728\ns_mul_i32 s94, s31, 10\nQmava 56,16,120\nbuffer_load_dwordx4 v70, s[48:51], s94 offen lds\nQmava 60,0,124\nv_mul_f32 v254, v57, v254\n"
"v_mul_f32 v255, v57, v255\nQmava 64,4,124\nQtv 36,59,56320\nQk 248,248,249\nQmava 68,8,124\nQtv 38,59,61440\nQk 249,250,251\nQmava 72,12,124\n"
"buffer_load_dwordx4 v71, s[48:51], s94 offen offset:1024 lds\nQmava 76,16,124\nQk 250,252,253\nQk 251,254,255\nQmava 80,0,128\nQda 140,49,24576\nv_accvgpr_read_b32 v40, a232\nQmava 84,4,128\n"
"Qda 144,49,25600\ns_mul_i32 s95, s90, 10\nQmava 88,8,128\nbuffer_load_dwordx4 v72, s[52:55], s95 offen offset:2048 lds\nQmava 92,12,128\nQda 148,49,26624\nv_accvgpr_read_b32 v41, a233\nQmava 96,16,128\n"
"v_accvgpr_read_b32 v42, a234\nv_accvgpr_read_b32 v43, a235\nQmava 100,0,132\nQda 152,49,27648\nv_accvgpr_read_b32 v44, a236\nQmava 104,4,132\nbuffer_load_dwordx4 v61, s[52:55], s95 offen offset:3072 lds\nQmava 108,8,132\n"
"Qda 156,49,28672\nv_accvgpr_read_b32 v45, a237\nQmava 176,12,132\nv_accvgpr_read_b32 v46, a238\nv_accvgpr_read_b32 v47, a239\nQmava 180,16,132\nQda 160,49,29696\ns_add_u32 m0, s91, 77824\n"
"Qmava 184,0,136\nbuffer_load_dwordx4 v62, s[52:55], s95 offen lds\nQmava 188,4,136\nQda 164,49,30720\nv_mul_f32 v40, v58, v40\nQmava 192,8,136\nv_mul_f32 v41, v58, v41\nv_mul_f32 v42, v58, v42\n"
"Qmava 196,12,136\nbuffer_load_dwordx4 v63, s[52:55], s95 offen offset:1024 lds\nv_add_u32 v75, 1, v75\nQmava 200,16,136\ns_waitcnt vmcnt(21)\nds_write_b32 v73, v75\ns_waitcnt lgkmcnt(5)\nQmava 0,20,140\n"
"Qtv 0,50,31744\ns_add_u32 s88, s71, 4096\nQmava 4,24,140\nbuffer_store_dwordx4 v[208:211], v60, s[56:59], s88 offen offset:0 sc1\nQmava 8,28,140\nQtv 2,50,36864\nv_mul_f32 v43, v58, v43\nQmava 12,32,140\n"
"Qtv 4,50,32000\nv_mul_f32 v44, v58, v44\nQmava 16,36,140\nv_mul_f32 v45, v58, v45\nv_mul_f32 v46, v58, v46\nQmava 20,20,144\nQtv 6,50,37120\nv_mul_f32 v47, v58, v47\n"
"Qmava 24,24,144\nQtv 8,50,32256\nQk 116,40,41\nQmava 28,28,144\nds_read_b32 v69, v74\nQk 117,42,43\nQmava 32,32,144\nQk 118,44,45\n"
"Qk 119,46,47\nQmava 36,36,144\nQtv 10,50,37376\nv_mul_f32 v96, v52, v96\nQmava 40,20,148\nQtv 12,50,32512\nv_mul_f32 v97, v52, v97\nQmava 44,24,148\n"
"s_waitcnt lgkmcnt(2)\nv_cmp_gt_u32 vcc, v75, v69\nQmava 48,28,148\ns_cbranch_vccnz .Lso5\n.Lro5:\nv_mul_f32 v98, v52, v98\nQmava 52,32,148\nQtv 14,50,37632\n"
"s_add_u32 m0, s91, 98304\ns_mul_i32 s94, s31, 11\nQmava 56,36,148\nbuffer_load_dwordx4 v70, s[48:51], s94 offen lds\nQmava 60,20,152\nv_mul_f32 v99, v52, v99\nv_mul_f32 v100, v52, v100\nQmava 64,24,152\n"
"Qtv 16,50,32768\nv_mul_f32 v101, v52, v101\nQmava 68,28,152\nQtv 18,50,37888\nv_mul_f32 v102, v52, v102\nQmava 72,32,152\nbuffer_load_dwordx4 v71, s[48:51], s94 offen offset:1024 lds\nQmava 76,36,152\n"
"v_mul_f32 v103, v52, v103\nQk 96,96,97\nQmava 80,20,156\nQda 112,49,58368\nQk 97,98,99\nQmava 84,24,156\nQda 116,49,59392\ns_mul_i32 s95, s90, 11\n"
"Qmava 88,28,156\nbuffer_load_dwordx4 v72, s[52:55], s95 offen offset:2048 lds\nQmava 92,32,156\nQda 120,49,60416\nQk 98,100,101\nQmava 96,36,156\nQk 99,102,103\nv_mul_f32 v128, v53, v128\n"
"Qmava 100,20,160\nQda 124,49,61440\nv_mul_f32 v129, v53, v129\nQmava 104,24,160\nbuffer_load_dwordx4 v61, s[52:55], s95 offen offset:3072 lds\nQmava 108,28,160\nQda 128,49,62464\nv_mul_f32 v130, v53, v130\n"
"Qmava 176,32,160\nv_mul_f32 v131, v53, v131\nv_mul_f32 v132, v53, v132\nQmava 180,36,160\nQda 132,49,63488\ns_add_u32 m0, s91, 102400\nQmava 184,20,164\nbuffer_load_dwordx4 v62, s[52:55], s95 offen lds\n"
"Qmava 188,24,164\nQda 136,49,64512\nv_mul_f32 v133, v53, v133\nQmava 192,28,164\nv_mul_f32 v134, v53, v134\nv_mul_f32 v135, v53, v135\nQmava 196,32,164\nbuffer_load_dwordx4 v63, s[52:55], s95 offen offset:1024 lds\n"
"v_add_u32 v75, 1, v75\nQmava 200,36,164\ns_waitcnt vmcnt(21)\nds_write_b32 v73, v75\ns_waitcnt lgkmcnt(5)\nQmava 0,0,112\nQtv 20,64,14336\nQk 128,128,129\n"
"Qmava 4,4,112\nbuffer_store_dwordx4 v[240:243], v60, s[56:59], s88 offen offset:1024 sc1\nQmava 8,8,112\nQtv 22,64,19456\nQk 129,130,131\nQmava 12,12,112\nQtv 24,64,14592\nQk 130,132,133\n"
"Qmava 16,16,112\nQk 131,134,135\nv_mul_f32 v160, v54, v160\nQmava 20,0,116\nQtv 26,64,19712\nv_mul_f32 v161, v54, v161\nQmava 24,4,116\nQtv 28,64,14848\n"
"v_mul_f32 v162, v54, v162\nQmava 28,8,116\nds_read_b32 v69, v74\nv_mul_f32 v163, v54, v163\nQmava 32,12,116\nv_mul_f32 v164, v54, v164\nv_mul_f32 v165, v54, v165\nQmava 36,16,116\n"
"Qtv 30,64,19968\nv_mul_f32 v166, v54, v166\nQmava 40,0,120\nQtv 32,64,15104\nv_mul_f32 v167, v54, v167\nQmava 44,4,120\ns_waitcnt lgkmcnt(2)\nv_cmp_gt_u32 vcc, v75, v69\n"
"Qmava 48,8,120\ns_cbranch_vccnz .Lso6\n.Lro6:\nQk 160,160,161\nQmava 52,12,120\nQtv 34,64,20224\ns_add_u32 m0, s91, 132096\ns_mul_i32 s94, s31, 12\n"
"Qmava 56,16,120\nbuffer_load_dwordx4 v70, s[48:51], s94 offen lds\nQmava 60,0,124\nQk 161,162,163\nQk 162,164,165\nQmava 64,4,124\nQtv 36,64,15360\nQk 163,166,167\n"
"Qmava 68,8,124\nQtv 38,64,20480\nv_mul_f32 v192, v55, v192\nQmava 72,12,124\nbuffer_load_dwordx4 v71, s[48:51], s94 offen offset:1024 lds\nQmava 76,16,124\nv_mul_f32 v193, v55, v193\nv_mul_f32 v194, v55, v194\n"
"Qmava 80,0,128\nQda 140,48,0\nv_mul_f32 v195, v55, v195\nQmava 84,4,128\nQda 144,48,1024\ns_mul_i32 s95, s90, 12\nQmava 88,8,128\nbuffer_load_dwordx4 v72, s[52:55], s95 offen offset:2048 lds\n"
"Qmava 92,12,128\nQda 148,48,2048\nv_mul_f32 v196, v55, v196\nQmava 96,16,128\nv_mul_f32 v197, v55, v197\nv_mul_f32 v198, v55, v198\nQmava 100,0,132\nQda 152,48,3072\n"
"v_mul_f32 v199, v55, v199\nQmava 104,4,132\nbuffer_load_dwordx4 v61, s[52:55], s95 offen offset:3072 lds\nQmava 108,8,132\nQda 156,48,4096\nQk 192,192,193\nQmava 176,12,132\nQk 193,194,195\n"
"Qk 194,196,197\nQmava 180,16,132\nQda 160,48,5120\ns_add_u32 m0, s91, 136192\nQmava 184,0,136\nbuffer_load_dwordx4 v62, s[52:55], s95 offen lds\nQmava 188,4,136\nQda 164,48,6144\n"
"Qk 195,198,199\nQmava 192,8,136\nv_mul_f32 v224, v56, v224\nv_mul_f32 v225, v56, v225\nQmava 196,12,136\nbuffer_load_dwordx4 v63, s[52:55], s95 offen offset:1024 lds\nv_add_u32 v75, 1, v75\nQmava 200,16,136\n"
"s_waitcnt vmcnt(21)\nds_write_b32 v73, v75\ns_waitcnt lgkmcnt(5)\nQmava 0,20,140\nQtv 0,64,38912\nv_mul_f32 v226, v56, v226\nQmava 4,24,140\nbuffer_store_dwordx4 v[84:87], v60, s[56:59], s88 offen offset:2048 sc1\n"
"Qmava 8,28,140\nQtv 2,64,44032\nv_mul_f32 v227, v56, v227\nQmava 12,32,140\nQtv 4,64,39168\nv_mul_f32 v228, v56, v228\nQmava 16,36,140\nv_mul_f32 v229, v56, v229\n"
"v_mul_f32 v230, v56, v230\nQmava 20,20,144\nQtv 6,64,44288\nv_mul_f32 v231, v56, v231\nQmava 24,24,144\nQtv 8,64,39424\nQk 224,224,225\nQmava 28,28,144\n"
"ds_read_b32 v69, v74\nQk 225,226,227\nQmava 32,32,144\nQk 226,228,229\nQk 227,230,231\nQmava 36,36,144\nQtv 10,64,44544\nv_accvgpr_read_b32 v40, a208\n"
"Qmava 40,20,148\nQtv 12,64,39680\nv_accvgpr_read_b32 v41, a209\nQmava 44,24,148\ns_waitcnt lgkmcnt(2)\nv_cmp_gt_u32 vcc, v75, v69\nQmava 48,28,148\ns_cbranch_vccnz .Lso7\n"
".Lro7:\nv_accvgpr_read_b32 v42, a210\nQmava 52,32,148\nQtv 14,64,44800\ns_add_u32 m0, s91, 0\ns_mul_i32 s94, s31, 13\nQmava 56,36,148\nbuffer_load_dwordx4 v70, s[48:51], s94 offen lds\n"
"Qmava 60,20,152\nv_accvgpr_read_b32 v43, a211\nv_accvgpr_read_b32 v44, a212\nQmava 64,24,152\nQtv 16,64,39936\nv_accvgpr_read_b32 v45, a213\nQmava 68,28,152\nQtv 18,64,45056\n"
"v_accvgpr_read_b32 v46, a214\nQmava 72,32,152\nbuffer_load_dwordx4 v71, s[48:51], s94 offen offset:1024 lds\nQmava 76,36,152\nv_accvgpr_read_b32 v47, a215\nv_mul_f32 v40, v57, v40\nQmava 80,20,156\nQda 112,48,24576\n"
"v_mul_f32 v41, v57, v41\nQmava 84,24,156\nQda 116,48,25600\ns_mul_i32 s95, s90, 13\nQmava 88,28,156\nbuffer_load_dwordx4 v72, s[52:55], s95 offen offset:2048 lds\nQmava 92,32,156\nQda 120,48,26624\n"
"v_mul_f32 v42, v57, v42\nQmava 96,36,156\nv_mul_f32 v43, v57, v43\nv_mul_f32 v44, v57, v44\nQmava 100,20,160\nQda 124,48,27648\nv_mul_f32 v45, v57, v45\nQmava 104,24,160\n"
"buffer_load_dwordx4 v61, s[52:55], s95 offen offset:3072 lds\nQmava 108,28,160\nQda 128,48,28672\nv_mul_f32 v46, v57, v46\nQmava 176,32,160\nv_mul_f32 v47, v57, v47\nQk 148,40,41\nQmava 180,36,160\n"
"Qda 132,48,29696\ns_add_u32 m0, s91, 4096\nQmava 184,20,164\nbuffer_load_dwordx4 v62, s[52:55], s95 offen lds\nQmava 188,24,164\nQda 136,48,30720\nQk 149,42,43\nQmava 192,28,164\n"
"Qk 150,44,45\nQk 151,46,47\nQmava 196,32,164\nbuffer_load_dwordx4 v63, s[52:55], s95 offen offset:1024 lds\nv_add_u32 v75, 1, v75\nQmava 200,36,164\ns_waitcnt vmcnt(21)\nds_write_b32 v73, v75\n"
"s_waitcnt lgkmcnt(5)\nQmava 0,0,112\nQtv 20,59,6144\ns_add_u32 s88, s71, 14336\nQmava 4,4,112\nbuffer_store_dwordx4 v[88:91], v60, s[56:59], s88 offen offset:0 sc1\nQmava 8,8,112\nQtv 22,59,11264\n"
"v_accvgpr_read_b32 v40, a240\nQmava 12,12,112\nQtv 24,59,6400\nv_accvgpr_read_b32 v41, a241\nQmava 16,16,112\nv_accvgpr_read_b32 v42, a242\nv_accvgpr_read_b32 v43, a243\nQmava 20,0,116\n"
"Qtv 26,59,11520\nv_accvgpr_read_b32 v44, a244\nQmava 24,4,116\nQtv 28,59,6656\nv_accvgpr_read_b32 v45, a245\nQmava 28,8,116\nds_read_b32 v69, v74\nv_accvgpr_read_b32 v46, a246\n"
"Qmava 32,12,116\nv_accvgpr_read_b32 v47, a247\nv_mul_f32 v40, v58, v40\nQmava 36,16,116\nQtv 30,59,11776\nv_mul_f32 v41, v58, v41\nQmava 40,0,120\nQtv 32,59,6912\n"
"v_mul_f32 v42, v58, v42\nQmava 44,4,120\ns_waitcnt lgkmcnt(2)\nv_cmp_gt_u32 vcc, v75, v69\nQmava 48,8,120\ns_cbranch_vccnz .Lso8\n.Lro8:\nv_mul_f32 v43, v58, v43\n"
"Qmava 52,12,120\nQtv 34,59,12032\ns_add_u32 m0, s91, 24576\ns_mul_i32 s94, s31, 14\nQmava 56,16,120\nbuffer_load_dwordx4 v70, s[48:51], s94 offen lds\nQmava 60,0,124\nv_mul_f32 v44, v58, v44\n"
"v_mul_f32 v45, v58, v45\nQmava 64,4,124\nQtv 36,59,7168\nv_mul_f32 v46, v58, v46\nQmava 68,8,124\nQtv 38,59,12288\nv_mul_f32 v47, v58, v47\nQmava 72,12,124\n"
"buffer_load_dwordx4 v71, s[48:51], s94 offen offset:1024 lds\nQmava 76,16,124\nQk 180,40,41\nQk 181,42,43\nQmava 80,0,128\nQda 140,48,49152\nQk 182,44,45\nQmava 84,4,128\n"
"Qda 144,48,50176\ns_mul_i32 s95, s90, 14\nQmava 88,8,128\nbuffer_load_dwordx4 v72, s[52:55], s95 offen offset:2048 lds\nQmava 92,12,128\nQda 148,48,51200\nQk 183,46,47\nQmava 96,16,128\n"
"v_mul_f32 v104, v52, v104\nv_mul_f32 v105, v52, v105\nQmava 100,0,132\nQda 152,48,52224\nv_mul_f32 v106, v52, v106\nQmava 104,4,132\nbuffer_load_dwordx4 v61, s[52:55], s95 offen offset:3072 lds\nQmava 108,8,132\n"
"Qda 156,48,53248\nv_mul_f32 v107, v52, v107\nQmava 176,12,132\nv_mul_f32 v108, v52, v108\nv_mul_f32 v109, v52, v109\nQmava 180,16,132\nQda 160,48,54272\ns_add_u32 m0, s91, 28672\n"
"Qmava 184,0,136\nbuffer_load_dwordx4 v62, s[52:55], s95 offen lds\nQmava 188,4,136\nQda 164,48,55296\nv_mul_f32 v110, v52, v110\nQmava 192,8,136\nv_mul_f32 v111, v52, v111\nQk 104,104,105\n"
"Qmava 196,12,136\nbuffer_load_dwordx4 v63, s[52:55], s95 offen offset:1024 lds\nv_add_u32 v75, 1, v75\nQmava 200,16,136\ns_waitcnt vmcnt(21)\nds_write_b32 v73, v75\ns_waitcnt lgkmcnt(5)\nQmava 0,20,140\n"
"Qtv 0,59,30720\nQk 105,106,107\nQmava 4,24,140\nbuffer_store_dwordx4 v[120:123], v60, s[56:59], s88 offen offset:1024 sc1\nQmava 8,28,140\nQtv 2,59,35840\nQk 106,108,109\nQmava 12,32,140\n"
"Qtv 4,59,30976\nQk 107,110,111\nQmava 16,36,140\nv_mul_f32 v136, v53, v136\nv_mul_f32 v137, v53, v137\nQmava 20,20,144\nQtv 6,59,36096\nv_mul_f32 v138, v53, v138\n"
"Qmava 24,24,144\nQtv 8,59,31232\nv_mul_f32 v139, v53, v139\nQmava 28,28,144\nds_read_b32 v69, v74\nv_mul_f32 v140, v53, v140\nQmava 32,32,144\nv_mul_f32 v141, v53, v141\n"
"v_mul_f32 v142, v53, v142\nQmava 36,36,144\nQtv 10,59,36352\nv_mul_f32 v143, v53, v143\nQmava 40,20,148\nQtv 12,59,31488\nQk 136,136,137\nQmava 44,24,148\n"
"s_waitcnt lgkmcnt(2)\nv_cmp_gt_u32 vcc, v75, v69\nQmava 48,28,148\ns_cbranch_vccnz .Lso9\n.Lro9:\nQk 137,138,139\nQmava 52,32,148\nQtv 14,59,36608\n"
"s_add_u32 m0, s91, 49152\ns_mul_i32 s94, s31, 15\nQmava 56,36,148\nbuffer_load_dwordx4 v70, s[48:51], s94 offen lds\nQmava 60,20,152\nQk 138,140,141\nQk 139,142,143\nQmava 64,24,152\n"
"Qtv 16,59,31744\nv_mul_f32 v168, v54, v168\nQmava 68,28,152\nQtv 18,59,36864\nv_mul_f32 v169, v54, v169\nQmava 72,32,152\nbuffer_load_dwordx4 v71, s[48:51], s94 offen offset:1024 lds\nQmava 76,36,152\n"
"v_mul_f32 v170, v54, v170\nv_mul_f32 v171, v54, v171\nQmava 80,20,156\nQda 112,49,0\nv_mul_f32 v172, v54, v172\nQmava 84,24,156\nQda 116,49,1024\ns_mul_i32 s95, s90, 15\n"
"Qmava 88,28,156\nbuffer_load_dwordx4 v72, s[52:55], s95 offen offset:2048 lds\nQmava 92,32,156\nQda 120,49,2048\nv_mul_f32 v173, v54, v173\nQmava 96,36,156\nv_mul_f32 v174, v54, v174\nv_mul_f32 v175, v54, v175\n"
"Qmava 100,20,160\nQda 124,49,3072\nQk 168,168,169\nQmava 104,24,160\nbuffer_load_dwordx4 v61, s[52:55], s95 offen offset:3072 lds\nQmava 108,28,160\nQda 128,49,4096\nQk 169,170,171\n"
"Qmava 176,32,160\nQk 170,172,173\nQk 171,174,175\nQmava 180,36,160\nQda 132,49,5120\ns_add_u32 m0, s91, 53248\nQmava 184,20,164\nbuffer_load_dwordx4 v62, s[52:55], s95 offen lds\n"
"Qmava 188,24,164\nQda 136,49,6144\nv_mul_f32 v200, v55, v200\nQmava 192,28,164\nv_mul_f32 v201, v55, v201\nv_mul_f32 v202, v55, v202\nQmava 196,32,164\nbuffer_load_dwordx4 v63, s[52:55], s95 offen offset:1024 lds\n"
"v_add_u32 v75, 1, v75\nQmava 200,36,164\ns_waitcnt vmcnt(21)\nds_write_b32 v73, v75\ns_waitcnt lgkmcnt(5)\nQmava 0,0,112\nQtv 20,59,55296\nv_mul_f32 v203, v55, v203\n"
"Qmava 4,4,112\nbuffer_store_dwordx4 v[152:155], v60, s[56:59], s88 offen offset:2048 sc1\nQmava 8,8,112\nQtv 22,59,60416\nv_mul_f32 v204, v55, v204\nQmava 12,12,112\nQtv 24,59,55552\nv_mul_f32 v205, v55, v205\n"
"Qmava 16,16,112\nv_mul_f32 v206, v55, v206\nv_mul_f32 v207, v55, v207\nQmava 20,0,116\nQtv 26,59,60672\nQk 200,200,201\nQmava 24,4,116\nQtv 28,59,55808\n"
"Qk 201,202,203\nQmava 28,8,116\nds_read_b32 v69, v74\nQk 202,204,205\nQmava 32,12,116\nQk 203,206,207\nv_mul_f32 v232, v56, v232\nQmava 36,16,116\n"
"Qtv 30,59,60928\nv_mul_f32 v233, v56, v233\nQmava 40,0,120\nQtv 32,59,56064\nv_mul_f32 v234, v56, v234\nQmava 44,4,120\ns_waitcnt lgkmcnt(2)\nv_cmp_gt_u32 vcc, v75, v69\n"
"Qmava 48,8,120\ns_cbranch_vccnz .Lso10\n.Lro10:\nv_mul_f32 v235, v56, v235\nQmava 52,12,120\nQtv 34,59,61184\ns_add_u32 m0, s91, 73728\ns_mul_i32 s94, s31, 16\n"
"Qmava 56,16,120\nbuffer_load_dwordx4 v70, s[48:51], s94 offen lds\nQmava 60,0,124\nv_mul_f32 v236, v56, v236\nv_mul_f32 v237, v56, v237\nQmava 64,4,124\nQtv 36,59,56320\nv_mul_f32 v238, v56, v238\n"
"Qmava 68,8,124\nQtv 38,59,61440\nv_mul_f32 v239, v56, v239\nQmava 72,12,124\nbuffer_load_dwordx4 v71, s[48:51], s94 offen offset:1024 lds\nQmava 76,16,124\nQk 232,232,233\nQk 233,234,235\n"
"Qmava 80,0,128\nQda 140,49,24576\nQk 234,236,237\nQmava 84,4,128\nQda 144,49,25600\ns_mul_i32 s95, s90, 16\nQmava 88,8,128\nbuffer_load_dwordx4 v72, s[52:55], s95 offen offset:2048 lds\n"
"Qmava 92,12,128\nQda 148,49,26624\nQk 235,238,239\nQmava 96,16,128\nv_accvgpr_read_b32 v40, a216\nv_accvgpr_read_b32 v41, a217\nQmava 100,0,132\nQda 152,49,27648\n"
"v_accvgpr_read_b32 v42, a218\nQmava 104,4,132\nbuffer_load_dwordx4 v61, s[52:55], s95 offen offset:3072 lds\nQmava 108,8,132\nQda 156,49,28672\nv_accvgpr_read_b32 v43, a219\nQmava 176,12,132\nv_accvgpr_read_b32 v44, a220\n"
"v_accvgpr_read_b32 v45, a221\nQmava 180,16,132\nQda 160,49,29696\ns_add_u32 m0, s91, 77824\nQmava 184,0,136\nbuffer_load_dwordx4 v62, s[52:55], s95 offen lds\nQmava 188,4,136\nQda 164,49,30720\n"
"v_accvgpr_read_b32 v46, a222\nQmava 192,8,136\nv_accvgpr_read_b32 v47, a223\nv_mul_f32 v40, v57, v40\nQmava 196,12,136\nbuffer_load_dwordx4 v63, s[52:55], s95 offen offset:1024 lds\nv_add_u32 v75, 1, v75\nQmava 200,16,136\n"
"s_waitcnt vmcnt(21)\nds_write_b32 v73, v75\ns_waitcnt lgkmcnt(5)\nQmava 0,20,140\nQtv 0,50,31744\nv_mul_f32 v41, v57, v41\nQmava 4,24,140\nbuffer_store_dwordx4 v[184:187], v60, s[56:59], s88 offen offset:3072 sc1\n"
"Qmava 8,28,140\nQtv 2,50,36864\nv_mul_f32 v42, v57, v42\nQmava 12,32,140\nQtv 4,50,32000\nv_mul_f32 v43, v57, v43\nQmava 16,36,140\nv_mul_f32 v44, v57, v44\n"
"v_mul_f32 v45, v57, v45\nQmava 20,20,144\nQtv 6,50,37120\nv_mul_f32 v46, v57, v46\nQmava 24,24,144\nQtv 8,50,32256\nv_mul_f32 v47, v57, v47\nQmava 28,28,144\n"
"ds_read_b32 v69, v74\nQk 212,40,41\nQmava 32,32,144\nQk 213,42,43\nQk 214,44,45\nQmava 36,36,144\nQtv 10,50,37376\nQk 215,46,47\n"
"Qmava 40,20,148\nQtv 12,50,32512\nv_accvgpr_read_b32 v40, a248\nQmava 44,24,148\ns_waitcnt lgkmcnt(2)\nv_cmp_gt_u32 vcc, v75, v69\nQmava 48,28,148\ns_cbranch_vccnz .Lso11\n"
".Lro11:\nv_accvgpr_read_b32 v41, a249\nQmava 52,32,148\nQtv 14,50,37632\ns_add_u32 m0, s91, 98304\ns_mul_i32 s94, s31, 17\nQmava 56,36,148\nbuffer_load_dwordx4 v70, s[48:51], s94 offen lds\n"
"Qmava 60,20,152\nv_accvgpr_read_b32 v42, a250\nv_accvgpr_read_b32 v43, a251\nQmava 64,24,152\nQtv 16,50,32768\nv_accvgpr_read_b32 v44, a252\nQmava 68,28,152\nQtv 18,50,37888\n"
"v_accvgpr_read_b32 v45, a253\nQmava 72,32,152\nbuffer_load_dwordx4 v71, s[48:51], s94 offen offset:1024 lds\nQmava 76,36,152\nv_accvgpr_read_b32 v46, a254\nv_accvgpr_read_b32 v47, a255\nQmava 80,20,156\nQda 112,49,58368\n"
"v_mul_f32 v40, v58, v40\nQmava 84,24,156\nQda 116,49,59392\ns_mul_i32 s95, s90, 17\nQmava 88,28,156\nbuffer_load_dwordx4 v72, s[52:55], s95 offen offset:2048 lds\nQmava 92,32,156\nQda 120,49,60416\n"
"v_mul_f32 v41, v58, v41\nQmava 96,36,156\nv_mul_f32 v42, v58, v42\nv_mul_f32 v43, v58, v43\nQmava 100,20,160\nQda 124,49,61440\nv_mul_f32 v44, v58, v44\nQmava 104,24,160\n"
"buffer_load_dwordx4 v61, s[52:55], s95 offen offset:3072 lds\nQmava 108,28,160\nQda 128,49,62464\nv_mul_f32 v45, v58, v45\nQmava 176,32,160\nv_mul_f32 v46, v58, v46\nv_mul_f32 v47, v58, v47\nQmava 180,36,160\n"
"Qda 132,49,63488\ns_add_u32 m0, s91, 102400\nQmava 184,20,164\nbuffer_load_dwordx4 v62, s[52:55], s95 offen lds\nQmava 188,24,164\nQda 136,49,64512\nQk 244,40,41\nQmava 192,28,164\n"
"Qk 245,42,43\nQk 246,44,45\nQmava 196,32,164\nbuffer_load_dwordx4 v63, s[52:55], s95 offen offset:1024 lds\nv_add_u32 v75, 1, v75\nQmava 200,36,164\ns_waitcnt vmcnt(21)\nds_write_b32 v73, v75\n"
"s_waitcnt lgkmcnt(5)\nQmava 0,0,112\nQtv 20,64,14336\ns_add_u32 s88, s71, 18432\nQmava 4,4,112\nbuffer_store_dwordx4 v[216:219], v60, s[56:59], s88 offen offset:0 sc1\nQmava 8,8,112\nQtv 22,64,19456\n"
"Qk 247,46,47\nQmava 12,12,112\nQtv 24,64,14592\nQmava 16,16,112\nQmava 20,0,116\nQtv 26,64,19712\nQmava 24,4,116\nQtv 28,64,14848\n"
"Qmava 28,8,116\nds_read_b32 v69, v74\nQmava 32,12,116\nQtv 30,64,19968\nQmava 36,16,116\nQmava 40,0,120\nQtv 32,64,15104\nv_add_u32 v67, 2, v65\n"
"Qmava 44,4,120\ns_waitcnt lgkmcnt(2)\nv_cmp_gt_u32 vcc, v75, v69\nQmava 48,8,120\ns_cbranch_vccnz .Lso12\n.Lro12:\nQmava 52,12,120\nbuffer_store_dword v67, v77, s[24:27], 0 offen sc1\n"
"s_add_u32 m0, s91, 132096\ns_mul_i32 s94, s31, 18\nQmava 56,16,120\nbuffer_load_dwordx4 v70, s[48:51], s94 offen lds\nQmava 60,0,124\nQtv 34,64,20224\nQmava 64,4,124\nQtv 36,64,15360\n"
"Qmava 68,8,124\nQmava 72,12,124\nbuffer_load_dwordx4 v71, s[48:51], s94 offen offset:1024 lds\nQmava 76,16,124\nQtv 38,64,20480\nQmava 80,0,128\nQda 140,48,0\nQmava 84,4,128\n"
"Qda 144,48,1024\ns_mul_i32 s95, s90, 18\nQmava 88,8,128\nbuffer_load_dwordx4 v72, s[52:55], s95 offen offset:2048 lds\nQmava 92,12,128\nQmava 96,16,128\nQda 148,48,2048\nQmava 100,0,132\n"
"Qda 152,48,3072\nQmava 104,4,132\nbuffer_load_dwordx4 v61, s[52:55], s95 offen offset:3072 lds\nQmava 108,8,132\nQda 156,48,4096\nQmava 176,12,132\nQmava 180,16,132\nQda 160,48,5120\n"
"s_add_u32 m0, s91, 136192\nQmava 184,0,136\nbuffer_load_dwordx4 v62, s[52:55], s95 offen lds\nQmava 188,4,136\nQda 164,48,6144\nQmava 192,8,136\nQmava 196,12,136\nbuffer_load_dwordx4 v63, s[52:55], s95 offen offset:1024 lds\n"
"v_add_u32 v75, 1, v75\nQmava 200,16,136\ns_waitcnt vmcnt(22)\nds_write_b32 v73, v75\ns_waitcnt lgkmcnt(5)\nQmava 0,20,140\nQtv 0,64,38912\nQmava 4,24,140\n"
"buffer_store_dwordx4 v[248:251], v60, s[56:59], s88 offen offset:1024 sc1\nQmava 8,28,140\nQtv 2,64,44032\nQmava 12,32,140\nQtv 4,64,39168\nQmava 16,36,140\nQmava 20,20,144\nQtv 6,64,44288\n"
"Qmava 24,24,144\nQtv 8,64,39424\nQmava 28,28,144\nds_read_b32 v69, v74\nQmava 32,32,144\nQmava 36,36,144\nQtv 10,64,44544\nQmava 40,20,148\n"
"Qtv 12,64,39680\nQmava 44,24,148\ns_waitcnt lgkmcnt(2)\nv_cmp_gt_u32 vcc, v75, v69\nQmava 48,28,148\ns_cbranch_vccnz .Lso13\n.Lro13:\nQmava 52,32,148\n"
"Qtv 14,64,44800\ns_add_u32 m0, s91, 0\ns_mul_i32 s94, s31, 19\nQmava 56,36,148\nbuffer_load_dwordx4 v70, s[48:51], s94 offen lds\nQmava 60,20,152\nQmava 64,24,152\nQtv 16,64,39936\n"
"Qmava 68,28,152\nQtv 18,64,45056\nQmava 72,32,152\nbuffer_load_dwordx4 v71, s[48:51], s94 offen offset:1024 lds\nQmava 76,36,152\nQmava 80,20,156\nQda 112,48,24576\nQmava 84,24,156\n"
"Qda 116,48,25600\ns_mul_i32 s95, s90, 19\nQmava 88,28,156\nbuffer_load_dwordx4 v72, s[52:55], s95 offen offset:2048 lds\nQmava 92,32,156\nQda 120,48,26624\nQmava 96,36,156\nQmava 100,20,160\n"
"Qda 124,48,27648\nQmava 104,24,160\nbuffer_load_dwordx4 v61, s[52:55], s95 offen offset:3072 lds\nQmava 108,28,160\nQda 128,48,28672\nQmava 176,32,160\nQmava 180,36,160\nQda 132,48,29696\n"
"s_add_u32 m0, s91, 4096\nQmava 184,20,164\nbuffer_load_dwordx4 v62, s[52:55], s95 offen lds\nQmava 188,24,164\nQda 136,48,30720\nQmava 192,28,164\nQmava 196,32,164\nbuffer_load_dwordx4 v63, s[52:55], s95 offen offset:1024 lds\n"
"v_add_u32 v75, 1, v75\nQmava 200,36,164\ns_waitcnt vmcnt(22)\nds_write_b32 v73, v75\ns_waitcnt lgkmcnt(5)\nQmava 0,0,112\nQtv 20,59,6144\nQmava 4,4,112\n"
"buffer_store_dwordx4 v[116:119], v60, s[56:59], s88 offen offset:2048 sc1\nQmava 8,8,112\nQtv 22,59,11264\nQmava 12,12,112\nQtv 24,59,6400\nQmava 16,16,112\nQmava 20,0,116\nQtv 26,59,11520\n"
"v_lshrrev_b32 v57, 8, v60\nQmava 24,4,116\nQtv 28,59,6656\ns_mul_i32 s68, s92, 20\nQmava 28,8,116\nds_read_b32 v69, v74\nv_accvgpr_read_b32 v92, a0\nQmava 32,12,116\n"
"v_accvgpr_read_b32 v93, a1\nv_accvgpr_read_b32 v94, a2\nQmava 36,16,116\nQtv 30,59,11776\nv_accvgpr_read_b32 v95, a3\nQmava 40,0,120\nQtv 32,59,6912\nv_accvgpr_read_b32 v124, a4\n"
"Qmava 44,4,120\ns_waitcnt lgkmcnt(2)\nv_cmp_gt_u32 vcc, v75, v69\nQmava 48,8,120\ns_cbranch_vccnz .Lso14\n.Lro14:\nv_accvgpr_read_b32 v125, a5\nQmava 52,12,120\n"
"Qtv 34,59,12032\ns_add_u32 m0, s91, 24576\ns_mul_i32 s94, s31, 20\nQmava 56,16,120\nbuffer_load_dwordx4 v70, s[48:51], s94 offen lds\nQmava 60,0,124\nv_accvgpr_read_b32 v126, a6\nv_accvgpr_read_b32 v127, a7\n"
"Qmava 64,4,124\nQtv 36,59,7168\nv_add_u32 v57, s68, v57\nQmava 68,8,124\nQtv 38,59,12288\nv_add_u32 v55, 0, v57\nQmava 72,12,124\nbuffer_load_dwordx4 v71, s[48:51], s94 offen offset:1024 lds\n"
"Qmava 76,16,124\nv_mul_u32_u24 v56, 205, v55\nv_lshrrev_b32 v56, 11, v56\nQmava 80,0,128\nQda 140,48,49152\nv_mul_u32_u24 v54, 10, v56\nQmava 84,4,128\nQda 144,48,50176\n"
"s_mul_i32 s95, s90, 20\nQmava 88,8,128\nbuffer_load_dwordx4 v72, s[52:55], s95 offen offset:2048 lds\nQmava 92,12,128\nQda 148,48,51200\nv_sub_u32 v55, v55, v54\nQmava 96,16,128\nv_lshlrev_b32 v55, 5, v55\n"
"v_bfe_u32 v54, v60, 5, 3\nQmava 100,0,132\nQda 152,48,52224\nv_lshl_add_u32 v56, v56, 3, v54\nQmava 104,4,132\nbuffer_load_dwordx4 v61, s[52:55], s95 offen offset:3072 lds\nQmava 108,8,132\nQda 156,48,53248\n"
"v_mul_u32_u24 v56, 1280, v56\nQmava 176,12,132\nv_and_b32 v54, 16, v60\nv_add3_u32 v55, v55, v56, v54\nQmava 180,16,132\nQda 160,48,54272\ns_add_u32 m0, s91, 28672\nQmava 184,0,136\n"
"buffer_load_dwordx4 v62, s[52:55], s95 offen lds\nQmava 188,4,136\nQda 164,48,55296\nv_add_u32 v40, 4096, v55\nQmava 192,8,136\nv_add_u32 v55, 4, v57\nv_mul_u32_u24 v56, 205, v55\nQmava 196,12,136\n"
"buffer_load_dwordx4 v63, s[52:55], s95 offen offset:1024 lds\nv_add_u32 v75, 1, v75\nQmava 200,16,136\ns_waitcnt vmcnt(22)\nds_write_b32 v73, v75\ns_waitcnt lgkmcnt(5)\nQmvva 92,20,140\nQtv 0,59,30720\n"
"s_add_u32 s88, s71, 28672\nQmvva 124,24,140\nbuffer_store_dwordx4 v[96:99], v60, s[56:59], s88 offen offset:0 sc1\nQmava 8,28,140\nQtv 2,59,35840\nv_lshrrev_b32 v56, 11, v56\nQmava 12,32,140\nQtv 4,59,30976\n"
"v_mul_u32_u24 v54, 10, v56\nQmava 16,36,140\nv_sub_u32 v55, v55, v54\nv_lshlrev_b32 v55, 5, v55\nQmava 20,20,144\nQtv 6,59,36096\nv_bfe_u32 v54, v60, 5, 3\nQmava 24,24,144\n"
"Qtv 8,59,31232\nv_lshl_add_u32 v56, v56, 3, v54\nQmava 28,28,144\nds_read_b32 v69, v74\nv_mul_u32_u24 v56, 1280, v56\nQmava 32,32,144\nv_and_b32 v54, 16, v60\nv_add3_u32 v55, v55, v56, v54\n"
"Qmava 36,36,144\nQtv 10,59,36352\nv_accvgpr_read_b32 v156, a8\nQmava 40,20,148\nQtv 12,59,31488\nv_accvgpr_read_b32 v157, a9\nQmava 44,24,148\ns_waitcnt lgkmcnt(2)\n"
"v_cmp_gt_u32 vcc, v75, v69\nQmava 48,28,148\ns_cbranch_vccnz .Lso15\n.Lro15:\nv_accvgpr_read_b32 v158, a10\nQmava 52,32,148\nQtv 14,59,36608\ns_add_u32 m0, s91, 49152\n"
"s_mul_i32 s94, s31, 21\nQmava 56,36,148\nbuffer_load_dwordx4 v70, s[48:51], s94 offen lds\nQmava 60,20,152\nv_accvgpr_read_b32 v159, a11\nv_accvgpr_read_b32 v188, a12\nQmava 64,24,152\nQtv 16,59,31744\n"
"v_accvgpr_read_b32 v189, a13\nQmava 68,28,152\nQtv 18,59,36864\nv_accvgpr_read_b32 v190, a14\nQmava 72,32,152\nbuffer_load_dwordx4 v71, s[48:51], s94 offen offset:1024 lds\nQmava 76,36,152\nv_accvgpr_read_b32 v191, a15\n"
"v_add_u32 v41, 3072, v55\nQmava 80,20,156\nQda 112,49,0\nv_add_u32 v55, 8, v57\nQmava 84,24,156\nQda 116,49,1024\ns_mul_i32 s95, s90, 21\nQmava 88,28,156\n"
"buffer_load_dwordx4 v72, s[52:55], s95 offen offset:2048 lds\nQmava 92,32,156\nQda 120,49,2048\nv_mul_u32_u24 v56, 205, v55\nQmava 96,36,156\nv_lshrrev_b32 v56, 11, v56\nv_mul_u32_u24 v54, 10, v56\nQmava 100,20,160\n"
"Qda 124,49,3072\nv_sub_u32 v55, v55, v54\nQmava 104,24,160\nbuffer_load_dwordx4 v61, s[52:55], s95 offen offset:3072 lds\nQmava 108,28,160\nQda 128,49,4096\nv_lshlrev_b32 v55, 5, v55\nQmava 176,32,160\n"
"v_bfe_u32 v54, v60, 5, 3\nv_lshl_add_u32 v56, v56, 3, v54\nQmava 180,36,160\nQda 132,49,5120\ns_add_u32 m0, s91, 53248\nQmava 184,20,164\nbuffer_load_dwordx4 v62, s[52:55], s95 offen lds\nQmava 188,24,164\n"
"Qda 136,49,6144\nv_mul_u32_u24 v56, 1280, v56\nQmava 192,28,164\nv_and_b32 v54, 16, v60\nv_add3_u32 v55, v55, v56, v54\nQmava 196,32,164\nbuffer_load_dwordx4 v63, s[52:55], s95 offen offset:1024 lds\nv_add_u32 v75, 1, v75\n"
"Qmava 200,36,164\ns_waitcnt vmcnt(21)\nds_write_b32 v73, v75\ns_waitcnt lgkmcnt(5)\nQmvva 92,0,112\nQtv 20,59,55296\nv_add_u32 v42, 2048, v55\nQmvva 124,4,112\n"
"buffer_store_dwordx4 v[128:131], v60, s[56:59], s88 offen offset:1024 sc1\nQmvva 156,8,112\nQtv 22,59,60416\nv_add_u32 v55, 12, v57\nQmvva 188,12,112\nQtv 24,59,55552\nv_mul_u32_u24 v56, 205, v55\nQmava 16,16,112\n"
"v_lshrrev_b32 v56, 11, v56\nv_mul_u32_u24 v54, 10, v56\nQmava 20,0,116\nQtv 26,59,60672\nv_sub_u32 v55, v55, v54\nQmava 24,4,116\nQtv 28,59,55808\nv_lshlrev_b32 v55, 5, v55\n"
"Qmava 28,8,116\nds_read_b32 v69, v74\nv_bfe_u32 v54, v60, 5, 3\nQmava 32,12,116\nv_lshl_add_u32 v56, v56, 3, v54\nv_mul_u32_u24 v56, 1280, v56\nQmava 36,16,116\nQtv 30,59,60928\n"
"v_and_b32 v54, 16, v60\nQmava 40,0,120\nQtv 32,59,56064\nv_add3_u32 v55, v55, v56, v54\nQmava 44,4,120\ns_waitcnt lgkmcnt(2)\nv_cmp_gt_u32 vcc, v75, v69\nQmava 48,8,120\n"
"s_cbranch_vccnz .Lso16\n.Lro16:\nv_accvgpr_read_b32 v220, a16\nQmava 52,12,120\nQtv 34,59,61184\ns_add_u32 m0, s91, 73728\ns_mul_i32 s94, s31, 22\nQmava 56,16,120\n"
"buffer_load_dwordx4 v70, s[48:51], s94 offen lds\nQmava 60,0,124\nv_accvgpr_read_b32 v221, a17\nv_accvgpr_read_b32 v222, a18\nQmava 64,4,124\nQtv 36,59,56320\nv_accvgpr_read_b32 v223, a19\nQmava 68,8,124\n"
"Qtv 38,59,61440\nv_accvgpr_read_b32 v252, a20\nQmava 72,12,124\nbuffer_load_dwordx4 v71, s[48:51], s94 offen offset:1024 lds\nQmava 76,16,124\nv_accvgpr_read_b32 v253, a21\nv_accvgpr_read_b32 v254, a22\nQmava 80,0,128\n"
"Qda 140,49,24576\nv_accvgpr_read_b32 v255, a23\nQmava 84,4,128\nQda 144,49,25600\ns_mul_i32 s95, s90, 22\nQmava 88,8,128\nbuffer_load_dwordx4 v72, s[52:55], s95 offen offset:2048 lds\nQmava 92,12,128\n"
"Qda 148,49,26624\nv_add_u32 v43, 1024, v55\nQmava 96,16,128\nv_add_u32 v55, 16, v57\nv_mul_u32_u24 v56, 205, v55\nQmava 100,0,132\nQda 152,49,27648\nv_lshrrev_b32 v56, 11, v56\n"
"Qmava 104,4,132\nbuffer_load_dwordx4 v61, s[52:55], s95 offen offset:3072 lds\nQmava 108,8,132\nQda 156,49,28672\nv_mul_u32_u24 v54, 10, v56\nQmava 176,12,132\nv_sub_u32 v55, v55, v54\nv_lshlrev_b32 v55, 5, v55\n"
"Qmava 180,16,132\nQda 160,49,29696\ns_add_u32 m0, s91, 77824\nQmava 184,0,136\nbuffer_load_dwordx4 v62, s[52:55], s95 offen lds\nQmava 188,4,136\nQda 164,49,30720\nv_bfe_u32 v54, v60, 5, 3\n"
"Qmava 192,8,136\nv_lshl_add_u32 v56, v56, 3, v54\nv_mul_u32_u24 v56, 1280, v56\nQmava 196,12,136\nbuffer_load_dwordx4 v63, s[52:55], s95 offen offset:1024 lds\nv_add_u32 v75, 1, v75\nQmava 200,16,136\ns_waitcnt vmcnt(21)\n"
"ds_write_b32 v73, v75\ns_waitcnt lgkmcnt(5)\nQmvva 92,20,140\nQtv 0,50,31744\nv_and_b32 v54, 16, v60\nQmvva 124,24,140\nbuffer_store_dwordx4 v[160:163], v60, s[56:59], s88 offen offset:2048 sc1\nQmvva 156,28,140\n"
"Qtv 2,50,36864\nv_add3_u32 v55, v55, v56, v54\nQmvva 188,32,140\nQtv 4,50,32000\nv_add_u32 v44, 4096, v55\nQmvva 220,36,140\ns_mul_i32 s96, s70, 5120\ns_add_u32 s96, s96, %10\n"
"Qmvva 252,20,144\nQtv 6,50,37120\ns_mul_i32 s68, s93, 10240\nQmava 24,24,144\nQtv 8,50,32256\ns_add_u32 s68, s68, 4096\nQmava 28,28,144\nds_read_b32 v69, v74\n"
"v_add_u32 v45, s68, v64\nQmava 32,32,144\nv_add_u32 v46, 61440, v45\ns_sub_u32 s68, 18432, s68\nQmava 36,36,144\nQtv 10,50,37376\nv_add_u32 v47, s68, v64\nQmava 40,20,148\n"
"Qtv 12,50,32512\nv_add_u32 v51, 61440, v47\nQmava 44,24,148\ns_waitcnt lgkmcnt(2)\nv_cmp_gt_u32 vcc, v75, v69\nQmava 48,28,148\ns_cbranch_vccnz .Lso17\n.Lro17:\n"
"s_lshl_b32 s68, s70, 10\nQmava 52,32,148\nQtv 14,50,37632\ns_add_u32 m0, s91, 98304\ns_mul_i32 s94, s31, 23\nQmava 56,36,148\nbuffer_load_dwordx4 v70, s[48:51], s94 offen lds\nQmava 60,20,152\n"
"v_accvgpr_read_b32 v100, a24\nv_accvgpr_read_b32 v101, a25\nQmava 64,24,152\nQtv 16,50,32768\nv_accvgpr_read_b32 v102, a26\nQmava 68,28,152\nQtv 18,50,37888\nv_accvgpr_read_b32 v103, a27\n"
"Qmava 72,32,152\nbuffer_load_dwordx4 v71, s[48:51], s94 offen offset:1024 lds\nQmava 76,36,152\nv_accvgpr_read_b32 v132, a28\nv_accvgpr_read_b32 v133, a29\nQmava 80,20,156\nQda 112,49,58368\nv_accvgpr_read_b32 v134, a30\n"
"Qmava 84,24,156\nQda 116,49,59392\ns_mul_i32 s95, s90, 23\nQmava 88,28,156\nbuffer_load_dwordx4 v72, s[52:55], s95 offen offset:2048 lds\nQmava 92,32,156\nQda 120,49,60416\nv_accvgpr_read_b32 v135, a31\n"
"Qmava 96,36,156\ns_add_u32 s69, %10, 106496\nv_add_u32 v54, s69, v60\nQmava 100,20,160\nQda 124,49,61440\nv_add_u32 v52, s68, v54\nQmava 104,24,160\nbuffer_load_dwordx4 v61, s[52:55], s95 offen offset:3072 lds\n"
"Qmava 108,28,160\nQda 128,49,62464\ns_xor_b32 s68, s68, 2048\nQmava 176,32,160\nv_add_u32 v53, s68, v54\ns_mul_i32 s68, s93, 163840\nQmava 180,36,160\nQda 132,49,63488\n"
"s_add_u32 m0, s91, 102400\nQmava 184,20,164\nbuffer_load_dwordx4 v62, s[52:55], s95 offen lds\nQmava 188,24,164\nQda 136,49,64512\nQmava 192,28,164\ns_add_u32 s62, s62, s68\ns_addc_u32 s63, s63, 0\n"
"Qmava 196,32,164\nbuffer_load_dwordx4 v63, s[52:55], s95 offen offset:1024 lds\nv_add_u32 v75, 1, v75\nQmava 200,36,164\ns_waitcnt vmcnt(21)\nds_write_b32 v73, v75\ns_waitcnt lgkmcnt(5)\nQmvva 92,0,112\n"
"Qtv 20,64,14336\ns_sub_u32 s86, s86, s68\nQmvva 124,4,112\nbuffer_store_dwordx4 v[192:195], v60, s[56:59], s88 offen offset:3072 sc1\nQmvva 156,8,112\nQtv 22,64,19456\nQmvva 188,12,112\nQtv 24,64,14592\n"
"Qmvva 220,16,112\nQmvva 252,0,116\nQtv 26,64,19712\nQmvva 100,4,116\nQtv 28,64,14848\nQmvva 132,8,116\nds_read_b32 v69, v74\nQmava 32,12,116\n"
"Qmava 36,16,116\nQtv 30,64,19968\nQmava 40,0,120\nQtv 32,64,15104\nQmava 44,4,120\ns_waitcnt lgkmcnt(2)\nv_cmp_gt_u32 vcc, v75, v69\nQmava 48,8,120\n"
"s_cbranch_vccnz .Lso18\n.Lro18:\nQmava 52,12,120\nQtv 34,64,20224\ns_add_u32 m0, s91, 132096\ns_mul_i32 s94, s31, 24\nQmava 56,16,120\nbuffer_load_dwordx4 v70, s[48:51], s94 offen lds\n"
"Qmava 60,0,124\nv_accvgpr_read_b32 v164, a32\nv_accvgpr_read_b32 v165, a33\nQmava 64,4,124\nQtv 36,64,15360\nv_accvgpr_read_b32 v166, a34\nQmava 68,8,124\nQtv 38,64,20480\n"
"v_accvgpr_read_b32 v167, a35\nQmava 72,12,124\nbuffer_load_dwordx4 v71, s[48:51], s94 offen offset:1024 lds\nQmava 76,16,124\nv_accvgpr_read_b32 v196, a36\nv_accvgpr_read_b32 v197, a37\nQmava 80,0,128\nQda 140,48,0\n"
"v_accvgpr_read_b32 v198, a38\nQmava 84,4,128\nQda 144,48,1024\ns_mul_i32 s95, s90, 24\nQmava 88,8,128\nbuffer_load_dwordx4 v72, s[52:55], s95 offen offset:2048 lds\nQmava 92,12,128\nQda 148,48,2048\n"
"v_accvgpr_read_b32 v199, a39\nQmava 96,16,128\nQmava 100,0,132\nQda 152,48,3072\nQmava 104,4,132\nbuffer_load_dwordx4 v61, s[52:55], s95 offen offset:3072 lds\nQmava 108,8,132\nQda 156,48,4096\n"
"Qmava 176,12,132\nQmava 180,16,132\nQda 160,48,5120\ns_add_u32 m0, s91, 136192\nQmava 184,0,136\nbuffer_load_dwordx4 v62, s[52:55], s95 offen lds\nQmava 188,4,136\nQda 164,48,6144\n"
"Qmava 192,8,136\nQmava 196,12,136\nbuffer_load_dwordx4 v63, s[52:55], s95 offen offset:1024 lds\nv_add_u32 v75, 1, v75\nQmava 200,16,136\ns_waitcnt vmcnt(21)\nds_write_b32 v73, v75\ns_waitcnt lgkmcnt(5)\n"
"Qmvva 92,20,140\nQtv 0,64,38912\ns_add_u32 s88, s71, 32768\nQmvva 124,24,140\nbuffer_store_dwordx4 v[224:227], v60, s[56:59], s88 offen offset:0 sc1\nQmvva 156,28,140\nQtv 2,64,44032\nQmvva 188,32,140\n"
"Qtv 4,64,39168\nQmvva 220,36,140\nQmvva 252,20,144\nQtv 6,64,44288\nQmvva 100,24,144\nQtv 8,64,39424\nQmvva 132,28,144\nds_read_b32 v69, v74\n"
"Qmvva 164,32,144\nQtv 10,64,44544\nQmvva 196,36,144\nQmava 40,20,148\nQtv 12,64,39680\nv_add_u32 v67, 3, v65\nQmava 44,24,148\ns_waitcnt lgkmcnt(2)\n"
"v_cmp_gt_u32 vcc, v75, v69\nQmava 48,28,148\ns_cbranch_vccnz .Lso19\n.Lro19:\nQmava 52,32,148\nbuffer_store_dword v67, v77, s[24:27], 0 offen sc1\ns_add_u32 m0, s91, 0\ns_mul_i32 s94, s31, 25\n"
"Qmava 56,36,148\nbuffer_load_dwordx4 v70, s[48:51], s94 offen lds\nQmava 60,20,152\nQtv 14,64,44800\nQmava 64,24,152\nQtv 16,64,39936\nQmava 68,28,152\nv_accvgpr_read_b32 v228, a40\n"
"v_accvgpr_read_b32 v229, a41\nQmava 72,32,152\nbuffer_load_dwordx4 v71, s[48:51], s94 offen offset:1024 lds\nQmava 76,36,152\nQtv 18,64,45056\nv_accvgpr_read_b32 v230, a42\nQmava 80,20,156\nQda 112,48,24576\n"
"v_accvgpr_read_b32 v231, a43\nQmava 84,24,156\nQda 116,48,25600\ns_mul_i32 s95, s90, 25\nQmava 88,28,156\nbuffer_load_dwordx4 v72, s[52:55], s95 offen offset:2048 lds\nQmava 92,32,156\nv_accvgpr_read_b32 v108, a44\n"
"v_accvgpr_read_b32 v109, a45\nQmava 96,36,156\nQda 120,48,26624\nv_accvgpr_read_b32 v110, a46\nQmava 100,20,160\nQda 124,48,27648\nv_accvgpr_read_b32 v111, a47\nQmava 104,24,160\n"
"buffer_load_dwordx4 v61, s[52:55], s95 offen offset:3072 lds\nQmava 108,28,160\nQda 128,48,28672\nQmava 176,32,160\nQmava 180,36,160\nQda 132,48,29696\ns_add_u32 m0, s91, 4096\nQmava 184,20,164\n"
"buffer_load_dwordx4 v62, s[52:55], s95 offen lds\nQmava 188,24,164\nQda 136,48,30720\nQmava 192,28,164\nQmava 196,32,164\nbuffer_load_dwordx4 v63, s[52:55], s95 offen offset:1024 lds\nv_add_u32 v75, 1, v75\nQmava 200,36,164\n"
"s_waitcnt vmcnt(22)\nds_write_b32 v73, v75\ns_waitcnt lgkmcnt(5)\nQmvva 92,0,112\nQtv 20,59,6144\nQmvva 124,4,112\nbuffer_store_dwordx4 v[148:151], v60, s[56:59], s88 offen offset:1024 sc1\nQmvva 156,8,112\n"
"Qtv 22,59,11264\nQmvva 188,12,112\nQtv 24,59,6400\nQmvva 220,16,112\nQmvva 252,0,116\nQtv 26,59,11520\nQmvva 100,4,116\nQtv 28,59,6656\n"
"Qmvva 132,8,116\nds_read_b32 v69, v74\nQmvva 164,12,116\nQmvva 196,16,116\nQtv 30,59,11776\nQmvva 228,0,120\nQtv 32,59,6912\nQmvva 108,4,120\n"
"s_waitcnt lgkmcnt(2)\nv_cmp_gt_u32 vcc, v75, v69\nQmava 48,8,120\ns_cbranch_vccnz .Lso20\n.Lro20:\nQmava 52,12,120\nQtv 34,59,12032\ns_add_u32 m0, s91, 24576\n"
"s_mul_i32 s94, s31, 26\nQmava 56,16,120\nbuffer_load_dwordx4 v70, s[48:51], s94 offen lds\nQmava 60,0,124\nQmava 64,4,124\nQtv 36,59,7168\nQmava 68,8,124\nQtv 38,59,12288\n"
"Qmava 72,12,124\nbuffer_load_dwordx4 v71, s[48:51], s94 offen offset:1024 lds\nQmava 76,16,124\nv_accvgpr_read_b32 v140, a48\nv_accvgpr_read_b32 v141, a49\nQmava 80,0,128\nQda 140,48,49152\nv_accvgpr_read_b32 v142, a50\n"
"Qmava 84,4,128\nQda 144,48,50176\ns_mul_i32 s95, s90, 26\nQmava 88,8,128\nbuffer_load_dwordx4 v72, s[52:55], s95 offen offset:2048 lds\nQmava 92,12,128\nQda 148,48,51200\nv_accvgpr_read_b32 v143, a51\n"
"Qmava 96,16,128\nv_accvgpr_read_b32 v172, a52\nv_accvgpr_read_b32 v173, a53\nQmava 100,0,132\nQda 152,48,52224\nv_accvgpr_read_b32 v174, a54\nQmava 104,4,132\nbuffer_load_dwordx4 v61, s[52:55], s95 offen offset:3072 lds\n"
"Qmava 108,8,132\nQda 156,48,53248\nv_accvgpr_read_b32 v175, a55\nQmava 176,12,132\nQmava 180,16,132\nQda 160,48,54272\ns_add_u32 m0, s91, 28672\nQmava 184,0,136\n"
"buffer_load_dwordx4 v62, s[52:55], s95 offen lds\nQmava 188,4,136\nQda 164,48,55296\nQmava 192,8,136\nQmava 196,12,136\nbuffer_load_dwordx4 v63, s[52:55], s95 offen offset:1024 lds\nv_add_u32 v75, 1, v75\nQmava 200,16,136\n"
"s_waitcnt vmcnt(22)\nds_write_b32 v73, v75\ns_waitcnt lgkmcnt(5)\nQmvva 92,20,140\nQtv 0,59,30720\nQmvva 124,24,140\nbuffer_store_dwordx4 v[180:183], v60, s[56:59], s88 offen offset:2048 sc1\nQmvva 156,28,140\n"
"Qtv 2,59,35840\nQmvva 188,32,140\nQtv 4,59,30976\nQmvva 220,36,140\nQmvva 252,20,144\nQtv 6,59,36096\nQmvva 100,24,144\nQtv 8,59,31232\n"
"Qmvva 132,28,144\nds_read_b32 v69, v74\nQmvva 164,32,144\nQmvva 196,36,144\nQtv 10,59,36352\nQmvva 228,20,148\nQtv 12,59,31488\nQmvva 108,24,148\n"
"s_waitcnt lgkmcnt(2)\nv_cmp_gt_u32 vcc, v75, v69\nQmvva 140,28,148\ns_cbranch_vccnz .Lso21\n.Lro21:\nQmvva 172,32,148\nQtv 14,59,36608\ns_add_u32 m0, s91, 49152\n"
"s_mul_i32 s94, s31, 27\nQmava 56,36,148\nbuffer_load_dwordx4 v70, s[48:51], s94 offen lds\nQmava 60,20,152\nQmava 64,24,152\nQtv 16,59,31744\nQmava 68,28,152\nQtv 18,59,36864\n"
"Qmava 72,32,152\nbuffer_load_dwordx4 v71, s[48:51], s94 offen offset:1024 lds\nQmava 76,36,152\nQmava 80,20,156\nQda 112,49,0\nQmava 84,24,156\nQda 116,49,1024\ns_mul_i32 s95, s90, 27\n"
"Qmava 88,28,156\nbuffer_load_dwordx4 v72, s[52:55], s95 offen offset:2048 lds\nQmava 92,32,156\nQda 120,49,2048\nv_accvgpr_read_b32 v204, a56\nQmava 96,36,156\nv_accvgpr_read_b32 v205, a57\nv_accvgpr_read_b32 v206, a58\n"
"Qmava 100,20,160\nQda 124,49,3072\nv_accvgpr_read_b32 v207, a59\nQmava 104,24,160\nbuffer_load_dwordx4 v61, s[52:55], s95 offen offset:3072 lds\nQmava 108,28,160\nQda 128,49,4096\nv_accvgpr_read_b32 v236, a60\n"
"Qmava 176,32,160\nv_accvgpr_read_b32 v237, a61\nv_accvgpr_read_b32 v238, a62\nQmava 180,36,160\nQda 132,49,5120\ns_add_u32 m0, s91, 53248\nQmava 184,20,164\nbuffer_load_dwordx4 v62, s[52:55], s95 offen lds\n"
"Qmava 188,24,164\nQda 136,49,6144\nv_accvgpr_read_b32 v239, a63\nQmava 192,28,164\nQmava 196,32,164\nbuffer_load_dwordx4 v63, s[52:55], s95 offen offset:1024 lds\nv_add_u32 v75, 1, v75\nQmava 200,36,164\n"
"s_waitcnt vmcnt(22)\nds_write_b32 v73, v75\ns_waitcnt lgkmcnt(5)\nQmvva 92,0,112\nQtv 20,59,55296\ns_add_u32 s88, s71, 43008\nQmvva 124,4,112\nbuffer_store_dwordx4 v[104:107], v60, s[56:59], s88 offen offset:0 sc1\n"
"Qmvva 156,8,112\nQtv 22,59,60416\nQmvva 188,12,112\nQtv 24,59,55552\nQmvva 220,16,112\nQmvva 252,0,116\nQtv 26,59,60672\nQmvva 100,4,116\n"
"Qtv 28,59,55808\nQmvva 132,8,116\nds_read_b32 v69, v74\nQmvva 164,12,116\nQmvva 196,16,116\nQtv 30,59,60928\nQmvva 228,0,120\nQtv 32,59,56064\n"
"Qmvva 108,4,120\ns_waitcnt lgkmcnt(2)\nv_cmp_gt_u32 vcc, v75, v69\nQmvva 140,8,120\ns_cbranch_vccnz .Lso22\n.Lro22:\nQmvva 172,12,120\nQtv 34,59,61184\n"
"s_add_u32 m0, s91, 73728\ns_mul_i32 s94, s31, 28\nQmvva 204,16,120\nbuffer_load_dwordx4 v70, s[48:51], s94 offen lds\nQmvva 236,0,124\nQmava 64,4,124\nQtv 36,59,56320\nQmava 68,8,124\n"
"Qtv 38,59,61440\nQmava 72,12,124\nbuffer_load_dwordx4 v71, s[48:51], s94 offen offset:1024 lds\nQmava 76,16,124\nQmava 80,0,128\nQda 140,49,24576\nQmava 84,4,128\nQda 144,49,25600\n"
"s_mul_i32 s95, s90, 28\nQmava 88,8,128\nbuffer_load_dwordx4 v72, s[52:55], s95 offen offset:2048 lds\nQmava 92,12,128\nQda 148,49,26624\nv_accvgpr_read_b32 v80, a64\nQmava 96,16,128\nv_accvgpr_read_b32 v81, a65\n"
"v_accvgpr_read_b32 v82, a66\nQmava 100,0,132\nQda 152,49,27648\nv_accvgpr_read_b32 v83, a67\nQmava 104,4,132\nbuffer_load_dwordx4 v61, s[52:55], s95 offen offset:3072 lds\nQmava 108,8,132\nQda 156,49,28672\n"
"v_accvgpr_read_b32 v112, a68\nQmava 176,12,132\nv_accvgpr_read_b32 v113, a69\nv_accvgpr_read_b32 v114, a70\nQmava 180,16,132\nQda 160,49,29696\ns_add_u32 m0, s91, 77824\nQmava 184,0,136\n"
"buffer_load_dwordx4 v62, s[52:55], s95 offen lds\nQmava 188,4,136\nQda 164,49,30720\nv_accvgpr_read_b32 v115, a71\nQmava 192,8,136\nQmava 196,12,136\nbuffer_load_dwordx4 v63, s[52:55], s95 offen offset:1024 lds\nv_add_u32 v75, 1, v75\n"
"Qmava 200,16,136\ns_waitcnt vmcnt(21)\nds_write_b32 v73, v75\ns_waitcnt lgkmcnt(5)\nQmvva 92,20,140\nbuffer_load_dword v78, v76, s[24:27], 0 offen sc1\nQmvva 124,24,140\nbuffer_store_dwordx4 v[136:139], v60, s[56:59], s88 offen offset:1024 sc1\n"
"Qmvva 156,28,140\nQtv 0,50,31744\nQmvva 188,32,140\nQtv 2,50,36864\nQmvva 220,36,140\nQtv 4,50,32000\nQmvva 252,20,144\nQmvva 100,24,144\n"
"Qtv 6,50,37120\nQmvva 132,28,144\nds_read_b32 v69, v74\nQmvva 164,32,144\nQtv 8,50,32256\nQmvva 196,36,144\nQtv 10,50,37376\nQmvva 228,20,148\n"
"Qmvva 108,24,148\ns_waitcnt lgkmcnt(2)\nv_cmp_gt_u32 vcc, v75, v69\nQmvva 140,28,148\ns_cbranch_vccnz .Lso23\n.Lro23:\nQmvva 172,32,148\nQtv 12,50,32512\n"
"s_add_u32 m0, s91, 98304\ns_mul_i32 s94, s31, 29\nQmvva 204,36,148\nbuffer_load_dwordx4 v70, s[48:51], s94 offen lds\nQmvva 236,20,152\nQtv 14,50,37632\nQmvva 80,24,152\nQtv 16,50,32768\n"
"Qmvva 112,28,152\nQmava 72,32,152\nbuffer_load_dwordx4 v71, s[48:51], s94 offen offset:1024 lds\nQmava 76,36,152\nQtv 18,50,37888\nQmava 80,20,156\nQda 112,49,58368\nQmava 84,24,156\n"
"Qda 116,49,59392\ns_mul_i32 s95, s90, 29\nQmava 88,28,156\nbuffer_load_dwordx4 v72, s[52:55], s95 offen offset:2048 lds\nQmava 92,32,156\nQmava 96,36,156\nQda 120,49,60416\nQmava 100,20,160\n"
"Qda 124,49,61440\nv_accvgpr_read_b32 v144, a72\nQmava 104,24,160\nbuffer_load_dwordx4 v61, s[52:55], s95 offen offset:3072 lds\nQmava 108,28,160\nQda 128,49,62464\nv_accvgpr_read_b32 v145, a73\nQmava 176,32,160\n"
"v_accvgpr_read_b32 v146, a74\nv_accvgpr_read_b32 v147, a75\nQmava 180,36,160\nQda 132,49,63488\ns_add_u32 m0, s91, 102400\nQmava 184,20,164\nbuffer_load_dwordx4 v62, s[52:55], s95 offen lds\nQmava 188,24,164\n"
"Qda 136,49,64512\nv_accvgpr_read_b32 v176, a76\nQmava 192,28,164\nv_accvgpr_read_b32 v177, a77\nv_accvgpr_read_b32 v178, a78\nQmava 196,32,164\nbuffer_load_dwordx4 v63, s[52:55], s95 offen offset:1024 lds\nv_add_u32 v75, 1, v75\n"
"Qmava 200,36,164\ns_waitcnt vmcnt(22)\nds_write_b32 v73, v75\ns_waitcnt lgkmcnt(5)\nQmvva 92,0,112\nQtv 20,64,14336\nv_accvgpr_read_b32 v179, a79\nQmvva 124,4,112\n"
"buffer_store_dwordx4 v[168:171], v60, s[56:59], s88 offen offset:2048 sc1\nQmvva 156,8,112\nQtv 22,64,19456\nQmvva 188,12,112\nQtv 24,64,14592\nQmvva 220,16,112\nQmvva 252,0,116\nQtv 26,64,19712\n"
"Qmvva 100,4,116\nQtv 28,64,14848\nQmvva 132,8,116\nds_read_b32 v69, v74\nQmvva 164,12,116\nQmvva 196,16,116\nQtv 30,64,19968\nQmvva 228,0,120\n"
"Qtv 32,64,15104\nQmvva 108,4,120\ns_waitcnt lgkmcnt(2)\nv_cmp_gt_u32 vcc, v75, v69\nQmvva 140,8,120\ns_cbranch_vccnz .Lso24\n.Lro24:\nQmvva 172,12,120\n"
"Qtv 34,64,20224\ns_add_u32 m0, s91, 132096\ns_mul_i32 s94, s31, 30\nQmvva 204,16,120\nbuffer_load_dwordx4 v70, s[48:51], s94 offen lds\nQmvva 236,0,124\nQmvva 80,4,124\nQtv 36,64,15360\n"
"Qmvva 112,8,124\nQtv 38,64,20480\nQmvva 144,12,124\nbuffer_load_dwordx4 v71, s[48:51], s94 offen offset:1024 lds\nQmvva 176,16,124\nQmava 80,0,128\nQda 140,48,0\nQmava 84,4,128\n"
"Qda 144,48,1024\ns_mul_i32 s95, s90, 30\nQmava 88,8,128\nbuffer_load_dwordx4 v72, s[52:55], s95 offen offset:2048 lds\nQmava 92,12,128\nQda 148,48,2048\nQmava 96,16,128\nQmava 100,0,132\n"
"Qda 152,48,3072\nQmava 104,4,132\nbuffer_load_dwordx4 v61, s[52:55], s95 offen offset:3072 lds\nQmava 108,8,132\nQda 156,48,4096\nv_accvgpr_read_b32 v208, a80\nQmava 176,12,132\nv_accvgpr_read_b32 v209, a81\n"
"v_accvgpr_read_b32 v210, a82\nQmava 180,16,132\nQda 160,48,5120\ns_add_u32 m0, s91, 136192\nQmava 184,0,136\nbuffer_load_dwordx4 v62, s[52:55], s95 offen lds\nQmava 188,4,136\nQda 164,48,6144\n"
"v_accvgpr_read_b32 v211, a83\nQmava 192,8,136\nv_accvgpr_read_b32 v240, a84\nv_accvgpr_read_b32 v241, a85\nQmava 196,12,136\nbuffer_load_dwordx4 v63, s[52:55], s95 offen offset:1024 lds\nv_add_u32 v75, 1, v75\nQmava 200,16,136\n"
"s_waitcnt vmcnt(22)\nds_write_b32 v73, v75\ns_waitcnt lgkmcnt(5)\nQmvva 92,20,140\ns_waitcnt vmcnt(14)\nv_sub_u32 v66, v78, v65\nv_cmp_gt_u32 vcc, 2, v66\ns_and_b64 vcc, vcc, s[28:29]\n"
"s_cbranch_scc1 .Lf2_w\n.Lf2_r:\nQmvva 124,24,140\nbuffer_store_dwordx4 v[200:203], v60, s[56:59], s88 offen offset:3072 sc1\nQmvva 156,28,140\nQtv 0,64,38912\nv_accvgpr_read_b32 v242, a86\nQmvva 188,32,140\n"
"Qtv 2,64,44032\nv_accvgpr_read_b32 v243, a87\nQmvva 220,36,140\nQtv 4,64,39168\nQmvva 252,20,144\nQmvva 100,24,144\nQtv 6,64,44288\nQmvva 132,28,144\n"
"ds_read_b32 v69, v74\nQmvva 164,32,144\nQtv 8,64,39424\nQmvva 196,36,144\nQtv 10,64,44544\nQmvva 228,20,148\nQmvva 108,24,148\ns_waitcnt lgkmcnt(2)\n"
"v_cmp_gt_u32 vcc, v75, v69\nQmvva 140,28,148\ns_cbranch_vccnz .Lso25\n.Lro25:\nQmvva 172,32,148\nQtv 12,64,39680\ns_add_u32 m0, s91, 0\ns_mul_i32 s94, s31, 31\n"
"Qmvva 204,36,148\nbuffer_load_dwordx4 v70, s[48:51], s94 offen lds\nQmvva 236,20,152\nQtv 14,64,44800\nQmvva 80,24,152\nQtv 16,64,39936\nQmvva 112,28,152\nQmvva 144,32,152\n"
"buffer_load_dwordx4 v71, s[48:51], s94 offen offset:1024 lds\nQmvva 176,36,152\nQtv 18,64,45056\nQmvva 208,20,156\nQda 112,48,24576\ns_lshr_b32 s68, %19, 2\nQmvva 240,24,156\nQda 116,48,25600\n"
"s_mul_i32 s95, s90, 31\ns_mul_i32 s68, s68, 917504\nQmava 88,28,156\nbuffer_load_dwordx4 v72, s[52:55], s95 offen offset:2048 lds\ns_mul_i32 s50, s92, 7168\nQmava 92,32,156\ns_mul_i32 s69, s93, 57344\nQmava 96,36,156\n"
"Qda 120,48,26624\ns_add_u32 s50, s50, s69\nQmava 100,20,160\nQda 124,48,27648\ns_add_u32 s68, s68, s50\nQmava 104,24,160\nbuffer_load_dwordx4 v61, s[52:55], s95 offen offset:3072 lds\ns_add_u32 s48, %8, s68\n"
"s_addc_u32 s49, %9, 0\nQmava 108,28,160\nQda 128,48,28672\ns_and_b32 s49, s49, 0xffff\nQmava 176,32,160\ns_sub_u32 s50, 917504, s50\nQmava 180,36,160\nQda 132,48,29696\n"
"s_add_u32 m0, s91, 4096\nQmava 184,20,164\nbuffer_load_dwordx4 v62, s[52:55], s95 offen lds\nQmava 188,24,164\nQda 136,48,30720\nv_accvgpr_read_b32 v84, a88\nQmava 192,28,164\nv_accvgpr_read_b32 v85, a89\n"
"v_accvgpr_read_b32 v86, a90\nQmava 196,32,164\nbuffer_load_dwordx4 v63, s[52:55], s95 offen offset:1024 lds\nv_add_u32 v75, 1, v75\nQmava 200,36,164\ns_waitcnt vmcnt(22)\nds_write_b32 v73, v75\ns_mov_b32 s52, s62\n"
"s_mov_b32 s53, s63\ns_mov_b32 s54, s86\ns_waitcnt lgkmcnt(5)\nQmvva 92,0,112\nQtv 20,59,6144\ns_add_u32 s88, s71, 47104\nQmvva 124,4,112\nbuffer_store_dwordx4 v[232:235], v60, s[56:59], s88 offen offset:0 sc1\n"
"s_mov_b32 s94, 458752\nQmvva 156,8,112\nbuffer_load_dwordx4 a[208:211], v60, s[48:51], s94 offen offset:0 sc1\nQmvva 188,12,112\nQtv 22,59,11264\nv_accvgpr_read_b32 v87, a91\nQmvva 220,16,112\nQtv 24,59,6400\n"
"v_accvgpr_read_b32 v88, a92\nQmvva 252,0,116\nQtv 26,59,11520\nv_accvgpr_read_b32 v89, a93\nQmvva 100,4,116\nv_accvgpr_read_b32 v90, a94\nv_accvgpr_read_b32 v91, a95\nQmvva 132,8,116\n"
"ds_read_b32 v69, v74\nQmvva 164,12,116\nQtv 28,59,6656\nQmvva 196,16,116\nQtv 30,59,11776\nQmvva 228,0,120\nQtv 32,59,6912\nv_add_u32 v67, 4, v65\n"
"Qmvva 108,4,120\ns_waitcnt lgkmcnt(3)\nv_cmp_gt_u32 vcc, v75, v69\nQmvva 140,8,120\ns_cbranch_vccnz .Lso26\n.Lro26:\nQmvva 172,12,120\nbuffer_load_dwordx4 a[212:215], v60, s[48:51], s94 offen offset:1024 sc1\n"
"Qmvva 204,16,120\nbuffer_store_dword v67, v77, s[24:27], 0 offen sc1\ns_add_u32 m0, s96, 24576\ns_mov_b32 s95, 1310720\nQmvva 236,0,124\nbuffer_load_dwordx4 v40, s[52:55], s95 offen lds\nQmvva 80,4,124\nQmvva 112,8,124\n"
"Qtv 34,59,12032\nQmvva 144,12,124\nQtv 36,59,7168\nQmvva 176,16,124\nQtv 38,59,12288\nQmvva 208,0,128\nQda 140,48,49152\nQmvva 240,4,128\n"
"buffer_load_dwordx4 a[216:219], v60, s[48:51], s94 offen offset:2048 sc1\nQmvva 84,8,128\nbuffer_load_dwordx4 v41, s[52:55], s95 offen offset:1024 lds\nQmvva 88,12,128\nQmava 96,16,128\nQda 144,48,50176\nQmava 100,0,132\nQda 148,48,51200\n"
"Qmava 104,4,132\nQda 152,48,52224\nQmava 108,8,132\nQmava 176,12,132\nQda 156,48,53248\nQmava 180,16,132\nbuffer_load_dwordx4 v42, s[52:55], s95 offen offset:2048 lds\nQmava 184,0,136\n"
"Qda 160,48,54272\nQmava 188,4,136\nbuffer_load_dwordx4 a[220:223], v60, s[48:51], s94 offen offset:3072 sc1\nQmava 192,8,136\nQda 164,48,55296\nv_accvgpr_read_b32 v120, a96\nQmava 196,12,136\nv_add_u32 v75, 1, v75\n"
"v_accvgpr_read_b32 v121, a97\nQmava 200,16,136\ns_waitcnt vmcnt(23)\nds_write_b32 v73, v75\ns_waitcnt lgkmcnt(5)\nQmvva 92,20,140\nQtv 0,59,30720\nv_accvgpr_read_b32 v122, a98\n"
"Qmvva 124,24,140\nbuffer_store_dwordx4 v[212:215], v60, s[56:59], s88 offen offset:1024 sc1\nQmvva 156,28,140\nbuffer_load_dwordx4 v43, s[52:55], s95 offen offset:3072 lds\nQmvva 188,32,140\nQtv 2,59,35840\nv_accvgpr_read_b32 v123, a99\nQmvva 220,36,140\n"
"Qtv 4,59,30976\nv_accvgpr_read_b32 v152, a100\nQmvva 252,20,144\nQtv 6,59,36096\ns_mov_b32 s94, 462848\nQmvva 100,24,144\nbuffer_load_dwordx4 a[224:227], v60, s[48:51], s94 offen offset:0 sc1\nQmvva 132,28,144\n"
"ds_read_b32 v69, v74\nv_accvgpr_read_b32 v153, a101\nQmvva 164,32,144\ns_add_u32 m0, s96, 28672\nv_accvgpr_read_b32 v154, a102\nQmvva 196,36,144\nbuffer_load_dwordx4 v44, s[52:55], s95 offen lds\nQmvva 228,20,148\n"
"Qtv 8,59,31232\nv_accvgpr_read_b32 v155, a103\nQmvva 108,24,148\ns_waitcnt lgkmcnt(1)\nv_cmp_gt_u32 vcc, v75, v69\nQmvva 140,28,148\ns_cbranch_vccnz .Lso27\n.Lro27:\n"
"Qmvva 172,32,148\nQtv 10,59,36352\nQmvva 204,36,148\nQtv 12,59,31488\nQmvva 236,20,152\nQmvva 80,24,152\nbuffer_load_dwordx4 a[228:231], v60, s[48:51], s94 offen offset:1024 sc1\ns_add_u32 m0, s96, 45056\n"
"s_mov_b32 s95, 1638400\nQmvva 112,28,152\nbuffer_load_dwordx4 v40, s[52:55], s95 offen lds\nQmvva 144,32,152\nQtv 14,59,36608\nQmvva 176,36,152\nQtv 16,59,31744\nQmvva 208,20,156\n"
"Qtv 18,59,36864\nQmvva 240,24,156\nQda 112,49,0\nQmvva 84,28,156\nQmvva 88,32,156\nQda 116,49,1024\nQmvva 120,36,156\nbuffer_load_dwordx4 v41, s[52:55], s95 offen offset:1024 lds\n"
"Qmvva 152,20,160\nQda 120,49,2048\nQmava 104,24,160\nbuffer_load_dwordx4 a[232:235], v60, s[48:51], s94 offen offset:2048 sc1\nQmava 108,28,160\nQda 124,49,3072\nQmava 176,32,160\nQmava 180,36,160\n"
"Qda 128,49,4096\nQmava 184,20,164\nQda 132,49,5120\nQmava 188,24,164\nbuffer_load_dwordx4 v42, s[52:55], s95 offen offset:2048 lds\nQmava 192,28,164\nQda 136,49,6144\nQmava 196,32,164\n"
"v_add_u32 v75, 1, v75\nv_accvgpr_read_b32 v184, a104\nQmava 200,36,164\ns_waitcnt vmcnt(25)\nds_write_b32 v73, v75\ns_waitcnt lgkmcnt(5)\nQmvva 92,0,112\nQtv 20,59,55296\n"
"v_accvgpr_read_b32 v185, a105\nQmvva 124,4,112\nbuffer_store_dwordx4 v[244:247], v60, s[56:59], s88 offen offset:2048 sc1\ns_mov_b32 s94, 573440\nQmvva 156,8,112\nbuffer_load_dwordx4 a[236:239], v60, s[48:51], s94 offen offset:0 sc1\nQmvva 188,12,112\nbuffer_load_dwordx4 v43, s[52:55], s95 offen offset:3072 lds\n"
"Qmvva 220,16,112\nQtv 22,59,60416\nv_accvgpr_read_b32 v186, a106\nQmvva 252,0,116\nQtv 24,59,55552\nv_accvgpr_read_b32 v187, a107\nQmvva 100,4,116\nQtv 26,59,60672\n"
"v_accvgpr_read_b32 v216, a108\nQmvva 132,8,116\nds_read_b32 v69, v74\nv_accvgpr_read_b32 v217, a109\nQmvva 164,12,116\nQtv 28,59,55808\nv_accvgpr_read_b32 v218, a110\nQmvva 196,16,116\n"
"s_add_u32 m0, s96, 49152\nv_accvgpr_read_b32 v219, a111\nQmvva 228,0,120\nbuffer_load_dwordx4 v44, s[52:55], s95 offen lds\nQmvva 108,4,120\ns_waitcnt lgkmcnt(1)\nv_cmp_gt_u32 vcc, v75, v69\nQmvva 140,8,120\n"
"s_cbranch_vccnz .Lso28\n.Lro28:\nQmvva 172,12,120\nbuffer_load_dwordx4 a[240:243], v60, s[48:51], s94 offen offset:1024 sc1\nQmvva 204,16,120\nQtv 30,59,60928\nQmvva 236,0,124\nQtv 32,59,56064\n"
"Qmvva 80,4,124\nQtv 34,59,61184\ns_add_u32 m0, s96, 65536\ns_mov_b32 s95, 1966080\nQmvva 112,8,124\nbuffer_load_dwordx4 v40, s[52:55], s95 offen lds\nQmvva 144,12,124\nQtv 36,59,56320\n"
"Qmvva 176,16,124\nQmvva 208,0,128\nQtv 38,59,61440\nQmvva 240,4,128\nbuffer_load_dwordx4 a[244:247], v60, s[48:51], s94 offen offset:2048 sc1\nQmvva 84,8,128\nQda 140,49,24576\nQmvva 88,12,128\n"
"Qda 144,49,25600\nQmvva 120,16,128\nbuffer_load_dwordx4 v41, s[52:55], s95 offen offset:1024 lds\nQmvva 152,0,132\nQda 148,49,26624\nQmvva 184,4,132\nQmvva 216,8,132\nQda 152,49,27648\n"
"Qmava 176,12,132\nQda 156,49,28672\nQmava 180,16,132\nQda 160,49,29696\nQmava 184,0,136\nQda 164,49,30720\nQmava 188,4,136\nbuffer_load_dwordx4 a[248:251], v60, s[48:51], s94 offen offset:3072 sc1\n"
"Qmava 192,8,136\nbuffer_load_dwordx4 v42, s[52:55], s95 offen offset:2048 lds\nQmava 196,12,136\nv_add_u32 v75, 1, v75\nQmava 200,16,136\ns_waitcnt vmcnt(28)\nds_write_b32 v73, v75\ns_waitcnt lgkmcnt(5)\n"
"Qmvva 92,20,140\nQtv 0,50,31744\nv_accvgpr_read_b32 v248, a176\nQmvva 124,24,140\nQtv 2,50,36864\nv_accvgpr_read_b32 v249, a177\nQmvva 156,28,140\nQtv 4,50,32000\n"
"v_accvgpr_read_b32 v250, a178\nQmvva 188,32,140\nv_accvgpr_read_b32 v251, a179\nv_accvgpr_read_b32 v116, a180\nQmvva 220,36,140\nbuffer_load_dwordx4 v43, s[52:55], s95 offen offset:3072 lds\nQmvva 252,20,144\nQtv 6,50,37120\n"
"s_mov_b32 s94, 577536\nQmvva 100,24,144\nbuffer_load_dwordx4 a[252:255], v60, s[48:51], s94 offen offset:0 sc1\nQmvva 132,28,144\nds_read_b32 v69, v74\nv_accvgpr_read_b32 v117, a181\nQmvva 164,32,144\nQtv 8,50,32256\n"
"v_accvgpr_read_b32 v118, a182\nQmvva 196,36,144\nv_accvgpr_read_b32 v119, a183\nQmvva 228,20,148\nQtv 10,50,37376\nQmvva 108,24,148\ns_waitcnt lgkmcnt(2)\nv_cmp_gt_u32 vcc, v75, v69\n"
"Qmvva 140,28,148\ns_add_u32 m0, s96, 69632\ns_cbranch_vccnz .Lso29\n.Lro29:\nQmvva 172,32,148\nbuffer_load_dwordx4 v44, s[52:55], s95 offen lds\nQmvva 204,36,148\nQtv 12,50,32512\n"
"Qmvva 236,20,152\nQtv 14,50,37632\nQmvva 80,24,152\nbuffer_load_dwordx4 a[168:171], v60, s[48:51], s94 offen offset:1024 sc1\nQmvva 112,28,152\nQmvva 144,32,152\nQtv 16,50,32768\nQmvva 176,36,152\n"
"Qtv 18,50,37888\ns_add_u32 m0, s96, 86016\ns_mov_b32 s95, 2293760\nQmvva 208,20,156\nbuffer_load_dwordx4 v40, s[52:55], s95 offen lds\nQmvva 240,24,156\nQmvva 84,28,156\nQda 112,49,58368\n"
"Qmvva 88,32,156\nQda 116,49,59392\nQmvva 120,36,156\nQda 120,49,60416\nQmvva 152,20,160\nQmvva 184,24,160\nbuffer_load_dwordx4 a[172:175], v60, s[48:51], s94 offen offset:2048 sc1\nQmvva 216,28,160\n"
"buffer_load_dwordx4 v41, s[52:55], s95 offen offset:1024 lds\nQmvva 248,32,160\nQda 124,49,61440\nQmvva 116,36,160\nQda 128,49,62464\nQmava 184,20,164\nQmava 188,24,164\nQda 132,49,63488\n"
"Qmava 192,28,164\nQda 136,49,64512\nQmava 196,32,164\nv_add_u32 v75, 1, v75\nQmava 200,36,164\ns_waitcnt vmcnt(22)\nds_write_b32 v73, v75\ns_waitcnt lgkmcnt(5)\n"
"Qmvva 92,0,112\nbuffer_load_dwordx4 v42, s[52:55], s95 offen offset:2048 lds\ns_mov_b32 s94, 688128\nQmvva 124,4,112\nbuffer_load_dwordx4 a[0:3], v60, s[48:51], s94 offen offset:0 sc1\nQmvva 156,8,112\nQtv 20,64,14336\nv_accvgpr_read_b32 v96, a184\n"
"Qmvva 188,12,112\nQtv 22,64,19456\nv_accvgpr_read_b32 v97, a185\nQmvva 220,16,112\nQtv 24,64,14592\nv_accvgpr_read_b32 v98, a186\nQmvva 252,0,116\nQtv 26,64,19712\n"
"v_accvgpr_read_b32 v99, a187\nQmvva 100,4,116\nQtv 28,64,14848\nv_accvgpr_read_b32 v128, a188\nQmvva 132,8,116\nds_read_b32 v69, v74\nv_accvgpr_read_b32 v129, a189\nQmvva 164,12,116\n"
"buffer_load_dwordx4 v43, s[52:55], s95 offen offset:3072 lds\nQmvva 196,16,116\nQtv 30,64,19968\nv_accvgpr_read_b32 v130, a190\nQmvva 228,0,120\nv_accvgpr_read_b32 v131, a191\nQmvva 108,4,120\ns_waitcnt lgkmcnt(1)\n"
"v_cmp_gt_u32 vcc, v75, v69\nQmvva 140,8,120\ns_cbranch_vccnz .Lso30\n.Lro30:\nQmvva 172,12,120\nbuffer_load_dwordx4 a[4:7], v60, s[48:51], s94 offen offset:1024 sc1\nQmvva 204,16,120\ns_waitcnt vmcnt(37)\n"
"ds_write_b128 v52, a[208:211] offset:0\nQmvva 236,0,124\ns_add_u32 m0, s96, 90112\nQmvva 80,4,124\nbuffer_load_dwordx4 v44, s[52:55], s95 offen lds\nQmvva 112,8,124\nQtv 32,64,15104\nQmvva 144,12,124\n"
"Qtv 34,64,20224\nQmvva 176,16,124\nQtv 36,64,15360\nQmvva 208,0,128\nQtv 38,64,20480\nQmvva 240,4,128\nbuffer_load_dwordx4 a[8:11], v60, s[48:51], s94 offen offset:2048 sc1\nQmvva 84,8,128\n"
"Qda 140,48,0\nQmvva 88,12,128\nQda 144,48,1024\nQmvva 120,16,128\ns_waitcnt vmcnt(38)\nds_write_b128 v52, a[212:215] offset:4096\nQmvva 152,0,132\nQmvva 184,4,132\n"
"Qmvva 216,8,132\nQda 148,48,2048\nQmvva 248,12,132\nQda 152,48,3072\nQmvva 116,16,132\nQda 156,48,4096\nQmvva 96,0,136\nQda 160,48,5120\n"
"Qmvva 128,4,136\nbuffer_load_dwordx4 a[12:15], v60, s[48:51], s94 offen offset:3072 sc1\nQmava 192,8,136\nQda 164,48,6144\nQmava 196,12,136\nv_add_u32 v75, 1, v75\nQmava 200,16,136\nds_write_b32 v73, v75\n"
"s_waitcnt lgkmcnt(5)\nQmvva 92,20,140\ns_waitcnt vmcnt(36)\nds_write_b128 v52, a[216:219] offset:8192\nQmvva 124,24,140\nQmvva 156,28,140\nbuffer_load_dword v78, v76, s[24:27], 0 offen sc1\nQmvva 188,32,140\n"
"Qtv 0,45,20480\nQmvva 220,36,140\nQtv 2,45,25600\nv_accvgpr_read_b32 v160, a192\nQmvva 252,20,144\nQtv 4,45,20736\ns_mov_b32 s94, 692224\nQmvva 100,24,144\n"
"buffer_load_dwordx4 a[16:19], v60, s[48:51], s94 offen offset:0 sc1\nQmvva 132,28,144\nds_read_b32 v69, v74\nv_accvgpr_read_b32 v161, a193\nQmvva 164,32,144\nQtv 6,45,25856\nv_accvgpr_read_b32 v162, a194\nQmvva 196,36,144\n"
"s_waitcnt vmcnt(35)\nds_write_b128 v52, a[220:223] offset:12288\nQmvva 228,20,148\nv_accvgpr_read_b32 v163, a195\nv_accvgpr_read_b32 v192, a196\nQmvva 108,24,148\ns_waitcnt lgkmcnt(2)\nv_cmp_gt_u32 vcc, v75, v69\n"
"Qmvva 140,28,148\ns_cbranch_vccnz .Lso31\n.Lro31:\nv_accvgpr_read_b32 v193, a197\nQmvva 172,32,148\ns_add_u32 m0, s96, 4096\ns_mov_b32 s95, 1351680\nv_accvgpr_read_b32 v194, a198\n"
"Qmvva 204,36,148\nbuffer_load_dwordx4 v40, s[52:55], s95 offen lds\nQmvva 236,20,152\nQtv 8,45,20992\nv_accvgpr_read_b32 v195, a199\nQmvva 80,24,152\nbuffer_load_dwordx4 a[20:23], v60, s[48:51], s94 offen offset:1024 sc1\nQmvva 112,28,152\n"
"Qtv 10,45,26112\nQmvva 144,32,152\nQtv 12,45,21248\nQmvva 176,36,152\ns_waitcnt vmcnt(34)\nds_write_b128 v52, a[224:227] offset:16384\nQmvva 208,20,156\nQmvva 240,24,156\n"
"buffer_load_dwordx4 v41, s[52:55], s95 offen offset:1024 lds\nQmvva 84,28,156\nQmvva 88,32,156\nQtv 14,45,26368\nQmvva 120,36,156\nQtv 16,45,21504\nQmvva 152,20,160\nQtv 18,45,26624\n"
"Qmvva 184,24,160\nbuffer_load_dwordx4 a[24:27], v60, s[48:51], s94 offen offset:2048 sc1\nQmvva 216,28,160\nQmvva 248,32,160\nbuffer_load_dwordx4 v42, s[52:55], s95 offen offset:2048 lds\nQmvva 116,36,160\ns_waitcnt vmcnt(35)\nds_write_b128 v52, a[228:231] offset:20480\n"
"Qmvva 96,20,164\nQmvva 128,24,164\nQmvva 160,28,164\nQmvva 192,32,164\nQda 48,53,0\nv_add_u32 v75, 1, v75\nQmava 200,36,164\ns_waitcnt vmcnt(27)\n"
"ds_write_b32 v73, v75\ns_waitcnt lgkmcnt(3)\nQmvva 92,0,208\nbuffer_load_dwordx4 v43, s[52:55], s95 offen offset:3072 lds\ns_mov_b32 s94, 802816\nQmvva 124,4,208\nbuffer_load_dwordx4 a[28:31], v60, s[48:51], s94 offen offset:0 sc1\nQmvva 156,8,208\n"
"Qtv 20,47,20480\nQmvva 188,12,208\nQtv 22,47,25600\nQmvva 220,16,208\ns_waitcnt vmcnt(34)\nds_write_b128 v52, a[232:235] offset:25600\nQmvva 252,0,212\nQmvva 100,4,212\n"
"Qtv 24,47,20736\nv_accvgpr_read_b32 v224, a200\nQmvva 132,8,212\nds_read_b32 v69, v74\nv_accvgpr_read_b32 v225, a201\nQmvva 164,12,212\nQda 52,53,4096\ns_add_u32 m0, s96, 8192\n"
"Qmvva 196,16,212\nbuffer_load_dwordx4 v44, s[52:55], s95 offen lds\nQmvva 228,0,216\nQtv 26,47,25856\nv_accvgpr_read_b32 v226, a202\nQmvva 108,4,216\ns_waitcnt lgkmcnt(2)\nv_cmp_gt_u32 vcc, v75, v69\n"
"Qmvva 140,8,216\ns_cbranch_vccnz .Lso32\n.Lro32:\nv_accvgpr_read_b32 v227, a203\nQmvva 172,12,216\nbuffer_load_dwordx4 a[32:35], v60, s[48:51], s94 offen offset:1024 sc1\nQmvva 204,16,216\ns_waitcnt vmcnt(33)\n"
"ds_write_b128 v52, a[236:239] offset:29696\nQmvva 236,0,220\nQmvva 80,4,220\nQtv 28,47,20992\nQmvva 112,8,220\nQmvva 144,12,220\nQda 56,53,8192\nQmvva 176,16,220\n"
"Qtv 30,47,26112\nQmvva 208,0,224\nQtv 32,47,21248\nQmvva 240,4,224\nbuffer_load_dwordx4 a[36:39], v60, s[48:51], s94 offen offset:2048 sc1\nQmvva 84,8,224\nQtv 34,47,26368\nQmvva 88,12,224\n"
"Qtv 36,47,21504\nQmvva 120,16,224\ns_waitcnt vmcnt(31)\nds_write_b128 v52, a[240:243] offset:33792\nQmvva 152,0,228\nQmvva 184,4,228\nQtv 38,47,26624\nQmvva 216,8,228\n"
"Qmvva 248,12,228\nQda 60,53,12288\nQmvva 116,16,228\nQmvva 96,0,232\nQmvva 128,4,232\nbuffer_load_dwordx4 a[40:43], v60, s[48:51], s94 offen offset:3072 sc1\nQmvva 160,8,232\nQmvva 192,12,232\n"
"v_add_u32 v75, 1, v75\nQmvva 224,16,232\ns_waitcnt vmcnt(36)\nds_write_b32 v73, v75\ns_waitcnt lgkmcnt(2)\nQmvva 92,20,48\ns_waitcnt vmcnt(30)\nds_write_b128 v52, a[244:247] offset:37888\n"
"Qmvva 124,24,48\nQmvva 156,28,48\ns_waitcnt vmcnt(12)\nv_sub_u32 v66, v78, v65\nv_cmp_gt_u32 vcc, 3, v66\ns_and_b64 vcc, vcc, s[28:29]\ns_cbranch_scc1 .Lf3_w\n.Lf3_r:\n"
"Qmvva 188,32,48\nQda 64,53,16384\nQmvva 220,36,48\nQtv 0,45,40960\nQmvva 252,20,52\nQtv 2,45,46080\ns_mov_b32 s94, 806912\nQmvva 100,24,52\n"
"buffer_load_dwordx4 a[44:47], v60, s[48:51], s94 offen offset:0 sc1\nQmvva 132,28,52\nds_read_b32 v69, v74\nQmvva 164,32,52\nQtv 4,45,41216\nQmvva 196,36,52\ns_waitcnt vmcnt(29)\nds_write_b128 v52, a[248:251] offset:41984\n"
"s_waitcnt lgkmcnt(15)\nQmvva 228,20,56\nv_add_u32 v67, 5, v65\nQmvva 108,24,56\ns_waitcnt lgkmcnt(2)\nv_cmp_gt_u32 vcc, v75, v69\nQmvva 140,28,56\ns_cbranch_vccnz .Lso33\n"
".Lro33:\nQmvva 172,32,56\nQda 68,53,20480\nQmvva 204,36,56\nbuffer_store_dword v67, v77, s[24:27], 0 offen sc1\ns_add_u32 m0, s96, 24576\ns_mov_b32 s95, 1679360\nQmvva 236,20,60\n"
"buffer_load_dwordx4 v40, s[52:55], s95 offen lds\nQmvva 80,24,60\nbuffer_load_dwordx4 a[208:211], v60, s[48:51], s94 offen offset:1024 sc1\nQmvva 112,28,60\nQtv 6,45,46336\nQmvva 144,32,60\nQtv 8,45,41472\nQmvva 176,36,60\n"
"s_waitcnt vmcnt(29)\nds_write_b128 v52, a[252:255] offset:46080\ns_waitcnt lgkmcnt(3)\nQmvva 208,20,64\nQmvva 240,24,64\nQtv 10,45,46592\nQmvva 84,28,64\nbuffer_load_dwordx4 v41, s[52:55], s95 offen offset:1024 lds\n"
"Qmvva 88,32,64\nQda 72,53,25600\nQmvva 120,36,64\nQtv 12,45,41728\nQmvva 152,20,68\nQtv 14,45,46848\nQmvva 184,24,68\nbuffer_load_dwordx4 a[212:215], v60, s[48:51], s94 offen offset:2048 sc1\n"
"Qmvva 216,28,68\nQtv 16,45,41984\nQmvva 248,32,68\nQtv 18,45,47104\nQmvva 116,36,68\ns_waitcnt vmcnt(29)\nds_write_b128 v52, a[168:171] offset:50176\ns_waitcnt lgkmcnt(5)\n"
"Qmvva 96,20,72\nQmvva 128,24,72\nbuffer_load_dwordx4 v42, s[52:55], s95 offen offset:2048 lds\nQmvva 160,28,72\nQmvva 192,32,72\nQda 76,53,29696\nv_add_u32 v75, 1, v75\nQmvva 224,36,72\n"
"s_waitcnt vmcnt(31)\nds_write_b32 v73, v75\ns_waitcnt lgkmcnt(3)\nQmvva 92,0,236\nQtv 20,47,40960\ns_mov_b32 s94, 473088\nQmvva 124,4,236\nbuffer_load_dwordx4 a[216:219], v60, s[48:51], s94 offen offset:0 sc1\n"
"Qmvva 156,8,236\nQtv 22,47,46080\nQmvva 188,12,236\nbuffer_load_dwordx4 v43, s[52:55], s95 offen offset:3072 lds\nQmvva 220,16,236\ns_waitcnt vmcnt(30)\nds_write_b128 v52, a[172:175] offset:0\nQmvva 252,0,240\n"
"Qmvva 100,4,240\nQtv 24,47,41216\nQmvva 132,8,240\nds_read_b32 v69, v74\nQmvva 164,12,240\nQda 80,53,33792\nQmvva 196,16,240\nQtv 26,47,46336\n"
"s_add_u32 m0, s96, 28672\nQmvva 228,0,244\nbuffer_load_dwordx4 v44, s[52:55], s95 offen lds\nQmvva 108,4,244\ns_waitcnt lgkmcnt(2)\nv_cmp_gt_u32 vcc, v75, v69\nQmvva 140,8,244\ns_cbranch_vccnz .Lso34\n"
".Lro34:\nQmvva 172,12,244\nbuffer_load_dwordx4 a[220:223], v60, s[48:51], s94 offen offset:1024 sc1\nQmvva 204,16,244\ns_waitcnt vmcnt(29)\nds_write_b128 v52, a[0:3] offset:4096\nQmvva 236,0,248\nQmvva 80,4,248\n"
"Qtv 28,47,41472\nQmvva 112,8,248\nQmvva 144,12,248\nQda 48,53,37888\nQmvva 176,16,248\nQtv 30,47,46592\nQmvva 208,0,252\nQtv 32,47,41728\n"
"Qmvva 240,4,252\nbuffer_load_dwordx4 a[224:227], v60, s[48:51], s94 offen offset:2048 sc1\nQmvva 84,8,252\nQtv 34,47,46848\nQmvva 88,12,252\nQtv 36,47,41984\nQmvva 120,16,252\ns_waitcnt vmcnt(28)\n"
"ds_write_b128 v52, a[4:7] offset:8192\nQmvva 152,0,168\nQmvva 184,4,168\nQtv 38,47,47104\nQmvva 216,8,168\nQmvva 248,12,168\nQda 52,53,41984\nQmvva 116,16,168\n"
"Qmvva 96,0,172\nQmvva 128,4,172\nbuffer_load_dwordx4 a[228:231], v60, s[48:51], s94 offen offset:3072 sc1\nQmvva 160,8,172\nQmvva 192,12,172\nv_add_u32 v75, 1, v75\nQmvva 224,16,172\nds_write_b32 v73, v75\n"
"s_waitcnt lgkmcnt(2)\nQmvva 92,20,76\ns_waitcnt vmcnt(27)\nds_write_b128 v52, a[8:11] offset:12288\nQmvva 124,24,76\nQmvva 156,28,76\nQta 112,46,0\nQmvva 188,32,76\n"
"Qda 56,53,46080\nQmvva 220,36,76\nQta 114,46,5120\nQmvva 252,20,80\nQta 116,46,256\ns_mov_b32 s94, 477184\nQmvva 100,24,80\nbuffer_load_dwordx4 a[232:235], v60, s[48:51], s94 offen offset:0 sc1\n"
"Qmvva 132,28,80\nds_read_b32 v69, v74\nQmvva 164,32,80\nQta 118,46,5376\nQmvva 196,36,80\ns_waitcnt vmcnt(27)\nds_write_b128 v52, a[12:15] offset:16384\ns_waitcnt lgkmcnt(15)\n"
"Qmvva 228,20,48\nQmvva 108,24,48\ns_waitcnt lgkmcnt(2)\nv_cmp_gt_u32 vcc, v75, v69\nQmvva 140,28,48\ns_cbranch_vccnz .Lso35\n.Lro35:\nQmvva 172,32,48\n"
"Qda 60,53,50176\ns_add_u32 m0, s96, 45056\ns_mov_b32 s95, 2007040\nQmvva 204,36,48\nbuffer_load_dwordx4 v40, s[52:55], s95 offen lds\nQmvva 236,20,52\nQta 120,46,512\nQmvva 80,24,52\n"
"buffer_load_dwordx4 a[236:239], v60, s[48:51], s94 offen offset:1024 sc1\nQmvva 112,28,52\nQta 122,46,5632\nQmvva 144,32,52\nQta 124,46,768\nQmvva 176,36,52\ns_waitcnt vmcnt(27)\nds_write_b128 v52, a[16:19] offset:20480\n"
"s_waitcnt lgkmcnt(4)\nQmvva 208,20,56\nQmvva 240,24,56\nbuffer_load_dwordx4 v41, s[52:55], s95 offen offset:1024 lds\nQmvva 84,28,56\nQta 126,46,5888\nQmvva 88,32,56\nQda 64,53,0\n"
"Qmvva 120,36,56\nQta 128,46,1024\nQmvva 152,20,60\nQta 130,46,6144\nQmvva 184,24,60\nbuffer_load_dwordx4 a[240:243], v60, s[48:51], s94 offen offset:2048 sc1\nQmvva 216,28,60\nQmvva 248,32,60\n"
"buffer_load_dwordx4 v42, s[52:55], s95 offen offset:2048 lds\nQmvva 116,36,60\ns_waitcnt vmcnt(28)\nds_write_b128 v52, a[20:23] offset:25600\ns_waitcnt lgkmcnt(3)\nQmvva 96,20,64\nQmvva 128,24,64\nQmvva 160,28,64\n"
"Qmvva 192,32,64\nQda 68,53,4096\nv_add_u32 v75, 1, v75\nQmvva 224,36,64\ns_waitcnt vmcnt(34)\nds_write_b32 v73, v75\ns_waitcnt lgkmcnt(3)\nQmvaa 92,112,0\n"
"buffer_load_dwordx4 v43, s[52:55], s95 offen offset:3072 lds\ns_mov_b32 s94, 587776\nQmvaa 124,116,0\nbuffer_load_dwordx4 a[244:247], v60, s[48:51], s94 offen offset:0 sc1\nQmvaa 156,120,0\nQta 132,51,0\nQmvaa 188,124,0\nQta 134,51,5120\n"
"Qmvaa 220,128,0\ns_waitcnt vmcnt(28)\nds_write_b128 v52, a[24:27] offset:29696\nQmvaa 252,112,4\nQmvaa 100,116,4\nQta 136,51,256\nQmvaa 132,120,4\nds_read_b32 v69, v74\n"
"Qmvaa 164,124,4\nQda 72,53,8192\ns_add_u32 m0, s96, 49152\nQmvaa 196,128,4\nbuffer_load_dwordx4 v44, s[52:55], s95 offen lds\nQmvaa 228,112,8\nQta 138,51,5376\nQmvaa 108,116,8\n"
"s_waitcnt lgkmcnt(2)\nv_cmp_gt_u32 vcc, v75, v69\nQmvaa 140,120,8\ns_cbranch_vccnz .Lso36\n.Lro36:\nQmvaa 172,124,8\nbuffer_load_dwordx4 a[248:251], v60, s[48:51], s94 offen offset:1024 sc1\nQmvaa 204,128,8\n"
"s_waitcnt vmcnt(27)\nds_write_b128 v52, a[28:31] offset:33792\nQmvaa 236,112,12\nQmvaa 80,116,12\nQta 140,51,512\nQmvaa 112,120,12\nQmvaa 144,124,12\nQda 76,53,12288\n"
"Qmvaa 176,128,12\nQta 142,51,5632\nQmvaa 208,112,16\nQta 144,51,768\nQmvaa 240,116,16\nbuffer_load_dwordx4 a[252:255], v60, s[48:51], s94 offen offset:2048 sc1\nQmvaa 84,120,16\nQta 146,51,5888\n"
"Qmvaa 88,124,16\nQta 148,51,1024\nQmvaa 120,128,16\ns_waitcnt vmcnt(26)\nds_write_b128 v52, a[32:35] offset:37888\nQmvaa 152,112,20\nQmvaa 184,116,20\nQta 150,51,6144\n"
"Qmvaa 216,120,20\nQmvaa 248,124,20\nQda 80,53,16384\nQmvaa 116,128,20\nQmvaa 96,112,24\nQmvaa 128,116,24\nbuffer_load_dwordx4 a[168:171], v60, s[48:51], s94 offen offset:3072 sc1\nQmvaa 160,120,24\n"
"Qmvaa 192,124,24\nv_add_u32 v75, 1, v75\nQmvaa 224,128,24\nds_write_b32 v73, v75\ns_waitcnt lgkmcnt(2)\nQmvaa 92,132,68\ns_waitcnt vmcnt(26)\nds_write_b128 v52, a[36:39] offset:41984\n"
"Qmvaa 124,136,68\nQmvaa 156,140,68\nQtv 0,46,20480\nQmvaa 188,144,68\nQda 48,53,20480\nQmvaa 220,148,68\nQtv 2,46,25600\nQmvaa 252,132,72\n"
"Qtv 4,46,20736\ns_mov_b32 s94, 591872\nQmvaa 100,136,72\nbuffer_load_dwordx4 a[172:175], v60, s[48:51], s94 offen offset:0 sc1\nQmvaa 132,140,72\nds_read_b32 v69, v74\ns_mov_b32 s40, %17\nQmvaa 164,144,72\n"
"Qtv 6,46,25856\ns_and_b32 s41, %18, 0xffff\nQmvaa 196,148,72\ns_waitcnt vmcnt(26)\nds_write_b128 v52, a[40:43] offset:46080\ns_waitcnt lgkmcnt(15)\nQmvaa 228,132,76\ns_mov_b32 s42, 571200\n"
"s_lshr_b32 s68, %19, 2\nQmvaa 108,136,76\ns_waitcnt lgkmcnt(2)\nv_cmp_gt_u32 vcc, v75, v69\nQmvaa 140,140,76\ns_cbranch_vccnz .Lso37\n.Lro37:\ns_mul_i32 s65, s68, 286720\n"
"Qmvaa 172,144,76\nQda 52,53,25600\ns_add_u32 m0, s96, 65536\ns_mov_b32 s95, 2334720\nQmvaa 204,148,76\nbuffer_load_dwordx4 v40, s[52:55], s95 offen lds\nQmvaa 236,132,80\nQtv 8,46,20992\n"
"s_sub_u32 s60, 286720, s65\nQmvaa 80,136,80\nbuffer_load_dwordx4 a[0:3], v60, s[48:51], s94 offen offset:1024 sc1\nQmvaa 112,140,80\nQtv 10,46,26112\ns_mul_i32 s68, s68, 114688\nQmvaa 144,144,80\nQtv 12,46,21248\n"
"s_add_u32 s64, s98, 57344\nQmvaa 176,148,80\ns_waitcnt vmcnt(27)\nds_write_b128 v52, a[44:47] offset:50176\ns_waitcnt lgkmcnt(4)\nQmvaa 208,132,48\ns_sub_u32 s64, s64, s68\ns_mul_i32 s68, s93, 160\n"
"Qmvaa 240,136,48\nbuffer_load_dwordx4 v41, s[52:55], s95 offen offset:1024 lds\nQmvaa 84,140,48\nQtv 14,46,26368\ns_add_u32 s68, s68, %10\nQmvaa 88,144,48\nQda 56,53,29696\nv_bfe_u32 v66, v60, 4, 4\n"
"Qmvaa 120,148,48\nQtv 16,46,21504\nv_mul_u32_u24 v66, 336, v66\nQmvaa 152,132,52\nQtv 18,46,26624\nv_lshrrev_b32 v67, 8, v60\nQmvaa 184,136,52\nbuffer_load_dwordx4 a[4:7], v60, s[48:51], s94 offen offset:2048 sc1\n"
"Qmvaa 216,140,52\nv_lshlrev_b32 v68, 3, v67\nv_add3_u32 v61, v66, v68, s68\nQmvaa 248,144,52\nbuffer_load_dwordx4 v42, s[52:55], s95 offen offset:2048 lds\nQmvaa 116,148,52\ns_waitcnt vmcnt(27)\nds_write_b128 v52, a[208:211] offset:0\n"
"s_waitcnt lgkmcnt(3)\nQmvaa 96,132,56\ns_lshl_b32 s68, s70, 2\nv_add_u32 v67, s68, v67\nQmvaa 128,136,56\ns_and_b32 s69, %19, 1\ns_lshl_b32 s68, s69, 6\nQmvaa 160,140,56\n"
"v_and_b32 v66, 255, v60\nv_add_u32 v66, s68, v66\nQmvaa 192,144,56\nQda 60,53,33792\nv_add_u32 v75, 1, v75\nQmvaa 224,148,56\ns_waitcnt vmcnt(34)\nds_write_b32 v73, v75\n"
"s_waitcnt lgkmcnt(3)\nQmvva 92,0,28\nbuffer_load_dwordx4 v43, s[52:55], s95 offen offset:3072 lds\ns_mov_b32 s94, 702464\nQmvva 124,4,28\nbuffer_load_dwordx4 a[8:11], v60, s[48:51], s94 offen offset:0 sc1\nQmvva 156,8,28\nQtv 20,51,20480\n"
"v_mul_u32_u24 v68, 336, v67\nQmvva 188,12,28\nQtv 22,51,25600\nv_add3_u32 v62, v66, v68, %10\nQmvva 220,16,28\ns_waitcnt vmcnt(27)\nds_write_b128 v52, a[212:215] offset:4096\nQmvva 252,0,32\n"
"v_mul_u32_u24 v68, 2560, v67\nv_add_u32 v63, v66, v68\nQmvva 100,4,32\nQtv 24,51,20736\ns_lshl_b32 s68, s70, 4\nQmvva 132,8,32\nds_read_b32 v69, v74\nv_lshrrev_b32 v67, 6, v60\n"
"Qmvva 164,12,32\nQda 64,53,37888\ns_add_u32 m0, s96, 69632\nQmvva 196,16,32\nbuffer_load_dwordx4 v44, s[52:55], s95 offen lds\nQmvva 228,0,36\nQtv 26,51,25856\nv_add_u32 v67, s68, v67\n"
"Qmvva 108,4,36\ns_waitcnt lgkmcnt(2)\nv_cmp_gt_u32 vcc, v75, v69\nQmvva 140,8,36\ns_cbranch_vccnz .Lso38\n.Lro38:\ns_xor_b32 s69, s69, 1\nQmvva 172,12,36\n"
"buffer_load_dwordx4 a[12:15], v60, s[48:51], s94 offen offset:1024 sc1\nQmvva 204,16,36\ns_waitcnt vmcnt(27)\nds_write_b128 v52, a[216:219] offset:8192\nQmvva 236,0,40\ns_lshl_b32 s68, s69, 8\nv_and_b32 v66, 63, v60\nQmvva 80,4,40\n"
"Qtv 28,51,20992\nv_add_u32 v66, s68, v66\nQmvva 112,8,40\nv_mul_u32_u24 v68, 336, v67\nv_add3_u32 v70, v66, v68, %10\nQmvva 144,12,40\nQda 68,53,41984\nv_mul_u32_u24 v68, 2560, v67\n"
"Qmvva 176,16,40\nQtv 30,51,26112\nv_add_u32 v71, v66, v68\nQmvva 208,0,44\nQtv 32,51,21248\nQmvva 240,4,44\nbuffer_load_dwordx4 a[16:19], v60, s[48:51], s94 offen offset:2048 sc1\nQmvva 84,8,44\n"
"Qtv 34,51,26368\nQmvva 88,12,44\nQtv 36,51,21504\nQmvva 120,16,44\ns_waitcnt vmcnt(25)\nds_write_b128 v52, a[220:223] offset:12288\nQmvva 152,0,208\nQmvva 184,4,208\n"
"Qtv 38,51,26624\nQmvva 216,8,208\nQmvva 248,12,208\nQda 72,53,46080\nQmvva 116,16,208\nQmvva 96,0,212\nQmvva 128,4,212\nbuffer_load_dwordx4 a[20:23], v60, s[48:51], s94 offen offset:3072 sc1\n"
"Qmvva 160,8,212\nQmvva 192,12,212\nv_add_u32 v75, 1, v75\nQmvva 224,16,212\nds_write_b32 v73, v75\ns_waitcnt lgkmcnt(2)\nQmvva 92,20,60\ns_waitcnt vmcnt(25)\n"
"ds_write_b128 v52, a[224:227] offset:16384\nQmvva 124,24,60\nQmvva 156,28,60\nbuffer_load_dword v78, v76, s[24:27], 0 offen sc1\nQmvva 188,32,60\nQda 76,53,50176\nQmvva 220,36,60\nQta 112,45,0\n"
"Qmvva 252,20,64\nQta 114,45,5120\ns_mov_b32 s94, 706560\nQmvva 100,24,64\nbuffer_load_dwordx4 a[24:27], v60, s[48:51], s94 offen offset:0 sc1\nQmvva 132,28,64\nds_read_b32 v69, v74\nQmvva 164,32,64\n"
"Qta 116,45,256\nQmvva 196,36,64\ns_waitcnt vmcnt(26)\nds_write_b128 v52, a[228:231] offset:20480\ns_waitcnt lgkmcnt(15)\nQmvva 228,20,68\nQmvva 108,24,68\ns_waitcnt lgkmcnt(2)\n"
"v_cmp_gt_u32 vcc, v75, v69\nQmvva 140,28,68\ns_cbranch_vccnz .Lso39\n.Lro39:\nQmvva 172,32,68\nQda 80,53,0\ns_add_u32 m0, s96, 86016\ns_mov_b32 s95, 1392640\n"
"Qmvva 204,36,68\nbuffer_load_dwordx4 v40, s[52:55], s95 offen lds\nQmvva 236,20,72\nQta 118,45,5376\nQmvva 80,24,72\nbuffer_load_dwordx4 a[28:31], v60, s[48:51], s94 offen offset:1024 sc1\nQmvva 112,28,72\nQta 120,45,512\n"
"Qmvva 144,32,72\nQta 122,45,5632\nQmvva 176,36,72\ns_waitcnt vmcnt(27)\nds_write_b128 v52, a[232:235] offset:25600\ns_waitcnt lgkmcnt(4)\nQmvva 208,20,76\nQmvva 240,24,76\n"
"buffer_load_dwordx4 v41, s[52:55], s95 offen offset:1024 lds\nQmvva 84,28,76\nQta 124,45,768\nQmvva 88,32,76\nQda 48,53,4096\nQmvva 120,36,76\nQta 126,45,5888\nQmvva 152,20,80\n"
"Qta 128,45,1024\nQmvva 184,24,80\nbuffer_load_dwordx4 a[32:35], v60, s[48:51], s94 offen offset:2048 sc1\nQmvva 216,28,80\nQta 130,45,6144\nQmvva 248,32,80\nbuffer_load_dwordx4 v42, s[52:55], s95 offen offset:2048 lds\nQmvva 116,36,80\n"
"s_waitcnt vmcnt(28)\nds_write_b128 v52, a[236:239] offset:29696\ns_waitcnt lgkmcnt(4)\nQmvva 96,20,48\nQmvva 128,24,48\nQmvva 160,28,48\nQmvva 192,32,48\nQda 52,53,8192\n"
"v_add_u32 v75, 1, v75\nQmvva 224,36,48\ns_waitcnt vmcnt(34)\nds_write_b32 v73, v75\ns_waitcnt lgkmcnt(3)\nQmvaa 92,112,216\nbuffer_load_dwordx4 v43, s[52:55], s95 offen offset:3072 lds\ns_mov_b32 s94, 817152\n"
"Qmvaa 124,116,216\nbuffer_load_dwordx4 a[36:39], v60, s[48:51], s94 offen offset:0 sc1\nQmvaa 156,120,216\nQta 132,47,0\nQmvaa 188,124,216\nQta 134,47,5120\nQmvaa 220,128,216\ns_waitcnt vmcnt(28)\n"
"ds_write_b128 v52, a[240:243] offset:33792\nQmvaa 252,112,220\nQmvaa 100,116,220\nQta 136,47,256\nQmvaa 132,120,220\nds_read_b32 v69, v74\nQmvaa 164,124,220\nQda 56,53,12288\n"
"s_add_u32 m0, s96, 90112\nQmvaa 196,128,220\nbuffer_load_dwordx4 v44, s[52:55], s95 offen lds\nQmvaa 228,112,224\nQta 138,47,5376\nQmvaa 108,116,224\ns_waitcnt lgkmcnt(2)\nv_cmp_gt_u32 vcc, v75, v69\n"
"Qmvaa 140,120,224\ns_cbranch_vccnz .Lso40\n.Lro40:\nQmvaa 172,124,224\nbuffer_load_dwordx4 a[40:43], v60, s[48:51], s94 offen offset:1024 sc1\nQmvaa 204,128,224\ns_waitcnt vmcnt(27)\nds_write_b128 v52, a[244:247] offset:37888\n"
"Qmvaa 236,112,228\nQmvaa 80,116,228\nQta 140,47,512\nQmvaa 112,120,228\nQmvaa 144,124,228\nQda 60,53,16384\nQmvaa 176,128,228\nQta 142,47,5632\n"
"Qmvaa 208,112,232\nQta 144,47,768\nQmvaa 240,116,232\nbuffer_load_dwordx4 a[44:47], v60, s[48:51], s94 offen offset:2048 sc1\nQmvaa 84,120,232\nQta 146,47,5888\nQmvaa 88,124,232\nQta 148,47,1024\n"
"Qmvaa 120,128,232\ns_waitcnt vmcnt(26)\nds_write_b128 v52, a[248:251] offset:41984\nQmvaa 152,112,236\nQmvaa 184,116,236\nQta 150,47,6144\nQmvaa 216,120,236\nQmvaa 248,124,236\n"
"Qda 64,53,20480\nQmvaa 116,128,236\nQmvaa 96,112,240\nQmvaa 128,116,240\nbuffer_load_dwordx4 a[208:211], v60, s[48:51], s94 offen offset:3072 sc1\nQmvaa 160,120,240\nQmvaa 192,124,240\nv_add_u32 v75, 1, v75\n"
"Qmvaa 224,128,240\nds_write_b32 v73, v75\ns_waitcnt lgkmcnt(2)\nQmvaa 92,132,52\ns_waitcnt vmcnt(26)\nds_write_b128 v52, a[252:255] offset:46080\nQmvaa 124,136,52\nQmvaa 156,140,52\n"
"s_waitcnt vmcnt(12)\nv_sub_u32 v66, v78, v65\nv_cmp_gt_u32 vcc, 4, v66\ns_and_b64 vcc, vcc, s[28:29]\ns_cbranch_scc1 .Lf4_w\n.Lf4_r:\nQmvaa 188,144,52\nQda 68,53,25600\n"
"Qmvaa 220,148,52\nQtv 0,45,20480\nQmvaa 252,132,56\nQtv 2,45,25600\ns_mov_b32 s94, 821248\nQmvaa 100,136,56\nbuffer_load_dwordx4 a[212:215], v60, s[48:51], s94 offen offset:0 sc1\nQmvaa 132,140,56\n"
"ds_read_b32 v69, v74\nQmvaa 164,144,56\nQtv 4,45,20736\nQmvaa 196,148,56\ns_waitcnt vmcnt(26)\nds_write_b128 v52, a[168:171] offset:50176\ns_waitcnt lgkmcnt(15)\nQmvaa 228,132,60\n"
"Qmvaa 108,136,60\ns_waitcnt lgkmcnt(2)\nv_cmp_gt_u32 vcc, v75, v69\nQmvaa 140,140,60\ns_cbranch_vccnz .Lso41\n.Lro41:\nQmvaa 172,144,60\nQda 72,53,29696\n"
"s_add_u32 m0, s96, 4096\ns_mov_b32 s95, 1720320\nQmvaa 204,148,60\nbuffer_load_dwordx4 v40, s[52:55], s95 offen lds\nQmvaa 236,132,64\nQtv 6,45,25856\nQmvaa 80,136,64\nbuffer_load_dwordx4 a[216:219], v60, s[48:51], s94 offen offset:1024 sc1\n"
"Qmvaa 112,140,64\nQtv 8,45,20992\nQmvaa 144,144,64\nQtv 10,45,26112\nQmvaa 176,148,64\ns_waitcnt vmcnt(27)\nds_write_b128 v52, a[172:175] offset:0\ns_waitcnt lgkmcnt(4)\n"
"Qmvaa 208,132,68\nQmvaa 240,136,68\nbuffer_load_dwordx4 v41, s[52:55], s95 offen offset:1024 lds\nQmvaa 84,140,68\nQtv 12,45,21248\nQmvaa 88,144,68\nQda 76,53,33792\nQmvaa 120,148,68\n"
"Qtv 14,45,26368\nQmvaa 152,132,72\nQtv 16,45,21504\nQmvaa 184,136,72\nbuffer_load_dwordx4 a[220:223], v60, s[48:51], s94 offen offset:2048 sc1\nQmvaa 216,140,72\nQtv 18,45,26624\nQmvaa 248,144,72\n"
"buffer_load_dwordx4 v42, s[52:55], s95 offen offset:2048 lds\nQmvaa 116,148,72\ns_waitcnt vmcnt(28)\nds_write_b128 v52, a[0:3] offset:4096\ns_waitcnt lgkmcnt(4)\nQmvaa 96,132,76\nQmvaa 128,136,76\nQmvaa 160,140,76\n"
"Qmvaa 192,144,76\nQda 80,53,37888\nv_add_u32 v75, 1, v75\nQmvaa 224,148,76\ns_waitcnt vmcnt(34)\nds_write_b32 v73, v75\ns_waitcnt lgkmcnt(3)\nQmvva 92,0,244\n"
"buffer_load_dwordx4 v43, s[52:55], s95 offen offset:3072 lds\ns_mov_b32 s94, 487424\nQmvva 124,4,244\nbuffer_load_dwordx4 a[224:227], v60, s[48:51], s94 offen offset:0 sc1\nQmvva 156,8,244\nQtv 20,47,20480\nQmvva 188,12,244\nQtv 22,47,25600\n"
"Qmvva 220,16,244\ns_waitcnt vmcnt(28)\nds_write_b128 v52, a[4:7] offset:8192\nQmvva 252,0,248\nQmvva 100,4,248\nQtv 24,47,20736\nQmvva 132,8,248\nds_read_b32 v69, v74\n"
"Qmvva 164,12,248\nQda 48,53,41984\ns_add_u32 m0, s96, 8192\nQmvva 196,16,248\nbuffer_load_dwordx4 v44, s[52:55], s95 offen lds\nQmvva 228,0,252\nQtv 26,47,25856\nQmvva 108,4,252\n"
"s_waitcnt lgkmcnt(2)\nv_cmp_gt_u32 vcc, v75, v69\nQmvva 140,8,252\ns_cbranch_vccnz .Lso42\n.Lro42:\nQmvva 172,12,252\nbuffer_load_dwordx4 a[228:231], v60, s[48:51], s94 offen offset:1024 sc1\nQmvva 204,16,252\n"
"s_waitcnt vmcnt(27)\nds_write_b128 v52, a[8:11] offset:12288\nQmvva 236,0,168\nQmvva 80,4,168\nQtv 28,47,20992\nQmvva 112,8,168\nQmvva 144,12,168\nQda 52,53,46080\n"
"Qmvva 176,16,168\nQtv 30,47,26112\nQmvva 208,0,172\nQtv 32,47,21248\nQmvva 240,4,172\nbuffer_load_dwordx4 a[232:235], v60, s[48:51], s94 offen offset:2048 sc1\nQmvva 84,8,172\nQtv 34,47,26368\n"
"Qmvva 88,12,172\nQtv 36,47,21504\nQmvva 120,16,172\ns_waitcnt vmcnt(26)\nds_write_b128 v52, a[12:15] offset:16384\nQmvva 152,0,0\nQmvva 184,4,0\nQtv 38,47,26624\n"
"Qmvva 216,8,0\nQmvva 248,12,0\nQda 56,53,50176\nQmvva 116,16,0\nQmvva 96,0,4\nQmvva 128,4,4\nbuffer_load_dwordx4 a[236:239], v60, s[48:51], s94 offen offset:3072 sc1\nQmvva 160,8,4\n"
"Qmvva 192,12,4\nv_add_u32 v75, 1, v75\nQmvva 224,16,4\nds_write_b32 v73, v75\ns_waitcnt lgkmcnt(2)\nQmvva 92,20,80\ns_waitcnt vmcnt(26)\nds_write_b128 v52, a[16:19] offset:20480\n"
"Qmvva 124,24,80\nQmvva 156,28,80\nQta 112,45,40960\nQmvva 188,32,80\nQda 60,53,0\nQmvva 220,36,80\nQta 114,45,46080\nQmvva 252,20,48\n"
"Qta 116,45,41216\ns_mov_b32 s94, 491520\nQmvva 100,24,48\nbuffer_load_dwordx4 a[240:243], v60, s[48:51], s94 offen offset:0 sc1\nQmvva 132,28,48\nds_read_b32 v69, v74\nQmvva 164,32,48\nQta 118,45,46336\n"
"Qmvva 196,36,48\ns_waitcnt vmcnt(26)\nds_write_b128 v52, a[20:23] offset:25600\ns_waitcnt lgkmcnt(15)\nQmvva 228,20,52\nQmvva 108,24,52\ns_waitcnt lgkmcnt(2)\nv_cmp_gt_u32 vcc, v75, v69\n"
"Qmvva 140,28,52\ns_cbranch_vccnz .Lso43\n.Lro43:\nQmvva 172,32,52\nQda 64,53,4096\ns_add_u32 m0, s96, 24576\ns_mov_b32 s95, 2048000\nQmvva 204,36,52\n"
"buffer_load_dwordx4 v40, s[52:55], s95 offen lds\nQmvva 236,20,56\nQta 120,45,41472\nQmvva 80,24,56\nbuffer_load_dwordx4 a[244:247], v60, s[48:51], s94 offen offset:1024 sc1\nQmvva 112,28,56\nQta 122,45,46592\nQmvva 144,32,56\n"
"Qta 124,45,41728\nQmvva 176,36,56\ns_waitcnt vmcnt(26)\nds_write_b128 v52, a[24:27] offset:29696\ns_waitcnt lgkmcnt(4)\nQmvva 208,20,60\nQmvva 240,24,60\nbuffer_load_dwordx4 v41, s[52:55], s95 offen offset:1024 lds\n"
"Qmvva 84,28,60\nQta 126,45,46848\nQmvva 88,32,60\nQda 68,53,8192\nQmvva 120,36,60\nQta 128,45,41984\nQmvva 152,20,64\nQta 130,45,47104\n"
"Qmvva 184,24,64\nbuffer_load_dwordx4 a[248:251], v60, s[48:51], s94 offen offset:2048 sc1\nQmvva 216,28,64\nQmvva 248,32,64\nbuffer_load_dwordx4 v42, s[52:55], s95 offen offset:2048 lds\nQmvva 116,36,64\ns_waitcnt vmcnt(27)\nds_write_b128 v52, a[28:31] offset:33792\n"
"s_waitcnt lgkmcnt(3)\nQmvva 96,20,68\nQmvva 128,24,68\nQmvva 160,28,68\nQmvva 192,32,68\nQda 72,53,12288\nv_add_u32 v75, 1, v75\nQmvva 224,36,68\n"
"s_waitcnt vmcnt(34)\nds_write_b32 v73, v75\ns_waitcnt lgkmcnt(3)\nQmvaa 92,112,8\nbuffer_load_dwordx4 v43, s[52:55], s95 offen offset:3072 lds\ns_mov_b32 s94, 602112\nQmvaa 124,116,8\nbuffer_load_dwordx4 a[252:255], v60, s[48:51], s94 offen offset:0 sc1\n"
"Qmvaa 156,120,8\nQta 132,47,40960\nQmvaa 188,124,8\nQta 134,47,46080\nQmvaa 220,128,8\ns_waitcnt vmcnt(27)\nds_write_b128 v52, a[32:35] offset:37888\nQmvaa 252,112,12\n"
"Qmvaa 100,116,12\nQta 136,47,41216\nQmvaa 132,120,12\nds_read_b32 v69, v74\nQmvaa 164,124,12\nQda 76,53,16384\ns_add_u32 m0, s96, 28672\nQmvaa 196,128,12\n"
"buffer_load_dwordx4 v44, s[52:55], s95 offen lds\nQmvaa 228,112,16\nQta 138,47,46336\nQmvaa 108,116,16\ns_waitcnt lgkmcnt(2)\nv_cmp_gt_u32 vcc, v75, v69\nQmvaa 140,120,16\ns_cbranch_vccnz .Lso44\n"
".Lro44:\nQmvaa 172,124,16\nbuffer_load_dwordx4 a[168:171], v60, s[48:51], s94 offen offset:1024 sc1\nQmvaa 204,128,16\ns_waitcnt vmcnt(26)\nds_write_b128 v52, a[36:39] offset:41984\nQmvaa 236,112,20\nQmvaa 80,116,20\n"
"Qta 140,47,41472\nQmvaa 112,120,20\nQmvaa 144,124,20\nQda 80,53,20480\nQmvaa 176,128,20\nQta 142,47,46592\nQmvaa 208,112,24\nQta 144,47,41728\n"
"Qmvaa 240,116,24\nbuffer_load_dwordx4 a[172:175], v60, s[48:51], s94 offen offset:2048 sc1\nQmvaa 84,120,24\nQta 146,47,46848\nQmvaa 88,124,24\nQta 148,47,41984\nQmvaa 120,128,24\ns_waitcnt vmcnt(25)\n"
"ds_write_b128 v52, a[40:43] offset:46080\nQmvaa 152,112,28\nQmvaa 184,116,28\nQta 150,47,47104\nQmvaa 216,120,28\nQmvaa 248,124,28\nQda 48,53,25600\nQmvaa 116,128,28\n"
"Qmvaa 96,112,32\nQmvaa 128,116,32\nbuffer_load_dwordx4 a[0:3], v60, s[48:51], s94 offen offset:3072 sc1\nQmvaa 160,120,32\nQmvaa 192,124,32\nv_add_u32 v75, 1, v75\nQmvaa 224,128,32\nds_write_b32 v73, v75\n"
"s_waitcnt lgkmcnt(2)\nQmvaa 92,132,72\ns_waitcnt vmcnt(25)\nds_write_b128 v52, a[44:47] offset:50176\nQmvaa 124,136,72\nQmvaa 156,140,72\nQtv 0,46,0\nQmvaa 188,144,72\n"
"Qda 52,53,29696\nQmvaa 220,148,72\nQtv 2,46,5120\nQmvaa 252,132,76\nQtv 4,46,256\ns_mov_b32 s94, 606208\nQmvaa 100,136,76\nbuffer_load_dwordx4 a[4:7], v60, s[48:51], s94 offen offset:0 sc1\n"
"Qmvaa 132,140,76\nds_read_b32 v69, v74\nQmvaa 164,144,76\nQtv 6,46,5376\nQmvaa 196,148,76\ns_waitcnt vmcnt(25)\nds_write_b128 v52, a[208:211] offset:0\ns_waitcnt lgkmcnt(15)\n"
"Qmvaa 228,132,80\nQmvaa 108,136,80\ns_waitcnt lgkmcnt(2)\nv_cmp_gt_u32 vcc, v75, v69\nQmvaa 140,140,80\ns_cbranch_vccnz .Lso45\n.Lro45:\nQmvaa 172,144,80\n"
"Qda 56,53,33792\ns_add_u32 m0, s96, 45056\ns_mov_b32 s95, 2375680\nQmvaa 204,148,80\nbuffer_load_dwordx4 v40, s[52:55], s95 offen lds\nQmvaa 236,132,48\nQtv 8,46,512\nQmvaa 80,136,48\n"
"buffer_load_dwordx4 a[8:11], v60, s[48:51], s94 offen offset:1024 sc1\nQmvaa 112,140,48\nQtv 10,46,5632\nQmvaa 144,144,48\nQtv 12,46,768\nQmvaa 176,148,48\ns_waitcnt vmcnt(26)\nds_write_b128 v52, a[212:215] offset:4096\n"
"s_waitcnt lgkmcnt(4)\nQmvaa 208,132,52\nQmvaa 240,136,52\nbuffer_load_dwordx4 v41, s[52:55], s95 offen offset:1024 lds\nQmvaa 84,140,52\nQtv 14,46,5888\nQmvaa 88,144,52\nQda 60,53,37888\n"
"Qmvaa 120,148,52\nQtv 16,46,1024\nQmvaa 152,132,56\nQtv 18,46,6144\nQmvaa 184,136,56\nbuffer_load_dwordx4 a[12:15], v60, s[48:51], s94 offen offset:2048 sc1\nQmvaa 216,140,56\nQmvaa 248,144,56\n"
"buffer_load_dwordx4 v42, s[52:55], s95 offen offset:2048 lds\nQmvaa 116,148,56\ns_waitcnt vmcnt(27)\nds_write_b128 v52, a[216:219] offset:8192\ns_waitcnt lgkmcnt(3)\nQmvaa 96,132,60\nQmvaa 128,136,60\nQmvaa 160,140,60\n"
"Qmvaa 192,144,60\nQda 64,53,41984\nv_add_u32 v75, 1, v75\nQmvaa 224,148,60\ns_waitcnt vmcnt(33)\nds_write_b32 v73, v75\ns_waitcnt lgkmcnt(3)\nQmvva 92,0,36\n"
"buffer_load_dword v78, v76, s[24:27], 0 offen sc1\ns_mov_b32 s94, 716800\nQmvva 124,4,36\nbuffer_load_dwordx4 a[16:19], v60, s[48:51], s94 offen offset:0 sc1\nQmvva 156,8,36\nbuffer_load_dwordx4 v43, s[52:55], s95 offen offset:3072 lds\nQmvva 188,12,36\nQtv 20,51,0\n"
"Qmvva 220,16,36\ns_waitcnt vmcnt(28)\nds_write_b128 v52, a[220:223] offset:12288\nQmvva 252,0,40\nQmvva 100,4,40\nQtv 22,51,5120\nQmvva 132,8,40\nds_read_b32 v69, v74\n"
"Qmvva 164,12,40\nQda 68,53,46080\ns_add_u32 m0, s96, 49152\nQmvva 196,16,40\nbuffer_load_dwordx4 v44, s[52:55], s95 offen lds\nQmvva 228,0,44\nQtv 24,51,256\nQmvva 108,4,44\n"
"s_waitcnt lgkmcnt(2)\nv_cmp_gt_u32 vcc, v75, v69\nQmvva 140,8,44\ns_cbranch_vccnz .Lso46\n.Lro46:\nQmvva 172,12,44\nbuffer_load_dwordx4 a[20:23], v60, s[48:51], s94 offen offset:1024 sc1\nQmvva 204,16,44\n"
"s_waitcnt vmcnt(27)\nds_write_b128 v52, a[224:227] offset:16384\nQmvva 236,0,208\nQmvva 80,4,208\nQtv 26,51,5376\nQmvva 112,8,208\nQtv 28,51,512\nQmvva 144,12,208\n"
"Qda 72,53,50176\nQmvva 176,16,208\nQtv 30,51,5632\nQmvva 208,0,212\nQtv 32,51,768\nQmvva 240,4,212\nbuffer_load_dwordx4 a[24:27], v60, s[48:51], s94 offen offset:2048 sc1\nQmvva 84,8,212\n"
"Qtv 34,51,5888\nQmvva 88,12,212\nQtv 36,51,1024\nQmvva 120,16,212\ns_waitcnt vmcnt(26)\nds_write_b128 v52, a[228:231] offset:20480\nQmvva 152,0,216\nQmvva 184,4,216\n"
"Qtv 38,51,6144\nQmvva 216,8,216\nQmvva 248,12,216\nQda 76,53,0\nQmvva 116,16,216\nQmvva 96,0,220\nQmvva 128,4,220\nbuffer_load_dwordx4 a[28:31], v60, s[48:51], s94 offen offset:3072 sc1\n"
"Qmvva 160,8,220\nQmvva 192,12,220\nv_add_u32 v75, 1, v75\nQmvva 224,16,220\nds_write_b32 v73, v75\ns_waitcnt lgkmcnt(2)\nQmvva 92,20,64\ns_waitcnt vmcnt(26)\n"
"ds_write_b128 v52, a[232:235] offset:25600\nQmvva 124,24,64\nQmvva 156,28,64\nQta 112,46,20480\nQmvva 188,32,64\nQda 80,53,4096\nQmvva 220,36,64\nQta 114,46,25600\n"
"Qmvva 252,20,68\nQta 116,46,20736\ns_mov_b32 s94, 720896\nQmvva 100,24,68\nbuffer_load_dwordx4 a[32:35], v60, s[48:51], s94 offen offset:0 sc1\nQmvva 132,28,68\nds_read_b32 v69, v74\nQmvva 164,32,68\n"
"Qta 118,46,25856\nQmvva 196,36,68\ns_waitcnt vmcnt(26)\nds_write_b128 v52, a[236:239] offset:29696\ns_waitcnt lgkmcnt(15)\nQmvva 228,20,72\nQmvva 108,24,72\ns_waitcnt lgkmcnt(2)\n"
"v_cmp_gt_u32 vcc, v75, v69\nQmvva 140,28,72\ns_cbranch_vccnz .Lso47\n.Lro47:\nQmvva 172,32,72\nQda 48,53,8192\ns_add_u32 m0, s96, 65536\ns_mov_b32 s95, 1433600\n"
"Qmvva 204,36,72\nbuffer_load_dwordx4 v40, s[52:55], s95 offen lds\nQmvva 236,20,76\nQta 120,46,20992\nQmvva 80,24,76\nbuffer_load_dwordx4 a[36:39], v60, s[48:51], s94 offen offset:1024 sc1\nQmvva 112,28,76\nQta 122,46,26112\n"
"Qmvva 144,32,76\nQta 124,46,21248\nQmvva 176,36,76\ns_waitcnt vmcnt(27)\nds_write_b128 v52, a[240:243] offset:33792\ns_waitcnt lgkmcnt(4)\nQmvva 208,20,80\nQmvva 240,24,80\n"
"buffer_load_dwordx4 v41, s[52:55], s95 offen offset:1024 lds\nQmvva 84,28,80\nQta 126,46,26368\nQmvva 88,32,80\nQda 52,53,12288\nQmvva 120,36,80\nQta 128,46,21504\nQmvva 152,20,48\n"
"Qta 130,46,26624\nQmvva 184,24,48\nbuffer_load_dwordx4 a[40:43], v60, s[48:51], s94 offen offset:2048 sc1\nQmvva 216,28,48\nQmvva 248,32,48\nbuffer_load_dwordx4 v42, s[52:55], s95 offen offset:2048 lds\nQmvva 116,36,48\ns_waitcnt vmcnt(28)\n"
"ds_write_b128 v52, a[244:247] offset:37888\ns_waitcnt lgkmcnt(3)\nQmvva 96,20,52\nQmvva 128,24,52\nQmvva 160,28,52\nQmvva 192,32,52\nQda 56,53,16384\nv_add_u32 v75, 1, v75\n"
"Qmvva 224,36,52\ns_waitcnt vmcnt(34)\nds_write_b32 v73, v75\ns_waitcnt lgkmcnt(3)\nQmvaa 92,112,224\ns_mov_b32 s94, 831488\ns_waitcnt vmcnt(12)\nv_sub_u32 v66, v78, v65\n"
"v_cmp_gt_u32 vcc, 5, v66\ns_and_b64 vcc, vcc, s[28:29]\ns_cbranch_scc1 .Lf5_w\n.Lf5_r:\nQmvaa 124,116,224\nbuffer_load_dwordx4 a[44:47], v60, s[48:51], s94 offen offset:0 sc1\nQmvaa 156,120,224\nbuffer_load_dwordx4 v43, s[52:55], s95 offen offset:3072 lds\n"
"Qmvaa 188,124,224\nQta 132,51,20480\nQmvaa 220,128,224\ns_waitcnt vmcnt(28)\nds_write_b128 v52, a[248:251] offset:41984\nQmvaa 252,112,228\nQmvaa 100,116,228\nQta 134,51,25600\n"
"Qmvaa 132,120,228\nds_read_b32 v69, v74\nQmvaa 164,124,228\nQda 60,53,20480\ns_add_u32 m0, s96, 69632\nQmvaa 196,128,228\nbuffer_load_dwordx4 v44, s[52:55], s95 offen lds\nQmvaa 228,112,232\n"
"Qta 136,51,20736\nQmvaa 108,116,232\ns_waitcnt lgkmcnt(2)\nv_cmp_gt_u32 vcc, v75, v69\nQmvaa 140,120,232\ns_cbranch_vccnz .Lso48\n.Lro48:\nQmvaa 172,124,232\n"
"buffer_load_dwordx4 a[208:211], v60, s[48:51], s94 offen offset:1024 sc1\nQmvaa 204,128,232\ns_waitcnt vmcnt(27)\nds_write_b128 v52, a[252:255] offset:46080\nQmvaa 236,112,236\nQmvaa 80,116,236\nQta 138,51,25856\nQmvaa 112,120,236\n"
"Qta 140,51,20992\nQmvaa 144,124,236\nQda 64,53,25600\nQmvaa 176,128,236\nQta 142,51,26112\nQmvaa 208,112,240\nQta 144,51,21248\nQmvaa 240,116,240\n"
"buffer_load_dwordx4 a[212:215], v60, s[48:51], s94 offen offset:2048 sc1\nQmvaa 84,120,240\nQta 146,51,26368\nQmvaa 88,124,240\nQta 148,51,21504\nQmvaa 120,128,240\ns_waitcnt vmcnt(26)\nds_write_b128 v52, a[168:171] offset:50176\n"
"Qmvaa 152,112,244\nQmvaa 184,116,244\nQta 150,51,26624\nQmvaa 216,120,244\nQmvaa 248,124,244\nQda 68,53,29696\nQmvaa 116,128,244\nQmvaa 96,112,248\n"
"Qmvaa 128,116,248\nbuffer_load_dwordx4 a[216:219], v60, s[48:51], s94 offen offset:3072 sc1\nQmvaa 160,120,248\nQmvaa 192,124,248\nv_add_u32 v75, 1, v75\nQmvaa 224,128,248\nds_write_b32 v73, v75\ns_lshr_b32 s68, %19, 2\n"
"s_xor_b32 s68, s68, s92\ns_cbranch_scc1 .Lrole1\ns_waitcnt lgkmcnt(2)\nQmvaa 92,132,56\ns_waitcnt vmcnt(26)\nds_write_b128 v52, a[172:175] offset:0\nQmvaa 124,136,56\nQmvaa 156,140,56\n"
"Qtv 0,45,0\nQmvaa 188,144,56\nQda 72,53,33792\nQmvaa 220,148,56\nQtv 2,45,5120\nQmvaa 252,132,60\nQtv 4,45,256\ns_mov_b32 s94, 835584\n"
"Qmvaa 100,136,60\nbuffer_load_dwordx4 a[220:223], v60, s[48:51], s94 offen offset:0 sc1\nQmvaa 132,140,60\nds_read_b32 v69, v74\nQmvaa 164,144,60\nQtv 6,45,5376\nQmvaa 196,148,60\ns_waitcnt vmcnt(26)\n"
"ds_write_b128 v52, a[0:3] offset:4096\ns_waitcnt lgkmcnt(15)\nQmvaa 228,132,64\nQmvaa 108,136,64\ns_waitcnt lgkmcnt(2)\nv_cmp_gt_u32 vcc, v75, v69\nQmvaa 140,140,64\ns_cbranch_vccnz .Lso49r0\n"
".Lro49r0:\nQmvaa 172,144,64\nQda 76,53,37888\ns_add_u32 m0, s96, 86016\ns_mov_b32 s95, 1761280\nQmvaa 204,148,64\nbuffer_load_dwordx4 v40, s[52:55], s95 offen lds\nQmvaa 236,132,68\n"
"Qtv 8,45,512\nQmvaa 80,136,68\nbuffer_load_dwordx4 a[224:227], v60, s[48:51], s94 offen offset:1024 sc1\nQmvaa 112,140,68\nQtv 10,45,5632\nQmvaa 144,144,68\nQtv 12,45,768\nQmvaa 176,148,68\n"
"s_waitcnt vmcnt(27)\nds_write_b128 v52, a[4:7] offset:8192\ns_waitcnt lgkmcnt(4)\nQmvaa 208,132,72\nQmvaa 240,136,72\nbuffer_load_dwordx4 v41, s[52:55], s95 offen offset:1024 lds\nQmvaa 84,140,72\nQtv 14,45,5888\n"
"Qmvaa 88,144,72\nQda 80,53,41984\nQmvaa 120,148,72\nQtv 16,45,1024\nQmvaa 152,132,76\nQtv 18,45,6144\nQmvaa 184,136,76\nbuffer_load_dwordx4 a[228:231], v60, s[48:51], s94 offen offset:2048 sc1\n"
"Qmvaa 216,140,76\nQmvaa 248,144,76\nbuffer_load_dwordx4 v42, s[52:55], s95 offen offset:2048 lds\nQmvaa 116,148,76\ns_waitcnt vmcnt(28)\nds_write_b128 v52, a[8:11] offset:12288\ns_waitcnt lgkmcnt(3)\nQmvaa 96,132,80\n"
"Qmvaa 128,136,80\nQmvaa 160,140,80\nQmvaa 192,144,80\nQda 48,53,46080\nv_add_u32 v75, 1, v75\nQmvaa 224,148,80\ns_waitcnt vmcnt(34)\nds_write_b32 v73, v75\n"
"s_waitcnt lgkmcnt(3)\nQmvva 92,0,252\nbuffer_load_dwordx4 v43, s[52:55], s95 offen offset:3072 lds\ns_mov_b32 s94, 505856\nQmvva 124,4,252\nbuffer_load_dwordx4 a[232:235], v60, s[48:51], s94 offen offset:0 sc1\nQmvva 156,8,252\nQtv 20,47,0\n"
"Qmvva 188,12,252\nQtv 22,47,5120\nQmvva 220,16,252\ns_waitcnt vmcnt(28)\nds_write_b128 v52, a[12:15] offset:16384\nQmvva 252,0,168\nQmvva 100,4,168\nQtv 24,47,256\n"
"Qmvva 132,8,168\nds_read_b32 v69, v74\nQmvva 164,12,168\nQda 52,53,50176\ns_add_u32 m0, s96, 90112\nQmvva 196,16,168\nbuffer_load_dwordx4 v44, s[52:55], s95 offen lds\nQmvva 228,0,172\n"
"Qtv 26,47,5376\nQmvva 108,4,172\ns_waitcnt lgkmcnt(2)\nv_cmp_gt_u32 vcc, v75, v69\nQmvva 140,8,172\ns_cbranch_vccnz .Lso50r0\n.Lro50r0:\nQmvva 172,12,172\n"
"buffer_load_dwordx4 a[236:239], v60, s[48:51], s94 offen offset:1024 sc1\nQmvva 204,16,172\ns_waitcnt vmcnt(27)\nds_write_b128 v52, a[16:19] offset:20480\nQmvva 236,0,0\nQmvva 80,4,0\nQtv 28,47,512\nQmvva 112,8,0\n"
"Qmvva 144,12,0\nQda 56,53,0\nQmvva 176,16,0\nQtv 30,47,5632\nQmvva 208,0,4\nQtv 32,47,768\nQmvva 240,4,4\nbuffer_load_dwordx4 a[240:243], v60, s[48:51], s94 offen offset:2048 sc1\n"
"Qmvva 84,8,4\nQtv 34,47,5888\nQmvva 88,12,4\nQtv 36,47,1024\nQmvva 120,16,4\ns_waitcnt vmcnt(25)\nds_write_b128 v52, a[20:23] offset:25600\nQmvva 152,0,8\n"
"Qmvva 184,4,8\nQtv 38,47,6144\nQmvva 216,8,8\nQmvva 248,12,8\nQda 60,53,4096\nQmvva 116,16,8\nQmvva 96,0,12\ns_mov_b32 s94, 620544\n"
"Qmvva 128,4,12\nbuffer_load_dwordx4 a[244:247], v60, s[48:51], s94 offen offset:0 sc1\nQmvva 160,8,12\nQmvva 192,12,12\nv_add_u32 v75, 1, v75\nQmvva 224,16,12\nds_write_b32 v73, v75\ns_waitcnt lgkmcnt(2)\n"
"Qmvva 92,20,48\ns_waitcnt vmcnt(25)\nds_write_b128 v52, a[24:27] offset:29696\nQmvva 124,24,48\nQmvva 156,28,48\nQta 112,45,20480\nQmvva 188,32,48\nQda 64,53,8192\n"
"Qmvva 220,36,48\nQta 114,45,25600\nQmvva 252,20,52\nQta 116,45,20736\nQmvva 100,24,52\nbuffer_load_dwordx4 a[248:251], v60, s[48:51], s94 offen offset:1024 sc1\nQmvva 132,28,52\nds_read_b32 v69, v74\n"
"Qmvva 164,32,52\nQta 118,45,25856\nQmvva 196,36,52\ns_waitcnt vmcnt(25)\nds_write_b128 v52, a[28:31] offset:33792\ns_waitcnt lgkmcnt(15)\nQmvva 228,20,56\nQmvva 108,24,56\n"
"s_waitcnt lgkmcnt(2)\nv_cmp_gt_u32 vcc, v75, v69\nQmvva 140,28,56\ns_cbranch_vccnz .Lso51r0\n.Lro51r0:\nQmvva 172,32,56\nQda 68,53,12288\ns_add_u32 m0, s96, 4096\n"
"s_mov_b32 s95, 2088960\nQmvva 204,36,56\nbuffer_load_dwordx4 v40, s[52:55], s95 offen lds\nQmvva 236,20,60\nQta 120,45,20992\nQmvva 80,24,60\nbuffer_load_dwordx4 a[252:255], v60, s[48:51], s94 offen offset:2048 sc1\nQmvva 112,28,60\n"
"Qta 122,45,26112\nQmvva 144,32,60\nQta 124,45,21248\nQmvva 176,36,60\ns_waitcnt vmcnt(26)\nds_write_b128 v52, a[32:35] offset:37888\ns_waitcnt lgkmcnt(4)\nQmvva 208,20,64\n"
"Qmvva 240,24,64\nbuffer_load_dwordx4 v41, s[52:55], s95 offen offset:1024 lds\nQmvva 84,28,64\nQta 126,45,26368\nQmvva 88,32,64\nQda 72,53,16384\nQmvva 120,36,64\nQta 128,45,21504\n"
"Qmvva 152,20,68\nQta 130,45,26624\ns_mov_b32 s94, 735232\nQmvva 184,24,68\nbuffer_load_dwordx4 a[168:171], v60, s[48:51], s94 offen offset:0 sc1\nQmvva 216,28,68\nQmvva 248,32,68\nbuffer_load_dwordx4 v42, s[52:55], s95 offen offset:2048 lds\n"
"Qmvva 116,36,68\ns_waitcnt vmcnt(27)\nds_write_b128 v52, a[36:39] offset:41984\ns_waitcnt lgkmcnt(3)\nQmvva 96,20,72\nQmvva 128,24,72\nQmvva 160,28,72\nQmvva 192,32,72\n"
"Qda 76,53,20480\nv_add_u32 v75, 1, v75\nQmvva 224,36,72\ns_waitcnt vmcnt(33)\nds_write_b32 v73, v75\ns_waitcnt lgkmcnt(3)\nQmvaa 92,112,16\nbuffer_load_dwordx4 v43, s[52:55], s95 offen offset:3072 lds\n"
"Qmvaa 124,116,16\nbuffer_load_dwordx4 a[172:175], v60, s[48:51], s94 offen offset:1024 sc1\nQmvaa 156,120,16\nQta 132,47,20480\nQmvaa 188,124,16\nQta 134,47,25600\nQmvaa 220,128,16\ns_waitcnt vmcnt(27)\n"
"ds_write_b128 v52, a[40:43] offset:46080\nQmvaa 252,112,20\nQmvaa 100,116,20\nQta 136,47,20736\nQmvaa 132,120,20\nds_read_b32 v69, v74\nQmvaa 164,124,20\nQda 80,53,25600\n"
"s_add_u32 m0, s96, 8192\nQmvaa 196,128,20\nbuffer_load_dwordx4 v44, s[52:55], s95 offen lds\nQmvaa 228,112,24\nQta 138,47,25856\nQmvaa 108,116,24\ns_waitcnt lgkmcnt(2)\nv_cmp_gt_u32 vcc, v75, v69\n"
"Qmvaa 140,120,24\ns_cbranch_vccnz .Lso52r0\n.Lro52r0:\nQmvaa 172,124,24\nbuffer_load_dwordx4 a[0:3], v60, s[48:51], s94 offen offset:2048 sc1\nQmvaa 204,128,24\ns_waitcnt vmcnt(27)\nds_write_b128 v52, a[44:47] offset:50176\n"
"Qmvaa 236,112,28\nQmvaa 80,116,28\nQta 140,47,20992\nQmvaa 112,120,28\nQmvaa 144,124,28\nQda 48,53,29696\nQmvaa 176,128,28\nQta 142,47,26112\n"
"Qmvaa 208,112,32\nQta 144,47,21248\ns_mov_b32 s94, 849920\nQmvaa 240,116,32\nbuffer_load_dwordx4 a[4:7], v60, s[48:51], s94 offen offset:0 sc1\nQmvaa 84,120,32\nQta 146,47,26368\nQmvaa 88,124,32\n"
"Qta 148,47,21504\nQmvaa 120,128,32\ns_waitcnt vmcnt(25)\nds_write_b128 v52, a[208:211] offset:0\nQmvaa 152,112,36\nQmvaa 184,116,36\nQta 150,47,26624\nQmvaa 216,120,36\n"
"Qmvaa 248,124,36\nQda 52,53,33792\nQmvaa 116,128,36\nQmvaa 96,112,40\nQmvaa 128,116,40\nbuffer_load_dwordx4 a[8:11], v60, s[48:51], s94 offen offset:1024 sc1\nQmvaa 160,120,40\nQmvaa 192,124,40\n"
"v_add_u32 v75, 1, v75\nQmvaa 224,128,40\nds_write_b32 v73, v75\ns_waitcnt lgkmcnt(2)\nQmvaa 92,132,76\ns_waitcnt vmcnt(25)\nds_write_b128 v52, a[212:215] offset:4096\nQmvaa 124,136,76\n"
"Qmvaa 156,140,76\nQtv 0,45,40960\nQmvaa 188,144,76\nQda 56,53,37888\nQmvaa 220,148,76\nQtv 2,45,46080\nQmvaa 252,132,80\nQtv 4,45,41216\n"
"Qmvaa 100,136,80\nbuffer_load_dwordx4 a[12:15], v60, s[48:51], s94 offen offset:2048 sc1\nQmvaa 132,140,80\nds_read_b32 v69, v74\nQmvaa 164,144,80\nQtv 6,45,46336\nQmvaa 196,148,80\ns_waitcnt vmcnt(25)\n"
"ds_write_b128 v52, a[216:219] offset:8192\ns_waitcnt lgkmcnt(15)\nQmvaa 228,132,48\nQmvaa 108,136,48\ns_waitcnt lgkmcnt(2)\nv_cmp_gt_u32 vcc, v75, v69\nQmvaa 140,140,48\ns_cbranch_vccnz .Lso53r0\n"
".Lro53r0:\nQmvaa 172,144,48\nQda 60,53,41984\ns_add_u32 m0, s96, 24576\ns_mov_b32 s95, 2416640\nQmvaa 204,148,48\nbuffer_load_dwordx4 v40, s[52:55], s95 offen lds\nQmvaa 236,132,52\n"
"Qtv 8,45,41472\ns_mov_b32 s94, 501760\nQmvaa 80,136,52\nbuffer_load_dwordx4 a[16:19], v60, s[48:51], s94 offen offset:0 sc1\nQmvaa 112,140,52\nQtv 10,45,46592\nQmvaa 144,144,52\nQtv 12,45,41728\n"
"Qmvaa 176,148,52\ns_waitcnt vmcnt(26)\nds_write_b128 v52, a[220:223] offset:12288\ns_waitcnt lgkmcnt(4)\nQmvaa 208,132,56\nQmvaa 240,136,56\nbuffer_load_dwordx4 v41, s[52:55], s95 offen offset:1024 lds\nQmvaa 84,140,56\n"
"Qtv 14,45,46848\nQmvaa 88,144,56\nQda 64,53,46080\nQmvaa 120,148,56\nQtv 16,45,41984\nQmvaa 152,132,60\nQtv 18,45,47104\nQmvaa 184,136,60\n"
"buffer_load_dwordx4 a[20:23], v60, s[48:51], s94 offen offset:1024 sc1\nQmvaa 216,140,60\nQmvaa 248,144,60\nbuffer_load_dwordx4 v42, s[52:55], s95 offen offset:2048 lds\nQmvaa 116,148,60\ns_waitcnt vmcnt(27)\nds_write_b128 v52, a[224:227] offset:16384\ns_waitcnt lgkmcnt(3)\n"
"Qmvaa 96,132,64\nQmvaa 128,136,64\nQmvaa 160,140,64\nQmvaa 192,144,64\nQda 68,53,50176\nv_add_u32 v75, 1, v75\nQmvaa 224,148,64\ns_waitcnt vmcnt(33)\n"
"ds_write_b32 v73, v75\ns_waitcnt lgkmcnt(3)\nQmvva 92,0,44\nbuffer_load_dwordx4 v43, s[52:55], s95 offen offset:3072 lds\nQmvva 124,4,44\nbuffer_load_dwordx4 a[24:27], v60, s[48:51], s94 offen offset:2048 sc1\nQmvva 156,8,44\nQtv 20,47,40960\n"
"Qmvva 188,12,44\nQtv 22,47,46080\nQmvva 220,16,44\ns_waitcnt vmcnt(27)\nds_write_b128 v52, a[228:231] offset:20480\nQmvva 252,0,208\nQmvva 100,4,208\nQtv 24,47,41216\n"
"Qmvva 132,8,208\nds_read_b32 v69, v74\nQmvva 164,12,208\nQda 72,53,0\ns_add_u32 m0, s96, 28672\nQmvva 196,16,208\nbuffer_load_dwordx4 v44, s[52:55], s95 offen lds\nQmvva 228,0,212\n"
"Qtv 26,47,46336\nQmvva 108,4,212\ns_waitcnt lgkmcnt(2)\nv_cmp_gt_u32 vcc, v75, v69\nQmvva 140,8,212\ns_cbranch_vccnz .Lso54r0\n.Lro54r0:\nQmvva 172,12,212\n"
"buffer_load_dwordx4 a[28:31], v60, s[48:51], s94 offen offset:3072 sc1\nQmvva 204,16,212\ns_waitcnt vmcnt(26)\nds_write_b128 v52, a[232:235] offset:25600\nQmvva 236,0,216\nQmvva 80,4,216\nQtv 28,47,41472\nQmvva 112,8,216\n"
"Qmvva 144,12,216\nQda 76,53,4096\nQmvva 176,16,216\nQtv 30,47,46592\nQmvva 208,0,220\nQtv 32,47,41728\ns_mov_b32 s94, 616448\nQmvva 240,4,220\n"
"buffer_load_dwordx4 a[32:35], v60, s[48:51], s94 offen offset:0 sc1\nQmvva 84,8,220\nQtv 34,47,46848\nQmvva 88,12,220\nQtv 36,47,41984\nQmvva 120,16,220\ns_waitcnt vmcnt(25)\nds_write_b128 v52, a[236:239] offset:29696\n"
"Qmvva 152,0,224\nQmvva 184,4,224\nQtv 38,47,47104\nQmvva 216,8,224\nQmvva 248,12,224\nQda 80,53,8192\nQmvva 116,16,224\nQmvva 96,0,228\n"
"Qmvva 128,4,228\nbuffer_load_dwordx4 a[36:39], v60, s[48:51], s94 offen offset:1024 sc1\nQmvva 160,8,228\nQmvva 192,12,228\nv_add_u32 v75, 1, v75\nQmvva 224,16,228\ns_waitcnt vmcnt(27)\nds_write_b32 v73, v75\n"
"s_waitcnt lgkmcnt(2)\nQmvva 92,20,68\ns_waitcnt vmcnt(25)\nds_write_b128 v52, a[240:243] offset:33792\nQmvva 124,24,68\nQmvva 156,28,68\nQta 112,46,0\nQmvva 188,32,68\n"
"Qda 48,53,12288\nQmvva 220,36,68\nQta 114,46,5120\nQmvva 252,20,72\nQta 116,46,256\nQmvva 100,24,72\nbuffer_load_dwordx4 a[40:43], v60, s[48:51], s94 offen offset:2048 sc1\nQmvva 132,28,72\n"
"ds_read_b32 v69, v74\nQmvva 164,32,72\nQmvva 196,36,72\ns_waitcnt vmcnt(25)\nds_write_b128 v52, a[244:247] offset:37888\ns_waitcnt lgkmcnt(15)\nQmvva 228,20,76\nQmvva 108,24,76\n"
"s_waitcnt lgkmcnt(1)\nv_cmp_gt_u32 vcc, v75, v69\nQmvva 140,28,76\ns_cbranch_vccnz .Lso55r0\n.Lro55r0:\nQmvva 172,32,76\nQda 52,53,16384\nQmvva 204,36,76\n"
"Qta 118,46,5376\nQmvva 236,20,80\nQta 120,46,512\nQmvva 80,24,80\nbuffer_load_dwordx4 a[44:47], v60, s[48:51], s94 offen offset:3072 sc1\nQmvva 112,28,80\nQmvva 144,32,80\nQta 122,46,5632\n"
"Qmvva 176,36,80\ns_waitcnt vmcnt(25)\nds_write_b128 v52, a[248:251] offset:41984\ns_waitcnt lgkmcnt(4)\nQmvva 208,20,48\nQmvva 240,24,48\nQta 124,46,768\nQmvva 84,28,48\n"
"Qta 126,46,5888\nQmvva 88,32,48\nQda 56,53,20480\nQmvva 120,36,48\nQmvva 152,20,52\nQta 128,46,1024\ns_mov_b32 s94, 731136\nQmvva 184,24,52\n"
"buffer_load_dwordx4 a[208:211], v60, s[48:51], s94 offen offset:0 sc1\nQmvva 216,28,52\nQta 130,46,6144\nQmvva 248,32,52\nQta 132,51,0\nQta 134,51,5120\nQmvva 116,36,52\ns_waitcnt vmcnt(24)\n"
"ds_write_b128 v52, a[252:255] offset:46080\ns_waitcnt lgkmcnt(5)\nQmvva 96,20,56\nQmvva 128,24,56\nQta 136,51,256\nQmvva 160,28,56\nQta 138,51,5376\nQmvva 192,32,56\n"
"Qda 60,53,25600\nv_add_u32 v75, 1, v75\nQmvva 224,36,56\ns_waitcnt vmcnt(18)\nds_write_b32 v73, v75\ns_waitcnt lgkmcnt(7)\nQmvaa 208,112,232\nQta 140,51,512\n"
"Qmvaa 240,116,232\nbuffer_load_dwordx4 a[212:215], v60, s[48:51], s94 offen offset:1024 sc1\nQmvaa 84,120,232\nQta 142,51,5632\nQmvaa 88,124,232\nQta 144,51,768\nQmvaa 120,128,232\ns_waitcnt vmcnt(23)\n"
"ds_write_b128 v52, a[168:171] offset:50176\nQmvaa 152,112,236\nQmvaa 184,116,236\nQta 146,51,5888\nQmvaa 216,120,236\nds_read_b32 v69, v74\nQmvaa 248,124,236\nQda 64,53,29696\n"
"Qmvaa 116,128,236\nQta 148,51,1024\nQmvaa 96,112,240\nQta 150,51,6144\nQmvaa 128,116,240\ns_waitcnt lgkmcnt(3)\nv_cmp_gt_u32 vcc, v75, v69\nQmvaa 160,120,240\n"
"s_cbranch_vccnz .Lso56r0\n.Lro56r0:\nQmvaa 192,124,240\nbuffer_load_dwordx4 a[216:219], v60, s[48:51], s94 offen offset:2048 sc1\nQmvaa 224,128,240\ns_waitcnt vmcnt(21)\nds_write_b128 v52, a[172:175] offset:0\ns_waitcnt lgkmcnt(1)\n"
"Qmvaa 208,132,60\nQmvaa 240,136,60\nQtv 0,46,20480\nQtv 2,46,25600\nQmvaa 84,140,60\nQtv 4,46,20736\nQtv 6,46,25856\nQmvaa 88,144,60\n"
"Qda 68,53,33792\nQmvaa 120,148,60\nQtv 8,46,20992\nQmvaa 152,132,64\nQtv 10,46,26112\nQtv 12,46,21248\nQmvaa 184,136,64\nbuffer_load_dwordx4 a[220:223], v60, s[48:51], s94 offen offset:3072 sc1\n"
"Qmvaa 216,140,64\nQtv 14,46,26368\nQtv 16,46,21504\nQmvaa 248,144,64\nQtv 18,46,26624\nQmvaa 116,148,64\ns_waitcnt vmcnt(20)\nds_write_b128 v52, a[0:3] offset:4096\n"
"s_waitcnt lgkmcnt(7)\nQmvaa 96,132,68\nQmvaa 128,136,68\nQtv 20,51,20480\nQtv 22,51,25600\nQmvaa 160,140,68\nQtv 24,51,20736\nQmvaa 192,144,68\n"
"Qda 72,53,37888\nQmvaa 224,148,68\nQtv 26,51,25856\ns_waitcnt lgkmcnt(6)\nQmvva 208,0,244\nQtv 28,51,20992\ns_mov_b32 s94, 845824\nQmvva 240,4,244\n"
"buffer_load_dwordx4 a[224:227], v60, s[48:51], s94 offen offset:0 sc1\nQmvva 84,8,244\nQtv 30,51,26112\nQmvva 88,12,244\nQtv 32,51,21248\nv_add_u32 v75, 1, v75\nQmvva 120,16,244\ns_waitcnt vmcnt(10)\n"
"ds_write_b32 v73, v75\nQmvva 152,0,248\ns_waitcnt vmcnt(20)\nds_write_b128 v52, a[4:7] offset:8192\nQmvva 184,4,248\nQmvva 216,8,248\nQtv 34,51,26368\nQmvva 248,12,248\n"
"Qda 76,53,41984\nQmvva 116,16,248\nQtv 36,51,21504\nQmvva 96,0,252\nQtv 38,51,26624\nQmvva 128,4,252\nbuffer_load_dwordx4 a[228:231], v60, s[48:51], s94 offen offset:1024 sc1\nQmvva 160,8,252\n"
"ds_read_b32 v69, v74\nQmvva 192,12,252\nQta 112,45,0\nQta 114,45,5120\nQmvva 224,16,252\ns_waitcnt vmcnt(20)\nds_write_b128 v52, a[8:11] offset:12288\ns_waitcnt lgkmcnt(13)\n"
"Qmvva 208,20,72\nQmvva 240,24,72\ns_waitcnt lgkmcnt(3)\nv_cmp_gt_u32 vcc, v75, v69\nQmvva 84,28,72\ns_cbranch_vccnz .Lso57r0\n.Lro57r0:\nQmvva 88,32,72\n"
"Qda 80,53,46080\nQmvva 120,36,72\nQta 116,45,256\nQta 118,45,5376\nQmvva 152,20,76\nQta 120,45,512\nQta 122,45,5632\nQmvva 184,24,76\n"
"buffer_load_dwordx4 a[232:235], v60, s[48:51], s94 offen offset:2048 sc1\nQmvva 216,28,76\nQta 124,45,768\nQta 126,45,5888\nQmvva 248,32,76\nQta 128,45,1024\nQta 130,45,6144\nQmvva 116,36,76\n"
"s_waitcnt vmcnt(20)\nds_write_b128 v52, a[12:15] offset:16384\ns_waitcnt lgkmcnt(9)\nQmvva 96,20,80\nQmvva 128,24,80\nQta 132,47,0\nQta 134,47,5120\nQmvva 160,28,80\n"
"Qta 136,47,256\nQmvva 192,32,80\nQda 48,53,50176\nQmvva 224,36,80\nQta 138,47,5376\ns_waitcnt lgkmcnt(6)\nQmvaa 208,112,168\nQta 140,47,512\n"
"Qmvaa 240,116,168\nbuffer_load_dwordx4 a[236:239], v60, s[48:51], s94 offen offset:3072 sc1\nQmvaa 84,120,168\nQta 142,47,5632\nQmvaa 88,124,168\nQta 144,47,768\nQmvaa 120,128,168\ns_waitcnt vmcnt(19)\n"
"ds_write_b128 v52, a[16:19] offset:20480\nQmvaa 152,112,172\nQmvaa 184,116,172\nQta 146,47,5888\nQmvaa 216,120,172\nQta 148,47,1024\nQmvaa 248,124,172\nQda 52,53,0\n"
"v_add_u32 v75, 1, v75\nQmvaa 116,128,172\nds_write_b32 v73, v75\nQmvaa 96,112,0\nQta 150,47,6144\nQmvaa 128,116,0\nQtv 0,45,20480\nQtv 2,45,25600\n"
"Qmvaa 160,120,0\nQtv 4,45,20736\nQmvaa 192,124,0\nQtv 6,45,25856\nQtv 8,45,20992\nQmvaa 224,128,0\ns_waitcnt vmcnt(17)\nds_write_b128 v52, a[20:23] offset:25600\n"
"s_waitcnt lgkmcnt(6)\nQmvaa 208,132,48\nQmvaa 240,136,48\nQtv 10,45,26112\nQmvaa 84,140,48\nds_read_b32 v69, v74\nQmvaa 88,144,48\nQda 56,53,4096\n"
"Qmvaa 120,148,48\nQtv 12,45,21248\nQtv 14,45,26368\nQmvaa 152,132,52\nQtv 16,45,21504\nQmvaa 184,136,52\ns_waitcnt lgkmcnt(4)\nv_cmp_gt_u32 vcc, v75, v69\n"
"Qmvaa 216,140,52\ns_cbranch_vccnz .Lso58r0\n.Lro58r0:\nQmvaa 248,144,52\nQtv 18,45,26624\nQmvaa 116,148,52\ns_waitcnt vmcnt(14)\nds_write_b128 v52, a[24:27] offset:29696\n"
"s_waitcnt lgkmcnt(5)\nQmvaa 96,132,56\nQmvaa 128,136,56\nQtv 20,47,20480\nQmvaa 160,140,56\nQtv 22,47,25600\nQmvaa 192,144,56\nQda 60,53,8192\n"
"Qmvaa 224,148,56\nQtv 24,47,20736\ns_waitcnt lgkmcnt(5)\nQmvva 208,0,4\nQtv 26,47,25856\nQmvva 240,4,4\nQtv 28,47,20992\nQmvva 84,8,4\n"
"Qtv 30,47,26112\nQmvva 88,12,4\nQtv 32,47,21248\nQmvva 120,16,4\ns_waitcnt vmcnt(12)\nds_write_b128 v52, a[28:31] offset:33792\nQmvva 152,0,8\nQmvva 184,4,8\n"
"Qtv 34,47,26368\nQmvva 216,8,8\nQtv 36,47,21504\nQmvva 248,12,8\nQda 64,53,12288\nQmvva 116,16,8\nQtv 38,47,26624\nQmvva 96,0,12\n"
"Qta 112,46,0\nQta 114,46,5120\nQmvva 128,4,12\nQta 116,46,256\nQmvva 160,8,12\nQta 118,46,5376\nQmvva 192,12,12\nQta 120,46,512\n"
"v_add_u32 v75, 1, v75\nQmvva 224,16,12\nds_write_b32 v73, v75\ns_waitcnt lgkmcnt(6)\nQmvva 208,20,60\ns_waitcnt vmcnt(11)\nds_write_b128 v52, a[32:35] offset:37888\nQmvva 240,24,60\n"
"s_add_u32 s68, s64, 0\nQmvva 84,28,60\nQta 122,46,5632\nQmvva 88,32,60\nQda 68,53,16384\nQmvva 120,36,60\nQta 124,46,768\nQmvva 152,20,64\n"
"Qta 126,46,5888\nQmvva 184,24,64\nQta 128,46,1024\nQk 208,208,209\nQmvva 216,28,64\nds_read_b32 v69, v74\nv_min_u32 v208, -2, v208\nQmvva 248,32,64\n"
"Qta 130,46,6144\nQk 209,210,211\nQmvva 116,36,64\ns_waitcnt vmcnt(10)\nds_write_b128 v52, a[36:39] offset:41984\ns_waitcnt lgkmcnt(6)\nQmvva 96,20,68\nv_min_u32 v209, -2, v209\n"
"Qk 210,240,241\nQmvva 128,24,68\ns_waitcnt lgkmcnt(2)\nv_cmp_gt_u32 vcc, v75, v69\nQmvva 160,28,68\ns_cbranch_vccnz .Lso59r0\n.Lro59r0:\nv_min_u32 v210, -2, v210\n"
"Qmvva 192,32,68\nQda 72,53,20480\nQk 211,242,243\nQmvva 224,36,68\nQta 132,51,0\nv_min_u32 v211, -2, v211\ns_waitcnt lgkmcnt(3)\nQmvaa 92,112,16\n"
"Qta 134,51,5120\nbuffer_store_dwordx4 v[208:211], v60, s[24:27], s68 offen offset:0 sc1\nQmvaa 124,116,16\nQta 136,51,256\nQk 84,84,85\nQmvaa 156,120,16\nQta 138,51,5376\nv_min_u32 v84, -2, v84\n"
"Qmvaa 188,124,16\nQta 140,51,512\nQk 85,86,87\nQmvaa 220,128,16\ns_waitcnt vmcnt(10)\nds_write_b128 v52, a[40:43] offset:46080\nQmvaa 252,112,20\nv_min_u32 v85, -2, v85\n"
"Qk 86,88,89\nQmvaa 100,116,20\nQta 142,51,5632\nv_min_u32 v86, -2, v86\nQmvaa 132,120,20\nQta 144,51,768\nQk 87,90,91\nQmvaa 164,124,20\n"
"Qda 76,53,25600\nv_min_u32 v87, -2, v87\nQmvaa 196,128,20\nQta 146,51,5888\nbuffer_store_dwordx4 v[84:87], v60, s[24:27], s68 offen offset:1024 sc1\nQmvaa 228,112,24\nQta 148,51,1024\nQk 120,120,121\n"
"Qmvaa 108,116,24\nQta 150,51,6144\nv_min_u32 v120, -2, v120\nQmvaa 140,120,24\nQtv 0,46,20480\nQk 121,122,123\nQmvaa 172,124,24\nQtv 2,46,25600\n"
"v_min_u32 v121, -2, v121\nQmvaa 204,128,24\ns_waitcnt vmcnt(10)\nds_write_b128 v52, a[44:47] offset:50176\nQmvaa 236,112,28\nQk 122,152,153\nv_min_u32 v122, -2, v122\nQmvaa 80,116,28\n"
"Qtv 4,46,20736\nQk 123,154,155\nQmvaa 112,120,28\nQtv 6,46,25856\nv_min_u32 v123, -2, v123\nQmvaa 144,124,28\nQda 80,53,29696\nv_add_u32 v75, 1, v75\n"
"Qmvaa 176,128,28\nds_write_b32 v73, v75\nbuffer_store_dwordx4 v[120:123], v60, s[24:27], s68 offen offset:2048 sc1\ns_waitcnt lgkmcnt(7)\nQmvaa 92,132,72\nQtv 8,46,20992\nQk 184,184,185\nQmvaa 124,136,72\n"
"Qtv 10,46,26112\nv_min_u32 v184, -2, v184\nQmvaa 156,140,72\nQtv 12,46,21248\nQk 185,186,187\nQmvaa 188,144,72\nQtv 14,46,26368\nv_min_u32 v185, -2, v185\n"
"Qmvaa 220,148,72\ns_waitcnt vmcnt(10)\nds_write_b128 v52, a[208:211] offset:0\nQmvaa 252,132,76\nQk 186,216,217\nv_min_u32 v186, -2, v186\nQmvaa 100,136,76\nQtv 16,46,21504\n"
"Qk 187,218,219\nQmvaa 132,140,76\nds_read_b32 v69, v74\nv_min_u32 v187, -2, v187\nQmvaa 164,144,76\nQda 48,53,33792\nbuffer_store_dwordx4 v[184:187], v60, s[24:27], s68 offen offset:3072 sc1\nQmvaa 196,148,76\n"
"Qtv 18,46,26624\nQk 248,248,249\ns_waitcnt lgkmcnt(10)\nQmvaa 228,132,80\nv_min_u32 v248, -2, v248\nQk 249,250,251\nQmvaa 108,136,80\ns_waitcnt lgkmcnt(1)\n"
"v_cmp_gt_u32 vcc, v75, v69\nQmvaa 140,140,80\ns_cbranch_vccnz .Lso60r0\n.Lro60r0:\nv_min_u32 v249, -2, v249\nQmvaa 172,144,80\nQtv 20,51,20480\nQk 250,116,117\n"
"Qmvaa 204,148,80\ns_waitcnt vmcnt(10)\nds_write_b128 v52, a[212:215] offset:4096\nQmvaa 236,132,48\nv_min_u32 v250, -2, v250\nQk 251,118,119\nQmvaa 80,136,48\nQtv 22,51,25600\n"
"v_min_u32 v251, -2, v251\nQmvaa 112,140,48\nQtv 24,51,20736\ns_add_u32 s68, s64, 4096\nQmvaa 144,144,48\nQda 52,53,37888\nbuffer_store_dwordx4 v[248:251], v60, s[24:27], s68 offen offset:0 sc1\nQmvaa 176,148,48\n"
"Qtv 26,51,25856\nQk 96,96,97\ns_waitcnt lgkmcnt(6)\nQmvva 92,0,32\nv_min_u32 v96, -2, v96\nQk 97,98,99\nQmvva 124,4,32\nQtv 28,51,20992\n"
"v_min_u32 v97, -2, v97\nQmvva 156,8,32\nQtv 30,51,26112\nQk 98,128,129\nQmvva 188,12,32\nQtv 32,51,21248\nv_min_u32 v98, -2, v98\nQmvva 220,16,32\n"
"s_waitcnt vmcnt(10)\nds_write_b128 v52, a[216:219] offset:8192\nQmvva 252,0,36\nQk 99,130,131\nv_min_u32 v99, -2, v99\nQmvva 100,4,36\nbuffer_store_dwordx4 v[96:99], v60, s[24:27], s68 offen offset:1024 sc1\nQk 160,160,161\n"
"Qmvva 132,8,36\nQtv 34,51,26368\nv_min_u32 v160, -2, v160\nQmvva 164,12,36\nQda 56,53,41984\nQk 161,162,163\nQmvva 196,16,36\nQtv 36,51,21504\n"
"v_min_u32 v161, -2, v161\nQmvva 228,0,40\nQtv 38,51,26624\nQk 162,192,193\nQmvva 108,4,40\nv_min_u32 v162, -2, v162\nQk 163,194,195\nQmvva 140,8,40\n"
"Qta 112,45,0\nv_min_u32 v163, -2, v163\nQmvva 172,12,40\nQta 114,45,5120\nv_add_u32 v75, 1, v75\nQmvva 204,16,40\nds_write_b32 v73, v75\nbuffer_store_dwordx4 v[160:163], v60, s[24:27], s68 offen offset:2048 sc1\n"
"Qmvva 236,0,44\ns_waitcnt vmcnt(11)\nds_write_b128 v52, a[220:223] offset:12288\nQmvva 80,4,44\nQk 224,224,225\nv_min_u32 v224, -2, v224\nQmvva 112,8,44\nQta 116,45,256\n"
"Qk 225,226,227\nQmvva 144,12,44\nQda 60,53,46080\nv_min_u32 v225, -2, v225\nQmvva 176,16,44\nQta 118,45,5376\ns_add_u32 s68, s64, 14336\ns_waitcnt lgkmcnt(7)\n"
"Qmvva 92,20,52\nQta 120,45,512\nbuffer_store_dwordx4 v[224:227], v60, s[24:27], s68 offen offset:0 sc1\nQmvva 124,24,52\nQta 122,45,5632\nQmvva 156,28,52\nds_read_b32 v69, v74\nQmvva 188,32,52\n"
"Qta 124,45,768\nQmvva 220,36,52\ns_waitcnt vmcnt(11)\nds_write_b128 v52, a[224:227] offset:16384\nQmvva 252,20,56\nQmvva 100,24,56\ns_waitcnt lgkmcnt(2)\nv_cmp_gt_u32 vcc, v75, v69\n"
"Qmvva 132,28,56\ns_cbranch_vccnz .Lso61r0\n.Lro61r0:\nQmvva 164,32,56\nQda 64,53,50176\nQmvva 196,36,56\nQta 126,45,5888\ns_waitcnt lgkmcnt(1)\n"
"Qmvva 228,20,60\nQta 128,45,1024\nQmvva 108,24,60\nQta 130,45,6144\nQmvva 140,28,60\nQta 132,47,0\nQmvva 172,32,60\nQta 134,47,5120\n"
"Qmvva 204,36,60\ns_waitcnt vmcnt(10)\nds_write_b128 v52, a[228:231] offset:20480\nQmvva 236,20,64\nQmvva 80,24,64\nQta 136,47,256\nQmvva 112,28,64\nQta 138,47,5376\n"
"Qmvva 144,32,64\nQda 68,53,0\nQmvva 176,36,64\ns_waitcnt lgkmcnt(6)\nQmvaa 92,112,208\nQta 140,47,512\nQmvaa 124,116,208\nQta 142,47,5632\n"
"Qmvaa 156,120,208\nQta 144,47,768\nQmvaa 188,124,208\nQmvaa 220,128,208\ns_waitcnt vmcnt(9)\nds_write_b128 v52, a[232:235] offset:25600\nQmvaa 252,112,212\nQmvaa 100,116,212\n"
"Qta 146,47,5888\nQmvaa 132,120,212\nQta 148,47,1024\nQmvaa 164,124,212\nQda 72,53,4096\nv_add_u32 v75, 1, v75\nQmvaa 196,128,212\nds_write_b32 v73, v75\n"
"Qmvaa 228,112,216\nQta 150,47,6144\nQmvaa 108,116,216\nQmvaa 140,120,216\nQtv 0,45,20480\nQmvaa 172,124,216\nQtv 2,45,25600\nQmvaa 204,128,216\n"
"s_waitcnt vmcnt(8)\nds_write_b128 v52, a[236:239] offset:29696\nQmvaa 236,112,220\nQmvaa 80,116,220\nQtv 4,45,20736\nQmvaa 112,120,220\nds_read_b32 v69, v74\nQmvaa 144,124,220\n"
"Qda 76,53,8192\nQmvaa 176,128,220\nQtv 6,45,25856\ns_waitcnt lgkmcnt(15)\nQmvaa 92,132,68\nQmvaa 124,136,68\ns_waitcnt lgkmcnt(2)\nv_cmp_gt_u32 vcc, v75, v69\n"
"Qmvaa 156,140,68\ns_cbranch_vccnz .Lso62r0\n.Lro62r0:\nQmvaa 188,144,68\nQtv 8,45,20992\nQmvaa 220,148,68\nQtv 10,45,26112\nQmvaa 252,132,72\n"
"Qtv 12,45,21248\nQmvaa 100,136,72\nQmvaa 132,140,72\nQtv 14,45,26368\nQmvaa 164,144,72\nQda 80,53,12288\nQmvaa 196,148,72\nQtv 16,45,21504\n"
"s_waitcnt lgkmcnt(1)\nQmvaa 228,132,76\nQtv 18,45,26624\nQmvaa 108,136,76\nQmvaa 140,140,76\nQtv 20,47,20480\nQmvaa 172,144,76\nQtv 22,47,25600\n"
"Qmvaa 204,148,76\nQmvaa 236,132,80\nQtv 24,47,20736\nQmvaa 80,136,80\nQmvaa 112,140,80\nQtv 26,47,25856\nQmvaa 144,144,80\nQda 48,53,16384\n"
"Qmvaa 176,148,80\nQtv 28,47,20992\ns_waitcnt lgkmcnt(6)\nQmvva 92,0,224\nQmvva 124,4,224\nQtv 30,47,26112\nQmvva 156,8,224\nQmvva 188,12,224\n"
"Qtv 32,47,21248\nv_add_u32 v75, 1, v75\nQmvva 220,16,224\nds_write_b32 v73, v75\nQmvva 252,0,228\nQtv 34,47,26368\nQmvva 100,4,228\nQmvva 132,8,228\n"
"Qtv 36,47,21504\nQmvva 164,12,228\nQda 52,53,20480\nQmvva 196,16,228\nQmvva 228,0,232\nQtv 38,47,26624\nQmvva 108,4,232\nQmvva 140,8,232\n"
"ds_read_b32 v69, v74\nQmvva 172,12,232\nQmvva 204,16,232\nQmvva 236,0,236\nQmvva 80,4,236\ns_waitcnt lgkmcnt(0)\nv_cmp_gt_u32 vcc, v75, v69\nQmvva 112,8,236\n"
"s_cbranch_vccnz .Lso63r0\n.Lro63r0:\nQmvva 144,12,236\nQda 56,53,25600\nQmvva 176,16,236\ns_waitcnt lgkmcnt(2)\nQmvva 92,20,48\ns_add_u32 s69, s98, 0\n"
"buffer_load_dwordx4 v[240:243], v60, s[24:27], s69 offen offset:0 sc1\nQmvva 124,24,48\nQmvva 156,28,48\nbuffer_load_dwordx4 v[88:91], v60, s[24:27], s69 offen offset:1024 sc1\nQmvva 188,32,48\nQmvva 220,36,48\nbuffer_load_dwordx4 v[152:155], v60, s[24:27], s69 offen offset:2048 sc1\nQmvva 252,20,52\n"
"Qmvva 100,24,52\nbuffer_load_dwordx4 v[216:219], v60, s[24:27], s69 offen offset:3072 sc1\nQmvva 132,28,52\nQmvva 164,32,52\nQda 60,53,29696\nQmvva 196,36,52\ns_add_u32 s69, s98, 4096\nbuffer_load_dwordx4 v[116:119], v60, s[24:27], s69 offen offset:0 sc1\n"
"s_waitcnt lgkmcnt(0)\nQmvva 228,20,56\nQmvva 108,24,56\nbuffer_load_dwordx4 v[128:131], v60, s[24:27], s69 offen offset:1024 sc1\nQmvva 140,28,56\nQmvva 172,32,56\nbuffer_load_dwordx4 v[192:195], v60, s[24:27], s69 offen offset:2048 sc1\nQmvva 204,36,56\n"
"Qmvva 236,20,60\ns_add_u32 s69, s98, 14336\nbuffer_load_dwordx4 v[148:151], v60, s[24:27], s69 offen offset:0 sc1\nQmvva 80,24,60\nQmvva 112,28,60\nbuffer_load_dwordx4 v[180:183], v60, s[24:27], s69 offen offset:1024 sc1\nQmvva 144,32,60\nQmvva 176,36,60\n"
"buffer_load_dwordx4 v[104:107], v60, s[24:27], s69 offen offset:2048 sc1\ns_barrier\ns_mov_b32 s97, 0\n.Lrx0:\ns_waitcnt vmcnt(0)\nv_max_u32 v40, v240, v241\nv_max3_u32 v40, v40, v242, v243\nv_max3_u32 v40, v40, v88, v89\n"
"v_max3_u32 v40, v40, v90, v91\nv_max3_u32 v40, v40, v152, v153\nv_max3_u32 v40, v40, v154, v155\nv_max3_u32 v40, v40, v216, v217\nv_max3_u32 v40, v40, v218, v219\nv_max3_u32 v40, v40, v116, v117\nv_max3_u32 v40, v40, v118, v119\nv_max3_u32 v40, v40, v128, v129\n"
"v_max3_u32 v40, v40, v130, v131\nv_max3_u32 v40, v40, v192, v193\nv_max3_u32 v40, v40, v194, v195\nv_max3_u32 v40, v40, v148, v149\nv_max3_u32 v40, v40, v150, v151\nv_max3_u32 v40, v40, v180, v181\nv_max3_u32 v40, v40, v182, v183\nv_max3_u32 v40, v40, v104, v105\n"
"v_max3_u32 v40, v40, v106, v107\nv_cmp_eq_u32 vcc, -1, v40\ns_cmp_eq_u64 vcc, 0\ns_cbranch_scc1 .Lrx0_ok\ns_add_u32 s97, s97, 1\ns_cmp_gt_u32 s97, 1048576\ns_cbranch_scc1 .Lrx0_ok\ns_add_u32 s69, s98, 0\n"
"buffer_load_dwordx4 v[240:243], v60, s[24:27], s69 offen offset:0 sc1\nbuffer_load_dwordx4 v[88:91], v60, s[24:27], s69 offen offset:1024 sc1\nbuffer_load_dwordx4 v[152:155], v60, s[24:27], s69 offen offset:2048 sc1\nbuffer_load_dwordx4 v[216:219], v60, s[24:27], s69 offen offset:3072 sc1\ns_add_u32 s69, s98, 4096\nbuffer_load_dwordx4 v[116:119], v60, s[24:27], s69 offen offset:0 sc1\nbuffer_load_dwordx4 v[128:131], v60, s[24:27], s69 offen offset:1024 sc1\nbuffer_load_dwordx4 v[192:195], v60, s[24:27], s69 offen offset:2048 sc1\n"
"s_add_u32 s69, s98, 14336\nbuffer_load_dwordx4 v[148:151], v60, s[24:27], s69 offen offset:0 sc1\nbuffer_load_dwordx4 v[180:183], v60, s[24:27], s69 offen offset:1024 sc1\nbuffer_load_dwordx4 v[104:107], v60, s[24:27], s69 offen offset:2048 sc1\ns_branch .Lrx0\n.Lrx0_ok:\nv_lshlrev_b32 v41, 16, v240\nv_and_b32 v42, 0xffff0000, v240\n"
"Qa 92,92,41\nQa 93,93,42\nv_lshlrev_b32 v43, 16, v241\nv_and_b32 v44, 0xffff0000, v241\nQa 94,94,43\nQa 95,95,44\nQk 92,92,93\nQk 93,94,95\n"
"ds_write_b64 v61, v[92:93] offset:0\nv_lshlrev_b32 v41, 16, v242\nv_and_b32 v42, 0xffff0000, v242\nQa 124,124,41\nQa 125,125,42\nv_lshlrev_b32 v43, 16, v243\nv_and_b32 v44, 0xffff0000, v243\nQa 126,126,43\n"
"Qa 127,127,44\nQk 124,124,125\nQk 125,126,127\nds_write_b64 v61, v[124:125] offset:32\nv_lshlrev_b32 v41, 16, v88\nv_and_b32 v42, 0xffff0000, v88\nQa 156,156,41\nQa 157,157,42\n"
"v_lshlrev_b32 v43, 16, v89\nv_and_b32 v44, 0xffff0000, v89\nQa 158,158,43\nQa 159,159,44\nQk 156,156,157\nQk 157,158,159\nds_write_b64 v61, v[156:157] offset:64\nv_lshlrev_b32 v41, 16, v90\n"
"v_and_b32 v42, 0xffff0000, v90\nQa 188,188,41\nQa 189,189,42\nv_lshlrev_b32 v43, 16, v91\nv_and_b32 v44, 0xffff0000, v91\nQa 190,190,43\nQa 191,191,44\nQk 188,188,189\n"
"Qk 189,190,191\nds_write_b64 v61, v[188:189] offset:96\nv_lshlrev_b32 v41, 16, v152\nv_and_b32 v42, 0xffff0000, v152\nQa 220,220,41\nQa 221,221,42\nv_lshlrev_b32 v43, 16, v153\nv_and_b32 v44, 0xffff0000, v153\n"
"Qa 222,222,43\nQa 223,223,44\nQk 220,220,221\nQk 221,222,223\nds_write_b64 v61, v[220:221] offset:128\nv_lshlrev_b32 v41, 16, v154\nv_and_b32 v42, 0xffff0000, v154\nQa 252,252,41\n"
"Qa 253,253,42\nv_lshlrev_b32 v43, 16, v155\nv_and_b32 v44, 0xffff0000, v155\nQa 254,254,43\nQa 255,255,44\nQk 252,252,253\nQk 253,254,255\nds_write_b64 v61, v[252:253] offset:5376\n"
"v_lshlrev_b32 v41, 16, v216\nv_and_b32 v42, 0xffff0000, v216\nQa 100,100,41\nQa 101,101,42\nv_lshlrev_b32 v43, 16, v217\nv_and_b32 v44, 0xffff0000, v217\nQa 102,102,43\nQa 103,103,44\n"
"Qk 100,100,101\nQk 101,102,103\nds_write_b64 v61, v[100:101] offset:5408\nv_lshlrev_b32 v41, 16, v218\nv_and_b32 v42, 0xffff0000, v218\nQa 132,132,41\nQa 133,133,42\nv_lshlrev_b32 v43, 16, v219\n"
"v_and_b32 v44, 0xffff0000, v219\nQa 134,134,43\nQa 135,135,44\nQk 132,132,133\nQk 133,134,135\nds_write_b64 v61, v[132:133] offset:5440\nv_lshlrev_b32 v41, 16, v116\nv_and_b32 v42, 0xffff0000, v116\n"
"Qa 164,164,41\nQa 165,165,42\nv_lshlrev_b32 v43, 16, v117\nv_and_b32 v44, 0xffff0000, v117\nQa 166,166,43\nQa 167,167,44\nQk 164,164,165\nQk 165,166,167\n"
"ds_write_b64 v61, v[164:165] offset:5472\nv_lshlrev_b32 v41, 16, v118\nv_and_b32 v42, 0xffff0000, v118\nQa 196,196,41\nQa 197,197,42\nv_lshlrev_b32 v43, 16, v119\nv_and_b32 v44, 0xffff0000, v119\nQa 198,198,43\n"
"Qa 199,199,44\nQk 196,196,197\nQk 197,198,199\nds_write_b64 v61, v[196:197] offset:5504\nv_lshlrev_b32 v41, 16, v128\nv_and_b32 v42, 0xffff0000, v128\nQa 228,228,41\nQa 229,229,42\n"
"v_lshlrev_b32 v43, 16, v129\nv_and_b32 v44, 0xffff0000, v129\nQa 230,230,43\nQa 231,231,44\nQk 228,228,229\nQk 229,230,231\nds_write_b64 v61, v[228:229] offset:10752\nv_lshlrev_b32 v41, 16, v130\n"
"v_and_b32 v42, 0xffff0000, v130\nQa 108,108,41\nQa 109,109,42\nv_lshlrev_b32 v43, 16, v131\nv_and_b32 v44, 0xffff0000, v131\nQa 110,110,43\nQa 111,111,44\nQk 108,108,109\n"
"Qk 109,110,111\nds_write_b64 v61, v[108:109] offset:10784\nv_lshlrev_b32 v41, 16, v192\nv_and_b32 v42, 0xffff0000, v192\nQa 140,140,41\nQa 141,141,42\nv_lshlrev_b32 v43, 16, v193\nv_and_b32 v44, 0xffff0000, v193\n"
"Qa 142,142,43\nQa 143,143,44\nQk 140,140,141\nQk 141,142,143\nds_write_b64 v61, v[140:141] offset:10816\nv_lshlrev_b32 v41, 16, v194\nv_and_b32 v42, 0xffff0000, v194\nQa 172,172,41\n"
"Qa 173,173,42\nv_lshlrev_b32 v43, 16, v195\nv_and_b32 v44, 0xffff0000, v195\nQa 174,174,43\nQa 175,175,44\nQk 172,172,173\nQk 173,174,175\nds_write_b64 v61, v[172:173] offset:10848\n"
"v_lshlrev_b32 v41, 16, v148\nv_and_b32 v42, 0xffff0000, v148\nQa 204,204,41\nQa 205,205,42\nv_lshlrev_b32 v43, 16, v149\nv_and_b32 v44, 0xffff0000, v149\nQa 206,206,43\nQa 207,207,44\n"
"Qk 204,204,205\nQk 205,206,207\nds_write_b64 v61, v[204:205] offset:10880\nv_lshlrev_b32 v41, 16, v150\nv_and_b32 v42, 0xffff0000, v150\nQa 236,236,41\nQa 237,237,42\nv_lshlrev_b32 v43, 16, v151\n"
"v_and_b32 v44, 0xffff0000, v151\nQa 238,238,43\nQa 239,239,44\nQk 236,236,237\nQk 237,238,239\nds_write_b64 v61, v[236:237] offset:16128\nv_lshlrev_b32 v41, 16, v180\nv_and_b32 v42, 0xffff0000, v180\n"
"Qa 80,80,41\nQa 81,81,42\nv_lshlrev_b32 v43, 16, v181\nv_and_b32 v44, 0xffff0000, v181\nQa 82,82,43\nQa 83,83,44\nQk 80,80,81\nQk 81,82,83\n"
"ds_write_b64 v61, v[80:81] offset:16160\nv_lshlrev_b32 v41, 16, v182\nv_and_b32 v42, 0xffff0000, v182\nQa 112,112,41\nQa 113,113,42\nv_lshlrev_b32 v43, 16, v183\nv_and_b32 v44, 0xffff0000, v183\nQa 114,114,43\n"
"Qa 115,115,44\nQk 112,112,113\nQk 113,114,115\nds_write_b64 v61, v[112:113] offset:16192\nv_lshlrev_b32 v41, 16, v104\nv_and_b32 v42, 0xffff0000, v104\nQa 144,144,41\nQa 145,145,42\n"
"v_lshlrev_b32 v43, 16, v105\nv_and_b32 v44, 0xffff0000, v105\nQa 146,146,43\nQa 147,147,44\nQk 144,144,145\nQk 145,146,147\nds_write_b64 v61, v[144:145] offset:16224\nv_lshlrev_b32 v41, 16, v106\n"
"v_and_b32 v42, 0xffff0000, v106\nQa 176,176,41\nQa 177,177,42\nv_lshlrev_b32 v43, 16, v107\nv_and_b32 v44, 0xffff0000, v107\nQa 178,178,43\nQa 179,179,44\nQk 176,176,177\n"
"Qk 177,178,179\nds_write_b64 v61, v[176:177] offset:16256\ns_branch .Lfin\n.Lrole1:\ns_waitcnt lgkmcnt(2)\nQmvaa 92,132,56\ns_waitcnt vmcnt(26)\nds_write_b128 v52, a[172:175] offset:0\n"
"Qmvaa 124,136,56\nQmvaa 156,140,56\nQtv 0,45,0\nQmvaa 188,144,56\nQda 72,53,33792\nQmvaa 220,148,56\nQtv 2,45,5120\nQmvaa 252,132,60\n"
"Qtv 4,45,256\ns_mov_b32 s94, 835584\nQmvaa 100,136,60\nbuffer_load_dwordx4 a[220:223], v60, s[48:51], s94 offen offset:0 sc1\nQmvaa 132,140,60\nds_read_b32 v69, v74\nQmvaa 164,144,60\nQtv 6,45,5376\n"
"Qmvaa 196,148,60\ns_waitcnt vmcnt(26)\nds_write_b128 v52, a[0:3] offset:4096\ns_waitcnt lgkmcnt(15)\nQmvaa 228,132,64\nQmvaa 108,136,64\ns_waitcnt lgkmcnt(2)\nv_cmp_gt_u32 vcc, v75, v69\n"
"Qmvaa 140,140,64\ns_cbranch_vccnz .Lso49r1\n.Lro49r1:\nQmvaa 172,144,64\nQda 76,53,37888\ns_add_u32 m0, s96, 86016\ns_mov_b32 s95, 1761280\nQmvaa 204,148,64\n"
"buffer_load_dwordx4 v40, s[52:55], s95 offen lds\nQmvaa 236,132,68\nQtv 8,45,512\nQmvaa 80,136,68\nbuffer_load_dwordx4 a[224:227], v60, s[48:51], s94 offen offset:1024 sc1\nQmvaa 112,140,68\nQtv 10,45,5632\nQmvaa 144,144,68\n"
"Qtv 12,45,768\nQmvaa 176,148,68\ns_waitcnt vmcnt(27)\nds_write_b128 v52, a[4:7] offset:8192\ns_waitcnt lgkmcnt(4)\nQmvaa 208,132,72\nQmvaa 240,136,72\nbuffer_load_dwordx4 v41, s[52:55], s95 offen offset:1024 lds\n"
"Qmvaa 84,140,72\nQtv 14,45,5888\nQmvaa 88,144,72\nQda 80,53,41984\nQmvaa 120,148,72\nQtv 16,45,1024\nQmvaa 152,132,76\nQtv 18,45,6144\n"
"Qmvaa 184,136,76\nbuffer_load_dwordx4 a[228:231], v60, s[48:51], s94 offen offset:2048 sc1\nQmvaa 216,140,76\nQmvaa 248,144,76\nbuffer_load_dwordx4 v42, s[52:55], s95 offen offset:2048 lds\nQmvaa 116,148,76\ns_waitcnt vmcnt(28)\nds_write_b128 v52, a[8:11] offset:12288\n"
"s_waitcnt lgkmcnt(3)\nQmvaa 96,132,80\nQmvaa 128,136,80\nQmvaa 160,140,80\nQmvaa 192,144,80\nQda 48,53,46080\nv_add_u32 v75, 1, v75\nQmvaa 224,148,80\n"
"s_waitcnt vmcnt(34)\nds_write_b32 v73, v75\ns_waitcnt lgkmcnt(3)\nQmvva 92,0,252\nbuffer_load_dwordx4 v43, s[52:55], s95 offen offset:3072 lds\ns_mov_b32 s94, 501760\nQmvva 124,4,252\nbuffer_load_dwordx4 a[232:235], v60, s[48:51], s94 offen offset:0 sc1\n"
"Qmvva 156,8,252\nQtv 20,47,0\nQmvva 188,12,252\nQtv 22,47,5120\nQmvva 220,16,252\ns_waitcnt vmcnt(28)\nds_write_b128 v52, a[12:15] offset:16384\nQmvva 252,0,168\n"
"Qmvva 100,4,168\nQtv 24,47,256\nQmvva 132,8,168\nds_read_b32 v69, v74\nQmvva 164,12,168\nQda 52,53,50176\ns_add_u32 m0, s96, 90112\nQmvva 196,16,168\n"
"buffer_load_dwordx4 v44, s[52:55], s95 offen lds\nQmvva 228,0,172\nQtv 26,47,5376\nQmvva 108,4,172\ns_waitcnt lgkmcnt(2)\nv_cmp_gt_u32 vcc, v75, v69\nQmvva 140,8,172\ns_cbranch_vccnz .Lso50r1\n"
".Lro50r1:\nQmvva 172,12,172\nbuffer_load_dwordx4 a[236:239], v60, s[48:51], s94 offen offset:1024 sc1\nQmvva 204,16,172\ns_waitcnt vmcnt(27)\nds_write_b128 v52, a[16:19] offset:20480\nQmvva 236,0,0\nQmvva 80,4,0\n"
"Qtv 28,47,512\nQmvva 112,8,0\nQmvva 144,12,0\nQda 56,53,0\nQmvva 176,16,0\nQtv 30,47,5632\nQmvva 208,0,4\nQtv 32,47,768\n"
"Qmvva 240,4,4\nbuffer_load_dwordx4 a[240:243], v60, s[48:51], s94 offen offset:2048 sc1\nQmvva 84,8,4\nQtv 34,47,5888\nQmvva 88,12,4\nQtv 36,47,1024\nQmvva 120,16,4\ns_waitcnt vmcnt(25)\n"
"ds_write_b128 v52, a[20:23] offset:25600\nQmvva 152,0,8\nQmvva 184,4,8\nQtv 38,47,6144\nQmvva 216,8,8\nQmvva 248,12,8\nQda 60,53,4096\nQmvva 116,16,8\n"
"Qmvva 96,0,12\nQmvva 128,4,12\nbuffer_load_dwordx4 a[244:247], v60, s[48:51], s94 offen offset:3072 sc1\nQmvva 160,8,12\nQmvva 192,12,12\nv_add_u32 v75, 1, v75\nQmvva 224,16,12\nds_write_b32 v73, v75\n"
"s_waitcnt lgkmcnt(2)\nQmvva 92,20,48\ns_waitcnt vmcnt(25)\nds_write_b128 v52, a[24:27] offset:29696\nQmvva 124,24,48\nQmvva 156,28,48\nQta 112,45,20480\nQmvva 188,32,48\n"
"Qda 64,53,8192\nQmvva 220,36,48\nQta 114,45,25600\nQmvva 252,20,52\nQta 116,45,20736\ns_mov_b32 s94, 616448\nQmvva 100,24,52\nbuffer_load_dwordx4 a[248:251], v60, s[48:51], s94 offen offset:0 sc1\n"
"Qmvva 132,28,52\nds_read_b32 v69, v74\nQmvva 164,32,52\nQta 118,45,25856\nQmvva 196,36,52\ns_waitcnt vmcnt(25)\nds_write_b128 v52, a[28:31] offset:33792\ns_waitcnt lgkmcnt(15)\n"
"Qmvva 228,20,56\nQmvva 108,24,56\ns_waitcnt lgkmcnt(2)\nv_cmp_gt_u32 vcc, v75, v69\nQmvva 140,28,56\ns_cbranch_vccnz .Lso51r1\n.Lro51r1:\nQmvva 172,32,56\n"
"Qda 68,53,12288\ns_add_u32 m0, s96, 4096\ns_mov_b32 s95, 2088960\nQmvva 204,36,56\nbuffer_load_dwordx4 v40, s[52:55], s95 offen lds\nQmvva 236,20,60\nQta 120,45,20992\nQmvva 80,24,60\n"
"buffer_load_dwordx4 a[252:255], v60, s[48:51], s94 offen offset:1024 sc1\nQmvva 112,28,60\nQta 122,45,26112\nQmvva 144,32,60\nQta 124,45,21248\nQmvva 176,36,60\ns_waitcnt vmcnt(26)\nds_write_b128 v52, a[32:35] offset:37888\n"
"s_waitcnt lgkmcnt(4)\nQmvva 208,20,64\nQmvva 240,24,64\nbuffer_load_dwordx4 v41, s[52:55], s95 offen offset:1024 lds\nQmvva 84,28,64\nQta 126,45,26368\nQmvva 88,32,64\nQda 72,53,16384\n"
"Qmvva 120,36,64\nQta 128,45,21504\nQmvva 152,20,68\nQta 130,45,26624\nQmvva 184,24,68\nbuffer_load_dwordx4 a[168:171], v60, s[48:51], s94 offen offset:2048 sc1\nQmvva 216,28,68\nQmvva 248,32,68\n"
"buffer_load_dwordx4 v42, s[52:55], s95 offen offset:2048 lds\nQmvva 116,36,68\ns_waitcnt vmcnt(27)\nds_write_b128 v52, a[36:39] offset:41984\ns_waitcnt lgkmcnt(3)\nQmvva 96,20,72\nQmvva 128,24,72\nQmvva 160,28,72\n"
"Qmvva 192,32,72\nQda 76,53,20480\nv_add_u32 v75, 1, v75\nQmvva 224,36,72\ns_waitcnt vmcnt(33)\nds_write_b32 v73, v75\ns_waitcnt lgkmcnt(3)\nQmvaa 92,112,16\n"
"buffer_load_dwordx4 v43, s[52:55], s95 offen offset:3072 lds\nQmvaa 124,116,16\nbuffer_load_dwordx4 a[172:175], v60, s[48:51], s94 offen offset:3072 sc1\nQmvaa 156,120,16\nQta 132,47,20480\nQmvaa 188,124,16\nQta 134,47,25600\nQmvaa 220,128,16\n"
"s_waitcnt vmcnt(27)\nds_write_b128 v52, a[40:43] offset:46080\nQmvaa 252,112,20\nQmvaa 100,116,20\nQta 136,47,20736\nQmvaa 132,120,20\nds_read_b32 v69, v74\nQmvaa 164,124,20\n"
"Qda 80,53,25600\ns_add_u32 m0, s96, 8192\nQmvaa 196,128,20\nbuffer_load_dwordx4 v44, s[52:55], s95 offen lds\nQmvaa 228,112,24\nQta 138,47,25856\nQmvaa 108,116,24\ns_waitcnt lgkmcnt(2)\n"
"v_cmp_gt_u32 vcc, v75, v69\nQmvaa 140,120,24\ns_mov_b32 s94, 731136\ns_cbranch_vccnz .Lso52r1\n.Lro52r1:\nQmvaa 172,124,24\nbuffer_load_dwordx4 a[0:3], v60, s[48:51], s94 offen offset:0 sc1\nQmvaa 204,128,24\n"
"s_waitcnt vmcnt(27)\nds_write_b128 v52, a[44:47] offset:50176\nQmvaa 236,112,28\nQmvaa 80,116,28\nQta 140,47,20992\nQmvaa 112,120,28\nQmvaa 144,124,28\nQda 48,53,29696\n"
"Qmvaa 176,128,28\nQta 142,47,26112\nQmvaa 208,112,32\nQta 144,47,21248\nQmvaa 240,116,32\nbuffer_load_dwordx4 a[4:7], v60, s[48:51], s94 offen offset:1024 sc1\nQmvaa 84,120,32\nQta 146,47,26368\n"
"Qmvaa 88,124,32\nQta 148,47,21504\nQmvaa 120,128,32\ns_waitcnt vmcnt(25)\nds_write_b128 v52, a[208:211] offset:0\nQmvaa 152,112,36\nQmvaa 184,116,36\nQta 150,47,26624\n"
"Qmvaa 216,120,36\nQmvaa 248,124,36\nQda 52,53,33792\nQmvaa 116,128,36\nQmvaa 96,112,40\nQmvaa 128,116,40\nbuffer_load_dwordx4 a[8:11], v60, s[48:51], s94 offen offset:2048 sc1\nQmvaa 160,120,40\n"
"Qmvaa 192,124,40\nv_add_u32 v75, 1, v75\nQmvaa 224,128,40\nds_write_b32 v73, v75\ns_waitcnt lgkmcnt(2)\nQmvaa 92,132,76\ns_waitcnt vmcnt(25)\nds_write_b128 v52, a[212:215] offset:4096\n"
"Qmvaa 124,136,76\nQmvaa 156,140,76\nQtv 0,45,40960\nQmvaa 188,144,76\nQda 56,53,37888\nQmvaa 220,148,76\nQtv 2,45,46080\nQmvaa 252,132,80\n"
"Qtv 4,45,41216\nQmvaa 100,136,80\nbuffer_load_dwordx4 a[12:15], v60, s[48:51], s94 offen offset:3072 sc1\nQmvaa 132,140,80\nds_read_b32 v69, v74\nQmvaa 164,144,80\nQtv 6,45,46336\nQmvaa 196,148,80\n"
"s_waitcnt vmcnt(25)\nds_write_b128 v52, a[216:219] offset:8192\ns_waitcnt lgkmcnt(15)\nQmvaa 228,132,48\nQmvaa 108,136,48\ns_waitcnt lgkmcnt(2)\nv_cmp_gt_u32 vcc, v75, v69\nQmvaa 140,140,48\n"
"s_cbranch_vccnz .Lso53r1\n.Lro53r1:\nQmvaa 172,144,48\nQda 60,53,41984\ns_add_u32 m0, s96, 24576\ns_mov_b32 s95, 2416640\nQmvaa 204,148,48\nbuffer_load_dwordx4 v40, s[52:55], s95 offen lds\n"
"Qmvaa 236,132,52\nQtv 8,45,41472\ns_mov_b32 s94, 845824\nQmvaa 80,136,52\nbuffer_load_dwordx4 a[16:19], v60, s[48:51], s94 offen offset:0 sc1\nQmvaa 112,140,52\nQtv 10,45,46592\nQmvaa 144,144,52\n"
"Qtv 12,45,41728\nQmvaa 176,148,52\ns_waitcnt vmcnt(26)\nds_write_b128 v52, a[220:223] offset:12288\ns_waitcnt lgkmcnt(4)\nQmvaa 208,132,56\nQmvaa 240,136,56\nbuffer_load_dwordx4 v41, s[52:55], s95 offen offset:1024 lds\n"
"Qmvaa 84,140,56\nQtv 14,45,46848\nQmvaa 88,144,56\nQda 64,53,46080\nQmvaa 120,148,56\nQtv 16,45,41984\nQmvaa 152,132,60\nQtv 18,45,47104\n"
"Qmvaa 184,136,60\nbuffer_load_dwordx4 a[20:23], v60, s[48:51], s94 offen offset:1024 sc1\nQmvaa 216,140,60\nQmvaa 248,144,60\nbuffer_load_dwordx4 v42, s[52:55], s95 offen offset:2048 lds\nQmvaa 116,148,60\ns_waitcnt vmcnt(27)\nds_write_b128 v52, a[224:227] offset:16384\n"
"s_waitcnt lgkmcnt(3)\nQmvaa 96,132,64\nQmvaa 128,136,64\nQmvaa 160,140,64\nQmvaa 192,144,64\nQda 68,53,50176\nv_add_u32 v75, 1, v75\nQmvaa 224,148,64\n"
"s_waitcnt vmcnt(33)\nds_write_b32 v73, v75\ns_waitcnt lgkmcnt(3)\nQmvva 92,0,44\nbuffer_load_dwordx4 v43, s[52:55], s95 offen offset:3072 lds\nQmvva 124,4,44\nbuffer_load_dwordx4 a[24:27], v60, s[48:51], s94 offen offset:2048 sc1\nQmvva 156,8,44\n"
"Qtv 20,47,40960\nQmvva 188,12,44\nQtv 22,47,46080\nQmvva 220,16,44\ns_waitcnt vmcnt(27)\nds_write_b128 v52, a[228:231] offset:20480\nQmvva 252,0,208\nQmvva 100,4,208\n"
"Qtv 24,47,41216\nQmvva 132,8,208\nds_read_b32 v69, v74\nQmvva 164,12,208\nQda 72,53,0\ns_add_u32 m0, s96, 28672\nQmvva 196,16,208\nbuffer_load_dwordx4 v44, s[52:55], s95 offen lds\n"
"Qmvva 228,0,212\nQtv 26,47,46336\nQmvva 108,4,212\ns_waitcnt lgkmcnt(2)\nv_cmp_gt_u32 vcc, v75, v69\nQmvva 140,8,212\ns_cbranch_vccnz .Lso54r1\n.Lro54r1:\n"
"Qmvva 172,12,212\nbuffer_load_dwordx4 a[28:31], v60, s[48:51], s94 offen offset:3072 sc1\nQmvva 204,16,212\ns_waitcnt vmcnt(26)\nds_write_b128 v52, a[232:235] offset:25600\nQmvva 236,0,216\nQmvva 80,4,216\nQtv 28,47,41472\n"
"Qmvva 112,8,216\nQmvva 144,12,216\nQda 76,53,4096\nQmvva 176,16,216\nQtv 30,47,46592\nQmvva 208,0,220\nQtv 32,47,41728\ns_mov_b32 s94, 505856\n"
"Qmvva 240,4,220\nbuffer_load_dwordx4 a[32:35], v60, s[48:51], s94 offen offset:0 sc1\nQmvva 84,8,220\nQtv 34,47,46848\nQmvva 88,12,220\nQtv 36,47,41984\nQmvva 120,16,220\ns_waitcnt vmcnt(25)\n"
"ds_write_b128 v52, a[236:239] offset:29696\nQmvva 152,0,224\nQmvva 184,4,224\nQtv 38,47,47104\nQmvva 216,8,224\nQmvva 248,12,224\nQda 80,53,8192\nQmvva 116,16,224\n"
"Qmvva 96,0,228\nQmvva 128,4,228\nbuffer_load_dwordx4 a[36:39], v60, s[48:51], s94 offen offset:1024 sc1\nQmvva 160,8,228\nQmvva 192,12,228\nv_add_u32 v75, 1, v75\nQmvva 224,16,228\ns_waitcnt vmcnt(27)\n"
"ds_write_b32 v73, v75\ns_waitcnt lgkmcnt(2)\nQmvva 92,20,68\ns_waitcnt vmcnt(25)\nds_write_b128 v52, a[240:243] offset:33792\nQmvva 124,24,68\nQmvva 156,28,68\nQta 112,46,0\n"
"Qmvva 188,32,68\nQda 48,53,12288\nQmvva 220,36,68\nQta 114,46,5120\nQmvva 252,20,72\nQta 116,46,256\nQmvva 100,24,72\nbuffer_load_dwordx4 a[40:43], v60, s[48:51], s94 offen offset:2048 sc1\n"
"Qmvva 132,28,72\nds_read_b32 v69, v74\nQmvva 164,32,72\nQmvva 196,36,72\ns_waitcnt vmcnt(25)\nds_write_b128 v52, a[244:247] offset:37888\ns_waitcnt lgkmcnt(15)\nQmvva 228,20,76\n"
"Qmvva 108,24,76\ns_waitcnt lgkmcnt(1)\nv_cmp_gt_u32 vcc, v75, v69\nQmvva 140,28,76\ns_cbranch_vccnz .Lso55r1\n.Lro55r1:\nQmvva 172,32,76\nQda 52,53,16384\n"
"Qmvva 204,36,76\nQta 118,46,5376\nQmvva 236,20,80\nQta 120,46,512\ns_mov_b32 s94, 620544\nQmvva 80,24,80\nbuffer_load_dwordx4 a[44:47], v60, s[48:51], s94 offen offset:0 sc1\nQmvva 112,28,80\n"
"Qmvva 144,32,80\nQta 122,46,5632\nQmvva 176,36,80\ns_waitcnt vmcnt(25)\nds_write_b128 v52, a[248:251] offset:41984\ns_waitcnt lgkmcnt(4)\nQmvva 208,20,48\nQmvva 240,24,48\n"
"Qta 124,46,768\nQmvva 84,28,48\nQta 126,46,5888\nQmvva 88,32,48\nQda 56,53,20480\nQmvva 120,36,48\nQmvva 152,20,52\nQta 128,46,1024\n"
"Qmvva 184,24,52\nbuffer_load_dwordx4 a[208:211], v60, s[48:51], s94 offen offset:1024 sc1\nQmvva 216,28,52\nQta 130,46,6144\nQmvva 248,32,52\nQta 132,51,0\nQta 134,51,5120\nQmvva 116,36,52\n"
"s_waitcnt vmcnt(24)\nds_write_b128 v52, a[252:255] offset:46080\ns_waitcnt lgkmcnt(5)\nQmvva 96,20,56\nQmvva 128,24,56\nQta 136,51,256\nQmvva 160,28,56\nQta 138,51,5376\n"
"Qmvva 192,32,56\nQda 60,53,25600\nv_add_u32 v75, 1, v75\nQmvva 224,36,56\nds_write_b32 v73, v75\ns_waitcnt lgkmcnt(7)\nQmvaa 92,112,232\nQta 140,51,512\n"
"Qmvaa 124,116,232\nbuffer_load_dwordx4 a[212:215], v60, s[48:51], s94 offen offset:2048 sc1\nQmvaa 156,120,232\nQta 142,51,5632\nQmvaa 188,124,232\nQta 144,51,768\nQmvaa 220,128,232\ns_waitcnt vmcnt(23)\n"
"ds_write_b128 v52, a[168:171] offset:50176\nQmvaa 252,112,236\nQmvaa 100,116,236\nQta 146,51,5888\nQmvaa 132,120,236\nds_read_b32 v69, v74\nQmvaa 164,124,236\nQda 64,53,29696\n"
"Qmvaa 196,128,236\nQta 148,51,1024\nQmvaa 228,112,240\nQta 150,51,6144\nQmvaa 108,116,240\ns_waitcnt lgkmcnt(3)\nv_cmp_gt_u32 vcc, v75, v69\nQmvaa 140,120,240\n"
"s_mov_b32 s94, 735232\ns_cbranch_vccnz .Lso56r1\n.Lro56r1:\nQmvaa 172,124,240\nbuffer_load_dwordx4 a[216:219], v60, s[48:51], s94 offen offset:0 sc1\nQmvaa 204,128,240\ns_waitcnt vmcnt(21)\nds_write_b128 v52, a[172:175] offset:0\n"
"Qmvaa 236,112,244\nQmvaa 80,116,244\nQtv 0,46,20480\nQmvaa 112,120,244\nQtv 2,46,25600\nQmvaa 144,124,244\nQda 68,53,33792\nQmvaa 176,128,244\n"
"Qtv 4,46,20736\ns_waitcnt lgkmcnt(5)\nQmvaa 92,132,60\nQtv 6,46,25856\nQmvaa 124,136,60\nbuffer_load_dwordx4 a[220:223], v60, s[48:51], s94 offen offset:1024 sc1\nQmvaa 156,140,60\nQtv 8,46,20992\n"
"Qmvaa 188,144,60\nQtv 10,46,26112\nQmvaa 220,148,60\ns_waitcnt vmcnt(20)\nds_write_b128 v52, a[0:3] offset:4096\nQmvaa 252,132,64\nQmvaa 100,136,64\nQtv 12,46,21248\n"
"Qmvaa 132,140,64\nQtv 14,46,26368\nQmvaa 164,144,64\nQda 72,53,37888\nQmvaa 196,148,64\nQtv 16,46,21504\ns_waitcnt lgkmcnt(1)\nQmvaa 228,132,68\n"
"Qtv 18,46,26624\nQmvaa 108,136,68\nbuffer_load_dwordx4 a[224:227], v60, s[48:51], s94 offen offset:2048 sc1\nQmvaa 140,140,68\nQtv 20,51,20480\nQmvaa 172,144,68\nQtv 22,51,25600\nv_add_u32 v75, 1, v75\n"
"Qmvaa 204,148,68\ns_waitcnt vmcnt(22)\nds_write_b32 v73, v75\nQmvaa 236,132,72\ns_waitcnt vmcnt(20)\nds_write_b128 v52, a[4:7] offset:8192\nQmvaa 80,136,72\nQmvaa 112,140,72\n"
"Qtv 24,51,20736\nQmvaa 144,144,72\nQda 76,53,41984\nQmvaa 176,148,72\nQtv 26,51,25856\ns_waitcnt lgkmcnt(7)\nQmvva 92,0,248\nQtv 28,51,20992\n"
"s_mov_b32 s94, 849920\nQmvva 124,4,248\nbuffer_load_dwordx4 a[228:231], v60, s[48:51], s94 offen offset:0 sc1\nQmvva 156,8,248\nds_read_b32 v69, v74\nQmvva 188,12,248\nQtv 30,51,26112\nQmvva 220,16,248\n"
"s_waitcnt vmcnt(20)\nds_write_b128 v52, a[8:11] offset:12288\nQmvva 252,0,252\nQmvva 100,4,252\ns_waitcnt lgkmcnt(2)\nv_cmp_gt_u32 vcc, v75, v69\nQmvva 132,8,252\ns_cbranch_vccnz .Lso57r1\n"
".Lro57r1:\nQmvva 164,12,252\nQda 80,53,46080\nQmvva 196,16,252\nQtv 32,51,21248\nQmvva 228,0,168\nQtv 34,51,26368\nQmvva 108,4,168\n"
"buffer_load_dwordx4 a[232:235], v60, s[48:51], s94 offen offset:1024 sc1\nQmvva 140,8,168\nQtv 36,51,21504\nQmvva 172,12,168\nQtv 38,51,26624\nQmvva 204,16,168\ns_waitcnt vmcnt(20)\nds_write_b128 v52, a[12:15] offset:16384\n"
"Qmvva 236,0,172\nQmvva 80,4,172\nQta 112,45,0\nQmvva 112,8,172\nQta 114,45,5120\nQmvva 144,12,172\nQda 48,53,50176\nQmvva 176,16,172\n"
"Qta 116,45,256\ns_waitcnt lgkmcnt(5)\nQmvva 92,20,76\nQta 118,45,5376\nQmvva 124,24,76\nbuffer_load_dwordx4 a[236:239], v60, s[48:51], s94 offen offset:2048 sc1\nQmvva 156,28,76\nQta 120,45,512\n"
"Qmvva 188,32,76\nQta 122,45,5632\nQmvva 220,36,76\ns_waitcnt vmcnt(19)\nds_write_b128 v52, a[16:19] offset:20480\nQmvva 252,20,80\nQmvva 100,24,80\nQta 124,45,768\n"
"Qmvva 132,28,80\nQta 126,45,5888\nQmvva 164,32,80\nQda 52,53,0\nv_add_u32 v75, 1, v75\nQmvva 196,36,80\ns_waitcnt vmcnt(13)\nds_write_b32 v73, v75\n"
"s_waitcnt lgkmcnt(1)\nQmvva 228,20,48\nQta 128,45,1024\nQmvva 108,24,48\nQta 130,45,6144\nQmvva 140,28,48\nQta 132,47,0\nQmvva 172,32,48\n"
"Qta 134,47,5120\nQmvva 204,36,48\ns_waitcnt vmcnt(17)\nds_write_b128 v52, a[20:23] offset:25600\nQmvva 236,20,52\nQmvva 80,24,52\nQta 136,47,256\nQmvva 112,28,52\n"
"ds_read_b32 v69, v74\nQmvva 144,32,52\nQda 56,53,4096\nQmvva 176,36,52\nQta 138,47,5376\ns_waitcnt lgkmcnt(15)\nQmvaa 92,112,0\nQta 140,47,512\n"
"Qmvaa 124,116,0\ns_waitcnt lgkmcnt(3)\nv_cmp_gt_u32 vcc, v75, v69\nQmvaa 156,120,0\ns_cbranch_vccnz .Lso58r1\n.Lro58r1:\nQmvaa 188,124,0\nQta 142,47,5632\n"
"Qmvaa 220,128,0\ns_waitcnt vmcnt(14)\nds_write_b128 v52, a[24:27] offset:29696\nQmvaa 252,112,4\nQmvaa 100,116,4\nQta 144,47,768\nQmvaa 132,120,4\nQta 146,47,5888\n"
"Qmvaa 164,124,4\nQda 60,53,8192\nQmvaa 196,128,4\nQta 148,47,1024\nQmvaa 228,112,8\nQta 150,47,6144\nQmvaa 108,116,8\nQmvaa 140,120,8\n"
"Qtv 0,45,20480\nQmvaa 172,124,8\nQtv 2,45,25600\nQmvaa 204,128,8\ns_waitcnt vmcnt(12)\nds_write_b128 v52, a[28:31] offset:33792\nQmvaa 236,112,12\nQmvaa 80,116,12\n"
"Qtv 4,45,20736\nQmvaa 112,120,12\nQtv 6,45,25856\nQmvaa 144,124,12\nQda 64,53,12288\nQmvaa 176,128,12\ns_waitcnt lgkmcnt(6)\nQmvaa 92,132,56\n"
"Qtv 8,45,20992\nQmvaa 124,136,56\nQtv 10,45,26112\nQmvaa 156,140,56\nQtv 12,45,21248\nQmvaa 188,144,56\nv_add_u32 v75, 1, v75\nQmvaa 220,148,56\n"
"ds_write_b32 v73, v75\nQmvaa 252,132,60\ns_waitcnt vmcnt(11)\nds_write_b128 v52, a[32:35] offset:37888\nQmvaa 100,136,60\nQmvaa 132,140,60\nQtv 14,45,26368\nQmvaa 164,144,60\n"
"Qda 68,53,16384\nQmvaa 196,148,60\nQtv 16,45,21504\ns_waitcnt lgkmcnt(1)\nQmvaa 228,132,64\nQtv 18,45,26624\nQmvaa 108,136,64\nQmvaa 140,140,64\n"
"ds_read_b32 v69, v74\nQmvaa 172,144,64\nQtv 20,47,20480\nQmvaa 204,148,64\ns_waitcnt vmcnt(10)\nds_write_b128 v52, a[36:39] offset:41984\nQmvaa 236,132,68\nQmvaa 80,136,68\n"
"s_waitcnt lgkmcnt(2)\nv_cmp_gt_u32 vcc, v75, v69\nQmvaa 112,140,68\ns_cbranch_vccnz .Lso59r1\n.Lro59r1:\nQmvaa 144,144,68\nQda 72,53,20480\nQmvaa 176,148,68\n"
"Qtv 22,47,25600\ns_waitcnt lgkmcnt(5)\nQmvva 92,0,16\nQtv 24,47,20736\nQmvva 124,4,16\nQtv 26,47,25856\nQmvva 156,8,16\nQtv 28,47,20992\n"
"Qmvva 188,12,16\nQtv 30,47,26112\nQmvva 220,16,16\ns_waitcnt vmcnt(9)\nds_write_b128 v52, a[40:43] offset:46080\nQmvva 252,0,20\nQmvva 100,4,20\nQtv 32,47,21248\n"
"Qmvva 132,8,20\nQtv 34,47,26368\nQmvva 164,12,20\nQda 76,53,25600\nQmvva 196,16,20\nQtv 36,47,21504\nQmvva 228,0,24\nQtv 38,47,26624\n"
"Qmvva 108,4,24\nQmvva 140,8,24\nQta 112,46,0\nQmvva 172,12,24\nQta 114,46,5120\nQmvva 204,16,24\ns_waitcnt vmcnt(8)\nds_write_b128 v52, a[44:47] offset:50176\n"
"Qmvva 236,0,28\nQmvva 80,4,28\nQta 116,46,256\nQmvva 112,8,28\nQta 118,46,5376\nQmvva 144,12,28\nQda 80,53,29696\nv_add_u32 v75, 1, v75\n"
"Qmvva 176,16,28\nds_write_b32 v73, v75\ns_waitcnt lgkmcnt(7)\nQmvva 92,20,72\nQta 120,46,512\ns_add_u32 s68, s64, 0\nQmvva 124,24,72\nQta 122,46,5632\n"
"Qmvva 156,28,72\nQta 124,46,768\nQmvva 188,32,72\nQta 126,46,5888\nQmvva 220,36,72\ns_waitcnt vmcnt(7)\nds_write_b128 v52, a[208:211] offset:0\nQmvva 252,20,76\n"
"Qmvva 100,24,76\nQta 128,46,1024\nQk 92,92,93\nQmvva 132,28,76\nds_read_b32 v69, v74\nv_min_u32 v92, -2, v92\nQmvva 164,32,76\nQda 48,53,33792\n"
"Qk 93,94,95\nQmvva 196,36,76\nQta 130,46,6144\nv_min_u32 v93, -2, v93\ns_waitcnt lgkmcnt(10)\nQmvva 228,20,80\nQk 94,124,125\nv_min_u32 v94, -2, v94\n"
"Qmvva 108,24,80\ns_waitcnt lgkmcnt(1)\nv_cmp_gt_u32 vcc, v75, v69\nQmvva 140,28,80\ns_cbranch_vccnz .Lso60r1\n.Lro60r1:\nQk 95,126,127\nQmvva 172,32,80\n"
"Qta 132,51,0\nv_min_u32 v95, -2, v95\nQmvva 204,36,80\ns_waitcnt vmcnt(6)\nds_write_b128 v52, a[212:215] offset:4096\nQmvva 236,20,48\nbuffer_store_dwordx4 v[92:95], v60, s[24:27], s68 offen offset:0 sc1\nQk 156,156,157\n"
"Qmvva 80,24,48\nQta 134,51,5120\nv_min_u32 v156, -2, v156\nQmvva 112,28,48\nQta 136,51,256\nQk 157,158,159\nQmvva 144,32,48\nQda 52,53,37888\n"
"v_min_u32 v157, -2, v157\nQmvva 176,36,48\nQta 138,51,5376\nQk 158,188,189\ns_waitcnt lgkmcnt(6)\nQmvaa 208,112,32\nQta 140,51,512\nv_min_u32 v158, -2, v158\n"
"Qmvaa 240,116,32\nQk 159,190,191\nv_min_u32 v159, -2, v159\nQmvaa 84,120,32\nQta 142,51,5632\nbuffer_store_dwordx4 v[156:159], v60, s[24:27], s68 offen offset:1024 sc1\nQmvaa 88,124,32\nQta 144,51,768\n"
"Qk 220,220,221\nQmvaa 120,128,32\ns_waitcnt vmcnt(7)\nds_write_b128 v52, a[216:219] offset:8192\nQmvaa 152,112,36\nv_min_u32 v220, -2, v220\nQk 221,222,223\nQmvaa 184,116,36\n"
"Qta 146,51,5888\nv_min_u32 v221, -2, v221\nQmvaa 216,120,36\nQta 148,51,1024\nQk 222,252,253\nQmvaa 248,124,36\nQda 56,53,41984\nv_min_u32 v222, -2, v222\n"
"Qmvaa 116,128,36\nQta 150,51,6144\nQk 223,254,255\nQmvaa 96,112,40\nQtv 0,46,20480\nQtv 2,46,25600\nQmvaa 128,116,40\nQtv 4,46,20736\n"
"v_min_u32 v223, -2, v223\nQmvaa 160,120,40\nQtv 6,46,25856\nbuffer_store_dwordx4 v[220:223], v60, s[24:27], s68 offen offset:2048 sc1\nQmvaa 192,124,40\nQtv 8,46,20992\nv_add_u32 v75, 1, v75\nQmvaa 224,128,40\n"
"ds_write_b32 v73, v75\nQk 100,100,101\ns_waitcnt lgkmcnt(6)\nQmvaa 208,132,52\ns_waitcnt vmcnt(7)\nds_write_b128 v52, a[220:223] offset:12288\nQmvaa 240,136,52\nv_min_u32 v100, -2, v100\n"
"Qk 101,102,103\nQmvaa 84,140,52\nQtv 10,46,26112\nv_min_u32 v101, -2, v101\nQmvaa 88,144,52\nQda 60,53,46080\nQk 102,132,133\nQmvaa 120,148,52\n"
"Qtv 12,46,21248\nv_min_u32 v102, -2, v102\nQmvaa 152,132,56\nQtv 14,46,26368\nQk 103,134,135\nQmvaa 184,136,56\nQtv 16,46,21504\nv_min_u32 v103, -2, v103\n"
"Qmvaa 216,140,56\nds_read_b32 v69, v74\nbuffer_store_dwordx4 v[100:103], v60, s[24:27], s68 offen offset:3072 sc1\nQmvaa 248,144,56\nQtv 18,46,26624\nQk 164,164,165\nQmvaa 116,148,56\ns_waitcnt vmcnt(7)\n"
"ds_write_b128 v52, a[224:227] offset:16384\ns_waitcnt lgkmcnt(6)\nQmvaa 96,132,60\nv_min_u32 v164, -2, v164\nQk 165,166,167\nQmvaa 128,136,60\ns_waitcnt lgkmcnt(2)\nv_cmp_gt_u32 vcc, v75, v69\n"
"Qmvaa 160,140,60\ns_cbranch_vccnz .Lso61r1\n.Lro61r1:\nv_min_u32 v165, -2, v165\nQmvaa 192,144,60\nQda 64,53,50176\nQk 166,196,197\nQmvaa 224,148,60\n"
"Qtv 20,51,20480\nQtv 22,51,25600\ns_waitcnt lgkmcnt(4)\nQmvva 208,0,44\nQtv 24,51,20736\nv_min_u32 v166, -2, v166\nQmvva 240,4,44\nQtv 26,51,25856\n"
"Qk 167,198,199\nQmvva 84,8,44\nQtv 28,51,20992\nv_min_u32 v167, -2, v167\nQmvva 88,12,44\nQtv 30,51,26112\ns_add_u32 s68, s64, 4096\nQmvva 120,16,44\n"
"s_waitcnt vmcnt(6)\nds_write_b128 v52, a[228:231] offset:20480\nQmvva 152,0,208\nbuffer_store_dwordx4 v[164:167], v60, s[24:27], s68 offen offset:0 sc1\nQk 228,228,229\nQmvva 184,4,208\nQtv 32,51,21248\nv_min_u32 v228, -2, v228\n"
"Qmvva 216,8,208\nQtv 34,51,26368\nQk 229,230,231\nQmvva 248,12,208\nQda 68,53,0\nv_min_u32 v229, -2, v229\nQmvva 116,16,208\nQtv 36,51,21504\n"
"Qk 230,108,109\nQmvva 96,0,212\nQtv 38,51,26624\nv_min_u32 v230, -2, v230\nQmvva 128,4,212\nQta 112,45,0\nQta 114,45,5120\nQmvva 160,8,212\n"
"Qta 116,45,256\nQk 231,110,111\nQmvva 192,12,212\nQta 118,45,5376\nv_min_u32 v231, -2, v231\nQmvva 224,16,212\ns_waitcnt vmcnt(6)\nds_write_b128 v52, a[232:235] offset:25600\n"
"s_waitcnt lgkmcnt(5)\nQmvva 208,20,64\nbuffer_store_dwordx4 v[228:231], v60, s[24:27], s68 offen offset:1024 sc1\nQk 140,140,141\nQmvva 240,24,64\nQta 120,45,512\nv_min_u32 v140, -2, v140\nQmvva 84,28,64\n"
"Qta 122,45,5632\nQk 141,142,143\nQmvva 88,32,64\nQda 72,53,4096\nv_add_u32 v75, 1, v75\nQmvva 120,36,64\nds_write_b32 v73, v75\nv_min_u32 v141, -2, v141\n"
"Qmvva 152,20,68\nQta 124,45,768\nQk 142,172,173\nQmvva 184,24,68\nQta 126,45,5888\nv_min_u32 v142, -2, v142\nQmvva 216,28,68\nQta 128,45,1024\n"
"Qk 143,174,175\nQmvva 248,32,68\nQta 130,45,6144\nv_min_u32 v143, -2, v143\nQmvva 116,36,68\ns_waitcnt vmcnt(6)\nds_write_b128 v52, a[236:239] offset:29696\ns_waitcnt lgkmcnt(6)\n"
"Qmvva 96,20,72\nbuffer_store_dwordx4 v[140:143], v60, s[24:27], s68 offen offset:2048 sc1\nQk 204,204,205\nQmvva 128,24,72\nQta 132,47,0\nv_min_u32 v204, -2, v204\nQmvva 160,28,72\nds_read_b32 v69, v74\n"
"Qk 205,206,207\nQmvva 192,32,72\nQda 76,53,8192\nv_min_u32 v205, -2, v205\nQmvva 224,36,72\nQta 134,47,5120\nQk 206,236,237\ns_waitcnt lgkmcnt(14)\n"
"Qmvaa 208,112,216\nQta 136,47,256\nv_min_u32 v206, -2, v206\nQmvaa 240,116,216\ns_waitcnt lgkmcnt(3)\nv_cmp_gt_u32 vcc, v75, v69\nQmvaa 84,120,216\ns_cbranch_vccnz .Lso62r1\n"
".Lro62r1:\nQk 207,238,239\nQmvaa 88,124,216\nQta 138,47,5376\nv_min_u32 v207, -2, v207\nQmvaa 120,128,216\nQta 140,47,512\ns_add_u32 s68, s64, 14336\n"
"Qmvaa 152,112,220\nQta 142,47,5632\nbuffer_store_dwordx4 v[204:207], v60, s[24:27], s68 offen offset:0 sc1\nQmvaa 184,116,220\nQta 144,47,768\nQk 80,80,81\nQmvaa 216,120,220\nQta 146,47,5888\n"
"v_min_u32 v80, -2, v80\nQmvaa 248,124,220\nQda 80,53,12288\nQk 81,82,83\nQmvaa 116,128,220\nQta 148,47,1024\nv_min_u32 v81, -2, v81\nQmvaa 96,112,224\n"
"Qta 150,47,6144\nQk 82,112,113\nQmvaa 128,116,224\nQtv 0,45,20480\nv_min_u32 v82, -2, v82\nQmvaa 160,120,224\nQtv 2,45,25600\nQk 83,114,115\n"
"Qmvaa 192,124,224\nQtv 4,45,20736\nv_min_u32 v83, -2, v83\nQmvaa 224,128,224\nQtv 6,45,25856\nbuffer_store_dwordx4 v[80:83], v60, s[24:27], s68 offen offset:1024 sc1\ns_waitcnt lgkmcnt(4)\nQmvaa 208,132,76\n"
"Qtv 8,45,20992\nQk 144,144,145\nQmvaa 240,136,76\nQtv 10,45,26112\nv_min_u32 v144, -2, v144\nQmvaa 84,140,76\nQtv 12,45,21248\nQk 145,146,147\n"
"Qmvaa 88,144,76\nQda 48,53,16384\nv_min_u32 v145, -2, v145\nQmvaa 120,148,76\nQtv 14,45,26368\nQk 146,176,177\nQmvaa 152,132,80\nQtv 16,45,21504\n"
"v_min_u32 v146, -2, v146\nQmvaa 184,136,80\nQtv 18,45,26624\nQk 147,178,179\nQmvaa 216,140,80\nQtv 20,47,20480\nv_min_u32 v147, -2, v147\nQmvaa 248,144,80\n"
"Qtv 22,47,25600\nv_add_u32 v75, 1, v75\nQmvaa 116,148,80\nds_write_b32 v73, v75\nbuffer_store_dwordx4 v[144:147], v60, s[24:27], s68 offen offset:2048 sc1\ns_waitcnt lgkmcnt(6)\nQmvaa 96,132,48\nQtv 24,47,20736\n"
"Qmvaa 128,136,48\nQtv 26,47,25856\nQmvaa 160,140,48\nQtv 28,47,20992\nQmvaa 192,144,48\nQda 52,53,20480\nQmvaa 224,148,48\nQtv 30,47,26112\n"
"s_waitcnt lgkmcnt(8)\nQmvva 208,0,228\nQtv 32,47,21248\nQmvva 240,4,228\nQtv 34,47,26368\nQmvva 84,8,228\nds_read_b32 v69, v74\nQmvva 88,12,228\n"
"Qtv 36,47,21504\nQmvva 120,16,228\nQtv 38,47,26624\nQmvva 152,0,232\nQmvva 184,4,232\ns_waitcnt lgkmcnt(2)\nv_cmp_gt_u32 vcc, v75, v69\nQmvva 216,8,232\n"
"s_cbranch_vccnz .Lso63r1\n.Lro63r1:\nQmvva 248,12,232\nQda 56,53,25600\nQmvva 116,16,232\nQmvva 96,0,236\ns_add_u32 s69, s98, 0\nbuffer_load_dwordx4 v[124:127], v60, s[24:27], s69 offen offset:0 sc1\n"
"Qmvva 128,4,236\nQmvva 160,8,236\nbuffer_load_dwordx4 v[188:191], v60, s[24:27], s69 offen offset:1024 sc1\nQmvva 192,12,236\nQmvva 224,16,236\nbuffer_load_dwordx4 v[252:255], v60, s[24:27], s69 offen offset:2048 sc1\ns_waitcnt lgkmcnt(0)\nQmvva 208,20,52\n"
"Qmvva 240,24,52\nbuffer_load_dwordx4 v[132:135], v60, s[24:27], s69 offen offset:3072 sc1\nQmvva 84,28,52\nQmvva 88,32,52\nQda 60,53,29696\nQmvva 120,36,52\ns_add_u32 s69, s98, 4096\nbuffer_load_dwordx4 v[196:199], v60, s[24:27], s69 offen offset:0 sc1\n"
"Qmvva 152,20,56\nQmvva 184,24,56\nbuffer_load_dwordx4 v[108:111], v60, s[24:27], s69 offen offset:1024 sc1\nQmvva 216,28,56\nQmvva 248,32,56\nbuffer_load_dwordx4 v[172:175], v60, s[24:27], s69 offen offset:2048 sc1\nQmvva 116,36,56\ns_waitcnt lgkmcnt(0)\n"
"Qmvva 96,20,60\ns_add_u32 s69, s98, 14336\nbuffer_load_dwordx4 v[236:239], v60, s[24:27], s69 offen offset:0 sc1\nQmvva 128,24,60\nQmvva 160,28,60\nQmvva 192,32,60\nQmvva 224,36,60\ns_barrier\n"
"s_mov_b32 s97, 0\n.Lrx1:\ns_waitcnt vmcnt(0)\nv_max_u32 v40, v124, v125\nv_max3_u32 v40, v40, v126, v127\nv_max3_u32 v40, v40, v188, v189\nv_max3_u32 v40, v40, v190, v191\nv_max3_u32 v40, v40, v252, v253\n"
"v_max3_u32 v40, v40, v254, v255\nv_max3_u32 v40, v40, v132, v133\nv_max3_u32 v40, v40, v134, v135\nv_max3_u32 v40, v40, v196, v197\nv_max3_u32 v40, v40, v198, v199\nv_max3_u32 v40, v40, v108, v109\nv_max3_u32 v40, v40, v110, v111\nv_max3_u32 v40, v40, v172, v173\n"
"v_max3_u32 v40, v40, v174, v175\nv_max3_u32 v40, v40, v236, v237\nv_cmp_eq_u32 vcc, -1, v40\ns_cmp_eq_u64 vcc, 0\ns_cbranch_scc1 .Lrx1_ok\ns_add_u32 s97, s97, 1\ns_cmp_gt_u32 s97, 1048576\ns_cbranch_scc1 .Lrx1_ok\n"
"s_add_u32 s69, s98, 0\nbuffer_load_dwordx4 v[124:127], v60, s[24:27], s69 offen offset:0 sc1\nbuffer_load_dwordx4 v[188:191], v60, s[24:27], s69 offen offset:1024 sc1\nbuffer_load_dwordx4 v[252:255], v60, s[24:27], s69 offen offset:2048 sc1\nbuffer_load_dwordx4 v[132:135], v60, s[24:27], s69 offen offset:3072 sc1\ns_add_u32 s69, s98, 4096\nbuffer_load_dwordx4 v[196:199], v60, s[24:27], s69 offen offset:0 sc1\nbuffer_load_dwordx4 v[108:111], v60, s[24:27], s69 offen offset:1024 sc1\n"
"buffer_load_dwordx4 v[172:175], v60, s[24:27], s69 offen offset:2048 sc1\ns_add_u32 s69, s98, 14336\nbuffer_load_dwordx4 v[236:239], v60, s[24:27], s69 offen offset:0 sc1\ns_branch .Lrx1\n.Lrx1_ok:\nv_lshlrev_b32 v41, 16, v124\nv_and_b32 v42, 0xffff0000, v124\nQa 208,208,41\n"
"Qa 209,209,42\nv_lshlrev_b32 v43, 16, v125\nv_and_b32 v44, 0xffff0000, v125\nQa 210,210,43\nQa 211,211,44\nQk 208,208,209\nQk 209,210,211\nds_write_b64 v61, v[208:209] offset:21504\n"
"v_lshlrev_b32 v41, 16, v126\nv_and_b32 v42, 0xffff0000, v126\nQa 240,240,41\nQa 241,241,42\nv_lshlrev_b32 v43, 16, v127\nv_and_b32 v44, 0xffff0000, v127\nQa 242,242,43\nQa 243,243,44\n"
"Qk 240,240,241\nQk 241,242,243\nds_write_b64 v61, v[240:241] offset:21536\nv_lshlrev_b32 v41, 16, v188\nv_and_b32 v42, 0xffff0000, v188\nQa 84,84,41\nQa 85,85,42\nv_lshlrev_b32 v43, 16, v189\n"
"v_and_b32 v44, 0xffff0000, v189\nQa 86,86,43\nQa 87,87,44\nQk 84,84,85\nQk 85,86,87\nds_write_b64 v61, v[84:85] offset:21568\nv_lshlrev_b32 v41, 16, v190\nv_and_b32 v42, 0xffff0000, v190\n"
"Qa 88,88,41\nQa 89,89,42\nv_lshlrev_b32 v43, 16, v191\nv_and_b32 v44, 0xffff0000, v191\nQa 90,90,43\nQa 91,91,44\nQk 88,88,89\nQk 89,90,91\n"
"ds_write_b64 v61, v[88:89] offset:21600\nv_lshlrev_b32 v41, 16, v252\nv_and_b32 v42, 0xffff0000, v252\nQa 120,120,41\nQa 121,121,42\nv_lshlrev_b32 v43, 16, v253\nv_and_b32 v44, 0xffff0000, v253\nQa 122,122,43\n"
"Qa 123,123,44\nQk 120,120,121\nQk 121,122,123\nds_write_b64 v61, v[120:121] offset:21632\nv_lshlrev_b32 v41, 16, v254\nv_and_b32 v42, 0xffff0000, v254\nQa 152,152,41\nQa 153,153,42\n"
"v_lshlrev_b32 v43, 16, v255\nv_and_b32 v44, 0xffff0000, v255\nQa 154,154,43\nQa 155,155,44\nQk 152,152,153\nQk 153,154,155\nds_write_b64 v61, v[152:153] offset:26880\nv_lshlrev_b32 v41, 16, v132\n"
"v_and_b32 v42, 0xffff0000, v132\nQa 184,184,41\nQa 185,185,42\nv_lshlrev_b32 v43, 16, v133\nv_and_b32 v44, 0xffff0000, v133\nQa 186,186,43\nQa 187,187,44\nQk 184,184,185\n"
"Qk 185,186,187\nds_write_b64 v61, v[184:185] offset:26912\nv_lshlrev_b32 v41, 16, v134\nv_and_b32 v42, 0xffff0000, v134\nQa 216,216,41\nQa 217,217,42\nv_lshlrev_b32 v43, 16, v135\nv_and_b32 v44, 0xffff0000, v135\n"
"Qa 218,218,43\nQa 219,219,44\nQk 216,216,217\nQk 217,218,219\nds_write_b64 v61, v[216:217] offset:26944\nv_lshlrev_b32 v41, 16, v196\nv_and_b32 v42, 0xffff0000, v196\nQa 248,248,41\n"
"Qa 249,249,42\nv_lshlrev_b32 v43, 16, v197\nv_and_b32 v44, 0xffff0000, v197\nQa 250,250,43\nQa 251,251,44\nQk 248,248,249\nQk 249,250,251\nds_write_b64 v61, v[248:249] offset:26976\n"
"v_lshlrev_b32 v41, 16, v198\nv_and_b32 v42, 0xffff0000, v198\nQa 116,116,41\nQa 117,117,42\nv_lshlrev_b32 v43, 16, v199\nv_and_b32 v44, 0xffff0000, v199\nQa 118,118,43\nQa 119,119,44\n"
"Qk 116,116,117\nQk 117,118,119\nds_write_b64 v61, v[116:117] offset:27008\nv_lshlrev_b32 v41, 16, v108\nv_and_b32 v42, 0xffff0000, v108\nQa 96,96,41\nQa 97,97,42\nv_lshlrev_b32 v43, 16, v109\n"
"v_and_b32 v44, 0xffff0000, v109\nQa 98,98,43\nQa 99,99,44\nQk 96,96,97\nQk 97,98,99\nds_write_b64 v61, v[96:97] offset:32256\nv_lshlrev_b32 v41, 16, v110\nv_and_b32 v42, 0xffff0000, v110\n"
"Qa 128,128,41\nQa 129,129,42\nv_lshlrev_b32 v43, 16, v111\nv_and_b32 v44, 0xffff0000, v111\nQa 130,130,43\nQa 131,131,44\nQk 128,128,129\nQk 129,130,131\n"
"ds_write_b64 v61, v[128:129] offset:32288\nv_lshlrev_b32 v41, 16, v172\nv_and_b32 v42, 0xffff0000, v172\nQa 160,160,41\nQa 161,161,42\nv_lshlrev_b32 v43, 16, v173\nv_and_b32 v44, 0xffff0000, v173\nQa 162,162,43\n"
"Qa 163,163,44\nQk 160,160,161\nQk 161,162,163\nds_write_b64 v61, v[160:161] offset:32320\nv_lshlrev_b32 v41, 16, v174\nv_and_b32 v42, 0xffff0000, v174\nQa 192,192,41\nQa 193,193,42\n"
"v_lshlrev_b32 v43, 16, v175\nv_and_b32 v44, 0xffff0000, v175\nQa 194,194,43\nQa 195,195,44\nQk 192,192,193\nQk 193,194,195\nds_write_b64 v61, v[192:193] offset:32352\nv_lshlrev_b32 v41, 16, v236\n"
"v_and_b32 v42, 0xffff0000, v236\nQa 224,224,41\nQa 225,225,42\nv_lshlrev_b32 v43, 16, v237\nv_and_b32 v44, 0xffff0000, v237\nQa 226,226,43\nQa 227,227,44\nQk 224,224,225\n"
"Qk 225,226,227\nds_write_b64 v61, v[224:225] offset:32384\n.Lfin:\ns_waitcnt lgkmcnt(0)\ns_barrier\nQdv 0,62,0\nQdv 4,62,5376\nQdv 8,62,10752\n"
"Qdv 12,62,16128\nQdv 16,62,21504\nQdv 20,62,26880\nQdv 24,62,32256\nQdv 28,70,0\nQdv 32,70,21504\ns_add_u32 s68, s65, 0\ns_waitcnt lgkmcnt(8)\n"
"buffer_store_dwordx4 v[0:3], v63, s[40:43], s68 offen sc1\ns_add_u32 s68, s65, 40960\ns_waitcnt lgkmcnt(7)\nbuffer_store_dwordx4 v[4:7], v63, s[40:43], s68 offen sc1\ns_add_u32 s68, s65, 81920\ns_waitcnt lgkmcnt(6)\nbuffer_store_dwordx4 v[8:11], v63, s[40:43], s68 offen sc1\ns_add_u32 s68, s65, 122880\n"
"s_waitcnt lgkmcnt(5)\nbuffer_store_dwordx4 v[12:15], v63, s[40:43], s68 offen sc1\ns_add_u32 s68, s60, 163840\ns_waitcnt lgkmcnt(4)\nbuffer_store_dwordx4 v[16:19], v63, s[40:43], s68 offen sc1\ns_add_u32 s68, s60, 204800\ns_waitcnt lgkmcnt(3)\nbuffer_store_dwordx4 v[20:23], v63, s[40:43], s68 offen sc1\n"
"s_add_u32 s68, s60, 245760\ns_waitcnt lgkmcnt(2)\nbuffer_store_dwordx4 v[24:27], v63, s[40:43], s68 offen sc1\ns_add_u32 s68, s60, 163840\ns_cmp_eq_u32 s70, 3\ns_cselect_b32 s68, 0x7ffff000, s68\ns_waitcnt lgkmcnt(1)\nbuffer_store_dwordx4 v[28:31], v71, s[40:43], s65 offen sc1\n"
"s_waitcnt lgkmcnt(0)\nbuffer_store_dwordx4 v[32:35], v71, s[40:43], s68 offen sc1\ns_waitcnt vmcnt(0) lgkmcnt(0)\n"
  :: "s"((unsigned)kp), "s"((unsigned)(kp >> 32)), "s"((unsigned)vp), "s"((unsigned)(vp >> 32)), "s"((unsigned)xp), "s"((unsigned)(xp >> 32)),
     "s"((unsigned)wp), "s"((unsigned)(wp >> 32)), "s"((unsigned)op), "s"((unsigned)(op >> 32)), "s"(ldsb), "s"(wid), "s"(head0),
     "s"((unsigned)dp), "s"((unsigned)(dp >> 32)), "s"((unsigned)wop), "s"((unsigned)(wop >> 32)), "s"((unsigned)yp), "s"((unsigned)(yp >> 32)),
     "s"(hg), "s"(b * 8u + hg)
  : "v0", "v1", "v2", "v3", "v4", "v5", "v6", "v7", "v8", "v9", "v10", "v11", "v12", "v13", "v14", "v15", "v16", "v17", "v18", "v19", "v20", "v21", "v22", "v23", "v24", "v25", "v26", "v27", "v28", "v29", "v30", "v31", "v32", "v33", "v34", "v35", "v36", "v37", "v38", "v39", "v40", "v41", "v42", "v43", "v44", "v45", "v46", "v47", "v48", "v49", "v50", "v51", "v52", "v53", "v54", "v55", "v56", "v57", "v58", "v59", "v60", "v61", "v62", "v63", "v64", "v65", "v66", "v67", "v68", "v69", "v70", "v71", "v72", "v73", "v74", "v75", "v76", "v77", "v78", "v79", "v80", "v81", "v82", "v83", "v84", "v85", "v86", "v87", "v88", "v89", "v90", "v91", "v92", "v93", "v94", "v95", "v96", "v97", "v98", "v99", "v100", "v101", "v102", "v103", "v104", "v105", "v106", "v107", "v108", "v109", "v110", "v111", "v112", "v113", "v114", "v115", "v116", "v117", "v118", "v119", "v120", "v121", "v122", "v123", "v124", "v125", "v126", "v127", "v128", "v129", "v130", "v131", "v132", "v133", "v134", "v135", "v136", "v137", "v138", "v139", "v140", "v141", "v142", "v143", "v144", "v145", "v146", "v147", "v148", "v149", "v150", "v151", "v152", "v153", "v154", "v155", "v156", "v157", "v158", "v159", "v160", "v161", "v162", "v163", "v164", "v165", "v166", "v167", "v168", "v169", "v170", "v171", "v172", "v173", "v174", "v175", "v176", "v177", "v178", "v179", "v180", "v181", "v182", "v183", "v184", "v185", "v186", "v187", "v188", "v189", "v190", "v191", "v192", "v193", "v194", "v195", "v196", "v197", "v198", "v199", "v200", "v201", "v202", "v203", "v204", "v205", "v206", "v207", "v208", "v209", "v210", "v211", "v212", "v213", "v214", "v215", "v216", "v217", "v218", "v219", "v220", "v221", "v222", "v223", "v224", "v225", "v226", "v227", "v228", "v229", "v230", "v231", "v232", "v233", "v234", "v235", "v236", "v237", "v238", "v239", "v240", "v241", "v242", "v243", "v244", "v245", "v246", "v247", "v248", "v249", "v250", "v251", "v252", "v253", "v254", "v255", "a0", "a1", "a2", "a3", "a4", "a5", "a6", "a7", "a8", "a9", "a10", "a11", "a12", "a13", "a14", "a15", "a16", "a17", "a18", "a19", "a20", "a21", "a22", "a23", "a24", "a25", "a26", "a27", "a28", "a29", "a30", "a31", "a32", "a33", "a34", "a35", "a36", "a37", "a38", "a39", "a40", "a41", "a42", "a43", "a44", "a45", "a46", "a47", "a48", "a49", "a50", "a51", "a52", "a53", "a54", "a55", "a56", "a57", "a58", "a59", "a60", "a61", "a62", "a63", "a64", "a65", "a66", "a67", "a68", "a69", "a70", "a71", "a72", "a73", "a74", "a75", "a76", "a77", "a78", "a79", "a80", "a81", "a82", "a83", "a84", "a85", "a86", "a87", "a88", "a89", "a90", "a91", "a92", "a93", "a94", "a95", "a96", "a97", "a98", "a99", "a100", "a101", "a102", "a103", "a104", "a105", "a106", "a107", "a108", "a109", "a110", "a111", "a112", "a113", "a114", "a115", "a116", "a117", "a118", "a119", "a120", "a121", "a122", "a123", "a124", "a125", "a126", "a127", "a128", "a129", "a130", "a131", "a132", "a133", "a134", "a135", "a136", "a137", "a138", "a139", "a140", "a141", "a142", "a143", "a144", "a145", "a146", "a147", "a148", "a149", "a150", "a151", "a152", "a153", "a154", "a155", "a156", "a157", "a158", "a159", "a160", "a161", "a162", "a163", "a164", "a165", "a166", "a167", "a168", "a169", "a170", "a171", "a172", "a173", "a174", "a175", "a176", "a177", "a178", "a179", "a180", "a181", "a182", "a183", "a184", "a185", "a186", "a187", "a188", "a189", "a190", "a191", "a192", "a193", "a194", "a195", "a196", "a197", "a198", "a199", "a200", "a201", "a202", "a203", "a204", "a205", "a206", "a207", "a208", "a209", "a210", "a211", "a212", "a213", "a214", "a215", "a216", "a217", "a218", "a219", "a220", "a221", "a222", "a223", "a224", "a225", "a226", "a227", "a228", "a229", "a230", "a231", "a232", "a233", "a234", "a235", "a236", "a237", "a238", "a239", "a240", "a241", "a242", "a243", "a244", "a245", "a246", "a247", "a248", "a249", "a250", "a251", "a252", "a253", "a254", "a255", "s24", "s25", "s26", "s27", "s28", "s29", "s30", "s31", "s40", "s41", "s42", "s43", "s44", "s45", "s46", "s47", "s48", "s49", "s50", "s51", "s52", "s53", "s54", "s55", "s56", "s57", "s58", "s59", "s60", "s61", "s62", "s63", "s64", "s65", "s66", "s67", "s68", "s69", "s70", "s71", "s72", "s73", "s74", "s75", "s76", "s77", "s78", "s79", "s80", "s81", "s82", "s83", "s84", "s85", "s86", "s87", "s88", "s89", "s90", "s91", "s92", "s93", "s94", "s95", "s96", "s97", "s98", "vcc", "scc", "memory");
}

extern "C" void kernel_launch(void* const* d_in, const int* in_sizes, int n_in, void* d_out, int out_size, void* d_ws, size_t ws_size, hipStream_t stream) {
  typedef const unsigned short* P;
  (void)hipFuncSetAttribute((const void*)mha2_asm, hipFuncAttributeMaxDynamicSharedMemorySize, 163840);
  hipLaunchKernelGGL(mha2_asm, dim3(256), dim3(256), 163840, stream, (P)d_in[0], (P)d_in[5], (P)d_in[3], (P)d_in[8], (P)d_in[1], (P)d_in[6], (P)d_in[2], (P)d_in[7],
                     (P)d_in[4], (P)d_in[9], (char*)d_ws, (unsigned short*)d_out);
}
```

```cpp
#include <hip/hip_runtime.h>
#include <stdint.h>

__global__ __launch_bounds__(256, 1)
void mha2_asm(const unsigned short* __restrict__ X0, const unsigned short* __restrict__ X1, const unsigned short* __restrict__ W0, const unsigned short* __restrict__ W1,
              const unsigned short* __restrict__ K0, const unsigned short* __restrict__ K1,
              const unsigned short* __restrict__ V0, const unsigned short* __restrict__ V1,
              const unsigned short* __restrict__ WO0, const unsigned short* __restrict__ WO1, char* ws, unsigned short* out) {
  extern __shared__ __attribute__((aligned(16))) char lds[];
  const unsigned c = blockIdx.x;
  const unsigned xcd = c & 7u, idx = c >> 3;
  const unsigned bbi = idx >> 3, hg = idx & 7u;
  const unsigned bb = xcd * 4u + bbi;
  const unsigned bk = bb >> 4, b = bb & 15u;
  const unsigned wid = __builtin_amdgcn_readfirstlane(threadIdx.x >> 6);
  const unsigned head0 = (hg >> 2) * 16u + (hg & 3u) * 2u + (wid >> 1);
  const unsigned long long kp = (unsigned long long)(bk ? K1 : K0) + ((unsigned long long)b * 32u + head0) * 262144ull - 1024ull;
  const unsigned long long vp = (unsigned long long)(bk ? V1 : V0) + ((unsigned long long)b * 32u + head0) * 262144ull - 1024ull;
  const unsigned long long xp = (unsigned long long)(bk ? X1 : X0) + (unsigned long long)b * (224u * 1280u);
  const unsigned long long wp = (unsigned long long)(bk ? W1 : W0);
  const unsigned long long op = (unsigned long long)ws + 0ull + (unsigned long long)bb * 2295808ull;
  const unsigned ldsb = (unsigned)(size_t)lds;
  const unsigned long long wop = (unsigned long long)(bk ? WO1 : WO0) - 4096ull;
  const unsigned long long yp = (unsigned long long)out + (unsigned long long)b * (224ull * 2560ull) + bk * 1280u + (hg & 3u) * 320u;
  const unsigned long long dp = (unsigned long long)ws + (100ull << 20) + (c * 4u + wid) * 256u;
  asm volatile(
".macro Qta d,v,o\nds_read_b64_tr_b16 a[\\d:\\d+1], v\\v offset:\\o\n.endm\n.macro Qdv d,v,o\nds_read_b128 v[\\d:\\d+3], v\\v offset:\\o\n.endm\n.macro Qda d,v,o\nds_read_b128 a[\\d:\\d+3], v\\v offset:\\o\n"
".endm\n.macro Qzvav d,a,b\nv_mfma_f32_16x16x32_bf16 v[\\d:\\d+3], a[\\a:\\a+3], v[\\b:\\b+3], 0\n.endm\n.macro Qzaav d,a,b\nv_mfma_f32_16x16x32_bf16 a[\\d:\\d+3], a[\\a:\\a+3], v[\\b:\\b+3], 0\n.endm\n.macro Qmvaa d,a,b\n"
"v_mfma_f32_16x16x32_bf16 v[\\d:\\d+3], a[\\a:\\a+3], a[\\b:\\b+3], v[\\d:\\d+3]\n.endm\n.macro Qmaaa d,a,b\nv_mfma_f32_16x16x32_bf16 a[\\d:\\d+3], a[\\a:\\a+3], a[\\b:\\b+3], a[\\d:\\d+3]\n.endm\n.macro Qmvav d,a,b\nv_mfma_f32_16x16x32_bf16 v[\\d:\\d+3], a[\\a:\\a+3], v[\\b:\\b+3], v[\\d:\\d+3]\n.endm\n"
".macro Qmaav d,a,b\nv_mfma_f32_16x16x32_bf16 a[\\d:\\d+3], a[\\a:\\a+3], v[\\b:\\b+3], a[\\d:\\d+3]\n.endm\n.macro Qk d,a,b\nv_cvt_pk_bf16_f32 v\\d, v\\a, v\\b\n.endm\n.macro Qzvaa d,a,b\nv_mfma_f32_16x16x32_bf16 v[\\d:\\d+3], a[\\a:\\a+3], a[\\b:\\b+3], 0\n"
".endm\n.macro Qa d,a,b\nv_add_f32 v\\d, v\\a, v\\b\n.endm\n.macro Qe d\nv_exp_f32 v\\d, v\\d\n.endm\n.macro Qcvaa d,a,b,c\n"
"v_mfma_f32_16x16x32_bf16 v[\\d:\\d+3], a[\\a:\\a+3], a[\\b:\\b+3], v[\\c:\\c+3]\n.endm\n.macro Qtv d,v,o\nds_read_b64_tr_b16 v[\\d:\\d+1], v\\v offset:\\o\n.endm\n.macro Qzava d,a,b\nv_mfma_f32_16x16x32_bf16 a[\\d:\\d+3], v[\\a:\\a+3], a[\\b:\\b+3], 0\n.endm\n"
".macro Qmava d,a,b\nv_mfma_f32_16x16x32_bf16 a[\\d:\\d+3], v[\\a:\\a+3], a[\\b:\\b+3], a[\\d:\\d+3]\n.endm\n.macro Qmvva d,a,b\nv_mfma_f32_16x16x32_bf16 v[\\d:\\d+3], v[\\a:\\a+3], a[\\b:\\b+3], v[\\d:\\d+3]\n.endm\ns_mov_b32 s52, %6\ns_and_b32 s53, %7, 0xffff\n"
"s_mov_b32 s54, 5242880\ns_mov_b32 s55, 0x20000\ns_mov_b32 s48, %4\ns_and_b32 s49, %5, 0xffff\ns_mov_b32 s50, 286720\ns_mov_b32 s51, 0x20000\ns_mov_b32 s70, %11\ns_mov_b32 s24, %8\n"
"s_and_b32 s25, %9, 0xffff\ns_mov_b32 s26, 2295808\ns_mov_b32 s27, 0x20000\ns_and_b32 s92, s70, 1\ns_lshr_b32 s93, s70, 1\nv_mbcnt_lo_u32_b32 v0, -1, 0\nv_mbcnt_hi_u32_b32 v0, -1, v0\nv_and_b32 v4, 7, v0\n"
"v_lshlrev_b32 v4, 7, v4\nv_add_u32 v76, 2293760, v4\nbuffer_load_dword v65, v76, s[24:27], 0 offen sc1\nv_lshrrev_b32 v2, 4, v0\ns_lshl_b32 s68, s92, 17\nv_and_b32 v4, 3, v0\nv_lshlrev_b32 v4, 4, v4\nv_lshl_add_u32 v4, v2, 6, v4\n"
"v_bfe_u32 v1, v0, 2, 2\nv_lshl_add_u32 v4, v1, 13, v4\nv_add_u32 v62, s68, v4\ns_lshl_b32 s87, %12, 8\ns_mul_i32 s62, s93, 8224\ns_mul_i32 s68, s92, 4112\ns_add_u32 s62, s62, s68\ns_add_u32 s62, s62, %10\n"
"s_add_u32 s62, s62, 33024\ns_lshl_b32 s69, s92, 1\nv_lshrrev_b32 v3, 5, v0\nv_and_b32 v4, 3, v0\nv_xor_b32 v4, s69, v4\nv_lshlrev_b32 v4, 4, v4\nv_lshl_add_u32 v4, v3, 6, v4\nv_bfe_u32 v1, v0, 2, 3\n"
"v_mul_u32_u24 v64, 1280, v1\nv_add_u32 v64, v64, v4\ns_mul_i32 s86, s93, 143360\ns_mul_i32 s68, s92, 10240\ns_add_u32 s86, s86, s68\ns_mul_i32 s63, s92, 14336\ns_mul_i32 s68, s93, 7168\ns_add_u32 s63, s63, s68\n"
"s_add_u32 s63, s63, %10\ns_add_u32 s63, s63, 99072\ns_nop 1\ns_add_u32 m0, s62, 0\ns_add_u32 s69, s87, 0\ns_nop 0\nbuffer_load_dwordx4 v62, s[52:55], s69 offen lds\ns_add_u32 s69, s87, 31744\n"
"s_nop 0\nbuffer_load_dwordx4 v62, s[52:55], s69 offen offset:1024 lds\ns_add_u32 s69, s87, 63480\ns_nop 0\nbuffer_load_dwordx4 v62, s[52:55], s69 offen offset:2056 lds\ns_add_u32 s69, s87, 95224\ns_nop 0\nbuffer_load_dwordx4 v62, s[52:55], s69 offen offset:3080 lds\n"
"s_add_u32 m0, s63, 33024\ns_add_u32 s88, s86, 0\ns_nop 0\nbuffer_load_dwordx4 v64, s[48:51], s88 offen lds\ns_add_u32 s88, s86, 19456\ns_nop 0\nbuffer_load_dwordx4 v64, s[48:51], s88 offen offset:1024 lds\ns_add_u32 s88, s86, 38912\n"
"s_nop 0\nbuffer_load_dwordx4 v64, s[48:51], s88 offen offset:2048 lds\ns_add_u32 s88, s86, 58368\ns_nop 0\nbuffer_load_dwordx4 v64, s[48:51], s88 offen offset:3072 lds\ns_add_u32 m0, s63, 37120\ns_add_u32 s88, s86, 81920\ns_nop 0\n"
"buffer_load_dwordx4 v64, s[48:51], s88 offen lds\ns_add_u32 s88, s86, 101376\ns_nop 0\nbuffer_load_dwordx4 v64, s[48:51], s88 offen offset:1024 lds\ns_add_u32 s88, s86, 120832\ns_nop 0\nbuffer_load_dwordx4 v64, s[48:51], s88 offen offset:2048 lds\ns_add_u32 m0, s62, 16448\n"
"s_add_u32 s69, s87, 262144\ns_nop 0\nbuffer_load_dwordx4 v62, s[52:55], s69 offen lds\ns_add_u32 s69, s87, 293888\ns_nop 0\nbuffer_load_dwordx4 v62, s[52:55], s69 offen offset:1024 lds\ns_add_u32 s69, s87, 325624\ns_nop 0\n"
"buffer_load_dwordx4 v62, s[52:55], s69 offen offset:2056 lds\ns_add_u32 s69, s87, 357368\ns_nop 0\nbuffer_load_dwordx4 v62, s[52:55], s69 offen offset:3080 lds\ns_add_u32 m0, s63, 0\ns_add_u32 s88, s86, 128\ns_nop 0\nbuffer_load_dwordx4 v64, s[48:51], s88 offen lds\n"
"s_add_u32 s88, s86, 19584\ns_nop 0\nbuffer_load_dwordx4 v64, s[48:51], s88 offen offset:1024 lds\ns_add_u32 s88, s86, 39040\ns_nop 0\nbuffer_load_dwordx4 v64, s[48:51], s88 offen offset:2048 lds\ns_add_u32 s88, s86, 58496\ns_nop 0\n"
"buffer_load_dwordx4 v64, s[48:51], s88 offen offset:3072 lds\ns_add_u32 m0, s63, 4096\ns_add_u32 s88, s86, 82048\ns_nop 0\nbuffer_load_dwordx4 v64, s[48:51], s88 offen lds\ns_add_u32 s88, s86, 101504\ns_nop 0\nbuffer_load_dwordx4 v64, s[48:51], s88 offen offset:1024 lds\n"
"s_add_u32 s88, s86, 120960\ns_nop 0\nbuffer_load_dwordx4 v64, s[48:51], s88 offen offset:2048 lds\ns_add_u32 m0, s62, 33024\ns_add_u32 s69, s87, 524288\ns_nop 0\nbuffer_load_dwordx4 v62, s[52:55], s69 offen lds\ns_add_u32 s69, s87, 556032\n"
"s_nop 0\nbuffer_load_dwordx4 v62, s[52:55], s69 offen offset:1024 lds\ns_add_u32 s69, s87, 587768\ns_nop 0\nbuffer_load_dwordx4 v62, s[52:55], s69 offen offset:2056 lds\ns_add_u32 s69, s87, 619512\ns_nop 0\nbuffer_load_dwordx4 v62, s[52:55], s69 offen offset:3080 lds\n"
"s_add_u32 m0, s62, 49472\ns_add_u32 s69, s87, 786432\ns_nop 0\nbuffer_load_dwordx4 v62, s[52:55], s69 offen lds\ns_add_u32 s69, s87, 818176\ns_nop 0\nbuffer_load_dwordx4 v62, s[52:55], s69 offen offset:1024 lds\ns_add_u32 s69, s87, 849912\n"
"s_nop 0\nbuffer_load_dwordx4 v62, s[52:55], s69 offen offset:2056 lds\ns_add_u32 s69, s87, 881656\ns_nop 0\nbuffer_load_dwordx4 v62, s[52:55], s69 offen offset:3080 lds\ns_mov_b32 s40, %0\ns_and_b32 s41, %1, 0xffff\ns_mov_b32 s42, 2360320\n"
"s_mov_b32 s43, 0x20000\ns_mov_b32 s44, %2\ns_and_b32 s45, %3, 0xffff\ns_mov_b32 s46, 2360320\ns_mov_b32 s47, 0x20000\ns_mov_b32 s56, %8\ns_and_b32 s57, %9, 0xffff\ns_mov_b32 s58, 0\n"
"s_mov_b32 s59, 0x20000\ns_mov_b32 s101, 0\ns_mov_b32 s89, s101\ns_mov_b32 s60, 0\ns_mov_b32 s67, 0\ns_mov_b32 s72, 0x3e0293ee\ns_lshl_b32 s68, s92, 12\ns_add_u32 s88, s68, 1024\n"
"v_lshl_add_u32 v4, v1, 8, v4\nv_add_u32 v56, s88, v4\nv_subrev_u32 v57, 896, v56\nv_and_b32 v4, 1, v0\nv_lshlrev_b32 v4, 4, v4\nv_lshl_add_u32 v4, v2, 5, v4\nv_bfe_u32 v1, v0, 1, 3\nv_lshl_add_u32 v4, v1, 8, v4\n"
"v_add_u32 v58, s88, v4\nv_subrev_u32 v59, 896, v58\ns_lshl_b32 s69, s93, 14\ns_add_u32 s69, s69, %10\ns_add_u32 s61, s69, s68\ns_nop 1\ns_add_u32 m0, s61, 0\ns_nop 0\n"
"buffer_load_dwordx4 v56, s[40:43], s89 offen nt lds\nbuffer_load_dwordx4 v57, s[40:43], s89 offen offset:1024 nt lds\nbuffer_load_dwordx4 v56, s[40:43], s89 offen offset:2048 nt lds\nbuffer_load_dwordx4 v57, s[40:43], s89 offen offset:3072 nt lds\ns_add_u32 m0, s61, 8192\ns_nop 0\nbuffer_load_dwordx4 v58, s[44:47], s89 offen nt lds\nbuffer_load_dwordx4 v59, s[44:47], s89 offen offset:1024 nt lds\n"
"buffer_load_dwordx4 v58, s[44:47], s89 offen offset:2048 nt lds\nbuffer_load_dwordx4 v59, s[44:47], s89 offen offset:3072 nt lds\ns_add_u32 s60, s60, 8192\ns_xor_b32 s89, s60, s101\nv_lshlrev_b32 v60, 4, v0\ns_add_u32 m0, %10, 160768\ns_getpc_b64 s[56:57]\n.Lpc0:\n"
"s_and_b32 s56, s56, 0xffffff80\ns_and_b32 s57, s57, 0xffff\ns_mov_b32 s58, .Lend-.Lpc0\ns_and_b32 s94, %20, 31\ns_lshl_b32 s94, s94, 2\ns_add_u32 s94, s94, s70\ns_lshl_b32 s94, s94, 10\nbuffer_load_dwordx4 v60, s[56:59], s94 offen lds\n"
"s_mov_b32 s56, %8\ns_and_b32 s57, %9, 0xffff\nv_bfe_u32 v3, v0, 3, 1\nv_lshlrev_b32 v4, 1, v3\nv_xor_b32 v4, v4, v2\nv_and_b32 v1, 7, v0\nv_lshl_add_u32 v4, v1, 2, v4\nv_lshlrev_b32 v4, 4, v4\n"
"v_lshl_add_u32 v1, v3, 12, v4\nv_add_u32 v52, s69, v1\nv_add_u32 v53, 66048, v52\ns_mul_i32 s68, s92, 7168\ns_add_u32 s68, s68, %10\ns_add_u32 s68, s68, 99072\nv_mul_u32_u24 v1, 14336, v3\nv_add3_u32 v63, v1, v4, s68\n"
"v_lshrrev_b32 v3, 5, v0\nv_and_b32 v4, 31, v0\nv_lshlrev_b32 v4, 3, v4\nv_lshl_add_u32 v4, v3, 12, v4\nv_add_u32 v4, 8192, v4\nv_add_u32 v54, s69, v4\nv_add_u32 v55, 66048, v54\ns_mul_i32 s68, s93, 8224\n"
"s_add_u32 s68, s68, %10\ns_add_u32 s68, s68, 33024\nv_mul_u32_u24 v3, 2056, v2\nv_and_b32 v4, 15, v0\nv_lshlrev_b32 v4, 4, v4\nv_add3_u32 v61, v3, v4, s68\ns_mul_i32 s68, s70, 6\ns_add_u32 s68, s68, 0\n"
"s_sub_u32 s69, s68, 0\ns_lshl_b32 s69, s69, 10\nv_add_u32 v3, s69, v60\ns_sub_u32 s69, s68, 14\ns_lshl_b32 s69, s69, 2\ns_and_b32 s69, s69, 63\nv_lshrrev_b32 v4, 8, v60\nv_add_u32 v4, s69, v4\n"
"v_mul_u32_u24 v5, 205, v4\nv_lshrrev_b32 v5, 11, v5\nv_mul_u32_u24 v70, 10, v5\nv_sub_u32 v4, v4, v70\nv_lshlrev_b32 v4, 5, v4\nv_bfe_u32 v70, v60, 5, 3\nv_lshl_add_u32 v5, v5, 3, v70\nv_mul_u32_u24 v5, 1280, v5\n"
"v_and_b32 v70, 16, v60\nv_add3_u32 v4, v4, v5, v70\nv_add_u32 v4, 4096, v4\ns_cmp_lt_u32 s68, 14\ns_cselect_b64 vcc, -1, 0\nv_cndmask_b32 v70, v4, v3, vcc\ns_mul_i32 s68, s70, 6\ns_add_u32 s68, s68, 1\n"
"s_sub_u32 s69, s68, 1\ns_lshl_b32 s69, s69, 10\nv_add_u32 v3, s69, v60\ns_sub_u32 s69, s68, 14\ns_lshl_b32 s69, s69, 2\ns_and_b32 s69, s69, 63\nv_lshrrev_b32 v4, 8, v60\nv_add_u32 v4, s69, v4\n"
"v_mul_u32_u24 v5, 205, v4\nv_lshrrev_b32 v5, 11, v5\nv_mul_u32_u24 v71, 10, v5\nv_sub_u32 v4, v4, v71\nv_lshlrev_b32 v4, 5, v4\nv_bfe_u32 v71, v60, 5, 3\nv_lshl_add_u32 v5, v5, 3, v71\nv_mul_u32_u24 v5, 1280, v5\n"
"v_and_b32 v71, 16, v60\nv_add3_u32 v4, v4, v5, v71\nv_add_u32 v4, 3072, v4\ns_cmp_lt_u32 s68, 14\ns_cselect_b64 vcc, -1, 0\nv_cndmask_b32 v71, v4, v3, vcc\ns_mul_i32 s68, s70, 6\ns_add_u32 s68, s68, 2\n"
"s_sub_u32 s69, s68, 2\ns_lshl_b32 s69, s69, 10\nv_add_u32 v3, s69, v60\ns_sub_u32 s69, s68, 14\ns_lshl_b32 s69, s69, 2\ns_and_b32 s69, s69, 63\nv_lshrrev_b32 v4, 8, v60\nv_add_u32 v4, s69, v4\n"
"v_mul_u32_u24 v5, 205, v4\nv_lshrrev_b32 v5, 11, v5\nv_mul_u32_u24 v72, 10, v5\nv_sub_u32 v4, v4, v72\nv_lshlrev_b32 v4, 5, v4\nv_bfe_u32 v72, v60, 5, 3\nv_lshl_add_u32 v5, v5, 3, v72\nv_mul_u32_u24 v5, 1280, v5\n"
"v_and_b32 v72, 16, v60\nv_add3_u32 v4, v4, v5, v72\nv_add_u32 v4, 2048, v4\ns_cmp_lt_u32 s68, 14\ns_cselect_b64 vcc, -1, 0\nv_cndmask_b32 v72, v4, v3, vcc\ns_lshl_b32 s68, s70, 2\ns_add_u32 s69, %10, 131840\n"
"s_add_u32 s68, s68, s69\nv_mov_b32 v73, s68\nv_and_b32 v3, 3, v0\nv_lshl_add_u32 v74, v3, 2, s69\nv_mov_b32 v75, 0\nds_write_b32 v73, v75\ns_waitcnt lgkmcnt(0)\ns_mov_b32 s68, 40960\n"
"s_cmp_lt_u32 s70, 3\ns_cselect_b32 s31, 14336, s68\ns_mov_b32 s68, 40960\ns_cmp_lt_u32 s70, 2\ns_cselect_b32 s90, 14336, s68\ns_mul_i32 s91, s70, 6144\ns_add_u32 s91, s91, %10\ns_cmp_eq_u32 s70, 0\n"
"s_cselect_b32 s68, %19, 64\nv_mov_b32 v3, 0x7ffff000\nv_cmp_eq_u32 vcc, s68, v0\ns_nop 1\nv_cndmask_b32 v77, v3, v76, vcc\ns_and_b32 s68, %19, 4\ns_lshl_b32 s28, 15, s68\ns_mov_b32 s29, 0\n"
"s_and_b32 s69, %19, 3\ns_lshl_b32 s69, s69, 2\ns_lshr_b32 s68, s68, 1\ns_add_u32 s69, s69, s68\ns_add_u32 s69, s69, s93\ns_mul_i32 s98, s69, 28672\ns_mul_i32 s68, s92, 7168\ns_add_u32 s98, s98, s68\n"
"s_add_u32 s98, s98, 1835008\ns_mov_b32 s71, s98\ns_add_u32 s58, s98, 17408\ns_and_b32 s68, %19, 3\ns_lshl_b32 s99, 3072, s68\ns_or_b32 s99, s99, 0x302\ns_mov_b32 s94, 0x7ffff000\ns_mul_i32 s68, %20, 40960\n"
"s_mul_i32 s69, s93, 20480\ns_add_u32 s68, s68, s69\ns_lshl_b32 s69, s92, 10\ns_add_u32 s68, s68, s69\ns_add_u32 s68, s68, 2048\ns_add_u32 s94, %6, s68\ns_addc_u32 s95, %7, 0\ns_and_b32 s95, s95, 0xffff\n"
"s_mov_b32 s100, 17408\nv_mov_b32 v3, -1\nv_accvgpr_write_b32 a0, v3\nv_accvgpr_write_b32 a1, v3\nv_accvgpr_write_b32 a2, v3\nv_accvgpr_write_b32 a3, v3\nv_accvgpr_write_b32 a16, v3\nv_accvgpr_write_b32 a17, v3\n"
"v_accvgpr_write_b32 a18, v3\nv_accvgpr_write_b32 a19, v3\nv_accvgpr_write_b32 a32, v3\nv_accvgpr_write_b32 a33, v3\nv_accvgpr_write_b32 a34, v3\nv_accvgpr_write_b32 a35, v3\nv_accvgpr_write_b32 a48, v3\nv_accvgpr_write_b32 a49, v3\n"
"v_accvgpr_write_b32 a50, v3\nv_accvgpr_write_b32 a51, v3\nv_accvgpr_write_b32 a64, v3\nv_accvgpr_write_b32 a65, v3\nv_accvgpr_write_b32 a66, v3\nv_accvgpr_write_b32 a67, v3\nv_accvgpr_write_b32 a80, v3\nv_accvgpr_write_b32 a81, v3\n"
"v_accvgpr_write_b32 a82, v3\nv_accvgpr_write_b32 a83, v3\nv_accvgpr_write_b32 a96, v3\nv_accvgpr_write_b32 a97, v3\nv_accvgpr_write_b32 a98, v3\nv_accvgpr_write_b32 a99, v3\nv_accvgpr_write_b32 a4, v3\nv_accvgpr_write_b32 a5, v3\n"
"v_accvgpr_write_b32 a6, v3\nv_accvgpr_write_b32 a7, v3\nv_accvgpr_write_b32 a20, v3\nv_accvgpr_write_b32 a21, v3\nv_accvgpr_write_b32 a22, v3\nv_accvgpr_write_b32 a23, v3\nv_accvgpr_write_b32 a36, v3\nv_accvgpr_write_b32 a37, v3\n"
"v_accvgpr_write_b32 a38, v3\nv_accvgpr_write_b32 a39, v3\n.Lround:\ns_waitcnt vmcnt(15)\ns_barrier\nQta 112,61,0\nQta 114,61,1024\nQdv 24,63,33024\n"
"Qta 116,61,8\nQta 118,61,1032\nQdv 28,63,34048\nQta 120,61,256\nQta 122,61,1280\nQdv 32,63,35072\nQta 124,61,264\nQta 126,61,1288\n"
"Qdv 36,63,36096\nQta 128,61,512\nQta 130,61,1536\nQdv 40,63,37120\nQta 132,61,520\nQta 134,61,1544\nQdv 44,63,38144\nQta 136,61,768\n"
"Qta 138,61,1792\nQdv 48,63,39168\nQta 140,61,776\nQta 142,61,1800\nQda 176,63,33536\nQda 180,63,34560\nQda 184,63,35584\nQda 188,63,36608\n"
"Qda 192,63,37632\nQda 196,63,38656\nQda 200,63,39680\ns_waitcnt lgkmcnt(7)\nQzvav 80,112,24\nQzvav 84,116,24\ns_add_u32 s68, s71, 0\nQzvav 88,120,24\n"
"buffer_store_dwordx4 a[0:3], v60, s[56:59], s68 offen offset:0 sc1\nQzvav 92,124,24\nQzvav 96,128,24\ns_add_u32 s68, s71, 14336\nQzvav 100,132,24\nbuffer_store_dwordx4 a[4:7], v60, s[56:59], s68 offen offset:0 sc1\nQzvav 104,136,24\nQzvav 108,140,24\n"
"s_waitcnt lgkmcnt(0)\nQzvav 112,112,28\ns_waitcnt vmcnt(24)\ns_barrier\nQzvav 116,116,28\nQta 144,61,16448\ns_add_u32 m0, s62, 0\ns_add_u32 s69, s87, 1048576\n"
"Qzvav 120,120,28\nbuffer_load_dwordx4 v62, s[52:55], s69 offen lds\nQzvav 124,124,28\nQta 146,61,17472\nQzvav 128,128,28\nQzvav 132,132,28\nQta 148,61,16456\ns_add_u32 s69, s87, 1080320\n"
"Qzvav 136,136,28\nbuffer_load_dwordx4 v62, s[52:55], s69 offen offset:1024 lds\nQzvav 140,140,28\nQta 150,61,17480\nQzvav 144,112,32\nQzvav 148,116,32\nQta 152,61,16704\ns_add_u32 s69, s87, 1112056\n"
"Qzvav 152,120,32\nbuffer_load_dwordx4 v62, s[52:55], s69 offen offset:2056 lds\nQzvav 156,124,32\nQta 154,61,17728\nQzvav 160,128,32\nQzvav 164,132,32\nQta 156,61,16712\ns_add_u32 s69, s87, 1143800\n"
"Qzvav 168,136,32\nbuffer_load_dwordx4 v62, s[52:55], s69 offen offset:3080 lds\nQzvav 172,140,32\nQta 158,61,17736\nQzvav 176,112,36\nQzvav 180,116,36\nQta 160,61,16960\ns_add_u32 m0, s63, 33024\n"
"s_add_u32 s88, s86, 256\nQzvav 184,120,36\nbuffer_load_dwordx4 v64, s[48:51], s88 offen lds\nQzvav 188,124,36\nQta 162,61,17984\nQzvav 192,128,36\nQzvav 196,132,36\nQta 164,61,16968\n"
"s_add_u32 s88, s86, 19712\nQzvav 200,136,36\nbuffer_load_dwordx4 v64, s[48:51], s88 offen offset:1024 lds\nQzvav 204,140,36\nQta 166,61,17992\nQzvav 208,112,40\nQzvav 212,116,40\nQta 168,61,17216\n"
"s_add_u32 s88, s86, 39168\nQzvav 216,120,40\nbuffer_load_dwordx4 v64, s[48:51], s88 offen offset:2048 lds\nQzvav 220,124,40\nQta 170,61,18240\nQzvav 224,128,40\nQzvav 228,132,40\nQta 172,61,17224\n"
"s_add_u32 s88, s86, 58624\nQzvav 232,136,40\nbuffer_load_dwordx4 v64, s[48:51], s88 offen offset:3072 lds\nQzvav 236,140,40\nQta 174,61,18248\nQzvav 240,112,44\nQzvav 244,116,44\ns_add_u32 m0, s63, 37120\n"
"s_add_u32 s88, s86, 82176\nQzvav 248,120,44\nbuffer_load_dwordx4 v64, s[48:51], s88 offen lds\nQzvav 252,124,44\nQzaav 208,128,44\nQzaav 212,132,44\ns_add_u32 s88, s86, 101632\nQzaav 216,136,44\n"
"buffer_load_dwordx4 v64, s[48:51], s88 offen offset:1024 lds\nQzaav 220,140,44\nQzaav 224,112,48\nQzaav 228,116,48\ns_add_u32 s88, s86, 121088\nQzaav 232,120,48\nbuffer_load_dwordx4 v64, s[48:51], s88 offen offset:2048 lds\nQzaav 236,124,48\n"
"Qzaav 240,128,48\nQzaav 244,132,48\nQzaav 248,136,48\nQzaav 252,140,48\ns_waitcnt lgkmcnt(0)\nQmvaa 80,144,176\nQmvaa 84,148,176\ns_add_u32 s68, s71, 28672\n"
"Qmvaa 88,152,176\nbuffer_store_dwordx4 a[8:11], v60, s[56:59], s68 offen offset:0 sc1\nQmvaa 92,156,176\nQmvaa 96,160,176\ns_add_u32 s68, s71, 43008\nQmvaa 100,164,176\nbuffer_store_dwordx4 a[12:15], v60, s[56:59], s68 offen offset:0 sc1\nQmvaa 104,168,176\n"
"Qmvaa 108,172,176\ns_waitcnt lgkmcnt(0)\nQmvaa 112,144,180\ns_waitcnt vmcnt(15)\ns_barrier\nQmvaa 116,148,180\nQda 176,63,512\ns_add_u32 m0, s62, 16448\n"
"s_add_u32 s69, s87, 1310720\nQmvaa 120,152,180\nbuffer_load_dwordx4 v62, s[52:55], s69 offen lds\nQmvaa 124,156,180\nQta 112,61,33024\nQmvaa 128,160,180\nQta 114,61,34048\nQmvaa 132,164,180\n"
"Qta 116,61,33032\ns_add_u32 s69, s87, 1342464\nQmvaa 136,168,180\nbuffer_load_dwordx4 v62, s[52:55], s69 offen offset:1024 lds\nQmvaa 140,172,180\nQda 180,63,1536\nQmvaa 144,144,184\nQta 118,61,34056\n"
"Qmvaa 148,148,184\nQdv 24,63,0\ns_add_u32 s69, s87, 1374200\nQmvaa 152,152,184\nbuffer_load_dwordx4 v62, s[52:55], s69 offen offset:2056 lds\nQmvaa 156,156,184\nQta 120,61,33280\nQmvaa 160,160,184\n"
"Qta 122,61,34304\nQmvaa 164,164,184\nQta 124,61,33288\ns_add_u32 s69, s87, 1405944\nQmvaa 168,168,184\nbuffer_load_dwordx4 v62, s[52:55], s69 offen offset:3080 lds\nQmvaa 172,172,184\nQda 184,63,2560\n"
"Qmvaa 176,144,188\nQta 126,61,34312\nQmvaa 180,148,188\nQta 128,61,33536\nQmvaa 184,152,188\nQta 130,61,34560\nQmvaa 188,156,188\nQta 132,61,33544\n"
"Qmvaa 192,160,188\nQta 134,61,34568\nQmvaa 196,164,188\nQta 136,61,33792\nQmvaa 200,168,188\nQta 138,61,34816\nQmvaa 204,172,188\nQda 188,63,3584\n"
"Qmvaa 208,144,192\nQta 140,61,33800\nQmvaa 212,148,192\nQta 142,61,34824\nQmvaa 216,152,192\nQdv 28,63,1024\nQmvaa 220,156,192\nQdv 32,63,2048\n"
"Qmvaa 224,160,192\nQdv 36,63,3072\nQmvaa 228,164,192\nQdv 40,63,4096\nQmvaa 232,168,192\nQdv 44,63,5120\nQmvaa 236,172,192\nQda 192,63,4608\n"
"Qmvaa 240,144,196\nQdv 48,63,6144\nQmvaa 244,148,196\nQmvaa 248,152,196\nQmvaa 252,156,196\nQmaaa 208,160,196\nQmaaa 212,164,196\nQmaaa 216,168,196\n"
"Qmaaa 220,172,196\nQda 196,63,5632\nQmaaa 224,144,200\nQmaaa 228,148,200\nQmaaa 232,152,200\nQmaaa 236,156,200\nQmaaa 240,160,200\nQmaaa 244,164,200\n"
"Qmaaa 248,168,200\nQmaaa 252,172,200\nQda 200,63,6656\ns_waitcnt lgkmcnt(9)\nQmvav 80,112,24\nQmvav 84,116,24\ns_add_u32 s68, s71, 0\nQmvav 88,120,24\n"
"buffer_store_dwordx4 a[16:19], v60, s[56:59], s68 offen offset:1024 sc1\nQmvav 92,124,24\nQmvav 96,128,24\ns_add_u32 s68, s71, 14336\nQmvav 100,132,24\nbuffer_store_dwordx4 a[20:23], v60, s[56:59], s68 offen offset:1024 sc1\nQmvav 104,136,24\nQmvav 108,140,24\n"
"s_waitcnt lgkmcnt(0)\nQmvav 112,112,28\ns_waitcnt vmcnt(28)\ns_barrier\nQmvav 116,116,28\nQta 144,61,49472\ns_add_u32 m0, s62, 33024\ns_add_u32 s69, s87, 1572864\n"
"Qmvav 120,120,28\nbuffer_load_dwordx4 v62, s[52:55], s69 offen lds\nQmvav 124,124,28\nQta 146,61,50496\nQmvav 128,128,28\nQmvav 132,132,28\nQta 148,61,49480\ns_add_u32 s69, s87, 1604608\n"
"Qmvav 136,136,28\nbuffer_load_dwordx4 v62, s[52:55], s69 offen offset:1024 lds\nQmvav 140,140,28\nQta 150,61,50504\nQmvav 144,112,32\nQmvav 148,116,32\nQta 152,61,49728\ns_add_u32 s69, s87, 1636344\n"
"Qmvav 152,120,32\nbuffer_load_dwordx4 v62, s[52:55], s69 offen offset:2056 lds\nQmvav 156,124,32\nQta 154,61,50752\nQmvav 160,128,32\nQmvav 164,132,32\nQta 156,61,49736\ns_add_u32 s69, s87, 1668088\n"
"Qmvav 168,136,32\nbuffer_load_dwordx4 v62, s[52:55], s69 offen offset:3080 lds\nQmvav 172,140,32\nQta 158,61,50760\nQmvav 176,112,36\nQmvav 180,116,36\nQta 160,61,49984\ns_add_u32 m0, s63, 0\n"
"s_add_u32 s88, s86, 384\nQmvav 184,120,36\nbuffer_load_dwordx4 v64, s[48:51], s88 offen lds\nQmvav 188,124,36\nQta 162,61,51008\nQmvav 192,128,36\nQmvav 196,132,36\nQta 164,61,49992\n"
"s_add_u32 s88, s86, 19840\nQmvav 200,136,36\nbuffer_load_dwordx4 v64, s[48:51], s88 offen offset:1024 lds\nQmvav 204,140,36\nQta 166,61,51016\nQmvav 208,112,40\nQmvav 212,116,40\nQta 168,61,50240\n"
"s_add_u32 s88, s86, 39296\nQmvav 216,120,40\nbuffer_load_dwordx4 v64, s[48:51], s88 offen offset:2048 lds\nQmvav 220,124,40\nQta 170,61,51264\nQmvav 224,128,40\nQmvav 228,132,40\nQta 172,61,50248\n"
"s_add_u32 s88, s86, 58752\nQmvav 232,136,40\nbuffer_load_dwordx4 v64, s[48:51], s88 offen offset:3072 lds\nQmvav 236,140,40\nQta 174,61,51272\nQmvav 240,112,44\nQmvav 244,116,44\ns_add_u32 m0, s63, 4096\n"
"s_add_u32 s88, s86, 82304\nQmvav 248,120,44\nbuffer_load_dwordx4 v64, s[48:51], s88 offen lds\nQmvav 252,124,44\nQmaav 208,128,44\nQmaav 212,132,44\ns_add_u32 s88, s86, 101760\nQmaav 216,136,44\n"
"buffer_load_dwordx4 v64, s[48:51], s88 offen offset:1024 lds\nQmaav 220,140,44\nQmaav 224,112,48\nQmaav 228,116,48\ns_add_u32 s88, s86, 121216\nQmaav 232,120,48\nbuffer_load_dwordx4 v64, s[48:51], s88 offen offset:2048 lds\nQmaav 236,124,48\n"
"Qmaav 240,128,48\nQmaav 244,132,48\nQmaav 248,136,48\nQmaav 252,140,48\ns_waitcnt lgkmcnt(0)\nQmvaa 80,144,176\nQmvaa 84,148,176\ns_add_u32 s68, s71, 28672\n"
"Qmvaa 88,152,176\nbuffer_store_dwordx4 a[24:27], v60, s[56:59], s68 offen offset:1024 sc1\nQmvaa 92,156,176\nQmvaa 96,160,176\ns_add_u32 s68, s71, 43008\nQmvaa 100,164,176\nbuffer_store_dwordx4 a[28:31], v60, s[56:59], s68 offen offset:1024 sc1\nQmvaa 104,168,176\n"
"Qmvaa 108,172,176\ns_waitcnt lgkmcnt(0)\nQmvaa 112,144,180\ns_waitcnt vmcnt(21)\ns_barrier\nQmvaa 116,148,180\nQda 176,63,33536\ns_add_u32 m0, s62, 49472\n"
"s_add_u32 s69, s87, 1835008\nQmvaa 120,152,180\nbuffer_load_dwordx4 v62, s[52:55], s69 offen lds\nQmvaa 124,156,180\nQta 112,61,0\nQmvaa 128,160,180\nQta 114,61,1024\nQmvaa 132,164,180\n"
"Qta 116,61,8\ns_add_u32 s69, s87, 1866752\nQmvaa 136,168,180\nbuffer_load_dwordx4 v62, s[52:55], s69 offen offset:1024 lds\nQmvaa 140,172,180\nQda 180,63,34560\nQmvaa 144,144,184\nQta 118,61,1032\n"
"Qmvaa 148,148,184\nQdv 24,63,33024\ns_add_u32 s69, s87, 1898488\nQmvaa 152,152,184\nbuffer_load_dwordx4 v62, s[52:55], s69 offen offset:2056 lds\nQmvaa 156,156,184\nQta 120,61,256\nQmvaa 160,160,184\n"
"Qta 122,61,1280\nQmvaa 164,164,184\nQta 124,61,264\ns_add_u32 s69, s87, 1930232\nQmvaa 168,168,184\nbuffer_load_dwordx4 v62, s[52:55], s69 offen offset:3080 lds\nQmvaa 172,172,184\nQda 184,63,35584\n"
"Qmvaa 176,144,188\nQta 126,61,1288\nQmvaa 180,148,188\nQta 128,61,512\nQmvaa 184,152,188\nQta 130,61,1536\nQmvaa 188,156,188\nQta 132,61,520\n"
"Qmvaa 192,160,188\nQta 134,61,1544\nQmvaa 196,164,188\nQta 136,61,768\nQmvaa 200,168,188\nQta 138,61,1792\nQmvaa 204,172,188\nQda 188,63,36608\n"
"Qmvaa 208,144,192\nQta 140,61,776\nQmvaa 212,148,192\nQta 142,61,1800\nQmvaa 216,152,192\nQdv 28,63,34048\nQmvaa 220,156,192\nQdv 32,63,35072\n"
"Qmvaa 224,160,192\nQdv 36,63,36096\nQmvaa 228,164,192\nQdv 40,63,37120\nQmvaa 232,168,192\nQdv 44,63,38144\nQmvaa 236,172,192\nQda 192,63,37632\n"
"Qmvaa 240,144,196\nQdv 48,63,39168\nQmvaa 244,148,196\nQmvaa 248,152,196\nQmvaa 252,156,196\nQmaaa 208,160,196\nQmaaa 212,164,196\nQmaaa 216,168,196\n"
"Qmaaa 220,172,196\nQda 196,63,38656\nQmaaa 224,144,200\nQmaaa 228,148,200\nQmaaa 232,152,200\nQmaaa 236,156,200\nQmaaa 240,160,200\nQmaaa 244,164,200\n"
"Qmaaa 248,168,200\nQmaaa 252,172,200\nQda 200,63,39680\ns_waitcnt lgkmcnt(9)\nQmvav 80,112,24\nQmvav 84,116,24\ns_add_u32 s68, s71, 0\nQmvav 88,120,24\n"
"buffer_store_dwordx4 a[32:35], v60, s[56:59], s68 offen offset:2048 sc1\nQmvav 92,124,24\nQmvav 96,128,24\ns_add_u32 s68, s71, 14336\nQmvav 100,132,24\nbuffer_store_dwordx4 a[36:39], v60, s[56:59], s68 offen offset:2048 sc1\nQmvav 104,136,24\nQmvav 108,140,24\n"
"s_waitcnt lgkmcnt(0)\nQmvav 112,112,28\ns_waitcnt vmcnt(21)\ns_barrier\nQmvav 116,116,28\nQta 144,61,16448\ns_add_u32 m0, s62, 0\ns_add_u32 s69, s87, 2097152\n"
"Qmvav 120,120,28\nbuffer_load_dwordx4 v62, s[52:55], s69 offen lds\nQmvav 124,124,28\nQta 146,61,17472\nQmvav 128,128,28\nQmvav 132,132,28\nQta 148,61,16456\ns_add_u32 s69, s87, 2128896\n"
"Qmvav 136,136,28\nbuffer_load_dwordx4 v62, s[52:55], s69 offen offset:1024 lds\nQmvav 140,140,28\nQta 150,61,17480\nQmvav 144,112,32\nQmvav 148,116,32\nQta 152,61,16704\ns_add_u32 s69, s87, 2160632\n"
"Qmvav 152,120,32\nbuffer_load_dwordx4 v62, s[52:55], s69 offen offset:2056 lds\nQmvav 156,124,32\nQta 154,61,17728\nQmvav 160,128,32\nQmvav 164,132,32\nQta 156,61,16712\ns_add_u32 s69, s87, 2192376\n"
"Qmvav 168,136,32\nbuffer_load_dwordx4 v62, s[52:55], s69 offen offset:3080 lds\nQmvav 172,140,32\nQta 158,61,17736\nQmvav 176,112,36\nQmvav 180,116,36\nQta 160,61,16960\ns_add_u32 m0, s63, 33024\n"
"s_add_u32 s88, s86, 512\nQmvav 184,120,36\nbuffer_load_dwordx4 v64, s[48:51], s88 offen lds\nQmvav 188,124,36\nQta 162,61,17984\nQmvav 192,128,36\nQmvav 196,132,36\nQta 164,61,16968\n"
"s_add_u32 s88, s86, 19968\nQmvav 200,136,36\nbuffer_load_dwordx4 v64, s[48:51], s88 offen offset:1024 lds\nQmvav 204,140,36\nQta 166,61,17992\nQmvav 208,112,40\nQmvav 212,116,40\nQta 168,61,17216\n"
"s_add_u32 s88, s86, 39424\nQmvav 216,120,40\nbuffer_load_dwordx4 v64, s[48:51], s88 offen offset:2048 lds\nQmvav 220,124,40\nQta 170,61,18240\nQmvav 224,128,40\nQmvav 228,132,40\nQta 172,61,17224\n"
"s_add_u32 s88, s86, 58880\nQmvav 232,136,40\nbuffer_load_dwordx4 v64, s[48:51], s88 offen offset:3072 lds\nQmvav 236,140,40\nQta 174,61,18248\nQmvav 240,112,44\nQmvav 244,116,44\ns_add_u32 m0, s63, 37120\n"
"s_add_u32 s88, s86, 82432\nQmvav 248,120,44\nbuffer_load_dwordx4 v64, s[48:51], s88 offen lds\nQmvav 252,124,44\nQmaav 208,128,44\nQmaav 212,132,44\ns_add_u32 s88, s86, 101888\nQmaav 216,136,44\n"
"buffer_load_dwordx4 v64, s[48:51], s88 offen offset:1024 lds\nQmaav 220,140,44\nQmaav 224,112,48\nQmaav 228,116,48\ns_add_u32 s88, s86, 121344\nQmaav 232,120,48\nbuffer_load_dwordx4 v64, s[48:51], s88 offen offset:2048 lds\nQmaav 236,124,48\n"
"Qmaav 240,128,48\nQmaav 244,132,48\nQmaav 248,136,48\nQmaav 252,140,48\ns_waitcnt lgkmcnt(0)\nQmvaa 80,144,176\nQmvaa 84,148,176\ns_add_u32 s68, s71, 28672\n"
"Qmvaa 88,152,176\nbuffer_store_dwordx4 a[40:43], v60, s[56:59], s68 offen offset:2048 sc1\nQmvaa 92,156,176\nQmvaa 96,160,176\ns_add_u32 s68, s71, 43008\nQmvaa 100,164,176\nbuffer_store_dwordx4 a[44:47], v60, s[56:59], s68 offen offset:2048 sc1\nQmvaa 104,168,176\n"
"Qmvaa 108,172,176\ns_waitcnt lgkmcnt(0)\nQmvaa 112,144,180\ns_waitcnt vmcnt(21)\ns_barrier\nQmvaa 116,148,180\nQda 176,63,512\ns_add_u32 m0, s62, 16448\n"
"s_add_u32 s69, s87, 2359296\nQmvaa 120,152,180\nbuffer_load_dwordx4 v62, s[52:55], s69 offen lds\nQmvaa 124,156,180\nQta 112,61,33024\nQmvaa 128,160,180\nQta 114,61,34048\nQmvaa 132,164,180\n"
"Qta 116,61,33032\ns_add_u32 s69, s87, 2391040\nQmvaa 136,168,180\nbuffer_load_dwordx4 v62, s[52:55], s69 offen offset:1024 lds\nQmvaa 140,172,180\nQda 180,63,1536\nQmvaa 144,144,184\nQta 118,61,34056\n"
"Qmvaa 148,148,184\nQdv 24,63,0\ns_add_u32 s69, s87, 2422776\nQmvaa 152,152,184\nbuffer_load_dwordx4 v62, s[52:55], s69 offen offset:2056 lds\nQmvaa 156,156,184\nQta 120,61,33280\nQmvaa 160,160,184\n"
"Qta 122,61,34304\nQmvaa 164,164,184\nQta 124,61,33288\ns_add_u32 s69, s87, 2454520\nQmvaa 168,168,184\nbuffer_load_dwordx4 v62, s[52:55], s69 offen offset:3080 lds\nQmvaa 172,172,184\nQda 184,63,2560\n"
"Qmvaa 176,144,188\nQta 126,61,34312\nQmvaa 180,148,188\nQta 128,61,33536\nQmvaa 184,152,188\nQta 130,61,34560\nQmvaa 188,156,188\nQta 132,61,33544\n"
"Qmvaa 192,160,188\nQta 134,61,34568\nQmvaa 196,164,188\nQta 136,61,33792\nQmvaa 200,168,188\nQta 138,61,34816\nQmvaa 204,172,188\nQda 188,63,3584\n"
"Qmvaa 208,144,192\nQta 140,61,33800\nQmvaa 212,148,192\nQta 142,61,34824\nQmvaa 216,152,192\nQdv 28,63,1024\nQmvaa 220,156,192\nQdv 32,63,2048\n"
"Qmvaa 224,160,192\nQdv 36,63,3072\nQmvaa 228,164,192\nQdv 40,63,4096\nQmvaa 232,168,192\nQdv 44,63,5120\nQmvaa 236,172,192\nQda 192,63,4608\n"
"Qmvaa 240,144,196\nQdv 48,63,6144\nQmvaa 244,148,196\nQmvaa 248,152,196\nQmvaa 252,156,196\nQmaaa 208,160,196\nQmaaa 212,164,196\nQmaaa 216,168,196\n"
"Qmaaa 220,172,196\nQda 196,63,5632\nQmaaa 224,144,200\nQmaaa 228,148,200\nQmaaa 232,152,200\nQmaaa 236,156,200\nQmaaa 240,160,200\nQmaaa 244,164,200\n"
"Qmaaa 248,168,200\nQmaaa 252,172,200\nQda 200,63,6656\ns_waitcnt lgkmcnt(9)\nQmvav 80,112,24\nQmvav 84,116,24\ns_add_u32 s68, s71, 0\nQmvav 88,120,24\n"
"buffer_store_dwordx4 a[48:51], v60, s[56:59], s68 offen offset:3072 sc1\nQmvav 92,124,24\nQmvav 96,128,24\ns_add_u32 s68, s71, 14336\nQmvav 100,132,24\nbuffer_store_dwordx4 a[52:55], v60, s[56:59], s68 offen offset:3072 sc1\nQmvav 104,136,24\nQmvav 108,140,24\n"
"s_waitcnt lgkmcnt(0)\nQmvav 112,112,28\ns_waitcnt vmcnt(21)\ns_barrier\nQmvav 116,116,28\nQta 144,61,49472\ns_add_u32 m0, s62, 33024\ns_add_u32 s69, s87, 2621440\n"
"Qmvav 120,120,28\nbuffer_load_dwordx4 v62, s[52:55], s69 offen lds\nQmvav 124,124,28\nQta 146,61,50496\nQmvav 128,128,28\nQmvav 132,132,28\nQta 148,61,49480\ns_add_u32 s69, s87, 2653184\n"
"Qmvav 136,136,28\nbuffer_load_dwordx4 v62, s[52:55], s69 offen offset:1024 lds\nQmvav 140,140,28\nQta 150,61,50504\nQmvav 144,112,32\nQmvav 148,116,32\nQta 152,61,49728\ns_add_u32 s69, s87, 2684920\n"
"Qmvav 152,120,32\nbuffer_load_dwordx4 v62, s[52:55], s69 offen offset:2056 lds\nQmvav 156,124,32\nQta 154,61,50752\nQmvav 160,128,32\nQmvav 164,132,32\nQta 156,61,49736\ns_add_u32 s69, s87, 2716664\n"
"Qmvav 168,136,32\nbuffer_load_dwordx4 v62, s[52:55], s69 offen offset:3080 lds\nQmvav 172,140,32\nQta 158,61,50760\nQmvav 176,112,36\nQmvav 180,116,36\nQta 160,61,49984\ns_add_u32 m0, s63, 0\n"
"s_add_u32 s88, s86, 640\nQmvav 184,120,36\nbuffer_load_dwordx4 v64, s[48:51], s88 offen lds\nQmvav 188,124,36\nQta 162,61,51008\nQmvav 192,128,36\nQmvav 196,132,36\nQta 164,61,49992\n"
"s_add_u32 s88, s86, 20096\nQmvav 200,136,36\nbuffer_load_dwordx4 v64, s[48:51], s88 offen offset:1024 lds\nQmvav 204,140,36\nQta 166,61,51016\nQmvav 208,112,40\nQmvav 212,116,40\nQta 168,61,50240\n"
"s_add_u32 s88, s86, 39552\nQmvav 216,120,40\nbuffer_load_dwordx4 v64, s[48:51], s88 offen offset:2048 lds\nQmvav 220,124,40\nQta 170,61,51264\nQmvav 224,128,40\nQmvav 228,132,40\nQta 172,61,50248\n"
"s_add_u32 s88, s86, 59008\nQmvav 232,136,40\nbuffer_load_dwordx4 v64, s[48:51], s88 offen offset:3072 lds\nQmvav 236,140,40\nQta 174,61,51272\nQmvav 240,112,44\nQmvav 244,116,44\ns_add_u32 m0, s63, 4096\n"
"s_add_u32 s88, s86, 82560\nQmvav 248,120,44\nbuffer_load_dwordx4 v64, s[48:51], s88 offen lds\nQmvav 252,124,44\nQmaav 208,128,44\nQmaav 212,132,44\ns_add_u32 s88, s86, 102016\nQmaav 216,136,44\n"
"buffer_load_dwordx4 v64, s[48:51], s88 offen offset:1024 lds\nQmaav 220,140,44\nQmaav 224,112,48\nQmaav 228,116,48\ns_add_u32 s88, s86, 121472\nQmaav 232,120,48\nbuffer_load_dwordx4 v64, s[48:51], s88 offen offset:2048 lds\nQmaav 236,124,48\n"
"Qmaav 240,128,48\nQmaav 244,132,48\nQmaav 248,136,48\nQmaav 252,140,48\ns_waitcnt lgkmcnt(0)\nQmvaa 80,144,176\nQmvaa 84,148,176\ns_add_u32 s68, s71, 28672\n"
"Qmvaa 88,152,176\nbuffer_store_dwordx4 a[56:59], v60, s[56:59], s68 offen offset:3072 sc1\nQmvaa 92,156,176\nQmvaa 96,160,176\ns_add_u32 s68, s71, 43008\nQmvaa 100,164,176\nbuffer_store_dwordx4 a[60:63], v60, s[56:59], s68 offen offset:3072 sc1\nQmvaa 104,168,176\n"
"Qmvaa 108,172,176\ns_waitcnt lgkmcnt(0)\nQmvaa 112,144,180\ns_waitcnt vmcnt(21)\ns_barrier\nQmvaa 116,148,180\nQda 176,63,33536\ns_add_u32 m0, s62, 49472\n"
"s_add_u32 s69, s87, 2883584\nQmvaa 120,152,180\nbuffer_load_dwordx4 v62, s[52:55], s69 offen lds\nQmvaa 124,156,180\nQta 112,61,0\nQmvaa 128,160,180\nQta 114,61,1024\nQmvaa 132,164,180\n"
"Qta 116,61,8\ns_add_u32 s69, s87, 2915328\nQmvaa 136,168,180\nbuffer_load_dwordx4 v62, s[52:55], s69 offen offset:1024 lds\nQmvaa 140,172,180\nQda 180,63,34560\nQmvaa 144,144,184\nQta 118,61,1032\n"
"Qmvaa 148,148,184\nQdv 24,63,33024\ns_add_u32 s69, s87, 2947064\nQmvaa 152,152,184\nbuffer_load_dwordx4 v62, s[52:55], s69 offen offset:2056 lds\nQmvaa 156,156,184\nQta 120,61,256\nQmvaa 160,160,184\n"
"Qta 122,61,1280\nQmvaa 164,164,184\nQta 124,61,264\ns_add_u32 s69, s87, 2978808\nQmvaa 168,168,184\nbuffer_load_dwordx4 v62, s[52:55], s69 offen offset:3080 lds\nQmvaa 172,172,184\nQda 184,63,35584\n"
"Qmvaa 176,144,188\nQta 126,61,1288\nQmvaa 180,148,188\nQta 128,61,512\nQmvaa 184,152,188\nQta 130,61,1536\nQmvaa 188,156,188\nQta 132,61,520\n"
"Qmvaa 192,160,188\nQta 134,61,1544\nQmvaa 196,164,188\nQta 136,61,768\nQmvaa 200,168,188\nQta 138,61,1792\nQmvaa 204,172,188\nQda 188,63,36608\n"
"Qmvaa 208,144,192\nQta 140,61,776\nQmvaa 212,148,192\nQta 142,61,1800\nQmvaa 216,152,192\nQdv 28,63,34048\nQmvaa 220,156,192\nQdv 32,63,35072\n"
"Qmvaa 224,160,192\nQdv 36,63,36096\nQmvaa 228,164,192\nQdv 40,63,37120\nQmvaa 232,168,192\nQdv 44,63,38144\nQmvaa 236,172,192\nQda 192,63,37632\n"
"Qmvaa 240,144,196\nQdv 48,63,39168\nQmvaa 244,148,196\nQmvaa 248,152,196\nQmvaa 252,156,196\nQmaaa 208,160,196\nQmaaa 212,164,196\nQmaaa 216,168,196\n"
"Qmaaa 220,172,196\nQda 196,63,38656\nQmaaa 224,144,200\nQmaaa 228,148,200\nQmaaa 232,152,200\nQmaaa 236,156,200\nQmaaa 240,160,200\nQmaaa 244,164,200\n"
"Qmaaa 248,168,200\nQmaaa 252,172,200\nQda 200,63,39680\ns_waitcnt lgkmcnt(9)\nQmvav 80,112,24\nQmvav 84,116,24\ns_add_u32 s68, s71, 4096\nQmvav 88,120,24\n"
"buffer_store_dwordx4 a[64:67], v60, s[56:59], s68 offen offset:0 sc1\nQmvav 92,124,24\nQmvav 96,128,24\ns_add_u32 s68, s71, 18432\nQmvav 100,132,24\nbuffer_store_dwordx4 a[68:71], v60, s[56:59], s68 offen offset:0 sc1\nQmvav 104,136,24\nQmvav 108,140,24\n"
"s_waitcnt lgkmcnt(0)\nQmvav 112,112,28\ns_waitcnt vmcnt(21)\ns_barrier\nQmvav 116,116,28\nQta 144,61,16448\ns_add_u32 m0, s62, 0\ns_add_u32 s69, s87, 3145728\n"
"Qmvav 120,120,28\nbuffer_load_dwordx4 v62, s[52:55], s69 offen lds\nQmvav 124,124,28\nQta 146,61,17472\nQmvav 128,128,28\nQmvav 132,132,28\nQta 148,61,16456\ns_add_u32 s69, s87, 3177472\n"
"Qmvav 136,136,28\nbuffer_load_dwordx4 v62, s[52:55], s69 offen offset:1024 lds\nQmvav 140,140,28\nQta 150,61,17480\nQmvav 144,112,32\nQmvav 148,116,32\nQta 152,61,16704\ns_add_u32 s69, s87, 3209208\n"
"Qmvav 152,120,32\nbuffer_load_dwordx4 v62, s[52:55], s69 offen offset:2056 lds\nQmvav 156,124,32\nQta 154,61,17728\nQmvav 160,128,32\nQmvav 164,132,32\nQta 156,61,16712\ns_add_u32 s69, s87, 3240952\n"
"Qmvav 168,136,32\nbuffer_load_dwordx4 v62, s[52:55], s69 offen offset:3080 lds\nQmvav 172,140,32\nQta 158,61,17736\nQmvav 176,112,36\nQmvav 180,116,36\nQta 160,61,16960\ns_add_u32 m0, s63, 33024\n"
"s_add_u32 s88, s86, 768\nQmvav 184,120,36\nbuffer_load_dwordx4 v64, s[48:51], s88 offen lds\nQmvav 188,124,36\nQta 162,61,17984\nQmvav 192,128,36\nQmvav 196,132,36\nQta 164,61,16968\n"
"s_add_u32 s88, s86, 20224\nQmvav 200,136,36\nbuffer_load_dwordx4 v64, s[48:51], s88 offen offset:1024 lds\nQmvav 204,140,36\nQta 166,61,17992\nQmvav 208,112,40\nQmvav 212,116,40\nQta 168,61,17216\n"
"s_add_u32 s88, s86, 39680\nQmvav 216,120,40\nbuffer_load_dwordx4 v64, s[48:51], s88 offen offset:2048 lds\nQmvav 220,124,40\nQta 170,61,18240\nQmvav 224,128,40\nQmvav 228,132,40\nQta 172,61,17224\n"
"s_add_u32 s88, s86, 59136\nQmvav 232,136,40\nbuffer_load_dwordx4 v64, s[48:51], s88 offen offset:3072 lds\nQmvav 236,140,40\nQta 174,61,18248\nQmvav 240,112,44\nQmvav 244,116,44\ns_add_u32 m0, s63, 37120\n"
"s_add_u32 s88, s86, 82688\nQmvav 248,120,44\nbuffer_load_dwordx4 v64, s[48:51], s88 offen lds\nQmvav 252,124,44\nQmaav 208,128,44\nQmaav 212,132,44\ns_add_u32 s88, s86, 102144\nQmaav 216,136,44\n"
"buffer_load_dwordx4 v64, s[48:51], s88 offen offset:1024 lds\nQmaav 220,140,44\nQmaav 224,112,48\nQmaav 228,116,48\ns_add_u32 s88, s86, 121600\nQmaav 232,120,48\nbuffer_load_dwordx4 v64, s[48:51], s88 offen offset:2048 lds\nQmaav 236,124,48\n"
"Qmaav 240,128,48\nQmaav 244,132,48\nQmaav 248,136,48\nQmaav 252,140,48\ns_waitcnt lgkmcnt(0)\nQmvaa 80,144,176\nQmvaa 84,148,176\ns_add_u32 s68, s71, 32768\n"
"Qmvaa 88,152,176\nbuffer_store_dwordx4 a[72:75], v60, s[56:59], s68 offen offset:0 sc1\nQmvaa 92,156,176\nQmvaa 96,160,176\ns_add_u32 s68, s71, 47104\nQmvaa 100,164,176\nbuffer_store_dwordx4 a[76:79], v60, s[56:59], s68 offen offset:0 sc1\nQmvaa 104,168,176\n"
"Qmvaa 108,172,176\ns_waitcnt lgkmcnt(0)\nQmvaa 112,144,180\ns_waitcnt vmcnt(21)\ns_barrier\nQmvaa 116,148,180\nQda 176,63,512\ns_add_u32 m0, s62, 16448\n"
"s_add_u32 s69, s87, 3407872\nQmvaa 120,152,180\nbuffer_load_dwordx4 v62, s[52:55], s69 offen lds\nQmvaa 124,156,180\nQta 112,61,33024\nQmvaa 128,160,180\nQta 114,61,34048\nQmvaa 132,164,180\n"
"Qta 116,61,33032\ns_add_u32 s69, s87, 3439616\nQmvaa 136,168,180\nbuffer_load_dwordx4 v62, s[52:55], s69 offen offset:1024 lds\nQmvaa 140,172,180\nQda 180,63,1536\nQmvaa 144,144,184\nQta 118,61,34056\n"
"Qmvaa 148,148,184\nQdv 24,63,0\ns_add_u32 s69, s87, 3471352\nQmvaa 152,152,184\nbuffer_load_dwordx4 v62, s[52:55], s69 offen offset:2056 lds\nQmvaa 156,156,184\nQta 120,61,33280\nQmvaa 160,160,184\n"
"Qta 122,61,34304\nQmvaa 164,164,184\nQta 124,61,33288\ns_add_u32 s69, s87, 3503096\nQmvaa 168,168,184\nbuffer_load_dwordx4 v62, s[52:55], s69 offen offset:3080 lds\nQmvaa 172,172,184\nQda 184,63,2560\n"
"Qmvaa 176,144,188\nQta 126,61,34312\nQmvaa 180,148,188\nQta 128,61,33536\nQmvaa 184,152,188\nQta 130,61,34560\nQmvaa 188,156,188\nQta 132,61,33544\n"
"Qmvaa 192,160,188\nQta 134,61,34568\nQmvaa 196,164,188\nQta 136,61,33792\nQmvaa 200,168,188\nQta 138,61,34816\nQmvaa 204,172,188\nQda 188,63,3584\n"
"Qmvaa 208,144,192\nQta 140,61,33800\nQmvaa 212,148,192\nQta 142,61,34824\nQmvaa 216,152,192\nQdv 28,63,1024\nQmvaa 220,156,192\nQdv 32,63,2048\n"
"Qmvaa 224,160,192\nQdv 36,63,3072\nQmvaa 228,164,192\nQdv 40,63,4096\nQmvaa 232,168,192\nQdv 44,63,5120\nQmvaa 236,172,192\nQda 192,63,4608\n"
"Qmvaa 240,144,196\nQdv 48,63,6144\nQmvaa 244,148,196\nQmvaa 248,152,196\nQmvaa 252,156,196\nQmaaa 208,160,196\nQmaaa 212,164,196\nQmaaa 216,168,196\n"
"Qmaaa 220,172,196\nQda 196,63,5632\nQmaaa 224,144,200\nQmaaa 228,148,200\nQmaaa 232,152,200\nQmaaa 236,156,200\nQmaaa 240,160,200\nQmaaa 244,164,200\n"
"Qmaaa 248,168,200\nQmaaa 252,172,200\nQda 200,63,6656\ns_waitcnt lgkmcnt(9)\nQmvav 80,112,24\nQmvav 84,116,24\ns_add_u32 s68, s71, 4096\nQmvav 88,120,24\n"
"buffer_store_dwordx4 a[80:83], v60, s[56:59], s68 offen offset:1024 sc1\nQmvav 92,124,24\nQmvav 96,128,24\ns_add_u32 s68, s71, 18432\nQmvav 100,132,24\nbuffer_store_dwordx4 a[84:87], v60, s[56:59], s68 offen offset:1024 sc1\nQmvav 104,136,24\nQmvav 108,140,24\n"
"s_waitcnt lgkmcnt(0)\nQmvav 112,112,28\ns_waitcnt vmcnt(21)\ns_barrier\nQmvav 116,116,28\nQta 144,61,49472\ns_add_u32 m0, s62, 33024\ns_add_u32 s69, s87, 3670016\n"
"Qmvav 120,120,28\nbuffer_load_dwordx4 v62, s[52:55], s69 offen lds\nQmvav 124,124,28\nQta 146,61,50496\nQmvav 128,128,28\nQmvav 132,132,28\nQta 148,61,49480\ns_add_u32 s69, s87, 3701760\n"
"Qmvav 136,136,28\nbuffer_load_dwordx4 v62, s[52:55], s69 offen offset:1024 lds\nQmvav 140,140,28\nQta 150,61,50504\nQmvav 144,112,32\nQmvav 148,116,32\nQta 152,61,49728\ns_add_u32 s69, s87, 3733496\n"
"Qmvav 152,120,32\nbuffer_load_dwordx4 v62, s[52:55], s69 offen offset:2056 lds\nQmvav 156,124,32\nQta 154,61,50752\nQmvav 160,128,32\nQmvav 164,132,32\nQta 156,61,49736\ns_add_u32 s69, s87, 3765240\n"
"Qmvav 168,136,32\nbuffer_load_dwordx4 v62, s[52:55], s69 offen offset:3080 lds\nQmvav 172,140,32\nQta 158,61,50760\nQmvav 176,112,36\nQmvav 180,116,36\nQta 160,61,49984\ns_add_u32 m0, s63, 0\n"
"s_add_u32 s88, s86, 896\nQmvav 184,120,36\nbuffer_load_dwordx4 v64, s[48:51], s88 offen lds\nQmvav 188,124,36\nQta 162,61,51008\nQmvav 192,128,36\nQmvav 196,132,36\nQta 164,61,49992\n"
"s_add_u32 s88, s86, 20352\nQmvav 200,136,36\nbuffer_load_dwordx4 v64, s[48:51], s88 offen offset:1024 lds\nQmvav 204,140,36\nQta 166,61,51016\nQmvav 208,112,40\nQmvav 212,116,40\nQta 168,61,50240\n"
"s_add_u32 s88, s86, 39808\nQmvav 216,120,40\nbuffer_load_dwordx4 v64, s[48:51], s88 offen offset:2048 lds\nQmvav 220,124,40\nQta 170,61,51264\nQmvav 224,128,40\nQmvav 228,132,40\nQta 172,61,50248\n"
"s_add_u32 s88, s86, 59264\nQmvav 232,136,40\nbuffer_load_dwordx4 v64, s[48:51], s88 offen offset:3072 lds\nQmvav 236,140,40\nQta 174,61,51272\nQmvav 240,112,44\nQmvav 244,116,44\ns_add_u32 m0, s63, 4096\n"
"s_add_u32 s88, s86, 82816\nQmvav 248,120,44\nbuffer_load_dwordx4 v64, s[48:51], s88 offen lds\nQmvav 252,124,44\nQmaav 208,128,44\nQmaav 212,132,44\ns_add_u32 s88, s86, 102272\nQmaav 216,136,44\n"
"buffer_load_dwordx4 v64, s[48:51], s88 offen offset:1024 lds\nQmaav 220,140,44\nQmaav 224,112,48\nQmaav 228,116,48\ns_add_u32 s88, s86, 121728\nQmaav 232,120,48\nbuffer_load_dwordx4 v64, s[48:51], s88 offen offset:2048 lds\nQmaav 236,124,48\n"
"Qmaav 240,128,48\nQmaav 244,132,48\nQmaav 248,136,48\nQmaav 252,140,48\ns_waitcnt lgkmcnt(0)\nQmvaa 80,144,176\nQmvaa 84,148,176\ns_add_u32 s68, s71, 32768\n"
"Qmvaa 88,152,176\nbuffer_store_dwordx4 a[88:91], v60, s[56:59], s68 offen offset:1024 sc1\nQmvaa 92,156,176\nQmvaa 96,160,176\ns_add_u32 s68, s71, 47104\nQmvaa 100,164,176\nbuffer_store_dwordx4 a[92:95], v60, s[56:59], s68 offen offset:1024 sc1\nQmvaa 104,168,176\n"
"Qmvaa 108,172,176\ns_waitcnt lgkmcnt(0)\nQmvaa 112,144,180\ns_waitcnt vmcnt(21)\ns_barrier\nQmvaa 116,148,180\nQda 176,63,33536\ns_add_u32 m0, s62, 49472\n"
"s_add_u32 s69, s87, 3932160\nQmvaa 120,152,180\nbuffer_load_dwordx4 v62, s[52:55], s69 offen lds\nQmvaa 124,156,180\nQta 112,61,0\nQmvaa 128,160,180\nQta 114,61,1024\nQmvaa 132,164,180\n"
"Qta 116,61,8\ns_add_u32 s69, s87, 3963904\nQmvaa 136,168,180\nbuffer_load_dwordx4 v62, s[52:55], s69 offen offset:1024 lds\nQmvaa 140,172,180\nQda 180,63,34560\nQmvaa 144,144,184\nQta 118,61,1032\n"
"Qmvaa 148,148,184\nQdv 24,63,33024\ns_add_u32 s69, s87, 3995640\nQmvaa 152,152,184\nbuffer_load_dwordx4 v62, s[52:55], s69 offen offset:2056 lds\nQmvaa 156,156,184\nQta 120,61,256\nQmvaa 160,160,184\n"
"Qta 122,61,1280\nQmvaa 164,164,184\nQta 124,61,264\ns_add_u32 s69, s87, 4027384\nQmvaa 168,168,184\nbuffer_load_dwordx4 v62, s[52:55], s69 offen offset:3080 lds\nQmvaa 172,172,184\nQda 184,63,35584\n"
"Qmvaa 176,144,188\nQta 126,61,1288\nQmvaa 180,148,188\nQta 128,61,512\nQmvaa 184,152,188\nQta 130,61,1536\nQmvaa 188,156,188\nQta 132,61,520\n"
"Qmvaa 192,160,188\nQta 134,61,1544\nQmvaa 196,164,188\nQta 136,61,768\nQmvaa 200,168,188\nQta 138,61,1792\nQmvaa 204,172,188\nQda 188,63,36608\n"
"Qmvaa 208,144,192\nQta 140,61,776\nQmvaa 212,148,192\nQta 142,61,1800\nQmvaa 216,152,192\nQdv 28,63,34048\nQmvaa 220,156,192\nQdv 32,63,35072\n"
"Qmvaa 224,160,192\nQdv 36,63,36096\nQmvaa 228,164,192\nQdv 40,63,37120\nQmvaa 232,168,192\nQdv 44,63,38144\nQmvaa 236,172,192\nQda 192,63,37632\n"
"Qmvaa 240,144,196\nQdv 48,63,39168\nQmvaa 244,148,196\nQmvaa 248,152,196\nQmvaa 252,156,196\nQmaaa 208,160,196\nQmaaa 212,164,196\nQmaaa 216,168,196\n"
"Qmaaa 220,172,196\nQda 196,63,38656\nQmaaa 224,144,200\nQmaaa 228,148,200\nQmaaa 232,152,200\nQmaaa 236,156,200\nQmaaa 240,160,200\nQmaaa 244,164,200\n"
"Qmaaa 248,168,200\nQmaaa 252,172,200\nQda 200,63,39680\ns_waitcnt lgkmcnt(9)\nQmvav 80,112,24\nQmvav 84,116,24\ns_add_u32 s68, s71, 4096\nQmvav 88,120,24\n"
"buffer_store_dwordx4 a[96:99], v60, s[56:59], s68 offen offset:2048 sc1\nQmvav 92,124,24\nQmvav 96,128,24\ns_add_u32 s68, s71, 18432\nQmvav 100,132,24\nbuffer_store_dwordx4 a[100:103], v60, s[56:59], s68 offen offset:2048 sc1\nQmvav 104,136,24\nQmvav 108,140,24\n"
"s_waitcnt lgkmcnt(0)\nQmvav 112,112,28\ns_waitcnt vmcnt(21)\ns_barrier\nQmvav 116,116,28\nQta 144,61,16448\ns_add_u32 m0, s62, 0\ns_add_u32 s69, s87, 4194304\n"
"Qmvav 120,120,28\nbuffer_load_dwordx4 v62, s[52:55], s69 offen lds\nQmvav 124,124,28\nQta 146,61,17472\nQmvav 128,128,28\nQmvav 132,132,28\nQta 148,61,16456\ns_add_u32 s69, s87, 4226048\n"
"Qmvav 136,136,28\nbuffer_load_dwordx4 v62, s[52:55], s69 offen offset:1024 lds\nQmvav 140,140,28\nQta 150,61,17480\nQmvav 144,112,32\nQmvav 148,116,32\nQta 152,61,16704\ns_add_u32 s69, s87, 4257784\n"
"Qmvav 152,120,32\nbuffer_load_dwordx4 v62, s[52:55], s69 offen offset:2056 lds\nQmvav 156,124,32\nQta 154,61,17728\nQmvav 160,128,32\nQmvav 164,132,32\nQta 156,61,16712\ns_add_u32 s69, s87, 4289528\n"
"Qmvav 168,136,32\nbuffer_load_dwordx4 v62, s[52:55], s69 offen offset:3080 lds\nQmvav 172,140,32\nQta 158,61,17736\nQmvav 176,112,36\nQmvav 180,116,36\nQta 160,61,16960\ns_add_u32 m0, s63, 33024\n"
"s_add_u32 s88, s86, 1024\nQmvav 184,120,36\nbuffer_load_dwordx4 v64, s[48:51], s88 offen lds\nQmvav 188,124,36\nQta 162,61,17984\nQmvav 192,128,36\nQmvav 196,132,36\nQta 164,61,16968\n"
"s_add_u32 s88, s86, 20480\nQmvav 200,136,36\nbuffer_load_dwordx4 v64, s[48:51], s88 offen offset:1024 lds\nQmvav 204,140,36\nQta 166,61,17992\nQmvav 208,112,40\nQmvav 212,116,40\nQta 168,61,17216\n"
"s_add_u32 s88, s86, 39936\nQmvav 216,120,40\nbuffer_load_dwordx4 v64, s[48:51], s88 offen offset:2048 lds\nQmvav 220,124,40\nQta 170,61,18240\nQmvav 224,128,40\nQmvav 228,132,40\nQta 172,61,17224\n"
"s_add_u32 s88, s86, 59392\nQmvav 232,136,40\nbuffer_load_dwordx4 v64, s[48:51], s88 offen offset:3072 lds\nQmvav 236,140,40\nQta 174,61,18248\nQmvav 240,112,44\nQmvav 244,116,44\ns_add_u32 m0, s63, 37120\n"
"s_add_u32 s88, s86, 82944\nQmvav 248,120,44\nbuffer_load_dwordx4 v64, s[48:51], s88 offen lds\nQmvav 252,124,44\nQmaav 208,128,44\nQmaav 212,132,44\ns_add_u32 s88, s86, 102400\nQmaav 216,136,44\n"
"buffer_load_dwordx4 v64, s[48:51], s88 offen offset:1024 lds\nQmaav 220,140,44\nQmaav 224,112,48\nQmaav 228,116,48\ns_add_u32 s88, s86, 121856\nQmaav 232,120,48\nbuffer_load_dwordx4 v64, s[48:51], s88 offen offset:2048 lds\nQmaav 236,124,48\n"
"Qmaav 240,128,48\nQmaav 244,132,48\nQmaav 248,136,48\nQmaav 252,140,48\ns_waitcnt lgkmcnt(0)\nQmvaa 80,144,176\nQmvaa 84,148,176\ns_add_u32 s68, s71, 32768\n"
"Qmvaa 88,152,176\nbuffer_store_dwordx4 a[104:107], v60, s[56:59], s68 offen offset:2048 sc1\nQmvaa 92,156,176\nQmvaa 96,160,176\ns_add_u32 s68, s71, 47104\nQmvaa 100,164,176\nbuffer_store_dwordx4 a[108:111], v60, s[56:59], s68 offen offset:2048 sc1\nQmvaa 104,168,176\n"
"Qmvaa 108,172,176\ns_waitcnt lgkmcnt(0)\nQmvaa 112,144,180\ns_waitcnt vmcnt(21)\ns_barrier\nQmvaa 116,148,180\nQda 176,63,512\ns_add_u32 m0, s62, 16448\n"
"s_add_u32 s69, s87, 4456448\nQmvaa 120,152,180\nbuffer_load_dwordx4 v62, s[52:55], s69 offen lds\nQmvaa 124,156,180\nQta 112,61,33024\nQmvaa 128,160,180\nQta 114,61,34048\nQmvaa 132,164,180\n"
"Qta 116,61,33032\ns_add_u32 s69, s87, 4488192\nQmvaa 136,168,180\nbuffer_load_dwordx4 v62, s[52:55], s69 offen offset:1024 lds\nQmvaa 140,172,180\nQda 180,63,1536\nQmvaa 144,144,184\nQta 118,61,34056\n"
"Qmvaa 148,148,184\nQdv 24,63,0\ns_add_u32 s69, s87, 4519928\nQmvaa 152,152,184\nbuffer_load_dwordx4 v62, s[52:55], s69 offen offset:2056 lds\nQmvaa 156,156,184\nQta 120,61,33280\nQmvaa 160,160,184\n"
"Qta 122,61,34304\nQmvaa 164,164,184\nQta 124,61,33288\ns_add_u32 s69, s87, 4551672\nQmvaa 168,168,184\nbuffer_load_dwordx4 v62, s[52:55], s69 offen offset:3080 lds\nQmvaa 172,172,184\nQda 184,63,2560\n"
"Qmvaa 176,144,188\nQta 126,61,34312\nQmvaa 180,148,188\nQta 128,61,33536\nQmvaa 184,152,188\nQta 130,61,34560\nQmvaa 188,156,188\nQta 132,61,33544\n"
"Qmvaa 192,160,188\nQta 134,61,34568\nQmvaa 196,164,188\nQta 136,61,33792\nQmvaa 200,168,188\nQta 138,61,34816\nQmvaa 204,172,188\nQda 188,63,3584\n"
"Qmvaa 208,144,192\nQta 140,61,33800\nQmvaa 212,148,192\nQta 142,61,34824\nQmvaa 216,152,192\nQdv 28,63,1024\nQmvaa 220,156,192\nQdv 32,63,2048\n"
"Qmvaa 224,160,192\nQdv 36,63,3072\nQmvaa 228,164,192\nQdv 40,63,4096\nQmvaa 232,168,192\nQdv 44,63,5120\nQmvaa 236,172,192\nQda 192,63,4608\n"
"Qmvaa 240,144,196\nQdv 48,63,6144\nQmvaa 244,148,196\nQmvaa 248,152,196\nQmvaa 252,156,196\nQmaaa 208,160,196\nQmaaa 212,164,196\nQmaaa 216,168,196\n"
"Qmaaa 220,172,196\nQda 196,63,5632\nQmaaa 224,144,200\nQmaaa 228,148,200\nQmaaa 232,152,200\nQmaaa 236,156,200\nQmaaa 240,160,200\nQmaaa 244,164,200\n"
"Qmaaa 248,168,200\nQmaaa 252,172,200\nQda 200,63,6656\ns_waitcnt lgkmcnt(9)\nQmvav 80,112,24\nQmvav 84,116,24\nQmvav 88,120,24\nQmvav 92,124,24\n"
"Qmvav 96,128,24\nQmvav 100,132,24\nQmvav 104,136,24\nQmvav 108,140,24\ns_waitcnt lgkmcnt(0)\nQmvav 112,112,28\ns_waitcnt vmcnt(19)\ns_barrier\n"
"Qmvav 116,116,28\nQta 144,61,49472\ns_add_u32 m0, s62, 33024\ns_add_u32 s69, s87, 4718592\nQmvav 120,120,28\nbuffer_load_dwordx4 v62, s[52:55], s69 offen lds\nQmvav 124,124,28\nQta 146,61,50496\n"
"Qmvav 128,128,28\nQmvav 132,132,28\nQta 148,61,49480\ns_add_u32 s69, s87, 4750336\nQmvav 136,136,28\nbuffer_load_dwordx4 v62, s[52:55], s69 offen offset:1024 lds\nQmvav 140,140,28\nQta 150,61,50504\n"
"Qmvav 144,112,32\nQmvav 148,116,32\nQta 152,61,49728\ns_add_u32 s69, s87, 4782072\nQmvav 152,120,32\nbuffer_load_dwordx4 v62, s[52:55], s69 offen offset:2056 lds\nQmvav 156,124,32\nQta 154,61,50752\n"
"Qmvav 160,128,32\nQmvav 164,132,32\nQta 156,61,49736\ns_add_u32 s69, s87, 4813816\nQmvav 168,136,32\nbuffer_load_dwordx4 v62, s[52:55], s69 offen offset:3080 lds\nQmvav 172,140,32\nQta 158,61,50760\n"
"Qmvav 176,112,36\nQmvav 180,116,36\nQta 160,61,49984\ns_add_u32 m0, s63, 0\ns_add_u32 s88, s86, 1152\nQmvav 184,120,36\nbuffer_load_dwordx4 v64, s[48:51], s88 offen lds\nQmvav 188,124,36\n"
"Qta 162,61,51008\nQmvav 192,128,36\nQmvav 196,132,36\nQta 164,61,49992\ns_add_u32 s88, s86, 20608\nQmvav 200,136,36\nbuffer_load_dwordx4 v64, s[48:51], s88 offen offset:1024 lds\nQmvav 204,140,36\n"
"Qta 166,61,51016\nQmvav 208,112,40\nQmvav 212,116,40\nQta 168,61,50240\ns_add_u32 s88, s86, 40064\nQmvav 216,120,40\nbuffer_load_dwordx4 v64, s[48:51], s88 offen offset:2048 lds\nQmvav 220,124,40\n"
"Qta 170,61,51264\nQmvav 224,128,40\nQmvav 228,132,40\nQta 172,61,50248\ns_add_u32 s88, s86, 59520\nQmvav 232,136,40\nbuffer_load_dwordx4 v64, s[48:51], s88 offen offset:3072 lds\nQmvav 236,140,40\n"
"Qta 174,61,51272\nQmvav 240,112,44\nQmvav 244,116,44\ns_add_u32 m0, s63, 4096\ns_add_u32 s88, s86, 83072\nQmvav 248,120,44\nbuffer_load_dwordx4 v64, s[48:51], s88 offen lds\nQmvav 252,124,44\n"
"Qmaav 208,128,44\nQmaav 212,132,44\ns_add_u32 s88, s86, 102528\nQmaav 216,136,44\nbuffer_load_dwordx4 v64, s[48:51], s88 offen offset:1024 lds\nQmaav 220,140,44\nQmaav 224,112,48\nQmaav 228,116,48\n"
"s_add_u32 s88, s86, 121984\nQmaav 232,120,48\nbuffer_load_dwordx4 v64, s[48:51], s88 offen offset:2048 lds\nQmaav 236,124,48\nQmaav 240,128,48\nQmaav 244,132,48\nQmaav 248,136,48\nQmaav 252,140,48\n"
"s_waitcnt lgkmcnt(0)\nQmvaa 80,144,176\nQmvaa 84,148,176\nQmvaa 88,152,176\nQmvaa 92,156,176\nQmvaa 96,160,176\nQmvaa 100,164,176\nQmvaa 104,168,176\n"
"Qmvaa 108,172,176\ns_waitcnt lgkmcnt(0)\nQmvaa 112,144,180\ns_waitcnt vmcnt(17)\ns_barrier\nQmvaa 116,148,180\nQda 176,63,33536\ns_add_u32 m0, s62, 49472\n"
"s_add_u32 s69, s87, 4980736\nQmvaa 120,152,180\nbuffer_load_dwordx4 v62, s[52:55], s69 offen lds\nQmvaa 124,156,180\nQta 112,61,0\nQmvaa 128,160,180\nQta 114,61,1024\nQmvaa 132,164,180\n"
"Qta 116,61,8\ns_add_u32 s69, s87, 5012480\nQmvaa 136,168,180\nbuffer_load_dwordx4 v62, s[52:55], s69 offen offset:1024 lds\nQmvaa 140,172,180\nQda 180,63,34560\nQmvaa 144,144,184\nQta 118,61,1032\n"
"Qmvaa 148,148,184\nQdv 24,63,33024\ns_add_u32 s69, s87, 5044216\nQmvaa 152,152,184\nbuffer_load_dwordx4 v62, s[52:55], s69 offen offset:2056 lds\nQmvaa 156,156,184\nQta 120,61,256\nQmvaa 160,160,184\n"
"Qta 122,61,1280\nQmvaa 164,164,184\nQta 124,61,264\ns_add_u32 s69, s87, 5075960\nQmvaa 168,168,184\nbuffer_load_dwordx4 v62, s[52:55], s69 offen offset:3080 lds\nQmvaa 172,172,184\nQda 184,63,35584\n"
"Qmvaa 176,144,188\nQta 126,61,1288\nQmvaa 180,148,188\nQta 128,61,512\nQmvaa 184,152,188\nQta 130,61,1536\nQmvaa 188,156,188\nQta 132,61,520\n"
"Qmvaa 192,160,188\nQta 134,61,1544\nQmvaa 196,164,188\nQta 136,61,768\nQmvaa 200,168,188\nQta 138,61,1792\nQmvaa 204,172,188\nQda 188,63,36608\n"
"Qmvaa 208,144,192\nQta 140,61,776\nQmvaa 212,148,192\nQta 142,61,1800\nQmvaa 216,152,192\nQdv 28,63,34048\nQmvaa 220,156,192\nQdv 32,63,35072\n"
"Qmvaa 224,160,192\nQdv 36,63,36096\nQmvaa 228,164,192\nQdv 40,63,37120\nQmvaa 232,168,192\nQdv 44,63,38144\nQmvaa 236,172,192\nQda 192,63,37632\n"
"Qmvaa 240,144,196\nQdv 48,63,39168\nQmvaa 244,148,196\nQmvaa 248,152,196\nQmvaa 252,156,196\nQmaaa 208,160,196\nQmaaa 212,164,196\nQmaaa 216,168,196\n"
"Qmaaa 220,172,196\nQda 196,63,38656\nQmaaa 224,144,200\nQmaaa 228,148,200\nQmaaa 232,152,200\nQmaaa 236,156,200\nQmaaa 240,160,200\nQmaaa 244,164,200\n"
"Qmaaa 248,168,200\nQmaaa 252,172,200\nQda 200,63,39680\ns_waitcnt lgkmcnt(9)\nQmvav 80,112,24\nQmvav 84,116,24\nQmvav 88,120,24\nQmvav 92,124,24\n"
"Qmvav 96,128,24\nQmvav 100,132,24\nQmvav 104,136,24\nQmvav 108,140,24\ns_waitcnt lgkmcnt(0)\nQmvav 112,112,28\ns_waitcnt vmcnt(15)\ns_barrier\n"
"Qmvav 116,116,28\nQta 144,61,16448\ns_mov_b32 s56, s94\ns_mov_b32 s57, s95\ns_mov_b32 s58, s100\ns_mov_b32 s94, 0\ns_add_u32 m0, %10, 160768\nQmvav 120,120,28\n"
"buffer_load_dwordx4 v60, s[56:59], s94 offen lds\nQmvav 124,124,28\nQta 146,61,17472\nQmvav 128,128,28\nQmvav 132,132,28\nQta 148,61,16456\ns_add_u32 s94, s94, 4096\nQmvav 136,136,28\n"
"buffer_load_dwordx4 v60, s[56:59], s94 offen lds\nQmvav 140,140,28\nQta 150,61,17480\nQmvav 144,112,32\nQmvav 148,116,32\nQta 152,61,16704\ns_add_u32 s94, s94, 4096\nQmvav 152,120,32\n"
"buffer_load_dwordx4 v60, s[56:59], s94 offen lds\nQmvav 156,124,32\nQta 154,61,17728\nQmvav 160,128,32\nQmvav 164,132,32\nQta 156,61,16712\ns_add_u32 s94, s94, 4096\nQmvav 168,136,32\n"
"buffer_load_dwordx4 v60, s[56:59], s94 offen lds\nQmvav 172,140,32\nQta 158,61,17736\nQmvav 176,112,36\nQmvav 180,116,36\nQta 160,61,16960\ns_add_u32 s94, s94, 4096\nQmvav 184,120,36\n"
"buffer_load_dwordx4 v60, s[56:59], s94 offen lds\nQmvav 188,124,36\nQta 162,61,17984\nQmvav 192,128,36\nQmvav 196,132,36\nQta 164,61,16968\ns_add_u32 s94, s94, 4096\nQmvav 200,136,36\n"
"buffer_load_dwordx4 v60, s[56:59], s94 offen lds\nQmvav 204,140,36\nQta 166,61,17992\nQmvav 208,112,40\nQmvav 212,116,40\nQta 168,61,17216\ns_add_u32 s94, s94, 4096\nQmvav 216,120,40\n"
"buffer_load_dwordx4 v60, s[56:59], s94 offen lds\nQmvav 220,124,40\nQta 170,61,18240\nQmvav 224,128,40\nQmvav 228,132,40\nQta 172,61,17224\ns_add_u32 s94, s94, 4096\nQmvav 232,136,40\n"
"buffer_load_dwordx4 v60, s[56:59], s94 offen lds\nQmvav 236,140,40\nQta 174,61,18248\nQmvav 240,112,44\nQmvav 244,116,44\ns_add_u32 s94, s94, 4096\nQmvav 248,120,44\nbuffer_load_dwordx4 v60, s[56:59], s94 offen lds\n"
"Qmvav 252,124,44\nQmaav 208,128,44\nQmaav 212,132,44\ns_add_u32 s94, s94, 4096\nQmaav 216,136,44\nbuffer_load_dwordx4 v60, s[56:59], s94 offen lds\nQmaav 220,140,44\nQmaav 224,112,48\n"
"Qmaav 228,116,48\nQmaav 232,120,48\nQmaav 236,124,48\nQmaav 240,128,48\nQmaav 244,132,48\nQmaav 248,136,48\nQmaav 252,140,48\ns_waitcnt lgkmcnt(0)\n"
"Qmvaa 80,144,176\nQmvaa 84,148,176\nQmvaa 88,152,176\nQmvaa 92,156,176\nQmvaa 96,160,176\nQmvaa 100,164,176\nQmvaa 104,168,176\nQmvaa 108,172,176\n"
"s_waitcnt lgkmcnt(0)\nQmvaa 112,144,180\ns_waitcnt vmcnt(14)\ns_barrier\nQmvaa 116,148,180\nQda 176,63,512\ns_add_u32 m0, s61, 33024\nQmvaa 120,152,180\n"
"buffer_load_dwordx4 v56, s[40:43], s89 offen nt lds\nQmvaa 124,156,180\nQta 112,61,33024\nQmvaa 128,160,180\nQta 114,61,34048\nQmvaa 132,164,180\nQta 116,61,33032\nQmvaa 136,168,180\n"
"buffer_load_dwordx4 v57, s[40:43], s89 offen offset:1024 nt lds\nQmvaa 140,172,180\nQda 180,63,1536\nQmvaa 144,144,184\nQta 118,61,34056\nQmvaa 148,148,184\nQdv 24,63,0\nQmvaa 152,152,184\n"
"buffer_load_dwordx4 v56, s[40:43], s89 offen offset:2048 nt lds\nQmvaa 156,156,184\nQta 120,61,33280\nQmvaa 160,160,184\nQta 122,61,34304\nQmvaa 164,164,184\nQta 124,61,33288\nQmvaa 168,168,184\n"
"buffer_load_dwordx4 v57, s[40:43], s89 offen offset:3072 nt lds\nQmvaa 172,172,184\nQda 184,63,2560\nQmvaa 176,144,188\nQta 126,61,34312\nQmvaa 180,148,188\nQta 128,61,33536\ns_add_u32 m0, s61, 41216\n"
"Qmvaa 184,152,188\nbuffer_load_dwordx4 v58, s[44:47], s89 offen nt lds\nQmvaa 188,156,188\nQta 130,61,34560\nQmvaa 192,160,188\nQta 132,61,33544\nQmvaa 196,164,188\nQta 134,61,34568\n"
"Qmvaa 200,168,188\nbuffer_load_dwordx4 v59, s[44:47], s89 offen offset:1024 nt lds\nQmvaa 204,172,188\nQda 188,63,3584\nQmvaa 208,144,192\nQta 136,61,33792\nQmvaa 212,148,192\nQta 138,61,34816\n"
"Qmvaa 216,152,192\nbuffer_load_dwordx4 v58, s[44:47], s89 offen offset:2048 nt lds\nQmvaa 220,156,192\nQta 140,61,33800\nQmvaa 224,160,192\nQta 142,61,34824\nQmvaa 228,164,192\nQdv 28,63,1024\n"
"Qmvaa 232,168,192\nbuffer_load_dwordx4 v59, s[44:47], s89 offen offset:3072 nt lds\ns_add_u32 s60, s60, 8192\ns_xor_b32 s89, s60, s101\ns_add_u32 s88, s60, 8192\ns_xor_b32 s88, s88, s101\nQmvaa 236,172,192\nQda 192,63,4608\n"
"Qmvaa 240,144,196\nQdv 32,63,2048\nQmvaa 244,148,196\nQdv 36,63,3072\nQmvaa 248,152,196\nQdv 40,63,4096\nQmvaa 252,156,196\nQdv 44,63,5120\n"
"Qmaaa 208,160,196\nQdv 48,63,6144\nQmaaa 212,164,196\nQmaaa 216,168,196\nQmaaa 220,172,196\nQda 196,63,5632\nQmaaa 224,144,200\nQmaaa 228,148,200\n"
"Qmaaa 232,152,200\nQmaaa 236,156,200\nQmaaa 240,160,200\nQmaaa 244,164,200\nQmaaa 248,168,200\nQmaaa 252,172,200\nQda 200,63,6656\ns_waitcnt lgkmcnt(9)\n"
"Qmvav 80,112,24\nQmvav 84,116,24\nQmvav 88,120,24\nQmvav 92,124,24\nQmvav 96,128,24\nQmvav 100,132,24\nQmvav 104,136,24\nQmvav 108,140,24\n"
"s_waitcnt lgkmcnt(0)\nQmvav 112,112,28\ns_waitcnt vmcnt(18)\ns_barrier\nQmvav 116,116,28\nQta 144,61,49472\ns_add_u32 m0, s61, 99072\nQmvav 120,120,28\n"
"buffer_load_dwordx4 v56, s[40:43], s88 offen nt lds\nQmvav 124,124,28\nQta 146,61,50496\nQmvav 128,128,28\nQmvav 132,132,28\nQta 148,61,49480\nQmvav 136,136,28\nbuffer_load_dwordx4 v57, s[40:43], s88 offen offset:1024 nt lds\n"
"Qmvav 140,140,28\nQta 150,61,50504\nQmvav 144,112,32\nQmvav 148,116,32\nQta 152,61,49728\nQmvav 152,120,32\nbuffer_load_dwordx4 v56, s[40:43], s88 offen offset:2048 nt lds\nQmvav 156,124,32\n"
"Qta 154,61,50752\nQmvav 160,128,32\nQmvav 164,132,32\nQta 156,61,49736\nQmvav 168,136,32\nbuffer_load_dwordx4 v57, s[40:43], s88 offen offset:3072 nt lds\nQmvav 172,140,32\nQta 158,61,50760\n"
"Qmvav 176,112,36\nQmvav 180,116,36\nQta 160,61,49984\ns_add_u32 m0, s61, 107264\nQmvav 184,120,36\nbuffer_load_dwordx4 v58, s[44:47], s88 offen nt lds\nQmvav 188,124,36\nQta 162,61,51008\n"
"Qmvav 192,128,36\nQmvav 196,132,36\nQta 164,61,49992\nQmvav 200,136,36\nbuffer_load_dwordx4 v59, s[44:47], s88 offen offset:1024 nt lds\nQmvav 204,140,36\nQta 166,61,51016\nQmvav 208,112,40\n"
"Qmvav 212,116,40\nQta 168,61,50240\nQmvav 216,120,40\nbuffer_load_dwordx4 v58, s[44:47], s88 offen offset:2048 nt lds\nQmvav 220,124,40\nQta 170,61,51264\nQmvav 224,128,40\nQmvav 228,132,40\n"
"Qta 172,61,50248\nQmvav 232,136,40\nbuffer_load_dwordx4 v59, s[44:47], s88 offen offset:3072 nt lds\nQmvav 236,140,40\nQta 174,61,51272\nQmvav 240,112,44\nQmvav 244,116,44\nQmvav 248,120,44\n"
"Qmvav 252,124,44\nQmaav 208,128,44\nQmaav 212,132,44\nQmaav 216,136,44\nQmaav 220,140,44\nQmaav 224,112,48\nQmaav 228,116,48\nQmaav 232,120,48\n"
"Qmaav 236,124,48\nQmaav 240,128,48\nQmaav 244,132,48\nQmaav 248,136,48\nQmaav 252,140,48\ns_waitcnt lgkmcnt(0)\nQmvaa 80,144,176\nQmvaa 84,148,176\n"
"Qmvaa 88,152,176\nQmvaa 92,156,176\nQmvaa 96,160,176\nQmvaa 100,164,176\nQmvaa 104,168,176\nQmvaa 108,172,176\ns_waitcnt lgkmcnt(0)\nQmvaa 112,144,180\n"
"s_waitcnt vmcnt(63)\ns_barrier\nQmvaa 116,148,180\nQda 112,52,0\ns_add_u32 m0, s61, 66048\nQmvaa 120,152,180\nbuffer_load_dwordx4 v56, s[40:43], s89 offen nt lds\nQmvaa 124,156,180\n"
"Qda 116,52,512\nQmvaa 128,160,180\nQmvaa 132,164,180\nQda 120,52,1024\nQmvaa 136,168,180\nbuffer_load_dwordx4 v57, s[40:43], s89 offen offset:1024 nt lds\nQmvaa 140,172,180\nQda 124,52,1536\n"
"Qmvaa 144,144,184\nQmvaa 148,148,184\nQda 128,52,2048\nQmvaa 152,152,184\nbuffer_load_dwordx4 v56, s[40:43], s89 offen offset:2048 nt lds\nQmvaa 156,156,184\nQda 132,52,2560\nQmvaa 160,160,184\n"
"Qmvaa 164,164,184\nQda 136,52,3072\nQmvaa 168,168,184\nbuffer_load_dwordx4 v57, s[40:43], s89 offen offset:3072 nt lds\nQmvaa 172,172,184\nQda 140,52,3584\nQmvaa 176,144,188\nQmvaa 180,148,188\n"
"s_add_u32 m0, s61, 74240\nQmvaa 184,152,188\nbuffer_load_dwordx4 v58, s[44:47], s89 offen nt lds\nQmvaa 188,156,188\nQmvaa 192,160,188\nQmvaa 196,164,188\nQmvaa 200,168,188\nbuffer_load_dwordx4 v59, s[44:47], s89 offen offset:1024 nt lds\n"
"Qmvaa 204,172,188\nQmvaa 208,144,192\nQmvaa 212,148,192\nQmvaa 216,152,192\nbuffer_load_dwordx4 v58, s[44:47], s89 offen offset:2048 nt lds\nQmvaa 220,156,192\nQmvaa 224,160,192\nQmvaa 228,164,192\n"
"Qmvaa 232,168,192\nbuffer_load_dwordx4 v59, s[44:47], s89 offen offset:3072 nt lds\ns_add_u32 s60, s60, 16384\ns_xor_b32 s89, s60, s101\nQmvaa 236,172,192\nQmvaa 240,144,196\nQmvaa 244,148,196\nQmvaa 248,152,196\n"
"Qmvaa 252,156,196\nQmaaa 208,160,196\nQmaaa 212,164,196\nQmaaa 216,168,196\nQmaaa 220,172,196\nQmaaa 224,144,200\nQmaaa 228,148,200\nQmaaa 232,152,200\n"
"Qmaaa 236,156,200\nQmaaa 240,160,200\nQmaaa 244,164,200\nQmaaa 248,168,200\nQmaaa 252,172,200\nQta 144,54,0\nQta 146,54,2048\nQta 148,54,256\n"
"Qta 150,54,2304\nQta 152,54,512\nQta 154,54,2560\nQta 156,54,768\nQta 158,54,2816\nQta 160,54,1024\nQta 162,54,3072\nQta 164,54,1280\n"
"Qta 166,54,3328\nQta 168,54,1536\nQta 170,54,3584\nQta 172,54,1792\nQta 174,54,3840\nv_pk_mul_f32 v[80:81], v[80:81], s[72:73] op_sel_hi:[1,0]\nv_pk_mul_f32 v[82:83], v[82:83], s[72:73] op_sel_hi:[1,0]\nv_pk_mul_f32 v[84:85], v[84:85], s[72:73] op_sel_hi:[1,0]\n"
"v_pk_mul_f32 v[86:87], v[86:87], s[72:73] op_sel_hi:[1,0]\nQk 80,80,81\nQk 81,82,83\nQk 82,84,85\nQk 83,86,87\nv_accvgpr_write_b32 a0, v80\nv_accvgpr_write_b32 a1, v81\nv_accvgpr_write_b32 a2, v82\n"
"v_accvgpr_write_b32 a3, v83\nv_pk_mul_f32 v[88:89], v[88:89], s[72:73] op_sel_hi:[1,0]\nv_pk_mul_f32 v[90:91], v[90:91], s[72:73] op_sel_hi:[1,0]\nv_pk_mul_f32 v[92:93], v[92:93], s[72:73] op_sel_hi:[1,0]\nv_pk_mul_f32 v[94:95], v[94:95], s[72:73] op_sel_hi:[1,0]\nQk 88,88,89\nQk 89,90,91\nQk 90,92,93\n"
"Qk 91,94,95\nv_accvgpr_write_b32 a4, v88\nv_accvgpr_write_b32 a5, v89\nv_accvgpr_write_b32 a6, v90\nv_accvgpr_write_b32 a7, v91\nv_pk_mul_f32 v[96:97], v[96:97], s[72:73] op_sel_hi:[1,0]\nv_pk_mul_f32 v[98:99], v[98:99], s[72:73] op_sel_hi:[1,0]\nv_pk_mul_f32 v[100:101], v[100:101], s[72:73] op_sel_hi:[1,0]\n"
"v_pk_mul_f32 v[102:103], v[102:103], s[72:73] op_sel_hi:[1,0]\nQk 96,96,97\nQk 97,98,99\nQk 98,100,101\nQk 99,102,103\nv_accvgpr_write_b32 a8, v96\nv_accvgpr_write_b32 a9, v97\nv_accvgpr_write_b32 a10, v98\n"
"v_accvgpr_write_b32 a11, v99\nv_pk_mul_f32 v[104:105], v[104:105], s[72:73] op_sel_hi:[1,0]\nv_pk_mul_f32 v[106:107], v[106:107], s[72:73] op_sel_hi:[1,0]\nv_pk_mul_f32 v[108:109], v[108:109], s[72:73] op_sel_hi:[1,0]\nv_pk_mul_f32 v[110:111], v[110:111], s[72:73] op_sel_hi:[1,0]\nQk 104,104,105\nQk 105,106,107\nQk 106,108,109\n"
"Qk 107,110,111\nv_accvgpr_write_b32 a12, v104\nv_accvgpr_write_b32 a13, v105\nv_accvgpr_write_b32 a14, v106\nv_accvgpr_write_b32 a15, v107\nv_pk_mul_f32 v[112:113], v[112:113], s[72:73] op_sel_hi:[1,0]\nv_pk_mul_f32 v[114:115], v[114:115], s[72:73] op_sel_hi:[1,0]\nv_pk_mul_f32 v[116:117], v[116:117], s[72:73] op_sel_hi:[1,0]\n"
"v_pk_mul_f32 v[118:119], v[118:119], s[72:73] op_sel_hi:[1,0]\nQk 112,112,113\nQk 113,114,115\nQk 114,116,117\nQk 115,118,119\nv_accvgpr_write_b32 a16, v112\nv_accvgpr_write_b32 a17, v113\nv_accvgpr_write_b32 a18, v114\n"
"v_accvgpr_write_b32 a19, v115\nv_pk_mul_f32 v[120:121], v[120:121], s[72:73] op_sel_hi:[1,0]\nv_pk_mul_f32 v[122:123], v[122:123], s[72:73] op_sel_hi:[1,0]\nv_pk_mul_f32 v[124:125], v[124:125], s[72:73] op_sel_hi:[1,0]\nv_pk_mul_f32 v[126:127], v[126:127], s[72:73] op_sel_hi:[1,0]\nQk 120,120,121\nQk 121,122,123\nQk 122,124,125\n"
"Qk 123,126,127\nv_accvgpr_write_b32 a20, v120\nv_accvgpr_write_b32 a21, v121\nv_accvgpr_write_b32 a22, v122\nv_accvgpr_write_b32 a23, v123\nv_pk_mul_f32 v[128:129], v[128:129], s[72:73] op_sel_hi:[1,0]\nv_pk_mul_f32 v[130:131], v[130:131], s[72:73] op_sel_hi:[1,0]\nv_pk_mul_f32 v[132:133], v[132:133], s[72:73] op_sel_hi:[1,0]\n"
"v_pk_mul_f32 v[134:135], v[134:135], s[72:73] op_sel_hi:[1,0]\nQk 128,128,129\nQk 129,130,131\nQk 130,132,133\nQk 131,134,135\nv_accvgpr_write_b32 a24, v128\nv_accvgpr_write_b32 a25, v129\nv_accvgpr_write_b32 a26, v130\n"
"v_accvgpr_write_b32 a27, v131\nv_pk_mul_f32 v[136:137], v[136:137], s[72:73] op_sel_hi:[1,0]\nv_pk_mul_f32 v[138:139], v[138:139], s[72:73] op_sel_hi:[1,0]\nv_pk_mul_f32 v[140:141], v[140:141], s[72:73] op_sel_hi:[1,0]\nv_pk_mul_f32 v[142:143], v[142:143], s[72:73] op_sel_hi:[1,0]\nQk 136,136,137\nQk 137,138,139\nQk 138,140,141\n"
"Qk 139,142,143\nv_accvgpr_write_b32 a28, v136\nv_accvgpr_write_b32 a29, v137\nv_accvgpr_write_b32 a30, v138\nv_accvgpr_write_b32 a31, v139\nv_pk_mul_f32 v[144:145], v[144:145], s[72:73] op_sel_hi:[1,0]\nv_pk_mul_f32 v[146:147], v[146:147], s[72:73] op_sel_hi:[1,0]\nv_pk_mul_f32 v[148:149], v[148:149], s[72:73] op_sel_hi:[1,0]\n"
"v_pk_mul_f32 v[150:151], v[150:151], s[72:73] op_sel_hi:[1,0]\nQk 144,144,145\nQk 145,146,147\nQk 146,148,149\nQk 147,150,151\nv_accvgpr_write_b32 a32, v144\nv_accvgpr_write_b32 a33, v145\nv_accvgpr_write_b32 a34, v146\n"
"v_accvgpr_write_b32 a35, v147\nv_pk_mul_f32 v[152:153], v[152:153], s[72:73] op_sel_hi:[1,0]\nv_pk_mul_f32 v[154:155], v[154:155], s[72:73] op_sel_hi:[1,0]\nv_pk_mul_f32 v[156:157], v[156:157], s[72:73] op_sel_hi:[1,0]\nv_pk_mul_f32 v[158:159], v[158:159], s[72:73] op_sel_hi:[1,0]\nQk 152,152,153\nQk 153,154,155\nQk 154,156,157\n"
"Qk 155,158,159\nv_accvgpr_write_b32 a36, v152\nv_accvgpr_write_b32 a37, v153\nv_accvgpr_write_b32 a38, v154\nv_accvgpr_write_b32 a39, v155\nv_pk_mul_f32 v[160:161], v[160:161], s[72:73] op_sel_hi:[1,0]\nv_pk_mul_f32 v[162:163], v[162:163], s[72:73] op_sel_hi:[1,0]\nv_pk_mul_f32 v[164:165], v[164:165], s[72:73] op_sel_hi:[1,0]\n"
"v_pk_mul_f32 v[166:167], v[166:167], s[72:73] op_sel_hi:[1,0]\nQk 160,160,161\nQk 161,162,163\nQk 162,164,165\nQk 163,166,167\nv_accvgpr_write_b32 a40, v160\nv_accvgpr_write_b32 a41, v161\nv_accvgpr_write_b32 a42, v162\n"
"v_accvgpr_write_b32 a43, v163\nv_pk_mul_f32 v[168:169], v[168:169], s[72:73] op_sel_hi:[1,0]\nv_pk_mul_f32 v[170:171], v[170:171], s[72:73] op_sel_hi:[1,0]\nv_pk_mul_f32 v[172:173], v[172:173], s[72:73] op_sel_hi:[1,0]\nv_pk_mul_f32 v[174:175], v[174:175], s[72:73] op_sel_hi:[1,0]\nQk 168,168,169\nQk 169,170,171\nQk 170,172,173\n"
"Qk 171,174,175\nv_accvgpr_write_b32 a44, v168\nv_accvgpr_write_b32 a45, v169\nv_accvgpr_write_b32 a46, v170\nv_accvgpr_write_b32 a47, v171\nv_pk_mul_f32 v[176:177], v[176:177], s[72:73] op_sel_hi:[1,0]\nv_pk_mul_f32 v[178:179], v[178:179], s[72:73] op_sel_hi:[1,0]\nv_pk_mul_f32 v[180:181], v[180:181], s[72:73] op_sel_hi:[1,0]\n"
"v_pk_mul_f32 v[182:183], v[182:183], s[72:73] op_sel_hi:[1,0]\nQk 176,176,177\nQk 177,178,179\nQk 178,180,181\nQk 179,182,183\nv_accvgpr_write_b32 a48, v176\nv_accvgpr_write_b32 a49, v177\nv_accvgpr_write_b32 a50, v178\n"
"v_accvgpr_write_b32 a51, v179\nv_pk_mul_f32 v[184:185], v[184:185], s[72:73] op_sel_hi:[1,0]\nv_pk_mul_f32 v[186:187], v[186:187], s[72:73] op_sel_hi:[1,0]\nv_pk_mul_f32 v[188:189], v[188:189], s[72:73] op_sel_hi:[1,0]\nv_pk_mul_f32 v[190:191], v[190:191], s[72:73] op_sel_hi:[1,0]\nQk 184,184,185\nQk 185,186,187\nQk 186,188,189\n"
"Qk 187,190,191\nv_accvgpr_write_b32 a52, v184\nv_accvgpr_write_b32 a53, v185\nv_accvgpr_write_b32 a54, v186\nv_accvgpr_write_b32 a55, v187\nv_pk_mul_f32 v[192:193], v[192:193], s[72:73] op_sel_hi:[1,0]\nv_pk_mul_f32 v[194:195], v[194:195], s[72:73] op_sel_hi:[1,0]\nv_pk_mul_f32 v[196:197], v[196:197], s[72:73] op_sel_hi:[1,0]\n"
"v_pk_mul_f32 v[198:199], v[198:199], s[72:73] op_sel_hi:[1,0]\nQk 192,192,193\nQk 193,194,195\nQk 194,196,197\nQk 195,198,199\nv_accvgpr_write_b32 a56, v192\nv_accvgpr_write_b32 a57, v193\nv_accvgpr_write_b32 a58, v194\n"
"v_accvgpr_write_b32 a59, v195\nv_pk_mul_f32 v[200:201], v[200:201], s[72:73] op_sel_hi:[1,0]\nv_pk_mul_f32 v[202:203], v[202:203], s[72:73] op_sel_hi:[1,0]\nv_pk_mul_f32 v[204:205], v[204:205], s[72:73] op_sel_hi:[1,0]\nv_pk_mul_f32 v[206:207], v[206:207], s[72:73] op_sel_hi:[1,0]\nQk 200,200,201\nQk 201,202,203\nQk 202,204,205\n"
"Qk 203,206,207\nv_accvgpr_write_b32 a60, v200\nv_accvgpr_write_b32 a61, v201\nv_accvgpr_write_b32 a62, v202\nv_accvgpr_write_b32 a63, v203\nv_pk_mul_f32 v[208:209], v[208:209], s[72:73] op_sel_hi:[1,0]\nv_pk_mul_f32 v[210:211], v[210:211], s[72:73] op_sel_hi:[1,0]\nv_pk_mul_f32 v[212:213], v[212:213], s[72:73] op_sel_hi:[1,0]\n"
"v_pk_mul_f32 v[214:215], v[214:215], s[72:73] op_sel_hi:[1,0]\nQk 208,208,209\nQk 209,210,211\nQk 210,212,213\nQk 211,214,215\nv_accvgpr_write_b32 a64, v208\nv_accvgpr_write_b32 a65, v209\nv_accvgpr_write_b32 a66, v210\n"
"v_accvgpr_write_b32 a67, v211\nv_pk_mul_f32 v[216:217], v[216:217], s[72:73] op_sel_hi:[1,0]\nv_pk_mul_f32 v[218:219], v[218:219], s[72:73] op_sel_hi:[1,0]\nv_pk_mul_f32 v[220:221], v[220:221], s[72:73] op_sel_hi:[1,0]\nv_pk_mul_f32 v[222:223], v[222:223], s[72:73] op_sel_hi:[1,0]\nQk 216,216,217\nQk 217,218,219\nQk 218,220,221\n"
"Qk 219,222,223\nv_accvgpr_write_b32 a68, v216\nv_accvgpr_write_b32 a69, v217\nv_accvgpr_write_b32 a70, v218\nv_accvgpr_write_b32 a71, v219\nv_pk_mul_f32 v[224:225], v[224:225], s[72:73] op_sel_hi:[1,0]\nv_pk_mul_f32 v[226:227], v[226:227], s[72:73] op_sel_hi:[1,0]\nv_pk_mul_f32 v[228:229], v[228:229], s[72:73] op_sel_hi:[1,0]\n"
"v_pk_mul_f32 v[230:231], v[230:231], s[72:73] op_sel_hi:[1,0]\nQk 224,224,225\nQk 225,226,227\nQk 226,228,229\nQk 227,230,231\nv_accvgpr_write_b32 a72, v224\nv_accvgpr_write_b32 a73, v225\nv_accvgpr_write_b32 a74, v226\n"
"v_accvgpr_write_b32 a75, v227\nv_pk_mul_f32 v[232:233], v[232:233], s[72:73] op_sel_hi:[1,0]\nv_pk_mul_f32 v[234:235], v[234:235], s[72:73] op_sel_hi:[1,0]\nv_pk_mul_f32 v[236:237], v[236:237], s[72:73] op_sel_hi:[1,0]\nv_pk_mul_f32 v[238:239], v[238:239], s[72:73] op_sel_hi:[1,0]\nQk 232,232,233\nQk 233,234,235\nQk 234,236,237\n"
"Qk 235,238,239\nv_accvgpr_write_b32 a76, v232\nv_accvgpr_write_b32 a77, v233\nv_accvgpr_write_b32 a78, v234\nv_accvgpr_write_b32 a79, v235\nv_pk_mul_f32 v[240:241], v[240:241], s[72:73] op_sel_hi:[1,0]\nv_pk_mul_f32 v[242:243], v[242:243], s[72:73] op_sel_hi:[1,0]\nv_pk_mul_f32 v[244:245], v[244:245], s[72:73] op_sel_hi:[1,0]\n"
"v_pk_mul_f32 v[246:247], v[246:247], s[72:73] op_sel_hi:[1,0]\nQk 240,240,241\nQk 241,242,243\nQk 242,244,245\nQk 243,246,247\nv_accvgpr_write_b32 a80, v240\nv_accvgpr_write_b32 a81, v241\nv_accvgpr_write_b32 a82, v242\n"
"v_accvgpr_write_b32 a83, v243\nv_pk_mul_f32 v[248:249], v[248:249], s[72:73] op_sel_hi:[1,0]\nv_pk_mul_f32 v[250:251], v[250:251], s[72:73] op_sel_hi:[1,0]\nv_pk_mul_f32 v[252:253], v[252:253], s[72:73] op_sel_hi:[1,0]\nv_pk_mul_f32 v[254:255], v[254:255], s[72:73] op_sel_hi:[1,0]\nQk 248,248,249\nQk 249,250,251\nQk 250,252,253\n"
"Qk 251,254,255\nv_accvgpr_write_b32 a84, v248\nv_accvgpr_write_b32 a85, v249\nv_accvgpr_write_b32 a86, v250\nv_accvgpr_write_b32 a87, v251\nv_accvgpr_read_b32 v0, a208\nv_accvgpr_read_b32 v1, a209\nv_accvgpr_read_b32 v2, a210\n"
"v_accvgpr_read_b32 v3, a211\nv_accvgpr_read_b32 v4, a212\nv_accvgpr_read_b32 v5, a213\nv_accvgpr_read_b32 v6, a214\nv_accvgpr_read_b32 v7, a215\nv_pk_mul_f32 v[0:1], v[0:1], s[72:73] op_sel_hi:[1,0]\nv_pk_mul_f32 v[2:3], v[2:3], s[72:73] op_sel_hi:[1,0]\nv_pk_mul_f32 v[4:5], v[4:5], s[72:73] op_sel_hi:[1,0]\n"
"v_pk_mul_f32 v[6:7], v[6:7], s[72:73] op_sel_hi:[1,0]\nQk 0,0,1\nQk 1,2,3\nQk 2,4,5\nQk 3,6,7\nv_accvgpr_write_b32 a88, v0\nv_accvgpr_write_b32 a89, v1\nv_accvgpr_write_b32 a90, v2\n"
"v_accvgpr_write_b32 a91, v3\nv_accvgpr_read_b32 v8, a216\nv_accvgpr_read_b32 v9, a217\nv_accvgpr_read_b32 v10, a218\nv_accvgpr_read_b32 v11, a219\nv_accvgpr_read_b32 v12, a220\nv_accvgpr_read_b32 v13, a221\nv_accvgpr_read_b32 v14, a222\n"
"v_accvgpr_read_b32 v15, a223\nv_pk_mul_f32 v[8:9], v[8:9], s[72:73] op_sel_hi:[1,0]\nv_pk_mul_f32 v[10:11], v[10:11], s[72:73] op_sel_hi:[1,0]\nv_pk_mul_f32 v[12:13], v[12:13], s[72:73] op_sel_hi:[1,0]\nv_pk_mul_f32 v[14:15], v[14:15], s[72:73] op_sel_hi:[1,0]\nQk 8,8,9\nQk 9,10,11\nQk 10,12,13\n"
"Qk 11,14,15\nv_accvgpr_write_b32 a92, v8\nv_accvgpr_write_b32 a93, v9\nv_accvgpr_write_b32 a94, v10\nv_accvgpr_write_b32 a95, v11\nv_accvgpr_read_b32 v0, a224\nv_accvgpr_read_b32 v1, a225\nv_accvgpr_read_b32 v2, a226\n"
"v_accvgpr_read_b32 v3, a227\nv_accvgpr_read_b32 v4, a228\nv_accvgpr_read_b32 v5, a229\nv_accvgpr_read_b32 v6, a230\nv_accvgpr_read_b32 v7, a231\nv_pk_mul_f32 v[0:1], v[0:1], s[72:73] op_sel_hi:[1,0]\nv_pk_mul_f32 v[2:3], v[2:3], s[72:73] op_sel_hi:[1,0]\nv_pk_mul_f32 v[4:5], v[4:5], s[72:73] op_sel_hi:[1,0]\n"
"v_pk_mul_f32 v[6:7], v[6:7], s[72:73] op_sel_hi:[1,0]\nQk 0,0,1\nQk 1,2,3\nQk 2,4,5\nQk 3,6,7\nv_accvgpr_write_b32 a96, v0\nv_accvgpr_write_b32 a97, v1\nv_accvgpr_write_b32 a98, v2\n"
"v_accvgpr_write_b32 a99, v3\nv_accvgpr_read_b32 v8, a232\nv_accvgpr_read_b32 v9, a233\nv_accvgpr_read_b32 v10, a234\nv_accvgpr_read_b32 v11, a235\nv_accvgpr_read_b32 v12, a236\nv_accvgpr_read_b32 v13, a237\nv_accvgpr_read_b32 v14, a238\n"
"v_accvgpr_read_b32 v15, a239\nv_pk_mul_f32 v[8:9], v[8:9], s[72:73] op_sel_hi:[1,0]\nv_pk_mul_f32 v[10:11], v[10:11], s[72:73] op_sel_hi:[1,0]\nv_pk_mul_f32 v[12:13], v[12:13], s[72:73] op_sel_hi:[1,0]\nv_pk_mul_f32 v[14:15], v[14:15], s[72:73] op_sel_hi:[1,0]\nQk 8,8,9\nQk 9,10,11\nQk 10,12,13\n"
"Qk 11,14,15\nv_accvgpr_write_b32 a100, v8\nv_accvgpr_write_b32 a101, v9\nv_accvgpr_write_b32 a102, v10\nv_accvgpr_write_b32 a103, v11\nv_accvgpr_read_b32 v0, a240\nv_accvgpr_read_b32 v1, a241\nv_accvgpr_read_b32 v2, a242\n"
"v_accvgpr_read_b32 v3, a243\nv_accvgpr_read_b32 v4, a244\nv_accvgpr_read_b32 v5, a245\nv_accvgpr_read_b32 v6, a246\nv_accvgpr_read_b32 v7, a247\nv_pk_mul_f32 v[0:1], v[0:1], s[72:73] op_sel_hi:[1,0]\nv_pk_mul_f32 v[2:3], v[2:3], s[72:73] op_sel_hi:[1,0]\nv_pk_mul_f32 v[4:5], v[4:5], s[72:73] op_sel_hi:[1,0]\n"
"v_pk_mul_f32 v[6:7], v[6:7], s[72:73] op_sel_hi:[1,0]\nQk 0,0,1\nQk 1,2,3\nQk 2,4,5\nQk 3,6,7\nv_accvgpr_write_b32 a104, v0\nv_accvgpr_write_b32 a105, v1\nv_accvgpr_write_b32 a106, v2\n"
"v_accvgpr_write_b32 a107, v3\nv_accvgpr_read_b32 v8, a248\nv_accvgpr_read_b32 v9, a249\nv_accvgpr_read_b32 v10, a250\nv_accvgpr_read_b32 v11, a251\nv_accvgpr_read_b32 v12, a252\nv_accvgpr_read_b32 v13, a253\nv_accvgpr_read_b32 v14, a254\n"
"v_accvgpr_read_b32 v15, a255\nv_pk_mul_f32 v[8:9], v[8:9], s[72:73] op_sel_hi:[1,0]\nv_pk_mul_f32 v[10:11], v[10:11], s[72:73] op_sel_hi:[1,0]\nv_pk_mul_f32 v[12:13], v[12:13], s[72:73] op_sel_hi:[1,0]\nv_pk_mul_f32 v[14:15], v[14:15], s[72:73] op_sel_hi:[1,0]\nQk 8,8,9\nQk 9,10,11\nQk 10,12,13\n"
"Qk 11,14,15\nv_accvgpr_write_b32 a108, v8\nv_accvgpr_write_b32 a109, v9\nv_accvgpr_write_b32 a110, v10\nv_accvgpr_write_b32 a111, v11\nv_mov_b32 v66, 0x3f803f80\nv_accvgpr_write_b32 a204, v66\nv_accvgpr_write_b32 a205, v66\n"
"v_accvgpr_write_b32 a206, v66\nv_accvgpr_write_b32 a207, v66\ns_add_u32 s87, s87, 2048\ns_waitcnt lgkmcnt(15)\nQzvaa 0,112,0\nQmvaa 0,116,4\nQmvaa 0,120,8\nQmvaa 0,124,12\n"
"Qzvaa 4,128,0\nQmvaa 4,132,4\nQmvaa 4,136,8\nQmvaa 4,140,12\ns_nop 3\nv_max3_f32 v66, v0, v1, v2\ns_nop 2\nv_max3_f32 v66, v66, v3, v4\n"
"v_max3_f32 v66, v66, v5, v6\nv_max_f32 v66, v66, v7\nQzvaa 8,112,16\nv_mov_b32 v67, v66\nQmvaa 8,116,20\ns_nop 0\nv_permlane32_swap_b32 v66, v67\nv_max_f32 v66, v66, v67\n"
"Qmvaa 8,120,24\nv_mov_b32 v67, v66\nQmvaa 8,124,28\ns_nop 0\nv_permlane16_swap_b32 v66, v67\nv_max_f32 v66, v66, v67\nQzvaa 12,128,16\nv_sub_f32 v24, 0xc1800000, v66\n"
"Qa 0,0,24\nQmvaa 12,132,20\nQa 1,1,24\nQa 2,2,24\nQmvaa 12,136,24\nQa 3,3,24\nQa 4,4,24\nQmvaa 12,140,28\n"
"Qe 0\nQe 1\nQe 2\nQe 3\nQk 16,0,1\nQk 17,2,3\nQa 5,5,24\nQa 6,6,24\n"
"Qa 7,7,24\nv_mov_b32 v25, v24\nQe 4\nQe 5\nQe 6\nQzvaa 0,112,32\nQe 7\nQmvaa 0,116,36\n"
"Qk 18,4,5\nQk 19,6,7\nQmvaa 0,120,40\nv_max3_f32 v68, v8, v9, v10\nv_max3_f32 v68, v68, v11, v12\nQmvaa 0,124,44\nv_max3_f32 v68, v68, v13, v14\nv_max_f32 v68, v68, v15\n"
"Qzvaa 4,128,32\nv_mov_b32 v69, v68\nv_mov_b32 v26, v24\nQmvaa 4,132,36\nv_permlane32_swap_b32 v68, v69\nv_max_f32 v68, v68, v69\nQmvaa 4,136,40\nv_mov_b32 v69, v68\n"
"v_mov_b32 v27, v24\nQmvaa 4,140,44\ns_waitcnt lgkmcnt(0)\nv_permlane16_swap_b32 v68, v69\nQzvav 80,144,16\nv_max_f32 v68, v68, v69\nv_sub_f32 v28, 0xc1800000, v68\nQzvav 84,148,16\n"
"Qa 8,8,28\nQa 9,9,28\nQzvav 88,152,16\nQa 10,10,28\nQa 11,11,28\nQzvav 92,156,16\nQe 8\nQzvav 96,160,16\n"
"Qe 9\nQzvav 100,164,16\nQe 10\nQzvav 104,168,16\nQe 11\nQzvav 108,172,16\nQk 20,8,9\nQk 21,10,11\n"
"Qzaav 176,204,16\nQa 12,12,28\nQa 13,13,28\nQzvaa 8,112,48\nQa 14,14,28\nQa 15,15,28\nQmvaa 8,116,52\nQe 12\n"
"Qmvaa 8,120,56\nQe 13\nQe 14\nQe 15\nQmvaa 8,124,60\nQk 22,12,13\nQk 23,14,15\nQzvaa 12,128,48\n"
"v_max3_f32 v66, v0, v1, v2\nv_max3_f32 v66, v66, v3, v4\nQmvaa 12,132,52\nv_max3_f32 v66, v66, v5, v6\nv_max_f32 v66, v66, v7\nQmvaa 12,136,56\nv_mov_b32 v67, v66\nv_mov_b32 v29, v28\n"
"Qmvaa 12,140,60\nv_permlane32_swap_b32 v66, v67\nv_max_f32 v66, v66, v67\nQzvav 112,144,20\nv_mov_b32 v67, v66\nv_mov_b32 v30, v28\nQzvav 116,148,20\nv_permlane16_swap_b32 v66, v67\n"
"v_max_f32 v66, v66, v67\nQzvav 120,152,20\nv_sub_f32 v32, 0xc1800000, v66\nQa 0,0,32\nQzvav 124,156,20\nQa 1,1,32\nQa 2,2,32\nQzvav 128,160,20\n"
"Qa 3,3,32\nQa 4,4,32\nQzvav 132,164,20\nQe 0\nQzvav 136,168,20\nQe 1\nQzvav 140,172,20\nQe 2\n"
"Qe 3\nQzaav 180,204,20\nQk 16,0,1\nQk 17,2,3\nQzvaa 0,112,64\nQa 5,5,32\nQa 6,6,32\nQmvaa 0,116,68\n"
"Qa 7,7,32\nv_max3_f32 v68, v8, v9, v10\nQmvaa 0,120,72\nQe 4\nQe 5\nQe 6\nQe 7\nQmvaa 0,124,76\n"
"Qk 18,4,5\nQk 19,6,7\nQzvaa 4,128,64\nv_max3_f32 v68, v68, v11, v12\nv_max3_f32 v68, v68, v13, v14\nQmvaa 4,132,68\nv_max_f32 v68, v68, v15\nv_mov_b32 v69, v68\n"
"Qmvaa 4,136,72\ns_nop 0\nv_permlane32_swap_b32 v68, v69\nv_max_f32 v68, v68, v69\nQmvaa 4,140,76\nv_mov_b32 v69, v68\nv_mov_b32 v31, v28\nQzvav 144,144,16\n"
"v_permlane16_swap_b32 v68, v69\nv_max_f32 v68, v68, v69\nQzvav 148,148,16\nv_sub_f32 v36, 0xc1800000, v68\nQa 8,8,36\nQzvav 152,152,16\nQa 9,9,36\nQa 10,10,36\n"
"Qzvav 156,156,16\nQa 11,11,36\nQa 12,12,36\nQzvav 160,160,16\nQe 8\nQzvav 164,164,16\nQe 9\nQzvav 168,168,16\n"
"Qe 10\nQzvav 172,172,16\nQe 11\nQzaav 184,204,16\nQk 20,8,9\nQk 21,10,11\nQzvaa 8,112,80\nQa 13,13,36\n"
"Qa 14,14,36\nQmvaa 8,116,84\nQa 15,15,36\nv_max3_f32 v66, v0, v1, v2\nQmvaa 8,120,88\nQe 12\nQe 13\nQe 14\n"
"Qe 15\nQmvaa 8,124,92\nQk 22,12,13\nQk 23,14,15\nQzvaa 12,128,80\nv_max3_f32 v66, v66, v3, v4\nv_max3_f32 v66, v66, v5, v6\nQmvaa 12,132,84\n"
"v_max_f32 v66, v66, v7\nv_mov_b32 v67, v66\nQmvaa 12,136,88\ns_nop 0\nv_permlane32_swap_b32 v66, v67\nv_max_f32 v66, v66, v67\nQmvaa 12,140,92\nv_mov_b32 v67, v66\n"
"v_mov_b32 v33, v32\nQzvav 176,144,20\nv_permlane16_swap_b32 v66, v67\nv_max_f32 v66, v66, v67\nQzvav 180,148,20\ns_waitcnt vmcnt(16)\ns_barrier\nQzvav 184,152,20\n"
"v_sub_f32 v40, 0xc1800000, v66\nQa 0,0,40\nQzvav 188,156,20\nQa 1,1,40\nQa 2,2,40\nQzvav 192,160,20\nQa 3,3,40\nQa 4,4,40\n"
"Qzvav 196,164,20\nQe 0\nQzvav 200,168,20\nQe 1\nQzvav 204,172,20\nQe 2\nQe 3\nQzaav 188,204,20\n"
"Qk 16,0,1\nQk 17,2,3\nQzvaa 0,112,96\nQda 112,52,33024\nQa 5,5,40\nQmvaa 0,116,100\nQda 116,52,33536\nQa 6,6,40\n"
"Qmvaa 0,120,104\nQda 120,52,34048\nQa 7,7,40\nQe 4\nQe 5\nQe 6\nQe 7\nQmvaa 0,124,108\n"
"Qda 124,52,34560\nQk 18,4,5\nQk 19,6,7\nQzvaa 4,128,96\nQda 128,52,35072\nv_max3_f32 v68, v8, v9, v10\nQmvaa 4,132,100\nQda 132,52,35584\n"
"v_max3_f32 v68, v68, v11, v12\nQmvaa 4,136,104\nQda 136,52,36096\nv_max3_f32 v68, v68, v13, v14\nQmvaa 4,140,108\nQda 140,52,36608\nv_max_f32 v68, v68, v15\nQzvav 208,144,16\n"
"s_memrealtime s[74:75]\nv_mov_b32 v69, v68\nQzvav 212,148,16\ns_add_u32 m0, s61, 0\nv_permlane32_swap_b32 v68, v69\nQzvav 216,152,16\nbuffer_load_dwordx4 v56, s[40:43], s89 offen nt lds\nQzvav 220,156,16\n"
"v_max_f32 v68, v68, v69\nv_mov_b32 v69, v68\nQzvav 224,160,16\ns_nop 0\nv_permlane16_swap_b32 v68, v69\nv_max_f32 v68, v68, v69\nQzvav 228,164,16\nv_sub_f32 v44, 0xc1800000, v68\n"
"Qa 8,8,44\nQzvav 232,168,16\nbuffer_load_dwordx4 v57, s[40:43], s89 offen offset:1024 nt lds\nQzvav 236,172,16\nQa 9,9,44\nQa 10,10,44\nQa 11,11,44\nQe 8\n"
"Qe 9\nQe 10\nQe 11\nQzaav 192,204,16\ns_waitcnt lgkmcnt(0)\nQk 20,8,9\nQk 21,10,11\nQcvaa 8,112,0,24\n"
"Qa 12,12,44\nQa 13,13,44\nQmvaa 8,116,4\nbuffer_load_dwordx4 v56, s[40:43], s89 offen offset:2048 nt lds\nQmvaa 8,120,8\nQa 14,14,44\nQa 15,15,44\nQe 12\n"
"Qe 13\nQe 14\nQe 15\nQmvaa 8,124,12\nQk 22,12,13\nQk 23,14,15\nQcvaa 12,128,0,24\nv_max3_f32 v66, v0, v1, v2\n"
"v_max3_f32 v66, v66, v3, v4\nQmvaa 12,132,4\nbuffer_load_dwordx4 v57, s[40:43], s89 offen offset:3072 nt lds\nQmvaa 12,136,8\nv_max3_f32 v66, v66, v5, v6\nv_max_f32 v66, v66, v7\nQmvaa 12,140,12\nv_mov_b32 v67, v66\n"
"v_mov_b32 v34, v32\nQzvav 240,144,20\ns_add_u32 m0, s61, 8192\nv_permlane32_swap_b32 v66, v67\nQzvav 244,148,20\nbuffer_load_dwordx4 v58, s[44:47], s89 offen nt lds\ns_waitcnt lgkmcnt(0)\nQzvav 248,152,20\n"
"v_max_f32 v66, v66, v67\nv_mov_b32 v67, v66\nQzvav 252,156,20\ns_add_u32 s77, s74, 124\nv_permlane16_swap_b32 v66, v67\nQzaav 208,160,20\nQe 8\nQzaav 212,164,20\n"
"buffer_load_dwordx4 v59, s[44:47], s89 offen offset:1024 nt lds\nQe 9\nQzaav 216,168,20\nQe 10\nQzaav 220,172,20\nQe 11\nv_max_f32 v66, v66, v67\nv_sub_f32 v48, 0xc1800000, v66\n"
"Qa 0,0,48\nQa 1,1,48\nQa 2,2,48\nQa 3,3,48\nQe 0\nQe 1\nQe 2\nQe 3\n"
"Qzaav 196,204,20\nQk 20,8,9\nQk 21,10,11\nQk 16,0,1\nQk 17,2,3\nQcvaa 0,112,16,28\nbuffer_load_dwordx4 v58, s[44:47], s89 offen offset:2048 nt lds\nQe 12\n"
"Qmvaa 0,116,20\nQe 13\nQmvaa 0,120,24\nQe 14\nQa 4,4,48\nQa 5,5,48\nQa 6,6,48\nQa 7,7,48\n"
"Qe 4\nQe 5\nQe 6\nQe 7\nQmvaa 0,124,28\nQe 15\nQk 18,4,5\nQk 19,6,7\n"
"Qcvaa 4,128,16,28\nbuffer_load_dwordx4 v59, s[44:47], s89 offen offset:3072 nt lds\nQk 22,12,13\nQk 23,14,15\nQmvaa 4,132,20\nv_mov_b32 v35, v32\nv_mov_b32 v37, v36\nQmvaa 4,136,24\n"
"s_add_u32 s60, s60, 8192\ns_xor_b32 s89, s60, s101\nv_mov_b32 v38, v36\nQmvaa 4,140,28\nv_mov_b32 v39, v36\nv_mov_b32 v41, v40\nQzaav 224,144,16\nQta 144,54,33024\n"
"Qta 146,54,35072\nv_mov_b32 v42, v40\nQzaav 228,148,16\nQta 148,54,33280\nQta 150,54,35328\nv_mov_b32 v43, v40\nv_mov_b32 v45, v44\nQzaav 232,152,16\n"
"Qta 152,54,33536\nQta 154,54,35584\nv_mov_b32 v46, v44\nQzaav 236,156,16\nQta 156,54,33792\nQta 158,54,35840\nv_mov_b32 v47, v44\nv_mov_b32 v49, v48\n"
"Qzaav 240,160,16\nQta 160,54,34048\nQta 162,54,36096\nv_mov_b32 v50, v48\nQzaav 244,164,16\nQta 164,54,34304\nQta 166,54,36352\nv_mov_b32 v51, v48\n"
"Qzaav 248,168,16\nQta 168,54,34560\nQta 170,54,36608\nQzaav 252,172,16\nQta 172,54,34816\nQta 174,54,36864\nQzaav 200,204,16\nQe 0\n"
"s_lshl_b32 s66, s67, 3\n.Lloop:\ns_bitcmp1_b32 s99, s66\ns_cbranch_scc1 .Lhook\n.Lhookret:\nQcvaa 8,112,32,32\nv_add_u32 v75, 1, v75\nQmvaa 8,116,36\n"
"s_waitcnt vmcnt(8)\nds_write_b32 v73, v75\nQmvaa 8,120,40\nQe 1\nQmvaa 8,124,44\nQe 2\nQcvaa 12,128,32,32\nQe 3\n"
"Qmvaa 12,132,36\nQk 16,0,1\nQk 17,2,3\nQmvaa 12,136,40\nQe 4\nQmvaa 12,140,44\ns_waitcnt lgkmcnt(1)\nQmvav 80,144,20\n"
"Qe 5\nQmvav 84,148,20\nQe 6\nQmvav 88,152,20\nQe 7\nQmvav 92,156,20\nQk 18,4,5\nQk 19,6,7\n"
"Qmvav 96,160,20\nQe 8\nQmvav 100,164,20\nQe 9\nQmvav 104,168,20\nQe 10\nQmvav 108,172,20\nQe 11\n"
"Qmaav 176,204,20\nQk 20,8,9\nQk 21,10,11\nQcvaa 0,112,48,36\nQe 12\nQmvaa 0,116,52\nds_read_b32 v69, v74\nQmvaa 0,120,56\n"
"Qe 13\nQmvaa 0,124,60\nQe 14\nQcvaa 4,128,48,36\nQe 15\nQmvaa 4,132,52\nQk 22,12,13\nQk 23,14,15\n"
"Qmvaa 4,136,56\ns_waitcnt lgkmcnt(0)\nv_cmp_gt_u32 vcc, v75, v69\nQmvaa 4,140,60\ns_cbranch_vccnz .Lsa1\n.Lra1:\nQmvav 112,144,16\ns_add_u32 m0, s61, 33024\n"
"Qmvav 116,148,16\nbuffer_load_dwordx4 v56, s[40:43], s89 offen nt lds\nQmvav 120,152,16\nQmvav 124,156,16\nQe 0\nQmvav 128,160,16\nQe 1\nQmvav 132,164,16\n"
"Qe 2\nQmvav 136,168,16\nbuffer_load_dwordx4 v57, s[40:43], s89 offen offset:1024 nt lds\nQmvav 140,172,16\nQe 3\nQmaav 180,204,16\nQk 16,0,1\nQk 17,2,3\n"
"Qcvaa 8,112,64,40\nQe 4\nQmvaa 8,116,68\nQe 5\nQmvaa 8,120,72\nbuffer_load_dwordx4 v56, s[40:43], s89 offen offset:2048 nt lds\nQmvaa 8,124,76\nQe 6\n"
"Qcvaa 12,128,64,40\nQe 7\nQmvaa 12,132,68\nQk 18,4,5\nQk 19,6,7\nQmvaa 12,136,72\nQmvaa 12,140,76\nbuffer_load_dwordx4 v57, s[40:43], s89 offen offset:3072 nt lds\n"
"Qmvav 144,144,20\nQmvav 148,148,20\nQmvav 152,152,20\nQmvav 156,156,20\ns_add_u32 m0, s61, 41216\nQmvav 160,160,20\nbuffer_load_dwordx4 v58, s[44:47], s89 offen nt lds\nQmvav 164,164,20\n"
"Qe 8\nQmvav 168,168,20\nQe 9\nQmvav 172,172,20\nQe 10\nQmaav 184,204,20\nQe 11\nQcvaa 0,112,80,44\n"
"buffer_load_dwordx4 v59, s[44:47], s89 offen offset:1024 nt lds\nQmvaa 0,116,84\nQk 20,8,9\nQk 21,10,11\nQmvaa 0,120,88\nQe 12\nQmvaa 0,124,92\nQe 13\n"
"Qcvaa 4,128,80,44\nQe 14\nQmvaa 4,132,84\nbuffer_load_dwordx4 v58, s[44:47], s89 offen offset:2048 nt lds\nQmvaa 4,136,88\nQe 15\nQmvaa 4,140,92\nQk 22,12,13\n"
"Qk 23,14,15\nQmvav 176,144,16\nQmvav 180,148,16\nQmvav 184,152,16\nbuffer_load_dwordx4 v59, s[44:47], s89 offen offset:3072 nt lds\nQmvav 188,156,16\nQe 0\nQmvav 192,160,16\n"
"Qe 1\nQmvav 196,164,16\ns_add_u32 s60, s60, 8192\ns_xor_b32 s89, s60, s101\nQmvav 200,168,16\nQe 2\nQmvav 204,172,16\nQe 3\n"
"Qmaav 188,204,16\nQk 16,0,1\nQk 17,2,3\nQcvaa 8,112,96,48\nQda 112,53,0\nQmvaa 8,116,100\nQda 116,53,512\nQmvaa 8,120,104\n"
"Qda 120,53,1024\nQmvaa 8,124,108\nQda 124,53,1536\nQcvaa 12,128,96,48\nQda 128,53,2048\nQmvaa 12,132,100\nQda 132,53,2560\nQmvaa 12,136,104\n"
"Qda 136,53,3072\nQmvaa 12,140,108\nQda 140,53,3584\nQmvav 208,144,20\ns_memrealtime s[74:75]\nQmvav 212,148,20\nQe 4\nQmvav 216,152,20\n"
"Qe 5\nQmvav 220,156,20\nQe 6\nQmvav 224,160,20\nQe 7\nQmvav 228,164,20\nQk 18,4,5\nQk 19,6,7\n"
"Qmvav 232,168,20\nQe 8\nQmvav 236,172,20\nQe 9\nQmaav 192,204,20\ns_waitcnt lgkmcnt(0)\nQk 20,8,9\nQcvaa 0,112,0,24\n"
"Qe 10\nQmvaa 0,116,4\nQe 11\nQmvaa 0,120,8\nQk 21,10,11\nQmvaa 0,124,12\nQe 12\nQcvaa 4,128,0,24\n"
"Qe 13\nQmvaa 4,132,4\nQe 14\nQmvaa 4,136,8\nQe 15\nQmvaa 4,140,12\nQk 22,12,13\nQk 23,14,15\n"
"Qmvav 240,144,16\nQmvav 244,148,16\ns_waitcnt lgkmcnt(0)\ns_sub_u32 s75, s77, s74\nQmvav 248,152,16\ns_cmp_lt_u32 s75, 125\ns_cbranch_scc1 .Lsp1\n.Lrp1:\n"
"Qmvav 252,156,16\ns_add_u32 s77, s74, 124\nQmaav 208,160,16\nQe 0\nQmaav 212,164,16\nQe 1\nQmaav 216,168,16\nQe 2\n"
"Qmaav 220,172,16\nQe 3\nQmaav 196,204,16\nQk 16,0,1\nQk 17,2,3\nQcvaa 8,112,16,28\nQe 4\nQmvaa 8,116,20\n"
"Qe 5\nQmvaa 8,120,24\nQe 6\nQmvaa 8,124,28\nQe 7\nQcvaa 12,128,16,28\nQk 18,4,5\nQk 19,6,7\n"
"Qmvaa 12,132,20\nQmvaa 12,136,24\nQmvaa 12,140,28\nQmaav 224,144,20\nQta 144,55,0\nQta 146,55,2048\nQmaav 228,148,20\nQta 148,55,256\n"
"Qta 150,55,2304\nQmaav 232,152,20\nQta 152,55,512\nQta 154,55,2560\nQmaav 236,156,20\nQta 156,55,768\nQta 158,55,2816\nQmaav 240,160,20\n"
"Qta 160,55,1024\nQta 162,55,3072\nQmaav 244,164,20\nQta 164,55,1280\nQta 166,55,3328\nQmaav 248,168,20\nQta 168,55,1536\nQta 170,55,3584\n"
"Qmaav 252,172,20\nQta 172,55,1792\nQta 174,55,3840\nQmaav 200,204,20\nQe 8\nQcvaa 0,112,32,32\nv_add_u32 v75, 1, v75\nQmvaa 0,116,36\n"
"s_waitcnt vmcnt(16)\nds_write_b32 v73, v75\nQmvaa 0,120,40\nQe 9\nQmvaa 0,124,44\nQe 10\nQcvaa 4,128,32,32\nQe 11\n"
"Qmvaa 4,132,36\nQk 20,8,9\nQk 21,10,11\nQmvaa 4,136,40\nQe 12\nQmvaa 4,140,44\ns_waitcnt lgkmcnt(1)\nQmvav 80,144,16\n"
"Qe 13\nQmvav 84,148,16\nQe 14\nQmvav 88,152,16\nQe 15\nQmvav 92,156,16\nQk 22,12,13\nQk 23,14,15\n"
"Qmvav 96,160,16\nQe 0\nQmvav 100,164,16\nQe 1\nQmvav 104,168,16\nQe 2\nQmvav 108,172,16\nQe 3\n"
"Qmaav 176,204,16\nQk 16,0,1\nQk 17,2,3\nQcvaa 8,112,48,36\nQe 4\nQmvaa 8,116,52\nds_read_b32 v69, v74\nQmvaa 8,120,56\n"
"Qe 5\nQmvaa 8,124,60\nQe 6\nQcvaa 12,128,48,36\nQe 7\nQmvaa 12,132,52\nQk 18,4,5\nQk 19,6,7\n"
"Qmvaa 12,136,56\ns_waitcnt lgkmcnt(0)\nv_cmp_gt_u32 vcc, v75, v69\nQmvaa 12,140,60\ns_cbranch_vccnz .Lsa2\n.Lra2:\nQmvav 112,144,20\ns_add_u32 m0, s61, 66048\n"
"Qmvav 116,148,20\nbuffer_load_dwordx4 v56, s[40:43], s89 offen nt lds\nQmvav 120,152,20\nQmvav 124,156,20\nQe 8\nQmvav 128,160,20\nQe 9\nQmvav 132,164,20\n"
"Qe 10\nQmvav 136,168,20\nbuffer_load_dwordx4 v57, s[40:43], s89 offen offset:1024 nt lds\nQmvav 140,172,20\nQe 11\nQmaav 180,204,20\nQk 20,8,9\nQk 21,10,11\n"
"Qcvaa 0,112,64,40\nQe 12\nQmvaa 0,116,68\nQe 13\nQmvaa 0,120,72\nbuffer_load_dwordx4 v56, s[40:43], s89 offen offset:2048 nt lds\nQmvaa 0,124,76\nQe 14\n"
"Qcvaa 4,128,64,40\nQe 15\nQmvaa 4,132,68\nQk 22,12,13\nQk 23,14,15\nQmvaa 4,136,72\nQmvaa 4,140,76\nbuffer_load_dwordx4 v57, s[40:43], s89 offen offset:3072 nt lds\n"
"Qmvav 144,144,16\nQmvav 148,148,16\nQmvav 152,152,16\nQmvav 156,156,16\ns_add_u32 m0, s61, 74240\nQmvav 160,160,16\nbuffer_load_dwordx4 v58, s[44:47], s89 offen nt lds\nQmvav 164,164,16\n"
"Qe 0\nQmvav 168,168,16\nQe 1\nQmvav 172,172,16\nQe 2\nQmaav 184,204,16\nQe 3\nQcvaa 8,112,80,44\n"
"buffer_load_dwordx4 v59, s[44:47], s89 offen offset:1024 nt lds\nQmvaa 8,116,84\nQk 16,0,1\nQk 17,2,3\nQmvaa 8,120,88\nQe 4\nQmvaa 8,124,92\nQe 5\n"
"Qcvaa 12,128,80,44\nQe 6\nQmvaa 12,132,84\nbuffer_load_dwordx4 v58, s[44:47], s89 offen offset:2048 nt lds\nQmvaa 12,136,88\nQe 7\nQmvaa 12,140,92\nQk 18,4,5\n"
"Qk 19,6,7\nQmvav 176,144,20\nQmvav 180,148,20\nQmvav 184,152,20\nbuffer_load_dwordx4 v59, s[44:47], s89 offen offset:3072 nt lds\nQmvav 188,156,20\nQe 8\nQmvav 192,160,20\n"
"Qe 9\nQmvav 196,164,20\ns_add_u32 s60, s60, 8192\ns_xor_b32 s89, s60, s101\nQmvav 200,168,20\nQe 10\nQmvav 204,172,20\nQe 11\n"
"Qmaav 188,204,20\nQk 20,8,9\nQk 21,10,11\nQcvaa 0,112,96,48\nQda 112,53,33024\nQmvaa 0,116,100\nQda 116,53,33536\nQmvaa 0,120,104\n"
"Qda 120,53,34048\nQmvaa 0,124,108\nQda 124,53,34560\nQcvaa 4,128,96,48\nQda 128,53,35072\nQmvaa 4,132,100\nQda 132,53,35584\nQmvaa 4,136,104\n"
"Qda 136,53,36096\nQmvaa 4,140,108\nQda 140,53,36608\nQmvav 208,144,16\ns_memrealtime s[74:75]\nQmvav 212,148,16\nQe 12\nQmvav 216,152,16\n"
"Qe 13\nQmvav 220,156,16\nQe 14\nQmvav 224,160,16\nQe 15\nQmvav 228,164,16\nQk 22,12,13\nQk 23,14,15\n"
"Qmvav 232,168,16\nQe 0\nQmvav 236,172,16\nQe 1\nQmaav 192,204,16\ns_waitcnt lgkmcnt(0)\nQk 16,0,1\nQcvaa 8,112,0,24\n"
"Qe 2\nQmvaa 8,116,4\nQe 3\nQmvaa 8,120,8\nQk 17,2,3\nQmvaa 8,124,12\nQe 4\nQcvaa 12,128,0,24\n"
"Qe 5\nQmvaa 12,132,4\nQe 6\nQmvaa 12,136,8\nQe 7\nQmvaa 12,140,12\nQk 18,4,5\nQk 19,6,7\n"
"Qmvav 240,144,20\nQmvav 244,148,20\ns_waitcnt lgkmcnt(0)\ns_sub_u32 s75, s77, s74\nQmvav 248,152,20\ns_cmp_lt_u32 s75, 125\ns_cbranch_scc1 .Lsp2\n.Lrp2:\n"
"Qmvav 252,156,20\ns_add_u32 s77, s74, 124\nQmaav 208,160,20\nQe 8\nQmaav 212,164,20\nQe 9\nQmaav 216,168,20\nQe 10\n"
"Qmaav 220,172,20\nQe 11\nQmaav 196,204,20\nQk 20,8,9\nQk 21,10,11\nQcvaa 0,112,16,28\nQe 12\nQmvaa 0,116,20\n"
"Qe 13\nQmvaa 0,120,24\nQe 14\nQmvaa 0,124,28\nQe 15\nQcvaa 4,128,16,28\nQk 22,12,13\nQk 23,14,15\n"
"Qmvaa 4,132,20\nQmvaa 4,136,24\nQmvaa 4,140,28\nQmaav 224,144,16\nQta 144,55,33024\nQta 146,55,35072\nQmaav 228,148,16\nQta 148,55,33280\n"
"Qta 150,55,35328\nQmaav 232,152,16\nQta 152,55,33536\nQta 154,55,35584\nQmaav 236,156,16\nQta 156,55,33792\nQta 158,55,35840\nQmaav 240,160,16\n"
"Qta 160,55,34048\nQta 162,55,36096\nQmaav 244,164,16\nQta 164,55,34304\nQta 166,55,36352\nQmaav 248,168,16\nQta 168,55,34560\nQta 170,55,36608\n"
"Qmaav 252,172,16\nQta 172,55,34816\nQta 174,55,36864\nQmaav 200,204,16\nQe 0\nQcvaa 8,112,32,32\nv_add_u32 v75, 1, v75\nQmvaa 8,116,36\n"
"s_waitcnt vmcnt(16)\nds_write_b32 v73, v75\nQmvaa 8,120,40\nQe 1\nQmvaa 8,124,44\nQe 2\nQcvaa 12,128,32,32\nQe 3\n"
"Qmvaa 12,132,36\nQk 16,0,1\nQk 17,2,3\nQmvaa 12,136,40\nQe 4\nQmvaa 12,140,44\ns_waitcnt lgkmcnt(1)\nQmvav 80,144,20\n"
"Qe 5\nQmvav 84,148,20\nQe 6\nQmvav 88,152,20\nQe 7\nQmvav 92,156,20\nQk 18,4,5\nQk 19,6,7\n"
"Qmvav 96,160,20\nQe 8\nQmvav 100,164,20\nQe 9\nQmvav 104,168,20\nQe 10\nQmvav 108,172,20\nQe 11\n"
"Qmaav 176,204,20\nQk 20,8,9\nQk 21,10,11\nQcvaa 0,112,48,36\nQe 12\nQmvaa 0,116,52\nds_read_b32 v69, v74\nQmvaa 0,120,56\n"
"Qe 13\nQmvaa 0,124,60\nQe 14\nQcvaa 4,128,48,36\nQe 15\nQmvaa 4,132,52\nQk 22,12,13\nQk 23,14,15\n"
"Qmvaa 4,136,56\ns_waitcnt lgkmcnt(0)\nv_cmp_gt_u32 vcc, v75, v69\nQmvaa 4,140,60\ns_cbranch_vccnz .Lsa3\n.Lra3:\nQmvav 112,144,16\ns_add_u32 m0, s61, 99072\n"
"Qmvav 116,148,16\nbuffer_load_dwordx4 v56, s[40:43], s89 offen nt lds\nQmvav 120,152,16\nQmvav 124,156,16\nQe 0\nQmvav 128,160,16\nQe 1\nQmvav 132,164,16\n"
"Qe 2\nQmvav 136,168,16\nbuffer_load_dwordx4 v57, s[40:43], s89 offen offset:1024 nt lds\nQmvav 140,172,16\nQe 3\nQmaav 180,204,16\nQk 16,0,1\nQk 17,2,3\n"
"Qcvaa 8,112,64,40\nQe 4\nQmvaa 8,116,68\nQe 5\nQmvaa 8,120,72\nbuffer_load_dwordx4 v56, s[40:43], s89 offen offset:2048 nt lds\nQmvaa 8,124,76\nQe 6\n"
"Qcvaa 12,128,64,40\nQe 7\nQmvaa 12,132,68\nQk 18,4,5\nQk 19,6,7\nQmvaa 12,136,72\nQmvaa 12,140,76\nbuffer_load_dwordx4 v57, s[40:43], s89 offen offset:3072 nt lds\n"
"Qmvav 144,144,20\nQmvav 148,148,20\nQmvav 152,152,20\nQmvav 156,156,20\ns_add_u32 m0, s61, 107264\nQmvav 160,160,20\nbuffer_load_dwordx4 v58, s[44:47], s89 offen nt lds\nQmvav 164,164,20\n"
"Qe 8\nQmvav 168,168,20\nQe 9\nQmvav 172,172,20\nQe 10\nQmaav 184,204,20\nQe 11\nQcvaa 0,112,80,44\n"
"buffer_load_dwordx4 v59, s[44:47], s89 offen offset:1024 nt lds\nQmvaa 0,116,84\nQk 20,8,9\nQk 21,10,11\nQmvaa 0,120,88\nQe 12\nQmvaa 0,124,92\nQe 13\n"
"Qcvaa 4,128,80,44\nQe 14\nQmvaa 4,132,84\nbuffer_load_dwordx4 v58, s[44:47], s89 offen offset:2048 nt lds\nQmvaa 4,136,88\nQe 15\nQmvaa 4,140,92\nQk 22,12,13\n"
"Qk 23,14,15\nQmvav 176,144,16\nQmvav 180,148,16\nQmvav 184,152,16\nbuffer_load_dwordx4 v59, s[44:47], s89 offen offset:3072 nt lds\nQmvav 188,156,16\nQe 0\nQmvav 192,160,16\n"
"Qe 1\nQmvav 196,164,16\ns_add_u32 s60, s60, 8192\ns_and_b32 s68, s60, 0x40000\ns_mul_i32 s68, s68, 7\ns_add_u32 s60, s60, s68\ns_xor_b32 s89, s60, s101\nQmvav 200,168,16\n"
"Qe 2\nQmvav 204,172,16\nQe 3\nQmaav 188,204,16\nQk 16,0,1\nQk 17,2,3\nQcvaa 8,112,96,48\nQda 112,52,0\n"
"Qmvaa 8,116,100\nQda 116,52,512\nQmvaa 8,120,104\nQda 120,52,1024\nQmvaa 8,124,108\nQda 124,52,1536\nQcvaa 12,128,96,48\nQda 128,52,2048\n"
"Qmvaa 12,132,100\nQda 132,52,2560\nQmvaa 12,136,104\nQda 136,52,3072\nQmvaa 12,140,108\nQda 140,52,3584\nQmvav 208,144,20\ns_memrealtime s[74:75]\n"
"Qmvav 212,148,20\nQe 4\nQmvav 216,152,20\nQe 5\nQmvav 220,156,20\nQe 6\nQmvav 224,160,20\nQe 7\n"
"Qmvav 228,164,20\nQk 18,4,5\nQk 19,6,7\nQmvav 232,168,20\nQe 8\nQmvav 236,172,20\nQe 9\nQmaav 192,204,20\n"
"s_waitcnt lgkmcnt(0)\nQk 20,8,9\nQcvaa 0,112,0,24\nQe 10\nQmvaa 0,116,4\nQe 11\nQmvaa 0,120,8\nQk 21,10,11\n"
"Qmvaa 0,124,12\nQe 12\nQcvaa 4,128,0,24\nQe 13\nQmvaa 4,132,4\nQe 14\nQmvaa 4,136,8\nQe 15\n"
"Qmvaa 4,140,12\nQk 22,12,13\nQk 23,14,15\nQmvav 240,144,16\nQmvav 244,148,16\ns_waitcnt lgkmcnt(0)\ns_sub_u32 s75, s77, s74\nQmvav 248,152,16\n"
"s_cmp_lt_u32 s75, 125\ns_cbranch_scc1 .Lsp3\n.Lrp3:\nQmvav 252,156,16\ns_add_u32 s77, s74, 124\nQmaav 208,160,16\nQe 0\nQmaav 212,164,16\n"
"Qe 1\nQmaav 216,168,16\nQe 2\nQmaav 220,172,16\nQe 3\nQmaav 196,204,16\nQk 16,0,1\nQk 17,2,3\n"
"Qcvaa 8,112,16,28\nQe 4\nQmvaa 8,116,20\nQe 5\nQmvaa 8,120,24\nQe 6\nQmvaa 8,124,28\nQe 7\n"
"Qcvaa 12,128,16,28\nQk 18,4,5\nQk 19,6,7\nQmvaa 12,132,20\nQmvaa 12,136,24\nQmvaa 12,140,28\nQmaav 224,144,20\nQta 144,54,0\n"
"Qta 146,54,2048\nQmaav 228,148,20\nQta 148,54,256\nQta 150,54,2304\nQmaav 232,152,20\nQta 152,54,512\nQta 154,54,2560\nQmaav 236,156,20\n"
"Qta 156,54,768\nQta 158,54,2816\nQmaav 240,160,20\nQta 160,54,1024\nQta 162,54,3072\nQmaav 244,164,20\nQta 164,54,1280\nQta 166,54,3328\n"
"Qmaav 248,168,20\nQta 168,54,1536\nQta 170,54,3584\nQmaav 252,172,20\nQta 172,54,1792\nQta 174,54,3840\nQmaav 200,204,20\nQe 8\n"
"Qcvaa 0,112,32,32\nv_add_u32 v75, 1, v75\nQmvaa 0,116,36\ns_waitcnt vmcnt(16)\nds_write_b32 v73, v75\nQmvaa 0,120,40\nQe 9\nQmvaa 0,124,44\n"
"Qe 10\nQcvaa 4,128,32,32\nQe 11\nQmvaa 4,132,36\nQk 20,8,9\nQk 21,10,11\nQmvaa 4,136,40\nQe 12\n"
"Qmvaa 4,140,44\ns_waitcnt lgkmcnt(1)\nQmvav 80,144,16\nQe 13\nQmvav 84,148,16\nQe 14\nQmvav 88,152,16\nQe 15\n"
"Qmvav 92,156,16\nQk 22,12,13\nQk 23,14,15\nQmvav 96,160,16\nQe 0\nQmvav 100,164,16\nQe 1\nQmvav 104,168,16\n"
"Qe 2\nQmvav 108,172,16\nQe 3\nQmaav 176,204,16\nQk 16,0,1\nQk 17,2,3\nQcvaa 8,112,48,36\nQe 4\n"
"Qmvaa 8,116,52\nds_read_b32 v69, v74\nQmvaa 8,120,56\nQe 5\nQmvaa 8,124,60\nQe 6\nQcvaa 12,128,48,36\nQe 7\n"
"Qmvaa 12,132,52\nQk 18,4,5\nQk 19,6,7\nQmvaa 12,136,56\ns_waitcnt lgkmcnt(0)\nv_cmp_gt_u32 vcc, v75, v69\nQmvaa 12,140,60\ns_cbranch_vccnz .Lsa4\n"
".Lra4:\nQmvav 112,144,20\ns_add_u32 m0, s61, 0\nQmvav 116,148,20\nbuffer_load_dwordx4 v56, s[40:43], s89 offen nt lds\nQmvav 120,152,20\nQmvav 124,156,20\nQe 8\n"
"Qmvav 128,160,20\nQe 9\nQmvav 132,164,20\nQe 10\nQmvav 136,168,20\nbuffer_load_dwordx4 v57, s[40:43], s89 offen offset:1024 nt lds\nQmvav 140,172,20\nQe 11\n"
"Qmaav 180,204,20\nQk 20,8,9\nQk 21,10,11\nQcvaa 0,112,64,40\nQe 12\nQmvaa 0,116,68\nQe 13\nQmvaa 0,120,72\n"
"buffer_load_dwordx4 v56, s[40:43], s89 offen offset:2048 nt lds\nQmvaa 0,124,76\nQe 14\nQcvaa 4,128,64,40\nQe 15\nQmvaa 4,132,68\nQk 22,12,13\nQk 23,14,15\n"
"Qmvaa 4,136,72\nQmvaa 4,140,76\nbuffer_load_dwordx4 v57, s[40:43], s89 offen offset:3072 nt lds\nQmvav 144,144,16\nQmvav 148,148,16\nQmvav 152,152,16\nQmvav 156,156,16\ns_add_u32 m0, s61, 8192\n"
"Qmvav 160,160,16\nbuffer_load_dwordx4 v58, s[44:47], s89 offen nt lds\nQmvav 164,164,16\nQe 0\nQmvav 168,168,16\nQe 1\nQmvav 172,172,16\nQe 2\n"
"Qmaav 184,204,16\nQe 3\nQcvaa 8,112,80,44\nbuffer_load_dwordx4 v59, s[44:47], s89 offen offset:1024 nt lds\nQmvaa 8,116,84\nQk 16,0,1\nQk 17,2,3\nQmvaa 8,120,88\n"
"Qe 4\nQmvaa 8,124,92\nQe 5\nQcvaa 12,128,80,44\nQe 6\nQmvaa 12,132,84\nbuffer_load_dwordx4 v58, s[44:47], s89 offen offset:2048 nt lds\nQmvaa 12,136,88\n"
"Qe 7\nQmvaa 12,140,92\nQk 18,4,5\nQk 19,6,7\nQmvav 176,144,20\nQmvav 180,148,20\nQmvav 184,152,20\nbuffer_load_dwordx4 v59, s[44:47], s89 offen offset:3072 nt lds\n"
"Qmvav 188,156,20\nQe 8\nQmvav 192,160,20\nQe 9\nQmvav 196,164,20\ns_add_u32 s60, s60, 8192\ns_xor_b32 s89, s60, s101\nQmvav 200,168,20\n"
"Qe 10\nQmvav 204,172,20\nQe 11\nQmaav 188,204,20\nQk 20,8,9\nQk 21,10,11\nQcvaa 0,112,96,48\nQda 112,52,33024\n"
"Qmvaa 0,116,100\nQda 116,52,33536\nQmvaa 0,120,104\nQda 120,52,34048\nQmvaa 0,124,108\nQda 124,52,34560\nQcvaa 4,128,96,48\nQda 128,52,35072\n"
"Qmvaa 4,132,100\nQda 132,52,35584\nQmvaa 4,136,104\nQda 136,52,36096\nQmvaa 4,140,108\nQda 140,52,36608\nQmvav 208,144,16\ns_memrealtime s[74:75]\n"
"Qmvav 212,148,16\nQe 12\nQmvav 216,152,16\nQe 13\nQmvav 220,156,16\nQe 14\nQmvav 224,160,16\nQe 15\n"
"Qmvav 228,164,16\nQk 22,12,13\nQk 23,14,15\nQmvav 232,168,16\nQe 0\nQmvav 236,172,16\nQe 1\nQmaav 192,204,16\n"
"s_waitcnt lgkmcnt(0)\nQk 16,0,1\nQcvaa 8,112,0,24\nQe 2\nQmvaa 8,116,4\nQe 3\nQmvaa 8,120,8\nQk 17,2,3\n"
"Qmvaa 8,124,12\nQe 4\nQcvaa 12,128,0,24\nQe 5\nQmvaa 12,132,4\nQe 6\nQmvaa 12,136,8\nQe 7\n"
"Qmvaa 12,140,12\nQk 18,4,5\nQk 19,6,7\nQmvav 240,144,20\nQmvav 244,148,20\ns_waitcnt lgkmcnt(0)\ns_sub_u32 s75, s77, s74\nQmvav 248,152,20\n"
"s_cmp_lt_u32 s75, 125\ns_cbranch_scc1 .Lsp4\n.Lrp4:\nQmvav 252,156,20\ns_add_u32 s77, s74, 124\nQmaav 208,160,20\nQe 8\nQmaav 212,164,20\n"
"Qe 9\nQmaav 216,168,20\nQe 10\nQmaav 220,172,20\nQe 11\nQmaav 196,204,20\nQk 20,8,9\nQk 21,10,11\n"
"Qcvaa 0,112,16,28\nQe 12\nQmvaa 0,116,20\nQe 13\nQmvaa 0,120,24\nQe 14\nQmvaa 0,124,28\nQe 15\n"
"Qcvaa 4,128,16,28\nQk 22,12,13\nQk 23,14,15\nQmvaa 4,132,20\nQmvaa 4,136,24\nQmvaa 4,140,28\nQmaav 224,144,16\nQta 144,54,33024\n"
"Qta 146,54,35072\nQmaav 228,148,16\nQta 148,54,33280\nQta 150,54,35328\nQmaav 232,152,16\nQta 152,54,33536\nQta 154,54,35584\nQmaav 236,156,16\n"
"Qta 156,54,33792\nQta 158,54,35840\nQmaav 240,160,16\nQta 160,54,34048\nQta 162,54,36096\nQmaav 244,164,16\nQta 164,54,34304\nQta 166,54,36352\n"
"Qmaav 248,168,16\nQta 168,54,34560\nQta 170,54,36608\nQmaav 252,172,16\nQta 172,54,34816\nQta 174,54,36864\nQmaav 200,204,16\nQe 0\n"
"s_add_u32 s66, s66, 1\ns_bitcmp1_b32 0x7f7f, s66\ns_cbranch_scc1 .Lloop\ns_cmp_lg_u32 s67, 0\ns_cbranch_scc1 .Ltail1\nQcvaa 8,112,32,32\nv_add_u32 v75, 1, v75\nQmvaa 8,116,36\n"
"s_waitcnt vmcnt(16)\nds_write_b32 v73, v75\nQmvaa 8,120,40\nQe 1\nQmvaa 8,124,44\nQe 2\nQcvaa 12,128,32,32\nQe 3\n"
"Qmvaa 12,132,36\nQk 16,0,1\nQk 17,2,3\nQmvaa 12,136,40\nQe 4\nQmvaa 12,140,44\ns_waitcnt lgkmcnt(1)\nQmvav 80,144,20\n"
"Qe 5\nQmvav 84,148,20\nQe 6\nQmvav 88,152,20\nQe 7\nQmvav 92,156,20\nQk 18,4,5\nQk 19,6,7\n"
"Qmvav 96,160,20\nQe 8\nQmvav 100,164,20\nQe 9\nQmvav 104,168,20\nQe 10\nQmvav 108,172,20\nQe 11\n"
"Qmaav 176,204,20\nQk 20,8,9\nQk 21,10,11\nQcvaa 0,112,48,36\nQe 12\nQmvaa 0,116,52\nds_read_b32 v69, v74\nQmvaa 0,120,56\n"
"Qe 13\nQmvaa 0,124,60\nQe 14\nQcvaa 4,128,48,36\nQe 15\nQmvaa 4,132,52\nQk 22,12,13\nQk 23,14,15\n"
"Qmvaa 4,136,56\ns_waitcnt lgkmcnt(0)\nv_cmp_gt_u32 vcc, v75, v69\nQmvaa 4,140,60\ns_cbranch_vccnz .Lsa29\n.Lra29:\nQmvav 112,144,16\ns_add_u32 m0, s62, 0\n"
"s_add_u32 s69, s87, 0\nQmvav 116,148,16\nbuffer_load_dwordx4 v62, s[52:55], s69 offen lds\nQmvav 120,152,16\nQmvav 124,156,16\nQe 0\nQmvav 128,160,16\nQe 1\n"
"Qmvav 132,164,16\ns_add_u32 s69, s87, 31744\nQe 2\nQmvav 136,168,16\nbuffer_load_dwordx4 v62, s[52:55], s69 offen offset:1024 lds\nQmvav 140,172,16\nQe 3\nQmaav 180,204,16\n"
"Qk 16,0,1\nQk 17,2,3\nQcvaa 8,112,64,40\nQe 4\nQmvaa 8,116,68\ns_add_u32 s69, s87, 63480\nQe 5\nQmvaa 8,120,72\n"
"buffer_load_dwordx4 v62, s[52:55], s69 offen offset:2056 lds\nQmvaa 8,124,76\nQe 6\nQcvaa 12,128,64,40\nQe 7\nQmvaa 12,132,68\nQk 18,4,5\nQk 19,6,7\n"
"Qmvaa 12,136,72\ns_add_u32 s69, s87, 95224\nQmvaa 12,140,76\nbuffer_load_dwordx4 v62, s[52:55], s69 offen offset:3080 lds\nQmvav 144,144,20\nQmvav 148,148,20\nQmvav 152,152,20\nQmvav 156,156,20\n"
"s_add_u32 m0, s62, 16448\ns_add_u32 s69, s87, 262144\nQmvav 160,160,20\nbuffer_load_dwordx4 v62, s[52:55], s69 offen lds\nQmvav 164,164,20\nQe 8\nQmvav 168,168,20\nQe 9\n"
"Qmvav 172,172,20\nQe 10\nQmaav 184,204,20\ns_add_u32 s69, s87, 293888\nQe 11\nQcvaa 0,112,80,44\nbuffer_load_dwordx4 v62, s[52:55], s69 offen offset:1024 lds\nQmvaa 0,116,84\n"
"Qk 20,8,9\nQk 21,10,11\nQmvaa 0,120,88\nQe 12\nQmvaa 0,124,92\nQe 13\nQcvaa 4,128,80,44\ns_add_u32 s69, s87, 325624\n"
"Qe 14\nQmvaa 4,132,84\nbuffer_load_dwordx4 v62, s[52:55], s69 offen offset:2056 lds\nQmvaa 4,136,88\nQe 15\nQmvaa 4,140,92\nQk 22,12,13\nQk 23,14,15\n"
"Qmvav 176,144,16\nQmvav 180,148,16\ns_add_u32 s69, s87, 357368\nQmvav 184,152,16\nbuffer_load_dwordx4 v62, s[52:55], s69 offen offset:3080 lds\nQmvav 188,156,16\nQe 0\nQmvav 192,160,16\n"
"Qe 1\nQmvav 196,164,16\nQmvav 200,168,16\nQe 2\nQmvav 204,172,16\nQe 3\nQmaav 188,204,16\nQk 16,0,1\n"
"Qk 17,2,3\nQcvaa 8,112,96,48\nQda 112,53,0\nQmvaa 8,116,100\nQda 116,53,512\nQmvaa 8,120,104\nQda 120,53,1024\nQmvaa 8,124,108\n"
"Qda 124,53,1536\nQcvaa 12,128,96,48\nQda 128,53,2048\nQmvaa 12,132,100\nQda 132,53,2560\nQmvaa 12,136,104\nQda 136,53,3072\nQmvaa 12,140,108\n"
"Qda 140,53,3584\nQmvav 208,144,20\nQmvav 212,148,20\ns_add_u32 m0, s63, 33024\ns_add_u32 s88, s86, 0\nQe 4\nQmvav 216,152,20\nbuffer_load_dwordx4 v64, s[48:51], s88 offen lds\n"
"Qe 5\nQmvav 220,156,20\nQe 6\nQmvav 224,160,20\nQe 7\nQmvav 228,164,20\ns_add_u32 s88, s86, 19456\nQk 18,4,5\n"
"Qk 19,6,7\nQmvav 232,168,20\nbuffer_load_dwordx4 v64, s[48:51], s88 offen offset:1024 lds\nQe 8\nQmvav 236,172,20\nQe 9\nQmaav 192,204,20\ns_waitcnt lgkmcnt(0)\n"
"Qk 20,8,9\nQcvaa 0,112,0,24\ns_add_u32 s88, s86, 38912\nQe 10\nQmvaa 0,116,4\nbuffer_load_dwordx4 v64, s[48:51], s88 offen offset:2048 lds\nQe 11\nQmvaa 0,120,8\n"
"Qk 21,10,11\nQmvaa 0,124,12\nQe 12\nQcvaa 4,128,0,24\ns_add_u32 s88, s86, 58368\nQe 13\nQmvaa 4,132,4\nbuffer_load_dwordx4 v64, s[48:51], s88 offen offset:3072 lds\n"
"Qe 14\nQmvaa 4,136,8\nQe 15\nQmvaa 4,140,12\nQk 22,12,13\nQk 23,14,15\nQmvav 240,144,16\ns_add_u32 m0, s63, 37120\n"
"s_add_u32 s88, s86, 81920\nQmvav 244,148,16\nbuffer_load_dwordx4 v64, s[48:51], s88 offen lds\nQmvav 248,152,16\nQmvav 252,156,16\nQmaav 208,160,16\ns_add_u32 s88, s86, 101376\nQe 0\n"
"Qmaav 212,164,16\nbuffer_load_dwordx4 v64, s[48:51], s88 offen offset:1024 lds\nQe 1\nQmaav 216,168,16\nQe 2\nQmaav 220,172,16\nQe 3\nQmaav 196,204,16\n"
"s_add_u32 s88, s86, 120832\nQk 16,0,1\nQk 17,2,3\nQcvaa 8,112,16,28\nbuffer_load_dwordx4 v64, s[48:51], s88 offen offset:2048 lds\nQe 4\nQmvaa 8,116,20\nQe 5\n"
"Qmvaa 8,120,24\nQe 6\nQmvaa 8,124,28\nQe 7\nQcvaa 12,128,16,28\nQk 18,4,5\nQk 19,6,7\nQmvaa 12,132,20\n"
"Qmvaa 12,136,24\nQmvaa 12,140,28\nQmaav 224,144,20\nQta 144,55,0\nQta 146,55,2048\nQmaav 228,148,20\nQta 148,55,256\nQta 150,55,2304\n"
"Qmaav 232,152,20\nQta 152,55,512\nQta 154,55,2560\nQmaav 236,156,20\nQta 156,55,768\nQta 158,55,2816\nQmaav 240,160,20\nQta 160,55,1024\n"
"Qta 162,55,3072\nQmaav 244,164,20\nQta 164,55,1280\nQta 166,55,3328\nQmaav 248,168,20\nQta 168,55,1536\nQta 170,55,3584\nQmaav 252,172,20\n"
"Qta 172,55,1792\nQta 174,55,3840\nQmaav 200,204,20\nQe 8\nQcvaa 0,112,32,32\nv_add_u32 v75, 1, v75\nQmvaa 0,116,36\ns_waitcnt vmcnt(23)\n"
"ds_write_b32 v73, v75\nQmvaa 0,120,40\nQe 9\nQmvaa 0,124,44\nQe 10\nQcvaa 4,128,32,32\nQe 11\nQmvaa 4,132,36\n"
"Qk 20,8,9\nQk 21,10,11\nQmvaa 4,136,40\nQe 12\nQmvaa 4,140,44\ns_waitcnt lgkmcnt(1)\nQmvav 80,144,16\nQe 13\n"
"Qmvav 84,148,16\nQe 14\nQmvav 88,152,16\nQe 15\nQmvav 92,156,16\nQk 22,12,13\nQk 23,14,15\nQmvav 96,160,16\n"
"Qe 0\nQmvav 100,164,16\nQe 1\nQmvav 104,168,16\nQe 2\nQmvav 108,172,16\nQe 3\nQmaav 176,204,16\n"
"Qk 16,0,1\nQk 17,2,3\nQcvaa 8,112,48,36\nQe 4\nQmvaa 8,116,52\nds_read_b32 v69, v74\nQmvaa 8,120,56\nQe 5\n"
"Qmvaa 8,124,60\nQe 6\nQcvaa 12,128,48,36\nQe 7\nQmvaa 12,132,52\nQk 18,4,5\nQk 19,6,7\nQmvaa 12,136,56\n"
"s_waitcnt lgkmcnt(0)\nv_cmp_gt_u32 vcc, v75, v69\nQmvaa 12,140,60\ns_cbranch_vccnz .Lsa30\n.Lra30:\nQmvav 112,144,20\ns_add_u32 m0, s62, 33024\ns_add_u32 s69, s87, 524288\n"
"Qmvav 116,148,20\nbuffer_load_dwordx4 v62, s[52:55], s69 offen lds\nQmvav 120,152,20\nQmvav 124,156,20\nQe 8\nQmvav 128,160,20\nQe 9\nQmvav 132,164,20\n"
"s_add_u32 s69, s87, 556032\nQe 10\nQmvav 136,168,20\nbuffer_load_dwordx4 v62, s[52:55], s69 offen offset:1024 lds\nQmvav 140,172,20\nQe 11\nQmaav 180,204,20\nQk 20,8,9\n"
"Qk 21,10,11\nQcvaa 0,112,64,40\nQe 12\nQmvaa 0,116,68\ns_add_u32 s69, s87, 587768\nQe 13\nQmvaa 0,120,72\nbuffer_load_dwordx4 v62, s[52:55], s69 offen offset:2056 lds\n"
"Qmvaa 0,124,76\nQe 14\nQcvaa 4,128,64,40\nQe 15\nQmvaa 4,132,68\nQk 22,12,13\nQk 23,14,15\nQmvaa 4,136,72\n"
"s_add_u32 s69, s87, 619512\nQmvaa 4,140,76\nbuffer_load_dwordx4 v62, s[52:55], s69 offen offset:3080 lds\nQmvav 144,144,16\nQmvav 148,148,16\nQmvav 152,152,16\nQmvav 156,156,16\ns_add_u32 m0, s62, 49472\n"
"s_add_u32 s69, s87, 786432\nQmvav 160,160,16\nbuffer_load_dwordx4 v62, s[52:55], s69 offen lds\nQmvav 164,164,16\nQe 0\nQmvav 168,168,16\nQe 1\nQmvav 172,172,16\n"
"Qe 2\nQmaav 184,204,16\ns_add_u32 s69, s87, 818176\nQe 3\nQcvaa 8,112,80,44\nbuffer_load_dwordx4 v62, s[52:55], s69 offen offset:1024 lds\nQmvaa 8,116,84\nQk 16,0,1\n"
"Qk 17,2,3\nQmvaa 8,120,88\nQe 4\nQmvaa 8,124,92\nQe 5\nQcvaa 12,128,80,44\ns_add_u32 s69, s87, 849912\nQe 6\n"
"Qmvaa 12,132,84\nbuffer_load_dwordx4 v62, s[52:55], s69 offen offset:2056 lds\nQmvaa 12,136,88\nQe 7\nQmvaa 12,140,92\nQk 18,4,5\nQk 19,6,7\nQmvav 176,144,20\n"
"Qmvav 180,148,20\ns_add_u32 s69, s87, 881656\nQmvav 184,152,20\nbuffer_load_dwordx4 v62, s[52:55], s69 offen offset:3080 lds\nQmvav 188,156,20\nQe 8\nQmvav 192,160,20\nQe 9\n"
"Qmvav 196,164,20\nQmvav 200,168,20\nQe 10\nQmvav 204,172,20\nQe 11\nQmaav 188,204,20\nQk 20,8,9\nQk 21,10,11\n"
"Qcvaa 0,112,96,48\nQda 112,53,33024\nQmvaa 0,116,100\nQda 116,53,33536\nQmvaa 0,120,104\nQda 120,53,34048\nQmvaa 0,124,108\nQda 124,53,34560\n"
"Qcvaa 4,128,96,48\nQda 128,53,35072\nQmvaa 4,132,100\nQda 132,53,35584\nQmvaa 4,136,104\nQda 136,53,36096\nQmvaa 4,140,108\nQda 140,53,36608\n"
"Qmvav 208,144,16\nQmvav 212,148,16\nQe 12\nQmvav 216,152,16\nQe 13\nQmvav 220,156,16\nQe 14\nQmvav 224,160,16\n"
"Qe 15\nQmvav 228,164,16\nQk 22,12,13\nQk 23,14,15\nQmvav 232,168,16\nQe 0\nQmvav 236,172,16\nQe 1\n"
"Qmaav 192,204,16\ns_waitcnt lgkmcnt(0)\nQk 16,0,1\nQcvaa 8,112,0,24\nQe 2\nQmvaa 8,116,4\nQe 3\nQmvaa 8,120,8\n"
"Qk 17,2,3\nQmvaa 8,124,12\nQe 4\nQcvaa 12,128,0,24\nQe 5\nQmvaa 12,132,4\nQe 6\nQmvaa 12,136,8\n"
"Qe 7\nQmvaa 12,140,12\nQk 18,4,5\nQk 19,6,7\nQmvav 240,144,20\nQmvav 244,148,20\nQmvav 248,152,20\nQmvav 252,156,20\n"
"Qmaav 208,160,20\nQe 8\nQmaav 212,164,20\nQe 9\nQmaav 216,168,20\nQe 10\nQmaav 220,172,20\nQe 11\n"
"Qmaav 196,204,20\nQk 20,8,9\nQk 21,10,11\nQcvaa 0,112,16,28\nQe 12\nQmvaa 0,116,20\nQe 13\nQmvaa 0,120,24\n"
"Qe 14\nQmvaa 0,124,28\nQe 15\nQcvaa 4,128,16,28\nQk 22,12,13\nQk 23,14,15\nQmvaa 4,132,20\nQmvaa 4,136,24\n"
"Qmvaa 4,140,28\nQmaav 224,144,16\nQta 144,55,33024\nQta 146,55,35072\nQmaav 228,148,16\nQta 148,55,33280\nQta 150,55,35328\nQmaav 232,152,16\n"
"Qta 152,55,33536\nQta 154,55,35584\nQmaav 236,156,16\nQta 156,55,33792\nQta 158,55,35840\nQmaav 240,160,16\nQta 160,55,34048\nQta 162,55,36096\n"
"Qmaav 244,164,16\nQta 164,55,34304\nQta 166,55,36352\nQmaav 248,168,16\nQta 168,55,34560\nQta 170,55,36608\nQmaav 252,172,16\nQta 172,55,34816\n"
"Qta 174,55,36864\nQmaav 200,204,16\nQe 0\nQcvaa 8,112,32,32\nv_add_u32 v75, 1, v75\nQmvaa 8,116,36\nds_write_b32 v73, v75\nQmvaa 8,120,40\n"
"Qe 1\nQmvaa 8,124,44\nQe 2\nQcvaa 12,128,32,32\nQe 3\nQmvaa 12,132,36\nQk 16,0,1\nQk 17,2,3\n"
"Qmvaa 12,136,40\nQe 4\nQmvaa 12,140,44\ns_waitcnt lgkmcnt(1)\nQmvav 80,144,20\nQe 5\nQmvav 84,148,20\nQe 6\n"
"Qmvav 88,152,20\nQe 7\nQmvav 92,156,20\nQk 18,4,5\nQk 19,6,7\nQmvav 96,160,20\nQe 8\nQmvav 100,164,20\n"
"Qe 9\nQmvav 104,168,20\nQe 10\nQmvav 108,172,20\nQe 11\nQmaav 176,204,20\nQk 20,8,9\nQk 21,10,11\n"
"Qcvaa 0,112,48,36\nQe 12\nQmvaa 0,116,52\nds_read_b32 v69, v74\nQmvaa 0,120,56\nQe 13\nQmvaa 0,124,60\nQe 14\n"
"Qcvaa 4,128,48,36\nQe 15\nQmvaa 4,132,52\nQk 22,12,13\nQk 23,14,15\nQmvaa 4,136,56\ns_waitcnt lgkmcnt(0)\nv_cmp_gt_u32 vcc, v75, v69\n"
"Qmvaa 4,140,60\ns_cbranch_vccnz .Lsa31\n.Lra31:\nQmvav 112,144,16\ns_add_u32 m0, s63, 0\ns_add_u32 s88, s86, 128\nQmvav 116,148,16\nbuffer_load_dwordx4 v64, s[48:51], s88 offen lds\n"
"Qmvav 120,152,16\nQmvav 124,156,16\nQe 0\nQmvav 128,160,16\ns_add_u32 s88, s86, 19584\nQe 1\nQmvav 132,164,16\nbuffer_load_dwordx4 v64, s[48:51], s88 offen offset:1024 lds\n"
"Qe 2\nQmvav 136,168,16\nQmvav 140,172,16\nQe 3\nQmaav 180,204,16\ns_add_u32 s88, s86, 39040\nQk 16,0,1\nQk 17,2,3\n"
"Qcvaa 8,112,64,40\nbuffer_load_dwordx4 v64, s[48:51], s88 offen offset:2048 lds\nQe 4\nQmvaa 8,116,68\nQe 5\nQmvaa 8,120,72\nQmvaa 8,124,76\ns_add_u32 s88, s86, 58496\n"
"Qe 6\nQcvaa 12,128,64,40\nbuffer_load_dwordx4 v64, s[48:51], s88 offen offset:3072 lds\nQe 7\nQmvaa 12,132,68\nQk 18,4,5\nQk 19,6,7\nQmvaa 12,136,72\n"
"Qmvaa 12,140,76\ns_add_u32 m0, s63, 4096\ns_add_u32 s88, s86, 82048\nQmvav 144,144,20\nbuffer_load_dwordx4 v64, s[48:51], s88 offen lds\nQmvav 148,148,20\nQmvav 152,152,20\nQmvav 156,156,20\n"
"s_add_u32 s88, s86, 101504\nQmvav 160,160,20\nbuffer_load_dwordx4 v64, s[48:51], s88 offen offset:1024 lds\nQmvav 164,164,20\nQe 8\nQmvav 168,168,20\nQe 9\nQmvav 172,172,20\n"
"s_add_u32 s88, s86, 120960\nQe 10\nQmaav 184,204,20\nbuffer_load_dwordx4 v64, s[48:51], s88 offen offset:2048 lds\nQe 11\nQcvaa 0,112,80,44\nQmvaa 0,116,84\nQk 20,8,9\n"
"Qk 21,10,11\nQmvaa 0,120,88\nQe 12\nQmvaa 0,124,92\nQe 13\nQcvaa 4,128,80,44\nQe 14\nQmvaa 4,132,84\n"
"Qmvaa 4,136,88\nQe 15\nQmvaa 4,140,92\nQk 22,12,13\nQk 23,14,15\nQmvav 176,144,16\nQmvav 180,148,16\nQmvav 184,152,16\n"
"Qmvav 188,156,16\nQe 0\nQmvav 192,160,16\nQe 1\nQmvav 196,164,16\nQmvav 200,168,16\nQe 2\nQmvav 204,172,16\n"
"Qe 3\nQmaav 188,204,16\nQk 16,0,1\nQk 17,2,3\nQcvaa 8,112,96,48\nQmvaa 8,116,100\nQmvaa 8,120,104\nQmvaa 8,124,108\n"
"Qcvaa 12,128,96,48\nQmvaa 12,132,100\nQmvaa 12,136,104\nQmvaa 12,140,108\nQmvav 208,144,20\nQmvav 212,148,20\nQe 4\nQmvav 216,152,20\n"
"Qe 5\nQmvav 220,156,20\nQe 6\nQmvav 224,160,20\nQe 7\nQmvav 228,164,20\nQk 18,4,5\nQk 19,6,7\n"
"Qmvav 232,168,20\nQe 8\nQmvav 236,172,20\nQe 9\nQmaav 192,204,20\nQk 20,8,9\nQe 10\nQe 11\n"
"s_nop 0\nQk 21,10,11\nQe 12\nQe 13\nQe 14\nQe 15\nQk 22,12,13\nQk 23,14,15\n"
"Qmvav 240,144,16\nQmvav 244,148,16\nQmvav 248,152,16\nQmvav 252,156,16\nQmaav 208,160,16\nQmaav 212,164,16\nQmaav 216,168,16\nQmaav 220,172,16\n"
"Qmaav 196,204,16\nQmaav 224,144,20\nQmaav 228,148,20\nQmaav 232,152,20\nQmaav 236,156,20\nQmaav 240,160,20\nQmaav 244,164,20\nQmaav 248,168,20\n"
"Qmaav 252,172,20\nQmaav 200,204,20\ns_nop 7\nv_accvgpr_read_b32 v24, a176\nv_accvgpr_read_b32 v28, a180\nv_accvgpr_read_b32 v32, a184\nv_accvgpr_read_b32 v36, a188\nv_accvgpr_read_b32 v40, a192\n"
"v_accvgpr_read_b32 v44, a196\nv_accvgpr_read_b32 v48, a200\nv_cmp_nge_f32 vcc, 0x44800000, v24\ns_mov_b64 s[74:75], vcc\nv_cmp_nge_f32 vcc, 0x44800000, v28\ns_or_b64 s[74:75], s[74:75], vcc\nv_cmp_nge_f32 vcc, 0x44800000, v32\ns_or_b64 s[74:75], s[74:75], vcc\n"
"v_cmp_nge_f32 vcc, 0x44800000, v36\ns_or_b64 s[74:75], s[74:75], vcc\nv_cmp_nge_f32 vcc, 0x44800000, v40\ns_or_b64 s[74:75], s[74:75], vcc\nv_cmp_nge_f32 vcc, 0x44800000, v44\ns_or_b64 s[74:75], s[74:75], vcc\nv_cmp_nge_f32 vcc, 0x44800000, v48\ns_or_b64 s[74:75], s[74:75], vcc\n"
"s_cmp_lg_u64 s[74:75], 0\ns_cbranch_scc1 .Lfb\n.Lfbret0:\nv_rcp_f32 v25, v24\nv_rcp_f32 v24, v24\nv_rcp_f32 v29, v28\nv_rcp_f32 v28, v28\nv_rcp_f32 v33, v32\n"
"v_rcp_f32 v32, v32\nv_rcp_f32 v37, v36\nv_rcp_f32 v36, v36\nv_rcp_f32 v41, v40\nv_rcp_f32 v40, v40\nv_rcp_f32 v45, v44\nv_rcp_f32 v44, v44\nv_rcp_f32 v49, v48\n"
"v_rcp_f32 v48, v48\nv_pk_mul_f32 v[80:81], v[80:81], v[24:25]\nv_pk_mul_f32 v[82:83], v[82:83], v[24:25]\nv_pk_mul_f32 v[84:85], v[84:85], v[24:25]\nv_pk_mul_f32 v[86:87], v[86:87], v[24:25]\nQk 80,80,81\nQk 81,82,83\nQk 82,84,85\n"
"Qk 83,86,87\nv_accvgpr_write_b32 a0, v80\nv_accvgpr_write_b32 a1, v81\nv_accvgpr_write_b32 a2, v82\nv_accvgpr_write_b32 a3, v83\nv_pk_mul_f32 v[88:89], v[88:89], v[24:25]\nv_pk_mul_f32 v[90:91], v[90:91], v[24:25]\nv_pk_mul_f32 v[92:93], v[92:93], v[24:25]\n"
"v_pk_mul_f32 v[94:95], v[94:95], v[24:25]\nQk 88,88,89\nQk 89,90,91\nQk 90,92,93\nQk 91,94,95\nv_accvgpr_write_b32 a4, v88\nv_accvgpr_write_b32 a5, v89\nv_accvgpr_write_b32 a6, v90\n"
"v_accvgpr_write_b32 a7, v91\nv_pk_mul_f32 v[96:97], v[96:97], v[24:25]\nv_pk_mul_f32 v[98:99], v[98:99], v[24:25]\nv_pk_mul_f32 v[100:101], v[100:101], v[24:25]\nv_pk_mul_f32 v[102:103], v[102:103], v[24:25]\nQk 96,96,97\nQk 97,98,99\nQk 98,100,101\n"
"Qk 99,102,103\nv_accvgpr_write_b32 a8, v96\nv_accvgpr_write_b32 a9, v97\nv_accvgpr_write_b32 a10, v98\nv_accvgpr_write_b32 a11, v99\nv_pk_mul_f32 v[104:105], v[104:105], v[24:25]\nv_pk_mul_f32 v[106:107], v[106:107], v[24:25]\nv_pk_mul_f32 v[108:109], v[108:109], v[24:25]\n"
"v_pk_mul_f32 v[110:111], v[110:111], v[24:25]\nQk 104,104,105\nQk 105,106,107\nQk 106,108,109\nQk 107,110,111\nv_accvgpr_write_b32 a12, v104\nv_accvgpr_write_b32 a13, v105\nv_accvgpr_write_b32 a14, v106\n"
"v_accvgpr_write_b32 a15, v107\nv_pk_mul_f32 v[112:113], v[112:113], v[28:29]\nv_pk_mul_f32 v[114:115], v[114:115], v[28:29]\nv_pk_mul_f32 v[116:117], v[116:117], v[28:29]\nv_pk_mul_f32 v[118:119], v[118:119], v[28:29]\nQk 112,112,113\nQk 113,114,115\nQk 114,116,117\n"
"Qk 115,118,119\nv_accvgpr_write_b32 a16, v112\nv_accvgpr_write_b32 a17, v113\nv_accvgpr_write_b32 a18, v114\nv_accvgpr_write_b32 a19, v115\nv_pk_mul_f32 v[120:121], v[120:121], v[28:29]\nv_pk_mul_f32 v[122:123], v[122:123], v[28:29]\nv_pk_mul_f32 v[124:125], v[124:125], v[28:29]\n"
"v_pk_mul_f32 v[126:127], v[126:127], v[28:29]\nQk 120,120,121\nQk 121,122,123\nQk 122,124,125\nQk 123,126,127\nv_accvgpr_write_b32 a20, v120\nv_accvgpr_write_b32 a21, v121\nv_accvgpr_write_b32 a22, v122\n"
"v_accvgpr_write_b32 a23, v123\nv_pk_mul_f32 v[128:129], v[128:129], v[28:29]\nv_pk_mul_f32 v[130:131], v[130:131], v[28:29]\nv_pk_mul_f32 v[132:133], v[132:133], v[28:29]\nv_pk_mul_f32 v[134:135], v[134:135], v[28:29]\nQk 128,128,129\nQk 129,130,131\nQk 130,132,133\n"
"Qk 131,134,135\nv_accvgpr_write_b32 a24, v128\nv_accvgpr_write_b32 a25, v129\nv_accvgpr_write_b32 a26, v130\nv_accvgpr_write_b32 a27, v131\nv_pk_mul_f32 v[136:137], v[136:137], v[28:29]\nv_pk_mul_f32 v[138:139], v[138:139], v[28:29]\nv_pk_mul_f32 v[140:141], v[140:141], v[28:29]\n"
"v_pk_mul_f32 v[142:143], v[142:143], v[28:29]\nQk 136,136,137\nQk 137,138,139\nQk 138,140,141\nQk 139,142,143\nv_accvgpr_write_b32 a28, v136\nv_accvgpr_write_b32 a29, v137\nv_accvgpr_write_b32 a30, v138\n"
"v_accvgpr_write_b32 a31, v139\nv_pk_mul_f32 v[144:145], v[144:145], v[32:33]\nv_pk_mul_f32 v[146:147], v[146:147], v[32:33]\nv_pk_mul_f32 v[148:149], v[148:149], v[32:33]\nv_pk_mul_f32 v[150:151], v[150:151], v[32:33]\nQk 144,144,145\nQk 145,146,147\nQk 146,148,149\n"
"Qk 147,150,151\nv_accvgpr_write_b32 a32, v144\nv_accvgpr_write_b32 a33, v145\nv_accvgpr_write_b32 a34, v146\nv_accvgpr_write_b32 a35, v147\nv_pk_mul_f32 v[152:153], v[152:153], v[32:33]\nv_pk_mul_f32 v[154:155], v[154:155], v[32:33]\nv_pk_mul_f32 v[156:157], v[156:157], v[32:33]\n"
"v_pk_mul_f32 v[158:159], v[158:159], v[32:33]\nQk 152,152,153\nQk 153,154,155\nQk 154,156,157\nQk 155,158,159\nv_accvgpr_write_b32 a36, v152\nv_accvgpr_write_b32 a37, v153\nv_accvgpr_write_b32 a38, v154\n"
"v_accvgpr_write_b32 a39, v155\nv_pk_mul_f32 v[160:161], v[160:161], v[32:33]\nv_pk_mul_f32 v[162:163], v[162:163], v[32:33]\nv_pk_mul_f32 v[164:165], v[164:165], v[32:33]\nv_pk_mul_f32 v[166:167], v[166:167], v[32:33]\nQk 160,160,161\nQk 161,162,163\nQk 162,164,165\n"
"Qk 163,166,167\nv_accvgpr_write_b32 a40, v160\nv_accvgpr_write_b32 a41, v161\nv_accvgpr_write_b32 a42, v162\nv_accvgpr_write_b32 a43, v163\nv_pk_mul_f32 v[168:169], v[168:169], v[32:33]\nv_pk_mul_f32 v[170:171], v[170:171], v[32:33]\nv_pk_mul_f32 v[172:173], v[172:173], v[32:33]\n"
"v_pk_mul_f32 v[174:175], v[174:175], v[32:33]\nQk 168,168,169\nQk 169,170,171\nQk 170,172,173\nQk 171,174,175\nv_accvgpr_write_b32 a44, v168\nv_accvgpr_write_b32 a45, v169\nv_accvgpr_write_b32 a46, v170\n"
"v_accvgpr_write_b32 a47, v171\nv_pk_mul_f32 v[176:177], v[176:177], v[36:37]\nv_pk_mul_f32 v[178:179], v[178:179], v[36:37]\nv_pk_mul_f32 v[180:181], v[180:181], v[36:37]\nv_pk_mul_f32 v[182:183], v[182:183], v[36:37]\nQk 176,176,177\nQk 177,178,179\nQk 178,180,181\n"
"Qk 179,182,183\nv_accvgpr_write_b32 a48, v176\nv_accvgpr_write_b32 a49, v177\nv_accvgpr_write_b32 a50, v178\nv_accvgpr_write_b32 a51, v179\nv_pk_mul_f32 v[184:185], v[184:185], v[36:37]\nv_pk_mul_f32 v[186:187], v[186:187], v[36:37]\nv_pk_mul_f32 v[188:189], v[188:189], v[36:37]\n"
"v_pk_mul_f32 v[190:191], v[190:191], v[36:37]\nQk 184,184,185\nQk 185,186,187\nQk 186,188,189\nQk 187,190,191\nv_accvgpr_write_b32 a52, v184\nv_accvgpr_write_b32 a53, v185\nv_accvgpr_write_b32 a54, v186\n"
"v_accvgpr_write_b32 a55, v187\nv_pk_mul_f32 v[192:193], v[192:193], v[36:37]\nv_pk_mul_f32 v[194:195], v[194:195], v[36:37]\nv_pk_mul_f32 v[196:197], v[196:197], v[36:37]\nv_pk_mul_f32 v[198:199], v[198:199], v[36:37]\nQk 192,192,193\nQk 193,194,195\nQk 194,196,197\n"
"Qk 195,198,199\nv_accvgpr_write_b32 a56, v192\nv_accvgpr_write_b32 a57, v193\nv_accvgpr_write_b32 a58, v194\nv_accvgpr_write_b32 a59, v195\nv_pk_mul_f32 v[200:201], v[200:201], v[36:37]\nv_pk_mul_f32 v[202:203], v[202:203], v[36:37]\nv_pk_mul_f32 v[204:205], v[204:205], v[36:37]\n"
"v_pk_mul_f32 v[206:207], v[206:207], v[36:37]\nQk 200,200,201\nQk 201,202,203\nQk 202,204,205\nQk 203,206,207\nv_accvgpr_write_b32 a60, v200\nv_accvgpr_write_b32 a61, v201\nv_accvgpr_write_b32 a62, v202\n"
"v_accvgpr_write_b32 a63, v203\nv_pk_mul_f32 v[208:209], v[208:209], v[40:41]\nv_pk_mul_f32 v[210:211], v[210:211], v[40:41]\nv_pk_mul_f32 v[212:213], v[212:213], v[40:41]\nv_pk_mul_f32 v[214:215], v[214:215], v[40:41]\nQk 208,208,209\nQk 209,210,211\nQk 210,212,213\n"
"Qk 211,214,215\nv_accvgpr_write_b32 a64, v208\nv_accvgpr_write_b32 a65, v209\nv_accvgpr_write_b32 a66, v210\nv_accvgpr_write_b32 a67, v211\nv_pk_mul_f32 v[216:217], v[216:217], v[40:41]\nv_pk_mul_f32 v[218:219], v[218:219], v[40:41]\nv_pk_mul_f32 v[220:221], v[220:221], v[40:41]\n"
"v_pk_mul_f32 v[222:223], v[222:223], v[40:41]\nQk 216,216,217\nQk 217,218,219\nQk 218,220,221\nQk 219,222,223\nv_accvgpr_write_b32 a68, v216\nv_accvgpr_write_b32 a69, v217\nv_accvgpr_write_b32 a70, v218\n"
"v_accvgpr_write_b32 a71, v219\nv_pk_mul_f32 v[224:225], v[224:225], v[40:41]\nv_pk_mul_f32 v[226:227], v[226:227], v[40:41]\nv_pk_mul_f32 v[228:229], v[228:229], v[40:41]\nv_pk_mul_f32 v[230:231], v[230:231], v[40:41]\nQk 224,224,225\nQk 225,226,227\nQk 226,228,229\n"
"Qk 227,230,231\nv_accvgpr_write_b32 a72, v224\nv_accvgpr_write_b32 a73, v225\nv_accvgpr_write_b32 a74, v226\nv_accvgpr_write_b32 a75, v227\nv_pk_mul_f32 v[232:233], v[232:233], v[40:41]\nv_pk_mul_f32 v[234:235], v[234:235], v[40:41]\nv_pk_mul_f32 v[236:237], v[236:237], v[40:41]\n"
"v_pk_mul_f32 v[238:239], v[238:239], v[40:41]\nQk 232,232,233\nQk 233,234,235\nQk 234,236,237\nQk 235,238,239\nv_accvgpr_write_b32 a76, v232\nv_accvgpr_write_b32 a77, v233\nv_accvgpr_write_b32 a78, v234\n"
"v_accvgpr_write_b32 a79, v235\nv_pk_mul_f32 v[240:241], v[240:241], v[44:45]\nv_pk_mul_f32 v[242:243], v[242:243], v[44:45]\nv_pk_mul_f32 v[244:245], v[244:245], v[44:45]\nv_pk_mul_f32 v[246:247], v[246:247], v[44:45]\nQk 240,240,241\nQk 241,242,243\nQk 242,244,245\n"
"Qk 243,246,247\nv_accvgpr_write_b32 a80, v240\nv_accvgpr_write_b32 a81, v241\nv_accvgpr_write_b32 a82, v242\nv_accvgpr_write_b32 a83, v243\nv_pk_mul_f32 v[248:249], v[248:249], v[44:45]\nv_pk_mul_f32 v[250:251], v[250:251], v[44:45]\nv_pk_mul_f32 v[252:253], v[252:253], v[44:45]\n"
"v_pk_mul_f32 v[254:255], v[254:255], v[44:45]\nQk 248,248,249\nQk 249,250,251\nQk 250,252,253\nQk 251,254,255\nv_accvgpr_write_b32 a84, v248\nv_accvgpr_write_b32 a85, v249\nv_accvgpr_write_b32 a86, v250\n"
"v_accvgpr_write_b32 a87, v251\nv_accvgpr_read_b32 v0, a208\nv_accvgpr_read_b32 v1, a209\nv_accvgpr_read_b32 v2, a210\nv_accvgpr_read_b32 v3, a211\nv_accvgpr_read_b32 v4, a212\nv_accvgpr_read_b32 v5, a213\nv_accvgpr_read_b32 v6, a214\n"
"v_accvgpr_read_b32 v7, a215\nv_pk_mul_f32 v[0:1], v[0:1], v[44:45]\nv_pk_mul_f32 v[2:3], v[2:3], v[44:45]\nv_pk_mul_f32 v[4:5], v[4:5], v[44:45]\nv_pk_mul_f32 v[6:7], v[6:7], v[44:45]\nQk 0,0,1\nQk 1,2,3\nQk 2,4,5\n"
"Qk 3,6,7\nv_accvgpr_write_b32 a88, v0\nv_accvgpr_write_b32 a89, v1\nv_accvgpr_write_b32 a90, v2\nv_accvgpr_write_b32 a91, v3\nv_accvgpr_read_b32 v8, a216\nv_accvgpr_read_b32 v9, a217\nv_accvgpr_read_b32 v10, a218\n"
"v_accvgpr_read_b32 v11, a219\nv_accvgpr_read_b32 v12, a220\nv_accvgpr_read_b32 v13, a221\nv_accvgpr_read_b32 v14, a222\nv_accvgpr_read_b32 v15, a223\nv_pk_mul_f32 v[8:9], v[8:9], v[44:45]\nv_pk_mul_f32 v[10:11], v[10:11], v[44:45]\nv_pk_mul_f32 v[12:13], v[12:13], v[44:45]\n"
"v_pk_mul_f32 v[14:15], v[14:15], v[44:45]\nQk 8,8,9\nQk 9,10,11\nQk 10,12,13\nQk 11,14,15\nv_accvgpr_write_b32 a92, v8\nv_accvgpr_write_b32 a93, v9\nv_accvgpr_write_b32 a94, v10\n"
"v_accvgpr_write_b32 a95, v11\nv_accvgpr_read_b32 v0, a224\nv_accvgpr_read_b32 v1, a225\nv_accvgpr_read_b32 v2, a226\nv_accvgpr_read_b32 v3, a227\nv_accvgpr_read_b32 v4, a228\nv_accvgpr_read_b32 v5, a229\nv_accvgpr_read_b32 v6, a230\n"
"v_accvgpr_read_b32 v7, a231\nv_pk_mul_f32 v[0:1], v[0:1], v[48:49]\nv_pk_mul_f32 v[2:3], v[2:3], v[48:49]\nv_pk_mul_f32 v[4:5], v[4:5], v[48:49]\nv_pk_mul_f32 v[6:7], v[6:7], v[48:49]\nQk 0,0,1\nQk 1,2,3\nQk 2,4,5\n"
"Qk 3,6,7\nv_accvgpr_write_b32 a96, v0\nv_accvgpr_write_b32 a97, v1\nv_accvgpr_write_b32 a98, v2\nv_accvgpr_write_b32 a99, v3\nv_accvgpr_read_b32 v8, a232\nv_accvgpr_read_b32 v9, a233\nv_accvgpr_read_b32 v10, a234\n"
"v_accvgpr_read_b32 v11, a235\nv_accvgpr_read_b32 v12, a236\nv_accvgpr_read_b32 v13, a237\nv_accvgpr_read_b32 v14, a238\nv_accvgpr_read_b32 v15, a239\nv_pk_mul_f32 v[8:9], v[8:9], v[48:49]\nv_pk_mul_f32 v[10:11], v[10:11], v[48:49]\nv_pk_mul_f32 v[12:13], v[12:13], v[48:49]\n"
"v_pk_mul_f32 v[14:15], v[14:15], v[48:49]\nQk 8,8,9\nQk 9,10,11\nQk 10,12,13\nQk 11,14,15\nv_accvgpr_write_b32 a100, v8\nv_accvgpr_write_b32 a101, v9\nv_accvgpr_write_b32 a102, v10\n"
"v_accvgpr_write_b32 a103, v11\nv_accvgpr_read_b32 v0, a240\nv_accvgpr_read_b32 v1, a241\nv_accvgpr_read_b32 v2, a242\nv_accvgpr_read_b32 v3, a243\nv_accvgpr_read_b32 v4, a244\nv_accvgpr_read_b32 v5, a245\nv_accvgpr_read_b32 v6, a246\n"
"v_accvgpr_read_b32 v7, a247\nv_pk_mul_f32 v[0:1], v[0:1], v[48:49]\nv_pk_mul_f32 v[2:3], v[2:3], v[48:49]\nv_pk_mul_f32 v[4:5], v[4:5], v[48:49]\nv_pk_mul_f32 v[6:7], v[6:7], v[48:49]\nQk 0,0,1\nQk 1,2,3\nQk 2,4,5\n"
"Qk 3,6,7\nv_accvgpr_write_b32 a104, v0\nv_accvgpr_write_b32 a105, v1\nv_accvgpr_write_b32 a106, v2\nv_accvgpr_write_b32 a107, v3\nv_accvgpr_read_b32 v8, a248\nv_accvgpr_read_b32 v9, a249\nv_accvgpr_read_b32 v10, a250\n"
"v_accvgpr_read_b32 v11, a251\nv_accvgpr_read_b32 v12, a252\nv_accvgpr_read_b32 v13, a253\nv_accvgpr_read_b32 v14, a254\nv_accvgpr_read_b32 v15, a255\nv_pk_mul_f32 v[8:9], v[8:9], v[48:49]\nv_pk_mul_f32 v[10:11], v[10:11], v[48:49]\nv_pk_mul_f32 v[12:13], v[12:13], v[48:49]\n"
"v_pk_mul_f32 v[14:15], v[14:15], v[48:49]\nQk 8,8,9\nQk 9,10,11\nQk 10,12,13\nQk 11,14,15\nv_accvgpr_write_b32 a108, v8\nv_accvgpr_write_b32 a109, v9\nv_accvgpr_write_b32 a110, v10\n"
"v_accvgpr_write_b32 a111, v11\ns_mov_b32 s56, %8\ns_and_b32 s57, %9, 0xffff\ns_mov_b32 s94, 0x7ffff000\ns_mul_i32 s68, %20, 40960\ns_lshl_b32 s69, s70, 10\ns_add_u32 s68, s68, s69\ns_add_u32 s68, s68, 4096\n"
"s_add_u32 s94, %15, s68\ns_addc_u32 s95, %16, 0\ns_and_b32 s95, s95, 0xffff\ns_mov_b32 s100, 37888\ns_mov_b32 s58, 1835008\ns_mov_b32 s67, 1\ns_mul_i32 s71, %12, 57344\ns_mul_i32 s68, s92, 7168\n"
"s_add_u32 s71, s71, s68\ns_branch .Lround\n.Lhook:\ns_cmp_eq_u32 s66, 1\ns_cbranch_scc1 .Lh1\ns_cmp_eq_u32 s66, 8\ns_cbranch_scc1 .Lh8\ns_cmp_eq_u32 s66, 9\n"
"s_cbranch_scc1 .Lh9\ns_lshr_b32 s68, s99, s66\ns_cmp_eq_u32 s68, 1\ns_cbranch_scc1 .Lh12\ns_branch .Lh11\n.Lh1:\ns_cmp_eq_u32 s70, 0\ns_cselect_b32 s68, 128, 0\n"
"v_mov_b32 v66, 0x7ffff000\nv_cmp_gt_u32 vcc, s68, v60\nv_not_b32 v67, v65\ns_lshl_b32 s68, %19, 2\nv_cndmask_b32 v66, v66, v76, vcc\nv_readlane_b32 s30, v67, %19\nbuffer_store_dword v67, v66, s[24:27], s68 offen offset:1024 sc1\ns_branch .Lhookret\n"
".Lh8:\nv_bfe_u32 v66, v60, 4, 3\ns_lshl_b32 s68, %19, 7\nv_lshlrev_b32 v66, 2, v66\ns_add_u32 s68, s68, 2293760\nbuffer_load_dword v78, v66, s[24:27], s68 offen offset:1024 sc1\ns_branch .Lhookret\n.Lh9:\n"
"s_waitcnt vmcnt(32)\ns_mov_b32 s97, 0\n.Lack_l:\nv_cmp_ne_u32 vcc, s30, v78\ns_cmp_eq_u64 vcc, 0\ns_cbranch_scc1 .Lack_ok\ns_sleep 1\nv_bfe_u32 v66, v60, 4, 3\n"
"s_lshl_b32 s68, %19, 7\nv_lshlrev_b32 v66, 2, v66\ns_add_u32 s68, s68, 2293760\nbuffer_load_dword v78, v66, s[24:27], s68 offen offset:1024 sc1\ns_waitcnt vmcnt(0)\ns_add_u32 s97, s97, 1\ns_cmp_lt_u32 s97, 1048576\ns_cbranch_scc1 .Lack_l\n"
".Lack_ok:\nv_add_u32 v66, 1, v65\nbuffer_store_dword v66, v77, s[24:27], 0 offen sc1\ns_branch .Lhookret\n.Lh11:\nbuffer_load_dword v78, v76, s[24:27], 0 offen sc1\ns_branch .Lhookret\n.Lh12:\n"
"s_waitcnt vmcnt(32)\nv_sub_u32 v66, v78, v65\nv_cmp_gt_u32 vcc, 1, v66\ns_and_b64 vcc, vcc, s[28:29]\ns_cbranch_scc1 .Lf1_w\n.Lf1_r:\ns_cmp_lg_u32 s70, 3\ns_cbranch_scc1 .Lf1_n\n"
"buffer_inv sc1\n.Lf1_n:\ns_lshr_b32 s88, %19, 2\ns_mul_i32 s68, s88, 917504\ns_add_u32 s48, %8, s68\ns_addc_u32 s49, %9, 0\ns_mul_i32 s68, s88, 2621440\ns_and_b32 s69, %19, 3\n"
"s_mul_i32 s69, s69, 320\ns_add_u32 s68, s68, s69\ns_sub_u32 s88, 5246976, s68\ns_add_u32 s68, %15, s68\ns_addc_u32 s69, %16, 0\ns_cmp_lt_u32 s70, 2\ns_cselect_b32 s52, s48, s68\ns_cselect_b32 s53, s49, s69\n"
"s_cselect_b32 s54, 917504, s88\ns_cmp_lt_u32 s70, 3\ns_cselect_b32 s48, s48, s68\ns_cselect_b32 s49, s49, s69\ns_cselect_b32 s50, 917504, s88\ns_and_b32 s49, s49, 0xffff\ns_and_b32 s53, s53, 0xffff\ns_add_u32 s71, s71, 458752\n"
"s_mov_b32 s62, s68\ns_and_b32 s63, s69, 0xffff\ns_mov_b32 s86, s88\ns_mul_i32 s68, s70, 6\ns_add_u32 s68, s68, 3\ns_sub_u32 s69, s68, 3\ns_lshl_b32 s69, s69, 10\nv_add_u32 v66, s69, v60\n"
"s_sub_u32 s69, s68, 14\ns_lshl_b32 s69, s69, 2\ns_and_b32 s69, s69, 63\nv_lshrrev_b32 v67, 8, v60\nv_add_u32 v67, s69, v67\nv_mul_u32_u24 v68, 205, v67\nv_lshrrev_b32 v68, 11, v68\nv_mul_u32_u24 v61, 10, v68\n"
"v_sub_u32 v67, v67, v61\nv_lshlrev_b32 v67, 5, v67\nv_bfe_u32 v61, v60, 5, 3\nv_lshl_add_u32 v68, v68, 3, v61\nv_mul_u32_u24 v68, 1280, v68\nv_and_b32 v61, 16, v60\nv_add3_u32 v67, v67, v68, v61\nv_add_u32 v67, 1024, v67\n"
"s_cmp_lt_u32 s68, 14\ns_cselect_b64 vcc, -1, 0\nv_cndmask_b32 v61, v67, v66, vcc\ns_mul_i32 s68, s70, 6\ns_add_u32 s68, s68, 4\ns_sub_u32 s69, s68, 0\ns_lshl_b32 s69, s69, 10\nv_add_u32 v66, s69, v60\n"
"s_sub_u32 s69, s68, 14\ns_lshl_b32 s69, s69, 2\ns_and_b32 s69, s69, 63\nv_lshrrev_b32 v67, 8, v60\nv_add_u32 v67, s69, v67\nv_mul_u32_u24 v68, 205, v67\nv_lshrrev_b32 v68, 11, v68\nv_mul_u32_u24 v62, 10, v68\n"
"v_sub_u32 v67, v67, v62\nv_lshlrev_b32 v67, 5, v67\nv_bfe_u32 v62, v60, 5, 3\nv_lshl_add_u32 v68, v68, 3, v62\nv_mul_u32_u24 v68, 1280, v68\nv_and_b32 v62, 16, v60\nv_add3_u32 v67, v67, v68, v62\nv_add_u32 v67, 4096, v67\n"
"s_cmp_lt_u32 s68, 14\ns_cselect_b64 vcc, -1, 0\nv_cndmask_b32 v62, v67, v66, vcc\ns_mul_i32 s68, s70, 6\ns_add_u32 s68, s68, 5\ns_sub_u32 s69, s68, 1\ns_lshl_b32 s69, s69, 10\nv_add_u32 v66, s69, v60\n"
"s_sub_u32 s69, s68, 14\ns_lshl_b32 s69, s69, 2\ns_and_b32 s69, s69, 63\nv_lshrrev_b32 v67, 8, v60\nv_add_u32 v67, s69, v67\nv_mul_u32_u24 v68, 205, v67\nv_lshrrev_b32 v68, 11, v68\nv_mul_u32_u24 v63, 10, v68\n"
"v_sub_u32 v67, v67, v63\nv_lshlrev_b32 v67, 5, v67\nv_bfe_u32 v63, v60, 5, 3\nv_lshl_add_u32 v68, v68, 3, v63\nv_mul_u32_u24 v68, 1280, v68\nv_and_b32 v63, 16, v60\nv_add3_u32 v67, v67, v68, v63\nv_add_u32 v67, 3072, v67\n"
"s_cmp_lt_u32 s68, 14\ns_cselect_b64 vcc, -1, 0\nv_cndmask_b32 v63, v67, v66, vcc\ns_branch .Lhookret\n.Lf1_w:\ns_mov_b32 s97, 0\n.Lf1_l:\ns_sleep 1\n"
"buffer_load_dword v78, v76, s[24:27], 0 offen sc1\ns_waitcnt vmcnt(0)\ns_add_u32 s97, s97, 1\ns_cmp_gt_u32 s97, 1048576\ns_cbranch_scc1 .Lf1_r\nv_sub_u32 v66, v78, v65\nv_cmp_gt_u32 vcc, 1, v66\ns_and_b64 vcc, vcc, s[28:29]\n"
"s_cbranch_scc1 .Lf1_l\ns_branch .Lf1_r\n.Lfb:\ns_waitcnt vmcnt(0) lgkmcnt(0)\nv_lshrrev_b32 v68, 4, v60\nv_and_b32 v69, 15, v68\nv_lshrrev_b32 v68, 4, v68\nv_lshlrev_b32 v66, 8, v69\n"
"v_lshl_add_u32 v66, v68, 4, v66\nv_add_u32 v66, 1024, v66\nv_lshlrev_b32 v67, 10, v68\nv_lshl_add_u32 v67, v69, 1, v67\nv_add_u32 v67, 1024, v67\nv_mov_b32 v24, 0xff800000\nv_mov_b32 v28, 0xff800000\nv_mov_b32 v32, 0xff800000\n"
"v_mov_b32 v36, 0xff800000\nv_mov_b32 v40, 0xff800000\nv_mov_b32 v44, 0xff800000\nv_mov_b32 v48, 0xff800000\ns_lshl_b32 s95, s67, 21\ns_mov_b32 s66, 0\n.Lfb1:\ns_add_u32 s68, s95, 4096\n"
"buffer_load_dwordx4 a[112:115], v66, s[40:43], s95 offen offset:0\nbuffer_load_dwordx4 a[116:119], v66, s[40:43], s95 offen offset:64\nbuffer_load_dwordx4 a[120:123], v66, s[40:43], s95 offen offset:128\nbuffer_load_dwordx4 a[124:127], v66, s[40:43], s95 offen offset:192\nbuffer_load_dwordx4 a[128:131], v66, s[40:43], s68 offen offset:0\nbuffer_load_dwordx4 a[132:135], v66, s[40:43], s68 offen offset:64\nbuffer_load_dwordx4 a[136:139], v66, s[40:43], s68 offen offset:128\nbuffer_load_dwordx4 a[140:143], v66, s[40:43], s68 offen offset:192\n"
"s_waitcnt vmcnt(0)\nQzvaa 0,112,0\nQmvaa 0,116,4\nQmvaa 0,120,8\nQmvaa 0,124,12\nQzvaa 4,128,0\nQmvaa 4,132,4\nQmvaa 4,136,8\n"
"Qmvaa 4,140,12\ns_nop 7\nv_max3_f32 v24, v24, v0, v1\nv_max3_f32 v24, v24, v2, v3\nv_max3_f32 v24, v24, v4, v5\nv_max3_f32 v24, v24, v6, v7\nQzvaa 0,112,16\nQmvaa 0,116,20\n"
"Qmvaa 0,120,24\nQmvaa 0,124,28\nQzvaa 4,128,16\nQmvaa 4,132,20\nQmvaa 4,136,24\nQmvaa 4,140,28\ns_nop 7\nv_max3_f32 v28, v28, v0, v1\n"
"v_max3_f32 v28, v28, v2, v3\nv_max3_f32 v28, v28, v4, v5\nv_max3_f32 v28, v28, v6, v7\nQzvaa 0,112,32\nQmvaa 0,116,36\nQmvaa 0,120,40\nQmvaa 0,124,44\nQzvaa 4,128,32\n"
"Qmvaa 4,132,36\nQmvaa 4,136,40\nQmvaa 4,140,44\ns_nop 7\nv_max3_f32 v32, v32, v0, v1\nv_max3_f32 v32, v32, v2, v3\nv_max3_f32 v32, v32, v4, v5\nv_max3_f32 v32, v32, v6, v7\n"
"Qzvaa 0,112,48\nQmvaa 0,116,52\nQmvaa 0,120,56\nQmvaa 0,124,60\nQzvaa 4,128,48\nQmvaa 4,132,52\nQmvaa 4,136,56\nQmvaa 4,140,60\n"
"s_nop 7\nv_max3_f32 v36, v36, v0, v1\nv_max3_f32 v36, v36, v2, v3\nv_max3_f32 v36, v36, v4, v5\nv_max3_f32 v36, v36, v6, v7\nQzvaa 0,112,64\nQmvaa 0,116,68\nQmvaa 0,120,72\n"
"Qmvaa 0,124,76\nQzvaa 4,128,64\nQmvaa 4,132,68\nQmvaa 4,136,72\nQmvaa 4,140,76\ns_nop 7\nv_max3_f32 v40, v40, v0, v1\nv_max3_f32 v40, v40, v2, v3\n"
"v_max3_f32 v40, v40, v4, v5\nv_max3_f32 v40, v40, v6, v7\nQzvaa 0,112,80\nQmvaa 0,116,84\nQmvaa 0,120,88\nQmvaa 0,124,92\nQzvaa 4,128,80\nQmvaa 4,132,84\n"
"Qmvaa 4,136,88\nQmvaa 4,140,92\ns_nop 7\nv_max3_f32 v44, v44, v0, v1\nv_max3_f32 v44, v44, v2, v3\nv_max3_f32 v44, v44, v4, v5\nv_max3_f32 v44, v44, v6, v7\nQzvaa 0,112,96\n"
"Qmvaa 0,116,100\nQmvaa 0,120,104\nQmvaa 0,124,108\nQzvaa 4,128,96\nQmvaa 4,132,100\nQmvaa 4,136,104\nQmvaa 4,140,108\ns_nop 7\n"
"v_max3_f32 v48, v48, v0, v1\nv_max3_f32 v48, v48, v2, v3\nv_max3_f32 v48, v48, v4, v5\nv_max3_f32 v48, v48, v6, v7\ns_add_u32 s95, s95, 8192\ns_add_u32 s66, s66, 1\ns_cmp_lt_u32 s66, 32\ns_cbranch_scc1 .Lfb1\n"
"v_mov_b32 v25, v24\ns_nop 1\nv_permlane32_swap_b32 v24, v25\nv_max_f32 v24, v24, v25\nv_mov_b32 v25, v24\ns_nop 1\nv_permlane16_swap_b32 v24, v25\nv_max_f32 v24, v24, v25\n"
"v_sub_f32 v24, 0, v24\nv_mov_b32 v25, v24\nv_mov_b32 v26, v24\nv_mov_b32 v27, v24\nv_mov_b32 v29, v28\ns_nop 1\nv_permlane32_swap_b32 v28, v29\nv_max_f32 v28, v28, v29\n"
"v_mov_b32 v29, v28\ns_nop 1\nv_permlane16_swap_b32 v28, v29\nv_max_f32 v28, v28, v29\nv_sub_f32 v28, 0, v28\nv_mov_b32 v29, v28\nv_mov_b32 v30, v28\nv_mov_b32 v31, v28\n"
"v_mov_b32 v33, v32\ns_nop 1\nv_permlane32_swap_b32 v32, v33\nv_max_f32 v32, v32, v33\nv_mov_b32 v33, v32\ns_nop 1\nv_permlane16_swap_b32 v32, v33\nv_max_f32 v32, v32, v33\n"
"v_sub_f32 v32, 0, v32\nv_mov_b32 v33, v32\nv_mov_b32 v34, v32\nv_mov_b32 v35, v32\nv_mov_b32 v37, v36\ns_nop 1\nv_permlane32_swap_b32 v36, v37\nv_max_f32 v36, v36, v37\n"
"v_mov_b32 v37, v36\ns_nop 1\nv_permlane16_swap_b32 v36, v37\nv_max_f32 v36, v36, v37\nv_sub_f32 v36, 0, v36\nv_mov_b32 v37, v36\nv_mov_b32 v38, v36\nv_mov_b32 v39, v36\n"
"v_mov_b32 v41, v40\ns_nop 1\nv_permlane32_swap_b32 v40, v41\nv_max_f32 v40, v40, v41\nv_mov_b32 v41, v40\ns_nop 1\nv_permlane16_swap_b32 v40, v41\nv_max_f32 v40, v40, v41\n"
"v_sub_f32 v40, 0, v40\nv_mov_b32 v41, v40\nv_mov_b32 v42, v40\nv_mov_b32 v43, v40\nv_mov_b32 v45, v44\ns_nop 1\nv_permlane32_swap_b32 v44, v45\nv_max_f32 v44, v44, v45\n"
"v_mov_b32 v45, v44\ns_nop 1\nv_permlane16_swap_b32 v44, v45\nv_max_f32 v44, v44, v45\nv_sub_f32 v44, 0, v44\nv_mov_b32 v45, v44\nv_mov_b32 v46, v44\nv_mov_b32 v47, v44\n"
"v_mov_b32 v49, v48\ns_nop 1\nv_permlane32_swap_b32 v48, v49\nv_max_f32 v48, v48, v49\nv_mov_b32 v49, v48\ns_nop 1\nv_permlane16_swap_b32 v48, v49\nv_max_f32 v48, v48, v49\n"
"v_sub_f32 v48, 0, v48\nv_mov_b32 v49, v48\nv_mov_b32 v50, v48\nv_mov_b32 v51, v48\nv_mov_b32 v68, 0\nv_mov_b32 v80, 0\nv_mov_b32 v81, 0\nv_mov_b32 v82, 0\n"
"v_mov_b32 v83, 0\nv_mov_b32 v84, 0\nv_mov_b32 v85, 0\nv_mov_b32 v86, 0\nv_mov_b32 v87, 0\nv_mov_b32 v88, 0\nv_mov_b32 v89, 0\nv_mov_b32 v90, 0\n"
"v_mov_b32 v91, 0\nv_mov_b32 v92, 0\nv_mov_b32 v93, 0\nv_mov_b32 v94, 0\nv_mov_b32 v95, 0\nv_mov_b32 v96, 0\nv_mov_b32 v97, 0\nv_mov_b32 v98, 0\n"
"v_mov_b32 v99, 0\nv_mov_b32 v100, 0\nv_mov_b32 v101, 0\nv_mov_b32 v102, 0\nv_mov_b32 v103, 0\nv_mov_b32 v104, 0\nv_mov_b32 v105, 0\nv_mov_b32 v106, 0\n"
"v_mov_b32 v107, 0\nv_mov_b32 v108, 0\nv_mov_b32 v109, 0\nv_mov_b32 v110, 0\nv_mov_b32 v111, 0\nv_accvgpr_write_b32 a176, v68\nv_accvgpr_write_b32 a177, v68\nv_accvgpr_write_b32 a178, v68\n"
"v_accvgpr_write_b32 a179, v68\nv_mov_b32 v112, 0\nv_mov_b32 v113, 0\nv_mov_b32 v114, 0\nv_mov_b32 v115, 0\nv_mov_b32 v116, 0\nv_mov_b32 v117, 0\nv_mov_b32 v118, 0\n"
"v_mov_b32 v119, 0\nv_mov_b32 v120, 0\nv_mov_b32 v121, 0\nv_mov_b32 v122, 0\nv_mov_b32 v123, 0\nv_mov_b32 v124, 0\nv_mov_b32 v125, 0\nv_mov_b32 v126, 0\n"
"v_mov_b32 v127, 0\nv_mov_b32 v128, 0\nv_mov_b32 v129, 0\nv_mov_b32 v130, 0\nv_mov_b32 v131, 0\nv_mov_b32 v132, 0\nv_mov_b32 v133, 0\nv_mov_b32 v134, 0\n"
"v_mov_b32 v135, 0\nv_mov_b32 v136, 0\nv_mov_b32 v137, 0\nv_mov_b32 v138, 0\nv_mov_b32 v139, 0\nv_mov_b32 v140, 0\nv_mov_b32 v141, 0\nv_mov_b32 v142, 0\n"
"v_mov_b32 v143, 0\nv_accvgpr_write_b32 a180, v68\nv_accvgpr_write_b32 a181, v68\nv_accvgpr_write_b32 a182, v68\nv_accvgpr_write_b32 a183, v68\nv_mov_b32 v144, 0\nv_mov_b32 v145, 0\nv_mov_b32 v146, 0\n"
"v_mov_b32 v147, 0\nv_mov_b32 v148, 0\nv_mov_b32 v149, 0\nv_mov_b32 v150, 0\nv_mov_b32 v151, 0\nv_mov_b32 v152, 0\nv_mov_b32 v153, 0\nv_mov_b32 v154, 0\n"
"v_mov_b32 v155, 0\nv_mov_b32 v156, 0\nv_mov_b32 v157, 0\nv_mov_b32 v158, 0\nv_mov_b32 v159, 0\nv_mov_b32 v160, 0\nv_mov_b32 v161, 0\nv_mov_b32 v162, 0\n"
"v_mov_b32 v163, 0\nv_mov_b32 v164, 0\nv_mov_b32 v165, 0\nv_mov_b32 v166, 0\nv_mov_b32 v167, 0\nv_mov_b32 v168, 0\nv_mov_b32 v169, 0\nv_mov_b32 v170, 0\n"
"v_mov_b32 v171, 0\nv_mov_b32 v172, 0\nv_mov_b32 v173, 0\nv_mov_b32 v174, 0\nv_mov_b32 v175, 0\nv_accvgpr_write_b32 a184, v68\nv_accvgpr_write_b32 a185, v68\nv_accvgpr_write_b32 a186, v68\n"
"v_accvgpr_write_b32 a187, v68\nv_mov_b32 v176, 0\nv_mov_b32 v177, 0\nv_mov_b32 v178, 0\nv_mov_b32 v179, 0\nv_mov_b32 v180, 0\nv_mov_b32 v181, 0\nv_mov_b32 v182, 0\n"
"v_mov_b32 v183, 0\nv_mov_b32 v184, 0\nv_mov_b32 v185, 0\nv_mov_b32 v186, 0\nv_mov_b32 v187, 0\nv_mov_b32 v188, 0\nv_mov_b32 v189, 0\nv_mov_b32 v190, 0\n"
"v_mov_b32 v191, 0\nv_mov_b32 v192, 0\nv_mov_b32 v193, 0\nv_mov_b32 v194, 0\nv_mov_b32 v195, 0\nv_mov_b32 v196, 0\nv_mov_b32 v197, 0\nv_mov_b32 v198, 0\n"
"v_mov_b32 v199, 0\nv_mov_b32 v200, 0\nv_mov_b32 v201, 0\nv_mov_b32 v202, 0\nv_mov_b32 v203, 0\nv_mov_b32 v204, 0\nv_mov_b32 v205, 0\nv_mov_b32 v206, 0\n"
"v_mov_b32 v207, 0\nv_accvgpr_write_b32 a188, v68\nv_accvgpr_write_b32 a189, v68\nv_accvgpr_write_b32 a190, v68\nv_accvgpr_write_b32 a191, v68\nv_mov_b32 v208, 0\nv_mov_b32 v209, 0\nv_mov_b32 v210, 0\n"
"v_mov_b32 v211, 0\nv_mov_b32 v212, 0\nv_mov_b32 v213, 0\nv_mov_b32 v214, 0\nv_mov_b32 v215, 0\nv_mov_b32 v216, 0\nv_mov_b32 v217, 0\nv_mov_b32 v218, 0\n"
"v_mov_b32 v219, 0\nv_mov_b32 v220, 0\nv_mov_b32 v221, 0\nv_mov_b32 v222, 0\nv_mov_b32 v223, 0\nv_mov_b32 v224, 0\nv_mov_b32 v225, 0\nv_mov_b32 v226, 0\n"
"v_mov_b32 v227, 0\nv_mov_b32 v228, 0\nv_mov_b32 v229, 0\nv_mov_b32 v230, 0\nv_mov_b32 v231, 0\nv_mov_b32 v232, 0\nv_mov_b32 v233, 0\nv_mov_b32 v234, 0\n"
"v_mov_b32 v235, 0\nv_mov_b32 v236, 0\nv_mov_b32 v237, 0\nv_mov_b32 v238, 0\nv_mov_b32 v239, 0\nv_accvgpr_write_b32 a192, v68\nv_accvgpr_write_b32 a193, v68\nv_accvgpr_write_b32 a194, v68\n"
"v_accvgpr_write_b32 a195, v68\nv_mov_b32 v240, 0\nv_mov_b32 v241, 0\nv_mov_b32 v242, 0\nv_mov_b32 v243, 0\nv_mov_b32 v244, 0\nv_mov_b32 v245, 0\nv_mov_b32 v246, 0\n"
"v_mov_b32 v247, 0\nv_mov_b32 v248, 0\nv_mov_b32 v249, 0\nv_mov_b32 v250, 0\nv_mov_b32 v251, 0\nv_mov_b32 v252, 0\nv_mov_b32 v253, 0\nv_mov_b32 v254, 0\n"
"v_mov_b32 v255, 0\nv_accvgpr_write_b32 a208, v68\nv_accvgpr_write_b32 a209, v68\nv_accvgpr_write_b32 a210, v68\nv_accvgpr_write_b32 a211, v68\nv_accvgpr_write_b32 a212, v68\nv_accvgpr_write_b32 a213, v68\nv_accvgpr_write_b32 a214, v68\n"
"v_accvgpr_write_b32 a215, v68\nv_accvgpr_write_b32 a216, v68\nv_accvgpr_write_b32 a217, v68\nv_accvgpr_write_b32 a218, v68\nv_accvgpr_write_b32 a219, v68\nv_accvgpr_write_b32 a220, v68\nv_accvgpr_write_b32 a221, v68\nv_accvgpr_write_b32 a222, v68\n"
"v_accvgpr_write_b32 a223, v68\nv_accvgpr_write_b32 a196, v68\nv_accvgpr_write_b32 a197, v68\nv_accvgpr_write_b32 a198, v68\nv_accvgpr_write_b32 a199, v68\nv_accvgpr_write_b32 a224, v68\nv_accvgpr_write_b32 a225, v68\nv_accvgpr_write_b32 a226, v68\n"
"v_accvgpr_write_b32 a227, v68\nv_accvgpr_write_b32 a228, v68\nv_accvgpr_write_b32 a229, v68\nv_accvgpr_write_b32 a230, v68\nv_accvgpr_write_b32 a231, v68\nv_accvgpr_write_b32 a232, v68\nv_accvgpr_write_b32 a233, v68\nv_accvgpr_write_b32 a234, v68\n"
"v_accvgpr_write_b32 a235, v68\nv_accvgpr_write_b32 a236, v68\nv_accvgpr_write_b32 a237, v68\nv_accvgpr_write_b32 a238, v68\nv_accvgpr_write_b32 a239, v68\nv_accvgpr_write_b32 a240, v68\nv_accvgpr_write_b32 a241, v68\nv_accvgpr_write_b32 a242, v68\n"
"v_accvgpr_write_b32 a243, v68\nv_accvgpr_write_b32 a244, v68\nv_accvgpr_write_b32 a245, v68\nv_accvgpr_write_b32 a246, v68\nv_accvgpr_write_b32 a247, v68\nv_accvgpr_write_b32 a248, v68\nv_accvgpr_write_b32 a249, v68\nv_accvgpr_write_b32 a250, v68\n"
"v_accvgpr_write_b32 a251, v68\nv_accvgpr_write_b32 a252, v68\nv_accvgpr_write_b32 a253, v68\nv_accvgpr_write_b32 a254, v68\nv_accvgpr_write_b32 a255, v68\nv_accvgpr_write_b32 a200, v68\nv_accvgpr_write_b32 a201, v68\nv_accvgpr_write_b32 a202, v68\n"
"v_accvgpr_write_b32 a203, v68\ns_lshl_b32 s95, s67, 21\ns_mov_b32 s66, 0\n.Lfb2:\ns_add_u32 s68, s95, 4096\nbuffer_load_dwordx4 a[112:115], v66, s[40:43], s95 offen offset:0\nbuffer_load_dwordx4 a[116:119], v66, s[40:43], s95 offen offset:64\nbuffer_load_dwordx4 a[120:123], v66, s[40:43], s95 offen offset:128\n"
"buffer_load_dwordx4 a[124:127], v66, s[40:43], s95 offen offset:192\nbuffer_load_dwordx4 a[128:131], v66, s[40:43], s68 offen offset:0\nbuffer_load_dwordx4 a[132:135], v66, s[40:43], s68 offen offset:64\nbuffer_load_dwordx4 a[136:139], v66, s[40:43], s68 offen offset:128\nbuffer_load_dwordx4 a[140:143], v66, s[40:43], s68 offen offset:192\nbuffer_load_ushort v8, v67, s[44:47], s95 offen offset:0\nbuffer_load_ushort v9, v67, s[44:47], s95 offen offset:256\nbuffer_load_ushort v10, v67, s[44:47], s95 offen offset:512\n"
"buffer_load_ushort v11, v67, s[44:47], s95 offen offset:768\nbuffer_load_ushort v12, v67, s[44:47], s68 offen offset:0\nbuffer_load_ushort v13, v67, s[44:47], s68 offen offset:256\nbuffer_load_ushort v14, v67, s[44:47], s68 offen offset:512\nbuffer_load_ushort v15, v67, s[44:47], s68 offen offset:768\ns_waitcnt vmcnt(0)\nv_lshl_or_b32 v8, v9, 16, v8\nv_accvgpr_write_b32 a144, v8\n"
"v_lshl_or_b32 v10, v11, 16, v10\nv_accvgpr_write_b32 a145, v10\nv_lshl_or_b32 v12, v13, 16, v12\nv_accvgpr_write_b32 a146, v12\nv_lshl_or_b32 v14, v15, 16, v14\nv_accvgpr_write_b32 a147, v14\nbuffer_load_ushort v8, v67, s[44:47], s95 offen offset:32\nbuffer_load_ushort v9, v67, s[44:47], s95 offen offset:288\n"
"buffer_load_ushort v10, v67, s[44:47], s95 offen offset:544\nbuffer_load_ushort v11, v67, s[44:47], s95 offen offset:800\nbuffer_load_ushort v12, v67, s[44:47], s68 offen offset:32\nbuffer_load_ushort v13, v67, s[44:47], s68 offen offset:288\nbuffer_load_ushort v14, v67, s[44:47], s68 offen offset:544\nbuffer_load_ushort v15, v67, s[44:47], s68 offen offset:800\ns_waitcnt vmcnt(0)\nv_lshl_or_b32 v8, v9, 16, v8\n"
"v_accvgpr_write_b32 a148, v8\nv_lshl_or_b32 v10, v11, 16, v10\nv_accvgpr_write_b32 a149, v10\nv_lshl_or_b32 v12, v13, 16, v12\nv_accvgpr_write_b32 a150, v12\nv_lshl_or_b32 v14, v15, 16, v14\nv_accvgpr_write_b32 a151, v14\nbuffer_load_ushort v8, v67, s[44:47], s95 offen offset:64\n"
"buffer_load_ushort v9, v67, s[44:47], s95 offen offset:320\nbuffer_load_ushort v10, v67, s[44:47], s95 offen offset:576\nbuffer_load_ushort v11, v67, s[44:47], s95 offen offset:832\nbuffer_load_ushort v12, v67, s[44:47], s68 offen offset:64\nbuffer_load_ushort v13, v67, s[44:47], s68 offen offset:320\nbuffer_load_ushort v14, v67, s[44:47], s68 offen offset:576\nbuffer_load_ushort v15, v67, s[44:47], s68 offen offset:832\ns_waitcnt vmcnt(0)\n"
"v_lshl_or_b32 v8, v9, 16, v8\nv_accvgpr_write_b32 a152, v8\nv_lshl_or_b32 v10, v11, 16, v10\nv_accvgpr_write_b32 a153, v10\nv_lshl_or_b32 v12, v13, 16, v12\nv_accvgpr_write_b32 a154, v12\nv_lshl_or_b32 v14, v15, 16, v14\nv_accvgpr_write_b32 a155, v14\n"
"buffer_load_ushort v8, v67, s[44:47], s95 offen offset:96\nbuffer_load_ushort v9, v67, s[44:47], s95 offen offset:352\nbuffer_load_ushort v10, v67, s[44:47], s95 offen offset:608\nbuffer_load_ushort v11, v67, s[44:47], s95 offen offset:864\nbuffer_load_ushort v12, v67, s[44:47], s68 offen offset:96\nbuffer_load_ushort v13, v67, s[44:47], s68 offen offset:352\nbuffer_load_ushort v14, v67, s[44:47], s68 offen offset:608\nbuffer_load_ushort v15, v67, s[44:47], s68 offen offset:864\n"
"s_waitcnt vmcnt(0)\nv_lshl_or_b32 v8, v9, 16, v8\nv_accvgpr_write_b32 a156, v8\nv_lshl_or_b32 v10, v11, 16, v10\nv_accvgpr_write_b32 a157, v10\nv_lshl_or_b32 v12, v13, 16, v12\nv_accvgpr_write_b32 a158, v12\nv_lshl_or_b32 v14, v15, 16, v14\n"
"v_accvgpr_write_b32 a159, v14\nbuffer_load_ushort v8, v67, s[44:47], s95 offen offset:128\nbuffer_load_ushort v9, v67, s[44:47], s95 offen offset:384\nbuffer_load_ushort v10, v67, s[44:47], s95 offen offset:640\nbuffer_load_ushort v11, v67, s[44:47], s95 offen offset:896\nbuffer_load_ushort v12, v67, s[44:47], s68 offen offset:128\nbuffer_load_ushort v13, v67, s[44:47], s68 offen offset:384\nbuffer_load_ushort v14, v67, s[44:47], s68 offen offset:640\n"
"buffer_load_ushort v15, v67, s[44:47], s68 offen offset:896\ns_waitcnt vmcnt(0)\nv_lshl_or_b32 v8, v9, 16, v8\nv_accvgpr_write_b32 a160, v8\nv_lshl_or_b32 v10, v11, 16, v10\nv_accvgpr_write_b32 a161, v10\nv_lshl_or_b32 v12, v13, 16, v12\nv_accvgpr_write_b32 a162, v12\n"
"v_lshl_or_b32 v14, v15, 16, v14\nv_accvgpr_write_b32 a163, v14\nbuffer_load_ushort v8, v67, s[44:47], s95 offen offset:160\nbuffer_load_ushort v9, v67, s[44:47], s95 offen offset:416\nbuffer_load_ushort v10, v67, s[44:47], s95 offen offset:672\nbuffer_load_ushort v11, v67, s[44:47], s95 offen offset:928\nbuffer_load_ushort v12, v67, s[44:47], s68 offen offset:160\nbuffer_load_ushort v13, v67, s[44:47], s68 offen offset:416\n"
"buffer_load_ushort v14, v67, s[44:47], s68 offen offset:672\nbuffer_load_ushort v15, v67, s[44:47], s68 offen offset:928\ns_waitcnt vmcnt(0)\nv_lshl_or_b32 v8, v9, 16, v8\nv_accvgpr_write_b32 a164, v8\nv_lshl_or_b32 v10, v11, 16, v10\nv_accvgpr_write_b32 a165, v10\nv_lshl_or_b32 v12, v13, 16, v12\n"
"v_accvgpr_write_b32 a166, v12\nv_lshl_or_b32 v14, v15, 16, v14\nv_accvgpr_write_b32 a167, v14\nbuffer_load_ushort v8, v67, s[44:47], s95 offen offset:192\nbuffer_load_ushort v9, v67, s[44:47], s95 offen offset:448\nbuffer_load_ushort v10, v67, s[44:47], s95 offen offset:704\nbuffer_load_ushort v11, v67, s[44:47], s95 offen offset:960\nbuffer_load_ushort v12, v67, s[44:47], s68 offen offset:192\n"
"buffer_load_ushort v13, v67, s[44:47], s68 offen offset:448\nbuffer_load_ushort v14, v67, s[44:47], s68 offen offset:704\nbuffer_load_ushort v15, v67, s[44:47], s68 offen offset:960\ns_waitcnt vmcnt(0)\nv_lshl_or_b32 v8, v9, 16, v8\nv_accvgpr_write_b32 a168, v8\nv_lshl_or_b32 v10, v11, 16, v10\nv_accvgpr_write_b32 a169, v10\n"
"v_lshl_or_b32 v12, v13, 16, v12\nv_accvgpr_write_b32 a170, v12\nv_lshl_or_b32 v14, v15, 16, v14\nv_accvgpr_write_b32 a171, v14\nbuffer_load_ushort v8, v67, s[44:47], s95 offen offset:224\nbuffer_load_ushort v9, v67, s[44:47], s95 offen offset:480\nbuffer_load_ushort v10, v67, s[44:47], s95 offen offset:736\nbuffer_load_ushort v11, v67, s[44:47], s95 offen offset:992\n"
"buffer_load_ushort v12, v67, s[44:47], s68 offen offset:224\nbuffer_load_ushort v13, v67, s[44:47], s68 offen offset:480\nbuffer_load_ushort v14, v67, s[44:47], s68 offen offset:736\nbuffer_load_ushort v15, v67, s[44:47], s68 offen offset:992\ns_waitcnt vmcnt(0)\nv_lshl_or_b32 v8, v9, 16, v8\nv_accvgpr_write_b32 a172, v8\nv_lshl_or_b32 v10, v11, 16, v10\n"
"v_accvgpr_write_b32 a173, v10\nv_lshl_or_b32 v12, v13, 16, v12\nv_accvgpr_write_b32 a174, v12\nv_lshl_or_b32 v14, v15, 16, v14\nv_accvgpr_write_b32 a175, v14\nQcvaa 0,112,0,24\nQmvaa 0,116,4\nQmvaa 0,120,8\n"
"Qmvaa 0,124,12\nQcvaa 4,128,0,24\nQmvaa 4,132,4\nQmvaa 4,136,8\nQmvaa 4,140,12\ns_nop 7\nQe 0\nQe 1\n"
"Qe 2\nQe 3\nQe 4\nQe 5\nQe 6\nQe 7\ns_nop 0\nQk 16,0,1\n"
"Qk 17,2,3\nQk 18,4,5\nQk 19,6,7\ns_nop 1\nQmvav 80,144,16\nQmvav 84,148,16\nQmvav 88,152,16\nQmvav 92,156,16\n"
"Qmvav 96,160,16\nQmvav 100,164,16\nQmvav 104,168,16\nQmvav 108,172,16\nQmaav 176,204,16\nQcvaa 0,112,16,28\nQmvaa 0,116,20\nQmvaa 0,120,24\n"
"Qmvaa 0,124,28\nQcvaa 4,128,16,28\nQmvaa 4,132,20\nQmvaa 4,136,24\nQmvaa 4,140,28\ns_nop 7\nQe 0\nQe 1\n"
"Qe 2\nQe 3\nQe 4\nQe 5\nQe 6\nQe 7\ns_nop 0\nQk 16,0,1\n"
"Qk 17,2,3\nQk 18,4,5\nQk 19,6,7\ns_nop 1\nQmvav 112,144,16\nQmvav 116,148,16\nQmvav 120,152,16\nQmvav 124,156,16\n"
"Qmvav 128,160,16\nQmvav 132,164,16\nQmvav 136,168,16\nQmvav 140,172,16\nQmaav 180,204,16\nQcvaa 0,112,32,32\nQmvaa 0,116,36\nQmvaa 0,120,40\n"
"Qmvaa 0,124,44\nQcvaa 4,128,32,32\nQmvaa 4,132,36\nQmvaa 4,136,40\nQmvaa 4,140,44\ns_nop 7\nQe 0\nQe 1\n"
"Qe 2\nQe 3\nQe 4\nQe 5\nQe 6\nQe 7\ns_nop 0\nQk 16,0,1\n"
"Qk 17,2,3\nQk 18,4,5\nQk 19,6,7\ns_nop 1\nQmvav 144,144,16\nQmvav 148,148,16\nQmvav 152,152,16\nQmvav 156,156,16\n"
"Qmvav 160,160,16\nQmvav 164,164,16\nQmvav 168,168,16\nQmvav 172,172,16\nQmaav 184,204,16\nQcvaa 0,112,48,36\nQmvaa 0,116,52\nQmvaa 0,120,56\n"
"Qmvaa 0,124,60\nQcvaa 4,128,48,36\nQmvaa 4,132,52\nQmvaa 4,136,56\nQmvaa 4,140,60\ns_nop 7\nQe 0\nQe 1\n"
"Qe 2\nQe 3\nQe 4\nQe 5\nQe 6\nQe 7\ns_nop 0\nQk 16,0,1\n"
"Qk 17,2,3\nQk 18,4,5\nQk 19,6,7\ns_nop 1\nQmvav 176,144,16\nQmvav 180,148,16\nQmvav 184,152,16\nQmvav 188,156,16\n"
"Qmvav 192,160,16\nQmvav 196,164,16\nQmvav 200,168,16\nQmvav 204,172,16\nQmaav 188,204,16\nQcvaa 0,112,64,40\nQmvaa 0,116,68\nQmvaa 0,120,72\n"
"Qmvaa 0,124,76\nQcvaa 4,128,64,40\nQmvaa 4,132,68\nQmvaa 4,136,72\nQmvaa 4,140,76\ns_nop 7\nQe 0\nQe 1\n"
"Qe 2\nQe 3\nQe 4\nQe 5\nQe 6\nQe 7\ns_nop 0\nQk 16,0,1\n"
"Qk 17,2,3\nQk 18,4,5\nQk 19,6,7\ns_nop 1\nQmvav 208,144,16\nQmvav 212,148,16\nQmvav 216,152,16\nQmvav 220,156,16\n"
"Qmvav 224,160,16\nQmvav 228,164,16\nQmvav 232,168,16\nQmvav 236,172,16\nQmaav 192,204,16\nQcvaa 0,112,80,44\nQmvaa 0,116,84\nQmvaa 0,120,88\n"
"Qmvaa 0,124,92\nQcvaa 4,128,80,44\nQmvaa 4,132,84\nQmvaa 4,136,88\nQmvaa 4,140,92\ns_nop 7\nQe 0\nQe 1\n"
"Qe 2\nQe 3\nQe 4\nQe 5\nQe 6\nQe 7\ns_nop 0\nQk 16,0,1\n"
"Qk 17,2,3\nQk 18,4,5\nQk 19,6,7\ns_nop 1\nQmvav 240,144,16\nQmvav 244,148,16\nQmvav 248,152,16\nQmvav 252,156,16\n"
"Qmaav 208,160,16\nQmaav 212,164,16\nQmaav 216,168,16\nQmaav 220,172,16\nQmaav 196,204,16\nQcvaa 0,112,96,48\nQmvaa 0,116,100\nQmvaa 0,120,104\n"
"Qmvaa 0,124,108\nQcvaa 4,128,96,48\nQmvaa 4,132,100\nQmvaa 4,136,104\nQmvaa 4,140,108\ns_nop 7\nQe 0\nQe 1\n"
"Qe 2\nQe 3\nQe 4\nQe 5\nQe 6\nQe 7\ns_nop 0\nQk 16,0,1\n"
"Qk 17,2,3\nQk 18,4,5\nQk 19,6,7\ns_nop 1\nQmaav 224,144,16\nQmaav 228,148,16\nQmaav 232,152,16\nQmaav 236,156,16\n"
"Qmaav 240,160,16\nQmaav 244,164,16\nQmaav 248,168,16\nQmaav 252,172,16\nQmaav 200,204,16\ns_add_u32 s95, s95, 8192\ns_add_u32 s66, s66, 1\ns_cmp_lt_u32 s66, 32\n"
"s_cbranch_scc1 .Lfb2\ns_nop 7\nv_accvgpr_read_b32 v24, a176\nv_accvgpr_read_b32 v28, a180\nv_accvgpr_read_b32 v32, a184\nv_accvgpr_read_b32 v36, a188\nv_accvgpr_read_b32 v40, a192\nv_accvgpr_read_b32 v44, a196\n"
"v_accvgpr_read_b32 v48, a200\ns_cmp_eq_u32 s67, 0\ns_cbranch_scc1 .Lfbret0\ns_branch .Lfbret1\n.Lf2_w:\ns_mov_b32 s97, 0\n.Lf2_l:\ns_sleep 1\n"
"buffer_load_dword v78, v76, s[24:27], 0 offen sc1\ns_waitcnt vmcnt(0)\ns_add_u32 s97, s97, 1\ns_cmp_gt_u32 s97, 1048576\ns_cbranch_scc1 .Lf2_r\nv_sub_u32 v66, v78, v65\nv_cmp_gt_u32 vcc, 2, v66\ns_and_b64 vcc, vcc, s[28:29]\n"
"s_cbranch_scc1 .Lf2_l\ns_branch .Lf2_r\n.Lf3_w:\ns_mov_b32 s97, 0\n.Lf3_l:\ns_sleep 1\nbuffer_load_dword v78, v76, s[24:27], 0 offen sc1\ns_waitcnt vmcnt(0)\n"
"s_add_u32 s97, s97, 1\ns_cmp_gt_u32 s97, 1048576\ns_cbranch_scc1 .Lf3_r\nv_sub_u32 v66, v78, v65\nv_cmp_gt_u32 vcc, 3, v66\ns_and_b64 vcc, vcc, s[28:29]\ns_cbranch_scc1 .Lf3_l\ns_branch .Lf3_r\n"
".Lf4_w:\ns_mov_b32 s97, 0\n.Lf4_l:\ns_sleep 1\nbuffer_load_dword v78, v76, s[24:27], 0 offen sc1\ns_waitcnt vmcnt(0)\ns_add_u32 s97, s97, 1\ns_cmp_gt_u32 s97, 1048576\n"
"s_cbranch_scc1 .Lf4_r\nv_sub_u32 v66, v78, v65\nv_cmp_gt_u32 vcc, 4, v66\ns_and_b64 vcc, vcc, s[28:29]\ns_cbranch_scc1 .Lf4_l\ns_branch .Lf4_r\n.Lf5_w:\ns_mov_b32 s97, 0\n"
".Lf5_l:\ns_sleep 1\nbuffer_load_dword v78, v76, s[24:27], 0 offen sc1\ns_waitcnt vmcnt(0)\ns_add_u32 s97, s97, 1\ns_cmp_gt_u32 s97, 1048576\ns_cbranch_scc1 .Lf5_r\nv_sub_u32 v66, v78, v65\n"
"v_cmp_gt_u32 vcc, 5, v66\ns_and_b64 vcc, vcc, s[28:29]\ns_cbranch_scc1 .Lf5_l\ns_branch .Lf5_r\n.Lsa1:\nds_read_b32 v69, v74\ns_waitcnt lgkmcnt(0)\nv_cmp_gt_u32 vcc, v75, v69\n"
"s_cbranch_vccnz .Lsa1\ns_branch .Lra1\n.Lsp1:\ns_memrealtime s[74:75]\ns_waitcnt lgkmcnt(0)\ns_sub_u32 s75, s77, s74\ns_cmp_lt_u32 s75, 125\ns_cbranch_scc1 .Lsp1\n"
"s_mov_b32 s74, s77\ns_branch .Lrp1\n.Lsa2:\nds_read_b32 v69, v74\ns_waitcnt lgkmcnt(0)\nv_cmp_gt_u32 vcc, v75, v69\ns_cbranch_vccnz .Lsa2\ns_branch .Lra2\n"
".Lsp2:\ns_memrealtime s[74:75]\ns_waitcnt lgkmcnt(0)\ns_sub_u32 s75, s77, s74\ns_cmp_lt_u32 s75, 125\ns_cbranch_scc1 .Lsp2\ns_mov_b32 s74, s77\ns_branch .Lrp2\n"
".Lsa3:\nds_read_b32 v69, v74\ns_waitcnt lgkmcnt(0)\nv_cmp_gt_u32 vcc, v75, v69\ns_cbranch_vccnz .Lsa3\ns_branch .Lra3\n.Lsp3:\ns_memrealtime s[74:75]\n"
"s_waitcnt lgkmcnt(0)\ns_sub_u32 s75, s77, s74\ns_cmp_lt_u32 s75, 125\ns_cbranch_scc1 .Lsp3\ns_mov_b32 s74, s77\ns_branch .Lrp3\n.Lsa4:\nds_read_b32 v69, v74\n"
"s_waitcnt lgkmcnt(0)\nv_cmp_gt_u32 vcc, v75, v69\ns_cbranch_vccnz .Lsa4\ns_branch .Lra4\n.Lsp4:\ns_memrealtime s[74:75]\ns_waitcnt lgkmcnt(0)\ns_sub_u32 s75, s77, s74\n"
"s_cmp_lt_u32 s75, 125\ns_cbranch_scc1 .Lsp4\ns_mov_b32 s74, s77\ns_branch .Lrp4\n.Lsa29:\nds_read_b32 v69, v74\ns_waitcnt lgkmcnt(0)\nv_cmp_gt_u32 vcc, v75, v69\n"
"s_cbranch_vccnz .Lsa29\ns_branch .Lra29\n.Lsa30:\nds_read_b32 v69, v74\ns_waitcnt lgkmcnt(0)\nv_cmp_gt_u32 vcc, v75, v69\ns_cbranch_vccnz .Lsa30\ns_branch .Lra30\n"
".Lsa31:\nds_read_b32 v69, v74\ns_waitcnt lgkmcnt(0)\nv_cmp_gt_u32 vcc, v75, v69\ns_cbranch_vccnz .Lsa31\ns_branch .Lra31\n.Lsa29x:\nds_read_b32 v69, v74\n"
"s_waitcnt lgkmcnt(0)\nv_cmp_gt_u32 vcc, v75, v69\ns_cbranch_vccnz .Lsa29x\ns_branch .Lra29x\n.Lsa30x:\nds_read_b32 v69, v74\ns_waitcnt lgkmcnt(0)\nv_cmp_gt_u32 vcc, v75, v69\n"
"s_cbranch_vccnz .Lsa30x\ns_branch .Lra30x\n.Lsa31x:\nds_read_b32 v69, v74\ns_waitcnt lgkmcnt(0)\nv_cmp_gt_u32 vcc, v75, v69\ns_cbranch_vccnz .Lsa31x\ns_branch .Lra31x\n"
".Lso0:\nds_read_b32 v69, v74\ns_waitcnt lgkmcnt(0)\nv_cmp_gt_u32 vcc, v75, v69\ns_cbranch_vccnz .Lso0\ns_branch .Lro0\n.Lso1:\nds_read_b32 v69, v74\n"
"s_waitcnt lgkmcnt(0)\nv_cmp_gt_u32 vcc, v75, v69\ns_cbranch_vccnz .Lso1\ns_branch .Lro1\n.Lso2:\nds_read_b32 v69, v74\ns_waitcnt lgkmcnt(0)\nv_cmp_gt_u32 vcc, v75, v69\n"
"s_cbranch_vccnz .Lso2\ns_branch .Lro2\n.Lso3:\nds_read_b32 v69, v74\ns_waitcnt lgkmcnt(0)\nv_cmp_gt_u32 vcc, v75, v69\ns_cbranch_vccnz .Lso3\ns_branch .Lro3\n"
".Lso4:\nds_read_b32 v69, v74\ns_waitcnt lgkmcnt(0)\nv_cmp_gt_u32 vcc, v75, v69\ns_cbranch_vccnz .Lso4\ns_branch .Lro4\n.Lso5:\nds_read_b32 v69, v74\n"
"s_waitcnt lgkmcnt(0)\nv_cmp_gt_u32 vcc, v75, v69\ns_cbranch_vccnz .Lso5\ns_branch .Lro5\n.Lso6:\nds_read_b32 v69, v74\ns_waitcnt lgkmcnt(0)\nv_cmp_gt_u32 vcc, v75, v69\n"
"s_cbranch_vccnz .Lso6\ns_branch .Lro6\n.Lso7:\nds_read_b32 v69, v74\ns_waitcnt lgkmcnt(0)\nv_cmp_gt_u32 vcc, v75, v69\ns_cbranch_vccnz .Lso7\ns_branch .Lro7\n"
".Lso8:\nds_read_b32 v69, v74\ns_waitcnt lgkmcnt(0)\nv_cmp_gt_u32 vcc, v75, v69\ns_cbranch_vccnz .Lso8\ns_branch .Lro8\n.Lso9:\nds_read_b32 v69, v74\n"
"s_waitcnt lgkmcnt(0)\nv_cmp_gt_u32 vcc, v75, v69\ns_cbranch_vccnz .Lso9\ns_branch .Lro9\n.Lso10:\nds_read_b32 v69, v74\ns_waitcnt lgkmcnt(0)\nv_cmp_gt_u32 vcc, v75, v69\n"
"s_cbranch_vccnz .Lso10\ns_branch .Lro10\n.Lso11:\nds_read_b32 v69, v74\ns_waitcnt lgkmcnt(0)\nv_cmp_gt_u32 vcc, v75, v69\ns_cbranch_vccnz .Lso11\ns_branch .Lro11\n"
".Lso12:\nds_read_b32 v69, v74\ns_waitcnt lgkmcnt(0)\nv_cmp_gt_u32 vcc, v75, v69\ns_cbranch_vccnz .Lso12\ns_branch .Lro12\n.Lso13:\nds_read_b32 v69, v74\n"
"s_waitcnt lgkmcnt(0)\nv_cmp_gt_u32 vcc, v75, v69\ns_cbranch_vccnz .Lso13\ns_branch .Lro13\n.Lso14:\nds_read_b32 v69, v74\ns_waitcnt lgkmcnt(0)\nv_cmp_gt_u32 vcc, v75, v69\n"
"s_cbranch_vccnz .Lso14\ns_branch .Lro14\n.Lso15:\nds_read_b32 v69, v74\ns_waitcnt lgkmcnt(0)\nv_cmp_gt_u32 vcc, v75, v69\ns_cbranch_vccnz .Lso15\ns_branch .Lro15\n"
".Lso16:\nds_read_b32 v69, v74\ns_waitcnt lgkmcnt(0)\nv_cmp_gt_u32 vcc, v75, v69\ns_cbranch_vccnz .Lso16\ns_branch .Lro16\n.Lso17:\nds_read_b32 v69, v74\n"
"s_waitcnt lgkmcnt(0)\nv_cmp_gt_u32 vcc, v75, v69\ns_cbranch_vccnz .Lso17\ns_branch .Lro17\n.Lso18:\nds_read_b32 v69, v74\ns_waitcnt lgkmcnt(0)\nv_cmp_gt_u32 vcc, v75, v69\n"
"s_cbranch_vccnz .Lso18\ns_branch .Lro18\n.Lso19:\nds_read_b32 v69, v74\ns_waitcnt lgkmcnt(0)\nv_cmp_gt_u32 vcc, v75, v69\ns_cbranch_vccnz .Lso19\ns_branch .Lro19\n"
".Lso20:\nds_read_b32 v69, v74\ns_waitcnt lgkmcnt(0)\nv_cmp_gt_u32 vcc, v75, v69\ns_cbranch_vccnz .Lso20\ns_branch .Lro20\n.Lso21:\nds_read_b32 v69, v74\n"
"s_waitcnt lgkmcnt(0)\nv_cmp_gt_u32 vcc, v75, v69\ns_cbranch_vccnz .Lso21\ns_branch .Lro21\n.Lso22:\nds_read_b32 v69, v74\ns_waitcnt lgkmcnt(0)\nv_cmp_gt_u32 vcc, v75, v69\n"
"s_cbranch_vccnz .Lso22\ns_branch .Lro22\n.Lso23:\nds_read_b32 v69, v74\ns_waitcnt lgkmcnt(0)\nv_cmp_gt_u32 vcc, v75, v69\ns_cbranch_vccnz .Lso23\ns_branch .Lro23\n"
".Lso24:\nds_read_b32 v69, v74\ns_waitcnt lgkmcnt(0)\nv_cmp_gt_u32 vcc, v75, v69\ns_cbranch_vccnz .Lso24\ns_branch .Lro24\n.Lso25:\nds_read_b32 v69, v74\n"
"s_waitcnt lgkmcnt(0)\nv_cmp_gt_u32 vcc, v75, v69\ns_cbranch_vccnz .Lso25\ns_branch .Lro25\n.Lso26:\nds_read_b32 v69, v74\ns_waitcnt lgkmcnt(0)\nv_cmp_gt_u32 vcc, v75, v69\n"
"s_cbranch_vccnz .Lso26\ns_branch .Lro26\n.Lso27:\nds_read_b32 v69, v74\ns_waitcnt lgkmcnt(0)\nv_cmp_gt_u32 vcc, v75, v69\ns_cbranch_vccnz .Lso27\ns_branch .Lro27\n"
".Lso28:\nds_read_b32 v69, v74\ns_waitcnt lgkmcnt(0)\nv_cmp_gt_u32 vcc, v75, v69\ns_cbranch_vccnz .Lso28\ns_branch .Lro28\n.Lso29:\nds_read_b32 v69, v74\n"
"s_waitcnt lgkmcnt(0)\nv_cmp_gt_u32 vcc, v75, v69\ns_cbranch_vccnz .Lso29\ns_branch .Lro29\n.Lso30:\nds_read_b32 v69, v74\ns_waitcnt lgkmcnt(0)\nv_cmp_gt_u32 vcc, v75, v69\n"
"s_cbranch_vccnz .Lso30\ns_branch .Lro30\n.Lso31:\nds_read_b32 v69, v74\ns_waitcnt lgkmcnt(0)\nv_cmp_gt_u32 vcc, v75, v69\ns_cbranch_vccnz .Lso31\ns_branch .Lro31\n"
".Lso32:\nds_read_b32 v69, v74\ns_waitcnt lgkmcnt(0)\nv_cmp_gt_u32 vcc, v75, v69\ns_cbranch_vccnz .Lso32\ns_branch .Lro32\n.Lso33:\nds_read_b32 v69, v74\n"
"s_waitcnt lgkmcnt(0)\nv_cmp_gt_u32 vcc, v75, v69\ns_cbranch_vccnz .Lso33\ns_branch .Lro33\n.Lso34:\nds_read_b32 v69, v74\ns_waitcnt lgkmcnt(0)\nv_cmp_gt_u32 vcc, v75, v69\n"
"s_cbranch_vccnz .Lso34\ns_branch .Lro34\n.Lso35:\nds_read_b32 v69, v74\ns_waitcnt lgkmcnt(0)\nv_cmp_gt_u32 vcc, v75, v69\ns_cbranch_vccnz .Lso35\ns_branch .Lro35\n"
".Lso36:\nds_read_b32 v69, v74\ns_waitcnt lgkmcnt(0)\nv_cmp_gt_u32 vcc, v75, v69\ns_cbranch_vccnz .Lso36\ns_branch .Lro36\n.Lso37:\nds_read_b32 v69, v74\n"
"s_waitcnt lgkmcnt(0)\nv_cmp_gt_u32 vcc, v75, v69\ns_cbranch_vccnz .Lso37\ns_branch .Lro37\n.Lso38:\nds_read_b32 v69, v74\ns_waitcnt lgkmcnt(0)\nv_cmp_gt_u32 vcc, v75, v69\n"
"s_cbranch_vccnz .Lso38\ns_branch .Lro38\n.Lso39:\nds_read_b32 v69, v74\ns_waitcnt lgkmcnt(0)\nv_cmp_gt_u32 vcc, v75, v69\ns_cbranch_vccnz .Lso39\ns_branch .Lro39\n"
".Lso40:\nds_read_b32 v69, v74\ns_waitcnt lgkmcnt(0)\nv_cmp_gt_u32 vcc, v75, v69\ns_cbranch_vccnz .Lso40\ns_branch .Lro40\n.Lso41:\nds_read_b32 v69, v74\n"
"s_waitcnt lgkmcnt(0)\nv_cmp_gt_u32 vcc, v75, v69\ns_cbranch_vccnz .Lso41\ns_branch .Lro41\n.Lso42:\nds_read_b32 v69, v74\ns_waitcnt lgkmcnt(0)\nv_cmp_gt_u32 vcc, v75, v69\n"
"s_cbranch_vccnz .Lso42\ns_branch .Lro42\n.Lso43:\nds_read_b32 v69, v74\ns_waitcnt lgkmcnt(0)\nv_cmp_gt_u32 vcc, v75, v69\ns_cbranch_vccnz .Lso43\ns_branch .Lro43\n"
".Lso44:\nds_read_b32 v69, v74\ns_waitcnt lgkmcnt(0)\nv_cmp_gt_u32 vcc, v75, v69\ns_cbranch_vccnz .Lso44\ns_branch .Lro44\n.Lso45:\nds_read_b32 v69, v74\n"
"s_waitcnt lgkmcnt(0)\nv_cmp_gt_u32 vcc, v75, v69\ns_cbranch_vccnz .Lso45\ns_branch .Lro45\n.Lso46:\nds_read_b32 v69, v74\ns_waitcnt lgkmcnt(0)\nv_cmp_gt_u32 vcc, v75, v69\n"
"s_cbranch_vccnz .Lso46\ns_branch .Lro46\n.Lso47:\nds_read_b32 v69, v74\ns_waitcnt lgkmcnt(0)\nv_cmp_gt_u32 vcc, v75, v69\ns_cbranch_vccnz .Lso47\ns_branch .Lro47\n"
".Lso48:\nds_read_b32 v69, v74\ns_waitcnt lgkmcnt(0)\nv_cmp_gt_u32 vcc, v75, v69\ns_cbranch_vccnz .Lso48\ns_branch .Lro48\n.Lso49r0:\nds_read_b32 v69, v74\n"
"s_waitcnt lgkmcnt(0)\nv_cmp_gt_u32 vcc, v75, v69\ns_cbranch_vccnz .Lso49r0\ns_branch .Lro49r0\n.Lso50r0:\nds_read_b32 v69, v74\ns_waitcnt lgkmcnt(0)\nv_cmp_gt_u32 vcc, v75, v69\n"
"s_cbranch_vccnz .Lso50r0\ns_branch .Lro50r0\n.Lso51r0:\nds_read_b32 v69, v74\ns_waitcnt lgkmcnt(0)\nv_cmp_gt_u32 vcc, v75, v69\ns_cbranch_vccnz .Lso51r0\ns_branch .Lro51r0\n"
".Lso52r0:\nds_read_b32 v69, v74\ns_waitcnt lgkmcnt(0)\nv_cmp_gt_u32 vcc, v75, v69\ns_cbranch_vccnz .Lso52r0\ns_branch .Lro52r0\n.Lso53r0:\nds_read_b32 v69, v74\n"
"s_waitcnt lgkmcnt(0)\nv_cmp_gt_u32 vcc, v75, v69\ns_cbranch_vccnz .Lso53r0\ns_branch .Lro53r0\n.Lso54r0:\nds_read_b32 v69, v74\ns_waitcnt lgkmcnt(0)\nv_cmp_gt_u32 vcc, v75, v69\n"
"s_cbranch_vccnz .Lso54r0\ns_branch .Lro54r0\n.Lso55r0:\nds_read_b32 v69, v74\ns_waitcnt lgkmcnt(0)\nv_cmp_gt_u32 vcc, v75, v69\ns_cbranch_vccnz .Lso55r0\ns_branch .Lro55r0\n"
".Lso56r0:\nds_read_b32 v69, v74\ns_waitcnt lgkmcnt(0)\nv_cmp_gt_u32 vcc, v75, v69\ns_cbranch_vccnz .Lso56r0\ns_branch .Lro56r0\n.Lso57r0:\nds_read_b32 v69, v74\n"
"s_waitcnt lgkmcnt(0)\nv_cmp_gt_u32 vcc, v75, v69\ns_cbranch_vccnz .Lso57r0\ns_branch .Lro57r0\n.Lso58r0:\nds_read_b32 v69, v74\ns_waitcnt lgkmcnt(0)\nv_cmp_gt_u32 vcc, v75, v69\n"
"s_cbranch_vccnz .Lso58r0\ns_branch .Lro58r0\n.Lso59r0:\nds_read_b32 v69, v74\ns_waitcnt lgkmcnt(0)\nv_cmp_gt_u32 vcc, v75, v69\ns_cbranch_vccnz .Lso59r0\ns_branch .Lro59r0\n"
".Lso60r0:\nds_read_b32 v69, v74\ns_waitcnt lgkmcnt(0)\nv_cmp_gt_u32 vcc, v75, v69\ns_cbranch_vccnz .Lso60r0\ns_branch .Lro60r0\n.Lso61r0:\nds_read_b32 v69, v74\n"
"s_waitcnt lgkmcnt(0)\nv_cmp_gt_u32 vcc, v75, v69\ns_cbranch_vccnz .Lso61r0\ns_branch .Lro61r0\n.Lso62r0:\nds_read_b32 v69, v74\ns_waitcnt lgkmcnt(0)\nv_cmp_gt_u32 vcc, v75, v69\n"
"s_cbranch_vccnz .Lso62r0\ns_branch .Lro62r0\n.Lso63r0:\nds_read_b32 v69, v74\ns_waitcnt lgkmcnt(0)\nv_cmp_gt_u32 vcc, v75, v69\ns_cbranch_vccnz .Lso63r0\ns_branch .Lro63r0\n"
".Lso49r1:\nds_read_b32 v69, v74\ns_waitcnt lgkmcnt(0)\nv_cmp_gt_u32 vcc, v75, v69\ns_cbranch_vccnz .Lso49r1\ns_branch .Lro49r1\n.Lso50r1:\nds_read_b32 v69, v74\n"
"s_waitcnt lgkmcnt(0)\nv_cmp_gt_u32 vcc, v75, v69\ns_cbranch_vccnz .Lso50r1\ns_branch .Lro50r1\n.Lso51r1:\nds_read_b32 v69, v74\ns_waitcnt lgkmcnt(0)\nv_cmp_gt_u32 vcc, v75, v69\n"
"s_cbranch_vccnz .Lso51r1\ns_branch .Lro51r1\n.Lso52r1:\nds_read_b32 v69, v74\ns_waitcnt lgkmcnt(0)\nv_cmp_gt_u32 vcc, v75, v69\ns_cbranch_vccnz .Lso52r1\ns_branch .Lro52r1\n"
".Lso53r1:\nds_read_b32 v69, v74\ns_waitcnt lgkmcnt(0)\nv_cmp_gt_u32 vcc, v75, v69\ns_cbranch_vccnz .Lso53r1\ns_branch .Lro53r1\n.Lso54r1:\nds_read_b32 v69, v74\n"
"s_waitcnt lgkmcnt(0)\nv_cmp_gt_u32 vcc, v75, v69\ns_cbranch_vccnz .Lso54r1\ns_branch .Lro54r1\n.Lso55r1:\nds_read_b32 v69, v74\ns_waitcnt lgkmcnt(0)\nv_cmp_gt_u32 vcc, v75, v69\n"
"s_cbranch_vccnz .Lso55r1\ns_branch .Lro55r1\n.Lso56r1:\nds_read_b32 v69, v74\ns_waitcnt lgkmcnt(0)\nv_cmp_gt_u32 vcc, v75, v69\ns_cbranch_vccnz .Lso56r1\ns_branch .Lro56r1\n"
".Lso57r1:\nds_read_b32 v69, v74\ns_waitcnt lgkmcnt(0)\nv_cmp_gt_u32 vcc, v75, v69\ns_cbranch_vccnz .Lso57r1\ns_branch .Lro57r1\n.Lso58r1:\nds_read_b32 v69, v74\n"
"s_waitcnt lgkmcnt(0)\nv_cmp_gt_u32 vcc, v75, v69\ns_cbranch_vccnz .Lso58r1\ns_branch .Lro58r1\n.Lso59r1:\nds_read_b32 v69, v74\ns_waitcnt lgkmcnt(0)\nv_cmp_gt_u32 vcc, v75, v69\n"
"s_cbranch_vccnz .Lso59r1\ns_branch .Lro59r1\n.Lso60r1:\nds_read_b32 v69, v74\ns_waitcnt lgkmcnt(0)\nv_cmp_gt_u32 vcc, v75, v69\ns_cbranch_vccnz .Lso60r1\ns_branch .Lro60r1\n"
".Lso61r1:\nds_read_b32 v69, v74\ns_waitcnt lgkmcnt(0)\nv_cmp_gt_u32 vcc, v75, v69\ns_cbranch_vccnz .Lso61r1\ns_branch .Lro61r1\n.Lso62r1:\nds_read_b32 v69, v74\n"
"s_waitcnt lgkmcnt(0)\nv_cmp_gt_u32 vcc, v75, v69\ns_cbranch_vccnz .Lso62r1\ns_branch .Lro62r1\n.Lso63r1:\nds_read_b32 v69, v74\ns_waitcnt lgkmcnt(0)\nv_cmp_gt_u32 vcc, v75, v69\n"
"s_cbranch_vccnz .Lso63r1\ns_branch .Lro63r1\n.Ltail1:\nQcvaa 8,112,32,32\nv_add_u32 v75, 1, v75\nQmvaa 8,116,36\ns_waitcnt vmcnt(16)\nds_write_b32 v73, v75\n"
"Qmvaa 8,120,40\nQe 1\nQmvaa 8,124,44\nQe 2\nQcvaa 12,128,32,32\nQe 3\nQmvaa 12,132,36\nQk 16,0,1\n"
"Qk 17,2,3\nQmvaa 12,136,40\nQe 4\nQmvaa 12,140,44\ns_waitcnt lgkmcnt(1)\nQmvav 80,144,20\nQe 5\nQmvav 84,148,20\n"
"Qe 6\nQmvav 88,152,20\nQe 7\nQmvav 92,156,20\nQk 18,4,5\nQk 19,6,7\nQmvav 96,160,20\nQe 8\n"
"Qmvav 100,164,20\nQe 9\nQmvav 104,168,20\nQe 10\nQmvav 108,172,20\nQe 11\nQmaav 176,204,20\nQk 20,8,9\n"
"Qk 21,10,11\nQcvaa 0,112,48,36\nQe 12\nQmvaa 0,116,52\nds_read_b32 v69, v74\nQmvaa 0,120,56\nQe 13\nQmvaa 0,124,60\n"
"Qe 14\nQcvaa 4,128,48,36\nQe 15\nQmvaa 4,132,52\nQk 22,12,13\nQk 23,14,15\nQmvaa 4,136,56\ns_waitcnt lgkmcnt(0)\n"
"v_cmp_gt_u32 vcc, v75, v69\nQmvaa 4,140,60\ns_cbranch_vccnz .Lsa29x\n.Lra29x:\nQmvav 112,144,16\ns_add_u32 m0, s91, 132096\ns_mul_i32 s94, s31, 0\nQmvav 116,148,16\n"
"buffer_load_dwordx4 v70, s[48:51], s94 offen lds\nQmvav 120,152,16\nQmvav 124,156,16\nQe 0\nQmvav 128,160,16\nQe 1\nQmvav 132,164,16\nQe 2\n"
"Qmvav 136,168,16\nbuffer_load_dwordx4 v71, s[48:51], s94 offen offset:1024 lds\nQmvav 140,172,16\nQe 3\nQmaav 180,204,16\nQk 16,0,1\nQk 17,2,3\nQcvaa 8,112,64,40\n"
"Qe 4\nQmvaa 8,116,68\ns_mul_i32 s95, s90, 0\nQe 5\nQmvaa 8,120,72\nbuffer_load_dwordx4 v72, s[52:55], s95 offen offset:2048 lds\nQmvaa 8,124,76\nQe 6\n"
"Qcvaa 12,128,64,40\nQe 7\nQmvaa 12,132,68\nQk 18,4,5\nQk 19,6,7\nQmvaa 12,136,72\nQmvaa 12,140,76\nbuffer_load_dwordx4 v61, s[52:55], s95 offen offset:3072 lds\n"
"Qmvav 144,144,20\nQmvav 148,148,20\nQmvav 152,152,20\nQmvav 156,156,20\ns_add_u32 m0, s91, 136192\nQmvav 160,160,20\nbuffer_load_dwordx4 v62, s[52:55], s95 offen lds\nQmvav 164,164,20\n"
"Qe 8\nQmvav 168,168,20\nQe 9\nQmvav 172,172,20\nQe 10\nQmaav 184,204,20\nQe 11\nQcvaa 0,112,80,44\n"
"buffer_load_dwordx4 v63, s[52:55], s95 offen offset:1024 lds\nQmvaa 0,116,84\nQk 20,8,9\nQk 21,10,11\nQmvaa 0,120,88\nQe 12\nQmvaa 0,124,92\nQe 13\n"
"Qcvaa 4,128,80,44\nQe 14\nQmvaa 4,132,84\nQmvaa 4,136,88\nQe 15\nQmvaa 4,140,92\nQk 22,12,13\nQk 23,14,15\n"
"Qmvav 176,144,16\nQmvav 180,148,16\nQmvav 184,152,16\nQmvav 188,156,16\nQe 0\nQmvav 192,160,16\nQe 1\nQmvav 196,164,16\n"
"Qmvav 200,168,16\nQe 2\nQmvav 204,172,16\nQe 3\nQmaav 188,204,16\nQk 16,0,1\nQk 17,2,3\nQcvaa 8,112,96,48\n"
"Qda 112,53,0\nQmvaa 8,116,100\nQda 116,53,512\nQmvaa 8,120,104\nQda 120,53,1024\nQmvaa 8,124,108\nQda 124,53,1536\nQcvaa 12,128,96,48\n"
"Qda 128,53,2048\nQmvaa 12,132,100\nQda 132,53,2560\nQmvaa 12,136,104\nQda 136,53,3072\nQmvaa 12,140,108\nQda 140,53,3584\nQmvav 208,144,20\n"
"Qmvav 212,148,20\nQe 4\nQmvav 216,152,20\nQe 5\nQmvav 220,156,20\nQe 6\nQmvav 224,160,20\nQe 7\n"
"Qmvav 228,164,20\nQk 18,4,5\nQk 19,6,7\nQmvav 232,168,20\nQe 8\nQmvav 236,172,20\nQe 9\nQmaav 192,204,20\n"
"s_waitcnt lgkmcnt(0)\nQk 20,8,9\nQcvaa 0,112,0,24\nQe 10\nQmvaa 0,116,4\nQe 11\nQmvaa 0,120,8\nQk 21,10,11\n"
"Qmvaa 0,124,12\nQe 12\nQcvaa 4,128,0,24\nQe 13\nQmvaa 4,132,4\nQe 14\nQmvaa 4,136,8\nQe 15\n"
"Qmvaa 4,140,12\nQk 22,12,13\nQk 23,14,15\nQmvav 240,144,16\nQmvav 244,148,16\nQmvav 248,152,16\nQmvav 252,156,16\nQmaav 208,160,16\n"
"Qe 0\nQmaav 212,164,16\nQe 1\nQmaav 216,168,16\nQe 2\nQmaav 220,172,16\nQe 3\nQmaav 196,204,16\n"
"Qk 16,0,1\nQk 17,2,3\nQcvaa 8,112,16,28\nQe 4\nQmvaa 8,116,20\nQe 5\nQmvaa 8,120,24\nQe 6\n"
"Qmvaa 8,124,28\nQe 7\nQcvaa 12,128,16,28\nQk 18,4,5\nQk 19,6,7\nQmvaa 12,132,20\nQmvaa 12,136,24\nQmvaa 12,140,28\n"
"Qmaav 224,144,20\nQta 144,55,0\nQta 146,55,2048\nQmaav 228,148,20\nQta 148,55,256\nQta 150,55,2304\nQmaav 232,152,20\nQta 152,55,512\n"
"Qta 154,55,2560\nQmaav 236,156,20\nQta 156,55,768\nQta 158,55,2816\nQmaav 240,160,20\nQta 160,55,1024\nQta 162,55,3072\nQmaav 244,164,20\n"
"Qta 164,55,1280\nQta 166,55,3328\nQmaav 248,168,20\nQta 168,55,1536\nQta 170,55,3584\nQmaav 252,172,20\nQta 172,55,1792\nQta 174,55,3840\n"
"Qmaav 200,204,20\nQe 8\nQcvaa 0,112,32,32\nv_add_u32 v75, 1, v75\nQmvaa 0,116,36\ns_waitcnt vmcnt(6)\nds_write_b32 v73, v75\nQmvaa 0,120,40\n"
"Qe 9\nQmvaa 0,124,44\nQe 10\nQcvaa 4,128,32,32\nQe 11\nQmvaa 4,132,36\nQk 20,8,9\nQk 21,10,11\n"
"Qmvaa 4,136,40\nQe 12\nQmvaa 4,140,44\ns_waitcnt lgkmcnt(1)\nQmvav 80,144,16\nQe 13\nQmvav 84,148,16\nQe 14\n"
"Qmvav 88,152,16\nQe 15\nQmvav 92,156,16\nQk 22,12,13\nQk 23,14,15\nQmvav 96,160,16\nQe 0\nQmvav 100,164,16\n"
"Qe 1\nQmvav 104,168,16\nQe 2\nQmvav 108,172,16\nQe 3\nQmaav 176,204,16\nQk 16,0,1\nQk 17,2,3\n"
"Qcvaa 8,112,48,36\nQe 4\nQmvaa 8,116,52\nds_read_b32 v69, v74\nQmvaa 8,120,56\nQe 5\nQmvaa 8,124,60\nQe 6\n"
"Qcvaa 12,128,48,36\nQe 7\nQmvaa 12,132,52\nQk 18,4,5\nQk 19,6,7\nQmvaa 12,136,56\ns_waitcnt lgkmcnt(0)\nv_cmp_gt_u32 vcc, v75, v69\n"
"Qmvaa 12,140,60\ns_cbranch_vccnz .Lsa30x\n.Lra30x:\nQmvav 112,144,20\ns_add_u32 m0, s91, 0\ns_mul_i32 s94, s31, 1\nQmvav 116,148,20\nbuffer_load_dwordx4 v70, s[48:51], s94 offen lds\n"
"Qmvav 120,152,20\nQmvav 124,156,20\nQe 8\nQmvav 128,160,20\nQe 9\nQmvav 132,164,20\nQe 10\nQmvav 136,168,20\n"
"buffer_load_dwordx4 v71, s[48:51], s94 offen offset:1024 lds\nQmvav 140,172,20\nQe 11\nQmaav 180,204,20\nQk 20,8,9\nQk 21,10,11\nQcvaa 0,112,64,40\nQe 12\n"
"Qmvaa 0,116,68\ns_mul_i32 s95, s90, 1\nQe 13\nQmvaa 0,120,72\nbuffer_load_dwordx4 v72, s[52:55], s95 offen offset:2048 lds\nQmvaa 0,124,76\nQe 14\nQcvaa 4,128,64,40\n"
"Qe 15\nQmvaa 4,132,68\nQk 22,12,13\nQk 23,14,15\nQmvaa 4,136,72\nQmvaa 4,140,76\nbuffer_load_dwordx4 v61, s[52:55], s95 offen offset:3072 lds\nQmvav 144,144,16\n"
"Qmvav 148,148,16\nQmvav 152,152,16\nQmvav 156,156,16\ns_add_u32 m0, s91, 4096\nQmvav 160,160,16\nbuffer_load_dwordx4 v62, s[52:55], s95 offen lds\nQmvav 164,164,16\nQe 0\n"
"Qmvav 168,168,16\nQe 1\nQmvav 172,172,16\nQe 2\nQmaav 184,204,16\nQe 3\nQcvaa 8,112,80,44\nbuffer_load_dwordx4 v63, s[52:55], s95 offen offset:1024 lds\n"
"Qmvaa 8,116,84\nQk 16,0,1\nQk 17,2,3\nQmvaa 8,120,88\nQe 4\nQmvaa 8,124,92\nQe 5\nQcvaa 12,128,80,44\n"
"s_add_u32 m0, s91, 24576\ns_mul_i32 s94, s31, 2\nQe 6\nQmvaa 12,132,84\nbuffer_load_dwordx4 v70, s[48:51], s94 offen lds\nQmvaa 12,136,88\nQe 7\nQmvaa 12,140,92\n"
"Qk 18,4,5\nQk 19,6,7\nQmvav 176,144,20\nQmvav 180,148,20\nQmvav 184,152,20\nbuffer_load_dwordx4 v71, s[48:51], s94 offen offset:1024 lds\nQmvav 188,156,20\nQe 8\n"
"Qmvav 192,160,20\nQe 9\nQmvav 196,164,20\nQmvav 200,168,20\ns_mul_i32 s95, s90, 2\nQe 10\nQmvav 204,172,20\nbuffer_load_dwordx4 v72, s[52:55], s95 offen offset:2048 lds\n"
"Qe 11\nQmaav 188,204,20\nQk 20,8,9\nQk 21,10,11\nQcvaa 0,112,96,48\nQda 112,53,33024\nQmvaa 0,116,100\nQda 116,53,33536\n"
"Qmvaa 0,120,104\nQda 120,53,34048\nQmvaa 0,124,108\nQda 124,53,34560\nbuffer_load_dwordx4 v61, s[52:55], s95 offen offset:3072 lds\nQcvaa 4,128,96,48\nQda 128,53,35072\nQmvaa 4,132,100\n"
"Qda 132,53,35584\nQmvaa 4,136,104\nQda 136,53,36096\nQmvaa 4,140,108\nQda 140,53,36608\ns_add_u32 m0, s91, 28672\nQmvav 208,144,16\nbuffer_load_dwordx4 v62, s[52:55], s95 offen lds\n"
"Qmvav 212,148,16\nQe 12\nQmvav 216,152,16\nQe 13\nQmvav 220,156,16\nQe 14\nQmvav 224,160,16\nQe 15\n"
"Qmvav 228,164,16\nbuffer_load_dwordx4 v63, s[52:55], s95 offen offset:1024 lds\nQk 22,12,13\nQk 23,14,15\nQmvav 232,168,16\nQe 0\nQmvav 236,172,16\nQe 1\n"
"Qmaav 192,204,16\ns_waitcnt lgkmcnt(0)\nQk 16,0,1\nQcvaa 8,112,0,24\ns_add_u32 m0, s91, 49152\ns_mul_i32 s94, s31, 3\nQe 2\nQmvaa 8,116,4\n"
"buffer_load_dwordx4 v70, s[48:51], s94 offen lds\nQe 3\nQmvaa 8,120,8\nQk 17,2,3\nQmvaa 8,124,12\nQe 4\nQcvaa 12,128,0,24\nQe 5\n"
"Qmvaa 12,132,4\nQe 6\nQmvaa 12,136,8\nbuffer_load_dwordx4 v71, s[48:51], s94 offen offset:1024 lds\nQe 7\nQmvaa 12,140,12\nQk 18,4,5\nQk 19,6,7\n"
"Qmvav 240,144,20\nQmvav 244,148,20\nQmvav 248,152,20\ns_mul_i32 s95, s90, 3\nQmvav 252,156,20\nbuffer_load_dwordx4 v72, s[52:55], s95 offen offset:2048 lds\nQmaav 208,160,20\nQe 8\n"
"Qmaav 212,164,20\nQe 9\nQmaav 216,168,20\nQe 10\nQmaav 220,172,20\nQe 11\nQmaav 196,204,20\nbuffer_load_dwordx4 v61, s[52:55], s95 offen offset:3072 lds\n"
"Qk 20,8,9\nQk 21,10,11\nQcvaa 0,112,16,28\nQe 12\nQmvaa 0,116,20\nQe 13\nQmvaa 0,120,24\nQe 14\n"
"Qmvaa 0,124,28\ns_add_u32 m0, s91, 53248\nQe 15\nQcvaa 4,128,16,28\nbuffer_load_dwordx4 v62, s[52:55], s95 offen lds\nQk 22,12,13\nQk 23,14,15\nQmvaa 4,132,20\n"
"Qmvaa 4,136,24\nQmvaa 4,140,28\nQmaav 224,144,16\nQta 144,55,33024\nQta 146,55,35072\nQmaav 228,148,16\nQta 148,55,33280\nQta 150,55,35328\n"
"buffer_load_dwordx4 v63, s[52:55], s95 offen offset:1024 lds\nQmaav 232,152,16\nQta 152,55,33536\nQta 154,55,35584\nQmaav 236,156,16\nQta 156,55,33792\nQta 158,55,35840\nQmaav 240,160,16\n"
"Qta 160,55,34048\nQta 162,55,36096\nQmaav 244,164,16\nQta 164,55,34304\nQta 166,55,36352\nQmaav 248,168,16\nQta 168,55,34560\nQta 170,55,36608\n"
"Qmaav 252,172,16\nQta 172,55,34816\nQta 174,55,36864\nQmaav 200,204,16\nQe 0\nQcvaa 8,112,32,32\nv_add_u32 v75, 1, v75\nQmvaa 8,116,36\n"
"ds_write_b32 v73, v75\nQmvaa 8,120,40\nQe 1\nQmvaa 8,124,44\nQe 2\nQcvaa 12,128,32,32\nQe 3\nQmvaa 12,132,36\n"
"Qk 16,0,1\nQk 17,2,3\nQmvaa 12,136,40\nQe 4\nQmvaa 12,140,44\ns_waitcnt lgkmcnt(1)\nQmvav 80,144,20\nQe 5\n"
"Qmvav 84,148,20\nQe 6\nQmvav 88,152,20\nQe 7\nQmvav 92,156,20\nQk 18,4,5\nQk 19,6,7\nQmvav 96,160,20\n"
"Qe 8\nQmvav 100,164,20\nQe 9\nQmvav 104,168,20\nQe 10\nQmvav 108,172,20\nQe 11\nQmaav 176,204,20\n"
"Qk 20,8,9\nQk 21,10,11\nQcvaa 0,112,48,36\nQe 12\nQmvaa 0,116,52\nds_read_b32 v69, v74\nQmvaa 0,120,56\nQe 13\n"
"Qmvaa 0,124,60\nQe 14\nQcvaa 4,128,48,36\nQe 15\nQmvaa 4,132,52\nQk 22,12,13\nQk 23,14,15\nQmvaa 4,136,56\n"
"s_waitcnt lgkmcnt(0)\nv_cmp_gt_u32 vcc, v75, v69\nQmvaa 4,140,60\ns_cbranch_vccnz .Lsa31x\n.Lra31x:\nQmvav 112,144,16\ns_add_u32 m0, s91, 73728\ns_mul_i32 s94, s31, 4\n"
"Qmvav 116,148,16\nbuffer_load_dwordx4 v70, s[48:51], s94 offen lds\nQmvav 120,152,16\nQmvav 124,156,16\nQe 0\nQmvav 128,160,16\nQe 1\nQmvav 132,164,16\n"
"Qe 2\nQmvav 136,168,16\nbuffer_load_dwordx4 v71, s[48:51], s94 offen offset:1024 lds\nQmvav 140,172,16\nQe 3\nQmaav 180,204,16\nQk 16,0,1\nQk 17,2,3\n"
"Qcvaa 8,112,64,40\nQe 4\nQmvaa 8,116,68\ns_mul_i32 s95, s90, 4\nQe 5\nQmvaa 8,120,72\nbuffer_load_dwordx4 v72, s[52:55], s95 offen offset:2048 lds\nQmvaa 8,124,76\n"
"Qe 6\nQcvaa 12,128,64,40\nQe 7\nQmvaa 12,132,68\nQk 18,4,5\nQk 19,6,7\nQmvaa 12,136,72\nQmvaa 12,140,76\n"
"buffer_load_dwordx4 v61, s[52:55], s95 offen offset:3072 lds\nQmvav 144,144,20\nQmvav 148,148,20\nQmvav 152,152,20\nQmvav 156,156,20\ns_add_u32 m0, s91, 77824\nQmvav 160,160,20\nbuffer_load_dwordx4 v62, s[52:55], s95 offen lds\n"
"Qmvav 164,164,20\nQe 8\nQmvav 168,168,20\nQe 9\nQmvav 172,172,20\nQe 10\nQmaav 184,204,20\nQe 11\n"
"Qcvaa 0,112,80,44\nbuffer_load_dwordx4 v63, s[52:55], s95 offen offset:1024 lds\nQmvaa 0,116,84\nQk 20,8,9\nQk 21,10,11\nQmvaa 0,120,88\nQe 12\nQmvaa 0,124,92\n"
"Qe 13\nQcvaa 4,128,80,44\ns_add_u32 m0, s91, 98304\ns_mul_i32 s94, s31, 5\nQe 14\nQmvaa 4,132,84\nbuffer_load_dwordx4 v70, s[48:51], s94 offen lds\nQmvaa 4,136,88\n"
"Qe 15\nQmvaa 4,140,92\nQk 22,12,13\nQk 23,14,15\nQmvav 176,144,16\nQmvav 180,148,16\nQmvav 184,152,16\nbuffer_load_dwordx4 v71, s[48:51], s94 offen offset:1024 lds\n"
"Qmvav 188,156,16\nQe 0\nQmvav 192,160,16\nQe 1\nQmvav 196,164,16\nQmvav 200,168,16\ns_mul_i32 s95, s90, 5\nQe 2\n"
"Qmvav 204,172,16\nbuffer_load_dwordx4 v72, s[52:55], s95 offen offset:2048 lds\nQe 3\nQmaav 188,204,16\nQk 16,0,1\nQk 17,2,3\nQcvaa 8,112,96,48\nQmvaa 8,116,100\n"
"Qmvaa 8,120,104\nQmvaa 8,124,108\nbuffer_load_dwordx4 v61, s[52:55], s95 offen offset:3072 lds\nQcvaa 12,128,96,48\nQmvaa 12,132,100\nQmvaa 12,136,104\nQmvaa 12,140,108\ns_add_u32 m0, s91, 102400\n"
"Qmvav 208,144,20\nbuffer_load_dwordx4 v62, s[52:55], s95 offen lds\nQmvav 212,148,20\nQe 4\nQmvav 216,152,20\nQe 5\nQmvav 220,156,20\nQe 6\n"
"Qmvav 224,160,20\nQe 7\nQmvav 228,164,20\nbuffer_load_dwordx4 v63, s[52:55], s95 offen offset:1024 lds\nQk 18,4,5\nQk 19,6,7\nQmvav 232,168,20\nQe 8\n"
"Qmvav 236,172,20\nQe 9\nQmaav 192,204,20\nQk 20,8,9\nQe 10\nQe 11\ns_nop 0\nQk 21,10,11\n"
"Qe 12\nQe 13\nQe 14\nQe 15\nQk 22,12,13\nQk 23,14,15\nQmvav 240,144,16\nQmvav 244,148,16\n"
"Qmvav 248,152,16\nQmvav 252,156,16\nQmaav 208,160,16\nQmaav 212,164,16\nQmaav 216,168,16\nQmaav 220,172,16\nQmaav 196,204,16\nQmaav 224,144,20\n"
"Qmaav 228,148,20\nQmaav 232,152,20\nQmaav 236,156,20\nQmaav 240,160,20\nQmaav 244,164,20\nQmaav 248,168,20\nQmaav 252,172,20\nQmaav 200,204,20\n"
"s_nop 7\nv_accvgpr_read_b32 v24, a176\nv_accvgpr_read_b32 v28, a180\nv_accvgpr_read_b32 v32, a184\nv_accvgpr_read_b32 v36, a188\nv_accvgpr_read_b32 v40, a192\nv_accvgpr_read_b32 v44, a196\nv_accvgpr_read_b32 v48, a200\n"
"v_cmp_nge_f32 vcc, 0x44800000, v24\ns_mov_b64 s[74:75], vcc\nv_cmp_nge_f32 vcc, 0x44800000, v28\ns_or_b64 s[74:75], s[74:75], vcc\nv_cmp_nge_f32 vcc, 0x44800000, v32\ns_or_b64 s[74:75], s[74:75], vcc\nv_cmp_nge_f32 vcc, 0x44800000, v36\ns_or_b64 s[74:75], s[74:75], vcc\n"
"v_cmp_nge_f32 vcc, 0x44800000, v40\ns_or_b64 s[74:75], s[74:75], vcc\nv_cmp_nge_f32 vcc, 0x44800000, v44\ns_or_b64 s[74:75], s[74:75], vcc\nv_cmp_nge_f32 vcc, 0x44800000, v48\ns_or_b64 s[74:75], s[74:75], vcc\ns_cmp_lg_u64 s[74:75], 0\ns_cbranch_scc1 .Lfb\n"
".Lfbret1:\nv_rcp_f32 v52, v24\nv_rcp_f32 v53, v28\nv_rcp_f32 v54, v32\nv_rcp_f32 v55, v36\nv_rcp_f32 v56, v40\nv_rcp_f32 v57, v44\nv_rcp_f32 v58, v48\n"
"s_mov_b32 s56, %8\ns_and_b32 s57, %9, 0xffff\ns_mov_b32 s58, 1835008\ns_mul_i32 s68, s92, 7168\ns_add_u32 s68, s68, %10\nv_add_u32 v48, s68, v60\nv_add_u32 v49, 73728, v48\ns_mul_i32 s68, s93, 1280\n"
"s_add_u32 s68, s68, %10\nv_lshrrev_b32 v66, 9, v60\nv_mul_u32_u24 v66, 2560, v66\nv_bfe_u32 v67, v60, 1, 8\nv_add3_u32 v64, v66, v67, s68\nv_add_u32 v59, 57344, v64\nv_add_u32 v50, 114688, v64\ns_waitcnt vmcnt(24)\n"
"s_barrier\nQtv 0,50,31744\nQtv 2,50,36864\nQtv 4,50,32000\nQtv 6,50,37120\nQtv 8,50,32256\nQtv 10,50,37376\nQtv 12,50,32512\n"
"Qtv 14,50,37632\nQtv 16,50,32768\nQtv 18,50,37888\nQda 112,49,58368\nQda 116,49,59392\nQda 120,49,60416\nQda 124,49,61440\nQda 128,49,62464\n"
"Qda 132,49,63488\nQda 136,49,64512\nv_add_u32 v75, 1, v75\ns_waitcnt vmcnt(18)\nds_write_b32 v73, v75\nv_mul_f32 v80, v52, v80\nv_mul_f32 v81, v52, v81\nv_mul_f32 v82, v52, v82\n"
"v_mul_f32 v83, v52, v83\nv_mul_f32 v84, v52, v84\nv_mul_f32 v85, v52, v85\nv_mul_f32 v86, v52, v86\nv_mul_f32 v87, v52, v87\nQk 80,80,81\nQk 81,82,83\nQk 82,84,85\n"
"Qk 83,86,87\nv_mul_f32 v112, v53, v112\nv_mul_f32 v113, v53, v113\nv_mul_f32 v114, v53, v114\nv_mul_f32 v115, v53, v115\nv_mul_f32 v116, v53, v116\nv_mul_f32 v117, v53, v117\nv_mul_f32 v118, v53, v118\n"
"v_mul_f32 v119, v53, v119\nQk 112,112,113\nQk 113,114,115\nQk 114,116,117\nQk 115,118,119\ns_waitcnt lgkmcnt(5)\nQzava 0,0,112\nQtv 20,64,14336\n"
"v_mul_f32 v144, v54, v144\nQzava 4,4,112\nQtv 22,64,19456\nv_mul_f32 v145, v54, v145\nQzava 8,8,112\nQtv 24,64,14592\nv_mul_f32 v146, v54, v146\nQzava 12,12,112\n"
"v_mul_f32 v147, v54, v147\nv_mul_f32 v148, v54, v148\nQzava 16,16,112\nQtv 26,64,19712\nv_mul_f32 v149, v54, v149\nQzava 20,0,116\nQtv 28,64,14848\nv_mul_f32 v150, v54, v150\n"
"Qzava 24,4,116\nv_mul_f32 v151, v54, v151\nQk 144,144,145\nQzava 28,8,116\nds_read_b32 v69, v74\nQk 145,146,147\nQzava 32,12,116\nQtv 30,64,19968\n"
"Qk 146,148,149\nQzava 36,16,116\nQtv 32,64,15104\nQk 147,150,151\nQzava 40,0,120\nv_mul_f32 v176, v55, v176\nv_mul_f32 v177, v55, v177\nQzava 44,4,120\n"
"s_waitcnt lgkmcnt(2)\nv_cmp_gt_u32 vcc, v75, v69\nQzava 48,8,120\ns_cbranch_vccnz .Lso0\n.Lro0:\nv_mul_f32 v178, v55, v178\nQzava 52,12,120\nQtv 34,64,20224\n"
"s_add_u32 m0, s91, 132096\ns_mul_i32 s94, s31, 6\nQzava 56,16,120\nbuffer_load_dwordx4 v70, s[48:51], s94 offen lds\nQzava 60,0,124\nQtv 36,64,15360\nv_mul_f32 v179, v55, v179\nQzava 64,4,124\n"
"v_mul_f32 v180, v55, v180\nv_mul_f32 v181, v55, v181\nQzava 68,8,124\nQtv 38,64,20480\nv_mul_f32 v182, v55, v182\nQzava 72,12,124\nbuffer_load_dwordx4 v71, s[48:51], s94 offen offset:1024 lds\nQzava 76,16,124\n"
"Qda 140,48,0\nv_mul_f32 v183, v55, v183\nQzava 80,0,128\nQk 176,176,177\nQk 177,178,179\nQzava 84,4,128\nQda 144,48,1024\ns_mul_i32 s95, s90, 6\n"
"Qzava 88,8,128\nbuffer_load_dwordx4 v72, s[52:55], s95 offen offset:2048 lds\nQzava 92,12,128\nQda 148,48,2048\nQk 178,180,181\nQzava 96,16,128\nQk 179,182,183\nv_mul_f32 v208, v56, v208\n"
"Qzava 100,0,132\nQda 152,48,3072\nv_mul_f32 v209, v56, v209\nQzava 104,4,132\nbuffer_load_dwordx4 v61, s[52:55], s95 offen offset:3072 lds\nQzava 108,8,132\nQda 156,48,4096\nv_mul_f32 v210, v56, v210\n"
"Qzava 176,12,132\nv_mul_f32 v211, v56, v211\nv_mul_f32 v212, v56, v212\nQzava 180,16,132\nQda 160,48,5120\ns_add_u32 m0, s91, 136192\nQzava 184,0,136\nbuffer_load_dwordx4 v62, s[52:55], s95 offen lds\n"
"Qzava 188,4,136\nQda 164,48,6144\nv_mul_f32 v213, v56, v213\nQzava 192,8,136\nv_mul_f32 v214, v56, v214\nv_mul_f32 v215, v56, v215\nQzava 196,12,136\nbuffer_load_dwordx4 v63, s[52:55], s95 offen offset:1024 lds\n"
"v_add_u32 v75, 1, v75\nQzava 200,16,136\ns_waitcnt vmcnt(18)\nds_write_b32 v73, v75\ns_waitcnt lgkmcnt(5)\nQmava 0,20,140\nQtv 0,64,38912\ns_add_u32 s88, s71, 0\n"
"Qmava 4,24,140\nbuffer_store_dwordx4 v[80:83], v60, s[56:59], s88 offen offset:0 sc1\nQmava 8,28,140\nQtv 2,64,44032\nQk 208,208,209\nQmava 12,32,140\nQtv 4,64,39168\nQk 209,210,211\n"
"Qmava 16,36,140\nQk 210,212,213\nQk 211,214,215\nQmava 20,20,144\nQtv 6,64,44288\nv_mul_f32 v240, v57, v240\nQmava 24,24,144\nQtv 8,64,39424\n"
"v_mul_f32 v241, v57, v241\nQmava 28,28,144\nds_read_b32 v69, v74\nv_mul_f32 v242, v57, v242\nQmava 32,32,144\nv_mul_f32 v243, v57, v243\nv_mul_f32 v244, v57, v244\nQmava 36,36,144\n"
"Qtv 10,64,44544\nv_mul_f32 v245, v57, v245\nQmava 40,20,148\nQtv 12,64,39680\nv_mul_f32 v246, v57, v246\nQmava 44,24,148\ns_waitcnt lgkmcnt(2)\nv_cmp_gt_u32 vcc, v75, v69\n"
"Qmava 48,28,148\ns_cbranch_vccnz .Lso1\n.Lro1:\nv_mul_f32 v247, v57, v247\nQmava 52,32,148\nQtv 14,64,44800\ns_add_u32 m0, s91, 0\ns_mul_i32 s94, s31, 7\n"
"Qmava 56,36,148\nbuffer_load_dwordx4 v70, s[48:51], s94 offen lds\nQmava 60,20,152\nQk 240,240,241\nQk 241,242,243\nQmava 64,24,152\nQtv 16,64,39936\nQk 242,244,245\n"
"Qmava 68,28,152\nQtv 18,64,45056\nQk 243,246,247\nQmava 72,32,152\nbuffer_load_dwordx4 v71, s[48:51], s94 offen offset:1024 lds\nQmava 76,36,152\nv_accvgpr_read_b32 v40, a224\nv_accvgpr_read_b32 v41, a225\n"
"Qmava 80,20,156\nQda 112,48,24576\nv_accvgpr_read_b32 v42, a226\nQmava 84,24,156\nQda 116,48,25600\ns_mul_i32 s95, s90, 7\nQmava 88,28,156\nbuffer_load_dwordx4 v72, s[52:55], s95 offen offset:2048 lds\n"
"Qmava 92,32,156\nQda 120,48,26624\nv_accvgpr_read_b32 v43, a227\nQmava 96,36,156\nv_accvgpr_read_b32 v44, a228\nv_accvgpr_read_b32 v45, a229\nQmava 100,20,160\nQda 124,48,27648\n"
"v_accvgpr_read_b32 v46, a230\nQmava 104,24,160\nbuffer_load_dwordx4 v61, s[52:55], s95 offen offset:3072 lds\nQmava 108,28,160\nQda 128,48,28672\nv_accvgpr_read_b32 v47, a231\nQmava 176,32,160\nv_mul_f32 v40, v58, v40\n"
"v_mul_f32 v41, v58, v41\nQmava 180,36,160\nQda 132,48,29696\ns_add_u32 m0, s91, 4096\nQmava 184,20,164\nbuffer_load_dwordx4 v62, s[52:55], s95 offen lds\nQmava 188,24,164\nQda 136,48,30720\n"
"v_mul_f32 v42, v58, v42\nQmava 192,28,164\nv_mul_f32 v43, v58, v43\nv_mul_f32 v44, v58, v44\nQmava 196,32,164\nbuffer_load_dwordx4 v63, s[52:55], s95 offen offset:1024 lds\nv_add_u32 v75, 1, v75\nQmava 200,36,164\n"
"s_waitcnt vmcnt(19)\nds_write_b32 v73, v75\ns_waitcnt lgkmcnt(5)\nQmava 0,0,112\nQtv 20,59,6144\nv_mul_f32 v45, v58, v45\nQmava 4,4,112\nbuffer_store_dwordx4 v[112:115], v60, s[56:59], s88 offen offset:1024 sc1\n"
"Qmava 8,8,112\nQtv 22,59,11264\nv_mul_f32 v46, v58, v46\nQmava 12,12,112\nQtv 24,59,6400\nv_mul_f32 v47, v58, v47\nQmava 16,16,112\nQk 84,40,41\n"
"Qk 85,42,43\nQmava 20,0,116\nQtv 26,59,11520\nQk 86,44,45\nQmava 24,4,116\nQtv 28,59,6656\nQk 87,46,47\nQmava 28,8,116\n"
"ds_read_b32 v69, v74\nv_mul_f32 v88, v52, v88\nQmava 32,12,116\nv_mul_f32 v89, v52, v89\nv_mul_f32 v90, v52, v90\nQmava 36,16,116\nQtv 30,59,11776\nv_mul_f32 v91, v52, v91\n"
"Qmava 40,0,120\nQtv 32,59,6912\nv_mul_f32 v92, v52, v92\nQmava 44,4,120\ns_waitcnt lgkmcnt(2)\nv_cmp_gt_u32 vcc, v75, v69\nQmava 48,8,120\ns_cbranch_vccnz .Lso2\n"
".Lro2:\nv_mul_f32 v93, v52, v93\nQmava 52,12,120\nQtv 34,59,12032\ns_add_u32 m0, s91, 24576\ns_mul_i32 s94, s31, 8\nQmava 56,16,120\nbuffer_load_dwordx4 v70, s[48:51], s94 offen lds\n"
"Qmava 60,0,124\nv_mul_f32 v94, v52, v94\nv_mul_f32 v95, v52, v95\nQmava 64,4,124\nQtv 36,59,7168\nQk 88,88,89\nQmava 68,8,124\nQtv 38,59,12288\n"
"Qk 89,90,91\nQmava 72,12,124\nbuffer_load_dwordx4 v71, s[48:51], s94 offen offset:1024 lds\nQmava 76,16,124\nQk 90,92,93\nQk 91,94,95\nQmava 80,0,128\nQda 140,48,49152\n"
"v_mul_f32 v120, v53, v120\nQmava 84,4,128\nQda 144,48,50176\ns_mul_i32 s95, s90, 8\nQmava 88,8,128\nbuffer_load_dwordx4 v72, s[52:55], s95 offen offset:2048 lds\nQmava 92,12,128\nQda 148,48,51200\n"
"v_mul_f32 v121, v53, v121\nQmava 96,16,128\nv_mul_f32 v122, v53, v122\nv_mul_f32 v123, v53, v123\nQmava 100,0,132\nQda 152,48,52224\nv_mul_f32 v124, v53, v124\nQmava 104,4,132\n"
"buffer_load_dwordx4 v61, s[52:55], s95 offen offset:3072 lds\nQmava 108,8,132\nQda 156,48,53248\nv_mul_f32 v125, v53, v125\nQmava 176,12,132\nv_mul_f32 v126, v53, v126\nv_mul_f32 v127, v53, v127\nQmava 180,16,132\n"
"Qda 160,48,54272\ns_add_u32 m0, s91, 28672\nQmava 184,0,136\nbuffer_load_dwordx4 v62, s[52:55], s95 offen lds\nQmava 188,4,136\nQda 164,48,55296\nQk 120,120,121\nQmava 192,8,136\n"
"Qk 121,122,123\nQk 122,124,125\nQmava 196,12,136\nbuffer_load_dwordx4 v63, s[52:55], s95 offen offset:1024 lds\nv_add_u32 v75, 1, v75\nQmava 200,16,136\ns_waitcnt vmcnt(20)\nds_write_b32 v73, v75\n"
"s_waitcnt lgkmcnt(5)\nQmava 0,20,140\nQtv 0,59,30720\nQk 123,126,127\nQmava 4,24,140\nbuffer_store_dwordx4 v[144:147], v60, s[56:59], s88 offen offset:2048 sc1\nQmava 8,28,140\nQtv 2,59,35840\n"
"v_mul_f32 v152, v54, v152\nQmava 12,32,140\nQtv 4,59,30976\nv_mul_f32 v153, v54, v153\nQmava 16,36,140\nv_mul_f32 v154, v54, v154\nv_mul_f32 v155, v54, v155\nQmava 20,20,144\n"
"Qtv 6,59,36096\nv_mul_f32 v156, v54, v156\nQmava 24,24,144\nQtv 8,59,31232\nv_mul_f32 v157, v54, v157\nQmava 28,28,144\nds_read_b32 v69, v74\nv_mul_f32 v158, v54, v158\n"
"Qmava 32,32,144\nv_mul_f32 v159, v54, v159\nQk 152,152,153\nQmava 36,36,144\nQtv 10,59,36352\nQk 153,154,155\nQmava 40,20,148\nQtv 12,59,31488\n"
"Qk 154,156,157\nQmava 44,24,148\ns_waitcnt lgkmcnt(2)\nv_cmp_gt_u32 vcc, v75, v69\nQmava 48,28,148\ns_cbranch_vccnz .Lso3\n.Lro3:\nQk 155,158,159\n"
"Qmava 52,32,148\nQtv 14,59,36608\ns_add_u32 m0, s91, 49152\ns_mul_i32 s94, s31, 9\nQmava 56,36,148\nbuffer_load_dwordx4 v70, s[48:51], s94 offen lds\nQmava 60,20,152\nv_mul_f32 v184, v55, v184\n"
"v_mul_f32 v185, v55, v185\nQmava 64,24,152\nQtv 16,59,31744\nv_mul_f32 v186, v55, v186\nQmava 68,28,152\nQtv 18,59,36864\nv_mul_f32 v187, v55, v187\nQmava 72,32,152\n"
"buffer_load_dwordx4 v71, s[48:51], s94 offen offset:1024 lds\nQmava 76,36,152\nv_mul_f32 v188, v55, v188\nv_mul_f32 v189, v55, v189\nQmava 80,20,156\nQda 112,49,0\nv_mul_f32 v190, v55, v190\nQmava 84,24,156\n"
"Qda 116,49,1024\ns_mul_i32 s95, s90, 9\nQmava 88,28,156\nbuffer_load_dwordx4 v72, s[52:55], s95 offen offset:2048 lds\nQmava 92,32,156\nQda 120,49,2048\nv_mul_f32 v191, v55, v191\nQmava 96,36,156\n"
"Qk 184,184,185\nQk 185,186,187\nQmava 100,20,160\nQda 124,49,3072\nQk 186,188,189\nQmava 104,24,160\nbuffer_load_dwordx4 v61, s[52:55], s95 offen offset:3072 lds\nQmava 108,28,160\n"
"Qda 128,49,4096\nQk 187,190,191\nQmava 176,32,160\nv_mul_f32 v216, v56, v216\nv_mul_f32 v217, v56, v217\nQmava 180,36,160\nQda 132,49,5120\ns_add_u32 m0, s91, 53248\n"
"Qmava 184,20,164\nbuffer_load_dwordx4 v62, s[52:55], s95 offen lds\nQmava 188,24,164\nQda 136,49,6144\nv_mul_f32 v218, v56, v218\nQmava 192,28,164\nv_mul_f32 v219, v56, v219\nv_mul_f32 v220, v56, v220\n"
"Qmava 196,32,164\nbuffer_load_dwordx4 v63, s[52:55], s95 offen offset:1024 lds\nv_add_u32 v75, 1, v75\nQmava 200,36,164\ns_waitcnt vmcnt(21)\nds_write_b32 v73, v75\ns_waitcnt lgkmcnt(5)\nQmava 0,0,112\n"
"Qtv 20,59,55296\nv_mul_f32 v221, v56, v221\nQmava 4,4,112\nbuffer_store_dwordx4 v[176:179], v60, s[56:59], s88 offen offset:3072 sc1\nQmava 8,8,112\nQtv 22,59,60416\nv_mul_f32 v222, v56, v222\nQmava 12,12,112\n"
"Qtv 24,59,55552\nv_mul_f32 v223, v56, v223\nQmava 16,16,112\nQk 216,216,217\nQk 217,218,219\nQmava 20,0,116\nQtv 26,59,60672\nQk 218,220,221\n"
"Qmava 24,4,116\nQtv 28,59,55808\nQk 219,222,223\nQmava 28,8,116\nds_read_b32 v69, v74\nv_mul_f32 v248, v57, v248\nQmava 32,12,116\nv_mul_f32 v249, v57, v249\n"
"v_mul_f32 v250, v57, v250\nQmava 36,16,116\nQtv 30,59,60928\nv_mul_f32 v251, v57, v251\nQmava 40,0,120\nQtv 32,59,56064\nv_mul_f32 v252, v57, v252\nQmava 44,4,120\n"
"s_waitcnt lgkmcnt(2)\nv_cmp_gt_u32 vcc, v75, v69\nQmava 48,8,120\ns_cbranch_vccnz .Lso4\n.Lro4:\nv_mul_f32 v253, v57, v253\nQmava 52,12,120\nQtv 34,59,61184\n"
"s_add_u32 m0, s91, 73728\ns_mul_i32 s94, s31, 10\nQmava 56,16,120\nbuffer_load_dwordx4 v70, s[48:51], s94 offen lds\nQmava 60,0,124\nv_mul_f32 v254, v57, v254\nv_mul_f32 v255, v57, v255\nQmava 64,4,124\n"
"Qtv 36,59,56320\nQk 248,248,249\nQmava 68,8,124\nQtv 38,59,61440\nQk 249,250,251\nQmava 72,12,124\nbuffer_load_dwordx4 v71, s[48:51], s94 offen offset:1024 lds\nQmava 76,16,124\n"
"Qk 250,252,253\nQk 251,254,255\nQmava 80,0,128\nQda 140,49,24576\nv_accvgpr_read_b32 v40, a232\nQmava 84,4,128\nQda 144,49,25600\ns_mul_i32 s95, s90, 10\n"
"Qmava 88,8,128\nbuffer_load_dwordx4 v72, s[52:55], s95 offen offset:2048 lds\nQmava 92,12,128\nQda 148,49,26624\nv_accvgpr_read_b32 v41, a233\nQmava 96,16,128\nv_accvgpr_read_b32 v42, a234\nv_accvgpr_read_b32 v43, a235\n"
"Qmava 100,0,132\nQda 152,49,27648\nv_accvgpr_read_b32 v44, a236\nQmava 104,4,132\nbuffer_load_dwordx4 v61, s[52:55], s95 offen offset:3072 lds\nQmava 108,8,132\nQda 156,49,28672\nv_accvgpr_read_b32 v45, a237\n"
"Qmava 176,12,132\nv_accvgpr_read_b32 v46, a238\nv_accvgpr_read_b32 v47, a239\nQmava 180,16,132\nQda 160,49,29696\ns_add_u32 m0, s91, 77824\nQmava 184,0,136\nbuffer_load_dwordx4 v62, s[52:55], s95 offen lds\n"
"Qmava 188,4,136\nQda 164,49,30720\nv_mul_f32 v40, v58, v40\nQmava 192,8,136\nv_mul_f32 v41, v58, v41\nv_mul_f32 v42, v58, v42\nQmava 196,12,136\nbuffer_load_dwordx4 v63, s[52:55], s95 offen offset:1024 lds\n"
"v_add_u32 v75, 1, v75\nQmava 200,16,136\ns_waitcnt vmcnt(21)\nds_write_b32 v73, v75\ns_waitcnt lgkmcnt(5)\nQmava 0,20,140\nQtv 0,50,31744\ns_add_u32 s88, s71, 4096\n"
"Qmava 4,24,140\nbuffer_store_dwordx4 v[208:211], v60, s[56:59], s88 offen offset:0 sc1\nQmava 8,28,140\nQtv 2,50,36864\nv_mul_f32 v43, v58, v43\nQmava 12,32,140\nQtv 4,50,32000\nv_mul_f32 v44, v58, v44\n"
"Qmava 16,36,140\nv_mul_f32 v45, v58, v45\nv_mul_f32 v46, v58, v46\nQmava 20,20,144\nQtv 6,50,37120\nv_mul_f32 v47, v58, v47\nQmava 24,24,144\nQtv 8,50,32256\n"
"Qk 116,40,41\nQmava 28,28,144\nds_read_b32 v69, v74\nQk 117,42,43\nQmava 32,32,144\nQk 118,44,45\nQk 119,46,47\nQmava 36,36,144\n"
"Qtv 10,50,37376\nv_mul_f32 v96, v52, v96\nQmava 40,20,148\nQtv 12,50,32512\nv_mul_f32 v97, v52, v97\nQmava 44,24,148\ns_waitcnt lgkmcnt(2)\nv_cmp_gt_u32 vcc, v75, v69\n"
"Qmava 48,28,148\ns_cbranch_vccnz .Lso5\n.Lro5:\nv_mul_f32 v98, v52, v98\nQmava 52,32,148\nQtv 14,50,37632\ns_add_u32 m0, s91, 98304\ns_mul_i32 s94, s31, 11\n"
"Qmava 56,36,148\nbuffer_load_dwordx4 v70, s[48:51], s94 offen lds\nQmava 60,20,152\nv_mul_f32 v99, v52, v99\nv_mul_f32 v100, v52, v100\nQmava 64,24,152\nQtv 16,50,32768\nv_mul_f32 v101, v52, v101\n"
"Qmava 68,28,152\nQtv 18,50,37888\nv_mul_f32 v102, v52, v102\nQmava 72,32,152\nbuffer_load_dwordx4 v71, s[48:51], s94 offen offset:1024 lds\nQmava 76,36,152\nv_mul_f32 v103, v52, v103\nQk 96,96,97\n"
"Qmava 80,20,156\nQda 112,49,58368\nQk 97,98,99\nQmava 84,24,156\nQda 116,49,59392\ns_mul_i32 s95, s90, 11\nQmava 88,28,156\nbuffer_load_dwordx4 v72, s[52:55], s95 offen offset:2048 lds\n"
"Qmava 92,32,156\nQda 120,49,60416\nQk 98,100,101\nQmava 96,36,156\nQk 99,102,103\nv_mul_f32 v128, v53, v128\nQmava 100,20,160\nQda 124,49,61440\n"
"v_mul_f32 v129, v53, v129\nQmava 104,24,160\nbuffer_load_dwordx4 v61, s[52:55], s95 offen offset:3072 lds\nQmava 108,28,160\nQda 128,49,62464\nv_mul_f32 v130, v53, v130\nQmava 176,32,160\nv_mul_f32 v131, v53, v131\n"
"v_mul_f32 v132, v53, v132\nQmava 180,36,160\nQda 132,49,63488\ns_add_u32 m0, s91, 102400\nQmava 184,20,164\nbuffer_load_dwordx4 v62, s[52:55], s95 offen lds\nQmava 188,24,164\nQda 136,49,64512\n"
"v_mul_f32 v133, v53, v133\nQmava 192,28,164\nv_mul_f32 v134, v53, v134\nv_mul_f32 v135, v53, v135\nQmava 196,32,164\nbuffer_load_dwordx4 v63, s[52:55], s95 offen offset:1024 lds\nv_add_u32 v75, 1, v75\nQmava 200,36,164\n"
"s_waitcnt vmcnt(21)\nds_write_b32 v73, v75\ns_waitcnt lgkmcnt(5)\nQmava 0,0,112\nQtv 20,64,14336\nQk 128,128,129\nQmava 4,4,112\nbuffer_store_dwordx4 v[240:243], v60, s[56:59], s88 offen offset:1024 sc1\n"
"Qmava 8,8,112\nQtv 22,64,19456\nQk 129,130,131\nQmava 12,12,112\nQtv 24,64,14592\nQk 130,132,133\nQmava 16,16,112\nQk 131,134,135\n"
"v_mul_f32 v160, v54, v160\nQmava 20,0,116\nQtv 26,64,19712\nv_mul_f32 v161, v54, v161\nQmava 24,4,116\nQtv 28,64,14848\nv_mul_f32 v162, v54, v162\nQmava 28,8,116\n"
"ds_read_b32 v69, v74\nv_mul_f32 v163, v54, v163\nQmava 32,12,116\nv_mul_f32 v164, v54, v164\nv_mul_f32 v165, v54, v165\nQmava 36,16,116\nQtv 30,64,19968\nv_mul_f32 v166, v54, v166\n"
"Qmava 40,0,120\nQtv 32,64,15104\nv_mul_f32 v167, v54, v167\nQmava 44,4,120\ns_waitcnt lgkmcnt(2)\nv_cmp_gt_u32 vcc, v75, v69\nQmava 48,8,120\ns_cbranch_vccnz .Lso6\n"
".Lro6:\nQk 160,160,161\nQmava 52,12,120\nQtv 34,64,20224\ns_add_u32 m0, s91, 132096\ns_mul_i32 s94, s31, 12\nQmava 56,16,120\nbuffer_load_dwordx4 v70, s[48:51], s94 offen lds\n"
"Qmava 60,0,124\nQk 161,162,163\nQk 162,164,165\nQmava 64,4,124\nQtv 36,64,15360\nQk 163,166,167\nQmava 68,8,124\nQtv 38,64,20480\n"
"v_mul_f32 v192, v55, v192\nQmava 72,12,124\nbuffer_load_dwordx4 v71, s[48:51], s94 offen offset:1024 lds\nQmava 76,16,124\nv_mul_f32 v193, v55, v193\nv_mul_f32 v194, v55, v194\nQmava 80,0,128\nQda 140,48,0\n"
"v_mul_f32 v195, v55, v195\nQmava 84,4,128\nQda 144,48,1024\ns_mul_i32 s95, s90, 12\nQmava 88,8,128\nbuffer_load_dwordx4 v72, s[52:55], s95 offen offset:2048 lds\nQmava 92,12,128\nQda 148,48,2048\n"
"v_mul_f32 v196, v55, v196\nQmava 96,16,128\nv_mul_f32 v197, v55, v197\nv_mul_f32 v198, v55, v198\nQmava 100,0,132\nQda 152,48,3072\nv_mul_f32 v199, v55, v199\nQmava 104,4,132\n"
"buffer_load_dwordx4 v61, s[52:55], s95 offen offset:3072 lds\nQmava 108,8,132\nQda 156,48,4096\nQk 192,192,193\nQmava 176,12,132\nQk 193,194,195\nQk 194,196,197\nQmava 180,16,132\n"
"Qda 160,48,5120\ns_add_u32 m0, s91, 136192\nQmava 184,0,136\nbuffer_load_dwordx4 v62, s[52:55], s95 offen lds\nQmava 188,4,136\nQda 164,48,6144\nQk 195,198,199\nQmava 192,8,136\n"
"v_mul_f32 v224, v56, v224\nv_mul_f32 v225, v56, v225\nQmava 196,12,136\nbuffer_load_dwordx4 v63, s[52:55], s95 offen offset:1024 lds\nv_add_u32 v75, 1, v75\nQmava 200,16,136\ns_waitcnt vmcnt(21)\nds_write_b32 v73, v75\n"
"s_waitcnt lgkmcnt(5)\nQmava 0,20,140\nQtv 0,64,38912\nv_mul_f32 v226, v56, v226\nQmava 4,24,140\nbuffer_store_dwordx4 v[84:87], v60, s[56:59], s88 offen offset:2048 sc1\nQmava 8,28,140\nQtv 2,64,44032\n"
"v_mul_f32 v227, v56, v227\nQmava 12,32,140\nQtv 4,64,39168\nv_mul_f32 v228, v56, v228\nQmava 16,36,140\nv_mul_f32 v229, v56, v229\nv_mul_f32 v230, v56, v230\nQmava 20,20,144\n"
"Qtv 6,64,44288\nv_mul_f32 v231, v56, v231\nQmava 24,24,144\nQtv 8,64,39424\nQk 224,224,225\nQmava 28,28,144\nds_read_b32 v69, v74\nQk 225,226,227\n"
"Qmava 32,32,144\nQk 226,228,229\nQk 227,230,231\nQmava 36,36,144\nQtv 10,64,44544\nv_accvgpr_read_b32 v40, a208\nQmava 40,20,148\nQtv 12,64,39680\n"
"v_accvgpr_read_b32 v41, a209\nQmava 44,24,148\ns_waitcnt lgkmcnt(2)\nv_cmp_gt_u32 vcc, v75, v69\nQmava 48,28,148\ns_cbranch_vccnz .Lso7\n.Lro7:\nv_accvgpr_read_b32 v42, a210\n"
"Qmava 52,32,148\nQtv 14,64,44800\ns_add_u32 m0, s91, 0\ns_mul_i32 s94, s31, 13\nQmava 56,36,148\nbuffer_load_dwordx4 v70, s[48:51], s94 offen lds\nQmava 60,20,152\nv_accvgpr_read_b32 v43, a211\n"
"v_accvgpr_read_b32 v44, a212\nQmava 64,24,152\nQtv 16,64,39936\nv_accvgpr_read_b32 v45, a213\nQmava 68,28,152\nQtv 18,64,45056\nv_accvgpr_read_b32 v46, a214\nQmava 72,32,152\n"
"buffer_load_dwordx4 v71, s[48:51], s94 offen offset:1024 lds\nQmava 76,36,152\nv_accvgpr_read_b32 v47, a215\nv_mul_f32 v40, v57, v40\nQmava 80,20,156\nQda 112,48,24576\nv_mul_f32 v41, v57, v41\nQmava 84,24,156\n"
"Qda 116,48,25600\ns_mul_i32 s95, s90, 13\nQmava 88,28,156\nbuffer_load_dwordx4 v72, s[52:55], s95 offen offset:2048 lds\nQmava 92,32,156\nQda 120,48,26624\nv_mul_f32 v42, v57, v42\nQmava 96,36,156\n"
"v_mul_f32 v43, v57, v43\nv_mul_f32 v44, v57, v44\nQmava 100,20,160\nQda 124,48,27648\nv_mul_f32 v45, v57, v45\nQmava 104,24,160\nbuffer_load_dwordx4 v61, s[52:55], s95 offen offset:3072 lds\nQmava 108,28,160\n"
"Qda 128,48,28672\nv_mul_f32 v46, v57, v46\nQmava 176,32,160\nv_mul_f32 v47, v57, v47\nQk 148,40,41\nQmava 180,36,160\nQda 132,48,29696\ns_add_u32 m0, s91, 4096\n"
"Qmava 184,20,164\nbuffer_load_dwordx4 v62, s[52:55], s95 offen lds\nQmava 188,24,164\nQda 136,48,30720\nQk 149,42,43\nQmava 192,28,164\nQk 150,44,45\nQk 151,46,47\n"
"Qmava 196,32,164\nbuffer_load_dwordx4 v63, s[52:55], s95 offen offset:1024 lds\nv_add_u32 v75, 1, v75\nQmava 200,36,164\ns_waitcnt vmcnt(21)\nds_write_b32 v73, v75\ns_waitcnt lgkmcnt(5)\nQmava 0,0,112\n"
"Qtv 20,59,6144\ns_add_u32 s88, s71, 14336\nQmava 4,4,112\nbuffer_store_dwordx4 v[88:91], v60, s[56:59], s88 offen offset:0 sc1\nQmava 8,8,112\nQtv 22,59,11264\nv_accvgpr_read_b32 v40, a240\nQmava 12,12,112\n"
"Qtv 24,59,6400\nv_accvgpr_read_b32 v41, a241\nQmava 16,16,112\nv_accvgpr_read_b32 v42, a242\nv_accvgpr_read_b32 v43, a243\nQmava 20,0,116\nQtv 26,59,11520\nv_accvgpr_read_b32 v44, a244\n"
"Qmava 24,4,116\nQtv 28,59,6656\nv_accvgpr_read_b32 v45, a245\nQmava 28,8,116\nds_read_b32 v69, v74\nv_accvgpr_read_b32 v46, a246\nQmava 32,12,116\nv_accvgpr_read_b32 v47, a247\n"
"v_mul_f32 v40, v58, v40\nQmava 36,16,116\nQtv 30,59,11776\nv_mul_f32 v41, v58, v41\nQmava 40,0,120\nQtv 32,59,6912\nv_mul_f32 v42, v58, v42\nQmava 44,4,120\n"
"s_waitcnt lgkmcnt(2)\nv_cmp_gt_u32 vcc, v75, v69\nQmava 48,8,120\ns_cbranch_vccnz .Lso8\n.Lro8:\nv_mul_f32 v43, v58, v43\nQmava 52,12,120\nQtv 34,59,12032\n"
"s_add_u32 m0, s91, 24576\ns_mul_i32 s94, s31, 14\nQmava 56,16,120\nbuffer_load_dwordx4 v70, s[48:51], s94 offen lds\nQmava 60,0,124\nv_mul_f32 v44, v58, v44\nv_mul_f32 v45, v58, v45\nQmava 64,4,124\n"
"Qtv 36,59,7168\nv_mul_f32 v46, v58, v46\nQmava 68,8,124\nQtv 38,59,12288\nv_mul_f32 v47, v58, v47\nQmava 72,12,124\nbuffer_load_dwordx4 v71, s[48:51], s94 offen offset:1024 lds\nQmava 76,16,124\n"
"Qk 180,40,41\nQk 181,42,43\nQmava 80,0,128\nQda 140,48,49152\nQk 182,44,45\nQmava 84,4,128\nQda 144,48,50176\ns_mul_i32 s95, s90, 14\n"
"Qmava 88,8,128\nbuffer_load_dwordx4 v72, s[52:55], s95 offen offset:2048 lds\nQmava 92,12,128\nQda 148,48,51200\nQk 183,46,47\nQmava 96,16,128\nv_mul_f32 v104, v52, v104\nv_mul_f32 v105, v52, v105\n"
"Qmava 100,0,132\nQda 152,48,52224\nv_mul_f32 v106, v52, v106\nQmava 104,4,132\nbuffer_load_dwordx4 v61, s[52:55], s95 offen offset:3072 lds\nQmava 108,8,132\nQda 156,48,53248\nv_mul_f32 v107, v52, v107\n"
"Qmava 176,12,132\nv_mul_f32 v108, v52, v108\nv_mul_f32 v109, v52, v109\nQmava 180,16,132\nQda 160,48,54272\ns_add_u32 m0, s91, 28672\nQmava 184,0,136\nbuffer_load_dwordx4 v62, s[52:55], s95 offen lds\n"
"Qmava 188,4,136\nQda 164,48,55296\nv_mul_f32 v110, v52, v110\nQmava 192,8,136\nv_mul_f32 v111, v52, v111\nQk 104,104,105\nQmava 196,12,136\nbuffer_load_dwordx4 v63, s[52:55], s95 offen offset:1024 lds\n"
"v_add_u32 v75, 1, v75\nQmava 200,16,136\ns_waitcnt vmcnt(21)\nds_write_b32 v73, v75\ns_waitcnt lgkmcnt(5)\nQmava 0,20,140\nQtv 0,59,30720\nQk 105,106,107\n"
"Qmava 4,24,140\nbuffer_store_dwordx4 v[120:123], v60, s[56:59], s88 offen offset:1024 sc1\nQmava 8,28,140\nQtv 2,59,35840\nQk 106,108,109\nQmava 12,32,140\nQtv 4,59,30976\nQk 107,110,111\n"
"Qmava 16,36,140\nv_mul_f32 v136, v53, v136\nv_mul_f32 v137, v53, v137\nQmava 20,20,144\nQtv 6,59,36096\nv_mul_f32 v138, v53, v138\nQmava 24,24,144\nQtv 8,59,31232\n"
"v_mul_f32 v139, v53, v139\nQmava 28,28,144\nds_read_b32 v69, v74\nv_mul_f32 v140, v53, v140\nQmava 32,32,144\nv_mul_f32 v141, v53, v141\nv_mul_f32 v142, v53, v142\nQmava 36,36,144\n"
"Qtv 10,59,36352\nv_mul_f32 v143, v53, v143\nQmava 40,20,148\nQtv 12,59,31488\nQk 136,136,137\nQmava 44,24,148\ns_waitcnt lgkmcnt(2)\nv_cmp_gt_u32 vcc, v75, v69\n"
"Qmava 48,28,148\ns_cbranch_vccnz .Lso9\n.Lro9:\nQk 137,138,139\nQmava 52,32,148\nQtv 14,59,36608\ns_add_u32 m0, s91, 49152\ns_mul_i32 s94, s31, 15\n"
"Qmava 56,36,148\nbuffer_load_dwordx4 v70, s[48:51], s94 offen lds\nQmava 60,20,152\nQk 138,140,141\nQk 139,142,143\nQmava 64,24,152\nQtv 16,59,31744\nv_mul_f32 v168, v54, v168\n"
"Qmava 68,28,152\nQtv 18,59,36864\nv_mul_f32 v169, v54, v169\nQmava 72,32,152\nbuffer_load_dwordx4 v71, s[48:51], s94 offen offset:1024 lds\nQmava 76,36,152\nv_mul_f32 v170, v54, v170\nv_mul_f32 v171, v54, v171\n"
"Qmava 80,20,156\nQda 112,49,0\nv_mul_f32 v172, v54, v172\nQmava 84,24,156\nQda 116,49,1024\ns_mul_i32 s95, s90, 15\nQmava 88,28,156\nbuffer_load_dwordx4 v72, s[52:55], s95 offen offset:2048 lds\n"
"Qmava 92,32,156\nQda 120,49,2048\nv_mul_f32 v173, v54, v173\nQmava 96,36,156\nv_mul_f32 v174, v54, v174\nv_mul_f32 v175, v54, v175\nQmava 100,20,160\nQda 124,49,3072\n"
"Qk 168,168,169\nQmava 104,24,160\nbuffer_load_dwordx4 v61, s[52:55], s95 offen offset:3072 lds\nQmava 108,28,160\nQda 128,49,4096\nQk 169,170,171\nQmava 176,32,160\nQk 170,172,173\n"
"Qk 171,174,175\nQmava 180,36,160\nQda 132,49,5120\ns_add_u32 m0, s91, 53248\nQmava 184,20,164\nbuffer_load_dwordx4 v62, s[52:55], s95 offen lds\nQmava 188,24,164\nQda 136,49,6144\n"
"v_mul_f32 v200, v55, v200\nQmava 192,28,164\nv_mul_f32 v201, v55, v201\nv_mul_f32 v202, v55, v202\nQmava 196,32,164\nbuffer_load_dwordx4 v63, s[52:55], s95 offen offset:1024 lds\nv_add_u32 v75, 1, v75\nQmava 200,36,164\n"
"s_waitcnt vmcnt(21)\nds_write_b32 v73, v75\ns_waitcnt lgkmcnt(5)\nQmava 0,0,112\nQtv 20,59,55296\nv_mul_f32 v203, v55, v203\nQmava 4,4,112\nbuffer_store_dwordx4 v[152:155], v60, s[56:59], s88 offen offset:2048 sc1\n"
"Qmava 8,8,112\nQtv 22,59,60416\nv_mul_f32 v204, v55, v204\nQmava 12,12,112\nQtv 24,59,55552\nv_mul_f32 v205, v55, v205\nQmava 16,16,112\nv_mul_f32 v206, v55, v206\n"
"v_mul_f32 v207, v55, v207\nQmava 20,0,116\nQtv 26,59,60672\nQk 200,200,201\nQmava 24,4,116\nQtv 28,59,55808\nQk 201,202,203\nQmava 28,8,116\n"
"ds_read_b32 v69, v74\nQk 202,204,205\nQmava 32,12,116\nQk 203,206,207\nv_mul_f32 v232, v56, v232\nQmava 36,16,116\nQtv 30,59,60928\nv_mul_f32 v233, v56, v233\n"
"Qmava 40,0,120\nQtv 32,59,56064\nv_mul_f32 v234, v56, v234\nQmava 44,4,120\ns_waitcnt lgkmcnt(2)\nv_cmp_gt_u32 vcc, v75, v69\nQmava 48,8,120\ns_cbranch_vccnz .Lso10\n"
".Lro10:\nv_mul_f32 v235, v56, v235\nQmava 52,12,120\nQtv 34,59,61184\ns_add_u32 m0, s91, 73728\ns_mul_i32 s94, s31, 16\nQmava 56,16,120\nbuffer_load_dwordx4 v70, s[48:51], s94 offen lds\n"
"Qmava 60,0,124\nv_mul_f32 v236, v56, v236\nv_mul_f32 v237, v56, v237\nQmava 64,4,124\nQtv 36,59,56320\nv_mul_f32 v238, v56, v238\nQmava 68,8,124\nQtv 38,59,61440\n"
"v_mul_f32 v239, v56, v239\nQmava 72,12,124\nbuffer_load_dwordx4 v71, s[48:51], s94 offen offset:1024 lds\nQmava 76,16,124\nQk 232,232,233\nQk 233,234,235\nQmava 80,0,128\nQda 140,49,24576\n"
"Qk 234,236,237\nQmava 84,4,128\nQda 144,49,25600\ns_mul_i32 s95, s90, 16\nQmava 88,8,128\nbuffer_load_dwordx4 v72, s[52:55], s95 offen offset:2048 lds\nQmava 92,12,128\nQda 148,49,26624\n"
"Qk 235,238,239\nQmava 96,16,128\nv_accvgpr_read_b32 v40, a216\nv_accvgpr_read_b32 v41, a217\nQmava 100,0,132\nQda 152,49,27648\nv_accvgpr_read_b32 v42, a218\nQmava 104,4,132\n"
"buffer_load_dwordx4 v61, s[52:55], s95 offen offset:3072 lds\nQmava 108,8,132\nQda 156,49,28672\nv_accvgpr_read_b32 v43, a219\nQmava 176,12,132\nv_accvgpr_read_b32 v44, a220\nv_accvgpr_read_b32 v45, a221\nQmava 180,16,132\n"
"Qda 160,49,29696\ns_add_u32 m0, s91, 77824\nQmava 184,0,136\nbuffer_load_dwordx4 v62, s[52:55], s95 offen lds\nQmava 188,4,136\nQda 164,49,30720\nv_accvgpr_read_b32 v46, a222\nQmava 192,8,136\n"
"v_accvgpr_read_b32 v47, a223\nv_mul_f32 v40, v57, v40\nQmava 196,12,136\nbuffer_load_dwordx4 v63, s[52:55], s95 offen offset:1024 lds\nv_add_u32 v75, 1, v75\nQmava 200,16,136\ns_waitcnt vmcnt(21)\nds_write_b32 v73, v75\n"
"s_waitcnt lgkmcnt(5)\nQmava 0,20,140\nQtv 0,50,31744\nv_mul_f32 v41, v57, v41\nQmava 4,24,140\nbuffer_store_dwordx4 v[184:187], v60, s[56:59], s88 offen offset:3072 sc1\nQmava 8,28,140\nQtv 2,50,36864\n"
"v_mul_f32 v42, v57, v42\nQmava 12,32,140\nQtv 4,50,32000\nv_mul_f32 v43, v57, v43\nQmava 16,36,140\nv_mul_f32 v44, v57, v44\nv_mul_f32 v45, v57, v45\nQmava 20,20,144\n"
"Qtv 6,50,37120\nv_mul_f32 v46, v57, v46\nQmava 24,24,144\nQtv 8,50,32256\nv_mul_f32 v47, v57, v47\nQmava 28,28,144\nds_read_b32 v69, v74\nQk 212,40,41\n"
"Qmava 32,32,144\nQk 213,42,43\nQk 214,44,45\nQmava 36,36,144\nQtv 10,50,37376\nQk 215,46,47\nQmava 40,20,148\nQtv 12,50,32512\n"
"v_accvgpr_read_b32 v40, a248\nQmava 44,24,148\ns_waitcnt lgkmcnt(2)\nv_cmp_gt_u32 vcc, v75, v69\nQmava 48,28,148\ns_cbranch_vccnz .Lso11\n.Lro11:\nv_accvgpr_read_b32 v41, a249\n"
"Qmava 52,32,148\nQtv 14,50,37632\ns_add_u32 m0, s91, 98304\ns_mul_i32 s94, s31, 17\nQmava 56,36,148\nbuffer_load_dwordx4 v70, s[48:51], s94 offen lds\nQmava 60,20,152\nv_accvgpr_read_b32 v42, a250\n"
"v_accvgpr_read_b32 v43, a251\nQmava 64,24,152\nQtv 16,50,32768\nv_accvgpr_read_b32 v44, a252\nQmava 68,28,152\nQtv 18,50,37888\nv_accvgpr_read_b32 v45, a253\nQmava 72,32,152\n"
"buffer_load_dwordx4 v71, s[48:51], s94 offen offset:1024 lds\nQmava 76,36,152\nv_accvgpr_read_b32 v46, a254\nv_accvgpr_read_b32 v47, a255\nQmava 80,20,156\nQda 112,49,58368\nv_mul_f32 v40, v58, v40\nQmava 84,24,156\n"
"Qda 116,49,59392\ns_mul_i32 s95, s90, 17\nQmava 88,28,156\nbuffer_load_dwordx4 v72, s[52:55], s95 offen offset:2048 lds\nQmava 92,32,156\nQda 120,49,60416\nv_mul_f32 v41, v58, v41\nQmava 96,36,156\n"
"v_mul_f32 v42, v58, v42\nv_mul_f32 v43, v58, v43\nQmava 100,20,160\nQda 124,49,61440\nv_mul_f32 v44, v58, v44\nQmava 104,24,160\nbuffer_load_dwordx4 v61, s[52:55], s95 offen offset:3072 lds\nQmava 108,28,160\n"
"Qda 128,49,62464\nv_mul_f32 v45, v58, v45\nQmava 176,32,160\nv_mul_f32 v46, v58, v46\nv_mul_f32 v47, v58, v47\nQmava 180,36,160\nQda 132,49,63488\ns_add_u32 m0, s91, 102400\n"
"Qmava 184,20,164\nbuffer_load_dwordx4 v62, s[52:55], s95 offen lds\nQmava 188,24,164\nQda 136,49,64512\nQk 244,40,41\nQmava 192,28,164\nQk 245,42,43\nQk 246,44,45\n"
"Qmava 196,32,164\nbuffer_load_dwordx4 v63, s[52:55], s95 offen offset:1024 lds\nv_add_u32 v75, 1, v75\nQmava 200,36,164\ns_waitcnt vmcnt(21)\nds_write_b32 v73, v75\ns_waitcnt lgkmcnt(5)\nQmava 0,0,112\n"
"Qtv 20,64,14336\ns_add_u32 s88, s71, 18432\nQmava 4,4,112\nbuffer_store_dwordx4 v[216:219], v60, s[56:59], s88 offen offset:0 sc1\nQmava 8,8,112\nQtv 22,64,19456\nQk 247,46,47\nQmava 12,12,112\n"
"Qtv 24,64,14592\nQmava 16,16,112\nQmava 20,0,116\nQtv 26,64,19712\nQmava 24,4,116\nQtv 28,64,14848\nQmava 28,8,116\nds_read_b32 v69, v74\n"
"Qmava 32,12,116\nQtv 30,64,19968\nQmava 36,16,116\nQmava 40,0,120\nQtv 32,64,15104\nv_add_u32 v67, 2, v65\nQmava 44,4,120\ns_waitcnt lgkmcnt(2)\n"
"v_cmp_gt_u32 vcc, v75, v69\nQmava 48,8,120\ns_cbranch_vccnz .Lso12\n.Lro12:\nQmava 52,12,120\nbuffer_store_dword v67, v77, s[24:27], 0 offen sc1\ns_add_u32 m0, s91, 132096\ns_mul_i32 s94, s31, 18\n"
"Qmava 56,16,120\nbuffer_load_dwordx4 v70, s[48:51], s94 offen lds\nQmava 60,0,124\nQtv 34,64,20224\nQmava 64,4,124\nQtv 36,64,15360\nQmava 68,8,124\nQmava 72,12,124\n"
"buffer_load_dwordx4 v71, s[48:51], s94 offen offset:1024 lds\nQmava 76,16,124\nQtv 38,64,20480\nQmava 80,0,128\nQda 140,48,0\nQmava 84,4,128\nQda 144,48,1024\ns_mul_i32 s95, s90, 18\n"
"Qmava 88,8,128\nbuffer_load_dwordx4 v72, s[52:55], s95 offen offset:2048 lds\nQmava 92,12,128\nQmava 96,16,128\nQda 148,48,2048\nQmava 100,0,132\nQda 152,48,3072\nQmava 104,4,132\n"
"buffer_load_dwordx4 v61, s[52:55], s95 offen offset:3072 lds\nQmava 108,8,132\nQda 156,48,4096\nQmava 176,12,132\nQmava 180,16,132\nQda 160,48,5120\ns_add_u32 m0, s91, 136192\nQmava 184,0,136\n"
"buffer_load_dwordx4 v62, s[52:55], s95 offen lds\nQmava 188,4,136\nQda 164,48,6144\nQmava 192,8,136\nQmava 196,12,136\nbuffer_load_dwordx4 v63, s[52:55], s95 offen offset:1024 lds\nv_add_u32 v75, 1, v75\nQmava 200,16,136\n"
"s_waitcnt vmcnt(22)\nds_write_b32 v73, v75\ns_waitcnt lgkmcnt(5)\nQmava 0,20,140\nQtv 0,64,38912\nQmava 4,24,140\nbuffer_store_dwordx4 v[248:251], v60, s[56:59], s88 offen offset:1024 sc1\nQmava 8,28,140\n"
"Qtv 2,64,44032\nQmava 12,32,140\nQtv 4,64,39168\nQmava 16,36,140\nQmava 20,20,144\nQtv 6,64,44288\nQmava 24,24,144\nQtv 8,64,39424\n"
"Qmava 28,28,144\nds_read_b32 v69, v74\nQmava 32,32,144\nQmava 36,36,144\nQtv 10,64,44544\nQmava 40,20,148\nQtv 12,64,39680\nQmava 44,24,148\n"
"s_waitcnt lgkmcnt(2)\nv_cmp_gt_u32 vcc, v75, v69\nQmava 48,28,148\ns_cbranch_vccnz .Lso13\n.Lro13:\nQmava 52,32,148\nQtv 14,64,44800\ns_add_u32 m0, s91, 0\n"
"s_mul_i32 s94, s31, 19\nQmava 56,36,148\nbuffer_load_dwordx4 v70, s[48:51], s94 offen lds\nQmava 60,20,152\nQmava 64,24,152\nQtv 16,64,39936\nQmava 68,28,152\nQtv 18,64,45056\n"
"Qmava 72,32,152\nbuffer_load_dwordx4 v71, s[48:51], s94 offen offset:1024 lds\nQmava 76,36,152\nQmava 80,20,156\nQda 112,48,24576\nQmava 84,24,156\nQda 116,48,25600\ns_mul_i32 s95, s90, 19\n"
"Qmava 88,28,156\nbuffer_load_dwordx4 v72, s[52:55], s95 offen offset:2048 lds\nQmava 92,32,156\nQda 120,48,26624\nQmava 96,36,156\nQmava 100,20,160\nQda 124,48,27648\nQmava 104,24,160\n"
"buffer_load_dwordx4 v61, s[52:55], s95 offen offset:3072 lds\nQmava 108,28,160\nQda 128,48,28672\nQmava 176,32,160\nQmava 180,36,160\nQda 132,48,29696\ns_add_u32 m0, s91, 4096\nQmava 184,20,164\n"
"buffer_load_dwordx4 v62, s[52:55], s95 offen lds\nQmava 188,24,164\nQda 136,48,30720\nQmava 192,28,164\nQmava 196,32,164\nbuffer_load_dwordx4 v63, s[52:55], s95 offen offset:1024 lds\nv_add_u32 v75, 1, v75\nQmava 200,36,164\n"
"s_waitcnt vmcnt(22)\nds_write_b32 v73, v75\ns_waitcnt lgkmcnt(5)\nQmava 0,0,112\nQtv 20,59,6144\nQmava 4,4,112\nbuffer_store_dwordx4 v[116:119], v60, s[56:59], s88 offen offset:2048 sc1\nQmava 8,8,112\n"
"Qtv 22,59,11264\nQmava 12,12,112\nQtv 24,59,6400\nQmava 16,16,112\nQmava 20,0,116\nQtv 26,59,11520\nv_lshrrev_b32 v57, 8, v60\nQmava 24,4,116\n"
"Qtv 28,59,6656\ns_mul_i32 s68, s92, 20\nQmava 28,8,116\nds_read_b32 v69, v74\nv_accvgpr_read_b32 v92, a0\nQmava 32,12,116\nv_accvgpr_read_b32 v93, a1\nv_accvgpr_read_b32 v94, a2\n"
"Qmava 36,16,116\nQtv 30,59,11776\nv_accvgpr_read_b32 v95, a3\nQmava 40,0,120\nQtv 32,59,6912\nv_accvgpr_read_b32 v124, a4\nQmava 44,4,120\ns_waitcnt lgkmcnt(2)\n"
"v_cmp_gt_u32 vcc, v75, v69\nQmava 48,8,120\ns_cbranch_vccnz .Lso14\n.Lro14:\nv_accvgpr_read_b32 v125, a5\nQmava 52,12,120\nQtv 34,59,12032\ns_add_u32 m0, s91, 24576\n"
"s_mul_i32 s94, s31, 20\nQmava 56,16,120\nbuffer_load_dwordx4 v70, s[48:51], s94 offen lds\nQmava 60,0,124\nv_accvgpr_read_b32 v126, a6\nv_accvgpr_read_b32 v127, a7\nQmava 64,4,124\nQtv 36,59,7168\n"
"v_add_u32 v57, s68, v57\nQmava 68,8,124\nQtv 38,59,12288\nv_add_u32 v55, 0, v57\nQmava 72,12,124\nbuffer_load_dwordx4 v71, s[48:51], s94 offen offset:1024 lds\nQmava 76,16,124\nv_mul_u32_u24 v56, 205, v55\n"
"v_lshrrev_b32 v56, 11, v56\nQmava 80,0,128\nQda 140,48,49152\nv_mul_u32_u24 v54, 10, v56\nQmava 84,4,128\nQda 144,48,50176\ns_mul_i32 s95, s90, 20\nQmava 88,8,128\n"
"buffer_load_dwordx4 v72, s[52:55], s95 offen offset:2048 lds\nQmava 92,12,128\nQda 148,48,51200\nv_sub_u32 v55, v55, v54\nQmava 96,16,128\nv_lshlrev_b32 v55, 5, v55\nv_bfe_u32 v54, v60, 5, 3\nQmava 100,0,132\n"
"Qda 152,48,52224\nv_lshl_add_u32 v56, v56, 3, v54\nQmava 104,4,132\nbuffer_load_dwordx4 v61, s[52:55], s95 offen offset:3072 lds\nQmava 108,8,132\nQda 156,48,53248\nv_mul_u32_u24 v56, 1280, v56\nQmava 176,12,132\n"
"v_and_b32 v54, 16, v60\nv_add3_u32 v55, v55, v56, v54\nQmava 180,16,132\nQda 160,48,54272\ns_add_u32 m0, s91, 28672\nQmava 184,0,136\nbuffer_load_dwordx4 v62, s[52:55], s95 offen lds\nQmava 188,4,136\n"
"Qda 164,48,55296\nv_add_u32 v40, 4096, v55\nQmava 192,8,136\nv_add_u32 v55, 4, v57\nv_mul_u32_u24 v56, 205, v55\nQmava 196,12,136\nbuffer_load_dwordx4 v63, s[52:55], s95 offen offset:1024 lds\nv_add_u32 v75, 1, v75\n"
"Qmava 200,16,136\ns_waitcnt vmcnt(22)\nds_write_b32 v73, v75\ns_waitcnt lgkmcnt(5)\nQmvva 92,20,140\nQtv 0,59,30720\ns_add_u32 s88, s71, 28672\nQmvva 124,24,140\n"
"buffer_store_dwordx4 v[96:99], v60, s[56:59], s88 offen offset:0 sc1\nQmava 8,28,140\nQtv 2,59,35840\nv_lshrrev_b32 v56, 11, v56\nQmava 12,32,140\nQtv 4,59,30976\nv_mul_u32_u24 v54, 10, v56\nQmava 16,36,140\n"
"v_sub_u32 v55, v55, v54\nv_lshlrev_b32 v55, 5, v55\nQmava 20,20,144\nQtv 6,59,36096\nv_bfe_u32 v54, v60, 5, 3\nQmava 24,24,144\nQtv 8,59,31232\nv_lshl_add_u32 v56, v56, 3, v54\n"
"Qmava 28,28,144\nds_read_b32 v69, v74\nv_mul_u32_u24 v56, 1280, v56\nQmava 32,32,144\nv_and_b32 v54, 16, v60\nv_add3_u32 v55, v55, v56, v54\nQmava 36,36,144\nQtv 10,59,36352\n"
"v_accvgpr_read_b32 v156, a8\nQmava 40,20,148\nQtv 12,59,31488\nv_accvgpr_read_b32 v157, a9\nQmava 44,24,148\ns_waitcnt lgkmcnt(2)\nv_cmp_gt_u32 vcc, v75, v69\nQmava 48,28,148\n"
"s_cbranch_vccnz .Lso15\n.Lro15:\nv_accvgpr_read_b32 v158, a10\nQmava 52,32,148\nQtv 14,59,36608\ns_add_u32 m0, s91, 49152\ns_mul_i32 s94, s31, 21\nQmava 56,36,148\n"
"buffer_load_dwordx4 v70, s[48:51], s94 offen lds\nQmava 60,20,152\nv_accvgpr_read_b32 v159, a11\nv_accvgpr_read_b32 v188, a12\nQmava 64,24,152\nQtv 16,59,31744\nv_accvgpr_read_b32 v189, a13\nQmava 68,28,152\n"
"Qtv 18,59,36864\nv_accvgpr_read_b32 v190, a14\nQmava 72,32,152\nbuffer_load_dwordx4 v71, s[48:51], s94 offen offset:1024 lds\nQmava 76,36,152\nv_accvgpr_read_b32 v191, a15\nv_add_u32 v41, 3072, v55\nQmava 80,20,156\n"
"Qda 112,49,0\nv_add_u32 v55, 8, v57\nQmava 84,24,156\nQda 116,49,1024\ns_mul_i32 s95, s90, 21\nQmava 88,28,156\nbuffer_load_dwordx4 v72, s[52:55], s95 offen offset:2048 lds\nQmava 92,32,156\n"
"Qda 120,49,2048\nv_mul_u32_u24 v56, 205, v55\nQmava 96,36,156\nv_lshrrev_b32 v56, 11, v56\nv_mul_u32_u24 v54, 10, v56\nQmava 100,20,160\nQda 124,49,3072\nv_sub_u32 v55, v55, v54\n"
"Qmava 104,24,160\nbuffer_load_dwordx4 v61, s[52:55], s95 offen offset:3072 lds\nQmava 108,28,160\nQda 128,49,4096\nv_lshlrev_b32 v55, 5, v55\nQmava 176,32,160\nv_bfe_u32 v54, v60, 5, 3\nv_lshl_add_u32 v56, v56, 3, v54\n"
"Qmava 180,36,160\nQda 132,49,5120\ns_add_u32 m0, s91, 53248\nQmava 184,20,164\nbuffer_load_dwordx4 v62, s[52:55], s95 offen lds\nQmava 188,24,164\nQda 136,49,6144\nv_mul_u32_u24 v56, 1280, v56\n"
"Qmava 192,28,164\nv_and_b32 v54, 16, v60\nv_add3_u32 v55, v55, v56, v54\nQmava 196,32,164\nbuffer_load_dwordx4 v63, s[52:55], s95 offen offset:1024 lds\nv_add_u32 v75, 1, v75\nQmava 200,36,164\ns_waitcnt vmcnt(21)\n"
"ds_write_b32 v73, v75\ns_waitcnt lgkmcnt(5)\nQmvva 92,0,112\nQtv 20,59,55296\nv_add_u32 v42, 2048, v55\nQmvva 124,4,112\nbuffer_store_dwordx4 v[128:131], v60, s[56:59], s88 offen offset:1024 sc1\nQmvva 156,8,112\n"
"Qtv 22,59,60416\nv_add_u32 v55, 12, v57\nQmvva 188,12,112\nQtv 24,59,55552\nv_mul_u32_u24 v56, 205, v55\nQmava 16,16,112\nv_lshrrev_b32 v56, 11, v56\nv_mul_u32_u24 v54, 10, v56\n"
"Qmava 20,0,116\nQtv 26,59,60672\nv_sub_u32 v55, v55, v54\nQmava 24,4,116\nQtv 28,59,55808\nv_lshlrev_b32 v55, 5, v55\nQmava 28,8,116\nds_read_b32 v69, v74\n"
"v_bfe_u32 v54, v60, 5, 3\nQmava 32,12,116\nv_lshl_add_u32 v56, v56, 3, v54\nv_mul_u32_u24 v56, 1280, v56\nQmava 36,16,116\nQtv 30,59,60928\nv_and_b32 v54, 16, v60\nQmava 40,0,120\n"
"Qtv 32,59,56064\nv_add3_u32 v55, v55, v56, v54\nQmava 44,4,120\ns_waitcnt lgkmcnt(2)\nv_cmp_gt_u32 vcc, v75, v69\nQmava 48,8,120\ns_cbranch_vccnz .Lso16\n.Lro16:\n"
"v_accvgpr_read_b32 v220, a16\nQmava 52,12,120\nQtv 34,59,61184\ns_add_u32 m0, s91, 73728\ns_mul_i32 s94, s31, 22\nQmava 56,16,120\nbuffer_load_dwordx4 v70, s[48:51], s94 offen lds\nQmava 60,0,124\n"
"v_accvgpr_read_b32 v221, a17\nv_accvgpr_read_b32 v222, a18\nQmava 64,4,124\nQtv 36,59,56320\nv_accvgpr_read_b32 v223, a19\nQmava 68,8,124\nQtv 38,59,61440\nv_accvgpr_read_b32 v252, a20\n"
"Qmava 72,12,124\nbuffer_load_dwordx4 v71, s[48:51], s94 offen offset:1024 lds\nQmava 76,16,124\nv_accvgpr_read_b32 v253, a21\nv_accvgpr_read_b32 v254, a22\nQmava 80,0,128\nQda 140,49,24576\nv_accvgpr_read_b32 v255, a23\n"
"Qmava 84,4,128\nQda 144,49,25600\ns_mul_i32 s95, s90, 22\nQmava 88,8,128\nbuffer_load_dwordx4 v72, s[52:55], s95 offen offset:2048 lds\nQmava 92,12,128\nQda 148,49,26624\nv_add_u32 v43, 1024, v55\n"
"Qmava 96,16,128\nv_add_u32 v55, 16, v57\nv_mul_u32_u24 v56, 205, v55\nQmava 100,0,132\nQda 152,49,27648\nv_lshrrev_b32 v56, 11, v56\nQmava 104,4,132\nbuffer_load_dwordx4 v61, s[52:55], s95 offen offset:3072 lds\n"
"Qmava 108,8,132\nQda 156,49,28672\nv_mul_u32_u24 v54, 10, v56\nQmava 176,12,132\nv_sub_u32 v55, v55, v54\nv_lshlrev_b32 v55, 5, v55\nQmava 180,16,132\nQda 160,49,29696\n"
"s_add_u32 m0, s91, 77824\nQmava 184,0,136\nbuffer_load_dwordx4 v62, s[52:55], s95 offen lds\nQmava 188,4,136\nQda 164,49,30720\nv_bfe_u32 v54, v60, 5, 3\nQmava 192,8,136\nv_lshl_add_u32 v56, v56, 3, v54\n"
"v_mul_u32_u24 v56, 1280, v56\nQmava 196,12,136\nbuffer_load_dwordx4 v63, s[52:55], s95 offen offset:1024 lds\nv_add_u32 v75, 1, v75\nQmava 200,16,136\ns_waitcnt vmcnt(21)\nds_write_b32 v73, v75\ns_waitcnt lgkmcnt(5)\n"
"Qmvva 92,20,140\nQtv 0,50,31744\nv_and_b32 v54, 16, v60\nQmvva 124,24,140\nbuffer_store_dwordx4 v[160:163], v60, s[56:59], s88 offen offset:2048 sc1\nQmvva 156,28,140\nQtv 2,50,36864\nv_add3_u32 v55, v55, v56, v54\n"
"Qmvva 188,32,140\nQtv 4,50,32000\nv_add_u32 v44, 4096, v55\nQmvva 220,36,140\ns_mul_i32 s96, s70, 5120\ns_add_u32 s96, s96, %10\nQmvva 252,20,144\nQtv 6,50,37120\n"
"s_mul_i32 s68, s93, 10240\nQmava 24,24,144\nQtv 8,50,32256\ns_add_u32 s68, s68, 4096\nQmava 28,28,144\nds_read_b32 v69, v74\nv_add_u32 v45, s68, v64\nQmava 32,32,144\n"
"v_add_u32 v46, 61440, v45\ns_sub_u32 s68, 18432, s68\nQmava 36,36,144\nQtv 10,50,37376\nv_add_u32 v47, s68, v64\nQmava 40,20,148\nQtv 12,50,32512\nv_add_u32 v51, 61440, v47\n"
"Qmava 44,24,148\ns_waitcnt lgkmcnt(2)\nv_cmp_gt_u32 vcc, v75, v69\nQmava 48,28,148\ns_cbranch_vccnz .Lso17\n.Lro17:\ns_lshl_b32 s68, s70, 10\nQmava 52,32,148\n"
"Qtv 14,50,37632\ns_add_u32 m0, s91, 98304\ns_mul_i32 s94, s31, 23\nQmava 56,36,148\nbuffer_load_dwordx4 v70, s[48:51], s94 offen lds\nQmava 60,20,152\nv_accvgpr_read_b32 v100, a24\nv_accvgpr_read_b32 v101, a25\n"
"Qmava 64,24,152\nQtv 16,50,32768\nv_accvgpr_read_b32 v102, a26\nQmava 68,28,152\nQtv 18,50,37888\nv_accvgpr_read_b32 v103, a27\nQmava 72,32,152\nbuffer_load_dwordx4 v71, s[48:51], s94 offen offset:1024 lds\n"
"Qmava 76,36,152\nv_accvgpr_read_b32 v132, a28\nv_accvgpr_read_b32 v133, a29\nQmava 80,20,156\nQda 112,49,58368\nv_accvgpr_read_b32 v134, a30\nQmava 84,24,156\nQda 116,49,59392\n"
"s_mul_i32 s95, s90, 23\nQmava 88,28,156\nbuffer_load_dwordx4 v72, s[52:55], s95 offen offset:2048 lds\nQmava 92,32,156\nQda 120,49,60416\nv_accvgpr_read_b32 v135, a31\nQmava 96,36,156\ns_add_u32 s69, %10, 106496\n"
"v_add_u32 v54, s69, v60\nQmava 100,20,160\nQda 124,49,61440\nv_add_u32 v52, s68, v54\nQmava 104,24,160\nbuffer_load_dwordx4 v61, s[52:55], s95 offen offset:3072 lds\nQmava 108,28,160\nQda 128,49,62464\n"
"s_xor_b32 s68, s68, 2048\nQmava 176,32,160\nv_add_u32 v53, s68, v54\ns_mul_i32 s68, s93, 163840\nQmava 180,36,160\nQda 132,49,63488\ns_add_u32 m0, s91, 102400\nQmava 184,20,164\n"
"buffer_load_dwordx4 v62, s[52:55], s95 offen lds\nQmava 188,24,164\nQda 136,49,64512\nQmava 192,28,164\ns_add_u32 s62, s62, s68\ns_addc_u32 s63, s63, 0\nQmava 196,32,164\nbuffer_load_dwordx4 v63, s[52:55], s95 offen offset:1024 lds\n"
"v_add_u32 v75, 1, v75\nQmava 200,36,164\ns_waitcnt vmcnt(21)\nds_write_b32 v73, v75\ns_waitcnt lgkmcnt(5)\nQmvva 92,0,112\nQtv 20,64,14336\ns_sub_u32 s86, s86, s68\n"
"Qmvva 124,4,112\nbuffer_store_dwordx4 v[192:195], v60, s[56:59], s88 offen offset:3072 sc1\nQmvva 156,8,112\nQtv 22,64,19456\nQmvva 188,12,112\nQtv 24,64,14592\nQmvva 220,16,112\nQmvva 252,0,116\n"
"Qtv 26,64,19712\nQmvva 100,4,116\nQtv 28,64,14848\nQmvva 132,8,116\nds_read_b32 v69, v74\nQmava 32,12,116\nQmava 36,16,116\nQtv 30,64,19968\n"
"Qmava 40,0,120\nQtv 32,64,15104\nQmava 44,4,120\ns_waitcnt lgkmcnt(2)\nv_cmp_gt_u32 vcc, v75, v69\nQmava 48,8,120\ns_cbranch_vccnz .Lso18\n.Lro18:\n"
"Qmava 52,12,120\nQtv 34,64,20224\ns_add_u32 m0, s91, 132096\ns_mul_i32 s94, s31, 24\nQmava 56,16,120\nbuffer_load_dwordx4 v70, s[48:51], s94 offen lds\nQmava 60,0,124\nv_accvgpr_read_b32 v164, a32\n"
"v_accvgpr_read_b32 v165, a33\nQmava 64,4,124\nQtv 36,64,15360\nv_accvgpr_read_b32 v166, a34\nQmava 68,8,124\nQtv 38,64,20480\nv_accvgpr_read_b32 v167, a35\nQmava 72,12,124\n"
"buffer_load_dwordx4 v71, s[48:51], s94 offen offset:1024 lds\nQmava 76,16,124\nv_accvgpr_read_b32 v196, a36\nv_accvgpr_read_b32 v197, a37\nQmava 80,0,128\nQda 140,48,0\nv_accvgpr_read_b32 v198, a38\nQmava 84,4,128\n"
"Qda 144,48,1024\ns_mul_i32 s95, s90, 24\nQmava 88,8,128\nbuffer_load_dwordx4 v72, s[52:55], s95 offen offset:2048 lds\nQmava 92,12,128\nQda 148,48,2048\nv_accvgpr_read_b32 v199, a39\nQmava 96,16,128\n"
"Qmava 100,0,132\nQda 152,48,3072\nQmava 104,4,132\nbuffer_load_dwordx4 v61, s[52:55], s95 offen offset:3072 lds\nQmava 108,8,132\nQda 156,48,4096\nQmava 176,12,132\nQmava 180,16,132\n"
"Qda 160,48,5120\ns_add_u32 m0, s91, 136192\nQmava 184,0,136\nbuffer_load_dwordx4 v62, s[52:55], s95 offen lds\nQmava 188,4,136\nQda 164,48,6144\nQmava 192,8,136\nQmava 196,12,136\n"
"buffer_load_dwordx4 v63, s[52:55], s95 offen offset:1024 lds\nv_add_u32 v75, 1, v75\nQmava 200,16,136\ns_waitcnt vmcnt(21)\nds_write_b32 v73, v75\ns_waitcnt lgkmcnt(5)\nQmvva 92,20,140\nQtv 0,64,38912\n"
"s_add_u32 s88, s71, 32768\nQmvva 124,24,140\nbuffer_store_dwordx4 v[224:227], v60, s[56:59], s88 offen offset:0 sc1\nQmvva 156,28,140\nQtv 2,64,44032\nQmvva 188,32,140\nQtv 4,64,39168\nQmvva 220,36,140\n"
"Qmvva 252,20,144\nQtv 6,64,44288\nQmvva 100,24,144\nQtv 8,64,39424\nQmvva 132,28,144\nds_read_b32 v69, v74\nQmvva 164,32,144\nQtv 10,64,44544\n"
"Qmvva 196,36,144\nQmava 40,20,148\nQtv 12,64,39680\nv_add_u32 v67, 3, v65\nQmava 44,24,148\ns_waitcnt lgkmcnt(2)\nv_cmp_gt_u32 vcc, v75, v69\nQmava 48,28,148\n"
"s_cbranch_vccnz .Lso19\n.Lro19:\nQmava 52,32,148\nbuffer_store_dword v67, v77, s[24:27], 0 offen sc1\ns_add_u32 m0, s91, 0\ns_mul_i32 s94, s31, 25\nQmava 56,36,148\nbuffer_load_dwordx4 v70, s[48:51], s94 offen lds\n"
"Qmava 60,20,152\nQtv 14,64,44800\nQmava 64,24,152\nQtv 16,64,39936\nQmava 68,28,152\nv_accvgpr_read_b32 v228, a40\nv_accvgpr_read_b32 v229, a41\nQmava 72,32,152\n"
"buffer_load_dwordx4 v71, s[48:51], s94 offen offset:1024 lds\nQmava 76,36,152\nQtv 18,64,45056\nv_accvgpr_read_b32 v230, a42\nQmava 80,20,156\nQda 112,48,24576\nv_accvgpr_read_b32 v231, a43\nQmava 84,24,156\n"
"Qda 116,48,25600\ns_mul_i32 s95, s90, 25\nQmava 88,28,156\nbuffer_load_dwordx4 v72, s[52:55], s95 offen offset:2048 lds\nQmava 92,32,156\nv_accvgpr_read_b32 v108, a44\nv_accvgpr_read_b32 v109, a45\nQmava 96,36,156\n"
"Qda 120,48,26624\nv_accvgpr_read_b32 v110, a46\nQmava 100,20,160\nQda 124,48,27648\nv_accvgpr_read_b32 v111, a47\nQmava 104,24,160\nbuffer_load_dwordx4 v61, s[52:55], s95 offen offset:3072 lds\nQmava 108,28,160\n"
"Qda 128,48,28672\nQmava 176,32,160\nQmava 180,36,160\nQda 132,48,29696\ns_add_u32 m0, s91, 4096\nQmava 184,20,164\nbuffer_load_dwordx4 v62, s[52:55], s95 offen lds\nQmava 188,24,164\n"
"Qda 136,48,30720\nQmava 192,28,164\nQmava 196,32,164\nbuffer_load_dwordx4 v63, s[52:55], s95 offen offset:1024 lds\nv_add_u32 v75, 1, v75\nQmava 200,36,164\ns_waitcnt vmcnt(22)\nds_write_b32 v73, v75\n"
"s_waitcnt lgkmcnt(5)\nQmvva 92,0,112\nQtv 20,59,6144\nQmvva 124,4,112\nbuffer_store_dwordx4 v[148:151], v60, s[56:59], s88 offen offset:1024 sc1\nQmvva 156,8,112\nQtv 22,59,11264\nQmvva 188,12,112\n"
"Qtv 24,59,6400\nQmvva 220,16,112\nQmvva 252,0,116\nQtv 26,59,11520\nQmvva 100,4,116\nQtv 28,59,6656\nQmvva 132,8,116\nds_read_b32 v69, v74\n"
"Qmvva 164,12,116\nQmvva 196,16,116\nQtv 30,59,11776\nQmvva 228,0,120\nQtv 32,59,6912\nQmvva 108,4,120\ns_waitcnt lgkmcnt(2)\nv_cmp_gt_u32 vcc, v75, v69\n"
"Qmava 48,8,120\ns_cbranch_vccnz .Lso20\n.Lro20:\nQmava 52,12,120\nQtv 34,59,12032\ns_add_u32 m0, s91, 24576\ns_mul_i32 s94, s31, 26\nQmava 56,16,120\n"
"buffer_load_dwordx4 v70, s[48:51], s94 offen lds\nQmava 60,0,124\nQmava 64,4,124\nQtv 36,59,7168\nQmava 68,8,124\nQtv 38,59,12288\nQmava 72,12,124\nbuffer_load_dwordx4 v71, s[48:51], s94 offen offset:1024 lds\n"
"Qmava 76,16,124\nv_accvgpr_read_b32 v140, a48\nv_accvgpr_read_b32 v141, a49\nQmava 80,0,128\nQda 140,48,49152\nv_accvgpr_read_b32 v142, a50\nQmava 84,4,128\nQda 144,48,50176\n"
"s_mul_i32 s95, s90, 26\nQmava 88,8,128\nbuffer_load_dwordx4 v72, s[52:55], s95 offen offset:2048 lds\nQmava 92,12,128\nQda 148,48,51200\nv_accvgpr_read_b32 v143, a51\nQmava 96,16,128\nv_accvgpr_read_b32 v172, a52\n"
"v_accvgpr_read_b32 v173, a53\nQmava 100,0,132\nQda 152,48,52224\nv_accvgpr_read_b32 v174, a54\nQmava 104,4,132\nbuffer_load_dwordx4 v61, s[52:55], s95 offen offset:3072 lds\nQmava 108,8,132\nQda 156,48,53248\n"
"v_accvgpr_read_b32 v175, a55\nQmava 176,12,132\nQmava 180,16,132\nQda 160,48,54272\ns_add_u32 m0, s91, 28672\nQmava 184,0,136\nbuffer_load_dwordx4 v62, s[52:55], s95 offen lds\nQmava 188,4,136\n"
"Qda 164,48,55296\nQmava 192,8,136\nQmava 196,12,136\nbuffer_load_dwordx4 v63, s[52:55], s95 offen offset:1024 lds\nv_add_u32 v75, 1, v75\nQmava 200,16,136\ns_waitcnt vmcnt(22)\nds_write_b32 v73, v75\n"
"s_waitcnt lgkmcnt(5)\nQmvva 92,20,140\nQtv 0,59,30720\nQmvva 124,24,140\nbuffer_store_dwordx4 v[180:183], v60, s[56:59], s88 offen offset:2048 sc1\nQmvva 156,28,140\nQtv 2,59,35840\nQmvva 188,32,140\n"
"Qtv 4,59,30976\nQmvva 220,36,140\nQmvva 252,20,144\nQtv 6,59,36096\nQmvva 100,24,144\nQtv 8,59,31232\nQmvva 132,28,144\nds_read_b32 v69, v74\n"
"Qmvva 164,32,144\nQmvva 196,36,144\nQtv 10,59,36352\nQmvva 228,20,148\nQtv 12,59,31488\nQmvva 108,24,148\ns_waitcnt lgkmcnt(2)\nv_cmp_gt_u32 vcc, v75, v69\n"
"Qmvva 140,28,148\ns_cbranch_vccnz .Lso21\n.Lro21:\nQmvva 172,32,148\nQtv 14,59,36608\ns_add_u32 m0, s91, 49152\ns_mul_i32 s94, s31, 27\nQmava 56,36,148\n"
"buffer_load_dwordx4 v70, s[48:51], s94 offen lds\nQmava 60,20,152\nQmava 64,24,152\nQtv 16,59,31744\nQmava 68,28,152\nQtv 18,59,36864\nQmava 72,32,152\nbuffer_load_dwordx4 v71, s[48:51], s94 offen offset:1024 lds\n"
"Qmava 76,36,152\nQmava 80,20,156\nQda 112,49,0\nQmava 84,24,156\nQda 116,49,1024\ns_mul_i32 s95, s90, 27\nQmava 88,28,156\nbuffer_load_dwordx4 v72, s[52:55], s95 offen offset:2048 lds\n"
"Qmava 92,32,156\nQda 120,49,2048\nv_accvgpr_read_b32 v204, a56\nQmava 96,36,156\nv_accvgpr_read_b32 v205, a57\nv_accvgpr_read_b32 v206, a58\nQmava 100,20,160\nQda 124,49,3072\n"
"v_accvgpr_read_b32 v207, a59\nQmava 104,24,160\nbuffer_load_dwordx4 v61, s[52:55], s95 offen offset:3072 lds\nQmava 108,28,160\nQda 128,49,4096\nv_accvgpr_read_b32 v236, a60\nQmava 176,32,160\nv_accvgpr_read_b32 v237, a61\n"
"v_accvgpr_read_b32 v238, a62\nQmava 180,36,160\nQda 132,49,5120\ns_add_u32 m0, s91, 53248\nQmava 184,20,164\nbuffer_load_dwordx4 v62, s[52:55], s95 offen lds\nQmava 188,24,164\nQda 136,49,6144\n"
"v_accvgpr_read_b32 v239, a63\nQmava 192,28,164\nQmava 196,32,164\nbuffer_load_dwordx4 v63, s[52:55], s95 offen offset:1024 lds\nv_add_u32 v75, 1, v75\nQmava 200,36,164\ns_waitcnt vmcnt(22)\nds_write_b32 v73, v75\n"
"s_waitcnt lgkmcnt(5)\nQmvva 92,0,112\nQtv 20,59,55296\ns_add_u32 s88, s71, 43008\nQmvva 124,4,112\nbuffer_store_dwordx4 v[104:107], v60, s[56:59], s88 offen offset:0 sc1\nQmvva 156,8,112\nQtv 22,59,60416\n"
"Qmvva 188,12,112\nQtv 24,59,55552\nQmvva 220,16,112\nQmvva 252,0,116\nQtv 26,59,60672\nQmvva 100,4,116\nQtv 28,59,55808\nQmvva 132,8,116\n"
"ds_read_b32 v69, v74\nQmvva 164,12,116\nQmvva 196,16,116\nQtv 30,59,60928\nQmvva 228,0,120\nQtv 32,59,56064\nQmvva 108,4,120\ns_waitcnt lgkmcnt(2)\n"
"v_cmp_gt_u32 vcc, v75, v69\nQmvva 140,8,120\ns_cbranch_vccnz .Lso22\n.Lro22:\nQmvva 172,12,120\nQtv 34,59,61184\ns_add_u32 m0, s91, 73728\ns_mul_i32 s94, s31, 28\n"
"Qmvva 204,16,120\nbuffer_load_dwordx4 v70, s[48:51], s94 offen lds\nQmvva 236,0,124\nQmava 64,4,124\nQtv 36,59,56320\nQmava 68,8,124\nQtv 38,59,61440\nQmava 72,12,124\n"
"buffer_load_dwordx4 v71, s[48:51], s94 offen offset:1024 lds\nQmava 76,16,124\nQmava 80,0,128\nQda 140,49,24576\nQmava 84,4,128\nQda 144,49,25600\ns_mul_i32 s95, s90, 28\nQmava 88,8,128\n"
"buffer_load_dwordx4 v72, s[52:55], s95 offen offset:2048 lds\nQmava 92,12,128\nQda 148,49,26624\nv_accvgpr_read_b32 v80, a64\nQmava 96,16,128\nv_accvgpr_read_b32 v81, a65\nv_accvgpr_read_b32 v82, a66\nQmava 100,0,132\n"
"Qda 152,49,27648\nv_accvgpr_read_b32 v83, a67\nQmava 104,4,132\nbuffer_load_dwordx4 v61, s[52:55], s95 offen offset:3072 lds\nQmava 108,8,132\nQda 156,49,28672\nv_accvgpr_read_b32 v112, a68\nQmava 176,12,132\n"
"v_accvgpr_read_b32 v113, a69\nv_accvgpr_read_b32 v114, a70\nQmava 180,16,132\nQda 160,49,29696\ns_add_u32 m0, s91, 77824\nQmava 184,0,136\nbuffer_load_dwordx4 v62, s[52:55], s95 offen lds\nQmava 188,4,136\n"
"Qda 164,49,30720\nv_accvgpr_read_b32 v115, a71\nQmava 192,8,136\nQmava 196,12,136\nbuffer_load_dwordx4 v63, s[52:55], s95 offen offset:1024 lds\nv_add_u32 v75, 1, v75\nQmava 200,16,136\ns_waitcnt vmcnt(21)\n"
"ds_write_b32 v73, v75\ns_waitcnt lgkmcnt(5)\nQmvva 92,20,140\nbuffer_load_dword v78, v76, s[24:27], 0 offen sc1\nQmvva 124,24,140\nbuffer_store_dwordx4 v[136:139], v60, s[56:59], s88 offen offset:1024 sc1\nQmvva 156,28,140\nQtv 0,50,31744\n"
"Qmvva 188,32,140\nQtv 2,50,36864\nQmvva 220,36,140\nQtv 4,50,32000\nQmvva 252,20,144\nQmvva 100,24,144\nQtv 6,50,37120\nQmvva 132,28,144\n"
"ds_read_b32 v69, v74\nQmvva 164,32,144\nQtv 8,50,32256\nQmvva 196,36,144\nQtv 10,50,37376\nQmvva 228,20,148\nQmvva 108,24,148\ns_waitcnt lgkmcnt(2)\n"
"v_cmp_gt_u32 vcc, v75, v69\nQmvva 140,28,148\ns_cbranch_vccnz .Lso23\n.Lro23:\nQmvva 172,32,148\nQtv 12,50,32512\ns_add_u32 m0, s91, 98304\ns_mul_i32 s94, s31, 29\n"
"Qmvva 204,36,148\nbuffer_load_dwordx4 v70, s[48:51], s94 offen lds\nQmvva 236,20,152\nQtv 14,50,37632\nQmvva 80,24,152\nQtv 16,50,32768\nQmvva 112,28,152\nQmava 72,32,152\n"
"buffer_load_dwordx4 v71, s[48:51], s94 offen offset:1024 lds\nQmava 76,36,152\nQtv 18,50,37888\nQmava 80,20,156\nQda 112,49,58368\nQmava 84,24,156\nQda 116,49,59392\ns_mul_i32 s95, s90, 29\n"
"Qmava 88,28,156\nbuffer_load_dwordx4 v72, s[52:55], s95 offen offset:2048 lds\nQmava 92,32,156\nQmava 96,36,156\nQda 120,49,60416\nQmava 100,20,160\nQda 124,49,61440\nv_accvgpr_read_b32 v144, a72\n"
"Qmava 104,24,160\nbuffer_load_dwordx4 v61, s[52:55], s95 offen offset:3072 lds\nQmava 108,28,160\nQda 128,49,62464\nv_accvgpr_read_b32 v145, a73\nQmava 176,32,160\nv_accvgpr_read_b32 v146, a74\nv_accvgpr_read_b32 v147, a75\n"
"Qmava 180,36,160\nQda 132,49,63488\ns_add_u32 m0, s91, 102400\nQmava 184,20,164\nbuffer_load_dwordx4 v62, s[52:55], s95 offen lds\nQmava 188,24,164\nQda 136,49,64512\nv_accvgpr_read_b32 v176, a76\n"
"Qmava 192,28,164\nv_accvgpr_read_b32 v177, a77\nv_accvgpr_read_b32 v178, a78\nQmava 196,32,164\nbuffer_load_dwordx4 v63, s[52:55], s95 offen offset:1024 lds\nv_add_u32 v75, 1, v75\nQmava 200,36,164\ns_waitcnt vmcnt(22)\n"
"ds_write_b32 v73, v75\ns_waitcnt lgkmcnt(5)\nQmvva 92,0,112\nQtv 20,64,14336\nv_accvgpr_read_b32 v179, a79\nQmvva 124,4,112\nbuffer_store_dwordx4 v[168:171], v60, s[56:59], s88 offen offset:2048 sc1\nQmvva 156,8,112\n"
"Qtv 22,64,19456\nQmvva 188,12,112\nQtv 24,64,14592\nQmvva 220,16,112\nQmvva 252,0,116\nQtv 26,64,19712\nQmvva 100,4,116\nQtv 28,64,14848\n"
"Qmvva 132,8,116\nds_read_b32 v69, v74\nQmvva 164,12,116\nQmvva 196,16,116\nQtv 30,64,19968\nQmvva 228,0,120\nQtv 32,64,15104\nQmvva 108,4,120\n"
"s_waitcnt lgkmcnt(2)\nv_cmp_gt_u32 vcc, v75, v69\nQmvva 140,8,120\ns_cbranch_vccnz .Lso24\n.Lro24:\nQmvva 172,12,120\nQtv 34,64,20224\ns_add_u32 m0, s91, 132096\n"
"s_mul_i32 s94, s31, 30\nQmvva 204,16,120\nbuffer_load_dwordx4 v70, s[48:51], s94 offen lds\nQmvva 236,0,124\nQmvva 80,4,124\nQtv 36,64,15360\nQmvva 112,8,124\nQtv 38,64,20480\n"
"Qmvva 144,12,124\nbuffer_load_dwordx4 v71, s[48:51], s94 offen offset:1024 lds\nQmvva 176,16,124\nQmava 80,0,128\nQda 140,48,0\nQmava 84,4,128\nQda 144,48,1024\ns_mul_i32 s95, s90, 30\n"
"Qmava 88,8,128\nbuffer_load_dwordx4 v72, s[52:55], s95 offen offset:2048 lds\nQmava 92,12,128\nQda 148,48,2048\nQmava 96,16,128\nQmava 100,0,132\nQda 152,48,3072\nQmava 104,4,132\n"
"buffer_load_dwordx4 v61, s[52:55], s95 offen offset:3072 lds\nQmava 108,8,132\nQda 156,48,4096\nv_accvgpr_read_b32 v208, a80\nQmava 176,12,132\nv_accvgpr_read_b32 v209, a81\nv_accvgpr_read_b32 v210, a82\nQmava 180,16,132\n"
"Qda 160,48,5120\ns_add_u32 m0, s91, 136192\nQmava 184,0,136\nbuffer_load_dwordx4 v62, s[52:55], s95 offen lds\nQmava 188,4,136\nQda 164,48,6144\nv_accvgpr_read_b32 v211, a83\nQmava 192,8,136\n"
"v_accvgpr_read_b32 v240, a84\nv_accvgpr_read_b32 v241, a85\nQmava 196,12,136\nbuffer_load_dwordx4 v63, s[52:55], s95 offen offset:1024 lds\nv_add_u32 v75, 1, v75\nQmava 200,16,136\ns_waitcnt vmcnt(22)\nds_write_b32 v73, v75\n"
"s_waitcnt lgkmcnt(5)\nQmvva 92,20,140\ns_waitcnt vmcnt(14)\nv_sub_u32 v66, v78, v65\nv_cmp_gt_u32 vcc, 2, v66\ns_and_b64 vcc, vcc, s[28:29]\ns_cbranch_scc1 .Lf2_w\n.Lf2_r:\n"
"Qmvva 124,24,140\nbuffer_store_dwordx4 v[200:203], v60, s[56:59], s88 offen offset:3072 sc1\nQmvva 156,28,140\nQtv 0,64,38912\nv_accvgpr_read_b32 v242, a86\nQmvva 188,32,140\nQtv 2,64,44032\nv_accvgpr_read_b32 v243, a87\n"
"Qmvva 220,36,140\nQtv 4,64,39168\nQmvva 252,20,144\nQmvva 100,24,144\nQtv 6,64,44288\nQmvva 132,28,144\nds_read_b32 v69, v74\nQmvva 164,32,144\n"
"Qtv 8,64,39424\nQmvva 196,36,144\nQtv 10,64,44544\nQmvva 228,20,148\nQmvva 108,24,148\ns_waitcnt lgkmcnt(2)\nv_cmp_gt_u32 vcc, v75, v69\nQmvva 140,28,148\n"
"s_cbranch_vccnz .Lso25\n.Lro25:\nQmvva 172,32,148\nQtv 12,64,39680\ns_add_u32 m0, s91, 0\ns_mul_i32 s94, s31, 31\nQmvva 204,36,148\nbuffer_load_dwordx4 v70, s[48:51], s94 offen lds\n"
"Qmvva 236,20,152\nQtv 14,64,44800\nQmvva 80,24,152\nQtv 16,64,39936\nQmvva 112,28,152\nQmvva 144,32,152\nbuffer_load_dwordx4 v71, s[48:51], s94 offen offset:1024 lds\nQmvva 176,36,152\n"
"Qtv 18,64,45056\nQmvva 208,20,156\nQda 112,48,24576\ns_lshr_b32 s68, %19, 2\nQmvva 240,24,156\nQda 116,48,25600\ns_mul_i32 s95, s90, 31\ns_mul_i32 s68, s68, 917504\n"
"Qmava 88,28,156\nbuffer_load_dwordx4 v72, s[52:55], s95 offen offset:2048 lds\ns_mul_i32 s50, s92, 7168\nQmava 92,32,156\ns_mul_i32 s69, s93, 57344\nQmava 96,36,156\nQda 120,48,26624\ns_add_u32 s50, s50, s69\n"
"Qmava 100,20,160\nQda 124,48,27648\ns_add_u32 s68, s68, s50\nQmava 104,24,160\nbuffer_load_dwordx4 v61, s[52:55], s95 offen offset:3072 lds\ns_add_u32 s48, %8, s68\ns_addc_u32 s49, %9, 0\nQmava 108,28,160\n"
"Qda 128,48,28672\ns_and_b32 s49, s49, 0xffff\nQmava 176,32,160\ns_sub_u32 s50, 917504, s50\nQmava 180,36,160\nQda 132,48,29696\ns_add_u32 m0, s91, 4096\nQmava 184,20,164\n"
"buffer_load_dwordx4 v62, s[52:55], s95 offen lds\nQmava 188,24,164\nQda 136,48,30720\nv_accvgpr_read_b32 v84, a88\nQmava 192,28,164\nv_accvgpr_read_b32 v85, a89\nv_accvgpr_read_b32 v86, a90\nQmava 196,32,164\n"
"buffer_load_dwordx4 v63, s[52:55], s95 offen offset:1024 lds\nv_add_u32 v75, 1, v75\nQmava 200,36,164\ns_waitcnt vmcnt(22)\nds_write_b32 v73, v75\ns_mov_b32 s52, s62\ns_mov_b32 s53, s63\ns_mov_b32 s54, s86\n"
"s_waitcnt lgkmcnt(5)\nQmvva 92,0,112\nQtv 20,59,6144\ns_add_u32 s88, s71, 47104\nQmvva 124,4,112\nbuffer_store_dwordx4 v[232:235], v60, s[56:59], s88 offen offset:0 sc1\ns_mov_b32 s94, 458752\nQmvva 156,8,112\n"
"buffer_load_dwordx4 a[208:211], v60, s[48:51], s94 offen offset:0 sc1\nQmvva 188,12,112\nQtv 22,59,11264\nv_accvgpr_read_b32 v87, a91\nQmvva 220,16,112\nQtv 24,59,6400\nv_accvgpr_read_b32 v88, a92\nQmvva 252,0,116\n"
"Qtv 26,59,11520\nv_accvgpr_read_b32 v89, a93\nQmvva 100,4,116\nv_accvgpr_read_b32 v90, a94\nv_accvgpr_read_b32 v91, a95\nQmvva 132,8,116\nds_read_b32 v69, v74\nQmvva 164,12,116\n"
"Qtv 28,59,6656\nQmvva 196,16,116\nQtv 30,59,11776\nQmvva 228,0,120\nQtv 32,59,6912\nv_add_u32 v67, 4, v65\nQmvva 108,4,120\ns_waitcnt lgkmcnt(3)\n"
"v_cmp_gt_u32 vcc, v75, v69\nQmvva 140,8,120\ns_cbranch_vccnz .Lso26\n.Lro26:\nQmvva 172,12,120\nbuffer_load_dwordx4 a[212:215], v60, s[48:51], s94 offen offset:1024 sc1\nQmvva 204,16,120\nbuffer_store_dword v67, v77, s[24:27], 0 offen sc1\n"
"s_add_u32 m0, s96, 24576\ns_mov_b32 s95, 1310720\nQmvva 236,0,124\nbuffer_load_dwordx4 v40, s[52:55], s95 offen lds\nQmvva 80,4,124\nQmvva 112,8,124\nQtv 34,59,12032\nQmvva 144,12,124\n"
"Qtv 36,59,7168\nQmvva 176,16,124\nQtv 38,59,12288\nQmvva 208,0,128\nQda 140,48,49152\nQmvva 240,4,128\nbuffer_load_dwordx4 a[216:219], v60, s[48:51], s94 offen offset:2048 sc1\nQmvva 84,8,128\n"
"buffer_load_dwordx4 v41, s[52:55], s95 offen offset:1024 lds\nQmvva 88,12,128\nQmava 96,16,128\nQda 144,48,50176\nQmava 100,0,132\nQda 148,48,51200\nQmava 104,4,132\nQda 152,48,52224\n"
"Qmava 108,8,132\nQmava 176,12,132\nQda 156,48,53248\nQmava 180,16,132\nbuffer_load_dwordx4 v42, s[52:55], s95 offen offset:2048 lds\nQmava 184,0,136\nQda 160,48,54272\nQmava 188,4,136\n"
"buffer_load_dwordx4 a[220:223], v60, s[48:51], s94 offen offset:3072 sc1\nQmava 192,8,136\nQda 164,48,55296\nv_accvgpr_read_b32 v120, a96\nQmava 196,12,136\nv_add_u32 v75, 1, v75\nv_accvgpr_read_b32 v121, a97\nQmava 200,16,136\n"
"s_waitcnt vmcnt(23)\nds_write_b32 v73, v75\ns_waitcnt lgkmcnt(5)\nQmvva 92,20,140\nQtv 0,59,30720\nv_accvgpr_read_b32 v122, a98\nQmvva 124,24,140\nbuffer_store_dwordx4 v[212:215], v60, s[56:59], s88 offen offset:1024 sc1\n"
"Qmvva 156,28,140\nbuffer_load_dwordx4 v43, s[52:55], s95 offen offset:3072 lds\nQmvva 188,32,140\nQtv 2,59,35840\nv_accvgpr_read_b32 v123, a99\nQmvva 220,36,140\nQtv 4,59,30976\nv_accvgpr_read_b32 v152, a100\n"
"Qmvva 252,20,144\nQtv 6,59,36096\ns_mov_b32 s94, 462848\nQmvva 100,24,144\nbuffer_load_dwordx4 a[224:227], v60, s[48:51], s94 offen offset:0 sc1\nQmvva 132,28,144\nds_read_b32 v69, v74\nv_accvgpr_read_b32 v153, a101\n"
"Qmvva 164,32,144\ns_add_u32 m0, s96, 28672\nv_accvgpr_read_b32 v154, a102\nQmvva 196,36,144\nbuffer_load_dwordx4 v44, s[52:55], s95 offen lds\nQmvva 228,20,148\nQtv 8,59,31232\nv_accvgpr_read_b32 v155, a103\n"
"Qmvva 108,24,148\ns_waitcnt lgkmcnt(1)\nv_cmp_gt_u32 vcc, v75, v69\nQmvva 140,28,148\ns_cbranch_vccnz .Lso27\n.Lro27:\nQmvva 172,32,148\nQtv 10,59,36352\n"
"Qmvva 204,36,148\nQtv 12,59,31488\nQmvva 236,20,152\nQmvva 80,24,152\nbuffer_load_dwordx4 a[228:231], v60, s[48:51], s94 offen offset:1024 sc1\ns_add_u32 m0, s96, 45056\ns_mov_b32 s95, 1638400\nQmvva 112,28,152\n"
"buffer_load_dwordx4 v40, s[52:55], s95 offen lds\nQmvva 144,32,152\nQtv 14,59,36608\nQmvva 176,36,152\nQtv 16,59,31744\nQmvva 208,20,156\nQtv 18,59,36864\nQmvva 240,24,156\n"
"Qda 112,49,0\nQmvva 84,28,156\nQmvva 88,32,156\nQda 116,49,1024\nQmvva 120,36,156\nbuffer_load_dwordx4 v41, s[52:55], s95 offen offset:1024 lds\nQmvva 152,20,160\nQda 120,49,2048\n"
"Qmava 104,24,160\nbuffer_load_dwordx4 a[232:235], v60, s[48:51], s94 offen offset:2048 sc1\nQmava 108,28,160\nQda 124,49,3072\nQmava 176,32,160\nQmava 180,36,160\nQda 128,49,4096\nQmava 184,20,164\n"
"Qda 132,49,5120\nQmava 188,24,164\nbuffer_load_dwordx4 v42, s[52:55], s95 offen offset:2048 lds\nQmava 192,28,164\nQda 136,49,6144\nQmava 196,32,164\nv_add_u32 v75, 1, v75\nv_accvgpr_read_b32 v184, a104\n"
"Qmava 200,36,164\ns_waitcnt vmcnt(25)\nds_write_b32 v73, v75\ns_waitcnt lgkmcnt(5)\nQmvva 92,0,112\nQtv 20,59,55296\nv_accvgpr_read_b32 v185, a105\nQmvva 124,4,112\n"
"buffer_store_dwordx4 v[244:247], v60, s[56:59], s88 offen offset:2048 sc1\ns_mov_b32 s94, 573440\nQmvva 156,8,112\nbuffer_load_dwordx4 a[236:239], v60, s[48:51], s94 offen offset:0 sc1\nQmvva 188,12,112\nbuffer_load_dwordx4 v43, s[52:55], s95 offen offset:3072 lds\nQmvva 220,16,112\nQtv 22,59,60416\n"
"v_accvgpr_read_b32 v186, a106\nQmvva 252,0,116\nQtv 24,59,55552\nv_accvgpr_read_b32 v187, a107\nQmvva 100,4,116\nQtv 26,59,60672\nv_accvgpr_read_b32 v216, a108\nQmvva 132,8,116\n"
"ds_read_b32 v69, v74\nv_accvgpr_read_b32 v217, a109\nQmvva 164,12,116\nQtv 28,59,55808\nv_accvgpr_read_b32 v218, a110\nQmvva 196,16,116\ns_add_u32 m0, s96, 49152\nv_accvgpr_read_b32 v219, a111\n"
"Qmvva 228,0,120\nbuffer_load_dwordx4 v44, s[52:55], s95 offen lds\nQmvva 108,4,120\ns_waitcnt lgkmcnt(1)\nv_cmp_gt_u32 vcc, v75, v69\nQmvva 140,8,120\ns_cbranch_vccnz .Lso28\n.Lro28:\n"
"Qmvva 172,12,120\nbuffer_load_dwordx4 a[240:243], v60, s[48:51], s94 offen offset:1024 sc1\nQmvva 204,16,120\nQtv 30,59,60928\nQmvva 236,0,124\nQtv 32,59,56064\nQmvva 80,4,124\nQtv 34,59,61184\n"
"s_add_u32 m0, s96, 65536\ns_mov_b32 s95, 1966080\nQmvva 112,8,124\nbuffer_load_dwordx4 v40, s[52:55], s95 offen lds\nQmvva 144,12,124\nQtv 36,59,56320\nQmvva 176,16,124\nQmvva 208,0,128\n"
"Qtv 38,59,61440\nQmvva 240,4,128\nbuffer_load_dwordx4 a[244:247], v60, s[48:51], s94 offen offset:2048 sc1\nQmvva 84,8,128\nQda 140,49,24576\nQmvva 88,12,128\nQda 144,49,25600\nQmvva 120,16,128\n"
"buffer_load_dwordx4 v41, s[52:55], s95 offen offset:1024 lds\nQmvva 152,0,132\nQda 148,49,26624\nQmvva 184,4,132\nQmvva 216,8,132\nQda 152,49,27648\nQmava 176,12,132\nQda 156,49,28672\n"
"Qmava 180,16,132\nQda 160,49,29696\nQmava 184,0,136\nQda 164,49,30720\nQmava 188,4,136\nbuffer_load_dwordx4 a[248:251], v60, s[48:51], s94 offen offset:3072 sc1\nQmava 192,8,136\nbuffer_load_dwordx4 v42, s[52:55], s95 offen offset:2048 lds\n"
"Qmava 196,12,136\nv_add_u32 v75, 1, v75\nQmava 200,16,136\ns_waitcnt vmcnt(28)\nds_write_b32 v73, v75\ns_waitcnt lgkmcnt(5)\nQmvva 92,20,140\nQtv 0,50,31744\n"
"v_accvgpr_read_b32 v248, a176\nQmvva 124,24,140\nQtv 2,50,36864\nv_accvgpr_read_b32 v249, a177\nQmvva 156,28,140\nQtv 4,50,32000\nv_accvgpr_read_b32 v250, a178\nQmvva 188,32,140\n"
"v_accvgpr_read_b32 v251, a179\nv_accvgpr_read_b32 v116, a180\nQmvva 220,36,140\nbuffer_load_dwordx4 v43, s[52:55], s95 offen offset:3072 lds\nQmvva 252,20,144\nQtv 6,50,37120\ns_mov_b32 s94, 577536\nQmvva 100,24,144\n"
"buffer_load_dwordx4 a[252:255], v60, s[48:51], s94 offen offset:0 sc1\nQmvva 132,28,144\nds_read_b32 v69, v74\nv_accvgpr_read_b32 v117, a181\nQmvva 164,32,144\nQtv 8,50,32256\nv_accvgpr_read_b32 v118, a182\nQmvva 196,36,144\n"
"v_accvgpr_read_b32 v119, a183\nQmvva 228,20,148\nQtv 10,50,37376\nQmvva 108,24,148\ns_waitcnt lgkmcnt(2)\nv_cmp_gt_u32 vcc, v75, v69\nQmvva 140,28,148\ns_add_u32 m0, s96, 69632\n"
"s_cbranch_vccnz .Lso29\n.Lro29:\nQmvva 172,32,148\nbuffer_load_dwordx4 v44, s[52:55], s95 offen lds\nQmvva 204,36,148\nQtv 12,50,32512\nQmvva 236,20,152\nQtv 14,50,37632\n"
"Qmvva 80,24,152\nbuffer_load_dwordx4 a[168:171], v60, s[48:51], s94 offen offset:1024 sc1\nQmvva 112,28,152\nQmvva 144,32,152\nQtv 16,50,32768\nQmvva 176,36,152\nQtv 18,50,37888\ns_add_u32 m0, s96, 86016\n"
"s_mov_b32 s95, 2293760\nQmvva 208,20,156\nbuffer_load_dwordx4 v40, s[52:55], s95 offen lds\nQmvva 240,24,156\nQmvva 84,28,156\nQda 112,49,58368\nQmvva 88,32,156\nQda 116,49,59392\n"
"Qmvva 120,36,156\nQda 120,49,60416\nQmvva 152,20,160\nQmvva 184,24,160\nbuffer_load_dwordx4 a[172:175], v60, s[48:51], s94 offen offset:2048 sc1\nQmvva 216,28,160\nbuffer_load_dwordx4 v41, s[52:55], s95 offen offset:1024 lds\nQmvva 248,32,160\n"
"Qda 124,49,61440\nQmvva 116,36,160\nQda 128,49,62464\nQmava 184,20,164\nQmava 188,24,164\nQda 132,49,63488\nQmava 192,28,164\nQda 136,49,64512\n"
"Qmava 196,32,164\nv_add_u32 v75, 1, v75\nQmava 200,36,164\ns_waitcnt vmcnt(22)\nds_write_b32 v73, v75\ns_waitcnt lgkmcnt(5)\nQmvva 92,0,112\nbuffer_load_dwordx4 v42, s[52:55], s95 offen offset:2048 lds\n"
"s_mov_b32 s94, 688128\nQmvva 124,4,112\nbuffer_load_dwordx4 a[0:3], v60, s[48:51], s94 offen offset:0 sc1\nQmvva 156,8,112\nQtv 20,64,14336\nv_accvgpr_read_b32 v96, a184\nQmvva 188,12,112\nQtv 22,64,19456\n"
"v_accvgpr_read_b32 v97, a185\nQmvva 220,16,112\nQtv 24,64,14592\nv_accvgpr_read_b32 v98, a186\nQmvva 252,0,116\nQtv 26,64,19712\nv_accvgpr_read_b32 v99, a187\nQmvva 100,4,116\n"
"Qtv 28,64,14848\nv_accvgpr_read_b32 v128, a188\nQmvva 132,8,116\nds_read_b32 v69, v74\nv_accvgpr_read_b32 v129, a189\nQmvva 164,12,116\nbuffer_load_dwordx4 v43, s[52:55], s95 offen offset:3072 lds\nQmvva 196,16,116\n"
"Qtv 30,64,19968\nv_accvgpr_read_b32 v130, a190\nQmvva 228,0,120\nv_accvgpr_read_b32 v131, a191\nQmvva 108,4,120\ns_waitcnt lgkmcnt(1)\nv_cmp_gt_u32 vcc, v75, v69\nQmvva 140,8,120\n"
"s_cbranch_vccnz .Lso30\n.Lro30:\nQmvva 172,12,120\nbuffer_load_dwordx4 a[4:7], v60, s[48:51], s94 offen offset:1024 sc1\nQmvva 204,16,120\ns_waitcnt vmcnt(37)\nds_write_b128 v52, a[208:211] offset:0\nQmvva 236,0,124\n"
"s_add_u32 m0, s96, 90112\nQmvva 80,4,124\nbuffer_load_dwordx4 v44, s[52:55], s95 offen lds\nQmvva 112,8,124\nQtv 32,64,15104\nQmvva 144,12,124\nQtv 34,64,20224\nQmvva 176,16,124\n"
"Qtv 36,64,15360\nQmvva 208,0,128\nQtv 38,64,20480\nQmvva 240,4,128\nbuffer_load_dwordx4 a[8:11], v60, s[48:51], s94 offen offset:2048 sc1\nQmvva 84,8,128\nQda 140,48,0\nQmvva 88,12,128\n"
"Qda 144,48,1024\nQmvva 120,16,128\ns_waitcnt vmcnt(38)\nds_write_b128 v52, a[212:215] offset:4096\nQmvva 152,0,132\nQmvva 184,4,132\nQmvva 216,8,132\nQda 148,48,2048\n"
"Qmvva 248,12,132\nQda 152,48,3072\nQmvva 116,16,132\nQda 156,48,4096\nQmvva 96,0,136\nQda 160,48,5120\nQmvva 128,4,136\nbuffer_load_dwordx4 a[12:15], v60, s[48:51], s94 offen offset:3072 sc1\n"
"Qmava 192,8,136\nQda 164,48,6144\nQmava 196,12,136\nv_add_u32 v75, 1, v75\nQmava 200,16,136\nds_write_b32 v73, v75\ns_waitcnt lgkmcnt(5)\nQmvva 92,20,140\n"
"s_waitcnt vmcnt(36)\nds_write_b128 v52, a[216:219] offset:8192\nQmvva 124,24,140\nQmvva 156,28,140\nbuffer_load_dword v78, v76, s[24:27], 0 offen sc1\nQmvva 188,32,140\nQtv 0,45,20480\nQmvva 220,36,140\n"
"Qtv 2,45,25600\nv_accvgpr_read_b32 v160, a192\nQmvva 252,20,144\nQtv 4,45,20736\ns_mov_b32 s94, 692224\nQmvva 100,24,144\nbuffer_load_dwordx4 a[16:19], v60, s[48:51], s94 offen offset:0 sc1\nQmvva 132,28,144\n"
"ds_read_b32 v69, v74\nv_accvgpr_read_b32 v161, a193\nQmvva 164,32,144\nQtv 6,45,25856\nv_accvgpr_read_b32 v162, a194\nQmvva 196,36,144\ns_waitcnt vmcnt(35)\nds_write_b128 v52, a[220:223] offset:12288\n"
"Qmvva 228,20,148\nv_accvgpr_read_b32 v163, a195\nv_accvgpr_read_b32 v192, a196\nQmvva 108,24,148\ns_waitcnt lgkmcnt(2)\nv_cmp_gt_u32 vcc, v75, v69\nQmvva 140,28,148\ns_cbranch_vccnz .Lso31\n"
".Lro31:\nv_accvgpr_read_b32 v193, a197\nQmvva 172,32,148\ns_add_u32 m0, s96, 4096\ns_mov_b32 s95, 1351680\nv_accvgpr_read_b32 v194, a198\nQmvva 204,36,148\nbuffer_load_dwordx4 v40, s[52:55], s95 offen lds\n"
"Qmvva 236,20,152\nQtv 8,45,20992\nv_accvgpr_read_b32 v195, a199\nQmvva 80,24,152\nbuffer_load_dwordx4 a[20:23], v60, s[48:51], s94 offen offset:1024 sc1\nQmvva 112,28,152\nQtv 10,45,26112\nQmvva 144,32,152\n"
"Qtv 12,45,21248\nQmvva 176,36,152\ns_waitcnt vmcnt(34)\nds_write_b128 v52, a[224:227] offset:16384\nQmvva 208,20,156\nQmvva 240,24,156\nbuffer_load_dwordx4 v41, s[52:55], s95 offen offset:1024 lds\nQmvva 84,28,156\n"
"Qmvva 88,32,156\nQtv 14,45,26368\nQmvva 120,36,156\nQtv 16,45,21504\nQmvva 152,20,160\nQtv 18,45,26624\nQmvva 184,24,160\nbuffer_load_dwordx4 a[24:27], v60, s[48:51], s94 offen offset:2048 sc1\n"
"Qmvva 216,28,160\nQmvva 248,32,160\nbuffer_load_dwordx4 v42, s[52:55], s95 offen offset:2048 lds\nQmvva 116,36,160\ns_waitcnt vmcnt(35)\nds_write_b128 v52, a[228:231] offset:20480\nQmvva 96,20,164\nQmvva 128,24,164\n"
"Qmvva 160,28,164\nQmvva 192,32,164\nQda 48,53,0\nv_add_u32 v75, 1, v75\nQmava 200,36,164\ns_waitcnt vmcnt(27)\nds_write_b32 v73, v75\ns_waitcnt lgkmcnt(3)\n"
"Qmvva 92,0,208\nbuffer_load_dwordx4 v43, s[52:55], s95 offen offset:3072 lds\ns_mov_b32 s94, 802816\nQmvva 124,4,208\nbuffer_load_dwordx4 a[28:31], v60, s[48:51], s94 offen offset:0 sc1\nQmvva 156,8,208\nQtv 20,47,20480\nQmvva 188,12,208\n"
"Qtv 22,47,25600\nQmvva 220,16,208\ns_waitcnt vmcnt(34)\nds_write_b128 v52, a[232:235] offset:25600\nQmvva 252,0,212\nQmvva 100,4,212\nQtv 24,47,20736\nv_accvgpr_read_b32 v224, a200\n"
"Qmvva 132,8,212\nds_read_b32 v69, v74\nv_accvgpr_read_b32 v225, a201\nQmvva 164,12,212\nQda 52,53,4096\ns_add_u32 m0, s96, 8192\nQmvva 196,16,212\nbuffer_load_dwordx4 v44, s[52:55], s95 offen lds\n"
"Qmvva 228,0,216\nQtv 26,47,25856\nv_accvgpr_read_b32 v226, a202\nQmvva 108,4,216\ns_waitcnt lgkmcnt(2)\nv_cmp_gt_u32 vcc, v75, v69\nQmvva 140,8,216\ns_cbranch_vccnz .Lso32\n"
".Lro32:\nv_accvgpr_read_b32 v227, a203\nQmvva 172,12,216\nbuffer_load_dwordx4 a[32:35], v60, s[48:51], s94 offen offset:1024 sc1\nQmvva 204,16,216\ns_waitcnt vmcnt(33)\nds_write_b128 v52, a[236:239] offset:29696\nQmvva 236,0,220\n"
"Qmvva 80,4,220\nQtv 28,47,20992\nQmvva 112,8,220\nQmvva 144,12,220\nQda 56,53,8192\nQmvva 176,16,220\nQtv 30,47,26112\nQmvva 208,0,224\n"
"Qtv 32,47,21248\nQmvva 240,4,224\nbuffer_load_dwordx4 a[36:39], v60, s[48:51], s94 offen offset:2048 sc1\nQmvva 84,8,224\nQtv 34,47,26368\nQmvva 88,12,224\nQtv 36,47,21504\nQmvva 120,16,224\n"
"s_waitcnt vmcnt(31)\nds_write_b128 v52, a[240:243] offset:33792\nQmvva 152,0,228\nQmvva 184,4,228\nQtv 38,47,26624\nQmvva 216,8,228\nQmvva 248,12,228\nQda 60,53,12288\n"
"Qmvva 116,16,228\nQmvva 96,0,232\nQmvva 128,4,232\nbuffer_load_dwordx4 a[40:43], v60, s[48:51], s94 offen offset:3072 sc1\nQmvva 160,8,232\nQmvva 192,12,232\nv_add_u32 v75, 1, v75\nQmvva 224,16,232\n"
"s_waitcnt vmcnt(36)\nds_write_b32 v73, v75\ns_waitcnt lgkmcnt(2)\nQmvva 92,20,48\ns_waitcnt vmcnt(30)\nds_write_b128 v52, a[244:247] offset:37888\nQmvva 124,24,48\nQmvva 156,28,48\n"
"s_waitcnt vmcnt(12)\nv_sub_u32 v66, v78, v65\nv_cmp_gt_u32 vcc, 3, v66\ns_and_b64 vcc, vcc, s[28:29]\ns_cbranch_scc1 .Lf3_w\n.Lf3_r:\nQmvva 188,32,48\nQda 64,53,16384\n"
"Qmvva 220,36,48\nQtv 0,45,40960\nQmvva 252,20,52\nQtv 2,45,46080\ns_mov_b32 s94, 806912\nQmvva 100,24,52\nbuffer_load_dwordx4 a[44:47], v60, s[48:51], s94 offen offset:0 sc1\nQmvva 132,28,52\n"
"ds_read_b32 v69, v74\nQmvva 164,32,52\nQtv 4,45,41216\nQmvva 196,36,52\ns_waitcnt vmcnt(29)\nds_write_b128 v52, a[248:251] offset:41984\ns_waitcnt lgkmcnt(15)\nQmvva 228,20,56\n"
"v_add_u32 v67, 5, v65\nQmvva 108,24,56\ns_waitcnt lgkmcnt(2)\nv_cmp_gt_u32 vcc, v75, v69\nQmvva 140,28,56\ns_cbranch_vccnz .Lso33\n.Lro33:\nQmvva 172,32,56\n"
"Qda 68,53,20480\nQmvva 204,36,56\nbuffer_store_dword v67, v77, s[24:27], 0 offen sc1\ns_add_u32 m0, s96, 24576\ns_mov_b32 s95, 1679360\nQmvva 236,20,60\nbuffer_load_dwordx4 v40, s[52:55], s95 offen lds\nQmvva 80,24,60\n"
"buffer_load_dwordx4 a[208:211], v60, s[48:51], s94 offen offset:1024 sc1\nQmvva 112,28,60\nQtv 6,45,46336\nQmvva 144,32,60\nQtv 8,45,41472\nQmvva 176,36,60\ns_waitcnt vmcnt(29)\nds_write_b128 v52, a[252:255] offset:46080\n"
"s_waitcnt lgkmcnt(3)\nQmvva 208,20,64\nQmvva 240,24,64\nQtv 10,45,46592\nQmvva 84,28,64\nbuffer_load_dwordx4 v41, s[52:55], s95 offen offset:1024 lds\nQmvva 88,32,64\nQda 72,53,25600\n"
"Qmvva 120,36,64\nQtv 12,45,41728\nQmvva 152,20,68\nQtv 14,45,46848\nQmvva 184,24,68\nbuffer_load_dwordx4 a[212:215], v60, s[48:51], s94 offen offset:2048 sc1\nQmvva 216,28,68\nQtv 16,45,41984\n"
"Qmvva 248,32,68\nQtv 18,45,47104\nQmvva 116,36,68\ns_waitcnt vmcnt(29)\nds_write_b128 v52, a[168:171] offset:50176\ns_waitcnt lgkmcnt(5)\nQmvva 96,20,72\nQmvva 128,24,72\n"
"buffer_load_dwordx4 v42, s[52:55], s95 offen offset:2048 lds\nQmvva 160,28,72\nQmvva 192,32,72\nQda 76,53,29696\nv_add_u32 v75, 1, v75\nQmvva 224,36,72\ns_waitcnt vmcnt(31)\nds_write_b32 v73, v75\n"
"s_waitcnt lgkmcnt(3)\nQmvva 92,0,236\nQtv 20,47,40960\ns_mov_b32 s94, 473088\nQmvva 124,4,236\nbuffer_load_dwordx4 a[216:219], v60, s[48:51], s94 offen offset:0 sc1\nQmvva 156,8,236\nQtv 22,47,46080\n"
"Qmvva 188,12,236\nbuffer_load_dwordx4 v43, s[52:55], s95 offen offset:3072 lds\nQmvva 220,16,236\ns_waitcnt vmcnt(30)\nds_write_b128 v52, a[172:175] offset:0\nQmvva 252,0,240\nQmvva 100,4,240\nQtv 24,47,41216\n"
"Qmvva 132,8,240\nds_read_b32 v69, v74\nQmvva 164,12,240\nQda 80,53,33792\nQmvva 196,16,240\nQtv 26,47,46336\ns_add_u32 m0, s96, 28672\nQmvva 228,0,244\n"
"buffer_load_dwordx4 v44, s[52:55], s95 offen lds\nQmvva 108,4,244\ns_waitcnt lgkmcnt(2)\nv_cmp_gt_u32 vcc, v75, v69\nQmvva 140,8,244\ns_cbranch_vccnz .Lso34\n.Lro34:\nQmvva 172,12,244\n"
"buffer_load_dwordx4 a[220:223], v60, s[48:51], s94 offen offset:1024 sc1\nQmvva 204,16,244\ns_waitcnt vmcnt(29)\nds_write_b128 v52, a[0:3] offset:4096\nQmvva 236,0,248\nQmvva 80,4,248\nQtv 28,47,41472\nQmvva 112,8,248\n"
"Qmvva 144,12,248\nQda 48,53,37888\nQmvva 176,16,248\nQtv 30,47,46592\nQmvva 208,0,252\nQtv 32,47,41728\nQmvva 240,4,252\nbuffer_load_dwordx4 a[224:227], v60, s[48:51], s94 offen offset:2048 sc1\n"
"Qmvva 84,8,252\nQtv 34,47,46848\nQmvva 88,12,252\nQtv 36,47,41984\nQmvva 120,16,252\ns_waitcnt vmcnt(28)\nds_write_b128 v52, a[4:7] offset:8192\nQmvva 152,0,168\n"
"Qmvva 184,4,168\nQtv 38,47,47104\nQmvva 216,8,168\nQmvva 248,12,168\nQda 52,53,41984\nQmvva 116,16,168\nQmvva 96,0,172\nQmvva 128,4,172\n"
"buffer_load_dwordx4 a[228:231], v60, s[48:51], s94 offen offset:3072 sc1\nQmvva 160,8,172\nQmvva 192,12,172\nv_add_u32 v75, 1, v75\nQmvva 224,16,172\nds_write_b32 v73, v75\ns_waitcnt lgkmcnt(2)\nQmvva 92,20,76\n"
"s_waitcnt vmcnt(27)\nds_write_b128 v52, a[8:11] offset:12288\nQmvva 124,24,76\nQmvva 156,28,76\nQta 112,46,0\nQmvva 188,32,76\nQda 56,53,46080\nQmvva 220,36,76\n"
"Qta 114,46,5120\nQmvva 252,20,80\nQta 116,46,256\ns_mov_b32 s94, 477184\nQmvva 100,24,80\nbuffer_load_dwordx4 a[232:235], v60, s[48:51], s94 offen offset:0 sc1\nQmvva 132,28,80\nds_read_b32 v69, v74\n"
"Qmvva 164,32,80\nQta 118,46,5376\nQmvva 196,36,80\ns_waitcnt vmcnt(27)\nds_write_b128 v52, a[12:15] offset:16384\ns_waitcnt lgkmcnt(15)\nQmvva 228,20,48\nQmvva 108,24,48\n"
"s_waitcnt lgkmcnt(2)\nv_cmp_gt_u32 vcc, v75, v69\nQmvva 140,28,48\ns_cbranch_vccnz .Lso35\n.Lro35:\nQmvva 172,32,48\nQda 60,53,50176\ns_add_u32 m0, s96, 45056\n"
"s_mov_b32 s95, 2007040\nQmvva 204,36,48\nbuffer_load_dwordx4 v40, s[52:55], s95 offen lds\nQmvva 236,20,52\nQta 120,46,512\nQmvva 80,24,52\nbuffer_load_dwordx4 a[236:239], v60, s[48:51], s94 offen offset:1024 sc1\nQmvva 112,28,52\n"
"Qta 122,46,5632\nQmvva 144,32,52\nQta 124,46,768\nQmvva 176,36,52\ns_waitcnt vmcnt(27)\nds_write_b128 v52, a[16:19] offset:20480\ns_waitcnt lgkmcnt(4)\nQmvva 208,20,56\n"
"Qmvva 240,24,56\nbuffer_load_dwordx4 v41, s[52:55], s95 offen offset:1024 lds\nQmvva 84,28,56\nQta 126,46,5888\nQmvva 88,32,56\nQda 64,53,0\nQmvva 120,36,56\nQta 128,46,1024\n"
"Qmvva 152,20,60\nQta 130,46,6144\nQmvva 184,24,60\nbuffer_load_dwordx4 a[240:243], v60, s[48:51], s94 offen offset:2048 sc1\nQmvva 216,28,60\nQmvva 248,32,60\nbuffer_load_dwordx4 v42, s[52:55], s95 offen offset:2048 lds\nQmvva 116,36,60\n"
"s_waitcnt vmcnt(28)\nds_write_b128 v52, a[20:23] offset:25600\ns_waitcnt lgkmcnt(3)\nQmvva 96,20,64\nQmvva 128,24,64\nQmvva 160,28,64\nQmvva 192,32,64\nQda 68,53,4096\n"
"v_add_u32 v75, 1, v75\nQmvva 224,36,64\ns_waitcnt vmcnt(34)\nds_write_b32 v73, v75\ns_waitcnt lgkmcnt(3)\nQmvaa 92,112,0\nbuffer_load_dwordx4 v43, s[52:55], s95 offen offset:3072 lds\ns_mov_b32 s94, 587776\n"
"Qmvaa 124,116,0\nbuffer_load_dwordx4 a[244:247], v60, s[48:51], s94 offen offset:0 sc1\nQmvaa 156,120,0\nQta 132,51,0\nQmvaa 188,124,0\nQta 134,51,5120\nQmvaa 220,128,0\ns_waitcnt vmcnt(28)\n"
"ds_write_b128 v52, a[24:27] offset:29696\nQmvaa 252,112,4\nQmvaa 100,116,4\nQta 136,51,256\nQmvaa 132,120,4\nds_read_b32 v69, v74\nQmvaa 164,124,4\nQda 72,53,8192\n"
"s_add_u32 m0, s96, 49152\nQmvaa 196,128,4\nbuffer_load_dwordx4 v44, s[52:55], s95 offen lds\nQmvaa 228,112,8\nQta 138,51,5376\nQmvaa 108,116,8\ns_waitcnt lgkmcnt(2)\nv_cmp_gt_u32 vcc, v75, v69\n"
"Qmvaa 140,120,8\ns_cbranch_vccnz .Lso36\n.Lro36:\nQmvaa 172,124,8\nbuffer_load_dwordx4 a[248:251], v60, s[48:51], s94 offen offset:1024 sc1\nQmvaa 204,128,8\ns_waitcnt vmcnt(27)\nds_write_b128 v52, a[28:31] offset:33792\n"
"Qmvaa 236,112,12\nQmvaa 80,116,12\nQta 140,51,512\nQmvaa 112,120,12\nQmvaa 144,124,12\nQda 76,53,12288\nQmvaa 176,128,12\nQta 142,51,5632\n"
"Qmvaa 208,112,16\nQta 144,51,768\nQmvaa 240,116,16\nbuffer_load_dwordx4 a[252:255], v60, s[48:51], s94 offen offset:2048 sc1\nQmvaa 84,120,16\nQta 146,51,5888\nQmvaa 88,124,16\nQta 148,51,1024\n"
"Qmvaa 120,128,16\ns_waitcnt vmcnt(26)\nds_write_b128 v52, a[32:35] offset:37888\nQmvaa 152,112,20\nQmvaa 184,116,20\nQta 150,51,6144\nQmvaa 216,120,20\nQmvaa 248,124,20\n"
"Qda 80,53,16384\nQmvaa 116,128,20\nQmvaa 96,112,24\nQmvaa 128,116,24\nbuffer_load_dwordx4 a[168:171], v60, s[48:51], s94 offen offset:3072 sc1\nQmvaa 160,120,24\nQmvaa 192,124,24\nv_add_u32 v75, 1, v75\n"
"Qmvaa 224,128,24\nds_write_b32 v73, v75\ns_waitcnt lgkmcnt(2)\nQmvaa 92,132,68\ns_waitcnt vmcnt(26)\nds_write_b128 v52, a[36:39] offset:41984\nQmvaa 124,136,68\nQmvaa 156,140,68\n"
"Qtv 0,46,20480\nQmvaa 188,144,68\nQda 48,53,20480\nQmvaa 220,148,68\nQtv 2,46,25600\nQmvaa 252,132,72\nQtv 4,46,20736\ns_mov_b32 s94, 591872\n"
"Qmvaa 100,136,72\nbuffer_load_dwordx4 a[172:175], v60, s[48:51], s94 offen offset:0 sc1\nQmvaa 132,140,72\nds_read_b32 v69, v74\ns_mov_b32 s40, %17\nQmvaa 164,144,72\nQtv 6,46,25856\ns_and_b32 s41, %18, 0xffff\n"
"Qmvaa 196,148,72\ns_waitcnt vmcnt(26)\nds_write_b128 v52, a[40:43] offset:46080\ns_waitcnt lgkmcnt(15)\nQmvaa 228,132,76\ns_mov_b32 s42, 571200\ns_lshr_b32 s68, %19, 2\nQmvaa 108,136,76\n"
"s_waitcnt lgkmcnt(2)\nv_cmp_gt_u32 vcc, v75, v69\nQmvaa 140,140,76\ns_cbranch_vccnz .Lso37\n.Lro37:\ns_mul_i32 s65, s68, 286720\nQmvaa 172,144,76\nQda 52,53,25600\n"
"s_add_u32 m0, s96, 65536\ns_mov_b32 s95, 2334720\nQmvaa 204,148,76\nbuffer_load_dwordx4 v40, s[52:55], s95 offen lds\nQmvaa 236,132,80\nQtv 8,46,20992\ns_sub_u32 s60, 286720, s65\nQmvaa 80,136,80\n"
"buffer_load_dwordx4 a[0:3], v60, s[48:51], s94 offen offset:1024 sc1\nQmvaa 112,140,80\nQtv 10,46,26112\ns_mul_i32 s68, s68, 114688\nQmvaa 144,144,80\nQtv 12,46,21248\ns_add_u32 s64, s98, 57344\nQmvaa 176,148,80\n"
"s_waitcnt vmcnt(27)\nds_write_b128 v52, a[44:47] offset:50176\ns_waitcnt lgkmcnt(4)\nQmvaa 208,132,48\ns_sub_u32 s64, s64, s68\ns_mul_i32 s68, s93, 160\nQmvaa 240,136,48\nbuffer_load_dwordx4 v41, s[52:55], s95 offen offset:1024 lds\n"
"Qmvaa 84,140,48\nQtv 14,46,26368\ns_add_u32 s68, s68, %10\nQmvaa 88,144,48\nQda 56,53,29696\nv_bfe_u32 v66, v60, 4, 4\nQmvaa 120,148,48\nQtv 16,46,21504\n"
"v_mul_u32_u24 v66, 336, v66\nQmvaa 152,132,52\nQtv 18,46,26624\nv_lshrrev_b32 v67, 8, v60\nQmvaa 184,136,52\nbuffer_load_dwordx4 a[4:7], v60, s[48:51], s94 offen offset:2048 sc1\nQmvaa 216,140,52\nv_lshlrev_b32 v68, 3, v67\n"
"v_add3_u32 v61, v66, v68, s68\nQmvaa 248,144,52\nbuffer_load_dwordx4 v42, s[52:55], s95 offen offset:2048 lds\nQmvaa 116,148,52\ns_waitcnt vmcnt(27)\nds_write_b128 v52, a[208:211] offset:0\ns_waitcnt lgkmcnt(3)\nQmvaa 96,132,56\n"
"s_lshl_b32 s68, s70, 2\nv_add_u32 v67, s68, v67\nQmvaa 128,136,56\ns_and_b32 s69, %19, 1\ns_lshl_b32 s68, s69, 6\nQmvaa 160,140,56\nv_and_b32 v66, 255, v60\nv_add_u32 v66, s68, v66\n"
"Qmvaa 192,144,56\nQda 60,53,33792\nv_add_u32 v75, 1, v75\nQmvaa 224,148,56\ns_waitcnt vmcnt(34)\nds_write_b32 v73, v75\ns_waitcnt lgkmcnt(3)\nQmvva 92,0,28\n"
"buffer_load_dwordx4 v43, s[52:55], s95 offen offset:3072 lds\ns_mov_b32 s94, 702464\nQmvva 124,4,28\nbuffer_load_dwordx4 a[8:11], v60, s[48:51], s94 offen offset:0 sc1\nQmvva 156,8,28\nQtv 20,51,20480\nv_mul_u32_u24 v68, 336, v67\nQmvva 188,12,28\n"
"Qtv 22,51,25600\nv_add3_u32 v62, v66, v68, %10\nQmvva 220,16,28\ns_waitcnt vmcnt(27)\nds_write_b128 v52, a[212:215] offset:4096\nQmvva 252,0,32\nv_mul_u32_u24 v68, 2560, v67\nv_add_u32 v63, v66, v68\n"
"Qmvva 100,4,32\nQtv 24,51,20736\ns_lshl_b32 s68, s70, 4\nQmvva 132,8,32\nds_read_b32 v69, v74\nv_lshrrev_b32 v67, 6, v60\nQmvva 164,12,32\nQda 64,53,37888\n"
"s_add_u32 m0, s96, 69632\nQmvva 196,16,32\nbuffer_load_dwordx4 v44, s[52:55], s95 offen lds\nQmvva 228,0,36\nQtv 26,51,25856\nv_add_u32 v67, s68, v67\nQmvva 108,4,36\ns_waitcnt lgkmcnt(2)\n"
"v_cmp_gt_u32 vcc, v75, v69\nQmvva 140,8,36\ns_cbranch_vccnz .Lso38\n.Lro38:\ns_xor_b32 s69, s69, 1\nQmvva 172,12,36\nbuffer_load_dwordx4 a[12:15], v60, s[48:51], s94 offen offset:1024 sc1\nQmvva 204,16,36\n"
"s_waitcnt vmcnt(27)\nds_write_b128 v52, a[216:219] offset:8192\nQmvva 236,0,40\ns_lshl_b32 s68, s69, 8\nv_and_b32 v66, 63, v60\nQmvva 80,4,40\nQtv 28,51,20992\nv_add_u32 v66, s68, v66\n"
"Qmvva 112,8,40\nv_mul_u32_u24 v68, 336, v67\nv_add3_u32 v70, v66, v68, %10\nQmvva 144,12,40\nQda 68,53,41984\nv_mul_u32_u24 v68, 2560, v67\nQmvva 176,16,40\nQtv 30,51,26112\n"
"v_add_u32 v71, v66, v68\nQmvva 208,0,44\nQtv 32,51,21248\nQmvva 240,4,44\nbuffer_load_dwordx4 a[16:19], v60, s[48:51], s94 offen offset:2048 sc1\nQmvva 84,8,44\nQtv 34,51,26368\nQmvva 88,12,44\n"
"Qtv 36,51,21504\nQmvva 120,16,44\ns_waitcnt vmcnt(25)\nds_write_b128 v52, a[220:223] offset:12288\nQmvva 152,0,208\nQmvva 184,4,208\nQtv 38,51,26624\nQmvva 216,8,208\n"
"Qmvva 248,12,208\nQda 72,53,46080\nQmvva 116,16,208\nQmvva 96,0,212\nQmvva 128,4,212\nbuffer_load_dwordx4 a[20:23], v60, s[48:51], s94 offen offset:3072 sc1\nQmvva 160,8,212\nQmvva 192,12,212\n"
"v_add_u32 v75, 1, v75\nQmvva 224,16,212\nds_write_b32 v73, v75\ns_waitcnt lgkmcnt(2)\nQmvva 92,20,60\ns_waitcnt vmcnt(25)\nds_write_b128 v52, a[224:227] offset:16384\nQmvva 124,24,60\n"
"Qmvva 156,28,60\nbuffer_load_dword v78, v76, s[24:27], 0 offen sc1\nQmvva 188,32,60\nQda 76,53,50176\nQmvva 220,36,60\nQta 112,45,0\nQmvva 252,20,64\nQta 114,45,5120\n"
"s_mov_b32 s94, 706560\nQmvva 100,24,64\nbuffer_load_dwordx4 a[24:27], v60, s[48:51], s94 offen offset:0 sc1\nQmvva 132,28,64\nds_read_b32 v69, v74\nQmvva 164,32,64\nQta 116,45,256\nQmvva 196,36,64\n"
"s_waitcnt vmcnt(26)\nds_write_b128 v52, a[228:231] offset:20480\ns_waitcnt lgkmcnt(15)\nQmvva 228,20,68\nQmvva 108,24,68\ns_waitcnt lgkmcnt(2)\nv_cmp_gt_u32 vcc, v75, v69\nQmvva 140,28,68\n"
"s_cbranch_vccnz .Lso39\n.Lro39:\nQmvva 172,32,68\nQda 80,53,0\ns_add_u32 m0, s96, 86016\ns_mov_b32 s95, 1392640\nQmvva 204,36,68\nbuffer_load_dwordx4 v40, s[52:55], s95 offen lds\n"
"Qmvva 236,20,72\nQta 118,45,5376\nQmvva 80,24,72\nbuffer_load_dwordx4 a[28:31], v60, s[48:51], s94 offen offset:1024 sc1\nQmvva 112,28,72\nQta 120,45,512\nQmvva 144,32,72\nQta 122,45,5632\n"
"Qmvva 176,36,72\ns_waitcnt vmcnt(27)\nds_write_b128 v52, a[232:235] offset:25600\ns_waitcnt lgkmcnt(4)\nQmvva 208,20,76\nQmvva 240,24,76\nbuffer_load_dwordx4 v41, s[52:55], s95 offen offset:1024 lds\nQmvva 84,28,76\n"
"Qta 124,45,768\nQmvva 88,32,76\nQda 48,53,4096\nQmvva 120,36,76\nQta 126,45,5888\nQmvva 152,20,80\nQta 128,45,1024\nQmvva 184,24,80\n"
"buffer_load_dwordx4 a[32:35], v60, s[48:51], s94 offen offset:2048 sc1\nQmvva 216,28,80\nQta 130,45,6144\nQmvva 248,32,80\nbuffer_load_dwordx4 v42, s[52:55], s95 offen offset:2048 lds\nQmvva 116,36,80\ns_waitcnt vmcnt(28)\nds_write_b128 v52, a[236:239] offset:29696\n"
"s_waitcnt lgkmcnt(4)\nQmvva 96,20,48\nQmvva 128,24,48\nQmvva 160,28,48\nQmvva 192,32,48\nQda 52,53,8192\nv_add_u32 v75, 1, v75\nQmvva 224,36,48\n"
"s_waitcnt vmcnt(34)\nds_write_b32 v73, v75\ns_waitcnt lgkmcnt(3)\nQmvaa 92,112,216\nbuffer_load_dwordx4 v43, s[52:55], s95 offen offset:3072 lds\ns_mov_b32 s94, 817152\nQmvaa 124,116,216\nbuffer_load_dwordx4 a[36:39], v60, s[48:51], s94 offen offset:0 sc1\n"
"Qmvaa 156,120,216\nQta 132,47,0\nQmvaa 188,124,216\nQta 134,47,5120\nQmvaa 220,128,216\ns_waitcnt vmcnt(28)\nds_write_b128 v52, a[240:243] offset:33792\nQmvaa 252,112,220\n"
"Qmvaa 100,116,220\nQta 136,47,256\nQmvaa 132,120,220\nds_read_b32 v69, v74\nQmvaa 164,124,220\nQda 56,53,12288\ns_add_u32 m0, s96, 90112\nQmvaa 196,128,220\n"
"buffer_load_dwordx4 v44, s[52:55], s95 offen lds\nQmvaa 228,112,224\nQta 138,47,5376\nQmvaa 108,116,224\ns_waitcnt lgkmcnt(2)\nv_cmp_gt_u32 vcc, v75, v69\nQmvaa 140,120,224\ns_cbranch_vccnz .Lso40\n"
".Lro40:\nQmvaa 172,124,224\nbuffer_load_dwordx4 a[40:43], v60, s[48:51], s94 offen offset:1024 sc1\nQmvaa 204,128,224\ns_waitcnt vmcnt(27)\nds_write_b128 v52, a[244:247] offset:37888\nQmvaa 236,112,228\nQmvaa 80,116,228\n"
"Qta 140,47,512\nQmvaa 112,120,228\nQmvaa 144,124,228\nQda 60,53,16384\nQmvaa 176,128,228\nQta 142,47,5632\nQmvaa 208,112,232\nQta 144,47,768\n"
"Qmvaa 240,116,232\nbuffer_load_dwordx4 a[44:47], v60, s[48:51], s94 offen offset:2048 sc1\nQmvaa 84,120,232\nQta 146,47,5888\nQmvaa 88,124,232\nQta 148,47,1024\nQmvaa 120,128,232\ns_waitcnt vmcnt(26)\n"
"ds_write_b128 v52, a[248:251] offset:41984\nQmvaa 152,112,236\nQmvaa 184,116,236\nQta 150,47,6144\nQmvaa 216,120,236\nQmvaa 248,124,236\nQda 64,53,20480\nQmvaa 116,128,236\n"
"Qmvaa 96,112,240\nQmvaa 128,116,240\nbuffer_load_dwordx4 a[208:211], v60, s[48:51], s94 offen offset:3072 sc1\nQmvaa 160,120,240\nQmvaa 192,124,240\nv_add_u32 v75, 1, v75\nQmvaa 224,128,240\nds_write_b32 v73, v75\n"
"s_waitcnt lgkmcnt(2)\nQmvaa 92,132,52\ns_waitcnt vmcnt(26)\nds_write_b128 v52, a[252:255] offset:46080\nQmvaa 124,136,52\nQmvaa 156,140,52\ns_waitcnt vmcnt(12)\nv_sub_u32 v66, v78, v65\n"
"v_cmp_gt_u32 vcc, 4, v66\ns_and_b64 vcc, vcc, s[28:29]\ns_cbranch_scc1 .Lf4_w\n.Lf4_r:\nQmvaa 188,144,52\nQda 68,53,25600\nQmvaa 220,148,52\nQtv 0,45,20480\n"
"Qmvaa 252,132,56\nQtv 2,45,25600\ns_mov_b32 s94, 821248\nQmvaa 100,136,56\nbuffer_load_dwordx4 a[212:215], v60, s[48:51], s94 offen offset:0 sc1\nQmvaa 132,140,56\nds_read_b32 v69, v74\nQmvaa 164,144,56\n"
"Qtv 4,45,20736\nQmvaa 196,148,56\ns_waitcnt vmcnt(26)\nds_write_b128 v52, a[168:171] offset:50176\ns_waitcnt lgkmcnt(15)\nQmvaa 228,132,60\nQmvaa 108,136,60\ns_waitcnt lgkmcnt(2)\n"
"v_cmp_gt_u32 vcc, v75, v69\nQmvaa 140,140,60\ns_cbranch_vccnz .Lso41\n.Lro41:\nQmvaa 172,144,60\nQda 72,53,29696\ns_add_u32 m0, s96, 4096\ns_mov_b32 s95, 1720320\n"
"Qmvaa 204,148,60\nbuffer_load_dwordx4 v40, s[52:55], s95 offen lds\nQmvaa 236,132,64\nQtv 6,45,25856\nQmvaa 80,136,64\nbuffer_load_dwordx4 a[216:219], v60, s[48:51], s94 offen offset:1024 sc1\nQmvaa 112,140,64\nQtv 8,45,20992\n"
"Qmvaa 144,144,64\nQtv 10,45,26112\nQmvaa 176,148,64\ns_waitcnt vmcnt(27)\nds_write_b128 v52, a[172:175] offset:0\ns_waitcnt lgkmcnt(4)\nQmvaa 208,132,68\nQmvaa 240,136,68\n"
"buffer_load_dwordx4 v41, s[52:55], s95 offen offset:1024 lds\nQmvaa 84,140,68\nQtv 12,45,21248\nQmvaa 88,144,68\nQda 76,53,33792\nQmvaa 120,148,68\nQtv 14,45,26368\nQmvaa 152,132,72\n"
"Qtv 16,45,21504\nQmvaa 184,136,72\nbuffer_load_dwordx4 a[220:223], v60, s[48:51], s94 offen offset:2048 sc1\nQmvaa 216,140,72\nQtv 18,45,26624\nQmvaa 248,144,72\nbuffer_load_dwordx4 v42, s[52:55], s95 offen offset:2048 lds\nQmvaa 116,148,72\n"
"s_waitcnt vmcnt(28)\nds_write_b128 v52, a[0:3] offset:4096\ns_waitcnt lgkmcnt(4)\nQmvaa 96,132,76\nQmvaa 128,136,76\nQmvaa 160,140,76\nQmvaa 192,144,76\nQda 80,53,37888\n"
"v_add_u32 v75, 1, v75\nQmvaa 224,148,76\ns_waitcnt vmcnt(34)\nds_write_b32 v73, v75\ns_waitcnt lgkmcnt(3)\nQmvva 92,0,244\nbuffer_load_dwordx4 v43, s[52:55], s95 offen offset:3072 lds\ns_mov_b32 s94, 487424\n"
"Qmvva 124,4,244\nbuffer_load_dwordx4 a[224:227], v60, s[48:51], s94 offen offset:0 sc1\nQmvva 156,8,244\nQtv 20,47,20480\nQmvva 188,12,244\nQtv 22,47,25600\nQmvva 220,16,244\ns_waitcnt vmcnt(28)\n"
"ds_write_b128 v52, a[4:7] offset:8192\nQmvva 252,0,248\nQmvva 100,4,248\nQtv 24,47,20736\nQmvva 132,8,248\nds_read_b32 v69, v74\nQmvva 164,12,248\nQda 48,53,41984\n"
"s_add_u32 m0, s96, 8192\nQmvva 196,16,248\nbuffer_load_dwordx4 v44, s[52:55], s95 offen lds\nQmvva 228,0,252\nQtv 26,47,25856\nQmvva 108,4,252\ns_waitcnt lgkmcnt(2)\nv_cmp_gt_u32 vcc, v75, v69\n"
"Qmvva 140,8,252\ns_cbranch_vccnz .Lso42\n.Lro42:\nQmvva 172,12,252\nbuffer_load_dwordx4 a[228:231], v60, s[48:51], s94 offen offset:1024 sc1\nQmvva 204,16,252\ns_waitcnt vmcnt(27)\nds_write_b128 v52, a[8:11] offset:12288\n"
"Qmvva 236,0,168\nQmvva 80,4,168\nQtv 28,47,20992\nQmvva 112,8,168\nQmvva 144,12,168\nQda 52,53,46080\nQmvva 176,16,168\nQtv 30,47,26112\n"
"Qmvva 208,0,172\nQtv 32,47,21248\nQmvva 240,4,172\nbuffer_load_dwordx4 a[232:235], v60, s[48:51], s94 offen offset:2048 sc1\nQmvva 84,8,172\nQtv 34,47,26368\nQmvva 88,12,172\nQtv 36,47,21504\n"
"Qmvva 120,16,172\ns_waitcnt vmcnt(26)\nds_write_b128 v52, a[12:15] offset:16384\nQmvva 152,0,0\nQmvva 184,4,0\nQtv 38,47,26624\nQmvva 216,8,0\nQmvva 248,12,0\n"
"Qda 56,53,50176\nQmvva 116,16,0\nQmvva 96,0,4\nQmvva 128,4,4\nbuffer_load_dwordx4 a[236:239], v60, s[48:51], s94 offen offset:3072 sc1\nQmvva 160,8,4\nQmvva 192,12,4\nv_add_u32 v75, 1, v75\n"
"Qmvva 224,16,4\nds_write_b32 v73, v75\ns_waitcnt lgkmcnt(2)\nQmvva 92,20,80\ns_waitcnt vmcnt(26)\nds_write_b128 v52, a[16:19] offset:20480\nQmvva 124,24,80\nQmvva 156,28,80\n"
"Qta 112,45,40960\nQmvva 188,32,80\nQda 60,53,0\nQmvva 220,36,80\nQta 114,45,46080\nQmvva 252,20,48\nQta 116,45,41216\ns_mov_b32 s94, 491520\n"
"Qmvva 100,24,48\nbuffer_load_dwordx4 a[240:243], v60, s[48:51], s94 offen offset:0 sc1\nQmvva 132,28,48\nds_read_b32 v69, v74\nQmvva 164,32,48\nQta 118,45,46336\nQmvva 196,36,48\ns_waitcnt vmcnt(26)\n"
"ds_write_b128 v52, a[20:23] offset:25600\ns_waitcnt lgkmcnt(15)\nQmvva 228,20,52\nQmvva 108,24,52\ns_waitcnt lgkmcnt(2)\nv_cmp_gt_u32 vcc, v75, v69\nQmvva 140,28,52\ns_cbranch_vccnz .Lso43\n"
".Lro43:\nQmvva 172,32,52\nQda 64,53,4096\ns_add_u32 m0, s96, 24576\ns_mov_b32 s95, 2048000\nQmvva 204,36,52\nbuffer_load_dwordx4 v40, s[52:55], s95 offen lds\nQmvva 236,20,56\n"
"Qta 120,45,41472\nQmvva 80,24,56\nbuffer_load_dwordx4 a[244:247], v60, s[48:51], s94 offen offset:1024 sc1\nQmvva 112,28,56\nQta 122,45,46592\nQmvva 144,32,56\nQta 124,45,41728\nQmvva 176,36,56\n"
"s_waitcnt vmcnt(26)\nds_write_b128 v52, a[24:27] offset:29696\ns_waitcnt lgkmcnt(4)\nQmvva 208,20,60\nQmvva 240,24,60\nbuffer_load_dwordx4 v41, s[52:55], s95 offen offset:1024 lds\nQmvva 84,28,60\nQta 126,45,46848\n"
"Qmvva 88,32,60\nQda 68,53,8192\nQmvva 120,36,60\nQta 128,45,41984\nQmvva 152,20,64\nQta 130,45,47104\nQmvva 184,24,64\nbuffer_load_dwordx4 a[248:251], v60, s[48:51], s94 offen offset:2048 sc1\n"
"Qmvva 216,28,64\nQmvva 248,32,64\nbuffer_load_dwordx4 v42, s[52:55], s95 offen offset:2048 lds\nQmvva 116,36,64\ns_waitcnt vmcnt(27)\nds_write_b128 v52, a[28:31] offset:33792\ns_waitcnt lgkmcnt(3)\nQmvva 96,20,68\n"
"Qmvva 128,24,68\nQmvva 160,28,68\nQmvva 192,32,68\nQda 72,53,12288\nv_add_u32 v75, 1, v75\nQmvva 224,36,68\ns_waitcnt vmcnt(34)\nds_write_b32 v73, v75\n"
"s_waitcnt lgkmcnt(3)\nQmvaa 92,112,8\nbuffer_load_dwordx4 v43, s[52:55], s95 offen offset:3072 lds\ns_mov_b32 s94, 602112\nQmvaa 124,116,8\nbuffer_load_dwordx4 a[252:255], v60, s[48:51], s94 offen offset:0 sc1\nQmvaa 156,120,8\nQta 132,47,40960\n"
"Qmvaa 188,124,8\nQta 134,47,46080\nQmvaa 220,128,8\ns_waitcnt vmcnt(27)\nds_write_b128 v52, a[32:35] offset:37888\nQmvaa 252,112,12\nQmvaa 100,116,12\nQta 136,47,41216\n"
"Qmvaa 132,120,12\nds_read_b32 v69, v74\nQmvaa 164,124,12\nQda 76,53,16384\ns_add_u32 m0, s96, 28672\nQmvaa 196,128,12\nbuffer_load_dwordx4 v44, s[52:55], s95 offen lds\nQmvaa 228,112,16\n"
"Qta 138,47,46336\nQmvaa 108,116,16\ns_waitcnt lgkmcnt(2)\nv_cmp_gt_u32 vcc, v75, v69\nQmvaa 140,120,16\ns_cbranch_vccnz .Lso44\n.Lro44:\nQmvaa 172,124,16\n"
"buffer_load_dwordx4 a[168:171], v60, s[48:51], s94 offen offset:1024 sc1\nQmvaa 204,128,16\ns_waitcnt vmcnt(26)\nds_write_b128 v52, a[36:39] offset:41984\nQmvaa 236,112,20\nQmvaa 80,116,20\nQta 140,47,41472\nQmvaa 112,120,20\n"
"Qmvaa 144,124,20\nQda 80,53,20480\nQmvaa 176,128,20\nQta 142,47,46592\nQmvaa 208,112,24\nQta 144,47,41728\nQmvaa 240,116,24\nbuffer_load_dwordx4 a[172:175], v60, s[48:51], s94 offen offset:2048 sc1\n"
"Qmvaa 84,120,24\nQta 146,47,46848\nQmvaa 88,124,24\nQta 148,47,41984\nQmvaa 120,128,24\ns_waitcnt vmcnt(25)\nds_write_b128 v52, a[40:43] offset:46080\nQmvaa 152,112,28\n"
"Qmvaa 184,116,28\nQta 150,47,47104\nQmvaa 216,120,28\nQmvaa 248,124,28\nQda 48,53,25600\nQmvaa 116,128,28\nQmvaa 96,112,32\nQmvaa 128,116,32\n"
"buffer_load_dwordx4 a[0:3], v60, s[48:51], s94 offen offset:3072 sc1\nQmvaa 160,120,32\nQmvaa 192,124,32\nv_add_u32 v75, 1, v75\nQmvaa 224,128,32\nds_write_b32 v73, v75\ns_waitcnt lgkmcnt(2)\nQmvaa 92,132,72\n"
"s_waitcnt vmcnt(25)\nds_write_b128 v52, a[44:47] offset:50176\nQmvaa 124,136,72\nQmvaa 156,140,72\nQtv 0,46,0\nQmvaa 188,144,72\nQda 52,53,29696\nQmvaa 220,148,72\n"
"Qtv 2,46,5120\nQmvaa 252,132,76\nQtv 4,46,256\ns_mov_b32 s94, 606208\nQmvaa 100,136,76\nbuffer_load_dwordx4 a[4:7], v60, s[48:51], s94 offen offset:0 sc1\nQmvaa 132,140,76\nds_read_b32 v69, v74\n"
"Qmvaa 164,144,76\nQtv 6,46,5376\nQmvaa 196,148,76\ns_waitcnt vmcnt(25)\nds_write_b128 v52, a[208:211] offset:0\ns_waitcnt lgkmcnt(15)\nQmvaa 228,132,80\nQmvaa 108,136,80\n"
"s_waitcnt lgkmcnt(2)\nv_cmp_gt_u32 vcc, v75, v69\nQmvaa 140,140,80\ns_cbranch_vccnz .Lso45\n.Lro45:\nQmvaa 172,144,80\nQda 56,53,33792\ns_add_u32 m0, s96, 45056\n"
"s_mov_b32 s95, 2375680\nQmvaa 204,148,80\nbuffer_load_dwordx4 v40, s[52:55], s95 offen lds\nQmvaa 236,132,48\nQtv 8,46,512\nQmvaa 80,136,48\nbuffer_load_dwordx4 a[8:11], v60, s[48:51], s94 offen offset:1024 sc1\nQmvaa 112,140,48\n"
"Qtv 10,46,5632\nQmvaa 144,144,48\nQtv 12,46,768\nQmvaa 176,148,48\ns_waitcnt vmcnt(26)\nds_write_b128 v52, a[212:215] offset:4096\ns_waitcnt lgkmcnt(4)\nQmvaa 208,132,52\n"
"Qmvaa 240,136,52\nbuffer_load_dwordx4 v41, s[52:55], s95 offen offset:1024 lds\nQmvaa 84,140,52\nQtv 14,46,5888\nQmvaa 88,144,52\nQda 60,53,37888\nQmvaa 120,148,52\nQtv 16,46,1024\n"
"Qmvaa 152,132,56\nQtv 18,46,6144\nQmvaa 184,136,56\nbuffer_load_dwordx4 a[12:15], v60, s[48:51], s94 offen offset:2048 sc1\nQmvaa 216,140,56\nQmvaa 248,144,56\nbuffer_load_dwordx4 v42, s[52:55], s95 offen offset:2048 lds\nQmvaa 116,148,56\n"
"s_waitcnt vmcnt(27)\nds_write_b128 v52, a[216:219] offset:8192\ns_waitcnt lgkmcnt(3)\nQmvaa 96,132,60\nQmvaa 128,136,60\nQmvaa 160,140,60\nQmvaa 192,144,60\nQda 64,53,41984\n"
"v_add_u32 v75, 1, v75\nQmvaa 224,148,60\ns_waitcnt vmcnt(33)\nds_write_b32 v73, v75\ns_waitcnt lgkmcnt(3)\nQmvva 92,0,36\nbuffer_load_dword v78, v76, s[24:27], 0 offen sc1\ns_mov_b32 s94, 716800\n"
"Qmvva 124,4,36\nbuffer_load_dwordx4 a[16:19], v60, s[48:51], s94 offen offset:0 sc1\nQmvva 156,8,36\nbuffer_load_dwordx4 v43, s[52:55], s95 offen offset:3072 lds\nQmvva 188,12,36\nQtv 20,51,0\nQmvva 220,16,36\ns_waitcnt vmcnt(28)\n"
"ds_write_b128 v52, a[220:223] offset:12288\nQmvva 252,0,40\nQmvva 100,4,40\nQtv 22,51,5120\nQmvva 132,8,40\nds_read_b32 v69, v74\nQmvva 164,12,40\nQda 68,53,46080\n"
"s_add_u32 m0, s96, 49152\nQmvva 196,16,40\nbuffer_load_dwordx4 v44, s[52:55], s95 offen lds\nQmvva 228,0,44\nQtv 24,51,256\nQmvva 108,4,44\ns_waitcnt lgkmcnt(2)\nv_cmp_gt_u32 vcc, v75, v69\n"
"Qmvva 140,8,44\ns_cbranch_vccnz .Lso46\n.Lro46:\nQmvva 172,12,44\nbuffer_load_dwordx4 a[20:23], v60, s[48:51], s94 offen offset:1024 sc1\nQmvva 204,16,44\ns_waitcnt vmcnt(27)\nds_write_b128 v52, a[224:227] offset:16384\n"
"Qmvva 236,0,208\nQmvva 80,4,208\nQtv 26,51,5376\nQmvva 112,8,208\nQtv 28,51,512\nQmvva 144,12,208\nQda 72,53,50176\nQmvva 176,16,208\n"
"Qtv 30,51,5632\nQmvva 208,0,212\nQtv 32,51,768\nQmvva 240,4,212\nbuffer_load_dwordx4 a[24:27], v60, s[48:51], s94 offen offset:2048 sc1\nQmvva 84,8,212\nQtv 34,51,5888\nQmvva 88,12,212\n"
"Qtv 36,51,1024\nQmvva 120,16,212\ns_waitcnt vmcnt(26)\nds_write_b128 v52, a[228:231] offset:20480\nQmvva 152,0,216\nQmvva 184,4,216\nQtv 38,51,6144\nQmvva 216,8,216\n"
"Qmvva 248,12,216\nQda 76,53,0\nQmvva 116,16,216\nQmvva 96,0,220\nQmvva 128,4,220\nbuffer_load_dwordx4 a[28:31], v60, s[48:51], s94 offen offset:3072 sc1\nQmvva 160,8,220\nQmvva 192,12,220\n"
"v_add_u32 v75, 1, v75\nQmvva 224,16,220\nds_write_b32 v73, v75\ns_waitcnt lgkmcnt(2)\nQmvva 92,20,64\ns_waitcnt vmcnt(26)\nds_write_b128 v52, a[232:235] offset:25600\nQmvva 124,24,64\n"
"Qmvva 156,28,64\nQta 112,46,20480\nQmvva 188,32,64\nQda 80,53,4096\nQmvva 220,36,64\nQta 114,46,25600\nQmvva 252,20,68\nQta 116,46,20736\n"
"s_mov_b32 s94, 720896\nQmvva 100,24,68\nbuffer_load_dwordx4 a[32:35], v60, s[48:51], s94 offen offset:0 sc1\nQmvva 132,28,68\nds_read_b32 v69, v74\nQmvva 164,32,68\nQta 118,46,25856\nQmvva 196,36,68\n"
"s_waitcnt vmcnt(26)\nds_write_b128 v52, a[236:239] offset:29696\ns_waitcnt lgkmcnt(15)\nQmvva 228,20,72\nQmvva 108,24,72\ns_waitcnt lgkmcnt(2)\nv_cmp_gt_u32 vcc, v75, v69\nQmvva 140,28,72\n"
"s_cbranch_vccnz .Lso47\n.Lro47:\nQmvva 172,32,72\nQda 48,53,8192\ns_add_u32 m0, s96, 65536\ns_mov_b32 s95, 1433600\nQmvva 204,36,72\nbuffer_load_dwordx4 v40, s[52:55], s95 offen lds\n"
"Qmvva 236,20,76\nQta 120,46,20992\nQmvva 80,24,76\nbuffer_load_dwordx4 a[36:39], v60, s[48:51], s94 offen offset:1024 sc1\nQmvva 112,28,76\nQta 122,46,26112\nQmvva 144,32,76\nQta 124,46,21248\n"
"Qmvva 176,36,76\ns_waitcnt vmcnt(27)\nds_write_b128 v52, a[240:243] offset:33792\ns_waitcnt lgkmcnt(4)\nQmvva 208,20,80\nQmvva 240,24,80\nbuffer_load_dwordx4 v41, s[52:55], s95 offen offset:1024 lds\nQmvva 84,28,80\n"
"Qta 126,46,26368\nQmvva 88,32,80\nQda 52,53,12288\nQmvva 120,36,80\nQta 128,46,21504\nQmvva 152,20,48\nQta 130,46,26624\nQmvva 184,24,48\n"
"buffer_load_dwordx4 a[40:43], v60, s[48:51], s94 offen offset:2048 sc1\nQmvva 216,28,48\nQmvva 248,32,48\nbuffer_load_dwordx4 v42, s[52:55], s95 offen offset:2048 lds\nQmvva 116,36,48\ns_waitcnt vmcnt(28)\nds_write_b128 v52, a[244:247] offset:37888\ns_waitcnt lgkmcnt(3)\n"
"Qmvva 96,20,52\nQmvva 128,24,52\nQmvva 160,28,52\nQmvva 192,32,52\nQda 56,53,16384\nv_add_u32 v75, 1, v75\nQmvva 224,36,52\ns_waitcnt vmcnt(34)\n"
"ds_write_b32 v73, v75\ns_waitcnt lgkmcnt(3)\nQmvaa 92,112,224\ns_mov_b32 s94, 831488\ns_waitcnt vmcnt(12)\nv_sub_u32 v66, v78, v65\nv_cmp_gt_u32 vcc, 5, v66\ns_and_b64 vcc, vcc, s[28:29]\n"
"s_cbranch_scc1 .Lf5_w\n.Lf5_r:\nQmvaa 124,116,224\nbuffer_load_dwordx4 a[44:47], v60, s[48:51], s94 offen offset:0 sc1\nQmvaa 156,120,224\nbuffer_load_dwordx4 v43, s[52:55], s95 offen offset:3072 lds\nQmvaa 188,124,224\nQta 132,51,20480\n"
"Qmvaa 220,128,224\ns_waitcnt vmcnt(28)\nds_write_b128 v52, a[248:251] offset:41984\nQmvaa 252,112,228\nQmvaa 100,116,228\nQta 134,51,25600\nQmvaa 132,120,228\nds_read_b32 v69, v74\n"
"Qmvaa 164,124,228\nQda 60,53,20480\ns_add_u32 m0, s96, 69632\nQmvaa 196,128,228\nbuffer_load_dwordx4 v44, s[52:55], s95 offen lds\nQmvaa 228,112,232\nQta 136,51,20736\nQmvaa 108,116,232\n"
"s_waitcnt lgkmcnt(2)\nv_cmp_gt_u32 vcc, v75, v69\nQmvaa 140,120,232\ns_cbranch_vccnz .Lso48\n.Lro48:\nQmvaa 172,124,232\nbuffer_load_dwordx4 a[208:211], v60, s[48:51], s94 offen offset:1024 sc1\nQmvaa 204,128,232\n"
"s_waitcnt vmcnt(27)\nds_write_b128 v52, a[252:255] offset:46080\nQmvaa 236,112,236\nQmvaa 80,116,236\nQta 138,51,25856\nQmvaa 112,120,236\nQta 140,51,20992\nQmvaa 144,124,236\n"
"Qda 64,53,25600\nQmvaa 176,128,236\nQta 142,51,26112\nQmvaa 208,112,240\nQta 144,51,21248\nQmvaa 240,116,240\nbuffer_load_dwordx4 a[212:215], v60, s[48:51], s94 offen offset:2048 sc1\nQmvaa 84,120,240\n"
"Qta 146,51,26368\nQmvaa 88,124,240\nQta 148,51,21504\nQmvaa 120,128,240\ns_waitcnt vmcnt(26)\nds_write_b128 v52, a[168:171] offset:50176\nQmvaa 152,112,244\nQmvaa 184,116,244\n"
"Qta 150,51,26624\nQmvaa 216,120,244\nQmvaa 248,124,244\nQda 68,53,29696\nQmvaa 116,128,244\nQmvaa 96,112,248\nQmvaa 128,116,248\nbuffer_load_dwordx4 a[216:219], v60, s[48:51], s94 offen offset:3072 sc1\n"
"Qmvaa 160,120,248\nQmvaa 192,124,248\nv_add_u32 v75, 1, v75\nQmvaa 224,128,248\nds_write_b32 v73, v75\ns_lshr_b32 s68, %19, 2\ns_xor_b32 s68, s68, s92\ns_cbranch_scc1 .Lrole1\n"
"s_waitcnt lgkmcnt(2)\nQmvaa 92,132,56\ns_waitcnt vmcnt(26)\nds_write_b128 v52, a[172:175] offset:0\nQmvaa 124,136,56\nQmvaa 156,140,56\nQtv 0,45,0\nQmvaa 188,144,56\n"
"Qda 72,53,33792\nQmvaa 220,148,56\nQtv 2,45,5120\nQmvaa 252,132,60\nQtv 4,45,256\ns_mov_b32 s94, 835584\nQmvaa 100,136,60\nbuffer_load_dwordx4 a[220:223], v60, s[48:51], s94 offen offset:0 sc1\n"
"Qmvaa 132,140,60\nds_read_b32 v69, v74\nQmvaa 164,144,60\nQtv 6,45,5376\nQmvaa 196,148,60\ns_waitcnt vmcnt(26)\nds_write_b128 v52, a[0:3] offset:4096\ns_waitcnt lgkmcnt(15)\n"
"Qmvaa 228,132,64\nQmvaa 108,136,64\ns_waitcnt lgkmcnt(2)\nv_cmp_gt_u32 vcc, v75, v69\nQmvaa 140,140,64\ns_cbranch_vccnz .Lso49r0\n.Lro49r0:\nQmvaa 172,144,64\n"
"Qda 76,53,37888\ns_add_u32 m0, s96, 86016\ns_mov_b32 s95, 1761280\nQmvaa 204,148,64\nbuffer_load_dwordx4 v40, s[52:55], s95 offen lds\nQmvaa 236,132,68\nQtv 8,45,512\nQmvaa 80,136,68\n"
"buffer_load_dwordx4 a[224:227], v60, s[48:51], s94 offen offset:1024 sc1\nQmvaa 112,140,68\nQtv 10,45,5632\nQmvaa 144,144,68\nQtv 12,45,768\nQmvaa 176,148,68\ns_waitcnt vmcnt(27)\nds_write_b128 v52, a[4:7] offset:8192\n"
"s_waitcnt lgkmcnt(4)\nQmvaa 208,132,72\nQmvaa 240,136,72\nbuffer_load_dwordx4 v41, s[52:55], s95 offen offset:1024 lds\nQmvaa 84,140,72\nQtv 14,45,5888\nQmvaa 88,144,72\nQda 80,53,41984\n"
"Qmvaa 120,148,72\nQtv 16,45,1024\nQmvaa 152,132,76\nQtv 18,45,6144\nQmvaa 184,136,76\nbuffer_load_dwordx4 a[228:231], v60, s[48:51], s94 offen offset:2048 sc1\nQmvaa 216,140,76\nQmvaa 248,144,76\n"
"buffer_load_dwordx4 v42, s[52:55], s95 offen offset:2048 lds\nQmvaa 116,148,76\ns_waitcnt vmcnt(28)\nds_write_b128 v52, a[8:11] offset:12288\ns_waitcnt lgkmcnt(3)\nQmvaa 96,132,80\nQmvaa 128,136,80\nQmvaa 160,140,80\n"
"Qmvaa 192,144,80\nQda 48,53,46080\nv_add_u32 v75, 1, v75\nQmvaa 224,148,80\ns_waitcnt vmcnt(34)\nds_write_b32 v73, v75\ns_waitcnt lgkmcnt(3)\nQmvva 92,0,252\n"
"buffer_load_dwordx4 v43, s[52:55], s95 offen offset:3072 lds\ns_mov_b32 s94, 505856\nQmvva 124,4,252\nbuffer_load_dwordx4 a[232:235], v60, s[48:51], s94 offen offset:0 sc1\nQmvva 156,8,252\nQtv 20,47,0\nQmvva 188,12,252\nQtv 22,47,5120\n"
"Qmvva 220,16,252\ns_waitcnt vmcnt(28)\nds_write_b128 v52, a[12:15] offset:16384\nQmvva 252,0,168\nQmvva 100,4,168\nQtv 24,47,256\nQmvva 132,8,168\nds_read_b32 v69, v74\n"
"Qmvva 164,12,168\nQda 52,53,50176\ns_add_u32 m0, s96, 90112\nQmvva 196,16,168\nbuffer_load_dwordx4 v44, s[52:55], s95 offen lds\nQmvva 228,0,172\nQtv 26,47,5376\nQmvva 108,4,172\n"
"s_waitcnt lgkmcnt(2)\nv_cmp_gt_u32 vcc, v75, v69\nQmvva 140,8,172\ns_cbranch_vccnz .Lso50r0\n.Lro50r0:\nQmvva 172,12,172\nbuffer_load_dwordx4 a[236:239], v60, s[48:51], s94 offen offset:1024 sc1\nQmvva 204,16,172\n"
"s_waitcnt vmcnt(27)\nds_write_b128 v52, a[16:19] offset:20480\nQmvva 236,0,0\nQmvva 80,4,0\nQtv 28,47,512\nQmvva 112,8,0\nQmvva 144,12,0\nQda 56,53,0\n"
"Qmvva 176,16,0\nQtv 30,47,5632\nQmvva 208,0,4\nQtv 32,47,768\nQmvva 240,4,4\nbuffer_load_dwordx4 a[240:243], v60, s[48:51], s94 offen offset:2048 sc1\nQmvva 84,8,4\nQtv 34,47,5888\n"
"Qmvva 88,12,4\nQtv 36,47,1024\nQmvva 120,16,4\ns_waitcnt vmcnt(25)\nds_write_b128 v52, a[20:23] offset:25600\nQmvva 152,0,8\nQmvva 184,4,8\nQtv 38,47,6144\n"
"Qmvva 216,8,8\nQmvva 248,12,8\nQda 60,53,4096\nQmvva 116,16,8\nQmvva 96,0,12\ns_mov_b32 s94, 620544\nQmvva 128,4,12\nbuffer_load_dwordx4 a[244:247], v60, s[48:51], s94 offen offset:0 sc1\n"
"Qmvva 160,8,12\nQmvva 192,12,12\nv_add_u32 v75, 1, v75\nQmvva 224,16,12\nds_write_b32 v73, v75\ns_waitcnt lgkmcnt(2)\nQmvva 92,20,48\ns_waitcnt vmcnt(25)\n"
"ds_write_b128 v52, a[24:27] offset:29696\nQmvva 124,24,48\nQmvva 156,28,48\nQta 112,45,20480\nQmvva 188,32,48\nQda 64,53,8192\nQmvva 220,36,48\nQta 114,45,25600\n"
"Qmvva 252,20,52\nQta 116,45,20736\nQmvva 100,24,52\nbuffer_load_dwordx4 a[248:251], v60, s[48:51], s94 offen offset:1024 sc1\nQmvva 132,28,52\nds_read_b32 v69, v74\nQmvva 164,32,52\nQta 118,45,25856\n"
"Qmvva 196,36,52\ns_waitcnt vmcnt(25)\nds_write_b128 v52, a[28:31] offset:33792\ns_waitcnt lgkmcnt(15)\nQmvva 228,20,56\nQmvva 108,24,56\ns_waitcnt lgkmcnt(2)\nv_cmp_gt_u32 vcc, v75, v69\n"
"Qmvva 140,28,56\ns_cbranch_vccnz .Lso51r0\n.Lro51r0:\nQmvva 172,32,56\nQda 68,53,12288\ns_add_u32 m0, s96, 4096\ns_mov_b32 s95, 2088960\nQmvva 204,36,56\n"
"buffer_load_dwordx4 v40, s[52:55], s95 offen lds\nQmvva 236,20,60\nQta 120,45,20992\nQmvva 80,24,60\nbuffer_load_dwordx4 a[252:255], v60, s[48:51], s94 offen offset:2048 sc1\nQmvva 112,28,60\nQta 122,45,26112\nQmvva 144,32,60\n"
"Qta 124,45,21248\nQmvva 176,36,60\ns_waitcnt vmcnt(26)\nds_write_b128 v52, a[32:35] offset:37888\ns_waitcnt lgkmcnt(4)\nQmvva 208,20,64\nQmvva 240,24,64\nbuffer_load_dwordx4 v41, s[52:55], s95 offen offset:1024 lds\n"
"Qmvva 84,28,64\nQta 126,45,26368\nQmvva 88,32,64\nQda 72,53,16384\nQmvva 120,36,64\nQta 128,45,21504\nQmvva 152,20,68\nQta 130,45,26624\n"
"s_mov_b32 s94, 735232\nQmvva 184,24,68\nbuffer_load_dwordx4 a[168:171], v60, s[48:51], s94 offen offset:0 sc1\nQmvva 216,28,68\nQmvva 248,32,68\nbuffer_load_dwordx4 v42, s[52:55], s95 offen offset:2048 lds\nQmvva 116,36,68\ns_waitcnt vmcnt(27)\n"
"ds_write_b128 v52, a[36:39] offset:41984\ns_waitcnt lgkmcnt(3)\nQmvva 96,20,72\nQmvva 128,24,72\nQmvva 160,28,72\nQmvva 192,32,72\nQda 76,53,20480\nv_add_u32 v75, 1, v75\n"
"Qmvva 224,36,72\ns_waitcnt vmcnt(33)\nds_write_b32 v73, v75\ns_waitcnt lgkmcnt(3)\nQmvaa 92,112,16\nbuffer_load_dwordx4 v43, s[52:55], s95 offen offset:3072 lds\nQmvaa 124,116,16\nbuffer_load_dwordx4 a[172:175], v60, s[48:51], s94 offen offset:1024 sc1\n"
"Qmvaa 156,120,16\nQta 132,47,20480\nQmvaa 188,124,16\nQta 134,47,25600\nQmvaa 220,128,16\ns_waitcnt vmcnt(27)\nds_write_b128 v52, a[40:43] offset:46080\nQmvaa 252,112,20\n"
"Qmvaa 100,116,20\nQta 136,47,20736\nQmvaa 132,120,20\nds_read_b32 v69, v74\nQmvaa 164,124,20\nQda 80,53,25600\ns_add_u32 m0, s96, 8192\nQmvaa 196,128,20\n"
"buffer_load_dwordx4 v44, s[52:55], s95 offen lds\nQmvaa 228,112,24\nQta 138,47,25856\nQmvaa 108,116,24\ns_waitcnt lgkmcnt(2)\nv_cmp_gt_u32 vcc, v75, v69\nQmvaa 140,120,24\ns_cbranch_vccnz .Lso52r0\n"
".Lro52r0:\nQmvaa 172,124,24\nbuffer_load_dwordx4 a[0:3], v60, s[48:51], s94 offen offset:2048 sc1\nQmvaa 204,128,24\ns_waitcnt vmcnt(27)\nds_write_b128 v52, a[44:47] offset:50176\nQmvaa 236,112,28\nQmvaa 80,116,28\n"
"Qta 140,47,20992\nQmvaa 112,120,28\nQmvaa 144,124,28\nQda 48,53,29696\nQmvaa 176,128,28\nQta 142,47,26112\nQmvaa 208,112,32\nQta 144,47,21248\n"
"s_mov_b32 s94, 849920\nQmvaa 240,116,32\nbuffer_load_dwordx4 a[4:7], v60, s[48:51], s94 offen offset:0 sc1\nQmvaa 84,120,32\nQta 146,47,26368\nQmvaa 88,124,32\nQta 148,47,21504\nQmvaa 120,128,32\n"
"s_waitcnt vmcnt(25)\nds_write_b128 v52, a[208:211] offset:0\nQmvaa 152,112,36\nQmvaa 184,116,36\nQta 150,47,26624\nQmvaa 216,120,36\nQmvaa 248,124,36\nQda 52,53,33792\n"
"Qmvaa 116,128,36\nQmvaa 96,112,40\nQmvaa 128,116,40\nbuffer_load_dwordx4 a[8:11], v60, s[48:51], s94 offen offset:1024 sc1\nQmvaa 160,120,40\nQmvaa 192,124,40\nv_add_u32 v75, 1, v75\nQmvaa 224,128,40\n"
"ds_write_b32 v73, v75\ns_waitcnt lgkmcnt(2)\nQmvaa 92,132,76\ns_waitcnt vmcnt(25)\nds_write_b128 v52, a[212:215] offset:4096\nQmvaa 124,136,76\nQmvaa 156,140,76\nQtv 0,45,40960\n"
"Qmvaa 188,144,76\nQda 56,53,37888\nQmvaa 220,148,76\nQtv 2,45,46080\nQmvaa 252,132,80\nQtv 4,45,41216\nQmvaa 100,136,80\nbuffer_load_dwordx4 a[12:15], v60, s[48:51], s94 offen offset:2048 sc1\n"
"Qmvaa 132,140,80\nds_read_b32 v69, v74\nQmvaa 164,144,80\nQtv 6,45,46336\nQmvaa 196,148,80\ns_waitcnt vmcnt(25)\nds_write_b128 v52, a[216:219] offset:8192\ns_waitcnt lgkmcnt(15)\n"
"Qmvaa 228,132,48\nQmvaa 108,136,48\ns_waitcnt lgkmcnt(2)\nv_cmp_gt_u32 vcc, v75, v69\nQmvaa 140,140,48\ns_cbranch_vccnz .Lso53r0\n.Lro53r0:\nQmvaa 172,144,48\n"
"Qda 60,53,41984\ns_add_u32 m0, s96, 24576\ns_mov_b32 s95, 2416640\nQmvaa 204,148,48\nbuffer_load_dwordx4 v40, s[52:55], s95 offen lds\nQmvaa 236,132,52\nQtv 8,45,41472\ns_mov_b32 s94, 501760\n"
"Qmvaa 80,136,52\nbuffer_load_dwordx4 a[16:19], v60, s[48:51], s94 offen offset:0 sc1\nQmvaa 112,140,52\nQtv 10,45,46592\nQmvaa 144,144,52\nQtv 12,45,41728\nQmvaa 176,148,52\ns_waitcnt vmcnt(26)\n"
"ds_write_b128 v52, a[220:223] offset:12288\ns_waitcnt lgkmcnt(4)\nQmvaa 208,132,56\nQmvaa 240,136,56\nbuffer_load_dwordx4 v41, s[52:55], s95 offen offset:1024 lds\nQmvaa 84,140,56\nQtv 14,45,46848\nQmvaa 88,144,56\n"
"Qda 64,53,46080\nQmvaa 120,148,56\nQtv 16,45,41984\nQmvaa 152,132,60\nQtv 18,45,47104\nQmvaa 184,136,60\nbuffer_load_dwordx4 a[20:23], v60, s[48:51], s94 offen offset:1024 sc1\nQmvaa 216,140,60\n"
"Qmvaa 248,144,60\nbuffer_load_dwordx4 v42, s[52:55], s95 offen offset:2048 lds\nQmvaa 116,148,60\ns_waitcnt vmcnt(27)\nds_write_b128 v52, a[224:227] offset:16384\ns_waitcnt lgkmcnt(3)\nQmvaa 96,132,64\nQmvaa 128,136,64\n"
"Qmvaa 160,140,64\nQmvaa 192,144,64\nQda 68,53,50176\nv_add_u32 v75, 1, v75\nQmvaa 224,148,64\ns_waitcnt vmcnt(33)\nds_write_b32 v73, v75\ns_waitcnt lgkmcnt(3)\n"
"Qmvva 92,0,44\nbuffer_load_dwordx4 v43, s[52:55], s95 offen offset:3072 lds\nQmvva 124,4,44\nbuffer_load_dwordx4 a[24:27], v60, s[48:51], s94 offen offset:2048 sc1\nQmvva 156,8,44\nQtv 20,47,40960\nQmvva 188,12,44\nQtv 22,47,46080\n"
"Qmvva 220,16,44\ns_waitcnt vmcnt(27)\nds_write_b128 v52, a[228:231] offset:20480\nQmvva 252,0,208\nQmvva 100,4,208\nQtv 24,47,41216\nQmvva 132,8,208\nds_read_b32 v69, v74\n"
"Qmvva 164,12,208\nQda 72,53,0\ns_add_u32 m0, s96, 28672\nQmvva 196,16,208\nbuffer_load_dwordx4 v44, s[52:55], s95 offen lds\nQmvva 228,0,212\nQtv 26,47,46336\nQmvva 108,4,212\n"
"s_waitcnt lgkmcnt(2)\nv_cmp_gt_u32 vcc, v75, v69\nQmvva 140,8,212\ns_cbranch_vccnz .Lso54r0\n.Lro54r0:\nQmvva 172,12,212\nbuffer_load_dwordx4 a[28:31], v60, s[48:51], s94 offen offset:3072 sc1\nQmvva 204,16,212\n"
"s_waitcnt vmcnt(26)\nds_write_b128 v52, a[232:235] offset:25600\nQmvva 236,0,216\nQmvva 80,4,216\nQtv 28,47,41472\nQmvva 112,8,216\nQmvva 144,12,216\nQda 76,53,4096\n"
"Qmvva 176,16,216\nQtv 30,47,46592\nQmvva 208,0,220\nQtv 32,47,41728\ns_mov_b32 s94, 616448\nQmvva 240,4,220\nbuffer_load_dwordx4 a[32:35], v60, s[48:51], s94 offen offset:0 sc1\nQmvva 84,8,220\n"
"Qtv 34,47,46848\nQmvva 88,12,220\nQtv 36,47,41984\nQmvva 120,16,220\ns_waitcnt vmcnt(25)\nds_write_b128 v52, a[236:239] offset:29696\nQmvva 152,0,224\nQmvva 184,4,224\n"
"Qtv 38,47,47104\nQmvva 216,8,224\nQmvva 248,12,224\nQda 80,53,8192\nQmvva 116,16,224\nQmvva 96,0,228\nQmvva 128,4,228\nbuffer_load_dwordx4 a[36:39], v60, s[48:51], s94 offen offset:1024 sc1\n"
"Qmvva 160,8,228\nQmvva 192,12,228\nv_add_u32 v75, 1, v75\nQmvva 224,16,228\ns_waitcnt vmcnt(27)\nds_write_b32 v73, v75\ns_waitcnt lgkmcnt(2)\nQmvva 92,20,68\n"
"s_waitcnt vmcnt(25)\nds_write_b128 v52, a[240:243] offset:33792\nQmvva 124,24,68\nQmvva 156,28,68\nQta 112,46,0\nQmvva 188,32,68\nQda 48,53,12288\nQmvva 220,36,68\n"
"Qta 114,46,5120\nQmvva 252,20,72\nQta 116,46,256\nQmvva 100,24,72\nbuffer_load_dwordx4 a[40:43], v60, s[48:51], s94 offen offset:2048 sc1\nQmvva 132,28,72\nds_read_b32 v69, v74\nQmvva 164,32,72\n"
"Qmvva 196,36,72\ns_waitcnt vmcnt(25)\nds_write_b128 v52, a[244:247] offset:37888\ns_waitcnt lgkmcnt(15)\nQmvva 228,20,76\nQmvva 108,24,76\ns_waitcnt lgkmcnt(1)\nv_cmp_gt_u32 vcc, v75, v69\n"
"Qmvva 140,28,76\ns_cbranch_vccnz .Lso55r0\n.Lro55r0:\nQmvva 172,32,76\nQda 52,53,16384\nQmvva 204,36,76\nQta 118,46,5376\nQmvva 236,20,80\n"
"Qta 120,46,512\nQmvva 80,24,80\nbuffer_load_dwordx4 a[44:47], v60, s[48:51], s94 offen offset:3072 sc1\nQmvva 112,28,80\nQmvva 144,32,80\nQta 122,46,5632\nQmvva 176,36,80\ns_waitcnt vmcnt(25)\n"
"ds_write_b128 v52, a[248:251] offset:41984\ns_waitcnt lgkmcnt(4)\nQmvva 208,20,48\nQmvva 240,24,48\nQta 124,46,768\nQmvva 84,28,48\nQta 126,46,5888\nQmvva 88,32,48\n"
"Qda 56,53,20480\nQmvva 120,36,48\nQmvva 152,20,52\nQta 128,46,1024\ns_mov_b32 s94, 731136\nQmvva 184,24,52\nbuffer_load_dwordx4 a[208:211], v60, s[48:51], s94 offen offset:0 sc1\nQmvva 216,28,52\n"
"Qta 130,46,6144\nQmvva 248,32,52\nQta 132,51,0\nQta 134,51,5120\nQmvva 116,36,52\ns_waitcnt vmcnt(24)\nds_write_b128 v52, a[252:255] offset:46080\ns_waitcnt lgkmcnt(5)\n"
"Qmvva 96,20,56\nQmvva 128,24,56\nQta 136,51,256\nQmvva 160,28,56\nQta 138,51,5376\nQmvva 192,32,56\nQda 60,53,25600\nv_add_u32 v75, 1, v75\n"
"Qmvva 224,36,56\ns_waitcnt vmcnt(18)\nds_write_b32 v73, v75\ns_waitcnt lgkmcnt(7)\nQmvaa 208,112,232\nQta 140,51,512\nQmvaa 240,116,232\nbuffer_load_dwordx4 a[212:215], v60, s[48:51], s94 offen offset:1024 sc1\n"
"Qmvaa 84,120,232\nQta 142,51,5632\nQmvaa 88,124,232\nQta 144,51,768\nQmvaa 120,128,232\ns_waitcnt vmcnt(23)\nds_write_b128 v52, a[168:171] offset:50176\nQmvaa 152,112,236\n"
"Qmvaa 184,116,236\nQta 146,51,5888\nQmvaa 216,120,236\nds_read_b32 v69, v74\nQmvaa 248,124,236\nQda 64,53,29696\nQmvaa 116,128,236\nQta 148,51,1024\n"
"Qmvaa 96,112,240\nQta 150,51,6144\nQmvaa 128,116,240\ns_waitcnt lgkmcnt(3)\nv_cmp_gt_u32 vcc, v75, v69\nQmvaa 160,120,240\ns_cbranch_vccnz .Lso56r0\n.Lro56r0:\n"
"Qmvaa 192,124,240\nbuffer_load_dwordx4 a[216:219], v60, s[48:51], s94 offen offset:2048 sc1\nQmvaa 224,128,240\ns_waitcnt vmcnt(21)\nds_write_b128 v52, a[172:175] offset:0\ns_waitcnt lgkmcnt(1)\nQmvaa 208,132,60\nQmvaa 240,136,60\n"
"Qtv 0,46,20480\nQtv 2,46,25600\nQmvaa 84,140,60\nQtv 4,46,20736\nQtv 6,46,25856\nQmvaa 88,144,60\nQda 68,53,33792\nQmvaa 120,148,60\n"
"Qtv 8,46,20992\nQmvaa 152,132,64\nQtv 10,46,26112\nQtv 12,46,21248\nQmvaa 184,136,64\nbuffer_load_dwordx4 a[220:223], v60, s[48:51], s94 offen offset:3072 sc1\nQmvaa 216,140,64\nQtv 14,46,26368\n"
"Qtv 16,46,21504\nQmvaa 248,144,64\nQtv 18,46,26624\nQmvaa 116,148,64\ns_waitcnt vmcnt(20)\nds_write_b128 v52, a[0:3] offset:4096\ns_waitcnt lgkmcnt(7)\nQmvaa 96,132,68\n"
"Qmvaa 128,136,68\nQtv 20,51,20480\nQtv 22,51,25600\nQmvaa 160,140,68\nQtv 24,51,20736\nQmvaa 192,144,68\nQda 72,53,37888\nQmvaa 224,148,68\n"
"Qtv 26,51,25856\ns_waitcnt lgkmcnt(6)\nQmvva 208,0,244\nQtv 28,51,20992\ns_mov_b32 s94, 845824\nQmvva 240,4,244\nbuffer_load_dwordx4 a[224:227], v60, s[48:51], s94 offen offset:0 sc1\nQmvva 84,8,244\n"
"Qtv 30,51,26112\nQmvva 88,12,244\nQtv 32,51,21248\nv_add_u32 v75, 1, v75\nQmvva 120,16,244\ns_waitcnt vmcnt(10)\nds_write_b32 v73, v75\nQmvva 152,0,248\n"
"s_waitcnt vmcnt(20)\nds_write_b128 v52, a[4:7] offset:8192\nQmvva 184,4,248\nQmvva 216,8,248\nQtv 34,51,26368\nQmvva 248,12,248\nQda 76,53,41984\nQmvva 116,16,248\n"
"Qtv 36,51,21504\nQmvva 96,0,252\nQtv 38,51,26624\nQmvva 128,4,252\nbuffer_load_dwordx4 a[228:231], v60, s[48:51], s94 offen offset:1024 sc1\nQmvva 160,8,252\nds_read_b32 v69, v74\nQmvva 192,12,252\n"
"Qta 112,45,0\nQta 114,45,5120\nQmvva 224,16,252\ns_waitcnt vmcnt(20)\nds_write_b128 v52, a[8:11] offset:12288\ns_waitcnt lgkmcnt(13)\nQmvva 208,20,72\nQmvva 240,24,72\n"
"s_waitcnt lgkmcnt(3)\nv_cmp_gt_u32 vcc, v75, v69\nQmvva 84,28,72\ns_cbranch_vccnz .Lso57r0\n.Lro57r0:\nQmvva 88,32,72\nQda 80,53,46080\nQmvva 120,36,72\n"
"Qta 116,45,256\nQta 118,45,5376\nQmvva 152,20,76\nQta 120,45,512\nQta 122,45,5632\nQmvva 184,24,76\nbuffer_load_dwordx4 a[232:235], v60, s[48:51], s94 offen offset:2048 sc1\nQmvva 216,28,76\n"
"Qta 124,45,768\nQta 126,45,5888\nQmvva 248,32,76\nQta 128,45,1024\nQta 130,45,6144\nQmvva 116,36,76\ns_waitcnt vmcnt(20)\nds_write_b128 v52, a[12:15] offset:16384\n"
"s_waitcnt lgkmcnt(9)\nQmvva 96,20,80\nQmvva 128,24,80\nQta 132,47,0\nQta 134,47,5120\nQmvva 160,28,80\nQta 136,47,256\nQmvva 192,32,80\n"
"Qda 48,53,50176\nQmvva 224,36,80\nQta 138,47,5376\ns_waitcnt lgkmcnt(6)\nQmvaa 208,112,168\nQta 140,47,512\nQmvaa 240,116,168\nbuffer_load_dwordx4 a[236:239], v60, s[48:51], s94 offen offset:3072 sc1\n"
"Qmvaa 84,120,168\nQta 142,47,5632\nQmvaa 88,124,168\nQta 144,47,768\nQmvaa 120,128,168\ns_waitcnt vmcnt(19)\nds_write_b128 v52, a[16:19] offset:20480\nQmvaa 152,112,172\n"
"Qmvaa 184,116,172\nQta 146,47,5888\nQmvaa 216,120,172\nQta 148,47,1024\nQmvaa 248,124,172\nQda 52,53,0\nv_add_u32 v75, 1, v75\nQmvaa 116,128,172\n"
"ds_write_b32 v73, v75\nQmvaa 96,112,0\nQta 150,47,6144\nQmvaa 128,116,0\nQtv 0,45,20480\nQtv 2,45,25600\nQmvaa 160,120,0\nQtv 4,45,20736\n"
"Qmvaa 192,124,0\nQtv 6,45,25856\nQtv 8,45,20992\nQmvaa 224,128,0\ns_waitcnt vmcnt(17)\nds_write_b128 v52, a[20:23] offset:25600\ns_waitcnt lgkmcnt(6)\nQmvaa 208,132,48\n"
"Qmvaa 240,136,48\nQtv 10,45,26112\nQmvaa 84,140,48\nds_read_b32 v69, v74\nQmvaa 88,144,48\nQda 56,53,4096\nQmvaa 120,148,48\nQtv 12,45,21248\n"
"Qtv 14,45,26368\nQmvaa 152,132,52\nQtv 16,45,21504\nQmvaa 184,136,52\ns_waitcnt lgkmcnt(4)\nv_cmp_gt_u32 vcc, v75, v69\nQmvaa 216,140,52\ns_cbranch_vccnz .Lso58r0\n"
".Lro58r0:\nQmvaa 248,144,52\nQtv 18,45,26624\nQmvaa 116,148,52\ns_waitcnt vmcnt(14)\nds_write_b128 v52, a[24:27] offset:29696\ns_waitcnt lgkmcnt(5)\nQmvaa 96,132,56\n"
"Qmvaa 128,136,56\nQtv 20,47,20480\nQmvaa 160,140,56\nQtv 22,47,25600\nQmvaa 192,144,56\nQda 60,53,8192\nQmvaa 224,148,56\nQtv 24,47,20736\n"
"s_waitcnt lgkmcnt(5)\nQmvva 208,0,4\nQtv 26,47,25856\nQmvva 240,4,4\nQtv 28,47,20992\nQmvva 84,8,4\nQtv 30,47,26112\nQmvva 88,12,4\n"
"Qtv 32,47,21248\nQmvva 120,16,4\ns_waitcnt vmcnt(12)\nds_write_b128 v52, a[28:31] offset:33792\nQmvva 152,0,8\nQmvva 184,4,8\nQtv 34,47,26368\nQmvva 216,8,8\n"
"Qtv 36,47,21504\nQmvva 248,12,8\nQda 64,53,12288\nQmvva 116,16,8\nQtv 38,47,26624\nQmvva 96,0,12\nQta 112,46,0\nQta 114,46,5120\n"
"Qmvva 128,4,12\nQta 116,46,256\nQmvva 160,8,12\nQta 118,46,5376\nQmvva 192,12,12\nQta 120,46,512\nv_add_u32 v75, 1, v75\nQmvva 224,16,12\n"
"ds_write_b32 v73, v75\ns_waitcnt lgkmcnt(6)\nQmvva 208,20,60\ns_waitcnt vmcnt(11)\nds_write_b128 v52, a[32:35] offset:37888\nQmvva 240,24,60\ns_add_u32 s68, s64, 0\nQmvva 84,28,60\n"
"Qta 122,46,5632\nQmvva 88,32,60\nQda 68,53,16384\nQmvva 120,36,60\nQta 124,46,768\nQmvva 152,20,64\nQta 126,46,5888\nQmvva 184,24,64\n"
"Qta 128,46,1024\nQk 208,208,209\nQmvva 216,28,64\nds_read_b32 v69, v74\nv_min_u32 v208, -2, v208\nQmvva 248,32,64\nQta 130,46,6144\nQk 209,210,211\n"
"Qmvva 116,36,64\ns_waitcnt vmcnt(10)\nds_write_b128 v52, a[36:39] offset:41984\ns_waitcnt lgkmcnt(6)\nQmvva 96,20,68\nv_min_u32 v209, -2, v209\nQk 210,240,241\nQmvva 128,24,68\n"
"s_waitcnt lgkmcnt(2)\nv_cmp_gt_u32 vcc, v75, v69\nQmvva 160,28,68\ns_cbranch_vccnz .Lso59r0\n.Lro59r0:\nv_min_u32 v210, -2, v210\nQmvva 192,32,68\nQda 72,53,20480\n"
"Qk 211,242,243\nQmvva 224,36,68\nQta 132,51,0\nv_min_u32 v211, -2, v211\ns_waitcnt lgkmcnt(3)\nQmvaa 92,112,16\nQta 134,51,5120\nbuffer_store_dwordx4 v[208:211], v60, s[24:27], s68 offen offset:0 sc1\n"
"Qmvaa 124,116,16\nQta 136,51,256\nQk 84,84,85\nQmvaa 156,120,16\nQta 138,51,5376\nv_min_u32 v84, -2, v84\nQmvaa 188,124,16\nQta 140,51,512\n"
"Qk 85,86,87\nQmvaa 220,128,16\ns_waitcnt vmcnt(10)\nds_write_b128 v52, a[40:43] offset:46080\nQmvaa 252,112,20\nv_min_u32 v85, -2, v85\nQk 86,88,89\nQmvaa 100,116,20\n"
"Qta 142,51,5632\nv_min_u32 v86, -2, v86\nQmvaa 132,120,20\nQta 144,51,768\nQk 87,90,91\nQmvaa 164,124,20\nQda 76,53,25600\nv_min_u32 v87, -2, v87\n"
"Qmvaa 196,128,20\nQta 146,51,5888\nbuffer_store_dwordx4 v[84:87], v60, s[24:27], s68 offen offset:1024 sc1\nQmvaa 228,112,24\nQta 148,51,1024\nQk 120,120,121\nQmvaa 108,116,24\nQta 150,51,6144\n"
"v_min_u32 v120, -2, v120\nQmvaa 140,120,24\nQtv 0,46,20480\nQk 121,122,123\nQmvaa 172,124,24\nQtv 2,46,25600\nv_min_u32 v121, -2, v121\nQmvaa 204,128,24\n"
"s_waitcnt vmcnt(10)\nds_write_b128 v52, a[44:47] offset:50176\nQmvaa 236,112,28\nQk 122,152,153\nv_min_u32 v122, -2, v122\nQmvaa 80,116,28\nQtv 4,46,20736\nQk 123,154,155\n"
"Qmvaa 112,120,28\nQtv 6,46,25856\nv_min_u32 v123, -2, v123\nQmvaa 144,124,28\nQda 80,53,29696\nv_add_u32 v75, 1, v75\nQmvaa 176,128,28\nds_write_b32 v73, v75\n"
"buffer_store_dwordx4 v[120:123], v60, s[24:27], s68 offen offset:2048 sc1\ns_waitcnt lgkmcnt(7)\nQmvaa 92,132,72\nQtv 8,46,20992\nQk 184,184,185\nQmvaa 124,136,72\nQtv 10,46,26112\nv_min_u32 v184, -2, v184\n"
"Qmvaa 156,140,72\nQtv 12,46,21248\nQk 185,186,187\nQmvaa 188,144,72\nQtv 14,46,26368\nv_min_u32 v185, -2, v185\nQmvaa 220,148,72\ns_waitcnt vmcnt(10)\n"
"ds_write_b128 v52, a[208:211] offset:0\nQmvaa 252,132,76\nQk 186,216,217\nv_min_u32 v186, -2, v186\nQmvaa 100,136,76\nQtv 16,46,21504\nQk 187,218,219\nQmvaa 132,140,76\n"
"ds_read_b32 v69, v74\nv_min_u32 v187, -2, v187\nQmvaa 164,144,76\nQda 48,53,33792\nbuffer_store_dwordx4 v[184:187], v60, s[24:27], s68 offen offset:3072 sc1\nQmvaa 196,148,76\nQtv 18,46,26624\nQk 248,248,249\n"
"s_waitcnt lgkmcnt(10)\nQmvaa 228,132,80\nv_min_u32 v248, -2, v248\nQk 249,250,251\nQmvaa 108,136,80\ns_waitcnt lgkmcnt(1)\nv_cmp_gt_u32 vcc, v75, v69\nQmvaa 140,140,80\n"
"s_cbranch_vccnz .Lso60r0\n.Lro60r0:\nv_min_u32 v249, -2, v249\nQmvaa 172,144,80\nQtv 20,51,20480\nQk 250,116,117\nQmvaa 204,148,80\ns_waitcnt vmcnt(10)\n"
"ds_write_b128 v52, a[212:215] offset:4096\nQmvaa 236,132,48\nv_min_u32 v250, -2, v250\nQk 251,118,119\nQmvaa 80,136,48\nQtv 22,51,25600\nv_min_u32 v251, -2, v251\nQmvaa 112,140,48\n"
"Qtv 24,51,20736\ns_add_u32 s68, s64, 4096\nQmvaa 144,144,48\nQda 52,53,37888\nbuffer_store_dwordx4 v[248:251], v60, s[24:27], s68 offen offset:0 sc1\nQmvaa 176,148,48\nQtv 26,51,25856\nQk 96,96,97\n"
"s_waitcnt lgkmcnt(6)\nQmvva 92,0,32\nv_min_u32 v96, -2, v96\nQk 97,98,99\nQmvva 124,4,32\nQtv 28,51,20992\nv_min_u32 v97, -2, v97\nQmvva 156,8,32\n"
"Qtv 30,51,26112\nQk 98,128,129\nQmvva 188,12,32\nQtv 32,51,21248\nv_min_u32 v98, -2, v98\nQmvva 220,16,32\ns_waitcnt vmcnt(10)\nds_write_b128 v52, a[216:219] offset:8192\n"
"Qmvva 252,0,36\nQk 99,130,131\nv_min_u32 v99, -2, v99\nQmvva 100,4,36\nbuffer_store_dwordx4 v[96:99], v60, s[24:27], s68 offen offset:1024 sc1\nQk 160,160,161\nQmvva 132,8,36\nQtv 34,51,26368\n"
"v_min_u32 v160, -2, v160\nQmvva 164,12,36\nQda 56,53,41984\nQk 161,162,163\nQmvva 196,16,36\nQtv 36,51,21504\nv_min_u32 v161, -2, v161\nQmvva 228,0,40\n"
"Qtv 38,51,26624\nQk 162,192,193\nQmvva 108,4,40\nv_min_u32 v162, -2, v162\nQk 163,194,195\nQmvva 140,8,40\nQta 112,45,0\nv_min_u32 v163, -2, v163\n"
"Qmvva 172,12,40\nQta 114,45,5120\nv_add_u32 v75, 1, v75\nQmvva 204,16,40\nds_write_b32 v73, v75\nbuffer_store_dwordx4 v[160:163], v60, s[24:27], s68 offen offset:2048 sc1\nQmvva 236,0,44\ns_waitcnt vmcnt(11)\n"
"ds_write_b128 v52, a[220:223] offset:12288\nQmvva 80,4,44\nQk 224,224,225\nv_min_u32 v224, -2, v224\nQmvva 112,8,44\nQta 116,45,256\nQk 225,226,227\nQmvva 144,12,44\n"
"Qda 60,53,46080\nv_min_u32 v225, -2, v225\nQmvva 176,16,44\nQta 118,45,5376\ns_add_u32 s68, s64, 14336\ns_waitcnt lgkmcnt(7)\nQmvva 92,20,52\nQta 120,45,512\n"
"buffer_store_dwordx4 v[224:227], v60, s[24:27], s68 offen offset:0 sc1\nQmvva 124,24,52\nQta 122,45,5632\nQmvva 156,28,52\nds_read_b32 v69, v74\nQmvva 188,32,52\nQta 124,45,768\nQmvva 220,36,52\n"
"s_waitcnt vmcnt(11)\nds_write_b128 v52, a[224:227] offset:16384\nQmvva 252,20,56\nQmvva 100,24,56\ns_waitcnt lgkmcnt(2)\nv_cmp_gt_u32 vcc, v75, v69\nQmvva 132,28,56\ns_cbranch_vccnz .Lso61r0\n"
".Lro61r0:\nQmvva 164,32,56\nQda 64,53,50176\nQmvva 196,36,56\nQta 126,45,5888\ns_waitcnt lgkmcnt(1)\nQmvva 228,20,60\nQta 128,45,1024\n"
"Qmvva 108,24,60\nQta 130,45,6144\nQmvva 140,28,60\nQta 132,47,0\nQmvva 172,32,60\nQta 134,47,5120\nQmvva 204,36,60\ns_waitcnt vmcnt(10)\n"
"ds_write_b128 v52, a[228:231] offset:20480\nQmvva 236,20,64\nQmvva 80,24,64\nQta 136,47,256\nQmvva 112,28,64\nQta 138,47,5376\nQmvva 144,32,64\nQda 68,53,0\n"
"Qmvva 176,36,64\ns_waitcnt lgkmcnt(6)\nQmvaa 92,112,208\nQta 140,47,512\nQmvaa 124,116,208\nQta 142,47,5632\nQmvaa 156,120,208\nQta 144,47,768\n"
"Qmvaa 188,124,208\nQmvaa 220,128,208\ns_waitcnt vmcnt(9)\nds_write_b128 v52, a[232:235] offset:25600\nQmvaa 252,112,212\nQmvaa 100,116,212\nQta 146,47,5888\nQmvaa 132,120,212\n"
"Qta 148,47,1024\nQmvaa 164,124,212\nQda 72,53,4096\nv_add_u32 v75, 1, v75\nQmvaa 196,128,212\nds_write_b32 v73, v75\nQmvaa 228,112,216\nQta 150,47,6144\n"
"Qmvaa 108,116,216\nQmvaa 140,120,216\nQtv 0,45,20480\nQmvaa 172,124,216\nQtv 2,45,25600\nQmvaa 204,128,216\ns_waitcnt vmcnt(8)\nds_write_b128 v52, a[236:239] offset:29696\n"
"Qmvaa 236,112,220\nQmvaa 80,116,220\nQtv 4,45,20736\nQmvaa 112,120,220\nds_read_b32 v69, v74\nQmvaa 144,124,220\nQda 76,53,8192\nQmvaa 176,128,220\n"
"Qtv 6,45,25856\ns_waitcnt lgkmcnt(15)\nQmvaa 92,132,68\nQmvaa 124,136,68\ns_waitcnt lgkmcnt(2)\nv_cmp_gt_u32 vcc, v75, v69\nQmvaa 156,140,68\ns_cbranch_vccnz .Lso62r0\n"
".Lro62r0:\nQmvaa 188,144,68\nQtv 8,45,20992\nQmvaa 220,148,68\nQtv 10,45,26112\nQmvaa 252,132,72\nQtv 12,45,21248\nQmvaa 100,136,72\n"
"Qmvaa 132,140,72\nQtv 14,45,26368\nQmvaa 164,144,72\nQda 80,53,12288\nQmvaa 196,148,72\nQtv 16,45,21504\ns_waitcnt lgkmcnt(1)\nQmvaa 228,132,76\n"
"Qtv 18,45,26624\nQmvaa 108,136,76\nQmvaa 140,140,76\nQtv 20,47,20480\nQmvaa 172,144,76\nQtv 22,47,25600\nQmvaa 204,148,76\nQmvaa 236,132,80\n"
"Qtv 24,47,20736\nQmvaa 80,136,80\nQmvaa 112,140,80\nQtv 26,47,25856\nQmvaa 144,144,80\nQda 48,53,16384\nQmvaa 176,148,80\nQtv 28,47,20992\n"
"s_waitcnt lgkmcnt(6)\nQmvva 92,0,224\nQmvva 124,4,224\nQtv 30,47,26112\nQmvva 156,8,224\nQmvva 188,12,224\nQtv 32,47,21248\nv_add_u32 v75, 1, v75\n"
"Qmvva 220,16,224\nds_write_b32 v73, v75\nQmvva 252,0,228\nQtv 34,47,26368\nQmvva 100,4,228\nQmvva 132,8,228\nQtv 36,47,21504\nQmvva 164,12,228\n"
"Qda 52,53,20480\nQmvva 196,16,228\nQmvva 228,0,232\nQtv 38,47,26624\nQmvva 108,4,232\nQmvva 140,8,232\nds_read_b32 v69, v74\nQmvva 172,12,232\n"
"Qmvva 204,16,232\nQmvva 236,0,236\nQmvva 80,4,236\ns_waitcnt lgkmcnt(0)\nv_cmp_gt_u32 vcc, v75, v69\nQmvva 112,8,236\ns_cbranch_vccnz .Lso63r0\n.Lro63r0:\n"
"Qmvva 144,12,236\nQda 56,53,25600\nQmvva 176,16,236\ns_waitcnt lgkmcnt(2)\nQmvva 92,20,48\ns_add_u32 s69, s98, 0\nbuffer_load_dwordx4 v[240:243], v60, s[24:27], s69 offen offset:0 sc1\nQmvva 124,24,48\n"
"Qmvva 156,28,48\nbuffer_load_dwordx4 v[88:91], v60, s[24:27], s69 offen offset:1024 sc1\nQmvva 188,32,48\nQmvva 220,36,48\nbuffer_load_dwordx4 v[152:155], v60, s[24:27], s69 offen offset:2048 sc1\nQmvva 252,20,52\nQmvva 100,24,52\nbuffer_load_dwordx4 v[216:219], v60, s[24:27], s69 offen offset:3072 sc1\n"
"Qmvva 132,28,52\nQmvva 164,32,52\nQda 60,53,29696\nQmvva 196,36,52\ns_add_u32 s69, s98, 4096\nbuffer_load_dwordx4 v[116:119], v60, s[24:27], s69 offen offset:0 sc1\ns_waitcnt lgkmcnt(0)\nQmvva 228,20,56\n"
"Qmvva 108,24,56\nbuffer_load_dwordx4 v[128:131], v60, s[24:27], s69 offen offset:1024 sc1\nQmvva 140,28,56\nQmvva 172,32,56\nbuffer_load_dwordx4 v[192:195], v60, s[24:27], s69 offen offset:2048 sc1\nQmvva 204,36,56\nQmvva 236,20,60\ns_add_u32 s69, s98, 14336\n"
"buffer_load_dwordx4 v[148:151], v60, s[24:27], s69 offen offset:0 sc1\nQmvva 80,24,60\nQmvva 112,28,60\nbuffer_load_dwordx4 v[180:183], v60, s[24:27], s69 offen offset:1024 sc1\nQmvva 144,32,60\nQmvva 176,36,60\nbuffer_load_dwordx4 v[104:107], v60, s[24:27], s69 offen offset:2048 sc1\ns_barrier\n"
"s_mov_b32 s97, 0\n.Lrx0:\ns_waitcnt vmcnt(0)\nv_max_u32 v40, v240, v241\nv_max3_u32 v40, v40, v242, v243\nv_max3_u32 v40, v40, v88, v89\nv_max3_u32 v40, v40, v90, v91\nv_max3_u32 v40, v40, v152, v153\n"
"v_max3_u32 v40, v40, v154, v155\nv_max3_u32 v40, v40, v216, v217\nv_max3_u32 v40, v40, v218, v219\nv_max3_u32 v40, v40, v116, v117\nv_max3_u32 v40, v40, v118, v119\nv_max3_u32 v40, v40, v128, v129\nv_max3_u32 v40, v40, v130, v131\nv_max3_u32 v40, v40, v192, v193\n"
"v_max3_u32 v40, v40, v194, v195\nv_max3_u32 v40, v40, v148, v149\nv_max3_u32 v40, v40, v150, v151\nv_max3_u32 v40, v40, v180, v181\nv_max3_u32 v40, v40, v182, v183\nv_max3_u32 v40, v40, v104, v105\nv_max3_u32 v40, v40, v106, v107\nv_cmp_eq_u32 vcc, -1, v40\n"
"s_cmp_eq_u64 vcc, 0\ns_cbranch_scc1 .Lrx0_ok\ns_add_u32 s97, s97, 1\ns_cmp_gt_u32 s97, 1048576\ns_cbranch_scc1 .Lrx0_ok\ns_add_u32 s69, s98, 0\nbuffer_load_dwordx4 v[240:243], v60, s[24:27], s69 offen offset:0 sc1\nbuffer_load_dwordx4 v[88:91], v60, s[24:27], s69 offen offset:1024 sc1\n"
"buffer_load_dwordx4 v[152:155], v60, s[24:27], s69 offen offset:2048 sc1\nbuffer_load_dwordx4 v[216:219], v60, s[24:27], s69 offen offset:3072 sc1\ns_add_u32 s69, s98, 4096\nbuffer_load_dwordx4 v[116:119], v60, s[24:27], s69 offen offset:0 sc1\nbuffer_load_dwordx4 v[128:131], v60, s[24:27], s69 offen offset:1024 sc1\nbuffer_load_dwordx4 v[192:195], v60, s[24:27], s69 offen offset:2048 sc1\ns_add_u32 s69, s98, 14336\nbuffer_load_dwordx4 v[148:151], v60, s[24:27], s69 offen offset:0 sc1\n"
"buffer_load_dwordx4 v[180:183], v60, s[24:27], s69 offen offset:1024 sc1\nbuffer_load_dwordx4 v[104:107], v60, s[24:27], s69 offen offset:2048 sc1\ns_branch .Lrx0\n.Lrx0_ok:\nv_lshlrev_b32 v41, 16, v240\nv_and_b32 v42, 0xffff0000, v240\nQa 92,92,41\nQa 93,93,42\n"
"v_lshlrev_b32 v43, 16, v241\nv_and_b32 v44, 0xffff0000, v241\nQa 94,94,43\nQa 95,95,44\nQk 92,92,93\nQk 93,94,95\nds_write_b64 v61, v[92:93] offset:0\nv_lshlrev_b32 v41, 16, v242\n"
"v_and_b32 v42, 0xffff0000, v242\nQa 124,124,41\nQa 125,125,42\nv_lshlrev_b32 v43, 16, v243\nv_and_b32 v44, 0xffff0000, v243\nQa 126,126,43\nQa 127,127,44\nQk 124,124,125\n"
"Qk 125,126,127\nds_write_b64 v61, v[124:125] offset:32\nv_lshlrev_b32 v41, 16, v88\nv_and_b32 v42, 0xffff0000, v88\nQa 156,156,41\nQa 157,157,42\nv_lshlrev_b32 v43, 16, v89\nv_and_b32 v44, 0xffff0000, v89\n"
"Qa 158,158,43\nQa 159,159,44\nQk 156,156,157\nQk 157,158,159\nds_write_b64 v61, v[156:157] offset:64\nv_lshlrev_b32 v41, 16, v90\nv_and_b32 v42, 0xffff0000, v90\nQa 188,188,41\n"
"Qa 189,189,42\nv_lshlrev_b32 v43, 16, v91\nv_and_b32 v44, 0xffff0000, v91\nQa 190,190,43\nQa 191,191,44\nQk 188,188,189\nQk 189,190,191\nds_write_b64 v61, v[188:189] offset:96\n"
"v_lshlrev_b32 v41, 16, v152\nv_and_b32 v42, 0xffff0000, v152\nQa 220,220,41\nQa 221,221,42\nv_lshlrev_b32 v43, 16, v153\nv_and_b32 v44, 0xffff0000, v153\nQa 222,222,43\nQa 223,223,44\n"
"Qk 220,220,221\nQk 221,222,223\nds_write_b64 v61, v[220:221] offset:128\nv_lshlrev_b32 v41, 16, v154\nv_and_b32 v42, 0xffff0000, v154\nQa 252,252,41\nQa 253,253,42\nv_lshlrev_b32 v43, 16, v155\n"
"v_and_b32 v44, 0xffff0000, v155\nQa 254,254,43\nQa 255,255,44\nQk 252,252,253\nQk 253,254,255\nds_write_b64 v61, v[252:253] offset:5376\nv_lshlrev_b32 v41, 16, v216\nv_and_b32 v42, 0xffff0000, v216\n"
"Qa 100,100,41\nQa 101,101,42\nv_lshlrev_b32 v43, 16, v217\nv_and_b32 v44, 0xffff0000, v217\nQa 102,102,43\nQa 103,103,44\nQk 100,100,101\nQk 101,102,103\n"
"ds_write_b64 v61, v[100:101] offset:5408\nv_lshlrev_b32 v41, 16, v218\nv_and_b32 v42, 0xffff0000, v218\nQa 132,132,41\nQa 133,133,42\nv_lshlrev_b32 v43, 16, v219\nv_and_b32 v44, 0xffff0000, v219\nQa 134,134,43\n"
"Qa 135,135,44\nQk 132,132,133\nQk 133,134,135\nds_write_b64 v61, v[132:133] offset:5440\nv_lshlrev_b32 v41, 16, v116\nv_and_b32 v42, 0xffff0000, v116\nQa 164,164,41\nQa 165,165,42\n"
"v_lshlrev_b32 v43, 16, v117\nv_and_b32 v44, 0xffff0000, v117\nQa 166,166,43\nQa 167,167,44\nQk 164,164,165\nQk 165,166,167\nds_write_b64 v61, v[164:165] offset:5472\nv_lshlrev_b32 v41, 16, v118\n"
"v_and_b32 v42, 0xffff0000, v118\nQa 196,196,41\nQa 197,197,42\nv_lshlrev_b32 v43, 16, v119\nv_and_b32 v44, 0xffff0000, v119\nQa 198,198,43\nQa 199,199,44\nQk 196,196,197\n"
"Qk 197,198,199\nds_write_b64 v61, v[196:197] offset:5504\nv_lshlrev_b32 v41, 16, v128\nv_and_b32 v42, 0xffff0000, v128\nQa 228,228,41\nQa 229,229,42\nv_lshlrev_b32 v43, 16, v129\nv_and_b32 v44, 0xffff0000, v129\n"
"Qa 230,230,43\nQa 231,231,44\nQk 228,228,229\nQk 229,230,231\nds_write_b64 v61, v[228:229] offset:10752\nv_lshlrev_b32 v41, 16, v130\nv_and_b32 v42, 0xffff0000, v130\nQa 108,108,41\n"
"Qa 109,109,42\nv_lshlrev_b32 v43, 16, v131\nv_and_b32 v44, 0xffff0000, v131\nQa 110,110,43\nQa 111,111,44\nQk 108,108,109\nQk 109,110,111\nds_write_b64 v61, v[108:109] offset:10784\n"
"v_lshlrev_b32 v41, 16, v192\nv_and_b32 v42, 0xffff0000, v192\nQa 140,140,41\nQa 141,141,42\nv_lshlrev_b32 v43, 16, v193\nv_and_b32 v44, 0xffff0000, v193\nQa 142,142,43\nQa 143,143,44\n"
"Qk 140,140,141\nQk 141,142,143\nds_write_b64 v61, v[140:141] offset:10816\nv_lshlrev_b32 v41, 16, v194\nv_and_b32 v42, 0xffff0000, v194\nQa 172,172,41\nQa 173,173,42\nv_lshlrev_b32 v43, 16, v195\n"
"v_and_b32 v44, 0xffff0000, v195\nQa 174,174,43\nQa 175,175,44\nQk 172,172,173\nQk 173,174,175\nds_write_b64 v61, v[172:173] offset:10848\nv_lshlrev_b32 v41, 16, v148\nv_and_b32 v42, 0xffff0000, v148\n"
"Qa 204,204,41\nQa 205,205,42\nv_lshlrev_b32 v43, 16, v149\nv_and_b32 v44, 0xffff0000, v149\nQa 206,206,43\nQa 207,207,44\nQk 204,204,205\nQk 205,206,207\n"
"ds_write_b64 v61, v[204:205] offset:10880\nv_lshlrev_b32 v41, 16, v150\nv_and_b32 v42, 0xffff0000, v150\nQa 236,236,41\nQa 237,237,42\nv_lshlrev_b32 v43, 16, v151\nv_and_b32 v44, 0xffff0000, v151\nQa 238,238,43\n"
"Qa 239,239,44\nQk 236,236,237\nQk 237,238,239\nds_write_b64 v61, v[236:237] offset:16128\nv_lshlrev_b32 v41, 16, v180\nv_and_b32 v42, 0xffff0000, v180\nQa 80,80,41\nQa 81,81,42\n"
"v_lshlrev_b32 v43, 16, v181\nv_and_b32 v44, 0xffff0000, v181\nQa 82,82,43\nQa 83,83,44\nQk 80,80,81\nQk 81,82,83\nds_write_b64 v61, v[80:81] offset:16160\nv_lshlrev_b32 v41, 16, v182\n"
"v_and_b32 v42, 0xffff0000, v182\nQa 112,112,41\nQa 113,113,42\nv_lshlrev_b32 v43, 16, v183\nv_and_b32 v44, 0xffff0000, v183\nQa 114,114,43\nQa 115,115,44\nQk 112,112,113\n"
"Qk 113,114,115\nds_write_b64 v61, v[112:113] offset:16192\nv_lshlrev_b32 v41, 16, v104\nv_and_b32 v42, 0xffff0000, v104\nQa 144,144,41\nQa 145,145,42\nv_lshlrev_b32 v43, 16, v105\nv_and_b32 v44, 0xffff0000, v105\n"
"Qa 146,146,43\nQa 147,147,44\nQk 144,144,145\nQk 145,146,147\nds_write_b64 v61, v[144:145] offset:16224\nv_lshlrev_b32 v41, 16, v106\nv_and_b32 v42, 0xffff0000, v106\nQa 176,176,41\n"
"Qa 177,177,42\nv_lshlrev_b32 v43, 16, v107\nv_and_b32 v44, 0xffff0000, v107\nQa 178,178,43\nQa 179,179,44\nQk 176,176,177\nQk 177,178,179\nds_write_b64 v61, v[176:177] offset:16256\n"
"s_branch .Lfin\n.Lrole1:\ns_waitcnt lgkmcnt(2)\nQmvaa 92,132,56\ns_waitcnt vmcnt(26)\nds_write_b128 v52, a[172:175] offset:0\nQmvaa 124,136,56\nQmvaa 156,140,56\n"
"Qtv 0,45,0\nQmvaa 188,144,56\nQda 72,53,33792\nQmvaa 220,148,56\nQtv 2,45,5120\nQmvaa 252,132,60\nQtv 4,45,256\ns_mov_b32 s94, 835584\n"
"Qmvaa 100,136,60\nbuffer_load_dwordx4 a[220:223], v60, s[48:51], s94 offen offset:0 sc1\nQmvaa 132,140,60\nds_read_b32 v69, v74\nQmvaa 164,144,60\nQtv 6,45,5376\nQmvaa 196,148,60\ns_waitcnt vmcnt(26)\n"
"ds_write_b128 v52, a[0:3] offset:4096\ns_waitcnt lgkmcnt(15)\nQmvaa 228,132,64\nQmvaa 108,136,64\ns_waitcnt lgkmcnt(2)\nv_cmp_gt_u32 vcc, v75, v69\nQmvaa 140,140,64\ns_cbranch_vccnz .Lso49r1\n"
".Lro49r1:\nQmvaa 172,144,64\nQda 76,53,37888\ns_add_u32 m0, s96, 86016\ns_mov_b32 s95, 1761280\nQmvaa 204,148,64\nbuffer_load_dwordx4 v40, s[52:55], s95 offen lds\nQmvaa 236,132,68\n"
"Qtv 8,45,512\nQmvaa 80,136,68\nbuffer_load_dwordx4 a[224:227], v60, s[48:51], s94 offen offset:1024 sc1\nQmvaa 112,140,68\nQtv 10,45,5632\nQmvaa 144,144,68\nQtv 12,45,768\nQmvaa 176,148,68\n"
"s_waitcnt vmcnt(27)\nds_write_b128 v52, a[4:7] offset:8192\ns_waitcnt lgkmcnt(4)\nQmvaa 208,132,72\nQmvaa 240,136,72\nbuffer_load_dwordx4 v41, s[52:55], s95 offen offset:1024 lds\nQmvaa 84,140,72\nQtv 14,45,5888\n"
"Qmvaa 88,144,72\nQda 80,53,41984\nQmvaa 120,148,72\nQtv 16,45,1024\nQmvaa 152,132,76\nQtv 18,45,6144\nQmvaa 184,136,76\nbuffer_load_dwordx4 a[228:231], v60, s[48:51], s94 offen offset:2048 sc1\n"
"Qmvaa 216,140,76\nQmvaa 248,144,76\nbuffer_load_dwordx4 v42, s[52:55], s95 offen offset:2048 lds\nQmvaa 116,148,76\ns_waitcnt vmcnt(28)\nds_write_b128 v52, a[8:11] offset:12288\ns_waitcnt lgkmcnt(3)\nQmvaa 96,132,80\n"
"Qmvaa 128,136,80\nQmvaa 160,140,80\nQmvaa 192,144,80\nQda 48,53,46080\nv_add_u32 v75, 1, v75\nQmvaa 224,148,80\ns_waitcnt vmcnt(34)\nds_write_b32 v73, v75\n"
"s_waitcnt lgkmcnt(3)\nQmvva 92,0,252\nbuffer_load_dwordx4 v43, s[52:55], s95 offen offset:3072 lds\ns_mov_b32 s94, 501760\nQmvva 124,4,252\nbuffer_load_dwordx4 a[232:235], v60, s[48:51], s94 offen offset:0 sc1\nQmvva 156,8,252\nQtv 20,47,0\n"
"Qmvva 188,12,252\nQtv 22,47,5120\nQmvva 220,16,252\ns_waitcnt vmcnt(28)\nds_write_b128 v52, a[12:15] offset:16384\nQmvva 252,0,168\nQmvva 100,4,168\nQtv 24,47,256\n"
"Qmvva 132,8,168\nds_read_b32 v69, v74\nQmvva 164,12,168\nQda 52,53,50176\ns_add_u32 m0, s96, 90112\nQmvva 196,16,168\nbuffer_load_dwordx4 v44, s[52:55], s95 offen lds\nQmvva 228,0,172\n"
"Qtv 26,47,5376\nQmvva 108,4,172\ns_waitcnt lgkmcnt(2)\nv_cmp_gt_u32 vcc, v75, v69\nQmvva 140,8,172\ns_cbranch_vccnz .Lso50r1\n.Lro50r1:\nQmvva 172,12,172\n"
"buffer_load_dwordx4 a[236:239], v60, s[48:51], s94 offen offset:1024 sc1\nQmvva 204,16,172\ns_waitcnt vmcnt(27)\nds_write_b128 v52, a[16:19] offset:20480\nQmvva 236,0,0\nQmvva 80,4,0\nQtv 28,47,512\nQmvva 112,8,0\n"
"Qmvva 144,12,0\nQda 56,53,0\nQmvva 176,16,0\nQtv 30,47,5632\nQmvva 208,0,4\nQtv 32,47,768\nQmvva 240,4,4\nbuffer_load_dwordx4 a[240:243], v60, s[48:51], s94 offen offset:2048 sc1\n"
"Qmvva 84,8,4\nQtv 34,47,5888\nQmvva 88,12,4\nQtv 36,47,1024\nQmvva 120,16,4\ns_waitcnt vmcnt(25)\nds_write_b128 v52, a[20:23] offset:25600\nQmvva 152,0,8\n"
"Qmvva 184,4,8\nQtv 38,47,6144\nQmvva 216,8,8\nQmvva 248,12,8\nQda 60,53,4096\nQmvva 116,16,8\nQmvva 96,0,12\nQmvva 128,4,12\n"
"buffer_load_dwordx4 a[244:247], v60, s[48:51], s94 offen offset:3072 sc1\nQmvva 160,8,12\nQmvva 192,12,12\nv_add_u32 v75, 1, v75\nQmvva 224,16,12\nds_write_b32 v73, v75\ns_waitcnt lgkmcnt(2)\nQmvva 92,20,48\n"
"s_waitcnt vmcnt(25)\nds_write_b128 v52, a[24:27] offset:29696\nQmvva 124,24,48\nQmvva 156,28,48\nQta 112,45,20480\nQmvva 188,32,48\nQda 64,53,8192\nQmvva 220,36,48\n"
"Qta 114,45,25600\nQmvva 252,20,52\nQta 116,45,20736\ns_mov_b32 s94, 616448\nQmvva 100,24,52\nbuffer_load_dwordx4 a[248:251], v60, s[48:51], s94 offen offset:0 sc1\nQmvva 132,28,52\nds_read_b32 v69, v74\n"
"Qmvva 164,32,52\nQta 118,45,25856\nQmvva 196,36,52\ns_waitcnt vmcnt(25)\nds_write_b128 v52, a[28:31] offset:33792\ns_waitcnt lgkmcnt(15)\nQmvva 228,20,56\nQmvva 108,24,56\n"
"s_waitcnt lgkmcnt(2)\nv_cmp_gt_u32 vcc, v75, v69\nQmvva 140,28,56\ns_cbranch_vccnz .Lso51r1\n.Lro51r1:\nQmvva 172,32,56\nQda 68,53,12288\ns_add_u32 m0, s96, 4096\n"
"s_mov_b32 s95, 2088960\nQmvva 204,36,56\nbuffer_load_dwordx4 v40, s[52:55], s95 offen lds\nQmvva 236,20,60\nQta 120,45,20992\nQmvva 80,24,60\nbuffer_load_dwordx4 a[252:255], v60, s[48:51], s94 offen offset:1024 sc1\nQmvva 112,28,60\n"
"Qta 122,45,26112\nQmvva 144,32,60\nQta 124,45,21248\nQmvva 176,36,60\ns_waitcnt vmcnt(26)\nds_write_b128 v52, a[32:35] offset:37888\ns_waitcnt lgkmcnt(4)\nQmvva 208,20,64\n"
"Qmvva 240,24,64\nbuffer_load_dwordx4 v41, s[52:55], s95 offen offset:1024 lds\nQmvva 84,28,64\nQta 126,45,26368\nQmvva 88,32,64\nQda 72,53,16384\nQmvva 120,36,64\nQta 128,45,21504\n"
"Qmvva 152,20,68\nQta 130,45,26624\nQmvva 184,24,68\nbuffer_load_dwordx4 a[168:171], v60, s[48:51], s94 offen offset:2048 sc1\nQmvva 216,28,68\nQmvva 248,32,68\nbuffer_load_dwordx4 v42, s[52:55], s95 offen offset:2048 lds\nQmvva 116,36,68\n"
"s_waitcnt vmcnt(27)\nds_write_b128 v52, a[36:39] offset:41984\ns_waitcnt lgkmcnt(3)\nQmvva 96,20,72\nQmvva 128,24,72\nQmvva 160,28,72\nQmvva 192,32,72\nQda 76,53,20480\n"
"v_add_u32 v75, 1, v75\nQmvva 224,36,72\ns_waitcnt vmcnt(33)\nds_write_b32 v73, v75\ns_waitcnt lgkmcnt(3)\nQmvaa 92,112,16\nbuffer_load_dwordx4 v43, s[52:55], s95 offen offset:3072 lds\nQmvaa 124,116,16\n"
"buffer_load_dwordx4 a[172:175], v60, s[48:51], s94 offen offset:3072 sc1\nQmvaa 156,120,16\nQta 132,47,20480\nQmvaa 188,124,16\nQta 134,47,25600\nQmvaa 220,128,16\ns_waitcnt vmcnt(27)\nds_write_b128 v52, a[40:43] offset:46080\n"
"Qmvaa 252,112,20\nQmvaa 100,116,20\nQta 136,47,20736\nQmvaa 132,120,20\nds_read_b32 v69, v74\nQmvaa 164,124,20\nQda 80,53,25600\ns_add_u32 m0, s96, 8192\n"
"Qmvaa 196,128,20\nbuffer_load_dwordx4 v44, s[52:55], s95 offen lds\nQmvaa 228,112,24\nQta 138,47,25856\nQmvaa 108,116,24\ns_waitcnt lgkmcnt(2)\nv_cmp_gt_u32 vcc, v75, v69\nQmvaa 140,120,24\n"
"s_mov_b32 s94, 731136\ns_cbranch_vccnz .Lso52r1\n.Lro52r1:\nQmvaa 172,124,24\nbuffer_load_dwordx4 a[0:3], v60, s[48:51], s94 offen offset:0 sc1\nQmvaa 204,128,24\ns_waitcnt vmcnt(27)\nds_write_b128 v52, a[44:47] offset:50176\n"
"Qmvaa 236,112,28\nQmvaa 80,116,28\nQta 140,47,20992\nQmvaa 112,120,28\nQmvaa 144,124,28\nQda 48,53,29696\nQmvaa 176,128,28\nQta 142,47,26112\n"
"Qmvaa 208,112,32\nQta 144,47,21248\nQmvaa 240,116,32\nbuffer_load_dwordx4 a[4:7], v60, s[48:51], s94 offen offset:1024 sc1\nQmvaa 84,120,32\nQta 146,47,26368\nQmvaa 88,124,32\nQta 148,47,21504\n"
"Qmvaa 120,128,32\ns_waitcnt vmcnt(25)\nds_write_b128 v52, a[208:211] offset:0\nQmvaa 152,112,36\nQmvaa 184,116,36\nQta 150,47,26624\nQmvaa 216,120,36\nQmvaa 248,124,36\n"
"Qda 52,53,33792\nQmvaa 116,128,36\nQmvaa 96,112,40\nQmvaa 128,116,40\nbuffer_load_dwordx4 a[8:11], v60, s[48:51], s94 offen offset:2048 sc1\nQmvaa 160,120,40\nQmvaa 192,124,40\nv_add_u32 v75, 1, v75\n"
"Qmvaa 224,128,40\nds_write_b32 v73, v75\ns_waitcnt lgkmcnt(2)\nQmvaa 92,132,76\ns_waitcnt vmcnt(25)\nds_write_b128 v52, a[212:215] offset:4096\nQmvaa 124,136,76\nQmvaa 156,140,76\n"
"Qtv 0,45,40960\nQmvaa 188,144,76\nQda 56,53,37888\nQmvaa 220,148,76\nQtv 2,45,46080\nQmvaa 252,132,80\nQtv 4,45,41216\nQmvaa 100,136,80\n"
"buffer_load_dwordx4 a[12:15], v60, s[48:51], s94 offen offset:3072 sc1\nQmvaa 132,140,80\nds_read_b32 v69, v74\nQmvaa 164,144,80\nQtv 6,45,46336\nQmvaa 196,148,80\ns_waitcnt vmcnt(25)\nds_write_b128 v52, a[216:219] offset:8192\n"
"s_waitcnt lgkmcnt(15)\nQmvaa 228,132,48\nQmvaa 108,136,48\ns_waitcnt lgkmcnt(2)\nv_cmp_gt_u32 vcc, v75, v69\nQmvaa 140,140,48\ns_cbranch_vccnz .Lso53r1\n.Lro53r1:\n"
"Qmvaa 172,144,48\nQda 60,53,41984\ns_add_u32 m0, s96, 24576\ns_mov_b32 s95, 2416640\nQmvaa 204,148,48\nbuffer_load_dwordx4 v40, s[52:55], s95 offen lds\nQmvaa 236,132,52\nQtv 8,45,41472\n"
"s_mov_b32 s94, 845824\nQmvaa 80,136,52\nbuffer_load_dwordx4 a[16:19], v60, s[48:51], s94 offen offset:0 sc1\nQmvaa 112,140,52\nQtv 10,45,46592\nQmvaa 144,144,52\nQtv 12,45,41728\nQmvaa 176,148,52\n"
"s_waitcnt vmcnt(26)\nds_write_b128 v52, a[220:223] offset:12288\ns_waitcnt lgkmcnt(4)\nQmvaa 208,132,56\nQmvaa 240,136,56\nbuffer_load_dwordx4 v41, s[52:55], s95 offen offset:1024 lds\nQmvaa 84,140,56\nQtv 14,45,46848\n"
"Qmvaa 88,144,56\nQda 64,53,46080\nQmvaa 120,148,56\nQtv 16,45,41984\nQmvaa 152,132,60\nQtv 18,45,47104\nQmvaa 184,136,60\nbuffer_load_dwordx4 a[20:23], v60, s[48:51], s94 offen offset:1024 sc1\n"
"Qmvaa 216,140,60\nQmvaa 248,144,60\nbuffer_load_dwordx4 v42, s[52:55], s95 offen offset:2048 lds\nQmvaa 116,148,60\ns_waitcnt vmcnt(27)\nds_write_b128 v52, a[224:227] offset:16384\ns_waitcnt lgkmcnt(3)\nQmvaa 96,132,64\n"
"Qmvaa 128,136,64\nQmvaa 160,140,64\nQmvaa 192,144,64\nQda 68,53,50176\nv_add_u32 v75, 1, v75\nQmvaa 224,148,64\ns_waitcnt vmcnt(33)\nds_write_b32 v73, v75\n"
"s_waitcnt lgkmcnt(3)\nQmvva 92,0,44\nbuffer_load_dwordx4 v43, s[52:55], s95 offen offset:3072 lds\nQmvva 124,4,44\nbuffer_load_dwordx4 a[24:27], v60, s[48:51], s94 offen offset:2048 sc1\nQmvva 156,8,44\nQtv 20,47,40960\nQmvva 188,12,44\n"
"Qtv 22,47,46080\nQmvva 220,16,44\ns_waitcnt vmcnt(27)\nds_write_b128 v52, a[228:231] offset:20480\nQmvva 252,0,208\nQmvva 100,4,208\nQtv 24,47,41216\nQmvva 132,8,208\n"
"ds_read_b32 v69, v74\nQmvva 164,12,208\nQda 72,53,0\ns_add_u32 m0, s96, 28672\nQmvva 196,16,208\nbuffer_load_dwordx4 v44, s[52:55], s95 offen lds\nQmvva 228,0,212\nQtv 26,47,46336\n"
"Qmvva 108,4,212\ns_waitcnt lgkmcnt(2)\nv_cmp_gt_u32 vcc, v75, v69\nQmvva 140,8,212\ns_cbranch_vccnz .Lso54r1\n.Lro54r1:\nQmvva 172,12,212\nbuffer_load_dwordx4 a[28:31], v60, s[48:51], s94 offen offset:3072 sc1\n"
"Qmvva 204,16,212\ns_waitcnt vmcnt(26)\nds_write_b128 v52, a[232:235] offset:25600\nQmvva 236,0,216\nQmvva 80,4,216\nQtv 28,47,41472\nQmvva 112,8,216\nQmvva 144,12,216\n"
"Qda 76,53,4096\nQmvva 176,16,216\nQtv 30,47,46592\nQmvva 208,0,220\nQtv 32,47,41728\ns_mov_b32 s94, 505856\nQmvva 240,4,220\nbuffer_load_dwordx4 a[32:35], v60, s[48:51], s94 offen offset:0 sc1\n"
"Qmvva 84,8,220\nQtv 34,47,46848\nQmvva 88,12,220\nQtv 36,47,41984\nQmvva 120,16,220\ns_waitcnt vmcnt(25)\nds_write_b128 v52, a[236:239] offset:29696\nQmvva 152,0,224\n"
"Qmvva 184,4,224\nQtv 38,47,47104\nQmvva 216,8,224\nQmvva 248,12,224\nQda 80,53,8192\nQmvva 116,16,224\nQmvva 96,0,228\nQmvva 128,4,228\n"
"buffer_load_dwordx4 a[36:39], v60, s[48:51], s94 offen offset:1024 sc1\nQmvva 160,8,228\nQmvva 192,12,228\nv_add_u32 v75, 1, v75\nQmvva 224,16,228\ns_waitcnt vmcnt(27)\nds_write_b32 v73, v75\ns_waitcnt lgkmcnt(2)\n"
"Qmvva 92,20,68\ns_waitcnt vmcnt(25)\nds_write_b128 v52, a[240:243] offset:33792\nQmvva 124,24,68\nQmvva 156,28,68\nQta 112,46,0\nQmvva 188,32,68\nQda 48,53,12288\n"
"Qmvva 220,36,68\nQta 114,46,5120\nQmvva 252,20,72\nQta 116,46,256\nQmvva 100,24,72\nbuffer_load_dwordx4 a[40:43], v60, s[48:51], s94 offen offset:2048 sc1\nQmvva 132,28,72\nds_read_b32 v69, v74\n"
"Qmvva 164,32,72\nQmvva 196,36,72\ns_waitcnt vmcnt(25)\nds_write_b128 v52, a[244:247] offset:37888\ns_waitcnt lgkmcnt(15)\nQmvva 228,20,76\nQmvva 108,24,76\ns_waitcnt lgkmcnt(1)\n"
"v_cmp_gt_u32 vcc, v75, v69\nQmvva 140,28,76\ns_cbranch_vccnz .Lso55r1\n.Lro55r1:\nQmvva 172,32,76\nQda 52,53,16384\nQmvva 204,36,76\nQta 118,46,5376\n"
"Qmvva 236,20,80\nQta 120,46,512\ns_mov_b32 s94, 620544\nQmvva 80,24,80\nbuffer_load_dwordx4 a[44:47], v60, s[48:51], s94 offen offset:0 sc1\nQmvva 112,28,80\nQmvva 144,32,80\nQta 122,46,5632\n"
"Qmvva 176,36,80\ns_waitcnt vmcnt(25)\nds_write_b128 v52, a[248:251] offset:41984\ns_waitcnt lgkmcnt(4)\nQmvva 208,20,48\nQmvva 240,24,48\nQta 124,46,768\nQmvva 84,28,48\n"
"Qta 126,46,5888\nQmvva 88,32,48\nQda 56,53,20480\nQmvva 120,36,48\nQmvva 152,20,52\nQta 128,46,1024\nQmvva 184,24,52\nbuffer_load_dwordx4 a[208:211], v60, s[48:51], s94 offen offset:1024 sc1\n"
"Qmvva 216,28,52\nQta 130,46,6144\nQmvva 248,32,52\nQta 132,51,0\nQta 134,51,5120\nQmvva 116,36,52\ns_waitcnt vmcnt(24)\nds_write_b128 v52, a[252:255] offset:46080\n"
"s_waitcnt lgkmcnt(5)\nQmvva 96,20,56\nQmvva 128,24,56\nQta 136,51,256\nQmvva 160,28,56\nQta 138,51,5376\nQmvva 192,32,56\nQda 60,53,25600\n"
"v_add_u32 v75, 1, v75\nQmvva 224,36,56\nds_write_b32 v73, v75\ns_waitcnt lgkmcnt(7)\nQmvaa 92,112,232\nQta 140,51,512\nQmvaa 124,116,232\nbuffer_load_dwordx4 a[212:215], v60, s[48:51], s94 offen offset:2048 sc1\n"
"Qmvaa 156,120,232\nQta 142,51,5632\nQmvaa 188,124,232\nQta 144,51,768\nQmvaa 220,128,232\ns_waitcnt vmcnt(23)\nds_write_b128 v52, a[168:171] offset:50176\nQmvaa 252,112,236\n"
"Qmvaa 100,116,236\nQta 146,51,5888\nQmvaa 132,120,236\nds_read_b32 v69, v74\nQmvaa 164,124,236\nQda 64,53,29696\nQmvaa 196,128,236\nQta 148,51,1024\n"
"Qmvaa 228,112,240\nQta 150,51,6144\nQmvaa 108,116,240\ns_waitcnt lgkmcnt(3)\nv_cmp_gt_u32 vcc, v75, v69\nQmvaa 140,120,240\ns_mov_b32 s94, 735232\ns_cbranch_vccnz .Lso56r1\n"
".Lro56r1:\nQmvaa 172,124,240\nbuffer_load_dwordx4 a[216:219], v60, s[48:51], s94 offen offset:0 sc1\nQmvaa 204,128,240\ns_waitcnt vmcnt(21)\nds_write_b128 v52, a[172:175] offset:0\nQmvaa 236,112,244\nQmvaa 80,116,244\n"
"Qtv 0,46,20480\nQmvaa 112,120,244\nQtv 2,46,25600\nQmvaa 144,124,244\nQda 68,53,33792\nQmvaa 176,128,244\nQtv 4,46,20736\ns_waitcnt lgkmcnt(5)\n"
"Qmvaa 92,132,60\nQtv 6,46,25856\nQmvaa 124,136,60\nbuffer_load_dwordx4 a[220:223], v60, s[48:51], s94 offen offset:1024 sc1\nQmvaa 156,140,60\nQtv 8,46,20992\nQmvaa 188,144,60\nQtv 10,46,26112\n"
"Qmvaa 220,148,60\ns_waitcnt vmcnt(20)\nds_write_b128 v52, a[0:3] offset:4096\nQmvaa 252,132,64\nQmvaa 100,136,64\nQtv 12,46,21248\nQmvaa 132,140,64\nQtv 14,46,26368\n"
"Qmvaa 164,144,64\nQda 72,53,37888\nQmvaa 196,148,64\nQtv 16,46,21504\ns_waitcnt lgkmcnt(1)\nQmvaa 228,132,68\nQtv 18,46,26624\nQmvaa 108,136,68\n"
"buffer_load_dwordx4 a[224:227], v60, s[48:51], s94 offen offset:2048 sc1\nQmvaa 140,140,68\nQtv 20,51,20480\nQmvaa 172,144,68\nQtv 22,51,25600\nv_add_u32 v75, 1, v75\nQmvaa 204,148,68\ns_waitcnt vmcnt(22)\n"
"ds_write_b32 v73, v75\nQmvaa 236,132,72\ns_waitcnt vmcnt(20)\nds_write_b128 v52, a[4:7] offset:8192\nQmvaa 80,136,72\nQmvaa 112,140,72\nQtv 24,51,20736\nQmvaa 144,144,72\n"
"Qda 76,53,41984\nQmvaa 176,148,72\nQtv 26,51,25856\ns_waitcnt lgkmcnt(7)\nQmvva 92,0,248\nQtv 28,51,20992\ns_mov_b32 s94, 849920\nQmvva 124,4,248\n"
"buffer_load_dwordx4 a[228:231], v60, s[48:51], s94 offen offset:0 sc1\nQmvva 156,8,248\nds_read_b32 v69, v74\nQmvva 188,12,248\nQtv 30,51,26112\nQmvva 220,16,248\ns_waitcnt vmcnt(20)\nds_write_b128 v52, a[8:11] offset:12288\n"
"Qmvva 252,0,252\nQmvva 100,4,252\ns_waitcnt lgkmcnt(2)\nv_cmp_gt_u32 vcc, v75, v69\nQmvva 132,8,252\ns_cbranch_vccnz .Lso57r1\n.Lro57r1:\nQmvva 164,12,252\n"
"Qda 80,53,46080\nQmvva 196,16,252\nQtv 32,51,21248\nQmvva 228,0,168\nQtv 34,51,26368\nQmvva 108,4,168\nbuffer_load_dwordx4 a[232:235], v60, s[48:51], s94 offen offset:1024 sc1\nQmvva 140,8,168\n"
"Qtv 36,51,21504\nQmvva 172,12,168\nQtv 38,51,26624\nQmvva 204,16,168\ns_waitcnt vmcnt(20)\nds_write_b128 v52, a[12:15] offset:16384\nQmvva 236,0,172\nQmvva 80,4,172\n"
"Qta 112,45,0\nQmvva 112,8,172\nQta 114,45,5120\nQmvva 144,12,172\nQda 48,53,50176\nQmvva 176,16,172\nQta 116,45,256\ns_waitcnt lgkmcnt(5)\n"
"Qmvva 92,20,76\nQta 118,45,5376\nQmvva 124,24,76\nbuffer_load_dwordx4 a[236:239], v60, s[48:51], s94 offen offset:2048 sc1\nQmvva 156,28,76\nQta 120,45,512\nQmvva 188,32,76\nQta 122,45,5632\n"
"Qmvva 220,36,76\ns_waitcnt vmcnt(19)\nds_write_b128 v52, a[16:19] offset:20480\nQmvva 252,20,80\nQmvva 100,24,80\nQta 124,45,768\nQmvva 132,28,80\nQta 126,45,5888\n"
"Qmvva 164,32,80\nQda 52,53,0\nv_add_u32 v75, 1, v75\nQmvva 196,36,80\ns_waitcnt vmcnt(13)\nds_write_b32 v73, v75\ns_waitcnt lgkmcnt(1)\nQmvva 228,20,48\n"
"Qta 128,45,1024\nQmvva 108,24,48\nQta 130,45,6144\nQmvva 140,28,48\nQta 132,47,0\nQmvva 172,32,48\nQta 134,47,5120\nQmvva 204,36,48\n"
"s_waitcnt vmcnt(17)\nds_write_b128 v52, a[20:23] offset:25600\nQmvva 236,20,52\nQmvva 80,24,52\nQta 136,47,256\nQmvva 112,28,52\nds_read_b32 v69, v74\nQmvva 144,32,52\n"
"Qda 56,53,4096\nQmvva 176,36,52\nQta 138,47,5376\ns_waitcnt lgkmcnt(15)\nQmvaa 92,112,0\nQta 140,47,512\nQmvaa 124,116,0\ns_waitcnt lgkmcnt(3)\n"
"v_cmp_gt_u32 vcc, v75, v69\nQmvaa 156,120,0\ns_cbranch_vccnz .Lso58r1\n.Lro58r1:\nQmvaa 188,124,0\nQta 142,47,5632\nQmvaa 220,128,0\ns_waitcnt vmcnt(14)\n"
"ds_write_b128 v52, a[24:27] offset:29696\nQmvaa 252,112,4\nQmvaa 100,116,4\nQta 144,47,768\nQmvaa 132,120,4\nQta 146,47,5888\nQmvaa 164,124,4\nQda 60,53,8192\n"
"Qmvaa 196,128,4\nQta 148,47,1024\nQmvaa 228,112,8\nQta 150,47,6144\nQmvaa 108,116,8\nQmvaa 140,120,8\nQtv 0,45,20480\nQmvaa 172,124,8\n"
"Qtv 2,45,25600\nQmvaa 204,128,8\ns_waitcnt vmcnt(12)\nds_write_b128 v52, a[28:31] offset:33792\nQmvaa 236,112,12\nQmvaa 80,116,12\nQtv 4,45,20736\nQmvaa 112,120,12\n"
"Qtv 6,45,25856\nQmvaa 144,124,12\nQda 64,53,12288\nQmvaa 176,128,12\ns_waitcnt lgkmcnt(6)\nQmvaa 92,132,56\nQtv 8,45,20992\nQmvaa 124,136,56\n"
"Qtv 10,45,26112\nQmvaa 156,140,56\nQtv 12,45,21248\nQmvaa 188,144,56\nv_add_u32 v75, 1, v75\nQmvaa 220,148,56\nds_write_b32 v73, v75\nQmvaa 252,132,60\n"
"s_waitcnt vmcnt(11)\nds_write_b128 v52, a[32:35] offset:37888\nQmvaa 100,136,60\nQmvaa 132,140,60\nQtv 14,45,26368\nQmvaa 164,144,60\nQda 68,53,16384\nQmvaa 196,148,60\n"
"Qtv 16,45,21504\ns_waitcnt lgkmcnt(1)\nQmvaa 228,132,64\nQtv 18,45,26624\nQmvaa 108,136,64\nQmvaa 140,140,64\nds_read_b32 v69, v74\nQmvaa 172,144,64\n"
"Qtv 20,47,20480\nQmvaa 204,148,64\ns_waitcnt vmcnt(10)\nds_write_b128 v52, a[36:39] offset:41984\nQmvaa 236,132,68\nQmvaa 80,136,68\ns_waitcnt lgkmcnt(2)\nv_cmp_gt_u32 vcc, v75, v69\n"
"Qmvaa 112,140,68\ns_cbranch_vccnz .Lso59r1\n.Lro59r1:\nQmvaa 144,144,68\nQda 72,53,20480\nQmvaa 176,148,68\nQtv 22,47,25600\ns_waitcnt lgkmcnt(5)\n"
"Qmvva 92,0,16\nQtv 24,47,20736\nQmvva 124,4,16\nQtv 26,47,25856\nQmvva 156,8,16\nQtv 28,47,20992\nQmvva 188,12,16\nQtv 30,47,26112\n"
"Qmvva 220,16,16\ns_waitcnt vmcnt(9)\nds_write_b128 v52, a[40:43] offset:46080\nQmvva 252,0,20\nQmvva 100,4,20\nQtv 32,47,21248\nQmvva 132,8,20\nQtv 34,47,26368\n"
"Qmvva 164,12,20\nQda 76,53,25600\nQmvva 196,16,20\nQtv 36,47,21504\nQmvva 228,0,24\nQtv 38,47,26624\nQmvva 108,4,24\nQmvva 140,8,24\n"
"Qta 112,46,0\nQmvva 172,12,24\nQta 114,46,5120\nQmvva 204,16,24\ns_waitcnt vmcnt(8)\nds_write_b128 v52, a[44:47] offset:50176\nQmvva 236,0,28\nQmvva 80,4,28\n"
"Qta 116,46,256\nQmvva 112,8,28\nQta 118,46,5376\nQmvva 144,12,28\nQda 80,53,29696\nv_add_u32 v75, 1, v75\nQmvva 176,16,28\nds_write_b32 v73, v75\n"
"s_waitcnt lgkmcnt(7)\nQmvva 92,20,72\nQta 120,46,512\ns_add_u32 s68, s64, 0\nQmvva 124,24,72\nQta 122,46,5632\nQmvva 156,28,72\nQta 124,46,768\n"
"Qmvva 188,32,72\nQta 126,46,5888\nQmvva 220,36,72\ns_waitcnt vmcnt(7)\nds_write_b128 v52, a[208:211] offset:0\nQmvva 252,20,76\nQmvva 100,24,76\nQta 128,46,1024\n"
"Qk 92,92,93\nQmvva 132,28,76\nds_read_b32 v69, v74\nv_min_u32 v92, -2, v92\nQmvva 164,32,76\nQda 48,53,33792\nQk 93,94,95\nQmvva 196,36,76\n"
"Qta 130,46,6144\nv_min_u32 v93, -2, v93\ns_waitcnt lgkmcnt(10)\nQmvva 228,20,80\nQk 94,124,125\nv_min_u32 v94, -2, v94\nQmvva 108,24,80\ns_waitcnt lgkmcnt(1)\n"
"v_cmp_gt_u32 vcc, v75, v69\nQmvva 140,28,80\ns_cbranch_vccnz .Lso60r1\n.Lro60r1:\nQk 95,126,127\nQmvva 172,32,80\nQta 132,51,0\nv_min_u32 v95, -2, v95\n"
"Qmvva 204,36,80\ns_waitcnt vmcnt(6)\nds_write_b128 v52, a[212:215] offset:4096\nQmvva 236,20,48\nbuffer_store_dwordx4 v[92:95], v60, s[24:27], s68 offen offset:0 sc1\nQk 156,156,157\nQmvva 80,24,48\nQta 134,51,5120\n"
"v_min_u32 v156, -2, v156\nQmvva 112,28,48\nQta 136,51,256\nQk 157,158,159\nQmvva 144,32,48\nQda 52,53,37888\nv_min_u32 v157, -2, v157\nQmvva 176,36,48\n"
"Qta 138,51,5376\nQk 158,188,189\ns_waitcnt lgkmcnt(6)\nQmvaa 208,112,32\nQta 140,51,512\nv_min_u32 v158, -2, v158\nQmvaa 240,116,32\nQk 159,190,191\n"
"v_min_u32 v159, -2, v159\nQmvaa 84,120,32\nQta 142,51,5632\nbuffer_store_dwordx4 v[156:159], v60, s[24:27], s68 offen offset:1024 sc1\nQmvaa 88,124,32\nQta 144,51,768\nQk 220,220,221\nQmvaa 120,128,32\n"
"s_waitcnt vmcnt(7)\nds_write_b128 v52, a[216:219] offset:8192\nQmvaa 152,112,36\nv_min_u32 v220, -2, v220\nQk 221,222,223\nQmvaa 184,116,36\nQta 146,51,5888\nv_min_u32 v221, -2, v221\n"
"Qmvaa 216,120,36\nQta 148,51,1024\nQk 222,252,253\nQmvaa 248,124,36\nQda 56,53,41984\nv_min_u32 v222, -2, v222\nQmvaa 116,128,36\nQta 150,51,6144\n"
"Qk 223,254,255\nQmvaa 96,112,40\nQtv 0,46,20480\nQtv 2,46,25600\nQmvaa 128,116,40\nQtv 4,46,20736\nv_min_u32 v223, -2, v223\nQmvaa 160,120,40\n"
"Qtv 6,46,25856\nbuffer_store_dwordx4 v[220:223], v60, s[24:27], s68 offen offset:2048 sc1\nQmvaa 192,124,40\nQtv 8,46,20992\nv_add_u32 v75, 1, v75\nQmvaa 224,128,40\nds_write_b32 v73, v75\nQk 100,100,101\n"
"s_waitcnt lgkmcnt(6)\nQmvaa 208,132,52\ns_waitcnt vmcnt(7)\nds_write_b128 v52, a[220:223] offset:12288\nQmvaa 240,136,52\nv_min_u32 v100, -2, v100\nQk 101,102,103\nQmvaa 84,140,52\n"
"Qtv 10,46,26112\nv_min_u32 v101, -2, v101\nQmvaa 88,144,52\nQda 60,53,46080\nQk 102,132,133\nQmvaa 120,148,52\nQtv 12,46,21248\nv_min_u32 v102, -2, v102\n"
"Qmvaa 152,132,56\nQtv 14,46,26368\nQk 103,134,135\nQmvaa 184,136,56\nQtv 16,46,21504\nv_min_u32 v103, -2, v103\nQmvaa 216,140,56\nds_read_b32 v69, v74\n"
"buffer_store_dwordx4 v[100:103], v60, s[24:27], s68 offen offset:3072 sc1\nQmvaa 248,144,56\nQtv 18,46,26624\nQk 164,164,165\nQmvaa 116,148,56\ns_waitcnt vmcnt(7)\nds_write_b128 v52, a[224:227] offset:16384\ns_waitcnt lgkmcnt(6)\n"
"Qmvaa 96,132,60\nv_min_u32 v164, -2, v164\nQk 165,166,167\nQmvaa 128,136,60\ns_waitcnt lgkmcnt(2)\nv_cmp_gt_u32 vcc, v75, v69\nQmvaa 160,140,60\ns_cbranch_vccnz .Lso61r1\n"
".Lro61r1:\nv_min_u32 v165, -2, v165\nQmvaa 192,144,60\nQda 64,53,50176\nQk 166,196,197\nQmvaa 224,148,60\nQtv 20,51,20480\nQtv 22,51,25600\n"
"s_waitcnt lgkmcnt(4)\nQmvva 208,0,44\nQtv 24,51,20736\nv_min_u32 v166, -2, v166\nQmvva 240,4,44\nQtv 26,51,25856\nQk 167,198,199\nQmvva 84,8,44\n"
"Qtv 28,51,20992\nv_min_u32 v167, -2, v167\nQmvva 88,12,44\nQtv 30,51,26112\ns_add_u32 s68, s64, 4096\nQmvva 120,16,44\ns_waitcnt vmcnt(6)\nds_write_b128 v52, a[228:231] offset:20480\n"
"Qmvva 152,0,208\nbuffer_store_dwordx4 v[164:167], v60, s[24:27], s68 offen offset:0 sc1\nQk 228,228,229\nQmvva 184,4,208\nQtv 32,51,21248\nv_min_u32 v228, -2, v228\nQmvva 216,8,208\nQtv 34,51,26368\n"
"Qk 229,230,231\nQmvva 248,12,208\nQda 68,53,0\nv_min_u32 v229, -2, v229\nQmvva 116,16,208\nQtv 36,51,21504\nQk 230,108,109\nQmvva 96,0,212\n"
"Qtv 38,51,26624\nv_min_u32 v230, -2, v230\nQmvva 128,4,212\nQta 112,45,0\nQta 114,45,5120\nQmvva 160,8,212\nQta 116,45,256\nQk 231,110,111\n"
"Qmvva 192,12,212\nQta 118,45,5376\nv_min_u32 v231, -2, v231\nQmvva 224,16,212\ns_waitcnt vmcnt(6)\nds_write_b128 v52, a[232:235] offset:25600\ns_waitcnt lgkmcnt(5)\nQmvva 208,20,64\n"
"buffer_store_dwordx4 v[228:231], v60, s[24:27], s68 offen offset:1024 sc1\nQk 140,140,141\nQmvva 240,24,64\nQta 120,45,512\nv_min_u32 v140, -2, v140\nQmvva 84,28,64\nQta 122,45,5632\nQk 141,142,143\n"
"Qmvva 88,32,64\nQda 72,53,4096\nv_add_u32 v75, 1, v75\nQmvva 120,36,64\nds_write_b32 v73, v75\nv_min_u32 v141, -2, v141\nQmvva 152,20,68\nQta 124,45,768\n"
"Qk 142,172,173\nQmvva 184,24,68\nQta 126,45,5888\nv_min_u32 v142, -2, v142\nQmvva 216,28,68\nQta 128,45,1024\nQk 143,174,175\nQmvva 248,32,68\n"
"Qta 130,45,6144\nv_min_u32 v143, -2, v143\nQmvva 116,36,68\ns_waitcnt vmcnt(6)\nds_write_b128 v52, a[236:239] offset:29696\ns_waitcnt lgkmcnt(6)\nQmvva 96,20,72\nbuffer_store_dwordx4 v[140:143], v60, s[24:27], s68 offen offset:2048 sc1\n"
"Qk 204,204,205\nQmvva 128,24,72\nQta 132,47,0\nv_min_u32 v204, -2, v204\nQmvva 160,28,72\nds_read_b32 v69, v74\nQk 205,206,207\nQmvva 192,32,72\n"
"Qda 76,53,8192\nv_min_u32 v205, -2, v205\nQmvva 224,36,72\nQta 134,47,5120\nQk 206,236,237\ns_waitcnt lgkmcnt(14)\nQmvaa 208,112,216\nQta 136,47,256\n"
"v_min_u32 v206, -2, v206\nQmvaa 240,116,216\ns_waitcnt lgkmcnt(3)\nv_cmp_gt_u32 vcc, v75, v69\nQmvaa 84,120,216\ns_cbranch_vccnz .Lso62r1\n.Lro62r1:\nQk 207,238,239\n"
"Qmvaa 88,124,216\nQta 138,47,5376\nv_min_u32 v207, -2, v207\nQmvaa 120,128,216\nQta 140,47,512\ns_add_u32 s68, s64, 14336\nQmvaa 152,112,220\nQta 142,47,5632\n"
"buffer_store_dwordx4 v[204:207], v60, s[24:27], s68 offen offset:0 sc1\nQmvaa 184,116,220\nQta 144,47,768\nQk 80,80,81\nQmvaa 216,120,220\nQta 146,47,5888\nv_min_u32 v80, -2, v80\nQmvaa 248,124,220\n"
"Qda 80,53,12288\nQk 81,82,83\nQmvaa 116,128,220\nQta 148,47,1024\nv_min_u32 v81, -2, v81\nQmvaa 96,112,224\nQta 150,47,6144\nQk 82,112,113\n"
"Qmvaa 128,116,224\nQtv 0,45,20480\nv_min_u32 v82, -2, v82\nQmvaa 160,120,224\nQtv 2,45,25600\nQk 83,114,115\nQmvaa 192,124,224\nQtv 4,45,20736\n"
"v_min_u32 v83, -2, v83\nQmvaa 224,128,224\nQtv 6,45,25856\nbuffer_store_dwordx4 v[80:83], v60, s[24:27], s68 offen offset:1024 sc1\ns_waitcnt lgkmcnt(4)\nQmvaa 208,132,76\nQtv 8,45,20992\nQk 144,144,145\n"
"Qmvaa 240,136,76\nQtv 10,45,26112\nv_min_u32 v144, -2, v144\nQmvaa 84,140,76\nQtv 12,45,21248\nQk 145,146,147\nQmvaa 88,144,76\nQda 48,53,16384\n"
"v_min_u32 v145, -2, v145\nQmvaa 120,148,76\nQtv 14,45,26368\nQk 146,176,177\nQmvaa 152,132,80\nQtv 16,45,21504\nv_min_u32 v146, -2, v146\nQmvaa 184,136,80\n"
"Qtv 18,45,26624\nQk 147,178,179\nQmvaa 216,140,80\nQtv 20,47,20480\nv_min_u32 v147, -2, v147\nQmvaa 248,144,80\nQtv 22,47,25600\nv_add_u32 v75, 1, v75\n"
"Qmvaa 116,148,80\nds_write_b32 v73, v75\nbuffer_store_dwordx4 v[144:147], v60, s[24:27], s68 offen offset:2048 sc1\ns_waitcnt lgkmcnt(6)\nQmvaa 96,132,48\nQtv 24,47,20736\nQmvaa 128,136,48\nQtv 26,47,25856\n"
"Qmvaa 160,140,48\nQtv 28,47,20992\nQmvaa 192,144,48\nQda 52,53,20480\nQmvaa 224,148,48\nQtv 30,47,26112\ns_waitcnt lgkmcnt(8)\nQmvva 208,0,228\n"
"Qtv 32,47,21248\nQmvva 240,4,228\nQtv 34,47,26368\nQmvva 84,8,228\nds_read_b32 v69, v74\nQmvva 88,12,228\nQtv 36,47,21504\nQmvva 120,16,228\n"
"Qtv 38,47,26624\nQmvva 152,0,232\nQmvva 184,4,232\ns_waitcnt lgkmcnt(2)\nv_cmp_gt_u32 vcc, v75, v69\nQmvva 216,8,232\ns_cbranch_vccnz .Lso63r1\n.Lro63r1:\n"
"Qmvva 248,12,232\nQda 56,53,25600\nQmvva 116,16,232\nQmvva 96,0,236\ns_add_u32 s69, s98, 0\nbuffer_load_dwordx4 v[124:127], v60, s[24:27], s69 offen offset:0 sc1\nQmvva 128,4,236\nQmvva 160,8,236\n"
"buffer_load_dwordx4 v[188:191], v60, s[24:27], s69 offen offset:1024 sc1\nQmvva 192,12,236\nQmvva 224,16,236\nbuffer_load_dwordx4 v[252:255], v60, s[24:27], s69 offen offset:2048 sc1\ns_waitcnt lgkmcnt(0)\nQmvva 208,20,52\nQmvva 240,24,52\nbuffer_load_dwordx4 v[132:135], v60, s[24:27], s69 offen offset:3072 sc1\n"
"Qmvva 84,28,52\nQmvva 88,32,52\nQda 60,53,29696\nQmvva 120,36,52\ns_add_u32 s69, s98, 4096\nbuffer_load_dwordx4 v[196:199], v60, s[24:27], s69 offen offset:0 sc1\nQmvva 152,20,56\nQmvva 184,24,56\n"
"buffer_load_dwordx4 v[108:111], v60, s[24:27], s69 offen offset:1024 sc1\nQmvva 216,28,56\nQmvva 248,32,56\nbuffer_load_dwordx4 v[172:175], v60, s[24:27], s69 offen offset:2048 sc1\nQmvva 116,36,56\ns_waitcnt lgkmcnt(0)\nQmvva 96,20,60\ns_add_u32 s69, s98, 14336\n"
"buffer_load_dwordx4 v[236:239], v60, s[24:27], s69 offen offset:0 sc1\nQmvva 128,24,60\nQmvva 160,28,60\nQmvva 192,32,60\nQmvva 224,36,60\ns_barrier\ns_mov_b32 s97, 0\n.Lrx1:\n"
"s_waitcnt vmcnt(0)\nv_max_u32 v40, v124, v125\nv_max3_u32 v40, v40, v126, v127\nv_max3_u32 v40, v40, v188, v189\nv_max3_u32 v40, v40, v190, v191\nv_max3_u32 v40, v40, v252, v253\nv_max3_u32 v40, v40, v254, v255\nv_max3_u32 v40, v40, v132, v133\n"
"v_max3_u32 v40, v40, v134, v135\nv_max3_u32 v40, v40, v196, v197\nv_max3_u32 v40, v40, v198, v199\nv_max3_u32 v40, v40, v108, v109\nv_max3_u32 v40, v40, v110, v111\nv_max3_u32 v40, v40, v172, v173\nv_max3_u32 v40, v40, v174, v175\nv_max3_u32 v40, v40, v236, v237\n"
"v_cmp_eq_u32 vcc, -1, v40\ns_cmp_eq_u64 vcc, 0\ns_cbranch_scc1 .Lrx1_ok\ns_add_u32 s97, s97, 1\ns_cmp_gt_u32 s97, 1048576\ns_cbranch_scc1 .Lrx1_ok\ns_add_u32 s69, s98, 0\nbuffer_load_dwordx4 v[124:127], v60, s[24:27], s69 offen offset:0 sc1\n"
"buffer_load_dwordx4 v[188:191], v60, s[24:27], s69 offen offset:1024 sc1\nbuffer_load_dwordx4 v[252:255], v60, s[24:27], s69 offen offset:2048 sc1\nbuffer_load_dwordx4 v[132:135], v60, s[24:27], s69 offen offset:3072 sc1\ns_add_u32 s69, s98, 4096\nbuffer_load_dwordx4 v[196:199], v60, s[24:27], s69 offen offset:0 sc1\nbuffer_load_dwordx4 v[108:111], v60, s[24:27], s69 offen offset:1024 sc1\nbuffer_load_dwordx4 v[172:175], v60, s[24:27], s69 offen offset:2048 sc1\ns_add_u32 s69, s98, 14336\n"
"buffer_load_dwordx4 v[236:239], v60, s[24:27], s69 offen offset:0 sc1\ns_branch .Lrx1\n.Lrx1_ok:\nv_lshlrev_b32 v41, 16, v124\nv_and_b32 v42, 0xffff0000, v124\nQa 208,208,41\nQa 209,209,42\nv_lshlrev_b32 v43, 16, v125\n"
"v_and_b32 v44, 0xffff0000, v125\nQa 210,210,43\nQa 211,211,44\nQk 208,208,209\nQk 209,210,211\nds_write_b64 v61, v[208:209] offset:21504\nv_lshlrev_b32 v41, 16, v126\nv_and_b32 v42, 0xffff0000, v126\n"
"Qa 240,240,41\nQa 241,241,42\nv_lshlrev_b32 v43, 16, v127\nv_and_b32 v44, 0xffff0000, v127\nQa 242,242,43\nQa 243,243,44\nQk 240,240,241\nQk 241,242,243\n"
"ds_write_b64 v61, v[240:241] offset:21536\nv_lshlrev_b32 v41, 16, v188\nv_and_b32 v42, 0xffff0000, v188\nQa 84,84,41\nQa 85,85,42\nv_lshlrev_b32 v43, 16, v189\nv_and_b32 v44, 0xffff0000, v189\nQa 86,86,43\n"
"Qa 87,87,44\nQk 84,84,85\nQk 85,86,87\nds_write_b64 v61, v[84:85] offset:21568\nv_lshlrev_b32 v41, 16, v190\nv_and_b32 v42, 0xffff0000, v190\nQa 88,88,41\nQa 89,89,42\n"
"v_lshlrev_b32 v43, 16, v191\nv_and_b32 v44, 0xffff0000, v191\nQa 90,90,43\nQa 91,91,44\nQk 88,88,89\nQk 89,90,91\nds_write_b64 v61, v[88:89] offset:21600\nv_lshlrev_b32 v41, 16, v252\n"
"v_and_b32 v42, 0xffff0000, v252\nQa 120,120,41\nQa 121,121,42\nv_lshlrev_b32 v43, 16, v253\nv_and_b32 v44, 0xffff0000, v253\nQa 122,122,43\nQa 123,123,44\nQk 120,120,121\n"
"Qk 121,122,123\nds_write_b64 v61, v[120:121] offset:21632\nv_lshlrev_b32 v41, 16, v254\nv_and_b32 v42, 0xffff0000, v254\nQa 152,152,41\nQa 153,153,42\nv_lshlrev_b32 v43, 16, v255\nv_and_b32 v44, 0xffff0000, v255\n"
"Qa 154,154,43\nQa 155,155,44\nQk 152,152,153\nQk 153,154,155\nds_write_b64 v61, v[152:153] offset:26880\nv_lshlrev_b32 v41, 16, v132\nv_and_b32 v42, 0xffff0000, v132\nQa 184,184,41\n"
"Qa 185,185,42\nv_lshlrev_b32 v43, 16, v133\nv_and_b32 v44, 0xffff0000, v133\nQa 186,186,43\nQa 187,187,44\nQk 184,184,185\nQk 185,186,187\nds_write_b64 v61, v[184:185] offset:26912\n"
"v_lshlrev_b32 v41, 16, v134\nv_and_b32 v42, 0xffff0000, v134\nQa 216,216,41\nQa 217,217,42\nv_lshlrev_b32 v43, 16, v135\nv_and_b32 v44, 0xffff0000, v135\nQa 218,218,43\nQa 219,219,44\n"
"Qk 216,216,217\nQk 217,218,219\nds_write_b64 v61, v[216:217] offset:26944\nv_lshlrev_b32 v41, 16, v196\nv_and_b32 v42, 0xffff0000, v196\nQa 248,248,41\nQa 249,249,42\nv_lshlrev_b32 v43, 16, v197\n"
"v_and_b32 v44, 0xffff0000, v197\nQa 250,250,43\nQa 251,251,44\nQk 248,248,249\nQk 249,250,251\nds_write_b64 v61, v[248:249] offset:26976\nv_lshlrev_b32 v41, 16, v198\nv_and_b32 v42, 0xffff0000, v198\n"
"Qa 116,116,41\nQa 117,117,42\nv_lshlrev_b32 v43, 16, v199\nv_and_b32 v44, 0xffff0000, v199\nQa 118,118,43\nQa 119,119,44\nQk 116,116,117\nQk 117,118,119\n"
"ds_write_b64 v61, v[116:117] offset:27008\nv_lshlrev_b32 v41, 16, v108\nv_and_b32 v42, 0xffff0000, v108\nQa 96,96,41\nQa 97,97,42\nv_lshlrev_b32 v43, 16, v109\nv_and_b32 v44, 0xffff0000, v109\nQa 98,98,43\n"
"Qa 99,99,44\nQk 96,96,97\nQk 97,98,99\nds_write_b64 v61, v[96:97] offset:32256\nv_lshlrev_b32 v41, 16, v110\nv_and_b32 v42, 0xffff0000, v110\nQa 128,128,41\nQa 129,129,42\n"
"v_lshlrev_b32 v43, 16, v111\nv_and_b32 v44, 0xffff0000, v111\nQa 130,130,43\nQa 131,131,44\nQk 128,128,129\nQk 129,130,131\nds_write_b64 v61, v[128:129] offset:32288\nv_lshlrev_b32 v41, 16, v172\n"
"v_and_b32 v42, 0xffff0000, v172\nQa 160,160,41\nQa 161,161,42\nv_lshlrev_b32 v43, 16, v173\nv_and_b32 v44, 0xffff0000, v173\nQa 162,162,43\nQa 163,163,44\nQk 160,160,161\n"
"Qk 161,162,163\nds_write_b64 v61, v[160:161] offset:32320\nv_lshlrev_b32 v41, 16, v174\nv_and_b32 v42, 0xffff0000, v174\nQa 192,192,41\nQa 193,193,42\nv_lshlrev_b32 v43, 16, v175\nv_and_b32 v44, 0xffff0000, v175\n"
"Qa 194,194,43\nQa 195,195,44\nQk 192,192,193\nQk 193,194,195\nds_write_b64 v61, v[192:193] offset:32352\nv_lshlrev_b32 v41, 16, v236\nv_and_b32 v42, 0xffff0000, v236\nQa 224,224,41\n"
"Qa 225,225,42\nv_lshlrev_b32 v43, 16, v237\nv_and_b32 v44, 0xffff0000, v237\nQa 226,226,43\nQa 227,227,44\nQk 224,224,225\nQk 225,226,227\nds_write_b64 v61, v[224:225] offset:32384\n"
".Lfin:\ns_waitcnt lgkmcnt(0)\ns_barrier\nQdv 0,62,0\nQdv 4,62,5376\nQdv 8,62,10752\nQdv 12,62,16128\nQdv 16,62,21504\n"
"Qdv 20,62,26880\nQdv 24,62,32256\nQdv 28,70,0\nQdv 32,70,21504\ns_add_u32 s68, s65, 0\ns_waitcnt lgkmcnt(8)\nbuffer_store_dwordx4 v[0:3], v63, s[40:43], s68 offen sc1\ns_add_u32 s68, s65, 40960\n"
"s_waitcnt lgkmcnt(7)\nbuffer_store_dwordx4 v[4:7], v63, s[40:43], s68 offen sc1\ns_add_u32 s68, s65, 81920\ns_waitcnt lgkmcnt(6)\nbuffer_store_dwordx4 v[8:11], v63, s[40:43], s68 offen sc1\ns_add_u32 s68, s65, 122880\ns_waitcnt lgkmcnt(5)\nbuffer_store_dwordx4 v[12:15], v63, s[40:43], s68 offen sc1\n"
"s_add_u32 s68, s60, 163840\ns_waitcnt lgkmcnt(4)\nbuffer_store_dwordx4 v[16:19], v63, s[40:43], s68 offen sc1\ns_add_u32 s68, s60, 204800\ns_waitcnt lgkmcnt(3)\nbuffer_store_dwordx4 v[20:23], v63, s[40:43], s68 offen sc1\ns_add_u32 s68, s60, 245760\ns_waitcnt lgkmcnt(2)\n"
"buffer_store_dwordx4 v[24:27], v63, s[40:43], s68 offen sc1\ns_add_u32 s68, s60, 163840\ns_cmp_eq_u32 s70, 3\ns_cselect_b32 s68, 0x7ffff000, s68\ns_waitcnt lgkmcnt(1)\nbuffer_store_dwordx4 v[28:31], v71, s[40:43], s65 offen sc1\ns_waitcnt lgkmcnt(0)\nbuffer_store_dwordx4 v[32:35], v71, s[40:43], s68 offen sc1\n"
"s_waitcnt vmcnt(0) lgkmcnt(0)\n.Lend:\n"
  :: "s"((unsigned)kp), "s"((unsigned)(kp >> 32)), "s"((unsigned)vp), "s"((unsigned)(vp >> 32)), "s"((unsigned)xp), "s"((unsigned)(xp >> 32)),
     "s"((unsigned)wp), "s"((unsigned)(wp >> 32)), "s"((unsigned)op), "s"((unsigned)(op >> 32)), "s"(ldsb), "s"(wid), "s"(head0),
     "s"((unsigned)dp), "s"((unsigned)(dp >> 32)), "s"((unsigned)wop), "s"((unsigned)(wop >> 32)), "s"((unsigned)yp), "s"((unsigned)(yp >> 32)),
     "s"(hg), "s"(b * 8u + hg)
  : "v0", "v1", "v2", "v3", "v4", "v5", "v6", "v7", "v8", "v9", "v10", "v11", "v12", "v13", "v14", "v15", "v16", "v17", "v18", "v19", "v20", "v21", "v22", "v23", "v24", "v25", "v26", "v27", "v28", "v29", "v30", "v31", "v32", "v33", "v34", "v35", "v36", "v37", "v38", "v39", "v40", "v41", "v42", "v43", "v44", "v45", "v46", "v47", "v48", "v49", "v50", "v51", "v52", "v53", "v54", "v55", "v56", "v57", "v58", "v59", "v60", "v61", "v62", "v63", "v64", "v65", "v66", "v67", "v68", "v69", "v70", "v71", "v72", "v73", "v74", "v75", "v76", "v77", "v78", "v79", "v80", "v81", "v82", "v83", "v84", "v85", "v86", "v87", "v88", "v89", "v90", "v91", "v92", "v93", "v94", "v95", "v96", "v97", "v98", "v99", "v100", "v101", "v102", "v103", "v104", "v105", "v106", "v107", "v108", "v109", "v110", "v111", "v112", "v113", "v114", "v115", "v116", "v117", "v118", "v119", "v120", "v121", "v122", "v123", "v124", "v125", "v126", "v127", "v128", "v129", "v130", "v131", "v132", "v133", "v134", "v135", "v136", "v137", "v138", "v139", "v140", "v141", "v142", "v143", "v144", "v145", "v146", "v147", "v148", "v149", "v150", "v151", "v152", "v153", "v154", "v155", "v156", "v157", "v158", "v159", "v160", "v161", "v162", "v163", "v164", "v165", "v166", "v167", "v168", "v169", "v170", "v171", "v172", "v173", "v174", "v175", "v176", "v177", "v178", "v179", "v180", "v181", "v182", "v183", "v184", "v185", "v186", "v187", "v188", "v189", "v190", "v191", "v192", "v193", "v194", "v195", "v196", "v197", "v198", "v199", "v200", "v201", "v202", "v203", "v204", "v205", "v206", "v207", "v208", "v209", "v210", "v211", "v212", "v213", "v214", "v215", "v216", "v217", "v218", "v219", "v220", "v221", "v222", "v223", "v224", "v225", "v226", "v227", "v228", "v229", "v230", "v231", "v232", "v233", "v234", "v235", "v236", "v237", "v238", "v239", "v240", "v241", "v242", "v243", "v244", "v245", "v246", "v247", "v248", "v249", "v250", "v251", "v252", "v253", "v254", "v255", "a0", "a1", "a2", "a3", "a4", "a5", "a6", "a7", "a8", "a9", "a10", "a11", "a12", "a13", "a14", "a15", "a16", "a17", "a18", "a19", "a20", "a21", "a22", "a23", "a24", "a25", "a26", "a27", "a28", "a29", "a30", "a31", "a32", "a33", "a34", "a35", "a36", "a37", "a38", "a39", "a40", "a41", "a42", "a43", "a44", "a45", "a46", "a47", "a48", "a49", "a50", "a51", "a52", "a53", "a54", "a55", "a56", "a57", "a58", "a59", "a60", "a61", "a62", "a63", "a64", "a65", "a66", "a67", "a68", "a69", "a70", "a71", "a72", "a73", "a74", "a75", "a76", "a77", "a78", "a79", "a80", "a81", "a82", "a83", "a84", "a85", "a86", "a87", "a88", "a89", "a90", "a91", "a92", "a93", "a94", "a95", "a96", "a97", "a98", "a99", "a100", "a101", "a102", "a103", "a104", "a105", "a106", "a107", "a108", "a109", "a110", "a111", "a112", "a113", "a114", "a115", "a116", "a117", "a118", "a119", "a120", "a121", "a122", "a123", "a124", "a125", "a126", "a127", "a128", "a129", "a130", "a131", "a132", "a133", "a134", "a135", "a136", "a137", "a138", "a139", "a140", "a141", "a142", "a143", "a144", "a145", "a146", "a147", "a148", "a149", "a150", "a151", "a152", "a153", "a154", "a155", "a156", "a157", "a158", "a159", "a160", "a161", "a162", "a163", "a164", "a165", "a166", "a167", "a168", "a169", "a170", "a171", "a172", "a173", "a174", "a175", "a176", "a177", "a178", "a179", "a180", "a181", "a182", "a183", "a184", "a185", "a186", "a187", "a188", "a189", "a190", "a191", "a192", "a193", "a194", "a195", "a196", "a197", "a198", "a199", "a200", "a201", "a202", "a203", "a204", "a205", "a206", "a207", "a208", "a209", "a210", "a211", "a212", "a213", "a214", "a215", "a216", "a217", "a218", "a219", "a220", "a221", "a222", "a223", "a224", "a225", "a226", "a227", "a228", "a229", "a230", "a231", "a232", "a233", "a234", "a235", "a236", "a237", "a238", "a239", "a240", "a241", "a242", "a243", "a244", "a245", "a246", "a247", "a248", "a249", "a250", "a251", "a252", "a253", "a254", "a255", "s24", "s25", "s26", "s27", "s28", "s29", "s30", "s31", "s40", "s41", "s42", "s43", "s44", "s45", "s46", "s47", "s48", "s49", "s50", "s51", "s52", "s53", "s54", "s55", "s56", "s57", "s58", "s59", "s60", "s61", "s62", "s63", "s64", "s65", "s66", "s67", "s68", "s69", "s70", "s71", "s72", "s73", "s74", "s75", "s76", "s77", "s78", "s79", "s80", "s81", "s82", "s83", "s84", "s85", "s86", "s87", "s88", "s89", "s90", "s91", "s92", "s93", "s94", "s95", "s96", "s97", "s98", "s99", "s100", "s101", "vcc", "scc", "memory");
}

extern "C" void kernel_launch(void* const* d_in, const int* in_sizes, int n_in, void* d_out, int out_size, void* d_ws, size_t ws_size, hipStream_t stream) {
  typedef const unsigned short* P;
  (void)hipFuncSetAttribute((const void*)mha2_asm, hipFuncAttributeMaxDynamicSharedMemorySize, 163840);
  hipLaunchKernelGGL(mha2_asm, dim3(256), dim3(256), 163840, stream, (P)d_in[0], (P)d_in[5], (P)d_in[3], (P)d_in[8], (P)d_in[1], (P)d_in[6], (P)d_in[2], (P)d_in[7],
                     (P)d_in[4], (P)d_in[9], (char*)d_ws, (unsigned short*)d_out);
}
```

```cpp
#include <hip/hip_runtime.h>
#include <stdint.h>

__global__ __launch_bounds__(256, 1) __attribute__((aligned(4096)))
void mha2_asm(const unsigned short* __restrict__ X0, const unsigned short* __restrict__ X1, const unsigned short* __restrict__ W0, const unsigned short* __restrict__ W1,
              const unsigned short* __restrict__ K0, const unsigned short* __restrict__ K1,
              const unsigned short* __restrict__ V0, const unsigned short* __restrict__ V1,
              const unsigned short* __restrict__ WO0, const unsigned short* __restrict__ WO1, char* ws, unsigned short* out) {
  extern __shared__ __attribute__((aligned(16))) char lds[];
  const unsigned c = blockIdx.x;
  const unsigned xcd = c & 7u, idx = c >> 3;
  const unsigned hg = idx & 7u, bb = xcd * 4u + (idx >> 3);
  const unsigned bk = bb >> 4, b = bb & 15u;
  const unsigned wid = __builtin_amdgcn_readfirstlane(threadIdx.x >> 6);
  const unsigned head0 = (hg >> 2) * 16u + (hg & 3u) * 2u + (wid >> 1);
  const unsigned long long kp = (unsigned long long)(bk ? K1 : K0) + ((unsigned long long)b * 32u + head0) * 262144ull - 1024ull;
  const unsigned long long vp = (unsigned long long)(bk ? V1 : V0) + ((unsigned long long)b * 32u + head0) * 262144ull - 1024ull;
  const unsigned long long xp = (unsigned long long)(bk ? X1 : X0) + (unsigned long long)b * (224u * 1280u);
  const unsigned long long wp = (unsigned long long)(bk ? W1 : W0);
  const unsigned long long op = (unsigned long long)ws + 0ull + (unsigned long long)bb * 2295808ull;
  const unsigned ldsb = (unsigned)(size_t)lds;
  const unsigned long long wop = (unsigned long long)(bk ? WO1 : WO0) - 4096ull;
  const unsigned long long yp = (unsigned long long)out + (unsigned long long)b * (224ull * 2560ull) + bk * 1280u + (hg & 3u) * 320u;
  const unsigned long long dp = (unsigned long long)ws + (100ull << 20) + (c * 4u + wid) * 256u;
  asm volatile(
".macro Qta d,v,o\nds_read_b64_tr_b16 a[\\d:\\d+1], v\\v offset:\\o\n.endm\n.macro Qdv d,v,o\nds_read_b128 v[\\d:\\d+3], v\\v offset:\\o\n.endm\n.macro Qda d,v,o\nds_read_b128 a[\\d:\\d+3], v\\v offset:\\o\n"
".endm\n.macro Qzvav d,a,b\nv_mfma_f32_16x16x32_bf16 v[\\d:\\d+3], a[\\a:\\a+3], v[\\b:\\b+3], 0\n.endm\n.macro Qzaav d,a,b\nv_mfma_f32_16x16x32_bf16 a[\\d:\\d+3], a[\\a:\\a+3], v[\\b:\\b+3], 0\n.endm\n.macro Qmvaa d,a,b\n"
"v_mfma_f32_16x16x32_bf16 v[\\d:\\d+3], a[\\a:\\a+3], a[\\b:\\b+3], v[\\d:\\d+3]\n.endm\n.macro Qmaaa d,a,b\nv_mfma_f32_16x16x32_bf16 a[\\d:\\d+3], a[\\a:\\a+3], a[\\b:\\b+3], a[\\d:\\d+3]\n.endm\n.macro Qmvav d,a,b\nv_mfma_f32_16x16x32_bf16 v[\\d:\\d+3], a[\\a:\\a+3], v[\\b:\\b+3], v[\\d:\\d+3]\n.endm\n"
".macro Qmaav d,a,b\nv_mfma_f32_16x16x32_bf16 a[\\d:\\d+3], a[\\a:\\a+3], v[\\b:\\b+3], a[\\d:\\d+3]\n.endm\n.macro Qk d,a,b\nv_cvt_pk_bf16_f32 v\\d, v\\a, v\\b\n.endm\n.macro Qzvaa d,a,b\nv_mfma_f32_16x16x32_bf16 v[\\d:\\d+3], a[\\a:\\a+3], a[\\b:\\b+3], 0\n"
".endm\n.macro Qa d,a,b\nv_add_f32 v\\d, v\\a, v\\b\n.endm\n.macro Qe d\nv_exp_f32 v\\d, v\\d\n.endm\n.macro Qcvaa d,a,b,c\n"
"v_mfma_f32_16x16x32_bf16 v[\\d:\\d+3], a[\\a:\\a+3], a[\\b:\\b+3], v[\\c:\\c+3]\n.endm\n.macro Qtv d,v,o\nds_read_b64_tr_b16 v[\\d:\\d+1], v\\v offset:\\o\n.endm\n.macro Qzava d,a,b\nv_mfma_f32_16x16x32_bf16 a[\\d:\\d+3], v[\\a:\\a+3], a[\\b:\\b+3], 0\n.endm\n"
".macro Qmava d,a,b\nv_mfma_f32_16x16x32_bf16 a[\\d:\\d+3], v[\\a:\\a+3], a[\\b:\\b+3], a[\\d:\\d+3]\n.endm\n.macro Qmvva d,a,b\nv_mfma_f32_16x16x32_bf16 v[\\d:\\d+3], v[\\a:\\a+3], a[\\b:\\b+3], v[\\d:\\d+3]\n.endm\n.p2align 7\ns_mov_b32 s52, %6\n"
"s_and_b32 s53, %7, 0xffff\ns_mov_b32 s54, 5242880\ns_mov_b32 s55, 0x20000\ns_mov_b32 s48, %4\ns_and_b32 s49, %5, 0xffff\ns_mov_b32 s50, 286720\ns_mov_b32 s51, 0x20000\ns_mov_b32 s70, %11\n"
"s_mov_b32 s24, %8\ns_and_b32 s25, %9, 0xffff\ns_mov_b32 s26, 2295808\ns_mov_b32 s27, 0x20000\ns_and_b32 s92, s70, 1\ns_lshr_b32 s93, s70, 1\nv_mbcnt_lo_u32_b32 v0, -1, 0\nv_mbcnt_hi_u32_b32 v0, -1, v0\n"
"v_and_b32 v4, 7, v0\nv_lshlrev_b32 v4, 7, v4\nv_add_u32 v76, 2293760, v4\nbuffer_load_dword v65, v76, s[24:27], 0 offen sc1\nv_lshrrev_b32 v2, 4, v0\ns_lshl_b32 s68, s92, 17\nv_and_b32 v4, 3, v0\nv_lshlrev_b32 v4, 4, v4\n"
"v_lshl_add_u32 v4, v2, 6, v4\nv_bfe_u32 v1, v0, 2, 2\nv_lshl_add_u32 v4, v1, 13, v4\nv_add_u32 v62, s68, v4\ns_lshl_b32 s87, %12, 8\ns_mul_i32 s62, s93, 8224\ns_mul_i32 s68, s92, 4112\ns_add_u32 s62, s62, s68\n"
"s_add_u32 s62, s62, %10\ns_add_u32 s62, s62, 33024\ns_lshl_b32 s69, s92, 1\nv_lshrrev_b32 v3, 5, v0\nv_and_b32 v4, 3, v0\nv_xor_b32 v4, s69, v4\nv_lshlrev_b32 v4, 4, v4\nv_lshl_add_u32 v4, v3, 6, v4\n"
"v_bfe_u32 v1, v0, 2, 3\nv_mul_u32_u24 v64, 1280, v1\nv_add_u32 v64, v64, v4\ns_mul_i32 s86, s93, 143360\ns_mul_i32 s68, s92, 10240\ns_add_u32 s86, s86, s68\ns_mul_i32 s63, s92, 14336\ns_mul_i32 s68, s93, 7168\n"
"s_add_u32 s63, s63, s68\ns_add_u32 s63, s63, %10\ns_add_u32 s63, s63, 99072\ns_nop 1\ns_add_u32 m0, s62, 0\ns_add_u32 s69, s87, 0\ns_nop 0\nbuffer_load_dwordx4 v62, s[52:55], s69 offen lds\n"
"s_add_u32 s69, s87, 31744\ns_nop 0\nbuffer_load_dwordx4 v62, s[52:55], s69 offen offset:1024 lds\ns_add_u32 s69, s87, 63480\ns_nop 0\nbuffer_load_dwordx4 v62, s[52:55], s69 offen offset:2056 lds\ns_add_u32 s69, s87, 95224\ns_nop 0\n"
"buffer_load_dwordx4 v62, s[52:55], s69 offen offset:3080 lds\ns_add_u32 m0, s63, 33024\ns_add_u32 s88, s86, 0\ns_nop 0\nbuffer_load_dwordx4 v64, s[48:51], s88 offen lds\ns_add_u32 s88, s86, 19456\ns_nop 0\nbuffer_load_dwordx4 v64, s[48:51], s88 offen offset:1024 lds\n"
"s_add_u32 s88, s86, 38912\ns_nop 0\nbuffer_load_dwordx4 v64, s[48:51], s88 offen offset:2048 lds\ns_add_u32 s88, s86, 58368\ns_nop 0\nbuffer_load_dwordx4 v64, s[48:51], s88 offen offset:3072 lds\ns_add_u32 m0, s63, 37120\ns_add_u32 s88, s86, 81920\n"
"s_nop 0\nbuffer_load_dwordx4 v64, s[48:51], s88 offen lds\ns_add_u32 s88, s86, 101376\ns_nop 0\nbuffer_load_dwordx4 v64, s[48:51], s88 offen offset:1024 lds\ns_add_u32 s88, s86, 120832\ns_nop 0\nbuffer_load_dwordx4 v64, s[48:51], s88 offen offset:2048 lds\n"
"s_add_u32 m0, s62, 16448\ns_add_u32 s69, s87, 262144\ns_nop 0\nbuffer_load_dwordx4 v62, s[52:55], s69 offen lds\ns_add_u32 s69, s87, 293888\ns_nop 0\nbuffer_load_dwordx4 v62, s[52:55], s69 offen offset:1024 lds\ns_add_u32 s69, s87, 325624\n"
"s_nop 0\nbuffer_load_dwordx4 v62, s[52:55], s69 offen offset:2056 lds\ns_add_u32 s69, s87, 357368\ns_nop 0\nbuffer_load_dwordx4 v62, s[52:55], s69 offen offset:3080 lds\ns_add_u32 m0, s63, 0\ns_add_u32 s88, s86, 128\ns_nop 0\n"
"buffer_load_dwordx4 v64, s[48:51], s88 offen lds\ns_add_u32 s88, s86, 19584\ns_nop 0\nbuffer_load_dwordx4 v64, s[48:51], s88 offen offset:1024 lds\ns_add_u32 s88, s86, 39040\ns_nop 0\nbuffer_load_dwordx4 v64, s[48:51], s88 offen offset:2048 lds\ns_add_u32 s88, s86, 58496\n"
"s_nop 0\nbuffer_load_dwordx4 v64, s[48:51], s88 offen offset:3072 lds\ns_add_u32 m0, s63, 4096\ns_add_u32 s88, s86, 82048\ns_nop 0\nbuffer_load_dwordx4 v64, s[48:51], s88 offen lds\ns_add_u32 s88, s86, 101504\ns_nop 0\n"
"buffer_load_dwordx4 v64, s[48:51], s88 offen offset:1024 lds\ns_add_u32 s88, s86, 120960\ns_nop 0\nbuffer_load_dwordx4 v64, s[48:51], s88 offen offset:2048 lds\ns_add_u32 m0, s62, 33024\ns_add_u32 s69, s87, 524288\ns_nop 0\nbuffer_load_dwordx4 v62, s[52:55], s69 offen lds\n"
"s_add_u32 s69, s87, 556032\ns_nop 0\nbuffer_load_dwordx4 v62, s[52:55], s69 offen offset:1024 lds\ns_add_u32 s69, s87, 587768\ns_nop 0\nbuffer_load_dwordx4 v62, s[52:55], s69 offen offset:2056 lds\ns_add_u32 s69, s87, 619512\ns_nop 0\n"
"buffer_load_dwordx4 v62, s[52:55], s69 offen offset:3080 lds\ns_add_u32 m0, s62, 49472\ns_add_u32 s69, s87, 786432\ns_nop 0\nbuffer_load_dwordx4 v62, s[52:55], s69 offen lds\ns_add_u32 s69, s87, 818176\ns_nop 0\nbuffer_load_dwordx4 v62, s[52:55], s69 offen offset:1024 lds\n"
"s_add_u32 s69, s87, 849912\ns_nop 0\nbuffer_load_dwordx4 v62, s[52:55], s69 offen offset:2056 lds\ns_add_u32 s69, s87, 881656\ns_nop 0\nbuffer_load_dwordx4 v62, s[52:55], s69 offen offset:3080 lds\ns_mov_b32 s40, %0\ns_and_b32 s41, %1, 0xffff\n"
"s_mov_b32 s42, 2360320\ns_mov_b32 s43, 0x20000\ns_mov_b32 s44, %2\ns_and_b32 s45, %3, 0xffff\ns_mov_b32 s46, 2360320\ns_mov_b32 s47, 0x20000\ns_mov_b32 s56, %8\ns_and_b32 s57, %9, 0xffff\n"
"s_mov_b32 s58, 0\ns_mov_b32 s59, 0x20000\ns_mov_b32 s60, 0\ns_mov_b32 s67, 0\ns_mov_b32 s72, 0x3e0293ee\ns_lshl_b32 s68, s92, 12\ns_add_u32 s88, s68, 1024\nv_lshl_add_u32 v4, v1, 8, v4\n"
"v_add_u32 v56, s88, v4\nv_subrev_u32 v57, 896, v56\nv_and_b32 v4, 1, v0\nv_lshlrev_b32 v4, 4, v4\nv_lshl_add_u32 v4, v2, 5, v4\nv_bfe_u32 v1, v0, 1, 3\nv_lshl_add_u32 v4, v1, 8, v4\nv_add_u32 v58, s88, v4\n"
"v_subrev_u32 v59, 896, v58\ns_lshl_b32 s69, s93, 14\ns_add_u32 s69, s69, %10\ns_add_u32 s61, s69, s68\ns_nop 1\ns_add_u32 m0, s61, 0\ns_nop 0\nbuffer_load_dwordx4 v56, s[40:43], s60 offen nt lds\n"
"buffer_load_dwordx4 v57, s[40:43], s60 offen offset:1024 nt lds\nbuffer_load_dwordx4 v56, s[40:43], s60 offen offset:2048 nt lds\nbuffer_load_dwordx4 v57, s[40:43], s60 offen offset:3072 nt lds\ns_add_u32 m0, s61, 8192\ns_nop 0\nbuffer_load_dwordx4 v58, s[44:47], s60 offen nt lds\nbuffer_load_dwordx4 v59, s[44:47], s60 offen offset:1024 nt lds\nbuffer_load_dwordx4 v58, s[44:47], s60 offen offset:2048 nt lds\n"
"buffer_load_dwordx4 v59, s[44:47], s60 offen offset:3072 nt lds\ns_add_u32 s60, s60, 8192\nv_lshlrev_b32 v60, 4, v0\ns_add_u32 m0, %10, 160768\ns_getpc_b64 s[56:57]\n.Lpc0:\ns_and_b32 s56, s56, 0xffffff80\ns_and_b32 s57, s57, 0xffff\n"
"s_mov_b32 s58, .Lend-.Lpc0\ns_and_b32 s94, %20, 31\ns_lshl_b32 s94, s94, 2\ns_add_u32 s94, s94, s70\ns_lshl_b32 s94, s94, 10\nbuffer_load_dwordx4 v60, s[56:59], s94 offen lds\ns_mov_b32 s56, %8\ns_and_b32 s57, %9, 0xffff\n"
"v_bfe_u32 v3, v0, 3, 1\nv_lshlrev_b32 v4, 1, v3\nv_xor_b32 v4, v4, v2\nv_and_b32 v1, 7, v0\nv_lshl_add_u32 v4, v1, 2, v4\nv_lshlrev_b32 v4, 4, v4\nv_lshl_add_u32 v1, v3, 12, v4\nv_add_u32 v52, s69, v1\n"
"v_add_u32 v53, 66048, v52\ns_mul_i32 s68, s92, 7168\ns_add_u32 s68, s68, %10\ns_add_u32 s68, s68, 99072\nv_mul_u32_u24 v1, 14336, v3\nv_add3_u32 v63, v1, v4, s68\nv_lshrrev_b32 v3, 5, v0\nv_and_b32 v4, 31, v0\n"
"v_lshlrev_b32 v4, 3, v4\nv_lshl_add_u32 v4, v3, 12, v4\nv_add_u32 v4, 8192, v4\nv_add_u32 v54, s69, v4\nv_add_u32 v55, 66048, v54\ns_mul_i32 s68, s93, 8224\ns_add_u32 s68, s68, %10\ns_add_u32 s68, s68, 33024\n"
"v_mul_u32_u24 v3, 2056, v2\nv_and_b32 v4, 15, v0\nv_lshlrev_b32 v4, 4, v4\nv_add3_u32 v61, v3, v4, s68\ns_mul_i32 s68, s70, 6\ns_add_u32 s68, s68, 0\ns_sub_u32 s69, s68, 0\ns_lshl_b32 s69, s69, 10\n"
"v_add_u32 v3, s69, v60\ns_sub_u32 s69, s68, 14\ns_lshl_b32 s69, s69, 2\ns_and_b32 s69, s69, 63\nv_lshrrev_b32 v4, 8, v60\nv_add_u32 v4, s69, v4\nv_mul_u32_u24 v5, 205, v4\nv_lshrrev_b32 v5, 11, v5\n"
"v_mul_u32_u24 v70, 10, v5\nv_sub_u32 v4, v4, v70\nv_lshlrev_b32 v4, 5, v4\nv_bfe_u32 v70, v60, 5, 3\nv_lshl_add_u32 v5, v5, 3, v70\nv_mul_u32_u24 v5, 1280, v5\nv_and_b32 v70, 16, v60\nv_add3_u32 v4, v4, v5, v70\n"
"v_add_u32 v4, 4096, v4\ns_cmp_lt_u32 s68, 14\ns_cselect_b64 vcc, -1, 0\nv_cndmask_b32 v70, v4, v3, vcc\ns_mul_i32 s68, s70, 6\ns_add_u32 s68, s68, 1\ns_sub_u32 s69, s68, 1\ns_lshl_b32 s69, s69, 10\n"
"v_add_u32 v3, s69, v60\ns_sub_u32 s69, s68, 14\ns_lshl_b32 s69, s69, 2\ns_and_b32 s69, s69, 63\nv_lshrrev_b32 v4, 8, v60\nv_add_u32 v4, s69, v4\nv_mul_u32_u24 v5, 205, v4\nv_lshrrev_b32 v5, 11, v5\n"
"v_mul_u32_u24 v71, 10, v5\nv_sub_u32 v4, v4, v71\nv_lshlrev_b32 v4, 5, v4\nv_bfe_u32 v71, v60, 5, 3\nv_lshl_add_u32 v5, v5, 3, v71\nv_mul_u32_u24 v5, 1280, v5\nv_and_b32 v71, 16, v60\nv_add3_u32 v4, v4, v5, v71\n"
"v_add_u32 v4, 3072, v4\ns_cmp_lt_u32 s68, 14\ns_cselect_b64 vcc, -1, 0\nv_cndmask_b32 v71, v4, v3, vcc\ns_mul_i32 s68, s70, 6\ns_add_u32 s68, s68, 2\ns_sub_u32 s69, s68, 2\ns_lshl_b32 s69, s69, 10\n"
"v_add_u32 v3, s69, v60\ns_sub_u32 s69, s68, 14\ns_lshl_b32 s69, s69, 2\ns_and_b32 s69, s69, 63\nv_lshrrev_b32 v4, 8, v60\nv_add_u32 v4, s69, v4\nv_mul_u32_u24 v5, 205, v4\nv_lshrrev_b32 v5, 11, v5\n"
"v_mul_u32_u24 v72, 10, v5\nv_sub_u32 v4, v4, v72\nv_lshlrev_b32 v4, 5, v4\nv_bfe_u32 v72, v60, 5, 3\nv_lshl_add_u32 v5, v5, 3, v72\nv_mul_u32_u24 v5, 1280, v5\nv_and_b32 v72, 16, v60\nv_add3_u32 v4, v4, v5, v72\n"
"v_add_u32 v4, 2048, v4\ns_cmp_lt_u32 s68, 14\ns_cselect_b64 vcc, -1, 0\nv_cndmask_b32 v72, v4, v3, vcc\ns_lshl_b32 s68, s70, 2\ns_add_u32 s69, %10, 131840\ns_add_u32 s68, s68, s69\nv_mov_b32 v73, s68\n"
"v_and_b32 v3, 3, v0\nv_lshl_add_u32 v74, v3, 2, s69\nv_mov_b32 v75, 0\nds_write_b32 v73, v75\ns_waitcnt lgkmcnt(0)\ns_mov_b32 s68, 40960\ns_cmp_lt_u32 s70, 3\ns_cselect_b32 s31, 14336, s68\n"
"s_mov_b32 s68, 40960\ns_cmp_lt_u32 s70, 2\ns_cselect_b32 s90, 14336, s68\ns_mul_i32 s91, s70, 6144\ns_add_u32 s91, s91, %10\nv_and_b32 v3, 1, v0\nv_mul_u32_u24 v3, 0xaaaa, v3\nv_or_b32 v77, 0x4444, v3\n"
"s_and_b32 s68, %19, 4\ns_lshl_b32 s28, 15, s68\ns_mov_b32 s29, 0\ns_and_b32 s69, %19, 3\ns_lshl_b32 s69, s69, 2\ns_lshr_b32 s68, s68, 1\ns_add_u32 s69, s69, s68\ns_add_u32 s69, s69, s93\n"
"s_mul_i32 s98, s69, 28672\ns_mul_i32 s68, s92, 7168\ns_add_u32 s98, s98, s68\ns_add_u32 s98, s98, 1835008\ns_mov_b32 s71, s98\ns_add_u32 s58, s98, 17408\ns_and_b32 s68, %19, 3\ns_lshl_b32 s99, 3072, s68\n"
"s_or_b32 s99, s99, 0x302\ns_mov_b32 s94, 0x7ffff000\ns_mul_i32 s68, %20, 40960\ns_mul_i32 s69, s93, 20480\ns_add_u32 s68, s68, s69\ns_lshl_b32 s69, s92, 10\ns_add_u32 s68, s68, s69\ns_add_u32 s68, s68, 2048\n"
"s_add_u32 s94, %6, s68\ns_addc_u32 s95, %7, 0\ns_and_b32 s95, s95, 0xffff\ns_mov_b32 s75, 17408\nv_mov_b32 v3, -1\nv_accvgpr_write_b32 a0, v3\nv_accvgpr_write_b32 a1, v3\nv_accvgpr_write_b32 a2, v3\n"
"v_accvgpr_write_b32 a3, v3\nv_accvgpr_write_b32 a16, v3\nv_accvgpr_write_b32 a17, v3\nv_accvgpr_write_b32 a18, v3\nv_accvgpr_write_b32 a19, v3\nv_accvgpr_write_b32 a32, v3\nv_accvgpr_write_b32 a33, v3\nv_accvgpr_write_b32 a34, v3\n"
"v_accvgpr_write_b32 a35, v3\nv_accvgpr_write_b32 a48, v3\nv_accvgpr_write_b32 a49, v3\nv_accvgpr_write_b32 a50, v3\nv_accvgpr_write_b32 a51, v3\nv_accvgpr_write_b32 a64, v3\nv_accvgpr_write_b32 a65, v3\nv_accvgpr_write_b32 a66, v3\n"
"v_accvgpr_write_b32 a67, v3\nv_accvgpr_write_b32 a80, v3\nv_accvgpr_write_b32 a81, v3\nv_accvgpr_write_b32 a82, v3\nv_accvgpr_write_b32 a83, v3\nv_accvgpr_write_b32 a96, v3\nv_accvgpr_write_b32 a97, v3\nv_accvgpr_write_b32 a98, v3\n"
"v_accvgpr_write_b32 a99, v3\nv_accvgpr_write_b32 a4, v3\nv_accvgpr_write_b32 a5, v3\nv_accvgpr_write_b32 a6, v3\nv_accvgpr_write_b32 a7, v3\nv_accvgpr_write_b32 a20, v3\nv_accvgpr_write_b32 a21, v3\nv_accvgpr_write_b32 a22, v3\n"
"v_accvgpr_write_b32 a23, v3\nv_accvgpr_write_b32 a36, v3\nv_accvgpr_write_b32 a37, v3\nv_accvgpr_write_b32 a38, v3\nv_accvgpr_write_b32 a39, v3\ns_mov_b64 exec, 0\ns_cmp_eq_u32 s70, 0\ns_cbranch_scc1 .Lpg0\n"
"s_cmp_eq_u32 s70, 1\ns_cbranch_scc1 .Lpg1\ns_cmp_eq_u32 s70, 2\ns_cbranch_scc1 .Lpg2\ns_branch .Lpg3\n.Ltr0:\ns_mov_b64 exec, -1\n.Lround:\n"
"s_waitcnt vmcnt(23)\ns_barrier\nQta 112,61,0\nQta 114,61,1024\nQdv 24,63,33024\nQta 116,61,8\nQta 118,61,1032\nQdv 28,63,34048\n"
"Qta 120,61,256\nQta 122,61,1280\nQdv 32,63,35072\nQta 124,61,264\nQta 126,61,1288\nQdv 36,63,36096\nQta 128,61,512\nQta 130,61,1536\n"
"Qdv 40,63,37120\nQta 132,61,520\nQta 134,61,1544\nQdv 44,63,38144\nQta 136,61,768\nQta 138,61,1792\nQdv 48,63,39168\nQta 140,61,776\n"
"Qta 142,61,1800\nQda 176,63,33536\nQda 180,63,34560\nQda 184,63,35584\nQda 188,63,36608\nQda 192,63,37632\nQda 196,63,38656\nQda 200,63,39680\n"
"s_waitcnt lgkmcnt(7)\nQzvav 80,112,24\nQzvav 84,116,24\ns_add_u32 s68, s71, 0\nQzvav 88,120,24\nbuffer_store_dwordx4 a[0:3], v60, s[56:59], s68 offen offset:0 sc1\nQzvav 92,124,24\nQzvav 96,128,24\n"
"s_add_u32 s68, s71, 14336\nQzvav 100,132,24\nbuffer_store_dwordx4 a[4:7], v60, s[56:59], s68 offen offset:0 sc1\nQzvav 104,136,24\nQzvav 108,140,24\ns_waitcnt lgkmcnt(0)\nQzvav 112,112,28\ns_waitcnt vmcnt(26)\n"
"s_barrier\nQzvav 116,116,28\nQta 144,61,16448\ns_add_u32 m0, s62, 0\ns_add_u32 s69, s87, 1048576\nQzvav 120,120,28\nbuffer_load_dwordx4 v62, s[52:55], s69 offen lds\nQzvav 124,124,28\n"
"Qta 146,61,17472\nQzvav 128,128,28\nQzvav 132,132,28\nQta 148,61,16456\ns_add_u32 s69, s87, 1080320\nQzvav 136,136,28\nbuffer_load_dwordx4 v62, s[52:55], s69 offen offset:1024 lds\nQzvav 140,140,28\n"
"Qta 150,61,17480\nQzvav 144,112,32\nQzvav 148,116,32\nQta 152,61,16704\ns_add_u32 s69, s87, 1112056\nQzvav 152,120,32\nbuffer_load_dwordx4 v62, s[52:55], s69 offen offset:2056 lds\nQzvav 156,124,32\n"
"Qta 154,61,17728\nQzvav 160,128,32\nQzvav 164,132,32\nQta 156,61,16712\ns_add_u32 s69, s87, 1143800\nQzvav 168,136,32\nbuffer_load_dwordx4 v62, s[52:55], s69 offen offset:3080 lds\nQzvav 172,140,32\n"
"Qta 158,61,17736\nQzvav 176,112,36\nQzvav 180,116,36\nQta 160,61,16960\ns_add_u32 m0, s63, 33024\ns_add_u32 s88, s86, 256\nQzvav 184,120,36\nbuffer_load_dwordx4 v64, s[48:51], s88 offen lds\n"
"Qzvav 188,124,36\nQta 162,61,17984\nQzvav 192,128,36\nQzvav 196,132,36\nQta 164,61,16968\ns_add_u32 s88, s86, 19712\nQzvav 200,136,36\nbuffer_load_dwordx4 v64, s[48:51], s88 offen offset:1024 lds\n"
"Qzvav 204,140,36\nQta 166,61,17992\nQzvav 208,112,40\nQzvav 212,116,40\nQta 168,61,17216\ns_add_u32 s88, s86, 39168\nQzvav 216,120,40\nbuffer_load_dwordx4 v64, s[48:51], s88 offen offset:2048 lds\n"
"Qzvav 220,124,40\nQta 170,61,18240\nQzvav 224,128,40\nQzvav 228,132,40\nQta 172,61,17224\ns_add_u32 s88, s86, 58624\nQzvav 232,136,40\nbuffer_load_dwordx4 v64, s[48:51], s88 offen offset:3072 lds\n"
"Qzvav 236,140,40\nQta 174,61,18248\nQzvav 240,112,44\nQzvav 244,116,44\ns_add_u32 m0, s63, 37120\ns_add_u32 s88, s86, 82176\nQzvav 248,120,44\nbuffer_load_dwordx4 v64, s[48:51], s88 offen lds\n"
"Qzvav 252,124,44\nQzaav 208,128,44\nQzaav 212,132,44\ns_add_u32 s88, s86, 101632\nQzaav 216,136,44\nbuffer_load_dwordx4 v64, s[48:51], s88 offen offset:1024 lds\nQzaav 220,140,44\nQzaav 224,112,48\n"
"Qzaav 228,116,48\ns_add_u32 s88, s86, 121088\nQzaav 232,120,48\nbuffer_load_dwordx4 v64, s[48:51], s88 offen offset:2048 lds\nQzaav 236,124,48\nQzaav 240,128,48\nQzaav 244,132,48\nQzaav 248,136,48\n"
"Qzaav 252,140,48\ns_waitcnt lgkmcnt(0)\nQmvaa 80,144,176\nQmvaa 84,148,176\ns_add_u32 s68, s71, 28672\nQmvaa 88,152,176\nbuffer_store_dwordx4 a[8:11], v60, s[56:59], s68 offen offset:0 sc1\nQmvaa 92,156,176\n"
"Qmvaa 96,160,176\ns_add_u32 s68, s71, 43008\nQmvaa 100,164,176\nbuffer_store_dwordx4 a[12:15], v60, s[56:59], s68 offen offset:0 sc1\nQmvaa 104,168,176\nQmvaa 108,172,176\ns_waitcnt lgkmcnt(0)\nQmvaa 112,144,180\n"
"s_waitcnt vmcnt(15)\ns_barrier\nQmvaa 116,148,180\nQda 176,63,512\ns_add_u32 m0, s62, 16448\ns_add_u32 s69, s87, 1310720\n.Lpg0:\ns_cbranch_execz .Lpg4\n"
"Qmvaa 120,152,180\nbuffer_load_dwordx4 v62, s[52:55], s69 offen lds\nQmvaa 124,156,180\nQta 112,61,33024\nQmvaa 128,160,180\nQta 114,61,34048\nQmvaa 132,164,180\nQta 116,61,33032\n"
"s_add_u32 s69, s87, 1342464\nQmvaa 136,168,180\nbuffer_load_dwordx4 v62, s[52:55], s69 offen offset:1024 lds\nQmvaa 140,172,180\nQda 180,63,1536\nQmvaa 144,144,184\nQta 118,61,34056\nQmvaa 148,148,184\n"
"Qdv 24,63,0\ns_add_u32 s69, s87, 1374200\nQmvaa 152,152,184\nbuffer_load_dwordx4 v62, s[52:55], s69 offen offset:2056 lds\nQmvaa 156,156,184\nQta 120,61,33280\nQmvaa 160,160,184\nQta 122,61,34304\n"
"Qmvaa 164,164,184\nQta 124,61,33288\ns_add_u32 s69, s87, 1405944\nQmvaa 168,168,184\nbuffer_load_dwordx4 v62, s[52:55], s69 offen offset:3080 lds\nQmvaa 172,172,184\nQda 184,63,2560\nQmvaa 176,144,188\n"
"Qta 126,61,34312\nQmvaa 180,148,188\nQta 128,61,33536\nQmvaa 184,152,188\nQta 130,61,34560\nQmvaa 188,156,188\nQta 132,61,33544\nQmvaa 192,160,188\n"
"Qta 134,61,34568\nQmvaa 196,164,188\nQta 136,61,33792\nQmvaa 200,168,188\nQta 138,61,34816\nQmvaa 204,172,188\nQda 188,63,3584\nQmvaa 208,144,192\n"
"Qta 140,61,33800\nQmvaa 212,148,192\nQta 142,61,34824\nQmvaa 216,152,192\nQdv 28,63,1024\nQmvaa 220,156,192\nQdv 32,63,2048\nQmvaa 224,160,192\n"
"Qdv 36,63,3072\nQmvaa 228,164,192\nQdv 40,63,4096\nQmvaa 232,168,192\nQdv 44,63,5120\nQmvaa 236,172,192\nQda 192,63,4608\nQmvaa 240,144,196\n"
"Qdv 48,63,6144\nQmvaa 244,148,196\nQmvaa 248,152,196\nQmvaa 252,156,196\nQmaaa 208,160,196\nQmaaa 212,164,196\nQmaaa 216,168,196\nQmaaa 220,172,196\n"
"Qda 196,63,5632\nQmaaa 224,144,200\nQmaaa 228,148,200\nQmaaa 232,152,200\nQmaaa 236,156,200\nQmaaa 240,160,200\nQmaaa 244,164,200\nQmaaa 248,168,200\n"
"Qmaaa 252,172,200\nQda 200,63,6656\ns_waitcnt lgkmcnt(9)\nQmvav 80,112,24\nQmvav 84,116,24\ns_add_u32 s68, s71, 0\nQmvav 88,120,24\nbuffer_store_dwordx4 a[16:19], v60, s[56:59], s68 offen offset:1024 sc1\n"
"Qmvav 92,124,24\nQmvav 96,128,24\ns_add_u32 s68, s71, 14336\nQmvav 100,132,24\nbuffer_store_dwordx4 a[20:23], v60, s[56:59], s68 offen offset:1024 sc1\nQmvav 104,136,24\nQmvav 108,140,24\ns_waitcnt lgkmcnt(0)\n"
"Qmvav 112,112,28\ns_waitcnt vmcnt(30)\ns_barrier\nQmvav 116,116,28\nQta 144,61,49472\ns_add_u32 m0, s62, 33024\ns_add_u32 s69, s87, 1572864\nQmvav 120,120,28\n"
"buffer_load_dwordx4 v62, s[52:55], s69 offen lds\nQmvav 124,124,28\nQta 146,61,50496\nQmvav 128,128,28\nQmvav 132,132,28\nQta 148,61,49480\ns_add_u32 s69, s87, 1604608\nQmvav 136,136,28\n"
"buffer_load_dwordx4 v62, s[52:55], s69 offen offset:1024 lds\nQmvav 140,140,28\nQta 150,61,50504\nQmvav 144,112,32\nQmvav 148,116,32\nQta 152,61,49728\ns_add_u32 s69, s87, 1636344\nQmvav 152,120,32\n"
"buffer_load_dwordx4 v62, s[52:55], s69 offen offset:2056 lds\nQmvav 156,124,32\nQta 154,61,50752\nQmvav 160,128,32\nQmvav 164,132,32\nQta 156,61,49736\ns_add_u32 s69, s87, 1668088\nQmvav 168,136,32\n"
"buffer_load_dwordx4 v62, s[52:55], s69 offen offset:3080 lds\nQmvav 172,140,32\nQta 158,61,50760\nQmvav 176,112,36\nQmvav 180,116,36\nQta 160,61,49984\ns_add_u32 m0, s63, 0\ns_add_u32 s88, s86, 384\n"
"Qmvav 184,120,36\nbuffer_load_dwordx4 v64, s[48:51], s88 offen lds\nQmvav 188,124,36\nQta 162,61,51008\nQmvav 192,128,36\nQmvav 196,132,36\nQta 164,61,49992\ns_add_u32 s88, s86, 19840\n"
"Qmvav 200,136,36\nbuffer_load_dwordx4 v64, s[48:51], s88 offen offset:1024 lds\nQmvav 204,140,36\nQta 166,61,51016\nQmvav 208,112,40\nQmvav 212,116,40\nQta 168,61,50240\ns_add_u32 s88, s86, 39296\n"
"Qmvav 216,120,40\nbuffer_load_dwordx4 v64, s[48:51], s88 offen offset:2048 lds\nQmvav 220,124,40\nQta 170,61,51264\nQmvav 224,128,40\nQmvav 228,132,40\nQta 172,61,50248\ns_add_u32 s88, s86, 58752\n"
"Qmvav 232,136,40\nbuffer_load_dwordx4 v64, s[48:51], s88 offen offset:3072 lds\nQmvav 236,140,40\nQta 174,61,51272\nQmvav 240,112,44\nQmvav 244,116,44\ns_add_u32 m0, s63, 4096\ns_add_u32 s88, s86, 82304\n"
"Qmvav 248,120,44\nbuffer_load_dwordx4 v64, s[48:51], s88 offen lds\nQmvav 252,124,44\nQmaav 208,128,44\nQmaav 212,132,44\ns_add_u32 s88, s86, 101760\nQmaav 216,136,44\nbuffer_load_dwordx4 v64, s[48:51], s88 offen offset:1024 lds\n"
"Qmaav 220,140,44\nQmaav 224,112,48\nQmaav 228,116,48\ns_add_u32 s88, s86, 121216\nQmaav 232,120,48\nbuffer_load_dwordx4 v64, s[48:51], s88 offen offset:2048 lds\nQmaav 236,124,48\nQmaav 240,128,48\n"
"Qmaav 244,132,48\nQmaav 248,136,48\nQmaav 252,140,48\ns_waitcnt lgkmcnt(0)\nQmvaa 80,144,176\nQmvaa 84,148,176\ns_add_u32 s68, s71, 28672\nQmvaa 88,152,176\n"
"buffer_store_dwordx4 a[24:27], v60, s[56:59], s68 offen offset:1024 sc1\nQmvaa 92,156,176\nQmvaa 96,160,176\ns_add_u32 s68, s71, 43008\nQmvaa 100,164,176\nbuffer_store_dwordx4 a[28:31], v60, s[56:59], s68 offen offset:1024 sc1\nQmvaa 104,168,176\nQmvaa 108,172,176\n"
"s_waitcnt lgkmcnt(0)\nQmvaa 112,144,180\ns_waitcnt vmcnt(21)\ns_barrier\nQmvaa 116,148,180\nQda 176,63,33536\ns_add_u32 m0, s62, 49472\ns_add_u32 s69, s87, 1835008\n"
"Qmvaa 120,152,180\nbuffer_load_dwordx4 v62, s[52:55], s69 offen lds\nQmvaa 124,156,180\nQta 112,61,0\nQmvaa 128,160,180\nQta 114,61,1024\nQmvaa 132,164,180\nQta 116,61,8\n"
"s_add_u32 s69, s87, 1866752\nQmvaa 136,168,180\nbuffer_load_dwordx4 v62, s[52:55], s69 offen offset:1024 lds\nQmvaa 140,172,180\nQda 180,63,34560\nQmvaa 144,144,184\nQta 118,61,1032\nQmvaa 148,148,184\n"
"Qdv 24,63,33024\ns_add_u32 s69, s87, 1898488\nQmvaa 152,152,184\nbuffer_load_dwordx4 v62, s[52:55], s69 offen offset:2056 lds\nQmvaa 156,156,184\nQta 120,61,256\nQmvaa 160,160,184\nQta 122,61,1280\n"
"Qmvaa 164,164,184\nQta 124,61,264\ns_add_u32 s69, s87, 1930232\nQmvaa 168,168,184\nbuffer_load_dwordx4 v62, s[52:55], s69 offen offset:3080 lds\nQmvaa 172,172,184\nQda 184,63,35584\nQmvaa 176,144,188\n"
"Qta 126,61,1288\nQmvaa 180,148,188\nQta 128,61,512\nQmvaa 184,152,188\nQta 130,61,1536\nQmvaa 188,156,188\nQta 132,61,520\nQmvaa 192,160,188\n"
"Qta 134,61,1544\nQmvaa 196,164,188\nQta 136,61,768\nQmvaa 200,168,188\nQta 138,61,1792\nQmvaa 204,172,188\nQda 188,63,36608\nQmvaa 208,144,192\n"
"Qta 140,61,776\nQmvaa 212,148,192\nQta 142,61,1800\nQmvaa 216,152,192\nQdv 28,63,34048\nQmvaa 220,156,192\nQdv 32,63,35072\nQmvaa 224,160,192\n"
"Qdv 36,63,36096\nQmvaa 228,164,192\nQdv 40,63,37120\nQmvaa 232,168,192\nQdv 44,63,38144\nQmvaa 236,172,192\nQda 192,63,37632\nQmvaa 240,144,196\n"
"Qdv 48,63,39168\nQmvaa 244,148,196\nQmvaa 248,152,196\nQmvaa 252,156,196\nQmaaa 208,160,196\nQmaaa 212,164,196\nQmaaa 216,168,196\nQmaaa 220,172,196\n"
"Qda 196,63,38656\nQmaaa 224,144,200\nQmaaa 228,148,200\nQmaaa 232,152,200\nQmaaa 236,156,200\nQmaaa 240,160,200\nQmaaa 244,164,200\nQmaaa 248,168,200\n"
"Qmaaa 252,172,200\nQda 200,63,39680\ns_waitcnt lgkmcnt(9)\nQmvav 80,112,24\nQmvav 84,116,24\ns_add_u32 s68, s71, 0\nQmvav 88,120,24\nbuffer_store_dwordx4 a[32:35], v60, s[56:59], s68 offen offset:2048 sc1\n"
"Qmvav 92,124,24\nQmvav 96,128,24\ns_add_u32 s68, s71, 14336\nQmvav 100,132,24\nbuffer_store_dwordx4 a[36:39], v60, s[56:59], s68 offen offset:2048 sc1\nQmvav 104,136,24\nQmvav 108,140,24\ns_waitcnt lgkmcnt(0)\n"
"Qmvav 112,112,28\ns_waitcnt vmcnt(21)\ns_barrier\nQmvav 116,116,28\nQta 144,61,16448\ns_add_u32 m0, s62, 0\ns_add_u32 s69, s87, 2097152\nQmvav 120,120,28\n"
"buffer_load_dwordx4 v62, s[52:55], s69 offen lds\nQmvav 124,124,28\nQta 146,61,17472\nQmvav 128,128,28\nQmvav 132,132,28\nQta 148,61,16456\ns_add_u32 s69, s87, 2128896\nQmvav 136,136,28\n"
"buffer_load_dwordx4 v62, s[52:55], s69 offen offset:1024 lds\nQmvav 140,140,28\nQta 150,61,17480\nQmvav 144,112,32\nQmvav 148,116,32\nQta 152,61,16704\ns_add_u32 s69, s87, 2160632\nQmvav 152,120,32\n"
"buffer_load_dwordx4 v62, s[52:55], s69 offen offset:2056 lds\nQmvav 156,124,32\nQta 154,61,17728\nQmvav 160,128,32\nQmvav 164,132,32\nQta 156,61,16712\ns_add_u32 s69, s87, 2192376\nQmvav 168,136,32\n"
"buffer_load_dwordx4 v62, s[52:55], s69 offen offset:3080 lds\nQmvav 172,140,32\nQta 158,61,17736\nQmvav 176,112,36\nQmvav 180,116,36\nQta 160,61,16960\ns_add_u32 m0, s63, 33024\ns_add_u32 s88, s86, 512\n"
"Qmvav 184,120,36\nbuffer_load_dwordx4 v64, s[48:51], s88 offen lds\nQmvav 188,124,36\nQta 162,61,17984\nQmvav 192,128,36\nQmvav 196,132,36\nQta 164,61,16968\ns_add_u32 s88, s86, 19968\n"
"Qmvav 200,136,36\nbuffer_load_dwordx4 v64, s[48:51], s88 offen offset:1024 lds\nQmvav 204,140,36\nQta 166,61,17992\nQmvav 208,112,40\nQmvav 212,116,40\nQta 168,61,17216\ns_add_u32 s88, s86, 39424\n"
"Qmvav 216,120,40\nbuffer_load_dwordx4 v64, s[48:51], s88 offen offset:2048 lds\nQmvav 220,124,40\nQta 170,61,18240\nQmvav 224,128,40\nQmvav 228,132,40\nQta 172,61,17224\ns_add_u32 s88, s86, 58880\n"
"Qmvav 232,136,40\nbuffer_load_dwordx4 v64, s[48:51], s88 offen offset:3072 lds\nQmvav 236,140,40\nQta 174,61,18248\nQmvav 240,112,44\nQmvav 244,116,44\ns_add_u32 m0, s63, 37120\ns_add_u32 s88, s86, 82432\n"
"Qmvav 248,120,44\nbuffer_load_dwordx4 v64, s[48:51], s88 offen lds\nQmvav 252,124,44\nQmaav 208,128,44\nQmaav 212,132,44\ns_add_u32 s88, s86, 101888\nQmaav 216,136,44\nbuffer_load_dwordx4 v64, s[48:51], s88 offen offset:1024 lds\n"
"Qmaav 220,140,44\nQmaav 224,112,48\nQmaav 228,116,48\ns_add_u32 s88, s86, 121344\nQmaav 232,120,48\nbuffer_load_dwordx4 v64, s[48:51], s88 offen offset:2048 lds\nQmaav 236,124,48\nQmaav 240,128,48\n"
"Qmaav 244,132,48\nQmaav 248,136,48\nQmaav 252,140,48\ns_waitcnt lgkmcnt(0)\nQmvaa 80,144,176\nQmvaa 84,148,176\ns_add_u32 s68, s71, 28672\nQmvaa 88,152,176\n"
"buffer_store_dwordx4 a[40:43], v60, s[56:59], s68 offen offset:2048 sc1\nQmvaa 92,156,176\nQmvaa 96,160,176\ns_add_u32 s68, s71, 43008\nQmvaa 100,164,176\nbuffer_store_dwordx4 a[44:47], v60, s[56:59], s68 offen offset:2048 sc1\nQmvaa 104,168,176\nQmvaa 108,172,176\n"
"s_waitcnt lgkmcnt(0)\nQmvaa 112,144,180\ns_waitcnt vmcnt(21)\ns_barrier\nQmvaa 116,148,180\nQda 176,63,512\ns_add_u32 m0, s62, 16448\ns_add_u32 s69, s87, 2359296\n"
"Qmvaa 120,152,180\nbuffer_load_dwordx4 v62, s[52:55], s69 offen lds\nQmvaa 124,156,180\nQta 112,61,33024\nQmvaa 128,160,180\nQta 114,61,34048\nQmvaa 132,164,180\nQta 116,61,33032\n"
"s_add_u32 s69, s87, 2391040\nQmvaa 136,168,180\nbuffer_load_dwordx4 v62, s[52:55], s69 offen offset:1024 lds\nQmvaa 140,172,180\nQda 180,63,1536\nQmvaa 144,144,184\nQta 118,61,34056\nQmvaa 148,148,184\n"
"Qdv 24,63,0\ns_add_u32 s69, s87, 2422776\nQmvaa 152,152,184\nbuffer_load_dwordx4 v62, s[52:55], s69 offen offset:2056 lds\nQmvaa 156,156,184\nQta 120,61,33280\nQmvaa 160,160,184\nQta 122,61,34304\n"
"Qmvaa 164,164,184\nQta 124,61,33288\ns_add_u32 s69, s87, 2454520\nQmvaa 168,168,184\nbuffer_load_dwordx4 v62, s[52:55], s69 offen offset:3080 lds\nQmvaa 172,172,184\nQda 184,63,2560\nQmvaa 176,144,188\n"
"Qta 126,61,34312\nQmvaa 180,148,188\nQta 128,61,33536\nQmvaa 184,152,188\nQta 130,61,34560\nQmvaa 188,156,188\nQta 132,61,33544\nQmvaa 192,160,188\n"
"Qta 134,61,34568\nQmvaa 196,164,188\nQta 136,61,33792\nQmvaa 200,168,188\nQta 138,61,34816\nQmvaa 204,172,188\nQda 188,63,3584\nQmvaa 208,144,192\n"
"Qta 140,61,33800\nQmvaa 212,148,192\nQta 142,61,34824\nQmvaa 216,152,192\nQdv 28,63,1024\nQmvaa 220,156,192\nQdv 32,63,2048\nQmvaa 224,160,192\n"
"Qdv 36,63,3072\nQmvaa 228,164,192\nQdv 40,63,4096\nQmvaa 232,168,192\nQdv 44,63,5120\nQmvaa 236,172,192\nQda 192,63,4608\nQmvaa 240,144,196\n"
"Qdv 48,63,6144\nQmvaa 244,148,196\nQmvaa 248,152,196\nQmvaa 252,156,196\nQmaaa 208,160,196\nQmaaa 212,164,196\nQmaaa 216,168,196\nQmaaa 220,172,196\n"
"Qda 196,63,5632\nQmaaa 224,144,200\nQmaaa 228,148,200\nQmaaa 232,152,200\nQmaaa 236,156,200\nQmaaa 240,160,200\nQmaaa 244,164,200\nQmaaa 248,168,200\n"
"Qmaaa 252,172,200\nQda 200,63,6656\ns_waitcnt lgkmcnt(9)\nQmvav 80,112,24\nQmvav 84,116,24\ns_add_u32 s68, s71, 0\nQmvav 88,120,24\nbuffer_store_dwordx4 a[48:51], v60, s[56:59], s68 offen offset:3072 sc1\n"
"Qmvav 92,124,24\nQmvav 96,128,24\ns_add_u32 s68, s71, 14336\nQmvav 100,132,24\nbuffer_store_dwordx4 a[52:55], v60, s[56:59], s68 offen offset:3072 sc1\nQmvav 104,136,24\nQmvav 108,140,24\ns_waitcnt lgkmcnt(0)\n"
"Qmvav 112,112,28\ns_waitcnt vmcnt(21)\ns_barrier\nQmvav 116,116,28\nQta 144,61,49472\ns_add_u32 m0, s62, 33024\ns_add_u32 s69, s87, 2621440\nQmvav 120,120,28\n"
"buffer_load_dwordx4 v62, s[52:55], s69 offen lds\nQmvav 124,124,28\nQta 146,61,50496\nQmvav 128,128,28\n.Lpg1:\ns_cbranch_execz .Lpg5\nQmvav 132,132,28\nQta 148,61,49480\n"
"s_add_u32 s69, s87, 2653184\nQmvav 136,136,28\nbuffer_load_dwordx4 v62, s[52:55], s69 offen offset:1024 lds\nQmvav 140,140,28\nQta 150,61,50504\nQmvav 144,112,32\nQmvav 148,116,32\nQta 152,61,49728\n"
"s_add_u32 s69, s87, 2684920\nQmvav 152,120,32\nbuffer_load_dwordx4 v62, s[52:55], s69 offen offset:2056 lds\nQmvav 156,124,32\nQta 154,61,50752\nQmvav 160,128,32\nQmvav 164,132,32\nQta 156,61,49736\n"
"s_add_u32 s69, s87, 2716664\nQmvav 168,136,32\nbuffer_load_dwordx4 v62, s[52:55], s69 offen offset:3080 lds\nQmvav 172,140,32\nQta 158,61,50760\nQmvav 176,112,36\nQmvav 180,116,36\nQta 160,61,49984\n"
"s_add_u32 m0, s63, 0\ns_add_u32 s88, s86, 640\nQmvav 184,120,36\nbuffer_load_dwordx4 v64, s[48:51], s88 offen lds\nQmvav 188,124,36\nQta 162,61,51008\nQmvav 192,128,36\nQmvav 196,132,36\n"
"Qta 164,61,49992\ns_add_u32 s88, s86, 20096\nQmvav 200,136,36\nbuffer_load_dwordx4 v64, s[48:51], s88 offen offset:1024 lds\nQmvav 204,140,36\nQta 166,61,51016\nQmvav 208,112,40\nQmvav 212,116,40\n"
"Qta 168,61,50240\ns_add_u32 s88, s86, 39552\nQmvav 216,120,40\nbuffer_load_dwordx4 v64, s[48:51], s88 offen offset:2048 lds\nQmvav 220,124,40\nQta 170,61,51264\nQmvav 224,128,40\nQmvav 228,132,40\n"
"Qta 172,61,50248\ns_add_u32 s88, s86, 59008\nQmvav 232,136,40\nbuffer_load_dwordx4 v64, s[48:51], s88 offen offset:3072 lds\nQmvav 236,140,40\nQta 174,61,51272\nQmvav 240,112,44\nQmvav 244,116,44\n"
"s_add_u32 m0, s63, 4096\ns_add_u32 s88, s86, 82560\nQmvav 248,120,44\nbuffer_load_dwordx4 v64, s[48:51], s88 offen lds\nQmvav 252,124,44\nQmaav 208,128,44\nQmaav 212,132,44\ns_add_u32 s88, s86, 102016\n"
"Qmaav 216,136,44\nbuffer_load_dwordx4 v64, s[48:51], s88 offen offset:1024 lds\nQmaav 220,140,44\nQmaav 224,112,48\nQmaav 228,116,48\ns_add_u32 s88, s86, 121472\nQmaav 232,120,48\nbuffer_load_dwordx4 v64, s[48:51], s88 offen offset:2048 lds\n"
"Qmaav 236,124,48\nQmaav 240,128,48\nQmaav 244,132,48\nQmaav 248,136,48\nQmaav 252,140,48\ns_waitcnt lgkmcnt(0)\nQmvaa 80,144,176\nQmvaa 84,148,176\n"
"s_add_u32 s68, s71, 28672\nQmvaa 88,152,176\nbuffer_store_dwordx4 a[56:59], v60, s[56:59], s68 offen offset:3072 sc1\nQmvaa 92,156,176\nQmvaa 96,160,176\ns_add_u32 s68, s71, 43008\nQmvaa 100,164,176\nbuffer_store_dwordx4 a[60:63], v60, s[56:59], s68 offen offset:3072 sc1\n"
"Qmvaa 104,168,176\nQmvaa 108,172,176\ns_waitcnt lgkmcnt(0)\nQmvaa 112,144,180\ns_waitcnt vmcnt(21)\ns_barrier\nQmvaa 116,148,180\nQda 176,63,33536\n"
"s_add_u32 m0, s62, 49472\ns_add_u32 s69, s87, 2883584\nQmvaa 120,152,180\nbuffer_load_dwordx4 v62, s[52:55], s69 offen lds\nQmvaa 124,156,180\nQta 112,61,0\nQmvaa 128,160,180\nQta 114,61,1024\n"
"Qmvaa 132,164,180\nQta 116,61,8\ns_add_u32 s69, s87, 2915328\nQmvaa 136,168,180\nbuffer_load_dwordx4 v62, s[52:55], s69 offen offset:1024 lds\nQmvaa 140,172,180\nQda 180,63,34560\nQmvaa 144,144,184\n"
"Qta 118,61,1032\nQmvaa 148,148,184\nQdv 24,63,33024\ns_add_u32 s69, s87, 2947064\nQmvaa 152,152,184\nbuffer_load_dwordx4 v62, s[52:55], s69 offen offset:2056 lds\nQmvaa 156,156,184\nQta 120,61,256\n"
"Qmvaa 160,160,184\nQta 122,61,1280\nQmvaa 164,164,184\nQta 124,61,264\ns_add_u32 s69, s87, 2978808\nQmvaa 168,168,184\nbuffer_load_dwordx4 v62, s[52:55], s69 offen offset:3080 lds\nQmvaa 172,172,184\n"
"Qda 184,63,35584\nQmvaa 176,144,188\nQta 126,61,1288\nQmvaa 180,148,188\nQta 128,61,512\nQmvaa 184,152,188\nQta 130,61,1536\nQmvaa 188,156,188\n"
"Qta 132,61,520\nQmvaa 192,160,188\nQta 134,61,1544\nQmvaa 196,164,188\nQta 136,61,768\nQmvaa 200,168,188\nQta 138,61,1792\nQmvaa 204,172,188\n"
"Qda 188,63,36608\nQmvaa 208,144,192\nQta 140,61,776\nQmvaa 212,148,192\nQta 142,61,1800\nQmvaa 216,152,192\nQdv 28,63,34048\nQmvaa 220,156,192\n"
"Qdv 32,63,35072\nQmvaa 224,160,192\nQdv 36,63,36096\nQmvaa 228,164,192\nQdv 40,63,37120\nQmvaa 232,168,192\nQdv 44,63,38144\nQmvaa 236,172,192\n"
"Qda 192,63,37632\nQmvaa 240,144,196\nQdv 48,63,39168\nQmvaa 244,148,196\nQmvaa 248,152,196\nQmvaa 252,156,196\nQmaaa 208,160,196\nQmaaa 212,164,196\n"
"Qmaaa 216,168,196\nQmaaa 220,172,196\nQda 196,63,38656\nQmaaa 224,144,200\nQmaaa 228,148,200\nQmaaa 232,152,200\nQmaaa 236,156,200\nQmaaa 240,160,200\n"
"Qmaaa 244,164,200\nQmaaa 248,168,200\nQmaaa 252,172,200\nQda 200,63,39680\ns_waitcnt lgkmcnt(9)\nQmvav 80,112,24\nQmvav 84,116,24\ns_add_u32 s68, s71, 4096\n"
"Qmvav 88,120,24\nbuffer_store_dwordx4 a[64:67], v60, s[56:59], s68 offen offset:0 sc1\nQmvav 92,124,24\nQmvav 96,128,24\ns_add_u32 s68, s71, 18432\nQmvav 100,132,24\nbuffer_store_dwordx4 a[68:71], v60, s[56:59], s68 offen offset:0 sc1\nQmvav 104,136,24\n"
"Qmvav 108,140,24\ns_waitcnt lgkmcnt(0)\nQmvav 112,112,28\ns_waitcnt vmcnt(21)\ns_barrier\nQmvav 116,116,28\nQta 144,61,16448\ns_add_u32 m0, s62, 0\n"
"s_add_u32 s69, s87, 3145728\nQmvav 120,120,28\nbuffer_load_dwordx4 v62, s[52:55], s69 offen lds\nQmvav 124,124,28\nQta 146,61,17472\nQmvav 128,128,28\nQmvav 132,132,28\nQta 148,61,16456\n"
"s_add_u32 s69, s87, 3177472\nQmvav 136,136,28\nbuffer_load_dwordx4 v62, s[52:55], s69 offen offset:1024 lds\nQmvav 140,140,28\nQta 150,61,17480\nQmvav 144,112,32\nQmvav 148,116,32\nQta 152,61,16704\n"
"s_add_u32 s69, s87, 3209208\nQmvav 152,120,32\nbuffer_load_dwordx4 v62, s[52:55], s69 offen offset:2056 lds\nQmvav 156,124,32\nQta 154,61,17728\nQmvav 160,128,32\nQmvav 164,132,32\nQta 156,61,16712\n"
"s_add_u32 s69, s87, 3240952\nQmvav 168,136,32\nbuffer_load_dwordx4 v62, s[52:55], s69 offen offset:3080 lds\nQmvav 172,140,32\nQta 158,61,17736\nQmvav 176,112,36\nQmvav 180,116,36\nQta 160,61,16960\n"
"s_add_u32 m0, s63, 33024\ns_add_u32 s88, s86, 768\nQmvav 184,120,36\nbuffer_load_dwordx4 v64, s[48:51], s88 offen lds\nQmvav 188,124,36\nQta 162,61,17984\nQmvav 192,128,36\nQmvav 196,132,36\n"
"Qta 164,61,16968\ns_add_u32 s88, s86, 20224\nQmvav 200,136,36\nbuffer_load_dwordx4 v64, s[48:51], s88 offen offset:1024 lds\nQmvav 204,140,36\nQta 166,61,17992\nQmvav 208,112,40\nQmvav 212,116,40\n"
"Qta 168,61,17216\ns_add_u32 s88, s86, 39680\nQmvav 216,120,40\nbuffer_load_dwordx4 v64, s[48:51], s88 offen offset:2048 lds\nQmvav 220,124,40\nQta 170,61,18240\nQmvav 224,128,40\nQmvav 228,132,40\n"
"Qta 172,61,17224\ns_add_u32 s88, s86, 59136\nQmvav 232,136,40\nbuffer_load_dwordx4 v64, s[48:51], s88 offen offset:3072 lds\nQmvav 236,140,40\nQta 174,61,18248\nQmvav 240,112,44\nQmvav 244,116,44\n"
"s_add_u32 m0, s63, 37120\ns_add_u32 s88, s86, 82688\nQmvav 248,120,44\nbuffer_load_dwordx4 v64, s[48:51], s88 offen lds\nQmvav 252,124,44\nQmaav 208,128,44\nQmaav 212,132,44\ns_add_u32 s88, s86, 102144\n"
"Qmaav 216,136,44\nbuffer_load_dwordx4 v64, s[48:51], s88 offen offset:1024 lds\nQmaav 220,140,44\nQmaav 224,112,48\nQmaav 228,116,48\ns_add_u32 s88, s86, 121600\nQmaav 232,120,48\nbuffer_load_dwordx4 v64, s[48:51], s88 offen offset:2048 lds\n"
"Qmaav 236,124,48\nQmaav 240,128,48\nQmaav 244,132,48\nQmaav 248,136,48\nQmaav 252,140,48\ns_waitcnt lgkmcnt(0)\nQmvaa 80,144,176\nQmvaa 84,148,176\n"
"s_add_u32 s68, s71, 32768\nQmvaa 88,152,176\nbuffer_store_dwordx4 a[72:75], v60, s[56:59], s68 offen offset:0 sc1\nQmvaa 92,156,176\nQmvaa 96,160,176\ns_add_u32 s68, s71, 47104\nQmvaa 100,164,176\nbuffer_store_dwordx4 a[76:79], v60, s[56:59], s68 offen offset:0 sc1\n"
"Qmvaa 104,168,176\nQmvaa 108,172,176\ns_waitcnt lgkmcnt(0)\nQmvaa 112,144,180\ns_waitcnt vmcnt(21)\ns_barrier\nQmvaa 116,148,180\nQda 176,63,512\n"
"s_add_u32 m0, s62, 16448\ns_add_u32 s69, s87, 3407872\nQmvaa 120,152,180\nbuffer_load_dwordx4 v62, s[52:55], s69 offen lds\nQmvaa 124,156,180\nQta 112,61,33024\nQmvaa 128,160,180\nQta 114,61,34048\n"
"Qmvaa 132,164,180\nQta 116,61,33032\ns_add_u32 s69, s87, 3439616\nQmvaa 136,168,180\nbuffer_load_dwordx4 v62, s[52:55], s69 offen offset:1024 lds\nQmvaa 140,172,180\nQda 180,63,1536\nQmvaa 144,144,184\n"
"Qta 118,61,34056\nQmvaa 148,148,184\nQdv 24,63,0\ns_add_u32 s69, s87, 3471352\nQmvaa 152,152,184\nbuffer_load_dwordx4 v62, s[52:55], s69 offen offset:2056 lds\nQmvaa 156,156,184\nQta 120,61,33280\n"
"Qmvaa 160,160,184\nQta 122,61,34304\nQmvaa 164,164,184\nQta 124,61,33288\ns_add_u32 s69, s87, 3503096\nQmvaa 168,168,184\nbuffer_load_dwordx4 v62, s[52:55], s69 offen offset:3080 lds\nQmvaa 172,172,184\n"
"Qda 184,63,2560\nQmvaa 176,144,188\nQta 126,61,34312\nQmvaa 180,148,188\nQta 128,61,33536\nQmvaa 184,152,188\nQta 130,61,34560\nQmvaa 188,156,188\n"
"Qta 132,61,33544\nQmvaa 192,160,188\nQta 134,61,34568\nQmvaa 196,164,188\nQta 136,61,33792\nQmvaa 200,168,188\nQta 138,61,34816\nQmvaa 204,172,188\n"
"Qda 188,63,3584\nQmvaa 208,144,192\nQta 140,61,33800\nQmvaa 212,148,192\nQta 142,61,34824\nQmvaa 216,152,192\nQdv 28,63,1024\nQmvaa 220,156,192\n"
"Qdv 32,63,2048\nQmvaa 224,160,192\nQdv 36,63,3072\nQmvaa 228,164,192\nQdv 40,63,4096\nQmvaa 232,168,192\nQdv 44,63,5120\nQmvaa 236,172,192\n"
"Qda 192,63,4608\nQmvaa 240,144,196\nQdv 48,63,6144\nQmvaa 244,148,196\nQmvaa 248,152,196\nQmvaa 252,156,196\nQmaaa 208,160,196\nQmaaa 212,164,196\n"
"Qmaaa 216,168,196\nQmaaa 220,172,196\nQda 196,63,5632\nQmaaa 224,144,200\nQmaaa 228,148,200\nQmaaa 232,152,200\nQmaaa 236,156,200\nQmaaa 240,160,200\n"
"Qmaaa 244,164,200\nQmaaa 248,168,200\nQmaaa 252,172,200\nQda 200,63,6656\ns_waitcnt lgkmcnt(9)\nQmvav 80,112,24\nQmvav 84,116,24\ns_add_u32 s68, s71, 4096\n"
"Qmvav 88,120,24\nbuffer_store_dwordx4 a[80:83], v60, s[56:59], s68 offen offset:1024 sc1\nQmvav 92,124,24\nQmvav 96,128,24\ns_add_u32 s68, s71, 18432\nQmvav 100,132,24\nbuffer_store_dwordx4 a[84:87], v60, s[56:59], s68 offen offset:1024 sc1\nQmvav 104,136,24\n"
"Qmvav 108,140,24\ns_waitcnt lgkmcnt(0)\nQmvav 112,112,28\ns_waitcnt vmcnt(21)\ns_barrier\nQmvav 116,116,28\nQta 144,61,49472\ns_add_u32 m0, s62, 33024\n"
"s_add_u32 s69, s87, 3670016\nQmvav 120,120,28\nbuffer_load_dwordx4 v62, s[52:55], s69 offen lds\nQmvav 124,124,28\nQta 146,61,50496\nQmvav 128,128,28\nQmvav 132,132,28\nQta 148,61,49480\n"
"s_add_u32 s69, s87, 3701760\nQmvav 136,136,28\nbuffer_load_dwordx4 v62, s[52:55], s69 offen offset:1024 lds\nQmvav 140,140,28\nQta 150,61,50504\nQmvav 144,112,32\nQmvav 148,116,32\nQta 152,61,49728\n"
"s_add_u32 s69, s87, 3733496\nQmvav 152,120,32\nbuffer_load_dwordx4 v62, s[52:55], s69 offen offset:2056 lds\nQmvav 156,124,32\nQta 154,61,50752\nQmvav 160,128,32\nQmvav 164,132,32\nQta 156,61,49736\n"
"s_add_u32 s69, s87, 3765240\nQmvav 168,136,32\nbuffer_load_dwordx4 v62, s[52:55], s69 offen offset:3080 lds\nQmvav 172,140,32\nQta 158,61,50760\nQmvav 176,112,36\nQmvav 180,116,36\nQta 160,61,49984\n"
"s_add_u32 m0, s63, 0\ns_add_u32 s88, s86, 896\nQmvav 184,120,36\nbuffer_load_dwordx4 v64, s[48:51], s88 offen lds\nQmvav 188,124,36\nQta 162,61,51008\nQmvav 192,128,36\nQmvav 196,132,36\n"
"Qta 164,61,49992\ns_add_u32 s88, s86, 20352\nQmvav 200,136,36\nbuffer_load_dwordx4 v64, s[48:51], s88 offen offset:1024 lds\nQmvav 204,140,36\nQta 166,61,51016\nQmvav 208,112,40\nQmvav 212,116,40\n"
"Qta 168,61,50240\ns_add_u32 s88, s86, 39808\nQmvav 216,120,40\nbuffer_load_dwordx4 v64, s[48:51], s88 offen offset:2048 lds\nQmvav 220,124,40\nQta 170,61,51264\nQmvav 224,128,40\nQmvav 228,132,40\n"
"Qta 172,61,50248\ns_add_u32 s88, s86, 59264\nQmvav 232,136,40\nbuffer_load_dwordx4 v64, s[48:51], s88 offen offset:3072 lds\nQmvav 236,140,40\nQta 174,61,51272\nQmvav 240,112,44\nQmvav 244,116,44\n"
"s_add_u32 m0, s63, 4096\ns_add_u32 s88, s86, 82816\nQmvav 248,120,44\nbuffer_load_dwordx4 v64, s[48:51], s88 offen lds\nQmvav 252,124,44\nQmaav 208,128,44\nQmaav 212,132,44\ns_add_u32 s88, s86, 102272\n"
"Qmaav 216,136,44\nbuffer_load_dwordx4 v64, s[48:51], s88 offen offset:1024 lds\nQmaav 220,140,44\nQmaav 224,112,48\nQmaav 228,116,48\ns_add_u32 s88, s86, 121728\nQmaav 232,120,48\nbuffer_load_dwordx4 v64, s[48:51], s88 offen offset:2048 lds\n"
"Qmaav 236,124,48\nQmaav 240,128,48\nQmaav 244,132,48\nQmaav 248,136,48\nQmaav 252,140,48\ns_waitcnt lgkmcnt(0)\nQmvaa 80,144,176\nQmvaa 84,148,176\n"
"s_add_u32 s68, s71, 32768\nQmvaa 88,152,176\nbuffer_store_dwordx4 a[88:91], v60, s[56:59], s68 offen offset:1024 sc1\nQmvaa 92,156,176\nQmvaa 96,160,176\ns_add_u32 s68, s71, 47104\nQmvaa 100,164,176\nbuffer_store_dwordx4 a[92:95], v60, s[56:59], s68 offen offset:1024 sc1\n"
"Qmvaa 104,168,176\nQmvaa 108,172,176\ns_waitcnt lgkmcnt(0)\nQmvaa 112,144,180\ns_waitcnt vmcnt(21)\ns_barrier\nQmvaa 116,148,180\nQda 176,63,33536\n"
"s_add_u32 m0, s62, 49472\ns_add_u32 s69, s87, 3932160\nQmvaa 120,152,180\nbuffer_load_dwordx4 v62, s[52:55], s69 offen lds\nQmvaa 124,156,180\nQta 112,61,0\nQmvaa 128,160,180\nQta 114,61,1024\n"
"Qmvaa 132,164,180\n.Lpg2:\ns_cbranch_execz .Lpg6\nQta 116,61,8\ns_add_u32 s69, s87, 3963904\nQmvaa 136,168,180\nbuffer_load_dwordx4 v62, s[52:55], s69 offen offset:1024 lds\nQmvaa 140,172,180\n"
"Qda 180,63,34560\nQmvaa 144,144,184\nQta 118,61,1032\nQmvaa 148,148,184\nQdv 24,63,33024\ns_add_u32 s69, s87, 3995640\nQmvaa 152,152,184\nbuffer_load_dwordx4 v62, s[52:55], s69 offen offset:2056 lds\n"
"Qmvaa 156,156,184\nQta 120,61,256\nQmvaa 160,160,184\nQta 122,61,1280\nQmvaa 164,164,184\nQta 124,61,264\ns_add_u32 s69, s87, 4027384\nQmvaa 168,168,184\n"
"buffer_load_dwordx4 v62, s[52:55], s69 offen offset:3080 lds\nQmvaa 172,172,184\nQda 184,63,35584\nQmvaa 176,144,188\nQta 126,61,1288\nQmvaa 180,148,188\nQta 128,61,512\nQmvaa 184,152,188\n"
"Qta 130,61,1536\nQmvaa 188,156,188\nQta 132,61,520\nQmvaa 192,160,188\nQta 134,61,1544\nQmvaa 196,164,188\nQta 136,61,768\nQmvaa 200,168,188\n"
"Qta 138,61,1792\nQmvaa 204,172,188\nQda 188,63,36608\nQmvaa 208,144,192\nQta 140,61,776\nQmvaa 212,148,192\nQta 142,61,1800\nQmvaa 216,152,192\n"
"Qdv 28,63,34048\nQmvaa 220,156,192\nQdv 32,63,35072\nQmvaa 224,160,192\nQdv 36,63,36096\nQmvaa 228,164,192\nQdv 40,63,37120\nQmvaa 232,168,192\n"
"Qdv 44,63,38144\nQmvaa 236,172,192\nQda 192,63,37632\nQmvaa 240,144,196\nQdv 48,63,39168\nQmvaa 244,148,196\nQmvaa 248,152,196\nQmvaa 252,156,196\n"
"Qmaaa 208,160,196\nQmaaa 212,164,196\nQmaaa 216,168,196\nQmaaa 220,172,196\nQda 196,63,38656\nQmaaa 224,144,200\nQmaaa 228,148,200\nQmaaa 232,152,200\n"
"Qmaaa 236,156,200\nQmaaa 240,160,200\nQmaaa 244,164,200\nQmaaa 248,168,200\nQmaaa 252,172,200\nQda 200,63,39680\ns_waitcnt lgkmcnt(9)\nQmvav 80,112,24\n"
"Qmvav 84,116,24\ns_add_u32 s68, s71, 4096\nQmvav 88,120,24\nbuffer_store_dwordx4 a[96:99], v60, s[56:59], s68 offen offset:2048 sc1\nQmvav 92,124,24\nQmvav 96,128,24\ns_add_u32 s68, s71, 18432\nQmvav 100,132,24\n"
"buffer_store_dwordx4 a[100:103], v60, s[56:59], s68 offen offset:2048 sc1\nQmvav 104,136,24\nQmvav 108,140,24\ns_waitcnt lgkmcnt(0)\nQmvav 112,112,28\ns_waitcnt vmcnt(21)\ns_barrier\nQmvav 116,116,28\n"
"Qta 144,61,16448\ns_add_u32 m0, s62, 0\ns_add_u32 s69, s87, 4194304\nQmvav 120,120,28\nbuffer_load_dwordx4 v62, s[52:55], s69 offen lds\nQmvav 124,124,28\nQta 146,61,17472\nQmvav 128,128,28\n"
"Qmvav 132,132,28\nQta 148,61,16456\ns_add_u32 s69, s87, 4226048\nQmvav 136,136,28\nbuffer_load_dwordx4 v62, s[52:55], s69 offen offset:1024 lds\nQmvav 140,140,28\nQta 150,61,17480\nQmvav 144,112,32\n"
"Qmvav 148,116,32\nQta 152,61,16704\ns_add_u32 s69, s87, 4257784\nQmvav 152,120,32\nbuffer_load_dwordx4 v62, s[52:55], s69 offen offset:2056 lds\nQmvav 156,124,32\nQta 154,61,17728\nQmvav 160,128,32\n"
"Qmvav 164,132,32\nQta 156,61,16712\ns_add_u32 s69, s87, 4289528\nQmvav 168,136,32\nbuffer_load_dwordx4 v62, s[52:55], s69 offen offset:3080 lds\nQmvav 172,140,32\nQta 158,61,17736\nQmvav 176,112,36\n"
"Qmvav 180,116,36\nQta 160,61,16960\ns_add_u32 m0, s63, 33024\ns_add_u32 s88, s86, 1024\nQmvav 184,120,36\nbuffer_load_dwordx4 v64, s[48:51], s88 offen lds\nQmvav 188,124,36\nQta 162,61,17984\n"
"Qmvav 192,128,36\nQmvav 196,132,36\nQta 164,61,16968\ns_add_u32 s88, s86, 20480\nQmvav 200,136,36\nbuffer_load_dwordx4 v64, s[48:51], s88 offen offset:1024 lds\nQmvav 204,140,36\nQta 166,61,17992\n"
"Qmvav 208,112,40\nQmvav 212,116,40\nQta 168,61,17216\ns_add_u32 s88, s86, 39936\nQmvav 216,120,40\nbuffer_load_dwordx4 v64, s[48:51], s88 offen offset:2048 lds\nQmvav 220,124,40\nQta 170,61,18240\n"
"Qmvav 224,128,40\nQmvav 228,132,40\nQta 172,61,17224\ns_add_u32 s88, s86, 59392\nQmvav 232,136,40\nbuffer_load_dwordx4 v64, s[48:51], s88 offen offset:3072 lds\nQmvav 236,140,40\nQta 174,61,18248\n"
"Qmvav 240,112,44\nQmvav 244,116,44\ns_add_u32 m0, s63, 37120\ns_add_u32 s88, s86, 82944\nQmvav 248,120,44\nbuffer_load_dwordx4 v64, s[48:51], s88 offen lds\nQmvav 252,124,44\nQmaav 208,128,44\n"
"Qmaav 212,132,44\ns_add_u32 s88, s86, 102400\nQmaav 216,136,44\nbuffer_load_dwordx4 v64, s[48:51], s88 offen offset:1024 lds\nQmaav 220,140,44\nQmaav 224,112,48\nQmaav 228,116,48\ns_add_u32 s88, s86, 121856\n"
"Qmaav 232,120,48\nbuffer_load_dwordx4 v64, s[48:51], s88 offen offset:2048 lds\nQmaav 236,124,48\nQmaav 240,128,48\nQmaav 244,132,48\nQmaav 248,136,48\nQmaav 252,140,48\ns_waitcnt lgkmcnt(0)\n"
"Qmvaa 80,144,176\nQmvaa 84,148,176\ns_add_u32 s68, s71, 32768\nQmvaa 88,152,176\nbuffer_store_dwordx4 a[104:107], v60, s[56:59], s68 offen offset:2048 sc1\nQmvaa 92,156,176\nQmvaa 96,160,176\ns_add_u32 s68, s71, 47104\n"
"Qmvaa 100,164,176\nbuffer_store_dwordx4 a[108:111], v60, s[56:59], s68 offen offset:2048 sc1\nQmvaa 104,168,176\nQmvaa 108,172,176\ns_waitcnt lgkmcnt(0)\nQmvaa 112,144,180\ns_waitcnt vmcnt(21)\ns_barrier\n"
"Qmvaa 116,148,180\nQda 176,63,512\ns_add_u32 m0, s62, 16448\ns_add_u32 s69, s87, 4456448\nQmvaa 120,152,180\nbuffer_load_dwordx4 v62, s[52:55], s69 offen lds\nQmvaa 124,156,180\nQta 112,61,33024\n"
"Qmvaa 128,160,180\nQta 114,61,34048\nQmvaa 132,164,180\nQta 116,61,33032\ns_add_u32 s69, s87, 4488192\nQmvaa 136,168,180\nbuffer_load_dwordx4 v62, s[52:55], s69 offen offset:1024 lds\nQmvaa 140,172,180\n"
"Qda 180,63,1536\nQmvaa 144,144,184\nQta 118,61,34056\nQmvaa 148,148,184\nQdv 24,63,0\ns_add_u32 s69, s87, 4519928\nQmvaa 152,152,184\nbuffer_load_dwordx4 v62, s[52:55], s69 offen offset:2056 lds\n"
"Qmvaa 156,156,184\nQta 120,61,33280\nQmvaa 160,160,184\nQta 122,61,34304\nQmvaa 164,164,184\nQta 124,61,33288\ns_add_u32 s69, s87, 4551672\nQmvaa 168,168,184\n"
"buffer_load_dwordx4 v62, s[52:55], s69 offen offset:3080 lds\nQmvaa 172,172,184\nQda 184,63,2560\nQmvaa 176,144,188\nQta 126,61,34312\nQmvaa 180,148,188\nQta 128,61,33536\nQmvaa 184,152,188\n"
"Qta 130,61,34560\nQmvaa 188,156,188\nQta 132,61,33544\nQmvaa 192,160,188\nQta 134,61,34568\nQmvaa 196,164,188\nQta 136,61,33792\nQmvaa 200,168,188\n"
"Qta 138,61,34816\nQmvaa 204,172,188\nQda 188,63,3584\nQmvaa 208,144,192\nQta 140,61,33800\nQmvaa 212,148,192\nQta 142,61,34824\nQmvaa 216,152,192\n"
"Qdv 28,63,1024\nQmvaa 220,156,192\nQdv 32,63,2048\nQmvaa 224,160,192\nQdv 36,63,3072\nQmvaa 228,164,192\nQdv 40,63,4096\nQmvaa 232,168,192\n"
"Qdv 44,63,5120\nQmvaa 236,172,192\nQda 192,63,4608\nQmvaa 240,144,196\nQdv 48,63,6144\nQmvaa 244,148,196\nQmvaa 248,152,196\nQmvaa 252,156,196\n"
"Qmaaa 208,160,196\nQmaaa 212,164,196\nQmaaa 216,168,196\nQmaaa 220,172,196\nQda 196,63,5632\nQmaaa 224,144,200\nQmaaa 228,148,200\nQmaaa 232,152,200\n"
"Qmaaa 236,156,200\nQmaaa 240,160,200\nQmaaa 244,164,200\nQmaaa 248,168,200\nQmaaa 252,172,200\nQda 200,63,6656\ns_waitcnt lgkmcnt(9)\nQmvav 80,112,24\n"
"Qmvav 84,116,24\nQmvav 88,120,24\nQmvav 92,124,24\nQmvav 96,128,24\nQmvav 100,132,24\nQmvav 104,136,24\nQmvav 108,140,24\ns_waitcnt lgkmcnt(0)\n"
"Qmvav 112,112,28\ns_waitcnt vmcnt(19)\ns_barrier\nQmvav 116,116,28\nQta 144,61,49472\ns_add_u32 m0, s62, 33024\ns_add_u32 s69, s87, 4718592\nQmvav 120,120,28\n"
"buffer_load_dwordx4 v62, s[52:55], s69 offen lds\nQmvav 124,124,28\nQta 146,61,50496\nQmvav 128,128,28\nQmvav 132,132,28\nQta 148,61,49480\ns_add_u32 s69, s87, 4750336\nQmvav 136,136,28\n"
"buffer_load_dwordx4 v62, s[52:55], s69 offen offset:1024 lds\nQmvav 140,140,28\nQta 150,61,50504\nQmvav 144,112,32\nQmvav 148,116,32\nQta 152,61,49728\ns_add_u32 s69, s87, 4782072\nQmvav 152,120,32\n"
"buffer_load_dwordx4 v62, s[52:55], s69 offen offset:2056 lds\nQmvav 156,124,32\nQta 154,61,50752\nQmvav 160,128,32\nQmvav 164,132,32\nQta 156,61,49736\ns_add_u32 s69, s87, 4813816\nQmvav 168,136,32\n"
"buffer_load_dwordx4 v62, s[52:55], s69 offen offset:3080 lds\nQmvav 172,140,32\nQta 158,61,50760\nQmvav 176,112,36\nQmvav 180,116,36\nQta 160,61,49984\ns_add_u32 m0, s63, 0\ns_add_u32 s88, s86, 1152\n"
"Qmvav 184,120,36\nbuffer_load_dwordx4 v64, s[48:51], s88 offen lds\nQmvav 188,124,36\nQta 162,61,51008\nQmvav 192,128,36\nQmvav 196,132,36\nQta 164,61,49992\ns_add_u32 s88, s86, 20608\n"
"Qmvav 200,136,36\nbuffer_load_dwordx4 v64, s[48:51], s88 offen offset:1024 lds\nQmvav 204,140,36\nQta 166,61,51016\nQmvav 208,112,40\nQmvav 212,116,40\nQta 168,61,50240\ns_add_u32 s88, s86, 40064\n"
"Qmvav 216,120,40\nbuffer_load_dwordx4 v64, s[48:51], s88 offen offset:2048 lds\nQmvav 220,124,40\nQta 170,61,51264\nQmvav 224,128,40\nQmvav 228,132,40\nQta 172,61,50248\ns_add_u32 s88, s86, 59520\n"
"Qmvav 232,136,40\nbuffer_load_dwordx4 v64, s[48:51], s88 offen offset:3072 lds\nQmvav 236,140,40\nQta 174,61,51272\nQmvav 240,112,44\nQmvav 244,116,44\ns_add_u32 m0, s63, 4096\ns_add_u32 s88, s86, 83072\n"
"Qmvav 248,120,44\nbuffer_load_dwordx4 v64, s[48:51], s88 offen lds\nQmvav 252,124,44\nQmaav 208,128,44\nQmaav 212,132,44\ns_add_u32 s88, s86, 102528\nQmaav 216,136,44\nbuffer_load_dwordx4 v64, s[48:51], s88 offen offset:1024 lds\n"
"Qmaav 220,140,44\nQmaav 224,112,48\nQmaav 228,116,48\ns_add_u32 s88, s86, 121984\nQmaav 232,120,48\nbuffer_load_dwordx4 v64, s[48:51], s88 offen offset:2048 lds\nQmaav 236,124,48\nQmaav 240,128,48\n"
"Qmaav 244,132,48\nQmaav 248,136,48\nQmaav 252,140,48\ns_waitcnt lgkmcnt(0)\nQmvaa 80,144,176\nQmvaa 84,148,176\nQmvaa 88,152,176\nQmvaa 92,156,176\n"
"Qmvaa 96,160,176\nQmvaa 100,164,176\nQmvaa 104,168,176\nQmvaa 108,172,176\ns_waitcnt lgkmcnt(0)\nQmvaa 112,144,180\ns_waitcnt vmcnt(17)\ns_barrier\n"
"Qmvaa 116,148,180\nQda 176,63,33536\ns_add_u32 m0, s62, 49472\ns_add_u32 s69, s87, 4980736\nQmvaa 120,152,180\nbuffer_load_dwordx4 v62, s[52:55], s69 offen lds\nQmvaa 124,156,180\nQta 112,61,0\n"
"Qmvaa 128,160,180\nQta 114,61,1024\nQmvaa 132,164,180\nQta 116,61,8\ns_add_u32 s69, s87, 5012480\nQmvaa 136,168,180\nbuffer_load_dwordx4 v62, s[52:55], s69 offen offset:1024 lds\nQmvaa 140,172,180\n"
"Qda 180,63,34560\nQmvaa 144,144,184\nQta 118,61,1032\nQmvaa 148,148,184\nQdv 24,63,33024\ns_add_u32 s69, s87, 5044216\nQmvaa 152,152,184\nbuffer_load_dwordx4 v62, s[52:55], s69 offen offset:2056 lds\n"
"Qmvaa 156,156,184\nQta 120,61,256\nQmvaa 160,160,184\nQta 122,61,1280\nQmvaa 164,164,184\nQta 124,61,264\ns_add_u32 s69, s87, 5075960\nQmvaa 168,168,184\n"
"buffer_load_dwordx4 v62, s[52:55], s69 offen offset:3080 lds\nQmvaa 172,172,184\nQda 184,63,35584\nQmvaa 176,144,188\nQta 126,61,1288\nQmvaa 180,148,188\nQta 128,61,512\nQmvaa 184,152,188\n"
"Qta 130,61,1536\nQmvaa 188,156,188\nQta 132,61,520\nQmvaa 192,160,188\nQta 134,61,1544\nQmvaa 196,164,188\nQta 136,61,768\nQmvaa 200,168,188\n"
"Qta 138,61,1792\nQmvaa 204,172,188\nQda 188,63,36608\nQmvaa 208,144,192\nQta 140,61,776\nQmvaa 212,148,192\nQta 142,61,1800\nQmvaa 216,152,192\n"
"Qdv 28,63,34048\nQmvaa 220,156,192\nQdv 32,63,35072\nQmvaa 224,160,192\nQdv 36,63,36096\nQmvaa 228,164,192\nQdv 40,63,37120\nQmvaa 232,168,192\n"
"Qdv 44,63,38144\nQmvaa 236,172,192\nQda 192,63,37632\nQmvaa 240,144,196\nQdv 48,63,39168\nQmvaa 244,148,196\nQmvaa 248,152,196\nQmvaa 252,156,196\n"
"Qmaaa 208,160,196\nQmaaa 212,164,196\nQmaaa 216,168,196\nQmaaa 220,172,196\nQda 196,63,38656\nQmaaa 224,144,200\nQmaaa 228,148,200\nQmaaa 232,152,200\n"
"Qmaaa 236,156,200\nQmaaa 240,160,200\nQmaaa 244,164,200\nQmaaa 248,168,200\nQmaaa 252,172,200\nQda 200,63,39680\ns_waitcnt lgkmcnt(9)\nQmvav 80,112,24\n"
"Qmvav 84,116,24\nQmvav 88,120,24\nQmvav 92,124,24\nQmvav 96,128,24\nQmvav 100,132,24\nQmvav 104,136,24\nQmvav 108,140,24\ns_waitcnt lgkmcnt(0)\n"
"Qmvav 112,112,28\ns_waitcnt vmcnt(15)\ns_barrier\nQmvav 116,116,28\nQta 144,61,16448\ns_mov_b32 s56, s94\ns_mov_b32 s57, s95\ns_mov_b32 s58, s75\n"
"s_mov_b32 s94, 0\ns_add_u32 m0, %10, 160768\nQmvav 120,120,28\nbuffer_load_dwordx4 v60, s[56:59], s94 offen lds\nQmvav 124,124,28\nQta 146,61,17472\nQmvav 128,128,28\nQmvav 132,132,28\n"
"Qta 148,61,16456\ns_add_u32 s94, s94, 4096\nQmvav 136,136,28\nbuffer_load_dwordx4 v60, s[56:59], s94 offen lds\nQmvav 140,140,28\nQta 150,61,17480\nQmvav 144,112,32\nQmvav 148,116,32\n"
"Qta 152,61,16704\ns_add_u32 s94, s94, 4096\nQmvav 152,120,32\nbuffer_load_dwordx4 v60, s[56:59], s94 offen lds\nQmvav 156,124,32\nQta 154,61,17728\nQmvav 160,128,32\nQmvav 164,132,32\n"
".Lpg3:\ns_cbranch_execz .Lpg7\nQta 156,61,16712\ns_add_u32 s94, s94, 4096\nQmvav 168,136,32\nbuffer_load_dwordx4 v60, s[56:59], s94 offen lds\nQmvav 172,140,32\nQta 158,61,17736\n"
"Qmvav 176,112,36\nQmvav 180,116,36\nQta 160,61,16960\ns_add_u32 s94, s94, 4096\nQmvav 184,120,36\nbuffer_load_dwordx4 v60, s[56:59], s94 offen lds\nQmvav 188,124,36\nQta 162,61,17984\n"
"Qmvav 192,128,36\nQmvav 196,132,36\nQta 164,61,16968\ns_add_u32 s94, s94, 4096\nQmvav 200,136,36\nbuffer_load_dwordx4 v60, s[56:59], s94 offen lds\nQmvav 204,140,36\nQta 166,61,17992\n"
"Qmvav 208,112,40\nQmvav 212,116,40\nQta 168,61,17216\ns_add_u32 s94, s94, 4096\nQmvav 216,120,40\nbuffer_load_dwordx4 v60, s[56:59], s94 offen lds\nQmvav 220,124,40\nQta 170,61,18240\n"
"Qmvav 224,128,40\nQmvav 228,132,40\nQta 172,61,17224\ns_add_u32 s94, s94, 4096\nQmvav 232,136,40\nbuffer_load_dwordx4 v60, s[56:59], s94 offen lds\nQmvav 236,140,40\nQta 174,61,18248\n"
"Qmvav 240,112,44\nQmvav 244,116,44\ns_add_u32 s94, s94, 4096\nQmvav 248,120,44\nbuffer_load_dwordx4 v60, s[56:59], s94 offen lds\nQmvav 252,124,44\nQmaav 208,128,44\nQmaav 212,132,44\n"
"s_add_u32 s94, s94, 4096\nQmaav 216,136,44\nbuffer_load_dwordx4 v60, s[56:59], s94 offen lds\nQmaav 220,140,44\nQmaav 224,112,48\nQmaav 228,116,48\nQmaav 232,120,48\nQmaav 236,124,48\n"
"Qmaav 240,128,48\nQmaav 244,132,48\nQmaav 248,136,48\nQmaav 252,140,48\ns_waitcnt lgkmcnt(0)\nQmvaa 80,144,176\nQmvaa 84,148,176\nQmvaa 88,152,176\n"
"Qmvaa 92,156,176\nQmvaa 96,160,176\nQmvaa 100,164,176\nQmvaa 104,168,176\nQmvaa 108,172,176\ns_waitcnt lgkmcnt(0)\nQmvaa 112,144,180\ns_waitcnt vmcnt(14)\n"
"s_barrier\nQmvaa 116,148,180\nQda 176,63,512\ns_add_u32 m0, s61, 33024\nQmvaa 120,152,180\nbuffer_load_dwordx4 v56, s[40:43], s60 offen nt lds\nQmvaa 124,156,180\nQta 112,61,33024\n"
"Qmvaa 128,160,180\nQta 114,61,34048\nQmvaa 132,164,180\nQta 116,61,33032\nQmvaa 136,168,180\nbuffer_load_dwordx4 v57, s[40:43], s60 offen offset:1024 nt lds\nQmvaa 140,172,180\nQda 180,63,1536\n"
"Qmvaa 144,144,184\nQta 118,61,34056\nQmvaa 148,148,184\nQdv 24,63,0\nQmvaa 152,152,184\nbuffer_load_dwordx4 v56, s[40:43], s60 offen offset:2048 nt lds\nQmvaa 156,156,184\nQta 120,61,33280\n"
"Qmvaa 160,160,184\nQta 122,61,34304\nQmvaa 164,164,184\nQta 124,61,33288\nQmvaa 168,168,184\nbuffer_load_dwordx4 v57, s[40:43], s60 offen offset:3072 nt lds\nQmvaa 172,172,184\nQda 184,63,2560\n"
"Qmvaa 176,144,188\nQta 126,61,34312\nQmvaa 180,148,188\nQta 128,61,33536\ns_add_u32 m0, s61, 41216\nQmvaa 184,152,188\nbuffer_load_dwordx4 v58, s[44:47], s60 offen nt lds\nQmvaa 188,156,188\n"
"Qta 130,61,34560\nQmvaa 192,160,188\nQta 132,61,33544\nQmvaa 196,164,188\nQta 134,61,34568\nQmvaa 200,168,188\nbuffer_load_dwordx4 v59, s[44:47], s60 offen offset:1024 nt lds\nQmvaa 204,172,188\n"
"Qda 188,63,3584\nQmvaa 208,144,192\nQta 136,61,33792\nQmvaa 212,148,192\nQta 138,61,34816\nQmvaa 216,152,192\nbuffer_load_dwordx4 v58, s[44:47], s60 offen offset:2048 nt lds\nQmvaa 220,156,192\n"
"Qta 140,61,33800\nQmvaa 224,160,192\nQta 142,61,34824\nQmvaa 228,164,192\nQdv 28,63,1024\nQmvaa 232,168,192\nbuffer_load_dwordx4 v59, s[44:47], s60 offen offset:3072 nt lds\ns_add_u32 s60, s60, 8192\n"
"s_add_u32 s88, s60, 8192\nQmvaa 236,172,192\nQda 192,63,4608\nQmvaa 240,144,196\nQdv 32,63,2048\nQmvaa 244,148,196\nQdv 36,63,3072\nQmvaa 248,152,196\n"
"Qdv 40,63,4096\nQmvaa 252,156,196\nQdv 44,63,5120\nQmaaa 208,160,196\nQdv 48,63,6144\nQmaaa 212,164,196\nQmaaa 216,168,196\nQmaaa 220,172,196\n"
"Qda 196,63,5632\nQmaaa 224,144,200\nQmaaa 228,148,200\nQmaaa 232,152,200\nQmaaa 236,156,200\nQmaaa 240,160,200\nQmaaa 244,164,200\nQmaaa 248,168,200\n"
"Qmaaa 252,172,200\nQda 200,63,6656\ns_waitcnt lgkmcnt(9)\nQmvav 80,112,24\nQmvav 84,116,24\nQmvav 88,120,24\nQmvav 92,124,24\nQmvav 96,128,24\n"
"Qmvav 100,132,24\nQmvav 104,136,24\nQmvav 108,140,24\ns_waitcnt lgkmcnt(0)\nQmvav 112,112,28\ns_waitcnt vmcnt(18)\ns_barrier\nQmvav 116,116,28\n"
"Qta 144,61,49472\nQmvav 120,120,28\nQmvav 124,124,28\nQta 146,61,50496\nQmvav 128,128,28\nQmvav 132,132,28\nQta 148,61,49480\nQmvav 136,136,28\n"
"Qmvav 140,140,28\nQta 150,61,50504\nQmvav 144,112,32\nQmvav 148,116,32\nQta 152,61,49728\nQmvav 152,120,32\nQmvav 156,124,32\nQta 154,61,50752\n"
"Qmvav 160,128,32\nQmvav 164,132,32\nQta 156,61,49736\nQmvav 168,136,32\nQmvav 172,140,32\nQta 158,61,50760\nQmvav 176,112,36\nQmvav 180,116,36\n"
"Qta 160,61,49984\nQmvav 184,120,36\nQmvav 188,124,36\nQta 162,61,51008\nQmvav 192,128,36\nQmvav 196,132,36\nQta 164,61,49992\nQmvav 200,136,36\n"
"Qmvav 204,140,36\nQta 166,61,51016\nQmvav 208,112,40\nQmvav 212,116,40\nQta 168,61,50240\nQmvav 216,120,40\nQmvav 220,124,40\nQta 170,61,51264\n"
"Qmvav 224,128,40\nQmvav 228,132,40\nQta 172,61,50248\nQmvav 232,136,40\nQmvav 236,140,40\nQta 174,61,51272\nQmvav 240,112,44\nQmvav 244,116,44\n"
"Qmvav 248,120,44\nQmvav 252,124,44\nQmaav 208,128,44\nQmaav 212,132,44\nQmaav 216,136,44\nQmaav 220,140,44\nQmaav 224,112,48\nQmaav 228,116,48\n"
"Qmaav 232,120,48\nQmaav 236,124,48\nQmaav 240,128,48\nQmaav 244,132,48\nQmaav 248,136,48\nQmaav 252,140,48\ns_waitcnt lgkmcnt(0)\nQmvaa 80,144,176\n"
"Qmvaa 84,148,176\nQmvaa 88,152,176\nQmvaa 92,156,176\nQmvaa 96,160,176\nQmvaa 100,164,176\nQmvaa 104,168,176\nQmvaa 108,172,176\ns_waitcnt lgkmcnt(0)\n"
"Qmvaa 112,144,180\ns_waitcnt vmcnt(63)\ns_barrier\nQmvaa 116,148,180\nQda 112,52,0\ns_add_u32 m0, s61, 66048\nv_accvgpr_write_b32 a176, 0\nQmvaa 120,152,180\n"
"buffer_load_dwordx4 v56, s[40:43], s60 offen nt lds\nQmvaa 124,156,180\nQda 116,52,512\nv_accvgpr_write_b32 a177, 0\nQmvaa 128,160,180\nQmvaa 132,164,180\nQda 120,52,1024\nv_accvgpr_write_b32 a178, 0\n"
"Qmvaa 136,168,180\nbuffer_load_dwordx4 v57, s[40:43], s60 offen offset:1024 nt lds\nQmvaa 140,172,180\nQda 124,52,1536\nv_accvgpr_write_b32 a179, 0\nQmvaa 144,144,184\nQmvaa 148,148,184\nQda 128,52,2048\n"
"v_accvgpr_write_b32 a180, 0\nQmvaa 152,152,184\nbuffer_load_dwordx4 v56, s[40:43], s60 offen offset:2048 nt lds\nQmvaa 156,156,184\nQda 132,52,2560\nv_accvgpr_write_b32 a181, 0\nQmvaa 160,160,184\nQmvaa 164,164,184\n"
"Qda 136,52,3072\nv_accvgpr_write_b32 a182, 0\nQmvaa 168,168,184\nbuffer_load_dwordx4 v57, s[40:43], s60 offen offset:3072 nt lds\nQmvaa 172,172,184\nQda 140,52,3584\nv_accvgpr_write_b32 a183, 0\nQmvaa 176,144,188\n"
"Qmvaa 180,148,188\ns_add_u32 m0, s61, 74240\nv_accvgpr_write_b32 a184, 0\nQmvaa 184,152,188\nbuffer_load_dwordx4 v58, s[44:47], s60 offen nt lds\nQmvaa 188,156,188\nv_accvgpr_write_b32 a185, 0\nQmvaa 192,160,188\n"
"Qmvaa 196,164,188\nv_accvgpr_write_b32 a186, 0\nQmvaa 200,168,188\nbuffer_load_dwordx4 v59, s[44:47], s60 offen offset:1024 nt lds\nQmvaa 204,172,188\nv_accvgpr_write_b32 a187, 0\nQmvaa 208,144,192\nQmvaa 212,148,192\n"
"v_accvgpr_write_b32 a188, 0\nQmvaa 216,152,192\nbuffer_load_dwordx4 v58, s[44:47], s60 offen offset:2048 nt lds\nQmvaa 220,156,192\nv_accvgpr_write_b32 a189, 0\nQmvaa 224,160,192\nQmvaa 228,164,192\nv_accvgpr_write_b32 a190, 0\n"
"Qmvaa 232,168,192\nbuffer_load_dwordx4 v59, s[44:47], s60 offen offset:3072 nt lds\ns_add_u32 s60, s60, 8192\nQmvaa 236,172,192\nv_accvgpr_write_b32 a191, 0\nQmvaa 240,144,196\nQmvaa 244,148,196\nv_accvgpr_write_b32 a192, 0\n"
"Qmvaa 248,152,196\nQmvaa 252,156,196\nv_accvgpr_write_b32 a193, 0\nQmaaa 208,160,196\nQmaaa 212,164,196\nv_accvgpr_write_b32 a194, 0\nQmaaa 216,168,196\nQmaaa 220,172,196\n"
"v_accvgpr_write_b32 a195, 0\nQmaaa 224,144,200\nQmaaa 228,148,200\nv_accvgpr_write_b32 a196, 0\nQmaaa 232,152,200\nQmaaa 236,156,200\nv_accvgpr_write_b32 a197, 0\nQmaaa 240,160,200\n"
"Qmaaa 244,164,200\nv_accvgpr_write_b32 a198, 0\nQmaaa 248,168,200\nQmaaa 252,172,200\nv_accvgpr_write_b32 a199, 0\nQta 144,54,0\nQta 146,54,2048\nQta 148,54,256\n"
"Qta 150,54,2304\nQta 152,54,512\nQta 154,54,2560\nQta 156,54,768\nQta 158,54,2816\nQta 160,54,1024\nQta 162,54,3072\nQta 164,54,1280\n"
"Qta 166,54,3328\nQta 168,54,1536\nQta 170,54,3584\nQta 172,54,1792\nQta 174,54,3840\nv_pk_mul_f32 v[80:81], v[80:81], s[72:73] op_sel_hi:[1,0]\nv_pk_mul_f32 v[82:83], v[82:83], s[72:73] op_sel_hi:[1,0]\nv_pk_mul_f32 v[84:85], v[84:85], s[72:73] op_sel_hi:[1,0]\n"
"v_pk_mul_f32 v[86:87], v[86:87], s[72:73] op_sel_hi:[1,0]\nQk 80,80,81\nQk 81,82,83\nQk 82,84,85\nQk 83,86,87\nv_accvgpr_write_b32 a0, v80\nv_accvgpr_write_b32 a1, v81\nv_accvgpr_write_b32 a2, v82\n"
"v_accvgpr_write_b32 a3, v83\nv_pk_mul_f32 v[88:89], v[88:89], s[72:73] op_sel_hi:[1,0]\nv_pk_mul_f32 v[90:91], v[90:91], s[72:73] op_sel_hi:[1,0]\nv_pk_mul_f32 v[92:93], v[92:93], s[72:73] op_sel_hi:[1,0]\nv_pk_mul_f32 v[94:95], v[94:95], s[72:73] op_sel_hi:[1,0]\nQk 88,88,89\nQk 89,90,91\nQk 90,92,93\n"
"Qk 91,94,95\nv_accvgpr_write_b32 a4, v88\nv_accvgpr_write_b32 a5, v89\nv_accvgpr_write_b32 a6, v90\nv_accvgpr_write_b32 a7, v91\nv_pk_mul_f32 v[96:97], v[96:97], s[72:73] op_sel_hi:[1,0]\nv_pk_mul_f32 v[98:99], v[98:99], s[72:73] op_sel_hi:[1,0]\nv_pk_mul_f32 v[100:101], v[100:101], s[72:73] op_sel_hi:[1,0]\n"
"v_pk_mul_f32 v[102:103], v[102:103], s[72:73] op_sel_hi:[1,0]\nQk 96,96,97\nQk 97,98,99\nQk 98,100,101\nQk 99,102,103\nv_accvgpr_write_b32 a8, v96\nv_accvgpr_write_b32 a9, v97\nv_accvgpr_write_b32 a10, v98\n"
"v_accvgpr_write_b32 a11, v99\nv_pk_mul_f32 v[104:105], v[104:105], s[72:73] op_sel_hi:[1,0]\nv_pk_mul_f32 v[106:107], v[106:107], s[72:73] op_sel_hi:[1,0]\nv_pk_mul_f32 v[108:109], v[108:109], s[72:73] op_sel_hi:[1,0]\nv_pk_mul_f32 v[110:111], v[110:111], s[72:73] op_sel_hi:[1,0]\nQk 104,104,105\nQk 105,106,107\nQk 106,108,109\n"
"Qk 107,110,111\nv_accvgpr_write_b32 a12, v104\nv_accvgpr_write_b32 a13, v105\nv_accvgpr_write_b32 a14, v106\nv_accvgpr_write_b32 a15, v107\nv_pk_mul_f32 v[112:113], v[112:113], s[72:73] op_sel_hi:[1,0]\nv_pk_mul_f32 v[114:115], v[114:115], s[72:73] op_sel_hi:[1,0]\nv_pk_mul_f32 v[116:117], v[116:117], s[72:73] op_sel_hi:[1,0]\n"
"v_pk_mul_f32 v[118:119], v[118:119], s[72:73] op_sel_hi:[1,0]\nQk 112,112,113\nQk 113,114,115\nQk 114,116,117\nQk 115,118,119\nv_accvgpr_write_b32 a16, v112\nv_accvgpr_write_b32 a17, v113\nv_accvgpr_write_b32 a18, v114\n"
"v_accvgpr_write_b32 a19, v115\nv_pk_mul_f32 v[120:121], v[120:121], s[72:73] op_sel_hi:[1,0]\nv_pk_mul_f32 v[122:123], v[122:123], s[72:73] op_sel_hi:[1,0]\nv_pk_mul_f32 v[124:125], v[124:125], s[72:73] op_sel_hi:[1,0]\nv_pk_mul_f32 v[126:127], v[126:127], s[72:73] op_sel_hi:[1,0]\nQk 120,120,121\nQk 121,122,123\nQk 122,124,125\n"
"Qk 123,126,127\nv_accvgpr_write_b32 a20, v120\nv_accvgpr_write_b32 a21, v121\nv_accvgpr_write_b32 a22, v122\nv_accvgpr_write_b32 a23, v123\nv_pk_mul_f32 v[128:129], v[128:129], s[72:73] op_sel_hi:[1,0]\nv_pk_mul_f32 v[130:131], v[130:131], s[72:73] op_sel_hi:[1,0]\nv_pk_mul_f32 v[132:133], v[132:133], s[72:73] op_sel_hi:[1,0]\n"
"v_pk_mul_f32 v[134:135], v[134:135], s[72:73] op_sel_hi:[1,0]\nQk 128,128,129\nQk 129,130,131\nQk 130,132,133\nQk 131,134,135\nv_accvgpr_write_b32 a24, v128\nv_accvgpr_write_b32 a25, v129\nv_accvgpr_write_b32 a26, v130\n"
"v_accvgpr_write_b32 a27, v131\nv_pk_mul_f32 v[136:137], v[136:137], s[72:73] op_sel_hi:[1,0]\nv_pk_mul_f32 v[138:139], v[138:139], s[72:73] op_sel_hi:[1,0]\nv_pk_mul_f32 v[140:141], v[140:141], s[72:73] op_sel_hi:[1,0]\nv_pk_mul_f32 v[142:143], v[142:143], s[72:73] op_sel_hi:[1,0]\nQk 136,136,137\nQk 137,138,139\nQk 138,140,141\n"
"Qk 139,142,143\nv_accvgpr_write_b32 a28, v136\nv_accvgpr_write_b32 a29, v137\nv_accvgpr_write_b32 a30, v138\nv_accvgpr_write_b32 a31, v139\nv_pk_mul_f32 v[144:145], v[144:145], s[72:73] op_sel_hi:[1,0]\nv_pk_mul_f32 v[146:147], v[146:147], s[72:73] op_sel_hi:[1,0]\nv_pk_mul_f32 v[148:149], v[148:149], s[72:73] op_sel_hi:[1,0]\n"
"v_pk_mul_f32 v[150:151], v[150:151], s[72:73] op_sel_hi:[1,0]\nQk 144,144,145\nQk 145,146,147\nQk 146,148,149\nQk 147,150,151\nv_accvgpr_write_b32 a32, v144\nv_accvgpr_write_b32 a33, v145\nv_accvgpr_write_b32 a34, v146\n"
"v_accvgpr_write_b32 a35, v147\nv_pk_mul_f32 v[152:153], v[152:153], s[72:73] op_sel_hi:[1,0]\nv_pk_mul_f32 v[154:155], v[154:155], s[72:73] op_sel_hi:[1,0]\nv_pk_mul_f32 v[156:157], v[156:157], s[72:73] op_sel_hi:[1,0]\nv_pk_mul_f32 v[158:159], v[158:159], s[72:73] op_sel_hi:[1,0]\nQk 152,152,153\nQk 153,154,155\nQk 154,156,157\n"
"Qk 155,158,159\nv_accvgpr_write_b32 a36, v152\nv_accvgpr_write_b32 a37, v153\nv_accvgpr_write_b32 a38, v154\nv_accvgpr_write_b32 a39, v155\nv_pk_mul_f32 v[160:161], v[160:161], s[72:73] op_sel_hi:[1,0]\nv_pk_mul_f32 v[162:163], v[162:163], s[72:73] op_sel_hi:[1,0]\nv_pk_mul_f32 v[164:165], v[164:165], s[72:73] op_sel_hi:[1,0]\n"
"v_pk_mul_f32 v[166:167], v[166:167], s[72:73] op_sel_hi:[1,0]\nQk 160,160,161\nQk 161,162,163\nQk 162,164,165\nQk 163,166,167\nv_accvgpr_write_b32 a40, v160\nv_accvgpr_write_b32 a41, v161\nv_accvgpr_write_b32 a42, v162\n"
"v_accvgpr_write_b32 a43, v163\nv_pk_mul_f32 v[168:169], v[168:169], s[72:73] op_sel_hi:[1,0]\nv_pk_mul_f32 v[170:171], v[170:171], s[72:73] op_sel_hi:[1,0]\nv_pk_mul_f32 v[172:173], v[172:173], s[72:73] op_sel_hi:[1,0]\nv_pk_mul_f32 v[174:175], v[174:175], s[72:73] op_sel_hi:[1,0]\nQk 168,168,169\nQk 169,170,171\nQk 170,172,173\n"
"Qk 171,174,175\nv_accvgpr_write_b32 a44, v168\nv_accvgpr_write_b32 a45, v169\nv_accvgpr_write_b32 a46, v170\nv_accvgpr_write_b32 a47, v171\nv_pk_mul_f32 v[176:177], v[176:177], s[72:73] op_sel_hi:[1,0]\nv_pk_mul_f32 v[178:179], v[178:179], s[72:73] op_sel_hi:[1,0]\nv_pk_mul_f32 v[180:181], v[180:181], s[72:73] op_sel_hi:[1,0]\n"
"v_pk_mul_f32 v[182:183], v[182:183], s[72:73] op_sel_hi:[1,0]\nQk 176,176,177\nQk 177,178,179\nQk 178,180,181\nQk 179,182,183\nv_accvgpr_write_b32 a48, v176\nv_accvgpr_write_b32 a49, v177\nv_accvgpr_write_b32 a50, v178\n"
"v_accvgpr_write_b32 a51, v179\nv_pk_mul_f32 v[184:185], v[184:185], s[72:73] op_sel_hi:[1,0]\nv_pk_mul_f32 v[186:187], v[186:187], s[72:73] op_sel_hi:[1,0]\nv_pk_mul_f32 v[188:189], v[188:189], s[72:73] op_sel_hi:[1,0]\nv_pk_mul_f32 v[190:191], v[190:191], s[72:73] op_sel_hi:[1,0]\nQk 184,184,185\nQk 185,186,187\n.Lpg4:\n"
"s_cbranch_execz .Lpg8\nQk 186,188,189\nQk 187,190,191\nv_accvgpr_write_b32 a52, v184\nv_accvgpr_write_b32 a53, v185\nv_accvgpr_write_b32 a54, v186\nv_accvgpr_write_b32 a55, v187\nv_pk_mul_f32 v[192:193], v[192:193], s[72:73] op_sel_hi:[1,0]\n"
"v_pk_mul_f32 v[194:195], v[194:195], s[72:73] op_sel_hi:[1,0]\nv_pk_mul_f32 v[196:197], v[196:197], s[72:73] op_sel_hi:[1,0]\nv_pk_mul_f32 v[198:199], v[198:199], s[72:73] op_sel_hi:[1,0]\nQk 192,192,193\nQk 193,194,195\nQk 194,196,197\nQk 195,198,199\nv_accvgpr_write_b32 a56, v192\n"
"v_accvgpr_write_b32 a57, v193\nv_accvgpr_write_b32 a58, v194\nv_accvgpr_write_b32 a59, v195\nv_pk_mul_f32 v[200:201], v[200:201], s[72:73] op_sel_hi:[1,0]\nv_pk_mul_f32 v[202:203], v[202:203], s[72:73] op_sel_hi:[1,0]\nv_pk_mul_f32 v[204:205], v[204:205], s[72:73] op_sel_hi:[1,0]\nv_pk_mul_f32 v[206:207], v[206:207], s[72:73] op_sel_hi:[1,0]\nQk 200,200,201\n"
"Qk 201,202,203\nQk 202,204,205\nQk 203,206,207\nv_accvgpr_write_b32 a60, v200\nv_accvgpr_write_b32 a61, v201\nv_accvgpr_write_b32 a62, v202\nv_accvgpr_write_b32 a63, v203\nv_pk_mul_f32 v[208:209], v[208:209], s[72:73] op_sel_hi:[1,0]\n"
"v_pk_mul_f32 v[210:211], v[210:211], s[72:73] op_sel_hi:[1,0]\nv_pk_mul_f32 v[212:213], v[212:213], s[72:73] op_sel_hi:[1,0]\nv_pk_mul_f32 v[214:215], v[214:215], s[72:73] op_sel_hi:[1,0]\nQk 208,208,209\nQk 209,210,211\nQk 210,212,213\nQk 211,214,215\nv_accvgpr_write_b32 a64, v208\n"
"v_accvgpr_write_b32 a65, v209\nv_accvgpr_write_b32 a66, v210\nv_accvgpr_write_b32 a67, v211\nv_pk_mul_f32 v[216:217], v[216:217], s[72:73] op_sel_hi:[1,0]\nv_pk_mul_f32 v[218:219], v[218:219], s[72:73] op_sel_hi:[1,0]\nv_pk_mul_f32 v[220:221], v[220:221], s[72:73] op_sel_hi:[1,0]\nv_pk_mul_f32 v[222:223], v[222:223], s[72:73] op_sel_hi:[1,0]\nQk 216,216,217\n"
"Qk 217,218,219\nQk 218,220,221\nQk 219,222,223\nv_accvgpr_write_b32 a68, v216\nv_accvgpr_write_b32 a69, v217\nv_accvgpr_write_b32 a70, v218\nv_accvgpr_write_b32 a71, v219\nv_pk_mul_f32 v[224:225], v[224:225], s[72:73] op_sel_hi:[1,0]\n"
"v_pk_mul_f32 v[226:227], v[226:227], s[72:73] op_sel_hi:[1,0]\nv_pk_mul_f32 v[228:229], v[228:229], s[72:73] op_sel_hi:[1,0]\nv_pk_mul_f32 v[230:231], v[230:231], s[72:73] op_sel_hi:[1,0]\nQk 224,224,225\nQk 225,226,227\nQk 226,228,229\nQk 227,230,231\nv_accvgpr_write_b32 a72, v224\n"
"v_accvgpr_write_b32 a73, v225\nv_accvgpr_write_b32 a74, v226\nv_accvgpr_write_b32 a75, v227\nv_pk_mul_f32 v[232:233], v[232:233], s[72:73] op_sel_hi:[1,0]\nv_pk_mul_f32 v[234:235], v[234:235], s[72:73] op_sel_hi:[1,0]\nv_pk_mul_f32 v[236:237], v[236:237], s[72:73] op_sel_hi:[1,0]\nv_pk_mul_f32 v[238:239], v[238:239], s[72:73] op_sel_hi:[1,0]\nQk 232,232,233\n"
"Qk 233,234,235\nQk 234,236,237\nQk 235,238,239\nv_accvgpr_write_b32 a76, v232\nv_accvgpr_write_b32 a77, v233\nv_accvgpr_write_b32 a78, v234\nv_accvgpr_write_b32 a79, v235\nv_pk_mul_f32 v[240:241], v[240:241], s[72:73] op_sel_hi:[1,0]\n"
"v_pk_mul_f32 v[242:243], v[242:243], s[72:73] op_sel_hi:[1,0]\nv_pk_mul_f32 v[244:245], v[244:245], s[72:73] op_sel_hi:[1,0]\nv_pk_mul_f32 v[246:247], v[246:247], s[72:73] op_sel_hi:[1,0]\nQk 240,240,241\nQk 241,242,243\nQk 242,244,245\nQk 243,246,247\nv_accvgpr_write_b32 a80, v240\n"
"v_accvgpr_write_b32 a81, v241\nv_accvgpr_write_b32 a82, v242\nv_accvgpr_write_b32 a83, v243\nv_pk_mul_f32 v[248:249], v[248:249], s[72:73] op_sel_hi:[1,0]\nv_pk_mul_f32 v[250:251], v[250:251], s[72:73] op_sel_hi:[1,0]\nv_pk_mul_f32 v[252:253], v[252:253], s[72:73] op_sel_hi:[1,0]\nv_pk_mul_f32 v[254:255], v[254:255], s[72:73] op_sel_hi:[1,0]\nQk 248,248,249\n"
"Qk 249,250,251\nQk 250,252,253\nQk 251,254,255\nv_accvgpr_write_b32 a84, v248\nv_accvgpr_write_b32 a85, v249\nv_accvgpr_write_b32 a86, v250\nv_accvgpr_write_b32 a87, v251\nv_accvgpr_read_b32 v0, a208\n"
"v_accvgpr_read_b32 v1, a209\nv_accvgpr_read_b32 v2, a210\nv_accvgpr_read_b32 v3, a211\nv_accvgpr_read_b32 v4, a212\nv_accvgpr_read_b32 v5, a213\nv_accvgpr_read_b32 v6, a214\nv_accvgpr_read_b32 v7, a215\nv_pk_mul_f32 v[0:1], v[0:1], s[72:73] op_sel_hi:[1,0]\n"
"v_pk_mul_f32 v[2:3], v[2:3], s[72:73] op_sel_hi:[1,0]\nv_pk_mul_f32 v[4:5], v[4:5], s[72:73] op_sel_hi:[1,0]\nv_pk_mul_f32 v[6:7], v[6:7], s[72:73] op_sel_hi:[1,0]\nQk 0,0,1\nQk 1,2,3\nQk 2,4,5\nQk 3,6,7\nv_accvgpr_write_b32 a88, v0\n"
"v_accvgpr_write_b32 a89, v1\nv_accvgpr_write_b32 a90, v2\nv_accvgpr_write_b32 a91, v3\nv_accvgpr_read_b32 v8, a216\nv_accvgpr_read_b32 v9, a217\nv_accvgpr_read_b32 v10, a218\nv_accvgpr_read_b32 v11, a219\nv_accvgpr_read_b32 v12, a220\n"
"v_accvgpr_read_b32 v13, a221\nv_accvgpr_read_b32 v14, a222\nv_accvgpr_read_b32 v15, a223\nv_pk_mul_f32 v[8:9], v[8:9], s[72:73] op_sel_hi:[1,0]\nv_pk_mul_f32 v[10:11], v[10:11], s[72:73] op_sel_hi:[1,0]\nv_pk_mul_f32 v[12:13], v[12:13], s[72:73] op_sel_hi:[1,0]\nv_pk_mul_f32 v[14:15], v[14:15], s[72:73] op_sel_hi:[1,0]\nQk 8,8,9\n"
"Qk 9,10,11\nQk 10,12,13\nQk 11,14,15\nv_accvgpr_write_b32 a92, v8\nv_accvgpr_write_b32 a93, v9\nv_accvgpr_write_b32 a94, v10\nv_accvgpr_write_b32 a95, v11\nv_accvgpr_read_b32 v0, a224\n"
"v_accvgpr_read_b32 v1, a225\nv_accvgpr_read_b32 v2, a226\nv_accvgpr_read_b32 v3, a227\nv_accvgpr_read_b32 v4, a228\nv_accvgpr_read_b32 v5, a229\nv_accvgpr_read_b32 v6, a230\nv_accvgpr_read_b32 v7, a231\nv_pk_mul_f32 v[0:1], v[0:1], s[72:73] op_sel_hi:[1,0]\n"
"v_pk_mul_f32 v[2:3], v[2:3], s[72:73] op_sel_hi:[1,0]\nv_pk_mul_f32 v[4:5], v[4:5], s[72:73] op_sel_hi:[1,0]\nv_pk_mul_f32 v[6:7], v[6:7], s[72:73] op_sel_hi:[1,0]\nQk 0,0,1\nQk 1,2,3\nQk 2,4,5\nQk 3,6,7\nv_accvgpr_write_b32 a96, v0\n"
"v_accvgpr_write_b32 a97, v1\nv_accvgpr_write_b32 a98, v2\nv_accvgpr_write_b32 a99, v3\nv_accvgpr_read_b32 v8, a232\nv_accvgpr_read_b32 v9, a233\nv_accvgpr_read_b32 v10, a234\nv_accvgpr_read_b32 v11, a235\nv_accvgpr_read_b32 v12, a236\n"
"v_accvgpr_read_b32 v13, a237\nv_accvgpr_read_b32 v14, a238\nv_accvgpr_read_b32 v15, a239\nv_pk_mul_f32 v[8:9], v[8:9], s[72:73] op_sel_hi:[1,0]\nv_pk_mul_f32 v[10:11], v[10:11], s[72:73] op_sel_hi:[1,0]\nv_pk_mul_f32 v[12:13], v[12:13], s[72:73] op_sel_hi:[1,0]\nv_pk_mul_f32 v[14:15], v[14:15], s[72:73] op_sel_hi:[1,0]\nQk 8,8,9\n"
"Qk 9,10,11\nQk 10,12,13\nQk 11,14,15\nv_accvgpr_write_b32 a100, v8\nv_accvgpr_write_b32 a101, v9\nv_accvgpr_write_b32 a102, v10\nv_accvgpr_write_b32 a103, v11\nv_accvgpr_read_b32 v0, a240\n"
"v_accvgpr_read_b32 v1, a241\nv_accvgpr_read_b32 v2, a242\nv_accvgpr_read_b32 v3, a243\nv_accvgpr_read_b32 v4, a244\nv_accvgpr_read_b32 v5, a245\nv_accvgpr_read_b32 v6, a246\nv_accvgpr_read_b32 v7, a247\nv_pk_mul_f32 v[0:1], v[0:1], s[72:73] op_sel_hi:[1,0]\n"
"v_pk_mul_f32 v[2:3], v[2:3], s[72:73] op_sel_hi:[1,0]\nv_pk_mul_f32 v[4:5], v[4:5], s[72:73] op_sel_hi:[1,0]\nv_pk_mul_f32 v[6:7], v[6:7], s[72:73] op_sel_hi:[1,0]\nQk 0,0,1\nQk 1,2,3\nQk 2,4,5\nQk 3,6,7\nv_accvgpr_write_b32 a104, v0\n"
"v_accvgpr_write_b32 a105, v1\nv_accvgpr_write_b32 a106, v2\nv_accvgpr_write_b32 a107, v3\nv_accvgpr_read_b32 v8, a248\nv_accvgpr_read_b32 v9, a249\nv_accvgpr_read_b32 v10, a250\nv_accvgpr_read_b32 v11, a251\nv_accvgpr_read_b32 v12, a252\n"
"v_accvgpr_read_b32 v13, a253\nv_accvgpr_read_b32 v14, a254\nv_accvgpr_read_b32 v15, a255\nv_pk_mul_f32 v[8:9], v[8:9], s[72:73] op_sel_hi:[1,0]\nv_pk_mul_f32 v[10:11], v[10:11], s[72:73] op_sel_hi:[1,0]\nv_pk_mul_f32 v[12:13], v[12:13], s[72:73] op_sel_hi:[1,0]\nv_pk_mul_f32 v[14:15], v[14:15], s[72:73] op_sel_hi:[1,0]\nQk 8,8,9\n"
"Qk 9,10,11\nQk 10,12,13\nQk 11,14,15\nv_accvgpr_write_b32 a108, v8\nv_accvgpr_write_b32 a109, v9\nv_accvgpr_write_b32 a110, v10\nv_accvgpr_write_b32 a111, v11\nv_mov_b32 v66, 0x3f803f80\n"
"v_accvgpr_write_b32 a200, 0\nv_accvgpr_write_b32 a201, 0\nv_accvgpr_write_b32 a202, 0\nv_accvgpr_write_b32 a203, 0\ns_mov_b32 vcc_lo, 0x33333333\ns_mov_b32 vcc_hi, 0xcccccccc\nv_cndmask_b32 v66, 0, v66, vcc\nv_accvgpr_write_b32 a204, v66\n"
"v_accvgpr_write_b32 a205, v66\nv_accvgpr_write_b32 a206, v66\nv_accvgpr_write_b32 a207, v66\ns_add_u32 s87, s87, 2048\ns_waitcnt lgkmcnt(15)\nQzvaa 0,112,0\nQmvaa 0,116,4\nQmvaa 0,120,8\n"
"Qmvaa 0,124,12\nQzvaa 4,128,0\nQmvaa 4,132,4\nQmvaa 4,136,8\nQmvaa 4,140,12\ns_nop 3\nv_max3_f32 v66, v0, v1, v2\ns_nop 2\n"
"v_max3_f32 v66, v66, v3, v4\nv_max3_f32 v66, v66, v5, v6\nv_max_f32 v66, v66, v7\nQzvaa 8,112,16\nv_mov_b32 v67, v66\nQmvaa 8,116,20\ns_nop 0\nv_permlane32_swap_b32 v66, v67\n"
"v_max_f32 v66, v66, v67\nQmvaa 8,120,24\nv_mov_b32 v67, v66\nQmvaa 8,124,28\ns_nop 0\nv_permlane16_swap_b32 v66, v67\nv_max_f32 v66, v66, v67\nQzvaa 12,128,16\n"
"v_sub_f32 v24, 0xc1800000, v66\nQa 0,0,24\nQmvaa 12,132,20\nQa 1,1,24\nQa 2,2,24\nQmvaa 12,136,24\nQa 3,3,24\nQa 4,4,24\n"
"Qmvaa 12,140,28\nQe 0\nQe 1\nQe 2\nQe 3\nQk 16,0,1\nQk 17,2,3\nQa 5,5,24\n"
"Qa 6,6,24\nQa 7,7,24\nv_mov_b32 v25, v24\nQe 4\nQe 5\nQe 6\nQzvaa 0,112,32\nQe 7\n"
"Qmvaa 0,116,36\nQk 18,4,5\nQk 19,6,7\nQmvaa 0,120,40\nv_max3_f32 v68, v8, v9, v10\nv_max3_f32 v68, v68, v11, v12\nQmvaa 0,124,44\nv_max3_f32 v68, v68, v13, v14\n"
"v_max_f32 v68, v68, v15\nQzvaa 4,128,32\nv_mov_b32 v69, v68\nv_mov_b32 v26, v24\nQmvaa 4,132,36\nv_permlane32_swap_b32 v68, v69\nv_max_f32 v68, v68, v69\nQmvaa 4,136,40\n"
"v_mov_b32 v69, v68\nv_mov_b32 v27, v24\nQmvaa 4,140,44\ns_waitcnt lgkmcnt(0)\nv_permlane16_swap_b32 v68, v69\nQzvav 80,144,16\nv_max_f32 v68, v68, v69\nv_sub_f32 v28, 0xc1800000, v68\n"
"Qzvav 84,148,16\nQa 8,8,28\nQa 9,9,28\nQzvav 88,152,16\nQa 10,10,28\nQa 11,11,28\nQzvav 92,156,16\nQe 8\n"
"Qzvav 96,160,16\nQe 9\nQzvav 100,164,16\nQe 10\nQzvav 104,168,16\nQe 11\nQzvav 108,172,16\nQk 20,8,9\n"
"Qk 21,10,11\nQzvaa 8,112,48\nQa 12,12,28\nQa 13,13,28\nQmvaa 8,116,52\nQa 14,14,28\nQa 15,15,28\nQmvaa 8,120,56\n"
"Qe 12\nQe 13\nQe 14\nQe 15\nQmvaa 8,124,60\nQk 22,12,13\nQk 23,14,15\nQzvaa 12,128,48\n"
"v_max3_f32 v66, v0, v1, v2\nv_max3_f32 v66, v66, v3, v4\nQmvaa 12,132,52\nv_max3_f32 v66, v66, v5, v6\nv_max_f32 v66, v66, v7\nv_smfmac_f32_16x16x64_bf16 a[176:179], a[204:207], v[16:23], v77\nv_mov_b32 v67, v66\nQmvaa 12,136,56\n"
"s_nop 0\nv_permlane32_swap_b32 v66, v67\nv_max_f32 v66, v66, v67\nQmvaa 12,140,60\nv_mov_b32 v67, v66\nv_mov_b32 v29, v28\nQzvav 112,144,20\ns_add_u32 m0, s61, 99072\n"
"v_permlane16_swap_b32 v66, v67\nQzvav 116,148,20\nbuffer_load_dwordx4 v56, s[40:43], s60 offen nt lds\nQzvav 120,152,20\nv_max_f32 v66, v66, v67\nv_sub_f32 v32, 0xc1800000, v66\nQzvav 124,156,20\nQa 0,0,32\n"
"Qa 1,1,32\nQzvav 128,160,20\nQa 2,2,32\nQa 3,3,32\nQzvav 132,164,20\nQe 0\nQzvav 136,168,20\nbuffer_load_dwordx4 v57, s[40:43], s60 offen offset:1024 nt lds\n"
"Qe 1\nQe 2\nQe 3\nQzvav 140,172,20\nQk 16,0,1\nQk 17,2,3\nQzvaa 0,112,64\nQa 4,4,32\n"
"Qa 5,5,32\nQmvaa 0,116,68\nQa 6,6,32\nQa 7,7,32\nQmvaa 0,120,72\nQe 4\nQe 5\nQe 6\n"
"Qe 7\nQmvaa 0,124,76\nbuffer_load_dwordx4 v56, s[40:43], s60 offen offset:2048 nt lds\nQk 18,4,5\nQk 19,6,7\nQzvaa 4,128,64\nv_max3_f32 v68, v8, v9, v10\nv_max3_f32 v68, v68, v11, v12\n"
"Qmvaa 4,132,68\nv_max3_f32 v68, v68, v13, v14\nv_max_f32 v68, v68, v15\nQmvaa 4,136,72\nv_mov_b32 v69, v68\nv_mov_b32 v30, v28\nQmvaa 4,140,76\nv_permlane32_swap_b32 v68, v69\n"
"v_max_f32 v68, v68, v69\nQzvav 144,144,16\nbuffer_load_dwordx4 v57, s[40:43], s60 offen offset:3072 nt lds\nQzvav 148,148,16\nv_mov_b32 v69, v68\nv_mov_b32 v31, v28\nQzvav 152,152,16\nv_permlane16_swap_b32 v68, v69\n"
"v_max_f32 v68, v68, v69\nQzvav 156,156,16\nv_sub_f32 v36, 0xc1800000, v68\nQa 8,8,36\nQzvav 160,160,16\ns_add_u32 m0, s61, 107264\nQa 9,9,36\nQzvav 164,164,16\n"
"buffer_load_dwordx4 v58, s[44:47], s60 offen nt lds\nQzvav 168,168,16\nQa 10,10,36\nQa 11,11,36\nQe 8\nQe 9\nQe 10\nQe 11\n"
"Qzvav 172,172,16\nQk 20,8,9\nQk 21,10,11\nQzvaa 8,112,80\nQa 12,12,36\nQa 13,13,36\nQmvaa 8,116,84\nQa 14,14,36\n"
"Qa 15,15,36\nQmvaa 8,120,88\nbuffer_load_dwordx4 v59, s[44:47], s60 offen offset:1024 nt lds\nQe 12\nQe 13\nQe 14\nQe 15\nQmvaa 8,124,92\n"
"Qk 22,12,13\nQk 23,14,15\nQzvaa 12,128,80\nv_max3_f32 v66, v0, v1, v2\nv_max3_f32 v66, v66, v3, v4\nQmvaa 12,132,84\nv_max3_f32 v66, v66, v5, v6\nv_max_f32 v66, v66, v7\n"
"v_smfmac_f32_16x16x64_bf16 a[180:183], a[204:207], v[16:23], v77\nv_mov_b32 v67, v66\nQmvaa 12,136,88\nbuffer_load_dwordx4 v58, s[44:47], s60 offen offset:2048 nt lds\nQmvaa 12,140,92\nv_permlane32_swap_b32 v66, v67\nv_max_f32 v66, v66, v67\nQzvav 176,144,20\n"
"v_mov_b32 v67, v66\nv_mov_b32 v33, v32\nQzvav 180,148,20\nv_permlane16_swap_b32 v66, v67\nv_max_f32 v66, v66, v67\nQzvav 184,152,20\ns_waitcnt vmcnt(15)\ns_barrier\n"
"Qzvav 188,156,20\nbuffer_load_dwordx4 v59, s[44:47], s60 offen offset:3072 nt lds\nQzvav 192,160,20\nv_sub_f32 v40, 0xc1800000, v66\nQa 0,0,40\nQzvav 196,164,20\nQa 1,1,40\nQa 2,2,40\n"
"Qzvav 200,168,20\ns_add_u32 s60, s60, 8192\ns_and_b32 s68, s60, 0x40000\ns_mul_i32 s68, s68, 7\ns_add_u32 s60, s60, s68\nQa 3,3,40\nQe 0\nQe 1\n"
"Qe 2\nQe 3\nQzvav 204,172,20\nQk 16,0,1\nQk 17,2,3\nQzvaa 0,112,96\nQda 112,52,33024\nQa 4,4,40\n"
"Qmvaa 0,116,100\nQda 116,52,33536\nQa 5,5,40\nQmvaa 0,120,104\nQda 120,52,34048\nQa 6,6,40\nQa 7,7,40\nQe 4\n"
"Qe 5\nQe 6\nQe 7\nQmvaa 0,124,108\nQda 124,52,34560\nQk 18,4,5\nQk 19,6,7\nQzvaa 4,128,96\n"
"Qda 128,52,35072\nv_max3_f32 v68, v8, v9, v10\nQmvaa 4,132,100\nQda 132,52,35584\nv_max3_f32 v68, v68, v11, v12\nQmvaa 4,136,104\nQda 136,52,36096\nv_max3_f32 v68, v68, v13, v14\n"
"Qmvaa 4,140,108\nQda 140,52,36608\nv_max_f32 v68, v68, v15\nQzvav 208,144,16\nv_mov_b32 v69, v68\nv_mov_b32 v34, v32\nQzvav 212,148,16\ns_memrealtime s[74:75]\n"
"v_permlane32_swap_b32 v68, v69\nQzvav 216,152,16\nv_max_f32 v68, v68, v69\nv_mov_b32 v69, v68\nQzvav 220,156,16\ns_nop 0\nv_permlane16_swap_b32 v68, v69\nv_max_f32 v68, v68, v69\n"
"Qzvav 224,160,16\nv_sub_f32 v44, 0xc1800000, v68\nQa 8,8,44\nQzvav 228,164,16\nQa 9,9,44\nQa 10,10,44\nQzvav 232,168,16\nQa 11,11,44\n"
"Qe 8\nQe 9\nQe 10\nQe 11\nQzvav 236,172,16\ns_waitcnt lgkmcnt(0)\nQk 20,8,9\nQk 21,10,11\n"
"Qcvaa 8,112,0,24\nQa 12,12,44\nQa 13,13,44\nQmvaa 8,116,4\nQa 14,14,44\nQa 15,15,44\nQmvaa 8,120,8\nQe 12\n"
"Qe 13\nQe 14\nQe 15\nQmvaa 8,124,12\nQk 22,12,13\nQk 23,14,15\nQcvaa 12,128,0,24\nv_max3_f32 v66, v0, v1, v2\n"
"v_max3_f32 v66, v66, v3, v4\nQmvaa 12,132,4\nv_max3_f32 v66, v66, v5, v6\nv_max_f32 v66, v66, v7\nv_smfmac_f32_16x16x64_bf16 a[184:187], a[204:207], v[16:23], v77\nv_mov_b32 v67, v66\nQmvaa 12,136,8\ns_nop 0\n"
"v_permlane32_swap_b32 v66, v67\nv_max_f32 v66, v66, v67\nQmvaa 12,140,12\nv_mov_b32 v67, v66\nv_mov_b32 v35, v32\nQzvav 240,144,20\ns_waitcnt lgkmcnt(0)\nQzvav 244,148,20\n"
"s_mov_b32 s73, 140\nv_permlane16_swap_b32 v66, v67\nQzvav 248,152,20\ns_add_u32 s77, s74, s73\nv_max_f32 v66, v66, v67\nQzvav 252,156,20\ns_sub_u32 s73, s73, 4\nv_sub_f32 v48, 0xc1800000, v66\n"
"Qzaav 208,160,20\ns_max_u32 s73, s73, 120\nQa 0,0,48\nQzaav 212,164,20\nQe 8\nQzaav 216,168,20\nQe 9\nQa 1,1,48\n"
"Qa 2,2,48\n.Lpg5:\ns_cbranch_execz .Lpg9\nQa 3,3,48\nQe 0\nQe 1\nQe 2\nQe 3\n"
"Qzaav 220,172,20\nQe 10\nQk 16,0,1\nQk 17,2,3\nQcvaa 0,112,16,28\nQe 11\nQmvaa 0,116,20\nQk 20,8,9\n"
"Qk 21,10,11\nQmvaa 0,120,24\nQe 12\nQa 4,4,48\nQa 5,5,48\nQa 6,6,48\nQa 7,7,48\nQe 4\n"
"Qe 5\nQe 6\nQe 7\nQmvaa 0,124,28\nQe 13\nQk 18,4,5\nQk 19,6,7\nQcvaa 4,128,16,28\n"
"Qe 14\nv_mov_b32 v37, v36\nQmvaa 4,132,20\nQe 15\nv_mov_b32 v38, v36\nQmvaa 4,136,24\nQk 22,12,13\nQk 23,14,15\n"
"v_mov_b32 v39, v36\nv_mov_b32 v41, v40\nQmvaa 4,140,28\nv_mov_b32 v42, v40\nv_mov_b32 v43, v40\nQzaav 224,144,16\nQta 144,54,33024\nQta 146,54,35072\n"
"v_mov_b32 v45, v44\nQzaav 228,148,16\nQta 148,54,33280\nQta 150,54,35328\nv_mov_b32 v46, v44\nQzaav 232,152,16\nQta 152,54,33536\nQta 154,54,35584\n"
"v_mov_b32 v47, v44\nv_mov_b32 v49, v48\nQzaav 236,156,16\nQta 156,54,33792\nQta 158,54,35840\nv_mov_b32 v50, v48\nQzaav 240,160,16\nQta 160,54,34048\n"
"Qta 162,54,36096\nv_mov_b32 v51, v48\nQzaav 244,164,16\nQta 164,54,34304\nQta 166,54,36352\nQzaav 248,168,16\nQta 168,54,34560\nQta 170,54,36608\n"
"Qzaav 252,172,16\nQta 172,54,34816\nQta 174,54,36864\ns_lshl_b32 s66, s67, 3\n.Lloop:\ns_bitcmp1_b32 s99, s66\ns_cbranch_scc1 .Lhook\n.Lhookret:\n"
"Qcvaa 8,112,32,32\nv_add_u32 v75, 1, v75\nQmvaa 8,116,36\ns_waitcnt vmcnt(8)\nds_write_b32 v73, v75\nQmvaa 8,120,40\nQe 0\nQmvaa 8,124,44\n"
"Qe 1\nQcvaa 12,128,32,32\nQe 2\nQmvaa 12,132,36\nQe 3\nv_smfmac_f32_16x16x64_bf16 a[188:191], a[204:207], v[16:23], v77\nQmvaa 12,136,40\ns_waitcnt lgkmcnt(1)\n"
"Qk 16,0,1\nQmvaa 12,140,44\nQk 17,2,3\nQmvav 80,144,20\nQe 4\nQmvav 84,148,20\nQe 5\nQmvav 88,152,20\n"
"Qe 6\nQmvav 92,156,20\nQe 7\nQmvav 96,160,20\nQk 18,4,5\nQk 19,6,7\nQmvav 100,164,20\nQe 8\n"
"Qmvav 104,168,20\nQe 9\nQmvav 108,172,20\nQe 10\nQcvaa 0,112,48,36\nQe 11\nQmvaa 0,116,52\nds_read_b32 v69, v74\n"
"Qk 20,8,9\nQmvaa 0,120,56\nQk 21,10,11\nQmvaa 0,124,60\nQe 12\nQcvaa 4,128,48,36\nQe 13\nQmvaa 4,132,52\n"
"Qe 14\nQmvaa 4,136,56\ns_waitcnt lgkmcnt(0)\nv_cmp_gt_u32 vcc, v75, v69\nQmvaa 4,140,60\ns_cbranch_vccnz .Lsa1\n.Lra1:\nQk 22,12,13\n"
"Qmvav 112,144,16\ns_add_u32 m0, s61, 0\nQmvav 116,148,16\nbuffer_load_dwordx4 v56, s[40:43], s60 offen nt lds\nQmvav 120,152,16\nQe 15\nQmvav 124,156,16\nQk 23,14,15\n"
"Qmvav 128,160,16\nQe 0\nQmvav 132,164,16\nQe 1\nQmvav 136,168,16\nbuffer_load_dwordx4 v57, s[40:43], s60 offen offset:1024 nt lds\nQmvav 140,172,16\nQe 2\n"
"Qcvaa 8,112,64,40\nQe 3\nQmvaa 8,116,68\nQe 4\nQmvaa 8,120,72\nQe 5\nQmvaa 8,124,76\nbuffer_load_dwordx4 v56, s[40:43], s60 offen offset:2048 nt lds\n"
"Qcvaa 12,128,64,40\nQe 6\nQmvaa 12,132,68\nQe 7\nv_smfmac_f32_16x16x64_bf16 a[192:195], a[204:207], v[16:23], v77\nQmvaa 12,136,72\nQk 16,0,1\nQk 17,2,3\n"
"Qmvaa 12,140,76\nbuffer_load_dwordx4 v57, s[40:43], s60 offen offset:3072 nt lds\nQmvav 144,144,20\nQk 18,4,5\nQk 19,6,7\nQmvav 148,148,20\nQmvav 152,152,20\nQe 8\n"
"Qmvav 156,156,20\ns_add_u32 m0, s61, 8192\nQmvav 160,160,20\nbuffer_load_dwordx4 v58, s[44:47], s60 offen nt lds\nQmvav 164,164,20\nQe 9\nQmvav 168,168,20\nQe 10\n"
"Qmvav 172,172,20\nQe 11\nQcvaa 0,112,80,44\nQk 20,8,9\nQk 21,10,11\nQmvaa 0,116,84\nbuffer_load_dwordx4 v59, s[44:47], s60 offen offset:1024 nt lds\nQmvaa 0,120,88\n"
"Qe 12\nQmvaa 0,124,92\nQe 13\nQcvaa 4,128,80,44\nQe 14\nQmvaa 4,132,84\nQe 15\nQmvaa 4,136,88\n"
"buffer_load_dwordx4 v58, s[44:47], s60 offen offset:2048 nt lds\nQmvaa 4,140,92\nQk 22,12,13\nQk 23,14,15\nQmvav 176,144,16\nQmvav 180,148,16\nQmvav 184,152,16\nQmvav 188,156,16\n"
"buffer_load_dwordx4 v59, s[44:47], s60 offen offset:3072 nt lds\nQmvav 192,160,16\nQe 0\nQmvav 196,164,16\nQe 1\nQmvav 200,168,16\ns_add_u32 s60, s60, 8192\nQmvav 204,172,16\n"
"Qe 2\nQcvaa 8,112,96,48\nQda 112,53,0\nQmvaa 8,116,100\nQda 116,53,512\nQmvaa 8,120,104\nQda 120,53,1024\nQmvaa 8,124,108\n"
"Qda 124,53,1536\nQcvaa 12,128,96,48\nQda 128,53,2048\nQmvaa 12,132,100\nQda 132,53,2560\nv_smfmac_f32_16x16x64_bf16 a[196:199], a[204:207], v[16:23], v77\nQmvaa 12,136,104\nQda 136,53,3072\n"
"Qk 16,0,1\nQmvaa 12,140,108\nQda 140,53,3584\nQmvav 208,144,20\ns_memrealtime s[74:75]\nQmvav 212,148,20\nQe 3\nQmvav 216,152,20\n"
"Qk 17,2,3\nQmvav 220,156,20\nQe 4\nQmvav 224,160,20\nQe 5\nQmvav 228,164,20\nQe 6\nQmvav 232,168,20\n"
"Qe 7\nQmvav 236,172,20\ns_waitcnt lgkmcnt(0)\nQk 18,4,5\nQcvaa 0,112,0,24\nQk 19,6,7\nQmvaa 0,116,4\nQe 8\n"
"Qmvaa 0,120,8\nQe 9\nQmvaa 0,124,12\nQe 10\nQcvaa 4,128,0,24\nQe 11\nQmvaa 4,132,4\nQk 20,8,9\n"
"Qk 21,10,11\nQmvaa 4,136,8\nQe 12\nQmvaa 4,140,12\nQe 13\nQmvav 240,144,16\nQe 14\nQmvav 244,148,16\n"
"s_waitcnt lgkmcnt(0)\ns_sub_u32 s75, s77, s74\nQmvav 248,152,16\ns_cmp_lt_u32 s75, 256\ns_cbranch_scc1 .Lsp1\n.Lrp1:\nQmvav 252,156,16\ns_add_u32 s77, s74, s73\n"
"Qk 22,12,13\nQmaav 208,160,16\ns_sub_u32 s73, s73, 4\nQmaav 212,164,16\ns_max_u32 s73, s73, 120\nQmaav 216,168,16\nQe 15\nQmaav 220,172,16\n"
"Qk 23,14,15\nQcvaa 8,112,16,28\nQe 0\nQmvaa 8,116,20\nQe 1\nQmvaa 8,120,24\nQe 2\nQmvaa 8,124,28\n"
"Qe 3\nQcvaa 12,128,16,28\nQe 4\nQmvaa 12,132,20\nQe 5\nv_smfmac_f32_16x16x64_bf16 a[200:203], a[204:207], v[16:23], v77\nQmvaa 12,136,24\nQk 16,0,1\n"
"Qk 17,2,3\nQmvaa 12,140,28\nQe 6\nQmaav 224,144,20\nQta 144,55,0\nQta 146,55,2048\nQmaav 228,148,20\nQta 148,55,256\n"
"Qta 150,55,2304\nQmaav 232,152,20\nQta 152,55,512\nQta 154,55,2560\nQmaav 236,156,20\nQta 156,55,768\nQta 158,55,2816\nQmaav 240,160,20\n"
"Qta 160,55,1024\nQta 162,55,3072\nQmaav 244,164,20\nQta 164,55,1280\nQta 166,55,3328\nQmaav 248,168,20\nQta 168,55,1536\nQta 170,55,3584\n"
"Qmaav 252,172,20\nQta 172,55,1792\nQta 174,55,3840\nQcvaa 0,112,32,32\nv_add_u32 v75, 1, v75\nQk 18,4,5\nQmvaa 0,116,36\ns_waitcnt vmcnt(8)\n"
"ds_write_b32 v73, v75\nQmvaa 0,120,40\nQe 7\nQmvaa 0,124,44\nQk 19,6,7\nQcvaa 4,128,32,32\nQe 8\nQmvaa 4,132,36\n"
"Qe 9\nQmvaa 4,136,40\nQe 10\nQmvaa 4,140,44\ns_waitcnt lgkmcnt(1)\nQk 20,8,9\nQmvav 80,144,16\nQe 11\n"
"Qmvav 84,148,16\nQk 21,10,11\nQmvav 88,152,16\nQe 12\nQmvav 92,156,16\nQe 13\nQmvav 96,160,16\nQe 14\n"
"Qmvav 100,164,16\nQe 15\nQmvav 104,168,16\nQk 22,12,13\nQk 23,14,15\nQmvav 108,172,16\nQe 0\nQcvaa 8,112,48,36\n"
"Qe 1\nQmvaa 8,116,52\nQe 2\nQmvaa 8,120,56\nds_read_b32 v69, v74\nQmvaa 8,124,60\nQe 3\nQcvaa 12,128,48,36\n"
"Qe 4\nQmvaa 12,132,52\nQe 5\nv_smfmac_f32_16x16x64_bf16 a[176:179], a[204:207], v[16:23], v77\nQmvaa 12,136,56\ns_waitcnt lgkmcnt(0)\nv_cmp_gt_u32 vcc, v75, v69\nQmvaa 12,140,60\n"
"s_cbranch_vccnz .Lsa2\n.Lra2:\nQk 16,0,1\nQmvav 112,144,20\ns_add_u32 m0, s61, 33024\nQk 17,2,3\nQmvav 116,148,20\nbuffer_load_dwordx4 v56, s[40:43], s60 offen nt lds\n"
"Qmvav 120,152,20\nQe 6\nQmvav 124,156,20\nQe 7\nQmvav 128,160,20\nQk 18,4,5\nQk 19,6,7\nQmvav 132,164,20\n"
"Qe 8\nQmvav 136,168,20\nbuffer_load_dwordx4 v57, s[40:43], s60 offen offset:1024 nt lds\nQmvav 140,172,20\nQe 9\nQcvaa 0,112,64,40\nQe 10\nQmvaa 0,116,68\n"
"Qe 11\nQmvaa 0,120,72\nQk 20,8,9\nQk 21,10,11\nQmvaa 0,124,76\nbuffer_load_dwordx4 v56, s[40:43], s60 offen offset:2048 nt lds\nQcvaa 4,128,64,40\nQe 12\n"
"Qmvaa 4,132,68\nQe 13\nQmvaa 4,136,72\nQe 14\nQmvaa 4,140,76\nQe 15\nQmvav 144,144,16\nbuffer_load_dwordx4 v57, s[40:43], s60 offen offset:3072 nt lds\n"
"Qmvav 148,148,16\nQk 22,12,13\nQk 23,14,15\nQmvav 152,152,16\nQmvav 156,156,16\nQe 0\nQmvav 160,160,16\ns_add_u32 m0, s61, 41216\n"
"Qmvav 164,164,16\nbuffer_load_dwordx4 v58, s[44:47], s60 offen nt lds\nQmvav 168,168,16\nQe 1\nQmvav 172,172,16\nQe 2\nQcvaa 8,112,80,44\nQe 3\n"
"Qmvaa 8,116,84\nQe 4\nQmvaa 8,120,88\nbuffer_load_dwordx4 v59, s[44:47], s60 offen offset:1024 nt lds\nQmvaa 8,124,92\nQe 5\nQcvaa 12,128,80,44\nQe 6\n"
"Qmvaa 12,132,84\nQe 7\nv_smfmac_f32_16x16x64_bf16 a[180:183], a[204:207], v[16:23], v77\nQmvaa 12,136,88\nbuffer_load_dwordx4 v58, s[44:47], s60 offen offset:2048 nt lds\nQmvaa 12,140,92\nQk 16,0,1\nQk 17,2,3\n"
"Qmvav 176,144,20\nQk 18,4,5\nQk 19,6,7\nQmvav 180,148,20\nQmvav 184,152,20\nQe 8\nQmvav 188,156,20\nbuffer_load_dwordx4 v59, s[44:47], s60 offen offset:3072 nt lds\n"
"Qmvav 192,160,20\nQe 9\nQmvav 196,164,20\nQe 10\nQmvav 200,168,20\ns_add_u32 s60, s60, 8192\nQmvav 204,172,20\nQe 11\n"
"Qcvaa 0,112,96,48\nQda 112,53,33024\nQk 20,8,9\nQmvaa 0,116,100\nQda 116,53,33536\nQk 21,10,11\nQmvaa 0,120,104\nQda 120,53,34048\n"
"Qmvaa 0,124,108\nQda 124,53,34560\nQcvaa 4,128,96,48\nQda 128,53,35072\nQmvaa 4,132,100\nQda 132,53,35584\nQmvaa 4,136,104\nQda 136,53,36096\n"
"Qmvaa 4,140,108\nQda 140,53,36608\nQmvav 208,144,16\nQe 12\nQmvav 212,148,16\ns_memrealtime s[74:75]\nQmvav 216,152,16\nQe 13\n"
"Qmvav 220,156,16\nQe 14\nQmvav 224,160,16\nQe 15\nQmvav 228,164,16\nQk 22,12,13\nQk 23,14,15\nQmvav 232,168,16\n"
"Qe 0\nQmvav 236,172,16\ns_waitcnt lgkmcnt(0)\nQcvaa 8,112,0,24\nQe 1\nQmvaa 8,116,4\nQe 2\nQmvaa 8,120,8\n"
"Qe 3\nQmvaa 8,124,12\nQe 4\nQcvaa 12,128,0,24\nQe 5\nQmvaa 12,132,4\nQe 6\nv_smfmac_f32_16x16x64_bf16 a[184:187], a[204:207], v[16:23], v77\n"
"Qmvaa 12,136,8\nQk 16,0,1\nQk 17,2,3\nQmvaa 12,140,12\nQe 7\nQmvav 240,144,20\ns_waitcnt lgkmcnt(0)\ns_sub_u32 s75, s77, s74\n"
"Qmvav 244,148,20\ns_cmp_lt_u32 s75, 256\ns_cbranch_scc1 .Lsp2\n.Lrp2:\nQmvav 248,152,20\ns_add_u32 s77, s74, s73\nQk 18,4,5\nQmvav 252,156,20\n"
"s_sub_u32 s73, s73, 4\nQk 19,6,7\nQmaav 208,160,20\ns_max_u32 s73, s73, 120\nQmaav 212,164,20\nQe 8\nQmaav 216,168,20\nQe 9\n"
"Qmaav 220,172,20\nQe 10\nQcvaa 0,112,16,28\nQe 11\nQmvaa 0,116,20\nQk 20,8,9\nQk 21,10,11\nQmvaa 0,120,24\n"
"Qe 12\nQmvaa 0,124,28\nQe 13\nQcvaa 4,128,16,28\nQe 14\nQmvaa 4,132,20\nQe 15\nQmvaa 4,136,24\n"
"Qk 22,12,13\nQk 23,14,15\nQmvaa 4,140,28\nQmaav 224,144,16\nQta 144,55,33024\nQta 146,55,35072\nQmaav 228,148,16\nQta 148,55,33280\n"
"Qta 150,55,35328\nQmaav 232,152,16\nQta 152,55,33536\nQta 154,55,35584\nQmaav 236,156,16\nQta 156,55,33792\nQta 158,55,35840\nQmaav 240,160,16\n"
"Qta 160,55,34048\nQta 162,55,36096\nQmaav 244,164,16\nQta 164,55,34304\nQta 166,55,36352\nQmaav 248,168,16\nQta 168,55,34560\nQta 170,55,36608\n"
"Qmaav 252,172,16\nQta 172,55,34816\nQta 174,55,36864\nQcvaa 8,112,32,32\nv_add_u32 v75, 1, v75\nQmvaa 8,116,36\ns_waitcnt vmcnt(8)\nds_write_b32 v73, v75\n"
"Qmvaa 8,120,40\nQe 0\nQmvaa 8,124,44\nQe 1\nQcvaa 12,128,32,32\nQe 2\nQmvaa 12,132,36\nQe 3\n"
"v_smfmac_f32_16x16x64_bf16 a[188:191], a[204:207], v[16:23], v77\nQmvaa 12,136,40\ns_waitcnt lgkmcnt(1)\nQk 16,0,1\nQmvaa 12,140,44\nQk 17,2,3\nQmvav 80,144,20\nQe 4\n"
"Qmvav 84,148,20\nQe 5\nQmvav 88,152,20\n.Lpg6:\ns_cbranch_execz .Lpg10\nQe 6\nQmvav 92,156,20\nQe 7\n"
"Qmvav 96,160,20\nQk 18,4,5\nQk 19,6,7\nQmvav 100,164,20\nQe 8\nQmvav 104,168,20\nQe 9\nQmvav 108,172,20\n"
"Qe 10\nQcvaa 0,112,48,36\nQe 11\nQmvaa 0,116,52\nds_read_b32 v69, v74\nQk 20,8,9\nQmvaa 0,120,56\nQk 21,10,11\n"
"Qmvaa 0,124,60\nQe 12\nQcvaa 4,128,48,36\nQe 13\nQmvaa 4,132,52\nQe 14\nQmvaa 4,136,56\ns_waitcnt lgkmcnt(0)\n"
"v_cmp_gt_u32 vcc, v75, v69\nQmvaa 4,140,60\ns_cbranch_vccnz .Lsa3\n.Lra3:\nQk 22,12,13\nQmvav 112,144,16\ns_add_u32 m0, s61, 66048\nQmvav 116,148,16\n"
"buffer_load_dwordx4 v56, s[40:43], s60 offen nt lds\nQmvav 120,152,16\nQe 15\nQmvav 124,156,16\nQk 23,14,15\nQmvav 128,160,16\nQe 0\nQmvav 132,164,16\n"
"Qe 1\nQmvav 136,168,16\nbuffer_load_dwordx4 v57, s[40:43], s60 offen offset:1024 nt lds\nQmvav 140,172,16\nQe 2\nQcvaa 8,112,64,40\nQe 3\nQmvaa 8,116,68\n"
"Qe 4\nQmvaa 8,120,72\nQe 5\nQmvaa 8,124,76\nbuffer_load_dwordx4 v56, s[40:43], s60 offen offset:2048 nt lds\nQcvaa 12,128,64,40\nQe 6\nQmvaa 12,132,68\n"
"Qe 7\nv_smfmac_f32_16x16x64_bf16 a[192:195], a[204:207], v[16:23], v77\nQmvaa 12,136,72\nQk 16,0,1\nQk 17,2,3\nQmvaa 12,140,76\nbuffer_load_dwordx4 v57, s[40:43], s60 offen offset:3072 nt lds\nQmvav 144,144,20\n"
"Qk 18,4,5\nQk 19,6,7\nQmvav 148,148,20\nQmvav 152,152,20\nQe 8\nQmvav 156,156,20\ns_add_u32 m0, s61, 74240\nQmvav 160,160,20\n"
"buffer_load_dwordx4 v58, s[44:47], s60 offen nt lds\nQmvav 164,164,20\nQe 9\nQmvav 168,168,20\nQe 10\nQmvav 172,172,20\nQe 11\nQcvaa 0,112,80,44\n"
"Qk 20,8,9\nQk 21,10,11\nQmvaa 0,116,84\nbuffer_load_dwordx4 v59, s[44:47], s60 offen offset:1024 nt lds\nQmvaa 0,120,88\nQe 12\nQmvaa 0,124,92\nQe 13\n"
"Qcvaa 4,128,80,44\nQe 14\nQmvaa 4,132,84\nQe 15\nQmvaa 4,136,88\nbuffer_load_dwordx4 v58, s[44:47], s60 offen offset:2048 nt lds\nQmvaa 4,140,92\nQk 22,12,13\n"
"Qk 23,14,15\nQmvav 176,144,16\nQmvav 180,148,16\nQmvav 184,152,16\nQmvav 188,156,16\nbuffer_load_dwordx4 v59, s[44:47], s60 offen offset:3072 nt lds\nQmvav 192,160,16\nQe 0\n"
"Qmvav 196,164,16\nQe 1\nQmvav 200,168,16\ns_add_u32 s60, s60, 8192\nQmvav 204,172,16\nQe 2\nQcvaa 8,112,96,48\nQda 112,52,0\n"
"Qmvaa 8,116,100\nQda 116,52,512\nQmvaa 8,120,104\nQda 120,52,1024\nQmvaa 8,124,108\nQda 124,52,1536\nQcvaa 12,128,96,48\nQda 128,52,2048\n"
"Qmvaa 12,132,100\nQda 132,52,2560\nv_smfmac_f32_16x16x64_bf16 a[196:199], a[204:207], v[16:23], v77\nQmvaa 12,136,104\nQda 136,52,3072\nQk 16,0,1\nQmvaa 12,140,108\nQda 140,52,3584\n"
"Qmvav 208,144,20\ns_memrealtime s[74:75]\nQmvav 212,148,20\nQe 3\nQmvav 216,152,20\nQk 17,2,3\nQmvav 220,156,20\nQe 4\n"
"Qmvav 224,160,20\nQe 5\nQmvav 228,164,20\nQe 6\nQmvav 232,168,20\nQe 7\nQmvav 236,172,20\ns_waitcnt lgkmcnt(0)\n"
"Qk 18,4,5\nQcvaa 0,112,0,24\nQk 19,6,7\nQmvaa 0,116,4\nQe 8\nQmvaa 0,120,8\nQe 9\nQmvaa 0,124,12\n"
"Qe 10\nQcvaa 4,128,0,24\nQe 11\nQmvaa 4,132,4\nQk 20,8,9\nQk 21,10,11\nQmvaa 4,136,8\nQe 12\n"
"Qmvaa 4,140,12\nQe 13\nQmvav 240,144,16\nQe 14\nQmvav 244,148,16\ns_waitcnt lgkmcnt(0)\ns_sub_u32 s75, s77, s74\nQmvav 248,152,16\n"
"s_cmp_lt_u32 s75, 256\ns_cbranch_scc1 .Lsp3\n.Lrp3:\nQmvav 252,156,16\ns_add_u32 s77, s74, s73\nQk 22,12,13\nQmaav 208,160,16\ns_sub_u32 s73, s73, 4\n"
"Qmaav 212,164,16\ns_max_u32 s73, s73, 120\nQmaav 216,168,16\nQe 15\nQmaav 220,172,16\nQk 23,14,15\nQcvaa 8,112,16,28\nQe 0\n"
"Qmvaa 8,116,20\nQe 1\nQmvaa 8,120,24\nQe 2\nQmvaa 8,124,28\nQe 3\nQcvaa 12,128,16,28\nQe 4\n"
"Qmvaa 12,132,20\nQe 5\nv_smfmac_f32_16x16x64_bf16 a[200:203], a[204:207], v[16:23], v77\nQmvaa 12,136,24\nQk 16,0,1\nQk 17,2,3\nQmvaa 12,140,28\nQe 6\n"
"Qmaav 224,144,20\nQta 144,54,0\nQta 146,54,2048\nQmaav 228,148,20\nQta 148,54,256\nQta 150,54,2304\nQmaav 232,152,20\nQta 152,54,512\n"
"Qta 154,54,2560\nQmaav 236,156,20\nQta 156,54,768\nQta 158,54,2816\nQmaav 240,160,20\nQta 160,54,1024\nQta 162,54,3072\nQmaav 244,164,20\n"
"Qta 164,54,1280\nQta 166,54,3328\nQmaav 248,168,20\nQta 168,54,1536\nQta 170,54,3584\nQmaav 252,172,20\nQta 172,54,1792\nQta 174,54,3840\n"
"Qcvaa 0,112,32,32\nv_add_u32 v75, 1, v75\nQk 18,4,5\nQmvaa 0,116,36\ns_waitcnt vmcnt(8)\nds_write_b32 v73, v75\nQmvaa 0,120,40\nQe 7\n"
"Qmvaa 0,124,44\nQk 19,6,7\nQcvaa 4,128,32,32\nQe 8\nQmvaa 4,132,36\nQe 9\nQmvaa 4,136,40\nQe 10\n"
"Qmvaa 4,140,44\ns_waitcnt lgkmcnt(1)\nQk 20,8,9\nQmvav 80,144,16\nQe 11\nQmvav 84,148,16\nQk 21,10,11\nQmvav 88,152,16\n"
"Qe 12\nQmvav 92,156,16\nQe 13\nQmvav 96,160,16\nQe 14\nQmvav 100,164,16\nQe 15\nQmvav 104,168,16\n"
"Qk 22,12,13\nQk 23,14,15\nQmvav 108,172,16\nQe 0\nQcvaa 8,112,48,36\nQe 1\nQmvaa 8,116,52\nQe 2\n"
"Qmvaa 8,120,56\nds_read_b32 v69, v74\nQmvaa 8,124,60\nQe 3\nQcvaa 12,128,48,36\nQe 4\nQmvaa 12,132,52\nQe 5\n"
"v_smfmac_f32_16x16x64_bf16 a[176:179], a[204:207], v[16:23], v77\nQmvaa 12,136,56\ns_waitcnt lgkmcnt(0)\nv_cmp_gt_u32 vcc, v75, v69\nQmvaa 12,140,60\ns_cbranch_vccnz .Lsa4\n.Lra4:\nQk 16,0,1\n"
"Qmvav 112,144,20\ns_add_u32 m0, s61, 99072\nQk 17,2,3\nQmvav 116,148,20\nbuffer_load_dwordx4 v56, s[40:43], s60 offen nt lds\nQmvav 120,152,20\nQe 6\nQmvav 124,156,20\n"
"Qe 7\nQmvav 128,160,20\nQk 18,4,5\nQk 19,6,7\nQmvav 132,164,20\nQe 8\nQmvav 136,168,20\nbuffer_load_dwordx4 v57, s[40:43], s60 offen offset:1024 nt lds\n"
"Qmvav 140,172,20\nQe 9\nQcvaa 0,112,64,40\nQe 10\nQmvaa 0,116,68\nQe 11\nQmvaa 0,120,72\nQk 20,8,9\n"
"Qk 21,10,11\nQmvaa 0,124,76\nbuffer_load_dwordx4 v56, s[40:43], s60 offen offset:2048 nt lds\nQcvaa 4,128,64,40\nQe 12\nQmvaa 4,132,68\nQe 13\nQmvaa 4,136,72\n"
"Qe 14\nQmvaa 4,140,76\nQe 15\nQmvav 144,144,16\nbuffer_load_dwordx4 v57, s[40:43], s60 offen offset:3072 nt lds\nQmvav 148,148,16\nQk 22,12,13\nQk 23,14,15\n"
"Qmvav 152,152,16\nQmvav 156,156,16\nQe 0\nQmvav 160,160,16\ns_add_u32 m0, s61, 107264\nQmvav 164,164,16\nbuffer_load_dwordx4 v58, s[44:47], s60 offen nt lds\nQmvav 168,168,16\n"
"Qe 1\nQmvav 172,172,16\nQe 2\nQcvaa 8,112,80,44\nQe 3\nQmvaa 8,116,84\nQe 4\nQmvaa 8,120,88\n"
"buffer_load_dwordx4 v59, s[44:47], s60 offen offset:1024 nt lds\nQmvaa 8,124,92\nQe 5\nQcvaa 12,128,80,44\nQe 6\nQmvaa 12,132,84\nQe 7\nv_smfmac_f32_16x16x64_bf16 a[180:183], a[204:207], v[16:23], v77\n"
"Qmvaa 12,136,88\nbuffer_load_dwordx4 v58, s[44:47], s60 offen offset:2048 nt lds\nQmvaa 12,140,92\nQk 16,0,1\nQk 17,2,3\nQmvav 176,144,20\nQk 18,4,5\nQk 19,6,7\n"
"Qmvav 180,148,20\nQmvav 184,152,20\nQe 8\nQmvav 188,156,20\nbuffer_load_dwordx4 v59, s[44:47], s60 offen offset:3072 nt lds\nQmvav 192,160,20\nQe 9\nQmvav 196,164,20\n"
"Qe 10\nQmvav 200,168,20\ns_add_u32 s60, s60, 8192\ns_and_b32 s68, s60, 0x40000\ns_mul_i32 s68, s68, 7\ns_add_u32 s60, s60, s68\nQmvav 204,172,20\nQe 11\n"
"Qcvaa 0,112,96,48\nQda 112,52,33024\nQk 20,8,9\nQmvaa 0,116,100\nQda 116,52,33536\nQk 21,10,11\nQmvaa 0,120,104\nQda 120,52,34048\n"
"Qmvaa 0,124,108\nQda 124,52,34560\nQcvaa 4,128,96,48\nQda 128,52,35072\nQmvaa 4,132,100\nQda 132,52,35584\nQmvaa 4,136,104\nQda 136,52,36096\n"
"Qmvaa 4,140,108\nQda 140,52,36608\nQmvav 208,144,16\nQe 12\nQmvav 212,148,16\ns_memrealtime s[74:75]\nQmvav 216,152,16\nQe 13\n"
"Qmvav 220,156,16\nQe 14\nQmvav 224,160,16\nQe 15\nQmvav 228,164,16\nQk 22,12,13\nQk 23,14,15\nQmvav 232,168,16\n"
"Qe 0\nQmvav 236,172,16\ns_waitcnt lgkmcnt(0)\nQcvaa 8,112,0,24\nQe 1\nQmvaa 8,116,4\nQe 2\nQmvaa 8,120,8\n"
"Qe 3\nQmvaa 8,124,12\nQe 4\nQcvaa 12,128,0,24\nQe 5\nQmvaa 12,132,4\nQe 6\nv_smfmac_f32_16x16x64_bf16 a[184:187], a[204:207], v[16:23], v77\n"
"Qmvaa 12,136,8\nQk 16,0,1\nQk 17,2,3\nQmvaa 12,140,12\nQe 7\nQmvav 240,144,20\ns_waitcnt lgkmcnt(0)\ns_sub_u32 s75, s77, s74\n"
"Qmvav 244,148,20\ns_cmp_lt_u32 s75, 256\ns_cbranch_scc1 .Lsp4\n.Lrp4:\nQmvav 248,152,20\ns_add_u32 s77, s74, s73\nQk 18,4,5\nQmvav 252,156,20\n"
"s_sub_u32 s73, s73, 4\nQk 19,6,7\nQmaav 208,160,20\ns_max_u32 s73, s73, 120\nQmaav 212,164,20\nQe 8\nQmaav 216,168,20\nQe 9\n"
"Qmaav 220,172,20\nQe 10\nQcvaa 0,112,16,28\nQe 11\nQmvaa 0,116,20\nQk 20,8,9\nQk 21,10,11\nQmvaa 0,120,24\n"
"Qe 12\nQmvaa 0,124,28\nQe 13\nQcvaa 4,128,16,28\nQe 14\nQmvaa 4,132,20\nQe 15\nQmvaa 4,136,24\n"
"Qk 22,12,13\nQk 23,14,15\nQmvaa 4,140,28\nQmaav 224,144,16\nQta 144,54,33024\nQta 146,54,35072\nQmaav 228,148,16\nQta 148,54,33280\n"
"Qta 150,54,35328\nQmaav 232,152,16\nQta 152,54,33536\nQta 154,54,35584\nQmaav 236,156,16\nQta 156,54,33792\nQta 158,54,35840\nQmaav 240,160,16\n"
"Qta 160,54,34048\nQta 162,54,36096\nQmaav 244,164,16\nQta 164,54,34304\nQta 166,54,36352\nQmaav 248,168,16\nQta 168,54,34560\nQta 170,54,36608\n"
"Qmaav 252,172,16\nQta 172,54,34816\nQta 174,54,36864\ns_add_u32 s66, s66, 1\ns_bitcmp1_b32 0x7f7f, s66\ns_cbranch_scc1 .Lloop\ns_cmp_lg_u32 s67, 0\ns_cbranch_scc1 .Ltail1\n"
"Qcvaa 8,112,32,32\nv_add_u32 v75, 1, v75\nQmvaa 8,116,36\ns_waitcnt vmcnt(8)\nds_write_b32 v73, v75\nQmvaa 8,120,40\nQe 0\nQmvaa 8,124,44\n"
"Qe 1\nQcvaa 12,128,32,32\nQe 2\nQmvaa 12,132,36\nQe 3\nv_smfmac_f32_16x16x64_bf16 a[188:191], a[204:207], v[16:23], v77\nQmvaa 12,136,40\ns_waitcnt lgkmcnt(1)\n"
"Qk 16,0,1\nQmvaa 12,140,44\nQk 17,2,3\nQmvav 80,144,20\nQe 4\nQmvav 84,148,20\nQe 5\nQmvav 88,152,20\n"
"Qe 6\nQmvav 92,156,20\nQe 7\nQmvav 96,160,20\nQk 18,4,5\nQk 19,6,7\nQmvav 100,164,20\nQe 8\n"
"Qmvav 104,168,20\nQe 9\nQmvav 108,172,20\nQe 10\nQcvaa 0,112,48,36\nQe 11\nQmvaa 0,116,52\nds_read_b32 v69, v74\n"
"Qk 20,8,9\nQmvaa 0,120,56\nQk 21,10,11\nQmvaa 0,124,60\nQe 12\nQcvaa 4,128,48,36\nQe 13\nQmvaa 4,132,52\n"
"Qe 14\nQmvaa 4,136,56\ns_waitcnt lgkmcnt(0)\nv_cmp_gt_u32 vcc, v75, v69\nQmvaa 4,140,60\ns_cbranch_vccnz .Lsa29\n.Lra29:\nQk 22,12,13\n"
"Qmvav 112,144,16\ns_add_u32 m0, s62, 0\ns_add_u32 s69, s87, 0\nQmvav 116,148,16\nbuffer_load_dwordx4 v62, s[52:55], s69 offen lds\nQmvav 120,152,16\nQe 15\nQmvav 124,156,16\n"
"Qk 23,14,15\nQmvav 128,160,16\nQe 0\nQmvav 132,164,16\ns_add_u32 s69, s87, 31744\nQe 1\nQmvav 136,168,16\nbuffer_load_dwordx4 v62, s[52:55], s69 offen offset:1024 lds\n"
"Qmvav 140,172,16\nQe 2\nQcvaa 8,112,64,40\nQe 3\nQmvaa 8,116,68\nQe 4\nQmvaa 8,120,72\ns_add_u32 s69, s87, 63480\n"
"Qe 5\nQmvaa 8,124,76\nbuffer_load_dwordx4 v62, s[52:55], s69 offen offset:2056 lds\nQcvaa 12,128,64,40\nQe 6\nQmvaa 12,132,68\nQe 7\nv_smfmac_f32_16x16x64_bf16 a[192:195], a[204:207], v[16:23], v77\n"
"Qmvaa 12,136,72\ns_add_u32 s69, s87, 95224\nQk 16,0,1\nQk 17,2,3\nQmvaa 12,140,76\nbuffer_load_dwordx4 v62, s[52:55], s69 offen offset:3080 lds\nQmvav 144,144,20\nQk 18,4,5\n"
"Qk 19,6,7\nQmvav 148,148,20\nQmvav 152,152,20\nQe 8\nQmvav 156,156,20\ns_add_u32 m0, s62, 16448\ns_add_u32 s69, s87, 262144\nQmvav 160,160,20\n"
"buffer_load_dwordx4 v62, s[52:55], s69 offen lds\nQmvav 164,164,20\nQe 9\nQmvav 168,168,20\nQe 10\n.Lpg7:\ns_cbranch_execz .Lpg11\nQmvav 172,172,20\n"
"Qe 11\nQcvaa 0,112,80,44\ns_add_u32 s69, s87, 293888\nQk 20,8,9\nQk 21,10,11\nQmvaa 0,116,84\nbuffer_load_dwordx4 v62, s[52:55], s69 offen offset:1024 lds\nQmvaa 0,120,88\n"
"Qe 12\nQmvaa 0,124,92\nQe 13\nQcvaa 4,128,80,44\nQe 14\nQmvaa 4,132,84\ns_add_u32 s69, s87, 325624\nQe 15\n"
"Qmvaa 4,136,88\nbuffer_load_dwordx4 v62, s[52:55], s69 offen offset:2056 lds\nQmvaa 4,140,92\nQk 22,12,13\nQk 23,14,15\nQmvav 176,144,16\nQmvav 180,148,16\nQmvav 184,152,16\n"
"s_add_u32 s69, s87, 357368\nQmvav 188,156,16\nbuffer_load_dwordx4 v62, s[52:55], s69 offen offset:3080 lds\nQmvav 192,160,16\nQe 0\nQmvav 196,164,16\nQe 1\nQmvav 200,168,16\n"
"Qmvav 204,172,16\nQe 2\nQcvaa 8,112,96,48\nQda 112,53,0\nQmvaa 8,116,100\nQda 116,53,512\nQmvaa 8,120,104\nQda 120,53,1024\n"
"Qmvaa 8,124,108\nQda 124,53,1536\nQcvaa 12,128,96,48\nQda 128,53,2048\nQmvaa 12,132,100\nQda 132,53,2560\nv_smfmac_f32_16x16x64_bf16 a[196:199], a[204:207], v[16:23], v77\nQmvaa 12,136,104\n"
"Qda 136,53,3072\nQk 16,0,1\nQmvaa 12,140,108\nQda 140,53,3584\nQmvav 208,144,20\ns_add_u32 m0, s63, 33024\ns_add_u32 s88, s86, 0\nQmvav 212,148,20\n"
"buffer_load_dwordx4 v64, s[48:51], s88 offen lds\nQe 3\nQmvav 216,152,20\nQk 17,2,3\nQmvav 220,156,20\nQe 4\nQmvav 224,160,20\ns_add_u32 s88, s86, 19456\n"
"Qe 5\nQmvav 228,164,20\nbuffer_load_dwordx4 v64, s[48:51], s88 offen offset:1024 lds\nQe 6\nQmvav 232,168,20\nQe 7\nQmvav 236,172,20\ns_waitcnt lgkmcnt(0)\n"
"Qk 18,4,5\nQcvaa 0,112,0,24\ns_add_u32 s88, s86, 38912\nQk 19,6,7\nQmvaa 0,116,4\nbuffer_load_dwordx4 v64, s[48:51], s88 offen offset:2048 lds\nQe 8\nQmvaa 0,120,8\n"
"Qe 9\nQmvaa 0,124,12\nQe 10\nQcvaa 4,128,0,24\ns_add_u32 s88, s86, 58368\nQe 11\nQmvaa 4,132,4\nbuffer_load_dwordx4 v64, s[48:51], s88 offen offset:3072 lds\n"
"Qk 20,8,9\nQk 21,10,11\nQmvaa 4,136,8\nQe 12\nQmvaa 4,140,12\nQe 13\nQmvav 240,144,16\ns_add_u32 m0, s63, 37120\n"
"s_add_u32 s88, s86, 81920\nQe 14\nQmvav 244,148,16\nbuffer_load_dwordx4 v64, s[48:51], s88 offen lds\nQmvav 248,152,16\nQmvav 252,156,16\nQk 22,12,13\nQmaav 208,160,16\n"
"s_add_u32 s88, s86, 101376\nQmaav 212,164,16\nbuffer_load_dwordx4 v64, s[48:51], s88 offen offset:1024 lds\nQmaav 216,168,16\nQe 15\nQmaav 220,172,16\nQk 23,14,15\nQcvaa 8,112,16,28\n"
"s_add_u32 s88, s86, 120832\nQe 0\nQmvaa 8,116,20\nbuffer_load_dwordx4 v64, s[48:51], s88 offen offset:2048 lds\nQe 1\nQmvaa 8,120,24\nQe 2\nQmvaa 8,124,28\n"
"Qe 3\nQcvaa 12,128,16,28\nQe 4\nQmvaa 12,132,20\nQe 5\nv_smfmac_f32_16x16x64_bf16 a[200:203], a[204:207], v[16:23], v77\nQmvaa 12,136,24\nQk 16,0,1\n"
"Qk 17,2,3\nQmvaa 12,140,28\nQe 6\nQmaav 224,144,20\nQta 144,55,0\nQta 146,55,2048\nQmaav 228,148,20\nQta 148,55,256\n"
"Qta 150,55,2304\nQmaav 232,152,20\nQta 152,55,512\nQta 154,55,2560\nQmaav 236,156,20\nQta 156,55,768\nQta 158,55,2816\nQmaav 240,160,20\n"
"Qta 160,55,1024\nQta 162,55,3072\nQmaav 244,164,20\nQta 164,55,1280\nQta 166,55,3328\nQmaav 248,168,20\nQta 168,55,1536\nQta 170,55,3584\n"
"Qmaav 252,172,20\nQta 172,55,1792\nQta 174,55,3840\nQcvaa 0,112,32,32\nv_add_u32 v75, 1, v75\nQk 18,4,5\nQmvaa 0,116,36\ns_waitcnt vmcnt(15)\n"
"ds_write_b32 v73, v75\nQmvaa 0,120,40\nQe 7\nQmvaa 0,124,44\nQk 19,6,7\nQcvaa 4,128,32,32\nQe 8\nQmvaa 4,132,36\n"
"Qe 9\nQmvaa 4,136,40\nQe 10\nQmvaa 4,140,44\ns_waitcnt lgkmcnt(1)\nQk 20,8,9\nQmvav 80,144,16\nQe 11\n"
"Qmvav 84,148,16\nQk 21,10,11\nQmvav 88,152,16\nQe 12\nQmvav 92,156,16\nQe 13\nQmvav 96,160,16\nQe 14\n"
"Qmvav 100,164,16\nQe 15\nQmvav 104,168,16\nQk 22,12,13\nQk 23,14,15\nQmvav 108,172,16\nQe 0\nQcvaa 8,112,48,36\n"
"Qe 1\nQmvaa 8,116,52\nQe 2\nQmvaa 8,120,56\nds_read_b32 v69, v74\nQmvaa 8,124,60\nQe 3\nQcvaa 12,128,48,36\n"
"Qe 4\nQmvaa 12,132,52\nQe 5\nv_smfmac_f32_16x16x64_bf16 a[176:179], a[204:207], v[16:23], v77\nQmvaa 12,136,56\ns_waitcnt lgkmcnt(0)\nv_cmp_gt_u32 vcc, v75, v69\nQmvaa 12,140,60\n"
"s_cbranch_vccnz .Lsa30\n.Lra30:\nQk 16,0,1\nQmvav 112,144,20\ns_add_u32 m0, s62, 33024\ns_add_u32 s69, s87, 524288\nQk 17,2,3\nQmvav 116,148,20\n"
"buffer_load_dwordx4 v62, s[52:55], s69 offen lds\nQmvav 120,152,20\nQe 6\nQmvav 124,156,20\nQe 7\nQmvav 128,160,20\nQk 18,4,5\nQk 19,6,7\n"
"Qmvav 132,164,20\ns_add_u32 s69, s87, 556032\nQe 8\nQmvav 136,168,20\nbuffer_load_dwordx4 v62, s[52:55], s69 offen offset:1024 lds\nQmvav 140,172,20\nQe 9\nQcvaa 0,112,64,40\n"
"Qe 10\nQmvaa 0,116,68\nQe 11\nQmvaa 0,120,72\ns_add_u32 s69, s87, 587768\nQk 20,8,9\nQk 21,10,11\nQmvaa 0,124,76\n"
"buffer_load_dwordx4 v62, s[52:55], s69 offen offset:2056 lds\nQcvaa 4,128,64,40\nQe 12\nQmvaa 4,132,68\nQe 13\nQmvaa 4,136,72\nQe 14\nQmvaa 4,140,76\n"
"s_add_u32 s69, s87, 619512\nQe 15\nQmvav 144,144,16\nbuffer_load_dwordx4 v62, s[52:55], s69 offen offset:3080 lds\nQmvav 148,148,16\nQk 22,12,13\nQk 23,14,15\nQmvav 152,152,16\n"
"Qmvav 156,156,16\nQe 0\nQmvav 160,160,16\ns_add_u32 m0, s62, 49472\ns_add_u32 s69, s87, 786432\nQmvav 164,164,16\nbuffer_load_dwordx4 v62, s[52:55], s69 offen lds\nQmvav 168,168,16\n"
"Qe 1\nQmvav 172,172,16\nQe 2\nQcvaa 8,112,80,44\nQe 3\nQmvaa 8,116,84\ns_add_u32 s69, s87, 818176\nQe 4\n"
"Qmvaa 8,120,88\nbuffer_load_dwordx4 v62, s[52:55], s69 offen offset:1024 lds\nQmvaa 8,124,92\nQe 5\nQcvaa 12,128,80,44\nQe 6\nQmvaa 12,132,84\nQe 7\n"
"v_smfmac_f32_16x16x64_bf16 a[180:183], a[204:207], v[16:23], v77\ns_add_u32 s69, s87, 849912\nQmvaa 12,136,88\nbuffer_load_dwordx4 v62, s[52:55], s69 offen offset:2056 lds\nQmvaa 12,140,92\nQk 16,0,1\nQk 17,2,3\nQmvav 176,144,20\n"
"Qk 18,4,5\nQk 19,6,7\nQmvav 180,148,20\nQmvav 184,152,20\ns_add_u32 s69, s87, 881656\nQe 8\nQmvav 188,156,20\nbuffer_load_dwordx4 v62, s[52:55], s69 offen offset:3080 lds\n"
"Qmvav 192,160,20\nQe 9\nQmvav 196,164,20\nQe 10\nQmvav 200,168,20\nQmvav 204,172,20\nQe 11\nQcvaa 0,112,96,48\n"
"Qda 112,53,33024\nQk 20,8,9\nQmvaa 0,116,100\nQda 116,53,33536\nQk 21,10,11\nQmvaa 0,120,104\nQda 120,53,34048\nQmvaa 0,124,108\n"
"Qda 124,53,34560\nQcvaa 4,128,96,48\nQda 128,53,35072\nQmvaa 4,132,100\nQda 132,53,35584\nQmvaa 4,136,104\nQda 136,53,36096\nQmvaa 4,140,108\n"
"Qda 140,53,36608\nQmvav 208,144,16\nQe 12\nQmvav 212,148,16\ns_add_u32 m0, s61, 0\nQmvav 216,152,16\nbuffer_load_dwordx4 v56, s[40:43], s60 offen nt lds\nQe 13\n"
"Qmvav 220,156,16\nQe 14\nQmvav 224,160,16\nQe 15\nQmvav 228,164,16\nQk 22,12,13\nQk 23,14,15\nQmvav 232,168,16\n"
"buffer_load_dwordx4 v57, s[40:43], s60 offen offset:1024 nt lds\nQe 0\nQmvav 236,172,16\ns_waitcnt lgkmcnt(0)\nQcvaa 8,112,0,24\nQe 1\nQmvaa 8,116,4\nQe 2\n"
"Qmvaa 8,120,8\nbuffer_load_dwordx4 v56, s[40:43], s60 offen offset:2048 nt lds\nQe 3\nQmvaa 8,124,12\nQe 4\nQcvaa 12,128,0,24\nQe 5\nQmvaa 12,132,4\n"
"Qe 6\nv_smfmac_f32_16x16x64_bf16 a[184:187], a[204:207], v[16:23], v77\nQmvaa 12,136,8\nbuffer_load_dwordx4 v57, s[40:43], s60 offen offset:3072 nt lds\nQk 16,0,1\nQk 17,2,3\nQmvaa 12,140,12\nQe 7\n"
"Qmvav 240,144,20\ns_add_u32 m0, s61, 8192\nQmvav 244,148,20\nbuffer_load_dwordx4 v58, s[44:47], s60 offen nt lds\nQmvav 248,152,20\nQk 18,4,5\nQmvav 252,156,20\nQk 19,6,7\n"
"Qmaav 208,160,20\nQmaav 212,164,20\nbuffer_load_dwordx4 v59, s[44:47], s60 offen offset:1024 nt lds\nQe 8\nQmaav 216,168,20\nQe 9\nQmaav 220,172,20\nQe 10\n"
"Qcvaa 0,112,16,28\nQe 11\nQmvaa 0,116,20\nbuffer_load_dwordx4 v58, s[44:47], s60 offen offset:2048 nt lds\nQk 20,8,9\nQk 21,10,11\nQmvaa 0,120,24\nQe 12\n"
"Qmvaa 0,124,28\nQe 13\nQcvaa 4,128,16,28\nQe 14\nQmvaa 4,132,20\nbuffer_load_dwordx4 v59, s[44:47], s60 offen offset:3072 nt lds\nQe 15\nQmvaa 4,136,24\n"
"Qk 22,12,13\nQk 23,14,15\nQmvaa 4,140,28\ns_add_u32 s60, s60, 8192\nQmaav 224,144,16\nQta 144,55,33024\nQta 146,55,35072\nQmaav 228,148,16\n"
"Qta 148,55,33280\nQta 150,55,35328\nQmaav 232,152,16\nQta 152,55,33536\nQta 154,55,35584\nQmaav 236,156,16\nQta 156,55,33792\nQta 158,55,35840\n"
"Qmaav 240,160,16\nQta 160,55,34048\nQta 162,55,36096\nQmaav 244,164,16\nQta 164,55,34304\nQta 166,55,36352\nQmaav 248,168,16\nQta 168,55,34560\n"
"Qta 170,55,36608\nQmaav 252,172,16\nQta 172,55,34816\nQta 174,55,36864\nQcvaa 8,112,32,32\nv_add_u32 v75, 1, v75\nQmvaa 8,116,36\nds_write_b32 v73, v75\n"
"Qmvaa 8,120,40\nQe 0\nQmvaa 8,124,44\nQe 1\nQcvaa 12,128,32,32\nQe 2\nQmvaa 12,132,36\nQe 3\n"
"v_smfmac_f32_16x16x64_bf16 a[188:191], a[204:207], v[16:23], v77\nQmvaa 12,136,40\ns_waitcnt lgkmcnt(1)\nQk 16,0,1\nQmvaa 12,140,44\nQk 17,2,3\nQmvav 80,144,20\nQe 4\n"
"Qmvav 84,148,20\nQe 5\nQmvav 88,152,20\nQe 6\nQmvav 92,156,20\nQe 7\nQmvav 96,160,20\nQk 18,4,5\n"
"Qk 19,6,7\nQmvav 100,164,20\nQe 8\nQmvav 104,168,20\nQe 9\nQmvav 108,172,20\nQe 10\nQcvaa 0,112,48,36\n"
"Qe 11\nQmvaa 0,116,52\nds_read_b32 v69, v74\nQk 20,8,9\nQmvaa 0,120,56\nQk 21,10,11\nQmvaa 0,124,60\nQe 12\n"
"Qcvaa 4,128,48,36\nQe 13\nQmvaa 4,132,52\nQe 14\nQmvaa 4,136,56\ns_waitcnt lgkmcnt(0)\nv_cmp_gt_u32 vcc, v75, v69\nQmvaa 4,140,60\n"
"s_cbranch_vccnz .Lsa31\n.Lra31:\nQk 22,12,13\nQmvav 112,144,16\ns_add_u32 m0, s63, 0\ns_add_u32 s88, s86, 128\nQmvav 116,148,16\nbuffer_load_dwordx4 v64, s[48:51], s88 offen lds\n"
"Qmvav 120,152,16\nQe 15\nQmvav 124,156,16\nQk 23,14,15\nQmvav 128,160,16\ns_add_u32 s88, s86, 19584\nQe 0\nQmvav 132,164,16\n"
"buffer_load_dwordx4 v64, s[48:51], s88 offen offset:1024 lds\nQe 1\nQmvav 136,168,16\nQmvav 140,172,16\nQe 2\nQcvaa 8,112,64,40\ns_add_u32 s88, s86, 39040\nQe 3\n"
"Qmvaa 8,116,68\nbuffer_load_dwordx4 v64, s[48:51], s88 offen offset:2048 lds\nQe 4\nQmvaa 8,120,72\nQe 5\nQmvaa 8,124,76\nQcvaa 12,128,64,40\ns_add_u32 s88, s86, 58496\n"
"Qe 6\nQmvaa 12,132,68\nbuffer_load_dwordx4 v64, s[48:51], s88 offen offset:3072 lds\nQe 7\nv_smfmac_f32_16x16x64_bf16 a[192:195], a[204:207], v[16:23], v77\nQmvaa 12,136,72\nQk 16,0,1\nQk 17,2,3\n"
"Qmvaa 12,140,76\ns_add_u32 m0, s63, 4096\ns_add_u32 s88, s86, 82048\nQmvav 144,144,20\nbuffer_load_dwordx4 v64, s[48:51], s88 offen lds\nQk 18,4,5\nQk 19,6,7\nQmvav 148,148,20\n"
"Qmvav 152,152,20\nQe 8\nQmvav 156,156,20\ns_add_u32 s88, s86, 101504\nQmvav 160,160,20\nbuffer_load_dwordx4 v64, s[48:51], s88 offen offset:1024 lds\nQmvav 164,164,20\nQe 9\n"
"Qmvav 168,168,20\nQe 10\nQmvav 172,172,20\ns_add_u32 s88, s86, 120960\nQe 11\nQcvaa 0,112,80,44\nbuffer_load_dwordx4 v64, s[48:51], s88 offen offset:2048 lds\nQk 20,8,9\n"
"Qk 21,10,11\nQmvaa 0,116,84\nQmvaa 0,120,88\nQe 12\nQmvaa 0,124,92\nQe 13\nQcvaa 4,128,80,44\nQe 14\n"
"Qmvaa 4,132,84\nQe 15\nQmvaa 4,136,88\nQmvaa 4,140,92\nQk 22,12,13\nQk 23,14,15\nQmvav 176,144,16\nQmvav 180,148,16\n"
"Qmvav 184,152,16\nQmvav 188,156,16\nQmvav 192,160,16\nQe 0\nQmvav 196,164,16\nQe 1\nQmvav 200,168,16\nQmvav 204,172,16\n"
"Qe 2\nQcvaa 8,112,96,48\nQmvaa 8,116,100\nQmvaa 8,120,104\nQmvaa 8,124,108\nQcvaa 12,128,96,48\nQmvaa 12,132,100\nv_smfmac_f32_16x16x64_bf16 a[196:199], a[204:207], v[16:23], v77\n"
"Qmvaa 12,136,104\nQk 16,0,1\nQmvaa 12,140,108\nQmvav 208,144,20\nQmvav 212,148,20\nQe 3\nQmvav 216,152,20\nQk 17,2,3\n"
"Qmvav 220,156,20\nQe 4\nQmvav 224,160,20\nQe 5\nQmvav 228,164,20\nQe 6\nQmvav 232,168,20\n.Lpg8:\n"
"s_cbranch_execz .Ltr0\nQe 7\nQmvav 236,172,20\nQk 18,4,5\nQk 19,6,7\nQe 8\nQe 9\nQe 10\n"
"Qe 11\nQk 20,8,9\nQk 21,10,11\nQe 12\nQe 13\nQmvav 240,144,16\nQe 14\nQmvav 244,148,16\n"
"Qmvav 248,152,16\nQmvav 252,156,16\nQk 22,12,13\nQmaav 208,160,16\nQmaav 212,164,16\nQmaav 216,168,16\nQe 15\nQmaav 220,172,16\n"
"Qk 23,14,15\ns_nop 1\nv_smfmac_f32_16x16x64_bf16 a[200:203], a[204:207], v[16:23], v77\nQmaav 224,144,20\nQmaav 228,148,20\nQmaav 232,152,20\nQmaav 236,156,20\nQmaav 240,160,20\n"
"Qmaav 244,164,20\nQmaav 248,168,20\nQmaav 252,172,20\nv_accvgpr_read_b32 v24, a176\nv_accvgpr_read_b32 v25, a177\nv_accvgpr_read_b32 v30, a178\nv_accvgpr_read_b32 v31, a179\nv_accvgpr_read_b32 v32, a180\n"
"v_accvgpr_read_b32 v33, a181\nv_accvgpr_read_b32 v38, a182\nv_accvgpr_read_b32 v39, a183\nv_accvgpr_read_b32 v40, a184\nv_accvgpr_read_b32 v41, a185\nv_accvgpr_read_b32 v46, a186\nv_accvgpr_read_b32 v47, a187\nv_accvgpr_read_b32 v48, a188\n"
"v_accvgpr_read_b32 v49, a189\nv_accvgpr_read_b32 v26, a190\nv_accvgpr_read_b32 v27, a191\nv_accvgpr_read_b32 v28, a192\nv_accvgpr_read_b32 v29, a193\nv_accvgpr_read_b32 v34, a194\nv_accvgpr_read_b32 v35, a195\nv_accvgpr_read_b32 v36, a196\n"
"v_accvgpr_read_b32 v37, a197\nv_accvgpr_read_b32 v42, a198\nv_accvgpr_read_b32 v43, a199\nv_accvgpr_read_b32 v44, a200\nv_accvgpr_read_b32 v45, a201\nv_accvgpr_read_b32 v50, a202\nv_accvgpr_read_b32 v51, a203\nv_pk_add_f32 v[24:25], v[24:25], v[26:27]\n"
"v_pk_add_f32 v[28:29], v[28:29], v[30:31]\nv_pk_add_f32 v[32:33], v[32:33], v[34:35]\nv_pk_add_f32 v[36:37], v[36:37], v[38:39]\nv_pk_add_f32 v[40:41], v[40:41], v[42:43]\nv_pk_add_f32 v[44:45], v[44:45], v[46:47]\nv_pk_add_f32 v[48:49], v[48:49], v[50:51]\nQa 24,24,25\nQa 28,28,29\n"
"Qa 32,32,33\nQa 36,36,37\nQa 40,40,41\nQa 44,44,45\nQa 48,48,49\nv_cmp_nge_f32 vcc, 0x44800000, v24\ns_mov_b64 s[74:75], vcc\nv_cmp_nge_f32 vcc, 0x44800000, v28\n"
"s_or_b64 s[74:75], s[74:75], vcc\nv_cmp_nge_f32 vcc, 0x44800000, v32\ns_or_b64 s[74:75], s[74:75], vcc\nv_cmp_nge_f32 vcc, 0x44800000, v36\ns_or_b64 s[74:75], s[74:75], vcc\nv_cmp_nge_f32 vcc, 0x44800000, v40\ns_or_b64 s[74:75], s[74:75], vcc\nv_cmp_nge_f32 vcc, 0x44800000, v44\n"
"s_or_b64 s[74:75], s[74:75], vcc\nv_cmp_nge_f32 vcc, 0x44800000, v48\ns_or_b64 s[74:75], s[74:75], vcc\ns_cmp_lg_u64 s[74:75], 0\ns_cbranch_scc1 .Lfb\n.Lfbret0:\nv_rcp_f32 v25, v24\nv_rcp_f32 v24, v24\n"
"v_rcp_f32 v29, v28\nv_rcp_f32 v28, v28\nv_rcp_f32 v33, v32\nv_rcp_f32 v32, v32\nv_rcp_f32 v37, v36\nv_rcp_f32 v36, v36\nv_rcp_f32 v41, v40\nv_rcp_f32 v40, v40\n"
"v_rcp_f32 v45, v44\nv_rcp_f32 v44, v44\nv_rcp_f32 v49, v48\nv_rcp_f32 v48, v48\nv_pk_mul_f32 v[80:81], v[80:81], v[24:25]\nv_pk_mul_f32 v[82:83], v[82:83], v[24:25]\nv_pk_mul_f32 v[84:85], v[84:85], v[24:25]\nv_pk_mul_f32 v[86:87], v[86:87], v[24:25]\n"
"Qk 80,80,81\nQk 81,82,83\nQk 82,84,85\nQk 83,86,87\nv_accvgpr_write_b32 a0, v80\nv_accvgpr_write_b32 a1, v81\nv_accvgpr_write_b32 a2, v82\nv_accvgpr_write_b32 a3, v83\n"
"v_pk_mul_f32 v[88:89], v[88:89], v[24:25]\nv_pk_mul_f32 v[90:91], v[90:91], v[24:25]\nv_pk_mul_f32 v[92:93], v[92:93], v[24:25]\nv_pk_mul_f32 v[94:95], v[94:95], v[24:25]\nQk 88,88,89\nQk 89,90,91\nQk 90,92,93\nQk 91,94,95\n"
"v_accvgpr_write_b32 a4, v88\nv_accvgpr_write_b32 a5, v89\nv_accvgpr_write_b32 a6, v90\nv_accvgpr_write_b32 a7, v91\nv_pk_mul_f32 v[96:97], v[96:97], v[24:25]\nv_pk_mul_f32 v[98:99], v[98:99], v[24:25]\nv_pk_mul_f32 v[100:101], v[100:101], v[24:25]\nv_pk_mul_f32 v[102:103], v[102:103], v[24:25]\n"
"Qk 96,96,97\nQk 97,98,99\nQk 98,100,101\nQk 99,102,103\nv_accvgpr_write_b32 a8, v96\nv_accvgpr_write_b32 a9, v97\nv_accvgpr_write_b32 a10, v98\nv_accvgpr_write_b32 a11, v99\n"
"v_pk_mul_f32 v[104:105], v[104:105], v[24:25]\nv_pk_mul_f32 v[106:107], v[106:107], v[24:25]\nv_pk_mul_f32 v[108:109], v[108:109], v[24:25]\nv_pk_mul_f32 v[110:111], v[110:111], v[24:25]\nQk 104,104,105\nQk 105,106,107\nQk 106,108,109\nQk 107,110,111\n"
"v_accvgpr_write_b32 a12, v104\nv_accvgpr_write_b32 a13, v105\nv_accvgpr_write_b32 a14, v106\nv_accvgpr_write_b32 a15, v107\nv_pk_mul_f32 v[112:113], v[112:113], v[28:29]\nv_pk_mul_f32 v[114:115], v[114:115], v[28:29]\nv_pk_mul_f32 v[116:117], v[116:117], v[28:29]\nv_pk_mul_f32 v[118:119], v[118:119], v[28:29]\n"
"Qk 112,112,113\nQk 113,114,115\nQk 114,116,117\nQk 115,118,119\nv_accvgpr_write_b32 a16, v112\nv_accvgpr_write_b32 a17, v113\nv_accvgpr_write_b32 a18, v114\nv_accvgpr_write_b32 a19, v115\n"
"v_pk_mul_f32 v[120:121], v[120:121], v[28:29]\nv_pk_mul_f32 v[122:123], v[122:123], v[28:29]\nv_pk_mul_f32 v[124:125], v[124:125], v[28:29]\nv_pk_mul_f32 v[126:127], v[126:127], v[28:29]\nQk 120,120,121\nQk 121,122,123\nQk 122,124,125\nQk 123,126,127\n"
"v_accvgpr_write_b32 a20, v120\nv_accvgpr_write_b32 a21, v121\nv_accvgpr_write_b32 a22, v122\nv_accvgpr_write_b32 a23, v123\nv_pk_mul_f32 v[128:129], v[128:129], v[28:29]\nv_pk_mul_f32 v[130:131], v[130:131], v[28:29]\nv_pk_mul_f32 v[132:133], v[132:133], v[28:29]\nv_pk_mul_f32 v[134:135], v[134:135], v[28:29]\n"
"Qk 128,128,129\nQk 129,130,131\nQk 130,132,133\nQk 131,134,135\nv_accvgpr_write_b32 a24, v128\nv_accvgpr_write_b32 a25, v129\nv_accvgpr_write_b32 a26, v130\nv_accvgpr_write_b32 a27, v131\n"
"v_pk_mul_f32 v[136:137], v[136:137], v[28:29]\nv_pk_mul_f32 v[138:139], v[138:139], v[28:29]\nv_pk_mul_f32 v[140:141], v[140:141], v[28:29]\nv_pk_mul_f32 v[142:143], v[142:143], v[28:29]\nQk 136,136,137\nQk 137,138,139\nQk 138,140,141\nQk 139,142,143\n"
"v_accvgpr_write_b32 a28, v136\nv_accvgpr_write_b32 a29, v137\nv_accvgpr_write_b32 a30, v138\nv_accvgpr_write_b32 a31, v139\nv_pk_mul_f32 v[144:145], v[144:145], v[32:33]\nv_pk_mul_f32 v[146:147], v[146:147], v[32:33]\nv_pk_mul_f32 v[148:149], v[148:149], v[32:33]\nv_pk_mul_f32 v[150:151], v[150:151], v[32:33]\n"
"Qk 144,144,145\nQk 145,146,147\nQk 146,148,149\nQk 147,150,151\nv_accvgpr_write_b32 a32, v144\nv_accvgpr_write_b32 a33, v145\nv_accvgpr_write_b32 a34, v146\nv_accvgpr_write_b32 a35, v147\n"
"v_pk_mul_f32 v[152:153], v[152:153], v[32:33]\nv_pk_mul_f32 v[154:155], v[154:155], v[32:33]\nv_pk_mul_f32 v[156:157], v[156:157], v[32:33]\nv_pk_mul_f32 v[158:159], v[158:159], v[32:33]\nQk 152,152,153\nQk 153,154,155\nQk 154,156,157\nQk 155,158,159\n"
"v_accvgpr_write_b32 a36, v152\nv_accvgpr_write_b32 a37, v153\nv_accvgpr_write_b32 a38, v154\nv_accvgpr_write_b32 a39, v155\nv_pk_mul_f32 v[160:161], v[160:161], v[32:33]\nv_pk_mul_f32 v[162:163], v[162:163], v[32:33]\nv_pk_mul_f32 v[164:165], v[164:165], v[32:33]\nv_pk_mul_f32 v[166:167], v[166:167], v[32:33]\n"
"Qk 160,160,161\nQk 161,162,163\nQk 162,164,165\nQk 163,166,167\nv_accvgpr_write_b32 a40, v160\nv_accvgpr_write_b32 a41, v161\nv_accvgpr_write_b32 a42, v162\nv_accvgpr_write_b32 a43, v163\n"
"v_pk_mul_f32 v[168:169], v[168:169], v[32:33]\nv_pk_mul_f32 v[170:171], v[170:171], v[32:33]\nv_pk_mul_f32 v[172:173], v[172:173], v[32:33]\nv_pk_mul_f32 v[174:175], v[174:175], v[32:33]\nQk 168,168,169\nQk 169,170,171\nQk 170,172,173\nQk 171,174,175\n"
"v_accvgpr_write_b32 a44, v168\nv_accvgpr_write_b32 a45, v169\nv_accvgpr_write_b32 a46, v170\nv_accvgpr_write_b32 a47, v171\nv_pk_mul_f32 v[176:177], v[176:177], v[36:37]\nv_pk_mul_f32 v[178:179], v[178:179], v[36:37]\nv_pk_mul_f32 v[180:181], v[180:181], v[36:37]\nv_pk_mul_f32 v[182:183], v[182:183], v[36:37]\n"
"Qk 176,176,177\nQk 177,178,179\nQk 178,180,181\nQk 179,182,183\nv_accvgpr_write_b32 a48, v176\nv_accvgpr_write_b32 a49, v177\nv_accvgpr_write_b32 a50, v178\nv_accvgpr_write_b32 a51, v179\n"
"v_pk_mul_f32 v[184:185], v[184:185], v[36:37]\nv_pk_mul_f32 v[186:187], v[186:187], v[36:37]\nv_pk_mul_f32 v[188:189], v[188:189], v[36:37]\nv_pk_mul_f32 v[190:191], v[190:191], v[36:37]\nQk 184,184,185\nQk 185,186,187\nQk 186,188,189\nQk 187,190,191\n"
"v_accvgpr_write_b32 a52, v184\nv_accvgpr_write_b32 a53, v185\nv_accvgpr_write_b32 a54, v186\nv_accvgpr_write_b32 a55, v187\nv_pk_mul_f32 v[192:193], v[192:193], v[36:37]\nv_pk_mul_f32 v[194:195], v[194:195], v[36:37]\nv_pk_mul_f32 v[196:197], v[196:197], v[36:37]\nv_pk_mul_f32 v[198:199], v[198:199], v[36:37]\n"
"Qk 192,192,193\nQk 193,194,195\nQk 194,196,197\nQk 195,198,199\nv_accvgpr_write_b32 a56, v192\nv_accvgpr_write_b32 a57, v193\nv_accvgpr_write_b32 a58, v194\nv_accvgpr_write_b32 a59, v195\n"
"v_pk_mul_f32 v[200:201], v[200:201], v[36:37]\nv_pk_mul_f32 v[202:203], v[202:203], v[36:37]\nv_pk_mul_f32 v[204:205], v[204:205], v[36:37]\nv_pk_mul_f32 v[206:207], v[206:207], v[36:37]\nQk 200,200,201\nQk 201,202,203\nQk 202,204,205\nQk 203,206,207\n"
"v_accvgpr_write_b32 a60, v200\nv_accvgpr_write_b32 a61, v201\nv_accvgpr_write_b32 a62, v202\nv_accvgpr_write_b32 a63, v203\nv_pk_mul_f32 v[208:209], v[208:209], v[40:41]\nv_pk_mul_f32 v[210:211], v[210:211], v[40:41]\nv_pk_mul_f32 v[212:213], v[212:213], v[40:41]\nv_pk_mul_f32 v[214:215], v[214:215], v[40:41]\n"
"Qk 208,208,209\nQk 209,210,211\nQk 210,212,213\nQk 211,214,215\nv_accvgpr_write_b32 a64, v208\nv_accvgpr_write_b32 a65, v209\nv_accvgpr_write_b32 a66, v210\nv_accvgpr_write_b32 a67, v211\n"
"v_pk_mul_f32 v[216:217], v[216:217], v[40:41]\nv_pk_mul_f32 v[218:219], v[218:219], v[40:41]\nv_pk_mul_f32 v[220:221], v[220:221], v[40:41]\nv_pk_mul_f32 v[222:223], v[222:223], v[40:41]\nQk 216,216,217\nQk 217,218,219\nQk 218,220,221\nQk 219,222,223\n"
"v_accvgpr_write_b32 a68, v216\nv_accvgpr_write_b32 a69, v217\nv_accvgpr_write_b32 a70, v218\nv_accvgpr_write_b32 a71, v219\nv_pk_mul_f32 v[224:225], v[224:225], v[40:41]\nv_pk_mul_f32 v[226:227], v[226:227], v[40:41]\nv_pk_mul_f32 v[228:229], v[228:229], v[40:41]\nv_pk_mul_f32 v[230:231], v[230:231], v[40:41]\n"
"Qk 224,224,225\nQk 225,226,227\nQk 226,228,229\nQk 227,230,231\nv_accvgpr_write_b32 a72, v224\nv_accvgpr_write_b32 a73, v225\nv_accvgpr_write_b32 a74, v226\nv_accvgpr_write_b32 a75, v227\n"
"v_pk_mul_f32 v[232:233], v[232:233], v[40:41]\nv_pk_mul_f32 v[234:235], v[234:235], v[40:41]\nv_pk_mul_f32 v[236:237], v[236:237], v[40:41]\nv_pk_mul_f32 v[238:239], v[238:239], v[40:41]\nQk 232,232,233\nQk 233,234,235\nQk 234,236,237\nQk 235,238,239\n"
"v_accvgpr_write_b32 a76, v232\nv_accvgpr_write_b32 a77, v233\nv_accvgpr_write_b32 a78, v234\nv_accvgpr_write_b32 a79, v235\nv_pk_mul_f32 v[240:241], v[240:241], v[44:45]\nv_pk_mul_f32 v[242:243], v[242:243], v[44:45]\nv_pk_mul_f32 v[244:245], v[244:245], v[44:45]\nv_pk_mul_f32 v[246:247], v[246:247], v[44:45]\n"
"Qk 240,240,241\nQk 241,242,243\nQk 242,244,245\nQk 243,246,247\nv_accvgpr_write_b32 a80, v240\nv_accvgpr_write_b32 a81, v241\nv_accvgpr_write_b32 a82, v242\nv_accvgpr_write_b32 a83, v243\n"
"v_pk_mul_f32 v[248:249], v[248:249], v[44:45]\nv_pk_mul_f32 v[250:251], v[250:251], v[44:45]\nv_pk_mul_f32 v[252:253], v[252:253], v[44:45]\nv_pk_mul_f32 v[254:255], v[254:255], v[44:45]\nQk 248,248,249\nQk 249,250,251\nQk 250,252,253\nQk 251,254,255\n"
"v_accvgpr_write_b32 a84, v248\nv_accvgpr_write_b32 a85, v249\nv_accvgpr_write_b32 a86, v250\nv_accvgpr_write_b32 a87, v251\nv_accvgpr_read_b32 v0, a208\nv_accvgpr_read_b32 v1, a209\nv_accvgpr_read_b32 v2, a210\nv_accvgpr_read_b32 v3, a211\n"
"v_accvgpr_read_b32 v4, a212\nv_accvgpr_read_b32 v5, a213\nv_accvgpr_read_b32 v6, a214\nv_accvgpr_read_b32 v7, a215\nv_pk_mul_f32 v[0:1], v[0:1], v[44:45]\nv_pk_mul_f32 v[2:3], v[2:3], v[44:45]\nv_pk_mul_f32 v[4:5], v[4:5], v[44:45]\nv_pk_mul_f32 v[6:7], v[6:7], v[44:45]\n"
"Qk 0,0,1\nQk 1,2,3\nQk 2,4,5\nQk 3,6,7\nv_accvgpr_write_b32 a88, v0\nv_accvgpr_write_b32 a89, v1\nv_accvgpr_write_b32 a90, v2\nv_accvgpr_write_b32 a91, v3\n"
"v_accvgpr_read_b32 v8, a216\nv_accvgpr_read_b32 v9, a217\nv_accvgpr_read_b32 v10, a218\nv_accvgpr_read_b32 v11, a219\nv_accvgpr_read_b32 v12, a220\nv_accvgpr_read_b32 v13, a221\nv_accvgpr_read_b32 v14, a222\nv_accvgpr_read_b32 v15, a223\n"
"v_pk_mul_f32 v[8:9], v[8:9], v[44:45]\nv_pk_mul_f32 v[10:11], v[10:11], v[44:45]\nv_pk_mul_f32 v[12:13], v[12:13], v[44:45]\nv_pk_mul_f32 v[14:15], v[14:15], v[44:45]\nQk 8,8,9\nQk 9,10,11\nQk 10,12,13\nQk 11,14,15\n"
"v_accvgpr_write_b32 a92, v8\nv_accvgpr_write_b32 a93, v9\nv_accvgpr_write_b32 a94, v10\nv_accvgpr_write_b32 a95, v11\nv_accvgpr_read_b32 v0, a224\nv_accvgpr_read_b32 v1, a225\nv_accvgpr_read_b32 v2, a226\nv_accvgpr_read_b32 v3, a227\n"
"v_accvgpr_read_b32 v4, a228\nv_accvgpr_read_b32 v5, a229\nv_accvgpr_read_b32 v6, a230\nv_accvgpr_read_b32 v7, a231\nv_pk_mul_f32 v[0:1], v[0:1], v[48:49]\nv_pk_mul_f32 v[2:3], v[2:3], v[48:49]\nv_pk_mul_f32 v[4:5], v[4:5], v[48:49]\nv_pk_mul_f32 v[6:7], v[6:7], v[48:49]\n"
"Qk 0,0,1\nQk 1,2,3\nQk 2,4,5\nQk 3,6,7\nv_accvgpr_write_b32 a96, v0\nv_accvgpr_write_b32 a97, v1\nv_accvgpr_write_b32 a98, v2\nv_accvgpr_write_b32 a99, v3\n"
"v_accvgpr_read_b32 v8, a232\nv_accvgpr_read_b32 v9, a233\nv_accvgpr_read_b32 v10, a234\nv_accvgpr_read_b32 v11, a235\nv_accvgpr_read_b32 v12, a236\nv_accvgpr_read_b32 v13, a237\nv_accvgpr_read_b32 v14, a238\nv_accvgpr_read_b32 v15, a239\n"
"v_pk_mul_f32 v[8:9], v[8:9], v[48:49]\nv_pk_mul_f32 v[10:11], v[10:11], v[48:49]\nv_pk_mul_f32 v[12:13], v[12:13], v[48:49]\nv_pk_mul_f32 v[14:15], v[14:15], v[48:49]\nQk 8,8,9\nQk 9,10,11\nQk 10,12,13\nQk 11,14,15\n"
"v_accvgpr_write_b32 a100, v8\nv_accvgpr_write_b32 a101, v9\nv_accvgpr_write_b32 a102, v10\nv_accvgpr_write_b32 a103, v11\nv_accvgpr_read_b32 v0, a240\nv_accvgpr_read_b32 v1, a241\nv_accvgpr_read_b32 v2, a242\nv_accvgpr_read_b32 v3, a243\n"
"v_accvgpr_read_b32 v4, a244\nv_accvgpr_read_b32 v5, a245\nv_accvgpr_read_b32 v6, a246\nv_accvgpr_read_b32 v7, a247\nv_pk_mul_f32 v[0:1], v[0:1], v[48:49]\nv_pk_mul_f32 v[2:3], v[2:3], v[48:49]\nv_pk_mul_f32 v[4:5], v[4:5], v[48:49]\nv_pk_mul_f32 v[6:7], v[6:7], v[48:49]\n"
"Qk 0,0,1\nQk 1,2,3\nQk 2,4,5\nQk 3,6,7\nv_accvgpr_write_b32 a104, v0\nv_accvgpr_write_b32 a105, v1\nv_accvgpr_write_b32 a106, v2\nv_accvgpr_write_b32 a107, v3\n"
"v_accvgpr_read_b32 v8, a248\nv_accvgpr_read_b32 v9, a249\nv_accvgpr_read_b32 v10, a250\nv_accvgpr_read_b32 v11, a251\nv_accvgpr_read_b32 v12, a252\nv_accvgpr_read_b32 v13, a253\nv_accvgpr_read_b32 v14, a254\nv_accvgpr_read_b32 v15, a255\n"
"v_pk_mul_f32 v[8:9], v[8:9], v[48:49]\nv_pk_mul_f32 v[10:11], v[10:11], v[48:49]\nv_pk_mul_f32 v[12:13], v[12:13], v[48:49]\nv_pk_mul_f32 v[14:15], v[14:15], v[48:49]\nQk 8,8,9\nQk 9,10,11\nQk 10,12,13\nQk 11,14,15\n"
"v_accvgpr_write_b32 a108, v8\nv_accvgpr_write_b32 a109, v9\nv_accvgpr_write_b32 a110, v10\nv_accvgpr_write_b32 a111, v11\ns_mov_b32 s56, %8\ns_and_b32 s57, %9, 0xffff\ns_mov_b32 s94, 0x7ffff000\ns_mul_i32 s68, %20, 40960\n"
"s_lshl_b32 s69, s70, 10\ns_add_u32 s68, s68, s69\ns_add_u32 s68, s68, 4096\ns_add_u32 s94, %15, s68\ns_addc_u32 s95, %16, 0\ns_and_b32 s95, s95, 0xffff\ns_mov_b32 s75, 37888\ns_mov_b32 s58, 1835008\n"
"s_mov_b32 s67, 1\ns_mul_i32 s71, %12, 57344\ns_mul_i32 s68, s92, 7168\ns_add_u32 s71, s71, s68\ns_mov_b64 exec, 0\ns_cmp_eq_u32 s70, 0\ns_cbranch_scc1 .Lpg12\ns_cmp_eq_u32 s70, 1\n"
"s_cbranch_scc1 .Lpg13\ns_cmp_eq_u32 s70, 2\ns_cbranch_scc1 .Lpg14\ns_branch .Lpg15\n.Ltr6:\ns_mov_b64 exec, -1\ns_branch .Lround\n.Lhook:\n"
".Lpg9:\ns_cbranch_execz .Ltr0\ns_cmp_eq_u32 s66, 1\ns_cbranch_scc1 .Lh1\ns_cmp_eq_u32 s66, 8\ns_cbranch_scc1 .Lh8\ns_cmp_eq_u32 s66, 9\ns_cbranch_scc1 .Lh9\n"
"s_lshr_b32 s68, s99, s66\ns_cmp_eq_u32 s68, 1\ns_cbranch_scc1 .Lh12\ns_branch .Lh11\n.Lh1:\ns_cmp_eq_u32 s70, 0\ns_cselect_b32 s68, 128, 0\nv_mov_b32 v66, 0x7ffff000\n"
"v_cmp_gt_u32 vcc, s68, v60\nv_not_b32 v67, v65\ns_lshl_b32 s68, %19, 2\nv_cndmask_b32 v66, v66, v76, vcc\nv_readlane_b32 s30, v67, %19\nbuffer_store_dword v67, v66, s[24:27], s68 offen offset:1024 sc1\ns_branch .Lhookret\n.Lh8:\n"
"v_bfe_u32 v66, v60, 4, 3\ns_lshl_b32 s68, %19, 7\nv_lshlrev_b32 v66, 2, v66\ns_add_u32 s68, s68, 2293760\nbuffer_load_dword v78, v66, s[24:27], s68 offen offset:1024 sc1\ns_branch .Lhookret\n.Lh9:\ns_waitcnt vmcnt(32)\n"
"s_mov_b32 s97, 0\n.Lack_l:\nv_cmp_ne_u32 vcc, s30, v78\ns_cmp_eq_u64 vcc, 0\ns_cbranch_scc1 .Lack_ok\ns_sleep 1\nv_bfe_u32 v66, v60, 4, 3\ns_lshl_b32 s68, %19, 7\n"
"v_lshlrev_b32 v66, 2, v66\ns_add_u32 s68, s68, 2293760\nbuffer_load_dword v78, v66, s[24:27], s68 offen offset:1024 sc1\n.Lpg10:\ns_cbranch_execz .Ltr0\ns_waitcnt vmcnt(0)\ns_add_u32 s97, s97, 1\ns_cmp_lt_u32 s97, 1048576\n"
"s_cbranch_scc1 .Lack_l\n.Lack_ok:\ns_lshl_b32 s68, %19, 4\ns_cmp_eq_u32 s70, 0\ns_cselect_b32 s68, s68, -1\nv_mov_b32 v67, 0x7ffff000\nv_cmp_eq_u32 vcc, s68, v60\ns_nop 1\n"
"v_cndmask_b32 v67, v67, v76, vcc\nv_add_u32 v66, 1, v65\nbuffer_store_dword v66, v67, s[24:27], 0 offen sc1\ns_branch .Lhookret\n.Lh11:\nbuffer_load_dword v78, v76, s[24:27], 0 offen sc1\ns_branch .Lhookret\n.Lh12:\n"
"s_waitcnt vmcnt(32)\nv_sub_u32 v66, v78, v65\nv_cmp_gt_u32 vcc, 1, v66\ns_and_b64 vcc, vcc, s[28:29]\ns_cbranch_scc1 .Lf1_w\n.Lf1_r:\ns_cmp_lg_u32 s70, 3\ns_cbranch_scc1 .Lf1_n\n"
"buffer_inv sc1\n.Lf1_n:\ns_lshr_b32 s88, %19, 2\ns_mul_i32 s68, s88, 917504\ns_add_u32 s48, %8, s68\ns_addc_u32 s49, %9, 0\ns_mul_i32 s68, s88, 2621440\ns_and_b32 s69, %19, 3\n"
"s_mul_i32 s69, s69, 320\ns_add_u32 s68, s68, s69\ns_sub_u32 s88, 5246976, s68\ns_add_u32 s68, %15, s68\ns_addc_u32 s69, %16, 0\ns_cmp_lt_u32 s70, 2\ns_cselect_b32 s52, s48, s68\ns_cselect_b32 s53, s49, s69\n"
"s_cselect_b32 s54, 917504, s88\ns_cmp_lt_u32 s70, 3\ns_cselect_b32 s48, s48, s68\ns_cselect_b32 s49, s49, s69\ns_cselect_b32 s50, 917504, s88\ns_and_b32 s49, s49, 0xffff\ns_and_b32 s53, s53, 0xffff\ns_add_u32 s71, s71, 458752\n"
"s_mov_b32 s62, s68\ns_and_b32 s63, s69, 0xffff\ns_mov_b32 s86, s88\ns_mul_i32 s68, s70, 6\ns_add_u32 s68, s68, 3\ns_sub_u32 s69, s68, 3\ns_lshl_b32 s69, s69, 10\nv_add_u32 v66, s69, v60\n"
"s_sub_u32 s69, s68, 14\ns_lshl_b32 s69, s69, 2\ns_and_b32 s69, s69, 63\nv_lshrrev_b32 v67, 8, v60\nv_add_u32 v67, s69, v67\nv_mul_u32_u24 v68, 205, v67\nv_lshrrev_b32 v68, 11, v68\nv_mul_u32_u24 v61, 10, v68\n"
"v_sub_u32 v67, v67, v61\nv_lshlrev_b32 v67, 5, v67\nv_bfe_u32 v61, v60, 5, 3\nv_lshl_add_u32 v68, v68, 3, v61\nv_mul_u32_u24 v68, 1280, v68\nv_and_b32 v61, 16, v60\nv_add3_u32 v67, v67, v68, v61\nv_add_u32 v67, 1024, v67\n"
"s_cmp_lt_u32 s68, 14\ns_cselect_b64 vcc, -1, 0\nv_cndmask_b32 v61, v67, v66, vcc\ns_mul_i32 s68, s70, 6\ns_add_u32 s68, s68, 4\ns_sub_u32 s69, s68, 0\ns_lshl_b32 s69, s69, 10\nv_add_u32 v66, s69, v60\n"
"s_sub_u32 s69, s68, 14\ns_lshl_b32 s69, s69, 2\ns_and_b32 s69, s69, 63\nv_lshrrev_b32 v67, 8, v60\nv_add_u32 v67, s69, v67\nv_mul_u32_u24 v68, 205, v67\nv_lshrrev_b32 v68, 11, v68\nv_mul_u32_u24 v62, 10, v68\n"
"v_sub_u32 v67, v67, v62\nv_lshlrev_b32 v67, 5, v67\nv_bfe_u32 v62, v60, 5, 3\nv_lshl_add_u32 v68, v68, 3, v62\nv_mul_u32_u24 v68, 1280, v68\nv_and_b32 v62, 16, v60\nv_add3_u32 v67, v67, v68, v62\nv_add_u32 v67, 4096, v67\n"
"s_cmp_lt_u32 s68, 14\ns_cselect_b64 vcc, -1, 0\nv_cndmask_b32 v62, v67, v66, vcc\ns_mul_i32 s68, s70, 6\ns_add_u32 s68, s68, 5\ns_sub_u32 s69, s68, 1\ns_lshl_b32 s69, s69, 10\nv_add_u32 v66, s69, v60\n"
"s_sub_u32 s69, s68, 14\ns_lshl_b32 s69, s69, 2\ns_and_b32 s69, s69, 63\nv_lshrrev_b32 v67, 8, v60\nv_add_u32 v67, s69, v67\nv_mul_u32_u24 v68, 205, v67\nv_lshrrev_b32 v68, 11, v68\nv_mul_u32_u24 v63, 10, v68\n"
"v_sub_u32 v67, v67, v63\nv_lshlrev_b32 v67, 5, v67\nv_bfe_u32 v63, v60, 5, 3\nv_lshl_add_u32 v68, v68, 3, v63\nv_mul_u32_u24 v68, 1280, v68\nv_and_b32 v63, 16, v60\nv_add3_u32 v67, v67, v68, v63\nv_add_u32 v67, 3072, v67\n"
"s_cmp_lt_u32 s68, 14\ns_cselect_b64 vcc, -1, 0\nv_cndmask_b32 v63, v67, v66, vcc\ns_branch .Lhookret\n.Lf1_w:\ns_mov_b32 s97, 0\n.Lf1_l:\ns_sleep 1\n"
"buffer_load_dword v78, v76, s[24:27], 0 offen sc1\ns_waitcnt vmcnt(0)\ns_add_u32 s97, s97, 1\ns_cmp_gt_u32 s97, 1048576\ns_cbranch_scc1 .Lf1_r\nv_sub_u32 v66, v78, v65\nv_cmp_gt_u32 vcc, 1, v66\ns_and_b64 vcc, vcc, s[28:29]\n"
"s_cbranch_scc1 .Lf1_l\ns_branch .Lf1_r\n.Lsa1:\nds_read_b32 v69, v74\ns_waitcnt lgkmcnt(0)\nv_cmp_gt_u32 vcc, v75, v69\ns_cbranch_vccnz .Lsa1\ns_branch .Lra1\n"
".Lsp1:\ns_memrealtime s[74:75]\ns_waitcnt lgkmcnt(0)\ns_sub_u32 s75, s77, s74\ns_cmp_lt_u32 s75, 256\ns_cbranch_scc1 .Lsp1\ns_mov_b32 s74, s77\ns_branch .Lrp1\n"
".Lsa2:\nds_read_b32 v69, v74\ns_waitcnt lgkmcnt(0)\nv_cmp_gt_u32 vcc, v75, v69\ns_cbranch_vccnz .Lsa2\ns_branch .Lra2\n.Lsp2:\ns_memrealtime s[74:75]\n"
"s_waitcnt lgkmcnt(0)\ns_sub_u32 s75, s77, s74\ns_cmp_lt_u32 s75, 256\ns_cbranch_scc1 .Lsp2\ns_mov_b32 s74, s77\ns_branch .Lrp2\n.Lsa3:\nds_read_b32 v69, v74\n"
"s_waitcnt lgkmcnt(0)\nv_cmp_gt_u32 vcc, v75, v69\ns_cbranch_vccnz .Lsa3\ns_branch .Lra3\n.Lsp3:\ns_memrealtime s[74:75]\ns_waitcnt lgkmcnt(0)\ns_sub_u32 s75, s77, s74\n"
"s_cmp_lt_u32 s75, 256\ns_cbranch_scc1 .Lsp3\ns_mov_b32 s74, s77\ns_branch .Lrp3\n.Lsa4:\nds_read_b32 v69, v74\ns_waitcnt lgkmcnt(0)\nv_cmp_gt_u32 vcc, v75, v69\n"
"s_cbranch_vccnz .Lsa4\ns_branch .Lra4\n.Lsp4:\ns_memrealtime s[74:75]\ns_waitcnt lgkmcnt(0)\ns_sub_u32 s75, s77, s74\ns_cmp_lt_u32 s75, 256\ns_cbranch_scc1 .Lsp4\n"
"s_mov_b32 s74, s77\ns_branch .Lrp4\n.Lsa29:\nds_read_b32 v69, v74\ns_waitcnt lgkmcnt(0)\nv_cmp_gt_u32 vcc, v75, v69\ns_cbranch_vccnz .Lsa29\ns_branch .Lra29\n"
".Lsa30:\nds_read_b32 v69, v74\ns_waitcnt lgkmcnt(0)\nv_cmp_gt_u32 vcc, v75, v69\ns_cbranch_vccnz .Lsa30\ns_branch .Lra30\n.Lsa31:\nds_read_b32 v69, v74\n"
"s_waitcnt lgkmcnt(0)\nv_cmp_gt_u32 vcc, v75, v69\ns_cbranch_vccnz .Lsa31\ns_branch .Lra31\n.Lsa29x:\nds_read_b32 v69, v74\ns_waitcnt lgkmcnt(0)\nv_cmp_gt_u32 vcc, v75, v69\n"
"s_cbranch_vccnz .Lsa29x\ns_branch .Lra29x\n.Lsa30x:\nds_read_b32 v69, v74\ns_waitcnt lgkmcnt(0)\nv_cmp_gt_u32 vcc, v75, v69\ns_cbranch_vccnz .Lsa30x\ns_branch .Lra30x\n"
".Lsa31x:\nds_read_b32 v69, v74\ns_waitcnt lgkmcnt(0)\nv_cmp_gt_u32 vcc, v75, v69\ns_cbranch_vccnz .Lsa31x\ns_branch .Lra31x\n.Lso0:\nds_read_b32 v69, v74\n"
"s_waitcnt lgkmcnt(0)\nv_cmp_gt_u32 vcc, v75, v69\ns_cbranch_vccnz .Lso0\ns_branch .Lro0\n.Lso1:\nds_read_b32 v69, v74\ns_waitcnt lgkmcnt(0)\nv_cmp_gt_u32 vcc, v75, v69\n"
"s_cbranch_vccnz .Lso1\ns_branch .Lro1\n.Lso2:\nds_read_b32 v69, v74\ns_waitcnt lgkmcnt(0)\nv_cmp_gt_u32 vcc, v75, v69\ns_cbranch_vccnz .Lso2\ns_branch .Lro2\n"
".Lso3:\nds_read_b32 v69, v74\ns_waitcnt lgkmcnt(0)\nv_cmp_gt_u32 vcc, v75, v69\ns_cbranch_vccnz .Lso3\ns_branch .Lro3\n.Lso4:\nds_read_b32 v69, v74\n"
"s_waitcnt lgkmcnt(0)\nv_cmp_gt_u32 vcc, v75, v69\ns_cbranch_vccnz .Lso4\ns_branch .Lro4\n.Lso5:\nds_read_b32 v69, v74\ns_waitcnt lgkmcnt(0)\nv_cmp_gt_u32 vcc, v75, v69\n"
"s_cbranch_vccnz .Lso5\ns_branch .Lro5\n.Lso6:\nds_read_b32 v69, v74\ns_waitcnt lgkmcnt(0)\nv_cmp_gt_u32 vcc, v75, v69\ns_cbranch_vccnz .Lso6\ns_branch .Lro6\n"
".Lso7:\nds_read_b32 v69, v74\ns_waitcnt lgkmcnt(0)\nv_cmp_gt_u32 vcc, v75, v69\ns_cbranch_vccnz .Lso7\ns_branch .Lro7\n.Lso8:\nds_read_b32 v69, v74\n"
"s_waitcnt lgkmcnt(0)\nv_cmp_gt_u32 vcc, v75, v69\ns_cbranch_vccnz .Lso8\ns_branch .Lro8\n.Lso9:\nds_read_b32 v69, v74\ns_waitcnt lgkmcnt(0)\nv_cmp_gt_u32 vcc, v75, v69\n"
"s_cbranch_vccnz .Lso9\ns_branch .Lro9\n.Lso10:\nds_read_b32 v69, v74\ns_waitcnt lgkmcnt(0)\nv_cmp_gt_u32 vcc, v75, v69\ns_cbranch_vccnz .Lso10\ns_branch .Lro10\n"
".Lso11:\nds_read_b32 v69, v74\ns_waitcnt lgkmcnt(0)\nv_cmp_gt_u32 vcc, v75, v69\ns_cbranch_vccnz .Lso11\ns_branch .Lro11\n.Lso12:\nds_read_b32 v69, v74\n"
"s_waitcnt lgkmcnt(0)\nv_cmp_gt_u32 vcc, v75, v69\ns_cbranch_vccnz .Lso12\ns_branch .Lro12\n.Lso13:\nds_read_b32 v69, v74\ns_waitcnt lgkmcnt(0)\nv_cmp_gt_u32 vcc, v75, v69\n"
"s_cbranch_vccnz .Lso13\ns_branch .Lro13\n.Lso14:\nds_read_b32 v69, v74\ns_waitcnt lgkmcnt(0)\nv_cmp_gt_u32 vcc, v75, v69\ns_cbranch_vccnz .Lso14\ns_branch .Lro14\n"
".Lso15:\nds_read_b32 v69, v74\ns_waitcnt lgkmcnt(0)\nv_cmp_gt_u32 vcc, v75, v69\ns_cbranch_vccnz .Lso15\ns_branch .Lro15\n.Lso16:\nds_read_b32 v69, v74\n"
"s_waitcnt lgkmcnt(0)\nv_cmp_gt_u32 vcc, v75, v69\ns_cbranch_vccnz .Lso16\ns_branch .Lro16\n.Lso17:\nds_read_b32 v69, v74\ns_waitcnt lgkmcnt(0)\nv_cmp_gt_u32 vcc, v75, v69\n"
"s_cbranch_vccnz .Lso17\ns_branch .Lro17\n.Lso18:\nds_read_b32 v69, v74\ns_waitcnt lgkmcnt(0)\nv_cmp_gt_u32 vcc, v75, v69\ns_cbranch_vccnz .Lso18\ns_branch .Lro18\n"
".Lso19:\nds_read_b32 v69, v74\ns_waitcnt lgkmcnt(0)\nv_cmp_gt_u32 vcc, v75, v69\ns_cbranch_vccnz .Lso19\ns_branch .Lro19\n.Lso20:\nds_read_b32 v69, v74\n"
"s_waitcnt lgkmcnt(0)\nv_cmp_gt_u32 vcc, v75, v69\ns_cbranch_vccnz .Lso20\ns_branch .Lro20\n.Lso21:\nds_read_b32 v69, v74\ns_waitcnt lgkmcnt(0)\nv_cmp_gt_u32 vcc, v75, v69\n"
"s_cbranch_vccnz .Lso21\ns_branch .Lro21\n.Lso22:\nds_read_b32 v69, v74\ns_waitcnt lgkmcnt(0)\nv_cmp_gt_u32 vcc, v75, v69\ns_cbranch_vccnz .Lso22\ns_branch .Lro22\n"
".Lso23:\nds_read_b32 v69, v74\ns_waitcnt lgkmcnt(0)\nv_cmp_gt_u32 vcc, v75, v69\ns_cbranch_vccnz .Lso23\ns_branch .Lro23\n.Lso24:\nds_read_b32 v69, v74\n"
"s_waitcnt lgkmcnt(0)\nv_cmp_gt_u32 vcc, v75, v69\ns_cbranch_vccnz .Lso24\ns_branch .Lro24\n.Lso25:\nds_read_b32 v69, v74\ns_waitcnt lgkmcnt(0)\nv_cmp_gt_u32 vcc, v75, v69\n"
"s_cbranch_vccnz .Lso25\ns_branch .Lro25\n.Lso26:\nds_read_b32 v69, v74\ns_waitcnt lgkmcnt(0)\nv_cmp_gt_u32 vcc, v75, v69\ns_cbranch_vccnz .Lso26\ns_branch .Lro26\n"
".Lso27:\nds_read_b32 v69, v74\ns_waitcnt lgkmcnt(0)\nv_cmp_gt_u32 vcc, v75, v69\ns_cbranch_vccnz .Lso27\ns_branch .Lro27\n.Lso28:\nds_read_b32 v69, v74\n"
"s_waitcnt lgkmcnt(0)\nv_cmp_gt_u32 vcc, v75, v69\ns_cbranch_vccnz .Lso28\ns_branch .Lro28\n.Lso29:\nds_read_b32 v69, v74\ns_waitcnt lgkmcnt(0)\nv_cmp_gt_u32 vcc, v75, v69\n"
"s_cbranch_vccnz .Lso29\ns_branch .Lro29\n.Lso30:\nds_read_b32 v69, v74\ns_waitcnt lgkmcnt(0)\nv_cmp_gt_u32 vcc, v75, v69\ns_cbranch_vccnz .Lso30\ns_branch .Lro30\n"
".Lso31:\nds_read_b32 v69, v74\ns_waitcnt lgkmcnt(0)\nv_cmp_gt_u32 vcc, v75, v69\ns_cbranch_vccnz .Lso31\ns_branch .Lro31\n.Lso32:\nds_read_b32 v69, v74\n"
"s_waitcnt lgkmcnt(0)\nv_cmp_gt_u32 vcc, v75, v69\ns_cbranch_vccnz .Lso32\ns_branch .Lro32\n.Lso33:\nds_read_b32 v69, v74\ns_waitcnt lgkmcnt(0)\nv_cmp_gt_u32 vcc, v75, v69\n"
"s_cbranch_vccnz .Lso33\ns_branch .Lro33\n.Lso34:\nds_read_b32 v69, v74\ns_waitcnt lgkmcnt(0)\nv_cmp_gt_u32 vcc, v75, v69\ns_cbranch_vccnz .Lso34\ns_branch .Lro34\n"
".Lso35:\nds_read_b32 v69, v74\ns_waitcnt lgkmcnt(0)\nv_cmp_gt_u32 vcc, v75, v69\ns_cbranch_vccnz .Lso35\ns_branch .Lro35\n.Lso36:\nds_read_b32 v69, v74\n"
"s_waitcnt lgkmcnt(0)\nv_cmp_gt_u32 vcc, v75, v69\ns_cbranch_vccnz .Lso36\ns_branch .Lro36\n.Lso37:\nds_read_b32 v69, v74\ns_waitcnt lgkmcnt(0)\nv_cmp_gt_u32 vcc, v75, v69\n"
"s_cbranch_vccnz .Lso37\ns_branch .Lro37\n.Lso38:\nds_read_b32 v69, v74\ns_waitcnt lgkmcnt(0)\nv_cmp_gt_u32 vcc, v75, v69\ns_cbranch_vccnz .Lso38\ns_branch .Lro38\n"
".Lso39:\nds_read_b32 v69, v74\ns_waitcnt lgkmcnt(0)\nv_cmp_gt_u32 vcc, v75, v69\ns_cbranch_vccnz .Lso39\ns_branch .Lro39\n.Lso40:\nds_read_b32 v69, v74\n"
"s_waitcnt lgkmcnt(0)\nv_cmp_gt_u32 vcc, v75, v69\ns_cbranch_vccnz .Lso40\ns_branch .Lro40\n.Lso41:\nds_read_b32 v69, v74\ns_waitcnt lgkmcnt(0)\nv_cmp_gt_u32 vcc, v75, v69\n"
"s_cbranch_vccnz .Lso41\ns_branch .Lro41\n.Lso42:\nds_read_b32 v69, v74\ns_waitcnt lgkmcnt(0)\nv_cmp_gt_u32 vcc, v75, v69\ns_cbranch_vccnz .Lso42\ns_branch .Lro42\n"
".Lso43:\nds_read_b32 v69, v74\ns_waitcnt lgkmcnt(0)\nv_cmp_gt_u32 vcc, v75, v69\ns_cbranch_vccnz .Lso43\ns_branch .Lro43\n.Lso44:\nds_read_b32 v69, v74\n"
"s_waitcnt lgkmcnt(0)\nv_cmp_gt_u32 vcc, v75, v69\ns_cbranch_vccnz .Lso44\ns_branch .Lro44\n.Lso45:\nds_read_b32 v69, v74\ns_waitcnt lgkmcnt(0)\nv_cmp_gt_u32 vcc, v75, v69\n"
"s_cbranch_vccnz .Lso45\ns_branch .Lro45\n.Lso46:\nds_read_b32 v69, v74\ns_waitcnt lgkmcnt(0)\nv_cmp_gt_u32 vcc, v75, v69\ns_cbranch_vccnz .Lso46\ns_branch .Lro46\n"
".Lso47:\nds_read_b32 v69, v74\ns_waitcnt lgkmcnt(0)\nv_cmp_gt_u32 vcc, v75, v69\ns_cbranch_vccnz .Lso47\ns_branch .Lro47\n.Lso48:\nds_read_b32 v69, v74\n"
"s_waitcnt lgkmcnt(0)\nv_cmp_gt_u32 vcc, v75, v69\ns_cbranch_vccnz .Lso48\ns_branch .Lro48\n.Lso49r0:\nds_read_b32 v69, v74\ns_waitcnt lgkmcnt(0)\nv_cmp_gt_u32 vcc, v75, v69\n"
"s_cbranch_vccnz .Lso49r0\ns_branch .Lro49r0\n.Lso50r0:\nds_read_b32 v69, v74\ns_waitcnt lgkmcnt(0)\nv_cmp_gt_u32 vcc, v75, v69\ns_cbranch_vccnz .Lso50r0\ns_branch .Lro50r0\n"
".Lso51r0:\nds_read_b32 v69, v74\ns_waitcnt lgkmcnt(0)\nv_cmp_gt_u32 vcc, v75, v69\ns_cbranch_vccnz .Lso51r0\ns_branch .Lro51r0\n.Lso52r0:\nds_read_b32 v69, v74\n"
"s_waitcnt lgkmcnt(0)\nv_cmp_gt_u32 vcc, v75, v69\ns_cbranch_vccnz .Lso52r0\ns_branch .Lro52r0\n.Lso53r0:\nds_read_b32 v69, v74\ns_waitcnt lgkmcnt(0)\nv_cmp_gt_u32 vcc, v75, v69\n"
"s_cbranch_vccnz .Lso53r0\ns_branch .Lro53r0\n.Lso54r0:\nds_read_b32 v69, v74\ns_waitcnt lgkmcnt(0)\nv_cmp_gt_u32 vcc, v75, v69\ns_cbranch_vccnz .Lso54r0\ns_branch .Lro54r0\n"
".Lso55r0:\nds_read_b32 v69, v74\ns_waitcnt lgkmcnt(0)\nv_cmp_gt_u32 vcc, v75, v69\ns_cbranch_vccnz .Lso55r0\ns_branch .Lro55r0\n.Lso56r0:\nds_read_b32 v69, v74\n"
"s_waitcnt lgkmcnt(0)\nv_cmp_gt_u32 vcc, v75, v69\ns_cbranch_vccnz .Lso56r0\ns_branch .Lro56r0\n.Lso57r0:\nds_read_b32 v69, v74\ns_waitcnt lgkmcnt(0)\nv_cmp_gt_u32 vcc, v75, v69\n"
"s_cbranch_vccnz .Lso57r0\ns_branch .Lro57r0\n.Lso58r0:\nds_read_b32 v69, v74\ns_waitcnt lgkmcnt(0)\nv_cmp_gt_u32 vcc, v75, v69\ns_cbranch_vccnz .Lso58r0\ns_branch .Lro58r0\n"
".Lso59r0:\nds_read_b32 v69, v74\ns_waitcnt lgkmcnt(0)\nv_cmp_gt_u32 vcc, v75, v69\ns_cbranch_vccnz .Lso59r0\ns_branch .Lro59r0\n.Lso60r0:\nds_read_b32 v69, v74\n"
"s_waitcnt lgkmcnt(0)\nv_cmp_gt_u32 vcc, v75, v69\ns_cbranch_vccnz .Lso60r0\ns_branch .Lro60r0\n.Lso61r0:\nds_read_b32 v69, v74\ns_waitcnt lgkmcnt(0)\nv_cmp_gt_u32 vcc, v75, v69\n"
"s_cbranch_vccnz .Lso61r0\ns_branch .Lro61r0\n.Lso62r0:\nds_read_b32 v69, v74\ns_waitcnt lgkmcnt(0)\nv_cmp_gt_u32 vcc, v75, v69\ns_cbranch_vccnz .Lso62r0\ns_branch .Lro62r0\n"
".Lso63r0:\nds_read_b32 v69, v74\ns_waitcnt lgkmcnt(0)\nv_cmp_gt_u32 vcc, v75, v69\ns_cbranch_vccnz .Lso63r0\ns_branch .Lro63r0\n.Lso49r1:\nds_read_b32 v69, v74\n"
"s_waitcnt lgkmcnt(0)\nv_cmp_gt_u32 vcc, v75, v69\ns_cbranch_vccnz .Lso49r1\ns_branch .Lro49r1\n.Lso50r1:\nds_read_b32 v69, v74\ns_waitcnt lgkmcnt(0)\nv_cmp_gt_u32 vcc, v75, v69\n"
"s_cbranch_vccnz .Lso50r1\ns_branch .Lro50r1\n.Lso51r1:\nds_read_b32 v69, v74\ns_waitcnt lgkmcnt(0)\nv_cmp_gt_u32 vcc, v75, v69\ns_cbranch_vccnz .Lso51r1\ns_branch .Lro51r1\n"
".Lso52r1:\nds_read_b32 v69, v74\ns_waitcnt lgkmcnt(0)\nv_cmp_gt_u32 vcc, v75, v69\ns_cbranch_vccnz .Lso52r1\ns_branch .Lro52r1\n.Lso53r1:\nds_read_b32 v69, v74\n"
"s_waitcnt lgkmcnt(0)\nv_cmp_gt_u32 vcc, v75, v69\ns_cbranch_vccnz .Lso53r1\ns_branch .Lro53r1\n.Lso54r1:\nds_read_b32 v69, v74\ns_waitcnt lgkmcnt(0)\nv_cmp_gt_u32 vcc, v75, v69\n"
"s_cbranch_vccnz .Lso54r1\ns_branch .Lro54r1\n.Lso55r1:\nds_read_b32 v69, v74\ns_waitcnt lgkmcnt(0)\nv_cmp_gt_u32 vcc, v75, v69\ns_cbranch_vccnz .Lso55r1\ns_branch .Lro55r1\n"
".Lso56r1:\nds_read_b32 v69, v74\ns_waitcnt lgkmcnt(0)\nv_cmp_gt_u32 vcc, v75, v69\ns_cbranch_vccnz .Lso56r1\ns_branch .Lro56r1\n.Lso57r1:\nds_read_b32 v69, v74\n"
"s_waitcnt lgkmcnt(0)\nv_cmp_gt_u32 vcc, v75, v69\ns_cbranch_vccnz .Lso57r1\ns_branch .Lro57r1\n.Lso58r1:\nds_read_b32 v69, v74\ns_waitcnt lgkmcnt(0)\nv_cmp_gt_u32 vcc, v75, v69\n"
"s_cbranch_vccnz .Lso58r1\ns_branch .Lro58r1\n.Lso59r1:\nds_read_b32 v69, v74\ns_waitcnt lgkmcnt(0)\nv_cmp_gt_u32 vcc, v75, v69\ns_cbranch_vccnz .Lso59r1\ns_branch .Lro59r1\n"
".Lso60r1:\nds_read_b32 v69, v74\ns_waitcnt lgkmcnt(0)\nv_cmp_gt_u32 vcc, v75, v69\ns_cbranch_vccnz .Lso60r1\ns_branch .Lro60r1\n.Lso61r1:\nds_read_b32 v69, v74\n"
"s_waitcnt lgkmcnt(0)\nv_cmp_gt_u32 vcc, v75, v69\ns_cbranch_vccnz .Lso61r1\ns_branch .Lro61r1\n.Lso62r1:\nds_read_b32 v69, v74\ns_waitcnt lgkmcnt(0)\nv_cmp_gt_u32 vcc, v75, v69\n"
"s_cbranch_vccnz .Lso62r1\ns_branch .Lro62r1\n.Lso63r1:\nds_read_b32 v69, v74\ns_waitcnt lgkmcnt(0)\nv_cmp_gt_u32 vcc, v75, v69\ns_cbranch_vccnz .Lso63r1\ns_branch .Lro63r1\n"
".Ltail1:\n.Lpg11:\ns_cbranch_execz .Ltr0\nQcvaa 8,112,32,32\nv_add_u32 v75, 1, v75\nQmvaa 8,116,36\ns_waitcnt vmcnt(8)\nds_write_b32 v73, v75\n"
"Qmvaa 8,120,40\nQe 0\nQmvaa 8,124,44\nQe 1\nQcvaa 12,128,32,32\nQe 2\nQmvaa 12,132,36\nQe 3\n"
"v_smfmac_f32_16x16x64_bf16 a[188:191], a[204:207], v[16:23], v77\nQmvaa 12,136,40\ns_waitcnt lgkmcnt(1)\nQk 16,0,1\nQmvaa 12,140,44\nQk 17,2,3\nQmvav 80,144,20\nQe 4\n"
"Qmvav 84,148,20\nQe 5\nQmvav 88,152,20\nQe 6\nQmvav 92,156,20\nQe 7\nQmvav 96,160,20\nQk 18,4,5\n"
"Qk 19,6,7\nQmvav 100,164,20\nQe 8\nQmvav 104,168,20\nQe 9\nQmvav 108,172,20\nQe 10\nQcvaa 0,112,48,36\n"
"Qe 11\nQmvaa 0,116,52\nds_read_b32 v69, v74\nQk 20,8,9\nQmvaa 0,120,56\nQk 21,10,11\nQmvaa 0,124,60\nQe 12\n"
"Qcvaa 4,128,48,36\nQe 13\nQmvaa 4,132,52\nQe 14\nQmvaa 4,136,56\ns_waitcnt lgkmcnt(0)\nv_cmp_gt_u32 vcc, v75, v69\nQmvaa 4,140,60\n"
"s_cbranch_vccnz .Lsa29x\n.Lra29x:\nQk 22,12,13\nQmvav 112,144,16\ns_add_u32 m0, s91, 132096\ns_mul_i32 s94, s31, 0\nQmvav 116,148,16\nbuffer_load_dwordx4 v70, s[48:51], s94 offen lds\n"
"Qmvav 120,152,16\nQe 15\nQmvav 124,156,16\nQk 23,14,15\nQmvav 128,160,16\nQe 0\nQmvav 132,164,16\nQe 1\n"
"Qmvav 136,168,16\nbuffer_load_dwordx4 v71, s[48:51], s94 offen offset:1024 lds\nQmvav 140,172,16\nQe 2\nQcvaa 8,112,64,40\nQe 3\nQmvaa 8,116,68\nQe 4\n"
"Qmvaa 8,120,72\ns_mul_i32 s95, s90, 0\nQe 5\nQmvaa 8,124,76\nbuffer_load_dwordx4 v72, s[52:55], s95 offen offset:2048 lds\nQcvaa 12,128,64,40\nQe 6\nQmvaa 12,132,68\n"
"Qe 7\nv_smfmac_f32_16x16x64_bf16 a[192:195], a[204:207], v[16:23], v77\nQmvaa 12,136,72\nQk 16,0,1\nQk 17,2,3\nQmvaa 12,140,76\nbuffer_load_dwordx4 v61, s[52:55], s95 offen offset:3072 lds\nQmvav 144,144,20\n"
"Qk 18,4,5\nQk 19,6,7\nQmvav 148,148,20\nQmvav 152,152,20\nQe 8\nQmvav 156,156,20\ns_add_u32 m0, s91, 136192\nQmvav 160,160,20\n"
"buffer_load_dwordx4 v62, s[52:55], s95 offen lds\nQmvav 164,164,20\nQe 9\nQmvav 168,168,20\nQe 10\nQmvav 172,172,20\nQe 11\nQcvaa 0,112,80,44\n"
"Qk 20,8,9\nQk 21,10,11\nQmvaa 0,116,84\nbuffer_load_dwordx4 v63, s[52:55], s95 offen offset:1024 lds\nQmvaa 0,120,88\nQe 12\nQmvaa 0,124,92\nQe 13\n"
"Qcvaa 4,128,80,44\nQe 14\nQmvaa 4,132,84\nQe 15\nQmvaa 4,136,88\nQmvaa 4,140,92\nQk 22,12,13\nQk 23,14,15\n"
"Qmvav 176,144,16\nQmvav 180,148,16\nQmvav 184,152,16\nQmvav 188,156,16\nQmvav 192,160,16\nQe 0\nQmvav 196,164,16\nQe 1\n"
"Qmvav 200,168,16\nQmvav 204,172,16\nQe 2\nQcvaa 8,112,96,48\nQda 112,53,0\nQmvaa 8,116,100\nQda 116,53,512\nQmvaa 8,120,104\n"
"Qda 120,53,1024\nQmvaa 8,124,108\nQda 124,53,1536\nQcvaa 12,128,96,48\nQda 128,53,2048\nQmvaa 12,132,100\nQda 132,53,2560\nv_smfmac_f32_16x16x64_bf16 a[196:199], a[204:207], v[16:23], v77\n"
"Qmvaa 12,136,104\nQda 136,53,3072\nQk 16,0,1\nQmvaa 12,140,108\nQda 140,53,3584\nQmvav 208,144,20\nQmvav 212,148,20\nQe 3\n"
"Qmvav 216,152,20\nQk 17,2,3\nQmvav 220,156,20\nQe 4\nQmvav 224,160,20\nQe 5\nQmvav 228,164,20\nQe 6\n"
"Qmvav 232,168,20\n.Lpg12:\ns_cbranch_execz .Ltr6\nQe 7\nQmvav 236,172,20\ns_waitcnt lgkmcnt(0)\nQk 18,4,5\nQcvaa 0,112,0,24\n"
"Qk 19,6,7\nQmvaa 0,116,4\nQe 8\nQmvaa 0,120,8\nQe 9\nQmvaa 0,124,12\nQe 10\nQcvaa 4,128,0,24\n"
"Qe 11\nQmvaa 4,132,4\nQk 20,8,9\nQk 21,10,11\nQmvaa 4,136,8\nQe 12\nQmvaa 4,140,12\nQe 13\n"
"Qmvav 240,144,16\nQe 14\nQmvav 244,148,16\nQmvav 248,152,16\nQmvav 252,156,16\nQk 22,12,13\nQmaav 208,160,16\nQmaav 212,164,16\n"
"Qmaav 216,168,16\nQe 15\nQmaav 220,172,16\nQk 23,14,15\nQcvaa 8,112,16,28\nQe 0\nQmvaa 8,116,20\nQe 1\n"
"Qmvaa 8,120,24\nQe 2\nQmvaa 8,124,28\nQe 3\nQcvaa 12,128,16,28\nQe 4\nQmvaa 12,132,20\nQe 5\n"
"v_smfmac_f32_16x16x64_bf16 a[200:203], a[204:207], v[16:23], v77\nQmvaa 12,136,24\nQk 16,0,1\nQk 17,2,3\nQmvaa 12,140,28\nQe 6\nQmaav 224,144,20\nQta 144,55,0\n"
"Qta 146,55,2048\nQmaav 228,148,20\nQta 148,55,256\nQta 150,55,2304\nQmaav 232,152,20\nQta 152,55,512\nQta 154,55,2560\nQmaav 236,156,20\n"
"Qta 156,55,768\nQta 158,55,2816\nQmaav 240,160,20\nQta 160,55,1024\nQta 162,55,3072\nQmaav 244,164,20\nQta 164,55,1280\nQta 166,55,3328\n"
"Qmaav 248,168,20\nQta 168,55,1536\nQta 170,55,3584\nQmaav 252,172,20\nQta 172,55,1792\nQta 174,55,3840\nQcvaa 0,112,32,32\nv_add_u32 v75, 1, v75\n"
"Qk 18,4,5\nQmvaa 0,116,36\ns_waitcnt vmcnt(6)\nds_write_b32 v73, v75\nQmvaa 0,120,40\nQe 7\nQmvaa 0,124,44\nQk 19,6,7\n"
"Qcvaa 4,128,32,32\nQe 8\nQmvaa 4,132,36\nQe 9\nQmvaa 4,136,40\nQe 10\nQmvaa 4,140,44\ns_waitcnt lgkmcnt(1)\n"
"Qk 20,8,9\nQmvav 80,144,16\nQe 11\nQmvav 84,148,16\nQk 21,10,11\nQmvav 88,152,16\nQe 12\nQmvav 92,156,16\n"
"Qe 13\nQmvav 96,160,16\nQe 14\nQmvav 100,164,16\nQe 15\nQmvav 104,168,16\nQk 22,12,13\nQk 23,14,15\n"
"Qmvav 108,172,16\nQe 0\nQcvaa 8,112,48,36\nQe 1\nQmvaa 8,116,52\nQe 2\nQmvaa 8,120,56\nds_read_b32 v69, v74\n"
"Qmvaa 8,124,60\nQe 3\nQcvaa 12,128,48,36\nQe 4\nQmvaa 12,132,52\nQe 5\nv_smfmac_f32_16x16x64_bf16 a[176:179], a[204:207], v[16:23], v77\nQmvaa 12,136,56\n"
"s_waitcnt lgkmcnt(0)\nv_cmp_gt_u32 vcc, v75, v69\nQmvaa 12,140,60\ns_cbranch_vccnz .Lsa30x\n.Lra30x:\nQk 16,0,1\nQmvav 112,144,20\ns_add_u32 m0, s91, 0\n"
"s_mul_i32 s94, s31, 1\nQk 17,2,3\nQmvav 116,148,20\nbuffer_load_dwordx4 v70, s[48:51], s94 offen lds\nQmvav 120,152,20\nQe 6\nQmvav 124,156,20\nQe 7\n"
"Qmvav 128,160,20\nQk 18,4,5\nQk 19,6,7\nQmvav 132,164,20\nQe 8\nQmvav 136,168,20\nbuffer_load_dwordx4 v71, s[48:51], s94 offen offset:1024 lds\nQmvav 140,172,20\n"
"Qe 9\nQcvaa 0,112,64,40\nQe 10\nQmvaa 0,116,68\nQe 11\nQmvaa 0,120,72\ns_mul_i32 s95, s90, 1\nQk 20,8,9\n"
"Qk 21,10,11\nQmvaa 0,124,76\nbuffer_load_dwordx4 v72, s[52:55], s95 offen offset:2048 lds\nQcvaa 4,128,64,40\nQe 12\nQmvaa 4,132,68\nQe 13\nQmvaa 4,136,72\n"
"Qe 14\nQmvaa 4,140,76\nQe 15\nQmvav 144,144,16\nbuffer_load_dwordx4 v61, s[52:55], s95 offen offset:3072 lds\nQmvav 148,148,16\nQk 22,12,13\nQk 23,14,15\n"
"Qmvav 152,152,16\nQmvav 156,156,16\nQe 0\nQmvav 160,160,16\ns_add_u32 m0, s91, 4096\nQmvav 164,164,16\nbuffer_load_dwordx4 v62, s[52:55], s95 offen lds\nQmvav 168,168,16\n"
"Qe 1\nQmvav 172,172,16\nQe 2\nQcvaa 8,112,80,44\nQe 3\nQmvaa 8,116,84\nQe 4\nQmvaa 8,120,88\n"
"buffer_load_dwordx4 v63, s[52:55], s95 offen offset:1024 lds\nQmvaa 8,124,92\nQe 5\nQcvaa 12,128,80,44\nQe 6\nQmvaa 12,132,84\nQe 7\nv_smfmac_f32_16x16x64_bf16 a[180:183], a[204:207], v[16:23], v77\n"
"s_add_u32 m0, s91, 24576\ns_mul_i32 s94, s31, 2\nQmvaa 12,136,88\nbuffer_load_dwordx4 v70, s[48:51], s94 offen lds\nQmvaa 12,140,92\nQk 16,0,1\nQk 17,2,3\nQmvav 176,144,20\n"
"Qk 18,4,5\nQk 19,6,7\nQmvav 180,148,20\nQmvav 184,152,20\nQe 8\nQmvav 188,156,20\nbuffer_load_dwordx4 v71, s[48:51], s94 offen offset:1024 lds\nQmvav 192,160,20\n"
"Qe 9\nQmvav 196,164,20\nQe 10\nQmvav 200,168,20\nQmvav 204,172,20\ns_mul_i32 s95, s90, 2\nQe 11\nQcvaa 0,112,96,48\n"
"Qda 112,53,33024\nbuffer_load_dwordx4 v72, s[52:55], s95 offen offset:2048 lds\nQk 20,8,9\nQmvaa 0,116,100\nQda 116,53,33536\nQk 21,10,11\nQmvaa 0,120,104\nQda 120,53,34048\n"
"Qmvaa 0,124,108\nQda 124,53,34560\nQcvaa 4,128,96,48\nQda 128,53,35072\nQmvaa 4,132,100\nQda 132,53,35584\nbuffer_load_dwordx4 v61, s[52:55], s95 offen offset:3072 lds\nQmvaa 4,136,104\n"
"Qda 136,53,36096\nQmvaa 4,140,108\nQda 140,53,36608\nQmvav 208,144,16\nQe 12\nQmvav 212,148,16\ns_add_u32 m0, s91, 28672\nQmvav 216,152,16\n"
"buffer_load_dwordx4 v62, s[52:55], s95 offen lds\nQe 13\nQmvav 220,156,16\nQe 14\nQmvav 224,160,16\nQe 15\nQmvav 228,164,16\nQk 22,12,13\n"
"Qk 23,14,15\nQmvav 232,168,16\nQe 0\nQmvav 236,172,16\nbuffer_load_dwordx4 v63, s[52:55], s95 offen offset:1024 lds\ns_waitcnt lgkmcnt(0)\nQcvaa 8,112,0,24\nQe 1\n"
"Qmvaa 8,116,4\nQe 2\nQmvaa 8,120,8\nQe 3\nQmvaa 8,124,12\ns_add_u32 m0, s91, 49152\ns_mul_i32 s94, s31, 3\nQe 4\n"
"Qcvaa 12,128,0,24\nbuffer_load_dwordx4 v70, s[48:51], s94 offen lds\nQe 5\nQmvaa 12,132,4\nQe 6\nv_smfmac_f32_16x16x64_bf16 a[184:187], a[204:207], v[16:23], v77\nQmvaa 12,136,8\nQk 16,0,1\n"
"Qk 17,2,3\nQmvaa 12,140,12\nQe 7\nQmvav 240,144,20\nbuffer_load_dwordx4 v71, s[48:51], s94 offen offset:1024 lds\nQmvav 244,148,20\nQmvav 248,152,20\nQk 18,4,5\n"
"Qmvav 252,156,20\nQk 19,6,7\nQmaav 208,160,20\ns_mul_i32 s95, s90, 3\nQmaav 212,164,20\nbuffer_load_dwordx4 v72, s[52:55], s95 offen offset:2048 lds\nQe 8\nQmaav 216,168,20\n"
"Qe 9\nQmaav 220,172,20\nQe 10\nQcvaa 0,112,16,28\nQe 11\nQmvaa 0,116,20\nQk 20,8,9\nQk 21,10,11\n"
"Qmvaa 0,120,24\nbuffer_load_dwordx4 v61, s[52:55], s95 offen offset:3072 lds\nQe 12\nQmvaa 0,124,28\nQe 13\nQcvaa 4,128,16,28\nQe 14\nQmvaa 4,132,20\n"
"Qe 15\nQmvaa 4,136,24\ns_add_u32 m0, s91, 53248\nQk 22,12,13\nQk 23,14,15\nQmvaa 4,140,28\nbuffer_load_dwordx4 v62, s[52:55], s95 offen lds\nQmaav 224,144,16\n"
"Qta 144,55,33024\nQta 146,55,35072\nQmaav 228,148,16\nQta 148,55,33280\nQta 150,55,35328\nQmaav 232,152,16\nQta 152,55,33536\nQta 154,55,35584\n"
"Qmaav 236,156,16\nQta 156,55,33792\nQta 158,55,35840\nQmaav 240,160,16\nQta 160,55,34048\nQta 162,55,36096\nbuffer_load_dwordx4 v63, s[52:55], s95 offen offset:1024 lds\nQmaav 244,164,16\n"
"Qta 164,55,34304\nQta 166,55,36352\nQmaav 248,168,16\nQta 168,55,34560\nQta 170,55,36608\nQmaav 252,172,16\nQta 172,55,34816\nQta 174,55,36864\n"
"Qcvaa 8,112,32,32\nv_add_u32 v75, 1, v75\nQmvaa 8,116,36\nds_write_b32 v73, v75\nQmvaa 8,120,40\nQe 0\nQmvaa 8,124,44\nQe 1\n"
"Qcvaa 12,128,32,32\nQe 2\nQmvaa 12,132,36\nQe 3\nv_smfmac_f32_16x16x64_bf16 a[188:191], a[204:207], v[16:23], v77\nQmvaa 12,136,40\ns_waitcnt lgkmcnt(1)\nQk 16,0,1\n"
"Qmvaa 12,140,44\nQk 17,2,3\nQmvav 80,144,20\nQe 4\nQmvav 84,148,20\nQe 5\nQmvav 88,152,20\nQe 6\n"
"Qmvav 92,156,20\nQe 7\nQmvav 96,160,20\nQk 18,4,5\nQk 19,6,7\nQmvav 100,164,20\nQe 8\nQmvav 104,168,20\n"
"Qe 9\nQmvav 108,172,20\nQe 10\nQcvaa 0,112,48,36\nQe 11\nQmvaa 0,116,52\nds_read_b32 v69, v74\nQk 20,8,9\n"
"Qmvaa 0,120,56\nQk 21,10,11\nQmvaa 0,124,60\nQe 12\nQcvaa 4,128,48,36\nQe 13\nQmvaa 4,132,52\nQe 14\n"
"Qmvaa 4,136,56\ns_waitcnt lgkmcnt(0)\nv_cmp_gt_u32 vcc, v75, v69\nQmvaa 4,140,60\ns_cbranch_vccnz .Lsa31x\n.Lra31x:\nQk 22,12,13\nQmvav 112,144,16\n"
"s_add_u32 m0, s91, 73728\ns_mul_i32 s94, s31, 4\nQmvav 116,148,16\nbuffer_load_dwordx4 v70, s[48:51], s94 offen lds\nQmvav 120,152,16\nQe 15\nQmvav 124,156,16\nQk 23,14,15\n"
"Qmvav 128,160,16\nQe 0\nQmvav 132,164,16\nQe 1\nQmvav 136,168,16\nbuffer_load_dwordx4 v71, s[48:51], s94 offen offset:1024 lds\nQmvav 140,172,16\nQe 2\n"
"Qcvaa 8,112,64,40\nQe 3\nQmvaa 8,116,68\nQe 4\nQmvaa 8,120,72\ns_mul_i32 s95, s90, 4\nQe 5\nQmvaa 8,124,76\n"
"buffer_load_dwordx4 v72, s[52:55], s95 offen offset:2048 lds\nQcvaa 12,128,64,40\nQe 6\nQmvaa 12,132,68\nQe 7\nv_smfmac_f32_16x16x64_bf16 a[192:195], a[204:207], v[16:23], v77\nQmvaa 12,136,72\nQk 16,0,1\n"
"Qk 17,2,3\nQmvaa 12,140,76\nbuffer_load_dwordx4 v61, s[52:55], s95 offen offset:3072 lds\nQmvav 144,144,20\nQk 18,4,5\nQk 19,6,7\nQmvav 148,148,20\nQmvav 152,152,20\n"
"Qe 8\nQmvav 156,156,20\ns_add_u32 m0, s91, 77824\nQmvav 160,160,20\nbuffer_load_dwordx4 v62, s[52:55], s95 offen lds\nQmvav 164,164,20\nQe 9\nQmvav 168,168,20\n"
"Qe 10\nQmvav 172,172,20\nQe 11\nQcvaa 0,112,80,44\nQk 20,8,9\nQk 21,10,11\nQmvaa 0,116,84\nbuffer_load_dwordx4 v63, s[52:55], s95 offen offset:1024 lds\n"
"Qmvaa 0,120,88\nQe 12\nQmvaa 0,124,92\nQe 13\nQcvaa 4,128,80,44\nQe 14\nQmvaa 4,132,84\ns_add_u32 m0, s91, 98304\n"
"s_mul_i32 s94, s31, 5\nQe 15\nQmvaa 4,136,88\nbuffer_load_dwordx4 v70, s[48:51], s94 offen lds\nQmvaa 4,140,92\nQk 22,12,13\nQk 23,14,15\nQmvav 176,144,16\n"
"Qmvav 180,148,16\nQmvav 184,152,16\nQmvav 188,156,16\nbuffer_load_dwordx4 v71, s[48:51], s94 offen offset:1024 lds\nQmvav 192,160,16\nQe 0\nQmvav 196,164,16\nQe 1\n"
"Qmvav 200,168,16\nQmvav 204,172,16\ns_mul_i32 s95, s90, 5\nQe 2\nQcvaa 8,112,96,48\nbuffer_load_dwordx4 v72, s[52:55], s95 offen offset:2048 lds\nQmvaa 8,116,100\nQmvaa 8,120,104\n"
"Qmvaa 8,124,108\nQcvaa 12,128,96,48\nQmvaa 12,132,100\nbuffer_load_dwordx4 v61, s[52:55], s95 offen offset:3072 lds\nv_smfmac_f32_16x16x64_bf16 a[196:199], a[204:207], v[16:23], v77\nQmvaa 12,136,104\nQk 16,0,1\nQmvaa 12,140,108\n"
"Qmvav 208,144,20\ns_add_u32 m0, s91, 102400\nQmvav 212,148,20\nbuffer_load_dwordx4 v62, s[52:55], s95 offen lds\nQe 3\nQmvav 216,152,20\nQk 17,2,3\nQmvav 220,156,20\n"
"Qe 4\nQmvav 224,160,20\nQe 5\nQmvav 228,164,20\nQe 6\nQmvav 232,168,20\nbuffer_load_dwordx4 v63, s[52:55], s95 offen offset:1024 lds\nQe 7\n"
"Qmvav 236,172,20\nQk 18,4,5\nQk 19,6,7\nQe 8\nQe 9\nQe 10\nQe 11\nQk 20,8,9\n"
"Qk 21,10,11\nQe 12\nQe 13\nQmvav 240,144,16\nQe 14\nQmvav 244,148,16\nQmvav 248,152,16\nQmvav 252,156,16\n"
"Qk 22,12,13\nQmaav 208,160,16\nQmaav 212,164,16\nQmaav 216,168,16\nQe 15\nQmaav 220,172,16\nQk 23,14,15\ns_nop 1\n"
"v_smfmac_f32_16x16x64_bf16 a[200:203], a[204:207], v[16:23], v77\nQmaav 224,144,20\nQmaav 228,148,20\nQmaav 232,152,20\nQmaav 236,156,20\nQmaav 240,160,20\nQmaav 244,164,20\nQmaav 248,168,20\n"
"Qmaav 252,172,20\nv_accvgpr_read_b32 v24, a176\nv_accvgpr_read_b32 v25, a177\nv_accvgpr_read_b32 v30, a178\nv_accvgpr_read_b32 v31, a179\nv_accvgpr_read_b32 v32, a180\nv_accvgpr_read_b32 v33, a181\nv_accvgpr_read_b32 v38, a182\n"
"v_accvgpr_read_b32 v39, a183\nv_accvgpr_read_b32 v40, a184\nv_accvgpr_read_b32 v41, a185\nv_accvgpr_read_b32 v46, a186\nv_accvgpr_read_b32 v47, a187\nv_accvgpr_read_b32 v48, a188\nv_accvgpr_read_b32 v49, a189\nv_accvgpr_read_b32 v26, a190\n"
"v_accvgpr_read_b32 v27, a191\nv_accvgpr_read_b32 v28, a192\nv_accvgpr_read_b32 v29, a193\nv_accvgpr_read_b32 v34, a194\nv_accvgpr_read_b32 v35, a195\nv_accvgpr_read_b32 v36, a196\nv_accvgpr_read_b32 v37, a197\nv_accvgpr_read_b32 v42, a198\n"
"v_accvgpr_read_b32 v43, a199\nv_accvgpr_read_b32 v44, a200\nv_accvgpr_read_b32 v45, a201\nv_accvgpr_read_b32 v50, a202\nv_accvgpr_read_b32 v51, a203\nv_pk_add_f32 v[24:25], v[24:25], v[26:27]\nv_pk_add_f32 v[28:29], v[28:29], v[30:31]\nv_pk_add_f32 v[32:33], v[32:33], v[34:35]\n"
"v_pk_add_f32 v[36:37], v[36:37], v[38:39]\nv_pk_add_f32 v[40:41], v[40:41], v[42:43]\nv_pk_add_f32 v[44:45], v[44:45], v[46:47]\nv_pk_add_f32 v[48:49], v[48:49], v[50:51]\nQa 24,24,25\nQa 28,28,29\nQa 32,32,33\nQa 36,36,37\n"
"Qa 40,40,41\nQa 44,44,45\nQa 48,48,49\nv_cmp_nge_f32 vcc, 0x44800000, v24\ns_mov_b64 s[74:75], vcc\nv_cmp_nge_f32 vcc, 0x44800000, v28\ns_or_b64 s[74:75], s[74:75], vcc\nv_cmp_nge_f32 vcc, 0x44800000, v32\n"
"s_or_b64 s[74:75], s[74:75], vcc\nv_cmp_nge_f32 vcc, 0x44800000, v36\n.Lpg13:\ns_cbranch_execz .Ltr6\ns_or_b64 s[74:75], s[74:75], vcc\nv_cmp_nge_f32 vcc, 0x44800000, v40\ns_or_b64 s[74:75], s[74:75], vcc\nv_cmp_nge_f32 vcc, 0x44800000, v44\n"
"s_or_b64 s[74:75], s[74:75], vcc\nv_cmp_nge_f32 vcc, 0x44800000, v48\ns_or_b64 s[74:75], s[74:75], vcc\ns_cmp_lg_u64 s[74:75], 0\ns_cbranch_scc1 .Lfb\n.Lfbret1:\nv_rcp_f32 v52, v24\nv_rcp_f32 v53, v28\n"
"v_rcp_f32 v54, v32\nv_rcp_f32 v55, v36\nv_rcp_f32 v56, v40\nv_rcp_f32 v57, v44\nv_rcp_f32 v58, v48\ns_mov_b32 s56, %8\ns_and_b32 s57, %9, 0xffff\ns_mov_b32 s58, 1835008\n"
"s_lshl_b32 s68, %19, 4\ns_cmp_eq_u32 s70, 0\ns_cselect_b32 s68, s68, -1\nv_mov_b32 v77, 0x7ffff000\nv_cmp_eq_u32 vcc, s68, v60\ns_nop 1\nv_cndmask_b32 v77, v77, v76, vcc\ns_mul_i32 s68, s92, 7168\n"
"s_add_u32 s68, s68, %10\nv_add_u32 v48, s68, v60\nv_add_u32 v49, 73728, v48\ns_mul_i32 s68, s93, 1280\ns_add_u32 s68, s68, %10\nv_lshrrev_b32 v66, 9, v60\nv_mul_u32_u24 v66, 2560, v66\nv_bfe_u32 v67, v60, 1, 8\n"
"v_add3_u32 v64, v66, v67, s68\nv_add_u32 v59, 57344, v64\nv_add_u32 v50, 114688, v64\ns_mov_b64 exec, 0\ns_cmp_eq_u32 s70, 0\ns_cbranch_scc1 .Lpg16\ns_cmp_eq_u32 s70, 1\ns_cbranch_scc1 .Lpg17\n"
"s_cmp_eq_u32 s70, 2\ns_cbranch_scc1 .Lpg18\ns_branch .Lpg19\n.Ltr8:\ns_mov_b64 exec, -1\ns_waitcnt vmcnt(24)\ns_barrier\nQtv 0,50,31744\n"
"Qtv 2,50,36864\nQtv 4,50,32000\nQtv 6,50,37120\nQtv 8,50,32256\nQtv 10,50,37376\nQtv 12,50,32512\nQtv 14,50,37632\nQtv 16,50,32768\n"
"Qtv 18,50,37888\nQda 112,49,58368\nQda 116,49,59392\nQda 120,49,60416\nQda 124,49,61440\nQda 128,49,62464\nQda 132,49,63488\nQda 136,49,64512\n"
"v_add_u32 v75, 1, v75\ns_waitcnt vmcnt(18)\nds_write_b32 v73, v75\nv_mul_f32 v80, v52, v80\nv_mul_f32 v81, v52, v81\nv_mul_f32 v82, v52, v82\nv_mul_f32 v83, v52, v83\nv_mul_f32 v84, v52, v84\n"
"v_mul_f32 v85, v52, v85\nv_mul_f32 v86, v52, v86\nv_mul_f32 v87, v52, v87\nQk 80,80,81\nQk 81,82,83\nQk 82,84,85\nQk 83,86,87\nv_mul_f32 v112, v53, v112\n"
"v_mul_f32 v113, v53, v113\nv_mul_f32 v114, v53, v114\nv_mul_f32 v115, v53, v115\nv_mul_f32 v116, v53, v116\nv_mul_f32 v117, v53, v117\nv_mul_f32 v118, v53, v118\nv_mul_f32 v119, v53, v119\nQk 112,112,113\n"
"Qk 113,114,115\nQk 114,116,117\nQk 115,118,119\ns_waitcnt lgkmcnt(5)\nQzava 0,0,112\nQtv 20,64,14336\nv_mul_f32 v144, v54, v144\nQzava 4,4,112\n"
"Qtv 22,64,19456\nv_mul_f32 v145, v54, v145\nQzava 8,8,112\nQtv 24,64,14592\nv_mul_f32 v146, v54, v146\nQzava 12,12,112\nv_mul_f32 v147, v54, v147\nv_mul_f32 v148, v54, v148\n"
"Qzava 16,16,112\nQtv 26,64,19712\nv_mul_f32 v149, v54, v149\nQzava 20,0,116\nQtv 28,64,14848\nv_mul_f32 v150, v54, v150\nQzava 24,4,116\nv_mul_f32 v151, v54, v151\n"
"Qk 144,144,145\nQzava 28,8,116\nds_read_b32 v69, v74\nQk 145,146,147\nQzava 32,12,116\nQtv 30,64,19968\nQk 146,148,149\nQzava 36,16,116\n"
"Qtv 32,64,15104\nQk 147,150,151\nQzava 40,0,120\nv_mul_f32 v176, v55, v176\nv_mul_f32 v177, v55, v177\nQzava 44,4,120\ns_waitcnt lgkmcnt(2)\nv_cmp_gt_u32 vcc, v75, v69\n"
"Qzava 48,8,120\ns_cbranch_vccnz .Lso0\n.Lro0:\nv_mul_f32 v178, v55, v178\nQzava 52,12,120\nQtv 34,64,20224\ns_add_u32 m0, s91, 132096\ns_mul_i32 s94, s31, 6\n"
"Qzava 56,16,120\nbuffer_load_dwordx4 v70, s[48:51], s94 offen lds\nQzava 60,0,124\nQtv 36,64,15360\nv_mul_f32 v179, v55, v179\nQzava 64,4,124\nv_mul_f32 v180, v55, v180\nv_mul_f32 v181, v55, v181\n"
"Qzava 68,8,124\nQtv 38,64,20480\nv_mul_f32 v182, v55, v182\nQzava 72,12,124\nbuffer_load_dwordx4 v71, s[48:51], s94 offen offset:1024 lds\nQzava 76,16,124\nQda 140,48,0\nv_mul_f32 v183, v55, v183\n"
"Qzava 80,0,128\nQk 176,176,177\nQk 177,178,179\nQzava 84,4,128\nQda 144,48,1024\ns_mul_i32 s95, s90, 6\nQzava 88,8,128\nbuffer_load_dwordx4 v72, s[52:55], s95 offen offset:2048 lds\n"
"Qzava 92,12,128\nQda 148,48,2048\nQk 178,180,181\nQzava 96,16,128\nQk 179,182,183\nv_mul_f32 v208, v56, v208\nQzava 100,0,132\nQda 152,48,3072\n"
"v_mul_f32 v209, v56, v209\nQzava 104,4,132\nbuffer_load_dwordx4 v61, s[52:55], s95 offen offset:3072 lds\nQzava 108,8,132\nQda 156,48,4096\nv_mul_f32 v210, v56, v210\nQzava 176,12,132\nv_mul_f32 v211, v56, v211\n"
"v_mul_f32 v212, v56, v212\nQzava 180,16,132\nQda 160,48,5120\ns_add_u32 m0, s91, 136192\nQzava 184,0,136\nbuffer_load_dwordx4 v62, s[52:55], s95 offen lds\nQzava 188,4,136\nQda 164,48,6144\n"
"v_mul_f32 v213, v56, v213\nQzava 192,8,136\nv_mul_f32 v214, v56, v214\nv_mul_f32 v215, v56, v215\nQzava 196,12,136\nbuffer_load_dwordx4 v63, s[52:55], s95 offen offset:1024 lds\nv_add_u32 v75, 1, v75\nQzava 200,16,136\n"
"s_waitcnt vmcnt(18)\nds_write_b32 v73, v75\ns_waitcnt lgkmcnt(5)\nQmava 0,20,140\nQtv 0,64,38912\ns_add_u32 s88, s71, 0\nQmava 4,24,140\nbuffer_store_dwordx4 v[80:83], v60, s[56:59], s88 offen offset:0 sc1\n"
"Qmava 8,28,140\nQtv 2,64,44032\nQk 208,208,209\nQmava 12,32,140\nQtv 4,64,39168\nQk 209,210,211\nQmava 16,36,140\nQk 210,212,213\n"
"Qk 211,214,215\nQmava 20,20,144\nQtv 6,64,44288\nv_mul_f32 v240, v57, v240\nQmava 24,24,144\nQtv 8,64,39424\nv_mul_f32 v241, v57, v241\nQmava 28,28,144\n"
"ds_read_b32 v69, v74\nv_mul_f32 v242, v57, v242\nQmava 32,32,144\nv_mul_f32 v243, v57, v243\nv_mul_f32 v244, v57, v244\nQmava 36,36,144\nQtv 10,64,44544\nv_mul_f32 v245, v57, v245\n"
"Qmava 40,20,148\nQtv 12,64,39680\nv_mul_f32 v246, v57, v246\nQmava 44,24,148\ns_waitcnt lgkmcnt(2)\nv_cmp_gt_u32 vcc, v75, v69\nQmava 48,28,148\ns_cbranch_vccnz .Lso1\n"
".Lro1:\nv_mul_f32 v247, v57, v247\nQmava 52,32,148\nQtv 14,64,44800\ns_add_u32 m0, s91, 0\ns_mul_i32 s94, s31, 7\nQmava 56,36,148\nbuffer_load_dwordx4 v70, s[48:51], s94 offen lds\n"
"Qmava 60,20,152\nQk 240,240,241\nQk 241,242,243\nQmava 64,24,152\nQtv 16,64,39936\nQk 242,244,245\nQmava 68,28,152\nQtv 18,64,45056\n"
"Qk 243,246,247\nQmava 72,32,152\nbuffer_load_dwordx4 v71, s[48:51], s94 offen offset:1024 lds\nQmava 76,36,152\nv_accvgpr_read_b32 v40, a224\nv_accvgpr_read_b32 v41, a225\nQmava 80,20,156\nQda 112,48,24576\n"
"v_accvgpr_read_b32 v42, a226\nQmava 84,24,156\nQda 116,48,25600\ns_mul_i32 s95, s90, 7\nQmava 88,28,156\nbuffer_load_dwordx4 v72, s[52:55], s95 offen offset:2048 lds\nQmava 92,32,156\nQda 120,48,26624\n"
"v_accvgpr_read_b32 v43, a227\nQmava 96,36,156\nv_accvgpr_read_b32 v44, a228\nv_accvgpr_read_b32 v45, a229\nQmava 100,20,160\nQda 124,48,27648\nv_accvgpr_read_b32 v46, a230\nQmava 104,24,160\n"
"buffer_load_dwordx4 v61, s[52:55], s95 offen offset:3072 lds\nQmava 108,28,160\nQda 128,48,28672\nv_accvgpr_read_b32 v47, a231\nQmava 176,32,160\nv_mul_f32 v40, v58, v40\nv_mul_f32 v41, v58, v41\nQmava 180,36,160\n"
"Qda 132,48,29696\ns_add_u32 m0, s91, 4096\nQmava 184,20,164\nbuffer_load_dwordx4 v62, s[52:55], s95 offen lds\nQmava 188,24,164\nQda 136,48,30720\nv_mul_f32 v42, v58, v42\nQmava 192,28,164\n"
"v_mul_f32 v43, v58, v43\nv_mul_f32 v44, v58, v44\nQmava 196,32,164\nbuffer_load_dwordx4 v63, s[52:55], s95 offen offset:1024 lds\nv_add_u32 v75, 1, v75\nQmava 200,36,164\ns_waitcnt vmcnt(19)\nds_write_b32 v73, v75\n"
"s_waitcnt lgkmcnt(5)\nQmava 0,0,112\nQtv 20,59,6144\nv_mul_f32 v45, v58, v45\nQmava 4,4,112\nbuffer_store_dwordx4 v[112:115], v60, s[56:59], s88 offen offset:1024 sc1\nQmava 8,8,112\nQtv 22,59,11264\n"
"v_mul_f32 v46, v58, v46\nQmava 12,12,112\nQtv 24,59,6400\nv_mul_f32 v47, v58, v47\nQmava 16,16,112\nQk 84,40,41\nQk 85,42,43\nQmava 20,0,116\n"
"Qtv 26,59,11520\nQk 86,44,45\nQmava 24,4,116\nQtv 28,59,6656\nQk 87,46,47\nQmava 28,8,116\nds_read_b32 v69, v74\nv_mul_f32 v88, v52, v88\n"
"Qmava 32,12,116\nv_mul_f32 v89, v52, v89\nv_mul_f32 v90, v52, v90\nQmava 36,16,116\nQtv 30,59,11776\nv_mul_f32 v91, v52, v91\nQmava 40,0,120\nQtv 32,59,6912\n"
"v_mul_f32 v92, v52, v92\nQmava 44,4,120\ns_waitcnt lgkmcnt(2)\nv_cmp_gt_u32 vcc, v75, v69\nQmava 48,8,120\ns_cbranch_vccnz .Lso2\n.Lro2:\nv_mul_f32 v93, v52, v93\n"
"Qmava 52,12,120\nQtv 34,59,12032\ns_add_u32 m0, s91, 24576\ns_mul_i32 s94, s31, 8\nQmava 56,16,120\nbuffer_load_dwordx4 v70, s[48:51], s94 offen lds\nQmava 60,0,124\nv_mul_f32 v94, v52, v94\n"
"v_mul_f32 v95, v52, v95\nQmava 64,4,124\nQtv 36,59,7168\nQk 88,88,89\nQmava 68,8,124\nQtv 38,59,12288\nQk 89,90,91\nQmava 72,12,124\n"
"buffer_load_dwordx4 v71, s[48:51], s94 offen offset:1024 lds\nQmava 76,16,124\nQk 90,92,93\nQk 91,94,95\nQmava 80,0,128\nQda 140,48,49152\nv_mul_f32 v120, v53, v120\nQmava 84,4,128\n"
"Qda 144,48,50176\ns_mul_i32 s95, s90, 8\nQmava 88,8,128\nbuffer_load_dwordx4 v72, s[52:55], s95 offen offset:2048 lds\nQmava 92,12,128\nQda 148,48,51200\nv_mul_f32 v121, v53, v121\nQmava 96,16,128\n"
"v_mul_f32 v122, v53, v122\nv_mul_f32 v123, v53, v123\nQmava 100,0,132\nQda 152,48,52224\nv_mul_f32 v124, v53, v124\nQmava 104,4,132\nbuffer_load_dwordx4 v61, s[52:55], s95 offen offset:3072 lds\nQmava 108,8,132\n"
"Qda 156,48,53248\nv_mul_f32 v125, v53, v125\nQmava 176,12,132\nv_mul_f32 v126, v53, v126\nv_mul_f32 v127, v53, v127\nQmava 180,16,132\nQda 160,48,54272\ns_add_u32 m0, s91, 28672\n"
"Qmava 184,0,136\nbuffer_load_dwordx4 v62, s[52:55], s95 offen lds\nQmava 188,4,136\nQda 164,48,55296\nQk 120,120,121\nQmava 192,8,136\nQk 121,122,123\nQk 122,124,125\n"
"Qmava 196,12,136\nbuffer_load_dwordx4 v63, s[52:55], s95 offen offset:1024 lds\nv_add_u32 v75, 1, v75\nQmava 200,16,136\ns_waitcnt vmcnt(20)\nds_write_b32 v73, v75\ns_waitcnt lgkmcnt(5)\nQmava 0,20,140\n"
"Qtv 0,59,30720\nQk 123,126,127\nQmava 4,24,140\nbuffer_store_dwordx4 v[144:147], v60, s[56:59], s88 offen offset:2048 sc1\nQmava 8,28,140\nQtv 2,59,35840\nv_mul_f32 v152, v54, v152\nQmava 12,32,140\n"
"Qtv 4,59,30976\nv_mul_f32 v153, v54, v153\nQmava 16,36,140\nv_mul_f32 v154, v54, v154\nv_mul_f32 v155, v54, v155\nQmava 20,20,144\nQtv 6,59,36096\nv_mul_f32 v156, v54, v156\n"
"Qmava 24,24,144\nQtv 8,59,31232\nv_mul_f32 v157, v54, v157\nQmava 28,28,144\nds_read_b32 v69, v74\nv_mul_f32 v158, v54, v158\nQmava 32,32,144\nv_mul_f32 v159, v54, v159\n"
"Qk 152,152,153\nQmava 36,36,144\nQtv 10,59,36352\nQk 153,154,155\nQmava 40,20,148\nQtv 12,59,31488\nQk 154,156,157\nQmava 44,24,148\n"
"s_waitcnt lgkmcnt(2)\nv_cmp_gt_u32 vcc, v75, v69\nQmava 48,28,148\ns_cbranch_vccnz .Lso3\n.Lro3:\nQk 155,158,159\nQmava 52,32,148\nQtv 14,59,36608\n"
"s_add_u32 m0, s91, 49152\ns_mul_i32 s94, s31, 9\nQmava 56,36,148\nbuffer_load_dwordx4 v70, s[48:51], s94 offen lds\nQmava 60,20,152\nv_mul_f32 v184, v55, v184\nv_mul_f32 v185, v55, v185\nQmava 64,24,152\n"
"Qtv 16,59,31744\nv_mul_f32 v186, v55, v186\nQmava 68,28,152\nQtv 18,59,36864\nv_mul_f32 v187, v55, v187\nQmava 72,32,152\nbuffer_load_dwordx4 v71, s[48:51], s94 offen offset:1024 lds\nQmava 76,36,152\n"
"v_mul_f32 v188, v55, v188\nv_mul_f32 v189, v55, v189\nQmava 80,20,156\nQda 112,49,0\nv_mul_f32 v190, v55, v190\nQmava 84,24,156\nQda 116,49,1024\ns_mul_i32 s95, s90, 9\n"
"Qmava 88,28,156\nbuffer_load_dwordx4 v72, s[52:55], s95 offen offset:2048 lds\nQmava 92,32,156\nQda 120,49,2048\nv_mul_f32 v191, v55, v191\nQmava 96,36,156\nQk 184,184,185\nQk 185,186,187\n"
"Qmava 100,20,160\nQda 124,49,3072\nQk 186,188,189\nQmava 104,24,160\nbuffer_load_dwordx4 v61, s[52:55], s95 offen offset:3072 lds\nQmava 108,28,160\nQda 128,49,4096\nQk 187,190,191\n"
"Qmava 176,32,160\nv_mul_f32 v216, v56, v216\nv_mul_f32 v217, v56, v217\nQmava 180,36,160\nQda 132,49,5120\ns_add_u32 m0, s91, 53248\nQmava 184,20,164\nbuffer_load_dwordx4 v62, s[52:55], s95 offen lds\n"
"Qmava 188,24,164\nQda 136,49,6144\nv_mul_f32 v218, v56, v218\nQmava 192,28,164\nv_mul_f32 v219, v56, v219\nv_mul_f32 v220, v56, v220\nQmava 196,32,164\nbuffer_load_dwordx4 v63, s[52:55], s95 offen offset:1024 lds\n"
"v_add_u32 v75, 1, v75\nQmava 200,36,164\ns_waitcnt vmcnt(21)\nds_write_b32 v73, v75\ns_waitcnt lgkmcnt(5)\nQmava 0,0,112\nQtv 20,59,55296\nv_mul_f32 v221, v56, v221\n"
"Qmava 4,4,112\nbuffer_store_dwordx4 v[176:179], v60, s[56:59], s88 offen offset:3072 sc1\nQmava 8,8,112\nQtv 22,59,60416\nv_mul_f32 v222, v56, v222\nQmava 12,12,112\nQtv 24,59,55552\nv_mul_f32 v223, v56, v223\n"
"Qmava 16,16,112\nQk 216,216,217\nQk 217,218,219\nQmava 20,0,116\nQtv 26,59,60672\nQk 218,220,221\nQmava 24,4,116\nQtv 28,59,55808\n"
"Qk 219,222,223\nQmava 28,8,116\nds_read_b32 v69, v74\nv_mul_f32 v248, v57, v248\nQmava 32,12,116\nv_mul_f32 v249, v57, v249\nv_mul_f32 v250, v57, v250\nQmava 36,16,116\n"
"Qtv 30,59,60928\nv_mul_f32 v251, v57, v251\nQmava 40,0,120\nQtv 32,59,56064\nv_mul_f32 v252, v57, v252\nQmava 44,4,120\ns_waitcnt lgkmcnt(2)\nv_cmp_gt_u32 vcc, v75, v69\n"
"Qmava 48,8,120\ns_cbranch_vccnz .Lso4\n.Lro4:\nv_mul_f32 v253, v57, v253\nQmava 52,12,120\nQtv 34,59,61184\ns_add_u32 m0, s91, 73728\ns_mul_i32 s94, s31, 10\n"
"Qmava 56,16,120\nbuffer_load_dwordx4 v70, s[48:51], s94 offen lds\nQmava 60,0,124\nv_mul_f32 v254, v57, v254\nv_mul_f32 v255, v57, v255\nQmava 64,4,124\nQtv 36,59,56320\nQk 248,248,249\n"
"Qmava 68,8,124\nQtv 38,59,61440\nQk 249,250,251\nQmava 72,12,124\nbuffer_load_dwordx4 v71, s[48:51], s94 offen offset:1024 lds\nQmava 76,16,124\nQk 250,252,253\nQk 251,254,255\n"
"Qmava 80,0,128\nQda 140,49,24576\nv_accvgpr_read_b32 v40, a232\nQmava 84,4,128\nQda 144,49,25600\ns_mul_i32 s95, s90, 10\nQmava 88,8,128\nbuffer_load_dwordx4 v72, s[52:55], s95 offen offset:2048 lds\n"
"Qmava 92,12,128\nQda 148,49,26624\nv_accvgpr_read_b32 v41, a233\nQmava 96,16,128\nv_accvgpr_read_b32 v42, a234\nv_accvgpr_read_b32 v43, a235\nQmava 100,0,132\nQda 152,49,27648\n"
"v_accvgpr_read_b32 v44, a236\nQmava 104,4,132\nbuffer_load_dwordx4 v61, s[52:55], s95 offen offset:3072 lds\nQmava 108,8,132\nQda 156,49,28672\nv_accvgpr_read_b32 v45, a237\nQmava 176,12,132\nv_accvgpr_read_b32 v46, a238\n"
"v_accvgpr_read_b32 v47, a239\nQmava 180,16,132\nQda 160,49,29696\ns_add_u32 m0, s91, 77824\nQmava 184,0,136\nbuffer_load_dwordx4 v62, s[52:55], s95 offen lds\nQmava 188,4,136\nQda 164,49,30720\n"
"v_mul_f32 v40, v58, v40\nQmava 192,8,136\nv_mul_f32 v41, v58, v41\nv_mul_f32 v42, v58, v42\nQmava 196,12,136\nbuffer_load_dwordx4 v63, s[52:55], s95 offen offset:1024 lds\nv_add_u32 v75, 1, v75\nQmava 200,16,136\n"
"s_waitcnt vmcnt(21)\nds_write_b32 v73, v75\ns_waitcnt lgkmcnt(5)\nQmava 0,20,140\nQtv 0,50,31744\ns_add_u32 s88, s71, 4096\nQmava 4,24,140\nbuffer_store_dwordx4 v[208:211], v60, s[56:59], s88 offen offset:0 sc1\n"
".Lpg14:\ns_cbranch_execz .Ltr6\nQmava 8,28,140\nQtv 2,50,36864\nv_mul_f32 v43, v58, v43\nQmava 12,32,140\nQtv 4,50,32000\nv_mul_f32 v44, v58, v44\n"
"Qmava 16,36,140\nv_mul_f32 v45, v58, v45\nv_mul_f32 v46, v58, v46\nQmava 20,20,144\nQtv 6,50,37120\nv_mul_f32 v47, v58, v47\nQmava 24,24,144\nQtv 8,50,32256\n"
"Qk 116,40,41\nQmava 28,28,144\nds_read_b32 v69, v74\nQk 117,42,43\nQmava 32,32,144\nQk 118,44,45\nQk 119,46,47\nQmava 36,36,144\n"
"Qtv 10,50,37376\nv_mul_f32 v96, v52, v96\nQmava 40,20,148\nQtv 12,50,32512\nv_mul_f32 v97, v52, v97\nQmava 44,24,148\ns_waitcnt lgkmcnt(2)\nv_cmp_gt_u32 vcc, v75, v69\n"
"Qmava 48,28,148\ns_cbranch_vccnz .Lso5\n.Lro5:\nv_mul_f32 v98, v52, v98\nQmava 52,32,148\nQtv 14,50,37632\ns_add_u32 m0, s91, 98304\ns_mul_i32 s94, s31, 11\n"
"Qmava 56,36,148\nbuffer_load_dwordx4 v70, s[48:51], s94 offen lds\nQmava 60,20,152\nv_mul_f32 v99, v52, v99\nv_mul_f32 v100, v52, v100\nQmava 64,24,152\nQtv 16,50,32768\nv_mul_f32 v101, v52, v101\n"
"Qmava 68,28,152\nQtv 18,50,37888\nv_mul_f32 v102, v52, v102\nQmava 72,32,152\nbuffer_load_dwordx4 v71, s[48:51], s94 offen offset:1024 lds\nQmava 76,36,152\nv_mul_f32 v103, v52, v103\nQk 96,96,97\n"
"Qmava 80,20,156\nQda 112,49,58368\nQk 97,98,99\nQmava 84,24,156\nQda 116,49,59392\ns_mul_i32 s95, s90, 11\nQmava 88,28,156\nbuffer_load_dwordx4 v72, s[52:55], s95 offen offset:2048 lds\n"
"Qmava 92,32,156\nQda 120,49,60416\nQk 98,100,101\nQmava 96,36,156\nQk 99,102,103\nv_mul_f32 v128, v53, v128\nQmava 100,20,160\nQda 124,49,61440\n"
"v_mul_f32 v129, v53, v129\nQmava 104,24,160\nbuffer_load_dwordx4 v61, s[52:55], s95 offen offset:3072 lds\nQmava 108,28,160\nQda 128,49,62464\nv_mul_f32 v130, v53, v130\nQmava 176,32,160\nv_mul_f32 v131, v53, v131\n"
"v_mul_f32 v132, v53, v132\nQmava 180,36,160\nQda 132,49,63488\ns_add_u32 m0, s91, 102400\nQmava 184,20,164\nbuffer_load_dwordx4 v62, s[52:55], s95 offen lds\nQmava 188,24,164\nQda 136,49,64512\n"
"v_mul_f32 v133, v53, v133\nQmava 192,28,164\nv_mul_f32 v134, v53, v134\nv_mul_f32 v135, v53, v135\nQmava 196,32,164\nbuffer_load_dwordx4 v63, s[52:55], s95 offen offset:1024 lds\nv_add_u32 v75, 1, v75\nQmava 200,36,164\n"
"s_waitcnt vmcnt(21)\nds_write_b32 v73, v75\ns_waitcnt lgkmcnt(5)\nQmava 0,0,112\nQtv 20,64,14336\nQk 128,128,129\nQmava 4,4,112\nbuffer_store_dwordx4 v[240:243], v60, s[56:59], s88 offen offset:1024 sc1\n"
"Qmava 8,8,112\nQtv 22,64,19456\nQk 129,130,131\nQmava 12,12,112\nQtv 24,64,14592\nQk 130,132,133\nQmava 16,16,112\nQk 131,134,135\n"
"v_mul_f32 v160, v54, v160\nQmava 20,0,116\nQtv 26,64,19712\nv_mul_f32 v161, v54, v161\nQmava 24,4,116\nQtv 28,64,14848\nv_mul_f32 v162, v54, v162\nQmava 28,8,116\n"
"ds_read_b32 v69, v74\nv_mul_f32 v163, v54, v163\nQmava 32,12,116\nv_mul_f32 v164, v54, v164\nv_mul_f32 v165, v54, v165\nQmava 36,16,116\nQtv 30,64,19968\nv_mul_f32 v166, v54, v166\n"
"Qmava 40,0,120\nQtv 32,64,15104\nv_mul_f32 v167, v54, v167\nQmava 44,4,120\ns_waitcnt lgkmcnt(2)\nv_cmp_gt_u32 vcc, v75, v69\nQmava 48,8,120\ns_cbranch_vccnz .Lso6\n"
".Lro6:\nQk 160,160,161\nQmava 52,12,120\nQtv 34,64,20224\ns_add_u32 m0, s91, 132096\ns_mul_i32 s94, s31, 12\nQmava 56,16,120\nbuffer_load_dwordx4 v70, s[48:51], s94 offen lds\n"
"Qmava 60,0,124\nQk 161,162,163\nQk 162,164,165\nQmava 64,4,124\nQtv 36,64,15360\nQk 163,166,167\nQmava 68,8,124\nQtv 38,64,20480\n"
"v_mul_f32 v192, v55, v192\nQmava 72,12,124\nbuffer_load_dwordx4 v71, s[48:51], s94 offen offset:1024 lds\nQmava 76,16,124\nv_mul_f32 v193, v55, v193\nv_mul_f32 v194, v55, v194\nQmava 80,0,128\nQda 140,48,0\n"
"v_mul_f32 v195, v55, v195\nQmava 84,4,128\nQda 144,48,1024\ns_mul_i32 s95, s90, 12\nQmava 88,8,128\nbuffer_load_dwordx4 v72, s[52:55], s95 offen offset:2048 lds\nQmava 92,12,128\nQda 148,48,2048\n"
"v_mul_f32 v196, v55, v196\nQmava 96,16,128\nv_mul_f32 v197, v55, v197\nv_mul_f32 v198, v55, v198\nQmava 100,0,132\nQda 152,48,3072\nv_mul_f32 v199, v55, v199\nQmava 104,4,132\n"
"buffer_load_dwordx4 v61, s[52:55], s95 offen offset:3072 lds\nQmava 108,8,132\nQda 156,48,4096\nQk 192,192,193\nQmava 176,12,132\nQk 193,194,195\nQk 194,196,197\nQmava 180,16,132\n"
"Qda 160,48,5120\ns_add_u32 m0, s91, 136192\nQmava 184,0,136\nbuffer_load_dwordx4 v62, s[52:55], s95 offen lds\nQmava 188,4,136\nQda 164,48,6144\nQk 195,198,199\nQmava 192,8,136\n"
"v_mul_f32 v224, v56, v224\nv_mul_f32 v225, v56, v225\nQmava 196,12,136\nbuffer_load_dwordx4 v63, s[52:55], s95 offen offset:1024 lds\nv_add_u32 v75, 1, v75\nQmava 200,16,136\ns_waitcnt vmcnt(21)\nds_write_b32 v73, v75\n"
"s_waitcnt lgkmcnt(5)\nQmava 0,20,140\nQtv 0,64,38912\nv_mul_f32 v226, v56, v226\nQmava 4,24,140\nbuffer_store_dwordx4 v[84:87], v60, s[56:59], s88 offen offset:2048 sc1\nQmava 8,28,140\nQtv 2,64,44032\n"
"v_mul_f32 v227, v56, v227\nQmava 12,32,140\nQtv 4,64,39168\nv_mul_f32 v228, v56, v228\nQmava 16,36,140\nv_mul_f32 v229, v56, v229\nv_mul_f32 v230, v56, v230\nQmava 20,20,144\n"
"Qtv 6,64,44288\nv_mul_f32 v231, v56, v231\nQmava 24,24,144\nQtv 8,64,39424\nQk 224,224,225\nQmava 28,28,144\nds_read_b32 v69, v74\nQk 225,226,227\n"
"Qmava 32,32,144\nQk 226,228,229\nQk 227,230,231\nQmava 36,36,144\nQtv 10,64,44544\nv_accvgpr_read_b32 v40, a208\nQmava 40,20,148\nQtv 12,64,39680\n"
"v_accvgpr_read_b32 v41, a209\nQmava 44,24,148\ns_waitcnt lgkmcnt(2)\nv_cmp_gt_u32 vcc, v75, v69\nQmava 48,28,148\ns_cbranch_vccnz .Lso7\n.Lro7:\nv_accvgpr_read_b32 v42, a210\n"
"Qmava 52,32,148\nQtv 14,64,44800\ns_add_u32 m0, s91, 0\ns_mul_i32 s94, s31, 13\nQmava 56,36,148\nbuffer_load_dwordx4 v70, s[48:51], s94 offen lds\nQmava 60,20,152\nv_accvgpr_read_b32 v43, a211\n"
"v_accvgpr_read_b32 v44, a212\nQmava 64,24,152\nQtv 16,64,39936\nv_accvgpr_read_b32 v45, a213\nQmava 68,28,152\nQtv 18,64,45056\nv_accvgpr_read_b32 v46, a214\nQmava 72,32,152\n"
"buffer_load_dwordx4 v71, s[48:51], s94 offen offset:1024 lds\nQmava 76,36,152\nv_accvgpr_read_b32 v47, a215\nv_mul_f32 v40, v57, v40\nQmava 80,20,156\nQda 112,48,24576\nv_mul_f32 v41, v57, v41\nQmava 84,24,156\n"
"Qda 116,48,25600\ns_mul_i32 s95, s90, 13\nQmava 88,28,156\nbuffer_load_dwordx4 v72, s[52:55], s95 offen offset:2048 lds\nQmava 92,32,156\nQda 120,48,26624\nv_mul_f32 v42, v57, v42\nQmava 96,36,156\n"
"v_mul_f32 v43, v57, v43\nv_mul_f32 v44, v57, v44\nQmava 100,20,160\nQda 124,48,27648\nv_mul_f32 v45, v57, v45\nQmava 104,24,160\nbuffer_load_dwordx4 v61, s[52:55], s95 offen offset:3072 lds\nQmava 108,28,160\n"
"Qda 128,48,28672\nv_mul_f32 v46, v57, v46\nQmava 176,32,160\nv_mul_f32 v47, v57, v47\nQk 148,40,41\nQmava 180,36,160\nQda 132,48,29696\ns_add_u32 m0, s91, 4096\n"
"Qmava 184,20,164\nbuffer_load_dwordx4 v62, s[52:55], s95 offen lds\nQmava 188,24,164\nQda 136,48,30720\nQk 149,42,43\nQmava 192,28,164\nQk 150,44,45\nQk 151,46,47\n"
"Qmava 196,32,164\nbuffer_load_dwordx4 v63, s[52:55], s95 offen offset:1024 lds\nv_add_u32 v75, 1, v75\nQmava 200,36,164\ns_waitcnt vmcnt(21)\nds_write_b32 v73, v75\ns_waitcnt lgkmcnt(5)\nQmava 0,0,112\n"
"Qtv 20,59,6144\ns_add_u32 s88, s71, 14336\nQmava 4,4,112\nbuffer_store_dwordx4 v[88:91], v60, s[56:59], s88 offen offset:0 sc1\nQmava 8,8,112\nQtv 22,59,11264\nv_accvgpr_read_b32 v40, a240\nQmava 12,12,112\n"
"Qtv 24,59,6400\nv_accvgpr_read_b32 v41, a241\nQmava 16,16,112\nv_accvgpr_read_b32 v42, a242\nv_accvgpr_read_b32 v43, a243\nQmava 20,0,116\nQtv 26,59,11520\nv_accvgpr_read_b32 v44, a244\n"
"Qmava 24,4,116\nQtv 28,59,6656\nv_accvgpr_read_b32 v45, a245\nQmava 28,8,116\nds_read_b32 v69, v74\nv_accvgpr_read_b32 v46, a246\nQmava 32,12,116\nv_accvgpr_read_b32 v47, a247\n"
"v_mul_f32 v40, v58, v40\nQmava 36,16,116\nQtv 30,59,11776\nv_mul_f32 v41, v58, v41\nQmava 40,0,120\nQtv 32,59,6912\nv_mul_f32 v42, v58, v42\nQmava 44,4,120\n"
"s_waitcnt lgkmcnt(2)\nv_cmp_gt_u32 vcc, v75, v69\nQmava 48,8,120\ns_cbranch_vccnz .Lso8\n.Lro8:\nv_mul_f32 v43, v58, v43\nQmava 52,12,120\nQtv 34,59,12032\n"
"s_add_u32 m0, s91, 24576\ns_mul_i32 s94, s31, 14\nQmava 56,16,120\nbuffer_load_dwordx4 v70, s[48:51], s94 offen lds\nQmava 60,0,124\nv_mul_f32 v44, v58, v44\nv_mul_f32 v45, v58, v45\nQmava 64,4,124\n"
"Qtv 36,59,7168\nv_mul_f32 v46, v58, v46\nQmava 68,8,124\nQtv 38,59,12288\nv_mul_f32 v47, v58, v47\nQmava 72,12,124\nbuffer_load_dwordx4 v71, s[48:51], s94 offen offset:1024 lds\nQmava 76,16,124\n"
"Qk 180,40,41\nQk 181,42,43\nQmava 80,0,128\nQda 140,48,49152\nQk 182,44,45\nQmava 84,4,128\nQda 144,48,50176\ns_mul_i32 s95, s90, 14\n"
"Qmava 88,8,128\nbuffer_load_dwordx4 v72, s[52:55], s95 offen offset:2048 lds\nQmava 92,12,128\nQda 148,48,51200\nQk 183,46,47\nQmava 96,16,128\nv_mul_f32 v104, v52, v104\nv_mul_f32 v105, v52, v105\n"
"Qmava 100,0,132\nQda 152,48,52224\nv_mul_f32 v106, v52, v106\nQmava 104,4,132\nbuffer_load_dwordx4 v61, s[52:55], s95 offen offset:3072 lds\nQmava 108,8,132\nQda 156,48,53248\nv_mul_f32 v107, v52, v107\n"
"Qmava 176,12,132\nv_mul_f32 v108, v52, v108\nv_mul_f32 v109, v52, v109\nQmava 180,16,132\nQda 160,48,54272\ns_add_u32 m0, s91, 28672\nQmava 184,0,136\nbuffer_load_dwordx4 v62, s[52:55], s95 offen lds\n"
"Qmava 188,4,136\nQda 164,48,55296\nv_mul_f32 v110, v52, v110\nQmava 192,8,136\nv_mul_f32 v111, v52, v111\nQk 104,104,105\nQmava 196,12,136\nbuffer_load_dwordx4 v63, s[52:55], s95 offen offset:1024 lds\n"
"v_add_u32 v75, 1, v75\nQmava 200,16,136\ns_waitcnt vmcnt(21)\nds_write_b32 v73, v75\ns_waitcnt lgkmcnt(5)\nQmava 0,20,140\nQtv 0,59,30720\nQk 105,106,107\n"
"Qmava 4,24,140\nbuffer_store_dwordx4 v[120:123], v60, s[56:59], s88 offen offset:1024 sc1\nQmava 8,28,140\nQtv 2,59,35840\nQk 106,108,109\nQmava 12,32,140\nQtv 4,59,30976\nQk 107,110,111\n"
"Qmava 16,36,140\nv_mul_f32 v136, v53, v136\nv_mul_f32 v137, v53, v137\nQmava 20,20,144\nQtv 6,59,36096\nv_mul_f32 v138, v53, v138\nQmava 24,24,144\nQtv 8,59,31232\n"
"v_mul_f32 v139, v53, v139\nQmava 28,28,144\nds_read_b32 v69, v74\nv_mul_f32 v140, v53, v140\nQmava 32,32,144\nv_mul_f32 v141, v53, v141\nv_mul_f32 v142, v53, v142\nQmava 36,36,144\n"
"Qtv 10,59,36352\nv_mul_f32 v143, v53, v143\nQmava 40,20,148\nQtv 12,59,31488\nQk 136,136,137\nQmava 44,24,148\ns_waitcnt lgkmcnt(2)\nv_cmp_gt_u32 vcc, v75, v69\n"
"Qmava 48,28,148\ns_cbranch_vccnz .Lso9\n.Lro9:\nQk 137,138,139\nQmava 52,32,148\nQtv 14,59,36608\ns_add_u32 m0, s91, 49152\ns_mul_i32 s94, s31, 15\n"
"Qmava 56,36,148\nbuffer_load_dwordx4 v70, s[48:51], s94 offen lds\nQmava 60,20,152\nQk 138,140,141\nQk 139,142,143\nQmava 64,24,152\nQtv 16,59,31744\nv_mul_f32 v168, v54, v168\n"
"Qmava 68,28,152\nQtv 18,59,36864\nv_mul_f32 v169, v54, v169\nQmava 72,32,152\nbuffer_load_dwordx4 v71, s[48:51], s94 offen offset:1024 lds\nQmava 76,36,152\nv_mul_f32 v170, v54, v170\nv_mul_f32 v171, v54, v171\n"
"Qmava 80,20,156\nQda 112,49,0\nv_mul_f32 v172, v54, v172\nQmava 84,24,156\nQda 116,49,1024\ns_mul_i32 s95, s90, 15\nQmava 88,28,156\nbuffer_load_dwordx4 v72, s[52:55], s95 offen offset:2048 lds\n"
"Qmava 92,32,156\nQda 120,49,2048\nv_mul_f32 v173, v54, v173\nQmava 96,36,156\nv_mul_f32 v174, v54, v174\nv_mul_f32 v175, v54, v175\nQmava 100,20,160\nQda 124,49,3072\n"
"Qk 168,168,169\nQmava 104,24,160\nbuffer_load_dwordx4 v61, s[52:55], s95 offen offset:3072 lds\nQmava 108,28,160\nQda 128,49,4096\nQk 169,170,171\nQmava 176,32,160\nQk 170,172,173\n"
"Qk 171,174,175\nQmava 180,36,160\nQda 132,49,5120\ns_add_u32 m0, s91, 53248\nQmava 184,20,164\nbuffer_load_dwordx4 v62, s[52:55], s95 offen lds\nQmava 188,24,164\nQda 136,49,6144\n"
"v_mul_f32 v200, v55, v200\nQmava 192,28,164\nv_mul_f32 v201, v55, v201\nv_mul_f32 v202, v55, v202\nQmava 196,32,164\nbuffer_load_dwordx4 v63, s[52:55], s95 offen offset:1024 lds\nv_add_u32 v75, 1, v75\nQmava 200,36,164\n"
"s_waitcnt vmcnt(21)\nds_write_b32 v73, v75\ns_waitcnt lgkmcnt(5)\nQmava 0,0,112\nQtv 20,59,55296\nv_mul_f32 v203, v55, v203\nQmava 4,4,112\nbuffer_store_dwordx4 v[152:155], v60, s[56:59], s88 offen offset:2048 sc1\n"
"Qmava 8,8,112\nQtv 22,59,60416\nv_mul_f32 v204, v55, v204\nQmava 12,12,112\nQtv 24,59,55552\nv_mul_f32 v205, v55, v205\nQmava 16,16,112\nv_mul_f32 v206, v55, v206\n"
"v_mul_f32 v207, v55, v207\nQmava 20,0,116\nQtv 26,59,60672\nQk 200,200,201\nQmava 24,4,116\nQtv 28,59,55808\nQk 201,202,203\nQmava 28,8,116\n"
"ds_read_b32 v69, v74\nQk 202,204,205\nQmava 32,12,116\nQk 203,206,207\nv_mul_f32 v232, v56, v232\nQmava 36,16,116\nQtv 30,59,60928\nv_mul_f32 v233, v56, v233\n"
"Qmava 40,0,120\nQtv 32,59,56064\nv_mul_f32 v234, v56, v234\nQmava 44,4,120\ns_waitcnt lgkmcnt(2)\nv_cmp_gt_u32 vcc, v75, v69\nQmava 48,8,120\ns_cbranch_vccnz .Lso10\n"
".Lro10:\nv_mul_f32 v235, v56, v235\nQmava 52,12,120\nQtv 34,59,61184\ns_add_u32 m0, s91, 73728\ns_mul_i32 s94, s31, 16\nQmava 56,16,120\nbuffer_load_dwordx4 v70, s[48:51], s94 offen lds\n"
"Qmava 60,0,124\nv_mul_f32 v236, v56, v236\nv_mul_f32 v237, v56, v237\nQmava 64,4,124\nQtv 36,59,56320\nv_mul_f32 v238, v56, v238\nQmava 68,8,124\nQtv 38,59,61440\n"
"v_mul_f32 v239, v56, v239\nQmava 72,12,124\nbuffer_load_dwordx4 v71, s[48:51], s94 offen offset:1024 lds\nQmava 76,16,124\nQk 232,232,233\nQk 233,234,235\nQmava 80,0,128\nQda 140,49,24576\n"
"Qk 234,236,237\nQmava 84,4,128\nQda 144,49,25600\ns_mul_i32 s95, s90, 16\nQmava 88,8,128\nbuffer_load_dwordx4 v72, s[52:55], s95 offen offset:2048 lds\nQmava 92,12,128\nQda 148,49,26624\n"
"Qk 235,238,239\nQmava 96,16,128\nv_accvgpr_read_b32 v40, a216\nv_accvgpr_read_b32 v41, a217\nQmava 100,0,132\nQda 152,49,27648\nv_accvgpr_read_b32 v42, a218\nQmava 104,4,132\n"
"buffer_load_dwordx4 v61, s[52:55], s95 offen offset:3072 lds\nQmava 108,8,132\nQda 156,49,28672\nv_accvgpr_read_b32 v43, a219\nQmava 176,12,132\nv_accvgpr_read_b32 v44, a220\nv_accvgpr_read_b32 v45, a221\nQmava 180,16,132\n"
"Qda 160,49,29696\n.Lpg15:\ns_cbranch_execz .Ltr6\ns_add_u32 m0, s91, 77824\nQmava 184,0,136\nbuffer_load_dwordx4 v62, s[52:55], s95 offen lds\nQmava 188,4,136\nQda 164,49,30720\n"
"v_accvgpr_read_b32 v46, a222\nQmava 192,8,136\nv_accvgpr_read_b32 v47, a223\nv_mul_f32 v40, v57, v40\nQmava 196,12,136\nbuffer_load_dwordx4 v63, s[52:55], s95 offen offset:1024 lds\nv_add_u32 v75, 1, v75\nQmava 200,16,136\n"
"s_waitcnt vmcnt(21)\nds_write_b32 v73, v75\ns_waitcnt lgkmcnt(5)\nQmava 0,20,140\nQtv 0,50,31744\nv_mul_f32 v41, v57, v41\nQmava 4,24,140\nbuffer_store_dwordx4 v[184:187], v60, s[56:59], s88 offen offset:3072 sc1\n"
"Qmava 8,28,140\nQtv 2,50,36864\nv_mul_f32 v42, v57, v42\nQmava 12,32,140\nQtv 4,50,32000\nv_mul_f32 v43, v57, v43\nQmava 16,36,140\nv_mul_f32 v44, v57, v44\n"
"v_mul_f32 v45, v57, v45\nQmava 20,20,144\nQtv 6,50,37120\nv_mul_f32 v46, v57, v46\nQmava 24,24,144\nQtv 8,50,32256\nv_mul_f32 v47, v57, v47\nQmava 28,28,144\n"
"ds_read_b32 v69, v74\nQk 212,40,41\nQmava 32,32,144\nQk 213,42,43\nQk 214,44,45\nQmava 36,36,144\nQtv 10,50,37376\nQk 215,46,47\n"
"Qmava 40,20,148\nQtv 12,50,32512\nv_accvgpr_read_b32 v40, a248\nQmava 44,24,148\ns_waitcnt lgkmcnt(2)\nv_cmp_gt_u32 vcc, v75, v69\nQmava 48,28,148\ns_cbranch_vccnz .Lso11\n"
".Lro11:\nv_accvgpr_read_b32 v41, a249\nQmava 52,32,148\nQtv 14,50,37632\ns_add_u32 m0, s91, 98304\ns_mul_i32 s94, s31, 17\nQmava 56,36,148\nbuffer_load_dwordx4 v70, s[48:51], s94 offen lds\n"
"Qmava 60,20,152\nv_accvgpr_read_b32 v42, a250\nv_accvgpr_read_b32 v43, a251\nQmava 64,24,152\nQtv 16,50,32768\nv_accvgpr_read_b32 v44, a252\nQmava 68,28,152\nQtv 18,50,37888\n"
"v_accvgpr_read_b32 v45, a253\nQmava 72,32,152\nbuffer_load_dwordx4 v71, s[48:51], s94 offen offset:1024 lds\nQmava 76,36,152\nv_accvgpr_read_b32 v46, a254\nv_accvgpr_read_b32 v47, a255\nQmava 80,20,156\nQda 112,49,58368\n"
"v_mul_f32 v40, v58, v40\nQmava 84,24,156\nQda 116,49,59392\ns_mul_i32 s95, s90, 17\nQmava 88,28,156\nbuffer_load_dwordx4 v72, s[52:55], s95 offen offset:2048 lds\nQmava 92,32,156\nQda 120,49,60416\n"
"v_mul_f32 v41, v58, v41\nQmava 96,36,156\nv_mul_f32 v42, v58, v42\nv_mul_f32 v43, v58, v43\nQmava 100,20,160\nQda 124,49,61440\nv_mul_f32 v44, v58, v44\nQmava 104,24,160\n"
"buffer_load_dwordx4 v61, s[52:55], s95 offen offset:3072 lds\nQmava 108,28,160\nQda 128,49,62464\nv_mul_f32 v45, v58, v45\nQmava 176,32,160\nv_mul_f32 v46, v58, v46\nv_mul_f32 v47, v58, v47\nQmava 180,36,160\n"
"Qda 132,49,63488\ns_add_u32 m0, s91, 102400\nQmava 184,20,164\nbuffer_load_dwordx4 v62, s[52:55], s95 offen lds\nQmava 188,24,164\nQda 136,49,64512\nQk 244,40,41\nQmava 192,28,164\n"
"Qk 245,42,43\nQk 246,44,45\nQmava 196,32,164\nbuffer_load_dwordx4 v63, s[52:55], s95 offen offset:1024 lds\nv_add_u32 v75, 1, v75\nQmava 200,36,164\ns_waitcnt vmcnt(21)\nds_write_b32 v73, v75\n"
"s_waitcnt lgkmcnt(5)\nQmava 0,0,112\nQtv 20,64,14336\ns_add_u32 s88, s71, 18432\nQmava 4,4,112\nbuffer_store_dwordx4 v[216:219], v60, s[56:59], s88 offen offset:0 sc1\nQmava 8,8,112\nQtv 22,64,19456\n"
"Qk 247,46,47\nQmava 12,12,112\nQtv 24,64,14592\nQmava 16,16,112\nQmava 20,0,116\nQtv 26,64,19712\nQmava 24,4,116\nQtv 28,64,14848\n"
"Qmava 28,8,116\nds_read_b32 v69, v74\nQmava 32,12,116\nQtv 30,64,19968\nQmava 36,16,116\nQmava 40,0,120\nQtv 32,64,15104\nv_add_u32 v67, 2, v65\n"
"Qmava 44,4,120\ns_waitcnt lgkmcnt(2)\nv_cmp_gt_u32 vcc, v75, v69\nQmava 48,8,120\ns_cbranch_vccnz .Lso12\n.Lro12:\nQmava 52,12,120\nbuffer_store_dword v67, v77, s[24:27], 0 offen sc1\n"
"s_add_u32 m0, s91, 132096\ns_mul_i32 s94, s31, 18\nQmava 56,16,120\nbuffer_load_dwordx4 v70, s[48:51], s94 offen lds\nQmava 60,0,124\nQtv 34,64,20224\nQmava 64,4,124\nQtv 36,64,15360\n"
"Qmava 68,8,124\nQmava 72,12,124\nbuffer_load_dwordx4 v71, s[48:51], s94 offen offset:1024 lds\nQmava 76,16,124\nQtv 38,64,20480\nQmava 80,0,128\nQda 140,48,0\nQmava 84,4,128\n"
"Qda 144,48,1024\ns_mul_i32 s95, s90, 18\nQmava 88,8,128\nbuffer_load_dwordx4 v72, s[52:55], s95 offen offset:2048 lds\nQmava 92,12,128\nQmava 96,16,128\nQda 148,48,2048\nQmava 100,0,132\n"
"Qda 152,48,3072\nQmava 104,4,132\nbuffer_load_dwordx4 v61, s[52:55], s95 offen offset:3072 lds\nQmava 108,8,132\nQda 156,48,4096\nQmava 176,12,132\nQmava 180,16,132\nQda 160,48,5120\n"
"s_add_u32 m0, s91, 136192\nQmava 184,0,136\nbuffer_load_dwordx4 v62, s[52:55], s95 offen lds\nQmava 188,4,136\nQda 164,48,6144\nQmava 192,8,136\nQmava 196,12,136\nbuffer_load_dwordx4 v63, s[52:55], s95 offen offset:1024 lds\n"
"v_add_u32 v75, 1, v75\nQmava 200,16,136\ns_waitcnt vmcnt(22)\nds_write_b32 v73, v75\ns_waitcnt lgkmcnt(5)\nQmava 0,20,140\nQtv 0,64,38912\nQmava 4,24,140\n"
"buffer_store_dwordx4 v[248:251], v60, s[56:59], s88 offen offset:1024 sc1\nQmava 8,28,140\nQtv 2,64,44032\nQmava 12,32,140\nQtv 4,64,39168\nQmava 16,36,140\nQmava 20,20,144\nQtv 6,64,44288\n"
"Qmava 24,24,144\nQtv 8,64,39424\nQmava 28,28,144\nds_read_b32 v69, v74\nQmava 32,32,144\nQmava 36,36,144\nQtv 10,64,44544\nQmava 40,20,148\n"
"Qtv 12,64,39680\nQmava 44,24,148\ns_waitcnt lgkmcnt(2)\nv_cmp_gt_u32 vcc, v75, v69\nQmava 48,28,148\ns_cbranch_vccnz .Lso13\n.Lro13:\nQmava 52,32,148\n"
"Qtv 14,64,44800\ns_add_u32 m0, s91, 0\ns_mul_i32 s94, s31, 19\nQmava 56,36,148\nbuffer_load_dwordx4 v70, s[48:51], s94 offen lds\nQmava 60,20,152\nQmava 64,24,152\nQtv 16,64,39936\n"
"Qmava 68,28,152\nQtv 18,64,45056\nQmava 72,32,152\nbuffer_load_dwordx4 v71, s[48:51], s94 offen offset:1024 lds\nQmava 76,36,152\nQmava 80,20,156\nQda 112,48,24576\nQmava 84,24,156\n"
"Qda 116,48,25600\ns_mul_i32 s95, s90, 19\nQmava 88,28,156\nbuffer_load_dwordx4 v72, s[52:55], s95 offen offset:2048 lds\nQmava 92,32,156\nQda 120,48,26624\nQmava 96,36,156\nQmava 100,20,160\n"
"Qda 124,48,27648\nQmava 104,24,160\nbuffer_load_dwordx4 v61, s[52:55], s95 offen offset:3072 lds\nQmava 108,28,160\nQda 128,48,28672\nQmava 176,32,160\nQmava 180,36,160\nQda 132,48,29696\n"
"s_add_u32 m0, s91, 4096\nQmava 184,20,164\nbuffer_load_dwordx4 v62, s[52:55], s95 offen lds\nQmava 188,24,164\nQda 136,48,30720\nQmava 192,28,164\nQmava 196,32,164\nbuffer_load_dwordx4 v63, s[52:55], s95 offen offset:1024 lds\n"
"v_add_u32 v75, 1, v75\nQmava 200,36,164\ns_waitcnt vmcnt(22)\nds_write_b32 v73, v75\ns_waitcnt lgkmcnt(5)\nQmava 0,0,112\nQtv 20,59,6144\nQmava 4,4,112\n"
"buffer_store_dwordx4 v[116:119], v60, s[56:59], s88 offen offset:2048 sc1\nQmava 8,8,112\nQtv 22,59,11264\nQmava 12,12,112\nQtv 24,59,6400\nQmava 16,16,112\nQmava 20,0,116\nQtv 26,59,11520\n"
"v_lshrrev_b32 v57, 8, v60\nQmava 24,4,116\nQtv 28,59,6656\ns_mul_i32 s68, s92, 20\nQmava 28,8,116\nds_read_b32 v69, v74\nv_accvgpr_read_b32 v92, a0\nQmava 32,12,116\n"
"v_accvgpr_read_b32 v93, a1\nv_accvgpr_read_b32 v94, a2\nQmava 36,16,116\nQtv 30,59,11776\nv_accvgpr_read_b32 v95, a3\nQmava 40,0,120\nQtv 32,59,6912\nv_accvgpr_read_b32 v124, a4\n"
"Qmava 44,4,120\ns_waitcnt lgkmcnt(2)\nv_cmp_gt_u32 vcc, v75, v69\nQmava 48,8,120\ns_cbranch_vccnz .Lso14\n.Lro14:\nv_accvgpr_read_b32 v125, a5\nQmava 52,12,120\n"
"Qtv 34,59,12032\ns_add_u32 m0, s91, 24576\ns_mul_i32 s94, s31, 20\nQmava 56,16,120\nbuffer_load_dwordx4 v70, s[48:51], s94 offen lds\nQmava 60,0,124\nv_accvgpr_read_b32 v126, a6\nv_accvgpr_read_b32 v127, a7\n"
"Qmava 64,4,124\nQtv 36,59,7168\nv_add_u32 v57, s68, v57\nQmava 68,8,124\nQtv 38,59,12288\nv_add_u32 v55, 0, v57\nQmava 72,12,124\nbuffer_load_dwordx4 v71, s[48:51], s94 offen offset:1024 lds\n"
"Qmava 76,16,124\nv_mul_u32_u24 v56, 205, v55\nv_lshrrev_b32 v56, 11, v56\nQmava 80,0,128\nQda 140,48,49152\nv_mul_u32_u24 v54, 10, v56\nQmava 84,4,128\nQda 144,48,50176\n"
"s_mul_i32 s95, s90, 20\nQmava 88,8,128\nbuffer_load_dwordx4 v72, s[52:55], s95 offen offset:2048 lds\nQmava 92,12,128\nQda 148,48,51200\nv_sub_u32 v55, v55, v54\nQmava 96,16,128\nv_lshlrev_b32 v55, 5, v55\n"
"v_bfe_u32 v54, v60, 5, 3\nQmava 100,0,132\nQda 152,48,52224\nv_lshl_add_u32 v56, v56, 3, v54\nQmava 104,4,132\nbuffer_load_dwordx4 v61, s[52:55], s95 offen offset:3072 lds\nQmava 108,8,132\nQda 156,48,53248\n"
"v_mul_u32_u24 v56, 1280, v56\nQmava 176,12,132\nv_and_b32 v54, 16, v60\nv_add3_u32 v55, v55, v56, v54\nQmava 180,16,132\nQda 160,48,54272\ns_add_u32 m0, s91, 28672\nQmava 184,0,136\n"
"buffer_load_dwordx4 v62, s[52:55], s95 offen lds\nQmava 188,4,136\nQda 164,48,55296\nv_add_u32 v40, 4096, v55\nQmava 192,8,136\nv_add_u32 v55, 4, v57\nv_mul_u32_u24 v56, 205, v55\nQmava 196,12,136\n"
"buffer_load_dwordx4 v63, s[52:55], s95 offen offset:1024 lds\nv_add_u32 v75, 1, v75\nQmava 200,16,136\ns_waitcnt vmcnt(22)\nds_write_b32 v73, v75\ns_waitcnt lgkmcnt(5)\nQmvva 92,20,140\nQtv 0,59,30720\n"
"s_add_u32 s88, s71, 28672\nQmvva 124,24,140\nbuffer_store_dwordx4 v[96:99], v60, s[56:59], s88 offen offset:0 sc1\nQmava 8,28,140\nQtv 2,59,35840\nv_lshrrev_b32 v56, 11, v56\nQmava 12,32,140\nQtv 4,59,30976\n"
"v_mul_u32_u24 v54, 10, v56\nQmava 16,36,140\nv_sub_u32 v55, v55, v54\nv_lshlrev_b32 v55, 5, v55\nQmava 20,20,144\nQtv 6,59,36096\nv_bfe_u32 v54, v60, 5, 3\nQmava 24,24,144\n"
"Qtv 8,59,31232\nv_lshl_add_u32 v56, v56, 3, v54\nQmava 28,28,144\nds_read_b32 v69, v74\nv_mul_u32_u24 v56, 1280, v56\nQmava 32,32,144\nv_and_b32 v54, 16, v60\nv_add3_u32 v55, v55, v56, v54\n"
"Qmava 36,36,144\nQtv 10,59,36352\nv_accvgpr_read_b32 v156, a8\nQmava 40,20,148\nQtv 12,59,31488\nv_accvgpr_read_b32 v157, a9\nQmava 44,24,148\ns_waitcnt lgkmcnt(2)\n"
"v_cmp_gt_u32 vcc, v75, v69\nQmava 48,28,148\ns_cbranch_vccnz .Lso15\n.Lro15:\nv_accvgpr_read_b32 v158, a10\nQmava 52,32,148\nQtv 14,59,36608\ns_add_u32 m0, s91, 49152\n"
"s_mul_i32 s94, s31, 21\nQmava 56,36,148\nbuffer_load_dwordx4 v70, s[48:51], s94 offen lds\nQmava 60,20,152\nv_accvgpr_read_b32 v159, a11\nv_accvgpr_read_b32 v188, a12\nQmava 64,24,152\nQtv 16,59,31744\n"
"v_accvgpr_read_b32 v189, a13\nQmava 68,28,152\nQtv 18,59,36864\nv_accvgpr_read_b32 v190, a14\nQmava 72,32,152\nbuffer_load_dwordx4 v71, s[48:51], s94 offen offset:1024 lds\nQmava 76,36,152\nv_accvgpr_read_b32 v191, a15\n"
"v_add_u32 v41, 3072, v55\nQmava 80,20,156\nQda 112,49,0\nv_add_u32 v55, 8, v57\nQmava 84,24,156\nQda 116,49,1024\ns_mul_i32 s95, s90, 21\nQmava 88,28,156\n"
"buffer_load_dwordx4 v72, s[52:55], s95 offen offset:2048 lds\nQmava 92,32,156\nQda 120,49,2048\nv_mul_u32_u24 v56, 205, v55\nQmava 96,36,156\nv_lshrrev_b32 v56, 11, v56\nv_mul_u32_u24 v54, 10, v56\nQmava 100,20,160\n"
"Qda 124,49,3072\nv_sub_u32 v55, v55, v54\nQmava 104,24,160\nbuffer_load_dwordx4 v61, s[52:55], s95 offen offset:3072 lds\nQmava 108,28,160\nQda 128,49,4096\nv_lshlrev_b32 v55, 5, v55\nQmava 176,32,160\n"
"v_bfe_u32 v54, v60, 5, 3\nv_lshl_add_u32 v56, v56, 3, v54\nQmava 180,36,160\nQda 132,49,5120\ns_add_u32 m0, s91, 53248\nQmava 184,20,164\nbuffer_load_dwordx4 v62, s[52:55], s95 offen lds\nQmava 188,24,164\n"
"Qda 136,49,6144\nv_mul_u32_u24 v56, 1280, v56\nQmava 192,28,164\nv_and_b32 v54, 16, v60\nv_add3_u32 v55, v55, v56, v54\nQmava 196,32,164\nbuffer_load_dwordx4 v63, s[52:55], s95 offen offset:1024 lds\nv_add_u32 v75, 1, v75\n"
"Qmava 200,36,164\ns_waitcnt vmcnt(21)\nds_write_b32 v73, v75\ns_waitcnt lgkmcnt(5)\nQmvva 92,0,112\nQtv 20,59,55296\nv_add_u32 v42, 2048, v55\nQmvva 124,4,112\n"
"buffer_store_dwordx4 v[128:131], v60, s[56:59], s88 offen offset:1024 sc1\nQmvva 156,8,112\nQtv 22,59,60416\nv_add_u32 v55, 12, v57\nQmvva 188,12,112\nQtv 24,59,55552\nv_mul_u32_u24 v56, 205, v55\nQmava 16,16,112\n"
"v_lshrrev_b32 v56, 11, v56\nv_mul_u32_u24 v54, 10, v56\nQmava 20,0,116\nQtv 26,59,60672\nv_sub_u32 v55, v55, v54\nQmava 24,4,116\nQtv 28,59,55808\nv_lshlrev_b32 v55, 5, v55\n"
"Qmava 28,8,116\nds_read_b32 v69, v74\nv_bfe_u32 v54, v60, 5, 3\nQmava 32,12,116\nv_lshl_add_u32 v56, v56, 3, v54\nv_mul_u32_u24 v56, 1280, v56\nQmava 36,16,116\nQtv 30,59,60928\n"
"v_and_b32 v54, 16, v60\nQmava 40,0,120\nQtv 32,59,56064\nv_add3_u32 v55, v55, v56, v54\nQmava 44,4,120\ns_waitcnt lgkmcnt(2)\nv_cmp_gt_u32 vcc, v75, v69\nQmava 48,8,120\n"
"s_cbranch_vccnz .Lso16\n.Lro16:\nv_accvgpr_read_b32 v220, a16\nQmava 52,12,120\nQtv 34,59,61184\ns_add_u32 m0, s91, 73728\ns_mul_i32 s94, s31, 22\nQmava 56,16,120\n"
"buffer_load_dwordx4 v70, s[48:51], s94 offen lds\nQmava 60,0,124\nv_accvgpr_read_b32 v221, a17\nv_accvgpr_read_b32 v222, a18\nQmava 64,4,124\nQtv 36,59,56320\nv_accvgpr_read_b32 v223, a19\nQmava 68,8,124\n"
"Qtv 38,59,61440\nv_accvgpr_read_b32 v252, a20\nQmava 72,12,124\nbuffer_load_dwordx4 v71, s[48:51], s94 offen offset:1024 lds\nQmava 76,16,124\nv_accvgpr_read_b32 v253, a21\nv_accvgpr_read_b32 v254, a22\nQmava 80,0,128\n"
"Qda 140,49,24576\nv_accvgpr_read_b32 v255, a23\nQmava 84,4,128\nQda 144,49,25600\ns_mul_i32 s95, s90, 22\nQmava 88,8,128\nbuffer_load_dwordx4 v72, s[52:55], s95 offen offset:2048 lds\nQmava 92,12,128\n"
"Qda 148,49,26624\nv_add_u32 v43, 1024, v55\nQmava 96,16,128\nv_add_u32 v55, 16, v57\nv_mul_u32_u24 v56, 205, v55\nQmava 100,0,132\nQda 152,49,27648\nv_lshrrev_b32 v56, 11, v56\n"
"Qmava 104,4,132\nbuffer_load_dwordx4 v61, s[52:55], s95 offen offset:3072 lds\nQmava 108,8,132\nQda 156,49,28672\nv_mul_u32_u24 v54, 10, v56\nQmava 176,12,132\nv_sub_u32 v55, v55, v54\nv_lshlrev_b32 v55, 5, v55\n"
"Qmava 180,16,132\nQda 160,49,29696\ns_add_u32 m0, s91, 77824\nQmava 184,0,136\nbuffer_load_dwordx4 v62, s[52:55], s95 offen lds\nQmava 188,4,136\nQda 164,49,30720\nv_bfe_u32 v54, v60, 5, 3\n"
"Qmava 192,8,136\nv_lshl_add_u32 v56, v56, 3, v54\nv_mul_u32_u24 v56, 1280, v56\nQmava 196,12,136\nbuffer_load_dwordx4 v63, s[52:55], s95 offen offset:1024 lds\nv_add_u32 v75, 1, v75\nQmava 200,16,136\ns_waitcnt vmcnt(21)\n"
"ds_write_b32 v73, v75\ns_waitcnt lgkmcnt(5)\nQmvva 92,20,140\n.Lpg16:\ns_cbranch_execz .Lpg20\nQtv 0,50,31744\nv_and_b32 v54, 16, v60\nQmvva 124,24,140\n"
"buffer_store_dwordx4 v[160:163], v60, s[56:59], s88 offen offset:2048 sc1\nQmvva 156,28,140\nQtv 2,50,36864\nv_add3_u32 v55, v55, v56, v54\nQmvva 188,32,140\nQtv 4,50,32000\nv_add_u32 v44, 4096, v55\nQmvva 220,36,140\n"
"s_mul_i32 s96, s70, 5120\ns_add_u32 s96, s96, %10\nQmvva 252,20,144\nQtv 6,50,37120\ns_mul_i32 s68, s93, 10240\nQmava 24,24,144\nQtv 8,50,32256\ns_add_u32 s68, s68, 4096\n"
"Qmava 28,28,144\nds_read_b32 v69, v74\nv_add_u32 v45, s68, v64\nQmava 32,32,144\nv_add_u32 v46, 61440, v45\ns_sub_u32 s68, 18432, s68\nQmava 36,36,144\nQtv 10,50,37376\n"
"v_add_u32 v47, s68, v64\nQmava 40,20,148\nQtv 12,50,32512\nv_add_u32 v51, 61440, v47\nQmava 44,24,148\ns_waitcnt lgkmcnt(2)\nv_cmp_gt_u32 vcc, v75, v69\nQmava 48,28,148\n"
"s_cbranch_vccnz .Lso17\n.Lro17:\ns_lshl_b32 s68, s70, 10\nQmava 52,32,148\nQtv 14,50,37632\ns_add_u32 m0, s91, 98304\ns_mul_i32 s94, s31, 23\nQmava 56,36,148\n"
"buffer_load_dwordx4 v70, s[48:51], s94 offen lds\nQmava 60,20,152\nv_accvgpr_read_b32 v100, a24\nv_accvgpr_read_b32 v101, a25\nQmava 64,24,152\nQtv 16,50,32768\nv_accvgpr_read_b32 v102, a26\nQmava 68,28,152\n"
"Qtv 18,50,37888\nv_accvgpr_read_b32 v103, a27\nQmava 72,32,152\nbuffer_load_dwordx4 v71, s[48:51], s94 offen offset:1024 lds\nQmava 76,36,152\nv_accvgpr_read_b32 v132, a28\nv_accvgpr_read_b32 v133, a29\nQmava 80,20,156\n"
"Qda 112,49,58368\nv_accvgpr_read_b32 v134, a30\nQmava 84,24,156\nQda 116,49,59392\ns_mul_i32 s95, s90, 23\nQmava 88,28,156\nbuffer_load_dwordx4 v72, s[52:55], s95 offen offset:2048 lds\nQmava 92,32,156\n"
"Qda 120,49,60416\nv_accvgpr_read_b32 v135, a31\nQmava 96,36,156\ns_add_u32 s69, %10, 106496\nv_add_u32 v54, s69, v60\nQmava 100,20,160\nQda 124,49,61440\nv_add_u32 v52, s68, v54\n"
"Qmava 104,24,160\nbuffer_load_dwordx4 v61, s[52:55], s95 offen offset:3072 lds\nQmava 108,28,160\nQda 128,49,62464\ns_xor_b32 s68, s68, 2048\nQmava 176,32,160\nv_add_u32 v53, s68, v54\ns_mul_i32 s68, s93, 163840\n"
"Qmava 180,36,160\nQda 132,49,63488\ns_add_u32 m0, s91, 102400\nQmava 184,20,164\nbuffer_load_dwordx4 v62, s[52:55], s95 offen lds\nQmava 188,24,164\nQda 136,49,64512\nQmava 192,28,164\n"
"s_add_u32 s62, s62, s68\ns_addc_u32 s63, s63, 0\nQmava 196,32,164\nbuffer_load_dwordx4 v63, s[52:55], s95 offen offset:1024 lds\nv_add_u32 v75, 1, v75\nQmava 200,36,164\ns_waitcnt vmcnt(21)\nds_write_b32 v73, v75\n"
"s_waitcnt lgkmcnt(5)\nQmvva 92,0,112\nQtv 20,64,14336\ns_sub_u32 s86, s86, s68\nQmvva 124,4,112\nbuffer_store_dwordx4 v[192:195], v60, s[56:59], s88 offen offset:3072 sc1\nQmvva 156,8,112\nQtv 22,64,19456\n"
"Qmvva 188,12,112\nQtv 24,64,14592\nQmvva 220,16,112\nQmvva 252,0,116\nQtv 26,64,19712\nQmvva 100,4,116\nQtv 28,64,14848\nQmvva 132,8,116\n"
"ds_read_b32 v69, v74\nQmava 32,12,116\nQmava 36,16,116\nQtv 30,64,19968\nQmava 40,0,120\nQtv 32,64,15104\nQmava 44,4,120\ns_waitcnt lgkmcnt(2)\n"
"v_cmp_gt_u32 vcc, v75, v69\nQmava 48,8,120\ns_cbranch_vccnz .Lso18\n.Lro18:\nQmava 52,12,120\nQtv 34,64,20224\ns_add_u32 m0, s91, 132096\ns_mul_i32 s94, s31, 24\n"
"Qmava 56,16,120\nbuffer_load_dwordx4 v70, s[48:51], s94 offen lds\nQmava 60,0,124\nv_accvgpr_read_b32 v164, a32\nv_accvgpr_read_b32 v165, a33\nQmava 64,4,124\nQtv 36,64,15360\nv_accvgpr_read_b32 v166, a34\n"
"Qmava 68,8,124\nQtv 38,64,20480\nv_accvgpr_read_b32 v167, a35\nQmava 72,12,124\nbuffer_load_dwordx4 v71, s[48:51], s94 offen offset:1024 lds\nQmava 76,16,124\nv_accvgpr_read_b32 v196, a36\nv_accvgpr_read_b32 v197, a37\n"
"Qmava 80,0,128\nQda 140,48,0\nv_accvgpr_read_b32 v198, a38\nQmava 84,4,128\nQda 144,48,1024\ns_mul_i32 s95, s90, 24\nQmava 88,8,128\nbuffer_load_dwordx4 v72, s[52:55], s95 offen offset:2048 lds\n"
"Qmava 92,12,128\nQda 148,48,2048\nv_accvgpr_read_b32 v199, a39\nQmava 96,16,128\nQmava 100,0,132\nQda 152,48,3072\nQmava 104,4,132\nbuffer_load_dwordx4 v61, s[52:55], s95 offen offset:3072 lds\n"
"Qmava 108,8,132\nQda 156,48,4096\nQmava 176,12,132\nQmava 180,16,132\nQda 160,48,5120\ns_add_u32 m0, s91, 136192\nQmava 184,0,136\nbuffer_load_dwordx4 v62, s[52:55], s95 offen lds\n"
"Qmava 188,4,136\nQda 164,48,6144\nQmava 192,8,136\nQmava 196,12,136\nbuffer_load_dwordx4 v63, s[52:55], s95 offen offset:1024 lds\nv_add_u32 v75, 1, v75\nQmava 200,16,136\ns_waitcnt vmcnt(21)\n"
"ds_write_b32 v73, v75\ns_waitcnt lgkmcnt(5)\nQmvva 92,20,140\nQtv 0,64,38912\ns_add_u32 s88, s71, 32768\nQmvva 124,24,140\nbuffer_store_dwordx4 v[224:227], v60, s[56:59], s88 offen offset:0 sc1\nQmvva 156,28,140\n"
"Qtv 2,64,44032\nQmvva 188,32,140\nQtv 4,64,39168\nQmvva 220,36,140\nQmvva 252,20,144\nQtv 6,64,44288\nQmvva 100,24,144\nQtv 8,64,39424\n"
"Qmvva 132,28,144\nds_read_b32 v69, v74\nQmvva 164,32,144\nQtv 10,64,44544\nQmvva 196,36,144\nQmava 40,20,148\nQtv 12,64,39680\nv_add_u32 v67, 3, v65\n"
"Qmava 44,24,148\ns_waitcnt lgkmcnt(2)\nv_cmp_gt_u32 vcc, v75, v69\nQmava 48,28,148\ns_cbranch_vccnz .Lso19\n.Lro19:\nQmava 52,32,148\nbuffer_store_dword v67, v77, s[24:27], 0 offen sc1\n"
"s_add_u32 m0, s91, 0\ns_mul_i32 s94, s31, 25\nQmava 56,36,148\nbuffer_load_dwordx4 v70, s[48:51], s94 offen lds\nQmava 60,20,152\nQtv 14,64,44800\nQmava 64,24,152\nQtv 16,64,39936\n"
"Qmava 68,28,152\nv_accvgpr_read_b32 v228, a40\nv_accvgpr_read_b32 v229, a41\nQmava 72,32,152\nbuffer_load_dwordx4 v71, s[48:51], s94 offen offset:1024 lds\nQmava 76,36,152\nQtv 18,64,45056\nv_accvgpr_read_b32 v230, a42\n"
"Qmava 80,20,156\nQda 112,48,24576\nv_accvgpr_read_b32 v231, a43\nQmava 84,24,156\nQda 116,48,25600\ns_mul_i32 s95, s90, 25\nQmava 88,28,156\nbuffer_load_dwordx4 v72, s[52:55], s95 offen offset:2048 lds\n"
"Qmava 92,32,156\nv_accvgpr_read_b32 v108, a44\nv_accvgpr_read_b32 v109, a45\nQmava 96,36,156\nQda 120,48,26624\nv_accvgpr_read_b32 v110, a46\nQmava 100,20,160\nQda 124,48,27648\n"
"v_accvgpr_read_b32 v111, a47\nQmava 104,24,160\nbuffer_load_dwordx4 v61, s[52:55], s95 offen offset:3072 lds\nQmava 108,28,160\nQda 128,48,28672\nQmava 176,32,160\nQmava 180,36,160\nQda 132,48,29696\n"
"s_add_u32 m0, s91, 4096\nQmava 184,20,164\nbuffer_load_dwordx4 v62, s[52:55], s95 offen lds\nQmava 188,24,164\nQda 136,48,30720\nQmava 192,28,164\nQmava 196,32,164\nbuffer_load_dwordx4 v63, s[52:55], s95 offen offset:1024 lds\n"
"v_add_u32 v75, 1, v75\nQmava 200,36,164\ns_waitcnt vmcnt(22)\nds_write_b32 v73, v75\ns_waitcnt lgkmcnt(5)\nQmvva 92,0,112\nQtv 20,59,6144\nQmvva 124,4,112\n"
"buffer_store_dwordx4 v[148:151], v60, s[56:59], s88 offen offset:1024 sc1\nQmvva 156,8,112\nQtv 22,59,11264\nQmvva 188,12,112\nQtv 24,59,6400\nQmvva 220,16,112\nQmvva 252,0,116\nQtv 26,59,11520\n"
"Qmvva 100,4,116\nQtv 28,59,6656\nQmvva 132,8,116\nds_read_b32 v69, v74\nQmvva 164,12,116\nQmvva 196,16,116\nQtv 30,59,11776\nQmvva 228,0,120\n"
"Qtv 32,59,6912\nQmvva 108,4,120\ns_waitcnt lgkmcnt(2)\nv_cmp_gt_u32 vcc, v75, v69\nQmava 48,8,120\ns_cbranch_vccnz .Lso20\n.Lro20:\nQmava 52,12,120\n"
"Qtv 34,59,12032\ns_add_u32 m0, s91, 24576\ns_mul_i32 s94, s31, 26\nQmava 56,16,120\nbuffer_load_dwordx4 v70, s[48:51], s94 offen lds\nQmava 60,0,124\nQmava 64,4,124\nQtv 36,59,7168\n"
"Qmava 68,8,124\nQtv 38,59,12288\nQmava 72,12,124\nbuffer_load_dwordx4 v71, s[48:51], s94 offen offset:1024 lds\nQmava 76,16,124\nv_accvgpr_read_b32 v140, a48\nv_accvgpr_read_b32 v141, a49\nQmava 80,0,128\n"
"Qda 140,48,49152\nv_accvgpr_read_b32 v142, a50\nQmava 84,4,128\nQda 144,48,50176\ns_mul_i32 s95, s90, 26\nQmava 88,8,128\nbuffer_load_dwordx4 v72, s[52:55], s95 offen offset:2048 lds\nQmava 92,12,128\n"
"Qda 148,48,51200\nv_accvgpr_read_b32 v143, a51\nQmava 96,16,128\nv_accvgpr_read_b32 v172, a52\nv_accvgpr_read_b32 v173, a53\nQmava 100,0,132\nQda 152,48,52224\nv_accvgpr_read_b32 v174, a54\n"
"Qmava 104,4,132\nbuffer_load_dwordx4 v61, s[52:55], s95 offen offset:3072 lds\nQmava 108,8,132\nQda 156,48,53248\nv_accvgpr_read_b32 v175, a55\nQmava 176,12,132\nQmava 180,16,132\nQda 160,48,54272\n"
"s_add_u32 m0, s91, 28672\nQmava 184,0,136\nbuffer_load_dwordx4 v62, s[52:55], s95 offen lds\nQmava 188,4,136\nQda 164,48,55296\nQmava 192,8,136\nQmava 196,12,136\nbuffer_load_dwordx4 v63, s[52:55], s95 offen offset:1024 lds\n"
"v_add_u32 v75, 1, v75\nQmava 200,16,136\ns_waitcnt vmcnt(22)\nds_write_b32 v73, v75\ns_waitcnt lgkmcnt(5)\nQmvva 92,20,140\nQtv 0,59,30720\nQmvva 124,24,140\n"
"buffer_store_dwordx4 v[180:183], v60, s[56:59], s88 offen offset:2048 sc1\nQmvva 156,28,140\nQtv 2,59,35840\nQmvva 188,32,140\nQtv 4,59,30976\nQmvva 220,36,140\nQmvva 252,20,144\nQtv 6,59,36096\n"
"Qmvva 100,24,144\nQtv 8,59,31232\nQmvva 132,28,144\nds_read_b32 v69, v74\nQmvva 164,32,144\nQmvva 196,36,144\nQtv 10,59,36352\nQmvva 228,20,148\n"
"Qtv 12,59,31488\nQmvva 108,24,148\ns_waitcnt lgkmcnt(2)\nv_cmp_gt_u32 vcc, v75, v69\nQmvva 140,28,148\ns_cbranch_vccnz .Lso21\n.Lro21:\nQmvva 172,32,148\n"
"Qtv 14,59,36608\ns_add_u32 m0, s91, 49152\ns_mul_i32 s94, s31, 27\nQmava 56,36,148\nbuffer_load_dwordx4 v70, s[48:51], s94 offen lds\nQmava 60,20,152\nQmava 64,24,152\nQtv 16,59,31744\n"
"Qmava 68,28,152\nQtv 18,59,36864\nQmava 72,32,152\nbuffer_load_dwordx4 v71, s[48:51], s94 offen offset:1024 lds\nQmava 76,36,152\nQmava 80,20,156\nQda 112,49,0\nQmava 84,24,156\n"
"Qda 116,49,1024\ns_mul_i32 s95, s90, 27\nQmava 88,28,156\nbuffer_load_dwordx4 v72, s[52:55], s95 offen offset:2048 lds\nQmava 92,32,156\nQda 120,49,2048\nv_accvgpr_read_b32 v204, a56\nQmava 96,36,156\n"
"v_accvgpr_read_b32 v205, a57\nv_accvgpr_read_b32 v206, a58\nQmava 100,20,160\nQda 124,49,3072\nv_accvgpr_read_b32 v207, a59\nQmava 104,24,160\nbuffer_load_dwordx4 v61, s[52:55], s95 offen offset:3072 lds\nQmava 108,28,160\n"
"Qda 128,49,4096\nv_accvgpr_read_b32 v236, a60\nQmava 176,32,160\nv_accvgpr_read_b32 v237, a61\nv_accvgpr_read_b32 v238, a62\nQmava 180,36,160\nQda 132,49,5120\ns_add_u32 m0, s91, 53248\n"
"Qmava 184,20,164\nbuffer_load_dwordx4 v62, s[52:55], s95 offen lds\nQmava 188,24,164\nQda 136,49,6144\nv_accvgpr_read_b32 v239, a63\nQmava 192,28,164\nQmava 196,32,164\nbuffer_load_dwordx4 v63, s[52:55], s95 offen offset:1024 lds\n"
"v_add_u32 v75, 1, v75\nQmava 200,36,164\ns_waitcnt vmcnt(22)\nds_write_b32 v73, v75\ns_waitcnt lgkmcnt(5)\nQmvva 92,0,112\nQtv 20,59,55296\ns_add_u32 s88, s71, 43008\n"
"Qmvva 124,4,112\nbuffer_store_dwordx4 v[104:107], v60, s[56:59], s88 offen offset:0 sc1\nQmvva 156,8,112\nQtv 22,59,60416\nQmvva 188,12,112\nQtv 24,59,55552\nQmvva 220,16,112\nQmvva 252,0,116\n"
"Qtv 26,59,60672\nQmvva 100,4,116\nQtv 28,59,55808\nQmvva 132,8,116\nds_read_b32 v69, v74\nQmvva 164,12,116\nQmvva 196,16,116\nQtv 30,59,60928\n"
"Qmvva 228,0,120\nQtv 32,59,56064\nQmvva 108,4,120\ns_waitcnt lgkmcnt(2)\nv_cmp_gt_u32 vcc, v75, v69\nQmvva 140,8,120\ns_cbranch_vccnz .Lso22\n.Lro22:\n"
"Qmvva 172,12,120\nQtv 34,59,61184\ns_add_u32 m0, s91, 73728\ns_mul_i32 s94, s31, 28\nQmvva 204,16,120\nbuffer_load_dwordx4 v70, s[48:51], s94 offen lds\nQmvva 236,0,124\nQmava 64,4,124\n"
"Qtv 36,59,56320\nQmava 68,8,124\nQtv 38,59,61440\nQmava 72,12,124\nbuffer_load_dwordx4 v71, s[48:51], s94 offen offset:1024 lds\nQmava 76,16,124\nQmava 80,0,128\nQda 140,49,24576\n"
"Qmava 84,4,128\nQda 144,49,25600\ns_mul_i32 s95, s90, 28\nQmava 88,8,128\nbuffer_load_dwordx4 v72, s[52:55], s95 offen offset:2048 lds\nQmava 92,12,128\nQda 148,49,26624\nv_accvgpr_read_b32 v80, a64\n"
"Qmava 96,16,128\nv_accvgpr_read_b32 v81, a65\nv_accvgpr_read_b32 v82, a66\nQmava 100,0,132\nQda 152,49,27648\nv_accvgpr_read_b32 v83, a67\nQmava 104,4,132\nbuffer_load_dwordx4 v61, s[52:55], s95 offen offset:3072 lds\n"
"Qmava 108,8,132\nQda 156,49,28672\nv_accvgpr_read_b32 v112, a68\nQmava 176,12,132\nv_accvgpr_read_b32 v113, a69\nv_accvgpr_read_b32 v114, a70\nQmava 180,16,132\nQda 160,49,29696\n"
"s_add_u32 m0, s91, 77824\nQmava 184,0,136\nbuffer_load_dwordx4 v62, s[52:55], s95 offen lds\nQmava 188,4,136\nQda 164,49,30720\nv_accvgpr_read_b32 v115, a71\nQmava 192,8,136\nQmava 196,12,136\n"
"buffer_load_dwordx4 v63, s[52:55], s95 offen offset:1024 lds\nv_add_u32 v75, 1, v75\nQmava 200,16,136\ns_waitcnt vmcnt(21)\nds_write_b32 v73, v75\ns_waitcnt lgkmcnt(5)\nQmvva 92,20,140\nbuffer_load_dword v78, v76, s[24:27], 0 offen sc1\n"
"Qmvva 124,24,140\nbuffer_store_dwordx4 v[136:139], v60, s[56:59], s88 offen offset:1024 sc1\nQmvva 156,28,140\nQtv 0,50,31744\nQmvva 188,32,140\nQtv 2,50,36864\nQmvva 220,36,140\nQtv 4,50,32000\n"
"Qmvva 252,20,144\nQmvva 100,24,144\nQtv 6,50,37120\nQmvva 132,28,144\nds_read_b32 v69, v74\nQmvva 164,32,144\nQtv 8,50,32256\nQmvva 196,36,144\n"
"Qtv 10,50,37376\nQmvva 228,20,148\nQmvva 108,24,148\ns_waitcnt lgkmcnt(2)\nv_cmp_gt_u32 vcc, v75, v69\nQmvva 140,28,148\ns_cbranch_vccnz .Lso23\n.Lro23:\n"
"Qmvva 172,32,148\nQtv 12,50,32512\ns_add_u32 m0, s91, 98304\ns_mul_i32 s94, s31, 29\nQmvva 204,36,148\nbuffer_load_dwordx4 v70, s[48:51], s94 offen lds\nQmvva 236,20,152\nQtv 14,50,37632\n"
"Qmvva 80,24,152\nQtv 16,50,32768\nQmvva 112,28,152\nQmava 72,32,152\nbuffer_load_dwordx4 v71, s[48:51], s94 offen offset:1024 lds\nQmava 76,36,152\nQtv 18,50,37888\nQmava 80,20,156\n"
"Qda 112,49,58368\nQmava 84,24,156\nQda 116,49,59392\ns_mul_i32 s95, s90, 29\nQmava 88,28,156\n.Lpg17:\ns_cbranch_execz .Lpg21\nbuffer_load_dwordx4 v72, s[52:55], s95 offen offset:2048 lds\n"
"Qmava 92,32,156\nQmava 96,36,156\nQda 120,49,60416\nQmava 100,20,160\nQda 124,49,61440\nv_accvgpr_read_b32 v144, a72\nQmava 104,24,160\nbuffer_load_dwordx4 v61, s[52:55], s95 offen offset:3072 lds\n"
"Qmava 108,28,160\nQda 128,49,62464\nv_accvgpr_read_b32 v145, a73\nQmava 176,32,160\nv_accvgpr_read_b32 v146, a74\nv_accvgpr_read_b32 v147, a75\nQmava 180,36,160\nQda 132,49,63488\n"
"s_add_u32 m0, s91, 102400\nQmava 184,20,164\nbuffer_load_dwordx4 v62, s[52:55], s95 offen lds\nQmava 188,24,164\nQda 136,49,64512\nv_accvgpr_read_b32 v176, a76\nQmava 192,28,164\nv_accvgpr_read_b32 v177, a77\n"
"v_accvgpr_read_b32 v178, a78\nQmava 196,32,164\nbuffer_load_dwordx4 v63, s[52:55], s95 offen offset:1024 lds\nv_add_u32 v75, 1, v75\nQmava 200,36,164\ns_waitcnt vmcnt(22)\nds_write_b32 v73, v75\ns_waitcnt lgkmcnt(5)\n"
"Qmvva 92,0,112\nQtv 20,64,14336\nv_accvgpr_read_b32 v179, a79\nQmvva 124,4,112\nbuffer_store_dwordx4 v[168:171], v60, s[56:59], s88 offen offset:2048 sc1\nQmvva 156,8,112\nQtv 22,64,19456\nQmvva 188,12,112\n"
"Qtv 24,64,14592\nQmvva 220,16,112\nQmvva 252,0,116\nQtv 26,64,19712\nQmvva 100,4,116\nQtv 28,64,14848\nQmvva 132,8,116\nds_read_b32 v69, v74\n"
"Qmvva 164,12,116\nQmvva 196,16,116\nQtv 30,64,19968\nQmvva 228,0,120\nQtv 32,64,15104\nQmvva 108,4,120\ns_waitcnt lgkmcnt(2)\nv_cmp_gt_u32 vcc, v75, v69\n"
"Qmvva 140,8,120\ns_cbranch_vccnz .Lso24\n.Lro24:\nQmvva 172,12,120\nQtv 34,64,20224\ns_add_u32 m0, s91, 132096\ns_mul_i32 s94, s31, 30\nQmvva 204,16,120\n"
"buffer_load_dwordx4 v70, s[48:51], s94 offen lds\nQmvva 236,0,124\nQmvva 80,4,124\nQtv 36,64,15360\nQmvva 112,8,124\nQtv 38,64,20480\nQmvva 144,12,124\nbuffer_load_dwordx4 v71, s[48:51], s94 offen offset:1024 lds\n"
"Qmvva 176,16,124\nQmava 80,0,128\nQda 140,48,0\nQmava 84,4,128\nQda 144,48,1024\ns_mul_i32 s95, s90, 30\nQmava 88,8,128\nbuffer_load_dwordx4 v72, s[52:55], s95 offen offset:2048 lds\n"
"Qmava 92,12,128\nQda 148,48,2048\nQmava 96,16,128\nQmava 100,0,132\nQda 152,48,3072\nQmava 104,4,132\nbuffer_load_dwordx4 v61, s[52:55], s95 offen offset:3072 lds\nQmava 108,8,132\n"
"Qda 156,48,4096\nv_accvgpr_read_b32 v208, a80\nQmava 176,12,132\nv_accvgpr_read_b32 v209, a81\nv_accvgpr_read_b32 v210, a82\nQmava 180,16,132\nQda 160,48,5120\ns_add_u32 m0, s91, 136192\n"
"Qmava 184,0,136\nbuffer_load_dwordx4 v62, s[52:55], s95 offen lds\nQmava 188,4,136\nQda 164,48,6144\nv_accvgpr_read_b32 v211, a83\nQmava 192,8,136\nv_accvgpr_read_b32 v240, a84\nv_accvgpr_read_b32 v241, a85\n"
"Qmava 196,12,136\nbuffer_load_dwordx4 v63, s[52:55], s95 offen offset:1024 lds\nv_add_u32 v75, 1, v75\nQmava 200,16,136\ns_waitcnt vmcnt(22)\nds_write_b32 v73, v75\ns_waitcnt lgkmcnt(5)\nQmvva 92,20,140\n"
"s_waitcnt vmcnt(14)\nv_sub_u32 v66, v78, v65\nv_cmp_gt_u32 vcc, 2, v66\ns_and_b64 vcc, vcc, s[28:29]\ns_cbranch_scc1 .Lf2_w\n.Lf2_r:\nQmvva 124,24,140\nbuffer_store_dwordx4 v[200:203], v60, s[56:59], s88 offen offset:3072 sc1\n"
"Qmvva 156,28,140\nQtv 0,64,38912\nv_accvgpr_read_b32 v242, a86\nQmvva 188,32,140\nQtv 2,64,44032\nv_accvgpr_read_b32 v243, a87\nQmvva 220,36,140\nQtv 4,64,39168\n"
"Qmvva 252,20,144\nQmvva 100,24,144\nQtv 6,64,44288\nQmvva 132,28,144\nds_read_b32 v69, v74\nQmvva 164,32,144\nQtv 8,64,39424\nQmvva 196,36,144\n"
"Qtv 10,64,44544\nQmvva 228,20,148\nQmvva 108,24,148\ns_waitcnt lgkmcnt(2)\nv_cmp_gt_u32 vcc, v75, v69\nQmvva 140,28,148\ns_cbranch_vccnz .Lso25\n.Lro25:\n"
"Qmvva 172,32,148\nQtv 12,64,39680\ns_add_u32 m0, s91, 0\ns_mul_i32 s94, s31, 31\nQmvva 204,36,148\nbuffer_load_dwordx4 v70, s[48:51], s94 offen lds\nQmvva 236,20,152\nQtv 14,64,44800\n"
"Qmvva 80,24,152\nQtv 16,64,39936\nQmvva 112,28,152\nQmvva 144,32,152\nbuffer_load_dwordx4 v71, s[48:51], s94 offen offset:1024 lds\nQmvva 176,36,152\nQtv 18,64,45056\nQmvva 208,20,156\n"
"Qda 112,48,24576\ns_lshr_b32 s68, %19, 2\nQmvva 240,24,156\nQda 116,48,25600\ns_mul_i32 s95, s90, 31\ns_mul_i32 s68, s68, 917504\nQmava 88,28,156\nbuffer_load_dwordx4 v72, s[52:55], s95 offen offset:2048 lds\n"
"s_mul_i32 s50, s92, 7168\nQmava 92,32,156\ns_mul_i32 s69, s93, 57344\nQmava 96,36,156\nQda 120,48,26624\ns_add_u32 s50, s50, s69\nQmava 100,20,160\nQda 124,48,27648\n"
"s_add_u32 s68, s68, s50\nQmava 104,24,160\nbuffer_load_dwordx4 v61, s[52:55], s95 offen offset:3072 lds\ns_add_u32 s48, %8, s68\ns_addc_u32 s49, %9, 0\nQmava 108,28,160\nQda 128,48,28672\ns_and_b32 s49, s49, 0xffff\n"
"Qmava 176,32,160\ns_sub_u32 s50, 917504, s50\nQmava 180,36,160\nQda 132,48,29696\ns_add_u32 m0, s91, 4096\nQmava 184,20,164\nbuffer_load_dwordx4 v62, s[52:55], s95 offen lds\nQmava 188,24,164\n"
"Qda 136,48,30720\nv_accvgpr_read_b32 v84, a88\nQmava 192,28,164\nv_accvgpr_read_b32 v85, a89\nv_accvgpr_read_b32 v86, a90\nQmava 196,32,164\nbuffer_load_dwordx4 v63, s[52:55], s95 offen offset:1024 lds\nv_add_u32 v75, 1, v75\n"
"Qmava 200,36,164\ns_waitcnt vmcnt(22)\nds_write_b32 v73, v75\ns_mov_b32 s52, s62\ns_mov_b32 s53, s63\ns_mov_b32 s54, s86\ns_waitcnt lgkmcnt(5)\nQmvva 92,0,112\n"
"Qtv 20,59,6144\ns_add_u32 s88, s71, 47104\nQmvva 124,4,112\nbuffer_store_dwordx4 v[232:235], v60, s[56:59], s88 offen offset:0 sc1\ns_mov_b32 s94, 458752\nQmvva 156,8,112\nbuffer_load_dwordx4 a[208:211], v60, s[48:51], s94 offen offset:0 sc1\nQmvva 188,12,112\n"
"Qtv 22,59,11264\nv_accvgpr_read_b32 v87, a91\nQmvva 220,16,112\nQtv 24,59,6400\nv_accvgpr_read_b32 v88, a92\nQmvva 252,0,116\nQtv 26,59,11520\nv_accvgpr_read_b32 v89, a93\n"
"Qmvva 100,4,116\nv_accvgpr_read_b32 v90, a94\nv_accvgpr_read_b32 v91, a95\nQmvva 132,8,116\nds_read_b32 v69, v74\nQmvva 164,12,116\nQtv 28,59,6656\nQmvva 196,16,116\n"
"Qtv 30,59,11776\nQmvva 228,0,120\nQtv 32,59,6912\nv_add_u32 v67, 4, v65\nQmvva 108,4,120\ns_waitcnt lgkmcnt(3)\nv_cmp_gt_u32 vcc, v75, v69\nQmvva 140,8,120\n"
"s_cbranch_vccnz .Lso26\n.Lro26:\nQmvva 172,12,120\nbuffer_load_dwordx4 a[212:215], v60, s[48:51], s94 offen offset:1024 sc1\nQmvva 204,16,120\nbuffer_store_dword v67, v77, s[24:27], 0 offen sc1\ns_add_u32 m0, s96, 24576\ns_mov_b32 s95, 1310720\n"
"Qmvva 236,0,124\nbuffer_load_dwordx4 v40, s[52:55], s95 offen lds\nQmvva 80,4,124\nQmvva 112,8,124\nQtv 34,59,12032\nQmvva 144,12,124\nQtv 36,59,7168\nQmvva 176,16,124\n"
"Qtv 38,59,12288\nQmvva 208,0,128\nQda 140,48,49152\nQmvva 240,4,128\nbuffer_load_dwordx4 a[216:219], v60, s[48:51], s94 offen offset:2048 sc1\nQmvva 84,8,128\nbuffer_load_dwordx4 v41, s[52:55], s95 offen offset:1024 lds\nQmvva 88,12,128\n"
"Qmava 96,16,128\nQda 144,48,50176\nQmava 100,0,132\nQda 148,48,51200\nQmava 104,4,132\nQda 152,48,52224\nQmava 108,8,132\nQmava 176,12,132\n"
"Qda 156,48,53248\nQmava 180,16,132\nbuffer_load_dwordx4 v42, s[52:55], s95 offen offset:2048 lds\nQmava 184,0,136\nQda 160,48,54272\nQmava 188,4,136\nbuffer_load_dwordx4 a[220:223], v60, s[48:51], s94 offen offset:3072 sc1\nQmava 192,8,136\n"
"Qda 164,48,55296\nv_accvgpr_read_b32 v120, a96\nQmava 196,12,136\nv_add_u32 v75, 1, v75\nv_accvgpr_read_b32 v121, a97\nQmava 200,16,136\ns_waitcnt vmcnt(23)\nds_write_b32 v73, v75\n"
"s_waitcnt lgkmcnt(5)\nQmvva 92,20,140\nQtv 0,59,30720\nv_accvgpr_read_b32 v122, a98\nQmvva 124,24,140\nbuffer_store_dwordx4 v[212:215], v60, s[56:59], s88 offen offset:1024 sc1\nQmvva 156,28,140\nbuffer_load_dwordx4 v43, s[52:55], s95 offen offset:3072 lds\n"
"Qmvva 188,32,140\nQtv 2,59,35840\nv_accvgpr_read_b32 v123, a99\nQmvva 220,36,140\nQtv 4,59,30976\nv_accvgpr_read_b32 v152, a100\nQmvva 252,20,144\nQtv 6,59,36096\n"
"s_mov_b32 s94, 462848\nQmvva 100,24,144\nbuffer_load_dwordx4 a[224:227], v60, s[48:51], s94 offen offset:0 sc1\nQmvva 132,28,144\nds_read_b32 v69, v74\nv_accvgpr_read_b32 v153, a101\nQmvva 164,32,144\ns_add_u32 m0, s96, 28672\n"
"v_accvgpr_read_b32 v154, a102\nQmvva 196,36,144\nbuffer_load_dwordx4 v44, s[52:55], s95 offen lds\nQmvva 228,20,148\nQtv 8,59,31232\nv_accvgpr_read_b32 v155, a103\nQmvva 108,24,148\ns_waitcnt lgkmcnt(1)\n"
"v_cmp_gt_u32 vcc, v75, v69\nQmvva 140,28,148\ns_cbranch_vccnz .Lso27\n.Lro27:\nQmvva 172,32,148\nQtv 10,59,36352\nQmvva 204,36,148\nQtv 12,59,31488\n"
"Qmvva 236,20,152\nQmvva 80,24,152\nbuffer_load_dwordx4 a[228:231], v60, s[48:51], s94 offen offset:1024 sc1\ns_add_u32 m0, s96, 45056\ns_mov_b32 s95, 1638400\nQmvva 112,28,152\nbuffer_load_dwordx4 v40, s[52:55], s95 offen lds\nQmvva 144,32,152\n"
"Qtv 14,59,36608\nQmvva 176,36,152\nQtv 16,59,31744\nQmvva 208,20,156\nQtv 18,59,36864\nQmvva 240,24,156\nQda 112,49,0\nQmvva 84,28,156\n"
"Qmvva 88,32,156\nQda 116,49,1024\nQmvva 120,36,156\nbuffer_load_dwordx4 v41, s[52:55], s95 offen offset:1024 lds\nQmvva 152,20,160\nQda 120,49,2048\nQmava 104,24,160\nbuffer_load_dwordx4 a[232:235], v60, s[48:51], s94 offen offset:2048 sc1\n"
"Qmava 108,28,160\nQda 124,49,3072\nQmava 176,32,160\nQmava 180,36,160\nQda 128,49,4096\nQmava 184,20,164\nQda 132,49,5120\nQmava 188,24,164\n"
"buffer_load_dwordx4 v42, s[52:55], s95 offen offset:2048 lds\nQmava 192,28,164\nQda 136,49,6144\nQmava 196,32,164\nv_add_u32 v75, 1, v75\nv_accvgpr_read_b32 v184, a104\nQmava 200,36,164\ns_waitcnt vmcnt(25)\n"
"ds_write_b32 v73, v75\ns_waitcnt lgkmcnt(5)\nQmvva 92,0,112\nQtv 20,59,55296\nv_accvgpr_read_b32 v185, a105\nQmvva 124,4,112\nbuffer_store_dwordx4 v[244:247], v60, s[56:59], s88 offen offset:2048 sc1\ns_mov_b32 s94, 573440\n"
"Qmvva 156,8,112\nbuffer_load_dwordx4 a[236:239], v60, s[48:51], s94 offen offset:0 sc1\nQmvva 188,12,112\nbuffer_load_dwordx4 v43, s[52:55], s95 offen offset:3072 lds\nQmvva 220,16,112\nQtv 22,59,60416\nv_accvgpr_read_b32 v186, a106\nQmvva 252,0,116\n"
"Qtv 24,59,55552\nv_accvgpr_read_b32 v187, a107\nQmvva 100,4,116\nQtv 26,59,60672\nv_accvgpr_read_b32 v216, a108\nQmvva 132,8,116\nds_read_b32 v69, v74\nv_accvgpr_read_b32 v217, a109\n"
"Qmvva 164,12,116\nQtv 28,59,55808\nv_accvgpr_read_b32 v218, a110\nQmvva 196,16,116\ns_add_u32 m0, s96, 49152\nv_accvgpr_read_b32 v219, a111\nQmvva 228,0,120\nbuffer_load_dwordx4 v44, s[52:55], s95 offen lds\n"
"Qmvva 108,4,120\ns_waitcnt lgkmcnt(1)\nv_cmp_gt_u32 vcc, v75, v69\nQmvva 140,8,120\ns_cbranch_vccnz .Lso28\n.Lro28:\nQmvva 172,12,120\nbuffer_load_dwordx4 a[240:243], v60, s[48:51], s94 offen offset:1024 sc1\n"
"Qmvva 204,16,120\nQtv 30,59,60928\nQmvva 236,0,124\nQtv 32,59,56064\nQmvva 80,4,124\nQtv 34,59,61184\ns_add_u32 m0, s96, 65536\ns_mov_b32 s95, 1966080\n"
"Qmvva 112,8,124\nbuffer_load_dwordx4 v40, s[52:55], s95 offen lds\nQmvva 144,12,124\nQtv 36,59,56320\nQmvva 176,16,124\nQmvva 208,0,128\nQtv 38,59,61440\nQmvva 240,4,128\n"
"buffer_load_dwordx4 a[244:247], v60, s[48:51], s94 offen offset:2048 sc1\nQmvva 84,8,128\nQda 140,49,24576\nQmvva 88,12,128\nQda 144,49,25600\nQmvva 120,16,128\nbuffer_load_dwordx4 v41, s[52:55], s95 offen offset:1024 lds\nQmvva 152,0,132\n"
"Qda 148,49,26624\nQmvva 184,4,132\nQmvva 216,8,132\nQda 152,49,27648\nQmava 176,12,132\nQda 156,49,28672\nQmava 180,16,132\nQda 160,49,29696\n"
"Qmava 184,0,136\nQda 164,49,30720\nQmava 188,4,136\nbuffer_load_dwordx4 a[248:251], v60, s[48:51], s94 offen offset:3072 sc1\nQmava 192,8,136\nbuffer_load_dwordx4 v42, s[52:55], s95 offen offset:2048 lds\nQmava 196,12,136\nv_add_u32 v75, 1, v75\n"
"Qmava 200,16,136\ns_waitcnt vmcnt(28)\nds_write_b32 v73, v75\ns_waitcnt lgkmcnt(5)\nQmvva 92,20,140\nQtv 0,50,31744\nv_accvgpr_read_b32 v248, a176\nQmvva 124,24,140\n"
"Qtv 2,50,36864\nv_accvgpr_read_b32 v249, a177\nQmvva 156,28,140\nQtv 4,50,32000\nv_accvgpr_read_b32 v250, a178\nQmvva 188,32,140\nv_accvgpr_read_b32 v251, a179\nv_accvgpr_read_b32 v116, a180\n"
"Qmvva 220,36,140\nbuffer_load_dwordx4 v43, s[52:55], s95 offen offset:3072 lds\nQmvva 252,20,144\nQtv 6,50,37120\ns_mov_b32 s94, 577536\nQmvva 100,24,144\nbuffer_load_dwordx4 a[252:255], v60, s[48:51], s94 offen offset:0 sc1\nQmvva 132,28,144\n"
"ds_read_b32 v69, v74\nv_accvgpr_read_b32 v117, a181\nQmvva 164,32,144\nQtv 8,50,32256\nv_accvgpr_read_b32 v118, a182\nQmvva 196,36,144\nv_accvgpr_read_b32 v119, a183\nQmvva 228,20,148\n"
"Qtv 10,50,37376\nQmvva 108,24,148\ns_waitcnt lgkmcnt(2)\nv_cmp_gt_u32 vcc, v75, v69\nQmvva 140,28,148\ns_add_u32 m0, s96, 69632\ns_cbranch_vccnz .Lso29\n.Lro29:\n"
"Qmvva 172,32,148\nbuffer_load_dwordx4 v44, s[52:55], s95 offen lds\nQmvva 204,36,148\nQtv 12,50,32512\nQmvva 236,20,152\nQtv 14,50,37632\nQmvva 80,24,152\nbuffer_load_dwordx4 a[168:171], v60, s[48:51], s94 offen offset:1024 sc1\n"
"Qmvva 112,28,152\nQmvva 144,32,152\nQtv 16,50,32768\nQmvva 176,36,152\nQtv 18,50,37888\ns_add_u32 m0, s96, 86016\ns_mov_b32 s95, 2293760\nQmvva 208,20,156\n"
"buffer_load_dwordx4 v40, s[52:55], s95 offen lds\nQmvva 240,24,156\nQmvva 84,28,156\nQda 112,49,58368\nQmvva 88,32,156\nQda 116,49,59392\nQmvva 120,36,156\nQda 120,49,60416\n"
"Qmvva 152,20,160\nQmvva 184,24,160\nbuffer_load_dwordx4 a[172:175], v60, s[48:51], s94 offen offset:2048 sc1\nQmvva 216,28,160\nbuffer_load_dwordx4 v41, s[52:55], s95 offen offset:1024 lds\nQmvva 248,32,160\nQda 124,49,61440\nQmvva 116,36,160\n"
"Qda 128,49,62464\nQmava 184,20,164\nQmava 188,24,164\nQda 132,49,63488\nQmava 192,28,164\nQda 136,49,64512\nQmava 196,32,164\nv_add_u32 v75, 1, v75\n"
"Qmava 200,36,164\ns_waitcnt vmcnt(22)\nds_write_b32 v73, v75\ns_waitcnt lgkmcnt(5)\nQmvva 92,0,112\nbuffer_load_dwordx4 v42, s[52:55], s95 offen offset:2048 lds\ns_mov_b32 s94, 688128\nQmvva 124,4,112\n"
"buffer_load_dwordx4 a[0:3], v60, s[48:51], s94 offen offset:0 sc1\nQmvva 156,8,112\nQtv 20,64,14336\nv_accvgpr_read_b32 v96, a184\nQmvva 188,12,112\nQtv 22,64,19456\nv_accvgpr_read_b32 v97, a185\nQmvva 220,16,112\n"
"Qtv 24,64,14592\nv_accvgpr_read_b32 v98, a186\n.Lpg18:\ns_cbranch_execz .Lpg22\nQmvva 252,0,116\nQtv 26,64,19712\nv_accvgpr_read_b32 v99, a187\nQmvva 100,4,116\n"
"Qtv 28,64,14848\nv_accvgpr_read_b32 v128, a188\nQmvva 132,8,116\nds_read_b32 v69, v74\nv_accvgpr_read_b32 v129, a189\nQmvva 164,12,116\nbuffer_load_dwordx4 v43, s[52:55], s95 offen offset:3072 lds\nQmvva 196,16,116\n"
"Qtv 30,64,19968\nv_accvgpr_read_b32 v130, a190\nQmvva 228,0,120\nv_accvgpr_read_b32 v131, a191\nQmvva 108,4,120\ns_waitcnt lgkmcnt(1)\nv_cmp_gt_u32 vcc, v75, v69\nQmvva 140,8,120\n"
"s_cbranch_vccnz .Lso30\n.Lro30:\nQmvva 172,12,120\nbuffer_load_dwordx4 a[4:7], v60, s[48:51], s94 offen offset:1024 sc1\nQmvva 204,16,120\ns_waitcnt vmcnt(37)\nds_write_b128 v52, a[208:211] offset:0\nQmvva 236,0,124\n"
"s_add_u32 m0, s96, 90112\nQmvva 80,4,124\nbuffer_load_dwordx4 v44, s[52:55], s95 offen lds\nQmvva 112,8,124\nQtv 32,64,15104\nQmvva 144,12,124\nQtv 34,64,20224\nQmvva 176,16,124\n"
"Qtv 36,64,15360\nQmvva 208,0,128\nQtv 38,64,20480\nQmvva 240,4,128\nbuffer_load_dwordx4 a[8:11], v60, s[48:51], s94 offen offset:2048 sc1\nQmvva 84,8,128\nQda 140,48,0\nQmvva 88,12,128\n"
"Qda 144,48,1024\nQmvva 120,16,128\ns_waitcnt vmcnt(38)\nds_write_b128 v52, a[212:215] offset:4096\nQmvva 152,0,132\nQmvva 184,4,132\nQmvva 216,8,132\nQda 148,48,2048\n"
"Qmvva 248,12,132\nQda 152,48,3072\nQmvva 116,16,132\nQda 156,48,4096\nQmvva 96,0,136\nQda 160,48,5120\nQmvva 128,4,136\nbuffer_load_dwordx4 a[12:15], v60, s[48:51], s94 offen offset:3072 sc1\n"
"Qmava 192,8,136\nQda 164,48,6144\nQmava 196,12,136\nv_add_u32 v75, 1, v75\nQmava 200,16,136\nds_write_b32 v73, v75\ns_waitcnt lgkmcnt(5)\nQmvva 92,20,140\n"
"s_waitcnt vmcnt(36)\nds_write_b128 v52, a[216:219] offset:8192\nQmvva 124,24,140\nQmvva 156,28,140\nbuffer_load_dword v78, v76, s[24:27], 0 offen sc1\nQmvva 188,32,140\nQtv 0,45,20480\nQmvva 220,36,140\n"
"Qtv 2,45,25600\nv_accvgpr_read_b32 v160, a192\nQmvva 252,20,144\nQtv 4,45,20736\ns_mov_b32 s94, 692224\nQmvva 100,24,144\nbuffer_load_dwordx4 a[16:19], v60, s[48:51], s94 offen offset:0 sc1\nQmvva 132,28,144\n"
"ds_read_b32 v69, v74\nv_accvgpr_read_b32 v161, a193\nQmvva 164,32,144\nQtv 6,45,25856\nv_accvgpr_read_b32 v162, a194\nQmvva 196,36,144\ns_waitcnt vmcnt(35)\nds_write_b128 v52, a[220:223] offset:12288\n"
"Qmvva 228,20,148\nv_accvgpr_read_b32 v163, a195\nv_accvgpr_read_b32 v192, a196\nQmvva 108,24,148\ns_waitcnt lgkmcnt(2)\nv_cmp_gt_u32 vcc, v75, v69\nQmvva 140,28,148\ns_cbranch_vccnz .Lso31\n"
".Lro31:\nv_accvgpr_read_b32 v193, a197\nQmvva 172,32,148\ns_add_u32 m0, s96, 4096\ns_mov_b32 s95, 1351680\nv_accvgpr_read_b32 v194, a198\nQmvva 204,36,148\nbuffer_load_dwordx4 v40, s[52:55], s95 offen lds\n"
"Qmvva 236,20,152\nQtv 8,45,20992\nv_accvgpr_read_b32 v195, a199\nQmvva 80,24,152\nbuffer_load_dwordx4 a[20:23], v60, s[48:51], s94 offen offset:1024 sc1\nQmvva 112,28,152\nQtv 10,45,26112\nQmvva 144,32,152\n"
"Qtv 12,45,21248\nQmvva 176,36,152\ns_waitcnt vmcnt(34)\nds_write_b128 v52, a[224:227] offset:16384\nQmvva 208,20,156\nQmvva 240,24,156\nbuffer_load_dwordx4 v41, s[52:55], s95 offen offset:1024 lds\nQmvva 84,28,156\n"
"Qmvva 88,32,156\nQtv 14,45,26368\nQmvva 120,36,156\nQtv 16,45,21504\nQmvva 152,20,160\nQtv 18,45,26624\nQmvva 184,24,160\nbuffer_load_dwordx4 a[24:27], v60, s[48:51], s94 offen offset:2048 sc1\n"
"Qmvva 216,28,160\nQmvva 248,32,160\nbuffer_load_dwordx4 v42, s[52:55], s95 offen offset:2048 lds\nQmvva 116,36,160\ns_waitcnt vmcnt(35)\nds_write_b128 v52, a[228:231] offset:20480\nQmvva 96,20,164\nQmvva 128,24,164\n"
"Qmvva 160,28,164\nQmvva 192,32,164\nQda 48,53,0\nv_add_u32 v75, 1, v75\nQmava 200,36,164\ns_waitcnt vmcnt(27)\nds_write_b32 v73, v75\ns_waitcnt lgkmcnt(3)\n"
"Qmvva 92,0,208\nbuffer_load_dwordx4 v43, s[52:55], s95 offen offset:3072 lds\ns_mov_b32 s94, 802816\nQmvva 124,4,208\nbuffer_load_dwordx4 a[28:31], v60, s[48:51], s94 offen offset:0 sc1\nQmvva 156,8,208\nQtv 20,47,20480\nQmvva 188,12,208\n"
"Qtv 22,47,25600\nQmvva 220,16,208\ns_waitcnt vmcnt(34)\nds_write_b128 v52, a[232:235] offset:25600\nQmvva 252,0,212\nQmvva 100,4,212\nQtv 24,47,20736\nv_accvgpr_read_b32 v224, a200\n"
"Qmvva 132,8,212\nds_read_b32 v69, v74\nv_accvgpr_read_b32 v225, a201\nQmvva 164,12,212\nQda 52,53,4096\ns_add_u32 m0, s96, 8192\nQmvva 196,16,212\nbuffer_load_dwordx4 v44, s[52:55], s95 offen lds\n"
"Qmvva 228,0,216\nQtv 26,47,25856\nv_accvgpr_read_b32 v226, a202\nQmvva 108,4,216\ns_waitcnt lgkmcnt(2)\nv_cmp_gt_u32 vcc, v75, v69\nQmvva 140,8,216\ns_cbranch_vccnz .Lso32\n"
".Lro32:\nv_accvgpr_read_b32 v227, a203\nQmvva 172,12,216\nbuffer_load_dwordx4 a[32:35], v60, s[48:51], s94 offen offset:1024 sc1\nQmvva 204,16,216\ns_waitcnt vmcnt(33)\nds_write_b128 v52, a[236:239] offset:29696\nQmvva 236,0,220\n"
"Qmvva 80,4,220\nQtv 28,47,20992\nQmvva 112,8,220\nQmvva 144,12,220\nQda 56,53,8192\nQmvva 176,16,220\nQtv 30,47,26112\nQmvva 208,0,224\n"
"Qtv 32,47,21248\nQmvva 240,4,224\nbuffer_load_dwordx4 a[36:39], v60, s[48:51], s94 offen offset:2048 sc1\nQmvva 84,8,224\nQtv 34,47,26368\nQmvva 88,12,224\nQtv 36,47,21504\nQmvva 120,16,224\n"
"s_waitcnt vmcnt(31)\nds_write_b128 v52, a[240:243] offset:33792\nQmvva 152,0,228\nQmvva 184,4,228\nQtv 38,47,26624\nQmvva 216,8,228\nQmvva 248,12,228\nQda 60,53,12288\n"
"Qmvva 116,16,228\nQmvva 96,0,232\nQmvva 128,4,232\nbuffer_load_dwordx4 a[40:43], v60, s[48:51], s94 offen offset:3072 sc1\nQmvva 160,8,232\nQmvva 192,12,232\nv_add_u32 v75, 1, v75\nQmvva 224,16,232\n"
"s_waitcnt vmcnt(36)\nds_write_b32 v73, v75\ns_waitcnt lgkmcnt(2)\nQmvva 92,20,48\ns_waitcnt vmcnt(30)\nds_write_b128 v52, a[244:247] offset:37888\nQmvva 124,24,48\nQmvva 156,28,48\n"
"s_waitcnt vmcnt(12)\nv_sub_u32 v66, v78, v65\nv_cmp_gt_u32 vcc, 3, v66\ns_and_b64 vcc, vcc, s[28:29]\ns_cbranch_scc1 .Lf3_w\n.Lf3_r:\nQmvva 188,32,48\nQda 64,53,16384\n"
"Qmvva 220,36,48\nQtv 0,45,40960\nQmvva 252,20,52\nQtv 2,45,46080\ns_mov_b32 s94, 806912\nQmvva 100,24,52\nbuffer_load_dwordx4 a[44:47], v60, s[48:51], s94 offen offset:0 sc1\nQmvva 132,28,52\n"
"ds_read_b32 v69, v74\nQmvva 164,32,52\nQtv 4,45,41216\nQmvva 196,36,52\ns_waitcnt vmcnt(29)\nds_write_b128 v52, a[248:251] offset:41984\ns_waitcnt lgkmcnt(15)\nQmvva 228,20,56\n"
"v_add_u32 v67, 5, v65\nQmvva 108,24,56\ns_waitcnt lgkmcnt(2)\nv_cmp_gt_u32 vcc, v75, v69\nQmvva 140,28,56\ns_cbranch_vccnz .Lso33\n.Lro33:\nQmvva 172,32,56\n"
"Qda 68,53,20480\nQmvva 204,36,56\nbuffer_store_dword v67, v77, s[24:27], 0 offen sc1\ns_add_u32 m0, s96, 24576\ns_mov_b32 s95, 1679360\nQmvva 236,20,60\nbuffer_load_dwordx4 v40, s[52:55], s95 offen lds\nQmvva 80,24,60\n"
"buffer_load_dwordx4 a[208:211], v60, s[48:51], s94 offen offset:1024 sc1\nQmvva 112,28,60\nQtv 6,45,46336\nQmvva 144,32,60\nQtv 8,45,41472\nQmvva 176,36,60\ns_waitcnt vmcnt(29)\nds_write_b128 v52, a[252:255] offset:46080\n"
"s_waitcnt lgkmcnt(3)\nQmvva 208,20,64\nQmvva 240,24,64\nQtv 10,45,46592\nQmvva 84,28,64\nbuffer_load_dwordx4 v41, s[52:55], s95 offen offset:1024 lds\nQmvva 88,32,64\nQda 72,53,25600\n"
"Qmvva 120,36,64\nQtv 12,45,41728\nQmvva 152,20,68\nQtv 14,45,46848\nQmvva 184,24,68\nbuffer_load_dwordx4 a[212:215], v60, s[48:51], s94 offen offset:2048 sc1\nQmvva 216,28,68\nQtv 16,45,41984\n"
"Qmvva 248,32,68\nQtv 18,45,47104\nQmvva 116,36,68\ns_waitcnt vmcnt(29)\nds_write_b128 v52, a[168:171] offset:50176\ns_waitcnt lgkmcnt(5)\nQmvva 96,20,72\nQmvva 128,24,72\n"
"buffer_load_dwordx4 v42, s[52:55], s95 offen offset:2048 lds\nQmvva 160,28,72\nQmvva 192,32,72\nQda 76,53,29696\nv_add_u32 v75, 1, v75\nQmvva 224,36,72\ns_waitcnt vmcnt(31)\nds_write_b32 v73, v75\n"
"s_waitcnt lgkmcnt(3)\nQmvva 92,0,236\nQtv 20,47,40960\ns_mov_b32 s94, 473088\nQmvva 124,4,236\nbuffer_load_dwordx4 a[216:219], v60, s[48:51], s94 offen offset:0 sc1\nQmvva 156,8,236\nQtv 22,47,46080\n"
"Qmvva 188,12,236\nbuffer_load_dwordx4 v43, s[52:55], s95 offen offset:3072 lds\nQmvva 220,16,236\ns_waitcnt vmcnt(30)\nds_write_b128 v52, a[172:175] offset:0\nQmvva 252,0,240\nQmvva 100,4,240\nQtv 24,47,41216\n"
"Qmvva 132,8,240\nds_read_b32 v69, v74\nQmvva 164,12,240\nQda 80,53,33792\nQmvva 196,16,240\nQtv 26,47,46336\ns_add_u32 m0, s96, 28672\nQmvva 228,0,244\n"
"buffer_load_dwordx4 v44, s[52:55], s95 offen lds\nQmvva 108,4,244\ns_waitcnt lgkmcnt(2)\nv_cmp_gt_u32 vcc, v75, v69\nQmvva 140,8,244\ns_cbranch_vccnz .Lso34\n.Lro34:\nQmvva 172,12,244\n"
"buffer_load_dwordx4 a[220:223], v60, s[48:51], s94 offen offset:1024 sc1\nQmvva 204,16,244\ns_waitcnt vmcnt(29)\nds_write_b128 v52, a[0:3] offset:4096\nQmvva 236,0,248\nQmvva 80,4,248\nQtv 28,47,41472\nQmvva 112,8,248\n"
"Qmvva 144,12,248\nQda 48,53,37888\nQmvva 176,16,248\nQtv 30,47,46592\nQmvva 208,0,252\nQtv 32,47,41728\nQmvva 240,4,252\nbuffer_load_dwordx4 a[224:227], v60, s[48:51], s94 offen offset:2048 sc1\n"
"Qmvva 84,8,252\nQtv 34,47,46848\nQmvva 88,12,252\nQtv 36,47,41984\nQmvva 120,16,252\ns_waitcnt vmcnt(28)\nds_write_b128 v52, a[4:7] offset:8192\nQmvva 152,0,168\n"
"Qmvva 184,4,168\nQtv 38,47,47104\nQmvva 216,8,168\nQmvva 248,12,168\nQda 52,53,41984\nQmvva 116,16,168\nQmvva 96,0,172\nQmvva 128,4,172\n"
"buffer_load_dwordx4 a[228:231], v60, s[48:51], s94 offen offset:3072 sc1\nQmvva 160,8,172\nQmvva 192,12,172\nv_add_u32 v75, 1, v75\nQmvva 224,16,172\nds_write_b32 v73, v75\ns_waitcnt lgkmcnt(2)\nQmvva 92,20,76\n"
"s_waitcnt vmcnt(27)\nds_write_b128 v52, a[8:11] offset:12288\nQmvva 124,24,76\nQmvva 156,28,76\nQta 112,46,0\nQmvva 188,32,76\nQda 56,53,46080\nQmvva 220,36,76\n"
"Qta 114,46,5120\nQmvva 252,20,80\nQta 116,46,256\ns_mov_b32 s94, 477184\nQmvva 100,24,80\nbuffer_load_dwordx4 a[232:235], v60, s[48:51], s94 offen offset:0 sc1\nQmvva 132,28,80\nds_read_b32 v69, v74\n"
"Qmvva 164,32,80\nQta 118,46,5376\nQmvva 196,36,80\ns_waitcnt vmcnt(27)\nds_write_b128 v52, a[12:15] offset:16384\ns_waitcnt lgkmcnt(15)\nQmvva 228,20,48\nQmvva 108,24,48\n"
"s_waitcnt lgkmcnt(2)\nv_cmp_gt_u32 vcc, v75, v69\nQmvva 140,28,48\ns_cbranch_vccnz .Lso35\n.Lro35:\nQmvva 172,32,48\nQda 60,53,50176\ns_add_u32 m0, s96, 45056\n"
"s_mov_b32 s95, 2007040\nQmvva 204,36,48\nbuffer_load_dwordx4 v40, s[52:55], s95 offen lds\nQmvva 236,20,52\nQta 120,46,512\nQmvva 80,24,52\nbuffer_load_dwordx4 a[236:239], v60, s[48:51], s94 offen offset:1024 sc1\nQmvva 112,28,52\n"
"Qta 122,46,5632\nQmvva 144,32,52\nQta 124,46,768\nQmvva 176,36,52\ns_waitcnt vmcnt(27)\nds_write_b128 v52, a[16:19] offset:20480\ns_waitcnt lgkmcnt(4)\nQmvva 208,20,56\n"
"Qmvva 240,24,56\nbuffer_load_dwordx4 v41, s[52:55], s95 offen offset:1024 lds\nQmvva 84,28,56\nQta 126,46,5888\nQmvva 88,32,56\nQda 64,53,0\nQmvva 120,36,56\nQta 128,46,1024\n"
"Qmvva 152,20,60\nQta 130,46,6144\nQmvva 184,24,60\nbuffer_load_dwordx4 a[240:243], v60, s[48:51], s94 offen offset:2048 sc1\nQmvva 216,28,60\nQmvva 248,32,60\nbuffer_load_dwordx4 v42, s[52:55], s95 offen offset:2048 lds\nQmvva 116,36,60\n"
"s_waitcnt vmcnt(28)\nds_write_b128 v52, a[20:23] offset:25600\ns_waitcnt lgkmcnt(3)\nQmvva 96,20,64\nQmvva 128,24,64\nQmvva 160,28,64\nQmvva 192,32,64\nQda 68,53,4096\n"
"v_add_u32 v75, 1, v75\nQmvva 224,36,64\ns_waitcnt vmcnt(34)\nds_write_b32 v73, v75\ns_waitcnt lgkmcnt(3)\nQmvaa 92,112,0\nbuffer_load_dwordx4 v43, s[52:55], s95 offen offset:3072 lds\ns_mov_b32 s94, 587776\n"
"Qmvaa 124,116,0\nbuffer_load_dwordx4 a[244:247], v60, s[48:51], s94 offen offset:0 sc1\nQmvaa 156,120,0\nQta 132,51,0\nQmvaa 188,124,0\nQta 134,51,5120\nQmvaa 220,128,0\ns_waitcnt vmcnt(28)\n"
"ds_write_b128 v52, a[24:27] offset:29696\nQmvaa 252,112,4\nQmvaa 100,116,4\nQta 136,51,256\nQmvaa 132,120,4\nds_read_b32 v69, v74\nQmvaa 164,124,4\nQda 72,53,8192\n"
"s_add_u32 m0, s96, 49152\nQmvaa 196,128,4\nbuffer_load_dwordx4 v44, s[52:55], s95 offen lds\nQmvaa 228,112,8\nQta 138,51,5376\nQmvaa 108,116,8\ns_waitcnt lgkmcnt(2)\nv_cmp_gt_u32 vcc, v75, v69\n"
"Qmvaa 140,120,8\ns_cbranch_vccnz .Lso36\n.Lro36:\nQmvaa 172,124,8\nbuffer_load_dwordx4 a[248:251], v60, s[48:51], s94 offen offset:1024 sc1\nQmvaa 204,128,8\ns_waitcnt vmcnt(27)\nds_write_b128 v52, a[28:31] offset:33792\n"
"Qmvaa 236,112,12\nQmvaa 80,116,12\nQta 140,51,512\nQmvaa 112,120,12\nQmvaa 144,124,12\nQda 76,53,12288\nQmvaa 176,128,12\nQta 142,51,5632\n"
"Qmvaa 208,112,16\nQta 144,51,768\nQmvaa 240,116,16\nbuffer_load_dwordx4 a[252:255], v60, s[48:51], s94 offen offset:2048 sc1\nQmvaa 84,120,16\nQta 146,51,5888\nQmvaa 88,124,16\nQta 148,51,1024\n"
"Qmvaa 120,128,16\ns_waitcnt vmcnt(26)\nds_write_b128 v52, a[32:35] offset:37888\nQmvaa 152,112,20\nQmvaa 184,116,20\nQta 150,51,6144\nQmvaa 216,120,20\nQmvaa 248,124,20\n"
"Qda 80,53,16384\nQmvaa 116,128,20\nQmvaa 96,112,24\nQmvaa 128,116,24\nbuffer_load_dwordx4 a[168:171], v60, s[48:51], s94 offen offset:3072 sc1\nQmvaa 160,120,24\nQmvaa 192,124,24\nv_add_u32 v75, 1, v75\n"
"Qmvaa 224,128,24\nds_write_b32 v73, v75\ns_waitcnt lgkmcnt(2)\nQmvaa 92,132,68\ns_waitcnt vmcnt(26)\nds_write_b128 v52, a[36:39] offset:41984\nQmvaa 124,136,68\nQmvaa 156,140,68\n"
"Qtv 0,46,20480\nQmvaa 188,144,68\nQda 48,53,20480\nQmvaa 220,148,68\nQtv 2,46,25600\nQmvaa 252,132,72\nQtv 4,46,20736\ns_mov_b32 s94, 591872\n"
"Qmvaa 100,136,72\nbuffer_load_dwordx4 a[172:175], v60, s[48:51], s94 offen offset:0 sc1\nQmvaa 132,140,72\nds_read_b32 v69, v74\ns_mov_b32 s40, %17\nQmvaa 164,144,72\nQtv 6,46,25856\ns_and_b32 s41, %18, 0xffff\n"
"Qmvaa 196,148,72\ns_waitcnt vmcnt(26)\nds_write_b128 v52, a[40:43] offset:46080\ns_waitcnt lgkmcnt(15)\nQmvaa 228,132,76\ns_mov_b32 s42, 571200\ns_lshr_b32 s68, %19, 2\nQmvaa 108,136,76\n"
"s_waitcnt lgkmcnt(2)\nv_cmp_gt_u32 vcc, v75, v69\nQmvaa 140,140,76\ns_cbranch_vccnz .Lso37\n.Lro37:\ns_mul_i32 s65, s68, 286720\n.Lpg19:\ns_cbranch_execz .Lpg23\n"
"Qmvaa 172,144,76\nQda 52,53,25600\ns_add_u32 m0, s96, 65536\ns_mov_b32 s95, 2334720\nQmvaa 204,148,76\nbuffer_load_dwordx4 v40, s[52:55], s95 offen lds\nQmvaa 236,132,80\nQtv 8,46,20992\n"
"s_sub_u32 s60, 286720, s65\nQmvaa 80,136,80\nbuffer_load_dwordx4 a[0:3], v60, s[48:51], s94 offen offset:1024 sc1\nQmvaa 112,140,80\nQtv 10,46,26112\ns_mul_i32 s68, s68, 114688\nQmvaa 144,144,80\nQtv 12,46,21248\n"
"s_add_u32 s64, s98, 57344\nQmvaa 176,148,80\ns_waitcnt vmcnt(27)\nds_write_b128 v52, a[44:47] offset:50176\ns_waitcnt lgkmcnt(4)\nQmvaa 208,132,48\ns_sub_u32 s64, s64, s68\ns_mul_i32 s68, s93, 160\n"
"Qmvaa 240,136,48\nbuffer_load_dwordx4 v41, s[52:55], s95 offen offset:1024 lds\nQmvaa 84,140,48\nQtv 14,46,26368\ns_add_u32 s68, s68, %10\nQmvaa 88,144,48\nQda 56,53,29696\nv_bfe_u32 v66, v60, 4, 4\n"
"Qmvaa 120,148,48\nQtv 16,46,21504\nv_mul_u32_u24 v66, 336, v66\nQmvaa 152,132,52\nQtv 18,46,26624\nv_lshrrev_b32 v67, 8, v60\nQmvaa 184,136,52\nbuffer_load_dwordx4 a[4:7], v60, s[48:51], s94 offen offset:2048 sc1\n"
"Qmvaa 216,140,52\nv_lshlrev_b32 v68, 3, v67\nv_add3_u32 v61, v66, v68, s68\nQmvaa 248,144,52\nbuffer_load_dwordx4 v42, s[52:55], s95 offen offset:2048 lds\nQmvaa 116,148,52\ns_waitcnt vmcnt(27)\nds_write_b128 v52, a[208:211] offset:0\n"
"s_waitcnt lgkmcnt(3)\nQmvaa 96,132,56\ns_lshl_b32 s68, s70, 2\nv_add_u32 v67, s68, v67\nQmvaa 128,136,56\ns_and_b32 s69, %19, 1\ns_lshl_b32 s68, s69, 6\nQmvaa 160,140,56\n"
"v_and_b32 v66, 255, v60\nv_add_u32 v66, s68, v66\nQmvaa 192,144,56\nQda 60,53,33792\nv_add_u32 v75, 1, v75\nQmvaa 224,148,56\ns_waitcnt vmcnt(34)\nds_write_b32 v73, v75\n"
"s_waitcnt lgkmcnt(3)\nQmvva 92,0,28\nbuffer_load_dwordx4 v43, s[52:55], s95 offen offset:3072 lds\ns_mov_b32 s94, 702464\nQmvva 124,4,28\nbuffer_load_dwordx4 a[8:11], v60, s[48:51], s94 offen offset:0 sc1\nQmvva 156,8,28\nQtv 20,51,20480\n"
"v_mul_u32_u24 v68, 336, v67\nQmvva 188,12,28\nQtv 22,51,25600\nv_add3_u32 v62, v66, v68, %10\nQmvva 220,16,28\ns_waitcnt vmcnt(27)\nds_write_b128 v52, a[212:215] offset:4096\nQmvva 252,0,32\n"
"v_mul_u32_u24 v68, 2560, v67\nv_add_u32 v63, v66, v68\nQmvva 100,4,32\nQtv 24,51,20736\ns_lshl_b32 s68, s70, 4\nQmvva 132,8,32\nds_read_b32 v69, v74\nv_lshrrev_b32 v67, 6, v60\n"
"Qmvva 164,12,32\nQda 64,53,37888\ns_add_u32 m0, s96, 69632\nQmvva 196,16,32\nbuffer_load_dwordx4 v44, s[52:55], s95 offen lds\nQmvva 228,0,36\nQtv 26,51,25856\nv_add_u32 v67, s68, v67\n"
"Qmvva 108,4,36\ns_waitcnt lgkmcnt(2)\nv_cmp_gt_u32 vcc, v75, v69\nQmvva 140,8,36\ns_cbranch_vccnz .Lso38\n.Lro38:\ns_xor_b32 s69, s69, 1\nQmvva 172,12,36\n"
"buffer_load_dwordx4 a[12:15], v60, s[48:51], s94 offen offset:1024 sc1\nQmvva 204,16,36\ns_waitcnt vmcnt(27)\nds_write_b128 v52, a[216:219] offset:8192\nQmvva 236,0,40\ns_lshl_b32 s68, s69, 8\nv_and_b32 v66, 63, v60\nQmvva 80,4,40\n"
"Qtv 28,51,20992\nv_add_u32 v66, s68, v66\nQmvva 112,8,40\nv_mul_u32_u24 v68, 336, v67\nv_add3_u32 v70, v66, v68, %10\nQmvva 144,12,40\nQda 68,53,41984\nv_mul_u32_u24 v68, 2560, v67\n"
"Qmvva 176,16,40\nQtv 30,51,26112\nv_add_u32 v71, v66, v68\nQmvva 208,0,44\nQtv 32,51,21248\nQmvva 240,4,44\nbuffer_load_dwordx4 a[16:19], v60, s[48:51], s94 offen offset:2048 sc1\nQmvva 84,8,44\n"
"Qtv 34,51,26368\nQmvva 88,12,44\nQtv 36,51,21504\nQmvva 120,16,44\ns_waitcnt vmcnt(25)\nds_write_b128 v52, a[220:223] offset:12288\nQmvva 152,0,208\nQmvva 184,4,208\n"
"Qtv 38,51,26624\nQmvva 216,8,208\nQmvva 248,12,208\nQda 72,53,46080\nQmvva 116,16,208\nQmvva 96,0,212\nQmvva 128,4,212\nbuffer_load_dwordx4 a[20:23], v60, s[48:51], s94 offen offset:3072 sc1\n"
"Qmvva 160,8,212\nQmvva 192,12,212\nv_add_u32 v75, 1, v75\nQmvva 224,16,212\nds_write_b32 v73, v75\ns_waitcnt lgkmcnt(2)\nQmvva 92,20,60\ns_waitcnt vmcnt(25)\n"
"ds_write_b128 v52, a[224:227] offset:16384\nQmvva 124,24,60\nQmvva 156,28,60\nbuffer_load_dword v78, v76, s[24:27], 0 offen sc1\nQmvva 188,32,60\nQda 76,53,50176\nQmvva 220,36,60\nQta 112,45,0\n"
"Qmvva 252,20,64\nQta 114,45,5120\ns_mov_b32 s94, 706560\nQmvva 100,24,64\nbuffer_load_dwordx4 a[24:27], v60, s[48:51], s94 offen offset:0 sc1\nQmvva 132,28,64\nds_read_b32 v69, v74\nQmvva 164,32,64\n"
"Qta 116,45,256\nQmvva 196,36,64\ns_waitcnt vmcnt(26)\nds_write_b128 v52, a[228:231] offset:20480\ns_waitcnt lgkmcnt(15)\nQmvva 228,20,68\nQmvva 108,24,68\ns_waitcnt lgkmcnt(2)\n"
"v_cmp_gt_u32 vcc, v75, v69\nQmvva 140,28,68\ns_cbranch_vccnz .Lso39\n.Lro39:\nQmvva 172,32,68\nQda 80,53,0\ns_add_u32 m0, s96, 86016\ns_mov_b32 s95, 1392640\n"
"Qmvva 204,36,68\nbuffer_load_dwordx4 v40, s[52:55], s95 offen lds\nQmvva 236,20,72\nQta 118,45,5376\nQmvva 80,24,72\nbuffer_load_dwordx4 a[28:31], v60, s[48:51], s94 offen offset:1024 sc1\nQmvva 112,28,72\nQta 120,45,512\n"
"Qmvva 144,32,72\nQta 122,45,5632\nQmvva 176,36,72\ns_waitcnt vmcnt(27)\nds_write_b128 v52, a[232:235] offset:25600\ns_waitcnt lgkmcnt(4)\nQmvva 208,20,76\nQmvva 240,24,76\n"
"buffer_load_dwordx4 v41, s[52:55], s95 offen offset:1024 lds\nQmvva 84,28,76\nQta 124,45,768\nQmvva 88,32,76\nQda 48,53,4096\nQmvva 120,36,76\nQta 126,45,5888\nQmvva 152,20,80\n"
"Qta 128,45,1024\nQmvva 184,24,80\nbuffer_load_dwordx4 a[32:35], v60, s[48:51], s94 offen offset:2048 sc1\nQmvva 216,28,80\nQta 130,45,6144\nQmvva 248,32,80\nbuffer_load_dwordx4 v42, s[52:55], s95 offen offset:2048 lds\nQmvva 116,36,80\n"
"s_waitcnt vmcnt(28)\nds_write_b128 v52, a[236:239] offset:29696\ns_waitcnt lgkmcnt(4)\nQmvva 96,20,48\nQmvva 128,24,48\nQmvva 160,28,48\nQmvva 192,32,48\nQda 52,53,8192\n"
"v_add_u32 v75, 1, v75\nQmvva 224,36,48\ns_waitcnt vmcnt(34)\nds_write_b32 v73, v75\ns_waitcnt lgkmcnt(3)\nQmvaa 92,112,216\nbuffer_load_dwordx4 v43, s[52:55], s95 offen offset:3072 lds\ns_mov_b32 s94, 817152\n"
"Qmvaa 124,116,216\nbuffer_load_dwordx4 a[36:39], v60, s[48:51], s94 offen offset:0 sc1\nQmvaa 156,120,216\nQta 132,47,0\nQmvaa 188,124,216\nQta 134,47,5120\nQmvaa 220,128,216\ns_waitcnt vmcnt(28)\n"
"ds_write_b128 v52, a[240:243] offset:33792\nQmvaa 252,112,220\nQmvaa 100,116,220\nQta 136,47,256\nQmvaa 132,120,220\nds_read_b32 v69, v74\nQmvaa 164,124,220\nQda 56,53,12288\n"
"s_add_u32 m0, s96, 90112\nQmvaa 196,128,220\nbuffer_load_dwordx4 v44, s[52:55], s95 offen lds\nQmvaa 228,112,224\nQta 138,47,5376\nQmvaa 108,116,224\ns_waitcnt lgkmcnt(2)\nv_cmp_gt_u32 vcc, v75, v69\n"
"Qmvaa 140,120,224\ns_cbranch_vccnz .Lso40\n.Lro40:\nQmvaa 172,124,224\nbuffer_load_dwordx4 a[40:43], v60, s[48:51], s94 offen offset:1024 sc1\nQmvaa 204,128,224\ns_waitcnt vmcnt(27)\nds_write_b128 v52, a[244:247] offset:37888\n"
"Qmvaa 236,112,228\nQmvaa 80,116,228\nQta 140,47,512\nQmvaa 112,120,228\nQmvaa 144,124,228\nQda 60,53,16384\nQmvaa 176,128,228\nQta 142,47,5632\n"
"Qmvaa 208,112,232\nQta 144,47,768\nQmvaa 240,116,232\nbuffer_load_dwordx4 a[44:47], v60, s[48:51], s94 offen offset:2048 sc1\nQmvaa 84,120,232\nQta 146,47,5888\nQmvaa 88,124,232\nQta 148,47,1024\n"
"Qmvaa 120,128,232\ns_waitcnt vmcnt(26)\nds_write_b128 v52, a[248:251] offset:41984\nQmvaa 152,112,236\nQmvaa 184,116,236\nQta 150,47,6144\nQmvaa 216,120,236\nQmvaa 248,124,236\n"
"Qda 64,53,20480\nQmvaa 116,128,236\nQmvaa 96,112,240\nQmvaa 128,116,240\nbuffer_load_dwordx4 a[208:211], v60, s[48:51], s94 offen offset:3072 sc1\nQmvaa 160,120,240\nQmvaa 192,124,240\nv_add_u32 v75, 1, v75\n"
"Qmvaa 224,128,240\nds_write_b32 v73, v75\ns_waitcnt lgkmcnt(2)\nQmvaa 92,132,52\ns_waitcnt vmcnt(26)\nds_write_b128 v52, a[252:255] offset:46080\nQmvaa 124,136,52\nQmvaa 156,140,52\n"
"s_waitcnt vmcnt(12)\nv_sub_u32 v66, v78, v65\nv_cmp_gt_u32 vcc, 4, v66\ns_and_b64 vcc, vcc, s[28:29]\ns_cbranch_scc1 .Lf4_w\n.Lf4_r:\nQmvaa 188,144,52\nQda 68,53,25600\n"
"Qmvaa 220,148,52\nQtv 0,45,20480\nQmvaa 252,132,56\nQtv 2,45,25600\ns_mov_b32 s94, 821248\nQmvaa 100,136,56\nbuffer_load_dwordx4 a[212:215], v60, s[48:51], s94 offen offset:0 sc1\nQmvaa 132,140,56\n"
"ds_read_b32 v69, v74\nQmvaa 164,144,56\nQtv 4,45,20736\nQmvaa 196,148,56\ns_waitcnt vmcnt(26)\nds_write_b128 v52, a[168:171] offset:50176\ns_waitcnt lgkmcnt(15)\nQmvaa 228,132,60\n"
"Qmvaa 108,136,60\ns_waitcnt lgkmcnt(2)\nv_cmp_gt_u32 vcc, v75, v69\nQmvaa 140,140,60\ns_cbranch_vccnz .Lso41\n.Lro41:\nQmvaa 172,144,60\nQda 72,53,29696\n"
"s_add_u32 m0, s96, 4096\ns_mov_b32 s95, 1720320\nQmvaa 204,148,60\nbuffer_load_dwordx4 v40, s[52:55], s95 offen lds\nQmvaa 236,132,64\nQtv 6,45,25856\nQmvaa 80,136,64\nbuffer_load_dwordx4 a[216:219], v60, s[48:51], s94 offen offset:1024 sc1\n"
"Qmvaa 112,140,64\nQtv 8,45,20992\nQmvaa 144,144,64\nQtv 10,45,26112\nQmvaa 176,148,64\ns_waitcnt vmcnt(27)\nds_write_b128 v52, a[172:175] offset:0\ns_waitcnt lgkmcnt(4)\n"
"Qmvaa 208,132,68\nQmvaa 240,136,68\nbuffer_load_dwordx4 v41, s[52:55], s95 offen offset:1024 lds\nQmvaa 84,140,68\nQtv 12,45,21248\nQmvaa 88,144,68\nQda 76,53,33792\nQmvaa 120,148,68\n"
"Qtv 14,45,26368\nQmvaa 152,132,72\nQtv 16,45,21504\nQmvaa 184,136,72\nbuffer_load_dwordx4 a[220:223], v60, s[48:51], s94 offen offset:2048 sc1\nQmvaa 216,140,72\nQtv 18,45,26624\nQmvaa 248,144,72\n"
"buffer_load_dwordx4 v42, s[52:55], s95 offen offset:2048 lds\nQmvaa 116,148,72\ns_waitcnt vmcnt(28)\nds_write_b128 v52, a[0:3] offset:4096\ns_waitcnt lgkmcnt(4)\nQmvaa 96,132,76\nQmvaa 128,136,76\nQmvaa 160,140,76\n"
"Qmvaa 192,144,76\nQda 80,53,37888\nv_add_u32 v75, 1, v75\nQmvaa 224,148,76\ns_waitcnt vmcnt(34)\nds_write_b32 v73, v75\ns_waitcnt lgkmcnt(3)\nQmvva 92,0,244\n"
"buffer_load_dwordx4 v43, s[52:55], s95 offen offset:3072 lds\ns_mov_b32 s94, 487424\nQmvva 124,4,244\nbuffer_load_dwordx4 a[224:227], v60, s[48:51], s94 offen offset:0 sc1\nQmvva 156,8,244\nQtv 20,47,20480\nQmvva 188,12,244\nQtv 22,47,25600\n"
"Qmvva 220,16,244\ns_waitcnt vmcnt(28)\nds_write_b128 v52, a[4:7] offset:8192\nQmvva 252,0,248\nQmvva 100,4,248\nQtv 24,47,20736\nQmvva 132,8,248\nds_read_b32 v69, v74\n"
"Qmvva 164,12,248\nQda 48,53,41984\ns_add_u32 m0, s96, 8192\nQmvva 196,16,248\nbuffer_load_dwordx4 v44, s[52:55], s95 offen lds\nQmvva 228,0,252\nQtv 26,47,25856\nQmvva 108,4,252\n"
"s_waitcnt lgkmcnt(2)\nv_cmp_gt_u32 vcc, v75, v69\nQmvva 140,8,252\ns_cbranch_vccnz .Lso42\n.Lro42:\nQmvva 172,12,252\nbuffer_load_dwordx4 a[228:231], v60, s[48:51], s94 offen offset:1024 sc1\nQmvva 204,16,252\n"
"s_waitcnt vmcnt(27)\nds_write_b128 v52, a[8:11] offset:12288\nQmvva 236,0,168\nQmvva 80,4,168\nQtv 28,47,20992\nQmvva 112,8,168\nQmvva 144,12,168\nQda 52,53,46080\n"
"Qmvva 176,16,168\nQtv 30,47,26112\nQmvva 208,0,172\nQtv 32,47,21248\nQmvva 240,4,172\nbuffer_load_dwordx4 a[232:235], v60, s[48:51], s94 offen offset:2048 sc1\nQmvva 84,8,172\nQtv 34,47,26368\n"
"Qmvva 88,12,172\nQtv 36,47,21504\nQmvva 120,16,172\ns_waitcnt vmcnt(26)\nds_write_b128 v52, a[12:15] offset:16384\nQmvva 152,0,0\nQmvva 184,4,0\nQtv 38,47,26624\n"
"Qmvva 216,8,0\nQmvva 248,12,0\nQda 56,53,50176\nQmvva 116,16,0\nQmvva 96,0,4\nQmvva 128,4,4\nbuffer_load_dwordx4 a[236:239], v60, s[48:51], s94 offen offset:3072 sc1\nQmvva 160,8,4\n"
"Qmvva 192,12,4\nv_add_u32 v75, 1, v75\nQmvva 224,16,4\nds_write_b32 v73, v75\ns_waitcnt lgkmcnt(2)\nQmvva 92,20,80\ns_waitcnt vmcnt(26)\nds_write_b128 v52, a[16:19] offset:20480\n"
"Qmvva 124,24,80\nQmvva 156,28,80\nQta 112,45,40960\nQmvva 188,32,80\nQda 60,53,0\nQmvva 220,36,80\nQta 114,45,46080\nQmvva 252,20,48\n"
"Qta 116,45,41216\ns_mov_b32 s94, 491520\nQmvva 100,24,48\nbuffer_load_dwordx4 a[240:243], v60, s[48:51], s94 offen offset:0 sc1\nQmvva 132,28,48\nds_read_b32 v69, v74\nQmvva 164,32,48\nQta 118,45,46336\n"
"Qmvva 196,36,48\ns_waitcnt vmcnt(26)\nds_write_b128 v52, a[20:23] offset:25600\ns_waitcnt lgkmcnt(15)\nQmvva 228,20,52\nQmvva 108,24,52\ns_waitcnt lgkmcnt(2)\nv_cmp_gt_u32 vcc, v75, v69\n"
"Qmvva 140,28,52\ns_cbranch_vccnz .Lso43\n.Lro43:\nQmvva 172,32,52\nQda 64,53,4096\ns_add_u32 m0, s96, 24576\ns_mov_b32 s95, 2048000\nQmvva 204,36,52\n"
"buffer_load_dwordx4 v40, s[52:55], s95 offen lds\nQmvva 236,20,56\nQta 120,45,41472\nQmvva 80,24,56\nbuffer_load_dwordx4 a[244:247], v60, s[48:51], s94 offen offset:1024 sc1\nQmvva 112,28,56\nQta 122,45,46592\nQmvva 144,32,56\n"
"Qta 124,45,41728\nQmvva 176,36,56\ns_waitcnt vmcnt(26)\nds_write_b128 v52, a[24:27] offset:29696\ns_waitcnt lgkmcnt(4)\nQmvva 208,20,60\nQmvva 240,24,60\nbuffer_load_dwordx4 v41, s[52:55], s95 offen offset:1024 lds\n"
"Qmvva 84,28,60\nQta 126,45,46848\nQmvva 88,32,60\nQda 68,53,8192\nQmvva 120,36,60\nQta 128,45,41984\nQmvva 152,20,64\nQta 130,45,47104\n"
"Qmvva 184,24,64\nbuffer_load_dwordx4 a[248:251], v60, s[48:51], s94 offen offset:2048 sc1\nQmvva 216,28,64\nQmvva 248,32,64\nbuffer_load_dwordx4 v42, s[52:55], s95 offen offset:2048 lds\nQmvva 116,36,64\ns_waitcnt vmcnt(27)\nds_write_b128 v52, a[28:31] offset:33792\n"
"s_waitcnt lgkmcnt(3)\nQmvva 96,20,68\nQmvva 128,24,68\nQmvva 160,28,68\nQmvva 192,32,68\nQda 72,53,12288\nv_add_u32 v75, 1, v75\nQmvva 224,36,68\n"
"s_waitcnt vmcnt(34)\nds_write_b32 v73, v75\ns_waitcnt lgkmcnt(3)\nQmvaa 92,112,8\nbuffer_load_dwordx4 v43, s[52:55], s95 offen offset:3072 lds\ns_mov_b32 s94, 602112\nQmvaa 124,116,8\nbuffer_load_dwordx4 a[252:255], v60, s[48:51], s94 offen offset:0 sc1\n"
"Qmvaa 156,120,8\nQta 132,47,40960\nQmvaa 188,124,8\nQta 134,47,46080\nQmvaa 220,128,8\ns_waitcnt vmcnt(27)\nds_write_b128 v52, a[32:35] offset:37888\nQmvaa 252,112,12\n"
"Qmvaa 100,116,12\nQta 136,47,41216\nQmvaa 132,120,12\nds_read_b32 v69, v74\nQmvaa 164,124,12\nQda 76,53,16384\ns_add_u32 m0, s96, 28672\nQmvaa 196,128,12\n"
"buffer_load_dwordx4 v44, s[52:55], s95 offen lds\nQmvaa 228,112,16\nQta 138,47,46336\nQmvaa 108,116,16\ns_waitcnt lgkmcnt(2)\nv_cmp_gt_u32 vcc, v75, v69\nQmvaa 140,120,16\ns_cbranch_vccnz .Lso44\n"
".Lro44:\nQmvaa 172,124,16\nbuffer_load_dwordx4 a[168:171], v60, s[48:51], s94 offen offset:1024 sc1\nQmvaa 204,128,16\ns_waitcnt vmcnt(26)\nds_write_b128 v52, a[36:39] offset:41984\nQmvaa 236,112,20\nQmvaa 80,116,20\n"
".Lpg20:\ns_cbranch_execz .Lpg24\nQta 140,47,41472\nQmvaa 112,120,20\nQmvaa 144,124,20\nQda 80,53,20480\nQmvaa 176,128,20\nQta 142,47,46592\n"
"Qmvaa 208,112,24\nQta 144,47,41728\nQmvaa 240,116,24\nbuffer_load_dwordx4 a[172:175], v60, s[48:51], s94 offen offset:2048 sc1\nQmvaa 84,120,24\nQta 146,47,46848\nQmvaa 88,124,24\nQta 148,47,41984\n"
"Qmvaa 120,128,24\ns_waitcnt vmcnt(25)\nds_write_b128 v52, a[40:43] offset:46080\nQmvaa 152,112,28\nQmvaa 184,116,28\nQta 150,47,47104\nQmvaa 216,120,28\nQmvaa 248,124,28\n"
"Qda 48,53,25600\nQmvaa 116,128,28\nQmvaa 96,112,32\nQmvaa 128,116,32\nbuffer_load_dwordx4 a[0:3], v60, s[48:51], s94 offen offset:3072 sc1\nQmvaa 160,120,32\nQmvaa 192,124,32\nv_add_u32 v75, 1, v75\n"
"Qmvaa 224,128,32\nds_write_b32 v73, v75\ns_waitcnt lgkmcnt(2)\nQmvaa 92,132,72\ns_waitcnt vmcnt(25)\nds_write_b128 v52, a[44:47] offset:50176\nQmvaa 124,136,72\nQmvaa 156,140,72\n"
"Qtv 0,46,0\nQmvaa 188,144,72\nQda 52,53,29696\nQmvaa 220,148,72\nQtv 2,46,5120\nQmvaa 252,132,76\nQtv 4,46,256\ns_mov_b32 s94, 606208\n"
"Qmvaa 100,136,76\nbuffer_load_dwordx4 a[4:7], v60, s[48:51], s94 offen offset:0 sc1\nQmvaa 132,140,76\nds_read_b32 v69, v74\nQmvaa 164,144,76\nQtv 6,46,5376\nQmvaa 196,148,76\ns_waitcnt vmcnt(25)\n"
"ds_write_b128 v52, a[208:211] offset:0\ns_waitcnt lgkmcnt(15)\nQmvaa 228,132,80\nQmvaa 108,136,80\ns_waitcnt lgkmcnt(2)\nv_cmp_gt_u32 vcc, v75, v69\nQmvaa 140,140,80\ns_cbranch_vccnz .Lso45\n"
".Lro45:\nQmvaa 172,144,80\nQda 56,53,33792\ns_add_u32 m0, s96, 45056\ns_mov_b32 s95, 2375680\nQmvaa 204,148,80\nbuffer_load_dwordx4 v40, s[52:55], s95 offen lds\nQmvaa 236,132,48\n"
"Qtv 8,46,512\nQmvaa 80,136,48\nbuffer_load_dwordx4 a[8:11], v60, s[48:51], s94 offen offset:1024 sc1\nQmvaa 112,140,48\nQtv 10,46,5632\nQmvaa 144,144,48\nQtv 12,46,768\nQmvaa 176,148,48\n"
"s_waitcnt vmcnt(26)\nds_write_b128 v52, a[212:215] offset:4096\ns_waitcnt lgkmcnt(4)\nQmvaa 208,132,52\nQmvaa 240,136,52\nbuffer_load_dwordx4 v41, s[52:55], s95 offen offset:1024 lds\nQmvaa 84,140,52\nQtv 14,46,5888\n"
"Qmvaa 88,144,52\nQda 60,53,37888\nQmvaa 120,148,52\nQtv 16,46,1024\nQmvaa 152,132,56\nQtv 18,46,6144\nQmvaa 184,136,56\nbuffer_load_dwordx4 a[12:15], v60, s[48:51], s94 offen offset:2048 sc1\n"
"Qmvaa 216,140,56\nQmvaa 248,144,56\nbuffer_load_dwordx4 v42, s[52:55], s95 offen offset:2048 lds\nQmvaa 116,148,56\ns_waitcnt vmcnt(27)\nds_write_b128 v52, a[216:219] offset:8192\ns_waitcnt lgkmcnt(3)\nQmvaa 96,132,60\n"
"Qmvaa 128,136,60\nQmvaa 160,140,60\nQmvaa 192,144,60\nQda 64,53,41984\nv_add_u32 v75, 1, v75\nQmvaa 224,148,60\ns_waitcnt vmcnt(33)\nds_write_b32 v73, v75\n"
"s_waitcnt lgkmcnt(3)\nQmvva 92,0,36\nbuffer_load_dword v78, v76, s[24:27], 0 offen sc1\ns_mov_b32 s94, 716800\nQmvva 124,4,36\nbuffer_load_dwordx4 a[16:19], v60, s[48:51], s94 offen offset:0 sc1\nQmvva 156,8,36\nbuffer_load_dwordx4 v43, s[52:55], s95 offen offset:3072 lds\n"
"Qmvva 188,12,36\nQtv 20,51,0\nQmvva 220,16,36\ns_waitcnt vmcnt(28)\nds_write_b128 v52, a[220:223] offset:12288\nQmvva 252,0,40\nQmvva 100,4,40\nQtv 22,51,5120\n"
"Qmvva 132,8,40\nds_read_b32 v69, v74\nQmvva 164,12,40\nQda 68,53,46080\ns_add_u32 m0, s96, 49152\nQmvva 196,16,40\nbuffer_load_dwordx4 v44, s[52:55], s95 offen lds\nQmvva 228,0,44\n"
"Qtv 24,51,256\nQmvva 108,4,44\ns_waitcnt lgkmcnt(2)\nv_cmp_gt_u32 vcc, v75, v69\nQmvva 140,8,44\ns_cbranch_vccnz .Lso46\n.Lro46:\nQmvva 172,12,44\n"
"buffer_load_dwordx4 a[20:23], v60, s[48:51], s94 offen offset:1024 sc1\nQmvva 204,16,44\ns_waitcnt vmcnt(27)\nds_write_b128 v52, a[224:227] offset:16384\nQmvva 236,0,208\nQmvva 80,4,208\nQtv 26,51,5376\nQmvva 112,8,208\n"
"Qtv 28,51,512\nQmvva 144,12,208\nQda 72,53,50176\nQmvva 176,16,208\nQtv 30,51,5632\nQmvva 208,0,212\nQtv 32,51,768\nQmvva 240,4,212\n"
"buffer_load_dwordx4 a[24:27], v60, s[48:51], s94 offen offset:2048 sc1\nQmvva 84,8,212\nQtv 34,51,5888\nQmvva 88,12,212\nQtv 36,51,1024\nQmvva 120,16,212\ns_waitcnt vmcnt(26)\nds_write_b128 v52, a[228:231] offset:20480\n"
"Qmvva 152,0,216\nQmvva 184,4,216\nQtv 38,51,6144\nQmvva 216,8,216\nQmvva 248,12,216\nQda 76,53,0\nQmvva 116,16,216\nQmvva 96,0,220\n"
"Qmvva 128,4,220\nbuffer_load_dwordx4 a[28:31], v60, s[48:51], s94 offen offset:3072 sc1\nQmvva 160,8,220\nQmvva 192,12,220\nv_add_u32 v75, 1, v75\nQmvva 224,16,220\nds_write_b32 v73, v75\ns_waitcnt lgkmcnt(2)\n"
"Qmvva 92,20,64\ns_waitcnt vmcnt(26)\nds_write_b128 v52, a[232:235] offset:25600\nQmvva 124,24,64\nQmvva 156,28,64\nQta 112,46,20480\nQmvva 188,32,64\nQda 80,53,4096\n"
"Qmvva 220,36,64\nQta 114,46,25600\nQmvva 252,20,68\nQta 116,46,20736\ns_mov_b32 s94, 720896\nQmvva 100,24,68\nbuffer_load_dwordx4 a[32:35], v60, s[48:51], s94 offen offset:0 sc1\nQmvva 132,28,68\n"
"ds_read_b32 v69, v74\nQmvva 164,32,68\nQta 118,46,25856\nQmvva 196,36,68\ns_waitcnt vmcnt(26)\nds_write_b128 v52, a[236:239] offset:29696\ns_waitcnt lgkmcnt(15)\nQmvva 228,20,72\n"
"Qmvva 108,24,72\ns_waitcnt lgkmcnt(2)\nv_cmp_gt_u32 vcc, v75, v69\nQmvva 140,28,72\ns_cbranch_vccnz .Lso47\n.Lro47:\nQmvva 172,32,72\nQda 48,53,8192\n"
"s_add_u32 m0, s96, 65536\ns_mov_b32 s95, 1433600\nQmvva 204,36,72\nbuffer_load_dwordx4 v40, s[52:55], s95 offen lds\nQmvva 236,20,76\nQta 120,46,20992\nQmvva 80,24,76\nbuffer_load_dwordx4 a[36:39], v60, s[48:51], s94 offen offset:1024 sc1\n"
"Qmvva 112,28,76\nQta 122,46,26112\nQmvva 144,32,76\nQta 124,46,21248\nQmvva 176,36,76\ns_waitcnt vmcnt(27)\nds_write_b128 v52, a[240:243] offset:33792\ns_waitcnt lgkmcnt(4)\n"
"Qmvva 208,20,80\nQmvva 240,24,80\nbuffer_load_dwordx4 v41, s[52:55], s95 offen offset:1024 lds\nQmvva 84,28,80\nQta 126,46,26368\nQmvva 88,32,80\nQda 52,53,12288\nQmvva 120,36,80\n"
"Qta 128,46,21504\nQmvva 152,20,48\nQta 130,46,26624\nQmvva 184,24,48\nbuffer_load_dwordx4 a[40:43], v60, s[48:51], s94 offen offset:2048 sc1\nQmvva 216,28,48\nQmvva 248,32,48\nbuffer_load_dwordx4 v42, s[52:55], s95 offen offset:2048 lds\n"
"Qmvva 116,36,48\ns_waitcnt vmcnt(28)\nds_write_b128 v52, a[244:247] offset:37888\ns_waitcnt lgkmcnt(3)\nQmvva 96,20,52\nQmvva 128,24,52\nQmvva 160,28,52\nQmvva 192,32,52\n"
"Qda 56,53,16384\nv_add_u32 v75, 1, v75\nQmvva 224,36,52\ns_waitcnt vmcnt(34)\nds_write_b32 v73, v75\ns_waitcnt lgkmcnt(3)\nQmvaa 92,112,224\ns_mov_b32 s94, 831488\n"
"s_waitcnt vmcnt(12)\nv_sub_u32 v66, v78, v65\nv_cmp_gt_u32 vcc, 5, v66\ns_and_b64 vcc, vcc, s[28:29]\ns_cbranch_scc1 .Lf5_w\n.Lf5_r:\nQmvaa 124,116,224\nbuffer_load_dwordx4 a[44:47], v60, s[48:51], s94 offen offset:0 sc1\n"
"Qmvaa 156,120,224\nbuffer_load_dwordx4 v43, s[52:55], s95 offen offset:3072 lds\nQmvaa 188,124,224\nQta 132,51,20480\nQmvaa 220,128,224\ns_waitcnt vmcnt(28)\nds_write_b128 v52, a[248:251] offset:41984\nQmvaa 252,112,228\n"
"Qmvaa 100,116,228\nQta 134,51,25600\nQmvaa 132,120,228\nds_read_b32 v69, v74\nQmvaa 164,124,228\nQda 60,53,20480\ns_add_u32 m0, s96, 69632\nQmvaa 196,128,228\n"
"buffer_load_dwordx4 v44, s[52:55], s95 offen lds\nQmvaa 228,112,232\nQta 136,51,20736\nQmvaa 108,116,232\ns_waitcnt lgkmcnt(2)\nv_cmp_gt_u32 vcc, v75, v69\nQmvaa 140,120,232\ns_cbranch_vccnz .Lso48\n"
".Lro48:\nQmvaa 172,124,232\nbuffer_load_dwordx4 a[208:211], v60, s[48:51], s94 offen offset:1024 sc1\nQmvaa 204,128,232\ns_waitcnt vmcnt(27)\nds_write_b128 v52, a[252:255] offset:46080\nQmvaa 236,112,236\nQmvaa 80,116,236\n"
"Qta 138,51,25856\nQmvaa 112,120,236\nQta 140,51,20992\nQmvaa 144,124,236\nQda 64,53,25600\nQmvaa 176,128,236\nQta 142,51,26112\nQmvaa 208,112,240\n"
"Qta 144,51,21248\nQmvaa 240,116,240\nbuffer_load_dwordx4 a[212:215], v60, s[48:51], s94 offen offset:2048 sc1\nQmvaa 84,120,240\nQta 146,51,26368\nQmvaa 88,124,240\nQta 148,51,21504\nQmvaa 120,128,240\n"
"s_waitcnt vmcnt(26)\nds_write_b128 v52, a[168:171] offset:50176\nQmvaa 152,112,244\nQmvaa 184,116,244\nQta 150,51,26624\nQmvaa 216,120,244\nQmvaa 248,124,244\nQda 68,53,29696\n"
"Qmvaa 116,128,244\nQmvaa 96,112,248\nQmvaa 128,116,248\nbuffer_load_dwordx4 a[216:219], v60, s[48:51], s94 offen offset:3072 sc1\nQmvaa 160,120,248\nQmvaa 192,124,248\nv_add_u32 v75, 1, v75\nQmvaa 224,128,248\n"
"ds_write_b32 v73, v75\ns_lshr_b32 s68, %19, 2\ns_xor_b32 s68, s68, s92\ns_cbranch_scc1 .Lrole1\ns_waitcnt lgkmcnt(2)\nQmvaa 92,132,56\ns_waitcnt vmcnt(26)\nds_write_b128 v52, a[172:175] offset:0\n"
"Qmvaa 124,136,56\nQmvaa 156,140,56\nQtv 0,45,0\nQmvaa 188,144,56\nQda 72,53,33792\nQmvaa 220,148,56\nQtv 2,45,5120\nQmvaa 252,132,60\n"
"Qtv 4,45,256\ns_mov_b32 s94, 835584\nQmvaa 100,136,60\nbuffer_load_dwordx4 a[220:223], v60, s[48:51], s94 offen offset:0 sc1\nQmvaa 132,140,60\nds_read_b32 v69, v74\nQmvaa 164,144,60\nQtv 6,45,5376\n"
"Qmvaa 196,148,60\ns_waitcnt vmcnt(26)\nds_write_b128 v52, a[0:3] offset:4096\ns_waitcnt lgkmcnt(15)\nQmvaa 228,132,64\nQmvaa 108,136,64\ns_waitcnt lgkmcnt(2)\nv_cmp_gt_u32 vcc, v75, v69\n"
"Qmvaa 140,140,64\ns_cbranch_vccnz .Lso49r0\n.Lro49r0:\nQmvaa 172,144,64\nQda 76,53,37888\ns_add_u32 m0, s96, 86016\ns_mov_b32 s95, 1761280\nQmvaa 204,148,64\n"
"buffer_load_dwordx4 v40, s[52:55], s95 offen lds\nQmvaa 236,132,68\nQtv 8,45,512\nQmvaa 80,136,68\nbuffer_load_dwordx4 a[224:227], v60, s[48:51], s94 offen offset:1024 sc1\nQmvaa 112,140,68\nQtv 10,45,5632\nQmvaa 144,144,68\n"
"Qtv 12,45,768\nQmvaa 176,148,68\ns_waitcnt vmcnt(27)\nds_write_b128 v52, a[4:7] offset:8192\ns_waitcnt lgkmcnt(4)\nQmvaa 208,132,72\nQmvaa 240,136,72\nbuffer_load_dwordx4 v41, s[52:55], s95 offen offset:1024 lds\n"
"Qmvaa 84,140,72\nQtv 14,45,5888\nQmvaa 88,144,72\nQda 80,53,41984\nQmvaa 120,148,72\nQtv 16,45,1024\nQmvaa 152,132,76\nQtv 18,45,6144\n"
"Qmvaa 184,136,76\nbuffer_load_dwordx4 a[228:231], v60, s[48:51], s94 offen offset:2048 sc1\nQmvaa 216,140,76\nQmvaa 248,144,76\nbuffer_load_dwordx4 v42, s[52:55], s95 offen offset:2048 lds\nQmvaa 116,148,76\ns_waitcnt vmcnt(28)\nds_write_b128 v52, a[8:11] offset:12288\n"
"s_waitcnt lgkmcnt(3)\nQmvaa 96,132,80\nQmvaa 128,136,80\nQmvaa 160,140,80\nQmvaa 192,144,80\nQda 48,53,46080\nv_add_u32 v75, 1, v75\nQmvaa 224,148,80\n"
"s_waitcnt vmcnt(34)\nds_write_b32 v73, v75\ns_waitcnt lgkmcnt(3)\nQmvva 92,0,252\nbuffer_load_dwordx4 v43, s[52:55], s95 offen offset:3072 lds\ns_mov_b32 s94, 505856\nQmvva 124,4,252\nbuffer_load_dwordx4 a[232:235], v60, s[48:51], s94 offen offset:0 sc1\n"
"Qmvva 156,8,252\nQtv 20,47,0\nQmvva 188,12,252\nQtv 22,47,5120\nQmvva 220,16,252\ns_waitcnt vmcnt(28)\nds_write_b128 v52, a[12:15] offset:16384\nQmvva 252,0,168\n"
"Qmvva 100,4,168\nQtv 24,47,256\nQmvva 132,8,168\nds_read_b32 v69, v74\nQmvva 164,12,168\nQda 52,53,50176\ns_add_u32 m0, s96, 90112\nQmvva 196,16,168\n"
"buffer_load_dwordx4 v44, s[52:55], s95 offen lds\nQmvva 228,0,172\nQtv 26,47,5376\nQmvva 108,4,172\ns_waitcnt lgkmcnt(2)\nv_cmp_gt_u32 vcc, v75, v69\nQmvva 140,8,172\ns_cbranch_vccnz .Lso50r0\n"
".Lro50r0:\nQmvva 172,12,172\nbuffer_load_dwordx4 a[236:239], v60, s[48:51], s94 offen offset:1024 sc1\nQmvva 204,16,172\ns_waitcnt vmcnt(27)\nds_write_b128 v52, a[16:19] offset:20480\nQmvva 236,0,0\nQmvva 80,4,0\n"
"Qtv 28,47,512\nQmvva 112,8,0\nQmvva 144,12,0\nQda 56,53,0\nQmvva 176,16,0\nQtv 30,47,5632\nQmvva 208,0,4\nQtv 32,47,768\n"
"Qmvva 240,4,4\nbuffer_load_dwordx4 a[240:243], v60, s[48:51], s94 offen offset:2048 sc1\nQmvva 84,8,4\nQtv 34,47,5888\nQmvva 88,12,4\nQtv 36,47,1024\nQmvva 120,16,4\ns_waitcnt vmcnt(25)\n"
"ds_write_b128 v52, a[20:23] offset:25600\nQmvva 152,0,8\nQmvva 184,4,8\nQtv 38,47,6144\nQmvva 216,8,8\nQmvva 248,12,8\nQda 60,53,4096\nQmvva 116,16,8\n"
"Qmvva 96,0,12\ns_mov_b32 s94, 620544\nQmvva 128,4,12\nbuffer_load_dwordx4 a[244:247], v60, s[48:51], s94 offen offset:0 sc1\nQmvva 160,8,12\nQmvva 192,12,12\nv_add_u32 v75, 1, v75\nQmvva 224,16,12\n"
"ds_write_b32 v73, v75\ns_waitcnt lgkmcnt(2)\nQmvva 92,20,48\ns_waitcnt vmcnt(25)\nds_write_b128 v52, a[24:27] offset:29696\nQmvva 124,24,48\nQmvva 156,28,48\nQta 112,45,20480\n"
"Qmvva 188,32,48\nQda 64,53,8192\nQmvva 220,36,48\nQta 114,45,25600\nQmvva 252,20,52\nQta 116,45,20736\nQmvva 100,24,52\nbuffer_load_dwordx4 a[248:251], v60, s[48:51], s94 offen offset:1024 sc1\n"
"Qmvva 132,28,52\nds_read_b32 v69, v74\nQmvva 164,32,52\nQta 118,45,25856\nQmvva 196,36,52\ns_waitcnt vmcnt(25)\nds_write_b128 v52, a[28:31] offset:33792\ns_waitcnt lgkmcnt(15)\n"
"Qmvva 228,20,56\nQmvva 108,24,56\ns_waitcnt lgkmcnt(2)\nv_cmp_gt_u32 vcc, v75, v69\nQmvva 140,28,56\ns_cbranch_vccnz .Lso51r0\n.Lro51r0:\nQmvva 172,32,56\n"
"Qda 68,53,12288\ns_add_u32 m0, s96, 4096\ns_mov_b32 s95, 2088960\nQmvva 204,36,56\nbuffer_load_dwordx4 v40, s[52:55], s95 offen lds\nQmvva 236,20,60\nQta 120,45,20992\nQmvva 80,24,60\n"
"buffer_load_dwordx4 a[252:255], v60, s[48:51], s94 offen offset:2048 sc1\nQmvva 112,28,60\nQta 122,45,26112\nQmvva 144,32,60\nQta 124,45,21248\nQmvva 176,36,60\ns_waitcnt vmcnt(26)\nds_write_b128 v52, a[32:35] offset:37888\n"
"s_waitcnt lgkmcnt(4)\nQmvva 208,20,64\nQmvva 240,24,64\nbuffer_load_dwordx4 v41, s[52:55], s95 offen offset:1024 lds\nQmvva 84,28,64\nQta 126,45,26368\nQmvva 88,32,64\nQda 72,53,16384\n"
"Qmvva 120,36,64\nQta 128,45,21504\nQmvva 152,20,68\nQta 130,45,26624\ns_mov_b32 s94, 735232\nQmvva 184,24,68\nbuffer_load_dwordx4 a[168:171], v60, s[48:51], s94 offen offset:0 sc1\nQmvva 216,28,68\n"
"Qmvva 248,32,68\nbuffer_load_dwordx4 v42, s[52:55], s95 offen offset:2048 lds\nQmvva 116,36,68\ns_waitcnt vmcnt(27)\nds_write_b128 v52, a[36:39] offset:41984\ns_waitcnt lgkmcnt(3)\nQmvva 96,20,72\nQmvva 128,24,72\n"
"Qmvva 160,28,72\nQmvva 192,32,72\nQda 76,53,20480\nv_add_u32 v75, 1, v75\n.Lpg21:\ns_cbranch_execz .Lpg25\nQmvva 224,36,72\ns_waitcnt vmcnt(33)\n"
"ds_write_b32 v73, v75\ns_waitcnt lgkmcnt(3)\nQmvaa 92,112,16\nbuffer_load_dwordx4 v43, s[52:55], s95 offen offset:3072 lds\nQmvaa 124,116,16\nbuffer_load_dwordx4 a[172:175], v60, s[48:51], s94 offen offset:1024 sc1\nQmvaa 156,120,16\nQta 132,47,20480\n"
"Qmvaa 188,124,16\nQta 134,47,25600\nQmvaa 220,128,16\ns_waitcnt vmcnt(27)\nds_write_b128 v52, a[40:43] offset:46080\nQmvaa 252,112,20\nQmvaa 100,116,20\nQta 136,47,20736\n"
"Qmvaa 132,120,20\nds_read_b32 v69, v74\nQmvaa 164,124,20\nQda 80,53,25600\ns_add_u32 m0, s96, 8192\nQmvaa 196,128,20\nbuffer_load_dwordx4 v44, s[52:55], s95 offen lds\nQmvaa 228,112,24\n"
"Qta 138,47,25856\nQmvaa 108,116,24\ns_waitcnt lgkmcnt(2)\nv_cmp_gt_u32 vcc, v75, v69\nQmvaa 140,120,24\ns_cbranch_vccnz .Lso52r0\n.Lro52r0:\nQmvaa 172,124,24\n"
"buffer_load_dwordx4 a[0:3], v60, s[48:51], s94 offen offset:2048 sc1\nQmvaa 204,128,24\ns_waitcnt vmcnt(27)\nds_write_b128 v52, a[44:47] offset:50176\nQmvaa 236,112,28\nQmvaa 80,116,28\nQta 140,47,20992\nQmvaa 112,120,28\n"
"Qmvaa 144,124,28\nQda 48,53,29696\nQmvaa 176,128,28\nQta 142,47,26112\nQmvaa 208,112,32\nQta 144,47,21248\ns_mov_b32 s94, 849920\nQmvaa 240,116,32\n"
"buffer_load_dwordx4 a[4:7], v60, s[48:51], s94 offen offset:0 sc1\nQmvaa 84,120,32\nQta 146,47,26368\nQmvaa 88,124,32\nQta 148,47,21504\nQmvaa 120,128,32\ns_waitcnt vmcnt(25)\nds_write_b128 v52, a[208:211] offset:0\n"
"Qmvaa 152,112,36\nQmvaa 184,116,36\nQta 150,47,26624\nQmvaa 216,120,36\nQmvaa 248,124,36\nQda 52,53,33792\nQmvaa 116,128,36\nQmvaa 96,112,40\n"
"Qmvaa 128,116,40\nbuffer_load_dwordx4 a[8:11], v60, s[48:51], s94 offen offset:1024 sc1\nQmvaa 160,120,40\nQmvaa 192,124,40\nv_add_u32 v75, 1, v75\nQmvaa 224,128,40\nds_write_b32 v73, v75\ns_waitcnt lgkmcnt(2)\n"
"Qmvaa 92,132,76\ns_waitcnt vmcnt(25)\nds_write_b128 v52, a[212:215] offset:4096\nQmvaa 124,136,76\nQmvaa 156,140,76\nQtv 0,45,40960\nQmvaa 188,144,76\nQda 56,53,37888\n"
"Qmvaa 220,148,76\nQtv 2,45,46080\nQmvaa 252,132,80\nQtv 4,45,41216\nQmvaa 100,136,80\nbuffer_load_dwordx4 a[12:15], v60, s[48:51], s94 offen offset:2048 sc1\nQmvaa 132,140,80\nds_read_b32 v69, v74\n"
"Qmvaa 164,144,80\nQtv 6,45,46336\nQmvaa 196,148,80\ns_waitcnt vmcnt(25)\nds_write_b128 v52, a[216:219] offset:8192\ns_waitcnt lgkmcnt(15)\nQmvaa 228,132,48\nQmvaa 108,136,48\n"
"s_waitcnt lgkmcnt(2)\nv_cmp_gt_u32 vcc, v75, v69\nQmvaa 140,140,48\ns_cbranch_vccnz .Lso53r0\n.Lro53r0:\nQmvaa 172,144,48\nQda 60,53,41984\ns_add_u32 m0, s96, 24576\n"
"s_mov_b32 s95, 2416640\nQmvaa 204,148,48\nbuffer_load_dwordx4 v40, s[52:55], s95 offen lds\nQmvaa 236,132,52\nQtv 8,45,41472\ns_mov_b32 s94, 501760\nQmvaa 80,136,52\nbuffer_load_dwordx4 a[16:19], v60, s[48:51], s94 offen offset:0 sc1\n"
"Qmvaa 112,140,52\nQtv 10,45,46592\nQmvaa 144,144,52\nQtv 12,45,41728\nQmvaa 176,148,52\ns_waitcnt vmcnt(26)\nds_write_b128 v52, a[220:223] offset:12288\ns_waitcnt lgkmcnt(4)\n"
"Qmvaa 208,132,56\nQmvaa 240,136,56\nbuffer_load_dwordx4 v41, s[52:55], s95 offen offset:1024 lds\nQmvaa 84,140,56\nQtv 14,45,46848\nQmvaa 88,144,56\nQda 64,53,46080\nQmvaa 120,148,56\n"
"Qtv 16,45,41984\nQmvaa 152,132,60\nQtv 18,45,47104\nQmvaa 184,136,60\nbuffer_load_dwordx4 a[20:23], v60, s[48:51], s94 offen offset:1024 sc1\nQmvaa 216,140,60\nQmvaa 248,144,60\nbuffer_load_dwordx4 v42, s[52:55], s95 offen offset:2048 lds\n"
"Qmvaa 116,148,60\ns_waitcnt vmcnt(27)\nds_write_b128 v52, a[224:227] offset:16384\ns_waitcnt lgkmcnt(3)\nQmvaa 96,132,64\nQmvaa 128,136,64\nQmvaa 160,140,64\nQmvaa 192,144,64\n"
"Qda 68,53,50176\nv_add_u32 v75, 1, v75\nQmvaa 224,148,64\ns_waitcnt vmcnt(33)\nds_write_b32 v73, v75\ns_waitcnt lgkmcnt(3)\nQmvva 92,0,44\nbuffer_load_dwordx4 v43, s[52:55], s95 offen offset:3072 lds\n"
"Qmvva 124,4,44\nbuffer_load_dwordx4 a[24:27], v60, s[48:51], s94 offen offset:2048 sc1\nQmvva 156,8,44\nQtv 20,47,40960\nQmvva 188,12,44\nQtv 22,47,46080\nQmvva 220,16,44\ns_waitcnt vmcnt(27)\n"
"ds_write_b128 v52, a[228:231] offset:20480\nQmvva 252,0,208\nQmvva 100,4,208\nQtv 24,47,41216\nQmvva 132,8,208\nds_read_b32 v69, v74\nQmvva 164,12,208\nQda 72,53,0\n"
"s_add_u32 m0, s96, 28672\nQmvva 196,16,208\nbuffer_load_dwordx4 v44, s[52:55], s95 offen lds\nQmvva 228,0,212\nQtv 26,47,46336\nQmvva 108,4,212\ns_waitcnt lgkmcnt(2)\nv_cmp_gt_u32 vcc, v75, v69\n"
"Qmvva 140,8,212\ns_cbranch_vccnz .Lso54r0\n.Lro54r0:\nQmvva 172,12,212\nbuffer_load_dwordx4 a[28:31], v60, s[48:51], s94 offen offset:3072 sc1\nQmvva 204,16,212\ns_waitcnt vmcnt(26)\nds_write_b128 v52, a[232:235] offset:25600\n"
"Qmvva 236,0,216\nQmvva 80,4,216\nQtv 28,47,41472\nQmvva 112,8,216\nQmvva 144,12,216\nQda 76,53,4096\nQmvva 176,16,216\nQtv 30,47,46592\n"
"Qmvva 208,0,220\nQtv 32,47,41728\ns_mov_b32 s94, 616448\nQmvva 240,4,220\nbuffer_load_dwordx4 a[32:35], v60, s[48:51], s94 offen offset:0 sc1\nQmvva 84,8,220\nQtv 34,47,46848\nQmvva 88,12,220\n"
"Qtv 36,47,41984\nQmvva 120,16,220\ns_waitcnt vmcnt(25)\nds_write_b128 v52, a[236:239] offset:29696\nQmvva 152,0,224\nQmvva 184,4,224\nQtv 38,47,47104\nQmvva 216,8,224\n"
"Qmvva 248,12,224\nQda 80,53,8192\nQmvva 116,16,224\nQmvva 96,0,228\nQmvva 128,4,228\nbuffer_load_dwordx4 a[36:39], v60, s[48:51], s94 offen offset:1024 sc1\nQmvva 160,8,228\nQmvva 192,12,228\n"
"v_add_u32 v75, 1, v75\nQmvva 224,16,228\ns_waitcnt vmcnt(27)\nds_write_b32 v73, v75\ns_waitcnt lgkmcnt(2)\nQmvva 92,20,68\ns_waitcnt vmcnt(25)\nds_write_b128 v52, a[240:243] offset:33792\n"
"Qmvva 124,24,68\nQmvva 156,28,68\nQta 112,46,0\nQmvva 188,32,68\nQda 48,53,12288\nQmvva 220,36,68\nQta 114,46,5120\nQmvva 252,20,72\n"
"Qta 116,46,256\nQmvva 100,24,72\nbuffer_load_dwordx4 a[40:43], v60, s[48:51], s94 offen offset:2048 sc1\nQmvva 132,28,72\nds_read_b32 v69, v74\nQmvva 164,32,72\nQmvva 196,36,72\ns_waitcnt vmcnt(25)\n"
"ds_write_b128 v52, a[244:247] offset:37888\ns_waitcnt lgkmcnt(15)\nQmvva 228,20,76\nQmvva 108,24,76\ns_waitcnt lgkmcnt(1)\nv_cmp_gt_u32 vcc, v75, v69\nQmvva 140,28,76\ns_cbranch_vccnz .Lso55r0\n"
".Lro55r0:\nQmvva 172,32,76\nQda 52,53,16384\nQmvva 204,36,76\nQta 118,46,5376\nQmvva 236,20,80\nQta 120,46,512\nQmvva 80,24,80\n"
"buffer_load_dwordx4 a[44:47], v60, s[48:51], s94 offen offset:3072 sc1\nQmvva 112,28,80\nQmvva 144,32,80\nQta 122,46,5632\nQmvva 176,36,80\ns_waitcnt vmcnt(25)\nds_write_b128 v52, a[248:251] offset:41984\ns_waitcnt lgkmcnt(4)\n"
"Qmvva 208,20,48\nQmvva 240,24,48\nQta 124,46,768\nQmvva 84,28,48\nQta 126,46,5888\nQmvva 88,32,48\nQda 56,53,20480\nQmvva 120,36,48\n"
"Qmvva 152,20,52\nQta 128,46,1024\ns_mov_b32 s94, 731136\nQmvva 184,24,52\nbuffer_load_dwordx4 a[208:211], v60, s[48:51], s94 offen offset:0 sc1\nQmvva 216,28,52\nQta 130,46,6144\nQmvva 248,32,52\n"
"Qta 132,51,0\nQta 134,51,5120\nQmvva 116,36,52\ns_waitcnt vmcnt(24)\nds_write_b128 v52, a[252:255] offset:46080\ns_waitcnt lgkmcnt(5)\nQmvva 96,20,56\nQmvva 128,24,56\n"
"Qta 136,51,256\nQmvva 160,28,56\nQta 138,51,5376\nQmvva 192,32,56\nQda 60,53,25600\nv_add_u32 v75, 1, v75\nQmvva 224,36,56\ns_waitcnt vmcnt(18)\n"
"ds_write_b32 v73, v75\ns_waitcnt lgkmcnt(7)\nQmvaa 208,112,232\nQta 140,51,512\nQmvaa 240,116,232\nbuffer_load_dwordx4 a[212:215], v60, s[48:51], s94 offen offset:1024 sc1\nQmvaa 84,120,232\nQta 142,51,5632\n"
"Qmvaa 88,124,232\nQta 144,51,768\nQmvaa 120,128,232\ns_waitcnt vmcnt(23)\nds_write_b128 v52, a[168:171] offset:50176\nQmvaa 152,112,236\nQmvaa 184,116,236\nQta 146,51,5888\n"
"Qmvaa 216,120,236\nds_read_b32 v69, v74\nQmvaa 248,124,236\nQda 64,53,29696\nQmvaa 116,128,236\nQta 148,51,1024\nQmvaa 96,112,240\nQta 150,51,6144\n"
"Qmvaa 128,116,240\ns_waitcnt lgkmcnt(3)\nv_cmp_gt_u32 vcc, v75, v69\nQmvaa 160,120,240\ns_cbranch_vccnz .Lso56r0\n.Lro56r0:\nQmvaa 192,124,240\nbuffer_load_dwordx4 a[216:219], v60, s[48:51], s94 offen offset:2048 sc1\n"
"Qmvaa 224,128,240\ns_waitcnt vmcnt(21)\nds_write_b128 v52, a[172:175] offset:0\ns_waitcnt lgkmcnt(1)\nQmvaa 208,132,60\nQmvaa 240,136,60\nQtv 0,46,20480\nQtv 2,46,25600\n"
"Qmvaa 84,140,60\nQtv 4,46,20736\nQtv 6,46,25856\nQmvaa 88,144,60\nQda 68,53,33792\nQmvaa 120,148,60\nQtv 8,46,20992\nQmvaa 152,132,64\n"
"Qtv 10,46,26112\nQtv 12,46,21248\nQmvaa 184,136,64\nbuffer_load_dwordx4 a[220:223], v60, s[48:51], s94 offen offset:3072 sc1\nQmvaa 216,140,64\nQtv 14,46,26368\nQtv 16,46,21504\nQmvaa 248,144,64\n"
"Qtv 18,46,26624\nQmvaa 116,148,64\ns_waitcnt vmcnt(20)\nds_write_b128 v52, a[0:3] offset:4096\ns_waitcnt lgkmcnt(7)\nQmvaa 96,132,68\nQmvaa 128,136,68\nQtv 20,51,20480\n"
"Qtv 22,51,25600\nQmvaa 160,140,68\nQtv 24,51,20736\nQmvaa 192,144,68\nQda 72,53,37888\nQmvaa 224,148,68\nQtv 26,51,25856\ns_waitcnt lgkmcnt(6)\n"
"Qmvva 208,0,244\nQtv 28,51,20992\ns_mov_b32 s94, 845824\nQmvva 240,4,244\nbuffer_load_dwordx4 a[224:227], v60, s[48:51], s94 offen offset:0 sc1\nQmvva 84,8,244\nQtv 30,51,26112\nQmvva 88,12,244\n"
"Qtv 32,51,21248\nv_add_u32 v75, 1, v75\nQmvva 120,16,244\ns_waitcnt vmcnt(10)\nds_write_b32 v73, v75\nQmvva 152,0,248\ns_waitcnt vmcnt(20)\nds_write_b128 v52, a[4:7] offset:8192\n"
"Qmvva 184,4,248\nQmvva 216,8,248\nQtv 34,51,26368\nQmvva 248,12,248\nQda 76,53,41984\nQmvva 116,16,248\nQtv 36,51,21504\nQmvva 96,0,252\n"
"Qtv 38,51,26624\nQmvva 128,4,252\nbuffer_load_dwordx4 a[228:231], v60, s[48:51], s94 offen offset:1024 sc1\nQmvva 160,8,252\nds_read_b32 v69, v74\nQmvva 192,12,252\nQta 112,45,0\nQta 114,45,5120\n"
"Qmvva 224,16,252\ns_waitcnt vmcnt(20)\nds_write_b128 v52, a[8:11] offset:12288\ns_waitcnt lgkmcnt(13)\nQmvva 208,20,72\nQmvva 240,24,72\ns_waitcnt lgkmcnt(3)\nv_cmp_gt_u32 vcc, v75, v69\n"
"Qmvva 84,28,72\ns_cbranch_vccnz .Lso57r0\n.Lro57r0:\nQmvva 88,32,72\nQda 80,53,46080\nQmvva 120,36,72\nQta 116,45,256\nQta 118,45,5376\n"
"Qmvva 152,20,76\nQta 120,45,512\nQta 122,45,5632\nQmvva 184,24,76\nbuffer_load_dwordx4 a[232:235], v60, s[48:51], s94 offen offset:2048 sc1\nQmvva 216,28,76\nQta 124,45,768\nQta 126,45,5888\n"
"Qmvva 248,32,76\nQta 128,45,1024\nQta 130,45,6144\nQmvva 116,36,76\ns_waitcnt vmcnt(20)\nds_write_b128 v52, a[12:15] offset:16384\ns_waitcnt lgkmcnt(9)\nQmvva 96,20,80\n"
"Qmvva 128,24,80\nQta 132,47,0\nQta 134,47,5120\nQmvva 160,28,80\nQta 136,47,256\nQmvva 192,32,80\nQda 48,53,50176\nQmvva 224,36,80\n"
"Qta 138,47,5376\ns_waitcnt lgkmcnt(6)\nQmvaa 208,112,168\nQta 140,47,512\nQmvaa 240,116,168\nbuffer_load_dwordx4 a[236:239], v60, s[48:51], s94 offen offset:3072 sc1\nQmvaa 84,120,168\nQta 142,47,5632\n"
"Qmvaa 88,124,168\nQta 144,47,768\nQmvaa 120,128,168\ns_waitcnt vmcnt(19)\nds_write_b128 v52, a[16:19] offset:20480\nQmvaa 152,112,172\nQmvaa 184,116,172\nQta 146,47,5888\n"
"Qmvaa 216,120,172\nQta 148,47,1024\nQmvaa 248,124,172\nQda 52,53,0\nv_add_u32 v75, 1, v75\nQmvaa 116,128,172\nds_write_b32 v73, v75\nQmvaa 96,112,0\n"
"Qta 150,47,6144\nQmvaa 128,116,0\nQtv 0,45,20480\nQtv 2,45,25600\nQmvaa 160,120,0\nQtv 4,45,20736\nQmvaa 192,124,0\nQtv 6,45,25856\n"
"Qtv 8,45,20992\nQmvaa 224,128,0\ns_waitcnt vmcnt(17)\nds_write_b128 v52, a[20:23] offset:25600\ns_waitcnt lgkmcnt(6)\nQmvaa 208,132,48\nQmvaa 240,136,48\nQtv 10,45,26112\n"
"Qmvaa 84,140,48\nds_read_b32 v69, v74\nQmvaa 88,144,48\nQda 56,53,4096\nQmvaa 120,148,48\nQtv 12,45,21248\nQtv 14,45,26368\nQmvaa 152,132,52\n"
"Qtv 16,45,21504\nQmvaa 184,136,52\ns_waitcnt lgkmcnt(4)\nv_cmp_gt_u32 vcc, v75, v69\nQmvaa 216,140,52\ns_cbranch_vccnz .Lso58r0\n.Lro58r0:\nQmvaa 248,144,52\n"
"Qtv 18,45,26624\nQmvaa 116,148,52\ns_waitcnt vmcnt(14)\nds_write_b128 v52, a[24:27] offset:29696\ns_waitcnt lgkmcnt(5)\nQmvaa 96,132,56\nQmvaa 128,136,56\nQtv 20,47,20480\n"
"Qmvaa 160,140,56\nQtv 22,47,25600\nQmvaa 192,144,56\nQda 60,53,8192\nQmvaa 224,148,56\nQtv 24,47,20736\ns_waitcnt lgkmcnt(5)\nQmvva 208,0,4\n"
"Qtv 26,47,25856\nQmvva 240,4,4\nQtv 28,47,20992\nQmvva 84,8,4\nQtv 30,47,26112\nQmvva 88,12,4\nQtv 32,47,21248\nQmvva 120,16,4\n"
"s_waitcnt vmcnt(12)\nds_write_b128 v52, a[28:31] offset:33792\nQmvva 152,0,8\nQmvva 184,4,8\nQtv 34,47,26368\nQmvva 216,8,8\nQtv 36,47,21504\nQmvva 248,12,8\n"
"Qda 64,53,12288\nQmvva 116,16,8\nQtv 38,47,26624\nQmvva 96,0,12\nQta 112,46,0\nQta 114,46,5120\nQmvva 128,4,12\nQta 116,46,256\n"
"Qmvva 160,8,12\nQta 118,46,5376\nQmvva 192,12,12\nQta 120,46,512\nv_add_u32 v75, 1, v75\nQmvva 224,16,12\nds_write_b32 v73, v75\ns_waitcnt lgkmcnt(6)\n"
"Qmvva 208,20,60\ns_waitcnt vmcnt(11)\nds_write_b128 v52, a[32:35] offset:37888\nQmvva 240,24,60\ns_add_u32 s68, s64, 0\nQmvva 84,28,60\nQta 122,46,5632\nQmvva 88,32,60\n"
"Qda 68,53,16384\nQmvva 120,36,60\nQta 124,46,768\nQmvva 152,20,64\nQta 126,46,5888\n.Lpg22:\ns_cbranch_execz .Lpg26\nQmvva 184,24,64\n"
"Qta 128,46,1024\nQk 208,208,209\nQmvva 216,28,64\nds_read_b32 v69, v74\nv_min_u32 v208, -2, v208\nQmvva 248,32,64\nQta 130,46,6144\nQk 209,210,211\n"
"Qmvva 116,36,64\ns_waitcnt vmcnt(10)\nds_write_b128 v52, a[36:39] offset:41984\ns_waitcnt lgkmcnt(6)\nQmvva 96,20,68\nv_min_u32 v209, -2, v209\nQk 210,240,241\nQmvva 128,24,68\n"
"s_waitcnt lgkmcnt(2)\nv_cmp_gt_u32 vcc, v75, v69\nQmvva 160,28,68\ns_cbranch_vccnz .Lso59r0\n.Lro59r0:\nv_min_u32 v210, -2, v210\nQmvva 192,32,68\nQda 72,53,20480\n"
"Qk 211,242,243\nQmvva 224,36,68\nQta 132,51,0\nv_min_u32 v211, -2, v211\ns_waitcnt lgkmcnt(3)\nQmvaa 92,112,16\nQta 134,51,5120\nbuffer_store_dwordx4 v[208:211], v60, s[24:27], s68 offen offset:0 sc1\n"
"Qmvaa 124,116,16\nQta 136,51,256\nQk 84,84,85\nQmvaa 156,120,16\nQta 138,51,5376\nv_min_u32 v84, -2, v84\nQmvaa 188,124,16\nQta 140,51,512\n"
"Qk 85,86,87\nQmvaa 220,128,16\ns_waitcnt vmcnt(10)\nds_write_b128 v52, a[40:43] offset:46080\nQmvaa 252,112,20\nv_min_u32 v85, -2, v85\nQk 86,88,89\nQmvaa 100,116,20\n"
"Qta 142,51,5632\nv_min_u32 v86, -2, v86\nQmvaa 132,120,20\nQta 144,51,768\nQk 87,90,91\nQmvaa 164,124,20\nQda 76,53,25600\nv_min_u32 v87, -2, v87\n"
"Qmvaa 196,128,20\nQta 146,51,5888\nbuffer_store_dwordx4 v[84:87], v60, s[24:27], s68 offen offset:1024 sc1\nQmvaa 228,112,24\nQta 148,51,1024\nQk 120,120,121\nQmvaa 108,116,24\nQta 150,51,6144\n"
"v_min_u32 v120, -2, v120\nQmvaa 140,120,24\nQtv 0,46,20480\nQk 121,122,123\nQmvaa 172,124,24\nQtv 2,46,25600\nv_min_u32 v121, -2, v121\nQmvaa 204,128,24\n"
"s_waitcnt vmcnt(10)\nds_write_b128 v52, a[44:47] offset:50176\nQmvaa 236,112,28\nQk 122,152,153\nv_min_u32 v122, -2, v122\nQmvaa 80,116,28\nQtv 4,46,20736\nQk 123,154,155\n"
"Qmvaa 112,120,28\nQtv 6,46,25856\nv_min_u32 v123, -2, v123\nQmvaa 144,124,28\nQda 80,53,29696\nv_add_u32 v75, 1, v75\nQmvaa 176,128,28\nds_write_b32 v73, v75\n"
"buffer_store_dwordx4 v[120:123], v60, s[24:27], s68 offen offset:2048 sc1\ns_waitcnt lgkmcnt(7)\nQmvaa 92,132,72\nQtv 8,46,20992\nQk 184,184,185\nQmvaa 124,136,72\nQtv 10,46,26112\nv_min_u32 v184, -2, v184\n"
"Qmvaa 156,140,72\nQtv 12,46,21248\nQk 185,186,187\nQmvaa 188,144,72\nQtv 14,46,26368\nv_min_u32 v185, -2, v185\nQmvaa 220,148,72\ns_waitcnt vmcnt(10)\n"
"ds_write_b128 v52, a[208:211] offset:0\nQmvaa 252,132,76\nQk 186,216,217\nv_min_u32 v186, -2, v186\nQmvaa 100,136,76\nQtv 16,46,21504\nQk 187,218,219\nQmvaa 132,140,76\n"
"ds_read_b32 v69, v74\nv_min_u32 v187, -2, v187\nQmvaa 164,144,76\nQda 48,53,33792\nbuffer_store_dwordx4 v[184:187], v60, s[24:27], s68 offen offset:3072 sc1\nQmvaa 196,148,76\nQtv 18,46,26624\nQk 248,248,249\n"
"s_waitcnt lgkmcnt(10)\nQmvaa 228,132,80\nv_min_u32 v248, -2, v248\nQk 249,250,251\nQmvaa 108,136,80\ns_waitcnt lgkmcnt(1)\nv_cmp_gt_u32 vcc, v75, v69\nQmvaa 140,140,80\n"
"s_cbranch_vccnz .Lso60r0\n.Lro60r0:\nv_min_u32 v249, -2, v249\nQmvaa 172,144,80\nQtv 20,51,20480\nQk 250,116,117\nQmvaa 204,148,80\ns_waitcnt vmcnt(10)\n"
"ds_write_b128 v52, a[212:215] offset:4096\nQmvaa 236,132,48\nv_min_u32 v250, -2, v250\nQk 251,118,119\nQmvaa 80,136,48\nQtv 22,51,25600\nv_min_u32 v251, -2, v251\nQmvaa 112,140,48\n"
"Qtv 24,51,20736\ns_add_u32 s68, s64, 4096\nQmvaa 144,144,48\nQda 52,53,37888\nbuffer_store_dwordx4 v[248:251], v60, s[24:27], s68 offen offset:0 sc1\nQmvaa 176,148,48\nQtv 26,51,25856\nQk 96,96,97\n"
"s_waitcnt lgkmcnt(6)\nQmvva 92,0,32\nv_min_u32 v96, -2, v96\nQk 97,98,99\nQmvva 124,4,32\nQtv 28,51,20992\nv_min_u32 v97, -2, v97\nQmvva 156,8,32\n"
"Qtv 30,51,26112\nQk 98,128,129\nQmvva 188,12,32\nQtv 32,51,21248\nv_min_u32 v98, -2, v98\nQmvva 220,16,32\ns_waitcnt vmcnt(10)\nds_write_b128 v52, a[216:219] offset:8192\n"
"Qmvva 252,0,36\nQk 99,130,131\nv_min_u32 v99, -2, v99\nQmvva 100,4,36\nbuffer_store_dwordx4 v[96:99], v60, s[24:27], s68 offen offset:1024 sc1\nQk 160,160,161\nQmvva 132,8,36\nQtv 34,51,26368\n"
"v_min_u32 v160, -2, v160\nQmvva 164,12,36\nQda 56,53,41984\nQk 161,162,163\nQmvva 196,16,36\nQtv 36,51,21504\nv_min_u32 v161, -2, v161\nQmvva 228,0,40\n"
"Qtv 38,51,26624\nQk 162,192,193\nQmvva 108,4,40\nv_min_u32 v162, -2, v162\nQk 163,194,195\nQmvva 140,8,40\nQta 112,45,0\nv_min_u32 v163, -2, v163\n"
"Qmvva 172,12,40\nQta 114,45,5120\nv_add_u32 v75, 1, v75\nQmvva 204,16,40\nds_write_b32 v73, v75\nbuffer_store_dwordx4 v[160:163], v60, s[24:27], s68 offen offset:2048 sc1\nQmvva 236,0,44\ns_waitcnt vmcnt(11)\n"
"ds_write_b128 v52, a[220:223] offset:12288\nQmvva 80,4,44\nQk 224,224,225\nv_min_u32 v224, -2, v224\nQmvva 112,8,44\nQta 116,45,256\nQk 225,226,227\nQmvva 144,12,44\n"
"Qda 60,53,46080\nv_min_u32 v225, -2, v225\nQmvva 176,16,44\nQta 118,45,5376\ns_add_u32 s68, s64, 14336\ns_waitcnt lgkmcnt(7)\nQmvva 92,20,52\nQta 120,45,512\n"
"buffer_store_dwordx4 v[224:227], v60, s[24:27], s68 offen offset:0 sc1\nQmvva 124,24,52\nQta 122,45,5632\nQmvva 156,28,52\nds_read_b32 v69, v74\nQmvva 188,32,52\nQta 124,45,768\nQmvva 220,36,52\n"
"s_waitcnt vmcnt(11)\nds_write_b128 v52, a[224:227] offset:16384\nQmvva 252,20,56\nQmvva 100,24,56\ns_waitcnt lgkmcnt(2)\nv_cmp_gt_u32 vcc, v75, v69\nQmvva 132,28,56\ns_cbranch_vccnz .Lso61r0\n"
".Lro61r0:\nQmvva 164,32,56\nQda 64,53,50176\nQmvva 196,36,56\nQta 126,45,5888\ns_waitcnt lgkmcnt(1)\nQmvva 228,20,60\nQta 128,45,1024\n"
"Qmvva 108,24,60\nQta 130,45,6144\nQmvva 140,28,60\nQta 132,47,0\nQmvva 172,32,60\nQta 134,47,5120\nQmvva 204,36,60\ns_waitcnt vmcnt(10)\n"
"ds_write_b128 v52, a[228:231] offset:20480\nQmvva 236,20,64\nQmvva 80,24,64\nQta 136,47,256\nQmvva 112,28,64\nQta 138,47,5376\nQmvva 144,32,64\nQda 68,53,0\n"
"Qmvva 176,36,64\ns_waitcnt lgkmcnt(6)\nQmvaa 92,112,208\nQta 140,47,512\nQmvaa 124,116,208\nQta 142,47,5632\nQmvaa 156,120,208\nQta 144,47,768\n"
"Qmvaa 188,124,208\nQmvaa 220,128,208\ns_waitcnt vmcnt(9)\nds_write_b128 v52, a[232:235] offset:25600\nQmvaa 252,112,212\nQmvaa 100,116,212\nQta 146,47,5888\nQmvaa 132,120,212\n"
"Qta 148,47,1024\nQmvaa 164,124,212\nQda 72,53,4096\nv_add_u32 v75, 1, v75\nQmvaa 196,128,212\nds_write_b32 v73, v75\nQmvaa 228,112,216\nQta 150,47,6144\n"
"Qmvaa 108,116,216\nQmvaa 140,120,216\nQtv 0,45,20480\nQmvaa 172,124,216\nQtv 2,45,25600\nQmvaa 204,128,216\ns_waitcnt vmcnt(8)\nds_write_b128 v52, a[236:239] offset:29696\n"
"Qmvaa 236,112,220\nQmvaa 80,116,220\nQtv 4,45,20736\nQmvaa 112,120,220\nds_read_b32 v69, v74\nQmvaa 144,124,220\nQda 76,53,8192\nQmvaa 176,128,220\n"
"Qtv 6,45,25856\ns_waitcnt lgkmcnt(15)\nQmvaa 92,132,68\nQmvaa 124,136,68\ns_waitcnt lgkmcnt(2)\nv_cmp_gt_u32 vcc, v75, v69\nQmvaa 156,140,68\ns_cbranch_vccnz .Lso62r0\n"
".Lro62r0:\nQmvaa 188,144,68\nQtv 8,45,20992\nQmvaa 220,148,68\nQtv 10,45,26112\nQmvaa 252,132,72\nQtv 12,45,21248\nQmvaa 100,136,72\n"
"Qmvaa 132,140,72\nQtv 14,45,26368\nQmvaa 164,144,72\nQda 80,53,12288\nQmvaa 196,148,72\nQtv 16,45,21504\ns_waitcnt lgkmcnt(1)\nQmvaa 228,132,76\n"
"Qtv 18,45,26624\nQmvaa 108,136,76\nQmvaa 140,140,76\nQtv 20,47,20480\nQmvaa 172,144,76\nQtv 22,47,25600\nQmvaa 204,148,76\nQmvaa 236,132,80\n"
"Qtv 24,47,20736\nQmvaa 80,136,80\nQmvaa 112,140,80\nQtv 26,47,25856\nQmvaa 144,144,80\nQda 48,53,16384\nQmvaa 176,148,80\nQtv 28,47,20992\n"
"s_waitcnt lgkmcnt(6)\nQmvva 92,0,224\nQmvva 124,4,224\nQtv 30,47,26112\nQmvva 156,8,224\nQmvva 188,12,224\nQtv 32,47,21248\nv_add_u32 v75, 1, v75\n"
"Qmvva 220,16,224\nds_write_b32 v73, v75\nQmvva 252,0,228\nQtv 34,47,26368\nQmvva 100,4,228\nQmvva 132,8,228\nQtv 36,47,21504\nQmvva 164,12,228\n"
"Qda 52,53,20480\nQmvva 196,16,228\nQmvva 228,0,232\nQtv 38,47,26624\nQmvva 108,4,232\nQmvva 140,8,232\nds_read_b32 v69, v74\nQmvva 172,12,232\n"
"Qmvva 204,16,232\nQmvva 236,0,236\nQmvva 80,4,236\ns_waitcnt lgkmcnt(0)\nv_cmp_gt_u32 vcc, v75, v69\nQmvva 112,8,236\ns_cbranch_vccnz .Lso63r0\n.Lro63r0:\n"
"Qmvva 144,12,236\nQda 56,53,25600\nQmvva 176,16,236\ns_waitcnt lgkmcnt(2)\nQmvva 92,20,48\ns_add_u32 s69, s98, 0\nbuffer_load_dwordx4 v[240:243], v60, s[24:27], s69 offen offset:0 sc1\nQmvva 124,24,48\n"
"Qmvva 156,28,48\nbuffer_load_dwordx4 v[88:91], v60, s[24:27], s69 offen offset:1024 sc1\nQmvva 188,32,48\nQmvva 220,36,48\nbuffer_load_dwordx4 v[152:155], v60, s[24:27], s69 offen offset:2048 sc1\nQmvva 252,20,52\nQmvva 100,24,52\nbuffer_load_dwordx4 v[216:219], v60, s[24:27], s69 offen offset:3072 sc1\n"
"Qmvva 132,28,52\nQmvva 164,32,52\nQda 60,53,29696\nQmvva 196,36,52\ns_add_u32 s69, s98, 4096\nbuffer_load_dwordx4 v[116:119], v60, s[24:27], s69 offen offset:0 sc1\ns_waitcnt lgkmcnt(0)\nQmvva 228,20,56\n"
"Qmvva 108,24,56\nbuffer_load_dwordx4 v[128:131], v60, s[24:27], s69 offen offset:1024 sc1\nQmvva 140,28,56\nQmvva 172,32,56\nbuffer_load_dwordx4 v[192:195], v60, s[24:27], s69 offen offset:2048 sc1\nQmvva 204,36,56\nQmvva 236,20,60\ns_add_u32 s69, s98, 14336\n"
"buffer_load_dwordx4 v[148:151], v60, s[24:27], s69 offen offset:0 sc1\nQmvva 80,24,60\nQmvva 112,28,60\nbuffer_load_dwordx4 v[180:183], v60, s[24:27], s69 offen offset:1024 sc1\nQmvva 144,32,60\nQmvva 176,36,60\nbuffer_load_dwordx4 v[104:107], v60, s[24:27], s69 offen offset:2048 sc1\ns_barrier\n"
"s_mov_b32 s97, 0\n.Lrx0:\ns_waitcnt vmcnt(0)\nv_max_u32 v40, v240, v241\nv_max3_u32 v40, v40, v242, v243\nv_max3_u32 v40, v40, v88, v89\nv_max3_u32 v40, v40, v90, v91\nv_max3_u32 v40, v40, v152, v153\n"
"v_max3_u32 v40, v40, v154, v155\nv_max3_u32 v40, v40, v216, v217\nv_max3_u32 v40, v40, v218, v219\nv_max3_u32 v40, v40, v116, v117\nv_max3_u32 v40, v40, v118, v119\nv_max3_u32 v40, v40, v128, v129\nv_max3_u32 v40, v40, v130, v131\nv_max3_u32 v40, v40, v192, v193\n"
"v_max3_u32 v40, v40, v194, v195\nv_max3_u32 v40, v40, v148, v149\nv_max3_u32 v40, v40, v150, v151\nv_max3_u32 v40, v40, v180, v181\nv_max3_u32 v40, v40, v182, v183\nv_max3_u32 v40, v40, v104, v105\nv_max3_u32 v40, v40, v106, v107\nv_cmp_eq_u32 vcc, -1, v40\n"
"s_cmp_eq_u64 vcc, 0\ns_cbranch_scc1 .Lrx0_ok\ns_add_u32 s97, s97, 1\ns_cmp_gt_u32 s97, 1048576\ns_cbranch_scc1 .Lrx0_ok\ns_add_u32 s69, s98, 0\nbuffer_load_dwordx4 v[240:243], v60, s[24:27], s69 offen offset:0 sc1\nbuffer_load_dwordx4 v[88:91], v60, s[24:27], s69 offen offset:1024 sc1\n"
"buffer_load_dwordx4 v[152:155], v60, s[24:27], s69 offen offset:2048 sc1\nbuffer_load_dwordx4 v[216:219], v60, s[24:27], s69 offen offset:3072 sc1\ns_add_u32 s69, s98, 4096\nbuffer_load_dwordx4 v[116:119], v60, s[24:27], s69 offen offset:0 sc1\nbuffer_load_dwordx4 v[128:131], v60, s[24:27], s69 offen offset:1024 sc1\nbuffer_load_dwordx4 v[192:195], v60, s[24:27], s69 offen offset:2048 sc1\ns_add_u32 s69, s98, 14336\nbuffer_load_dwordx4 v[148:151], v60, s[24:27], s69 offen offset:0 sc1\n"
"buffer_load_dwordx4 v[180:183], v60, s[24:27], s69 offen offset:1024 sc1\nbuffer_load_dwordx4 v[104:107], v60, s[24:27], s69 offen offset:2048 sc1\ns_branch .Lrx0\n.Lrx0_ok:\nv_lshlrev_b32 v41, 16, v240\nv_and_b32 v42, 0xffff0000, v240\nQa 92,92,41\nQa 93,93,42\n"
"v_lshlrev_b32 v43, 16, v241\nv_and_b32 v44, 0xffff0000, v241\nQa 94,94,43\nQa 95,95,44\nQk 92,92,93\nQk 93,94,95\nds_write_b64 v61, v[92:93] offset:0\nv_lshlrev_b32 v41, 16, v242\n"
"v_and_b32 v42, 0xffff0000, v242\nQa 124,124,41\nQa 125,125,42\nv_lshlrev_b32 v43, 16, v243\nv_and_b32 v44, 0xffff0000, v243\nQa 126,126,43\nQa 127,127,44\nQk 124,124,125\n"
"Qk 125,126,127\nds_write_b64 v61, v[124:125] offset:32\nv_lshlrev_b32 v41, 16, v88\nv_and_b32 v42, 0xffff0000, v88\nQa 156,156,41\nQa 157,157,42\nv_lshlrev_b32 v43, 16, v89\nv_and_b32 v44, 0xffff0000, v89\n"
"Qa 158,158,43\nQa 159,159,44\nQk 156,156,157\nQk 157,158,159\nds_write_b64 v61, v[156:157] offset:64\nv_lshlrev_b32 v41, 16, v90\nv_and_b32 v42, 0xffff0000, v90\nQa 188,188,41\n"
"Qa 189,189,42\nv_lshlrev_b32 v43, 16, v91\nv_and_b32 v44, 0xffff0000, v91\nQa 190,190,43\nQa 191,191,44\nQk 188,188,189\nQk 189,190,191\nds_write_b64 v61, v[188:189] offset:96\n"
"v_lshlrev_b32 v41, 16, v152\nv_and_b32 v42, 0xffff0000, v152\nQa 220,220,41\nQa 221,221,42\nv_lshlrev_b32 v43, 16, v153\nv_and_b32 v44, 0xffff0000, v153\nQa 222,222,43\nQa 223,223,44\n"
"Qk 220,220,221\nQk 221,222,223\nds_write_b64 v61, v[220:221] offset:128\nv_lshlrev_b32 v41, 16, v154\nv_and_b32 v42, 0xffff0000, v154\nQa 252,252,41\nQa 253,253,42\nv_lshlrev_b32 v43, 16, v155\n"
"v_and_b32 v44, 0xffff0000, v155\nQa 254,254,43\nQa 255,255,44\nQk 252,252,253\nQk 253,254,255\nds_write_b64 v61, v[252:253] offset:5376\nv_lshlrev_b32 v41, 16, v216\nv_and_b32 v42, 0xffff0000, v216\n"
"Qa 100,100,41\nQa 101,101,42\nv_lshlrev_b32 v43, 16, v217\nv_and_b32 v44, 0xffff0000, v217\nQa 102,102,43\nQa 103,103,44\nQk 100,100,101\nQk 101,102,103\n"
"ds_write_b64 v61, v[100:101] offset:5408\nv_lshlrev_b32 v41, 16, v218\nv_and_b32 v42, 0xffff0000, v218\nQa 132,132,41\nQa 133,133,42\nv_lshlrev_b32 v43, 16, v219\nv_and_b32 v44, 0xffff0000, v219\nQa 134,134,43\n"
"Qa 135,135,44\nQk 132,132,133\nQk 133,134,135\nds_write_b64 v61, v[132:133] offset:5440\nv_lshlrev_b32 v41, 16, v116\nv_and_b32 v42, 0xffff0000, v116\nQa 164,164,41\nQa 165,165,42\n"
"v_lshlrev_b32 v43, 16, v117\nv_and_b32 v44, 0xffff0000, v117\nQa 166,166,43\nQa 167,167,44\nQk 164,164,165\nQk 165,166,167\nds_write_b64 v61, v[164:165] offset:5472\nv_lshlrev_b32 v41, 16, v118\n"
"v_and_b32 v42, 0xffff0000, v118\nQa 196,196,41\nQa 197,197,42\nv_lshlrev_b32 v43, 16, v119\nv_and_b32 v44, 0xffff0000, v119\nQa 198,198,43\nQa 199,199,44\nQk 196,196,197\n"
"Qk 197,198,199\nds_write_b64 v61, v[196:197] offset:5504\nv_lshlrev_b32 v41, 16, v128\nv_and_b32 v42, 0xffff0000, v128\nQa 228,228,41\nQa 229,229,42\nv_lshlrev_b32 v43, 16, v129\nv_and_b32 v44, 0xffff0000, v129\n"
"Qa 230,230,43\nQa 231,231,44\nQk 228,228,229\nQk 229,230,231\nds_write_b64 v61, v[228:229] offset:10752\nv_lshlrev_b32 v41, 16, v130\nv_and_b32 v42, 0xffff0000, v130\nQa 108,108,41\n"
"Qa 109,109,42\nv_lshlrev_b32 v43, 16, v131\nv_and_b32 v44, 0xffff0000, v131\nQa 110,110,43\nQa 111,111,44\nQk 108,108,109\nQk 109,110,111\nds_write_b64 v61, v[108:109] offset:10784\n"
"v_lshlrev_b32 v41, 16, v192\nv_and_b32 v42, 0xffff0000, v192\nQa 140,140,41\nQa 141,141,42\nv_lshlrev_b32 v43, 16, v193\nv_and_b32 v44, 0xffff0000, v193\nQa 142,142,43\nQa 143,143,44\n"
"Qk 140,140,141\nQk 141,142,143\nds_write_b64 v61, v[140:141] offset:10816\nv_lshlrev_b32 v41, 16, v194\nv_and_b32 v42, 0xffff0000, v194\nQa 172,172,41\nQa 173,173,42\nv_lshlrev_b32 v43, 16, v195\n"
"v_and_b32 v44, 0xffff0000, v195\n.Lpg23:\ns_cbranch_execz .Lpg27\nQa 174,174,43\nQa 175,175,44\nQk 172,172,173\nQk 173,174,175\nds_write_b64 v61, v[172:173] offset:10848\n"
"v_lshlrev_b32 v41, 16, v148\nv_and_b32 v42, 0xffff0000, v148\nQa 204,204,41\nQa 205,205,42\nv_lshlrev_b32 v43, 16, v149\nv_and_b32 v44, 0xffff0000, v149\nQa 206,206,43\nQa 207,207,44\n"
"Qk 204,204,205\nQk 205,206,207\nds_write_b64 v61, v[204:205] offset:10880\nv_lshlrev_b32 v41, 16, v150\nv_and_b32 v42, 0xffff0000, v150\nQa 236,236,41\nQa 237,237,42\nv_lshlrev_b32 v43, 16, v151\n"
"v_and_b32 v44, 0xffff0000, v151\nQa 238,238,43\nQa 239,239,44\nQk 236,236,237\nQk 237,238,239\nds_write_b64 v61, v[236:237] offset:16128\nv_lshlrev_b32 v41, 16, v180\nv_and_b32 v42, 0xffff0000, v180\n"
"Qa 80,80,41\nQa 81,81,42\nv_lshlrev_b32 v43, 16, v181\nv_and_b32 v44, 0xffff0000, v181\nQa 82,82,43\nQa 83,83,44\nQk 80,80,81\nQk 81,82,83\n"
"ds_write_b64 v61, v[80:81] offset:16160\nv_lshlrev_b32 v41, 16, v182\nv_and_b32 v42, 0xffff0000, v182\nQa 112,112,41\nQa 113,113,42\nv_lshlrev_b32 v43, 16, v183\nv_and_b32 v44, 0xffff0000, v183\nQa 114,114,43\n"
"Qa 115,115,44\nQk 112,112,113\nQk 113,114,115\nds_write_b64 v61, v[112:113] offset:16192\nv_lshlrev_b32 v41, 16, v104\nv_and_b32 v42, 0xffff0000, v104\nQa 144,144,41\nQa 145,145,42\n"
"v_lshlrev_b32 v43, 16, v105\nv_and_b32 v44, 0xffff0000, v105\nQa 146,146,43\nQa 147,147,44\nQk 144,144,145\nQk 145,146,147\nds_write_b64 v61, v[144:145] offset:16224\nv_lshlrev_b32 v41, 16, v106\n"
"v_and_b32 v42, 0xffff0000, v106\nQa 176,176,41\nQa 177,177,42\nv_lshlrev_b32 v43, 16, v107\nv_and_b32 v44, 0xffff0000, v107\nQa 178,178,43\nQa 179,179,44\nQk 176,176,177\n"
"Qk 177,178,179\nds_write_b64 v61, v[176:177] offset:16256\ns_branch .Lfin\n.Lrole1:\n.Lpg24:\ns_cbranch_execz .Ltr8\ns_waitcnt lgkmcnt(2)\nQmvaa 92,132,56\n"
"s_waitcnt vmcnt(26)\nds_write_b128 v52, a[172:175] offset:0\nQmvaa 124,136,56\nQmvaa 156,140,56\nQtv 0,45,0\nQmvaa 188,144,56\nQda 72,53,33792\nQmvaa 220,148,56\n"
"Qtv 2,45,5120\nQmvaa 252,132,60\nQtv 4,45,256\ns_mov_b32 s94, 835584\nQmvaa 100,136,60\nbuffer_load_dwordx4 a[220:223], v60, s[48:51], s94 offen offset:0 sc1\nQmvaa 132,140,60\nds_read_b32 v69, v74\n"
"Qmvaa 164,144,60\nQtv 6,45,5376\nQmvaa 196,148,60\ns_waitcnt vmcnt(26)\nds_write_b128 v52, a[0:3] offset:4096\ns_waitcnt lgkmcnt(15)\nQmvaa 228,132,64\nQmvaa 108,136,64\n"
"s_waitcnt lgkmcnt(2)\nv_cmp_gt_u32 vcc, v75, v69\nQmvaa 140,140,64\ns_cbranch_vccnz .Lso49r1\n.Lro49r1:\nQmvaa 172,144,64\nQda 76,53,37888\ns_add_u32 m0, s96, 86016\n"
"s_mov_b32 s95, 1761280\nQmvaa 204,148,64\nbuffer_load_dwordx4 v40, s[52:55], s95 offen lds\nQmvaa 236,132,68\nQtv 8,45,512\nQmvaa 80,136,68\nbuffer_load_dwordx4 a[224:227], v60, s[48:51], s94 offen offset:1024 sc1\nQmvaa 112,140,68\n"
"Qtv 10,45,5632\nQmvaa 144,144,68\nQtv 12,45,768\nQmvaa 176,148,68\ns_waitcnt vmcnt(27)\nds_write_b128 v52, a[4:7] offset:8192\ns_waitcnt lgkmcnt(4)\nQmvaa 208,132,72\n"
"Qmvaa 240,136,72\nbuffer_load_dwordx4 v41, s[52:55], s95 offen offset:1024 lds\nQmvaa 84,140,72\nQtv 14,45,5888\nQmvaa 88,144,72\nQda 80,53,41984\nQmvaa 120,148,72\nQtv 16,45,1024\n"
"Qmvaa 152,132,76\nQtv 18,45,6144\nQmvaa 184,136,76\nbuffer_load_dwordx4 a[228:231], v60, s[48:51], s94 offen offset:2048 sc1\nQmvaa 216,140,76\nQmvaa 248,144,76\nbuffer_load_dwordx4 v42, s[52:55], s95 offen offset:2048 lds\nQmvaa 116,148,76\n"
"s_waitcnt vmcnt(28)\nds_write_b128 v52, a[8:11] offset:12288\ns_waitcnt lgkmcnt(3)\nQmvaa 96,132,80\nQmvaa 128,136,80\nQmvaa 160,140,80\nQmvaa 192,144,80\nQda 48,53,46080\n"
"v_add_u32 v75, 1, v75\nQmvaa 224,148,80\ns_waitcnt vmcnt(34)\nds_write_b32 v73, v75\ns_waitcnt lgkmcnt(3)\nQmvva 92,0,252\nbuffer_load_dwordx4 v43, s[52:55], s95 offen offset:3072 lds\ns_mov_b32 s94, 501760\n"
"Qmvva 124,4,252\nbuffer_load_dwordx4 a[232:235], v60, s[48:51], s94 offen offset:0 sc1\nQmvva 156,8,252\nQtv 20,47,0\nQmvva 188,12,252\nQtv 22,47,5120\nQmvva 220,16,252\ns_waitcnt vmcnt(28)\n"
"ds_write_b128 v52, a[12:15] offset:16384\nQmvva 252,0,168\nQmvva 100,4,168\nQtv 24,47,256\nQmvva 132,8,168\nds_read_b32 v69, v74\nQmvva 164,12,168\nQda 52,53,50176\n"
"s_add_u32 m0, s96, 90112\nQmvva 196,16,168\nbuffer_load_dwordx4 v44, s[52:55], s95 offen lds\nQmvva 228,0,172\nQtv 26,47,5376\nQmvva 108,4,172\ns_waitcnt lgkmcnt(2)\nv_cmp_gt_u32 vcc, v75, v69\n"
"Qmvva 140,8,172\ns_cbranch_vccnz .Lso50r1\n.Lro50r1:\nQmvva 172,12,172\nbuffer_load_dwordx4 a[236:239], v60, s[48:51], s94 offen offset:1024 sc1\nQmvva 204,16,172\ns_waitcnt vmcnt(27)\nds_write_b128 v52, a[16:19] offset:20480\n"
"Qmvva 236,0,0\nQmvva 80,4,0\nQtv 28,47,512\nQmvva 112,8,0\nQmvva 144,12,0\nQda 56,53,0\nQmvva 176,16,0\nQtv 30,47,5632\n"
"Qmvva 208,0,4\nQtv 32,47,768\nQmvva 240,4,4\nbuffer_load_dwordx4 a[240:243], v60, s[48:51], s94 offen offset:2048 sc1\nQmvva 84,8,4\nQtv 34,47,5888\nQmvva 88,12,4\nQtv 36,47,1024\n"
"Qmvva 120,16,4\ns_waitcnt vmcnt(25)\nds_write_b128 v52, a[20:23] offset:25600\nQmvva 152,0,8\nQmvva 184,4,8\nQtv 38,47,6144\nQmvva 216,8,8\nQmvva 248,12,8\n"
"Qda 60,53,4096\nQmvva 116,16,8\nQmvva 96,0,12\nQmvva 128,4,12\nbuffer_load_dwordx4 a[244:247], v60, s[48:51], s94 offen offset:3072 sc1\nQmvva 160,8,12\nQmvva 192,12,12\nv_add_u32 v75, 1, v75\n"
"Qmvva 224,16,12\nds_write_b32 v73, v75\ns_waitcnt lgkmcnt(2)\nQmvva 92,20,48\ns_waitcnt vmcnt(25)\nds_write_b128 v52, a[24:27] offset:29696\nQmvva 124,24,48\nQmvva 156,28,48\n"
"Qta 112,45,20480\nQmvva 188,32,48\nQda 64,53,8192\nQmvva 220,36,48\nQta 114,45,25600\nQmvva 252,20,52\nQta 116,45,20736\ns_mov_b32 s94, 616448\n"
"Qmvva 100,24,52\nbuffer_load_dwordx4 a[248:251], v60, s[48:51], s94 offen offset:0 sc1\nQmvva 132,28,52\nds_read_b32 v69, v74\nQmvva 164,32,52\nQta 118,45,25856\nQmvva 196,36,52\ns_waitcnt vmcnt(25)\n"
"ds_write_b128 v52, a[28:31] offset:33792\ns_waitcnt lgkmcnt(15)\nQmvva 228,20,56\nQmvva 108,24,56\ns_waitcnt lgkmcnt(2)\nv_cmp_gt_u32 vcc, v75, v69\nQmvva 140,28,56\ns_cbranch_vccnz .Lso51r1\n"
".Lro51r1:\nQmvva 172,32,56\nQda 68,53,12288\ns_add_u32 m0, s96, 4096\ns_mov_b32 s95, 2088960\nQmvva 204,36,56\nbuffer_load_dwordx4 v40, s[52:55], s95 offen lds\nQmvva 236,20,60\n"
"Qta 120,45,20992\nQmvva 80,24,60\nbuffer_load_dwordx4 a[252:255], v60, s[48:51], s94 offen offset:1024 sc1\nQmvva 112,28,60\nQta 122,45,26112\nQmvva 144,32,60\nQta 124,45,21248\nQmvva 176,36,60\n"
"s_waitcnt vmcnt(26)\nds_write_b128 v52, a[32:35] offset:37888\ns_waitcnt lgkmcnt(4)\nQmvva 208,20,64\nQmvva 240,24,64\nbuffer_load_dwordx4 v41, s[52:55], s95 offen offset:1024 lds\nQmvva 84,28,64\nQta 126,45,26368\n"
"Qmvva 88,32,64\nQda 72,53,16384\nQmvva 120,36,64\nQta 128,45,21504\nQmvva 152,20,68\nQta 130,45,26624\nQmvva 184,24,68\nbuffer_load_dwordx4 a[168:171], v60, s[48:51], s94 offen offset:2048 sc1\n"
"Qmvva 216,28,68\nQmvva 248,32,68\nbuffer_load_dwordx4 v42, s[52:55], s95 offen offset:2048 lds\nQmvva 116,36,68\ns_waitcnt vmcnt(27)\nds_write_b128 v52, a[36:39] offset:41984\ns_waitcnt lgkmcnt(3)\nQmvva 96,20,72\n"
"Qmvva 128,24,72\nQmvva 160,28,72\nQmvva 192,32,72\nQda 76,53,20480\nv_add_u32 v75, 1, v75\nQmvva 224,36,72\ns_waitcnt vmcnt(33)\nds_write_b32 v73, v75\n"
"s_waitcnt lgkmcnt(3)\nQmvaa 92,112,16\nbuffer_load_dwordx4 v43, s[52:55], s95 offen offset:3072 lds\nQmvaa 124,116,16\nbuffer_load_dwordx4 a[172:175], v60, s[48:51], s94 offen offset:3072 sc1\nQmvaa 156,120,16\nQta 132,47,20480\nQmvaa 188,124,16\n"
"Qta 134,47,25600\nQmvaa 220,128,16\ns_waitcnt vmcnt(27)\nds_write_b128 v52, a[40:43] offset:46080\nQmvaa 252,112,20\nQmvaa 100,116,20\nQta 136,47,20736\nQmvaa 132,120,20\n"
"ds_read_b32 v69, v74\nQmvaa 164,124,20\nQda 80,53,25600\ns_add_u32 m0, s96, 8192\nQmvaa 196,128,20\nbuffer_load_dwordx4 v44, s[52:55], s95 offen lds\nQmvaa 228,112,24\nQta 138,47,25856\n"
"Qmvaa 108,116,24\ns_waitcnt lgkmcnt(2)\nv_cmp_gt_u32 vcc, v75, v69\nQmvaa 140,120,24\ns_mov_b32 s94, 731136\ns_cbranch_vccnz .Lso52r1\n.Lro52r1:\nQmvaa 172,124,24\n"
"buffer_load_dwordx4 a[0:3], v60, s[48:51], s94 offen offset:0 sc1\nQmvaa 204,128,24\ns_waitcnt vmcnt(27)\nds_write_b128 v52, a[44:47] offset:50176\nQmvaa 236,112,28\nQmvaa 80,116,28\nQta 140,47,20992\nQmvaa 112,120,28\n"
"Qmvaa 144,124,28\nQda 48,53,29696\nQmvaa 176,128,28\nQta 142,47,26112\nQmvaa 208,112,32\nQta 144,47,21248\nQmvaa 240,116,32\nbuffer_load_dwordx4 a[4:7], v60, s[48:51], s94 offen offset:1024 sc1\n"
"Qmvaa 84,120,32\nQta 146,47,26368\nQmvaa 88,124,32\nQta 148,47,21504\nQmvaa 120,128,32\ns_waitcnt vmcnt(25)\nds_write_b128 v52, a[208:211] offset:0\nQmvaa 152,112,36\n"
"Qmvaa 184,116,36\nQta 150,47,26624\nQmvaa 216,120,36\nQmvaa 248,124,36\nQda 52,53,33792\nQmvaa 116,128,36\nQmvaa 96,112,40\nQmvaa 128,116,40\n"
"buffer_load_dwordx4 a[8:11], v60, s[48:51], s94 offen offset:2048 sc1\nQmvaa 160,120,40\nQmvaa 192,124,40\nv_add_u32 v75, 1, v75\nQmvaa 224,128,40\nds_write_b32 v73, v75\ns_waitcnt lgkmcnt(2)\nQmvaa 92,132,76\n"
"s_waitcnt vmcnt(25)\nds_write_b128 v52, a[212:215] offset:4096\nQmvaa 124,136,76\nQmvaa 156,140,76\nQtv 0,45,40960\nQmvaa 188,144,76\nQda 56,53,37888\nQmvaa 220,148,76\n"
"Qtv 2,45,46080\nQmvaa 252,132,80\nQtv 4,45,41216\nQmvaa 100,136,80\nbuffer_load_dwordx4 a[12:15], v60, s[48:51], s94 offen offset:3072 sc1\nQmvaa 132,140,80\nds_read_b32 v69, v74\nQmvaa 164,144,80\n"
"Qtv 6,45,46336\nQmvaa 196,148,80\ns_waitcnt vmcnt(25)\nds_write_b128 v52, a[216:219] offset:8192\ns_waitcnt lgkmcnt(15)\nQmvaa 228,132,48\nQmvaa 108,136,48\ns_waitcnt lgkmcnt(2)\n"
"v_cmp_gt_u32 vcc, v75, v69\nQmvaa 140,140,48\ns_cbranch_vccnz .Lso53r1\n.Lro53r1:\nQmvaa 172,144,48\nQda 60,53,41984\ns_add_u32 m0, s96, 24576\ns_mov_b32 s95, 2416640\n"
"Qmvaa 204,148,48\nbuffer_load_dwordx4 v40, s[52:55], s95 offen lds\nQmvaa 236,132,52\nQtv 8,45,41472\ns_mov_b32 s94, 845824\nQmvaa 80,136,52\nbuffer_load_dwordx4 a[16:19], v60, s[48:51], s94 offen offset:0 sc1\nQmvaa 112,140,52\n"
"Qtv 10,45,46592\nQmvaa 144,144,52\nQtv 12,45,41728\nQmvaa 176,148,52\ns_waitcnt vmcnt(26)\nds_write_b128 v52, a[220:223] offset:12288\ns_waitcnt lgkmcnt(4)\nQmvaa 208,132,56\n"
"Qmvaa 240,136,56\nbuffer_load_dwordx4 v41, s[52:55], s95 offen offset:1024 lds\nQmvaa 84,140,56\nQtv 14,45,46848\nQmvaa 88,144,56\nQda 64,53,46080\nQmvaa 120,148,56\nQtv 16,45,41984\n"
"Qmvaa 152,132,60\nQtv 18,45,47104\nQmvaa 184,136,60\nbuffer_load_dwordx4 a[20:23], v60, s[48:51], s94 offen offset:1024 sc1\nQmvaa 216,140,60\nQmvaa 248,144,60\nbuffer_load_dwordx4 v42, s[52:55], s95 offen offset:2048 lds\nQmvaa 116,148,60\n"
"s_waitcnt vmcnt(27)\nds_write_b128 v52, a[224:227] offset:16384\ns_waitcnt lgkmcnt(3)\nQmvaa 96,132,64\nQmvaa 128,136,64\nQmvaa 160,140,64\nQmvaa 192,144,64\nQda 68,53,50176\n"
"v_add_u32 v75, 1, v75\nQmvaa 224,148,64\ns_waitcnt vmcnt(33)\nds_write_b32 v73, v75\ns_waitcnt lgkmcnt(3)\nQmvva 92,0,44\nbuffer_load_dwordx4 v43, s[52:55], s95 offen offset:3072 lds\nQmvva 124,4,44\n"
"buffer_load_dwordx4 a[24:27], v60, s[48:51], s94 offen offset:2048 sc1\nQmvva 156,8,44\nQtv 20,47,40960\nQmvva 188,12,44\nQtv 22,47,46080\nQmvva 220,16,44\ns_waitcnt vmcnt(27)\nds_write_b128 v52, a[228:231] offset:20480\n"
"Qmvva 252,0,208\nQmvva 100,4,208\nQtv 24,47,41216\nQmvva 132,8,208\nds_read_b32 v69, v74\nQmvva 164,12,208\nQda 72,53,0\ns_add_u32 m0, s96, 28672\n"
"Qmvva 196,16,208\nbuffer_load_dwordx4 v44, s[52:55], s95 offen lds\nQmvva 228,0,212\nQtv 26,47,46336\nQmvva 108,4,212\ns_waitcnt lgkmcnt(2)\nv_cmp_gt_u32 vcc, v75, v69\nQmvva 140,8,212\n"
"s_cbranch_vccnz .Lso54r1\n.Lro54r1:\nQmvva 172,12,212\nbuffer_load_dwordx4 a[28:31], v60, s[48:51], s94 offen offset:3072 sc1\nQmvva 204,16,212\ns_waitcnt vmcnt(26)\nds_write_b128 v52, a[232:235] offset:25600\nQmvva 236,0,216\n"
"Qmvva 80,4,216\nQtv 28,47,41472\nQmvva 112,8,216\nQmvva 144,12,216\nQda 76,53,4096\nQmvva 176,16,216\nQtv 30,47,46592\nQmvva 208,0,220\n"
"Qtv 32,47,41728\ns_mov_b32 s94, 505856\nQmvva 240,4,220\nbuffer_load_dwordx4 a[32:35], v60, s[48:51], s94 offen offset:0 sc1\nQmvva 84,8,220\nQtv 34,47,46848\nQmvva 88,12,220\nQtv 36,47,41984\n"
"Qmvva 120,16,220\ns_waitcnt vmcnt(25)\nds_write_b128 v52, a[236:239] offset:29696\nQmvva 152,0,224\nQmvva 184,4,224\nQtv 38,47,47104\nQmvva 216,8,224\nQmvva 248,12,224\n"
"Qda 80,53,8192\nQmvva 116,16,224\nQmvva 96,0,228\nQmvva 128,4,228\nbuffer_load_dwordx4 a[36:39], v60, s[48:51], s94 offen offset:1024 sc1\nQmvva 160,8,228\nQmvva 192,12,228\nv_add_u32 v75, 1, v75\n"
"Qmvva 224,16,228\ns_waitcnt vmcnt(27)\nds_write_b32 v73, v75\ns_waitcnt lgkmcnt(2)\nQmvva 92,20,68\ns_waitcnt vmcnt(25)\nds_write_b128 v52, a[240:243] offset:33792\nQmvva 124,24,68\n"
"Qmvva 156,28,68\nQta 112,46,0\nQmvva 188,32,68\nQda 48,53,12288\nQmvva 220,36,68\nQta 114,46,5120\nQmvva 252,20,72\nQta 116,46,256\n"
"Qmvva 100,24,72\nbuffer_load_dwordx4 a[40:43], v60, s[48:51], s94 offen offset:2048 sc1\nQmvva 132,28,72\nds_read_b32 v69, v74\nQmvva 164,32,72\nQmvva 196,36,72\ns_waitcnt vmcnt(25)\nds_write_b128 v52, a[244:247] offset:37888\n"
"s_waitcnt lgkmcnt(15)\nQmvva 228,20,76\nQmvva 108,24,76\ns_waitcnt lgkmcnt(1)\nv_cmp_gt_u32 vcc, v75, v69\nQmvva 140,28,76\ns_cbranch_vccnz .Lso55r1\n.Lro55r1:\n"
"Qmvva 172,32,76\nQda 52,53,16384\nQmvva 204,36,76\nQta 118,46,5376\nQmvva 236,20,80\nQta 120,46,512\ns_mov_b32 s94, 620544\nQmvva 80,24,80\n"
"buffer_load_dwordx4 a[44:47], v60, s[48:51], s94 offen offset:0 sc1\nQmvva 112,28,80\nQmvva 144,32,80\nQta 122,46,5632\nQmvva 176,36,80\ns_waitcnt vmcnt(25)\nds_write_b128 v52, a[248:251] offset:41984\ns_waitcnt lgkmcnt(4)\n"
"Qmvva 208,20,48\nQmvva 240,24,48\nQta 124,46,768\nQmvva 84,28,48\nQta 126,46,5888\nQmvva 88,32,48\nQda 56,53,20480\nQmvva 120,36,48\n"
"Qmvva 152,20,52\nQta 128,46,1024\nQmvva 184,24,52\nbuffer_load_dwordx4 a[208:211], v60, s[48:51], s94 offen offset:1024 sc1\n.Lpg25:\ns_cbranch_execz .Ltr8\nQmvva 216,28,52\nQta 130,46,6144\n"
"Qmvva 248,32,52\nQta 132,51,0\nQta 134,51,5120\nQmvva 116,36,52\ns_waitcnt vmcnt(24)\nds_write_b128 v52, a[252:255] offset:46080\ns_waitcnt lgkmcnt(5)\nQmvva 96,20,56\n"
"Qmvva 128,24,56\nQta 136,51,256\nQmvva 160,28,56\nQta 138,51,5376\nQmvva 192,32,56\nQda 60,53,25600\nv_add_u32 v75, 1, v75\nQmvva 224,36,56\n"
"ds_write_b32 v73, v75\ns_waitcnt lgkmcnt(7)\nQmvaa 92,112,232\nQta 140,51,512\nQmvaa 124,116,232\nbuffer_load_dwordx4 a[212:215], v60, s[48:51], s94 offen offset:2048 sc1\nQmvaa 156,120,232\nQta 142,51,5632\n"
"Qmvaa 188,124,232\nQta 144,51,768\nQmvaa 220,128,232\ns_waitcnt vmcnt(23)\nds_write_b128 v52, a[168:171] offset:50176\nQmvaa 252,112,236\nQmvaa 100,116,236\nQta 146,51,5888\n"
"Qmvaa 132,120,236\nds_read_b32 v69, v74\nQmvaa 164,124,236\nQda 64,53,29696\nQmvaa 196,128,236\nQta 148,51,1024\nQmvaa 228,112,240\nQta 150,51,6144\n"
"Qmvaa 108,116,240\ns_waitcnt lgkmcnt(3)\nv_cmp_gt_u32 vcc, v75, v69\nQmvaa 140,120,240\ns_mov_b32 s94, 735232\ns_cbranch_vccnz .Lso56r1\n.Lro56r1:\nQmvaa 172,124,240\n"
"buffer_load_dwordx4 a[216:219], v60, s[48:51], s94 offen offset:0 sc1\nQmvaa 204,128,240\ns_waitcnt vmcnt(21)\nds_write_b128 v52, a[172:175] offset:0\nQmvaa 236,112,244\nQmvaa 80,116,244\nQtv 0,46,20480\nQmvaa 112,120,244\n"
"Qtv 2,46,25600\nQmvaa 144,124,244\nQda 68,53,33792\nQmvaa 176,128,244\nQtv 4,46,20736\ns_waitcnt lgkmcnt(5)\nQmvaa 92,132,60\nQtv 6,46,25856\n"
"Qmvaa 124,136,60\nbuffer_load_dwordx4 a[220:223], v60, s[48:51], s94 offen offset:1024 sc1\nQmvaa 156,140,60\nQtv 8,46,20992\nQmvaa 188,144,60\nQtv 10,46,26112\nQmvaa 220,148,60\ns_waitcnt vmcnt(20)\n"
"ds_write_b128 v52, a[0:3] offset:4096\nQmvaa 252,132,64\nQmvaa 100,136,64\nQtv 12,46,21248\nQmvaa 132,140,64\nQtv 14,46,26368\nQmvaa 164,144,64\nQda 72,53,37888\n"
"Qmvaa 196,148,64\nQtv 16,46,21504\ns_waitcnt lgkmcnt(1)\nQmvaa 228,132,68\nQtv 18,46,26624\nQmvaa 108,136,68\nbuffer_load_dwordx4 a[224:227], v60, s[48:51], s94 offen offset:2048 sc1\nQmvaa 140,140,68\n"
"Qtv 20,51,20480\nQmvaa 172,144,68\nQtv 22,51,25600\nv_add_u32 v75, 1, v75\nQmvaa 204,148,68\ns_waitcnt vmcnt(22)\nds_write_b32 v73, v75\nQmvaa 236,132,72\n"
"s_waitcnt vmcnt(20)\nds_write_b128 v52, a[4:7] offset:8192\nQmvaa 80,136,72\nQmvaa 112,140,72\nQtv 24,51,20736\nQmvaa 144,144,72\nQda 76,53,41984\nQmvaa 176,148,72\n"
"Qtv 26,51,25856\ns_waitcnt lgkmcnt(7)\nQmvva 92,0,248\nQtv 28,51,20992\ns_mov_b32 s94, 849920\nQmvva 124,4,248\nbuffer_load_dwordx4 a[228:231], v60, s[48:51], s94 offen offset:0 sc1\nQmvva 156,8,248\n"
"ds_read_b32 v69, v74\nQmvva 188,12,248\nQtv 30,51,26112\nQmvva 220,16,248\ns_waitcnt vmcnt(20)\nds_write_b128 v52, a[8:11] offset:12288\nQmvva 252,0,252\nQmvva 100,4,252\n"
"s_waitcnt lgkmcnt(2)\nv_cmp_gt_u32 vcc, v75, v69\nQmvva 132,8,252\ns_cbranch_vccnz .Lso57r1\n.Lro57r1:\nQmvva 164,12,252\nQda 80,53,46080\nQmvva 196,16,252\n"
"Qtv 32,51,21248\nQmvva 228,0,168\nQtv 34,51,26368\nQmvva 108,4,168\nbuffer_load_dwordx4 a[232:235], v60, s[48:51], s94 offen offset:1024 sc1\nQmvva 140,8,168\nQtv 36,51,21504\nQmvva 172,12,168\n"
"Qtv 38,51,26624\nQmvva 204,16,168\ns_waitcnt vmcnt(20)\nds_write_b128 v52, a[12:15] offset:16384\nQmvva 236,0,172\nQmvva 80,4,172\nQta 112,45,0\nQmvva 112,8,172\n"
"Qta 114,45,5120\nQmvva 144,12,172\nQda 48,53,50176\nQmvva 176,16,172\nQta 116,45,256\ns_waitcnt lgkmcnt(5)\nQmvva 92,20,76\nQta 118,45,5376\n"
"Qmvva 124,24,76\nbuffer_load_dwordx4 a[236:239], v60, s[48:51], s94 offen offset:2048 sc1\nQmvva 156,28,76\nQta 120,45,512\nQmvva 188,32,76\nQta 122,45,5632\nQmvva 220,36,76\ns_waitcnt vmcnt(19)\n"
"ds_write_b128 v52, a[16:19] offset:20480\nQmvva 252,20,80\nQmvva 100,24,80\nQta 124,45,768\nQmvva 132,28,80\nQta 126,45,5888\nQmvva 164,32,80\nQda 52,53,0\n"
"v_add_u32 v75, 1, v75\nQmvva 196,36,80\ns_waitcnt vmcnt(13)\nds_write_b32 v73, v75\ns_waitcnt lgkmcnt(1)\nQmvva 228,20,48\nQta 128,45,1024\nQmvva 108,24,48\n"
"Qta 130,45,6144\nQmvva 140,28,48\nQta 132,47,0\nQmvva 172,32,48\nQta 134,47,5120\nQmvva 204,36,48\ns_waitcnt vmcnt(17)\nds_write_b128 v52, a[20:23] offset:25600\n"
"Qmvva 236,20,52\nQmvva 80,24,52\nQta 136,47,256\nQmvva 112,28,52\nds_read_b32 v69, v74\nQmvva 144,32,52\nQda 56,53,4096\nQmvva 176,36,52\n"
"Qta 138,47,5376\ns_waitcnt lgkmcnt(15)\nQmvaa 92,112,0\nQta 140,47,512\nQmvaa 124,116,0\ns_waitcnt lgkmcnt(3)\nv_cmp_gt_u32 vcc, v75, v69\nQmvaa 156,120,0\n"
"s_cbranch_vccnz .Lso58r1\n.Lro58r1:\nQmvaa 188,124,0\nQta 142,47,5632\nQmvaa 220,128,0\ns_waitcnt vmcnt(14)\nds_write_b128 v52, a[24:27] offset:29696\nQmvaa 252,112,4\n"
"Qmvaa 100,116,4\nQta 144,47,768\nQmvaa 132,120,4\nQta 146,47,5888\nQmvaa 164,124,4\nQda 60,53,8192\nQmvaa 196,128,4\nQta 148,47,1024\n"
"Qmvaa 228,112,8\nQta 150,47,6144\nQmvaa 108,116,8\nQmvaa 140,120,8\nQtv 0,45,20480\nQmvaa 172,124,8\nQtv 2,45,25600\nQmvaa 204,128,8\n"
"s_waitcnt vmcnt(12)\nds_write_b128 v52, a[28:31] offset:33792\nQmvaa 236,112,12\nQmvaa 80,116,12\nQtv 4,45,20736\nQmvaa 112,120,12\nQtv 6,45,25856\nQmvaa 144,124,12\n"
"Qda 64,53,12288\nQmvaa 176,128,12\ns_waitcnt lgkmcnt(6)\nQmvaa 92,132,56\nQtv 8,45,20992\nQmvaa 124,136,56\nQtv 10,45,26112\nQmvaa 156,140,56\n"
"Qtv 12,45,21248\nQmvaa 188,144,56\nv_add_u32 v75, 1, v75\nQmvaa 220,148,56\nds_write_b32 v73, v75\nQmvaa 252,132,60\ns_waitcnt vmcnt(11)\nds_write_b128 v52, a[32:35] offset:37888\n"
"Qmvaa 100,136,60\nQmvaa 132,140,60\nQtv 14,45,26368\nQmvaa 164,144,60\nQda 68,53,16384\nQmvaa 196,148,60\nQtv 16,45,21504\ns_waitcnt lgkmcnt(1)\n"
"Qmvaa 228,132,64\nQtv 18,45,26624\nQmvaa 108,136,64\nQmvaa 140,140,64\nds_read_b32 v69, v74\nQmvaa 172,144,64\nQtv 20,47,20480\nQmvaa 204,148,64\n"
"s_waitcnt vmcnt(10)\nds_write_b128 v52, a[36:39] offset:41984\nQmvaa 236,132,68\nQmvaa 80,136,68\ns_waitcnt lgkmcnt(2)\nv_cmp_gt_u32 vcc, v75, v69\nQmvaa 112,140,68\ns_cbranch_vccnz .Lso59r1\n"
".Lro59r1:\nQmvaa 144,144,68\nQda 72,53,20480\nQmvaa 176,148,68\nQtv 22,47,25600\ns_waitcnt lgkmcnt(5)\nQmvva 92,0,16\nQtv 24,47,20736\n"
"Qmvva 124,4,16\nQtv 26,47,25856\nQmvva 156,8,16\nQtv 28,47,20992\nQmvva 188,12,16\nQtv 30,47,26112\nQmvva 220,16,16\ns_waitcnt vmcnt(9)\n"
"ds_write_b128 v52, a[40:43] offset:46080\nQmvva 252,0,20\nQmvva 100,4,20\nQtv 32,47,21248\nQmvva 132,8,20\nQtv 34,47,26368\nQmvva 164,12,20\nQda 76,53,25600\n"
"Qmvva 196,16,20\nQtv 36,47,21504\nQmvva 228,0,24\nQtv 38,47,26624\nQmvva 108,4,24\nQmvva 140,8,24\nQta 112,46,0\nQmvva 172,12,24\n"
"Qta 114,46,5120\nQmvva 204,16,24\ns_waitcnt vmcnt(8)\nds_write_b128 v52, a[44:47] offset:50176\nQmvva 236,0,28\nQmvva 80,4,28\nQta 116,46,256\nQmvva 112,8,28\n"
"Qta 118,46,5376\nQmvva 144,12,28\nQda 80,53,29696\nv_add_u32 v75, 1, v75\nQmvva 176,16,28\nds_write_b32 v73, v75\ns_waitcnt lgkmcnt(7)\nQmvva 92,20,72\n"
"Qta 120,46,512\ns_add_u32 s68, s64, 0\nQmvva 124,24,72\nQta 122,46,5632\nQmvva 156,28,72\nQta 124,46,768\nQmvva 188,32,72\nQta 126,46,5888\n"
"Qmvva 220,36,72\ns_waitcnt vmcnt(7)\nds_write_b128 v52, a[208:211] offset:0\nQmvva 252,20,76\nQmvva 100,24,76\nQta 128,46,1024\nQk 92,92,93\nQmvva 132,28,76\n"
"ds_read_b32 v69, v74\nv_min_u32 v92, -2, v92\nQmvva 164,32,76\nQda 48,53,33792\nQk 93,94,95\nQmvva 196,36,76\nQta 130,46,6144\nv_min_u32 v93, -2, v93\n"
"s_waitcnt lgkmcnt(10)\nQmvva 228,20,80\nQk 94,124,125\nv_min_u32 v94, -2, v94\nQmvva 108,24,80\ns_waitcnt lgkmcnt(1)\nv_cmp_gt_u32 vcc, v75, v69\nQmvva 140,28,80\n"
"s_cbranch_vccnz .Lso60r1\n.Lro60r1:\nQk 95,126,127\nQmvva 172,32,80\nQta 132,51,0\nv_min_u32 v95, -2, v95\nQmvva 204,36,80\ns_waitcnt vmcnt(6)\n"
"ds_write_b128 v52, a[212:215] offset:4096\nQmvva 236,20,48\nbuffer_store_dwordx4 v[92:95], v60, s[24:27], s68 offen offset:0 sc1\nQk 156,156,157\nQmvva 80,24,48\nQta 134,51,5120\nv_min_u32 v156, -2, v156\nQmvva 112,28,48\n"
"Qta 136,51,256\nQk 157,158,159\nQmvva 144,32,48\nQda 52,53,37888\nv_min_u32 v157, -2, v157\nQmvva 176,36,48\nQta 138,51,5376\nQk 158,188,189\n"
"s_waitcnt lgkmcnt(6)\nQmvaa 208,112,32\nQta 140,51,512\nv_min_u32 v158, -2, v158\nQmvaa 240,116,32\nQk 159,190,191\nv_min_u32 v159, -2, v159\nQmvaa 84,120,32\n"
"Qta 142,51,5632\nbuffer_store_dwordx4 v[156:159], v60, s[24:27], s68 offen offset:1024 sc1\nQmvaa 88,124,32\nQta 144,51,768\nQk 220,220,221\nQmvaa 120,128,32\ns_waitcnt vmcnt(7)\nds_write_b128 v52, a[216:219] offset:8192\n"
"Qmvaa 152,112,36\nv_min_u32 v220, -2, v220\nQk 221,222,223\nQmvaa 184,116,36\nQta 146,51,5888\nv_min_u32 v221, -2, v221\nQmvaa 216,120,36\nQta 148,51,1024\n"
"Qk 222,252,253\nQmvaa 248,124,36\nQda 56,53,41984\nv_min_u32 v222, -2, v222\nQmvaa 116,128,36\nQta 150,51,6144\nQk 223,254,255\nQmvaa 96,112,40\n"
"Qtv 0,46,20480\nQtv 2,46,25600\nQmvaa 128,116,40\nQtv 4,46,20736\nv_min_u32 v223, -2, v223\nQmvaa 160,120,40\nQtv 6,46,25856\nbuffer_store_dwordx4 v[220:223], v60, s[24:27], s68 offen offset:2048 sc1\n"
"Qmvaa 192,124,40\nQtv 8,46,20992\nv_add_u32 v75, 1, v75\nQmvaa 224,128,40\nds_write_b32 v73, v75\nQk 100,100,101\ns_waitcnt lgkmcnt(6)\nQmvaa 208,132,52\n"
"s_waitcnt vmcnt(7)\nds_write_b128 v52, a[220:223] offset:12288\nQmvaa 240,136,52\nv_min_u32 v100, -2, v100\nQk 101,102,103\nQmvaa 84,140,52\nQtv 10,46,26112\nv_min_u32 v101, -2, v101\n"
"Qmvaa 88,144,52\nQda 60,53,46080\nQk 102,132,133\nQmvaa 120,148,52\nQtv 12,46,21248\nv_min_u32 v102, -2, v102\nQmvaa 152,132,56\nQtv 14,46,26368\n"
"Qk 103,134,135\nQmvaa 184,136,56\nQtv 16,46,21504\nv_min_u32 v103, -2, v103\nQmvaa 216,140,56\nds_read_b32 v69, v74\nbuffer_store_dwordx4 v[100:103], v60, s[24:27], s68 offen offset:3072 sc1\nQmvaa 248,144,56\n"
"Qtv 18,46,26624\nQk 164,164,165\nQmvaa 116,148,56\ns_waitcnt vmcnt(7)\nds_write_b128 v52, a[224:227] offset:16384\ns_waitcnt lgkmcnt(6)\nQmvaa 96,132,60\nv_min_u32 v164, -2, v164\n"
"Qk 165,166,167\nQmvaa 128,136,60\ns_waitcnt lgkmcnt(2)\nv_cmp_gt_u32 vcc, v75, v69\nQmvaa 160,140,60\ns_cbranch_vccnz .Lso61r1\n.Lro61r1:\nv_min_u32 v165, -2, v165\n"
"Qmvaa 192,144,60\nQda 64,53,50176\nQk 166,196,197\nQmvaa 224,148,60\nQtv 20,51,20480\nQtv 22,51,25600\ns_waitcnt lgkmcnt(4)\nQmvva 208,0,44\n"
"Qtv 24,51,20736\nv_min_u32 v166, -2, v166\nQmvva 240,4,44\nQtv 26,51,25856\nQk 167,198,199\nQmvva 84,8,44\nQtv 28,51,20992\nv_min_u32 v167, -2, v167\n"
"Qmvva 88,12,44\nQtv 30,51,26112\ns_add_u32 s68, s64, 4096\nQmvva 120,16,44\ns_waitcnt vmcnt(6)\nds_write_b128 v52, a[228:231] offset:20480\nQmvva 152,0,208\nbuffer_store_dwordx4 v[164:167], v60, s[24:27], s68 offen offset:0 sc1\n"
"Qk 228,228,229\nQmvva 184,4,208\nQtv 32,51,21248\nv_min_u32 v228, -2, v228\nQmvva 216,8,208\nQtv 34,51,26368\nQk 229,230,231\nQmvva 248,12,208\n"
"Qda 68,53,0\nv_min_u32 v229, -2, v229\nQmvva 116,16,208\nQtv 36,51,21504\nQk 230,108,109\nQmvva 96,0,212\nQtv 38,51,26624\nv_min_u32 v230, -2, v230\n"
"Qmvva 128,4,212\nQta 112,45,0\nQta 114,45,5120\nQmvva 160,8,212\nQta 116,45,256\nQk 231,110,111\nQmvva 192,12,212\nQta 118,45,5376\n"
"v_min_u32 v231, -2, v231\nQmvva 224,16,212\ns_waitcnt vmcnt(6)\nds_write_b128 v52, a[232:235] offset:25600\ns_waitcnt lgkmcnt(5)\nQmvva 208,20,64\nbuffer_store_dwordx4 v[228:231], v60, s[24:27], s68 offen offset:1024 sc1\nQk 140,140,141\n"
"Qmvva 240,24,64\nQta 120,45,512\nv_min_u32 v140, -2, v140\nQmvva 84,28,64\nQta 122,45,5632\nQk 141,142,143\nQmvva 88,32,64\nQda 72,53,4096\n"
"v_add_u32 v75, 1, v75\nQmvva 120,36,64\nds_write_b32 v73, v75\nv_min_u32 v141, -2, v141\nQmvva 152,20,68\nQta 124,45,768\nQk 142,172,173\nQmvva 184,24,68\n"
"Qta 126,45,5888\nv_min_u32 v142, -2, v142\nQmvva 216,28,68\nQta 128,45,1024\nQk 143,174,175\nQmvva 248,32,68\nQta 130,45,6144\nv_min_u32 v143, -2, v143\n"
"Qmvva 116,36,68\ns_waitcnt vmcnt(6)\nds_write_b128 v52, a[236:239] offset:29696\ns_waitcnt lgkmcnt(6)\nQmvva 96,20,72\nbuffer_store_dwordx4 v[140:143], v60, s[24:27], s68 offen offset:2048 sc1\nQk 204,204,205\nQmvva 128,24,72\n"
"Qta 132,47,0\nv_min_u32 v204, -2, v204\nQmvva 160,28,72\nds_read_b32 v69, v74\nQk 205,206,207\nQmvva 192,32,72\nQda 76,53,8192\nv_min_u32 v205, -2, v205\n"
"Qmvva 224,36,72\nQta 134,47,5120\nQk 206,236,237\ns_waitcnt lgkmcnt(14)\nQmvaa 208,112,216\nQta 136,47,256\n.Lpg26:\ns_cbranch_execz .Ltr8\n"
"v_min_u32 v206, -2, v206\nQmvaa 240,116,216\ns_waitcnt lgkmcnt(3)\nv_cmp_gt_u32 vcc, v75, v69\nQmvaa 84,120,216\ns_cbranch_vccnz .Lso62r1\n.Lro62r1:\nQk 207,238,239\n"
"Qmvaa 88,124,216\nQta 138,47,5376\nv_min_u32 v207, -2, v207\nQmvaa 120,128,216\nQta 140,47,512\ns_add_u32 s68, s64, 14336\nQmvaa 152,112,220\nQta 142,47,5632\n"
"buffer_store_dwordx4 v[204:207], v60, s[24:27], s68 offen offset:0 sc1\nQmvaa 184,116,220\nQta 144,47,768\nQk 80,80,81\nQmvaa 216,120,220\nQta 146,47,5888\nv_min_u32 v80, -2, v80\nQmvaa 248,124,220\n"
"Qda 80,53,12288\nQk 81,82,83\nQmvaa 116,128,220\nQta 148,47,1024\nv_min_u32 v81, -2, v81\nQmvaa 96,112,224\nQta 150,47,6144\nQk 82,112,113\n"
"Qmvaa 128,116,224\nQtv 0,45,20480\nv_min_u32 v82, -2, v82\nQmvaa 160,120,224\nQtv 2,45,25600\nQk 83,114,115\nQmvaa 192,124,224\nQtv 4,45,20736\n"
"v_min_u32 v83, -2, v83\nQmvaa 224,128,224\nQtv 6,45,25856\nbuffer_store_dwordx4 v[80:83], v60, s[24:27], s68 offen offset:1024 sc1\ns_waitcnt lgkmcnt(4)\nQmvaa 208,132,76\nQtv 8,45,20992\nQk 144,144,145\n"
"Qmvaa 240,136,76\nQtv 10,45,26112\nv_min_u32 v144, -2, v144\nQmvaa 84,140,76\nQtv 12,45,21248\nQk 145,146,147\nQmvaa 88,144,76\nQda 48,53,16384\n"
"v_min_u32 v145, -2, v145\nQmvaa 120,148,76\nQtv 14,45,26368\nQk 146,176,177\nQmvaa 152,132,80\nQtv 16,45,21504\nv_min_u32 v146, -2, v146\nQmvaa 184,136,80\n"
"Qtv 18,45,26624\nQk 147,178,179\nQmvaa 216,140,80\nQtv 20,47,20480\nv_min_u32 v147, -2, v147\nQmvaa 248,144,80\nQtv 22,47,25600\nv_add_u32 v75, 1, v75\n"
"Qmvaa 116,148,80\nds_write_b32 v73, v75\nbuffer_store_dwordx4 v[144:147], v60, s[24:27], s68 offen offset:2048 sc1\ns_waitcnt lgkmcnt(6)\nQmvaa 96,132,48\nQtv 24,47,20736\nQmvaa 128,136,48\nQtv 26,47,25856\n"
"Qmvaa 160,140,48\nQtv 28,47,20992\nQmvaa 192,144,48\nQda 52,53,20480\nQmvaa 224,148,48\nQtv 30,47,26112\ns_waitcnt lgkmcnt(8)\nQmvva 208,0,228\n"
"Qtv 32,47,21248\nQmvva 240,4,228\nQtv 34,47,26368\nQmvva 84,8,228\nds_read_b32 v69, v74\nQmvva 88,12,228\nQtv 36,47,21504\nQmvva 120,16,228\n"
"Qtv 38,47,26624\nQmvva 152,0,232\nQmvva 184,4,232\ns_waitcnt lgkmcnt(2)\nv_cmp_gt_u32 vcc, v75, v69\nQmvva 216,8,232\ns_cbranch_vccnz .Lso63r1\n.Lro63r1:\n"
"Qmvva 248,12,232\nQda 56,53,25600\nQmvva 116,16,232\nQmvva 96,0,236\ns_add_u32 s69, s98, 0\nbuffer_load_dwordx4 v[124:127], v60, s[24:27], s69 offen offset:0 sc1\nQmvva 128,4,236\nQmvva 160,8,236\n"
"buffer_load_dwordx4 v[188:191], v60, s[24:27], s69 offen offset:1024 sc1\nQmvva 192,12,236\nQmvva 224,16,236\nbuffer_load_dwordx4 v[252:255], v60, s[24:27], s69 offen offset:2048 sc1\ns_waitcnt lgkmcnt(0)\nQmvva 208,20,52\nQmvva 240,24,52\nbuffer_load_dwordx4 v[132:135], v60, s[24:27], s69 offen offset:3072 sc1\n"
"Qmvva 84,28,52\nQmvva 88,32,52\nQda 60,53,29696\nQmvva 120,36,52\ns_add_u32 s69, s98, 4096\nbuffer_load_dwordx4 v[196:199], v60, s[24:27], s69 offen offset:0 sc1\nQmvva 152,20,56\nQmvva 184,24,56\n"
"buffer_load_dwordx4 v[108:111], v60, s[24:27], s69 offen offset:1024 sc1\nQmvva 216,28,56\nQmvva 248,32,56\nbuffer_load_dwordx4 v[172:175], v60, s[24:27], s69 offen offset:2048 sc1\nQmvva 116,36,56\ns_waitcnt lgkmcnt(0)\nQmvva 96,20,60\ns_add_u32 s69, s98, 14336\n"
"buffer_load_dwordx4 v[236:239], v60, s[24:27], s69 offen offset:0 sc1\nQmvva 128,24,60\nQmvva 160,28,60\nQmvva 192,32,60\nQmvva 224,36,60\ns_barrier\ns_mov_b32 s97, 0\n.Lrx1:\n"
"s_waitcnt vmcnt(0)\nv_max_u32 v40, v124, v125\nv_max3_u32 v40, v40, v126, v127\nv_max3_u32 v40, v40, v188, v189\nv_max3_u32 v40, v40, v190, v191\nv_max3_u32 v40, v40, v252, v253\nv_max3_u32 v40, v40, v254, v255\nv_max3_u32 v40, v40, v132, v133\n"
"v_max3_u32 v40, v40, v134, v135\nv_max3_u32 v40, v40, v196, v197\nv_max3_u32 v40, v40, v198, v199\nv_max3_u32 v40, v40, v108, v109\nv_max3_u32 v40, v40, v110, v111\nv_max3_u32 v40, v40, v172, v173\nv_max3_u32 v40, v40, v174, v175\nv_max3_u32 v40, v40, v236, v237\n"
"v_cmp_eq_u32 vcc, -1, v40\ns_cmp_eq_u64 vcc, 0\ns_cbranch_scc1 .Lrx1_ok\ns_add_u32 s97, s97, 1\ns_cmp_gt_u32 s97, 1048576\ns_cbranch_scc1 .Lrx1_ok\ns_add_u32 s69, s98, 0\nbuffer_load_dwordx4 v[124:127], v60, s[24:27], s69 offen offset:0 sc1\n"
"buffer_load_dwordx4 v[188:191], v60, s[24:27], s69 offen offset:1024 sc1\nbuffer_load_dwordx4 v[252:255], v60, s[24:27], s69 offen offset:2048 sc1\nbuffer_load_dwordx4 v[132:135], v60, s[24:27], s69 offen offset:3072 sc1\ns_add_u32 s69, s98, 4096\nbuffer_load_dwordx4 v[196:199], v60, s[24:27], s69 offen offset:0 sc1\nbuffer_load_dwordx4 v[108:111], v60, s[24:27], s69 offen offset:1024 sc1\nbuffer_load_dwordx4 v[172:175], v60, s[24:27], s69 offen offset:2048 sc1\ns_add_u32 s69, s98, 14336\n"
"buffer_load_dwordx4 v[236:239], v60, s[24:27], s69 offen offset:0 sc1\ns_branch .Lrx1\n.Lrx1_ok:\nv_lshlrev_b32 v41, 16, v124\nv_and_b32 v42, 0xffff0000, v124\nQa 208,208,41\nQa 209,209,42\nv_lshlrev_b32 v43, 16, v125\n"
"v_and_b32 v44, 0xffff0000, v125\nQa 210,210,43\nQa 211,211,44\nQk 208,208,209\nQk 209,210,211\nds_write_b64 v61, v[208:209] offset:21504\nv_lshlrev_b32 v41, 16, v126\nv_and_b32 v42, 0xffff0000, v126\n"
"Qa 240,240,41\nQa 241,241,42\nv_lshlrev_b32 v43, 16, v127\nv_and_b32 v44, 0xffff0000, v127\nQa 242,242,43\nQa 243,243,44\nQk 240,240,241\nQk 241,242,243\n"
"ds_write_b64 v61, v[240:241] offset:21536\nv_lshlrev_b32 v41, 16, v188\nv_and_b32 v42, 0xffff0000, v188\nQa 84,84,41\nQa 85,85,42\nv_lshlrev_b32 v43, 16, v189\nv_and_b32 v44, 0xffff0000, v189\nQa 86,86,43\n"
"Qa 87,87,44\nQk 84,84,85\nQk 85,86,87\nds_write_b64 v61, v[84:85] offset:21568\nv_lshlrev_b32 v41, 16, v190\nv_and_b32 v42, 0xffff0000, v190\nQa 88,88,41\nQa 89,89,42\n"
"v_lshlrev_b32 v43, 16, v191\nv_and_b32 v44, 0xffff0000, v191\nQa 90,90,43\nQa 91,91,44\nQk 88,88,89\nQk 89,90,91\nds_write_b64 v61, v[88:89] offset:21600\nv_lshlrev_b32 v41, 16, v252\n"
"v_and_b32 v42, 0xffff0000, v252\nQa 120,120,41\nQa 121,121,42\nv_lshlrev_b32 v43, 16, v253\nv_and_b32 v44, 0xffff0000, v253\nQa 122,122,43\nQa 123,123,44\nQk 120,120,121\n"
"Qk 121,122,123\nds_write_b64 v61, v[120:121] offset:21632\nv_lshlrev_b32 v41, 16, v254\nv_and_b32 v42, 0xffff0000, v254\nQa 152,152,41\nQa 153,153,42\nv_lshlrev_b32 v43, 16, v255\nv_and_b32 v44, 0xffff0000, v255\n"
"Qa 154,154,43\nQa 155,155,44\nQk 152,152,153\nQk 153,154,155\nds_write_b64 v61, v[152:153] offset:26880\nv_lshlrev_b32 v41, 16, v132\nv_and_b32 v42, 0xffff0000, v132\nQa 184,184,41\n"
"Qa 185,185,42\nv_lshlrev_b32 v43, 16, v133\nv_and_b32 v44, 0xffff0000, v133\nQa 186,186,43\nQa 187,187,44\nQk 184,184,185\nQk 185,186,187\nds_write_b64 v61, v[184:185] offset:26912\n"
"v_lshlrev_b32 v41, 16, v134\nv_and_b32 v42, 0xffff0000, v134\nQa 216,216,41\nQa 217,217,42\nv_lshlrev_b32 v43, 16, v135\nv_and_b32 v44, 0xffff0000, v135\nQa 218,218,43\nQa 219,219,44\n"
"Qk 216,216,217\nQk 217,218,219\nds_write_b64 v61, v[216:217] offset:26944\nv_lshlrev_b32 v41, 16, v196\nv_and_b32 v42, 0xffff0000, v196\nQa 248,248,41\nQa 249,249,42\nv_lshlrev_b32 v43, 16, v197\n"
"v_and_b32 v44, 0xffff0000, v197\nQa 250,250,43\nQa 251,251,44\nQk 248,248,249\nQk 249,250,251\nds_write_b64 v61, v[248:249] offset:26976\nv_lshlrev_b32 v41, 16, v198\nv_and_b32 v42, 0xffff0000, v198\n"
"Qa 116,116,41\nQa 117,117,42\nv_lshlrev_b32 v43, 16, v199\nv_and_b32 v44, 0xffff0000, v199\nQa 118,118,43\nQa 119,119,44\nQk 116,116,117\nQk 117,118,119\n"
"ds_write_b64 v61, v[116:117] offset:27008\nv_lshlrev_b32 v41, 16, v108\nv_and_b32 v42, 0xffff0000, v108\nQa 96,96,41\nQa 97,97,42\nv_lshlrev_b32 v43, 16, v109\nv_and_b32 v44, 0xffff0000, v109\nQa 98,98,43\n"
"Qa 99,99,44\nQk 96,96,97\nQk 97,98,99\nds_write_b64 v61, v[96:97] offset:32256\nv_lshlrev_b32 v41, 16, v110\nv_and_b32 v42, 0xffff0000, v110\nQa 128,128,41\nQa 129,129,42\n"
"v_lshlrev_b32 v43, 16, v111\nv_and_b32 v44, 0xffff0000, v111\nQa 130,130,43\nQa 131,131,44\nQk 128,128,129\nQk 129,130,131\nds_write_b64 v61, v[128:129] offset:32288\nv_lshlrev_b32 v41, 16, v172\n"
"v_and_b32 v42, 0xffff0000, v172\nQa 160,160,41\nQa 161,161,42\nv_lshlrev_b32 v43, 16, v173\nv_and_b32 v44, 0xffff0000, v173\nQa 162,162,43\nQa 163,163,44\nQk 160,160,161\n"
"Qk 161,162,163\nds_write_b64 v61, v[160:161] offset:32320\nv_lshlrev_b32 v41, 16, v174\nv_and_b32 v42, 0xffff0000, v174\nQa 192,192,41\nQa 193,193,42\nv_lshlrev_b32 v43, 16, v175\nv_and_b32 v44, 0xffff0000, v175\n"
"Qa 194,194,43\nQa 195,195,44\nQk 192,192,193\nQk 193,194,195\nds_write_b64 v61, v[192:193] offset:32352\nv_lshlrev_b32 v41, 16, v236\nv_and_b32 v42, 0xffff0000, v236\nQa 224,224,41\n"
"Qa 225,225,42\nv_lshlrev_b32 v43, 16, v237\nv_and_b32 v44, 0xffff0000, v237\nQa 226,226,43\nQa 227,227,44\nQk 224,224,225\nQk 225,226,227\nds_write_b64 v61, v[224:225] offset:32384\n"
".Lfin:\n.Lpg27:\ns_cbranch_execz .Ltr8\ns_waitcnt lgkmcnt(0)\ns_barrier\nQdv 0,62,0\nQdv 4,62,5376\nQdv 8,62,10752\n"
"Qdv 12,62,16128\nQdv 16,62,21504\nQdv 20,62,26880\nQdv 24,62,32256\nQdv 28,70,0\nQdv 32,70,21504\ns_add_u32 s68, s65, 0\ns_waitcnt lgkmcnt(8)\n"
"buffer_store_dwordx4 v[0:3], v63, s[40:43], s68 offen sc1\ns_add_u32 s68, s65, 40960\ns_waitcnt lgkmcnt(7)\nbuffer_store_dwordx4 v[4:7], v63, s[40:43], s68 offen sc1\ns_add_u32 s68, s65, 81920\ns_waitcnt lgkmcnt(6)\nbuffer_store_dwordx4 v[8:11], v63, s[40:43], s68 offen sc1\ns_add_u32 s68, s65, 122880\n"
"s_waitcnt lgkmcnt(5)\nbuffer_store_dwordx4 v[12:15], v63, s[40:43], s68 offen sc1\ns_add_u32 s68, s60, 163840\ns_waitcnt lgkmcnt(4)\nbuffer_store_dwordx4 v[16:19], v63, s[40:43], s68 offen sc1\ns_add_u32 s68, s60, 204800\ns_waitcnt lgkmcnt(3)\nbuffer_store_dwordx4 v[20:23], v63, s[40:43], s68 offen sc1\n"
"s_add_u32 s68, s60, 245760\ns_waitcnt lgkmcnt(2)\nbuffer_store_dwordx4 v[24:27], v63, s[40:43], s68 offen sc1\ns_add_u32 s68, s60, 163840\ns_cmp_eq_u32 s70, 3\ns_cselect_b32 s68, 0x7ffff000, s68\ns_waitcnt lgkmcnt(1)\nbuffer_store_dwordx4 v[28:31], v71, s[40:43], s65 offen sc1\n"
"s_waitcnt lgkmcnt(0)\nbuffer_store_dwordx4 v[32:35], v71, s[40:43], s68 offen sc1\ns_waitcnt vmcnt(0) lgkmcnt(0)\ns_branch .Lexit\n.Lend:\n.Lfb:\ns_waitcnt vmcnt(0) lgkmcnt(0)\nv_lshrrev_b32 v68, 4, v60\n"
"v_and_b32 v69, 15, v68\nv_lshrrev_b32 v68, 4, v68\nv_lshlrev_b32 v66, 8, v69\nv_lshl_add_u32 v66, v68, 4, v66\nv_add_u32 v66, 1024, v66\nv_lshlrev_b32 v67, 10, v68\nv_lshl_add_u32 v67, v69, 1, v67\nv_add_u32 v67, 1024, v67\n"
"v_mov_b32 v68, 0x3f803f80\nv_accvgpr_write_b32 a204, v68\nv_accvgpr_write_b32 a205, v68\nv_accvgpr_write_b32 a206, v68\nv_accvgpr_write_b32 a207, v68\nv_mov_b32 v24, 0xff800000\nv_mov_b32 v28, 0xff800000\nv_mov_b32 v32, 0xff800000\n"
"v_mov_b32 v36, 0xff800000\nv_mov_b32 v40, 0xff800000\nv_mov_b32 v44, 0xff800000\nv_mov_b32 v48, 0xff800000\ns_lshl_b32 s89, s67, 21\ns_mov_b32 s66, 0\n.Lfb1:\ns_add_u32 s68, s89, 4096\n"
"buffer_load_dwordx4 a[112:115], v66, s[40:43], s89 offen offset:0\nbuffer_load_dwordx4 a[116:119], v66, s[40:43], s89 offen offset:64\nbuffer_load_dwordx4 a[120:123], v66, s[40:43], s89 offen offset:128\nbuffer_load_dwordx4 a[124:127], v66, s[40:43], s89 offen offset:192\nbuffer_load_dwordx4 a[128:131], v66, s[40:43], s68 offen offset:0\nbuffer_load_dwordx4 a[132:135], v66, s[40:43], s68 offen offset:64\nbuffer_load_dwordx4 a[136:139], v66, s[40:43], s68 offen offset:128\nbuffer_load_dwordx4 a[140:143], v66, s[40:43], s68 offen offset:192\n"
"s_waitcnt vmcnt(0)\nQzvaa 0,112,0\nQmvaa 0,116,4\nQmvaa 0,120,8\nQmvaa 0,124,12\nQzvaa 4,128,0\nQmvaa 4,132,4\nQmvaa 4,136,8\n"
"Qmvaa 4,140,12\ns_nop 7\nv_max3_f32 v24, v24, v0, v1\nv_max3_f32 v24, v24, v2, v3\nv_max3_f32 v24, v24, v4, v5\nv_max3_f32 v24, v24, v6, v7\nQzvaa 0,112,16\nQmvaa 0,116,20\n"
"Qmvaa 0,120,24\nQmvaa 0,124,28\nQzvaa 4,128,16\nQmvaa 4,132,20\nQmvaa 4,136,24\nQmvaa 4,140,28\ns_nop 7\nv_max3_f32 v28, v28, v0, v1\n"
"v_max3_f32 v28, v28, v2, v3\nv_max3_f32 v28, v28, v4, v5\nv_max3_f32 v28, v28, v6, v7\nQzvaa 0,112,32\nQmvaa 0,116,36\nQmvaa 0,120,40\nQmvaa 0,124,44\nQzvaa 4,128,32\n"
"Qmvaa 4,132,36\nQmvaa 4,136,40\nQmvaa 4,140,44\ns_nop 7\nv_max3_f32 v32, v32, v0, v1\nv_max3_f32 v32, v32, v2, v3\nv_max3_f32 v32, v32, v4, v5\nv_max3_f32 v32, v32, v6, v7\n"
"Qzvaa 0,112,48\nQmvaa 0,116,52\nQmvaa 0,120,56\nQmvaa 0,124,60\nQzvaa 4,128,48\nQmvaa 4,132,52\nQmvaa 4,136,56\nQmvaa 4,140,60\n"
"s_nop 7\nv_max3_f32 v36, v36, v0, v1\nv_max3_f32 v36, v36, v2, v3\nv_max3_f32 v36, v36, v4, v5\nv_max3_f32 v36, v36, v6, v7\nQzvaa 0,112,64\nQmvaa 0,116,68\nQmvaa 0,120,72\n"
"Qmvaa 0,124,76\nQzvaa 4,128,64\nQmvaa 4,132,68\nQmvaa 4,136,72\nQmvaa 4,140,76\ns_nop 7\nv_max3_f32 v40, v40, v0, v1\nv_max3_f32 v40, v40, v2, v3\n"
"v_max3_f32 v40, v40, v4, v5\nv_max3_f32 v40, v40, v6, v7\nQzvaa 0,112,80\nQmvaa 0,116,84\nQmvaa 0,120,88\nQmvaa 0,124,92\nQzvaa 4,128,80\nQmvaa 4,132,84\n"
"Qmvaa 4,136,88\nQmvaa 4,140,92\ns_nop 7\nv_max3_f32 v44, v44, v0, v1\nv_max3_f32 v44, v44, v2, v3\nv_max3_f32 v44, v44, v4, v5\nv_max3_f32 v44, v44, v6, v7\nQzvaa 0,112,96\n"
"Qmvaa 0,116,100\nQmvaa 0,120,104\nQmvaa 0,124,108\nQzvaa 4,128,96\nQmvaa 4,132,100\nQmvaa 4,136,104\nQmvaa 4,140,108\ns_nop 7\n"
"v_max3_f32 v48, v48, v0, v1\nv_max3_f32 v48, v48, v2, v3\nv_max3_f32 v48, v48, v4, v5\nv_max3_f32 v48, v48, v6, v7\ns_add_u32 s89, s89, 8192\ns_add_u32 s66, s66, 1\ns_cmp_lt_u32 s66, 32\ns_cbranch_scc1 .Lfb1\n"
"v_mov_b32 v25, v24\ns_nop 1\nv_permlane32_swap_b32 v24, v25\nv_max_f32 v24, v24, v25\nv_mov_b32 v25, v24\ns_nop 1\nv_permlane16_swap_b32 v24, v25\nv_max_f32 v24, v24, v25\n"
"v_sub_f32 v24, 0, v24\nv_mov_b32 v25, v24\nv_mov_b32 v26, v24\nv_mov_b32 v27, v24\nv_mov_b32 v29, v28\ns_nop 1\nv_permlane32_swap_b32 v28, v29\nv_max_f32 v28, v28, v29\n"
"v_mov_b32 v29, v28\ns_nop 1\nv_permlane16_swap_b32 v28, v29\nv_max_f32 v28, v28, v29\nv_sub_f32 v28, 0, v28\nv_mov_b32 v29, v28\nv_mov_b32 v30, v28\nv_mov_b32 v31, v28\n"
"v_mov_b32 v33, v32\ns_nop 1\nv_permlane32_swap_b32 v32, v33\nv_max_f32 v32, v32, v33\nv_mov_b32 v33, v32\ns_nop 1\nv_permlane16_swap_b32 v32, v33\nv_max_f32 v32, v32, v33\n"
"v_sub_f32 v32, 0, v32\nv_mov_b32 v33, v32\nv_mov_b32 v34, v32\nv_mov_b32 v35, v32\nv_mov_b32 v37, v36\ns_nop 1\nv_permlane32_swap_b32 v36, v37\nv_max_f32 v36, v36, v37\n"
"v_mov_b32 v37, v36\ns_nop 1\nv_permlane16_swap_b32 v36, v37\nv_max_f32 v36, v36, v37\nv_sub_f32 v36, 0, v36\nv_mov_b32 v37, v36\nv_mov_b32 v38, v36\nv_mov_b32 v39, v36\n"
"v_mov_b32 v41, v40\ns_nop 1\nv_permlane32_swap_b32 v40, v41\nv_max_f32 v40, v40, v41\nv_mov_b32 v41, v40\ns_nop 1\nv_permlane16_swap_b32 v40, v41\nv_max_f32 v40, v40, v41\n"
"v_sub_f32 v40, 0, v40\nv_mov_b32 v41, v40\nv_mov_b32 v42, v40\nv_mov_b32 v43, v40\nv_mov_b32 v45, v44\ns_nop 1\nv_permlane32_swap_b32 v44, v45\nv_max_f32 v44, v44, v45\n"
"v_mov_b32 v45, v44\ns_nop 1\nv_permlane16_swap_b32 v44, v45\nv_max_f32 v44, v44, v45\nv_sub_f32 v44, 0, v44\nv_mov_b32 v45, v44\nv_mov_b32 v46, v44\nv_mov_b32 v47, v44\n"
"v_mov_b32 v49, v48\ns_nop 1\nv_permlane32_swap_b32 v48, v49\nv_max_f32 v48, v48, v49\nv_mov_b32 v49, v48\ns_nop 1\nv_permlane16_swap_b32 v48, v49\nv_max_f32 v48, v48, v49\n"
"v_sub_f32 v48, 0, v48\nv_mov_b32 v49, v48\nv_mov_b32 v50, v48\nv_mov_b32 v51, v48\nv_mov_b32 v68, 0\nv_mov_b32 v80, 0\nv_mov_b32 v81, 0\nv_mov_b32 v82, 0\n"
"v_mov_b32 v83, 0\nv_mov_b32 v84, 0\nv_mov_b32 v85, 0\nv_mov_b32 v86, 0\nv_mov_b32 v87, 0\nv_mov_b32 v88, 0\nv_mov_b32 v89, 0\nv_mov_b32 v90, 0\n"
"v_mov_b32 v91, 0\nv_mov_b32 v92, 0\nv_mov_b32 v93, 0\nv_mov_b32 v94, 0\nv_mov_b32 v95, 0\nv_mov_b32 v96, 0\nv_mov_b32 v97, 0\nv_mov_b32 v98, 0\n"
"v_mov_b32 v99, 0\nv_mov_b32 v100, 0\nv_mov_b32 v101, 0\nv_mov_b32 v102, 0\nv_mov_b32 v103, 0\nv_mov_b32 v104, 0\nv_mov_b32 v105, 0\nv_mov_b32 v106, 0\n"
"v_mov_b32 v107, 0\nv_mov_b32 v108, 0\nv_mov_b32 v109, 0\nv_mov_b32 v110, 0\nv_mov_b32 v111, 0\nv_accvgpr_write_b32 a176, v68\nv_accvgpr_write_b32 a177, v68\nv_accvgpr_write_b32 a178, v68\n"
"v_accvgpr_write_b32 a179, v68\nv_mov_b32 v112, 0\nv_mov_b32 v113, 0\nv_mov_b32 v114, 0\nv_mov_b32 v115, 0\nv_mov_b32 v116, 0\nv_mov_b32 v117, 0\nv_mov_b32 v118, 0\n"
"v_mov_b32 v119, 0\nv_mov_b32 v120, 0\nv_mov_b32 v121, 0\nv_mov_b32 v122, 0\nv_mov_b32 v123, 0\nv_mov_b32 v124, 0\nv_mov_b32 v125, 0\nv_mov_b32 v126, 0\n"
"v_mov_b32 v127, 0\nv_mov_b32 v128, 0\nv_mov_b32 v129, 0\nv_mov_b32 v130, 0\nv_mov_b32 v131, 0\nv_mov_b32 v132, 0\nv_mov_b32 v133, 0\nv_mov_b32 v134, 0\n"
"v_mov_b32 v135, 0\nv_mov_b32 v136, 0\nv_mov_b32 v137, 0\nv_mov_b32 v138, 0\nv_mov_b32 v139, 0\nv_mov_b32 v140, 0\nv_mov_b32 v141, 0\nv_mov_b32 v142, 0\n"
"v_mov_b32 v143, 0\nv_accvgpr_write_b32 a180, v68\nv_accvgpr_write_b32 a181, v68\nv_accvgpr_write_b32 a182, v68\nv_accvgpr_write_b32 a183, v68\nv_mov_b32 v144, 0\nv_mov_b32 v145, 0\nv_mov_b32 v146, 0\n"
"v_mov_b32 v147, 0\nv_mov_b32 v148, 0\nv_mov_b32 v149, 0\nv_mov_b32 v150, 0\nv_mov_b32 v151, 0\nv_mov_b32 v152, 0\nv_mov_b32 v153, 0\nv_mov_b32 v154, 0\n"
"v_mov_b32 v155, 0\nv_mov_b32 v156, 0\nv_mov_b32 v157, 0\nv_mov_b32 v158, 0\nv_mov_b32 v159, 0\nv_mov_b32 v160, 0\nv_mov_b32 v161, 0\nv_mov_b32 v162, 0\n"
"v_mov_b32 v163, 0\nv_mov_b32 v164, 0\nv_mov_b32 v165, 0\nv_mov_b32 v166, 0\nv_mov_b32 v167, 0\nv_mov_b32 v168, 0\nv_mov_b32 v169, 0\nv_mov_b32 v170, 0\n"
"v_mov_b32 v171, 0\nv_mov_b32 v172, 0\nv_mov_b32 v173, 0\nv_mov_b32 v174, 0\nv_mov_b32 v175, 0\nv_accvgpr_write_b32 a184, v68\nv_accvgpr_write_b32 a185, v68\nv_accvgpr_write_b32 a186, v68\n"
"v_accvgpr_write_b32 a187, v68\nv_mov_b32 v176, 0\nv_mov_b32 v177, 0\nv_mov_b32 v178, 0\nv_mov_b32 v179, 0\nv_mov_b32 v180, 0\nv_mov_b32 v181, 0\nv_mov_b32 v182, 0\n"
"v_mov_b32 v183, 0\nv_mov_b32 v184, 0\nv_mov_b32 v185, 0\nv_mov_b32 v186, 0\nv_mov_b32 v187, 0\nv_mov_b32 v188, 0\nv_mov_b32 v189, 0\nv_mov_b32 v190, 0\n"
"v_mov_b32 v191, 0\nv_mov_b32 v192, 0\nv_mov_b32 v193, 0\nv_mov_b32 v194, 0\nv_mov_b32 v195, 0\nv_mov_b32 v196, 0\nv_mov_b32 v197, 0\nv_mov_b32 v198, 0\n"
"v_mov_b32 v199, 0\nv_mov_b32 v200, 0\nv_mov_b32 v201, 0\nv_mov_b32 v202, 0\nv_mov_b32 v203, 0\nv_mov_b32 v204, 0\nv_mov_b32 v205, 0\nv_mov_b32 v206, 0\n"
"v_mov_b32 v207, 0\nv_accvgpr_write_b32 a188, v68\nv_accvgpr_write_b32 a189, v68\nv_accvgpr_write_b32 a190, v68\nv_accvgpr_write_b32 a191, v68\nv_mov_b32 v208, 0\nv_mov_b32 v209, 0\nv_mov_b32 v210, 0\n"
"v_mov_b32 v211, 0\nv_mov_b32 v212, 0\nv_mov_b32 v213, 0\nv_mov_b32 v214, 0\nv_mov_b32 v215, 0\nv_mov_b32 v216, 0\nv_mov_b32 v217, 0\nv_mov_b32 v218, 0\n"
"v_mov_b32 v219, 0\nv_mov_b32 v220, 0\nv_mov_b32 v221, 0\nv_mov_b32 v222, 0\nv_mov_b32 v223, 0\nv_mov_b32 v224, 0\nv_mov_b32 v225, 0\nv_mov_b32 v226, 0\n"
"v_mov_b32 v227, 0\nv_mov_b32 v228, 0\nv_mov_b32 v229, 0\nv_mov_b32 v230, 0\nv_mov_b32 v231, 0\nv_mov_b32 v232, 0\nv_mov_b32 v233, 0\nv_mov_b32 v234, 0\n"
"v_mov_b32 v235, 0\nv_mov_b32 v236, 0\nv_mov_b32 v237, 0\nv_mov_b32 v238, 0\nv_mov_b32 v239, 0\nv_accvgpr_write_b32 a192, v68\nv_accvgpr_write_b32 a193, v68\nv_accvgpr_write_b32 a194, v68\n"
"v_accvgpr_write_b32 a195, v68\nv_mov_b32 v240, 0\nv_mov_b32 v241, 0\nv_mov_b32 v242, 0\nv_mov_b32 v243, 0\nv_mov_b32 v244, 0\nv_mov_b32 v245, 0\nv_mov_b32 v246, 0\n"
"v_mov_b32 v247, 0\nv_mov_b32 v248, 0\nv_mov_b32 v249, 0\nv_mov_b32 v250, 0\nv_mov_b32 v251, 0\nv_mov_b32 v252, 0\nv_mov_b32 v253, 0\nv_mov_b32 v254, 0\n"
"v_mov_b32 v255, 0\nv_accvgpr_write_b32 a208, v68\nv_accvgpr_write_b32 a209, v68\nv_accvgpr_write_b32 a210, v68\nv_accvgpr_write_b32 a211, v68\nv_accvgpr_write_b32 a212, v68\nv_accvgpr_write_b32 a213, v68\nv_accvgpr_write_b32 a214, v68\n"
"v_accvgpr_write_b32 a215, v68\nv_accvgpr_write_b32 a216, v68\nv_accvgpr_write_b32 a217, v68\nv_accvgpr_write_b32 a218, v68\nv_accvgpr_write_b32 a219, v68\nv_accvgpr_write_b32 a220, v68\nv_accvgpr_write_b32 a221, v68\nv_accvgpr_write_b32 a222, v68\n"
"v_accvgpr_write_b32 a223, v68\nv_accvgpr_write_b32 a196, v68\nv_accvgpr_write_b32 a197, v68\nv_accvgpr_write_b32 a198, v68\nv_accvgpr_write_b32 a199, v68\nv_accvgpr_write_b32 a224, v68\nv_accvgpr_write_b32 a225, v68\nv_accvgpr_write_b32 a226, v68\n"
"v_accvgpr_write_b32 a227, v68\nv_accvgpr_write_b32 a228, v68\nv_accvgpr_write_b32 a229, v68\nv_accvgpr_write_b32 a230, v68\nv_accvgpr_write_b32 a231, v68\nv_accvgpr_write_b32 a232, v68\nv_accvgpr_write_b32 a233, v68\nv_accvgpr_write_b32 a234, v68\n"
"v_accvgpr_write_b32 a235, v68\nv_accvgpr_write_b32 a236, v68\nv_accvgpr_write_b32 a237, v68\nv_accvgpr_write_b32 a238, v68\nv_accvgpr_write_b32 a239, v68\nv_accvgpr_write_b32 a240, v68\nv_accvgpr_write_b32 a241, v68\nv_accvgpr_write_b32 a242, v68\n"
"v_accvgpr_write_b32 a243, v68\nv_accvgpr_write_b32 a244, v68\nv_accvgpr_write_b32 a245, v68\nv_accvgpr_write_b32 a246, v68\nv_accvgpr_write_b32 a247, v68\nv_accvgpr_write_b32 a248, v68\nv_accvgpr_write_b32 a249, v68\nv_accvgpr_write_b32 a250, v68\n"
"v_accvgpr_write_b32 a251, v68\nv_accvgpr_write_b32 a252, v68\nv_accvgpr_write_b32 a253, v68\nv_accvgpr_write_b32 a254, v68\nv_accvgpr_write_b32 a255, v68\nv_accvgpr_write_b32 a200, v68\nv_accvgpr_write_b32 a201, v68\nv_accvgpr_write_b32 a202, v68\n"
"v_accvgpr_write_b32 a203, v68\ns_lshl_b32 s89, s67, 21\ns_mov_b32 s66, 0\n.Lfb2:\ns_add_u32 s68, s89, 4096\nbuffer_load_dwordx4 a[112:115], v66, s[40:43], s89 offen offset:0\nbuffer_load_dwordx4 a[116:119], v66, s[40:43], s89 offen offset:64\nbuffer_load_dwordx4 a[120:123], v66, s[40:43], s89 offen offset:128\n"
"buffer_load_dwordx4 a[124:127], v66, s[40:43], s89 offen offset:192\nbuffer_load_dwordx4 a[128:131], v66, s[40:43], s68 offen offset:0\nbuffer_load_dwordx4 a[132:135], v66, s[40:43], s68 offen offset:64\nbuffer_load_dwordx4 a[136:139], v66, s[40:43], s68 offen offset:128\nbuffer_load_dwordx4 a[140:143], v66, s[40:43], s68 offen offset:192\nbuffer_load_ushort v8, v67, s[44:47], s89 offen offset:0\nbuffer_load_ushort v9, v67, s[44:47], s89 offen offset:256\nbuffer_load_ushort v10, v67, s[44:47], s89 offen offset:512\n"
"buffer_load_ushort v11, v67, s[44:47], s89 offen offset:768\nbuffer_load_ushort v12, v67, s[44:47], s68 offen offset:0\nbuffer_load_ushort v13, v67, s[44:47], s68 offen offset:256\nbuffer_load_ushort v14, v67, s[44:47], s68 offen offset:512\nbuffer_load_ushort v15, v67, s[44:47], s68 offen offset:768\ns_waitcnt vmcnt(0)\nv_lshl_or_b32 v8, v9, 16, v8\nv_accvgpr_write_b32 a144, v8\n"
"v_lshl_or_b32 v10, v11, 16, v10\nv_accvgpr_write_b32 a145, v10\nv_lshl_or_b32 v12, v13, 16, v12\nv_accvgpr_write_b32 a146, v12\nv_lshl_or_b32 v14, v15, 16, v14\nv_accvgpr_write_b32 a147, v14\nbuffer_load_ushort v8, v67, s[44:47], s89 offen offset:32\nbuffer_load_ushort v9, v67, s[44:47], s89 offen offset:288\n"
"buffer_load_ushort v10, v67, s[44:47], s89 offen offset:544\nbuffer_load_ushort v11, v67, s[44:47], s89 offen offset:800\nbuffer_load_ushort v12, v67, s[44:47], s68 offen offset:32\nbuffer_load_ushort v13, v67, s[44:47], s68 offen offset:288\nbuffer_load_ushort v14, v67, s[44:47], s68 offen offset:544\nbuffer_load_ushort v15, v67, s[44:47], s68 offen offset:800\ns_waitcnt vmcnt(0)\nv_lshl_or_b32 v8, v9, 16, v8\n"
"v_accvgpr_write_b32 a148, v8\nv_lshl_or_b32 v10, v11, 16, v10\nv_accvgpr_write_b32 a149, v10\nv_lshl_or_b32 v12, v13, 16, v12\nv_accvgpr_write_b32 a150, v12\nv_lshl_or_b32 v14, v15, 16, v14\nv_accvgpr_write_b32 a151, v14\nbuffer_load_ushort v8, v67, s[44:47], s89 offen offset:64\n"
"buffer_load_ushort v9, v67, s[44:47], s89 offen offset:320\nbuffer_load_ushort v10, v67, s[44:47], s89 offen offset:576\nbuffer_load_ushort v11, v67, s[44:47], s89 offen offset:832\nbuffer_load_ushort v12, v67, s[44:47], s68 offen offset:64\nbuffer_load_ushort v13, v67, s[44:47], s68 offen offset:320\nbuffer_load_ushort v14, v67, s[44:47], s68 offen offset:576\nbuffer_load_ushort v15, v67, s[44:47], s68 offen offset:832\ns_waitcnt vmcnt(0)\n"
"v_lshl_or_b32 v8, v9, 16, v8\nv_accvgpr_write_b32 a152, v8\nv_lshl_or_b32 v10, v11, 16, v10\nv_accvgpr_write_b32 a153, v10\nv_lshl_or_b32 v12, v13, 16, v12\nv_accvgpr_write_b32 a154, v12\nv_lshl_or_b32 v14, v15, 16, v14\nv_accvgpr_write_b32 a155, v14\n"
"buffer_load_ushort v8, v67, s[44:47], s89 offen offset:96\nbuffer_load_ushort v9, v67, s[44:47], s89 offen offset:352\nbuffer_load_ushort v10, v67, s[44:47], s89 offen offset:608\nbuffer_load_ushort v11, v67, s[44:47], s89 offen offset:864\nbuffer_load_ushort v12, v67, s[44:47], s68 offen offset:96\nbuffer_load_ushort v13, v67, s[44:47], s68 offen offset:352\nbuffer_load_ushort v14, v67, s[44:47], s68 offen offset:608\nbuffer_load_ushort v15, v67, s[44:47], s68 offen offset:864\n"
"s_waitcnt vmcnt(0)\nv_lshl_or_b32 v8, v9, 16, v8\nv_accvgpr_write_b32 a156, v8\nv_lshl_or_b32 v10, v11, 16, v10\nv_accvgpr_write_b32 a157, v10\nv_lshl_or_b32 v12, v13, 16, v12\nv_accvgpr_write_b32 a158, v12\nv_lshl_or_b32 v14, v15, 16, v14\n"
"v_accvgpr_write_b32 a159, v14\nbuffer_load_ushort v8, v67, s[44:47], s89 offen offset:128\nbuffer_load_ushort v9, v67, s[44:47], s89 offen offset:384\nbuffer_load_ushort v10, v67, s[44:47], s89 offen offset:640\nbuffer_load_ushort v11, v67, s[44:47], s89 offen offset:896\nbuffer_load_ushort v12, v67, s[44:47], s68 offen offset:128\nbuffer_load_ushort v13, v67, s[44:47], s68 offen offset:384\nbuffer_load_ushort v14, v67, s[44:47], s68 offen offset:640\n"
"buffer_load_ushort v15, v67, s[44:47], s68 offen offset:896\ns_waitcnt vmcnt(0)\nv_lshl_or_b32 v8, v9, 16, v8\nv_accvgpr_write_b32 a160, v8\nv_lshl_or_b32 v10, v11, 16, v10\nv_accvgpr_write_b32 a161, v10\nv_lshl_or_b32 v12, v13, 16, v12\nv_accvgpr_write_b32 a162, v12\n"
"v_lshl_or_b32 v14, v15, 16, v14\nv_accvgpr_write_b32 a163, v14\nbuffer_load_ushort v8, v67, s[44:47], s89 offen offset:160\nbuffer_load_ushort v9, v67, s[44:47], s89 offen offset:416\nbuffer_load_ushort v10, v67, s[44:47], s89 offen offset:672\nbuffer_load_ushort v11, v67, s[44:47], s89 offen offset:928\nbuffer_load_ushort v12, v67, s[44:47], s68 offen offset:160\nbuffer_load_ushort v13, v67, s[44:47], s68 offen offset:416\n"
"buffer_load_ushort v14, v67, s[44:47], s68 offen offset:672\nbuffer_load_ushort v15, v67, s[44:47], s68 offen offset:928\ns_waitcnt vmcnt(0)\nv_lshl_or_b32 v8, v9, 16, v8\nv_accvgpr_write_b32 a164, v8\nv_lshl_or_b32 v10, v11, 16, v10\nv_accvgpr_write_b32 a165, v10\nv_lshl_or_b32 v12, v13, 16, v12\n"
"v_accvgpr_write_b32 a166, v12\nv_lshl_or_b32 v14, v15, 16, v14\nv_accvgpr_write_b32 a167, v14\nbuffer_load_ushort v8, v67, s[44:47], s89 offen offset:192\nbuffer_load_ushort v9, v67, s[44:47], s89 offen offset:448\nbuffer_load_ushort v10, v67, s[44:47], s89 offen offset:704\nbuffer_load_ushort v11, v67, s[44:47], s89 offen offset:960\nbuffer_load_ushort v12, v67, s[44:47], s68 offen offset:192\n"
"buffer_load_ushort v13, v67, s[44:47], s68 offen offset:448\nbuffer_load_ushort v14, v67, s[44:47], s68 offen offset:704\nbuffer_load_ushort v15, v67, s[44:47], s68 offen offset:960\ns_waitcnt vmcnt(0)\nv_lshl_or_b32 v8, v9, 16, v8\nv_accvgpr_write_b32 a168, v8\nv_lshl_or_b32 v10, v11, 16, v10\nv_accvgpr_write_b32 a169, v10\n"
"v_lshl_or_b32 v12, v13, 16, v12\nv_accvgpr_write_b32 a170, v12\nv_lshl_or_b32 v14, v15, 16, v14\nv_accvgpr_write_b32 a171, v14\nbuffer_load_ushort v8, v67, s[44:47], s89 offen offset:224\nbuffer_load_ushort v9, v67, s[44:47], s89 offen offset:480\nbuffer_load_ushort v10, v67, s[44:47], s89 offen offset:736\nbuffer_load_ushort v11, v67, s[44:47], s89 offen offset:992\n"
"buffer_load_ushort v12, v67, s[44:47], s68 offen offset:224\nbuffer_load_ushort v13, v67, s[44:47], s68 offen offset:480\nbuffer_load_ushort v14, v67, s[44:47], s68 offen offset:736\nbuffer_load_ushort v15, v67, s[44:47], s68 offen offset:992\ns_waitcnt vmcnt(0)\nv_lshl_or_b32 v8, v9, 16, v8\nv_accvgpr_write_b32 a172, v8\nv_lshl_or_b32 v10, v11, 16, v10\n"
"v_accvgpr_write_b32 a173, v10\nv_lshl_or_b32 v12, v13, 16, v12\nv_accvgpr_write_b32 a174, v12\nv_lshl_or_b32 v14, v15, 16, v14\nv_accvgpr_write_b32 a175, v14\nQcvaa 0,112,0,24\nQmvaa 0,116,4\nQmvaa 0,120,8\n"
"Qmvaa 0,124,12\nQcvaa 4,128,0,24\nQmvaa 4,132,4\nQmvaa 4,136,8\nQmvaa 4,140,12\ns_nop 7\nQe 0\nQe 1\n"
"Qe 2\nQe 3\nQe 4\nQe 5\nQe 6\nQe 7\ns_nop 0\nQk 16,0,1\n"
"Qk 17,2,3\nQk 18,4,5\nQk 19,6,7\ns_nop 1\nQmvav 80,144,16\nQmvav 84,148,16\nQmvav 88,152,16\nQmvav 92,156,16\n"
"Qmvav 96,160,16\nQmvav 100,164,16\nQmvav 104,168,16\nQmvav 108,172,16\nQmaav 176,204,16\nQcvaa 0,112,16,28\nQmvaa 0,116,20\nQmvaa 0,120,24\n"
"Qmvaa 0,124,28\nQcvaa 4,128,16,28\nQmvaa 4,132,20\nQmvaa 4,136,24\nQmvaa 4,140,28\ns_nop 7\nQe 0\nQe 1\n"
"Qe 2\nQe 3\nQe 4\nQe 5\nQe 6\nQe 7\ns_nop 0\nQk 16,0,1\n"
"Qk 17,2,3\nQk 18,4,5\nQk 19,6,7\ns_nop 1\nQmvav 112,144,16\nQmvav 116,148,16\nQmvav 120,152,16\nQmvav 124,156,16\n"
"Qmvav 128,160,16\nQmvav 132,164,16\nQmvav 136,168,16\nQmvav 140,172,16\nQmaav 180,204,16\nQcvaa 0,112,32,32\nQmvaa 0,116,36\nQmvaa 0,120,40\n"
"Qmvaa 0,124,44\nQcvaa 4,128,32,32\nQmvaa 4,132,36\nQmvaa 4,136,40\nQmvaa 4,140,44\ns_nop 7\nQe 0\nQe 1\n"
"Qe 2\nQe 3\nQe 4\nQe 5\nQe 6\nQe 7\ns_nop 0\nQk 16,0,1\n"
"Qk 17,2,3\nQk 18,4,5\nQk 19,6,7\ns_nop 1\nQmvav 144,144,16\nQmvav 148,148,16\nQmvav 152,152,16\nQmvav 156,156,16\n"
"Qmvav 160,160,16\nQmvav 164,164,16\nQmvav 168,168,16\nQmvav 172,172,16\nQmaav 184,204,16\nQcvaa 0,112,48,36\nQmvaa 0,116,52\nQmvaa 0,120,56\n"
"Qmvaa 0,124,60\nQcvaa 4,128,48,36\nQmvaa 4,132,52\nQmvaa 4,136,56\nQmvaa 4,140,60\ns_nop 7\nQe 0\nQe 1\n"
"Qe 2\nQe 3\nQe 4\nQe 5\nQe 6\nQe 7\ns_nop 0\nQk 16,0,1\n"
"Qk 17,2,3\nQk 18,4,5\nQk 19,6,7\ns_nop 1\nQmvav 176,144,16\nQmvav 180,148,16\nQmvav 184,152,16\nQmvav 188,156,16\n"
"Qmvav 192,160,16\nQmvav 196,164,16\nQmvav 200,168,16\nQmvav 204,172,16\nQmaav 188,204,16\nQcvaa 0,112,64,40\nQmvaa 0,116,68\nQmvaa 0,120,72\n"
"Qmvaa 0,124,76\nQcvaa 4,128,64,40\nQmvaa 4,132,68\nQmvaa 4,136,72\nQmvaa 4,140,76\ns_nop 7\nQe 0\nQe 1\n"
"Qe 2\nQe 3\nQe 4\nQe 5\nQe 6\nQe 7\ns_nop 0\nQk 16,0,1\n"
"Qk 17,2,3\nQk 18,4,5\nQk 19,6,7\ns_nop 1\nQmvav 208,144,16\nQmvav 212,148,16\nQmvav 216,152,16\nQmvav 220,156,16\n"
"Qmvav 224,160,16\nQmvav 228,164,16\nQmvav 232,168,16\nQmvav 236,172,16\nQmaav 192,204,16\nQcvaa 0,112,80,44\nQmvaa 0,116,84\nQmvaa 0,120,88\n"
"Qmvaa 0,124,92\nQcvaa 4,128,80,44\nQmvaa 4,132,84\nQmvaa 4,136,88\nQmvaa 4,140,92\ns_nop 7\nQe 0\nQe 1\n"
"Qe 2\nQe 3\nQe 4\nQe 5\nQe 6\nQe 7\ns_nop 0\nQk 16,0,1\n"
"Qk 17,2,3\nQk 18,4,5\nQk 19,6,7\ns_nop 1\nQmvav 240,144,16\nQmvav 244,148,16\nQmvav 248,152,16\nQmvav 252,156,16\n"
"Qmaav 208,160,16\nQmaav 212,164,16\nQmaav 216,168,16\nQmaav 220,172,16\nQmaav 196,204,16\nQcvaa 0,112,96,48\nQmvaa 0,116,100\nQmvaa 0,120,104\n"
"Qmvaa 0,124,108\nQcvaa 4,128,96,48\nQmvaa 4,132,100\nQmvaa 4,136,104\nQmvaa 4,140,108\ns_nop 7\nQe 0\nQe 1\n"
"Qe 2\nQe 3\nQe 4\nQe 5\nQe 6\nQe 7\ns_nop 0\nQk 16,0,1\n"
"Qk 17,2,3\nQk 18,4,5\nQk 19,6,7\ns_nop 1\nQmaav 224,144,16\nQmaav 228,148,16\nQmaav 232,152,16\nQmaav 236,156,16\n"
"Qmaav 240,160,16\nQmaav 244,164,16\nQmaav 248,168,16\nQmaav 252,172,16\nQmaav 200,204,16\ns_add_u32 s89, s89, 8192\ns_add_u32 s66, s66, 1\ns_cmp_lt_u32 s66, 32\n"
"s_cbranch_scc1 .Lfb2\ns_nop 7\nv_accvgpr_read_b32 v24, a176\nv_accvgpr_read_b32 v28, a180\nv_accvgpr_read_b32 v32, a184\nv_accvgpr_read_b32 v36, a188\nv_accvgpr_read_b32 v40, a192\nv_accvgpr_read_b32 v44, a196\n"
"v_accvgpr_read_b32 v48, a200\ns_cmp_eq_u32 s67, 0\ns_cbranch_scc1 .Lfbret0\ns_branch .Lfbret1\n.Lf2_w:\ns_mov_b32 s97, 0\n.Lf2_l:\ns_sleep 1\n"
"buffer_load_dword v78, v76, s[24:27], 0 offen sc1\ns_waitcnt vmcnt(0)\ns_add_u32 s97, s97, 1\ns_cmp_gt_u32 s97, 1048576\ns_cbranch_scc1 .Lf2_r\nv_sub_u32 v66, v78, v65\nv_cmp_gt_u32 vcc, 2, v66\ns_and_b64 vcc, vcc, s[28:29]\n"
"s_cbranch_scc1 .Lf2_l\ns_branch .Lf2_r\n.Lf3_w:\ns_mov_b32 s97, 0\n.Lf3_l:\ns_sleep 1\nbuffer_load_dword v78, v76, s[24:27], 0 offen sc1\ns_waitcnt vmcnt(0)\n"
"s_add_u32 s97, s97, 1\ns_cmp_gt_u32 s97, 1048576\ns_cbranch_scc1 .Lf3_r\nv_sub_u32 v66, v78, v65\nv_cmp_gt_u32 vcc, 3, v66\ns_and_b64 vcc, vcc, s[28:29]\ns_cbranch_scc1 .Lf3_l\ns_branch .Lf3_r\n"
".Lf4_w:\ns_mov_b32 s97, 0\n.Lf4_l:\ns_sleep 1\nbuffer_load_dword v78, v76, s[24:27], 0 offen sc1\ns_waitcnt vmcnt(0)\ns_add_u32 s97, s97, 1\ns_cmp_gt_u32 s97, 1048576\n"
"s_cbranch_scc1 .Lf4_r\nv_sub_u32 v66, v78, v65\nv_cmp_gt_u32 vcc, 4, v66\ns_and_b64 vcc, vcc, s[28:29]\ns_cbranch_scc1 .Lf4_l\ns_branch .Lf4_r\n.Lf5_w:\ns_mov_b32 s97, 0\n"
".Lf5_l:\ns_sleep 1\nbuffer_load_dword v78, v76, s[24:27], 0 offen sc1\ns_waitcnt vmcnt(0)\ns_add_u32 s97, s97, 1\ns_cmp_gt_u32 s97, 1048576\ns_cbranch_scc1 .Lf5_r\nv_sub_u32 v66, v78, v65\n"
"v_cmp_gt_u32 vcc, 5, v66\ns_and_b64 vcc, vcc, s[28:29]\ns_cbranch_scc1 .Lf5_l\ns_branch .Lf5_r\n.Lexit:\n"
  :: "s"((unsigned)kp), "s"((unsigned)(kp >> 32)), "s"((unsigned)vp), "s"((unsigned)(vp >> 32)), "s"((unsigned)xp), "s"((unsigned)(xp >> 32)),
     "s"((unsigned)wp), "s"((unsigned)(wp >> 32)), "s"((unsigned)op), "s"((unsigned)(op >> 32)), "s"(ldsb), "s"(wid), "s"(head0),
     "s"((unsigned)dp), "s"((unsigned)(dp >> 32)), "s"((unsigned)wop), "s"((unsigned)(wop >> 32)), "s"((unsigned)yp), "s"((unsigned)(yp >> 32)),
     "s"(hg), "s"(b * 8u + hg)
  : "v0", "v1", "v2", "v3", "v4", "v5", "v6", "v7", "v8", "v9", "v10", "v11", "v12", "v13", "v14", "v15", "v16", "v17", "v18", "v19", "v20", "v21", "v22", "v23", "v24", "v25", "v26", "v27", "v28", "v29", "v30", "v31", "v32", "v33", "v34", "v35", "v36", "v37", "v38", "v39", "v40", "v41", "v42", "v43", "v44", "v45", "v46", "v47", "v48", "v49", "v50", "v51", "v52", "v53", "v54", "v55", "v56", "v57", "v58", "v59", "v60", "v61", "v62", "v63", "v64", "v65", "v66", "v67", "v68", "v69", "v70", "v71", "v72", "v73", "v74", "v75", "v76", "v77", "v78", "v79", "v80", "v81", "v82", "v83", "v84", "v85", "v86", "v87", "v88", "v89", "v90", "v91", "v92", "v93", "v94", "v95", "v96", "v97", "v98", "v99", "v100", "v101", "v102", "v103", "v104", "v105", "v106", "v107", "v108", "v109", "v110", "v111", "v112", "v113", "v114", "v115", "v116", "v117", "v118", "v119", "v120", "v121", "v122", "v123", "v124", "v125", "v126", "v127", "v128", "v129", "v130", "v131", "v132", "v133", "v134", "v135", "v136", "v137", "v138", "v139", "v140", "v141", "v142", "v143", "v144", "v145", "v146", "v147", "v148", "v149", "v150", "v151", "v152", "v153", "v154", "v155", "v156", "v157", "v158", "v159", "v160", "v161", "v162", "v163", "v164", "v165", "v166", "v167", "v168", "v169", "v170", "v171", "v172", "v173", "v174", "v175", "v176", "v177", "v178", "v179", "v180", "v181", "v182", "v183", "v184", "v185", "v186", "v187", "v188", "v189", "v190", "v191", "v192", "v193", "v194", "v195", "v196", "v197", "v198", "v199", "v200", "v201", "v202", "v203", "v204", "v205", "v206", "v207", "v208", "v209", "v210", "v211", "v212", "v213", "v214", "v215", "v216", "v217", "v218", "v219", "v220", "v221", "v222", "v223", "v224", "v225", "v226", "v227", "v228", "v229", "v230", "v231", "v232", "v233", "v234", "v235", "v236", "v237", "v238", "v239", "v240", "v241", "v242", "v243", "v244", "v245", "v246", "v247", "v248", "v249", "v250", "v251", "v252", "v253", "v254", "v255", "a0", "a1", "a2", "a3", "a4", "a5", "a6", "a7", "a8", "a9", "a10", "a11", "a12", "a13", "a14", "a15", "a16", "a17", "a18", "a19", "a20", "a21", "a22", "a23", "a24", "a25", "a26", "a27", "a28", "a29", "a30", "a31", "a32", "a33", "a34", "a35", "a36", "a37", "a38", "a39", "a40", "a41", "a42", "a43", "a44", "a45", "a46", "a47", "a48", "a49", "a50", "a51", "a52", "a53", "a54", "a55", "a56", "a57", "a58", "a59", "a60", "a61", "a62", "a63", "a64", "a65", "a66", "a67", "a68", "a69", "a70", "a71", "a72", "a73", "a74", "a75", "a76", "a77", "a78", "a79", "a80", "a81", "a82", "a83", "a84", "a85", "a86", "a87", "a88", "a89", "a90", "a91", "a92", "a93", "a94", "a95", "a96", "a97", "a98", "a99", "a100", "a101", "a102", "a103", "a104", "a105", "a106", "a107", "a108", "a109", "a110", "a111", "a112", "a113", "a114", "a115", "a116", "a117", "a118", "a119", "a120", "a121", "a122", "a123", "a124", "a125", "a126", "a127", "a128", "a129", "a130", "a131", "a132", "a133", "a134", "a135", "a136", "a137", "a138", "a139", "a140", "a141", "a142", "a143", "a144", "a145", "a146", "a147", "a148", "a149", "a150", "a151", "a152", "a153", "a154", "a155", "a156", "a157", "a158", "a159", "a160", "a161", "a162", "a163", "a164", "a165", "a166", "a167", "a168", "a169", "a170", "a171", "a172", "a173", "a174", "a175", "a176", "a177", "a178", "a179", "a180", "a181", "a182", "a183", "a184", "a185", "a186", "a187", "a188", "a189", "a190", "a191", "a192", "a193", "a194", "a195", "a196", "a197", "a198", "a199", "a200", "a201", "a202", "a203", "a204", "a205", "a206", "a207", "a208", "a209", "a210", "a211", "a212", "a213", "a214", "a215", "a216", "a217", "a218", "a219", "a220", "a221", "a222", "a223", "a224", "a225", "a226", "a227", "a228", "a229", "a230", "a231", "a232", "a233", "a234", "a235", "a236", "a237", "a238", "a239", "a240", "a241", "a242", "a243", "a244", "a245", "a246", "a247", "a248", "a249", "a250", "a251", "a252", "a253", "a254", "a255", "s24", "s25", "s26", "s27", "s28", "s29", "s30", "s31", "s40", "s41", "s42", "s43", "s44", "s45", "s46", "s47", "s48", "s49", "s50", "s51", "s52", "s53", "s54", "s55", "s56", "s57", "s58", "s59", "s60", "s61", "s62", "s63", "s64", "s65", "s66", "s67", "s68", "s69", "s70", "s71", "s72", "s73", "s74", "s75", "s76", "s77", "s78", "s79", "s80", "s81", "s82", "s83", "s84", "s85", "s86", "s87", "s88", "s89", "s90", "s91", "s92", "s93", "s94", "s95", "s96", "s97", "s98", "s99", "vcc", "scc", "memory");
}

extern "C" void kernel_launch(void* const* d_in, const int* in_sizes, int n_in, void* d_out, int out_size, void* d_ws, size_t ws_size, hipStream_t stream) {
  typedef const unsigned short* P;
  (void)hipFuncSetAttribute((const void*)mha2_asm, hipFuncAttributeMaxDynamicSharedMemorySize, 163840);
  hipLaunchKernelGGL(mha2_asm, dim3(256), dim3(256), 163840, stream, (P)d_in[0], (P)d_in[5], (P)d_in[3], (P)d_in[8], (P)d_in[1], (P)d_in[6], (P)d_in[2], (P)d_in[7],
                     (P)d_in[4], (P)d_in[9], (char*)d_ws, (unsigned short*)d_out);
}
```

```cpp
#include <hip/hip_runtime.h>
#include <stdint.h>

__global__ __launch_bounds__(256, 1) __attribute__((aligned(4096)))
void mha2_asm(const unsigned short* __restrict__ X0, const unsigned short* __restrict__ X1, const unsigned short* __restrict__ W0, const unsigned short* __restrict__ W1,
              const unsigned short* __restrict__ K0, const unsigned short* __restrict__ K1,
              const unsigned short* __restrict__ V0, const unsigned short* __restrict__ V1,
              const unsigned short* __restrict__ WO0, const unsigned short* __restrict__ WO1, char* ws, unsigned short* out) {
  extern __shared__ __attribute__((aligned(16))) char lds[];
  const unsigned c = blockIdx.x;
  const unsigned xcd = c & 7u, idx = c >> 3;
  const unsigned hg = idx & 7u, bb = xcd * 4u + (idx >> 3);
  const unsigned bk = bb >> 4, b = bb & 15u;
  const unsigned wid = __builtin_amdgcn_readfirstlane(threadIdx.x >> 6);
  const unsigned head0 = (hg >> 2) * 16u + (hg & 3u) * 2u + (wid >> 1);
  const unsigned long long kp = (unsigned long long)(bk ? K1 : K0) + ((unsigned long long)b * 32u + head0) * 262144ull - 1024ull;
  const unsigned long long vp = (unsigned long long)(bk ? V1 : V0) + ((unsigned long long)b * 32u + head0) * 262144ull - 1024ull;
  const unsigned long long xp = (unsigned long long)(bk ? X1 : X0) + (unsigned long long)b * (224u * 1280u);
  const unsigned long long wp = (unsigned long long)(bk ? W1 : W0);
  const unsigned long long op = (unsigned long long)ws + 0ull + (unsigned long long)bb * 2295808ull;
  const unsigned ldsb = (unsigned)(size_t)lds;
  const unsigned long long wop = (unsigned long long)(bk ? WO1 : WO0) - 4096ull;
  const unsigned long long yp = (unsigned long long)out + (unsigned long long)b * (224ull * 2560ull) + bk * 1280u + (hg & 3u) * 320u;
  const unsigned long long dp = (unsigned long long)ws + (100ull << 20) + (c * 4u + wid) * 256u;
  asm volatile(
".macro Qta d,v,o\nds_read_b64_tr_b16 a[\\d:\\d+1], v\\v offset:\\o\n.endm\n.macro Qdv d,v,o\nds_read_b128 v[\\d:\\d+3], v\\v offset:\\o\n.endm\n.macro Qda d,v,o\nds_read_b128 a[\\d:\\d+3], v\\v offset:\\o\n"
".endm\n.macro Qzvav d,a,b\nv_mfma_f32_16x16x32_bf16 v[\\d:\\d+3], a[\\a:\\a+3], v[\\b:\\b+3], 0\n.endm\n.macro Qzaav d,a,b\nv_mfma_f32_16x16x32_bf16 a[\\d:\\d+3], a[\\a:\\a+3], v[\\b:\\b+3], 0\n.endm\n.macro Qmvaa d,a,b\n"
"v_mfma_f32_16x16x32_bf16 v[\\d:\\d+3], a[\\a:\\a+3], a[\\b:\\b+3], v[\\d:\\d+3]\n.endm\n.macro Qmaaa d,a,b\nv_mfma_f32_16x16x32_bf16 a[\\d:\\d+3], a[\\a:\\a+3], a[\\b:\\b+3], a[\\d:\\d+3]\n.endm\n.macro Qmvav d,a,b\nv_mfma_f32_16x16x32_bf16 v[\\d:\\d+3], a[\\a:\\a+3], v[\\b:\\b+3], v[\\d:\\d+3]\n.endm\n"
".macro Qmaav d,a,b\nv_mfma_f32_16x16x32_bf16 a[\\d:\\d+3], a[\\a:\\a+3], v[\\b:\\b+3], a[\\d:\\d+3]\n.endm\n.macro Qk d,a,b\nv_cvt_pk_bf16_f32 v\\d, v\\a, v\\b\n.endm\n.macro Qzvaa d,a,b\nv_mfma_f32_16x16x32_bf16 v[\\d:\\d+3], a[\\a:\\a+3], a[\\b:\\b+3], 0\n"
".endm\n.macro Qa d,a,b\nv_add_f32 v\\d, v\\a, v\\b\n.endm\n.macro Qe d\nv_exp_f32 v\\d, v\\d\n.endm\n.macro Qcvaa d,a,b,c\n"
"v_mfma_f32_16x16x32_bf16 v[\\d:\\d+3], a[\\a:\\a+3], a[\\b:\\b+3], v[\\c:\\c+3]\n.endm\n.macro Qtv d,v,o\nds_read_b64_tr_b16 v[\\d:\\d+1], v\\v offset:\\o\n.endm\n.macro Qzava d,a,b\nv_mfma_f32_16x16x32_bf16 a[\\d:\\d+3], v[\\a:\\a+3], a[\\b:\\b+3], 0\n.endm\n"
".macro Qmava d,a,b\nv_mfma_f32_16x16x32_bf16 a[\\d:\\d+3], v[\\a:\\a+3], a[\\b:\\b+3], a[\\d:\\d+3]\n.endm\n.macro Qmvva d,a,b\nv_mfma_f32_16x16x32_bf16 v[\\d:\\d+3], v[\\a:\\a+3], a[\\b:\\b+3], v[\\d:\\d+3]\n.endm\n.p2align 7\nv_mbcnt_lo_u32_b32 v0, -1, 0\n"
"v_mbcnt_hi_u32_b32 v0, -1, v0\nv_lshlrev_b32 v60, 4, v0\ns_add_u32 m0, %10, 160768\ns_getpc_b64 s[56:57]\n.Lpc0:\ns_and_b32 s56, s56, 0xffffff80\ns_and_b32 s57, s57, 0xffff\ns_mov_b32 s58, .Lend-.Lpc0\n"
"s_mov_b32 s59, 0x20000\ns_and_b32 s94, %20, 31\ns_lshl_b32 s94, s94, 2\ns_add_u32 s94, s94, %11\ns_lshl_b32 s94, s94, 10\nbuffer_load_dwordx4 v60, s[56:59], s94 offen lds\ns_mov_b32 s52, %6\ns_and_b32 s53, %7, 0xffff\n"
"s_mov_b32 s54, 5242880\ns_mov_b32 s55, 0x20000\ns_mov_b32 s48, %4\ns_and_b32 s49, %5, 0xffff\ns_mov_b32 s50, 286720\ns_mov_b32 s51, 0x20000\ns_mov_b32 s70, %11\ns_and_b32 s92, s70, 1\n"
"s_lshr_b32 s93, s70, 1\nv_lshrrev_b32 v2, 4, v0\ns_lshl_b32 s68, s92, 17\nv_and_b32 v4, 3, v0\nv_lshlrev_b32 v4, 4, v4\nv_lshl_add_u32 v4, v2, 6, v4\nv_bfe_u32 v1, v0, 2, 2\nv_lshl_add_u32 v4, v1, 13, v4\n"
"v_add_u32 v62, s68, v4\ns_lshl_b32 s87, %12, 8\ns_mul_i32 s62, s93, 8224\ns_mul_i32 s68, s92, 4112\ns_add_u32 s62, s62, s68\ns_add_u32 s62, s62, %10\ns_add_u32 s62, s62, 33024\ns_add_u32 m0, s62, 0\n"
"s_add_u32 s66, s87, 0\ns_nop 0\nbuffer_load_dwordx4 v62, s[52:55], s66 offen lds\ns_add_u32 s66, s87, 31744\ns_nop 0\nbuffer_load_dwordx4 v62, s[52:55], s66 offen offset:1024 lds\ns_add_u32 s66, s87, 63480\ns_nop 0\n"
"buffer_load_dwordx4 v62, s[52:55], s66 offen offset:2056 lds\ns_add_u32 s66, s87, 95224\ns_nop 0\nbuffer_load_dwordx4 v62, s[52:55], s66 offen offset:3080 lds\ns_lshl_b32 s69, s92, 1\nv_lshrrev_b32 v3, 5, v0\nv_and_b32 v4, 3, v0\nv_xor_b32 v4, s69, v4\n"
"v_lshlrev_b32 v4, 4, v4\nv_lshl_add_u32 v4, v3, 6, v4\nv_bfe_u32 v1, v0, 2, 3\nv_mul_u32_u24 v64, 1280, v1\nv_add_u32 v64, v64, v4\ns_mul_i32 s86, s93, 143360\ns_mul_i32 s68, s92, 10240\ns_add_u32 s86, s86, s68\n"
"s_mul_i32 s63, s92, 14336\ns_mul_i32 s68, s93, 7168\ns_add_u32 s63, s63, s68\ns_add_u32 s63, s63, %10\ns_add_u32 s63, s63, 99072\ns_add_u32 m0, s63, 33024\ns_add_u32 s97, s86, 0\ns_nop 0\n"
"buffer_load_dwordx4 v64, s[48:51], s97 offen lds\ns_add_u32 s97, s86, 19456\ns_nop 0\nbuffer_load_dwordx4 v64, s[48:51], s97 offen offset:1024 lds\ns_add_u32 s97, s86, 38912\ns_nop 0\nbuffer_load_dwordx4 v64, s[48:51], s97 offen offset:2048 lds\ns_add_u32 s97, s86, 58368\n"
"s_nop 0\nbuffer_load_dwordx4 v64, s[48:51], s97 offen offset:3072 lds\ns_add_u32 m0, s63, 37120\ns_add_u32 s97, s86, 81920\ns_nop 0\nbuffer_load_dwordx4 v64, s[48:51], s97 offen lds\ns_add_u32 s97, s86, 101376\ns_nop 0\n"
"buffer_load_dwordx4 v64, s[48:51], s97 offen offset:1024 lds\ns_add_u32 s97, s86, 120832\ns_nop 0\nbuffer_load_dwordx4 v64, s[48:51], s97 offen offset:2048 lds\ns_add_u32 m0, s62, 16448\ns_add_u32 s66, s87, 262144\ns_nop 0\nbuffer_load_dwordx4 v62, s[52:55], s66 offen lds\n"
"s_mov_b32 s24, %8\ns_and_b32 s25, %9, 0xffff\ns_mov_b32 s26, 2295808\ns_mov_b32 s27, 0x20000\nv_and_b32 v5, 7, v0\nv_lshlrev_b32 v5, 7, v5\ns_add_u32 s66, s87, 293888\ns_nop 0\n"
"buffer_load_dwordx4 v62, s[52:55], s66 offen offset:1024 lds\nv_add_u32 v76, 2293760, v5\nbuffer_load_dword v65, v76, s[24:27], 0 offen sc1\ns_mov_b32 s40, %0\ns_and_b32 s41, %1, 0xffff\ns_mov_b32 s42, 2360320\ns_mov_b32 s43, 0x20000\ns_add_u32 s66, s87, 325624\n"
"s_nop 0\nbuffer_load_dwordx4 v62, s[52:55], s66 offen offset:2056 lds\ns_mov_b32 s44, %2\ns_and_b32 s45, %3, 0xffff\ns_mov_b32 s46, 2360320\ns_mov_b32 s47, 0x20000\ns_mov_b32 s56, %8\ns_and_b32 s57, %9, 0xffff\n"
"s_add_u32 s66, s87, 357368\ns_nop 0\nbuffer_load_dwordx4 v62, s[52:55], s66 offen offset:3080 lds\ns_mov_b32 s58, 0\ns_mov_b32 s59, 0x20000\ns_mov_b32 s60, 0\ns_mov_b32 s67, 0\ns_mov_b32 s72, 0x3e0293ee\n"
"s_lshl_b32 s68, s92, 12\ns_mov_b64 exec, 0\ns_cmp_eq_u32 s70, 0\ns_cbranch_scc1 .Lpg0\ns_cmp_eq_u32 s70, 1\ns_cbranch_scc1 .Lpg1\ns_cmp_eq_u32 s70, 2\ns_cbranch_scc1 .Lpg2\n"
"s_branch .Lpg3\n.Ltr0:\ns_mov_b64 exec, -1\ns_add_u32 s88, s68, 1024\nv_lshl_add_u32 v4, v1, 8, v4\nv_add_u32 v56, s88, v4\nv_subrev_u32 v57, 896, v56\nv_and_b32 v4, 1, v0\n"
"v_lshlrev_b32 v4, 4, v4\ns_add_u32 m0, s63, 0\ns_add_u32 s97, s86, 128\ns_nop 0\nbuffer_load_dwordx4 v64, s[48:51], s97 offen lds\nv_lshl_add_u32 v4, v2, 5, v4\nv_bfe_u32 v1, v0, 1, 3\nv_lshl_add_u32 v4, v1, 8, v4\n"
"v_add_u32 v58, s88, v4\nv_subrev_u32 v59, 896, v58\ns_lshl_b32 s69, s93, 14\ns_add_u32 s97, s86, 19584\ns_nop 0\nbuffer_load_dwordx4 v64, s[48:51], s97 offen offset:1024 lds\ns_add_u32 s69, s69, %10\ns_add_u32 s61, s69, s68\n"
"v_bfe_u32 v3, v0, 3, 1\nv_lshlrev_b32 v4, 1, v3\nv_xor_b32 v4, v4, v2\nv_and_b32 v1, 7, v0\ns_add_u32 s97, s86, 39040\ns_nop 0\nbuffer_load_dwordx4 v64, s[48:51], s97 offen offset:2048 lds\nv_lshl_add_u32 v4, v1, 2, v4\n"
"v_lshlrev_b32 v4, 4, v4\nv_lshl_add_u32 v1, v3, 12, v4\nv_add_u32 v52, s69, v1\nv_add_u32 v53, 66048, v52\ns_mul_i32 s68, s92, 7168\ns_add_u32 s97, s86, 58496\ns_nop 0\nbuffer_load_dwordx4 v64, s[48:51], s97 offen offset:3072 lds\n"
"s_add_u32 s68, s68, %10\ns_add_u32 s68, s68, 99072\nv_mul_u32_u24 v1, 14336, v3\nv_add3_u32 v63, v1, v4, s68\nv_lshrrev_b32 v3, 5, v0\nv_and_b32 v4, 31, v0\ns_add_u32 m0, s63, 4096\ns_add_u32 s97, s86, 82048\n"
"s_nop 0\nbuffer_load_dwordx4 v64, s[48:51], s97 offen lds\nv_lshlrev_b32 v4, 3, v4\nv_lshl_add_u32 v4, v3, 12, v4\nv_add_u32 v4, 8192, v4\nv_add_u32 v54, s69, v4\nv_add_u32 v55, 66048, v54\ns_mul_i32 s68, s93, 8224\n"
"s_add_u32 s97, s86, 101504\ns_nop 0\nbuffer_load_dwordx4 v64, s[48:51], s97 offen offset:1024 lds\ns_add_u32 s68, s68, %10\ns_add_u32 s68, s68, 33024\nv_mul_u32_u24 v3, 2056, v2\nv_and_b32 v4, 15, v0\nv_lshlrev_b32 v4, 4, v4\n"
"v_add3_u32 v61, v3, v4, s68\ns_add_u32 s97, s86, 120960\ns_nop 0\nbuffer_load_dwordx4 v64, s[48:51], s97 offen offset:2048 lds\ns_mul_i32 s68, s70, 6\ns_add_u32 s68, s68, 0\ns_sub_u32 s69, s68, 0\ns_lshl_b32 s69, s69, 10\n"
"v_add_u32 v3, s69, v60\ns_sub_u32 s69, s68, 14\ns_add_u32 m0, s62, 33024\ns_add_u32 s66, s87, 524288\ns_nop 0\nbuffer_load_dwordx4 v62, s[52:55], s66 offen lds\ns_lshl_b32 s69, s69, 2\ns_and_b32 s69, s69, 63\n"
"v_lshrrev_b32 v4, 8, v60\nv_add_u32 v4, s69, v4\nv_mul_u32_u24 v5, 205, v4\nv_lshrrev_b32 v5, 11, v5\ns_add_u32 s66, s87, 556032\ns_nop 0\nbuffer_load_dwordx4 v62, s[52:55], s66 offen offset:1024 lds\nv_mul_u32_u24 v70, 10, v5\n"
"v_sub_u32 v4, v4, v70\nv_lshlrev_b32 v4, 5, v4\nv_bfe_u32 v70, v60, 5, 3\nv_lshl_add_u32 v5, v5, 3, v70\nv_mul_u32_u24 v5, 1280, v5\ns_add_u32 s66, s87, 587768\ns_nop 0\nbuffer_load_dwordx4 v62, s[52:55], s66 offen offset:2056 lds\n"
"v_and_b32 v70, 16, v60\nv_add3_u32 v4, v4, v5, v70\nv_add_u32 v4, 4096, v4\ns_cmp_lt_u32 s68, 14\ns_cselect_b64 vcc, -1, 0\nv_cndmask_b32 v70, v4, v3, vcc\ns_add_u32 s66, s87, 619512\ns_nop 0\n"
"buffer_load_dwordx4 v62, s[52:55], s66 offen offset:3080 lds\ns_mul_i32 s68, s70, 6\ns_add_u32 s68, s68, 1\ns_sub_u32 s69, s68, 1\ns_lshl_b32 s69, s69, 10\nv_add_u32 v3, s69, v60\ns_sub_u32 s69, s68, 14\ns_mov_b64 exec, 0\n"
"s_cmp_eq_u32 s70, 0\ns_cbranch_scc1 .Lpg4\ns_cmp_eq_u32 s70, 1\ns_cbranch_scc1 .Lpg5\ns_cmp_eq_u32 s70, 2\ns_cbranch_scc1 .Lpg6\ns_branch .Lpg7\n.Ltr9:\n"
"s_mov_b64 exec, -1\ns_lshl_b32 s69, s69, 2\ns_and_b32 s69, s69, 63\nv_lshrrev_b32 v4, 8, v60\nv_add_u32 v4, s69, v4\nv_mul_u32_u24 v5, 205, v4\nv_lshrrev_b32 v5, 11, v5\ns_add_u32 m0, s62, 49472\n"
"s_add_u32 s66, s87, 786432\ns_nop 0\nbuffer_load_dwordx4 v62, s[52:55], s66 offen lds\nv_mul_u32_u24 v71, 10, v5\nv_sub_u32 v4, v4, v71\nv_lshlrev_b32 v4, 5, v4\nv_bfe_u32 v71, v60, 5, 3\nv_lshl_add_u32 v5, v5, 3, v71\n"
"v_mul_u32_u24 v5, 1280, v5\ns_add_u32 s66, s87, 818176\ns_nop 0\nbuffer_load_dwordx4 v62, s[52:55], s66 offen offset:1024 lds\nv_and_b32 v71, 16, v60\nv_add3_u32 v4, v4, v5, v71\nv_add_u32 v4, 3072, v4\ns_cmp_lt_u32 s68, 14\n"
"s_cselect_b64 vcc, -1, 0\nv_cndmask_b32 v71, v4, v3, vcc\ns_add_u32 s66, s87, 849912\ns_nop 0\nbuffer_load_dwordx4 v62, s[52:55], s66 offen offset:2056 lds\ns_mul_i32 s68, s70, 6\ns_add_u32 s68, s68, 2\ns_sub_u32 s69, s68, 2\n"
"s_lshl_b32 s69, s69, 10\nv_add_u32 v3, s69, v60\ns_sub_u32 s69, s68, 14\ns_add_u32 s66, s87, 881656\ns_nop 0\nbuffer_load_dwordx4 v62, s[52:55], s66 offen offset:3080 lds\ns_lshl_b32 s69, s69, 2\ns_and_b32 s69, s69, 63\n"
"v_lshrrev_b32 v4, 8, v60\nv_add_u32 v4, s69, v4\nv_mul_u32_u24 v5, 205, v4\nv_lshrrev_b32 v5, 11, v5\ns_mov_b64 exec, 0\ns_cmp_eq_u32 s70, 0\ns_cbranch_scc1 .Lpg8\ns_cmp_eq_u32 s70, 1\n"
"s_cbranch_scc1 .Lpg9\ns_cmp_eq_u32 s70, 2\ns_cbranch_scc1 .Lpg10\ns_branch .Lpg11\n.Ltr10:\ns_mov_b64 exec, -1\nv_mul_u32_u24 v72, 10, v5\nv_sub_u32 v4, v4, v72\n"
"v_lshlrev_b32 v4, 5, v4\nv_bfe_u32 v72, v60, 5, 3\nv_lshl_add_u32 v5, v5, 3, v72\nv_mul_u32_u24 v5, 1280, v5\nv_and_b32 v72, 16, v60\nv_add3_u32 v4, v4, v5, v72\nv_add_u32 v4, 2048, v4\ns_cmp_lt_u32 s68, 14\n"
"s_cselect_b64 vcc, -1, 0\nv_cndmask_b32 v72, v4, v3, vcc\ns_lshl_b32 s68, s70, 2\ns_add_u32 s69, %10, 131840\ns_add_u32 s68, s68, s69\nv_mov_b32 v73, s68\nv_and_b32 v3, 3, v0\nv_lshl_add_u32 v74, v3, 2, s69\n"
"v_mov_b32 v75, 0\nds_write_b32 v73, v75\ns_waitcnt lgkmcnt(0)\ns_mov_b32 s68, 40960\ns_cmp_lt_u32 s70, 3\ns_cselect_b32 s31, 14336, s68\ns_mov_b32 s68, 40960\ns_cmp_lt_u32 s70, 2\n"
"s_cselect_b32 s90, 14336, s68\ns_mul_i32 s91, s70, 6144\ns_add_u32 s91, s91, %10\nv_and_b32 v3, 1, v0\nv_mul_u32_u24 v3, 0xaaaa, v3\nv_or_b32 v77, 0x4444, v3\ns_and_b32 s68, %19, 4\ns_lshl_b32 s28, 15, s68\n"
"s_mov_b32 s29, 0\ns_and_b32 s69, %19, 3\ns_lshl_b32 s69, s69, 2\ns_lshr_b32 s68, s68, 1\ns_add_u32 s69, s69, s68\ns_add_u32 s69, s69, s93\ns_mul_i32 s98, s69, 28672\ns_mul_i32 s68, s92, 7168\n"
"s_add_u32 s98, s98, s68\ns_add_u32 s98, s98, 1835008\ns_mov_b32 s71, s98\ns_add_u32 s58, s98, 17408\ns_and_b32 s68, %19, 3\ns_lshl_b32 s99, 3072, s68\ns_or_b32 s99, s99, 0x302\ns_mov_b32 s94, 0x7ffff000\n"
"s_mul_i32 s68, %20, 40960\ns_mul_i32 s69, s93, 20480\ns_add_u32 s68, s68, s69\ns_lshl_b32 s69, s92, 10\ns_add_u32 s68, s68, s69\ns_add_u32 s68, s68, 2048\ns_add_u32 s94, %6, s68\ns_addc_u32 s95, %7, 0\n"
"s_and_b32 s95, s95, 0xffff\ns_mov_b32 s75, 17408\nv_mov_b32 v3, -1\nv_accvgpr_write_b32 a0, v3\nv_accvgpr_write_b32 a1, v3\nv_accvgpr_write_b32 a2, v3\nv_accvgpr_write_b32 a3, v3\nv_accvgpr_write_b32 a16, v3\n"
"v_accvgpr_write_b32 a17, v3\nv_accvgpr_write_b32 a18, v3\nv_accvgpr_write_b32 a19, v3\nv_accvgpr_write_b32 a32, v3\nv_accvgpr_write_b32 a33, v3\nv_accvgpr_write_b32 a34, v3\nv_accvgpr_write_b32 a35, v3\nv_accvgpr_write_b32 a48, v3\n"
"v_accvgpr_write_b32 a49, v3\nv_accvgpr_write_b32 a50, v3\nv_accvgpr_write_b32 a51, v3\nv_accvgpr_write_b32 a64, v3\nv_accvgpr_write_b32 a65, v3\nv_accvgpr_write_b32 a66, v3\nv_accvgpr_write_b32 a67, v3\nv_accvgpr_write_b32 a80, v3\n"
"v_accvgpr_write_b32 a81, v3\nv_accvgpr_write_b32 a82, v3\nv_accvgpr_write_b32 a83, v3\nv_accvgpr_write_b32 a96, v3\nv_accvgpr_write_b32 a97, v3\nv_accvgpr_write_b32 a98, v3\nv_accvgpr_write_b32 a99, v3\nv_accvgpr_write_b32 a4, v3\n"
"v_accvgpr_write_b32 a5, v3\nv_accvgpr_write_b32 a6, v3\nv_accvgpr_write_b32 a7, v3\nv_accvgpr_write_b32 a20, v3\nv_accvgpr_write_b32 a21, v3\nv_accvgpr_write_b32 a22, v3\nv_accvgpr_write_b32 a23, v3\nv_accvgpr_write_b32 a36, v3\n"
"v_accvgpr_write_b32 a37, v3\nv_accvgpr_write_b32 a38, v3\nv_accvgpr_write_b32 a39, v3\n.Lround:\ns_cmp_lg_u32 s67, 0\ns_cselect_b64 vcc, -1, 0\ns_waitcnt vmcnt(20)\ns_barrier\n"
"Qta 112,61,0\nQta 114,61,1024\nQdv 24,63,33024\nQta 116,61,8\nQta 118,61,1032\nQdv 28,63,34048\nQta 120,61,256\nQta 122,61,1280\n"
"Qdv 32,63,35072\nQta 124,61,264\nQta 126,61,1288\nQdv 36,63,36096\nQta 128,61,512\nQta 130,61,1536\nQdv 40,63,37120\nQta 132,61,520\n"
"Qta 134,61,1544\nQdv 44,63,38144\nQta 136,61,768\nQta 138,61,1792\nQdv 48,63,39168\nQta 140,61,776\nQta 142,61,1800\nQda 176,63,33536\n"
"Qda 180,63,34560\nQda 184,63,35584\nQda 188,63,36608\nQda 192,63,37632\nQda 196,63,38656\nQda 200,63,39680\ns_waitcnt lgkmcnt(7)\nQzvav 80,112,24\n"
"Qzvav 84,116,24\ns_add_u32 s68, s71, 0\nQzvav 88,120,24\nbuffer_store_dwordx4 a[0:3], v60, s[56:59], s68 offen offset:0 sc1\nQzvav 92,124,24\nQzvav 96,128,24\ns_add_u32 s68, s71, 14336\nQzvav 100,132,24\n"
"buffer_store_dwordx4 a[4:7], v60, s[56:59], s68 offen offset:0 sc1\nQzvav 104,136,24\nQzvav 108,140,24\ns_waitcnt lgkmcnt(0)\nQzvav 112,112,28\ns_waitcnt vmcnt(17)\ns_barrier\nQzvav 116,116,28\n"
"Qta 144,61,16448\ns_add_u32 m0, s62, 0\ns_add_u32 s69, s87, 1048576\nQzvav 120,120,28\nbuffer_load_dwordx4 v62, s[52:55], s69 offen lds\nQzvav 124,124,28\nQta 146,61,17472\nQzvav 128,128,28\n"
"Qzvav 132,132,28\nQta 148,61,16456\ns_add_u32 s69, s87, 1080320\nQzvav 136,136,28\nbuffer_load_dwordx4 v62, s[52:55], s69 offen offset:1024 lds\nQzvav 140,140,28\nQta 150,61,17480\nQzvav 144,112,32\n"
"Qzvav 148,116,32\nQta 152,61,16704\ns_add_u32 s69, s87, 1112056\nQzvav 152,120,32\nbuffer_load_dwordx4 v62, s[52:55], s69 offen offset:2056 lds\nQzvav 156,124,32\nQta 154,61,17728\nQzvav 160,128,32\n"
"Qzvav 164,132,32\nQta 156,61,16712\ns_add_u32 s69, s87, 1143800\nQzvav 168,136,32\nbuffer_load_dwordx4 v62, s[52:55], s69 offen offset:3080 lds\nQzvav 172,140,32\nQta 158,61,17736\nQzvav 176,112,36\n"
"Qzvav 180,116,36\nQta 160,61,16960\ns_add_u32 m0, s63, 33024\ns_add_u32 s88, s86, 256\nQzvav 184,120,36\nbuffer_load_dwordx4 v64, s[48:51], s88 offen lds\nQzvav 188,124,36\nQta 162,61,17984\n"
"Qzvav 192,128,36\nQzvav 196,132,36\nQta 164,61,16968\ns_add_u32 s88, s86, 19712\nQzvav 200,136,36\nbuffer_load_dwordx4 v64, s[48:51], s88 offen offset:1024 lds\nQzvav 204,140,36\nQta 166,61,17992\n"
"Qzvav 208,112,40\nQzvav 212,116,40\nQta 168,61,17216\ns_add_u32 s88, s86, 39168\nQzvav 216,120,40\nbuffer_load_dwordx4 v64, s[48:51], s88 offen offset:2048 lds\nQzvav 220,124,40\nQta 170,61,18240\n"
"Qzvav 224,128,40\nQzvav 228,132,40\nQta 172,61,17224\ns_add_u32 s88, s86, 58624\nQzvav 232,136,40\nbuffer_load_dwordx4 v64, s[48:51], s88 offen offset:3072 lds\nQzvav 236,140,40\nQta 174,61,18248\n"
"Qzvav 240,112,44\nQzvav 244,116,44\ns_add_u32 m0, s63, 37120\ns_add_u32 s88, s86, 82176\nQzvav 248,120,44\nbuffer_load_dwordx4 v64, s[48:51], s88 offen lds\nQzvav 252,124,44\nQzaav 208,128,44\n"
"Qzaav 212,132,44\ns_add_u32 s88, s86, 101632\nQzaav 216,136,44\nbuffer_load_dwordx4 v64, s[48:51], s88 offen offset:1024 lds\nQzaav 220,140,44\nQzaav 224,112,48\nQzaav 228,116,48\ns_add_u32 s88, s86, 121088\n"
"Qzaav 232,120,48\nbuffer_load_dwordx4 v64, s[48:51], s88 offen offset:2048 lds\nQzaav 236,124,48\nQzaav 240,128,48\nQzaav 244,132,48\nQzaav 248,136,48\nQzaav 252,140,48\ns_waitcnt lgkmcnt(0)\n"
"Qmvaa 80,144,176\nQmvaa 84,148,176\ns_add_u32 s68, s71, 28672\nQmvaa 88,152,176\nbuffer_store_dwordx4 a[8:11], v60, s[56:59], s68 offen offset:0 sc1\nQmvaa 92,156,176\nQmvaa 96,160,176\ns_add_u32 s68, s71, 43008\n"
"Qmvaa 100,164,176\nbuffer_store_dwordx4 a[12:15], v60, s[56:59], s68 offen offset:0 sc1\nQmvaa 104,168,176\nQmvaa 108,172,176\ns_waitcnt lgkmcnt(0)\nQmvaa 112,144,180\ns_waitcnt vmcnt(15)\ns_barrier\n"
"Qmvaa 116,148,180\nQda 176,63,512\ns_add_u32 m0, s62, 16448\ns_add_u32 s69, s87, 1310720\nQmvaa 120,152,180\nbuffer_load_dwordx4 v62, s[52:55], s69 offen lds\nQmvaa 124,156,180\n.Lpg0:\n"
"s_cbranch_execz .Ltr0\nQta 112,61,33024\nQmvaa 128,160,180\nQta 114,61,34048\nQmvaa 132,164,180\nQta 116,61,33032\ns_add_u32 s69, s87, 1342464\nQmvaa 136,168,180\n"
"buffer_load_dwordx4 v62, s[52:55], s69 offen offset:1024 lds\nQmvaa 140,172,180\nQda 180,63,1536\nQmvaa 144,144,184\nQta 118,61,34056\nQmvaa 148,148,184\nQdv 24,63,0\ns_add_u32 s69, s87, 1374200\n"
"Qmvaa 152,152,184\nbuffer_load_dwordx4 v62, s[52:55], s69 offen offset:2056 lds\nQmvaa 156,156,184\nQta 120,61,33280\nQmvaa 160,160,184\nQta 122,61,34304\nQmvaa 164,164,184\nQta 124,61,33288\n"
"s_add_u32 s69, s87, 1405944\nQmvaa 168,168,184\nbuffer_load_dwordx4 v62, s[52:55], s69 offen offset:3080 lds\nQmvaa 172,172,184\nQda 184,63,2560\nQmvaa 176,144,188\nQta 126,61,34312\nQmvaa 180,148,188\n"
"Qta 128,61,33536\ns_add_u32 m0, s61, 0\nQmvaa 184,152,188\ns_cbranch_vccnz .Ld0q0\nbuffer_load_dwordx4 v56, s[40:43], s60 offen nt lds\n.Ld0q0:\nQmvaa 188,156,188\nQta 130,61,34560\n"
"Qmvaa 192,160,188\nQta 132,61,33544\nQmvaa 196,164,188\nQta 134,61,34568\nQmvaa 200,168,188\nQta 136,61,33792\nQmvaa 204,172,188\nQda 188,63,3584\n"
"Qmvaa 208,144,192\nQta 138,61,34816\nQmvaa 212,148,192\nQta 140,61,33800\nQmvaa 216,152,192\nQta 142,61,34824\nQmvaa 220,156,192\nQdv 28,63,1024\n"
"Qmvaa 224,160,192\nQdv 32,63,2048\nQmvaa 228,164,192\nQdv 36,63,3072\nQmvaa 232,168,192\nQdv 40,63,4096\nQmvaa 236,172,192\nQda 192,63,4608\n"
"Qmvaa 240,144,196\nQdv 44,63,5120\nQmvaa 244,148,196\nQdv 48,63,6144\nQmvaa 248,152,196\nQmvaa 252,156,196\nQmaaa 208,160,196\nQmaaa 212,164,196\n"
"Qmaaa 216,168,196\nQmaaa 220,172,196\nQda 196,63,5632\nQmaaa 224,144,200\nQmaaa 228,148,200\nQmaaa 232,152,200\nQmaaa 236,156,200\nQmaaa 240,160,200\n"
"Qmaaa 244,164,200\nQmaaa 248,168,200\nQmaaa 252,172,200\nQda 200,63,6656\ns_waitcnt lgkmcnt(9)\nQmvav 80,112,24\nQmvav 84,116,24\ns_add_u32 s68, s71, 0\n"
"Qmvav 88,120,24\nbuffer_store_dwordx4 a[16:19], v60, s[56:59], s68 offen offset:1024 sc1\nQmvav 92,124,24\nQmvav 96,128,24\ns_add_u32 s68, s71, 14336\nQmvav 100,132,24\nbuffer_store_dwordx4 a[20:23], v60, s[56:59], s68 offen offset:1024 sc1\nQmvav 104,136,24\n"
"Qmvav 108,140,24\ns_waitcnt lgkmcnt(0)\nQmvav 112,112,28\ns_waitcnt vmcnt(22)\ns_barrier\nQmvav 116,116,28\nQta 144,61,49472\ns_add_u32 m0, s62, 33024\n"
"s_add_u32 s69, s87, 1572864\nQmvav 120,120,28\nbuffer_load_dwordx4 v62, s[52:55], s69 offen lds\nQmvav 124,124,28\nQta 146,61,50496\nQmvav 128,128,28\nQmvav 132,132,28\nQta 148,61,49480\n"
"s_add_u32 s69, s87, 1604608\nQmvav 136,136,28\nbuffer_load_dwordx4 v62, s[52:55], s69 offen offset:1024 lds\nQmvav 140,140,28\nQta 150,61,50504\nQmvav 144,112,32\nQmvav 148,116,32\nQta 152,61,49728\n"
"s_add_u32 s69, s87, 1636344\nQmvav 152,120,32\nbuffer_load_dwordx4 v62, s[52:55], s69 offen offset:2056 lds\nQmvav 156,124,32\nQta 154,61,50752\nQmvav 160,128,32\nQmvav 164,132,32\nQta 156,61,49736\n"
"s_add_u32 s69, s87, 1668088\nQmvav 168,136,32\nbuffer_load_dwordx4 v62, s[52:55], s69 offen offset:3080 lds\nQmvav 172,140,32\nQta 158,61,50760\nQmvav 176,112,36\nQmvav 180,116,36\nQta 160,61,49984\n"
"s_add_u32 m0, s63, 0\ns_add_u32 s88, s86, 384\nQmvav 184,120,36\nbuffer_load_dwordx4 v64, s[48:51], s88 offen lds\nQmvav 188,124,36\nQta 162,61,51008\nQmvav 192,128,36\nQmvav 196,132,36\n"
"Qta 164,61,49992\ns_add_u32 s88, s86, 19840\nQmvav 200,136,36\nbuffer_load_dwordx4 v64, s[48:51], s88 offen offset:1024 lds\nQmvav 204,140,36\nQta 166,61,51016\nQmvav 208,112,40\nQmvav 212,116,40\n"
"Qta 168,61,50240\ns_add_u32 s88, s86, 39296\nQmvav 216,120,40\nbuffer_load_dwordx4 v64, s[48:51], s88 offen offset:2048 lds\nQmvav 220,124,40\nQta 170,61,51264\nQmvav 224,128,40\nQmvav 228,132,40\n"
"Qta 172,61,50248\ns_add_u32 s88, s86, 58752\nQmvav 232,136,40\nbuffer_load_dwordx4 v64, s[48:51], s88 offen offset:3072 lds\nQmvav 236,140,40\nQta 174,61,51272\nQmvav 240,112,44\nQmvav 244,116,44\n"
"s_add_u32 m0, s63, 4096\ns_add_u32 s88, s86, 82304\nQmvav 248,120,44\nbuffer_load_dwordx4 v64, s[48:51], s88 offen lds\nQmvav 252,124,44\nQmaav 208,128,44\nQmaav 212,132,44\ns_add_u32 s88, s86, 101760\n"
"Qmaav 216,136,44\nbuffer_load_dwordx4 v64, s[48:51], s88 offen offset:1024 lds\nQmaav 220,140,44\nQmaav 224,112,48\nQmaav 228,116,48\ns_add_u32 s88, s86, 121216\nQmaav 232,120,48\nbuffer_load_dwordx4 v64, s[48:51], s88 offen offset:2048 lds\n"
"Qmaav 236,124,48\nQmaav 240,128,48\nQmaav 244,132,48\nQmaav 248,136,48\nQmaav 252,140,48\ns_waitcnt lgkmcnt(0)\nQmvaa 80,144,176\nQmvaa 84,148,176\n"
"s_add_u32 s68, s71, 28672\nQmvaa 88,152,176\nbuffer_store_dwordx4 a[24:27], v60, s[56:59], s68 offen offset:1024 sc1\nQmvaa 92,156,176\nQmvaa 96,160,176\ns_add_u32 s68, s71, 43008\nQmvaa 100,164,176\nbuffer_store_dwordx4 a[28:31], v60, s[56:59], s68 offen offset:1024 sc1\n"
"Qmvaa 104,168,176\nQmvaa 108,172,176\ns_waitcnt lgkmcnt(0)\nQmvaa 112,144,180\ns_waitcnt vmcnt(21)\ns_barrier\nQmvaa 116,148,180\nQda 176,63,33536\n"
"s_add_u32 m0, s62, 49472\ns_add_u32 s69, s87, 1835008\nQmvaa 120,152,180\nbuffer_load_dwordx4 v62, s[52:55], s69 offen lds\nQmvaa 124,156,180\nQta 112,61,0\nQmvaa 128,160,180\nQta 114,61,1024\n"
"Qmvaa 132,164,180\nQta 116,61,8\ns_add_u32 s69, s87, 1866752\nQmvaa 136,168,180\nbuffer_load_dwordx4 v62, s[52:55], s69 offen offset:1024 lds\nQmvaa 140,172,180\nQda 180,63,34560\nQmvaa 144,144,184\n"
"Qta 118,61,1032\nQmvaa 148,148,184\nQdv 24,63,33024\ns_add_u32 s69, s87, 1898488\nQmvaa 152,152,184\nbuffer_load_dwordx4 v62, s[52:55], s69 offen offset:2056 lds\nQmvaa 156,156,184\nQta 120,61,256\n"
"Qmvaa 160,160,184\nQta 122,61,1280\nQmvaa 164,164,184\nQta 124,61,264\ns_add_u32 s69, s87, 1930232\nQmvaa 168,168,184\nbuffer_load_dwordx4 v62, s[52:55], s69 offen offset:3080 lds\nQmvaa 172,172,184\n"
"Qda 184,63,35584\nQmvaa 176,144,188\nQta 126,61,1288\nQmvaa 180,148,188\nQta 128,61,512\ns_add_u32 m0, s61, 0\nQmvaa 184,152,188\ns_cbranch_vccnz .Ld0q1\n"
"buffer_load_dwordx4 v57, s[40:43], s60 offen offset:1024 nt lds\n.Ld0q1:\nQmvaa 188,156,188\nQta 130,61,1536\nQmvaa 192,160,188\nQta 132,61,520\nQmvaa 196,164,188\nQta 134,61,1544\n"
"Qmvaa 200,168,188\nQta 136,61,768\nQmvaa 204,172,188\nQda 188,63,36608\nQmvaa 208,144,192\nQta 138,61,1792\nQmvaa 212,148,192\nQta 140,61,776\n"
"Qmvaa 216,152,192\nQta 142,61,1800\nQmvaa 220,156,192\nQdv 28,63,34048\nQmvaa 224,160,192\nQdv 32,63,35072\nQmvaa 228,164,192\nQdv 36,63,36096\n"
"Qmvaa 232,168,192\nQdv 40,63,37120\nQmvaa 236,172,192\nQda 192,63,37632\nQmvaa 240,144,196\nQdv 44,63,38144\nQmvaa 244,148,196\nQdv 48,63,39168\n"
"Qmvaa 248,152,196\nQmvaa 252,156,196\nQmaaa 208,160,196\nQmaaa 212,164,196\nQmaaa 216,168,196\nQmaaa 220,172,196\nQda 196,63,38656\nQmaaa 224,144,200\n"
"Qmaaa 228,148,200\nQmaaa 232,152,200\nQmaaa 236,156,200\nQmaaa 240,160,200\nQmaaa 244,164,200\nQmaaa 248,168,200\nQmaaa 252,172,200\nQda 200,63,39680\n"
"s_waitcnt lgkmcnt(9)\nQmvav 80,112,24\nQmvav 84,116,24\ns_add_u32 s68, s71, 0\nQmvav 88,120,24\nbuffer_store_dwordx4 a[32:35], v60, s[56:59], s68 offen offset:2048 sc1\nQmvav 92,124,24\nQmvav 96,128,24\n"
"s_add_u32 s68, s71, 14336\nQmvav 100,132,24\nbuffer_store_dwordx4 a[36:39], v60, s[56:59], s68 offen offset:2048 sc1\nQmvav 104,136,24\nQmvav 108,140,24\ns_waitcnt lgkmcnt(0)\nQmvav 112,112,28\ns_waitcnt vmcnt(21)\n"
"s_barrier\nQmvav 116,116,28\nQta 144,61,16448\ns_add_u32 m0, s62, 0\ns_add_u32 s69, s87, 2097152\nQmvav 120,120,28\nbuffer_load_dwordx4 v62, s[52:55], s69 offen lds\nQmvav 124,124,28\n"
"Qta 146,61,17472\nQmvav 128,128,28\nQmvav 132,132,28\nQta 148,61,16456\ns_add_u32 s69, s87, 2128896\nQmvav 136,136,28\nbuffer_load_dwordx4 v62, s[52:55], s69 offen offset:1024 lds\nQmvav 140,140,28\n"
"Qta 150,61,17480\nQmvav 144,112,32\nQmvav 148,116,32\nQta 152,61,16704\ns_add_u32 s69, s87, 2160632\nQmvav 152,120,32\nbuffer_load_dwordx4 v62, s[52:55], s69 offen offset:2056 lds\nQmvav 156,124,32\n"
"Qta 154,61,17728\nQmvav 160,128,32\nQmvav 164,132,32\nQta 156,61,16712\ns_add_u32 s69, s87, 2192376\nQmvav 168,136,32\nbuffer_load_dwordx4 v62, s[52:55], s69 offen offset:3080 lds\nQmvav 172,140,32\n"
"Qta 158,61,17736\nQmvav 176,112,36\nQmvav 180,116,36\nQta 160,61,16960\ns_add_u32 m0, s63, 33024\ns_add_u32 s88, s86, 512\nQmvav 184,120,36\nbuffer_load_dwordx4 v64, s[48:51], s88 offen lds\n"
"Qmvav 188,124,36\nQta 162,61,17984\nQmvav 192,128,36\nQmvav 196,132,36\nQta 164,61,16968\ns_add_u32 s88, s86, 19968\nQmvav 200,136,36\nbuffer_load_dwordx4 v64, s[48:51], s88 offen offset:1024 lds\n"
"Qmvav 204,140,36\nQta 166,61,17992\nQmvav 208,112,40\nQmvav 212,116,40\nQta 168,61,17216\ns_add_u32 s88, s86, 39424\nQmvav 216,120,40\nbuffer_load_dwordx4 v64, s[48:51], s88 offen offset:2048 lds\n"
"Qmvav 220,124,40\nQta 170,61,18240\nQmvav 224,128,40\nQmvav 228,132,40\nQta 172,61,17224\ns_add_u32 s88, s86, 58880\nQmvav 232,136,40\nbuffer_load_dwordx4 v64, s[48:51], s88 offen offset:3072 lds\n"
"Qmvav 236,140,40\nQta 174,61,18248\nQmvav 240,112,44\nQmvav 244,116,44\ns_add_u32 m0, s63, 37120\ns_add_u32 s88, s86, 82432\nQmvav 248,120,44\nbuffer_load_dwordx4 v64, s[48:51], s88 offen lds\n"
"Qmvav 252,124,44\nQmaav 208,128,44\nQmaav 212,132,44\ns_add_u32 s88, s86, 101888\nQmaav 216,136,44\nbuffer_load_dwordx4 v64, s[48:51], s88 offen offset:1024 lds\nQmaav 220,140,44\nQmaav 224,112,48\n"
"Qmaav 228,116,48\ns_add_u32 s88, s86, 121344\nQmaav 232,120,48\nbuffer_load_dwordx4 v64, s[48:51], s88 offen offset:2048 lds\nQmaav 236,124,48\nQmaav 240,128,48\nQmaav 244,132,48\nQmaav 248,136,48\n"
"Qmaav 252,140,48\ns_waitcnt lgkmcnt(0)\nQmvaa 80,144,176\nQmvaa 84,148,176\ns_add_u32 s68, s71, 28672\nQmvaa 88,152,176\nbuffer_store_dwordx4 a[40:43], v60, s[56:59], s68 offen offset:2048 sc1\nQmvaa 92,156,176\n"
"Qmvaa 96,160,176\ns_add_u32 s68, s71, 43008\nQmvaa 100,164,176\nbuffer_store_dwordx4 a[44:47], v60, s[56:59], s68 offen offset:2048 sc1\nQmvaa 104,168,176\nQmvaa 108,172,176\ns_waitcnt lgkmcnt(0)\nQmvaa 112,144,180\n"
"s_waitcnt vmcnt(21)\ns_barrier\nQmvaa 116,148,180\nQda 176,63,512\ns_add_u32 m0, s62, 16448\ns_add_u32 s69, s87, 2359296\nQmvaa 120,152,180\nbuffer_load_dwordx4 v62, s[52:55], s69 offen lds\n"
"Qmvaa 124,156,180\nQta 112,61,33024\nQmvaa 128,160,180\nQta 114,61,34048\nQmvaa 132,164,180\nQta 116,61,33032\ns_add_u32 s69, s87, 2391040\nQmvaa 136,168,180\n"
"buffer_load_dwordx4 v62, s[52:55], s69 offen offset:1024 lds\nQmvaa 140,172,180\nQda 180,63,1536\nQmvaa 144,144,184\nQta 118,61,34056\nQmvaa 148,148,184\nQdv 24,63,0\ns_add_u32 s69, s87, 2422776\n"
"Qmvaa 152,152,184\nbuffer_load_dwordx4 v62, s[52:55], s69 offen offset:2056 lds\nQmvaa 156,156,184\nQta 120,61,33280\nQmvaa 160,160,184\nQta 122,61,34304\nQmvaa 164,164,184\nQta 124,61,33288\n"
"s_add_u32 s69, s87, 2454520\nQmvaa 168,168,184\nbuffer_load_dwordx4 v62, s[52:55], s69 offen offset:3080 lds\nQmvaa 172,172,184\nQda 184,63,2560\nQmvaa 176,144,188\nQta 126,61,34312\nQmvaa 180,148,188\n"
"Qta 128,61,33536\ns_add_u32 m0, s61, 0\nQmvaa 184,152,188\ns_cbranch_vccnz .Ld0q2\nbuffer_load_dwordx4 v56, s[40:43], s60 offen offset:2048 nt lds\n.Ld0q2:\nQmvaa 188,156,188\nQta 130,61,34560\n"
"Qmvaa 192,160,188\nQta 132,61,33544\nQmvaa 196,164,188\nQta 134,61,34568\nQmvaa 200,168,188\nQta 136,61,33792\nQmvaa 204,172,188\nQda 188,63,3584\n"
"Qmvaa 208,144,192\nQta 138,61,34816\nQmvaa 212,148,192\nQta 140,61,33800\nQmvaa 216,152,192\nQta 142,61,34824\nQmvaa 220,156,192\nQdv 28,63,1024\n"
"Qmvaa 224,160,192\nQdv 32,63,2048\nQmvaa 228,164,192\nQdv 36,63,3072\nQmvaa 232,168,192\nQdv 40,63,4096\nQmvaa 236,172,192\nQda 192,63,4608\n"
"Qmvaa 240,144,196\nQdv 44,63,5120\nQmvaa 244,148,196\nQdv 48,63,6144\nQmvaa 248,152,196\nQmvaa 252,156,196\nQmaaa 208,160,196\nQmaaa 212,164,196\n"
"Qmaaa 216,168,196\nQmaaa 220,172,196\nQda 196,63,5632\nQmaaa 224,144,200\nQmaaa 228,148,200\nQmaaa 232,152,200\nQmaaa 236,156,200\nQmaaa 240,160,200\n"
"Qmaaa 244,164,200\nQmaaa 248,168,200\nQmaaa 252,172,200\nQda 200,63,6656\ns_waitcnt lgkmcnt(9)\nQmvav 80,112,24\nQmvav 84,116,24\ns_add_u32 s68, s71, 0\n"
"Qmvav 88,120,24\nbuffer_store_dwordx4 a[48:51], v60, s[56:59], s68 offen offset:3072 sc1\nQmvav 92,124,24\nQmvav 96,128,24\ns_add_u32 s68, s71, 14336\nQmvav 100,132,24\nbuffer_store_dwordx4 a[52:55], v60, s[56:59], s68 offen offset:3072 sc1\nQmvav 104,136,24\n"
"Qmvav 108,140,24\ns_waitcnt lgkmcnt(0)\nQmvav 112,112,28\ns_waitcnt vmcnt(21)\ns_barrier\nQmvav 116,116,28\nQta 144,61,49472\ns_add_u32 m0, s62, 33024\n"
"s_add_u32 s69, s87, 2621440\nQmvav 120,120,28\nbuffer_load_dwordx4 v62, s[52:55], s69 offen lds\n.Lpg1:\ns_cbranch_execz .Ltr0\nQmvav 124,124,28\nQta 146,61,50496\nQmvav 128,128,28\n"
"Qmvav 132,132,28\nQta 148,61,49480\ns_add_u32 s69, s87, 2653184\nQmvav 136,136,28\nbuffer_load_dwordx4 v62, s[52:55], s69 offen offset:1024 lds\nQmvav 140,140,28\nQta 150,61,50504\nQmvav 144,112,32\n"
"Qmvav 148,116,32\nQta 152,61,49728\ns_add_u32 s69, s87, 2684920\nQmvav 152,120,32\nbuffer_load_dwordx4 v62, s[52:55], s69 offen offset:2056 lds\nQmvav 156,124,32\nQta 154,61,50752\nQmvav 160,128,32\n"
"Qmvav 164,132,32\nQta 156,61,49736\ns_add_u32 s69, s87, 2716664\nQmvav 168,136,32\nbuffer_load_dwordx4 v62, s[52:55], s69 offen offset:3080 lds\nQmvav 172,140,32\nQta 158,61,50760\nQmvav 176,112,36\n"
"Qmvav 180,116,36\nQta 160,61,49984\ns_add_u32 m0, s63, 0\ns_add_u32 s88, s86, 640\nQmvav 184,120,36\nbuffer_load_dwordx4 v64, s[48:51], s88 offen lds\nQmvav 188,124,36\nQta 162,61,51008\n"
"Qmvav 192,128,36\nQmvav 196,132,36\nQta 164,61,49992\ns_add_u32 s88, s86, 20096\nQmvav 200,136,36\nbuffer_load_dwordx4 v64, s[48:51], s88 offen offset:1024 lds\nQmvav 204,140,36\nQta 166,61,51016\n"
"Qmvav 208,112,40\nQmvav 212,116,40\nQta 168,61,50240\ns_add_u32 s88, s86, 39552\nQmvav 216,120,40\nbuffer_load_dwordx4 v64, s[48:51], s88 offen offset:2048 lds\nQmvav 220,124,40\nQta 170,61,51264\n"
"Qmvav 224,128,40\nQmvav 228,132,40\nQta 172,61,50248\ns_add_u32 s88, s86, 59008\nQmvav 232,136,40\nbuffer_load_dwordx4 v64, s[48:51], s88 offen offset:3072 lds\nQmvav 236,140,40\nQta 174,61,51272\n"
"Qmvav 240,112,44\nQmvav 244,116,44\ns_add_u32 m0, s63, 4096\ns_add_u32 s88, s86, 82560\nQmvav 248,120,44\nbuffer_load_dwordx4 v64, s[48:51], s88 offen lds\nQmvav 252,124,44\nQmaav 208,128,44\n"
"Qmaav 212,132,44\ns_add_u32 s88, s86, 102016\nQmaav 216,136,44\nbuffer_load_dwordx4 v64, s[48:51], s88 offen offset:1024 lds\nQmaav 220,140,44\nQmaav 224,112,48\nQmaav 228,116,48\ns_add_u32 s88, s86, 121472\n"
"Qmaav 232,120,48\nbuffer_load_dwordx4 v64, s[48:51], s88 offen offset:2048 lds\nQmaav 236,124,48\nQmaav 240,128,48\nQmaav 244,132,48\nQmaav 248,136,48\nQmaav 252,140,48\ns_waitcnt lgkmcnt(0)\n"
"Qmvaa 80,144,176\nQmvaa 84,148,176\ns_add_u32 s68, s71, 28672\nQmvaa 88,152,176\nbuffer_store_dwordx4 a[56:59], v60, s[56:59], s68 offen offset:3072 sc1\nQmvaa 92,156,176\nQmvaa 96,160,176\ns_add_u32 s68, s71, 43008\n"
"Qmvaa 100,164,176\nbuffer_store_dwordx4 a[60:63], v60, s[56:59], s68 offen offset:3072 sc1\nQmvaa 104,168,176\nQmvaa 108,172,176\ns_waitcnt lgkmcnt(0)\nQmvaa 112,144,180\ns_waitcnt vmcnt(21)\ns_barrier\n"
"Qmvaa 116,148,180\nQda 176,63,33536\ns_add_u32 m0, s62, 49472\ns_add_u32 s69, s87, 2883584\nQmvaa 120,152,180\nbuffer_load_dwordx4 v62, s[52:55], s69 offen lds\nQmvaa 124,156,180\nQta 112,61,0\n"
"Qmvaa 128,160,180\nQta 114,61,1024\nQmvaa 132,164,180\nQta 116,61,8\ns_add_u32 s69, s87, 2915328\nQmvaa 136,168,180\nbuffer_load_dwordx4 v62, s[52:55], s69 offen offset:1024 lds\nQmvaa 140,172,180\n"
"Qda 180,63,34560\nQmvaa 144,144,184\nQta 118,61,1032\nQmvaa 148,148,184\nQdv 24,63,33024\ns_add_u32 s69, s87, 2947064\nQmvaa 152,152,184\nbuffer_load_dwordx4 v62, s[52:55], s69 offen offset:2056 lds\n"
"Qmvaa 156,156,184\nQta 120,61,256\nQmvaa 160,160,184\nQta 122,61,1280\nQmvaa 164,164,184\nQta 124,61,264\ns_add_u32 s69, s87, 2978808\nQmvaa 168,168,184\n"
"buffer_load_dwordx4 v62, s[52:55], s69 offen offset:3080 lds\nQmvaa 172,172,184\nQda 184,63,35584\nQmvaa 176,144,188\nQta 126,61,1288\nQmvaa 180,148,188\nQta 128,61,512\ns_add_u32 m0, s61, 0\n"
"Qmvaa 184,152,188\ns_cbranch_vccnz .Ld0q3\nbuffer_load_dwordx4 v57, s[40:43], s60 offen offset:3072 nt lds\n.Ld0q3:\nQmvaa 188,156,188\nQta 130,61,1536\nQmvaa 192,160,188\nQta 132,61,520\n"
"Qmvaa 196,164,188\nQta 134,61,1544\nQmvaa 200,168,188\nQta 136,61,768\nQmvaa 204,172,188\nQda 188,63,36608\nQmvaa 208,144,192\nQta 138,61,1792\n"
"Qmvaa 212,148,192\nQta 140,61,776\nQmvaa 216,152,192\nQta 142,61,1800\nQmvaa 220,156,192\nQdv 28,63,34048\nQmvaa 224,160,192\nQdv 32,63,35072\n"
"Qmvaa 228,164,192\nQdv 36,63,36096\nQmvaa 232,168,192\nQdv 40,63,37120\nQmvaa 236,172,192\nQda 192,63,37632\nQmvaa 240,144,196\nQdv 44,63,38144\n"
"Qmvaa 244,148,196\nQdv 48,63,39168\nQmvaa 248,152,196\nQmvaa 252,156,196\nQmaaa 208,160,196\nQmaaa 212,164,196\nQmaaa 216,168,196\nQmaaa 220,172,196\n"
"Qda 196,63,38656\nQmaaa 224,144,200\nQmaaa 228,148,200\nQmaaa 232,152,200\nQmaaa 236,156,200\nQmaaa 240,160,200\nQmaaa 244,164,200\nQmaaa 248,168,200\n"
"Qmaaa 252,172,200\nQda 200,63,39680\ns_waitcnt lgkmcnt(9)\nQmvav 80,112,24\nQmvav 84,116,24\ns_add_u32 s68, s71, 4096\nQmvav 88,120,24\nbuffer_store_dwordx4 a[64:67], v60, s[56:59], s68 offen offset:0 sc1\n"
"Qmvav 92,124,24\nQmvav 96,128,24\ns_add_u32 s68, s71, 18432\nQmvav 100,132,24\nbuffer_store_dwordx4 a[68:71], v60, s[56:59], s68 offen offset:0 sc1\nQmvav 104,136,24\nQmvav 108,140,24\ns_waitcnt lgkmcnt(0)\n"
"Qmvav 112,112,28\ns_waitcnt vmcnt(21)\ns_barrier\nQmvav 116,116,28\nQta 144,61,16448\ns_add_u32 m0, s62, 0\ns_add_u32 s69, s87, 3145728\nQmvav 120,120,28\n"
"buffer_load_dwordx4 v62, s[52:55], s69 offen lds\nQmvav 124,124,28\nQta 146,61,17472\nQmvav 128,128,28\nQmvav 132,132,28\nQta 148,61,16456\ns_add_u32 s69, s87, 3177472\nQmvav 136,136,28\n"
"buffer_load_dwordx4 v62, s[52:55], s69 offen offset:1024 lds\nQmvav 140,140,28\nQta 150,61,17480\nQmvav 144,112,32\nQmvav 148,116,32\nQta 152,61,16704\ns_add_u32 s69, s87, 3209208\nQmvav 152,120,32\n"
"buffer_load_dwordx4 v62, s[52:55], s69 offen offset:2056 lds\nQmvav 156,124,32\nQta 154,61,17728\nQmvav 160,128,32\nQmvav 164,132,32\nQta 156,61,16712\ns_add_u32 s69, s87, 3240952\nQmvav 168,136,32\n"
"buffer_load_dwordx4 v62, s[52:55], s69 offen offset:3080 lds\nQmvav 172,140,32\nQta 158,61,17736\nQmvav 176,112,36\nQmvav 180,116,36\nQta 160,61,16960\ns_add_u32 m0, s63, 33024\ns_add_u32 s88, s86, 768\n"
"Qmvav 184,120,36\nbuffer_load_dwordx4 v64, s[48:51], s88 offen lds\nQmvav 188,124,36\nQta 162,61,17984\nQmvav 192,128,36\nQmvav 196,132,36\nQta 164,61,16968\ns_add_u32 s88, s86, 20224\n"
"Qmvav 200,136,36\nbuffer_load_dwordx4 v64, s[48:51], s88 offen offset:1024 lds\nQmvav 204,140,36\nQta 166,61,17992\nQmvav 208,112,40\nQmvav 212,116,40\nQta 168,61,17216\ns_add_u32 s88, s86, 39680\n"
"Qmvav 216,120,40\nbuffer_load_dwordx4 v64, s[48:51], s88 offen offset:2048 lds\nQmvav 220,124,40\nQta 170,61,18240\nQmvav 224,128,40\nQmvav 228,132,40\nQta 172,61,17224\ns_add_u32 s88, s86, 59136\n"
"Qmvav 232,136,40\nbuffer_load_dwordx4 v64, s[48:51], s88 offen offset:3072 lds\nQmvav 236,140,40\nQta 174,61,18248\nQmvav 240,112,44\nQmvav 244,116,44\ns_add_u32 m0, s63, 37120\ns_add_u32 s88, s86, 82688\n"
"Qmvav 248,120,44\nbuffer_load_dwordx4 v64, s[48:51], s88 offen lds\nQmvav 252,124,44\nQmaav 208,128,44\nQmaav 212,132,44\ns_add_u32 s88, s86, 102144\nQmaav 216,136,44\nbuffer_load_dwordx4 v64, s[48:51], s88 offen offset:1024 lds\n"
"Qmaav 220,140,44\nQmaav 224,112,48\nQmaav 228,116,48\ns_add_u32 s88, s86, 121600\nQmaav 232,120,48\nbuffer_load_dwordx4 v64, s[48:51], s88 offen offset:2048 lds\nQmaav 236,124,48\nQmaav 240,128,48\n"
"Qmaav 244,132,48\nQmaav 248,136,48\nQmaav 252,140,48\ns_waitcnt lgkmcnt(0)\nQmvaa 80,144,176\nQmvaa 84,148,176\ns_add_u32 s68, s71, 32768\nQmvaa 88,152,176\n"
"buffer_store_dwordx4 a[72:75], v60, s[56:59], s68 offen offset:0 sc1\nQmvaa 92,156,176\nQmvaa 96,160,176\ns_add_u32 s68, s71, 47104\nQmvaa 100,164,176\nbuffer_store_dwordx4 a[76:79], v60, s[56:59], s68 offen offset:0 sc1\nQmvaa 104,168,176\nQmvaa 108,172,176\n"
"s_waitcnt lgkmcnt(0)\nQmvaa 112,144,180\ns_waitcnt vmcnt(21)\ns_barrier\nQmvaa 116,148,180\nQda 176,63,512\ns_add_u32 m0, s62, 16448\ns_add_u32 s69, s87, 3407872\n"
"Qmvaa 120,152,180\nbuffer_load_dwordx4 v62, s[52:55], s69 offen lds\nQmvaa 124,156,180\nQta 112,61,33024\nQmvaa 128,160,180\nQta 114,61,34048\nQmvaa 132,164,180\nQta 116,61,33032\n"
"s_add_u32 s69, s87, 3439616\nQmvaa 136,168,180\nbuffer_load_dwordx4 v62, s[52:55], s69 offen offset:1024 lds\nQmvaa 140,172,180\nQda 180,63,1536\nQmvaa 144,144,184\nQta 118,61,34056\nQmvaa 148,148,184\n"
"Qdv 24,63,0\ns_add_u32 s69, s87, 3471352\nQmvaa 152,152,184\nbuffer_load_dwordx4 v62, s[52:55], s69 offen offset:2056 lds\nQmvaa 156,156,184\nQta 120,61,33280\nQmvaa 160,160,184\nQta 122,61,34304\n"
"Qmvaa 164,164,184\nQta 124,61,33288\ns_add_u32 s69, s87, 3503096\nQmvaa 168,168,184\nbuffer_load_dwordx4 v62, s[52:55], s69 offen offset:3080 lds\nQmvaa 172,172,184\nQda 184,63,2560\nQmvaa 176,144,188\n"
"Qta 126,61,34312\nQmvaa 180,148,188\nQta 128,61,33536\ns_add_u32 m0, s61, 8192\nQmvaa 184,152,188\ns_cbranch_vccnz .Ld0q4\nbuffer_load_dwordx4 v58, s[44:47], s60 offen nt lds\n.Ld0q4:\n"
"Qmvaa 188,156,188\nQta 130,61,34560\nQmvaa 192,160,188\nQta 132,61,33544\nQmvaa 196,164,188\nQta 134,61,34568\nQmvaa 200,168,188\nQta 136,61,33792\n"
"Qmvaa 204,172,188\nQda 188,63,3584\nQmvaa 208,144,192\nQta 138,61,34816\nQmvaa 212,148,192\nQta 140,61,33800\nQmvaa 216,152,192\nQta 142,61,34824\n"
"Qmvaa 220,156,192\nQdv 28,63,1024\nQmvaa 224,160,192\nQdv 32,63,2048\nQmvaa 228,164,192\nQdv 36,63,3072\nQmvaa 232,168,192\nQdv 40,63,4096\n"
"Qmvaa 236,172,192\nQda 192,63,4608\nQmvaa 240,144,196\nQdv 44,63,5120\nQmvaa 244,148,196\nQdv 48,63,6144\nQmvaa 248,152,196\nQmvaa 252,156,196\n"
"Qmaaa 208,160,196\nQmaaa 212,164,196\nQmaaa 216,168,196\nQmaaa 220,172,196\nQda 196,63,5632\nQmaaa 224,144,200\nQmaaa 228,148,200\nQmaaa 232,152,200\n"
"Qmaaa 236,156,200\nQmaaa 240,160,200\nQmaaa 244,164,200\nQmaaa 248,168,200\nQmaaa 252,172,200\nQda 200,63,6656\ns_waitcnt lgkmcnt(9)\nQmvav 80,112,24\n"
"Qmvav 84,116,24\ns_add_u32 s68, s71, 4096\nQmvav 88,120,24\nbuffer_store_dwordx4 a[80:83], v60, s[56:59], s68 offen offset:1024 sc1\nQmvav 92,124,24\nQmvav 96,128,24\ns_add_u32 s68, s71, 18432\nQmvav 100,132,24\n"
"buffer_store_dwordx4 a[84:87], v60, s[56:59], s68 offen offset:1024 sc1\nQmvav 104,136,24\nQmvav 108,140,24\ns_waitcnt lgkmcnt(0)\nQmvav 112,112,28\ns_waitcnt vmcnt(21)\ns_barrier\nQmvav 116,116,28\n"
"Qta 144,61,49472\ns_add_u32 m0, s62, 33024\ns_add_u32 s69, s87, 3670016\nQmvav 120,120,28\nbuffer_load_dwordx4 v62, s[52:55], s69 offen lds\nQmvav 124,124,28\nQta 146,61,50496\nQmvav 128,128,28\n"
"Qmvav 132,132,28\nQta 148,61,49480\ns_add_u32 s69, s87, 3701760\nQmvav 136,136,28\nbuffer_load_dwordx4 v62, s[52:55], s69 offen offset:1024 lds\nQmvav 140,140,28\nQta 150,61,50504\nQmvav 144,112,32\n"
"Qmvav 148,116,32\nQta 152,61,49728\ns_add_u32 s69, s87, 3733496\nQmvav 152,120,32\nbuffer_load_dwordx4 v62, s[52:55], s69 offen offset:2056 lds\nQmvav 156,124,32\nQta 154,61,50752\nQmvav 160,128,32\n"
"Qmvav 164,132,32\nQta 156,61,49736\ns_add_u32 s69, s87, 3765240\nQmvav 168,136,32\nbuffer_load_dwordx4 v62, s[52:55], s69 offen offset:3080 lds\nQmvav 172,140,32\nQta 158,61,50760\nQmvav 176,112,36\n"
"Qmvav 180,116,36\nQta 160,61,49984\ns_add_u32 m0, s63, 0\ns_add_u32 s88, s86, 896\nQmvav 184,120,36\nbuffer_load_dwordx4 v64, s[48:51], s88 offen lds\nQmvav 188,124,36\nQta 162,61,51008\n"
"Qmvav 192,128,36\nQmvav 196,132,36\nQta 164,61,49992\ns_add_u32 s88, s86, 20352\nQmvav 200,136,36\nbuffer_load_dwordx4 v64, s[48:51], s88 offen offset:1024 lds\nQmvav 204,140,36\nQta 166,61,51016\n"
"Qmvav 208,112,40\nQmvav 212,116,40\nQta 168,61,50240\ns_add_u32 s88, s86, 39808\nQmvav 216,120,40\nbuffer_load_dwordx4 v64, s[48:51], s88 offen offset:2048 lds\nQmvav 220,124,40\nQta 170,61,51264\n"
"Qmvav 224,128,40\nQmvav 228,132,40\nQta 172,61,50248\ns_add_u32 s88, s86, 59264\nQmvav 232,136,40\nbuffer_load_dwordx4 v64, s[48:51], s88 offen offset:3072 lds\nQmvav 236,140,40\nQta 174,61,51272\n"
"Qmvav 240,112,44\nQmvav 244,116,44\ns_add_u32 m0, s63, 4096\ns_add_u32 s88, s86, 82816\nQmvav 248,120,44\nbuffer_load_dwordx4 v64, s[48:51], s88 offen lds\nQmvav 252,124,44\nQmaav 208,128,44\n"
"Qmaav 212,132,44\ns_add_u32 s88, s86, 102272\nQmaav 216,136,44\nbuffer_load_dwordx4 v64, s[48:51], s88 offen offset:1024 lds\nQmaav 220,140,44\nQmaav 224,112,48\nQmaav 228,116,48\ns_add_u32 s88, s86, 121728\n"
"Qmaav 232,120,48\nbuffer_load_dwordx4 v64, s[48:51], s88 offen offset:2048 lds\nQmaav 236,124,48\nQmaav 240,128,48\nQmaav 244,132,48\nQmaav 248,136,48\nQmaav 252,140,48\ns_waitcnt lgkmcnt(0)\n"
"Qmvaa 80,144,176\nQmvaa 84,148,176\ns_add_u32 s68, s71, 32768\nQmvaa 88,152,176\nbuffer_store_dwordx4 a[88:91], v60, s[56:59], s68 offen offset:1024 sc1\nQmvaa 92,156,176\nQmvaa 96,160,176\ns_add_u32 s68, s71, 47104\n"
"Qmvaa 100,164,176\nbuffer_store_dwordx4 a[92:95], v60, s[56:59], s68 offen offset:1024 sc1\nQmvaa 104,168,176\nQmvaa 108,172,176\ns_waitcnt lgkmcnt(0)\nQmvaa 112,144,180\ns_waitcnt vmcnt(21)\ns_barrier\n"
"Qmvaa 116,148,180\nQda 176,63,33536\ns_add_u32 m0, s62, 49472\ns_add_u32 s69, s87, 3932160\n.Lpg2:\ns_cbranch_execz .Ltr0\nQmvaa 120,152,180\nbuffer_load_dwordx4 v62, s[52:55], s69 offen lds\n"
"Qmvaa 124,156,180\nQta 112,61,0\nQmvaa 128,160,180\nQta 114,61,1024\nQmvaa 132,164,180\nQta 116,61,8\ns_add_u32 s69, s87, 3963904\nQmvaa 136,168,180\n"
"buffer_load_dwordx4 v62, s[52:55], s69 offen offset:1024 lds\nQmvaa 140,172,180\nQda 180,63,34560\nQmvaa 144,144,184\nQta 118,61,1032\nQmvaa 148,148,184\nQdv 24,63,33024\ns_add_u32 s69, s87, 3995640\n"
"Qmvaa 152,152,184\nbuffer_load_dwordx4 v62, s[52:55], s69 offen offset:2056 lds\nQmvaa 156,156,184\nQta 120,61,256\nQmvaa 160,160,184\nQta 122,61,1280\nQmvaa 164,164,184\nQta 124,61,264\n"
"s_add_u32 s69, s87, 4027384\nQmvaa 168,168,184\nbuffer_load_dwordx4 v62, s[52:55], s69 offen offset:3080 lds\nQmvaa 172,172,184\nQda 184,63,35584\nQmvaa 176,144,188\nQta 126,61,1288\nQmvaa 180,148,188\n"
"Qta 128,61,512\ns_add_u32 m0, s61, 8192\nQmvaa 184,152,188\ns_cbranch_vccnz .Ld0q5\nbuffer_load_dwordx4 v59, s[44:47], s60 offen offset:1024 nt lds\n.Ld0q5:\nQmvaa 188,156,188\nQta 130,61,1536\n"
"Qmvaa 192,160,188\nQta 132,61,520\nQmvaa 196,164,188\nQta 134,61,1544\nQmvaa 200,168,188\nQta 136,61,768\nQmvaa 204,172,188\nQda 188,63,36608\n"
"Qmvaa 208,144,192\nQta 138,61,1792\nQmvaa 212,148,192\nQta 140,61,776\nQmvaa 216,152,192\nQta 142,61,1800\nQmvaa 220,156,192\nQdv 28,63,34048\n"
"Qmvaa 224,160,192\nQdv 32,63,35072\nQmvaa 228,164,192\nQdv 36,63,36096\nQmvaa 232,168,192\nQdv 40,63,37120\nQmvaa 236,172,192\nQda 192,63,37632\n"
"Qmvaa 240,144,196\nQdv 44,63,38144\nQmvaa 244,148,196\nQdv 48,63,39168\nQmvaa 248,152,196\nQmvaa 252,156,196\nQmaaa 208,160,196\nQmaaa 212,164,196\n"
"Qmaaa 216,168,196\nQmaaa 220,172,196\nQda 196,63,38656\nQmaaa 224,144,200\nQmaaa 228,148,200\nQmaaa 232,152,200\nQmaaa 236,156,200\nQmaaa 240,160,200\n"
"Qmaaa 244,164,200\nQmaaa 248,168,200\nQmaaa 252,172,200\nQda 200,63,39680\ns_waitcnt lgkmcnt(9)\nQmvav 80,112,24\nQmvav 84,116,24\ns_add_u32 s68, s71, 4096\n"
"Qmvav 88,120,24\nbuffer_store_dwordx4 a[96:99], v60, s[56:59], s68 offen offset:2048 sc1\nQmvav 92,124,24\nQmvav 96,128,24\ns_add_u32 s68, s71, 18432\nQmvav 100,132,24\nbuffer_store_dwordx4 a[100:103], v60, s[56:59], s68 offen offset:2048 sc1\nQmvav 104,136,24\n"
"Qmvav 108,140,24\ns_waitcnt lgkmcnt(0)\nQmvav 112,112,28\ns_waitcnt vmcnt(21)\ns_barrier\nQmvav 116,116,28\nQta 144,61,16448\ns_add_u32 m0, s62, 0\n"
"s_add_u32 s69, s87, 4194304\nQmvav 120,120,28\nbuffer_load_dwordx4 v62, s[52:55], s69 offen lds\nQmvav 124,124,28\nQta 146,61,17472\nQmvav 128,128,28\nQmvav 132,132,28\nQta 148,61,16456\n"
"s_add_u32 s69, s87, 4226048\nQmvav 136,136,28\nbuffer_load_dwordx4 v62, s[52:55], s69 offen offset:1024 lds\nQmvav 140,140,28\nQta 150,61,17480\nQmvav 144,112,32\nQmvav 148,116,32\nQta 152,61,16704\n"
"s_add_u32 s69, s87, 4257784\nQmvav 152,120,32\nbuffer_load_dwordx4 v62, s[52:55], s69 offen offset:2056 lds\nQmvav 156,124,32\nQta 154,61,17728\nQmvav 160,128,32\nQmvav 164,132,32\nQta 156,61,16712\n"
"s_add_u32 s69, s87, 4289528\nQmvav 168,136,32\nbuffer_load_dwordx4 v62, s[52:55], s69 offen offset:3080 lds\nQmvav 172,140,32\nQta 158,61,17736\nQmvav 176,112,36\nQmvav 180,116,36\nQta 160,61,16960\n"
"s_add_u32 m0, s63, 33024\ns_add_u32 s88, s86, 1024\nQmvav 184,120,36\nbuffer_load_dwordx4 v64, s[48:51], s88 offen lds\nQmvav 188,124,36\nQta 162,61,17984\nQmvav 192,128,36\nQmvav 196,132,36\n"
"Qta 164,61,16968\ns_add_u32 s88, s86, 20480\nQmvav 200,136,36\nbuffer_load_dwordx4 v64, s[48:51], s88 offen offset:1024 lds\nQmvav 204,140,36\nQta 166,61,17992\nQmvav 208,112,40\nQmvav 212,116,40\n"
"Qta 168,61,17216\ns_add_u32 s88, s86, 39936\nQmvav 216,120,40\nbuffer_load_dwordx4 v64, s[48:51], s88 offen offset:2048 lds\nQmvav 220,124,40\nQta 170,61,18240\nQmvav 224,128,40\nQmvav 228,132,40\n"
"Qta 172,61,17224\ns_add_u32 s88, s86, 59392\nQmvav 232,136,40\nbuffer_load_dwordx4 v64, s[48:51], s88 offen offset:3072 lds\nQmvav 236,140,40\nQta 174,61,18248\nQmvav 240,112,44\nQmvav 244,116,44\n"
"s_add_u32 m0, s63, 37120\ns_add_u32 s88, s86, 82944\nQmvav 248,120,44\nbuffer_load_dwordx4 v64, s[48:51], s88 offen lds\nQmvav 252,124,44\nQmaav 208,128,44\nQmaav 212,132,44\ns_add_u32 s88, s86, 102400\n"
"Qmaav 216,136,44\nbuffer_load_dwordx4 v64, s[48:51], s88 offen offset:1024 lds\nQmaav 220,140,44\nQmaav 224,112,48\nQmaav 228,116,48\ns_add_u32 s88, s86, 121856\nQmaav 232,120,48\nbuffer_load_dwordx4 v64, s[48:51], s88 offen offset:2048 lds\n"
"Qmaav 236,124,48\nQmaav 240,128,48\nQmaav 244,132,48\nQmaav 248,136,48\nQmaav 252,140,48\ns_waitcnt lgkmcnt(0)\nQmvaa 80,144,176\nQmvaa 84,148,176\n"
"s_add_u32 s68, s71, 32768\nQmvaa 88,152,176\nbuffer_store_dwordx4 a[104:107], v60, s[56:59], s68 offen offset:2048 sc1\nQmvaa 92,156,176\nQmvaa 96,160,176\ns_add_u32 s68, s71, 47104\nQmvaa 100,164,176\nbuffer_store_dwordx4 a[108:111], v60, s[56:59], s68 offen offset:2048 sc1\n"
"Qmvaa 104,168,176\nQmvaa 108,172,176\ns_waitcnt lgkmcnt(0)\nQmvaa 112,144,180\ns_waitcnt vmcnt(21)\ns_barrier\nQmvaa 116,148,180\nQda 176,63,512\n"
"s_add_u32 m0, s62, 16448\ns_add_u32 s69, s87, 4456448\nQmvaa 120,152,180\nbuffer_load_dwordx4 v62, s[52:55], s69 offen lds\nQmvaa 124,156,180\nQta 112,61,33024\nQmvaa 128,160,180\nQta 114,61,34048\n"
"Qmvaa 132,164,180\nQta 116,61,33032\ns_add_u32 s69, s87, 4488192\nQmvaa 136,168,180\nbuffer_load_dwordx4 v62, s[52:55], s69 offen offset:1024 lds\nQmvaa 140,172,180\nQda 180,63,1536\nQmvaa 144,144,184\n"
"Qta 118,61,34056\nQmvaa 148,148,184\nQdv 24,63,0\ns_add_u32 s69, s87, 4519928\nQmvaa 152,152,184\nbuffer_load_dwordx4 v62, s[52:55], s69 offen offset:2056 lds\nQmvaa 156,156,184\nQta 120,61,33280\n"
"Qmvaa 160,160,184\nQta 122,61,34304\nQmvaa 164,164,184\nQta 124,61,33288\ns_add_u32 s69, s87, 4551672\nQmvaa 168,168,184\nbuffer_load_dwordx4 v62, s[52:55], s69 offen offset:3080 lds\nQmvaa 172,172,184\n"
"Qda 184,63,2560\nQmvaa 176,144,188\nQta 126,61,34312\nQmvaa 180,148,188\nQta 128,61,33536\ns_add_u32 m0, s61, 8192\nQmvaa 184,152,188\ns_cbranch_vccnz .Ld0q6\n"
"buffer_load_dwordx4 v58, s[44:47], s60 offen offset:2048 nt lds\n.Ld0q6:\nQmvaa 188,156,188\nQta 130,61,34560\nQmvaa 192,160,188\nQta 132,61,33544\nQmvaa 196,164,188\nQta 134,61,34568\n"
"Qmvaa 200,168,188\nQta 136,61,33792\nQmvaa 204,172,188\nQda 188,63,3584\nQmvaa 208,144,192\nQta 138,61,34816\nQmvaa 212,148,192\nQta 140,61,33800\n"
"Qmvaa 216,152,192\nQta 142,61,34824\nQmvaa 220,156,192\nQdv 28,63,1024\nQmvaa 224,160,192\nQdv 32,63,2048\nQmvaa 228,164,192\nQdv 36,63,3072\n"
"Qmvaa 232,168,192\nQdv 40,63,4096\nQmvaa 236,172,192\nQda 192,63,4608\nQmvaa 240,144,196\nQdv 44,63,5120\nQmvaa 244,148,196\nQdv 48,63,6144\n"
"Qmvaa 248,152,196\nQmvaa 252,156,196\nQmaaa 208,160,196\nQmaaa 212,164,196\nQmaaa 216,168,196\nQmaaa 220,172,196\nQda 196,63,5632\nQmaaa 224,144,200\n"
"Qmaaa 228,148,200\nQmaaa 232,152,200\nQmaaa 236,156,200\nQmaaa 240,160,200\nQmaaa 244,164,200\nQmaaa 248,168,200\nQmaaa 252,172,200\nQda 200,63,6656\n"
"s_waitcnt lgkmcnt(9)\nQmvav 80,112,24\nQmvav 84,116,24\nQmvav 88,120,24\nQmvav 92,124,24\nQmvav 96,128,24\nQmvav 100,132,24\nQmvav 104,136,24\n"
"Qmvav 108,140,24\ns_waitcnt lgkmcnt(0)\nQmvav 112,112,28\ns_waitcnt vmcnt(19)\ns_barrier\nQmvav 116,116,28\nQta 144,61,49472\ns_add_u32 m0, s62, 33024\n"
"s_add_u32 s69, s87, 4718592\nQmvav 120,120,28\nbuffer_load_dwordx4 v62, s[52:55], s69 offen lds\nQmvav 124,124,28\nQta 146,61,50496\nQmvav 128,128,28\nQmvav 132,132,28\nQta 148,61,49480\n"
"s_add_u32 s69, s87, 4750336\nQmvav 136,136,28\nbuffer_load_dwordx4 v62, s[52:55], s69 offen offset:1024 lds\nQmvav 140,140,28\nQta 150,61,50504\nQmvav 144,112,32\nQmvav 148,116,32\nQta 152,61,49728\n"
"s_add_u32 s69, s87, 4782072\nQmvav 152,120,32\nbuffer_load_dwordx4 v62, s[52:55], s69 offen offset:2056 lds\nQmvav 156,124,32\nQta 154,61,50752\nQmvav 160,128,32\nQmvav 164,132,32\nQta 156,61,49736\n"
"s_add_u32 s69, s87, 4813816\nQmvav 168,136,32\nbuffer_load_dwordx4 v62, s[52:55], s69 offen offset:3080 lds\nQmvav 172,140,32\nQta 158,61,50760\nQmvav 176,112,36\nQmvav 180,116,36\nQta 160,61,49984\n"
"s_add_u32 m0, s63, 0\ns_add_u32 s88, s86, 1152\nQmvav 184,120,36\nbuffer_load_dwordx4 v64, s[48:51], s88 offen lds\nQmvav 188,124,36\nQta 162,61,51008\nQmvav 192,128,36\nQmvav 196,132,36\n"
"Qta 164,61,49992\ns_add_u32 s88, s86, 20608\nQmvav 200,136,36\nbuffer_load_dwordx4 v64, s[48:51], s88 offen offset:1024 lds\nQmvav 204,140,36\nQta 166,61,51016\nQmvav 208,112,40\nQmvav 212,116,40\n"
"Qta 168,61,50240\ns_add_u32 s88, s86, 40064\nQmvav 216,120,40\nbuffer_load_dwordx4 v64, s[48:51], s88 offen offset:2048 lds\nQmvav 220,124,40\nQta 170,61,51264\nQmvav 224,128,40\nQmvav 228,132,40\n"
"Qta 172,61,50248\ns_add_u32 s88, s86, 59520\nQmvav 232,136,40\nbuffer_load_dwordx4 v64, s[48:51], s88 offen offset:3072 lds\nQmvav 236,140,40\nQta 174,61,51272\nQmvav 240,112,44\nQmvav 244,116,44\n"
"s_add_u32 m0, s63, 4096\ns_add_u32 s88, s86, 83072\nQmvav 248,120,44\nbuffer_load_dwordx4 v64, s[48:51], s88 offen lds\nQmvav 252,124,44\nQmaav 208,128,44\nQmaav 212,132,44\ns_add_u32 s88, s86, 102528\n"
"Qmaav 216,136,44\nbuffer_load_dwordx4 v64, s[48:51], s88 offen offset:1024 lds\nQmaav 220,140,44\nQmaav 224,112,48\nQmaav 228,116,48\ns_add_u32 s88, s86, 121984\nQmaav 232,120,48\nbuffer_load_dwordx4 v64, s[48:51], s88 offen offset:2048 lds\n"
"Qmaav 236,124,48\nQmaav 240,128,48\nQmaav 244,132,48\nQmaav 248,136,48\nQmaav 252,140,48\ns_waitcnt lgkmcnt(0)\nQmvaa 80,144,176\nQmvaa 84,148,176\n"
"Qmvaa 88,152,176\nQmvaa 92,156,176\nQmvaa 96,160,176\nQmvaa 100,164,176\nQmvaa 104,168,176\nQmvaa 108,172,176\ns_waitcnt lgkmcnt(0)\nQmvaa 112,144,180\n"
"s_waitcnt vmcnt(17)\ns_barrier\nQmvaa 116,148,180\nQda 176,63,33536\ns_add_u32 m0, s62, 49472\ns_add_u32 s69, s87, 4980736\nQmvaa 120,152,180\nbuffer_load_dwordx4 v62, s[52:55], s69 offen lds\n"
"Qmvaa 124,156,180\nQta 112,61,0\nQmvaa 128,160,180\nQta 114,61,1024\nQmvaa 132,164,180\nQta 116,61,8\ns_add_u32 s69, s87, 5012480\nQmvaa 136,168,180\n"
"buffer_load_dwordx4 v62, s[52:55], s69 offen offset:1024 lds\nQmvaa 140,172,180\nQda 180,63,34560\nQmvaa 144,144,184\nQta 118,61,1032\nQmvaa 148,148,184\nQdv 24,63,33024\ns_add_u32 s69, s87, 5044216\n"
"Qmvaa 152,152,184\nbuffer_load_dwordx4 v62, s[52:55], s69 offen offset:2056 lds\nQmvaa 156,156,184\nQta 120,61,256\nQmvaa 160,160,184\nQta 122,61,1280\nQmvaa 164,164,184\nQta 124,61,264\n"
"s_add_u32 s69, s87, 5075960\nQmvaa 168,168,184\nbuffer_load_dwordx4 v62, s[52:55], s69 offen offset:3080 lds\nQmvaa 172,172,184\nQda 184,63,35584\nQmvaa 176,144,188\nQta 126,61,1288\nQmvaa 180,148,188\n"
"Qta 128,61,512\ns_add_u32 m0, s61, 8192\nQmvaa 184,152,188\ns_cbranch_vccnz .Ld0q7\nbuffer_load_dwordx4 v59, s[44:47], s60 offen offset:3072 nt lds\ns_add_u32 s60, s60, 8192\n.Ld0q7:\nQmvaa 188,156,188\n"
"Qta 130,61,1536\nQmvaa 192,160,188\nQta 132,61,520\nQmvaa 196,164,188\nQta 134,61,1544\nQmvaa 200,168,188\nQta 136,61,768\nQmvaa 204,172,188\n"
"Qda 188,63,36608\nQmvaa 208,144,192\nQta 138,61,1792\nQmvaa 212,148,192\nQta 140,61,776\nQmvaa 216,152,192\nQta 142,61,1800\nQmvaa 220,156,192\n"
"Qdv 28,63,34048\nQmvaa 224,160,192\nQdv 32,63,35072\nQmvaa 228,164,192\nQdv 36,63,36096\nQmvaa 232,168,192\nQdv 40,63,37120\nQmvaa 236,172,192\n"
"Qda 192,63,37632\nQmvaa 240,144,196\nQdv 44,63,38144\nQmvaa 244,148,196\nQdv 48,63,39168\nQmvaa 248,152,196\nQmvaa 252,156,196\nQmaaa 208,160,196\n"
"Qmaaa 212,164,196\nQmaaa 216,168,196\nQmaaa 220,172,196\nQda 196,63,38656\nQmaaa 224,144,200\nQmaaa 228,148,200\nQmaaa 232,152,200\nQmaaa 236,156,200\n"
"Qmaaa 240,160,200\nQmaaa 244,164,200\nQmaaa 248,168,200\nQmaaa 252,172,200\nQda 200,63,39680\ns_waitcnt lgkmcnt(9)\nQmvav 80,112,24\nQmvav 84,116,24\n"
"Qmvav 88,120,24\nQmvav 92,124,24\nQmvav 96,128,24\nQmvav 100,132,24\nQmvav 104,136,24\nQmvav 108,140,24\ns_waitcnt lgkmcnt(0)\nQmvav 112,112,28\n"
"s_waitcnt vmcnt(15)\ns_barrier\nQmvav 116,116,28\nQta 144,61,16448\ns_mov_b32 s56, s94\ns_mov_b32 s57, s95\ns_mov_b32 s58, s75\ns_mov_b32 s94, 0\n"
"s_add_u32 m0, %10, 160768\nQmvav 120,120,28\nbuffer_load_dwordx4 v60, s[56:59], s94 offen lds\nQmvav 124,124,28\nQta 146,61,17472\nQmvav 128,128,28\nQmvav 132,132,28\n.Lpg3:\n"
"s_cbranch_execz .Ltr0\nQta 148,61,16456\ns_add_u32 s94, s94, 4096\nQmvav 136,136,28\nbuffer_load_dwordx4 v60, s[56:59], s94 offen lds\nQmvav 140,140,28\nQta 150,61,17480\nQmvav 144,112,32\n"
"Qmvav 148,116,32\nQta 152,61,16704\ns_add_u32 s94, s94, 4096\nQmvav 152,120,32\nbuffer_load_dwordx4 v60, s[56:59], s94 offen lds\nQmvav 156,124,32\nQta 154,61,17728\nQmvav 160,128,32\n"
"Qmvav 164,132,32\nQta 156,61,16712\ns_add_u32 s94, s94, 4096\nQmvav 168,136,32\nbuffer_load_dwordx4 v60, s[56:59], s94 offen lds\nQmvav 172,140,32\nQta 158,61,17736\nQmvav 176,112,36\n"
"Qmvav 180,116,36\nQta 160,61,16960\ns_add_u32 s94, s94, 4096\nQmvav 184,120,36\nbuffer_load_dwordx4 v60, s[56:59], s94 offen lds\nQmvav 188,124,36\nQta 162,61,17984\nQmvav 192,128,36\n"
"Qmvav 196,132,36\nQta 164,61,16968\ns_add_u32 s94, s94, 4096\nQmvav 200,136,36\nbuffer_load_dwordx4 v60, s[56:59], s94 offen lds\nQmvav 204,140,36\nQta 166,61,17992\nQmvav 208,112,40\n"
"Qmvav 212,116,40\nQta 168,61,17216\ns_add_u32 s94, s94, 4096\nQmvav 216,120,40\nbuffer_load_dwordx4 v60, s[56:59], s94 offen lds\nQmvav 220,124,40\nQta 170,61,18240\nQmvav 224,128,40\n"
"Qmvav 228,132,40\nQta 172,61,17224\ns_add_u32 s94, s94, 4096\nQmvav 232,136,40\nbuffer_load_dwordx4 v60, s[56:59], s94 offen lds\nQmvav 236,140,40\nQta 174,61,18248\nQmvav 240,112,44\n"
"Qmvav 244,116,44\ns_add_u32 s94, s94, 4096\nQmvav 248,120,44\nbuffer_load_dwordx4 v60, s[56:59], s94 offen lds\nQmvav 252,124,44\nQmaav 208,128,44\nQmaav 212,132,44\ns_add_u32 s94, s94, 4096\n"
"Qmaav 216,136,44\nbuffer_load_dwordx4 v60, s[56:59], s94 offen lds\nQmaav 220,140,44\nQmaav 224,112,48\nQmaav 228,116,48\nQmaav 232,120,48\nQmaav 236,124,48\nQmaav 240,128,48\n"
"Qmaav 244,132,48\nQmaav 248,136,48\nQmaav 252,140,48\ns_waitcnt lgkmcnt(0)\nQmvaa 80,144,176\nQmvaa 84,148,176\nQmvaa 88,152,176\nQmvaa 92,156,176\n"
"Qmvaa 96,160,176\nQmvaa 100,164,176\nQmvaa 104,168,176\nQmvaa 108,172,176\ns_waitcnt lgkmcnt(0)\nQmvaa 112,144,180\ns_waitcnt vmcnt(14)\ns_barrier\n"
"Qmvaa 116,148,180\nQda 176,63,512\ns_add_u32 m0, s61, 33024\nQmvaa 120,152,180\nbuffer_load_dwordx4 v56, s[40:43], s60 offen nt lds\nQmvaa 124,156,180\nQta 112,61,33024\nQmvaa 128,160,180\n"
"Qta 114,61,34048\nQmvaa 132,164,180\nQta 116,61,33032\nQmvaa 136,168,180\nbuffer_load_dwordx4 v57, s[40:43], s60 offen offset:1024 nt lds\nQmvaa 140,172,180\nQda 180,63,1536\nQmvaa 144,144,184\n"
"Qta 118,61,34056\nQmvaa 148,148,184\nQdv 24,63,0\nQmvaa 152,152,184\nbuffer_load_dwordx4 v56, s[40:43], s60 offen offset:2048 nt lds\nQmvaa 156,156,184\nQta 120,61,33280\nQmvaa 160,160,184\n"
"Qta 122,61,34304\nQmvaa 164,164,184\nQta 124,61,33288\nQmvaa 168,168,184\nbuffer_load_dwordx4 v57, s[40:43], s60 offen offset:3072 nt lds\nQmvaa 172,172,184\nQda 184,63,2560\nQmvaa 176,144,188\n"
"Qta 126,61,34312\nQmvaa 180,148,188\nQta 128,61,33536\ns_add_u32 m0, s61, 41216\nQmvaa 184,152,188\nbuffer_load_dwordx4 v58, s[44:47], s60 offen nt lds\nQmvaa 188,156,188\nQta 130,61,34560\n"
"Qmvaa 192,160,188\nQta 132,61,33544\nQmvaa 196,164,188\nQta 134,61,34568\nQmvaa 200,168,188\nbuffer_load_dwordx4 v59, s[44:47], s60 offen offset:1024 nt lds\nQmvaa 204,172,188\nQda 188,63,3584\n"
"Qmvaa 208,144,192\nQta 136,61,33792\nQmvaa 212,148,192\nQta 138,61,34816\nQmvaa 216,152,192\nbuffer_load_dwordx4 v58, s[44:47], s60 offen offset:2048 nt lds\nQmvaa 220,156,192\nQta 140,61,33800\n"
"Qmvaa 224,160,192\nQta 142,61,34824\nQmvaa 228,164,192\nQdv 28,63,1024\nQmvaa 232,168,192\nbuffer_load_dwordx4 v59, s[44:47], s60 offen offset:3072 nt lds\ns_add_u32 s60, s60, 8192\ns_add_u32 s88, s60, 8192\n"
"Qmvaa 236,172,192\nQda 192,63,4608\nQmvaa 240,144,196\nQdv 32,63,2048\nQmvaa 244,148,196\nQdv 36,63,3072\nQmvaa 248,152,196\nQdv 40,63,4096\n"
"Qmvaa 252,156,196\nQdv 44,63,5120\nQmaaa 208,160,196\nQdv 48,63,6144\nQmaaa 212,164,196\nQmaaa 216,168,196\nQmaaa 220,172,196\nQda 196,63,5632\n"
"Qmaaa 224,144,200\nQmaaa 228,148,200\nQmaaa 232,152,200\nQmaaa 236,156,200\nQmaaa 240,160,200\nQmaaa 244,164,200\nQmaaa 248,168,200\nQmaaa 252,172,200\n"
"Qda 200,63,6656\ns_waitcnt lgkmcnt(9)\nQmvav 80,112,24\nQmvav 84,116,24\nQmvav 88,120,24\nQmvav 92,124,24\nQmvav 96,128,24\nQmvav 100,132,24\n"
"Qmvav 104,136,24\nQmvav 108,140,24\ns_waitcnt lgkmcnt(0)\nQmvav 112,112,28\ns_waitcnt vmcnt(18)\ns_barrier\nQmvav 116,116,28\nQta 144,61,49472\n"
"Qmvav 120,120,28\nQmvav 124,124,28\nQta 146,61,50496\nQmvav 128,128,28\nQmvav 132,132,28\nQta 148,61,49480\nQmvav 136,136,28\nQmvav 140,140,28\n"
"Qta 150,61,50504\nQmvav 144,112,32\nQmvav 148,116,32\nQta 152,61,49728\nQmvav 152,120,32\nQmvav 156,124,32\nQta 154,61,50752\nQmvav 160,128,32\n"
"Qmvav 164,132,32\nQta 156,61,49736\nQmvav 168,136,32\nQmvav 172,140,32\nQta 158,61,50760\nQmvav 176,112,36\nQmvav 180,116,36\nQta 160,61,49984\n"
"Qmvav 184,120,36\nQmvav 188,124,36\nQta 162,61,51008\nQmvav 192,128,36\nQmvav 196,132,36\nQta 164,61,49992\nQmvav 200,136,36\nQmvav 204,140,36\n"
"Qta 166,61,51016\nQmvav 208,112,40\nQmvav 212,116,40\nQta 168,61,50240\nQmvav 216,120,40\nQmvav 220,124,40\nQta 170,61,51264\nQmvav 224,128,40\n"
"Qmvav 228,132,40\nQta 172,61,50248\nQmvav 232,136,40\nQmvav 236,140,40\nQta 174,61,51272\nQmvav 240,112,44\nQmvav 244,116,44\nQmvav 248,120,44\n"
"Qmvav 252,124,44\nQmaav 208,128,44\nQmaav 212,132,44\nQmaav 216,136,44\nQmaav 220,140,44\nQmaav 224,112,48\nQmaav 228,116,48\nQmaav 232,120,48\n"
"Qmaav 236,124,48\nQmaav 240,128,48\nQmaav 244,132,48\nQmaav 248,136,48\nQmaav 252,140,48\ns_waitcnt lgkmcnt(0)\nQmvaa 80,144,176\nQmvaa 84,148,176\n"
"Qmvaa 88,152,176\nQmvaa 92,156,176\nQmvaa 96,160,176\nQmvaa 100,164,176\nQmvaa 104,168,176\nQmvaa 108,172,176\ns_waitcnt lgkmcnt(0)\nQmvaa 112,144,180\n"
"s_waitcnt vmcnt(18)\ns_barrier\nQmvaa 116,148,180\nQda 112,52,0\ns_add_u32 m0, s61, 66048\nv_accvgpr_write_b32 a176, 0\nQmvaa 120,152,180\nbuffer_load_dwordx4 v56, s[40:43], s60 offen nt lds\n"
"Qmvaa 124,156,180\nQda 116,52,512\nv_accvgpr_write_b32 a177, 0\nQmvaa 128,160,180\nQmvaa 132,164,180\nQda 120,52,1024\nv_accvgpr_write_b32 a178, 0\nQmvaa 136,168,180\n"
"buffer_load_dwordx4 v57, s[40:43], s60 offen offset:1024 nt lds\nQmvaa 140,172,180\nQda 124,52,1536\nv_accvgpr_write_b32 a179, 0\nQmvaa 144,144,184\nQmvaa 148,148,184\nQda 128,52,2048\nv_accvgpr_write_b32 a180, 0\n"
"Qmvaa 152,152,184\nbuffer_load_dwordx4 v56, s[40:43], s60 offen offset:2048 nt lds\nQmvaa 156,156,184\nQda 132,52,2560\nv_accvgpr_write_b32 a181, 0\nQmvaa 160,160,184\nQmvaa 164,164,184\nQda 136,52,3072\n"
"v_accvgpr_write_b32 a182, 0\nQmvaa 168,168,184\nbuffer_load_dwordx4 v57, s[40:43], s60 offen offset:3072 nt lds\nQmvaa 172,172,184\nQda 140,52,3584\nv_accvgpr_write_b32 a183, 0\nQmvaa 176,144,188\nQmvaa 180,148,188\n"
"s_add_u32 m0, s61, 74240\nv_accvgpr_write_b32 a184, 0\nQmvaa 184,152,188\nbuffer_load_dwordx4 v58, s[44:47], s60 offen nt lds\nQmvaa 188,156,188\nv_accvgpr_write_b32 a185, 0\nQmvaa 192,160,188\nQmvaa 196,164,188\n"
"v_accvgpr_write_b32 a186, 0\nQmvaa 200,168,188\nbuffer_load_dwordx4 v59, s[44:47], s60 offen offset:1024 nt lds\nQmvaa 204,172,188\nv_accvgpr_write_b32 a187, 0\nQmvaa 208,144,192\nQmvaa 212,148,192\nv_accvgpr_write_b32 a188, 0\n"
"Qmvaa 216,152,192\nbuffer_load_dwordx4 v58, s[44:47], s60 offen offset:2048 nt lds\nQmvaa 220,156,192\nv_accvgpr_write_b32 a189, 0\nQmvaa 224,160,192\nQmvaa 228,164,192\nv_accvgpr_write_b32 a190, 0\nQmvaa 232,168,192\n"
"buffer_load_dwordx4 v59, s[44:47], s60 offen offset:3072 nt lds\ns_add_u32 s60, s60, 8192\nQmvaa 236,172,192\nv_accvgpr_write_b32 a191, 0\nQmvaa 240,144,196\nQmvaa 244,148,196\nv_accvgpr_write_b32 a192, 0\nQmvaa 248,152,196\n"
"Qmvaa 252,156,196\nv_accvgpr_write_b32 a193, 0\nQmaaa 208,160,196\nQmaaa 212,164,196\nv_accvgpr_write_b32 a194, 0\nQmaaa 216,168,196\nQmaaa 220,172,196\nv_accvgpr_write_b32 a195, 0\n"
"Qmaaa 224,144,200\nQmaaa 228,148,200\nv_accvgpr_write_b32 a196, 0\nQmaaa 232,152,200\nQmaaa 236,156,200\nv_accvgpr_write_b32 a197, 0\nQmaaa 240,160,200\nQmaaa 244,164,200\n"
"v_accvgpr_write_b32 a198, 0\nQmaaa 248,168,200\nQmaaa 252,172,200\nv_accvgpr_write_b32 a199, 0\nQta 144,54,0\nQta 146,54,2048\nQta 148,54,256\nQta 150,54,2304\n"
"Qta 152,54,512\nQta 154,54,2560\nQta 156,54,768\nQta 158,54,2816\nQta 160,54,1024\nQta 162,54,3072\nQta 164,54,1280\nQta 166,54,3328\n"
"Qta 168,54,1536\nQta 170,54,3584\nQta 172,54,1792\nQta 174,54,3840\nv_pk_mul_f32 v[80:81], v[80:81], s[72:73] op_sel_hi:[1,0]\nv_pk_mul_f32 v[82:83], v[82:83], s[72:73] op_sel_hi:[1,0]\nv_pk_mul_f32 v[84:85], v[84:85], s[72:73] op_sel_hi:[1,0]\nv_pk_mul_f32 v[86:87], v[86:87], s[72:73] op_sel_hi:[1,0]\n"
"Qk 80,80,81\nQk 81,82,83\nQk 82,84,85\nQk 83,86,87\nv_accvgpr_write_b32 a0, v80\nv_accvgpr_write_b32 a1, v81\nv_accvgpr_write_b32 a2, v82\nv_accvgpr_write_b32 a3, v83\n"
"v_pk_mul_f32 v[88:89], v[88:89], s[72:73] op_sel_hi:[1,0]\nv_pk_mul_f32 v[90:91], v[90:91], s[72:73] op_sel_hi:[1,0]\nv_pk_mul_f32 v[92:93], v[92:93], s[72:73] op_sel_hi:[1,0]\nv_pk_mul_f32 v[94:95], v[94:95], s[72:73] op_sel_hi:[1,0]\nQk 88,88,89\nQk 89,90,91\nQk 90,92,93\nQk 91,94,95\n"
"v_accvgpr_write_b32 a4, v88\nv_accvgpr_write_b32 a5, v89\nv_accvgpr_write_b32 a6, v90\nv_accvgpr_write_b32 a7, v91\nv_pk_mul_f32 v[96:97], v[96:97], s[72:73] op_sel_hi:[1,0]\nv_pk_mul_f32 v[98:99], v[98:99], s[72:73] op_sel_hi:[1,0]\nv_pk_mul_f32 v[100:101], v[100:101], s[72:73] op_sel_hi:[1,0]\nv_pk_mul_f32 v[102:103], v[102:103], s[72:73] op_sel_hi:[1,0]\n"
"Qk 96,96,97\nQk 97,98,99\nQk 98,100,101\nQk 99,102,103\nv_accvgpr_write_b32 a8, v96\nv_accvgpr_write_b32 a9, v97\nv_accvgpr_write_b32 a10, v98\nv_accvgpr_write_b32 a11, v99\n"
"v_pk_mul_f32 v[104:105], v[104:105], s[72:73] op_sel_hi:[1,0]\nv_pk_mul_f32 v[106:107], v[106:107], s[72:73] op_sel_hi:[1,0]\nv_pk_mul_f32 v[108:109], v[108:109], s[72:73] op_sel_hi:[1,0]\nv_pk_mul_f32 v[110:111], v[110:111], s[72:73] op_sel_hi:[1,0]\nQk 104,104,105\nQk 105,106,107\nQk 106,108,109\nQk 107,110,111\n"
"v_accvgpr_write_b32 a12, v104\nv_accvgpr_write_b32 a13, v105\nv_accvgpr_write_b32 a14, v106\nv_accvgpr_write_b32 a15, v107\nv_pk_mul_f32 v[112:113], v[112:113], s[72:73] op_sel_hi:[1,0]\nv_pk_mul_f32 v[114:115], v[114:115], s[72:73] op_sel_hi:[1,0]\nv_pk_mul_f32 v[116:117], v[116:117], s[72:73] op_sel_hi:[1,0]\nv_pk_mul_f32 v[118:119], v[118:119], s[72:73] op_sel_hi:[1,0]\n"
"Qk 112,112,113\nQk 113,114,115\nQk 114,116,117\nQk 115,118,119\nv_accvgpr_write_b32 a16, v112\nv_accvgpr_write_b32 a17, v113\nv_accvgpr_write_b32 a18, v114\nv_accvgpr_write_b32 a19, v115\n"
"v_pk_mul_f32 v[120:121], v[120:121], s[72:73] op_sel_hi:[1,0]\nv_pk_mul_f32 v[122:123], v[122:123], s[72:73] op_sel_hi:[1,0]\nv_pk_mul_f32 v[124:125], v[124:125], s[72:73] op_sel_hi:[1,0]\nv_pk_mul_f32 v[126:127], v[126:127], s[72:73] op_sel_hi:[1,0]\nQk 120,120,121\nQk 121,122,123\nQk 122,124,125\nQk 123,126,127\n"
"v_accvgpr_write_b32 a20, v120\nv_accvgpr_write_b32 a21, v121\nv_accvgpr_write_b32 a22, v122\nv_accvgpr_write_b32 a23, v123\nv_pk_mul_f32 v[128:129], v[128:129], s[72:73] op_sel_hi:[1,0]\nv_pk_mul_f32 v[130:131], v[130:131], s[72:73] op_sel_hi:[1,0]\nv_pk_mul_f32 v[132:133], v[132:133], s[72:73] op_sel_hi:[1,0]\nv_pk_mul_f32 v[134:135], v[134:135], s[72:73] op_sel_hi:[1,0]\n"
"Qk 128,128,129\nQk 129,130,131\nQk 130,132,133\nQk 131,134,135\nv_accvgpr_write_b32 a24, v128\nv_accvgpr_write_b32 a25, v129\nv_accvgpr_write_b32 a26, v130\nv_accvgpr_write_b32 a27, v131\n"
"v_pk_mul_f32 v[136:137], v[136:137], s[72:73] op_sel_hi:[1,0]\nv_pk_mul_f32 v[138:139], v[138:139], s[72:73] op_sel_hi:[1,0]\nv_pk_mul_f32 v[140:141], v[140:141], s[72:73] op_sel_hi:[1,0]\nv_pk_mul_f32 v[142:143], v[142:143], s[72:73] op_sel_hi:[1,0]\nQk 136,136,137\nQk 137,138,139\nQk 138,140,141\nQk 139,142,143\n"
"v_accvgpr_write_b32 a28, v136\nv_accvgpr_write_b32 a29, v137\nv_accvgpr_write_b32 a30, v138\nv_accvgpr_write_b32 a31, v139\nv_pk_mul_f32 v[144:145], v[144:145], s[72:73] op_sel_hi:[1,0]\nv_pk_mul_f32 v[146:147], v[146:147], s[72:73] op_sel_hi:[1,0]\nv_pk_mul_f32 v[148:149], v[148:149], s[72:73] op_sel_hi:[1,0]\nv_pk_mul_f32 v[150:151], v[150:151], s[72:73] op_sel_hi:[1,0]\n"
"Qk 144,144,145\nQk 145,146,147\nQk 146,148,149\nQk 147,150,151\nv_accvgpr_write_b32 a32, v144\nv_accvgpr_write_b32 a33, v145\nv_accvgpr_write_b32 a34, v146\nv_accvgpr_write_b32 a35, v147\n"
"v_pk_mul_f32 v[152:153], v[152:153], s[72:73] op_sel_hi:[1,0]\nv_pk_mul_f32 v[154:155], v[154:155], s[72:73] op_sel_hi:[1,0]\nv_pk_mul_f32 v[156:157], v[156:157], s[72:73] op_sel_hi:[1,0]\nv_pk_mul_f32 v[158:159], v[158:159], s[72:73] op_sel_hi:[1,0]\nQk 152,152,153\nQk 153,154,155\nQk 154,156,157\nQk 155,158,159\n"
"v_accvgpr_write_b32 a36, v152\nv_accvgpr_write_b32 a37, v153\nv_accvgpr_write_b32 a38, v154\nv_accvgpr_write_b32 a39, v155\nv_pk_mul_f32 v[160:161], v[160:161], s[72:73] op_sel_hi:[1,0]\nv_pk_mul_f32 v[162:163], v[162:163], s[72:73] op_sel_hi:[1,0]\nv_pk_mul_f32 v[164:165], v[164:165], s[72:73] op_sel_hi:[1,0]\nv_pk_mul_f32 v[166:167], v[166:167], s[72:73] op_sel_hi:[1,0]\n"
"Qk 160,160,161\nQk 161,162,163\nQk 162,164,165\nQk 163,166,167\nv_accvgpr_write_b32 a40, v160\nv_accvgpr_write_b32 a41, v161\nv_accvgpr_write_b32 a42, v162\nv_accvgpr_write_b32 a43, v163\n"
"v_pk_mul_f32 v[168:169], v[168:169], s[72:73] op_sel_hi:[1,0]\nv_pk_mul_f32 v[170:171], v[170:171], s[72:73] op_sel_hi:[1,0]\nv_pk_mul_f32 v[172:173], v[172:173], s[72:73] op_sel_hi:[1,0]\nv_pk_mul_f32 v[174:175], v[174:175], s[72:73] op_sel_hi:[1,0]\nQk 168,168,169\nQk 169,170,171\nQk 170,172,173\nQk 171,174,175\n"
"v_accvgpr_write_b32 a44, v168\nv_accvgpr_write_b32 a45, v169\nv_accvgpr_write_b32 a46, v170\nv_accvgpr_write_b32 a47, v171\nv_pk_mul_f32 v[176:177], v[176:177], s[72:73] op_sel_hi:[1,0]\nv_pk_mul_f32 v[178:179], v[178:179], s[72:73] op_sel_hi:[1,0]\n.Lpg4:\ns_cbranch_execz .Ltr9\n"
"v_pk_mul_f32 v[180:181], v[180:181], s[72:73] op_sel_hi:[1,0]\nv_pk_mul_f32 v[182:183], v[182:183], s[72:73] op_sel_hi:[1,0]\nQk 176,176,177\nQk 177,178,179\nQk 178,180,181\nQk 179,182,183\nv_accvgpr_write_b32 a48, v176\nv_accvgpr_write_b32 a49, v177\n"
"v_accvgpr_write_b32 a50, v178\nv_accvgpr_write_b32 a51, v179\nv_pk_mul_f32 v[184:185], v[184:185], s[72:73] op_sel_hi:[1,0]\nv_pk_mul_f32 v[186:187], v[186:187], s[72:73] op_sel_hi:[1,0]\nv_pk_mul_f32 v[188:189], v[188:189], s[72:73] op_sel_hi:[1,0]\nv_pk_mul_f32 v[190:191], v[190:191], s[72:73] op_sel_hi:[1,0]\nQk 184,184,185\nQk 185,186,187\n"
"Qk 186,188,189\nQk 187,190,191\nv_accvgpr_write_b32 a52, v184\nv_accvgpr_write_b32 a53, v185\nv_accvgpr_write_b32 a54, v186\nv_accvgpr_write_b32 a55, v187\nv_pk_mul_f32 v[192:193], v[192:193], s[72:73] op_sel_hi:[1,0]\nv_pk_mul_f32 v[194:195], v[194:195], s[72:73] op_sel_hi:[1,0]\n"
"v_pk_mul_f32 v[196:197], v[196:197], s[72:73] op_sel_hi:[1,0]\nv_pk_mul_f32 v[198:199], v[198:199], s[72:73] op_sel_hi:[1,0]\nQk 192,192,193\nQk 193,194,195\nQk 194,196,197\nQk 195,198,199\nv_accvgpr_write_b32 a56, v192\nv_accvgpr_write_b32 a57, v193\n"
"v_accvgpr_write_b32 a58, v194\nv_accvgpr_write_b32 a59, v195\nv_pk_mul_f32 v[200:201], v[200:201], s[72:73] op_sel_hi:[1,0]\nv_pk_mul_f32 v[202:203], v[202:203], s[72:73] op_sel_hi:[1,0]\nv_pk_mul_f32 v[204:205], v[204:205], s[72:73] op_sel_hi:[1,0]\nv_pk_mul_f32 v[206:207], v[206:207], s[72:73] op_sel_hi:[1,0]\nQk 200,200,201\nQk 201,202,203\n"
"Qk 202,204,205\nQk 203,206,207\nv_accvgpr_write_b32 a60, v200\nv_accvgpr_write_b32 a61, v201\nv_accvgpr_write_b32 a62, v202\nv_accvgpr_write_b32 a63, v203\nv_pk_mul_f32 v[208:209], v[208:209], s[72:73] op_sel_hi:[1,0]\nv_pk_mul_f32 v[210:211], v[210:211], s[72:73] op_sel_hi:[1,0]\n"
"v_pk_mul_f32 v[212:213], v[212:213], s[72:73] op_sel_hi:[1,0]\nv_pk_mul_f32 v[214:215], v[214:215], s[72:73] op_sel_hi:[1,0]\nQk 208,208,209\nQk 209,210,211\nQk 210,212,213\nQk 211,214,215\nv_accvgpr_write_b32 a64, v208\nv_accvgpr_write_b32 a65, v209\n"
"v_accvgpr_write_b32 a66, v210\nv_accvgpr_write_b32 a67, v211\nv_pk_mul_f32 v[216:217], v[216:217], s[72:73] op_sel_hi:[1,0]\nv_pk_mul_f32 v[218:219], v[218:219], s[72:73] op_sel_hi:[1,0]\nv_pk_mul_f32 v[220:221], v[220:221], s[72:73] op_sel_hi:[1,0]\nv_pk_mul_f32 v[222:223], v[222:223], s[72:73] op_sel_hi:[1,0]\nQk 216,216,217\nQk 217,218,219\n"
"Qk 218,220,221\nQk 219,222,223\nv_accvgpr_write_b32 a68, v216\nv_accvgpr_write_b32 a69, v217\nv_accvgpr_write_b32 a70, v218\nv_accvgpr_write_b32 a71, v219\nv_pk_mul_f32 v[224:225], v[224:225], s[72:73] op_sel_hi:[1,0]\nv_pk_mul_f32 v[226:227], v[226:227], s[72:73] op_sel_hi:[1,0]\n"
"v_pk_mul_f32 v[228:229], v[228:229], s[72:73] op_sel_hi:[1,0]\nv_pk_mul_f32 v[230:231], v[230:231], s[72:73] op_sel_hi:[1,0]\nQk 224,224,225\nQk 225,226,227\nQk 226,228,229\nQk 227,230,231\nv_accvgpr_write_b32 a72, v224\nv_accvgpr_write_b32 a73, v225\n"
"v_accvgpr_write_b32 a74, v226\nv_accvgpr_write_b32 a75, v227\nv_pk_mul_f32 v[232:233], v[232:233], s[72:73] op_sel_hi:[1,0]\nv_pk_mul_f32 v[234:235], v[234:235], s[72:73] op_sel_hi:[1,0]\nv_pk_mul_f32 v[236:237], v[236:237], s[72:73] op_sel_hi:[1,0]\nv_pk_mul_f32 v[238:239], v[238:239], s[72:73] op_sel_hi:[1,0]\nQk 232,232,233\nQk 233,234,235\n"
"Qk 234,236,237\nQk 235,238,239\nv_accvgpr_write_b32 a76, v232\nv_accvgpr_write_b32 a77, v233\nv_accvgpr_write_b32 a78, v234\nv_accvgpr_write_b32 a79, v235\nv_pk_mul_f32 v[240:241], v[240:241], s[72:73] op_sel_hi:[1,0]\nv_pk_mul_f32 v[242:243], v[242:243], s[72:73] op_sel_hi:[1,0]\n"
"v_pk_mul_f32 v[244:245], v[244:245], s[72:73] op_sel_hi:[1,0]\nv_pk_mul_f32 v[246:247], v[246:247], s[72:73] op_sel_hi:[1,0]\nQk 240,240,241\nQk 241,242,243\nQk 242,244,245\nQk 243,246,247\nv_accvgpr_write_b32 a80, v240\nv_accvgpr_write_b32 a81, v241\n"
"v_accvgpr_write_b32 a82, v242\nv_accvgpr_write_b32 a83, v243\nv_pk_mul_f32 v[248:249], v[248:249], s[72:73] op_sel_hi:[1,0]\nv_pk_mul_f32 v[250:251], v[250:251], s[72:73] op_sel_hi:[1,0]\nv_pk_mul_f32 v[252:253], v[252:253], s[72:73] op_sel_hi:[1,0]\nv_pk_mul_f32 v[254:255], v[254:255], s[72:73] op_sel_hi:[1,0]\nQk 248,248,249\nQk 249,250,251\n"
"Qk 250,252,253\nQk 251,254,255\nv_accvgpr_write_b32 a84, v248\nv_accvgpr_write_b32 a85, v249\nv_accvgpr_write_b32 a86, v250\nv_accvgpr_write_b32 a87, v251\nv_accvgpr_read_b32 v0, a208\nv_accvgpr_read_b32 v1, a209\n"
"v_accvgpr_read_b32 v2, a210\nv_accvgpr_read_b32 v3, a211\nv_accvgpr_read_b32 v4, a212\nv_accvgpr_read_b32 v5, a213\nv_accvgpr_read_b32 v6, a214\nv_accvgpr_read_b32 v7, a215\nv_pk_mul_f32 v[0:1], v[0:1], s[72:73] op_sel_hi:[1,0]\nv_pk_mul_f32 v[2:3], v[2:3], s[72:73] op_sel_hi:[1,0]\n"
"v_pk_mul_f32 v[4:5], v[4:5], s[72:73] op_sel_hi:[1,0]\nv_pk_mul_f32 v[6:7], v[6:7], s[72:73] op_sel_hi:[1,0]\nQk 0,0,1\nQk 1,2,3\nQk 2,4,5\nQk 3,6,7\nv_accvgpr_write_b32 a88, v0\nv_accvgpr_write_b32 a89, v1\n"
"v_accvgpr_write_b32 a90, v2\nv_accvgpr_write_b32 a91, v3\nv_accvgpr_read_b32 v8, a216\nv_accvgpr_read_b32 v9, a217\nv_accvgpr_read_b32 v10, a218\nv_accvgpr_read_b32 v11, a219\nv_accvgpr_read_b32 v12, a220\nv_accvgpr_read_b32 v13, a221\n"
"v_accvgpr_read_b32 v14, a222\nv_accvgpr_read_b32 v15, a223\nv_pk_mul_f32 v[8:9], v[8:9], s[72:73] op_sel_hi:[1,0]\nv_pk_mul_f32 v[10:11], v[10:11], s[72:73] op_sel_hi:[1,0]\nv_pk_mul_f32 v[12:13], v[12:13], s[72:73] op_sel_hi:[1,0]\nv_pk_mul_f32 v[14:15], v[14:15], s[72:73] op_sel_hi:[1,0]\nQk 8,8,9\nQk 9,10,11\n"
"Qk 10,12,13\nQk 11,14,15\nv_accvgpr_write_b32 a92, v8\nv_accvgpr_write_b32 a93, v9\nv_accvgpr_write_b32 a94, v10\nv_accvgpr_write_b32 a95, v11\nv_accvgpr_read_b32 v0, a224\nv_accvgpr_read_b32 v1, a225\n"
"v_accvgpr_read_b32 v2, a226\nv_accvgpr_read_b32 v3, a227\nv_accvgpr_read_b32 v4, a228\nv_accvgpr_read_b32 v5, a229\nv_accvgpr_read_b32 v6, a230\nv_accvgpr_read_b32 v7, a231\nv_pk_mul_f32 v[0:1], v[0:1], s[72:73] op_sel_hi:[1,0]\nv_pk_mul_f32 v[2:3], v[2:3], s[72:73] op_sel_hi:[1,0]\n"
"v_pk_mul_f32 v[4:5], v[4:5], s[72:73] op_sel_hi:[1,0]\nv_pk_mul_f32 v[6:7], v[6:7], s[72:73] op_sel_hi:[1,0]\nQk 0,0,1\nQk 1,2,3\nQk 2,4,5\nQk 3,6,7\nv_accvgpr_write_b32 a96, v0\nv_accvgpr_write_b32 a97, v1\n"
"v_accvgpr_write_b32 a98, v2\nv_accvgpr_write_b32 a99, v3\nv_accvgpr_read_b32 v8, a232\nv_accvgpr_read_b32 v9, a233\nv_accvgpr_read_b32 v10, a234\nv_accvgpr_read_b32 v11, a235\nv_accvgpr_read_b32 v12, a236\nv_accvgpr_read_b32 v13, a237\n"
"v_accvgpr_read_b32 v14, a238\nv_accvgpr_read_b32 v15, a239\nv_pk_mul_f32 v[8:9], v[8:9], s[72:73] op_sel_hi:[1,0]\nv_pk_mul_f32 v[10:11], v[10:11], s[72:73] op_sel_hi:[1,0]\nv_pk_mul_f32 v[12:13], v[12:13], s[72:73] op_sel_hi:[1,0]\nv_pk_mul_f32 v[14:15], v[14:15], s[72:73] op_sel_hi:[1,0]\nQk 8,8,9\nQk 9,10,11\n"
"Qk 10,12,13\nQk 11,14,15\nv_accvgpr_write_b32 a100, v8\nv_accvgpr_write_b32 a101, v9\nv_accvgpr_write_b32 a102, v10\nv_accvgpr_write_b32 a103, v11\nv_accvgpr_read_b32 v0, a240\nv_accvgpr_read_b32 v1, a241\n"
"v_accvgpr_read_b32 v2, a242\nv_accvgpr_read_b32 v3, a243\nv_accvgpr_read_b32 v4, a244\nv_accvgpr_read_b32 v5, a245\nv_accvgpr_read_b32 v6, a246\nv_accvgpr_read_b32 v7, a247\nv_pk_mul_f32 v[0:1], v[0:1], s[72:73] op_sel_hi:[1,0]\nv_pk_mul_f32 v[2:3], v[2:3], s[72:73] op_sel_hi:[1,0]\n"
"v_pk_mul_f32 v[4:5], v[4:5], s[72:73] op_sel_hi:[1,0]\nv_pk_mul_f32 v[6:7], v[6:7], s[72:73] op_sel_hi:[1,0]\nQk 0,0,1\nQk 1,2,3\nQk 2,4,5\nQk 3,6,7\nv_accvgpr_write_b32 a104, v0\nv_accvgpr_write_b32 a105, v1\n"
"v_accvgpr_write_b32 a106, v2\nv_accvgpr_write_b32 a107, v3\nv_accvgpr_read_b32 v8, a248\nv_accvgpr_read_b32 v9, a249\nv_accvgpr_read_b32 v10, a250\nv_accvgpr_read_b32 v11, a251\nv_accvgpr_read_b32 v12, a252\nv_accvgpr_read_b32 v13, a253\n"
"v_accvgpr_read_b32 v14, a254\nv_accvgpr_read_b32 v15, a255\nv_pk_mul_f32 v[8:9], v[8:9], s[72:73] op_sel_hi:[1,0]\nv_pk_mul_f32 v[10:11], v[10:11], s[72:73] op_sel_hi:[1,0]\nv_pk_mul_f32 v[12:13], v[12:13], s[72:73] op_sel_hi:[1,0]\nv_pk_mul_f32 v[14:15], v[14:15], s[72:73] op_sel_hi:[1,0]\nQk 8,8,9\nQk 9,10,11\n"
"Qk 10,12,13\nQk 11,14,15\nv_accvgpr_write_b32 a108, v8\nv_accvgpr_write_b32 a109, v9\nv_accvgpr_write_b32 a110, v10\nv_accvgpr_write_b32 a111, v11\nv_mov_b32 v66, 0x3f803f80\nv_accvgpr_write_b32 a200, 0\n"
"v_accvgpr_write_b32 a201, 0\nv_accvgpr_write_b32 a202, 0\nv_accvgpr_write_b32 a203, 0\ns_mov_b32 vcc_lo, 0x33333333\ns_mov_b32 vcc_hi, 0xcccccccc\nv_cndmask_b32 v66, 0, v66, vcc\nv_accvgpr_write_b32 a204, v66\nv_accvgpr_write_b32 a205, v66\n"
"v_accvgpr_write_b32 a206, v66\nv_accvgpr_write_b32 a207, v66\ns_add_u32 s87, s87, 2048\ns_waitcnt lgkmcnt(15)\nQzvaa 0,112,0\nQmvaa 0,116,4\nQmvaa 0,120,8\nQmvaa 0,124,12\n"
"Qzvaa 4,128,0\nQmvaa 4,132,4\nQmvaa 4,136,8\nQmvaa 4,140,12\ns_nop 3\nv_max3_f32 v66, v0, v1, v2\ns_nop 2\nv_max3_f32 v66, v66, v3, v4\n"
"v_max3_f32 v66, v66, v5, v6\nv_max_f32 v66, v66, v7\nQzvaa 8,112,16\nv_mov_b32 v67, v66\nQmvaa 8,116,20\ns_nop 0\nv_permlane32_swap_b32 v66, v67\nv_max_f32 v66, v66, v67\n"
"Qmvaa 8,120,24\nv_mov_b32 v67, v66\nQmvaa 8,124,28\ns_nop 0\nv_permlane16_swap_b32 v66, v67\nv_max_f32 v66, v66, v67\nQzvaa 12,128,16\nv_sub_f32 v24, 0xc1800000, v66\n"
"Qa 0,0,24\nQmvaa 12,132,20\nQa 1,1,24\nQa 2,2,24\nQmvaa 12,136,24\nQa 3,3,24\nQa 4,4,24\nQmvaa 12,140,28\n"
"Qe 0\nQe 1\nQe 2\nQe 3\nQk 16,0,1\nQk 17,2,3\nQa 5,5,24\nQa 6,6,24\n"
"Qa 7,7,24\nv_mov_b32 v25, v24\nQe 4\nQe 5\nQe 6\nQzvaa 0,112,32\nQe 7\nQmvaa 0,116,36\n"
"Qk 18,4,5\nQk 19,6,7\nQmvaa 0,120,40\nv_max3_f32 v68, v8, v9, v10\nv_max3_f32 v68, v68, v11, v12\nQmvaa 0,124,44\nv_max3_f32 v68, v68, v13, v14\nv_max_f32 v68, v68, v15\n"
"Qzvaa 4,128,32\nv_mov_b32 v69, v68\nv_mov_b32 v26, v24\nQmvaa 4,132,36\nv_permlane32_swap_b32 v68, v69\nv_max_f32 v68, v68, v69\nQmvaa 4,136,40\nv_mov_b32 v69, v68\n"
"v_mov_b32 v27, v24\nQmvaa 4,140,44\ns_waitcnt lgkmcnt(0)\nv_permlane16_swap_b32 v68, v69\nQzvav 80,144,16\nv_max_f32 v68, v68, v69\nv_sub_f32 v28, 0xc1800000, v68\nQzvav 84,148,16\n"
"Qa 8,8,28\nQa 9,9,28\nQzvav 88,152,16\nQa 10,10,28\nQa 11,11,28\nQzvav 92,156,16\nQe 8\nQzvav 96,160,16\n"
"Qe 9\nQzvav 100,164,16\nQe 10\nQzvav 104,168,16\nQe 11\nQzvav 108,172,16\nQk 20,8,9\nQk 21,10,11\n"
"Qzvaa 8,112,48\nQa 12,12,28\nQa 13,13,28\nQmvaa 8,116,52\nQa 14,14,28\nQa 15,15,28\nQmvaa 8,120,56\nQe 12\n"
"Qe 13\nQe 14\nQe 15\nQmvaa 8,124,60\nQk 22,12,13\nQk 23,14,15\nQzvaa 12,128,48\nv_max3_f32 v66, v0, v1, v2\n"
"v_max3_f32 v66, v66, v3, v4\nQmvaa 12,132,52\nv_max3_f32 v66, v66, v5, v6\nv_max_f32 v66, v66, v7\nv_smfmac_f32_16x16x64_bf16 a[176:179], a[204:207], v[16:23], v77\nv_mov_b32 v67, v66\nQmvaa 12,136,56\ns_nop 0\n"
"v_permlane32_swap_b32 v66, v67\nv_max_f32 v66, v66, v67\nQmvaa 12,140,60\nv_mov_b32 v67, v66\nv_mov_b32 v29, v28\nQzvav 112,144,20\ns_add_u32 m0, s61, 99072\nv_permlane16_swap_b32 v66, v67\n"
"Qzvav 116,148,20\nbuffer_load_dwordx4 v56, s[40:43], s60 offen nt lds\nQzvav 120,152,20\nv_max_f32 v66, v66, v67\nv_sub_f32 v32, 0xc1800000, v66\nQzvav 124,156,20\nQa 0,0,32\nQa 1,1,32\n"
"Qzvav 128,160,20\nQa 2,2,32\nQa 3,3,32\nQzvav 132,164,20\nQe 0\nQzvav 136,168,20\nbuffer_load_dwordx4 v57, s[40:43], s60 offen offset:1024 nt lds\nQe 1\n"
"Qe 2\nQe 3\nQzvav 140,172,20\nQk 16,0,1\nQk 17,2,3\nQzvaa 0,112,64\nQa 4,4,32\nQa 5,5,32\n"
"Qmvaa 0,116,68\nQa 6,6,32\nQa 7,7,32\nQmvaa 0,120,72\nQe 4\nQe 5\nQe 6\nQe 7\n"
"Qmvaa 0,124,76\nbuffer_load_dwordx4 v56, s[40:43], s60 offen offset:2048 nt lds\nQk 18,4,5\nQk 19,6,7\nQzvaa 4,128,64\nv_max3_f32 v68, v8, v9, v10\nv_max3_f32 v68, v68, v11, v12\nQmvaa 4,132,68\n"
"v_max3_f32 v68, v68, v13, v14\nv_max_f32 v68, v68, v15\nQmvaa 4,136,72\nv_mov_b32 v69, v68\nv_mov_b32 v30, v28\nQmvaa 4,140,76\nv_permlane32_swap_b32 v68, v69\nv_max_f32 v68, v68, v69\n"
"Qzvav 144,144,16\nbuffer_load_dwordx4 v57, s[40:43], s60 offen offset:3072 nt lds\nQzvav 148,148,16\nv_mov_b32 v69, v68\nv_mov_b32 v31, v28\nQzvav 152,152,16\nv_permlane16_swap_b32 v68, v69\nv_max_f32 v68, v68, v69\n"
"Qzvav 156,156,16\nv_sub_f32 v36, 0xc1800000, v68\nQa 8,8,36\nQzvav 160,160,16\ns_add_u32 m0, s61, 107264\nQa 9,9,36\nQzvav 164,164,16\nbuffer_load_dwordx4 v58, s[44:47], s60 offen nt lds\n"
"Qzvav 168,168,16\nQa 10,10,36\nQa 11,11,36\nQe 8\nQe 9\nQe 10\nQe 11\nQzvav 172,172,16\n"
"Qk 20,8,9\nQk 21,10,11\nQzvaa 8,112,80\nQa 12,12,36\nQa 13,13,36\nQmvaa 8,116,84\nQa 14,14,36\nQa 15,15,36\n"
"Qmvaa 8,120,88\nbuffer_load_dwordx4 v59, s[44:47], s60 offen offset:1024 nt lds\nQe 12\nQe 13\nQe 14\nQe 15\nQmvaa 8,124,92\nQk 22,12,13\n"
"Qk 23,14,15\nQzvaa 12,128,80\nv_max3_f32 v66, v0, v1, v2\nv_max3_f32 v66, v66, v3, v4\nQmvaa 12,132,84\nv_max3_f32 v66, v66, v5, v6\nv_max_f32 v66, v66, v7\nv_smfmac_f32_16x16x64_bf16 a[180:183], a[204:207], v[16:23], v77\n"
"v_mov_b32 v67, v66\nQmvaa 12,136,88\nbuffer_load_dwordx4 v58, s[44:47], s60 offen offset:2048 nt lds\nQmvaa 12,140,92\nv_permlane32_swap_b32 v66, v67\nv_max_f32 v66, v66, v67\nQzvav 176,144,20\nv_mov_b32 v67, v66\n"
"v_mov_b32 v33, v32\nQzvav 180,148,20\nv_permlane16_swap_b32 v66, v67\nv_max_f32 v66, v66, v67\nQzvav 184,152,20\ns_waitcnt vmcnt(15)\ns_barrier\nQzvav 188,156,20\n"
"buffer_load_dwordx4 v59, s[44:47], s60 offen offset:3072 nt lds\nQzvav 192,160,20\nv_sub_f32 v40, 0xc1800000, v66\nQa 0,0,40\nQzvav 196,164,20\nQa 1,1,40\nQa 2,2,40\nQzvav 200,168,20\n"
"s_add_u32 s60, s60, 8192\ns_and_b32 s68, s60, 0x40000\ns_mul_i32 s68, s68, 7\ns_add_u32 s60, s60, s68\nQa 3,3,40\nQe 0\nQe 1\nQe 2\n"
"Qe 3\nQzvav 204,172,20\nQk 16,0,1\nQk 17,2,3\nQzvaa 0,112,96\nQda 112,52,33024\nQa 4,4,40\nQmvaa 0,116,100\n"
"Qda 116,52,33536\nQa 5,5,40\nQmvaa 0,120,104\nQda 120,52,34048\nQa 6,6,40\nQa 7,7,40\nQe 4\nQe 5\n"
"Qe 6\nQe 7\nQmvaa 0,124,108\nQda 124,52,34560\nQk 18,4,5\nQk 19,6,7\nQzvaa 4,128,96\nQda 128,52,35072\n"
"v_max3_f32 v68, v8, v9, v10\nQmvaa 4,132,100\nQda 132,52,35584\nv_max3_f32 v68, v68, v11, v12\nQmvaa 4,136,104\nQda 136,52,36096\nv_max3_f32 v68, v68, v13, v14\nQmvaa 4,140,108\n"
"Qda 140,52,36608\nv_max_f32 v68, v68, v15\nQzvav 208,144,16\nv_mov_b32 v69, v68\nv_mov_b32 v34, v32\nQzvav 212,148,16\ns_memrealtime s[74:75]\nv_permlane32_swap_b32 v68, v69\n"
"Qzvav 216,152,16\nv_max_f32 v68, v68, v69\nv_mov_b32 v69, v68\nQzvav 220,156,16\ns_nop 0\nv_permlane16_swap_b32 v68, v69\nv_max_f32 v68, v68, v69\nQzvav 224,160,16\n"
"v_sub_f32 v44, 0xc1800000, v68\nQa 8,8,44\nQzvav 228,164,16\nQa 9,9,44\nQa 10,10,44\nQzvav 232,168,16\nQa 11,11,44\nQe 8\n"
"Qe 9\nQe 10\nQe 11\nQzvav 236,172,16\ns_waitcnt lgkmcnt(0)\nQk 20,8,9\nQk 21,10,11\nQcvaa 8,112,0,24\n"
"Qa 12,12,44\nQa 13,13,44\nQmvaa 8,116,4\nQa 14,14,44\nQa 15,15,44\nQmvaa 8,120,8\nQe 12\nQe 13\n"
"Qe 14\nQe 15\nQmvaa 8,124,12\nQk 22,12,13\nQk 23,14,15\nQcvaa 12,128,0,24\nv_max3_f32 v66, v0, v1, v2\nv_max3_f32 v66, v66, v3, v4\n"
"Qmvaa 12,132,4\nv_max3_f32 v66, v66, v5, v6\nv_max_f32 v66, v66, v7\nv_smfmac_f32_16x16x64_bf16 a[184:187], a[204:207], v[16:23], v77\nv_mov_b32 v67, v66\nQmvaa 12,136,8\ns_nop 0\nv_permlane32_swap_b32 v66, v67\n"
"v_max_f32 v66, v66, v67\nQmvaa 12,140,12\n.Lpg5:\ns_cbranch_execz .Ltr9\nv_mov_b32 v67, v66\nv_mov_b32 v35, v32\nQzvav 240,144,20\ns_waitcnt lgkmcnt(0)\n"
"Qzvav 244,148,20\ns_mov_b32 s73, 140\nv_permlane16_swap_b32 v66, v67\nQzvav 248,152,20\ns_add_u32 s77, s74, s73\nv_max_f32 v66, v66, v67\nQzvav 252,156,20\ns_sub_u32 s73, s73, 4\n"
"v_sub_f32 v48, 0xc1800000, v66\nQzaav 208,160,20\ns_max_u32 s73, s73, 120\nQa 0,0,48\nQzaav 212,164,20\nQe 8\nQzaav 216,168,20\nQe 9\n"
"Qa 1,1,48\nQa 2,2,48\nQa 3,3,48\nQe 0\nQe 1\nQe 2\nQe 3\nQzaav 220,172,20\n"
"Qe 10\nQk 16,0,1\nQk 17,2,3\nQcvaa 0,112,16,28\nQe 11\nQmvaa 0,116,20\nQk 20,8,9\nQk 21,10,11\n"
"Qmvaa 0,120,24\nQe 12\nQa 4,4,48\nQa 5,5,48\nQa 6,6,48\nQa 7,7,48\nQe 4\nQe 5\n"
"Qe 6\nQe 7\nQmvaa 0,124,28\nQe 13\nQk 18,4,5\nQk 19,6,7\nQcvaa 4,128,16,28\nQe 14\n"
"v_mov_b32 v37, v36\nQmvaa 4,132,20\nQe 15\nv_mov_b32 v38, v36\nQmvaa 4,136,24\nQk 22,12,13\nQk 23,14,15\nv_mov_b32 v39, v36\n"
"v_mov_b32 v41, v40\nQmvaa 4,140,28\nv_mov_b32 v42, v40\nv_mov_b32 v43, v40\nQzaav 224,144,16\nQta 144,54,33024\nQta 146,54,35072\nv_mov_b32 v45, v44\n"
"Qzaav 228,148,16\nQta 148,54,33280\nQta 150,54,35328\nv_mov_b32 v46, v44\nQzaav 232,152,16\nQta 152,54,33536\nQta 154,54,35584\nv_mov_b32 v47, v44\n"
"v_mov_b32 v49, v48\nQzaav 236,156,16\nQta 156,54,33792\nQta 158,54,35840\nv_mov_b32 v50, v48\nQzaav 240,160,16\nQta 160,54,34048\nQta 162,54,36096\n"
"v_mov_b32 v51, v48\nQzaav 244,164,16\nQta 164,54,34304\nQta 166,54,36352\nQzaav 248,168,16\nQta 168,54,34560\nQta 170,54,36608\nQzaav 252,172,16\n"
"Qta 172,54,34816\nQta 174,54,36864\ns_lshl_b32 s66, s67, 3\n.Lloop:\ns_bitcmp1_b32 s99, s66\ns_cbranch_scc1 .Lhook\n.Lhookret:\nQcvaa 8,112,32,32\n"
"v_add_u32 v75, 1, v75\nQmvaa 8,116,36\ns_waitcnt vmcnt(8)\nds_write_b32 v73, v75\nQmvaa 8,120,40\nQe 0\nQmvaa 8,124,44\nQe 1\n"
"Qcvaa 12,128,32,32\nQe 2\nQmvaa 12,132,36\nQe 3\nv_smfmac_f32_16x16x64_bf16 a[188:191], a[204:207], v[16:23], v77\nQmvaa 12,136,40\ns_waitcnt lgkmcnt(1)\nQk 16,0,1\n"
"Qmvaa 12,140,44\nQk 17,2,3\nQmvav 80,144,20\nQe 4\nQmvav 84,148,20\nQe 5\nQmvav 88,152,20\nQe 6\n"
"Qmvav 92,156,20\nQe 7\nQmvav 96,160,20\nQk 18,4,5\nQk 19,6,7\nQmvav 100,164,20\nQe 8\nQmvav 104,168,20\n"
"Qe 9\nQmvav 108,172,20\nQe 10\nQcvaa 0,112,48,36\nQe 11\nQmvaa 0,116,52\nds_read_b32 v69, v74\nQk 20,8,9\n"
"Qmvaa 0,120,56\nQk 21,10,11\nQmvaa 0,124,60\nQe 12\nQcvaa 4,128,48,36\nQe 13\nQmvaa 4,132,52\nQe 14\n"
"Qmvaa 4,136,56\ns_waitcnt lgkmcnt(0)\nv_cmp_gt_u32 vcc, v75, v69\nQmvaa 4,140,60\ns_cbranch_vccnz .Lsa1\n.Lra1:\nQk 22,12,13\nQmvav 112,144,16\n"
"s_add_u32 m0, s61, 0\nQmvav 116,148,16\nbuffer_load_dwordx4 v56, s[40:43], s60 offen nt lds\nQmvav 120,152,16\nQe 15\nQmvav 124,156,16\nQk 23,14,15\nQmvav 128,160,16\n"
"Qe 0\nQmvav 132,164,16\nQe 1\nQmvav 136,168,16\nbuffer_load_dwordx4 v57, s[40:43], s60 offen offset:1024 nt lds\nQmvav 140,172,16\nQe 2\nQcvaa 8,112,64,40\n"
"Qe 3\nQmvaa 8,116,68\nQe 4\nQmvaa 8,120,72\nQe 5\nQmvaa 8,124,76\nbuffer_load_dwordx4 v56, s[40:43], s60 offen offset:2048 nt lds\nQcvaa 12,128,64,40\n"
"Qe 6\nQmvaa 12,132,68\nQe 7\nv_smfmac_f32_16x16x64_bf16 a[192:195], a[204:207], v[16:23], v77\nQmvaa 12,136,72\nQk 16,0,1\nQk 17,2,3\nQmvaa 12,140,76\n"
"buffer_load_dwordx4 v57, s[40:43], s60 offen offset:3072 nt lds\nQmvav 144,144,20\nQk 18,4,5\nQk 19,6,7\nQmvav 148,148,20\nQmvav 152,152,20\nQe 8\nQmvav 156,156,20\n"
"s_add_u32 m0, s61, 8192\nQmvav 160,160,20\nbuffer_load_dwordx4 v58, s[44:47], s60 offen nt lds\nQmvav 164,164,20\nQe 9\nQmvav 168,168,20\nQe 10\nQmvav 172,172,20\n"
"Qe 11\nQcvaa 0,112,80,44\nQk 20,8,9\nQk 21,10,11\nQmvaa 0,116,84\nbuffer_load_dwordx4 v59, s[44:47], s60 offen offset:1024 nt lds\nQmvaa 0,120,88\nQe 12\n"
"Qmvaa 0,124,92\nQe 13\nQcvaa 4,128,80,44\nQe 14\nQmvaa 4,132,84\nQe 15\nQmvaa 4,136,88\nbuffer_load_dwordx4 v58, s[44:47], s60 offen offset:2048 nt lds\n"
"Qmvaa 4,140,92\nQk 22,12,13\nQk 23,14,15\nQmvav 176,144,16\nQmvav 180,148,16\nQmvav 184,152,16\nQmvav 188,156,16\nbuffer_load_dwordx4 v59, s[44:47], s60 offen offset:3072 nt lds\n"
"Qmvav 192,160,16\nQe 0\nQmvav 196,164,16\nQe 1\nQmvav 200,168,16\ns_add_u32 s60, s60, 8192\nQmvav 204,172,16\nQe 2\n"
"Qcvaa 8,112,96,48\nQda 112,53,0\nQmvaa 8,116,100\nQda 116,53,512\nQmvaa 8,120,104\nQda 120,53,1024\nQmvaa 8,124,108\nQda 124,53,1536\n"
"Qcvaa 12,128,96,48\nQda 128,53,2048\nQmvaa 12,132,100\nQda 132,53,2560\nv_smfmac_f32_16x16x64_bf16 a[196:199], a[204:207], v[16:23], v77\nQmvaa 12,136,104\nQda 136,53,3072\nQk 16,0,1\n"
"Qmvaa 12,140,108\nQda 140,53,3584\nQmvav 208,144,20\ns_memrealtime s[74:75]\nQmvav 212,148,20\nQe 3\nQmvav 216,152,20\nQk 17,2,3\n"
"Qmvav 220,156,20\nQe 4\nQmvav 224,160,20\nQe 5\nQmvav 228,164,20\nQe 6\nQmvav 232,168,20\nQe 7\n"
"Qmvav 236,172,20\ns_waitcnt lgkmcnt(0)\nQk 18,4,5\nQcvaa 0,112,0,24\nQk 19,6,7\nQmvaa 0,116,4\nQe 8\nQmvaa 0,120,8\n"
"Qe 9\nQmvaa 0,124,12\nQe 10\nQcvaa 4,128,0,24\nQe 11\nQmvaa 4,132,4\nQk 20,8,9\nQk 21,10,11\n"
"Qmvaa 4,136,8\nQe 12\nQmvaa 4,140,12\nQe 13\nQmvav 240,144,16\nQe 14\nQmvav 244,148,16\ns_waitcnt lgkmcnt(0)\n"
"s_sub_u32 s75, s77, s74\nQmvav 248,152,16\ns_cmp_lt_u32 s75, 256\ns_cbranch_scc1 .Lsp1\n.Lrp1:\nQmvav 252,156,16\ns_add_u32 s77, s74, s73\nQk 22,12,13\n"
"Qmaav 208,160,16\ns_sub_u32 s73, s73, 4\nQmaav 212,164,16\ns_max_u32 s73, s73, 120\nQmaav 216,168,16\nQe 15\nQmaav 220,172,16\nQk 23,14,15\n"
"Qcvaa 8,112,16,28\nQe 0\nQmvaa 8,116,20\nQe 1\nQmvaa 8,120,24\nQe 2\nQmvaa 8,124,28\nQe 3\n"
"Qcvaa 12,128,16,28\nQe 4\nQmvaa 12,132,20\nQe 5\nv_smfmac_f32_16x16x64_bf16 a[200:203], a[204:207], v[16:23], v77\nQmvaa 12,136,24\nQk 16,0,1\nQk 17,2,3\n"
"Qmvaa 12,140,28\nQe 6\nQmaav 224,144,20\nQta 144,55,0\nQta 146,55,2048\nQmaav 228,148,20\nQta 148,55,256\nQta 150,55,2304\n"
"Qmaav 232,152,20\nQta 152,55,512\nQta 154,55,2560\nQmaav 236,156,20\nQta 156,55,768\nQta 158,55,2816\nQmaav 240,160,20\nQta 160,55,1024\n"
"Qta 162,55,3072\nQmaav 244,164,20\nQta 164,55,1280\nQta 166,55,3328\nQmaav 248,168,20\nQta 168,55,1536\nQta 170,55,3584\nQmaav 252,172,20\n"
"Qta 172,55,1792\nQta 174,55,3840\nQcvaa 0,112,32,32\nv_add_u32 v75, 1, v75\nQk 18,4,5\nQmvaa 0,116,36\ns_waitcnt vmcnt(8)\nds_write_b32 v73, v75\n"
"Qmvaa 0,120,40\nQe 7\nQmvaa 0,124,44\nQk 19,6,7\nQcvaa 4,128,32,32\nQe 8\nQmvaa 4,132,36\nQe 9\n"
"Qmvaa 4,136,40\nQe 10\nQmvaa 4,140,44\ns_waitcnt lgkmcnt(1)\nQk 20,8,9\nQmvav 80,144,16\nQe 11\nQmvav 84,148,16\n"
"Qk 21,10,11\nQmvav 88,152,16\nQe 12\nQmvav 92,156,16\nQe 13\nQmvav 96,160,16\nQe 14\nQmvav 100,164,16\n"
"Qe 15\nQmvav 104,168,16\nQk 22,12,13\nQk 23,14,15\nQmvav 108,172,16\nQe 0\nQcvaa 8,112,48,36\nQe 1\n"
"Qmvaa 8,116,52\nQe 2\nQmvaa 8,120,56\nds_read_b32 v69, v74\nQmvaa 8,124,60\nQe 3\nQcvaa 12,128,48,36\nQe 4\n"
"Qmvaa 12,132,52\nQe 5\nv_smfmac_f32_16x16x64_bf16 a[176:179], a[204:207], v[16:23], v77\nQmvaa 12,136,56\ns_waitcnt lgkmcnt(0)\nv_cmp_gt_u32 vcc, v75, v69\nQmvaa 12,140,60\ns_cbranch_vccnz .Lsa2\n"
".Lra2:\nQk 16,0,1\nQmvav 112,144,20\ns_add_u32 m0, s61, 33024\nQk 17,2,3\nQmvav 116,148,20\nbuffer_load_dwordx4 v56, s[40:43], s60 offen nt lds\nQmvav 120,152,20\n"
"Qe 6\nQmvav 124,156,20\nQe 7\nQmvav 128,160,20\nQk 18,4,5\nQk 19,6,7\nQmvav 132,164,20\nQe 8\n"
"Qmvav 136,168,20\nbuffer_load_dwordx4 v57, s[40:43], s60 offen offset:1024 nt lds\nQmvav 140,172,20\nQe 9\nQcvaa 0,112,64,40\nQe 10\nQmvaa 0,116,68\nQe 11\n"
"Qmvaa 0,120,72\nQk 20,8,9\nQk 21,10,11\nQmvaa 0,124,76\nbuffer_load_dwordx4 v56, s[40:43], s60 offen offset:2048 nt lds\nQcvaa 4,128,64,40\nQe 12\nQmvaa 4,132,68\n"
"Qe 13\nQmvaa 4,136,72\nQe 14\nQmvaa 4,140,76\nQe 15\nQmvav 144,144,16\nbuffer_load_dwordx4 v57, s[40:43], s60 offen offset:3072 nt lds\nQmvav 148,148,16\n"
"Qk 22,12,13\nQk 23,14,15\nQmvav 152,152,16\nQmvav 156,156,16\nQe 0\nQmvav 160,160,16\ns_add_u32 m0, s61, 41216\nQmvav 164,164,16\n"
"buffer_load_dwordx4 v58, s[44:47], s60 offen nt lds\nQmvav 168,168,16\nQe 1\nQmvav 172,172,16\nQe 2\nQcvaa 8,112,80,44\nQe 3\nQmvaa 8,116,84\n"
"Qe 4\nQmvaa 8,120,88\nbuffer_load_dwordx4 v59, s[44:47], s60 offen offset:1024 nt lds\nQmvaa 8,124,92\nQe 5\nQcvaa 12,128,80,44\nQe 6\nQmvaa 12,132,84\n"
"Qe 7\nv_smfmac_f32_16x16x64_bf16 a[180:183], a[204:207], v[16:23], v77\nQmvaa 12,136,88\nbuffer_load_dwordx4 v58, s[44:47], s60 offen offset:2048 nt lds\nQmvaa 12,140,92\nQk 16,0,1\nQk 17,2,3\nQmvav 176,144,20\n"
"Qk 18,4,5\nQk 19,6,7\nQmvav 180,148,20\nQmvav 184,152,20\nQe 8\nQmvav 188,156,20\nbuffer_load_dwordx4 v59, s[44:47], s60 offen offset:3072 nt lds\nQmvav 192,160,20\n"
"Qe 9\nQmvav 196,164,20\nQe 10\nQmvav 200,168,20\ns_add_u32 s60, s60, 8192\nQmvav 204,172,20\nQe 11\nQcvaa 0,112,96,48\n"
"Qda 112,53,33024\nQk 20,8,9\nQmvaa 0,116,100\nQda 116,53,33536\nQk 21,10,11\nQmvaa 0,120,104\nQda 120,53,34048\nQmvaa 0,124,108\n"
"Qda 124,53,34560\nQcvaa 4,128,96,48\nQda 128,53,35072\nQmvaa 4,132,100\nQda 132,53,35584\nQmvaa 4,136,104\nQda 136,53,36096\nQmvaa 4,140,108\n"
"Qda 140,53,36608\nQmvav 208,144,16\nQe 12\nQmvav 212,148,16\ns_memrealtime s[74:75]\nQmvav 216,152,16\nQe 13\nQmvav 220,156,16\n"
"Qe 14\nQmvav 224,160,16\nQe 15\nQmvav 228,164,16\nQk 22,12,13\nQk 23,14,15\nQmvav 232,168,16\nQe 0\n"
"Qmvav 236,172,16\ns_waitcnt lgkmcnt(0)\nQcvaa 8,112,0,24\nQe 1\nQmvaa 8,116,4\nQe 2\nQmvaa 8,120,8\nQe 3\n"
"Qmvaa 8,124,12\nQe 4\nQcvaa 12,128,0,24\nQe 5\nQmvaa 12,132,4\nQe 6\nv_smfmac_f32_16x16x64_bf16 a[184:187], a[204:207], v[16:23], v77\nQmvaa 12,136,8\n"
"Qk 16,0,1\nQk 17,2,3\nQmvaa 12,140,12\nQe 7\nQmvav 240,144,20\ns_waitcnt lgkmcnt(0)\ns_sub_u32 s75, s77, s74\nQmvav 244,148,20\n"
"s_cmp_lt_u32 s75, 256\ns_cbranch_scc1 .Lsp2\n.Lrp2:\nQmvav 248,152,20\ns_add_u32 s77, s74, s73\nQk 18,4,5\nQmvav 252,156,20\ns_sub_u32 s73, s73, 4\n"
"Qk 19,6,7\nQmaav 208,160,20\ns_max_u32 s73, s73, 120\nQmaav 212,164,20\nQe 8\nQmaav 216,168,20\nQe 9\nQmaav 220,172,20\n"
"Qe 10\nQcvaa 0,112,16,28\nQe 11\nQmvaa 0,116,20\nQk 20,8,9\nQk 21,10,11\nQmvaa 0,120,24\nQe 12\n"
"Qmvaa 0,124,28\nQe 13\nQcvaa 4,128,16,28\nQe 14\nQmvaa 4,132,20\nQe 15\nQmvaa 4,136,24\nQk 22,12,13\n"
"Qk 23,14,15\nQmvaa 4,140,28\nQmaav 224,144,16\nQta 144,55,33024\nQta 146,55,35072\nQmaav 228,148,16\nQta 148,55,33280\nQta 150,55,35328\n"
"Qmaav 232,152,16\nQta 152,55,33536\nQta 154,55,35584\nQmaav 236,156,16\nQta 156,55,33792\nQta 158,55,35840\nQmaav 240,160,16\nQta 160,55,34048\n"
"Qta 162,55,36096\nQmaav 244,164,16\nQta 164,55,34304\nQta 166,55,36352\nQmaav 248,168,16\nQta 168,55,34560\nQta 170,55,36608\nQmaav 252,172,16\n"
"Qta 172,55,34816\nQta 174,55,36864\nQcvaa 8,112,32,32\nv_add_u32 v75, 1, v75\nQmvaa 8,116,36\ns_waitcnt vmcnt(8)\n.Lpg6:\ns_cbranch_execz .Ltr9\n"
"ds_write_b32 v73, v75\nQmvaa 8,120,40\nQe 0\nQmvaa 8,124,44\nQe 1\nQcvaa 12,128,32,32\nQe 2\nQmvaa 12,132,36\n"
"Qe 3\nv_smfmac_f32_16x16x64_bf16 a[188:191], a[204:207], v[16:23], v77\nQmvaa 12,136,40\ns_waitcnt lgkmcnt(1)\nQk 16,0,1\nQmvaa 12,140,44\nQk 17,2,3\nQmvav 80,144,20\n"
"Qe 4\nQmvav 84,148,20\nQe 5\nQmvav 88,152,20\nQe 6\nQmvav 92,156,20\nQe 7\nQmvav 96,160,20\n"
"Qk 18,4,5\nQk 19,6,7\nQmvav 100,164,20\nQe 8\nQmvav 104,168,20\nQe 9\nQmvav 108,172,20\nQe 10\n"
"Qcvaa 0,112,48,36\nQe 11\nQmvaa 0,116,52\nds_read_b32 v69, v74\nQk 20,8,9\nQmvaa 0,120,56\nQk 21,10,11\nQmvaa 0,124,60\n"
"Qe 12\nQcvaa 4,128,48,36\nQe 13\nQmvaa 4,132,52\nQe 14\nQmvaa 4,136,56\ns_waitcnt lgkmcnt(0)\nv_cmp_gt_u32 vcc, v75, v69\n"
"Qmvaa 4,140,60\ns_cbranch_vccnz .Lsa3\n.Lra3:\nQk 22,12,13\nQmvav 112,144,16\ns_add_u32 m0, s61, 66048\nQmvav 116,148,16\nbuffer_load_dwordx4 v56, s[40:43], s60 offen nt lds\n"
"Qmvav 120,152,16\nQe 15\nQmvav 124,156,16\nQk 23,14,15\nQmvav 128,160,16\nQe 0\nQmvav 132,164,16\nQe 1\n"
"Qmvav 136,168,16\nbuffer_load_dwordx4 v57, s[40:43], s60 offen offset:1024 nt lds\nQmvav 140,172,16\nQe 2\nQcvaa 8,112,64,40\nQe 3\nQmvaa 8,116,68\nQe 4\n"
"Qmvaa 8,120,72\nQe 5\nQmvaa 8,124,76\nbuffer_load_dwordx4 v56, s[40:43], s60 offen offset:2048 nt lds\nQcvaa 12,128,64,40\nQe 6\nQmvaa 12,132,68\nQe 7\n"
"v_smfmac_f32_16x16x64_bf16 a[192:195], a[204:207], v[16:23], v77\nQmvaa 12,136,72\nQk 16,0,1\nQk 17,2,3\nQmvaa 12,140,76\nbuffer_load_dwordx4 v57, s[40:43], s60 offen offset:3072 nt lds\nQmvav 144,144,20\nQk 18,4,5\n"
"Qk 19,6,7\nQmvav 148,148,20\nQmvav 152,152,20\nQe 8\nQmvav 156,156,20\ns_add_u32 m0, s61, 74240\nQmvav 160,160,20\nbuffer_load_dwordx4 v58, s[44:47], s60 offen nt lds\n"
"Qmvav 164,164,20\nQe 9\nQmvav 168,168,20\nQe 10\nQmvav 172,172,20\nQe 11\nQcvaa 0,112,80,44\nQk 20,8,9\n"
"Qk 21,10,11\nQmvaa 0,116,84\nbuffer_load_dwordx4 v59, s[44:47], s60 offen offset:1024 nt lds\nQmvaa 0,120,88\nQe 12\nQmvaa 0,124,92\nQe 13\nQcvaa 4,128,80,44\n"
"Qe 14\nQmvaa 4,132,84\nQe 15\nQmvaa 4,136,88\nbuffer_load_dwordx4 v58, s[44:47], s60 offen offset:2048 nt lds\nQmvaa 4,140,92\nQk 22,12,13\nQk 23,14,15\n"
"Qmvav 176,144,16\nQmvav 180,148,16\nQmvav 184,152,16\nQmvav 188,156,16\nbuffer_load_dwordx4 v59, s[44:47], s60 offen offset:3072 nt lds\nQmvav 192,160,16\nQe 0\nQmvav 196,164,16\n"
"Qe 1\nQmvav 200,168,16\ns_add_u32 s60, s60, 8192\nQmvav 204,172,16\nQe 2\nQcvaa 8,112,96,48\nQda 112,52,0\nQmvaa 8,116,100\n"
"Qda 116,52,512\nQmvaa 8,120,104\nQda 120,52,1024\nQmvaa 8,124,108\nQda 124,52,1536\nQcvaa 12,128,96,48\nQda 128,52,2048\nQmvaa 12,132,100\n"
"Qda 132,52,2560\nv_smfmac_f32_16x16x64_bf16 a[196:199], a[204:207], v[16:23], v77\nQmvaa 12,136,104\nQda 136,52,3072\nQk 16,0,1\nQmvaa 12,140,108\nQda 140,52,3584\nQmvav 208,144,20\n"
"s_memrealtime s[74:75]\nQmvav 212,148,20\nQe 3\nQmvav 216,152,20\nQk 17,2,3\nQmvav 220,156,20\nQe 4\nQmvav 224,160,20\n"
"Qe 5\nQmvav 228,164,20\nQe 6\nQmvav 232,168,20\nQe 7\nQmvav 236,172,20\ns_waitcnt lgkmcnt(0)\nQk 18,4,5\n"
"Qcvaa 0,112,0,24\nQk 19,6,7\nQmvaa 0,116,4\nQe 8\nQmvaa 0,120,8\nQe 9\nQmvaa 0,124,12\nQe 10\n"
"Qcvaa 4,128,0,24\nQe 11\nQmvaa 4,132,4\nQk 20,8,9\nQk 21,10,11\nQmvaa 4,136,8\nQe 12\nQmvaa 4,140,12\n"
"Qe 13\nQmvav 240,144,16\nQe 14\nQmvav 244,148,16\ns_waitcnt lgkmcnt(0)\ns_sub_u32 s75, s77, s74\nQmvav 248,152,16\ns_cmp_lt_u32 s75, 256\n"
"s_cbranch_scc1 .Lsp3\n.Lrp3:\nQmvav 252,156,16\ns_add_u32 s77, s74, s73\nQk 22,12,13\nQmaav 208,160,16\ns_sub_u32 s73, s73, 4\nQmaav 212,164,16\n"
"s_max_u32 s73, s73, 120\nQmaav 216,168,16\nQe 15\nQmaav 220,172,16\nQk 23,14,15\nQcvaa 8,112,16,28\nQe 0\nQmvaa 8,116,20\n"
"Qe 1\nQmvaa 8,120,24\nQe 2\nQmvaa 8,124,28\nQe 3\nQcvaa 12,128,16,28\nQe 4\nQmvaa 12,132,20\n"
"Qe 5\nv_smfmac_f32_16x16x64_bf16 a[200:203], a[204:207], v[16:23], v77\nQmvaa 12,136,24\nQk 16,0,1\nQk 17,2,3\nQmvaa 12,140,28\nQe 6\nQmaav 224,144,20\n"
"Qta 144,54,0\nQta 146,54,2048\nQmaav 228,148,20\nQta 148,54,256\nQta 150,54,2304\nQmaav 232,152,20\nQta 152,54,512\nQta 154,54,2560\n"
"Qmaav 236,156,20\nQta 156,54,768\nQta 158,54,2816\nQmaav 240,160,20\nQta 160,54,1024\nQta 162,54,3072\nQmaav 244,164,20\nQta 164,54,1280\n"
"Qta 166,54,3328\nQmaav 248,168,20\nQta 168,54,1536\nQta 170,54,3584\nQmaav 252,172,20\nQta 172,54,1792\nQta 174,54,3840\nQcvaa 0,112,32,32\n"
"v_add_u32 v75, 1, v75\nQk 18,4,5\nQmvaa 0,116,36\ns_waitcnt vmcnt(8)\nds_write_b32 v73, v75\nQmvaa 0,120,40\nQe 7\nQmvaa 0,124,44\n"
"Qk 19,6,7\nQcvaa 4,128,32,32\nQe 8\nQmvaa 4,132,36\nQe 9\nQmvaa 4,136,40\nQe 10\nQmvaa 4,140,44\n"
"s_waitcnt lgkmcnt(1)\nQk 20,8,9\nQmvav 80,144,16\nQe 11\nQmvav 84,148,16\nQk 21,10,11\nQmvav 88,152,16\nQe 12\n"
"Qmvav 92,156,16\nQe 13\nQmvav 96,160,16\nQe 14\nQmvav 100,164,16\nQe 15\nQmvav 104,168,16\nQk 22,12,13\n"
"Qk 23,14,15\nQmvav 108,172,16\nQe 0\nQcvaa 8,112,48,36\nQe 1\nQmvaa 8,116,52\nQe 2\nQmvaa 8,120,56\n"
"ds_read_b32 v69, v74\nQmvaa 8,124,60\nQe 3\nQcvaa 12,128,48,36\nQe 4\nQmvaa 12,132,52\nQe 5\nv_smfmac_f32_16x16x64_bf16 a[176:179], a[204:207], v[16:23], v77\n"
"Qmvaa 12,136,56\ns_waitcnt lgkmcnt(0)\nv_cmp_gt_u32 vcc, v75, v69\nQmvaa 12,140,60\ns_cbranch_vccnz .Lsa4\n.Lra4:\nQk 16,0,1\nQmvav 112,144,20\n"
"s_add_u32 m0, s61, 99072\nQk 17,2,3\nQmvav 116,148,20\nbuffer_load_dwordx4 v56, s[40:43], s60 offen nt lds\nQmvav 120,152,20\nQe 6\nQmvav 124,156,20\nQe 7\n"
"Qmvav 128,160,20\nQk 18,4,5\nQk 19,6,7\nQmvav 132,164,20\nQe 8\nQmvav 136,168,20\nbuffer_load_dwordx4 v57, s[40:43], s60 offen offset:1024 nt lds\nQmvav 140,172,20\n"
"Qe 9\nQcvaa 0,112,64,40\nQe 10\nQmvaa 0,116,68\nQe 11\nQmvaa 0,120,72\nQk 20,8,9\nQk 21,10,11\n"
"Qmvaa 0,124,76\nbuffer_load_dwordx4 v56, s[40:43], s60 offen offset:2048 nt lds\nQcvaa 4,128,64,40\nQe 12\nQmvaa 4,132,68\nQe 13\nQmvaa 4,136,72\nQe 14\n"
"Qmvaa 4,140,76\nQe 15\nQmvav 144,144,16\nbuffer_load_dwordx4 v57, s[40:43], s60 offen offset:3072 nt lds\nQmvav 148,148,16\nQk 22,12,13\nQk 23,14,15\nQmvav 152,152,16\n"
"Qmvav 156,156,16\nQe 0\nQmvav 160,160,16\ns_add_u32 m0, s61, 107264\nQmvav 164,164,16\nbuffer_load_dwordx4 v58, s[44:47], s60 offen nt lds\nQmvav 168,168,16\nQe 1\n"
"Qmvav 172,172,16\nQe 2\nQcvaa 8,112,80,44\nQe 3\nQmvaa 8,116,84\nQe 4\nQmvaa 8,120,88\nbuffer_load_dwordx4 v59, s[44:47], s60 offen offset:1024 nt lds\n"
"Qmvaa 8,124,92\nQe 5\nQcvaa 12,128,80,44\nQe 6\nQmvaa 12,132,84\nQe 7\nv_smfmac_f32_16x16x64_bf16 a[180:183], a[204:207], v[16:23], v77\nQmvaa 12,136,88\n"
"buffer_load_dwordx4 v58, s[44:47], s60 offen offset:2048 nt lds\nQmvaa 12,140,92\nQk 16,0,1\nQk 17,2,3\nQmvav 176,144,20\nQk 18,4,5\nQk 19,6,7\nQmvav 180,148,20\n"
"Qmvav 184,152,20\nQe 8\nQmvav 188,156,20\nbuffer_load_dwordx4 v59, s[44:47], s60 offen offset:3072 nt lds\nQmvav 192,160,20\nQe 9\nQmvav 196,164,20\nQe 10\n"
"Qmvav 200,168,20\ns_add_u32 s60, s60, 8192\ns_and_b32 s68, s60, 0x40000\ns_mul_i32 s68, s68, 7\ns_add_u32 s60, s60, s68\nQmvav 204,172,20\nQe 11\nQcvaa 0,112,96,48\n"
"Qda 112,52,33024\nQk 20,8,9\nQmvaa 0,116,100\nQda 116,52,33536\nQk 21,10,11\nQmvaa 0,120,104\nQda 120,52,34048\nQmvaa 0,124,108\n"
"Qda 124,52,34560\nQcvaa 4,128,96,48\nQda 128,52,35072\nQmvaa 4,132,100\nQda 132,52,35584\nQmvaa 4,136,104\nQda 136,52,36096\nQmvaa 4,140,108\n"
"Qda 140,52,36608\nQmvav 208,144,16\nQe 12\nQmvav 212,148,16\ns_memrealtime s[74:75]\nQmvav 216,152,16\nQe 13\nQmvav 220,156,16\n"
"Qe 14\nQmvav 224,160,16\nQe 15\nQmvav 228,164,16\nQk 22,12,13\nQk 23,14,15\nQmvav 232,168,16\nQe 0\n"
"Qmvav 236,172,16\ns_waitcnt lgkmcnt(0)\nQcvaa 8,112,0,24\nQe 1\nQmvaa 8,116,4\nQe 2\nQmvaa 8,120,8\nQe 3\n"
"Qmvaa 8,124,12\nQe 4\nQcvaa 12,128,0,24\nQe 5\nQmvaa 12,132,4\nQe 6\nv_smfmac_f32_16x16x64_bf16 a[184:187], a[204:207], v[16:23], v77\nQmvaa 12,136,8\n"
"Qk 16,0,1\nQk 17,2,3\nQmvaa 12,140,12\nQe 7\nQmvav 240,144,20\ns_waitcnt lgkmcnt(0)\ns_sub_u32 s75, s77, s74\nQmvav 244,148,20\n"
"s_cmp_lt_u32 s75, 256\ns_cbranch_scc1 .Lsp4\n.Lrp4:\nQmvav 248,152,20\ns_add_u32 s77, s74, s73\nQk 18,4,5\nQmvav 252,156,20\ns_sub_u32 s73, s73, 4\n"
"Qk 19,6,7\nQmaav 208,160,20\ns_max_u32 s73, s73, 120\nQmaav 212,164,20\nQe 8\nQmaav 216,168,20\nQe 9\nQmaav 220,172,20\n"
"Qe 10\nQcvaa 0,112,16,28\nQe 11\nQmvaa 0,116,20\nQk 20,8,9\nQk 21,10,11\nQmvaa 0,120,24\nQe 12\n"
"Qmvaa 0,124,28\nQe 13\nQcvaa 4,128,16,28\nQe 14\nQmvaa 4,132,20\nQe 15\nQmvaa 4,136,24\nQk 22,12,13\n"
"Qk 23,14,15\nQmvaa 4,140,28\nQmaav 224,144,16\nQta 144,54,33024\nQta 146,54,35072\nQmaav 228,148,16\nQta 148,54,33280\nQta 150,54,35328\n"
"Qmaav 232,152,16\nQta 152,54,33536\nQta 154,54,35584\nQmaav 236,156,16\nQta 156,54,33792\nQta 158,54,35840\nQmaav 240,160,16\nQta 160,54,34048\n"
"Qta 162,54,36096\nQmaav 244,164,16\nQta 164,54,34304\nQta 166,54,36352\nQmaav 248,168,16\nQta 168,54,34560\nQta 170,54,36608\nQmaav 252,172,16\n"
"Qta 172,54,34816\nQta 174,54,36864\ns_add_u32 s66, s66, 1\ns_bitcmp1_b32 0x7f7f, s66\ns_cbranch_scc1 .Lloop\ns_cmp_lg_u32 s67, 0\ns_cbranch_scc1 .Ltail1\nQcvaa 8,112,32,32\n"
"v_add_u32 v75, 1, v75\nQmvaa 8,116,36\ns_waitcnt vmcnt(8)\nds_write_b32 v73, v75\nQmvaa 8,120,40\nQe 0\nQmvaa 8,124,44\nQe 1\n"
"Qcvaa 12,128,32,32\nQe 2\nQmvaa 12,132,36\nQe 3\nv_smfmac_f32_16x16x64_bf16 a[188:191], a[204:207], v[16:23], v77\nQmvaa 12,136,40\ns_waitcnt lgkmcnt(1)\nQk 16,0,1\n"
"Qmvaa 12,140,44\nQk 17,2,3\nQmvav 80,144,20\nQe 4\nQmvav 84,148,20\nQe 5\nQmvav 88,152,20\nQe 6\n"
"Qmvav 92,156,20\nQe 7\nQmvav 96,160,20\nQk 18,4,5\nQk 19,6,7\nQmvav 100,164,20\nQe 8\nQmvav 104,168,20\n"
"Qe 9\nQmvav 108,172,20\nQe 10\nQcvaa 0,112,48,36\nQe 11\nQmvaa 0,116,52\nds_read_b32 v69, v74\nQk 20,8,9\n"
"Qmvaa 0,120,56\nQk 21,10,11\nQmvaa 0,124,60\nQe 12\nQcvaa 4,128,48,36\nQe 13\nQmvaa 4,132,52\nQe 14\n"
"Qmvaa 4,136,56\ns_waitcnt lgkmcnt(0)\nv_cmp_gt_u32 vcc, v75, v69\nQmvaa 4,140,60\ns_cbranch_vccnz .Lsa29\n.Lra29:\nQk 22,12,13\nQmvav 112,144,16\n"
"s_add_u32 m0, s62, 0\ns_add_u32 s69, s87, 0\nQmvav 116,148,16\nbuffer_load_dwordx4 v62, s[52:55], s69 offen lds\nQmvav 120,152,16\nQe 15\nQmvav 124,156,16\nQk 23,14,15\n"
"Qmvav 128,160,16\nQe 0\nQmvav 132,164,16\ns_add_u32 s69, s87, 31744\nQe 1\nQmvav 136,168,16\nbuffer_load_dwordx4 v62, s[52:55], s69 offen offset:1024 lds\nQmvav 140,172,16\n"
"Qe 2\nQcvaa 8,112,64,40\nQe 3\nQmvaa 8,116,68\nQe 4\nQmvaa 8,120,72\ns_add_u32 s69, s87, 63480\nQe 5\n"
"Qmvaa 8,124,76\nbuffer_load_dwordx4 v62, s[52:55], s69 offen offset:2056 lds\nQcvaa 12,128,64,40\nQe 6\nQmvaa 12,132,68\nQe 7\nv_smfmac_f32_16x16x64_bf16 a[192:195], a[204:207], v[16:23], v77\nQmvaa 12,136,72\n"
"s_add_u32 s69, s87, 95224\nQk 16,0,1\nQk 17,2,3\n.Lpg7:\ns_cbranch_execz .Ltr9\nQmvaa 12,140,76\nbuffer_load_dwordx4 v62, s[52:55], s69 offen offset:3080 lds\nQmvav 144,144,20\n"
"Qk 18,4,5\nQk 19,6,7\nQmvav 148,148,20\nQmvav 152,152,20\nQe 8\nQmvav 156,156,20\ns_add_u32 m0, s62, 16448\ns_add_u32 s69, s87, 262144\n"
"Qmvav 160,160,20\nbuffer_load_dwordx4 v62, s[52:55], s69 offen lds\nQmvav 164,164,20\nQe 9\nQmvav 168,168,20\nQe 10\nQmvav 172,172,20\nQe 11\n"
"Qcvaa 0,112,80,44\ns_add_u32 s69, s87, 293888\nQk 20,8,9\nQk 21,10,11\nQmvaa 0,116,84\nbuffer_load_dwordx4 v62, s[52:55], s69 offen offset:1024 lds\nQmvaa 0,120,88\nQe 12\n"
"Qmvaa 0,124,92\nQe 13\nQcvaa 4,128,80,44\nQe 14\nQmvaa 4,132,84\ns_add_u32 s69, s87, 325624\nQe 15\nQmvaa 4,136,88\n"
"buffer_load_dwordx4 v62, s[52:55], s69 offen offset:2056 lds\nQmvaa 4,140,92\nQk 22,12,13\nQk 23,14,15\nQmvav 176,144,16\nQmvav 180,148,16\nQmvav 184,152,16\ns_add_u32 s69, s87, 357368\n"
"Qmvav 188,156,16\nbuffer_load_dwordx4 v62, s[52:55], s69 offen offset:3080 lds\nQmvav 192,160,16\nQe 0\nQmvav 196,164,16\nQe 1\nQmvav 200,168,16\nQmvav 204,172,16\n"
"Qe 2\nQcvaa 8,112,96,48\nQda 112,53,0\nQmvaa 8,116,100\nQda 116,53,512\nQmvaa 8,120,104\nQda 120,53,1024\nQmvaa 8,124,108\n"
"Qda 124,53,1536\nQcvaa 12,128,96,48\nQda 128,53,2048\nQmvaa 12,132,100\nQda 132,53,2560\nv_smfmac_f32_16x16x64_bf16 a[196:199], a[204:207], v[16:23], v77\nQmvaa 12,136,104\nQda 136,53,3072\n"
"Qk 16,0,1\nQmvaa 12,140,108\nQda 140,53,3584\nQmvav 208,144,20\ns_add_u32 m0, s63, 33024\ns_add_u32 s88, s86, 0\nQmvav 212,148,20\nbuffer_load_dwordx4 v64, s[48:51], s88 offen lds\n"
"Qe 3\nQmvav 216,152,20\nQk 17,2,3\nQmvav 220,156,20\nQe 4\nQmvav 224,160,20\ns_add_u32 s88, s86, 19456\nQe 5\n"
"Qmvav 228,164,20\nbuffer_load_dwordx4 v64, s[48:51], s88 offen offset:1024 lds\nQe 6\nQmvav 232,168,20\nQe 7\nQmvav 236,172,20\ns_waitcnt lgkmcnt(0)\nQk 18,4,5\n"
"Qcvaa 0,112,0,24\ns_add_u32 s88, s86, 38912\nQk 19,6,7\nQmvaa 0,116,4\nbuffer_load_dwordx4 v64, s[48:51], s88 offen offset:2048 lds\nQe 8\nQmvaa 0,120,8\nQe 9\n"
"Qmvaa 0,124,12\nQe 10\nQcvaa 4,128,0,24\ns_add_u32 s88, s86, 58368\nQe 11\nQmvaa 4,132,4\nbuffer_load_dwordx4 v64, s[48:51], s88 offen offset:3072 lds\nQk 20,8,9\n"
"Qk 21,10,11\nQmvaa 4,136,8\nQe 12\nQmvaa 4,140,12\nQe 13\nQmvav 240,144,16\ns_add_u32 m0, s63, 37120\ns_add_u32 s88, s86, 81920\n"
"Qe 14\nQmvav 244,148,16\nbuffer_load_dwordx4 v64, s[48:51], s88 offen lds\nQmvav 248,152,16\nQmvav 252,156,16\nQk 22,12,13\nQmaav 208,160,16\ns_add_u32 s88, s86, 101376\n"
"Qmaav 212,164,16\nbuffer_load_dwordx4 v64, s[48:51], s88 offen offset:1024 lds\nQmaav 216,168,16\nQe 15\nQmaav 220,172,16\nQk 23,14,15\nQcvaa 8,112,16,28\ns_add_u32 s88, s86, 120832\n"
"Qe 0\nQmvaa 8,116,20\nbuffer_load_dwordx4 v64, s[48:51], s88 offen offset:2048 lds\nQe 1\nQmvaa 8,120,24\nQe 2\nQmvaa 8,124,28\nQe 3\n"
"Qcvaa 12,128,16,28\nQe 4\nQmvaa 12,132,20\nQe 5\nv_smfmac_f32_16x16x64_bf16 a[200:203], a[204:207], v[16:23], v77\nQmvaa 12,136,24\nQk 16,0,1\nQk 17,2,3\n"
"Qmvaa 12,140,28\nQe 6\nQmaav 224,144,20\nQta 144,55,0\nQta 146,55,2048\nQmaav 228,148,20\nQta 148,55,256\nQta 150,55,2304\n"
"Qmaav 232,152,20\nQta 152,55,512\nQta 154,55,2560\nQmaav 236,156,20\nQta 156,55,768\nQta 158,55,2816\nQmaav 240,160,20\nQta 160,55,1024\n"
"Qta 162,55,3072\nQmaav 244,164,20\nQta 164,55,1280\nQta 166,55,3328\nQmaav 248,168,20\nQta 168,55,1536\nQta 170,55,3584\nQmaav 252,172,20\n"
"Qta 172,55,1792\nQta 174,55,3840\nQcvaa 0,112,32,32\nv_add_u32 v75, 1, v75\nQk 18,4,5\nQmvaa 0,116,36\ns_waitcnt vmcnt(15)\nds_write_b32 v73, v75\n"
"Qmvaa 0,120,40\nQe 7\nQmvaa 0,124,44\nQk 19,6,7\nQcvaa 4,128,32,32\nQe 8\nQmvaa 4,132,36\nQe 9\n"
"Qmvaa 4,136,40\nQe 10\nQmvaa 4,140,44\ns_waitcnt lgkmcnt(1)\nQk 20,8,9\nQmvav 80,144,16\nQe 11\nQmvav 84,148,16\n"
"Qk 21,10,11\nQmvav 88,152,16\nQe 12\nQmvav 92,156,16\nQe 13\nQmvav 96,160,16\nQe 14\nQmvav 100,164,16\n"
"Qe 15\nQmvav 104,168,16\nQk 22,12,13\nQk 23,14,15\nQmvav 108,172,16\nQe 0\nQcvaa 8,112,48,36\nQe 1\n"
"Qmvaa 8,116,52\nQe 2\nQmvaa 8,120,56\nds_read_b32 v69, v74\nQmvaa 8,124,60\nQe 3\nQcvaa 12,128,48,36\nQe 4\n"
"Qmvaa 12,132,52\nQe 5\nv_smfmac_f32_16x16x64_bf16 a[176:179], a[204:207], v[16:23], v77\nQmvaa 12,136,56\ns_waitcnt lgkmcnt(0)\nv_cmp_gt_u32 vcc, v75, v69\nQmvaa 12,140,60\ns_cbranch_vccnz .Lsa30\n"
".Lra30:\nQk 16,0,1\nQmvav 112,144,20\ns_add_u32 m0, s62, 33024\ns_add_u32 s69, s87, 524288\nQk 17,2,3\nQmvav 116,148,20\nbuffer_load_dwordx4 v62, s[52:55], s69 offen lds\n"
"Qmvav 120,152,20\nQe 6\nQmvav 124,156,20\nQe 7\nQmvav 128,160,20\nQk 18,4,5\nQk 19,6,7\nQmvav 132,164,20\n"
"s_add_u32 s69, s87, 556032\nQe 8\nQmvav 136,168,20\nbuffer_load_dwordx4 v62, s[52:55], s69 offen offset:1024 lds\nQmvav 140,172,20\nQe 9\nQcvaa 0,112,64,40\nQe 10\n"
"Qmvaa 0,116,68\nQe 11\nQmvaa 0,120,72\ns_add_u32 s69, s87, 587768\nQk 20,8,9\nQk 21,10,11\nQmvaa 0,124,76\nbuffer_load_dwordx4 v62, s[52:55], s69 offen offset:2056 lds\n"
"Qcvaa 4,128,64,40\nQe 12\nQmvaa 4,132,68\nQe 13\nQmvaa 4,136,72\nQe 14\nQmvaa 4,140,76\ns_add_u32 s69, s87, 619512\n"
"Qe 15\nQmvav 144,144,16\nbuffer_load_dwordx4 v62, s[52:55], s69 offen offset:3080 lds\nQmvav 148,148,16\nQk 22,12,13\nQk 23,14,15\nQmvav 152,152,16\nQmvav 156,156,16\n"
"Qe 0\nQmvav 160,160,16\ns_add_u32 m0, s62, 49472\ns_add_u32 s69, s87, 786432\nQmvav 164,164,16\nbuffer_load_dwordx4 v62, s[52:55], s69 offen lds\nQmvav 168,168,16\nQe 1\n"
"Qmvav 172,172,16\nQe 2\nQcvaa 8,112,80,44\nQe 3\nQmvaa 8,116,84\ns_add_u32 s69, s87, 818176\nQe 4\nQmvaa 8,120,88\n"
"buffer_load_dwordx4 v62, s[52:55], s69 offen offset:1024 lds\nQmvaa 8,124,92\nQe 5\nQcvaa 12,128,80,44\nQe 6\nQmvaa 12,132,84\nQe 7\nv_smfmac_f32_16x16x64_bf16 a[180:183], a[204:207], v[16:23], v77\n"
"s_add_u32 s69, s87, 849912\nQmvaa 12,136,88\nbuffer_load_dwordx4 v62, s[52:55], s69 offen offset:2056 lds\nQmvaa 12,140,92\nQk 16,0,1\nQk 17,2,3\nQmvav 176,144,20\nQk 18,4,5\n"
"Qk 19,6,7\nQmvav 180,148,20\nQmvav 184,152,20\ns_add_u32 s69, s87, 881656\nQe 8\nQmvav 188,156,20\nbuffer_load_dwordx4 v62, s[52:55], s69 offen offset:3080 lds\nQmvav 192,160,20\n"
"Qe 9\nQmvav 196,164,20\nQe 10\nQmvav 200,168,20\nQmvav 204,172,20\nQe 11\nQcvaa 0,112,96,48\nQda 112,53,33024\n"
"Qk 20,8,9\nQmvaa 0,116,100\nQda 116,53,33536\nQk 21,10,11\nQmvaa 0,120,104\nQda 120,53,34048\nQmvaa 0,124,108\nQda 124,53,34560\n"
"Qcvaa 4,128,96,48\nQda 128,53,35072\nQmvaa 4,132,100\nQda 132,53,35584\nQmvaa 4,136,104\nQda 136,53,36096\nQmvaa 4,140,108\nQda 140,53,36608\n"
"Qmvav 208,144,16\nQe 12\nQmvav 212,148,16\ns_add_u32 m0, s61, 0\nQmvav 216,152,16\nbuffer_load_dwordx4 v56, s[40:43], s60 offen nt lds\nQe 13\nQmvav 220,156,16\n"
"Qe 14\nQmvav 224,160,16\nQe 15\nQmvav 228,164,16\nQk 22,12,13\nQk 23,14,15\nQmvav 232,168,16\nbuffer_load_dwordx4 v57, s[40:43], s60 offen offset:1024 nt lds\n"
"Qe 0\nQmvav 236,172,16\ns_waitcnt lgkmcnt(0)\nQcvaa 8,112,0,24\nQe 1\nQmvaa 8,116,4\nQe 2\nQmvaa 8,120,8\n"
"buffer_load_dwordx4 v56, s[40:43], s60 offen offset:2048 nt lds\nQe 3\nQmvaa 8,124,12\nQe 4\nQcvaa 12,128,0,24\nQe 5\nQmvaa 12,132,4\nQe 6\n"
"v_smfmac_f32_16x16x64_bf16 a[184:187], a[204:207], v[16:23], v77\nQmvaa 12,136,8\nbuffer_load_dwordx4 v57, s[40:43], s60 offen offset:3072 nt lds\nQk 16,0,1\nQk 17,2,3\nQmvaa 12,140,12\nQe 7\nQmvav 240,144,20\n"
"s_add_u32 m0, s61, 8192\nQmvav 244,148,20\nbuffer_load_dwordx4 v58, s[44:47], s60 offen nt lds\nQmvav 248,152,20\nQk 18,4,5\nQmvav 252,156,20\nQk 19,6,7\nQmaav 208,160,20\n"
"Qmaav 212,164,20\nbuffer_load_dwordx4 v59, s[44:47], s60 offen offset:1024 nt lds\nQe 8\nQmaav 216,168,20\nQe 9\nQmaav 220,172,20\nQe 10\nQcvaa 0,112,16,28\n"
"Qe 11\nQmvaa 0,116,20\nbuffer_load_dwordx4 v58, s[44:47], s60 offen offset:2048 nt lds\nQk 20,8,9\nQk 21,10,11\nQmvaa 0,120,24\nQe 12\nQmvaa 0,124,28\n"
"Qe 13\nQcvaa 4,128,16,28\nQe 14\nQmvaa 4,132,20\nbuffer_load_dwordx4 v59, s[44:47], s60 offen offset:3072 nt lds\nQe 15\nQmvaa 4,136,24\nQk 22,12,13\n"
"Qk 23,14,15\nQmvaa 4,140,28\ns_add_u32 s60, s60, 8192\nQmaav 224,144,16\nQta 144,55,33024\nQta 146,55,35072\nQmaav 228,148,16\nQta 148,55,33280\n"
"Qta 150,55,35328\nQmaav 232,152,16\nQta 152,55,33536\nQta 154,55,35584\nQmaav 236,156,16\nQta 156,55,33792\nQta 158,55,35840\nQmaav 240,160,16\n"
"Qta 160,55,34048\nQta 162,55,36096\nQmaav 244,164,16\nQta 164,55,34304\nQta 166,55,36352\nQmaav 248,168,16\nQta 168,55,34560\nQta 170,55,36608\n"
"Qmaav 252,172,16\nQta 172,55,34816\nQta 174,55,36864\nQcvaa 8,112,32,32\nv_add_u32 v75, 1, v75\nQmvaa 8,116,36\nds_write_b32 v73, v75\nQmvaa 8,120,40\n"
"Qe 0\nQmvaa 8,124,44\nQe 1\nQcvaa 12,128,32,32\nQe 2\nQmvaa 12,132,36\nQe 3\nv_smfmac_f32_16x16x64_bf16 a[188:191], a[204:207], v[16:23], v77\n"
"Qmvaa 12,136,40\ns_waitcnt lgkmcnt(1)\nQk 16,0,1\nQmvaa 12,140,44\nQk 17,2,3\nQmvav 80,144,20\nQe 4\nQmvav 84,148,20\n"
"Qe 5\nQmvav 88,152,20\nQe 6\nQmvav 92,156,20\nQe 7\nQmvav 96,160,20\nQk 18,4,5\nQk 19,6,7\n"
"Qmvav 100,164,20\nQe 8\nQmvav 104,168,20\nQe 9\nQmvav 108,172,20\nQe 10\nQcvaa 0,112,48,36\nQe 11\n"
"Qmvaa 0,116,52\nds_read_b32 v69, v74\nQk 20,8,9\nQmvaa 0,120,56\nQk 21,10,11\nQmvaa 0,124,60\nQe 12\nQcvaa 4,128,48,36\n"
"Qe 13\nQmvaa 4,132,52\nQe 14\nQmvaa 4,136,56\ns_waitcnt lgkmcnt(0)\nv_cmp_gt_u32 vcc, v75, v69\nQmvaa 4,140,60\ns_cbranch_vccnz .Lsa31\n"
".Lra31:\nQk 22,12,13\nQmvav 112,144,16\ns_add_u32 m0, s63, 0\ns_add_u32 s88, s86, 128\nQmvav 116,148,16\nbuffer_load_dwordx4 v64, s[48:51], s88 offen lds\nQmvav 120,152,16\n"
"Qe 15\nQmvav 124,156,16\nQk 23,14,15\nQmvav 128,160,16\ns_add_u32 s88, s86, 19584\nQe 0\nQmvav 132,164,16\nbuffer_load_dwordx4 v64, s[48:51], s88 offen offset:1024 lds\n"
"Qe 1\nQmvav 136,168,16\nQmvav 140,172,16\nQe 2\nQcvaa 8,112,64,40\ns_add_u32 s88, s86, 39040\nQe 3\nQmvaa 8,116,68\n"
"buffer_load_dwordx4 v64, s[48:51], s88 offen offset:2048 lds\nQe 4\nQmvaa 8,120,72\nQe 5\nQmvaa 8,124,76\nQcvaa 12,128,64,40\ns_add_u32 s88, s86, 58496\nQe 6\n"
"Qmvaa 12,132,68\nbuffer_load_dwordx4 v64, s[48:51], s88 offen offset:3072 lds\nQe 7\nv_smfmac_f32_16x16x64_bf16 a[192:195], a[204:207], v[16:23], v77\nQmvaa 12,136,72\nQk 16,0,1\nQk 17,2,3\nQmvaa 12,140,76\n"
"s_add_u32 m0, s63, 4096\ns_add_u32 s88, s86, 82048\nQmvav 144,144,20\nbuffer_load_dwordx4 v64, s[48:51], s88 offen lds\nQk 18,4,5\nQk 19,6,7\nQmvav 148,148,20\nQmvav 152,152,20\n"
"Qe 8\nQmvav 156,156,20\ns_add_u32 s88, s86, 101504\nQmvav 160,160,20\nbuffer_load_dwordx4 v64, s[48:51], s88 offen offset:1024 lds\nQmvav 164,164,20\nQe 9\nQmvav 168,168,20\n"
"Qe 10\nQmvav 172,172,20\ns_add_u32 s88, s86, 120960\nQe 11\nQcvaa 0,112,80,44\nbuffer_load_dwordx4 v64, s[48:51], s88 offen offset:2048 lds\nQk 20,8,9\nQk 21,10,11\n"
"Qmvaa 0,116,84\nQmvaa 0,120,88\nQe 12\nQmvaa 0,124,92\nQe 13\nQcvaa 4,128,80,44\nQe 14\nQmvaa 4,132,84\n"
"Qe 15\nQmvaa 4,136,88\nQmvaa 4,140,92\nQk 22,12,13\nQk 23,14,15\nQmvav 176,144,16\nQmvav 180,148,16\nQmvav 184,152,16\n"
"Qmvav 188,156,16\nQmvav 192,160,16\nQe 0\nQmvav 196,164,16\nQe 1\nQmvav 200,168,16\nQmvav 204,172,16\nQe 2\n"
"Qcvaa 8,112,96,48\nQmvaa 8,116,100\nQmvaa 8,120,104\nQmvaa 8,124,108\n.Lpg8:\ns_cbranch_execz .Ltr10\nQcvaa 12,128,96,48\nQmvaa 12,132,100\n"
"v_smfmac_f32_16x16x64_bf16 a[196:199], a[204:207], v[16:23], v77\nQmvaa 12,136,104\nQk 16,0,1\nQmvaa 12,140,108\nQmvav 208,144,20\nQmvav 212,148,20\nQe 3\nQmvav 216,152,20\n"
"Qk 17,2,3\nQmvav 220,156,20\nQe 4\nQmvav 224,160,20\nQe 5\nQmvav 228,164,20\nQe 6\nQmvav 232,168,20\n"
"Qe 7\nQmvav 236,172,20\nQk 18,4,5\nQk 19,6,7\nQe 8\nQe 9\nQe 10\nQe 11\n"
"Qk 20,8,9\nQk 21,10,11\nQe 12\nQe 13\nQmvav 240,144,16\nQe 14\nQmvav 244,148,16\nQmvav 248,152,16\n"
"Qmvav 252,156,16\nQk 22,12,13\nQmaav 208,160,16\nQmaav 212,164,16\nQmaav 216,168,16\nQe 15\nQmaav 220,172,16\nQk 23,14,15\n"
"s_nop 1\nv_smfmac_f32_16x16x64_bf16 a[200:203], a[204:207], v[16:23], v77\nQmaav 224,144,20\nQmaav 228,148,20\nQmaav 232,152,20\nQmaav 236,156,20\nQmaav 240,160,20\nQmaav 244,164,20\n"
"Qmaav 248,168,20\nQmaav 252,172,20\nv_accvgpr_read_b32 v24, a176\nv_accvgpr_read_b32 v25, a177\nv_accvgpr_read_b32 v30, a178\nv_accvgpr_read_b32 v31, a179\nv_accvgpr_read_b32 v32, a180\nv_accvgpr_read_b32 v33, a181\n"
"v_accvgpr_read_b32 v38, a182\nv_accvgpr_read_b32 v39, a183\nv_accvgpr_read_b32 v40, a184\nv_accvgpr_read_b32 v41, a185\nv_accvgpr_read_b32 v46, a186\nv_accvgpr_read_b32 v47, a187\nv_accvgpr_read_b32 v48, a188\nv_accvgpr_read_b32 v49, a189\n"
"v_accvgpr_read_b32 v26, a190\nv_accvgpr_read_b32 v27, a191\nv_accvgpr_read_b32 v28, a192\nv_accvgpr_read_b32 v29, a193\nv_accvgpr_read_b32 v34, a194\nv_accvgpr_read_b32 v35, a195\nv_accvgpr_read_b32 v36, a196\nv_accvgpr_read_b32 v37, a197\n"
"v_accvgpr_read_b32 v42, a198\nv_accvgpr_read_b32 v43, a199\nv_accvgpr_read_b32 v44, a200\nv_accvgpr_read_b32 v45, a201\nv_accvgpr_read_b32 v50, a202\nv_accvgpr_read_b32 v51, a203\nv_pk_add_f32 v[24:25], v[24:25], v[26:27]\nv_pk_add_f32 v[28:29], v[28:29], v[30:31]\n"
"v_pk_add_f32 v[32:33], v[32:33], v[34:35]\nv_pk_add_f32 v[36:37], v[36:37], v[38:39]\nv_pk_add_f32 v[40:41], v[40:41], v[42:43]\nv_pk_add_f32 v[44:45], v[44:45], v[46:47]\nv_pk_add_f32 v[48:49], v[48:49], v[50:51]\nQa 24,24,25\nQa 28,28,29\nQa 32,32,33\n"
"Qa 36,36,37\nQa 40,40,41\nQa 44,44,45\nQa 48,48,49\nv_cmp_nge_f32 vcc, 0x44800000, v24\ns_mov_b64 s[74:75], vcc\nv_cmp_nge_f32 vcc, 0x44800000, v28\ns_or_b64 s[74:75], s[74:75], vcc\n"
"v_cmp_nge_f32 vcc, 0x44800000, v32\ns_or_b64 s[74:75], s[74:75], vcc\nv_cmp_nge_f32 vcc, 0x44800000, v36\ns_or_b64 s[74:75], s[74:75], vcc\nv_cmp_nge_f32 vcc, 0x44800000, v40\ns_or_b64 s[74:75], s[74:75], vcc\nv_cmp_nge_f32 vcc, 0x44800000, v44\ns_or_b64 s[74:75], s[74:75], vcc\n"
"v_cmp_nge_f32 vcc, 0x44800000, v48\ns_or_b64 s[74:75], s[74:75], vcc\ns_cmp_lg_u64 s[74:75], 0\ns_cbranch_scc1 .Lfb\n.Lfbret0:\nv_rcp_f32 v25, v24\nv_rcp_f32 v24, v24\nv_rcp_f32 v29, v28\n"
"v_rcp_f32 v28, v28\nv_rcp_f32 v33, v32\nv_rcp_f32 v32, v32\nv_rcp_f32 v37, v36\nv_rcp_f32 v36, v36\nv_rcp_f32 v41, v40\nv_rcp_f32 v40, v40\nv_rcp_f32 v45, v44\n"
"v_rcp_f32 v44, v44\nv_rcp_f32 v49, v48\nv_rcp_f32 v48, v48\nv_pk_mul_f32 v[80:81], v[80:81], v[24:25]\nv_pk_mul_f32 v[82:83], v[82:83], v[24:25]\nv_pk_mul_f32 v[84:85], v[84:85], v[24:25]\nv_pk_mul_f32 v[86:87], v[86:87], v[24:25]\nQk 80,80,81\n"
"Qk 81,82,83\nQk 82,84,85\nQk 83,86,87\nv_accvgpr_write_b32 a0, v80\nv_accvgpr_write_b32 a1, v81\nv_accvgpr_write_b32 a2, v82\nv_accvgpr_write_b32 a3, v83\nv_pk_mul_f32 v[88:89], v[88:89], v[24:25]\n"
"v_pk_mul_f32 v[90:91], v[90:91], v[24:25]\nv_pk_mul_f32 v[92:93], v[92:93], v[24:25]\nv_pk_mul_f32 v[94:95], v[94:95], v[24:25]\nQk 88,88,89\nQk 89,90,91\nQk 90,92,93\nQk 91,94,95\nv_accvgpr_write_b32 a4, v88\n"
"v_accvgpr_write_b32 a5, v89\nv_accvgpr_write_b32 a6, v90\nv_accvgpr_write_b32 a7, v91\nv_pk_mul_f32 v[96:97], v[96:97], v[24:25]\nv_pk_mul_f32 v[98:99], v[98:99], v[24:25]\nv_pk_mul_f32 v[100:101], v[100:101], v[24:25]\nv_pk_mul_f32 v[102:103], v[102:103], v[24:25]\nQk 96,96,97\n"
"Qk 97,98,99\nQk 98,100,101\nQk 99,102,103\nv_accvgpr_write_b32 a8, v96\nv_accvgpr_write_b32 a9, v97\nv_accvgpr_write_b32 a10, v98\nv_accvgpr_write_b32 a11, v99\nv_pk_mul_f32 v[104:105], v[104:105], v[24:25]\n"
"v_pk_mul_f32 v[106:107], v[106:107], v[24:25]\nv_pk_mul_f32 v[108:109], v[108:109], v[24:25]\nv_pk_mul_f32 v[110:111], v[110:111], v[24:25]\nQk 104,104,105\nQk 105,106,107\nQk 106,108,109\nQk 107,110,111\nv_accvgpr_write_b32 a12, v104\n"
"v_accvgpr_write_b32 a13, v105\nv_accvgpr_write_b32 a14, v106\nv_accvgpr_write_b32 a15, v107\nv_pk_mul_f32 v[112:113], v[112:113], v[28:29]\nv_pk_mul_f32 v[114:115], v[114:115], v[28:29]\nv_pk_mul_f32 v[116:117], v[116:117], v[28:29]\nv_pk_mul_f32 v[118:119], v[118:119], v[28:29]\nQk 112,112,113\n"
"Qk 113,114,115\nQk 114,116,117\nQk 115,118,119\nv_accvgpr_write_b32 a16, v112\nv_accvgpr_write_b32 a17, v113\nv_accvgpr_write_b32 a18, v114\nv_accvgpr_write_b32 a19, v115\nv_pk_mul_f32 v[120:121], v[120:121], v[28:29]\n"
"v_pk_mul_f32 v[122:123], v[122:123], v[28:29]\nv_pk_mul_f32 v[124:125], v[124:125], v[28:29]\nv_pk_mul_f32 v[126:127], v[126:127], v[28:29]\nQk 120,120,121\nQk 121,122,123\nQk 122,124,125\nQk 123,126,127\nv_accvgpr_write_b32 a20, v120\n"
"v_accvgpr_write_b32 a21, v121\nv_accvgpr_write_b32 a22, v122\nv_accvgpr_write_b32 a23, v123\nv_pk_mul_f32 v[128:129], v[128:129], v[28:29]\nv_pk_mul_f32 v[130:131], v[130:131], v[28:29]\nv_pk_mul_f32 v[132:133], v[132:133], v[28:29]\nv_pk_mul_f32 v[134:135], v[134:135], v[28:29]\nQk 128,128,129\n"
"Qk 129,130,131\nQk 130,132,133\nQk 131,134,135\nv_accvgpr_write_b32 a24, v128\nv_accvgpr_write_b32 a25, v129\nv_accvgpr_write_b32 a26, v130\nv_accvgpr_write_b32 a27, v131\nv_pk_mul_f32 v[136:137], v[136:137], v[28:29]\n"
"v_pk_mul_f32 v[138:139], v[138:139], v[28:29]\nv_pk_mul_f32 v[140:141], v[140:141], v[28:29]\nv_pk_mul_f32 v[142:143], v[142:143], v[28:29]\nQk 136,136,137\nQk 137,138,139\nQk 138,140,141\nQk 139,142,143\nv_accvgpr_write_b32 a28, v136\n"
"v_accvgpr_write_b32 a29, v137\nv_accvgpr_write_b32 a30, v138\nv_accvgpr_write_b32 a31, v139\nv_pk_mul_f32 v[144:145], v[144:145], v[32:33]\nv_pk_mul_f32 v[146:147], v[146:147], v[32:33]\nv_pk_mul_f32 v[148:149], v[148:149], v[32:33]\nv_pk_mul_f32 v[150:151], v[150:151], v[32:33]\nQk 144,144,145\n"
"Qk 145,146,147\nQk 146,148,149\nQk 147,150,151\nv_accvgpr_write_b32 a32, v144\nv_accvgpr_write_b32 a33, v145\nv_accvgpr_write_b32 a34, v146\nv_accvgpr_write_b32 a35, v147\nv_pk_mul_f32 v[152:153], v[152:153], v[32:33]\n"
"v_pk_mul_f32 v[154:155], v[154:155], v[32:33]\nv_pk_mul_f32 v[156:157], v[156:157], v[32:33]\nv_pk_mul_f32 v[158:159], v[158:159], v[32:33]\nQk 152,152,153\nQk 153,154,155\nQk 154,156,157\nQk 155,158,159\nv_accvgpr_write_b32 a36, v152\n"
"v_accvgpr_write_b32 a37, v153\nv_accvgpr_write_b32 a38, v154\nv_accvgpr_write_b32 a39, v155\nv_pk_mul_f32 v[160:161], v[160:161], v[32:33]\nv_pk_mul_f32 v[162:163], v[162:163], v[32:33]\nv_pk_mul_f32 v[164:165], v[164:165], v[32:33]\nv_pk_mul_f32 v[166:167], v[166:167], v[32:33]\nQk 160,160,161\n"
"Qk 161,162,163\nQk 162,164,165\nQk 163,166,167\nv_accvgpr_write_b32 a40, v160\nv_accvgpr_write_b32 a41, v161\nv_accvgpr_write_b32 a42, v162\nv_accvgpr_write_b32 a43, v163\nv_pk_mul_f32 v[168:169], v[168:169], v[32:33]\n"
"v_pk_mul_f32 v[170:171], v[170:171], v[32:33]\nv_pk_mul_f32 v[172:173], v[172:173], v[32:33]\nv_pk_mul_f32 v[174:175], v[174:175], v[32:33]\nQk 168,168,169\nQk 169,170,171\nQk 170,172,173\nQk 171,174,175\nv_accvgpr_write_b32 a44, v168\n"
"v_accvgpr_write_b32 a45, v169\nv_accvgpr_write_b32 a46, v170\nv_accvgpr_write_b32 a47, v171\nv_pk_mul_f32 v[176:177], v[176:177], v[36:37]\nv_pk_mul_f32 v[178:179], v[178:179], v[36:37]\nv_pk_mul_f32 v[180:181], v[180:181], v[36:37]\nv_pk_mul_f32 v[182:183], v[182:183], v[36:37]\nQk 176,176,177\n"
"Qk 177,178,179\nQk 178,180,181\nQk 179,182,183\nv_accvgpr_write_b32 a48, v176\nv_accvgpr_write_b32 a49, v177\nv_accvgpr_write_b32 a50, v178\nv_accvgpr_write_b32 a51, v179\nv_pk_mul_f32 v[184:185], v[184:185], v[36:37]\n"
"v_pk_mul_f32 v[186:187], v[186:187], v[36:37]\nv_pk_mul_f32 v[188:189], v[188:189], v[36:37]\nv_pk_mul_f32 v[190:191], v[190:191], v[36:37]\nQk 184,184,185\nQk 185,186,187\nQk 186,188,189\nQk 187,190,191\nv_accvgpr_write_b32 a52, v184\n"
"v_accvgpr_write_b32 a53, v185\nv_accvgpr_write_b32 a54, v186\nv_accvgpr_write_b32 a55, v187\nv_pk_mul_f32 v[192:193], v[192:193], v[36:37]\nv_pk_mul_f32 v[194:195], v[194:195], v[36:37]\nv_pk_mul_f32 v[196:197], v[196:197], v[36:37]\nv_pk_mul_f32 v[198:199], v[198:199], v[36:37]\nQk 192,192,193\n"
"Qk 193,194,195\nQk 194,196,197\nQk 195,198,199\nv_accvgpr_write_b32 a56, v192\nv_accvgpr_write_b32 a57, v193\nv_accvgpr_write_b32 a58, v194\nv_accvgpr_write_b32 a59, v195\nv_pk_mul_f32 v[200:201], v[200:201], v[36:37]\n"
"v_pk_mul_f32 v[202:203], v[202:203], v[36:37]\nv_pk_mul_f32 v[204:205], v[204:205], v[36:37]\nv_pk_mul_f32 v[206:207], v[206:207], v[36:37]\nQk 200,200,201\nQk 201,202,203\nQk 202,204,205\nQk 203,206,207\nv_accvgpr_write_b32 a60, v200\n"
"v_accvgpr_write_b32 a61, v201\nv_accvgpr_write_b32 a62, v202\nv_accvgpr_write_b32 a63, v203\nv_pk_mul_f32 v[208:209], v[208:209], v[40:41]\nv_pk_mul_f32 v[210:211], v[210:211], v[40:41]\nv_pk_mul_f32 v[212:213], v[212:213], v[40:41]\nv_pk_mul_f32 v[214:215], v[214:215], v[40:41]\nQk 208,208,209\n"
"Qk 209,210,211\nQk 210,212,213\nQk 211,214,215\nv_accvgpr_write_b32 a64, v208\nv_accvgpr_write_b32 a65, v209\nv_accvgpr_write_b32 a66, v210\nv_accvgpr_write_b32 a67, v211\nv_pk_mul_f32 v[216:217], v[216:217], v[40:41]\n"
"v_pk_mul_f32 v[218:219], v[218:219], v[40:41]\nv_pk_mul_f32 v[220:221], v[220:221], v[40:41]\nv_pk_mul_f32 v[222:223], v[222:223], v[40:41]\nQk 216,216,217\nQk 217,218,219\nQk 218,220,221\nQk 219,222,223\nv_accvgpr_write_b32 a68, v216\n"
"v_accvgpr_write_b32 a69, v217\nv_accvgpr_write_b32 a70, v218\nv_accvgpr_write_b32 a71, v219\nv_pk_mul_f32 v[224:225], v[224:225], v[40:41]\nv_pk_mul_f32 v[226:227], v[226:227], v[40:41]\nv_pk_mul_f32 v[228:229], v[228:229], v[40:41]\nv_pk_mul_f32 v[230:231], v[230:231], v[40:41]\nQk 224,224,225\n"
"Qk 225,226,227\nQk 226,228,229\nQk 227,230,231\nv_accvgpr_write_b32 a72, v224\nv_accvgpr_write_b32 a73, v225\nv_accvgpr_write_b32 a74, v226\nv_accvgpr_write_b32 a75, v227\nv_pk_mul_f32 v[232:233], v[232:233], v[40:41]\n"
"v_pk_mul_f32 v[234:235], v[234:235], v[40:41]\nv_pk_mul_f32 v[236:237], v[236:237], v[40:41]\nv_pk_mul_f32 v[238:239], v[238:239], v[40:41]\nQk 232,232,233\nQk 233,234,235\nQk 234,236,237\nQk 235,238,239\nv_accvgpr_write_b32 a76, v232\n"
"v_accvgpr_write_b32 a77, v233\nv_accvgpr_write_b32 a78, v234\nv_accvgpr_write_b32 a79, v235\nv_pk_mul_f32 v[240:241], v[240:241], v[44:45]\nv_pk_mul_f32 v[242:243], v[242:243], v[44:45]\nv_pk_mul_f32 v[244:245], v[244:245], v[44:45]\nv_pk_mul_f32 v[246:247], v[246:247], v[44:45]\nQk 240,240,241\n"
"Qk 241,242,243\nQk 242,244,245\nQk 243,246,247\nv_accvgpr_write_b32 a80, v240\nv_accvgpr_write_b32 a81, v241\nv_accvgpr_write_b32 a82, v242\nv_accvgpr_write_b32 a83, v243\nv_pk_mul_f32 v[248:249], v[248:249], v[44:45]\n"
"v_pk_mul_f32 v[250:251], v[250:251], v[44:45]\nv_pk_mul_f32 v[252:253], v[252:253], v[44:45]\nv_pk_mul_f32 v[254:255], v[254:255], v[44:45]\nQk 248,248,249\nQk 249,250,251\nQk 250,252,253\nQk 251,254,255\nv_accvgpr_write_b32 a84, v248\n"
"v_accvgpr_write_b32 a85, v249\nv_accvgpr_write_b32 a86, v250\nv_accvgpr_write_b32 a87, v251\nv_accvgpr_read_b32 v0, a208\nv_accvgpr_read_b32 v1, a209\nv_accvgpr_read_b32 v2, a210\nv_accvgpr_read_b32 v3, a211\nv_accvgpr_read_b32 v4, a212\n"
"v_accvgpr_read_b32 v5, a213\nv_accvgpr_read_b32 v6, a214\nv_accvgpr_read_b32 v7, a215\nv_pk_mul_f32 v[0:1], v[0:1], v[44:45]\nv_pk_mul_f32 v[2:3], v[2:3], v[44:45]\nv_pk_mul_f32 v[4:5], v[4:5], v[44:45]\nv_pk_mul_f32 v[6:7], v[6:7], v[44:45]\nQk 0,0,1\n"
"Qk 1,2,3\nQk 2,4,5\nQk 3,6,7\nv_accvgpr_write_b32 a88, v0\nv_accvgpr_write_b32 a89, v1\nv_accvgpr_write_b32 a90, v2\nv_accvgpr_write_b32 a91, v3\nv_accvgpr_read_b32 v8, a216\n"
"v_accvgpr_read_b32 v9, a217\nv_accvgpr_read_b32 v10, a218\nv_accvgpr_read_b32 v11, a219\nv_accvgpr_read_b32 v12, a220\nv_accvgpr_read_b32 v13, a221\nv_accvgpr_read_b32 v14, a222\nv_accvgpr_read_b32 v15, a223\nv_pk_mul_f32 v[8:9], v[8:9], v[44:45]\n"
"v_pk_mul_f32 v[10:11], v[10:11], v[44:45]\nv_pk_mul_f32 v[12:13], v[12:13], v[44:45]\nv_pk_mul_f32 v[14:15], v[14:15], v[44:45]\nQk 8,8,9\nQk 9,10,11\nQk 10,12,13\nQk 11,14,15\nv_accvgpr_write_b32 a92, v8\n"
"v_accvgpr_write_b32 a93, v9\nv_accvgpr_write_b32 a94, v10\nv_accvgpr_write_b32 a95, v11\nv_accvgpr_read_b32 v0, a224\nv_accvgpr_read_b32 v1, a225\nv_accvgpr_read_b32 v2, a226\nv_accvgpr_read_b32 v3, a227\nv_accvgpr_read_b32 v4, a228\n"
"v_accvgpr_read_b32 v5, a229\nv_accvgpr_read_b32 v6, a230\nv_accvgpr_read_b32 v7, a231\nv_pk_mul_f32 v[0:1], v[0:1], v[48:49]\nv_pk_mul_f32 v[2:3], v[2:3], v[48:49]\nv_pk_mul_f32 v[4:5], v[4:5], v[48:49]\nv_pk_mul_f32 v[6:7], v[6:7], v[48:49]\nQk 0,0,1\n"
"Qk 1,2,3\nQk 2,4,5\nQk 3,6,7\nv_accvgpr_write_b32 a96, v0\nv_accvgpr_write_b32 a97, v1\nv_accvgpr_write_b32 a98, v2\nv_accvgpr_write_b32 a99, v3\nv_accvgpr_read_b32 v8, a232\n"
"v_accvgpr_read_b32 v9, a233\nv_accvgpr_read_b32 v10, a234\nv_accvgpr_read_b32 v11, a235\nv_accvgpr_read_b32 v12, a236\nv_accvgpr_read_b32 v13, a237\nv_accvgpr_read_b32 v14, a238\nv_accvgpr_read_b32 v15, a239\nv_pk_mul_f32 v[8:9], v[8:9], v[48:49]\n"
"v_pk_mul_f32 v[10:11], v[10:11], v[48:49]\nv_pk_mul_f32 v[12:13], v[12:13], v[48:49]\nv_pk_mul_f32 v[14:15], v[14:15], v[48:49]\nQk 8,8,9\nQk 9,10,11\nQk 10,12,13\nQk 11,14,15\nv_accvgpr_write_b32 a100, v8\n"
"v_accvgpr_write_b32 a101, v9\nv_accvgpr_write_b32 a102, v10\nv_accvgpr_write_b32 a103, v11\nv_accvgpr_read_b32 v0, a240\nv_accvgpr_read_b32 v1, a241\nv_accvgpr_read_b32 v2, a242\nv_accvgpr_read_b32 v3, a243\nv_accvgpr_read_b32 v4, a244\n"
"v_accvgpr_read_b32 v5, a245\nv_accvgpr_read_b32 v6, a246\nv_accvgpr_read_b32 v7, a247\nv_pk_mul_f32 v[0:1], v[0:1], v[48:49]\nv_pk_mul_f32 v[2:3], v[2:3], v[48:49]\nv_pk_mul_f32 v[4:5], v[4:5], v[48:49]\nv_pk_mul_f32 v[6:7], v[6:7], v[48:49]\nQk 0,0,1\n"
"Qk 1,2,3\nQk 2,4,5\nQk 3,6,7\nv_accvgpr_write_b32 a104, v0\nv_accvgpr_write_b32 a105, v1\nv_accvgpr_write_b32 a106, v2\nv_accvgpr_write_b32 a107, v3\nv_accvgpr_read_b32 v8, a248\n"
"v_accvgpr_read_b32 v9, a249\nv_accvgpr_read_b32 v10, a250\nv_accvgpr_read_b32 v11, a251\nv_accvgpr_read_b32 v12, a252\nv_accvgpr_read_b32 v13, a253\nv_accvgpr_read_b32 v14, a254\nv_accvgpr_read_b32 v15, a255\nv_pk_mul_f32 v[8:9], v[8:9], v[48:49]\n"
"v_pk_mul_f32 v[10:11], v[10:11], v[48:49]\nv_pk_mul_f32 v[12:13], v[12:13], v[48:49]\nv_pk_mul_f32 v[14:15], v[14:15], v[48:49]\nQk 8,8,9\nQk 9,10,11\nQk 10,12,13\nQk 11,14,15\nv_accvgpr_write_b32 a108, v8\n"
"v_accvgpr_write_b32 a109, v9\nv_accvgpr_write_b32 a110, v10\nv_accvgpr_write_b32 a111, v11\ns_mov_b32 s56, %8\ns_and_b32 s57, %9, 0xffff\ns_mov_b32 s94, 0x7ffff000\ns_mul_i32 s68, %20, 40960\ns_lshl_b32 s69, s70, 10\n"
"s_add_u32 s68, s68, s69\ns_add_u32 s68, s68, 4096\ns_add_u32 s94, %15, s68\ns_addc_u32 s95, %16, 0\ns_and_b32 s95, s95, 0xffff\ns_mov_b32 s75, 37888\ns_mov_b32 s58, 1835008\ns_mov_b32 s67, 1\n"
"s_mul_i32 s71, %12, 57344\ns_mul_i32 s68, s92, 7168\ns_add_u32 s71, s71, s68\ns_mov_b64 exec, 0\ns_cmp_eq_u32 s70, 0\ns_cbranch_scc1 .Lpg12\ns_cmp_eq_u32 s70, 1\ns_cbranch_scc1 .Lpg13\n"
"s_cmp_eq_u32 s70, 2\ns_cbranch_scc1 .Lpg14\ns_branch .Lpg15\n.Ltr6:\ns_mov_b64 exec, -1\ns_branch .Lround\n.Lhook:\n.Lpg9:\n"
"s_cbranch_execz .Ltr10\ns_cmp_eq_u32 s66, 1\ns_cbranch_scc1 .Lh1\ns_cmp_eq_u32 s66, 8\ns_cbranch_scc1 .Lh8\ns_cmp_eq_u32 s66, 9\ns_cbranch_scc1 .Lh9\ns_lshr_b32 s68, s99, s66\n"
"s_cmp_eq_u32 s68, 1\ns_cbranch_scc1 .Lh12\ns_branch .Lh11\n.Lh1:\ns_cmp_eq_u32 s70, 0\ns_cselect_b32 s68, 128, 0\nv_mov_b32 v66, 0x7ffff000\n.Lpg10:\n"
"s_cbranch_execz .Ltr10\nv_cmp_gt_u32 vcc, s68, v60\nv_not_b32 v67, v65\ns_lshl_b32 s68, %19, 2\nv_cndmask_b32 v66, v66, v76, vcc\nv_readlane_b32 s30, v67, %19\nbuffer_store_dword v67, v66, s[24:27], s68 offen offset:1024 sc1\ns_branch .Lhookret\n"
".Lh8:\nv_bfe_u32 v66, v60, 4, 3\ns_lshl_b32 s68, %19, 7\nv_lshlrev_b32 v66, 2, v66\ns_add_u32 s68, s68, 2293760\nbuffer_load_dword v78, v66, s[24:27], s68 offen offset:1024 sc1\ns_branch .Lhookret\n.Lh9:\n"
"s_waitcnt vmcnt(32)\ns_mov_b32 s97, 0\n.Lack_l:\nv_cmp_ne_u32 vcc, s30, v78\ns_cmp_eq_u64 vcc, 0\ns_cbranch_scc1 .Lack_ok\ns_sleep 1\nv_bfe_u32 v66, v60, 4, 3\n"
"s_lshl_b32 s68, %19, 7\nv_lshlrev_b32 v66, 2, v66\ns_add_u32 s68, s68, 2293760\nbuffer_load_dword v78, v66, s[24:27], s68 offen offset:1024 sc1\ns_waitcnt vmcnt(0)\ns_add_u32 s97, s97, 1\ns_cmp_lt_u32 s97, 1048576\ns_cbranch_scc1 .Lack_l\n"
".Lack_ok:\ns_lshl_b32 s68, %19, 4\ns_cmp_eq_u32 s70, 0\ns_cselect_b32 s68, s68, -1\nv_mov_b32 v67, 0x7ffff000\nv_cmp_eq_u32 vcc, s68, v60\ns_nop 1\nv_cndmask_b32 v67, v67, v76, vcc\n"
"v_add_u32 v66, 1, v65\nbuffer_store_dword v66, v67, s[24:27], 0 offen sc1\ns_branch .Lhookret\n.Lh11:\nbuffer_load_dword v78, v76, s[24:27], 0 offen sc1\ns_branch .Lhookret\n.Lh12:\ns_waitcnt vmcnt(32)\n"
"v_sub_u32 v66, v78, v65\nv_cmp_gt_u32 vcc, 1, v66\ns_and_b64 vcc, vcc, s[28:29]\ns_cbranch_scc1 .Lf1_w\n.Lf1_r:\ns_cmp_lg_u32 s70, 3\ns_cbranch_scc1 .Lf1_n\nbuffer_inv sc1\n"
".Lf1_n:\ns_lshr_b32 s88, %19, 2\ns_mul_i32 s68, s88, 917504\ns_add_u32 s48, %8, s68\ns_addc_u32 s49, %9, 0\ns_mul_i32 s68, s88, 2621440\ns_and_b32 s69, %19, 3\ns_mul_i32 s69, s69, 320\n"
"s_add_u32 s68, s68, s69\ns_sub_u32 s88, 5246976, s68\ns_add_u32 s68, %15, s68\ns_addc_u32 s69, %16, 0\ns_cmp_lt_u32 s70, 2\ns_cselect_b32 s52, s48, s68\ns_cselect_b32 s53, s49, s69\ns_cselect_b32 s54, 917504, s88\n"
"s_cmp_lt_u32 s70, 3\ns_cselect_b32 s48, s48, s68\ns_cselect_b32 s49, s49, s69\ns_cselect_b32 s50, 917504, s88\ns_and_b32 s49, s49, 0xffff\ns_and_b32 s53, s53, 0xffff\ns_add_u32 s71, s71, 458752\ns_mov_b32 s62, s68\n"
"s_and_b32 s63, s69, 0xffff\ns_mov_b32 s86, s88\ns_mul_i32 s68, s70, 6\ns_add_u32 s68, s68, 3\ns_sub_u32 s69, s68, 3\ns_lshl_b32 s69, s69, 10\nv_add_u32 v66, s69, v60\ns_sub_u32 s69, s68, 14\n"
"s_lshl_b32 s69, s69, 2\ns_and_b32 s69, s69, 63\nv_lshrrev_b32 v67, 8, v60\nv_add_u32 v67, s69, v67\nv_mul_u32_u24 v68, 205, v67\nv_lshrrev_b32 v68, 11, v68\nv_mul_u32_u24 v61, 10, v68\nv_sub_u32 v67, v67, v61\n"
"v_lshlrev_b32 v67, 5, v67\nv_bfe_u32 v61, v60, 5, 3\nv_lshl_add_u32 v68, v68, 3, v61\nv_mul_u32_u24 v68, 1280, v68\nv_and_b32 v61, 16, v60\nv_add3_u32 v67, v67, v68, v61\nv_add_u32 v67, 1024, v67\ns_cmp_lt_u32 s68, 14\n"
"s_cselect_b64 vcc, -1, 0\nv_cndmask_b32 v61, v67, v66, vcc\ns_mul_i32 s68, s70, 6\ns_add_u32 s68, s68, 4\ns_sub_u32 s69, s68, 0\ns_lshl_b32 s69, s69, 10\nv_add_u32 v66, s69, v60\ns_sub_u32 s69, s68, 14\n"
"s_lshl_b32 s69, s69, 2\ns_and_b32 s69, s69, 63\nv_lshrrev_b32 v67, 8, v60\nv_add_u32 v67, s69, v67\nv_mul_u32_u24 v68, 205, v67\nv_lshrrev_b32 v68, 11, v68\nv_mul_u32_u24 v62, 10, v68\nv_sub_u32 v67, v67, v62\n"
"v_lshlrev_b32 v67, 5, v67\nv_bfe_u32 v62, v60, 5, 3\nv_lshl_add_u32 v68, v68, 3, v62\nv_mul_u32_u24 v68, 1280, v68\nv_and_b32 v62, 16, v60\nv_add3_u32 v67, v67, v68, v62\nv_add_u32 v67, 4096, v67\ns_cmp_lt_u32 s68, 14\n"
"s_cselect_b64 vcc, -1, 0\nv_cndmask_b32 v62, v67, v66, vcc\ns_mul_i32 s68, s70, 6\ns_add_u32 s68, s68, 5\ns_sub_u32 s69, s68, 1\ns_lshl_b32 s69, s69, 10\nv_add_u32 v66, s69, v60\ns_sub_u32 s69, s68, 14\n"
"s_lshl_b32 s69, s69, 2\ns_and_b32 s69, s69, 63\nv_lshrrev_b32 v67, 8, v60\nv_add_u32 v67, s69, v67\nv_mul_u32_u24 v68, 205, v67\nv_lshrrev_b32 v68, 11, v68\nv_mul_u32_u24 v63, 10, v68\nv_sub_u32 v67, v67, v63\n"
"v_lshlrev_b32 v67, 5, v67\nv_bfe_u32 v63, v60, 5, 3\nv_lshl_add_u32 v68, v68, 3, v63\nv_mul_u32_u24 v68, 1280, v68\nv_and_b32 v63, 16, v60\nv_add3_u32 v67, v67, v68, v63\nv_add_u32 v67, 3072, v67\ns_cmp_lt_u32 s68, 14\n"
"s_cselect_b64 vcc, -1, 0\nv_cndmask_b32 v63, v67, v66, vcc\ns_branch .Lhookret\n.Lf1_w:\ns_mov_b32 s97, 0\n.Lf1_l:\ns_sleep 1\nbuffer_load_dword v78, v76, s[24:27], 0 offen sc1\n"
"s_waitcnt vmcnt(0)\ns_add_u32 s97, s97, 1\ns_cmp_gt_u32 s97, 1048576\ns_cbranch_scc1 .Lf1_r\nv_sub_u32 v66, v78, v65\nv_cmp_gt_u32 vcc, 1, v66\ns_and_b64 vcc, vcc, s[28:29]\ns_cbranch_scc1 .Lf1_l\n"
"s_branch .Lf1_r\n.Lsa1:\nds_read_b32 v69, v74\ns_waitcnt lgkmcnt(0)\nv_cmp_gt_u32 vcc, v75, v69\ns_cbranch_vccnz .Lsa1\ns_branch .Lra1\n.Lsp1:\n"
"s_memrealtime s[74:75]\ns_waitcnt lgkmcnt(0)\ns_sub_u32 s75, s77, s74\ns_cmp_lt_u32 s75, 256\ns_cbranch_scc1 .Lsp1\ns_mov_b32 s74, s77\ns_branch .Lrp1\n.Lsa2:\n"
"ds_read_b32 v69, v74\ns_waitcnt lgkmcnt(0)\nv_cmp_gt_u32 vcc, v75, v69\ns_cbranch_vccnz .Lsa2\ns_branch .Lra2\n.Lsp2:\ns_memrealtime s[74:75]\ns_waitcnt lgkmcnt(0)\n"
"s_sub_u32 s75, s77, s74\ns_cmp_lt_u32 s75, 256\ns_cbranch_scc1 .Lsp2\ns_mov_b32 s74, s77\ns_branch .Lrp2\n.Lsa3:\nds_read_b32 v69, v74\ns_waitcnt lgkmcnt(0)\n"
"v_cmp_gt_u32 vcc, v75, v69\ns_cbranch_vccnz .Lsa3\ns_branch .Lra3\n.Lsp3:\ns_memrealtime s[74:75]\ns_waitcnt lgkmcnt(0)\ns_sub_u32 s75, s77, s74\ns_cmp_lt_u32 s75, 256\n"
"s_cbranch_scc1 .Lsp3\ns_mov_b32 s74, s77\ns_branch .Lrp3\n.Lsa4:\nds_read_b32 v69, v74\ns_waitcnt lgkmcnt(0)\nv_cmp_gt_u32 vcc, v75, v69\ns_cbranch_vccnz .Lsa4\n"
"s_branch .Lra4\n.Lsp4:\ns_memrealtime s[74:75]\ns_waitcnt lgkmcnt(0)\ns_sub_u32 s75, s77, s74\ns_cmp_lt_u32 s75, 256\ns_cbranch_scc1 .Lsp4\ns_mov_b32 s74, s77\n"
"s_branch .Lrp4\n.Lsa29:\nds_read_b32 v69, v74\ns_waitcnt lgkmcnt(0)\nv_cmp_gt_u32 vcc, v75, v69\ns_cbranch_vccnz .Lsa29\ns_branch .Lra29\n.Lsa30:\n"
"ds_read_b32 v69, v74\ns_waitcnt lgkmcnt(0)\nv_cmp_gt_u32 vcc, v75, v69\ns_cbranch_vccnz .Lsa30\ns_branch .Lra30\n.Lsa31:\nds_read_b32 v69, v74\ns_waitcnt lgkmcnt(0)\n"
"v_cmp_gt_u32 vcc, v75, v69\ns_cbranch_vccnz .Lsa31\ns_branch .Lra31\n.Lsa29x:\nds_read_b32 v69, v74\ns_waitcnt lgkmcnt(0)\nv_cmp_gt_u32 vcc, v75, v69\ns_cbranch_vccnz .Lsa29x\n"
"s_branch .Lra29x\n.Lsa30x:\nds_read_b32 v69, v74\ns_waitcnt lgkmcnt(0)\nv_cmp_gt_u32 vcc, v75, v69\ns_cbranch_vccnz .Lsa30x\ns_branch .Lra30x\n.Lsa31x:\n"
"ds_read_b32 v69, v74\ns_waitcnt lgkmcnt(0)\nv_cmp_gt_u32 vcc, v75, v69\ns_cbranch_vccnz .Lsa31x\ns_branch .Lra31x\n.Lso0:\nds_read_b32 v69, v74\ns_waitcnt lgkmcnt(0)\n"
"v_cmp_gt_u32 vcc, v75, v69\ns_cbranch_vccnz .Lso0\ns_branch .Lro0\n.Lso1:\nds_read_b32 v69, v74\ns_waitcnt lgkmcnt(0)\nv_cmp_gt_u32 vcc, v75, v69\ns_cbranch_vccnz .Lso1\n"
"s_branch .Lro1\n.Lso2:\nds_read_b32 v69, v74\ns_waitcnt lgkmcnt(0)\nv_cmp_gt_u32 vcc, v75, v69\ns_cbranch_vccnz .Lso2\ns_branch .Lro2\n.Lso3:\n"
"ds_read_b32 v69, v74\ns_waitcnt lgkmcnt(0)\nv_cmp_gt_u32 vcc, v75, v69\ns_cbranch_vccnz .Lso3\ns_branch .Lro3\n.Lso4:\nds_read_b32 v69, v74\ns_waitcnt lgkmcnt(0)\n"
"v_cmp_gt_u32 vcc, v75, v69\ns_cbranch_vccnz .Lso4\ns_branch .Lro4\n.Lso5:\nds_read_b32 v69, v74\ns_waitcnt lgkmcnt(0)\nv_cmp_gt_u32 vcc, v75, v69\ns_cbranch_vccnz .Lso5\n"
"s_branch .Lro5\n.Lso6:\nds_read_b32 v69, v74\ns_waitcnt lgkmcnt(0)\nv_cmp_gt_u32 vcc, v75, v69\ns_cbranch_vccnz .Lso6\ns_branch .Lro6\n.Lso7:\n"
"ds_read_b32 v69, v74\ns_waitcnt lgkmcnt(0)\nv_cmp_gt_u32 vcc, v75, v69\ns_cbranch_vccnz .Lso7\ns_branch .Lro7\n.Lso8:\nds_read_b32 v69, v74\ns_waitcnt lgkmcnt(0)\n"
"v_cmp_gt_u32 vcc, v75, v69\ns_cbranch_vccnz .Lso8\ns_branch .Lro8\n.Lso9:\nds_read_b32 v69, v74\ns_waitcnt lgkmcnt(0)\nv_cmp_gt_u32 vcc, v75, v69\ns_cbranch_vccnz .Lso9\n"
"s_branch .Lro9\n.Lso10:\nds_read_b32 v69, v74\ns_waitcnt lgkmcnt(0)\nv_cmp_gt_u32 vcc, v75, v69\ns_cbranch_vccnz .Lso10\ns_branch .Lro10\n.Lso11:\n"
"ds_read_b32 v69, v74\ns_waitcnt lgkmcnt(0)\nv_cmp_gt_u32 vcc, v75, v69\ns_cbranch_vccnz .Lso11\ns_branch .Lro11\n.Lso12:\nds_read_b32 v69, v74\ns_waitcnt lgkmcnt(0)\n"
"v_cmp_gt_u32 vcc, v75, v69\ns_cbranch_vccnz .Lso12\ns_branch .Lro12\n.Lso13:\nds_read_b32 v69, v74\ns_waitcnt lgkmcnt(0)\nv_cmp_gt_u32 vcc, v75, v69\ns_cbranch_vccnz .Lso13\n"
"s_branch .Lro13\n.Lso14:\nds_read_b32 v69, v74\ns_waitcnt lgkmcnt(0)\nv_cmp_gt_u32 vcc, v75, v69\ns_cbranch_vccnz .Lso14\ns_branch .Lro14\n.Lso15:\n"
"ds_read_b32 v69, v74\ns_waitcnt lgkmcnt(0)\nv_cmp_gt_u32 vcc, v75, v69\ns_cbranch_vccnz .Lso15\ns_branch .Lro15\n.Lso16:\nds_read_b32 v69, v74\ns_waitcnt lgkmcnt(0)\n"
"v_cmp_gt_u32 vcc, v75, v69\ns_cbranch_vccnz .Lso16\ns_branch .Lro16\n.Lso17:\nds_read_b32 v69, v74\ns_waitcnt lgkmcnt(0)\nv_cmp_gt_u32 vcc, v75, v69\ns_cbranch_vccnz .Lso17\n"
"s_branch .Lro17\n.Lso18:\nds_read_b32 v69, v74\ns_waitcnt lgkmcnt(0)\nv_cmp_gt_u32 vcc, v75, v69\ns_cbranch_vccnz .Lso18\ns_branch .Lro18\n.Lso19:\n"
"ds_read_b32 v69, v74\ns_waitcnt lgkmcnt(0)\nv_cmp_gt_u32 vcc, v75, v69\ns_cbranch_vccnz .Lso19\ns_branch .Lro19\n.Lso20:\nds_read_b32 v69, v74\ns_waitcnt lgkmcnt(0)\n"
"v_cmp_gt_u32 vcc, v75, v69\ns_cbranch_vccnz .Lso20\ns_branch .Lro20\n.Lso21:\nds_read_b32 v69, v74\ns_waitcnt lgkmcnt(0)\nv_cmp_gt_u32 vcc, v75, v69\ns_cbranch_vccnz .Lso21\n"
"s_branch .Lro21\n.Lso22:\nds_read_b32 v69, v74\ns_waitcnt lgkmcnt(0)\nv_cmp_gt_u32 vcc, v75, v69\ns_cbranch_vccnz .Lso22\ns_branch .Lro22\n.Lso23:\n"
"ds_read_b32 v69, v74\ns_waitcnt lgkmcnt(0)\nv_cmp_gt_u32 vcc, v75, v69\ns_cbranch_vccnz .Lso23\ns_branch .Lro23\n.Lso24:\nds_read_b32 v69, v74\ns_waitcnt lgkmcnt(0)\n"
"v_cmp_gt_u32 vcc, v75, v69\ns_cbranch_vccnz .Lso24\ns_branch .Lro24\n.Lso25:\nds_read_b32 v69, v74\ns_waitcnt lgkmcnt(0)\nv_cmp_gt_u32 vcc, v75, v69\ns_cbranch_vccnz .Lso25\n"
"s_branch .Lro25\n.Lso26:\nds_read_b32 v69, v74\ns_waitcnt lgkmcnt(0)\nv_cmp_gt_u32 vcc, v75, v69\ns_cbranch_vccnz .Lso26\ns_branch .Lro26\n.Lso27:\n"
"ds_read_b32 v69, v74\ns_waitcnt lgkmcnt(0)\nv_cmp_gt_u32 vcc, v75, v69\ns_cbranch_vccnz .Lso27\ns_branch .Lro27\n.Lso28:\nds_read_b32 v69, v74\ns_waitcnt lgkmcnt(0)\n"
"v_cmp_gt_u32 vcc, v75, v69\ns_cbranch_vccnz .Lso28\ns_branch .Lro28\n.Lso29:\nds_read_b32 v69, v74\ns_waitcnt lgkmcnt(0)\nv_cmp_gt_u32 vcc, v75, v69\ns_cbranch_vccnz .Lso29\n"
"s_branch .Lro29\n.Lso30:\nds_read_b32 v69, v74\ns_waitcnt lgkmcnt(0)\nv_cmp_gt_u32 vcc, v75, v69\ns_cbranch_vccnz .Lso30\ns_branch .Lro30\n.Lso31:\n"
"ds_read_b32 v69, v74\ns_waitcnt lgkmcnt(0)\nv_cmp_gt_u32 vcc, v75, v69\ns_cbranch_vccnz .Lso31\ns_branch .Lro31\n.Lso32:\nds_read_b32 v69, v74\ns_waitcnt lgkmcnt(0)\n"
"v_cmp_gt_u32 vcc, v75, v69\ns_cbranch_vccnz .Lso32\ns_branch .Lro32\n.Lso33:\nds_read_b32 v69, v74\ns_waitcnt lgkmcnt(0)\nv_cmp_gt_u32 vcc, v75, v69\ns_cbranch_vccnz .Lso33\n"
"s_branch .Lro33\n.Lso34:\nds_read_b32 v69, v74\ns_waitcnt lgkmcnt(0)\nv_cmp_gt_u32 vcc, v75, v69\ns_cbranch_vccnz .Lso34\ns_branch .Lro34\n.Lso35:\n"
"ds_read_b32 v69, v74\ns_waitcnt lgkmcnt(0)\nv_cmp_gt_u32 vcc, v75, v69\ns_cbranch_vccnz .Lso35\ns_branch .Lro35\n.Lso36:\nds_read_b32 v69, v74\ns_waitcnt lgkmcnt(0)\n"
"v_cmp_gt_u32 vcc, v75, v69\ns_cbranch_vccnz .Lso36\ns_branch .Lro36\n.Lso37:\nds_read_b32 v69, v74\ns_waitcnt lgkmcnt(0)\nv_cmp_gt_u32 vcc, v75, v69\ns_cbranch_vccnz .Lso37\n"
"s_branch .Lro37\n.Lso38:\nds_read_b32 v69, v74\ns_waitcnt lgkmcnt(0)\nv_cmp_gt_u32 vcc, v75, v69\ns_cbranch_vccnz .Lso38\ns_branch .Lro38\n.Lso39:\n"
"ds_read_b32 v69, v74\ns_waitcnt lgkmcnt(0)\nv_cmp_gt_u32 vcc, v75, v69\ns_cbranch_vccnz .Lso39\ns_branch .Lro39\n.Lso40:\nds_read_b32 v69, v74\ns_waitcnt lgkmcnt(0)\n"
"v_cmp_gt_u32 vcc, v75, v69\ns_cbranch_vccnz .Lso40\ns_branch .Lro40\n.Lso41:\nds_read_b32 v69, v74\ns_waitcnt lgkmcnt(0)\nv_cmp_gt_u32 vcc, v75, v69\ns_cbranch_vccnz .Lso41\n"
"s_branch .Lro41\n.Lso42:\nds_read_b32 v69, v74\ns_waitcnt lgkmcnt(0)\nv_cmp_gt_u32 vcc, v75, v69\ns_cbranch_vccnz .Lso42\ns_branch .Lro42\n.Lso43:\n"
"ds_read_b32 v69, v74\ns_waitcnt lgkmcnt(0)\nv_cmp_gt_u32 vcc, v75, v69\ns_cbranch_vccnz .Lso43\ns_branch .Lro43\n.Lso44:\nds_read_b32 v69, v74\ns_waitcnt lgkmcnt(0)\n"
"v_cmp_gt_u32 vcc, v75, v69\ns_cbranch_vccnz .Lso44\ns_branch .Lro44\n.Lso45:\nds_read_b32 v69, v74\ns_waitcnt lgkmcnt(0)\nv_cmp_gt_u32 vcc, v75, v69\ns_cbranch_vccnz .Lso45\n"
"s_branch .Lro45\n.Lso46:\nds_read_b32 v69, v74\ns_waitcnt lgkmcnt(0)\nv_cmp_gt_u32 vcc, v75, v69\ns_cbranch_vccnz .Lso46\ns_branch .Lro46\n.Lso47:\n"
"ds_read_b32 v69, v74\ns_waitcnt lgkmcnt(0)\nv_cmp_gt_u32 vcc, v75, v69\ns_cbranch_vccnz .Lso47\ns_branch .Lro47\n.Lso48:\nds_read_b32 v69, v74\ns_waitcnt lgkmcnt(0)\n"
"v_cmp_gt_u32 vcc, v75, v69\ns_cbranch_vccnz .Lso48\ns_branch .Lro48\n.Lso49r0:\nds_read_b32 v69, v74\ns_waitcnt lgkmcnt(0)\nv_cmp_gt_u32 vcc, v75, v69\ns_cbranch_vccnz .Lso49r0\n"
"s_branch .Lro49r0\n.Lso50r0:\nds_read_b32 v69, v74\ns_waitcnt lgkmcnt(0)\nv_cmp_gt_u32 vcc, v75, v69\ns_cbranch_vccnz .Lso50r0\ns_branch .Lro50r0\n.Lso51r0:\n"
"ds_read_b32 v69, v74\ns_waitcnt lgkmcnt(0)\nv_cmp_gt_u32 vcc, v75, v69\ns_cbranch_vccnz .Lso51r0\ns_branch .Lro51r0\n.Lso52r0:\nds_read_b32 v69, v74\ns_waitcnt lgkmcnt(0)\n"
"v_cmp_gt_u32 vcc, v75, v69\ns_cbranch_vccnz .Lso52r0\ns_branch .Lro52r0\n.Lso53r0:\nds_read_b32 v69, v74\ns_waitcnt lgkmcnt(0)\nv_cmp_gt_u32 vcc, v75, v69\ns_cbranch_vccnz .Lso53r0\n"
"s_branch .Lro53r0\n.Lso54r0:\nds_read_b32 v69, v74\ns_waitcnt lgkmcnt(0)\nv_cmp_gt_u32 vcc, v75, v69\ns_cbranch_vccnz .Lso54r0\ns_branch .Lro54r0\n.Lso55r0:\n"
"ds_read_b32 v69, v74\ns_waitcnt lgkmcnt(0)\nv_cmp_gt_u32 vcc, v75, v69\ns_cbranch_vccnz .Lso55r0\ns_branch .Lro55r0\n.Lso56r0:\nds_read_b32 v69, v74\ns_waitcnt lgkmcnt(0)\n"
"v_cmp_gt_u32 vcc, v75, v69\ns_cbranch_vccnz .Lso56r0\ns_branch .Lro56r0\n.Lso57r0:\nds_read_b32 v69, v74\ns_waitcnt lgkmcnt(0)\nv_cmp_gt_u32 vcc, v75, v69\ns_cbranch_vccnz .Lso57r0\n"
"s_branch .Lro57r0\n.Lso58r0:\nds_read_b32 v69, v74\ns_waitcnt lgkmcnt(0)\nv_cmp_gt_u32 vcc, v75, v69\ns_cbranch_vccnz .Lso58r0\ns_branch .Lro58r0\n.Lso59r0:\n"
"ds_read_b32 v69, v74\ns_waitcnt lgkmcnt(0)\nv_cmp_gt_u32 vcc, v75, v69\ns_cbranch_vccnz .Lso59r0\ns_branch .Lro59r0\n.Lso60r0:\nds_read_b32 v69, v74\ns_waitcnt lgkmcnt(0)\n"
"v_cmp_gt_u32 vcc, v75, v69\ns_cbranch_vccnz .Lso60r0\ns_branch .Lro60r0\n.Lso61r0:\nds_read_b32 v69, v74\ns_waitcnt lgkmcnt(0)\nv_cmp_gt_u32 vcc, v75, v69\ns_cbranch_vccnz .Lso61r0\n"
"s_branch .Lro61r0\n.Lso62r0:\nds_read_b32 v69, v74\ns_waitcnt lgkmcnt(0)\nv_cmp_gt_u32 vcc, v75, v69\ns_cbranch_vccnz .Lso62r0\ns_branch .Lro62r0\n.Lso63r0:\n"
"ds_read_b32 v69, v74\ns_waitcnt lgkmcnt(0)\nv_cmp_gt_u32 vcc, v75, v69\ns_cbranch_vccnz .Lso63r0\ns_branch .Lro63r0\n.Lso49r1:\nds_read_b32 v69, v74\ns_waitcnt lgkmcnt(0)\n"
"v_cmp_gt_u32 vcc, v75, v69\ns_cbranch_vccnz .Lso49r1\ns_branch .Lro49r1\n.Lso50r1:\nds_read_b32 v69, v74\ns_waitcnt lgkmcnt(0)\nv_cmp_gt_u32 vcc, v75, v69\ns_cbranch_vccnz .Lso50r1\n"
"s_branch .Lro50r1\n.Lso51r1:\nds_read_b32 v69, v74\ns_waitcnt lgkmcnt(0)\nv_cmp_gt_u32 vcc, v75, v69\ns_cbranch_vccnz .Lso51r1\ns_branch .Lro51r1\n.Lso52r1:\n"
"ds_read_b32 v69, v74\ns_waitcnt lgkmcnt(0)\nv_cmp_gt_u32 vcc, v75, v69\ns_cbranch_vccnz .Lso52r1\ns_branch .Lro52r1\n.Lso53r1:\nds_read_b32 v69, v74\ns_waitcnt lgkmcnt(0)\n"
"v_cmp_gt_u32 vcc, v75, v69\ns_cbranch_vccnz .Lso53r1\ns_branch .Lro53r1\n.Lso54r1:\nds_read_b32 v69, v74\ns_waitcnt lgkmcnt(0)\nv_cmp_gt_u32 vcc, v75, v69\ns_cbranch_vccnz .Lso54r1\n"
"s_branch .Lro54r1\n.Lso55r1:\nds_read_b32 v69, v74\ns_waitcnt lgkmcnt(0)\nv_cmp_gt_u32 vcc, v75, v69\ns_cbranch_vccnz .Lso55r1\ns_branch .Lro55r1\n.Lso56r1:\n"
"ds_read_b32 v69, v74\ns_waitcnt lgkmcnt(0)\nv_cmp_gt_u32 vcc, v75, v69\ns_cbranch_vccnz .Lso56r1\ns_branch .Lro56r1\n.Lso57r1:\nds_read_b32 v69, v74\ns_waitcnt lgkmcnt(0)\n"
"v_cmp_gt_u32 vcc, v75, v69\ns_cbranch_vccnz .Lso57r1\ns_branch .Lro57r1\n.Lso58r1:\nds_read_b32 v69, v74\ns_waitcnt lgkmcnt(0)\nv_cmp_gt_u32 vcc, v75, v69\ns_cbranch_vccnz .Lso58r1\n"
"s_branch .Lro58r1\n.Lso59r1:\nds_read_b32 v69, v74\ns_waitcnt lgkmcnt(0)\nv_cmp_gt_u32 vcc, v75, v69\ns_cbranch_vccnz .Lso59r1\ns_branch .Lro59r1\n.Lso60r1:\n"
"ds_read_b32 v69, v74\ns_waitcnt lgkmcnt(0)\nv_cmp_gt_u32 vcc, v75, v69\ns_cbranch_vccnz .Lso60r1\ns_branch .Lro60r1\n.Lso61r1:\nds_read_b32 v69, v74\ns_waitcnt lgkmcnt(0)\n"
"v_cmp_gt_u32 vcc, v75, v69\ns_cbranch_vccnz .Lso61r1\ns_branch .Lro61r1\n.Lso62r1:\nds_read_b32 v69, v74\ns_waitcnt lgkmcnt(0)\nv_cmp_gt_u32 vcc, v75, v69\ns_cbranch_vccnz .Lso62r1\n"
"s_branch .Lro62r1\n.Lso63r1:\nds_read_b32 v69, v74\ns_waitcnt lgkmcnt(0)\nv_cmp_gt_u32 vcc, v75, v69\ns_cbranch_vccnz .Lso63r1\ns_branch .Lro63r1\n.Ltail1:\n"
".Lpg11:\ns_cbranch_execz .Ltr10\nQcvaa 8,112,32,32\nv_add_u32 v75, 1, v75\nQmvaa 8,116,36\ns_waitcnt vmcnt(8)\nds_write_b32 v73, v75\nQmvaa 8,120,40\n"
"Qe 0\nQmvaa 8,124,44\nQe 1\nQcvaa 12,128,32,32\nQe 2\nQmvaa 12,132,36\nQe 3\nv_smfmac_f32_16x16x64_bf16 a[188:191], a[204:207], v[16:23], v77\n"
"Qmvaa 12,136,40\ns_waitcnt lgkmcnt(1)\nQk 16,0,1\nQmvaa 12,140,44\nQk 17,2,3\nQmvav 80,144,20\nQe 4\nQmvav 84,148,20\n"
"Qe 5\nQmvav 88,152,20\nQe 6\nQmvav 92,156,20\nQe 7\nQmvav 96,160,20\nQk 18,4,5\nQk 19,6,7\n"
"Qmvav 100,164,20\nQe 8\nQmvav 104,168,20\nQe 9\nQmvav 108,172,20\nQe 10\nQcvaa 0,112,48,36\nQe 11\n"
"Qmvaa 0,116,52\nds_read_b32 v69, v74\nQk 20,8,9\nQmvaa 0,120,56\nQk 21,10,11\nQmvaa 0,124,60\nQe 12\nQcvaa 4,128,48,36\n"
"Qe 13\nQmvaa 4,132,52\nQe 14\nQmvaa 4,136,56\ns_waitcnt lgkmcnt(0)\nv_cmp_gt_u32 vcc, v75, v69\nQmvaa 4,140,60\ns_cbranch_vccnz .Lsa29x\n"
".Lra29x:\nQk 22,12,13\nQmvav 112,144,16\ns_add_u32 m0, s91, 132096\ns_mul_i32 s94, s31, 0\nQmvav 116,148,16\nbuffer_load_dwordx4 v70, s[48:51], s94 offen lds\nQmvav 120,152,16\n"
"Qe 15\nQmvav 124,156,16\nQk 23,14,15\nQmvav 128,160,16\nQe 0\nQmvav 132,164,16\nQe 1\nQmvav 136,168,16\n"
"buffer_load_dwordx4 v71, s[48:51], s94 offen offset:1024 lds\nQmvav 140,172,16\nQe 2\nQcvaa 8,112,64,40\nQe 3\nQmvaa 8,116,68\nQe 4\nQmvaa 8,120,72\n"
"s_mul_i32 s95, s90, 0\nQe 5\nQmvaa 8,124,76\nbuffer_load_dwordx4 v72, s[52:55], s95 offen offset:2048 lds\nQcvaa 12,128,64,40\nQe 6\nQmvaa 12,132,68\nQe 7\n"
"v_smfmac_f32_16x16x64_bf16 a[192:195], a[204:207], v[16:23], v77\nQmvaa 12,136,72\nQk 16,0,1\nQk 17,2,3\nQmvaa 12,140,76\nbuffer_load_dwordx4 v61, s[52:55], s95 offen offset:3072 lds\nQmvav 144,144,20\nQk 18,4,5\n"
"Qk 19,6,7\nQmvav 148,148,20\nQmvav 152,152,20\nQe 8\nQmvav 156,156,20\ns_add_u32 m0, s91, 136192\nQmvav 160,160,20\nbuffer_load_dwordx4 v62, s[52:55], s95 offen lds\n"
"Qmvav 164,164,20\nQe 9\nQmvav 168,168,20\nQe 10\nQmvav 172,172,20\nQe 11\nQcvaa 0,112,80,44\nQk 20,8,9\n"
"Qk 21,10,11\nQmvaa 0,116,84\nbuffer_load_dwordx4 v63, s[52:55], s95 offen offset:1024 lds\nQmvaa 0,120,88\nQe 12\nQmvaa 0,124,92\nQe 13\nQcvaa 4,128,80,44\n"
"Qe 14\nQmvaa 4,132,84\nQe 15\nQmvaa 4,136,88\nQmvaa 4,140,92\nQk 22,12,13\nQk 23,14,15\nQmvav 176,144,16\n"
"Qmvav 180,148,16\nQmvav 184,152,16\nQmvav 188,156,16\nQmvav 192,160,16\nQe 0\nQmvav 196,164,16\nQe 1\nQmvav 200,168,16\n"
"Qmvav 204,172,16\nQe 2\nQcvaa 8,112,96,48\nQda 112,53,0\nQmvaa 8,116,100\nQda 116,53,512\nQmvaa 8,120,104\nQda 120,53,1024\n"
"Qmvaa 8,124,108\nQda 124,53,1536\nQcvaa 12,128,96,48\nQda 128,53,2048\nQmvaa 12,132,100\nQda 132,53,2560\n.Lpg12:\ns_cbranch_execz .Ltr6\n"
"v_smfmac_f32_16x16x64_bf16 a[196:199], a[204:207], v[16:23], v77\nQmvaa 12,136,104\nQda 136,53,3072\nQk 16,0,1\nQmvaa 12,140,108\nQda 140,53,3584\nQmvav 208,144,20\nQmvav 212,148,20\n"
"Qe 3\nQmvav 216,152,20\nQk 17,2,3\nQmvav 220,156,20\nQe 4\nQmvav 224,160,20\nQe 5\nQmvav 228,164,20\n"
"Qe 6\nQmvav 232,168,20\nQe 7\nQmvav 236,172,20\ns_waitcnt lgkmcnt(0)\nQk 18,4,5\nQcvaa 0,112,0,24\nQk 19,6,7\n"
"Qmvaa 0,116,4\nQe 8\nQmvaa 0,120,8\nQe 9\nQmvaa 0,124,12\nQe 10\nQcvaa 4,128,0,24\nQe 11\n"
"Qmvaa 4,132,4\nQk 20,8,9\nQk 21,10,11\nQmvaa 4,136,8\nQe 12\nQmvaa 4,140,12\nQe 13\nQmvav 240,144,16\n"
"Qe 14\nQmvav 244,148,16\nQmvav 248,152,16\nQmvav 252,156,16\nQk 22,12,13\nQmaav 208,160,16\nQmaav 212,164,16\nQmaav 216,168,16\n"
"Qe 15\nQmaav 220,172,16\nQk 23,14,15\nQcvaa 8,112,16,28\nQe 0\nQmvaa 8,116,20\nQe 1\nQmvaa 8,120,24\n"
"Qe 2\nQmvaa 8,124,28\nQe 3\nQcvaa 12,128,16,28\nQe 4\nQmvaa 12,132,20\nQe 5\nv_smfmac_f32_16x16x64_bf16 a[200:203], a[204:207], v[16:23], v77\n"
"Qmvaa 12,136,24\nQk 16,0,1\nQk 17,2,3\nQmvaa 12,140,28\nQe 6\nQmaav 224,144,20\nQta 144,55,0\nQta 146,55,2048\n"
"Qmaav 228,148,20\nQta 148,55,256\nQta 150,55,2304\nQmaav 232,152,20\nQta 152,55,512\nQta 154,55,2560\nQmaav 236,156,20\nQta 156,55,768\n"
"Qta 158,55,2816\nQmaav 240,160,20\nQta 160,55,1024\nQta 162,55,3072\nQmaav 244,164,20\nQta 164,55,1280\nQta 166,55,3328\nQmaav 248,168,20\n"
"Qta 168,55,1536\nQta 170,55,3584\nQmaav 252,172,20\nQta 172,55,1792\nQta 174,55,3840\nQcvaa 0,112,32,32\nv_add_u32 v75, 1, v75\nQk 18,4,5\n"
"Qmvaa 0,116,36\ns_waitcnt vmcnt(6)\nds_write_b32 v73, v75\nQmvaa 0,120,40\nQe 7\nQmvaa 0,124,44\nQk 19,6,7\nQcvaa 4,128,32,32\n"
"Qe 8\nQmvaa 4,132,36\nQe 9\nQmvaa 4,136,40\nQe 10\nQmvaa 4,140,44\ns_waitcnt lgkmcnt(1)\nQk 20,8,9\n"
"Qmvav 80,144,16\nQe 11\nQmvav 84,148,16\nQk 21,10,11\nQmvav 88,152,16\nQe 12\nQmvav 92,156,16\nQe 13\n"
"Qmvav 96,160,16\nQe 14\nQmvav 100,164,16\nQe 15\nQmvav 104,168,16\nQk 22,12,13\nQk 23,14,15\nQmvav 108,172,16\n"
"Qe 0\nQcvaa 8,112,48,36\nQe 1\nQmvaa 8,116,52\nQe 2\nQmvaa 8,120,56\nds_read_b32 v69, v74\nQmvaa 8,124,60\n"
"Qe 3\nQcvaa 12,128,48,36\nQe 4\nQmvaa 12,132,52\nQe 5\nv_smfmac_f32_16x16x64_bf16 a[176:179], a[204:207], v[16:23], v77\nQmvaa 12,136,56\ns_waitcnt lgkmcnt(0)\n"
"v_cmp_gt_u32 vcc, v75, v69\nQmvaa 12,140,60\ns_cbranch_vccnz .Lsa30x\n.Lra30x:\nQk 16,0,1\nQmvav 112,144,20\ns_add_u32 m0, s91, 0\ns_mul_i32 s94, s31, 1\n"
"Qk 17,2,3\nQmvav 116,148,20\nbuffer_load_dwordx4 v70, s[48:51], s94 offen lds\nQmvav 120,152,20\nQe 6\nQmvav 124,156,20\nQe 7\nQmvav 128,160,20\n"
"Qk 18,4,5\nQk 19,6,7\nQmvav 132,164,20\nQe 8\nQmvav 136,168,20\nbuffer_load_dwordx4 v71, s[48:51], s94 offen offset:1024 lds\nQmvav 140,172,20\nQe 9\n"
"Qcvaa 0,112,64,40\nQe 10\nQmvaa 0,116,68\nQe 11\nQmvaa 0,120,72\ns_mul_i32 s95, s90, 1\nQk 20,8,9\nQk 21,10,11\n"
"Qmvaa 0,124,76\nbuffer_load_dwordx4 v72, s[52:55], s95 offen offset:2048 lds\nQcvaa 4,128,64,40\nQe 12\nQmvaa 4,132,68\nQe 13\nQmvaa 4,136,72\nQe 14\n"
"Qmvaa 4,140,76\nQe 15\nQmvav 144,144,16\nbuffer_load_dwordx4 v61, s[52:55], s95 offen offset:3072 lds\nQmvav 148,148,16\nQk 22,12,13\nQk 23,14,15\nQmvav 152,152,16\n"
"Qmvav 156,156,16\nQe 0\nQmvav 160,160,16\ns_add_u32 m0, s91, 4096\nQmvav 164,164,16\nbuffer_load_dwordx4 v62, s[52:55], s95 offen lds\nQmvav 168,168,16\nQe 1\n"
"Qmvav 172,172,16\nQe 2\nQcvaa 8,112,80,44\nQe 3\nQmvaa 8,116,84\nQe 4\nQmvaa 8,120,88\nbuffer_load_dwordx4 v63, s[52:55], s95 offen offset:1024 lds\n"
"Qmvaa 8,124,92\nQe 5\nQcvaa 12,128,80,44\nQe 6\nQmvaa 12,132,84\nQe 7\nv_smfmac_f32_16x16x64_bf16 a[180:183], a[204:207], v[16:23], v77\ns_add_u32 m0, s91, 24576\n"
"s_mul_i32 s94, s31, 2\nQmvaa 12,136,88\nbuffer_load_dwordx4 v70, s[48:51], s94 offen lds\nQmvaa 12,140,92\nQk 16,0,1\nQk 17,2,3\nQmvav 176,144,20\nQk 18,4,5\n"
"Qk 19,6,7\nQmvav 180,148,20\nQmvav 184,152,20\nQe 8\nQmvav 188,156,20\nbuffer_load_dwordx4 v71, s[48:51], s94 offen offset:1024 lds\nQmvav 192,160,20\nQe 9\n"
"Qmvav 196,164,20\nQe 10\nQmvav 200,168,20\nQmvav 204,172,20\ns_mul_i32 s95, s90, 2\nQe 11\nQcvaa 0,112,96,48\nQda 112,53,33024\n"
"buffer_load_dwordx4 v72, s[52:55], s95 offen offset:2048 lds\nQk 20,8,9\nQmvaa 0,116,100\nQda 116,53,33536\nQk 21,10,11\nQmvaa 0,120,104\nQda 120,53,34048\nQmvaa 0,124,108\n"
"Qda 124,53,34560\nQcvaa 4,128,96,48\nQda 128,53,35072\nQmvaa 4,132,100\nQda 132,53,35584\nbuffer_load_dwordx4 v61, s[52:55], s95 offen offset:3072 lds\nQmvaa 4,136,104\nQda 136,53,36096\n"
"Qmvaa 4,140,108\nQda 140,53,36608\nQmvav 208,144,16\nQe 12\nQmvav 212,148,16\ns_add_u32 m0, s91, 28672\nQmvav 216,152,16\nbuffer_load_dwordx4 v62, s[52:55], s95 offen lds\n"
"Qe 13\nQmvav 220,156,16\nQe 14\nQmvav 224,160,16\nQe 15\nQmvav 228,164,16\nQk 22,12,13\nQk 23,14,15\n"
"Qmvav 232,168,16\nQe 0\nQmvav 236,172,16\nbuffer_load_dwordx4 v63, s[52:55], s95 offen offset:1024 lds\ns_waitcnt lgkmcnt(0)\nQcvaa 8,112,0,24\nQe 1\nQmvaa 8,116,4\n"
"Qe 2\nQmvaa 8,120,8\nQe 3\nQmvaa 8,124,12\ns_add_u32 m0, s91, 49152\ns_mul_i32 s94, s31, 3\nQe 4\nQcvaa 12,128,0,24\n"
"buffer_load_dwordx4 v70, s[48:51], s94 offen lds\nQe 5\nQmvaa 12,132,4\nQe 6\nv_smfmac_f32_16x16x64_bf16 a[184:187], a[204:207], v[16:23], v77\nQmvaa 12,136,8\nQk 16,0,1\nQk 17,2,3\n"
"Qmvaa 12,140,12\nQe 7\nQmvav 240,144,20\nbuffer_load_dwordx4 v71, s[48:51], s94 offen offset:1024 lds\nQmvav 244,148,20\nQmvav 248,152,20\nQk 18,4,5\nQmvav 252,156,20\n"
"Qk 19,6,7\nQmaav 208,160,20\ns_mul_i32 s95, s90, 3\nQmaav 212,164,20\nbuffer_load_dwordx4 v72, s[52:55], s95 offen offset:2048 lds\nQe 8\nQmaav 216,168,20\nQe 9\n"
"Qmaav 220,172,20\nQe 10\nQcvaa 0,112,16,28\nQe 11\nQmvaa 0,116,20\nQk 20,8,9\nQk 21,10,11\nQmvaa 0,120,24\n"
"buffer_load_dwordx4 v61, s[52:55], s95 offen offset:3072 lds\nQe 12\nQmvaa 0,124,28\nQe 13\nQcvaa 4,128,16,28\nQe 14\nQmvaa 4,132,20\nQe 15\n"
"Qmvaa 4,136,24\ns_add_u32 m0, s91, 53248\nQk 22,12,13\nQk 23,14,15\nQmvaa 4,140,28\nbuffer_load_dwordx4 v62, s[52:55], s95 offen lds\nQmaav 224,144,16\nQta 144,55,33024\n"
"Qta 146,55,35072\nQmaav 228,148,16\nQta 148,55,33280\nQta 150,55,35328\nQmaav 232,152,16\nQta 152,55,33536\nQta 154,55,35584\nQmaav 236,156,16\n"
"Qta 156,55,33792\nQta 158,55,35840\nQmaav 240,160,16\nQta 160,55,34048\nQta 162,55,36096\nbuffer_load_dwordx4 v63, s[52:55], s95 offen offset:1024 lds\nQmaav 244,164,16\nQta 164,55,34304\n"
"Qta 166,55,36352\nQmaav 248,168,16\nQta 168,55,34560\nQta 170,55,36608\nQmaav 252,172,16\nQta 172,55,34816\nQta 174,55,36864\nQcvaa 8,112,32,32\n"
"v_add_u32 v75, 1, v75\nQmvaa 8,116,36\nds_write_b32 v73, v75\nQmvaa 8,120,40\nQe 0\nQmvaa 8,124,44\nQe 1\nQcvaa 12,128,32,32\n"
"Qe 2\nQmvaa 12,132,36\nQe 3\nv_smfmac_f32_16x16x64_bf16 a[188:191], a[204:207], v[16:23], v77\nQmvaa 12,136,40\ns_waitcnt lgkmcnt(1)\nQk 16,0,1\nQmvaa 12,140,44\n"
"Qk 17,2,3\nQmvav 80,144,20\nQe 4\nQmvav 84,148,20\nQe 5\nQmvav 88,152,20\nQe 6\nQmvav 92,156,20\n"
"Qe 7\nQmvav 96,160,20\nQk 18,4,5\nQk 19,6,7\nQmvav 100,164,20\nQe 8\nQmvav 104,168,20\nQe 9\n"
"Qmvav 108,172,20\nQe 10\nQcvaa 0,112,48,36\nQe 11\nQmvaa 0,116,52\nds_read_b32 v69, v74\nQk 20,8,9\nQmvaa 0,120,56\n"
"Qk 21,10,11\nQmvaa 0,124,60\nQe 12\nQcvaa 4,128,48,36\nQe 13\nQmvaa 4,132,52\nQe 14\nQmvaa 4,136,56\n"
"s_waitcnt lgkmcnt(0)\nv_cmp_gt_u32 vcc, v75, v69\nQmvaa 4,140,60\ns_cbranch_vccnz .Lsa31x\n.Lra31x:\nQk 22,12,13\nQmvav 112,144,16\ns_add_u32 m0, s91, 73728\n"
"s_mul_i32 s94, s31, 4\nQmvav 116,148,16\nbuffer_load_dwordx4 v70, s[48:51], s94 offen lds\nQmvav 120,152,16\nQe 15\nQmvav 124,156,16\nQk 23,14,15\nQmvav 128,160,16\n"
"Qe 0\nQmvav 132,164,16\nQe 1\nQmvav 136,168,16\nbuffer_load_dwordx4 v71, s[48:51], s94 offen offset:1024 lds\nQmvav 140,172,16\nQe 2\nQcvaa 8,112,64,40\n"
"Qe 3\nQmvaa 8,116,68\nQe 4\nQmvaa 8,120,72\ns_mul_i32 s95, s90, 4\nQe 5\nQmvaa 8,124,76\nbuffer_load_dwordx4 v72, s[52:55], s95 offen offset:2048 lds\n"
"Qcvaa 12,128,64,40\nQe 6\nQmvaa 12,132,68\nQe 7\nv_smfmac_f32_16x16x64_bf16 a[192:195], a[204:207], v[16:23], v77\nQmvaa 12,136,72\nQk 16,0,1\nQk 17,2,3\n"
"Qmvaa 12,140,76\nbuffer_load_dwordx4 v61, s[52:55], s95 offen offset:3072 lds\nQmvav 144,144,20\nQk 18,4,5\nQk 19,6,7\nQmvav 148,148,20\nQmvav 152,152,20\nQe 8\n"
"Qmvav 156,156,20\ns_add_u32 m0, s91, 77824\nQmvav 160,160,20\nbuffer_load_dwordx4 v62, s[52:55], s95 offen lds\nQmvav 164,164,20\nQe 9\nQmvav 168,168,20\nQe 10\n"
"Qmvav 172,172,20\nQe 11\nQcvaa 0,112,80,44\nQk 20,8,9\nQk 21,10,11\nQmvaa 0,116,84\nbuffer_load_dwordx4 v63, s[52:55], s95 offen offset:1024 lds\nQmvaa 0,120,88\n"
"Qe 12\nQmvaa 0,124,92\nQe 13\nQcvaa 4,128,80,44\nQe 14\nQmvaa 4,132,84\ns_add_u32 m0, s91, 98304\ns_mul_i32 s94, s31, 5\n"
"Qe 15\nQmvaa 4,136,88\nbuffer_load_dwordx4 v70, s[48:51], s94 offen lds\nQmvaa 4,140,92\nQk 22,12,13\nQk 23,14,15\nQmvav 176,144,16\nQmvav 180,148,16\n"
"Qmvav 184,152,16\nQmvav 188,156,16\nbuffer_load_dwordx4 v71, s[48:51], s94 offen offset:1024 lds\nQmvav 192,160,16\nQe 0\nQmvav 196,164,16\nQe 1\nQmvav 200,168,16\n"
"Qmvav 204,172,16\ns_mul_i32 s95, s90, 5\nQe 2\nQcvaa 8,112,96,48\nbuffer_load_dwordx4 v72, s[52:55], s95 offen offset:2048 lds\nQmvaa 8,116,100\nQmvaa 8,120,104\nQmvaa 8,124,108\n"
"Qcvaa 12,128,96,48\nQmvaa 12,132,100\nbuffer_load_dwordx4 v61, s[52:55], s95 offen offset:3072 lds\nv_smfmac_f32_16x16x64_bf16 a[196:199], a[204:207], v[16:23], v77\nQmvaa 12,136,104\nQk 16,0,1\nQmvaa 12,140,108\nQmvav 208,144,20\n"
"s_add_u32 m0, s91, 102400\nQmvav 212,148,20\nbuffer_load_dwordx4 v62, s[52:55], s95 offen lds\nQe 3\nQmvav 216,152,20\nQk 17,2,3\nQmvav 220,156,20\nQe 4\n"
"Qmvav 224,160,20\nQe 5\nQmvav 228,164,20\nQe 6\nQmvav 232,168,20\nbuffer_load_dwordx4 v63, s[52:55], s95 offen offset:1024 lds\nQe 7\nQmvav 236,172,20\n"
"Qk 18,4,5\nQk 19,6,7\nQe 8\nQe 9\nQe 10\nQe 11\nQk 20,8,9\nQk 21,10,11\n"
"Qe 12\nQe 13\nQmvav 240,144,16\nQe 14\nQmvav 244,148,16\nQmvav 248,152,16\nQmvav 252,156,16\nQk 22,12,13\n"
"Qmaav 208,160,16\nQmaav 212,164,16\nQmaav 216,168,16\nQe 15\nQmaav 220,172,16\nQk 23,14,15\ns_nop 1\nv_smfmac_f32_16x16x64_bf16 a[200:203], a[204:207], v[16:23], v77\n"
"Qmaav 224,144,20\nQmaav 228,148,20\nQmaav 232,152,20\nQmaav 236,156,20\nQmaav 240,160,20\nQmaav 244,164,20\nQmaav 248,168,20\nQmaav 252,172,20\n"
"v_accvgpr_read_b32 v24, a176\nv_accvgpr_read_b32 v25, a177\nv_accvgpr_read_b32 v30, a178\nv_accvgpr_read_b32 v31, a179\nv_accvgpr_read_b32 v32, a180\nv_accvgpr_read_b32 v33, a181\nv_accvgpr_read_b32 v38, a182\nv_accvgpr_read_b32 v39, a183\n"
"v_accvgpr_read_b32 v40, a184\nv_accvgpr_read_b32 v41, a185\nv_accvgpr_read_b32 v46, a186\nv_accvgpr_read_b32 v47, a187\nv_accvgpr_read_b32 v48, a188\nv_accvgpr_read_b32 v49, a189\nv_accvgpr_read_b32 v26, a190\nv_accvgpr_read_b32 v27, a191\n"
"v_accvgpr_read_b32 v28, a192\nv_accvgpr_read_b32 v29, a193\nv_accvgpr_read_b32 v34, a194\nv_accvgpr_read_b32 v35, a195\nv_accvgpr_read_b32 v36, a196\nv_accvgpr_read_b32 v37, a197\nv_accvgpr_read_b32 v42, a198\nv_accvgpr_read_b32 v43, a199\n"
"v_accvgpr_read_b32 v44, a200\nv_accvgpr_read_b32 v45, a201\nv_accvgpr_read_b32 v50, a202\nv_accvgpr_read_b32 v51, a203\n.Lpg13:\ns_cbranch_execz .Ltr6\nv_pk_add_f32 v[24:25], v[24:25], v[26:27]\nv_pk_add_f32 v[28:29], v[28:29], v[30:31]\n"
"v_pk_add_f32 v[32:33], v[32:33], v[34:35]\nv_pk_add_f32 v[36:37], v[36:37], v[38:39]\nv_pk_add_f32 v[40:41], v[40:41], v[42:43]\nv_pk_add_f32 v[44:45], v[44:45], v[46:47]\nv_pk_add_f32 v[48:49], v[48:49], v[50:51]\nQa 24,24,25\nQa 28,28,29\nQa 32,32,33\n"
"Qa 36,36,37\nQa 40,40,41\nQa 44,44,45\nQa 48,48,49\nv_cmp_nge_f32 vcc, 0x44800000, v24\ns_mov_b64 s[74:75], vcc\nv_cmp_nge_f32 vcc, 0x44800000, v28\ns_or_b64 s[74:75], s[74:75], vcc\n"
"v_cmp_nge_f32 vcc, 0x44800000, v32\ns_or_b64 s[74:75], s[74:75], vcc\nv_cmp_nge_f32 vcc, 0x44800000, v36\ns_or_b64 s[74:75], s[74:75], vcc\nv_cmp_nge_f32 vcc, 0x44800000, v40\ns_or_b64 s[74:75], s[74:75], vcc\nv_cmp_nge_f32 vcc, 0x44800000, v44\ns_or_b64 s[74:75], s[74:75], vcc\n"
"v_cmp_nge_f32 vcc, 0x44800000, v48\ns_or_b64 s[74:75], s[74:75], vcc\ns_cmp_lg_u64 s[74:75], 0\ns_cbranch_scc1 .Lfb\n.Lfbret1:\nv_rcp_f32 v52, v24\nv_rcp_f32 v53, v28\nv_rcp_f32 v54, v32\n"
"v_rcp_f32 v55, v36\nv_rcp_f32 v56, v40\nv_rcp_f32 v57, v44\nv_rcp_f32 v58, v48\ns_mov_b32 s56, %8\ns_and_b32 s57, %9, 0xffff\ns_mov_b32 s58, 1835008\ns_lshl_b32 s68, %19, 4\n"
"s_cmp_eq_u32 s70, 0\ns_cselect_b32 s68, s68, -1\nv_mov_b32 v77, 0x7ffff000\nv_cmp_eq_u32 vcc, s68, v60\ns_nop 1\nv_cndmask_b32 v77, v77, v76, vcc\ns_mul_i32 s68, s92, 7168\ns_add_u32 s68, s68, %10\n"
"v_add_u32 v48, s68, v60\nv_add_u32 v49, 73728, v48\ns_mul_i32 s68, s93, 1280\ns_add_u32 s68, s68, %10\nv_lshrrev_b32 v66, 9, v60\nv_mul_u32_u24 v66, 2560, v66\nv_bfe_u32 v67, v60, 1, 8\nv_add3_u32 v64, v66, v67, s68\n"
"v_add_u32 v59, 57344, v64\nv_add_u32 v50, 114688, v64\ns_mov_b64 exec, 0\ns_cmp_eq_u32 s70, 0\ns_cbranch_scc1 .Lpg16\ns_cmp_eq_u32 s70, 1\ns_cbranch_scc1 .Lpg17\ns_cmp_eq_u32 s70, 2\n"
"s_cbranch_scc1 .Lpg18\ns_branch .Lpg19\n.Ltr8:\ns_mov_b64 exec, -1\ns_waitcnt vmcnt(24)\ns_barrier\nQtv 0,50,31744\nQtv 2,50,36864\n"
"Qtv 4,50,32000\nQtv 6,50,37120\nQtv 8,50,32256\nQtv 10,50,37376\nQtv 12,50,32512\nQtv 14,50,37632\nQtv 16,50,32768\nQtv 18,50,37888\n"
"Qda 112,49,58368\nQda 116,49,59392\nQda 120,49,60416\nQda 124,49,61440\nQda 128,49,62464\nQda 132,49,63488\nQda 136,49,64512\nv_add_u32 v75, 1, v75\n"
"s_waitcnt vmcnt(18)\nds_write_b32 v73, v75\nv_mul_f32 v80, v52, v80\nv_mul_f32 v81, v52, v81\nv_mul_f32 v82, v52, v82\nv_mul_f32 v83, v52, v83\nv_mul_f32 v84, v52, v84\nv_mul_f32 v85, v52, v85\n"
"v_mul_f32 v86, v52, v86\nv_mul_f32 v87, v52, v87\nQk 80,80,81\nQk 81,82,83\nQk 82,84,85\nQk 83,86,87\nv_mul_f32 v112, v53, v112\nv_mul_f32 v113, v53, v113\n"
"v_mul_f32 v114, v53, v114\nv_mul_f32 v115, v53, v115\nv_mul_f32 v116, v53, v116\nv_mul_f32 v117, v53, v117\nv_mul_f32 v118, v53, v118\nv_mul_f32 v119, v53, v119\nQk 112,112,113\nQk 113,114,115\n"
"Qk 114,116,117\nQk 115,118,119\ns_waitcnt lgkmcnt(5)\nQzava 0,0,112\nQtv 20,64,14336\nv_mul_f32 v144, v54, v144\nQzava 4,4,112\nQtv 22,64,19456\n"
"v_mul_f32 v145, v54, v145\nQzava 8,8,112\nQtv 24,64,14592\nv_mul_f32 v146, v54, v146\nQzava 12,12,112\nv_mul_f32 v147, v54, v147\nv_mul_f32 v148, v54, v148\nQzava 16,16,112\n"
"Qtv 26,64,19712\nv_mul_f32 v149, v54, v149\nQzava 20,0,116\nQtv 28,64,14848\nv_mul_f32 v150, v54, v150\nQzava 24,4,116\nv_mul_f32 v151, v54, v151\nQk 144,144,145\n"
"Qzava 28,8,116\nds_read_b32 v69, v74\nQk 145,146,147\nQzava 32,12,116\nQtv 30,64,19968\nQk 146,148,149\nQzava 36,16,116\nQtv 32,64,15104\n"
"Qk 147,150,151\nQzava 40,0,120\nv_mul_f32 v176, v55, v176\nv_mul_f32 v177, v55, v177\nQzava 44,4,120\ns_waitcnt lgkmcnt(2)\nv_cmp_gt_u32 vcc, v75, v69\nQzava 48,8,120\n"
"s_cbranch_vccnz .Lso0\n.Lro0:\nv_mul_f32 v178, v55, v178\nQzava 52,12,120\nQtv 34,64,20224\ns_add_u32 m0, s91, 132096\ns_mul_i32 s94, s31, 6\nQzava 56,16,120\n"
"buffer_load_dwordx4 v70, s[48:51], s94 offen lds\nQzava 60,0,124\nQtv 36,64,15360\nv_mul_f32 v179, v55, v179\nQzava 64,4,124\nv_mul_f32 v180, v55, v180\nv_mul_f32 v181, v55, v181\nQzava 68,8,124\n"
"Qtv 38,64,20480\nv_mul_f32 v182, v55, v182\nQzava 72,12,124\nbuffer_load_dwordx4 v71, s[48:51], s94 offen offset:1024 lds\nQzava 76,16,124\nQda 140,48,0\nv_mul_f32 v183, v55, v183\nQzava 80,0,128\n"
"Qk 176,176,177\nQk 177,178,179\nQzava 84,4,128\nQda 144,48,1024\ns_mul_i32 s95, s90, 6\nQzava 88,8,128\nbuffer_load_dwordx4 v72, s[52:55], s95 offen offset:2048 lds\nQzava 92,12,128\n"
"Qda 148,48,2048\nQk 178,180,181\nQzava 96,16,128\nQk 179,182,183\nv_mul_f32 v208, v56, v208\nQzava 100,0,132\nQda 152,48,3072\nv_mul_f32 v209, v56, v209\n"
"Qzava 104,4,132\nbuffer_load_dwordx4 v61, s[52:55], s95 offen offset:3072 lds\nQzava 108,8,132\nQda 156,48,4096\nv_mul_f32 v210, v56, v210\nQzava 176,12,132\nv_mul_f32 v211, v56, v211\nv_mul_f32 v212, v56, v212\n"
"Qzava 180,16,132\nQda 160,48,5120\ns_add_u32 m0, s91, 136192\nQzava 184,0,136\nbuffer_load_dwordx4 v62, s[52:55], s95 offen lds\nQzava 188,4,136\nQda 164,48,6144\nv_mul_f32 v213, v56, v213\n"
"Qzava 192,8,136\nv_mul_f32 v214, v56, v214\nv_mul_f32 v215, v56, v215\nQzava 196,12,136\nbuffer_load_dwordx4 v63, s[52:55], s95 offen offset:1024 lds\nv_add_u32 v75, 1, v75\nQzava 200,16,136\ns_waitcnt vmcnt(18)\n"
"ds_write_b32 v73, v75\ns_waitcnt lgkmcnt(5)\nQmava 0,20,140\nQtv 0,64,38912\ns_add_u32 s88, s71, 0\nQmava 4,24,140\nbuffer_store_dwordx4 v[80:83], v60, s[56:59], s88 offen offset:0 sc1\nQmava 8,28,140\n"
"Qtv 2,64,44032\nQk 208,208,209\nQmava 12,32,140\nQtv 4,64,39168\nQk 209,210,211\nQmava 16,36,140\nQk 210,212,213\nQk 211,214,215\n"
"Qmava 20,20,144\nQtv 6,64,44288\nv_mul_f32 v240, v57, v240\nQmava 24,24,144\nQtv 8,64,39424\nv_mul_f32 v241, v57, v241\nQmava 28,28,144\nds_read_b32 v69, v74\n"
"v_mul_f32 v242, v57, v242\nQmava 32,32,144\nv_mul_f32 v243, v57, v243\nv_mul_f32 v244, v57, v244\nQmava 36,36,144\nQtv 10,64,44544\nv_mul_f32 v245, v57, v245\nQmava 40,20,148\n"
"Qtv 12,64,39680\nv_mul_f32 v246, v57, v246\nQmava 44,24,148\ns_waitcnt lgkmcnt(2)\nv_cmp_gt_u32 vcc, v75, v69\nQmava 48,28,148\ns_cbranch_vccnz .Lso1\n.Lro1:\n"
"v_mul_f32 v247, v57, v247\nQmava 52,32,148\nQtv 14,64,44800\ns_add_u32 m0, s91, 0\ns_mul_i32 s94, s31, 7\nQmava 56,36,148\nbuffer_load_dwordx4 v70, s[48:51], s94 offen lds\nQmava 60,20,152\n"
"Qk 240,240,241\nQk 241,242,243\nQmava 64,24,152\nQtv 16,64,39936\nQk 242,244,245\nQmava 68,28,152\nQtv 18,64,45056\nQk 243,246,247\n"
"Qmava 72,32,152\nbuffer_load_dwordx4 v71, s[48:51], s94 offen offset:1024 lds\nQmava 76,36,152\nv_accvgpr_read_b32 v40, a224\nv_accvgpr_read_b32 v41, a225\nQmava 80,20,156\nQda 112,48,24576\nv_accvgpr_read_b32 v42, a226\n"
"Qmava 84,24,156\nQda 116,48,25600\ns_mul_i32 s95, s90, 7\nQmava 88,28,156\nbuffer_load_dwordx4 v72, s[52:55], s95 offen offset:2048 lds\nQmava 92,32,156\nQda 120,48,26624\nv_accvgpr_read_b32 v43, a227\n"
"Qmava 96,36,156\nv_accvgpr_read_b32 v44, a228\nv_accvgpr_read_b32 v45, a229\nQmava 100,20,160\nQda 124,48,27648\nv_accvgpr_read_b32 v46, a230\nQmava 104,24,160\nbuffer_load_dwordx4 v61, s[52:55], s95 offen offset:3072 lds\n"
"Qmava 108,28,160\nQda 128,48,28672\nv_accvgpr_read_b32 v47, a231\nQmava 176,32,160\nv_mul_f32 v40, v58, v40\nv_mul_f32 v41, v58, v41\nQmava 180,36,160\nQda 132,48,29696\n"
"s_add_u32 m0, s91, 4096\nQmava 184,20,164\nbuffer_load_dwordx4 v62, s[52:55], s95 offen lds\nQmava 188,24,164\nQda 136,48,30720\nv_mul_f32 v42, v58, v42\nQmava 192,28,164\nv_mul_f32 v43, v58, v43\n"
"v_mul_f32 v44, v58, v44\nQmava 196,32,164\nbuffer_load_dwordx4 v63, s[52:55], s95 offen offset:1024 lds\nv_add_u32 v75, 1, v75\nQmava 200,36,164\ns_waitcnt vmcnt(19)\nds_write_b32 v73, v75\ns_waitcnt lgkmcnt(5)\n"
"Qmava 0,0,112\nQtv 20,59,6144\nv_mul_f32 v45, v58, v45\nQmava 4,4,112\nbuffer_store_dwordx4 v[112:115], v60, s[56:59], s88 offen offset:1024 sc1\nQmava 8,8,112\nQtv 22,59,11264\nv_mul_f32 v46, v58, v46\n"
"Qmava 12,12,112\nQtv 24,59,6400\nv_mul_f32 v47, v58, v47\nQmava 16,16,112\nQk 84,40,41\nQk 85,42,43\nQmava 20,0,116\nQtv 26,59,11520\n"
"Qk 86,44,45\nQmava 24,4,116\nQtv 28,59,6656\nQk 87,46,47\nQmava 28,8,116\nds_read_b32 v69, v74\nv_mul_f32 v88, v52, v88\nQmava 32,12,116\n"
"v_mul_f32 v89, v52, v89\nv_mul_f32 v90, v52, v90\nQmava 36,16,116\nQtv 30,59,11776\nv_mul_f32 v91, v52, v91\nQmava 40,0,120\nQtv 32,59,6912\nv_mul_f32 v92, v52, v92\n"
"Qmava 44,4,120\ns_waitcnt lgkmcnt(2)\nv_cmp_gt_u32 vcc, v75, v69\nQmava 48,8,120\ns_cbranch_vccnz .Lso2\n.Lro2:\nv_mul_f32 v93, v52, v93\nQmava 52,12,120\n"
"Qtv 34,59,12032\ns_add_u32 m0, s91, 24576\ns_mul_i32 s94, s31, 8\nQmava 56,16,120\nbuffer_load_dwordx4 v70, s[48:51], s94 offen lds\nQmava 60,0,124\nv_mul_f32 v94, v52, v94\nv_mul_f32 v95, v52, v95\n"
"Qmava 64,4,124\nQtv 36,59,7168\nQk 88,88,89\nQmava 68,8,124\nQtv 38,59,12288\nQk 89,90,91\nQmava 72,12,124\nbuffer_load_dwordx4 v71, s[48:51], s94 offen offset:1024 lds\n"
"Qmava 76,16,124\nQk 90,92,93\nQk 91,94,95\nQmava 80,0,128\nQda 140,48,49152\nv_mul_f32 v120, v53, v120\nQmava 84,4,128\nQda 144,48,50176\n"
"s_mul_i32 s95, s90, 8\nQmava 88,8,128\nbuffer_load_dwordx4 v72, s[52:55], s95 offen offset:2048 lds\nQmava 92,12,128\nQda 148,48,51200\nv_mul_f32 v121, v53, v121\nQmava 96,16,128\nv_mul_f32 v122, v53, v122\n"
"v_mul_f32 v123, v53, v123\nQmava 100,0,132\nQda 152,48,52224\nv_mul_f32 v124, v53, v124\nQmava 104,4,132\nbuffer_load_dwordx4 v61, s[52:55], s95 offen offset:3072 lds\nQmava 108,8,132\nQda 156,48,53248\n"
"v_mul_f32 v125, v53, v125\nQmava 176,12,132\nv_mul_f32 v126, v53, v126\nv_mul_f32 v127, v53, v127\nQmava 180,16,132\nQda 160,48,54272\ns_add_u32 m0, s91, 28672\nQmava 184,0,136\n"
"buffer_load_dwordx4 v62, s[52:55], s95 offen lds\nQmava 188,4,136\nQda 164,48,55296\nQk 120,120,121\nQmava 192,8,136\nQk 121,122,123\nQk 122,124,125\nQmava 196,12,136\n"
"buffer_load_dwordx4 v63, s[52:55], s95 offen offset:1024 lds\nv_add_u32 v75, 1, v75\nQmava 200,16,136\ns_waitcnt vmcnt(20)\nds_write_b32 v73, v75\ns_waitcnt lgkmcnt(5)\nQmava 0,20,140\nQtv 0,59,30720\n"
"Qk 123,126,127\nQmava 4,24,140\nbuffer_store_dwordx4 v[144:147], v60, s[56:59], s88 offen offset:2048 sc1\nQmava 8,28,140\nQtv 2,59,35840\nv_mul_f32 v152, v54, v152\nQmava 12,32,140\nQtv 4,59,30976\n"
"v_mul_f32 v153, v54, v153\nQmava 16,36,140\nv_mul_f32 v154, v54, v154\nv_mul_f32 v155, v54, v155\nQmava 20,20,144\nQtv 6,59,36096\nv_mul_f32 v156, v54, v156\nQmava 24,24,144\n"
"Qtv 8,59,31232\nv_mul_f32 v157, v54, v157\nQmava 28,28,144\nds_read_b32 v69, v74\nv_mul_f32 v158, v54, v158\nQmava 32,32,144\nv_mul_f32 v159, v54, v159\nQk 152,152,153\n"
"Qmava 36,36,144\nQtv 10,59,36352\nQk 153,154,155\nQmava 40,20,148\nQtv 12,59,31488\nQk 154,156,157\nQmava 44,24,148\ns_waitcnt lgkmcnt(2)\n"
"v_cmp_gt_u32 vcc, v75, v69\nQmava 48,28,148\ns_cbranch_vccnz .Lso3\n.Lro3:\nQk 155,158,159\nQmava 52,32,148\nQtv 14,59,36608\ns_add_u32 m0, s91, 49152\n"
"s_mul_i32 s94, s31, 9\nQmava 56,36,148\nbuffer_load_dwordx4 v70, s[48:51], s94 offen lds\nQmava 60,20,152\nv_mul_f32 v184, v55, v184\nv_mul_f32 v185, v55, v185\nQmava 64,24,152\nQtv 16,59,31744\n"
"v_mul_f32 v186, v55, v186\nQmava 68,28,152\nQtv 18,59,36864\nv_mul_f32 v187, v55, v187\nQmava 72,32,152\nbuffer_load_dwordx4 v71, s[48:51], s94 offen offset:1024 lds\nQmava 76,36,152\nv_mul_f32 v188, v55, v188\n"
"v_mul_f32 v189, v55, v189\nQmava 80,20,156\nQda 112,49,0\nv_mul_f32 v190, v55, v190\nQmava 84,24,156\nQda 116,49,1024\ns_mul_i32 s95, s90, 9\nQmava 88,28,156\n"
"buffer_load_dwordx4 v72, s[52:55], s95 offen offset:2048 lds\nQmava 92,32,156\nQda 120,49,2048\nv_mul_f32 v191, v55, v191\nQmava 96,36,156\nQk 184,184,185\nQk 185,186,187\nQmava 100,20,160\n"
"Qda 124,49,3072\nQk 186,188,189\nQmava 104,24,160\nbuffer_load_dwordx4 v61, s[52:55], s95 offen offset:3072 lds\nQmava 108,28,160\nQda 128,49,4096\nQk 187,190,191\nQmava 176,32,160\n"
"v_mul_f32 v216, v56, v216\nv_mul_f32 v217, v56, v217\nQmava 180,36,160\nQda 132,49,5120\ns_add_u32 m0, s91, 53248\nQmava 184,20,164\nbuffer_load_dwordx4 v62, s[52:55], s95 offen lds\nQmava 188,24,164\n"
"Qda 136,49,6144\nv_mul_f32 v218, v56, v218\nQmava 192,28,164\nv_mul_f32 v219, v56, v219\nv_mul_f32 v220, v56, v220\nQmava 196,32,164\nbuffer_load_dwordx4 v63, s[52:55], s95 offen offset:1024 lds\nv_add_u32 v75, 1, v75\n"
"Qmava 200,36,164\ns_waitcnt vmcnt(21)\nds_write_b32 v73, v75\ns_waitcnt lgkmcnt(5)\nQmava 0,0,112\nQtv 20,59,55296\nv_mul_f32 v221, v56, v221\nQmava 4,4,112\n"
"buffer_store_dwordx4 v[176:179], v60, s[56:59], s88 offen offset:3072 sc1\nQmava 8,8,112\nQtv 22,59,60416\nv_mul_f32 v222, v56, v222\nQmava 12,12,112\nQtv 24,59,55552\nv_mul_f32 v223, v56, v223\nQmava 16,16,112\n"
"Qk 216,216,217\nQk 217,218,219\nQmava 20,0,116\nQtv 26,59,60672\nQk 218,220,221\nQmava 24,4,116\nQtv 28,59,55808\nQk 219,222,223\n"
"Qmava 28,8,116\nds_read_b32 v69, v74\nv_mul_f32 v248, v57, v248\nQmava 32,12,116\nv_mul_f32 v249, v57, v249\nv_mul_f32 v250, v57, v250\nQmava 36,16,116\nQtv 30,59,60928\n"
"v_mul_f32 v251, v57, v251\nQmava 40,0,120\nQtv 32,59,56064\nv_mul_f32 v252, v57, v252\nQmava 44,4,120\ns_waitcnt lgkmcnt(2)\nv_cmp_gt_u32 vcc, v75, v69\nQmava 48,8,120\n"
"s_cbranch_vccnz .Lso4\n.Lro4:\nv_mul_f32 v253, v57, v253\nQmava 52,12,120\nQtv 34,59,61184\ns_add_u32 m0, s91, 73728\ns_mul_i32 s94, s31, 10\nQmava 56,16,120\n"
"buffer_load_dwordx4 v70, s[48:51], s94 offen lds\nQmava 60,0,124\nv_mul_f32 v254, v57, v254\nv_mul_f32 v255, v57, v255\nQmava 64,4,124\nQtv 36,59,56320\nQk 248,248,249\nQmava 68,8,124\n"
"Qtv 38,59,61440\nQk 249,250,251\nQmava 72,12,124\nbuffer_load_dwordx4 v71, s[48:51], s94 offen offset:1024 lds\nQmava 76,16,124\nQk 250,252,253\nQk 251,254,255\nQmava 80,0,128\n"
"Qda 140,49,24576\nv_accvgpr_read_b32 v40, a232\nQmava 84,4,128\nQda 144,49,25600\ns_mul_i32 s95, s90, 10\nQmava 88,8,128\nbuffer_load_dwordx4 v72, s[52:55], s95 offen offset:2048 lds\nQmava 92,12,128\n"
"Qda 148,49,26624\nv_accvgpr_read_b32 v41, a233\nQmava 96,16,128\nv_accvgpr_read_b32 v42, a234\nv_accvgpr_read_b32 v43, a235\nQmava 100,0,132\nQda 152,49,27648\nv_accvgpr_read_b32 v44, a236\n"
"Qmava 104,4,132\nbuffer_load_dwordx4 v61, s[52:55], s95 offen offset:3072 lds\nQmava 108,8,132\nQda 156,49,28672\nv_accvgpr_read_b32 v45, a237\nQmava 176,12,132\nv_accvgpr_read_b32 v46, a238\nv_accvgpr_read_b32 v47, a239\n"
"Qmava 180,16,132\nQda 160,49,29696\ns_add_u32 m0, s91, 77824\nQmava 184,0,136\n.Lpg14:\ns_cbranch_execz .Ltr6\nbuffer_load_dwordx4 v62, s[52:55], s95 offen lds\nQmava 188,4,136\n"
"Qda 164,49,30720\nv_mul_f32 v40, v58, v40\nQmava 192,8,136\nv_mul_f32 v41, v58, v41\nv_mul_f32 v42, v58, v42\nQmava 196,12,136\nbuffer_load_dwordx4 v63, s[52:55], s95 offen offset:1024 lds\nv_add_u32 v75, 1, v75\n"
"Qmava 200,16,136\ns_waitcnt vmcnt(21)\nds_write_b32 v73, v75\ns_waitcnt lgkmcnt(5)\nQmava 0,20,140\nQtv 0,50,31744\ns_add_u32 s88, s71, 4096\nQmava 4,24,140\n"
"buffer_store_dwordx4 v[208:211], v60, s[56:59], s88 offen offset:0 sc1\nQmava 8,28,140\nQtv 2,50,36864\nv_mul_f32 v43, v58, v43\nQmava 12,32,140\nQtv 4,50,32000\nv_mul_f32 v44, v58, v44\nQmava 16,36,140\n"
"v_mul_f32 v45, v58, v45\nv_mul_f32 v46, v58, v46\nQmava 20,20,144\nQtv 6,50,37120\nv_mul_f32 v47, v58, v47\nQmava 24,24,144\nQtv 8,50,32256\nQk 116,40,41\n"
"Qmava 28,28,144\nds_read_b32 v69, v74\nQk 117,42,43\nQmava 32,32,144\nQk 118,44,45\nQk 119,46,47\nQmava 36,36,144\nQtv 10,50,37376\n"
"v_mul_f32 v96, v52, v96\nQmava 40,20,148\nQtv 12,50,32512\nv_mul_f32 v97, v52, v97\nQmava 44,24,148\ns_waitcnt lgkmcnt(2)\nv_cmp_gt_u32 vcc, v75, v69\nQmava 48,28,148\n"
"s_cbranch_vccnz .Lso5\n.Lro5:\nv_mul_f32 v98, v52, v98\nQmava 52,32,148\nQtv 14,50,37632\ns_add_u32 m0, s91, 98304\ns_mul_i32 s94, s31, 11\nQmava 56,36,148\n"
"buffer_load_dwordx4 v70, s[48:51], s94 offen lds\nQmava 60,20,152\nv_mul_f32 v99, v52, v99\nv_mul_f32 v100, v52, v100\nQmava 64,24,152\nQtv 16,50,32768\nv_mul_f32 v101, v52, v101\nQmava 68,28,152\n"
"Qtv 18,50,37888\nv_mul_f32 v102, v52, v102\nQmava 72,32,152\nbuffer_load_dwordx4 v71, s[48:51], s94 offen offset:1024 lds\nQmava 76,36,152\nv_mul_f32 v103, v52, v103\nQk 96,96,97\nQmava 80,20,156\n"
"Qda 112,49,58368\nQk 97,98,99\nQmava 84,24,156\nQda 116,49,59392\ns_mul_i32 s95, s90, 11\nQmava 88,28,156\nbuffer_load_dwordx4 v72, s[52:55], s95 offen offset:2048 lds\nQmava 92,32,156\n"
"Qda 120,49,60416\nQk 98,100,101\nQmava 96,36,156\nQk 99,102,103\nv_mul_f32 v128, v53, v128\nQmava 100,20,160\nQda 124,49,61440\nv_mul_f32 v129, v53, v129\n"
"Qmava 104,24,160\nbuffer_load_dwordx4 v61, s[52:55], s95 offen offset:3072 lds\nQmava 108,28,160\nQda 128,49,62464\nv_mul_f32 v130, v53, v130\nQmava 176,32,160\nv_mul_f32 v131, v53, v131\nv_mul_f32 v132, v53, v132\n"
"Qmava 180,36,160\nQda 132,49,63488\ns_add_u32 m0, s91, 102400\nQmava 184,20,164\nbuffer_load_dwordx4 v62, s[52:55], s95 offen lds\nQmava 188,24,164\nQda 136,49,64512\nv_mul_f32 v133, v53, v133\n"
"Qmava 192,28,164\nv_mul_f32 v134, v53, v134\nv_mul_f32 v135, v53, v135\nQmava 196,32,164\nbuffer_load_dwordx4 v63, s[52:55], s95 offen offset:1024 lds\nv_add_u32 v75, 1, v75\nQmava 200,36,164\ns_waitcnt vmcnt(21)\n"
"ds_write_b32 v73, v75\ns_waitcnt lgkmcnt(5)\nQmava 0,0,112\nQtv 20,64,14336\nQk 128,128,129\nQmava 4,4,112\nbuffer_store_dwordx4 v[240:243], v60, s[56:59], s88 offen offset:1024 sc1\nQmava 8,8,112\n"
"Qtv 22,64,19456\nQk 129,130,131\nQmava 12,12,112\nQtv 24,64,14592\nQk 130,132,133\nQmava 16,16,112\nQk 131,134,135\nv_mul_f32 v160, v54, v160\n"
"Qmava 20,0,116\nQtv 26,64,19712\nv_mul_f32 v161, v54, v161\nQmava 24,4,116\nQtv 28,64,14848\nv_mul_f32 v162, v54, v162\nQmava 28,8,116\nds_read_b32 v69, v74\n"
"v_mul_f32 v163, v54, v163\nQmava 32,12,116\nv_mul_f32 v164, v54, v164\nv_mul_f32 v165, v54, v165\nQmava 36,16,116\nQtv 30,64,19968\nv_mul_f32 v166, v54, v166\nQmava 40,0,120\n"
"Qtv 32,64,15104\nv_mul_f32 v167, v54, v167\nQmava 44,4,120\ns_waitcnt lgkmcnt(2)\nv_cmp_gt_u32 vcc, v75, v69\nQmava 48,8,120\ns_cbranch_vccnz .Lso6\n.Lro6:\n"
"Qk 160,160,161\nQmava 52,12,120\nQtv 34,64,20224\ns_add_u32 m0, s91, 132096\ns_mul_i32 s94, s31, 12\nQmava 56,16,120\nbuffer_load_dwordx4 v70, s[48:51], s94 offen lds\nQmava 60,0,124\n"
"Qk 161,162,163\nQk 162,164,165\nQmava 64,4,124\nQtv 36,64,15360\nQk 163,166,167\nQmava 68,8,124\nQtv 38,64,20480\nv_mul_f32 v192, v55, v192\n"
"Qmava 72,12,124\nbuffer_load_dwordx4 v71, s[48:51], s94 offen offset:1024 lds\nQmava 76,16,124\nv_mul_f32 v193, v55, v193\nv_mul_f32 v194, v55, v194\nQmava 80,0,128\nQda 140,48,0\nv_mul_f32 v195, v55, v195\n"
"Qmava 84,4,128\nQda 144,48,1024\ns_mul_i32 s95, s90, 12\nQmava 88,8,128\nbuffer_load_dwordx4 v72, s[52:55], s95 offen offset:2048 lds\nQmava 92,12,128\nQda 148,48,2048\nv_mul_f32 v196, v55, v196\n"
"Qmava 96,16,128\nv_mul_f32 v197, v55, v197\nv_mul_f32 v198, v55, v198\nQmava 100,0,132\nQda 152,48,3072\nv_mul_f32 v199, v55, v199\nQmava 104,4,132\nbuffer_load_dwordx4 v61, s[52:55], s95 offen offset:3072 lds\n"
"Qmava 108,8,132\nQda 156,48,4096\nQk 192,192,193\nQmava 176,12,132\nQk 193,194,195\nQk 194,196,197\nQmava 180,16,132\nQda 160,48,5120\n"
"s_add_u32 m0, s91, 136192\nQmava 184,0,136\nbuffer_load_dwordx4 v62, s[52:55], s95 offen lds\nQmava 188,4,136\nQda 164,48,6144\nQk 195,198,199\nQmava 192,8,136\nv_mul_f32 v224, v56, v224\n"
"v_mul_f32 v225, v56, v225\nQmava 196,12,136\nbuffer_load_dwordx4 v63, s[52:55], s95 offen offset:1024 lds\nv_add_u32 v75, 1, v75\nQmava 200,16,136\ns_waitcnt vmcnt(21)\nds_write_b32 v73, v75\ns_waitcnt lgkmcnt(5)\n"
"Qmava 0,20,140\nQtv 0,64,38912\nv_mul_f32 v226, v56, v226\nQmava 4,24,140\nbuffer_store_dwordx4 v[84:87], v60, s[56:59], s88 offen offset:2048 sc1\nQmava 8,28,140\nQtv 2,64,44032\nv_mul_f32 v227, v56, v227\n"
"Qmava 12,32,140\nQtv 4,64,39168\nv_mul_f32 v228, v56, v228\nQmava 16,36,140\nv_mul_f32 v229, v56, v229\nv_mul_f32 v230, v56, v230\nQmava 20,20,144\nQtv 6,64,44288\n"
"v_mul_f32 v231, v56, v231\nQmava 24,24,144\nQtv 8,64,39424\nQk 224,224,225\nQmava 28,28,144\nds_read_b32 v69, v74\nQk 225,226,227\nQmava 32,32,144\n"
"Qk 226,228,229\nQk 227,230,231\nQmava 36,36,144\nQtv 10,64,44544\nv_accvgpr_read_b32 v40, a208\nQmava 40,20,148\nQtv 12,64,39680\nv_accvgpr_read_b32 v41, a209\n"
"Qmava 44,24,148\ns_waitcnt lgkmcnt(2)\nv_cmp_gt_u32 vcc, v75, v69\nQmava 48,28,148\ns_cbranch_vccnz .Lso7\n.Lro7:\nv_accvgpr_read_b32 v42, a210\nQmava 52,32,148\n"
"Qtv 14,64,44800\ns_add_u32 m0, s91, 0\ns_mul_i32 s94, s31, 13\nQmava 56,36,148\nbuffer_load_dwordx4 v70, s[48:51], s94 offen lds\nQmava 60,20,152\nv_accvgpr_read_b32 v43, a211\nv_accvgpr_read_b32 v44, a212\n"
"Qmava 64,24,152\nQtv 16,64,39936\nv_accvgpr_read_b32 v45, a213\nQmava 68,28,152\nQtv 18,64,45056\nv_accvgpr_read_b32 v46, a214\nQmava 72,32,152\nbuffer_load_dwordx4 v71, s[48:51], s94 offen offset:1024 lds\n"
"Qmava 76,36,152\nv_accvgpr_read_b32 v47, a215\nv_mul_f32 v40, v57, v40\nQmava 80,20,156\nQda 112,48,24576\nv_mul_f32 v41, v57, v41\nQmava 84,24,156\nQda 116,48,25600\n"
"s_mul_i32 s95, s90, 13\nQmava 88,28,156\nbuffer_load_dwordx4 v72, s[52:55], s95 offen offset:2048 lds\nQmava 92,32,156\nQda 120,48,26624\nv_mul_f32 v42, v57, v42\nQmava 96,36,156\nv_mul_f32 v43, v57, v43\n"
"v_mul_f32 v44, v57, v44\nQmava 100,20,160\nQda 124,48,27648\nv_mul_f32 v45, v57, v45\nQmava 104,24,160\nbuffer_load_dwordx4 v61, s[52:55], s95 offen offset:3072 lds\nQmava 108,28,160\nQda 128,48,28672\n"
"v_mul_f32 v46, v57, v46\nQmava 176,32,160\nv_mul_f32 v47, v57, v47\nQk 148,40,41\nQmava 180,36,160\nQda 132,48,29696\ns_add_u32 m0, s91, 4096\nQmava 184,20,164\n"
"buffer_load_dwordx4 v62, s[52:55], s95 offen lds\nQmava 188,24,164\nQda 136,48,30720\nQk 149,42,43\nQmava 192,28,164\nQk 150,44,45\nQk 151,46,47\nQmava 196,32,164\n"
"buffer_load_dwordx4 v63, s[52:55], s95 offen offset:1024 lds\nv_add_u32 v75, 1, v75\nQmava 200,36,164\ns_waitcnt vmcnt(21)\nds_write_b32 v73, v75\ns_waitcnt lgkmcnt(5)\nQmava 0,0,112\nQtv 20,59,6144\n"
"s_add_u32 s88, s71, 14336\nQmava 4,4,112\nbuffer_store_dwordx4 v[88:91], v60, s[56:59], s88 offen offset:0 sc1\nQmava 8,8,112\nQtv 22,59,11264\nv_accvgpr_read_b32 v40, a240\nQmava 12,12,112\nQtv 24,59,6400\n"
"v_accvgpr_read_b32 v41, a241\nQmava 16,16,112\nv_accvgpr_read_b32 v42, a242\nv_accvgpr_read_b32 v43, a243\nQmava 20,0,116\nQtv 26,59,11520\nv_accvgpr_read_b32 v44, a244\nQmava 24,4,116\n"
"Qtv 28,59,6656\nv_accvgpr_read_b32 v45, a245\nQmava 28,8,116\nds_read_b32 v69, v74\nv_accvgpr_read_b32 v46, a246\nQmava 32,12,116\nv_accvgpr_read_b32 v47, a247\nv_mul_f32 v40, v58, v40\n"
"Qmava 36,16,116\nQtv 30,59,11776\nv_mul_f32 v41, v58, v41\nQmava 40,0,120\nQtv 32,59,6912\nv_mul_f32 v42, v58, v42\nQmava 44,4,120\ns_waitcnt lgkmcnt(2)\n"
"v_cmp_gt_u32 vcc, v75, v69\nQmava 48,8,120\ns_cbranch_vccnz .Lso8\n.Lro8:\nv_mul_f32 v43, v58, v43\nQmava 52,12,120\nQtv 34,59,12032\ns_add_u32 m0, s91, 24576\n"
"s_mul_i32 s94, s31, 14\nQmava 56,16,120\nbuffer_load_dwordx4 v70, s[48:51], s94 offen lds\nQmava 60,0,124\nv_mul_f32 v44, v58, v44\nv_mul_f32 v45, v58, v45\nQmava 64,4,124\nQtv 36,59,7168\n"
"v_mul_f32 v46, v58, v46\nQmava 68,8,124\nQtv 38,59,12288\nv_mul_f32 v47, v58, v47\nQmava 72,12,124\nbuffer_load_dwordx4 v71, s[48:51], s94 offen offset:1024 lds\nQmava 76,16,124\nQk 180,40,41\n"
"Qk 181,42,43\nQmava 80,0,128\nQda 140,48,49152\nQk 182,44,45\nQmava 84,4,128\nQda 144,48,50176\ns_mul_i32 s95, s90, 14\nQmava 88,8,128\n"
"buffer_load_dwordx4 v72, s[52:55], s95 offen offset:2048 lds\nQmava 92,12,128\nQda 148,48,51200\nQk 183,46,47\nQmava 96,16,128\nv_mul_f32 v104, v52, v104\nv_mul_f32 v105, v52, v105\nQmava 100,0,132\n"
"Qda 152,48,52224\nv_mul_f32 v106, v52, v106\nQmava 104,4,132\nbuffer_load_dwordx4 v61, s[52:55], s95 offen offset:3072 lds\nQmava 108,8,132\nQda 156,48,53248\nv_mul_f32 v107, v52, v107\nQmava 176,12,132\n"
"v_mul_f32 v108, v52, v108\nv_mul_f32 v109, v52, v109\nQmava 180,16,132\nQda 160,48,54272\ns_add_u32 m0, s91, 28672\nQmava 184,0,136\nbuffer_load_dwordx4 v62, s[52:55], s95 offen lds\nQmava 188,4,136\n"
"Qda 164,48,55296\nv_mul_f32 v110, v52, v110\nQmava 192,8,136\nv_mul_f32 v111, v52, v111\nQk 104,104,105\nQmava 196,12,136\nbuffer_load_dwordx4 v63, s[52:55], s95 offen offset:1024 lds\nv_add_u32 v75, 1, v75\n"
"Qmava 200,16,136\ns_waitcnt vmcnt(21)\nds_write_b32 v73, v75\ns_waitcnt lgkmcnt(5)\nQmava 0,20,140\nQtv 0,59,30720\nQk 105,106,107\nQmava 4,24,140\n"
"buffer_store_dwordx4 v[120:123], v60, s[56:59], s88 offen offset:1024 sc1\nQmava 8,28,140\nQtv 2,59,35840\nQk 106,108,109\nQmava 12,32,140\nQtv 4,59,30976\nQk 107,110,111\nQmava 16,36,140\n"
"v_mul_f32 v136, v53, v136\nv_mul_f32 v137, v53, v137\nQmava 20,20,144\nQtv 6,59,36096\nv_mul_f32 v138, v53, v138\nQmava 24,24,144\nQtv 8,59,31232\nv_mul_f32 v139, v53, v139\n"
"Qmava 28,28,144\nds_read_b32 v69, v74\nv_mul_f32 v140, v53, v140\nQmava 32,32,144\nv_mul_f32 v141, v53, v141\nv_mul_f32 v142, v53, v142\nQmava 36,36,144\nQtv 10,59,36352\n"
"v_mul_f32 v143, v53, v143\nQmava 40,20,148\nQtv 12,59,31488\nQk 136,136,137\nQmava 44,24,148\ns_waitcnt lgkmcnt(2)\nv_cmp_gt_u32 vcc, v75, v69\nQmava 48,28,148\n"
"s_cbranch_vccnz .Lso9\n.Lro9:\nQk 137,138,139\nQmava 52,32,148\nQtv 14,59,36608\ns_add_u32 m0, s91, 49152\ns_mul_i32 s94, s31, 15\nQmava 56,36,148\n"
"buffer_load_dwordx4 v70, s[48:51], s94 offen lds\nQmava 60,20,152\nQk 138,140,141\nQk 139,142,143\nQmava 64,24,152\nQtv 16,59,31744\nv_mul_f32 v168, v54, v168\nQmava 68,28,152\n"
"Qtv 18,59,36864\nv_mul_f32 v169, v54, v169\nQmava 72,32,152\nbuffer_load_dwordx4 v71, s[48:51], s94 offen offset:1024 lds\nQmava 76,36,152\nv_mul_f32 v170, v54, v170\nv_mul_f32 v171, v54, v171\nQmava 80,20,156\n"
"Qda 112,49,0\nv_mul_f32 v172, v54, v172\nQmava 84,24,156\nQda 116,49,1024\ns_mul_i32 s95, s90, 15\nQmava 88,28,156\nbuffer_load_dwordx4 v72, s[52:55], s95 offen offset:2048 lds\nQmava 92,32,156\n"
"Qda 120,49,2048\nv_mul_f32 v173, v54, v173\nQmava 96,36,156\nv_mul_f32 v174, v54, v174\nv_mul_f32 v175, v54, v175\nQmava 100,20,160\nQda 124,49,3072\nQk 168,168,169\n"
"Qmava 104,24,160\nbuffer_load_dwordx4 v61, s[52:55], s95 offen offset:3072 lds\nQmava 108,28,160\nQda 128,49,4096\nQk 169,170,171\nQmava 176,32,160\nQk 170,172,173\nQk 171,174,175\n"
"Qmava 180,36,160\nQda 132,49,5120\ns_add_u32 m0, s91, 53248\nQmava 184,20,164\nbuffer_load_dwordx4 v62, s[52:55], s95 offen lds\nQmava 188,24,164\nQda 136,49,6144\nv_mul_f32 v200, v55, v200\n"
"Qmava 192,28,164\nv_mul_f32 v201, v55, v201\nv_mul_f32 v202, v55, v202\nQmava 196,32,164\nbuffer_load_dwordx4 v63, s[52:55], s95 offen offset:1024 lds\nv_add_u32 v75, 1, v75\nQmava 200,36,164\ns_waitcnt vmcnt(21)\n"
"ds_write_b32 v73, v75\ns_waitcnt lgkmcnt(5)\nQmava 0,0,112\nQtv 20,59,55296\nv_mul_f32 v203, v55, v203\nQmava 4,4,112\nbuffer_store_dwordx4 v[152:155], v60, s[56:59], s88 offen offset:2048 sc1\nQmava 8,8,112\n"
"Qtv 22,59,60416\nv_mul_f32 v204, v55, v204\nQmava 12,12,112\nQtv 24,59,55552\nv_mul_f32 v205, v55, v205\nQmava 16,16,112\nv_mul_f32 v206, v55, v206\nv_mul_f32 v207, v55, v207\n"
"Qmava 20,0,116\nQtv 26,59,60672\nQk 200,200,201\nQmava 24,4,116\nQtv 28,59,55808\nQk 201,202,203\nQmava 28,8,116\nds_read_b32 v69, v74\n"
"Qk 202,204,205\nQmava 32,12,116\nQk 203,206,207\nv_mul_f32 v232, v56, v232\nQmava 36,16,116\nQtv 30,59,60928\nv_mul_f32 v233, v56, v233\nQmava 40,0,120\n"
"Qtv 32,59,56064\nv_mul_f32 v234, v56, v234\nQmava 44,4,120\ns_waitcnt lgkmcnt(2)\nv_cmp_gt_u32 vcc, v75, v69\nQmava 48,8,120\ns_cbranch_vccnz .Lso10\n.Lro10:\n"
"v_mul_f32 v235, v56, v235\nQmava 52,12,120\nQtv 34,59,61184\ns_add_u32 m0, s91, 73728\ns_mul_i32 s94, s31, 16\nQmava 56,16,120\nbuffer_load_dwordx4 v70, s[48:51], s94 offen lds\nQmava 60,0,124\n"
"v_mul_f32 v236, v56, v236\nv_mul_f32 v237, v56, v237\nQmava 64,4,124\nQtv 36,59,56320\nv_mul_f32 v238, v56, v238\nQmava 68,8,124\nQtv 38,59,61440\nv_mul_f32 v239, v56, v239\n"
"Qmava 72,12,124\nbuffer_load_dwordx4 v71, s[48:51], s94 offen offset:1024 lds\nQmava 76,16,124\nQk 232,232,233\nQk 233,234,235\nQmava 80,0,128\nQda 140,49,24576\nQk 234,236,237\n"
"Qmava 84,4,128\nQda 144,49,25600\ns_mul_i32 s95, s90, 16\nQmava 88,8,128\nbuffer_load_dwordx4 v72, s[52:55], s95 offen offset:2048 lds\nQmava 92,12,128\nQda 148,49,26624\nQk 235,238,239\n"
".Lpg15:\ns_cbranch_execz .Ltr6\nQmava 96,16,128\nv_accvgpr_read_b32 v40, a216\nv_accvgpr_read_b32 v41, a217\nQmava 100,0,132\nQda 152,49,27648\nv_accvgpr_read_b32 v42, a218\n"
"Qmava 104,4,132\nbuffer_load_dwordx4 v61, s[52:55], s95 offen offset:3072 lds\nQmava 108,8,132\nQda 156,49,28672\nv_accvgpr_read_b32 v43, a219\nQmava 176,12,132\nv_accvgpr_read_b32 v44, a220\nv_accvgpr_read_b32 v45, a221\n"
"Qmava 180,16,132\nQda 160,49,29696\ns_add_u32 m0, s91, 77824\nQmava 184,0,136\nbuffer_load_dwordx4 v62, s[52:55], s95 offen lds\nQmava 188,4,136\nQda 164,49,30720\nv_accvgpr_read_b32 v46, a222\n"
"Qmava 192,8,136\nv_accvgpr_read_b32 v47, a223\nv_mul_f32 v40, v57, v40\nQmava 196,12,136\nbuffer_load_dwordx4 v63, s[52:55], s95 offen offset:1024 lds\nv_add_u32 v75, 1, v75\nQmava 200,16,136\ns_waitcnt vmcnt(21)\n"
"ds_write_b32 v73, v75\ns_waitcnt lgkmcnt(5)\nQmava 0,20,140\nQtv 0,50,31744\nv_mul_f32 v41, v57, v41\nQmava 4,24,140\nbuffer_store_dwordx4 v[184:187], v60, s[56:59], s88 offen offset:3072 sc1\nQmava 8,28,140\n"
"Qtv 2,50,36864\nv_mul_f32 v42, v57, v42\nQmava 12,32,140\nQtv 4,50,32000\nv_mul_f32 v43, v57, v43\nQmava 16,36,140\nv_mul_f32 v44, v57, v44\nv_mul_f32 v45, v57, v45\n"
"Qmava 20,20,144\nQtv 6,50,37120\nv_mul_f32 v46, v57, v46\nQmava 24,24,144\nQtv 8,50,32256\nv_mul_f32 v47, v57, v47\nQmava 28,28,144\nds_read_b32 v69, v74\n"
"Qk 212,40,41\nQmava 32,32,144\nQk 213,42,43\nQk 214,44,45\nQmava 36,36,144\nQtv 10,50,37376\nQk 215,46,47\nQmava 40,20,148\n"
"Qtv 12,50,32512\nv_accvgpr_read_b32 v40, a248\nQmava 44,24,148\ns_waitcnt lgkmcnt(2)\nv_cmp_gt_u32 vcc, v75, v69\nQmava 48,28,148\ns_cbranch_vccnz .Lso11\n.Lro11:\n"
"v_accvgpr_read_b32 v41, a249\nQmava 52,32,148\nQtv 14,50,37632\ns_add_u32 m0, s91, 98304\ns_mul_i32 s94, s31, 17\nQmava 56,36,148\nbuffer_load_dwordx4 v70, s[48:51], s94 offen lds\nQmava 60,20,152\n"
"v_accvgpr_read_b32 v42, a250\nv_accvgpr_read_b32 v43, a251\nQmava 64,24,152\nQtv 16,50,32768\nv_accvgpr_read_b32 v44, a252\nQmava 68,28,152\nQtv 18,50,37888\nv_accvgpr_read_b32 v45, a253\n"
"Qmava 72,32,152\nbuffer_load_dwordx4 v71, s[48:51], s94 offen offset:1024 lds\nQmava 76,36,152\nv_accvgpr_read_b32 v46, a254\nv_accvgpr_read_b32 v47, a255\nQmava 80,20,156\nQda 112,49,58368\nv_mul_f32 v40, v58, v40\n"
"Qmava 84,24,156\nQda 116,49,59392\ns_mul_i32 s95, s90, 17\nQmava 88,28,156\nbuffer_load_dwordx4 v72, s[52:55], s95 offen offset:2048 lds\nQmava 92,32,156\nQda 120,49,60416\nv_mul_f32 v41, v58, v41\n"
"Qmava 96,36,156\nv_mul_f32 v42, v58, v42\nv_mul_f32 v43, v58, v43\nQmava 100,20,160\nQda 124,49,61440\nv_mul_f32 v44, v58, v44\nQmava 104,24,160\nbuffer_load_dwordx4 v61, s[52:55], s95 offen offset:3072 lds\n"
"Qmava 108,28,160\nQda 128,49,62464\nv_mul_f32 v45, v58, v45\nQmava 176,32,160\nv_mul_f32 v46, v58, v46\nv_mul_f32 v47, v58, v47\nQmava 180,36,160\nQda 132,49,63488\n"
"s_add_u32 m0, s91, 102400\nQmava 184,20,164\nbuffer_load_dwordx4 v62, s[52:55], s95 offen lds\nQmava 188,24,164\nQda 136,49,64512\nQk 244,40,41\nQmava 192,28,164\nQk 245,42,43\n"
"Qk 246,44,45\nQmava 196,32,164\nbuffer_load_dwordx4 v63, s[52:55], s95 offen offset:1024 lds\nv_add_u32 v75, 1, v75\nQmava 200,36,164\ns_waitcnt vmcnt(21)\nds_write_b32 v73, v75\ns_waitcnt lgkmcnt(5)\n"
"Qmava 0,0,112\nQtv 20,64,14336\ns_add_u32 s88, s71, 18432\nQmava 4,4,112\nbuffer_store_dwordx4 v[216:219], v60, s[56:59], s88 offen offset:0 sc1\nQmava 8,8,112\nQtv 22,64,19456\nQk 247,46,47\n"
"Qmava 12,12,112\nQtv 24,64,14592\nQmava 16,16,112\nQmava 20,0,116\nQtv 26,64,19712\nQmava 24,4,116\nQtv 28,64,14848\nQmava 28,8,116\n"
"ds_read_b32 v69, v74\nQmava 32,12,116\nQtv 30,64,19968\nQmava 36,16,116\nQmava 40,0,120\nQtv 32,64,15104\nv_add_u32 v67, 2, v65\nQmava 44,4,120\n"
"s_waitcnt lgkmcnt(2)\nv_cmp_gt_u32 vcc, v75, v69\nQmava 48,8,120\ns_cbranch_vccnz .Lso12\n.Lro12:\nQmava 52,12,120\nbuffer_store_dword v67, v77, s[24:27], 0 offen sc1\ns_add_u32 m0, s91, 132096\n"
"s_mul_i32 s94, s31, 18\nQmava 56,16,120\nbuffer_load_dwordx4 v70, s[48:51], s94 offen lds\nQmava 60,0,124\nQtv 34,64,20224\nQmava 64,4,124\nQtv 36,64,15360\nQmava 68,8,124\n"
"Qmava 72,12,124\nbuffer_load_dwordx4 v71, s[48:51], s94 offen offset:1024 lds\nQmava 76,16,124\nQtv 38,64,20480\nQmava 80,0,128\nQda 140,48,0\nQmava 84,4,128\nQda 144,48,1024\n"
"s_mul_i32 s95, s90, 18\nQmava 88,8,128\nbuffer_load_dwordx4 v72, s[52:55], s95 offen offset:2048 lds\nQmava 92,12,128\nQmava 96,16,128\nQda 148,48,2048\nQmava 100,0,132\nQda 152,48,3072\n"
"Qmava 104,4,132\nbuffer_load_dwordx4 v61, s[52:55], s95 offen offset:3072 lds\nQmava 108,8,132\nQda 156,48,4096\nQmava 176,12,132\nQmava 180,16,132\nQda 160,48,5120\ns_add_u32 m0, s91, 136192\n"
"Qmava 184,0,136\nbuffer_load_dwordx4 v62, s[52:55], s95 offen lds\nQmava 188,4,136\nQda 164,48,6144\nQmava 192,8,136\nQmava 196,12,136\nbuffer_load_dwordx4 v63, s[52:55], s95 offen offset:1024 lds\nv_add_u32 v75, 1, v75\n"
"Qmava 200,16,136\ns_waitcnt vmcnt(22)\nds_write_b32 v73, v75\ns_waitcnt lgkmcnt(5)\nQmava 0,20,140\nQtv 0,64,38912\nQmava 4,24,140\nbuffer_store_dwordx4 v[248:251], v60, s[56:59], s88 offen offset:1024 sc1\n"
"Qmava 8,28,140\nQtv 2,64,44032\nQmava 12,32,140\nQtv 4,64,39168\nQmava 16,36,140\nQmava 20,20,144\nQtv 6,64,44288\nQmava 24,24,144\n"
"Qtv 8,64,39424\nQmava 28,28,144\nds_read_b32 v69, v74\nQmava 32,32,144\nQmava 36,36,144\nQtv 10,64,44544\nQmava 40,20,148\nQtv 12,64,39680\n"
"Qmava 44,24,148\ns_waitcnt lgkmcnt(2)\nv_cmp_gt_u32 vcc, v75, v69\nQmava 48,28,148\ns_cbranch_vccnz .Lso13\n.Lro13:\nQmava 52,32,148\nQtv 14,64,44800\n"
"s_add_u32 m0, s91, 0\ns_mul_i32 s94, s31, 19\nQmava 56,36,148\nbuffer_load_dwordx4 v70, s[48:51], s94 offen lds\nQmava 60,20,152\nQmava 64,24,152\nQtv 16,64,39936\nQmava 68,28,152\n"
"Qtv 18,64,45056\nQmava 72,32,152\nbuffer_load_dwordx4 v71, s[48:51], s94 offen offset:1024 lds\nQmava 76,36,152\nQmava 80,20,156\nQda 112,48,24576\nQmava 84,24,156\nQda 116,48,25600\n"
"s_mul_i32 s95, s90, 19\nQmava 88,28,156\nbuffer_load_dwordx4 v72, s[52:55], s95 offen offset:2048 lds\nQmava 92,32,156\nQda 120,48,26624\nQmava 96,36,156\nQmava 100,20,160\nQda 124,48,27648\n"
"Qmava 104,24,160\nbuffer_load_dwordx4 v61, s[52:55], s95 offen offset:3072 lds\nQmava 108,28,160\nQda 128,48,28672\nQmava 176,32,160\nQmava 180,36,160\nQda 132,48,29696\ns_add_u32 m0, s91, 4096\n"
"Qmava 184,20,164\nbuffer_load_dwordx4 v62, s[52:55], s95 offen lds\nQmava 188,24,164\nQda 136,48,30720\nQmava 192,28,164\nQmava 196,32,164\nbuffer_load_dwordx4 v63, s[52:55], s95 offen offset:1024 lds\nv_add_u32 v75, 1, v75\n"
"Qmava 200,36,164\ns_waitcnt vmcnt(22)\nds_write_b32 v73, v75\ns_waitcnt lgkmcnt(5)\nQmava 0,0,112\nQtv 20,59,6144\nQmava 4,4,112\nbuffer_store_dwordx4 v[116:119], v60, s[56:59], s88 offen offset:2048 sc1\n"
"Qmava 8,8,112\nQtv 22,59,11264\nQmava 12,12,112\nQtv 24,59,6400\nQmava 16,16,112\nQmava 20,0,116\nQtv 26,59,11520\nv_lshrrev_b32 v57, 8, v60\n"
"Qmava 24,4,116\nQtv 28,59,6656\ns_mul_i32 s68, s92, 20\nQmava 28,8,116\nds_read_b32 v69, v74\nv_accvgpr_read_b32 v92, a0\nQmava 32,12,116\nv_accvgpr_read_b32 v93, a1\n"
"v_accvgpr_read_b32 v94, a2\nQmava 36,16,116\nQtv 30,59,11776\nv_accvgpr_read_b32 v95, a3\nQmava 40,0,120\nQtv 32,59,6912\nv_accvgpr_read_b32 v124, a4\nQmava 44,4,120\n"
"s_waitcnt lgkmcnt(2)\nv_cmp_gt_u32 vcc, v75, v69\nQmava 48,8,120\ns_cbranch_vccnz .Lso14\n.Lro14:\nv_accvgpr_read_b32 v125, a5\nQmava 52,12,120\nQtv 34,59,12032\n"
"s_add_u32 m0, s91, 24576\ns_mul_i32 s94, s31, 20\nQmava 56,16,120\nbuffer_load_dwordx4 v70, s[48:51], s94 offen lds\nQmava 60,0,124\nv_accvgpr_read_b32 v126, a6\nv_accvgpr_read_b32 v127, a7\nQmava 64,4,124\n"
"Qtv 36,59,7168\nv_add_u32 v57, s68, v57\nQmava 68,8,124\nQtv 38,59,12288\nv_add_u32 v55, 0, v57\nQmava 72,12,124\nbuffer_load_dwordx4 v71, s[48:51], s94 offen offset:1024 lds\nQmava 76,16,124\n"
"v_mul_u32_u24 v56, 205, v55\nv_lshrrev_b32 v56, 11, v56\nQmava 80,0,128\nQda 140,48,49152\nv_mul_u32_u24 v54, 10, v56\nQmava 84,4,128\nQda 144,48,50176\ns_mul_i32 s95, s90, 20\n"
"Qmava 88,8,128\nbuffer_load_dwordx4 v72, s[52:55], s95 offen offset:2048 lds\nQmava 92,12,128\nQda 148,48,51200\nv_sub_u32 v55, v55, v54\nQmava 96,16,128\nv_lshlrev_b32 v55, 5, v55\nv_bfe_u32 v54, v60, 5, 3\n"
"Qmava 100,0,132\nQda 152,48,52224\nv_lshl_add_u32 v56, v56, 3, v54\nQmava 104,4,132\nbuffer_load_dwordx4 v61, s[52:55], s95 offen offset:3072 lds\nQmava 108,8,132\nQda 156,48,53248\nv_mul_u32_u24 v56, 1280, v56\n"
"Qmava 176,12,132\nv_and_b32 v54, 16, v60\nv_add3_u32 v55, v55, v56, v54\nQmava 180,16,132\nQda 160,48,54272\ns_add_u32 m0, s91, 28672\nQmava 184,0,136\nbuffer_load_dwordx4 v62, s[52:55], s95 offen lds\n"
"Qmava 188,4,136\nQda 164,48,55296\nv_add_u32 v40, 4096, v55\nQmava 192,8,136\nv_add_u32 v55, 4, v57\nv_mul_u32_u24 v56, 205, v55\nQmava 196,12,136\nbuffer_load_dwordx4 v63, s[52:55], s95 offen offset:1024 lds\n"
"v_add_u32 v75, 1, v75\nQmava 200,16,136\ns_waitcnt vmcnt(22)\nds_write_b32 v73, v75\ns_waitcnt lgkmcnt(5)\nQmvva 92,20,140\nQtv 0,59,30720\ns_add_u32 s88, s71, 28672\n"
"Qmvva 124,24,140\nbuffer_store_dwordx4 v[96:99], v60, s[56:59], s88 offen offset:0 sc1\nQmava 8,28,140\nQtv 2,59,35840\nv_lshrrev_b32 v56, 11, v56\nQmava 12,32,140\nQtv 4,59,30976\nv_mul_u32_u24 v54, 10, v56\n"
"Qmava 16,36,140\nv_sub_u32 v55, v55, v54\nv_lshlrev_b32 v55, 5, v55\nQmava 20,20,144\nQtv 6,59,36096\nv_bfe_u32 v54, v60, 5, 3\nQmava 24,24,144\nQtv 8,59,31232\n"
"v_lshl_add_u32 v56, v56, 3, v54\nQmava 28,28,144\nds_read_b32 v69, v74\nv_mul_u32_u24 v56, 1280, v56\nQmava 32,32,144\nv_and_b32 v54, 16, v60\nv_add3_u32 v55, v55, v56, v54\nQmava 36,36,144\n"
"Qtv 10,59,36352\nv_accvgpr_read_b32 v156, a8\nQmava 40,20,148\nQtv 12,59,31488\nv_accvgpr_read_b32 v157, a9\nQmava 44,24,148\ns_waitcnt lgkmcnt(2)\nv_cmp_gt_u32 vcc, v75, v69\n"
"Qmava 48,28,148\ns_cbranch_vccnz .Lso15\n.Lro15:\nv_accvgpr_read_b32 v158, a10\nQmava 52,32,148\nQtv 14,59,36608\ns_add_u32 m0, s91, 49152\ns_mul_i32 s94, s31, 21\n"
"Qmava 56,36,148\nbuffer_load_dwordx4 v70, s[48:51], s94 offen lds\nQmava 60,20,152\nv_accvgpr_read_b32 v159, a11\nv_accvgpr_read_b32 v188, a12\nQmava 64,24,152\nQtv 16,59,31744\nv_accvgpr_read_b32 v189, a13\n"
"Qmava 68,28,152\nQtv 18,59,36864\nv_accvgpr_read_b32 v190, a14\nQmava 72,32,152\nbuffer_load_dwordx4 v71, s[48:51], s94 offen offset:1024 lds\nQmava 76,36,152\nv_accvgpr_read_b32 v191, a15\nv_add_u32 v41, 3072, v55\n"
"Qmava 80,20,156\nQda 112,49,0\nv_add_u32 v55, 8, v57\nQmava 84,24,156\nQda 116,49,1024\ns_mul_i32 s95, s90, 21\nQmava 88,28,156\nbuffer_load_dwordx4 v72, s[52:55], s95 offen offset:2048 lds\n"
"Qmava 92,32,156\nQda 120,49,2048\nv_mul_u32_u24 v56, 205, v55\nQmava 96,36,156\nv_lshrrev_b32 v56, 11, v56\nv_mul_u32_u24 v54, 10, v56\nQmava 100,20,160\nQda 124,49,3072\n"
"v_sub_u32 v55, v55, v54\nQmava 104,24,160\nbuffer_load_dwordx4 v61, s[52:55], s95 offen offset:3072 lds\nQmava 108,28,160\nQda 128,49,4096\nv_lshlrev_b32 v55, 5, v55\nQmava 176,32,160\nv_bfe_u32 v54, v60, 5, 3\n"
"v_lshl_add_u32 v56, v56, 3, v54\nQmava 180,36,160\nQda 132,49,5120\ns_add_u32 m0, s91, 53248\nQmava 184,20,164\nbuffer_load_dwordx4 v62, s[52:55], s95 offen lds\nQmava 188,24,164\nQda 136,49,6144\n"
"v_mul_u32_u24 v56, 1280, v56\nQmava 192,28,164\nv_and_b32 v54, 16, v60\nv_add3_u32 v55, v55, v56, v54\nQmava 196,32,164\nbuffer_load_dwordx4 v63, s[52:55], s95 offen offset:1024 lds\nv_add_u32 v75, 1, v75\nQmava 200,36,164\n"
"s_waitcnt vmcnt(21)\nds_write_b32 v73, v75\ns_waitcnt lgkmcnt(5)\nQmvva 92,0,112\nQtv 20,59,55296\nv_add_u32 v42, 2048, v55\nQmvva 124,4,112\nbuffer_store_dwordx4 v[128:131], v60, s[56:59], s88 offen offset:1024 sc1\n"
"Qmvva 156,8,112\nQtv 22,59,60416\nv_add_u32 v55, 12, v57\nQmvva 188,12,112\nQtv 24,59,55552\nv_mul_u32_u24 v56, 205, v55\nQmava 16,16,112\nv_lshrrev_b32 v56, 11, v56\n"
"v_mul_u32_u24 v54, 10, v56\nQmava 20,0,116\nQtv 26,59,60672\nv_sub_u32 v55, v55, v54\nQmava 24,4,116\nQtv 28,59,55808\nv_lshlrev_b32 v55, 5, v55\nQmava 28,8,116\n"
"ds_read_b32 v69, v74\nv_bfe_u32 v54, v60, 5, 3\nQmava 32,12,116\nv_lshl_add_u32 v56, v56, 3, v54\nv_mul_u32_u24 v56, 1280, v56\nQmava 36,16,116\nQtv 30,59,60928\nv_and_b32 v54, 16, v60\n"
"Qmava 40,0,120\nQtv 32,59,56064\nv_add3_u32 v55, v55, v56, v54\nQmava 44,4,120\ns_waitcnt lgkmcnt(2)\nv_cmp_gt_u32 vcc, v75, v69\nQmava 48,8,120\ns_cbranch_vccnz .Lso16\n"
".Lro16:\nv_accvgpr_read_b32 v220, a16\nQmava 52,12,120\nQtv 34,59,61184\ns_add_u32 m0, s91, 73728\ns_mul_i32 s94, s31, 22\nQmava 56,16,120\nbuffer_load_dwordx4 v70, s[48:51], s94 offen lds\n"
"Qmava 60,0,124\nv_accvgpr_read_b32 v221, a17\nv_accvgpr_read_b32 v222, a18\nQmava 64,4,124\nQtv 36,59,56320\nv_accvgpr_read_b32 v223, a19\nQmava 68,8,124\nQtv 38,59,61440\n"
"v_accvgpr_read_b32 v252, a20\nQmava 72,12,124\nbuffer_load_dwordx4 v71, s[48:51], s94 offen offset:1024 lds\nQmava 76,16,124\nv_accvgpr_read_b32 v253, a21\nv_accvgpr_read_b32 v254, a22\nQmava 80,0,128\nQda 140,49,24576\n"
"v_accvgpr_read_b32 v255, a23\nQmava 84,4,128\nQda 144,49,25600\ns_mul_i32 s95, s90, 22\nQmava 88,8,128\nbuffer_load_dwordx4 v72, s[52:55], s95 offen offset:2048 lds\nQmava 92,12,128\nQda 148,49,26624\n"
"v_add_u32 v43, 1024, v55\nQmava 96,16,128\nv_add_u32 v55, 16, v57\nv_mul_u32_u24 v56, 205, v55\nQmava 100,0,132\nQda 152,49,27648\nv_lshrrev_b32 v56, 11, v56\nQmava 104,4,132\n"
"buffer_load_dwordx4 v61, s[52:55], s95 offen offset:3072 lds\nQmava 108,8,132\nQda 156,49,28672\nv_mul_u32_u24 v54, 10, v56\nQmava 176,12,132\nv_sub_u32 v55, v55, v54\nv_lshlrev_b32 v55, 5, v55\nQmava 180,16,132\n"
"Qda 160,49,29696\n.Lpg16:\ns_cbranch_execz .Lpg20\ns_add_u32 m0, s91, 77824\nQmava 184,0,136\nbuffer_load_dwordx4 v62, s[52:55], s95 offen lds\nQmava 188,4,136\nQda 164,49,30720\n"
"v_bfe_u32 v54, v60, 5, 3\nQmava 192,8,136\nv_lshl_add_u32 v56, v56, 3, v54\nv_mul_u32_u24 v56, 1280, v56\nQmava 196,12,136\nbuffer_load_dwordx4 v63, s[52:55], s95 offen offset:1024 lds\nv_add_u32 v75, 1, v75\nQmava 200,16,136\n"
"s_waitcnt vmcnt(21)\nds_write_b32 v73, v75\ns_waitcnt lgkmcnt(5)\nQmvva 92,20,140\nQtv 0,50,31744\nv_and_b32 v54, 16, v60\nQmvva 124,24,140\nbuffer_store_dwordx4 v[160:163], v60, s[56:59], s88 offen offset:2048 sc1\n"
"Qmvva 156,28,140\nQtv 2,50,36864\nv_add3_u32 v55, v55, v56, v54\nQmvva 188,32,140\nQtv 4,50,32000\nv_add_u32 v44, 4096, v55\nQmvva 220,36,140\ns_mul_i32 s96, s70, 5120\n"
"s_add_u32 s96, s96, %10\nQmvva 252,20,144\nQtv 6,50,37120\ns_mul_i32 s68, s93, 10240\nQmava 24,24,144\nQtv 8,50,32256\ns_add_u32 s68, s68, 4096\nQmava 28,28,144\n"
"ds_read_b32 v69, v74\nv_add_u32 v45, s68, v64\nQmava 32,32,144\nv_add_u32 v46, 61440, v45\ns_sub_u32 s68, 18432, s68\nQmava 36,36,144\nQtv 10,50,37376\nv_add_u32 v47, s68, v64\n"
"Qmava 40,20,148\nQtv 12,50,32512\nv_add_u32 v51, 61440, v47\nQmava 44,24,148\ns_waitcnt lgkmcnt(2)\nv_cmp_gt_u32 vcc, v75, v69\nQmava 48,28,148\ns_cbranch_vccnz .Lso17\n"
".Lro17:\ns_lshl_b32 s68, s70, 10\nQmava 52,32,148\nQtv 14,50,37632\ns_add_u32 m0, s91, 98304\ns_mul_i32 s94, s31, 23\nQmava 56,36,148\nbuffer_load_dwordx4 v70, s[48:51], s94 offen lds\n"
"Qmava 60,20,152\nv_accvgpr_read_b32 v100, a24\nv_accvgpr_read_b32 v101, a25\nQmava 64,24,152\nQtv 16,50,32768\nv_accvgpr_read_b32 v102, a26\nQmava 68,28,152\nQtv 18,50,37888\n"
"v_accvgpr_read_b32 v103, a27\nQmava 72,32,152\nbuffer_load_dwordx4 v71, s[48:51], s94 offen offset:1024 lds\nQmava 76,36,152\nv_accvgpr_read_b32 v132, a28\nv_accvgpr_read_b32 v133, a29\nQmava 80,20,156\nQda 112,49,58368\n"
"v_accvgpr_read_b32 v134, a30\nQmava 84,24,156\nQda 116,49,59392\ns_mul_i32 s95, s90, 23\nQmava 88,28,156\nbuffer_load_dwordx4 v72, s[52:55], s95 offen offset:2048 lds\nQmava 92,32,156\nQda 120,49,60416\n"
"v_accvgpr_read_b32 v135, a31\nQmava 96,36,156\ns_add_u32 s69, %10, 106496\nv_add_u32 v54, s69, v60\nQmava 100,20,160\nQda 124,49,61440\nv_add_u32 v52, s68, v54\nQmava 104,24,160\n"
"buffer_load_dwordx4 v61, s[52:55], s95 offen offset:3072 lds\nQmava 108,28,160\nQda 128,49,62464\ns_xor_b32 s68, s68, 2048\nQmava 176,32,160\nv_add_u32 v53, s68, v54\ns_mul_i32 s68, s93, 163840\nQmava 180,36,160\n"
"Qda 132,49,63488\ns_add_u32 m0, s91, 102400\nQmava 184,20,164\nbuffer_load_dwordx4 v62, s[52:55], s95 offen lds\nQmava 188,24,164\nQda 136,49,64512\nQmava 192,28,164\ns_add_u32 s62, s62, s68\n"
"s_addc_u32 s63, s63, 0\nQmava 196,32,164\nbuffer_load_dwordx4 v63, s[52:55], s95 offen offset:1024 lds\nv_add_u32 v75, 1, v75\nQmava 200,36,164\ns_waitcnt vmcnt(21)\nds_write_b32 v73, v75\ns_waitcnt lgkmcnt(5)\n"
"Qmvva 92,0,112\nQtv 20,64,14336\ns_sub_u32 s86, s86, s68\nQmvva 124,4,112\nbuffer_store_dwordx4 v[192:195], v60, s[56:59], s88 offen offset:3072 sc1\nQmvva 156,8,112\nQtv 22,64,19456\nQmvva 188,12,112\n"
"Qtv 24,64,14592\nQmvva 220,16,112\nQmvva 252,0,116\nQtv 26,64,19712\nQmvva 100,4,116\nQtv 28,64,14848\nQmvva 132,8,116\nds_read_b32 v69, v74\n"
"Qmava 32,12,116\nQmava 36,16,116\nQtv 30,64,19968\nQmava 40,0,120\nQtv 32,64,15104\nQmava 44,4,120\ns_waitcnt lgkmcnt(2)\nv_cmp_gt_u32 vcc, v75, v69\n"
"Qmava 48,8,120\ns_cbranch_vccnz .Lso18\n.Lro18:\nQmava 52,12,120\nQtv 34,64,20224\ns_add_u32 m0, s91, 132096\ns_mul_i32 s94, s31, 24\nQmava 56,16,120\n"
"buffer_load_dwordx4 v70, s[48:51], s94 offen lds\nQmava 60,0,124\nv_accvgpr_read_b32 v164, a32\nv_accvgpr_read_b32 v165, a33\nQmava 64,4,124\nQtv 36,64,15360\nv_accvgpr_read_b32 v166, a34\nQmava 68,8,124\n"
"Qtv 38,64,20480\nv_accvgpr_read_b32 v167, a35\nQmava 72,12,124\nbuffer_load_dwordx4 v71, s[48:51], s94 offen offset:1024 lds\nQmava 76,16,124\nv_accvgpr_read_b32 v196, a36\nv_accvgpr_read_b32 v197, a37\nQmava 80,0,128\n"
"Qda 140,48,0\nv_accvgpr_read_b32 v198, a38\nQmava 84,4,128\nQda 144,48,1024\ns_mul_i32 s95, s90, 24\nQmava 88,8,128\nbuffer_load_dwordx4 v72, s[52:55], s95 offen offset:2048 lds\nQmava 92,12,128\n"
"Qda 148,48,2048\nv_accvgpr_read_b32 v199, a39\nQmava 96,16,128\nQmava 100,0,132\nQda 152,48,3072\nQmava 104,4,132\nbuffer_load_dwordx4 v61, s[52:55], s95 offen offset:3072 lds\nQmava 108,8,132\n"
"Qda 156,48,4096\nQmava 176,12,132\nQmava 180,16,132\nQda 160,48,5120\ns_add_u32 m0, s91, 136192\nQmava 184,0,136\nbuffer_load_dwordx4 v62, s[52:55], s95 offen lds\nQmava 188,4,136\n"
"Qda 164,48,6144\nQmava 192,8,136\nQmava 196,12,136\nbuffer_load_dwordx4 v63, s[52:55], s95 offen offset:1024 lds\nv_add_u32 v75, 1, v75\nQmava 200,16,136\ns_waitcnt vmcnt(21)\nds_write_b32 v73, v75\n"
"s_waitcnt lgkmcnt(5)\nQmvva 92,20,140\nQtv 0,64,38912\ns_add_u32 s88, s71, 32768\nQmvva 124,24,140\nbuffer_store_dwordx4 v[224:227], v60, s[56:59], s88 offen offset:0 sc1\nQmvva 156,28,140\nQtv 2,64,44032\n"
"Qmvva 188,32,140\nQtv 4,64,39168\nQmvva 220,36,140\nQmvva 252,20,144\nQtv 6,64,44288\nQmvva 100,24,144\nQtv 8,64,39424\nQmvva 132,28,144\n"
"ds_read_b32 v69, v74\nQmvva 164,32,144\nQtv 10,64,44544\nQmvva 196,36,144\nQmava 40,20,148\nQtv 12,64,39680\nv_add_u32 v67, 3, v65\nQmava 44,24,148\n"
"s_waitcnt lgkmcnt(2)\nv_cmp_gt_u32 vcc, v75, v69\nQmava 48,28,148\ns_cbranch_vccnz .Lso19\n.Lro19:\nQmava 52,32,148\nbuffer_store_dword v67, v77, s[24:27], 0 offen sc1\ns_add_u32 m0, s91, 0\n"
"s_mul_i32 s94, s31, 25\nQmava 56,36,148\nbuffer_load_dwordx4 v70, s[48:51], s94 offen lds\nQmava 60,20,152\nQtv 14,64,44800\nQmava 64,24,152\nQtv 16,64,39936\nQmava 68,28,152\n"
"v_accvgpr_read_b32 v228, a40\nv_accvgpr_read_b32 v229, a41\nQmava 72,32,152\nbuffer_load_dwordx4 v71, s[48:51], s94 offen offset:1024 lds\nQmava 76,36,152\nQtv 18,64,45056\nv_accvgpr_read_b32 v230, a42\nQmava 80,20,156\n"
"Qda 112,48,24576\nv_accvgpr_read_b32 v231, a43\nQmava 84,24,156\nQda 116,48,25600\ns_mul_i32 s95, s90, 25\nQmava 88,28,156\nbuffer_load_dwordx4 v72, s[52:55], s95 offen offset:2048 lds\nQmava 92,32,156\n"
"v_accvgpr_read_b32 v108, a44\nv_accvgpr_read_b32 v109, a45\nQmava 96,36,156\nQda 120,48,26624\nv_accvgpr_read_b32 v110, a46\nQmava 100,20,160\nQda 124,48,27648\nv_accvgpr_read_b32 v111, a47\n"
"Qmava 104,24,160\nbuffer_load_dwordx4 v61, s[52:55], s95 offen offset:3072 lds\nQmava 108,28,160\nQda 128,48,28672\nQmava 176,32,160\nQmava 180,36,160\nQda 132,48,29696\ns_add_u32 m0, s91, 4096\n"
"Qmava 184,20,164\nbuffer_load_dwordx4 v62, s[52:55], s95 offen lds\nQmava 188,24,164\nQda 136,48,30720\nQmava 192,28,164\nQmava 196,32,164\nbuffer_load_dwordx4 v63, s[52:55], s95 offen offset:1024 lds\nv_add_u32 v75, 1, v75\n"
"Qmava 200,36,164\ns_waitcnt vmcnt(22)\nds_write_b32 v73, v75\ns_waitcnt lgkmcnt(5)\nQmvva 92,0,112\nQtv 20,59,6144\nQmvva 124,4,112\nbuffer_store_dwordx4 v[148:151], v60, s[56:59], s88 offen offset:1024 sc1\n"
"Qmvva 156,8,112\nQtv 22,59,11264\nQmvva 188,12,112\nQtv 24,59,6400\nQmvva 220,16,112\nQmvva 252,0,116\nQtv 26,59,11520\nQmvva 100,4,116\n"
"Qtv 28,59,6656\nQmvva 132,8,116\nds_read_b32 v69, v74\nQmvva 164,12,116\nQmvva 196,16,116\nQtv 30,59,11776\nQmvva 228,0,120\nQtv 32,59,6912\n"
"Qmvva 108,4,120\ns_waitcnt lgkmcnt(2)\nv_cmp_gt_u32 vcc, v75, v69\nQmava 48,8,120\ns_cbranch_vccnz .Lso20\n.Lro20:\nQmava 52,12,120\nQtv 34,59,12032\n"
"s_add_u32 m0, s91, 24576\ns_mul_i32 s94, s31, 26\nQmava 56,16,120\nbuffer_load_dwordx4 v70, s[48:51], s94 offen lds\nQmava 60,0,124\nQmava 64,4,124\nQtv 36,59,7168\nQmava 68,8,124\n"
"Qtv 38,59,12288\nQmava 72,12,124\nbuffer_load_dwordx4 v71, s[48:51], s94 offen offset:1024 lds\nQmava 76,16,124\nv_accvgpr_read_b32 v140, a48\nv_accvgpr_read_b32 v141, a49\nQmava 80,0,128\nQda 140,48,49152\n"
"v_accvgpr_read_b32 v142, a50\nQmava 84,4,128\nQda 144,48,50176\ns_mul_i32 s95, s90, 26\nQmava 88,8,128\nbuffer_load_dwordx4 v72, s[52:55], s95 offen offset:2048 lds\nQmava 92,12,128\nQda 148,48,51200\n"
"v_accvgpr_read_b32 v143, a51\nQmava 96,16,128\nv_accvgpr_read_b32 v172, a52\nv_accvgpr_read_b32 v173, a53\nQmava 100,0,132\nQda 152,48,52224\nv_accvgpr_read_b32 v174, a54\nQmava 104,4,132\n"
"buffer_load_dwordx4 v61, s[52:55], s95 offen offset:3072 lds\nQmava 108,8,132\nQda 156,48,53248\nv_accvgpr_read_b32 v175, a55\nQmava 176,12,132\nQmava 180,16,132\nQda 160,48,54272\ns_add_u32 m0, s91, 28672\n"
"Qmava 184,0,136\nbuffer_load_dwordx4 v62, s[52:55], s95 offen lds\nQmava 188,4,136\nQda 164,48,55296\nQmava 192,8,136\nQmava 196,12,136\nbuffer_load_dwordx4 v63, s[52:55], s95 offen offset:1024 lds\nv_add_u32 v75, 1, v75\n"
"Qmava 200,16,136\ns_waitcnt vmcnt(22)\nds_write_b32 v73, v75\ns_waitcnt lgkmcnt(5)\nQmvva 92,20,140\nQtv 0,59,30720\nQmvva 124,24,140\nbuffer_store_dwordx4 v[180:183], v60, s[56:59], s88 offen offset:2048 sc1\n"
"Qmvva 156,28,140\nQtv 2,59,35840\nQmvva 188,32,140\nQtv 4,59,30976\nQmvva 220,36,140\nQmvva 252,20,144\nQtv 6,59,36096\nQmvva 100,24,144\n"
"Qtv 8,59,31232\nQmvva 132,28,144\nds_read_b32 v69, v74\nQmvva 164,32,144\nQmvva 196,36,144\nQtv 10,59,36352\nQmvva 228,20,148\nQtv 12,59,31488\n"
"Qmvva 108,24,148\ns_waitcnt lgkmcnt(2)\nv_cmp_gt_u32 vcc, v75, v69\nQmvva 140,28,148\ns_cbranch_vccnz .Lso21\n.Lro21:\nQmvva 172,32,148\nQtv 14,59,36608\n"
"s_add_u32 m0, s91, 49152\ns_mul_i32 s94, s31, 27\nQmava 56,36,148\nbuffer_load_dwordx4 v70, s[48:51], s94 offen lds\nQmava 60,20,152\nQmava 64,24,152\nQtv 16,59,31744\nQmava 68,28,152\n"
"Qtv 18,59,36864\nQmava 72,32,152\nbuffer_load_dwordx4 v71, s[48:51], s94 offen offset:1024 lds\nQmava 76,36,152\nQmava 80,20,156\nQda 112,49,0\nQmava 84,24,156\nQda 116,49,1024\n"
"s_mul_i32 s95, s90, 27\nQmava 88,28,156\nbuffer_load_dwordx4 v72, s[52:55], s95 offen offset:2048 lds\nQmava 92,32,156\nQda 120,49,2048\nv_accvgpr_read_b32 v204, a56\nQmava 96,36,156\nv_accvgpr_read_b32 v205, a57\n"
"v_accvgpr_read_b32 v206, a58\nQmava 100,20,160\nQda 124,49,3072\nv_accvgpr_read_b32 v207, a59\nQmava 104,24,160\nbuffer_load_dwordx4 v61, s[52:55], s95 offen offset:3072 lds\nQmava 108,28,160\nQda 128,49,4096\n"
"v_accvgpr_read_b32 v236, a60\nQmava 176,32,160\nv_accvgpr_read_b32 v237, a61\nv_accvgpr_read_b32 v238, a62\nQmava 180,36,160\nQda 132,49,5120\ns_add_u32 m0, s91, 53248\nQmava 184,20,164\n"
"buffer_load_dwordx4 v62, s[52:55], s95 offen lds\nQmava 188,24,164\nQda 136,49,6144\nv_accvgpr_read_b32 v239, a63\nQmava 192,28,164\nQmava 196,32,164\nbuffer_load_dwordx4 v63, s[52:55], s95 offen offset:1024 lds\nv_add_u32 v75, 1, v75\n"
"Qmava 200,36,164\ns_waitcnt vmcnt(22)\nds_write_b32 v73, v75\ns_waitcnt lgkmcnt(5)\nQmvva 92,0,112\nQtv 20,59,55296\ns_add_u32 s88, s71, 43008\nQmvva 124,4,112\n"
"buffer_store_dwordx4 v[104:107], v60, s[56:59], s88 offen offset:0 sc1\nQmvva 156,8,112\nQtv 22,59,60416\nQmvva 188,12,112\nQtv 24,59,55552\nQmvva 220,16,112\nQmvva 252,0,116\nQtv 26,59,60672\n"
"Qmvva 100,4,116\nQtv 28,59,55808\nQmvva 132,8,116\nds_read_b32 v69, v74\nQmvva 164,12,116\nQmvva 196,16,116\nQtv 30,59,60928\nQmvva 228,0,120\n"
"Qtv 32,59,56064\nQmvva 108,4,120\ns_waitcnt lgkmcnt(2)\nv_cmp_gt_u32 vcc, v75, v69\nQmvva 140,8,120\ns_cbranch_vccnz .Lso22\n.Lro22:\nQmvva 172,12,120\n"
"Qtv 34,59,61184\ns_add_u32 m0, s91, 73728\ns_mul_i32 s94, s31, 28\nQmvva 204,16,120\nbuffer_load_dwordx4 v70, s[48:51], s94 offen lds\nQmvva 236,0,124\nQmava 64,4,124\nQtv 36,59,56320\n"
"Qmava 68,8,124\nQtv 38,59,61440\nQmava 72,12,124\nbuffer_load_dwordx4 v71, s[48:51], s94 offen offset:1024 lds\nQmava 76,16,124\nQmava 80,0,128\nQda 140,49,24576\nQmava 84,4,128\n"
"Qda 144,49,25600\ns_mul_i32 s95, s90, 28\nQmava 88,8,128\nbuffer_load_dwordx4 v72, s[52:55], s95 offen offset:2048 lds\nQmava 92,12,128\nQda 148,49,26624\nv_accvgpr_read_b32 v80, a64\nQmava 96,16,128\n"
"v_accvgpr_read_b32 v81, a65\nv_accvgpr_read_b32 v82, a66\nQmava 100,0,132\nQda 152,49,27648\nv_accvgpr_read_b32 v83, a67\nQmava 104,4,132\nbuffer_load_dwordx4 v61, s[52:55], s95 offen offset:3072 lds\nQmava 108,8,132\n"
"Qda 156,49,28672\nv_accvgpr_read_b32 v112, a68\nQmava 176,12,132\nv_accvgpr_read_b32 v113, a69\nv_accvgpr_read_b32 v114, a70\nQmava 180,16,132\nQda 160,49,29696\ns_add_u32 m0, s91, 77824\n"
"Qmava 184,0,136\nbuffer_load_dwordx4 v62, s[52:55], s95 offen lds\nQmava 188,4,136\nQda 164,49,30720\nv_accvgpr_read_b32 v115, a71\nQmava 192,8,136\nQmava 196,12,136\nbuffer_load_dwordx4 v63, s[52:55], s95 offen offset:1024 lds\n"
"v_add_u32 v75, 1, v75\nQmava 200,16,136\ns_waitcnt vmcnt(21)\nds_write_b32 v73, v75\ns_waitcnt lgkmcnt(5)\nQmvva 92,20,140\nbuffer_load_dword v78, v76, s[24:27], 0 offen sc1\nQmvva 124,24,140\n"
"buffer_store_dwordx4 v[136:139], v60, s[56:59], s88 offen offset:1024 sc1\nQmvva 156,28,140\nQtv 0,50,31744\nQmvva 188,32,140\nQtv 2,50,36864\nQmvva 220,36,140\nQtv 4,50,32000\nQmvva 252,20,144\n"
"Qmvva 100,24,144\nQtv 6,50,37120\nQmvva 132,28,144\nds_read_b32 v69, v74\nQmvva 164,32,144\nQtv 8,50,32256\nQmvva 196,36,144\nQtv 10,50,37376\n"
"Qmvva 228,20,148\nQmvva 108,24,148\ns_waitcnt lgkmcnt(2)\nv_cmp_gt_u32 vcc, v75, v69\nQmvva 140,28,148\ns_cbranch_vccnz .Lso23\n.Lro23:\nQmvva 172,32,148\n"
"Qtv 12,50,32512\ns_add_u32 m0, s91, 98304\ns_mul_i32 s94, s31, 29\n.Lpg17:\ns_cbranch_execz .Lpg21\nQmvva 204,36,148\nbuffer_load_dwordx4 v70, s[48:51], s94 offen lds\nQmvva 236,20,152\n"
"Qtv 14,50,37632\nQmvva 80,24,152\nQtv 16,50,32768\nQmvva 112,28,152\nQmava 72,32,152\nbuffer_load_dwordx4 v71, s[48:51], s94 offen offset:1024 lds\nQmava 76,36,152\nQtv 18,50,37888\n"
"Qmava 80,20,156\nQda 112,49,58368\nQmava 84,24,156\nQda 116,49,59392\ns_mul_i32 s95, s90, 29\nQmava 88,28,156\nbuffer_load_dwordx4 v72, s[52:55], s95 offen offset:2048 lds\nQmava 92,32,156\n"
"Qmava 96,36,156\nQda 120,49,60416\nQmava 100,20,160\nQda 124,49,61440\nv_accvgpr_read_b32 v144, a72\nQmava 104,24,160\nbuffer_load_dwordx4 v61, s[52:55], s95 offen offset:3072 lds\nQmava 108,28,160\n"
"Qda 128,49,62464\nv_accvgpr_read_b32 v145, a73\nQmava 176,32,160\nv_accvgpr_read_b32 v146, a74\nv_accvgpr_read_b32 v147, a75\nQmava 180,36,160\nQda 132,49,63488\ns_add_u32 m0, s91, 102400\n"
"Qmava 184,20,164\nbuffer_load_dwordx4 v62, s[52:55], s95 offen lds\nQmava 188,24,164\nQda 136,49,64512\nv_accvgpr_read_b32 v176, a76\nQmava 192,28,164\nv_accvgpr_read_b32 v177, a77\nv_accvgpr_read_b32 v178, a78\n"
"Qmava 196,32,164\nbuffer_load_dwordx4 v63, s[52:55], s95 offen offset:1024 lds\nv_add_u32 v75, 1, v75\nQmava 200,36,164\ns_waitcnt vmcnt(22)\nds_write_b32 v73, v75\ns_waitcnt lgkmcnt(5)\nQmvva 92,0,112\n"
"Qtv 20,64,14336\nv_accvgpr_read_b32 v179, a79\nQmvva 124,4,112\nbuffer_store_dwordx4 v[168:171], v60, s[56:59], s88 offen offset:2048 sc1\nQmvva 156,8,112\nQtv 22,64,19456\nQmvva 188,12,112\nQtv 24,64,14592\n"
"Qmvva 220,16,112\nQmvva 252,0,116\nQtv 26,64,19712\nQmvva 100,4,116\nQtv 28,64,14848\nQmvva 132,8,116\nds_read_b32 v69, v74\nQmvva 164,12,116\n"
"Qmvva 196,16,116\nQtv 30,64,19968\nQmvva 228,0,120\nQtv 32,64,15104\nQmvva 108,4,120\ns_waitcnt lgkmcnt(2)\nv_cmp_gt_u32 vcc, v75, v69\nQmvva 140,8,120\n"
"s_cbranch_vccnz .Lso24\n.Lro24:\nQmvva 172,12,120\nQtv 34,64,20224\ns_add_u32 m0, s91, 132096\ns_mul_i32 s94, s31, 30\nQmvva 204,16,120\nbuffer_load_dwordx4 v70, s[48:51], s94 offen lds\n"
"Qmvva 236,0,124\nQmvva 80,4,124\nQtv 36,64,15360\nQmvva 112,8,124\nQtv 38,64,20480\nQmvva 144,12,124\nbuffer_load_dwordx4 v71, s[48:51], s94 offen offset:1024 lds\nQmvva 176,16,124\n"
"Qmava 80,0,128\nQda 140,48,0\nQmava 84,4,128\nQda 144,48,1024\ns_mul_i32 s95, s90, 30\nQmava 88,8,128\nbuffer_load_dwordx4 v72, s[52:55], s95 offen offset:2048 lds\nQmava 92,12,128\n"
"Qda 148,48,2048\nQmava 96,16,128\nQmava 100,0,132\nQda 152,48,3072\nQmava 104,4,132\nbuffer_load_dwordx4 v61, s[52:55], s95 offen offset:3072 lds\nQmava 108,8,132\nQda 156,48,4096\n"
"v_accvgpr_read_b32 v208, a80\nQmava 176,12,132\nv_accvgpr_read_b32 v209, a81\nv_accvgpr_read_b32 v210, a82\nQmava 180,16,132\nQda 160,48,5120\ns_add_u32 m0, s91, 136192\nQmava 184,0,136\n"
"buffer_load_dwordx4 v62, s[52:55], s95 offen lds\nQmava 188,4,136\nQda 164,48,6144\nv_accvgpr_read_b32 v211, a83\nQmava 192,8,136\nv_accvgpr_read_b32 v240, a84\nv_accvgpr_read_b32 v241, a85\nQmava 196,12,136\n"
"buffer_load_dwordx4 v63, s[52:55], s95 offen offset:1024 lds\nv_add_u32 v75, 1, v75\nQmava 200,16,136\ns_waitcnt vmcnt(22)\nds_write_b32 v73, v75\ns_waitcnt lgkmcnt(5)\nQmvva 92,20,140\ns_waitcnt vmcnt(14)\n"
"v_sub_u32 v66, v78, v65\nv_cmp_gt_u32 vcc, 2, v66\ns_and_b64 vcc, vcc, s[28:29]\ns_cbranch_scc1 .Lf2_w\n.Lf2_r:\nQmvva 124,24,140\nbuffer_store_dwordx4 v[200:203], v60, s[56:59], s88 offen offset:3072 sc1\nQmvva 156,28,140\n"
"Qtv 0,64,38912\nv_accvgpr_read_b32 v242, a86\nQmvva 188,32,140\nQtv 2,64,44032\nv_accvgpr_read_b32 v243, a87\nQmvva 220,36,140\nQtv 4,64,39168\nQmvva 252,20,144\n"
"Qmvva 100,24,144\nQtv 6,64,44288\nQmvva 132,28,144\nds_read_b32 v69, v74\nQmvva 164,32,144\nQtv 8,64,39424\nQmvva 196,36,144\nQtv 10,64,44544\n"
"Qmvva 228,20,148\nQmvva 108,24,148\ns_waitcnt lgkmcnt(2)\nv_cmp_gt_u32 vcc, v75, v69\nQmvva 140,28,148\ns_cbranch_vccnz .Lso25\n.Lro25:\nQmvva 172,32,148\n"
"Qtv 12,64,39680\ns_add_u32 m0, s91, 0\ns_mul_i32 s94, s31, 31\nQmvva 204,36,148\nbuffer_load_dwordx4 v70, s[48:51], s94 offen lds\nQmvva 236,20,152\nQtv 14,64,44800\nQmvva 80,24,152\n"
"Qtv 16,64,39936\nQmvva 112,28,152\nQmvva 144,32,152\nbuffer_load_dwordx4 v71, s[48:51], s94 offen offset:1024 lds\nQmvva 176,36,152\nQtv 18,64,45056\nQmvva 208,20,156\nQda 112,48,24576\n"
"s_lshr_b32 s68, %19, 2\nQmvva 240,24,156\nQda 116,48,25600\ns_mul_i32 s95, s90, 31\ns_mul_i32 s68, s68, 917504\nQmava 88,28,156\nbuffer_load_dwordx4 v72, s[52:55], s95 offen offset:2048 lds\ns_mul_i32 s50, s92, 7168\n"
"Qmava 92,32,156\ns_mul_i32 s69, s93, 57344\nQmava 96,36,156\nQda 120,48,26624\ns_add_u32 s50, s50, s69\nQmava 100,20,160\nQda 124,48,27648\ns_add_u32 s68, s68, s50\n"
"Qmava 104,24,160\nbuffer_load_dwordx4 v61, s[52:55], s95 offen offset:3072 lds\ns_add_u32 s48, %8, s68\ns_addc_u32 s49, %9, 0\nQmava 108,28,160\nQda 128,48,28672\ns_and_b32 s49, s49, 0xffff\nQmava 176,32,160\n"
"s_sub_u32 s50, 917504, s50\nQmava 180,36,160\nQda 132,48,29696\ns_add_u32 m0, s91, 4096\nQmava 184,20,164\nbuffer_load_dwordx4 v62, s[52:55], s95 offen lds\nQmava 188,24,164\nQda 136,48,30720\n"
"v_accvgpr_read_b32 v84, a88\nQmava 192,28,164\nv_accvgpr_read_b32 v85, a89\nv_accvgpr_read_b32 v86, a90\nQmava 196,32,164\nbuffer_load_dwordx4 v63, s[52:55], s95 offen offset:1024 lds\nv_add_u32 v75, 1, v75\nQmava 200,36,164\n"
"s_waitcnt vmcnt(22)\nds_write_b32 v73, v75\ns_mov_b32 s52, s62\ns_mov_b32 s53, s63\ns_mov_b32 s54, s86\ns_waitcnt lgkmcnt(5)\nQmvva 92,0,112\nQtv 20,59,6144\n"
"s_add_u32 s88, s71, 47104\nQmvva 124,4,112\nbuffer_store_dwordx4 v[232:235], v60, s[56:59], s88 offen offset:0 sc1\ns_mov_b32 s94, 458752\nQmvva 156,8,112\nbuffer_load_dwordx4 a[208:211], v60, s[48:51], s94 offen offset:0 sc1\nQmvva 188,12,112\nQtv 22,59,11264\n"
"v_accvgpr_read_b32 v87, a91\nQmvva 220,16,112\nQtv 24,59,6400\nv_accvgpr_read_b32 v88, a92\nQmvva 252,0,116\nQtv 26,59,11520\nv_accvgpr_read_b32 v89, a93\nQmvva 100,4,116\n"
"v_accvgpr_read_b32 v90, a94\nv_accvgpr_read_b32 v91, a95\nQmvva 132,8,116\nds_read_b32 v69, v74\nQmvva 164,12,116\nQtv 28,59,6656\nQmvva 196,16,116\nQtv 30,59,11776\n"
"Qmvva 228,0,120\nQtv 32,59,6912\nv_add_u32 v67, 4, v65\nQmvva 108,4,120\ns_waitcnt lgkmcnt(3)\nv_cmp_gt_u32 vcc, v75, v69\nQmvva 140,8,120\ns_cbranch_vccnz .Lso26\n"
".Lro26:\nQmvva 172,12,120\nbuffer_load_dwordx4 a[212:215], v60, s[48:51], s94 offen offset:1024 sc1\nQmvva 204,16,120\nbuffer_store_dword v67, v77, s[24:27], 0 offen sc1\ns_add_u32 m0, s96, 24576\ns_mov_b32 s95, 1310720\nQmvva 236,0,124\n"
"buffer_load_dwordx4 v40, s[52:55], s95 offen lds\nQmvva 80,4,124\nQmvva 112,8,124\nQtv 34,59,12032\nQmvva 144,12,124\nQtv 36,59,7168\nQmvva 176,16,124\nQtv 38,59,12288\n"
"Qmvva 208,0,128\nQda 140,48,49152\nQmvva 240,4,128\nbuffer_load_dwordx4 a[216:219], v60, s[48:51], s94 offen offset:2048 sc1\nQmvva 84,8,128\nbuffer_load_dwordx4 v41, s[52:55], s95 offen offset:1024 lds\nQmvva 88,12,128\nQmava 96,16,128\n"
"Qda 144,48,50176\nQmava 100,0,132\nQda 148,48,51200\nQmava 104,4,132\nQda 152,48,52224\nQmava 108,8,132\nQmava 176,12,132\nQda 156,48,53248\n"
"Qmava 180,16,132\nbuffer_load_dwordx4 v42, s[52:55], s95 offen offset:2048 lds\nQmava 184,0,136\nQda 160,48,54272\nQmava 188,4,136\nbuffer_load_dwordx4 a[220:223], v60, s[48:51], s94 offen offset:3072 sc1\nQmava 192,8,136\nQda 164,48,55296\n"
"v_accvgpr_read_b32 v120, a96\nQmava 196,12,136\nv_add_u32 v75, 1, v75\nv_accvgpr_read_b32 v121, a97\nQmava 200,16,136\ns_waitcnt vmcnt(23)\nds_write_b32 v73, v75\ns_waitcnt lgkmcnt(5)\n"
"Qmvva 92,20,140\nQtv 0,59,30720\nv_accvgpr_read_b32 v122, a98\nQmvva 124,24,140\nbuffer_store_dwordx4 v[212:215], v60, s[56:59], s88 offen offset:1024 sc1\nQmvva 156,28,140\nbuffer_load_dwordx4 v43, s[52:55], s95 offen offset:3072 lds\nQmvva 188,32,140\n"
"Qtv 2,59,35840\nv_accvgpr_read_b32 v123, a99\nQmvva 220,36,140\nQtv 4,59,30976\nv_accvgpr_read_b32 v152, a100\nQmvva 252,20,144\nQtv 6,59,36096\ns_mov_b32 s94, 462848\n"
"Qmvva 100,24,144\nbuffer_load_dwordx4 a[224:227], v60, s[48:51], s94 offen offset:0 sc1\nQmvva 132,28,144\nds_read_b32 v69, v74\nv_accvgpr_read_b32 v153, a101\nQmvva 164,32,144\ns_add_u32 m0, s96, 28672\nv_accvgpr_read_b32 v154, a102\n"
"Qmvva 196,36,144\nbuffer_load_dwordx4 v44, s[52:55], s95 offen lds\nQmvva 228,20,148\nQtv 8,59,31232\nv_accvgpr_read_b32 v155, a103\nQmvva 108,24,148\ns_waitcnt lgkmcnt(1)\nv_cmp_gt_u32 vcc, v75, v69\n"
"Qmvva 140,28,148\ns_cbranch_vccnz .Lso27\n.Lro27:\nQmvva 172,32,148\nQtv 10,59,36352\nQmvva 204,36,148\nQtv 12,59,31488\nQmvva 236,20,152\n"
"Qmvva 80,24,152\nbuffer_load_dwordx4 a[228:231], v60, s[48:51], s94 offen offset:1024 sc1\ns_add_u32 m0, s96, 45056\ns_mov_b32 s95, 1638400\nQmvva 112,28,152\nbuffer_load_dwordx4 v40, s[52:55], s95 offen lds\nQmvva 144,32,152\nQtv 14,59,36608\n"
"Qmvva 176,36,152\nQtv 16,59,31744\nQmvva 208,20,156\nQtv 18,59,36864\nQmvva 240,24,156\nQda 112,49,0\nQmvva 84,28,156\nQmvva 88,32,156\n"
"Qda 116,49,1024\nQmvva 120,36,156\nbuffer_load_dwordx4 v41, s[52:55], s95 offen offset:1024 lds\nQmvva 152,20,160\nQda 120,49,2048\nQmava 104,24,160\nbuffer_load_dwordx4 a[232:235], v60, s[48:51], s94 offen offset:2048 sc1\nQmava 108,28,160\n"
"Qda 124,49,3072\nQmava 176,32,160\nQmava 180,36,160\nQda 128,49,4096\nQmava 184,20,164\nQda 132,49,5120\nQmava 188,24,164\nbuffer_load_dwordx4 v42, s[52:55], s95 offen offset:2048 lds\n"
"Qmava 192,28,164\nQda 136,49,6144\nQmava 196,32,164\nv_add_u32 v75, 1, v75\nv_accvgpr_read_b32 v184, a104\nQmava 200,36,164\ns_waitcnt vmcnt(25)\nds_write_b32 v73, v75\n"
"s_waitcnt lgkmcnt(5)\nQmvva 92,0,112\nQtv 20,59,55296\nv_accvgpr_read_b32 v185, a105\nQmvva 124,4,112\nbuffer_store_dwordx4 v[244:247], v60, s[56:59], s88 offen offset:2048 sc1\ns_mov_b32 s94, 573440\nQmvva 156,8,112\n"
"buffer_load_dwordx4 a[236:239], v60, s[48:51], s94 offen offset:0 sc1\nQmvva 188,12,112\nbuffer_load_dwordx4 v43, s[52:55], s95 offen offset:3072 lds\nQmvva 220,16,112\nQtv 22,59,60416\nv_accvgpr_read_b32 v186, a106\nQmvva 252,0,116\nQtv 24,59,55552\n"
"v_accvgpr_read_b32 v187, a107\nQmvva 100,4,116\nQtv 26,59,60672\nv_accvgpr_read_b32 v216, a108\nQmvva 132,8,116\nds_read_b32 v69, v74\nv_accvgpr_read_b32 v217, a109\nQmvva 164,12,116\n"
"Qtv 28,59,55808\nv_accvgpr_read_b32 v218, a110\nQmvva 196,16,116\ns_add_u32 m0, s96, 49152\nv_accvgpr_read_b32 v219, a111\nQmvva 228,0,120\nbuffer_load_dwordx4 v44, s[52:55], s95 offen lds\nQmvva 108,4,120\n"
"s_waitcnt lgkmcnt(1)\nv_cmp_gt_u32 vcc, v75, v69\nQmvva 140,8,120\ns_cbranch_vccnz .Lso28\n.Lro28:\nQmvva 172,12,120\nbuffer_load_dwordx4 a[240:243], v60, s[48:51], s94 offen offset:1024 sc1\nQmvva 204,16,120\n"
"Qtv 30,59,60928\nQmvva 236,0,124\nQtv 32,59,56064\nQmvva 80,4,124\nQtv 34,59,61184\ns_add_u32 m0, s96, 65536\ns_mov_b32 s95, 1966080\nQmvva 112,8,124\n"
"buffer_load_dwordx4 v40, s[52:55], s95 offen lds\nQmvva 144,12,124\nQtv 36,59,56320\nQmvva 176,16,124\nQmvva 208,0,128\nQtv 38,59,61440\nQmvva 240,4,128\nbuffer_load_dwordx4 a[244:247], v60, s[48:51], s94 offen offset:2048 sc1\n"
"Qmvva 84,8,128\nQda 140,49,24576\nQmvva 88,12,128\nQda 144,49,25600\nQmvva 120,16,128\nbuffer_load_dwordx4 v41, s[52:55], s95 offen offset:1024 lds\nQmvva 152,0,132\nQda 148,49,26624\n"
"Qmvva 184,4,132\nQmvva 216,8,132\nQda 152,49,27648\nQmava 176,12,132\nQda 156,49,28672\nQmava 180,16,132\nQda 160,49,29696\nQmava 184,0,136\n"
"Qda 164,49,30720\nQmava 188,4,136\nbuffer_load_dwordx4 a[248:251], v60, s[48:51], s94 offen offset:3072 sc1\nQmava 192,8,136\nbuffer_load_dwordx4 v42, s[52:55], s95 offen offset:2048 lds\nQmava 196,12,136\nv_add_u32 v75, 1, v75\nQmava 200,16,136\n"
"s_waitcnt vmcnt(28)\nds_write_b32 v73, v75\ns_waitcnt lgkmcnt(5)\nQmvva 92,20,140\nQtv 0,50,31744\nv_accvgpr_read_b32 v248, a176\nQmvva 124,24,140\nQtv 2,50,36864\n"
"v_accvgpr_read_b32 v249, a177\nQmvva 156,28,140\nQtv 4,50,32000\nv_accvgpr_read_b32 v250, a178\nQmvva 188,32,140\nv_accvgpr_read_b32 v251, a179\nv_accvgpr_read_b32 v116, a180\nQmvva 220,36,140\n"
"buffer_load_dwordx4 v43, s[52:55], s95 offen offset:3072 lds\nQmvva 252,20,144\nQtv 6,50,37120\ns_mov_b32 s94, 577536\nQmvva 100,24,144\nbuffer_load_dwordx4 a[252:255], v60, s[48:51], s94 offen offset:0 sc1\nQmvva 132,28,144\nds_read_b32 v69, v74\n"
"v_accvgpr_read_b32 v117, a181\nQmvva 164,32,144\nQtv 8,50,32256\nv_accvgpr_read_b32 v118, a182\nQmvva 196,36,144\nv_accvgpr_read_b32 v119, a183\nQmvva 228,20,148\nQtv 10,50,37376\n"
"Qmvva 108,24,148\ns_waitcnt lgkmcnt(2)\nv_cmp_gt_u32 vcc, v75, v69\nQmvva 140,28,148\ns_add_u32 m0, s96, 69632\ns_cbranch_vccnz .Lso29\n.Lro29:\nQmvva 172,32,148\n"
"buffer_load_dwordx4 v44, s[52:55], s95 offen lds\nQmvva 204,36,148\nQtv 12,50,32512\nQmvva 236,20,152\nQtv 14,50,37632\nQmvva 80,24,152\nbuffer_load_dwordx4 a[168:171], v60, s[48:51], s94 offen offset:1024 sc1\nQmvva 112,28,152\n"
"Qmvva 144,32,152\nQtv 16,50,32768\nQmvva 176,36,152\nQtv 18,50,37888\ns_add_u32 m0, s96, 86016\ns_mov_b32 s95, 2293760\nQmvva 208,20,156\nbuffer_load_dwordx4 v40, s[52:55], s95 offen lds\n"
"Qmvva 240,24,156\nQmvva 84,28,156\nQda 112,49,58368\nQmvva 88,32,156\nQda 116,49,59392\nQmvva 120,36,156\nQda 120,49,60416\nQmvva 152,20,160\n"
"Qmvva 184,24,160\nbuffer_load_dwordx4 a[172:175], v60, s[48:51], s94 offen offset:2048 sc1\nQmvva 216,28,160\nbuffer_load_dwordx4 v41, s[52:55], s95 offen offset:1024 lds\nQmvva 248,32,160\nQda 124,49,61440\nQmvva 116,36,160\nQda 128,49,62464\n"
"Qmava 184,20,164\nQmava 188,24,164\nQda 132,49,63488\nQmava 192,28,164\nQda 136,49,64512\nQmava 196,32,164\nv_add_u32 v75, 1, v75\nQmava 200,36,164\n"
".Lpg18:\ns_cbranch_execz .Lpg22\ns_waitcnt vmcnt(22)\nds_write_b32 v73, v75\ns_waitcnt lgkmcnt(5)\nQmvva 92,0,112\nbuffer_load_dwordx4 v42, s[52:55], s95 offen offset:2048 lds\ns_mov_b32 s94, 688128\n"
"Qmvva 124,4,112\nbuffer_load_dwordx4 a[0:3], v60, s[48:51], s94 offen offset:0 sc1\nQmvva 156,8,112\nQtv 20,64,14336\nv_accvgpr_read_b32 v96, a184\nQmvva 188,12,112\nQtv 22,64,19456\nv_accvgpr_read_b32 v97, a185\n"
"Qmvva 220,16,112\nQtv 24,64,14592\nv_accvgpr_read_b32 v98, a186\nQmvva 252,0,116\nQtv 26,64,19712\nv_accvgpr_read_b32 v99, a187\nQmvva 100,4,116\nQtv 28,64,14848\n"
"v_accvgpr_read_b32 v128, a188\nQmvva 132,8,116\nds_read_b32 v69, v74\nv_accvgpr_read_b32 v129, a189\nQmvva 164,12,116\nbuffer_load_dwordx4 v43, s[52:55], s95 offen offset:3072 lds\nQmvva 196,16,116\nQtv 30,64,19968\n"
"v_accvgpr_read_b32 v130, a190\nQmvva 228,0,120\nv_accvgpr_read_b32 v131, a191\nQmvva 108,4,120\ns_waitcnt lgkmcnt(1)\nv_cmp_gt_u32 vcc, v75, v69\nQmvva 140,8,120\ns_cbranch_vccnz .Lso30\n"
".Lro30:\nQmvva 172,12,120\nbuffer_load_dwordx4 a[4:7], v60, s[48:51], s94 offen offset:1024 sc1\nQmvva 204,16,120\ns_waitcnt vmcnt(37)\nds_write_b128 v52, a[208:211] offset:0\nQmvva 236,0,124\ns_add_u32 m0, s96, 90112\n"
"Qmvva 80,4,124\nbuffer_load_dwordx4 v44, s[52:55], s95 offen lds\nQmvva 112,8,124\nQtv 32,64,15104\nQmvva 144,12,124\nQtv 34,64,20224\nQmvva 176,16,124\nQtv 36,64,15360\n"
"Qmvva 208,0,128\nQtv 38,64,20480\nQmvva 240,4,128\nbuffer_load_dwordx4 a[8:11], v60, s[48:51], s94 offen offset:2048 sc1\nQmvva 84,8,128\nQda 140,48,0\nQmvva 88,12,128\nQda 144,48,1024\n"
"Qmvva 120,16,128\ns_waitcnt vmcnt(38)\nds_write_b128 v52, a[212:215] offset:4096\nQmvva 152,0,132\nQmvva 184,4,132\nQmvva 216,8,132\nQda 148,48,2048\nQmvva 248,12,132\n"
"Qda 152,48,3072\nQmvva 116,16,132\nQda 156,48,4096\nQmvva 96,0,136\nQda 160,48,5120\nQmvva 128,4,136\nbuffer_load_dwordx4 a[12:15], v60, s[48:51], s94 offen offset:3072 sc1\nQmava 192,8,136\n"
"Qda 164,48,6144\nQmava 196,12,136\nv_add_u32 v75, 1, v75\nQmava 200,16,136\nds_write_b32 v73, v75\ns_waitcnt lgkmcnt(5)\nQmvva 92,20,140\ns_waitcnt vmcnt(36)\n"
"ds_write_b128 v52, a[216:219] offset:8192\nQmvva 124,24,140\nQmvva 156,28,140\nbuffer_load_dword v78, v76, s[24:27], 0 offen sc1\nQmvva 188,32,140\nQtv 0,45,20480\nQmvva 220,36,140\nQtv 2,45,25600\n"
"v_accvgpr_read_b32 v160, a192\nQmvva 252,20,144\nQtv 4,45,20736\ns_mov_b32 s94, 692224\nQmvva 100,24,144\nbuffer_load_dwordx4 a[16:19], v60, s[48:51], s94 offen offset:0 sc1\nQmvva 132,28,144\nds_read_b32 v69, v74\n"
"v_accvgpr_read_b32 v161, a193\nQmvva 164,32,144\nQtv 6,45,25856\nv_accvgpr_read_b32 v162, a194\nQmvva 196,36,144\ns_waitcnt vmcnt(35)\nds_write_b128 v52, a[220:223] offset:12288\nQmvva 228,20,148\n"
"v_accvgpr_read_b32 v163, a195\nv_accvgpr_read_b32 v192, a196\nQmvva 108,24,148\ns_waitcnt lgkmcnt(2)\nv_cmp_gt_u32 vcc, v75, v69\nQmvva 140,28,148\ns_cbranch_vccnz .Lso31\n.Lro31:\n"
"v_accvgpr_read_b32 v193, a197\nQmvva 172,32,148\ns_add_u32 m0, s96, 4096\ns_mov_b32 s95, 1351680\nv_accvgpr_read_b32 v194, a198\nQmvva 204,36,148\nbuffer_load_dwordx4 v40, s[52:55], s95 offen lds\nQmvva 236,20,152\n"
"Qtv 8,45,20992\nv_accvgpr_read_b32 v195, a199\nQmvva 80,24,152\nbuffer_load_dwordx4 a[20:23], v60, s[48:51], s94 offen offset:1024 sc1\nQmvva 112,28,152\nQtv 10,45,26112\nQmvva 144,32,152\nQtv 12,45,21248\n"
"Qmvva 176,36,152\ns_waitcnt vmcnt(34)\nds_write_b128 v52, a[224:227] offset:16384\nQmvva 208,20,156\nQmvva 240,24,156\nbuffer_load_dwordx4 v41, s[52:55], s95 offen offset:1024 lds\nQmvva 84,28,156\nQmvva 88,32,156\n"
"Qtv 14,45,26368\nQmvva 120,36,156\nQtv 16,45,21504\nQmvva 152,20,160\nQtv 18,45,26624\nQmvva 184,24,160\nbuffer_load_dwordx4 a[24:27], v60, s[48:51], s94 offen offset:2048 sc1\nQmvva 216,28,160\n"
"Qmvva 248,32,160\nbuffer_load_dwordx4 v42, s[52:55], s95 offen offset:2048 lds\nQmvva 116,36,160\ns_waitcnt vmcnt(35)\nds_write_b128 v52, a[228:231] offset:20480\nQmvva 96,20,164\nQmvva 128,24,164\nQmvva 160,28,164\n"
"Qmvva 192,32,164\nQda 48,53,0\nv_add_u32 v75, 1, v75\nQmava 200,36,164\ns_waitcnt vmcnt(27)\nds_write_b32 v73, v75\ns_waitcnt lgkmcnt(3)\nQmvva 92,0,208\n"
"buffer_load_dwordx4 v43, s[52:55], s95 offen offset:3072 lds\ns_mov_b32 s94, 802816\nQmvva 124,4,208\nbuffer_load_dwordx4 a[28:31], v60, s[48:51], s94 offen offset:0 sc1\nQmvva 156,8,208\nQtv 20,47,20480\nQmvva 188,12,208\nQtv 22,47,25600\n"
"Qmvva 220,16,208\ns_waitcnt vmcnt(34)\nds_write_b128 v52, a[232:235] offset:25600\nQmvva 252,0,212\nQmvva 100,4,212\nQtv 24,47,20736\nv_accvgpr_read_b32 v224, a200\nQmvva 132,8,212\n"
"ds_read_b32 v69, v74\nv_accvgpr_read_b32 v225, a201\nQmvva 164,12,212\nQda 52,53,4096\ns_add_u32 m0, s96, 8192\nQmvva 196,16,212\nbuffer_load_dwordx4 v44, s[52:55], s95 offen lds\nQmvva 228,0,216\n"
"Qtv 26,47,25856\nv_accvgpr_read_b32 v226, a202\nQmvva 108,4,216\ns_waitcnt lgkmcnt(2)\nv_cmp_gt_u32 vcc, v75, v69\nQmvva 140,8,216\ns_cbranch_vccnz .Lso32\n.Lro32:\n"
"v_accvgpr_read_b32 v227, a203\nQmvva 172,12,216\nbuffer_load_dwordx4 a[32:35], v60, s[48:51], s94 offen offset:1024 sc1\nQmvva 204,16,216\ns_waitcnt vmcnt(33)\nds_write_b128 v52, a[236:239] offset:29696\nQmvva 236,0,220\nQmvva 80,4,220\n"
"Qtv 28,47,20992\nQmvva 112,8,220\nQmvva 144,12,220\nQda 56,53,8192\nQmvva 176,16,220\nQtv 30,47,26112\nQmvva 208,0,224\nQtv 32,47,21248\n"
"Qmvva 240,4,224\nbuffer_load_dwordx4 a[36:39], v60, s[48:51], s94 offen offset:2048 sc1\nQmvva 84,8,224\nQtv 34,47,26368\nQmvva 88,12,224\nQtv 36,47,21504\nQmvva 120,16,224\ns_waitcnt vmcnt(31)\n"
"ds_write_b128 v52, a[240:243] offset:33792\nQmvva 152,0,228\nQmvva 184,4,228\nQtv 38,47,26624\nQmvva 216,8,228\nQmvva 248,12,228\nQda 60,53,12288\nQmvva 116,16,228\n"
"Qmvva 96,0,232\nQmvva 128,4,232\nbuffer_load_dwordx4 a[40:43], v60, s[48:51], s94 offen offset:3072 sc1\nQmvva 160,8,232\nQmvva 192,12,232\nv_add_u32 v75, 1, v75\nQmvva 224,16,232\ns_waitcnt vmcnt(36)\n"
"ds_write_b32 v73, v75\ns_waitcnt lgkmcnt(2)\nQmvva 92,20,48\ns_waitcnt vmcnt(30)\nds_write_b128 v52, a[244:247] offset:37888\nQmvva 124,24,48\nQmvva 156,28,48\ns_waitcnt vmcnt(12)\n"
"v_sub_u32 v66, v78, v65\nv_cmp_gt_u32 vcc, 3, v66\ns_and_b64 vcc, vcc, s[28:29]\ns_cbranch_scc1 .Lf3_w\n.Lf3_r:\nQmvva 188,32,48\nQda 64,53,16384\nQmvva 220,36,48\n"
"Qtv 0,45,40960\nQmvva 252,20,52\nQtv 2,45,46080\ns_mov_b32 s94, 806912\nQmvva 100,24,52\nbuffer_load_dwordx4 a[44:47], v60, s[48:51], s94 offen offset:0 sc1\nQmvva 132,28,52\nds_read_b32 v69, v74\n"
"Qmvva 164,32,52\nQtv 4,45,41216\nQmvva 196,36,52\ns_waitcnt vmcnt(29)\nds_write_b128 v52, a[248:251] offset:41984\ns_waitcnt lgkmcnt(15)\nQmvva 228,20,56\nv_add_u32 v67, 5, v65\n"
"Qmvva 108,24,56\ns_waitcnt lgkmcnt(2)\nv_cmp_gt_u32 vcc, v75, v69\nQmvva 140,28,56\ns_cbranch_vccnz .Lso33\n.Lro33:\nQmvva 172,32,56\nQda 68,53,20480\n"
"Qmvva 204,36,56\nbuffer_store_dword v67, v77, s[24:27], 0 offen sc1\ns_add_u32 m0, s96, 24576\ns_mov_b32 s95, 1679360\nQmvva 236,20,60\nbuffer_load_dwordx4 v40, s[52:55], s95 offen lds\nQmvva 80,24,60\nbuffer_load_dwordx4 a[208:211], v60, s[48:51], s94 offen offset:1024 sc1\n"
"Qmvva 112,28,60\nQtv 6,45,46336\nQmvva 144,32,60\nQtv 8,45,41472\nQmvva 176,36,60\ns_waitcnt vmcnt(29)\nds_write_b128 v52, a[252:255] offset:46080\ns_waitcnt lgkmcnt(3)\n"
"Qmvva 208,20,64\nQmvva 240,24,64\nQtv 10,45,46592\nQmvva 84,28,64\nbuffer_load_dwordx4 v41, s[52:55], s95 offen offset:1024 lds\nQmvva 88,32,64\nQda 72,53,25600\nQmvva 120,36,64\n"
"Qtv 12,45,41728\nQmvva 152,20,68\nQtv 14,45,46848\nQmvva 184,24,68\nbuffer_load_dwordx4 a[212:215], v60, s[48:51], s94 offen offset:2048 sc1\nQmvva 216,28,68\nQtv 16,45,41984\nQmvva 248,32,68\n"
"Qtv 18,45,47104\nQmvva 116,36,68\ns_waitcnt vmcnt(29)\nds_write_b128 v52, a[168:171] offset:50176\ns_waitcnt lgkmcnt(5)\nQmvva 96,20,72\nQmvva 128,24,72\nbuffer_load_dwordx4 v42, s[52:55], s95 offen offset:2048 lds\n"
"Qmvva 160,28,72\nQmvva 192,32,72\nQda 76,53,29696\nv_add_u32 v75, 1, v75\nQmvva 224,36,72\ns_waitcnt vmcnt(31)\nds_write_b32 v73, v75\ns_waitcnt lgkmcnt(3)\n"
"Qmvva 92,0,236\nQtv 20,47,40960\ns_mov_b32 s94, 473088\nQmvva 124,4,236\nbuffer_load_dwordx4 a[216:219], v60, s[48:51], s94 offen offset:0 sc1\nQmvva 156,8,236\nQtv 22,47,46080\nQmvva 188,12,236\n"
"buffer_load_dwordx4 v43, s[52:55], s95 offen offset:3072 lds\nQmvva 220,16,236\ns_waitcnt vmcnt(30)\nds_write_b128 v52, a[172:175] offset:0\nQmvva 252,0,240\nQmvva 100,4,240\nQtv 24,47,41216\nQmvva 132,8,240\n"
"ds_read_b32 v69, v74\nQmvva 164,12,240\nQda 80,53,33792\nQmvva 196,16,240\nQtv 26,47,46336\ns_add_u32 m0, s96, 28672\nQmvva 228,0,244\nbuffer_load_dwordx4 v44, s[52:55], s95 offen lds\n"
"Qmvva 108,4,244\ns_waitcnt lgkmcnt(2)\nv_cmp_gt_u32 vcc, v75, v69\nQmvva 140,8,244\ns_cbranch_vccnz .Lso34\n.Lro34:\nQmvva 172,12,244\nbuffer_load_dwordx4 a[220:223], v60, s[48:51], s94 offen offset:1024 sc1\n"
"Qmvva 204,16,244\ns_waitcnt vmcnt(29)\nds_write_b128 v52, a[0:3] offset:4096\nQmvva 236,0,248\nQmvva 80,4,248\nQtv 28,47,41472\nQmvva 112,8,248\nQmvva 144,12,248\n"
"Qda 48,53,37888\nQmvva 176,16,248\nQtv 30,47,46592\nQmvva 208,0,252\nQtv 32,47,41728\nQmvva 240,4,252\nbuffer_load_dwordx4 a[224:227], v60, s[48:51], s94 offen offset:2048 sc1\nQmvva 84,8,252\n"
"Qtv 34,47,46848\nQmvva 88,12,252\nQtv 36,47,41984\nQmvva 120,16,252\ns_waitcnt vmcnt(28)\nds_write_b128 v52, a[4:7] offset:8192\nQmvva 152,0,168\nQmvva 184,4,168\n"
"Qtv 38,47,47104\nQmvva 216,8,168\nQmvva 248,12,168\nQda 52,53,41984\nQmvva 116,16,168\nQmvva 96,0,172\nQmvva 128,4,172\nbuffer_load_dwordx4 a[228:231], v60, s[48:51], s94 offen offset:3072 sc1\n"
"Qmvva 160,8,172\nQmvva 192,12,172\nv_add_u32 v75, 1, v75\nQmvva 224,16,172\nds_write_b32 v73, v75\ns_waitcnt lgkmcnt(2)\nQmvva 92,20,76\ns_waitcnt vmcnt(27)\n"
"ds_write_b128 v52, a[8:11] offset:12288\nQmvva 124,24,76\nQmvva 156,28,76\nQta 112,46,0\nQmvva 188,32,76\nQda 56,53,46080\nQmvva 220,36,76\nQta 114,46,5120\n"
"Qmvva 252,20,80\nQta 116,46,256\ns_mov_b32 s94, 477184\nQmvva 100,24,80\nbuffer_load_dwordx4 a[232:235], v60, s[48:51], s94 offen offset:0 sc1\nQmvva 132,28,80\nds_read_b32 v69, v74\nQmvva 164,32,80\n"
"Qta 118,46,5376\nQmvva 196,36,80\ns_waitcnt vmcnt(27)\nds_write_b128 v52, a[12:15] offset:16384\ns_waitcnt lgkmcnt(15)\nQmvva 228,20,48\nQmvva 108,24,48\ns_waitcnt lgkmcnt(2)\n"
"v_cmp_gt_u32 vcc, v75, v69\nQmvva 140,28,48\ns_cbranch_vccnz .Lso35\n.Lro35:\nQmvva 172,32,48\nQda 60,53,50176\ns_add_u32 m0, s96, 45056\ns_mov_b32 s95, 2007040\n"
"Qmvva 204,36,48\nbuffer_load_dwordx4 v40, s[52:55], s95 offen lds\nQmvva 236,20,52\nQta 120,46,512\nQmvva 80,24,52\nbuffer_load_dwordx4 a[236:239], v60, s[48:51], s94 offen offset:1024 sc1\nQmvva 112,28,52\nQta 122,46,5632\n"
"Qmvva 144,32,52\nQta 124,46,768\nQmvva 176,36,52\ns_waitcnt vmcnt(27)\nds_write_b128 v52, a[16:19] offset:20480\ns_waitcnt lgkmcnt(4)\nQmvva 208,20,56\nQmvva 240,24,56\n"
"buffer_load_dwordx4 v41, s[52:55], s95 offen offset:1024 lds\nQmvva 84,28,56\nQta 126,46,5888\nQmvva 88,32,56\nQda 64,53,0\nQmvva 120,36,56\nQta 128,46,1024\nQmvva 152,20,60\n"
"Qta 130,46,6144\nQmvva 184,24,60\nbuffer_load_dwordx4 a[240:243], v60, s[48:51], s94 offen offset:2048 sc1\nQmvva 216,28,60\nQmvva 248,32,60\nbuffer_load_dwordx4 v42, s[52:55], s95 offen offset:2048 lds\nQmvva 116,36,60\ns_waitcnt vmcnt(28)\n"
"ds_write_b128 v52, a[20:23] offset:25600\ns_waitcnt lgkmcnt(3)\nQmvva 96,20,64\nQmvva 128,24,64\nQmvva 160,28,64\nQmvva 192,32,64\nQda 68,53,4096\nv_add_u32 v75, 1, v75\n"
"Qmvva 224,36,64\ns_waitcnt vmcnt(34)\nds_write_b32 v73, v75\ns_waitcnt lgkmcnt(3)\nQmvaa 92,112,0\nbuffer_load_dwordx4 v43, s[52:55], s95 offen offset:3072 lds\ns_mov_b32 s94, 587776\nQmvaa 124,116,0\n"
"buffer_load_dwordx4 a[244:247], v60, s[48:51], s94 offen offset:0 sc1\nQmvaa 156,120,0\nQta 132,51,0\nQmvaa 188,124,0\nQta 134,51,5120\nQmvaa 220,128,0\ns_waitcnt vmcnt(28)\nds_write_b128 v52, a[24:27] offset:29696\n"
"Qmvaa 252,112,4\nQmvaa 100,116,4\nQta 136,51,256\nQmvaa 132,120,4\nds_read_b32 v69, v74\nQmvaa 164,124,4\nQda 72,53,8192\ns_add_u32 m0, s96, 49152\n"
"Qmvaa 196,128,4\nbuffer_load_dwordx4 v44, s[52:55], s95 offen lds\nQmvaa 228,112,8\nQta 138,51,5376\nQmvaa 108,116,8\ns_waitcnt lgkmcnt(2)\nv_cmp_gt_u32 vcc, v75, v69\nQmvaa 140,120,8\n"
"s_cbranch_vccnz .Lso36\n.Lro36:\nQmvaa 172,124,8\nbuffer_load_dwordx4 a[248:251], v60, s[48:51], s94 offen offset:1024 sc1\nQmvaa 204,128,8\ns_waitcnt vmcnt(27)\nds_write_b128 v52, a[28:31] offset:33792\nQmvaa 236,112,12\n"
"Qmvaa 80,116,12\nQta 140,51,512\nQmvaa 112,120,12\nQmvaa 144,124,12\nQda 76,53,12288\nQmvaa 176,128,12\nQta 142,51,5632\nQmvaa 208,112,16\n"
"Qta 144,51,768\nQmvaa 240,116,16\nbuffer_load_dwordx4 a[252:255], v60, s[48:51], s94 offen offset:2048 sc1\nQmvaa 84,120,16\nQta 146,51,5888\nQmvaa 88,124,16\nQta 148,51,1024\nQmvaa 120,128,16\n"
"s_waitcnt vmcnt(26)\nds_write_b128 v52, a[32:35] offset:37888\nQmvaa 152,112,20\nQmvaa 184,116,20\nQta 150,51,6144\nQmvaa 216,120,20\nQmvaa 248,124,20\nQda 80,53,16384\n"
"Qmvaa 116,128,20\nQmvaa 96,112,24\nQmvaa 128,116,24\nbuffer_load_dwordx4 a[168:171], v60, s[48:51], s94 offen offset:3072 sc1\nQmvaa 160,120,24\nQmvaa 192,124,24\nv_add_u32 v75, 1, v75\nQmvaa 224,128,24\n"
"ds_write_b32 v73, v75\ns_waitcnt lgkmcnt(2)\nQmvaa 92,132,68\ns_waitcnt vmcnt(26)\nds_write_b128 v52, a[36:39] offset:41984\nQmvaa 124,136,68\nQmvaa 156,140,68\nQtv 0,46,20480\n"
"Qmvaa 188,144,68\nQda 48,53,20480\nQmvaa 220,148,68\nQtv 2,46,25600\nQmvaa 252,132,72\nQtv 4,46,20736\ns_mov_b32 s94, 591872\nQmvaa 100,136,72\n"
".Lpg19:\ns_cbranch_execz .Lpg23\nbuffer_load_dwordx4 a[172:175], v60, s[48:51], s94 offen offset:0 sc1\nQmvaa 132,140,72\nds_read_b32 v69, v74\ns_mov_b32 s40, %17\nQmvaa 164,144,72\nQtv 6,46,25856\n"
"s_and_b32 s41, %18, 0xffff\nQmvaa 196,148,72\ns_waitcnt vmcnt(26)\nds_write_b128 v52, a[40:43] offset:46080\ns_waitcnt lgkmcnt(15)\nQmvaa 228,132,76\ns_mov_b32 s42, 571200\ns_lshr_b32 s68, %19, 2\n"
"Qmvaa 108,136,76\ns_waitcnt lgkmcnt(2)\nv_cmp_gt_u32 vcc, v75, v69\nQmvaa 140,140,76\ns_cbranch_vccnz .Lso37\n.Lro37:\ns_mul_i32 s65, s68, 286720\nQmvaa 172,144,76\n"
"Qda 52,53,25600\ns_add_u32 m0, s96, 65536\ns_mov_b32 s95, 2334720\nQmvaa 204,148,76\nbuffer_load_dwordx4 v40, s[52:55], s95 offen lds\nQmvaa 236,132,80\nQtv 8,46,20992\ns_sub_u32 s60, 286720, s65\n"
"Qmvaa 80,136,80\nbuffer_load_dwordx4 a[0:3], v60, s[48:51], s94 offen offset:1024 sc1\nQmvaa 112,140,80\nQtv 10,46,26112\ns_mul_i32 s68, s68, 114688\nQmvaa 144,144,80\nQtv 12,46,21248\ns_add_u32 s64, s98, 57344\n"
"Qmvaa 176,148,80\ns_waitcnt vmcnt(27)\nds_write_b128 v52, a[44:47] offset:50176\ns_waitcnt lgkmcnt(4)\nQmvaa 208,132,48\ns_sub_u32 s64, s64, s68\ns_mul_i32 s68, s93, 160\nQmvaa 240,136,48\n"
"buffer_load_dwordx4 v41, s[52:55], s95 offen offset:1024 lds\nQmvaa 84,140,48\nQtv 14,46,26368\ns_add_u32 s68, s68, %10\nQmvaa 88,144,48\nQda 56,53,29696\nv_bfe_u32 v66, v60, 4, 4\nQmvaa 120,148,48\n"
"Qtv 16,46,21504\nv_mul_u32_u24 v66, 336, v66\nQmvaa 152,132,52\nQtv 18,46,26624\nv_lshrrev_b32 v67, 8, v60\nQmvaa 184,136,52\nbuffer_load_dwordx4 a[4:7], v60, s[48:51], s94 offen offset:2048 sc1\nQmvaa 216,140,52\n"
"v_lshlrev_b32 v68, 3, v67\nv_add3_u32 v61, v66, v68, s68\nQmvaa 248,144,52\nbuffer_load_dwordx4 v42, s[52:55], s95 offen offset:2048 lds\nQmvaa 116,148,52\ns_waitcnt vmcnt(27)\nds_write_b128 v52, a[208:211] offset:0\ns_waitcnt lgkmcnt(3)\n"
"Qmvaa 96,132,56\ns_lshl_b32 s68, s70, 2\nv_add_u32 v67, s68, v67\nQmvaa 128,136,56\ns_and_b32 s69, %19, 1\ns_lshl_b32 s68, s69, 6\nQmvaa 160,140,56\nv_and_b32 v66, 255, v60\n"
"v_add_u32 v66, s68, v66\nQmvaa 192,144,56\nQda 60,53,33792\nv_add_u32 v75, 1, v75\nQmvaa 224,148,56\ns_waitcnt vmcnt(34)\nds_write_b32 v73, v75\ns_waitcnt lgkmcnt(3)\n"
"Qmvva 92,0,28\nbuffer_load_dwordx4 v43, s[52:55], s95 offen offset:3072 lds\ns_mov_b32 s94, 702464\nQmvva 124,4,28\nbuffer_load_dwordx4 a[8:11], v60, s[48:51], s94 offen offset:0 sc1\nQmvva 156,8,28\nQtv 20,51,20480\nv_mul_u32_u24 v68, 336, v67\n"
"Qmvva 188,12,28\nQtv 22,51,25600\nv_add3_u32 v62, v66, v68, %10\nQmvva 220,16,28\ns_waitcnt vmcnt(27)\nds_write_b128 v52, a[212:215] offset:4096\nQmvva 252,0,32\nv_mul_u32_u24 v68, 2560, v67\n"
"v_add_u32 v63, v66, v68\nQmvva 100,4,32\nQtv 24,51,20736\ns_lshl_b32 s68, s70, 4\nQmvva 132,8,32\nds_read_b32 v69, v74\nv_lshrrev_b32 v67, 6, v60\nQmvva 164,12,32\n"
"Qda 64,53,37888\ns_add_u32 m0, s96, 69632\nQmvva 196,16,32\nbuffer_load_dwordx4 v44, s[52:55], s95 offen lds\nQmvva 228,0,36\nQtv 26,51,25856\nv_add_u32 v67, s68, v67\nQmvva 108,4,36\n"
"s_waitcnt lgkmcnt(2)\nv_cmp_gt_u32 vcc, v75, v69\nQmvva 140,8,36\ns_cbranch_vccnz .Lso38\n.Lro38:\ns_xor_b32 s69, s69, 1\nQmvva 172,12,36\nbuffer_load_dwordx4 a[12:15], v60, s[48:51], s94 offen offset:1024 sc1\n"
"Qmvva 204,16,36\ns_waitcnt vmcnt(27)\nds_write_b128 v52, a[216:219] offset:8192\nQmvva 236,0,40\ns_lshl_b32 s68, s69, 8\nv_and_b32 v66, 63, v60\nQmvva 80,4,40\nQtv 28,51,20992\n"
"v_add_u32 v66, s68, v66\nQmvva 112,8,40\nv_mul_u32_u24 v68, 336, v67\nv_add3_u32 v70, v66, v68, %10\nQmvva 144,12,40\nQda 68,53,41984\nv_mul_u32_u24 v68, 2560, v67\nQmvva 176,16,40\n"
"Qtv 30,51,26112\nv_add_u32 v71, v66, v68\nQmvva 208,0,44\nQtv 32,51,21248\nQmvva 240,4,44\nbuffer_load_dwordx4 a[16:19], v60, s[48:51], s94 offen offset:2048 sc1\nQmvva 84,8,44\nQtv 34,51,26368\n"
"Qmvva 88,12,44\nQtv 36,51,21504\nQmvva 120,16,44\ns_waitcnt vmcnt(25)\nds_write_b128 v52, a[220:223] offset:12288\nQmvva 152,0,208\nQmvva 184,4,208\nQtv 38,51,26624\n"
"Qmvva 216,8,208\nQmvva 248,12,208\nQda 72,53,46080\nQmvva 116,16,208\nQmvva 96,0,212\nQmvva 128,4,212\nbuffer_load_dwordx4 a[20:23], v60, s[48:51], s94 offen offset:3072 sc1\nQmvva 160,8,212\n"
"Qmvva 192,12,212\nv_add_u32 v75, 1, v75\nQmvva 224,16,212\nds_write_b32 v73, v75\ns_waitcnt lgkmcnt(2)\nQmvva 92,20,60\ns_waitcnt vmcnt(25)\nds_write_b128 v52, a[224:227] offset:16384\n"
"Qmvva 124,24,60\nQmvva 156,28,60\nbuffer_load_dword v78, v76, s[24:27], 0 offen sc1\nQmvva 188,32,60\nQda 76,53,50176\nQmvva 220,36,60\nQta 112,45,0\nQmvva 252,20,64\n"
"Qta 114,45,5120\ns_mov_b32 s94, 706560\nQmvva 100,24,64\nbuffer_load_dwordx4 a[24:27], v60, s[48:51], s94 offen offset:0 sc1\nQmvva 132,28,64\nds_read_b32 v69, v74\nQmvva 164,32,64\nQta 116,45,256\n"
"Qmvva 196,36,64\ns_waitcnt vmcnt(26)\nds_write_b128 v52, a[228:231] offset:20480\ns_waitcnt lgkmcnt(15)\nQmvva 228,20,68\nQmvva 108,24,68\ns_waitcnt lgkmcnt(2)\nv_cmp_gt_u32 vcc, v75, v69\n"
"Qmvva 140,28,68\ns_cbranch_vccnz .Lso39\n.Lro39:\nQmvva 172,32,68\nQda 80,53,0\ns_add_u32 m0, s96, 86016\ns_mov_b32 s95, 1392640\nQmvva 204,36,68\n"
"buffer_load_dwordx4 v40, s[52:55], s95 offen lds\nQmvva 236,20,72\nQta 118,45,5376\nQmvva 80,24,72\nbuffer_load_dwordx4 a[28:31], v60, s[48:51], s94 offen offset:1024 sc1\nQmvva 112,28,72\nQta 120,45,512\nQmvva 144,32,72\n"
"Qta 122,45,5632\nQmvva 176,36,72\ns_waitcnt vmcnt(27)\nds_write_b128 v52, a[232:235] offset:25600\ns_waitcnt lgkmcnt(4)\nQmvva 208,20,76\nQmvva 240,24,76\nbuffer_load_dwordx4 v41, s[52:55], s95 offen offset:1024 lds\n"
"Qmvva 84,28,76\nQta 124,45,768\nQmvva 88,32,76\nQda 48,53,4096\nQmvva 120,36,76\nQta 126,45,5888\nQmvva 152,20,80\nQta 128,45,1024\n"
"Qmvva 184,24,80\nbuffer_load_dwordx4 a[32:35], v60, s[48:51], s94 offen offset:2048 sc1\nQmvva 216,28,80\nQta 130,45,6144\nQmvva 248,32,80\nbuffer_load_dwordx4 v42, s[52:55], s95 offen offset:2048 lds\nQmvva 116,36,80\ns_waitcnt vmcnt(28)\n"
"ds_write_b128 v52, a[236:239] offset:29696\ns_waitcnt lgkmcnt(4)\nQmvva 96,20,48\nQmvva 128,24,48\nQmvva 160,28,48\nQmvva 192,32,48\nQda 52,53,8192\nv_add_u32 v75, 1, v75\n"
"Qmvva 224,36,48\ns_waitcnt vmcnt(34)\nds_write_b32 v73, v75\ns_waitcnt lgkmcnt(3)\nQmvaa 92,112,216\nbuffer_load_dwordx4 v43, s[52:55], s95 offen offset:3072 lds\ns_mov_b32 s94, 817152\nQmvaa 124,116,216\n"
"buffer_load_dwordx4 a[36:39], v60, s[48:51], s94 offen offset:0 sc1\nQmvaa 156,120,216\nQta 132,47,0\nQmvaa 188,124,216\nQta 134,47,5120\nQmvaa 220,128,216\ns_waitcnt vmcnt(28)\nds_write_b128 v52, a[240:243] offset:33792\n"
"Qmvaa 252,112,220\nQmvaa 100,116,220\nQta 136,47,256\nQmvaa 132,120,220\nds_read_b32 v69, v74\nQmvaa 164,124,220\nQda 56,53,12288\ns_add_u32 m0, s96, 90112\n"
"Qmvaa 196,128,220\nbuffer_load_dwordx4 v44, s[52:55], s95 offen lds\nQmvaa 228,112,224\nQta 138,47,5376\nQmvaa 108,116,224\ns_waitcnt lgkmcnt(2)\nv_cmp_gt_u32 vcc, v75, v69\nQmvaa 140,120,224\n"
"s_cbranch_vccnz .Lso40\n.Lro40:\nQmvaa 172,124,224\nbuffer_load_dwordx4 a[40:43], v60, s[48:51], s94 offen offset:1024 sc1\nQmvaa 204,128,224\ns_waitcnt vmcnt(27)\nds_write_b128 v52, a[244:247] offset:37888\nQmvaa 236,112,228\n"
"Qmvaa 80,116,228\nQta 140,47,512\nQmvaa 112,120,228\nQmvaa 144,124,228\nQda 60,53,16384\nQmvaa 176,128,228\nQta 142,47,5632\nQmvaa 208,112,232\n"
"Qta 144,47,768\nQmvaa 240,116,232\nbuffer_load_dwordx4 a[44:47], v60, s[48:51], s94 offen offset:2048 sc1\nQmvaa 84,120,232\nQta 146,47,5888\nQmvaa 88,124,232\nQta 148,47,1024\nQmvaa 120,128,232\n"
"s_waitcnt vmcnt(26)\nds_write_b128 v52, a[248:251] offset:41984\nQmvaa 152,112,236\nQmvaa 184,116,236\nQta 150,47,6144\nQmvaa 216,120,236\nQmvaa 248,124,236\nQda 64,53,20480\n"
"Qmvaa 116,128,236\nQmvaa 96,112,240\nQmvaa 128,116,240\nbuffer_load_dwordx4 a[208:211], v60, s[48:51], s94 offen offset:3072 sc1\nQmvaa 160,120,240\nQmvaa 192,124,240\nv_add_u32 v75, 1, v75\nQmvaa 224,128,240\n"
"ds_write_b32 v73, v75\ns_waitcnt lgkmcnt(2)\nQmvaa 92,132,52\ns_waitcnt vmcnt(26)\nds_write_b128 v52, a[252:255] offset:46080\nQmvaa 124,136,52\nQmvaa 156,140,52\ns_waitcnt vmcnt(12)\n"
"v_sub_u32 v66, v78, v65\nv_cmp_gt_u32 vcc, 4, v66\ns_and_b64 vcc, vcc, s[28:29]\ns_cbranch_scc1 .Lf4_w\n.Lf4_r:\nQmvaa 188,144,52\nQda 68,53,25600\nQmvaa 220,148,52\n"
"Qtv 0,45,20480\nQmvaa 252,132,56\nQtv 2,45,25600\ns_mov_b32 s94, 821248\nQmvaa 100,136,56\nbuffer_load_dwordx4 a[212:215], v60, s[48:51], s94 offen offset:0 sc1\nQmvaa 132,140,56\nds_read_b32 v69, v74\n"
"Qmvaa 164,144,56\nQtv 4,45,20736\nQmvaa 196,148,56\ns_waitcnt vmcnt(26)\nds_write_b128 v52, a[168:171] offset:50176\ns_waitcnt lgkmcnt(15)\nQmvaa 228,132,60\nQmvaa 108,136,60\n"
"s_waitcnt lgkmcnt(2)\nv_cmp_gt_u32 vcc, v75, v69\nQmvaa 140,140,60\ns_cbranch_vccnz .Lso41\n.Lro41:\nQmvaa 172,144,60\nQda 72,53,29696\ns_add_u32 m0, s96, 4096\n"
"s_mov_b32 s95, 1720320\nQmvaa 204,148,60\nbuffer_load_dwordx4 v40, s[52:55], s95 offen lds\nQmvaa 236,132,64\nQtv 6,45,25856\nQmvaa 80,136,64\nbuffer_load_dwordx4 a[216:219], v60, s[48:51], s94 offen offset:1024 sc1\nQmvaa 112,140,64\n"
"Qtv 8,45,20992\nQmvaa 144,144,64\nQtv 10,45,26112\nQmvaa 176,148,64\ns_waitcnt vmcnt(27)\nds_write_b128 v52, a[172:175] offset:0\ns_waitcnt lgkmcnt(4)\nQmvaa 208,132,68\n"
"Qmvaa 240,136,68\nbuffer_load_dwordx4 v41, s[52:55], s95 offen offset:1024 lds\nQmvaa 84,140,68\nQtv 12,45,21248\nQmvaa 88,144,68\nQda 76,53,33792\nQmvaa 120,148,68\nQtv 14,45,26368\n"
"Qmvaa 152,132,72\nQtv 16,45,21504\nQmvaa 184,136,72\nbuffer_load_dwordx4 a[220:223], v60, s[48:51], s94 offen offset:2048 sc1\nQmvaa 216,140,72\nQtv 18,45,26624\nQmvaa 248,144,72\nbuffer_load_dwordx4 v42, s[52:55], s95 offen offset:2048 lds\n"
"Qmvaa 116,148,72\ns_waitcnt vmcnt(28)\nds_write_b128 v52, a[0:3] offset:4096\ns_waitcnt lgkmcnt(4)\nQmvaa 96,132,76\nQmvaa 128,136,76\nQmvaa 160,140,76\nQmvaa 192,144,76\n"
"Qda 80,53,37888\nv_add_u32 v75, 1, v75\nQmvaa 224,148,76\ns_waitcnt vmcnt(34)\nds_write_b32 v73, v75\ns_waitcnt lgkmcnt(3)\nQmvva 92,0,244\nbuffer_load_dwordx4 v43, s[52:55], s95 offen offset:3072 lds\n"
"s_mov_b32 s94, 487424\nQmvva 124,4,244\nbuffer_load_dwordx4 a[224:227], v60, s[48:51], s94 offen offset:0 sc1\nQmvva 156,8,244\nQtv 20,47,20480\nQmvva 188,12,244\nQtv 22,47,25600\nQmvva 220,16,244\n"
"s_waitcnt vmcnt(28)\nds_write_b128 v52, a[4:7] offset:8192\nQmvva 252,0,248\nQmvva 100,4,248\nQtv 24,47,20736\nQmvva 132,8,248\nds_read_b32 v69, v74\nQmvva 164,12,248\n"
"Qda 48,53,41984\ns_add_u32 m0, s96, 8192\nQmvva 196,16,248\nbuffer_load_dwordx4 v44, s[52:55], s95 offen lds\nQmvva 228,0,252\nQtv 26,47,25856\nQmvva 108,4,252\ns_waitcnt lgkmcnt(2)\n"
"v_cmp_gt_u32 vcc, v75, v69\nQmvva 140,8,252\ns_cbranch_vccnz .Lso42\n.Lro42:\nQmvva 172,12,252\nbuffer_load_dwordx4 a[228:231], v60, s[48:51], s94 offen offset:1024 sc1\nQmvva 204,16,252\ns_waitcnt vmcnt(27)\n"
"ds_write_b128 v52, a[8:11] offset:12288\nQmvva 236,0,168\nQmvva 80,4,168\nQtv 28,47,20992\nQmvva 112,8,168\nQmvva 144,12,168\nQda 52,53,46080\nQmvva 176,16,168\n"
"Qtv 30,47,26112\nQmvva 208,0,172\nQtv 32,47,21248\nQmvva 240,4,172\nbuffer_load_dwordx4 a[232:235], v60, s[48:51], s94 offen offset:2048 sc1\nQmvva 84,8,172\nQtv 34,47,26368\nQmvva 88,12,172\n"
"Qtv 36,47,21504\nQmvva 120,16,172\ns_waitcnt vmcnt(26)\nds_write_b128 v52, a[12:15] offset:16384\nQmvva 152,0,0\nQmvva 184,4,0\nQtv 38,47,26624\nQmvva 216,8,0\n"
"Qmvva 248,12,0\nQda 56,53,50176\nQmvva 116,16,0\nQmvva 96,0,4\nQmvva 128,4,4\nbuffer_load_dwordx4 a[236:239], v60, s[48:51], s94 offen offset:3072 sc1\nQmvva 160,8,4\nQmvva 192,12,4\n"
"v_add_u32 v75, 1, v75\nQmvva 224,16,4\nds_write_b32 v73, v75\ns_waitcnt lgkmcnt(2)\nQmvva 92,20,80\ns_waitcnt vmcnt(26)\nds_write_b128 v52, a[16:19] offset:20480\nQmvva 124,24,80\n"
"Qmvva 156,28,80\nQta 112,45,40960\nQmvva 188,32,80\nQda 60,53,0\nQmvva 220,36,80\nQta 114,45,46080\nQmvva 252,20,48\nQta 116,45,41216\n"
"s_mov_b32 s94, 491520\nQmvva 100,24,48\nbuffer_load_dwordx4 a[240:243], v60, s[48:51], s94 offen offset:0 sc1\nQmvva 132,28,48\nds_read_b32 v69, v74\nQmvva 164,32,48\nQta 118,45,46336\nQmvva 196,36,48\n"
"s_waitcnt vmcnt(26)\nds_write_b128 v52, a[20:23] offset:25600\ns_waitcnt lgkmcnt(15)\nQmvva 228,20,52\nQmvva 108,24,52\ns_waitcnt lgkmcnt(2)\nv_cmp_gt_u32 vcc, v75, v69\nQmvva 140,28,52\n"
"s_cbranch_vccnz .Lso43\n.Lro43:\nQmvva 172,32,52\nQda 64,53,4096\ns_add_u32 m0, s96, 24576\ns_mov_b32 s95, 2048000\nQmvva 204,36,52\nbuffer_load_dwordx4 v40, s[52:55], s95 offen lds\n"
"Qmvva 236,20,56\nQta 120,45,41472\nQmvva 80,24,56\nbuffer_load_dwordx4 a[244:247], v60, s[48:51], s94 offen offset:1024 sc1\nQmvva 112,28,56\nQta 122,45,46592\nQmvva 144,32,56\nQta 124,45,41728\n"
"Qmvva 176,36,56\ns_waitcnt vmcnt(26)\nds_write_b128 v52, a[24:27] offset:29696\ns_waitcnt lgkmcnt(4)\nQmvva 208,20,60\nQmvva 240,24,60\nbuffer_load_dwordx4 v41, s[52:55], s95 offen offset:1024 lds\nQmvva 84,28,60\n"
"Qta 126,45,46848\nQmvva 88,32,60\nQda 68,53,8192\nQmvva 120,36,60\nQta 128,45,41984\nQmvva 152,20,64\nQta 130,45,47104\nQmvva 184,24,64\n"
"buffer_load_dwordx4 a[248:251], v60, s[48:51], s94 offen offset:2048 sc1\nQmvva 216,28,64\nQmvva 248,32,64\nbuffer_load_dwordx4 v42, s[52:55], s95 offen offset:2048 lds\nQmvva 116,36,64\ns_waitcnt vmcnt(27)\nds_write_b128 v52, a[28:31] offset:33792\ns_waitcnt lgkmcnt(3)\n"
"Qmvva 96,20,68\nQmvva 128,24,68\nQmvva 160,28,68\nQmvva 192,32,68\nQda 72,53,12288\nv_add_u32 v75, 1, v75\nQmvva 224,36,68\ns_waitcnt vmcnt(34)\n"
"ds_write_b32 v73, v75\ns_waitcnt lgkmcnt(3)\nQmvaa 92,112,8\nbuffer_load_dwordx4 v43, s[52:55], s95 offen offset:3072 lds\ns_mov_b32 s94, 602112\nQmvaa 124,116,8\nbuffer_load_dwordx4 a[252:255], v60, s[48:51], s94 offen offset:0 sc1\nQmvaa 156,120,8\n"
"Qta 132,47,40960\nQmvaa 188,124,8\nQta 134,47,46080\nQmvaa 220,128,8\ns_waitcnt vmcnt(27)\nds_write_b128 v52, a[32:35] offset:37888\nQmvaa 252,112,12\nQmvaa 100,116,12\n"
"Qta 136,47,41216\nQmvaa 132,120,12\nds_read_b32 v69, v74\nQmvaa 164,124,12\n.Lpg20:\ns_cbranch_execz .Lpg24\nQda 76,53,16384\ns_add_u32 m0, s96, 28672\n"
"Qmvaa 196,128,12\nbuffer_load_dwordx4 v44, s[52:55], s95 offen lds\nQmvaa 228,112,16\nQta 138,47,46336\nQmvaa 108,116,16\ns_waitcnt lgkmcnt(2)\nv_cmp_gt_u32 vcc, v75, v69\nQmvaa 140,120,16\n"
"s_cbranch_vccnz .Lso44\n.Lro44:\nQmvaa 172,124,16\nbuffer_load_dwordx4 a[168:171], v60, s[48:51], s94 offen offset:1024 sc1\nQmvaa 204,128,16\ns_waitcnt vmcnt(26)\nds_write_b128 v52, a[36:39] offset:41984\nQmvaa 236,112,20\n"
"Qmvaa 80,116,20\nQta 140,47,41472\nQmvaa 112,120,20\nQmvaa 144,124,20\nQda 80,53,20480\nQmvaa 176,128,20\nQta 142,47,46592\nQmvaa 208,112,24\n"
"Qta 144,47,41728\nQmvaa 240,116,24\nbuffer_load_dwordx4 a[172:175], v60, s[48:51], s94 offen offset:2048 sc1\nQmvaa 84,120,24\nQta 146,47,46848\nQmvaa 88,124,24\nQta 148,47,41984\nQmvaa 120,128,24\n"
"s_waitcnt vmcnt(25)\nds_write_b128 v52, a[40:43] offset:46080\nQmvaa 152,112,28\nQmvaa 184,116,28\nQta 150,47,47104\nQmvaa 216,120,28\nQmvaa 248,124,28\nQda 48,53,25600\n"
"Qmvaa 116,128,28\nQmvaa 96,112,32\nQmvaa 128,116,32\nbuffer_load_dwordx4 a[0:3], v60, s[48:51], s94 offen offset:3072 sc1\nQmvaa 160,120,32\nQmvaa 192,124,32\nv_add_u32 v75, 1, v75\nQmvaa 224,128,32\n"
"ds_write_b32 v73, v75\ns_waitcnt lgkmcnt(2)\nQmvaa 92,132,72\ns_waitcnt vmcnt(25)\nds_write_b128 v52, a[44:47] offset:50176\nQmvaa 124,136,72\nQmvaa 156,140,72\nQtv 0,46,0\n"
"Qmvaa 188,144,72\nQda 52,53,29696\nQmvaa 220,148,72\nQtv 2,46,5120\nQmvaa 252,132,76\nQtv 4,46,256\ns_mov_b32 s94, 606208\nQmvaa 100,136,76\n"
"buffer_load_dwordx4 a[4:7], v60, s[48:51], s94 offen offset:0 sc1\nQmvaa 132,140,76\nds_read_b32 v69, v74\nQmvaa 164,144,76\nQtv 6,46,5376\nQmvaa 196,148,76\ns_waitcnt vmcnt(25)\nds_write_b128 v52, a[208:211] offset:0\n"
"s_waitcnt lgkmcnt(15)\nQmvaa 228,132,80\nQmvaa 108,136,80\ns_waitcnt lgkmcnt(2)\nv_cmp_gt_u32 vcc, v75, v69\nQmvaa 140,140,80\ns_cbranch_vccnz .Lso45\n.Lro45:\n"
"Qmvaa 172,144,80\nQda 56,53,33792\ns_add_u32 m0, s96, 45056\ns_mov_b32 s95, 2375680\nQmvaa 204,148,80\nbuffer_load_dwordx4 v40, s[52:55], s95 offen lds\nQmvaa 236,132,48\nQtv 8,46,512\n"
"Qmvaa 80,136,48\nbuffer_load_dwordx4 a[8:11], v60, s[48:51], s94 offen offset:1024 sc1\nQmvaa 112,140,48\nQtv 10,46,5632\nQmvaa 144,144,48\nQtv 12,46,768\nQmvaa 176,148,48\ns_waitcnt vmcnt(26)\n"
"ds_write_b128 v52, a[212:215] offset:4096\ns_waitcnt lgkmcnt(4)\nQmvaa 208,132,52\nQmvaa 240,136,52\nbuffer_load_dwordx4 v41, s[52:55], s95 offen offset:1024 lds\nQmvaa 84,140,52\nQtv 14,46,5888\nQmvaa 88,144,52\n"
"Qda 60,53,37888\nQmvaa 120,148,52\nQtv 16,46,1024\nQmvaa 152,132,56\nQtv 18,46,6144\nQmvaa 184,136,56\nbuffer_load_dwordx4 a[12:15], v60, s[48:51], s94 offen offset:2048 sc1\nQmvaa 216,140,56\n"
"Qmvaa 248,144,56\nbuffer_load_dwordx4 v42, s[52:55], s95 offen offset:2048 lds\nQmvaa 116,148,56\ns_waitcnt vmcnt(27)\nds_write_b128 v52, a[216:219] offset:8192\ns_waitcnt lgkmcnt(3)\nQmvaa 96,132,60\nQmvaa 128,136,60\n"
"Qmvaa 160,140,60\nQmvaa 192,144,60\nQda 64,53,41984\nv_add_u32 v75, 1, v75\nQmvaa 224,148,60\ns_waitcnt vmcnt(33)\nds_write_b32 v73, v75\ns_waitcnt lgkmcnt(3)\n"
"Qmvva 92,0,36\nbuffer_load_dword v78, v76, s[24:27], 0 offen sc1\ns_mov_b32 s94, 716800\nQmvva 124,4,36\nbuffer_load_dwordx4 a[16:19], v60, s[48:51], s94 offen offset:0 sc1\nQmvva 156,8,36\nbuffer_load_dwordx4 v43, s[52:55], s95 offen offset:3072 lds\nQmvva 188,12,36\n"
"Qtv 20,51,0\nQmvva 220,16,36\ns_waitcnt vmcnt(28)\nds_write_b128 v52, a[220:223] offset:12288\nQmvva 252,0,40\nQmvva 100,4,40\nQtv 22,51,5120\nQmvva 132,8,40\n"
"ds_read_b32 v69, v74\nQmvva 164,12,40\nQda 68,53,46080\ns_add_u32 m0, s96, 49152\nQmvva 196,16,40\nbuffer_load_dwordx4 v44, s[52:55], s95 offen lds\nQmvva 228,0,44\nQtv 24,51,256\n"
"Qmvva 108,4,44\ns_waitcnt lgkmcnt(2)\nv_cmp_gt_u32 vcc, v75, v69\nQmvva 140,8,44\ns_cbranch_vccnz .Lso46\n.Lro46:\nQmvva 172,12,44\nbuffer_load_dwordx4 a[20:23], v60, s[48:51], s94 offen offset:1024 sc1\n"
"Qmvva 204,16,44\ns_waitcnt vmcnt(27)\nds_write_b128 v52, a[224:227] offset:16384\nQmvva 236,0,208\nQmvva 80,4,208\nQtv 26,51,5376\nQmvva 112,8,208\nQtv 28,51,512\n"
"Qmvva 144,12,208\nQda 72,53,50176\nQmvva 176,16,208\nQtv 30,51,5632\nQmvva 208,0,212\nQtv 32,51,768\nQmvva 240,4,212\nbuffer_load_dwordx4 a[24:27], v60, s[48:51], s94 offen offset:2048 sc1\n"
"Qmvva 84,8,212\nQtv 34,51,5888\nQmvva 88,12,212\nQtv 36,51,1024\nQmvva 120,16,212\ns_waitcnt vmcnt(26)\nds_write_b128 v52, a[228:231] offset:20480\nQmvva 152,0,216\n"
"Qmvva 184,4,216\nQtv 38,51,6144\nQmvva 216,8,216\nQmvva 248,12,216\nQda 76,53,0\nQmvva 116,16,216\nQmvva 96,0,220\nQmvva 128,4,220\n"
"buffer_load_dwordx4 a[28:31], v60, s[48:51], s94 offen offset:3072 sc1\nQmvva 160,8,220\nQmvva 192,12,220\nv_add_u32 v75, 1, v75\nQmvva 224,16,220\nds_write_b32 v73, v75\ns_waitcnt lgkmcnt(2)\nQmvva 92,20,64\n"
"s_waitcnt vmcnt(26)\nds_write_b128 v52, a[232:235] offset:25600\nQmvva 124,24,64\nQmvva 156,28,64\nQta 112,46,20480\nQmvva 188,32,64\nQda 80,53,4096\nQmvva 220,36,64\n"
"Qta 114,46,25600\nQmvva 252,20,68\nQta 116,46,20736\ns_mov_b32 s94, 720896\nQmvva 100,24,68\nbuffer_load_dwordx4 a[32:35], v60, s[48:51], s94 offen offset:0 sc1\nQmvva 132,28,68\nds_read_b32 v69, v74\n"
"Qmvva 164,32,68\nQta 118,46,25856\nQmvva 196,36,68\ns_waitcnt vmcnt(26)\nds_write_b128 v52, a[236:239] offset:29696\ns_waitcnt lgkmcnt(15)\nQmvva 228,20,72\nQmvva 108,24,72\n"
"s_waitcnt lgkmcnt(2)\nv_cmp_gt_u32 vcc, v75, v69\nQmvva 140,28,72\ns_cbranch_vccnz .Lso47\n.Lro47:\nQmvva 172,32,72\nQda 48,53,8192\ns_add_u32 m0, s96, 65536\n"
"s_mov_b32 s95, 1433600\nQmvva 204,36,72\nbuffer_load_dwordx4 v40, s[52:55], s95 offen lds\nQmvva 236,20,76\nQta 120,46,20992\nQmvva 80,24,76\nbuffer_load_dwordx4 a[36:39], v60, s[48:51], s94 offen offset:1024 sc1\nQmvva 112,28,76\n"
"Qta 122,46,26112\nQmvva 144,32,76\nQta 124,46,21248\nQmvva 176,36,76\ns_waitcnt vmcnt(27)\nds_write_b128 v52, a[240:243] offset:33792\ns_waitcnt lgkmcnt(4)\nQmvva 208,20,80\n"
"Qmvva 240,24,80\nbuffer_load_dwordx4 v41, s[52:55], s95 offen offset:1024 lds\nQmvva 84,28,80\nQta 126,46,26368\nQmvva 88,32,80\nQda 52,53,12288\nQmvva 120,36,80\nQta 128,46,21504\n"
"Qmvva 152,20,48\nQta 130,46,26624\nQmvva 184,24,48\nbuffer_load_dwordx4 a[40:43], v60, s[48:51], s94 offen offset:2048 sc1\nQmvva 216,28,48\nQmvva 248,32,48\nbuffer_load_dwordx4 v42, s[52:55], s95 offen offset:2048 lds\nQmvva 116,36,48\n"
"s_waitcnt vmcnt(28)\nds_write_b128 v52, a[244:247] offset:37888\ns_waitcnt lgkmcnt(3)\nQmvva 96,20,52\nQmvva 128,24,52\nQmvva 160,28,52\nQmvva 192,32,52\nQda 56,53,16384\n"
"v_add_u32 v75, 1, v75\nQmvva 224,36,52\ns_waitcnt vmcnt(34)\nds_write_b32 v73, v75\ns_waitcnt lgkmcnt(3)\nQmvaa 92,112,224\ns_mov_b32 s94, 831488\ns_waitcnt vmcnt(12)\n"
"v_sub_u32 v66, v78, v65\nv_cmp_gt_u32 vcc, 5, v66\ns_and_b64 vcc, vcc, s[28:29]\ns_cbranch_scc1 .Lf5_w\n.Lf5_r:\nQmvaa 124,116,224\nbuffer_load_dwordx4 a[44:47], v60, s[48:51], s94 offen offset:0 sc1\nQmvaa 156,120,224\n"
"buffer_load_dwordx4 v43, s[52:55], s95 offen offset:3072 lds\nQmvaa 188,124,224\nQta 132,51,20480\nQmvaa 220,128,224\ns_waitcnt vmcnt(28)\nds_write_b128 v52, a[248:251] offset:41984\nQmvaa 252,112,228\nQmvaa 100,116,228\n"
"Qta 134,51,25600\nQmvaa 132,120,228\nds_read_b32 v69, v74\nQmvaa 164,124,228\nQda 60,53,20480\ns_add_u32 m0, s96, 69632\nQmvaa 196,128,228\nbuffer_load_dwordx4 v44, s[52:55], s95 offen lds\n"
"Qmvaa 228,112,232\nQta 136,51,20736\nQmvaa 108,116,232\ns_waitcnt lgkmcnt(2)\nv_cmp_gt_u32 vcc, v75, v69\nQmvaa 140,120,232\ns_cbranch_vccnz .Lso48\n.Lro48:\n"
"Qmvaa 172,124,232\nbuffer_load_dwordx4 a[208:211], v60, s[48:51], s94 offen offset:1024 sc1\nQmvaa 204,128,232\ns_waitcnt vmcnt(27)\nds_write_b128 v52, a[252:255] offset:46080\nQmvaa 236,112,236\nQmvaa 80,116,236\nQta 138,51,25856\n"
"Qmvaa 112,120,236\nQta 140,51,20992\nQmvaa 144,124,236\nQda 64,53,25600\nQmvaa 176,128,236\nQta 142,51,26112\nQmvaa 208,112,240\nQta 144,51,21248\n"
"Qmvaa 240,116,240\nbuffer_load_dwordx4 a[212:215], v60, s[48:51], s94 offen offset:2048 sc1\nQmvaa 84,120,240\nQta 146,51,26368\nQmvaa 88,124,240\nQta 148,51,21504\nQmvaa 120,128,240\ns_waitcnt vmcnt(26)\n"
"ds_write_b128 v52, a[168:171] offset:50176\nQmvaa 152,112,244\nQmvaa 184,116,244\nQta 150,51,26624\nQmvaa 216,120,244\nQmvaa 248,124,244\nQda 68,53,29696\nQmvaa 116,128,244\n"
"Qmvaa 96,112,248\nQmvaa 128,116,248\nbuffer_load_dwordx4 a[216:219], v60, s[48:51], s94 offen offset:3072 sc1\nQmvaa 160,120,248\nQmvaa 192,124,248\nv_add_u32 v75, 1, v75\nQmvaa 224,128,248\nds_write_b32 v73, v75\n"
"s_lshr_b32 s68, %19, 2\ns_xor_b32 s68, s68, s92\ns_cbranch_scc1 .Lrole1\ns_waitcnt lgkmcnt(2)\nQmvaa 92,132,56\ns_waitcnt vmcnt(26)\nds_write_b128 v52, a[172:175] offset:0\nQmvaa 124,136,56\n"
"Qmvaa 156,140,56\nQtv 0,45,0\nQmvaa 188,144,56\nQda 72,53,33792\nQmvaa 220,148,56\nQtv 2,45,5120\nQmvaa 252,132,60\nQtv 4,45,256\n"
"s_mov_b32 s94, 835584\nQmvaa 100,136,60\nbuffer_load_dwordx4 a[220:223], v60, s[48:51], s94 offen offset:0 sc1\nQmvaa 132,140,60\nds_read_b32 v69, v74\nQmvaa 164,144,60\nQtv 6,45,5376\nQmvaa 196,148,60\n"
"s_waitcnt vmcnt(26)\nds_write_b128 v52, a[0:3] offset:4096\ns_waitcnt lgkmcnt(15)\nQmvaa 228,132,64\nQmvaa 108,136,64\ns_waitcnt lgkmcnt(2)\nv_cmp_gt_u32 vcc, v75, v69\nQmvaa 140,140,64\n"
"s_cbranch_vccnz .Lso49r0\n.Lro49r0:\nQmvaa 172,144,64\nQda 76,53,37888\ns_add_u32 m0, s96, 86016\ns_mov_b32 s95, 1761280\nQmvaa 204,148,64\nbuffer_load_dwordx4 v40, s[52:55], s95 offen lds\n"
"Qmvaa 236,132,68\nQtv 8,45,512\nQmvaa 80,136,68\nbuffer_load_dwordx4 a[224:227], v60, s[48:51], s94 offen offset:1024 sc1\nQmvaa 112,140,68\nQtv 10,45,5632\nQmvaa 144,144,68\nQtv 12,45,768\n"
"Qmvaa 176,148,68\ns_waitcnt vmcnt(27)\nds_write_b128 v52, a[4:7] offset:8192\ns_waitcnt lgkmcnt(4)\nQmvaa 208,132,72\nQmvaa 240,136,72\nbuffer_load_dwordx4 v41, s[52:55], s95 offen offset:1024 lds\nQmvaa 84,140,72\n"
"Qtv 14,45,5888\nQmvaa 88,144,72\nQda 80,53,41984\nQmvaa 120,148,72\nQtv 16,45,1024\nQmvaa 152,132,76\nQtv 18,45,6144\nQmvaa 184,136,76\n"
"buffer_load_dwordx4 a[228:231], v60, s[48:51], s94 offen offset:2048 sc1\nQmvaa 216,140,76\nQmvaa 248,144,76\nbuffer_load_dwordx4 v42, s[52:55], s95 offen offset:2048 lds\nQmvaa 116,148,76\ns_waitcnt vmcnt(28)\nds_write_b128 v52, a[8:11] offset:12288\ns_waitcnt lgkmcnt(3)\n"
"Qmvaa 96,132,80\nQmvaa 128,136,80\nQmvaa 160,140,80\nQmvaa 192,144,80\nQda 48,53,46080\nv_add_u32 v75, 1, v75\nQmvaa 224,148,80\ns_waitcnt vmcnt(34)\n"
"ds_write_b32 v73, v75\ns_waitcnt lgkmcnt(3)\nQmvva 92,0,252\nbuffer_load_dwordx4 v43, s[52:55], s95 offen offset:3072 lds\ns_mov_b32 s94, 505856\nQmvva 124,4,252\nbuffer_load_dwordx4 a[232:235], v60, s[48:51], s94 offen offset:0 sc1\nQmvva 156,8,252\n"
"Qtv 20,47,0\nQmvva 188,12,252\nQtv 22,47,5120\nQmvva 220,16,252\ns_waitcnt vmcnt(28)\nds_write_b128 v52, a[12:15] offset:16384\nQmvva 252,0,168\nQmvva 100,4,168\n"
"Qtv 24,47,256\nQmvva 132,8,168\nds_read_b32 v69, v74\nQmvva 164,12,168\nQda 52,53,50176\ns_add_u32 m0, s96, 90112\nQmvva 196,16,168\nbuffer_load_dwordx4 v44, s[52:55], s95 offen lds\n"
"Qmvva 228,0,172\nQtv 26,47,5376\nQmvva 108,4,172\ns_waitcnt lgkmcnt(2)\nv_cmp_gt_u32 vcc, v75, v69\nQmvva 140,8,172\ns_cbranch_vccnz .Lso50r0\n.Lro50r0:\n"
"Qmvva 172,12,172\nbuffer_load_dwordx4 a[236:239], v60, s[48:51], s94 offen offset:1024 sc1\nQmvva 204,16,172\ns_waitcnt vmcnt(27)\nds_write_b128 v52, a[16:19] offset:20480\nQmvva 236,0,0\nQmvva 80,4,0\nQtv 28,47,512\n"
"Qmvva 112,8,0\nQmvva 144,12,0\nQda 56,53,0\nQmvva 176,16,0\nQtv 30,47,5632\nQmvva 208,0,4\nQtv 32,47,768\nQmvva 240,4,4\n"
"buffer_load_dwordx4 a[240:243], v60, s[48:51], s94 offen offset:2048 sc1\nQmvva 84,8,4\nQtv 34,47,5888\nQmvva 88,12,4\nQtv 36,47,1024\nQmvva 120,16,4\ns_waitcnt vmcnt(25)\nds_write_b128 v52, a[20:23] offset:25600\n"
"Qmvva 152,0,8\nQmvva 184,4,8\nQtv 38,47,6144\nQmvva 216,8,8\nQmvva 248,12,8\nQda 60,53,4096\nQmvva 116,16,8\nQmvva 96,0,12\n"
"s_mov_b32 s94, 620544\nQmvva 128,4,12\nbuffer_load_dwordx4 a[244:247], v60, s[48:51], s94 offen offset:0 sc1\nQmvva 160,8,12\nQmvva 192,12,12\nv_add_u32 v75, 1, v75\nQmvva 224,16,12\nds_write_b32 v73, v75\n"
"s_waitcnt lgkmcnt(2)\nQmvva 92,20,48\ns_waitcnt vmcnt(25)\nds_write_b128 v52, a[24:27] offset:29696\nQmvva 124,24,48\nQmvva 156,28,48\nQta 112,45,20480\nQmvva 188,32,48\n"
"Qda 64,53,8192\nQmvva 220,36,48\nQta 114,45,25600\nQmvva 252,20,52\nQta 116,45,20736\nQmvva 100,24,52\nbuffer_load_dwordx4 a[248:251], v60, s[48:51], s94 offen offset:1024 sc1\nQmvva 132,28,52\n"
"ds_read_b32 v69, v74\nQmvva 164,32,52\nQta 118,45,25856\nQmvva 196,36,52\ns_waitcnt vmcnt(25)\nds_write_b128 v52, a[28:31] offset:33792\ns_waitcnt lgkmcnt(15)\nQmvva 228,20,56\n"
"Qmvva 108,24,56\ns_waitcnt lgkmcnt(2)\nv_cmp_gt_u32 vcc, v75, v69\nQmvva 140,28,56\ns_cbranch_vccnz .Lso51r0\n.Lro51r0:\nQmvva 172,32,56\nQda 68,53,12288\n"
"s_add_u32 m0, s96, 4096\ns_mov_b32 s95, 2088960\nQmvva 204,36,56\nbuffer_load_dwordx4 v40, s[52:55], s95 offen lds\nQmvva 236,20,60\nQta 120,45,20992\nQmvva 80,24,60\nbuffer_load_dwordx4 a[252:255], v60, s[48:51], s94 offen offset:2048 sc1\n"
"Qmvva 112,28,60\nQta 122,45,26112\nQmvva 144,32,60\nQta 124,45,21248\nQmvva 176,36,60\ns_waitcnt vmcnt(26)\nds_write_b128 v52, a[32:35] offset:37888\ns_waitcnt lgkmcnt(4)\n"
"Qmvva 208,20,64\nQmvva 240,24,64\nbuffer_load_dwordx4 v41, s[52:55], s95 offen offset:1024 lds\nQmvva 84,28,64\nQta 126,45,26368\nQmvva 88,32,64\nQda 72,53,16384\nQmvva 120,36,64\n"
"Qta 128,45,21504\nQmvva 152,20,68\n.Lpg21:\ns_cbranch_execz .Lpg25\nQta 130,45,26624\ns_mov_b32 s94, 735232\nQmvva 184,24,68\nbuffer_load_dwordx4 a[168:171], v60, s[48:51], s94 offen offset:0 sc1\n"
"Qmvva 216,28,68\nQmvva 248,32,68\nbuffer_load_dwordx4 v42, s[52:55], s95 offen offset:2048 lds\nQmvva 116,36,68\ns_waitcnt vmcnt(27)\nds_write_b128 v52, a[36:39] offset:41984\ns_waitcnt lgkmcnt(3)\nQmvva 96,20,72\n"
"Qmvva 128,24,72\nQmvva 160,28,72\nQmvva 192,32,72\nQda 76,53,20480\nv_add_u32 v75, 1, v75\nQmvva 224,36,72\ns_waitcnt vmcnt(33)\nds_write_b32 v73, v75\n"
"s_waitcnt lgkmcnt(3)\nQmvaa 92,112,16\nbuffer_load_dwordx4 v43, s[52:55], s95 offen offset:3072 lds\nQmvaa 124,116,16\nbuffer_load_dwordx4 a[172:175], v60, s[48:51], s94 offen offset:1024 sc1\nQmvaa 156,120,16\nQta 132,47,20480\nQmvaa 188,124,16\n"
"Qta 134,47,25600\nQmvaa 220,128,16\ns_waitcnt vmcnt(27)\nds_write_b128 v52, a[40:43] offset:46080\nQmvaa 252,112,20\nQmvaa 100,116,20\nQta 136,47,20736\nQmvaa 132,120,20\n"
"ds_read_b32 v69, v74\nQmvaa 164,124,20\nQda 80,53,25600\ns_add_u32 m0, s96, 8192\nQmvaa 196,128,20\nbuffer_load_dwordx4 v44, s[52:55], s95 offen lds\nQmvaa 228,112,24\nQta 138,47,25856\n"
"Qmvaa 108,116,24\ns_waitcnt lgkmcnt(2)\nv_cmp_gt_u32 vcc, v75, v69\nQmvaa 140,120,24\ns_cbranch_vccnz .Lso52r0\n.Lro52r0:\nQmvaa 172,124,24\nbuffer_load_dwordx4 a[0:3], v60, s[48:51], s94 offen offset:2048 sc1\n"
"Qmvaa 204,128,24\ns_waitcnt vmcnt(27)\nds_write_b128 v52, a[44:47] offset:50176\nQmvaa 236,112,28\nQmvaa 80,116,28\nQta 140,47,20992\nQmvaa 112,120,28\nQmvaa 144,124,28\n"
"Qda 48,53,29696\nQmvaa 176,128,28\nQta 142,47,26112\nQmvaa 208,112,32\nQta 144,47,21248\ns_mov_b32 s94, 849920\nQmvaa 240,116,32\nbuffer_load_dwordx4 a[4:7], v60, s[48:51], s94 offen offset:0 sc1\n"
"Qmvaa 84,120,32\nQta 146,47,26368\nQmvaa 88,124,32\nQta 148,47,21504\nQmvaa 120,128,32\ns_waitcnt vmcnt(25)\nds_write_b128 v52, a[208:211] offset:0\nQmvaa 152,112,36\n"
"Qmvaa 184,116,36\nQta 150,47,26624\nQmvaa 216,120,36\nQmvaa 248,124,36\nQda 52,53,33792\nQmvaa 116,128,36\nQmvaa 96,112,40\nQmvaa 128,116,40\n"
"buffer_load_dwordx4 a[8:11], v60, s[48:51], s94 offen offset:1024 sc1\nQmvaa 160,120,40\nQmvaa 192,124,40\nv_add_u32 v75, 1, v75\nQmvaa 224,128,40\nds_write_b32 v73, v75\ns_waitcnt lgkmcnt(2)\nQmvaa 92,132,76\n"
"s_waitcnt vmcnt(25)\nds_write_b128 v52, a[212:215] offset:4096\nQmvaa 124,136,76\nQmvaa 156,140,76\nQtv 0,45,40960\nQmvaa 188,144,76\nQda 56,53,37888\nQmvaa 220,148,76\n"
"Qtv 2,45,46080\nQmvaa 252,132,80\nQtv 4,45,41216\nQmvaa 100,136,80\nbuffer_load_dwordx4 a[12:15], v60, s[48:51], s94 offen offset:2048 sc1\nQmvaa 132,140,80\nds_read_b32 v69, v74\nQmvaa 164,144,80\n"
"Qtv 6,45,46336\nQmvaa 196,148,80\ns_waitcnt vmcnt(25)\nds_write_b128 v52, a[216:219] offset:8192\ns_waitcnt lgkmcnt(15)\nQmvaa 228,132,48\nQmvaa 108,136,48\ns_waitcnt lgkmcnt(2)\n"
"v_cmp_gt_u32 vcc, v75, v69\nQmvaa 140,140,48\ns_cbranch_vccnz .Lso53r0\n.Lro53r0:\nQmvaa 172,144,48\nQda 60,53,41984\ns_add_u32 m0, s96, 24576\ns_mov_b32 s95, 2416640\n"
"Qmvaa 204,148,48\nbuffer_load_dwordx4 v40, s[52:55], s95 offen lds\nQmvaa 236,132,52\nQtv 8,45,41472\ns_mov_b32 s94, 501760\nQmvaa 80,136,52\nbuffer_load_dwordx4 a[16:19], v60, s[48:51], s94 offen offset:0 sc1\nQmvaa 112,140,52\n"
"Qtv 10,45,46592\nQmvaa 144,144,52\nQtv 12,45,41728\nQmvaa 176,148,52\ns_waitcnt vmcnt(26)\nds_write_b128 v52, a[220:223] offset:12288\ns_waitcnt lgkmcnt(4)\nQmvaa 208,132,56\n"
"Qmvaa 240,136,56\nbuffer_load_dwordx4 v41, s[52:55], s95 offen offset:1024 lds\nQmvaa 84,140,56\nQtv 14,45,46848\nQmvaa 88,144,56\nQda 64,53,46080\nQmvaa 120,148,56\nQtv 16,45,41984\n"
"Qmvaa 152,132,60\nQtv 18,45,47104\nQmvaa 184,136,60\nbuffer_load_dwordx4 a[20:23], v60, s[48:51], s94 offen offset:1024 sc1\nQmvaa 216,140,60\nQmvaa 248,144,60\nbuffer_load_dwordx4 v42, s[52:55], s95 offen offset:2048 lds\nQmvaa 116,148,60\n"
"s_waitcnt vmcnt(27)\nds_write_b128 v52, a[224:227] offset:16384\ns_waitcnt lgkmcnt(3)\nQmvaa 96,132,64\nQmvaa 128,136,64\nQmvaa 160,140,64\nQmvaa 192,144,64\nQda 68,53,50176\n"
"v_add_u32 v75, 1, v75\nQmvaa 224,148,64\ns_waitcnt vmcnt(33)\nds_write_b32 v73, v75\ns_waitcnt lgkmcnt(3)\nQmvva 92,0,44\nbuffer_load_dwordx4 v43, s[52:55], s95 offen offset:3072 lds\nQmvva 124,4,44\n"
"buffer_load_dwordx4 a[24:27], v60, s[48:51], s94 offen offset:2048 sc1\nQmvva 156,8,44\nQtv 20,47,40960\nQmvva 188,12,44\nQtv 22,47,46080\nQmvva 220,16,44\ns_waitcnt vmcnt(27)\nds_write_b128 v52, a[228:231] offset:20480\n"
"Qmvva 252,0,208\nQmvva 100,4,208\nQtv 24,47,41216\nQmvva 132,8,208\nds_read_b32 v69, v74\nQmvva 164,12,208\nQda 72,53,0\ns_add_u32 m0, s96, 28672\n"
"Qmvva 196,16,208\nbuffer_load_dwordx4 v44, s[52:55], s95 offen lds\nQmvva 228,0,212\nQtv 26,47,46336\nQmvva 108,4,212\ns_waitcnt lgkmcnt(2)\nv_cmp_gt_u32 vcc, v75, v69\nQmvva 140,8,212\n"
"s_cbranch_vccnz .Lso54r0\n.Lro54r0:\nQmvva 172,12,212\nbuffer_load_dwordx4 a[28:31], v60, s[48:51], s94 offen offset:3072 sc1\nQmvva 204,16,212\ns_waitcnt vmcnt(26)\nds_write_b128 v52, a[232:235] offset:25600\nQmvva 236,0,216\n"
"Qmvva 80,4,216\nQtv 28,47,41472\nQmvva 112,8,216\nQmvva 144,12,216\nQda 76,53,4096\nQmvva 176,16,216\nQtv 30,47,46592\nQmvva 208,0,220\n"
"Qtv 32,47,41728\ns_mov_b32 s94, 616448\nQmvva 240,4,220\nbuffer_load_dwordx4 a[32:35], v60, s[48:51], s94 offen offset:0 sc1\nQmvva 84,8,220\nQtv 34,47,46848\nQmvva 88,12,220\nQtv 36,47,41984\n"
"Qmvva 120,16,220\ns_waitcnt vmcnt(25)\nds_write_b128 v52, a[236:239] offset:29696\nQmvva 152,0,224\nQmvva 184,4,224\nQtv 38,47,47104\nQmvva 216,8,224\nQmvva 248,12,224\n"
"Qda 80,53,8192\nQmvva 116,16,224\nQmvva 96,0,228\nQmvva 128,4,228\nbuffer_load_dwordx4 a[36:39], v60, s[48:51], s94 offen offset:1024 sc1\nQmvva 160,8,228\nQmvva 192,12,228\nv_add_u32 v75, 1, v75\n"
"Qmvva 224,16,228\ns_waitcnt vmcnt(27)\nds_write_b32 v73, v75\ns_waitcnt lgkmcnt(2)\nQmvva 92,20,68\ns_waitcnt vmcnt(25)\nds_write_b128 v52, a[240:243] offset:33792\nQmvva 124,24,68\n"
"Qmvva 156,28,68\nQta 112,46,0\nQmvva 188,32,68\nQda 48,53,12288\nQmvva 220,36,68\nQta 114,46,5120\nQmvva 252,20,72\nQta 116,46,256\n"
"Qmvva 100,24,72\nbuffer_load_dwordx4 a[40:43], v60, s[48:51], s94 offen offset:2048 sc1\nQmvva 132,28,72\nds_read_b32 v69, v74\nQmvva 164,32,72\nQmvva 196,36,72\ns_waitcnt vmcnt(25)\nds_write_b128 v52, a[244:247] offset:37888\n"
"s_waitcnt lgkmcnt(15)\nQmvva 228,20,76\nQmvva 108,24,76\ns_waitcnt lgkmcnt(1)\nv_cmp_gt_u32 vcc, v75, v69\nQmvva 140,28,76\ns_cbranch_vccnz .Lso55r0\n.Lro55r0:\n"
"Qmvva 172,32,76\nQda 52,53,16384\nQmvva 204,36,76\nQta 118,46,5376\nQmvva 236,20,80\nQta 120,46,512\nQmvva 80,24,80\nbuffer_load_dwordx4 a[44:47], v60, s[48:51], s94 offen offset:3072 sc1\n"
"Qmvva 112,28,80\nQmvva 144,32,80\nQta 122,46,5632\nQmvva 176,36,80\ns_waitcnt vmcnt(25)\nds_write_b128 v52, a[248:251] offset:41984\ns_waitcnt lgkmcnt(4)\nQmvva 208,20,48\n"
"Qmvva 240,24,48\nQta 124,46,768\nQmvva 84,28,48\nQta 126,46,5888\nQmvva 88,32,48\nQda 56,53,20480\nQmvva 120,36,48\nQmvva 152,20,52\n"
"Qta 128,46,1024\ns_mov_b32 s94, 731136\nQmvva 184,24,52\nbuffer_load_dwordx4 a[208:211], v60, s[48:51], s94 offen offset:0 sc1\nQmvva 216,28,52\nQta 130,46,6144\nQmvva 248,32,52\nQta 132,51,0\n"
"Qta 134,51,5120\nQmvva 116,36,52\ns_waitcnt vmcnt(24)\nds_write_b128 v52, a[252:255] offset:46080\ns_waitcnt lgkmcnt(5)\nQmvva 96,20,56\nQmvva 128,24,56\nQta 136,51,256\n"
"Qmvva 160,28,56\nQta 138,51,5376\nQmvva 192,32,56\nQda 60,53,25600\nv_add_u32 v75, 1, v75\nQmvva 224,36,56\ns_waitcnt vmcnt(18)\nds_write_b32 v73, v75\n"
"s_waitcnt lgkmcnt(7)\nQmvaa 208,112,232\nQta 140,51,512\nQmvaa 240,116,232\nbuffer_load_dwordx4 a[212:215], v60, s[48:51], s94 offen offset:1024 sc1\nQmvaa 84,120,232\nQta 142,51,5632\nQmvaa 88,124,232\n"
"Qta 144,51,768\nQmvaa 120,128,232\ns_waitcnt vmcnt(23)\nds_write_b128 v52, a[168:171] offset:50176\nQmvaa 152,112,236\nQmvaa 184,116,236\nQta 146,51,5888\nQmvaa 216,120,236\n"
"ds_read_b32 v69, v74\nQmvaa 248,124,236\nQda 64,53,29696\nQmvaa 116,128,236\nQta 148,51,1024\nQmvaa 96,112,240\nQta 150,51,6144\nQmvaa 128,116,240\n"
"s_waitcnt lgkmcnt(3)\nv_cmp_gt_u32 vcc, v75, v69\nQmvaa 160,120,240\ns_cbranch_vccnz .Lso56r0\n.Lro56r0:\nQmvaa 192,124,240\nbuffer_load_dwordx4 a[216:219], v60, s[48:51], s94 offen offset:2048 sc1\nQmvaa 224,128,240\n"
"s_waitcnt vmcnt(21)\nds_write_b128 v52, a[172:175] offset:0\ns_waitcnt lgkmcnt(1)\nQmvaa 208,132,60\nQmvaa 240,136,60\nQtv 0,46,20480\nQtv 2,46,25600\nQmvaa 84,140,60\n"
"Qtv 4,46,20736\nQtv 6,46,25856\nQmvaa 88,144,60\nQda 68,53,33792\nQmvaa 120,148,60\nQtv 8,46,20992\nQmvaa 152,132,64\nQtv 10,46,26112\n"
"Qtv 12,46,21248\nQmvaa 184,136,64\nbuffer_load_dwordx4 a[220:223], v60, s[48:51], s94 offen offset:3072 sc1\nQmvaa 216,140,64\nQtv 14,46,26368\nQtv 16,46,21504\nQmvaa 248,144,64\nQtv 18,46,26624\n"
"Qmvaa 116,148,64\ns_waitcnt vmcnt(20)\nds_write_b128 v52, a[0:3] offset:4096\ns_waitcnt lgkmcnt(7)\nQmvaa 96,132,68\nQmvaa 128,136,68\nQtv 20,51,20480\nQtv 22,51,25600\n"
"Qmvaa 160,140,68\nQtv 24,51,20736\nQmvaa 192,144,68\nQda 72,53,37888\nQmvaa 224,148,68\nQtv 26,51,25856\ns_waitcnt lgkmcnt(6)\nQmvva 208,0,244\n"
"Qtv 28,51,20992\ns_mov_b32 s94, 845824\nQmvva 240,4,244\nbuffer_load_dwordx4 a[224:227], v60, s[48:51], s94 offen offset:0 sc1\nQmvva 84,8,244\nQtv 30,51,26112\nQmvva 88,12,244\nQtv 32,51,21248\n"
"v_add_u32 v75, 1, v75\nQmvva 120,16,244\ns_waitcnt vmcnt(10)\nds_write_b32 v73, v75\nQmvva 152,0,248\ns_waitcnt vmcnt(20)\nds_write_b128 v52, a[4:7] offset:8192\nQmvva 184,4,248\n"
"Qmvva 216,8,248\nQtv 34,51,26368\nQmvva 248,12,248\nQda 76,53,41984\nQmvva 116,16,248\nQtv 36,51,21504\nQmvva 96,0,252\nQtv 38,51,26624\n"
"Qmvva 128,4,252\nbuffer_load_dwordx4 a[228:231], v60, s[48:51], s94 offen offset:1024 sc1\nQmvva 160,8,252\nds_read_b32 v69, v74\nQmvva 192,12,252\nQta 112,45,0\nQta 114,45,5120\nQmvva 224,16,252\n"
"s_waitcnt vmcnt(20)\nds_write_b128 v52, a[8:11] offset:12288\ns_waitcnt lgkmcnt(13)\nQmvva 208,20,72\nQmvva 240,24,72\ns_waitcnt lgkmcnt(3)\nv_cmp_gt_u32 vcc, v75, v69\nQmvva 84,28,72\n"
"s_cbranch_vccnz .Lso57r0\n.Lro57r0:\nQmvva 88,32,72\nQda 80,53,46080\nQmvva 120,36,72\nQta 116,45,256\nQta 118,45,5376\nQmvva 152,20,76\n"
"Qta 120,45,512\nQta 122,45,5632\nQmvva 184,24,76\nbuffer_load_dwordx4 a[232:235], v60, s[48:51], s94 offen offset:2048 sc1\nQmvva 216,28,76\nQta 124,45,768\nQta 126,45,5888\nQmvva 248,32,76\n"
"Qta 128,45,1024\nQta 130,45,6144\nQmvva 116,36,76\ns_waitcnt vmcnt(20)\nds_write_b128 v52, a[12:15] offset:16384\ns_waitcnt lgkmcnt(9)\nQmvva 96,20,80\nQmvva 128,24,80\n"
"Qta 132,47,0\nQta 134,47,5120\nQmvva 160,28,80\nQta 136,47,256\nQmvva 192,32,80\nQda 48,53,50176\nQmvva 224,36,80\nQta 138,47,5376\n"
"s_waitcnt lgkmcnt(6)\nQmvaa 208,112,168\nQta 140,47,512\nQmvaa 240,116,168\nbuffer_load_dwordx4 a[236:239], v60, s[48:51], s94 offen offset:3072 sc1\nQmvaa 84,120,168\nQta 142,47,5632\nQmvaa 88,124,168\n"
"Qta 144,47,768\nQmvaa 120,128,168\ns_waitcnt vmcnt(19)\nds_write_b128 v52, a[16:19] offset:20480\nQmvaa 152,112,172\nQmvaa 184,116,172\nQta 146,47,5888\nQmvaa 216,120,172\n"
"Qta 148,47,1024\nQmvaa 248,124,172\nQda 52,53,0\nv_add_u32 v75, 1, v75\nQmvaa 116,128,172\nds_write_b32 v73, v75\nQmvaa 96,112,0\nQta 150,47,6144\n"
"Qmvaa 128,116,0\nQtv 0,45,20480\nQtv 2,45,25600\nQmvaa 160,120,0\nQtv 4,45,20736\nQmvaa 192,124,0\nQtv 6,45,25856\nQtv 8,45,20992\n"
"Qmvaa 224,128,0\ns_waitcnt vmcnt(17)\nds_write_b128 v52, a[20:23] offset:25600\ns_waitcnt lgkmcnt(6)\nQmvaa 208,132,48\nQmvaa 240,136,48\nQtv 10,45,26112\nQmvaa 84,140,48\n"
"ds_read_b32 v69, v74\nQmvaa 88,144,48\nQda 56,53,4096\nQmvaa 120,148,48\nQtv 12,45,21248\nQtv 14,45,26368\nQmvaa 152,132,52\nQtv 16,45,21504\n"
"Qmvaa 184,136,52\ns_waitcnt lgkmcnt(4)\nv_cmp_gt_u32 vcc, v75, v69\nQmvaa 216,140,52\ns_cbranch_vccnz .Lso58r0\n.Lro58r0:\nQmvaa 248,144,52\nQtv 18,45,26624\n"
"Qmvaa 116,148,52\ns_waitcnt vmcnt(14)\nds_write_b128 v52, a[24:27] offset:29696\ns_waitcnt lgkmcnt(5)\nQmvaa 96,132,56\nQmvaa 128,136,56\nQtv 20,47,20480\nQmvaa 160,140,56\n"
"Qtv 22,47,25600\nQmvaa 192,144,56\nQda 60,53,8192\nQmvaa 224,148,56\nQtv 24,47,20736\ns_waitcnt lgkmcnt(5)\nQmvva 208,0,4\nQtv 26,47,25856\n"
"Qmvva 240,4,4\nQtv 28,47,20992\nQmvva 84,8,4\nQtv 30,47,26112\nQmvva 88,12,4\nQtv 32,47,21248\nQmvva 120,16,4\ns_waitcnt vmcnt(12)\n"
"ds_write_b128 v52, a[28:31] offset:33792\nQmvva 152,0,8\nQmvva 184,4,8\nQtv 34,47,26368\nQmvva 216,8,8\nQtv 36,47,21504\nQmvva 248,12,8\nQda 64,53,12288\n"
"Qmvva 116,16,8\nQtv 38,47,26624\nQmvva 96,0,12\nQta 112,46,0\nQta 114,46,5120\nQmvva 128,4,12\nQta 116,46,256\nQmvva 160,8,12\n"
"Qta 118,46,5376\nQmvva 192,12,12\n.Lpg22:\ns_cbranch_execz .Lpg26\nQta 120,46,512\nv_add_u32 v75, 1, v75\nQmvva 224,16,12\nds_write_b32 v73, v75\n"
"s_waitcnt lgkmcnt(6)\nQmvva 208,20,60\ns_waitcnt vmcnt(11)\nds_write_b128 v52, a[32:35] offset:37888\nQmvva 240,24,60\ns_add_u32 s68, s64, 0\nQmvva 84,28,60\nQta 122,46,5632\n"
"Qmvva 88,32,60\nQda 68,53,16384\nQmvva 120,36,60\nQta 124,46,768\nQmvva 152,20,64\nQta 126,46,5888\nQmvva 184,24,64\nQta 128,46,1024\n"
"Qk 208,208,209\nQmvva 216,28,64\nds_read_b32 v69, v74\nv_min_u32 v208, -2, v208\nQmvva 248,32,64\nQta 130,46,6144\nQk 209,210,211\nQmvva 116,36,64\n"
"s_waitcnt vmcnt(10)\nds_write_b128 v52, a[36:39] offset:41984\ns_waitcnt lgkmcnt(6)\nQmvva 96,20,68\nv_min_u32 v209, -2, v209\nQk 210,240,241\nQmvva 128,24,68\ns_waitcnt lgkmcnt(2)\n"
"v_cmp_gt_u32 vcc, v75, v69\nQmvva 160,28,68\ns_cbranch_vccnz .Lso59r0\n.Lro59r0:\nv_min_u32 v210, -2, v210\nQmvva 192,32,68\nQda 72,53,20480\nQk 211,242,243\n"
"Qmvva 224,36,68\nQta 132,51,0\nv_min_u32 v211, -2, v211\ns_waitcnt lgkmcnt(3)\nQmvaa 92,112,16\nQta 134,51,5120\nbuffer_store_dwordx4 v[208:211], v60, s[24:27], s68 offen offset:0 sc1\nQmvaa 124,116,16\n"
"Qta 136,51,256\nQk 84,84,85\nQmvaa 156,120,16\nQta 138,51,5376\nv_min_u32 v84, -2, v84\nQmvaa 188,124,16\nQta 140,51,512\nQk 85,86,87\n"
"Qmvaa 220,128,16\ns_waitcnt vmcnt(10)\nds_write_b128 v52, a[40:43] offset:46080\nQmvaa 252,112,20\nv_min_u32 v85, -2, v85\nQk 86,88,89\nQmvaa 100,116,20\nQta 142,51,5632\n"
"v_min_u32 v86, -2, v86\nQmvaa 132,120,20\nQta 144,51,768\nQk 87,90,91\nQmvaa 164,124,20\nQda 76,53,25600\nv_min_u32 v87, -2, v87\nQmvaa 196,128,20\n"
"Qta 146,51,5888\nbuffer_store_dwordx4 v[84:87], v60, s[24:27], s68 offen offset:1024 sc1\nQmvaa 228,112,24\nQta 148,51,1024\nQk 120,120,121\nQmvaa 108,116,24\nQta 150,51,6144\nv_min_u32 v120, -2, v120\n"
"Qmvaa 140,120,24\nQtv 0,46,20480\nQk 121,122,123\nQmvaa 172,124,24\nQtv 2,46,25600\nv_min_u32 v121, -2, v121\nQmvaa 204,128,24\ns_waitcnt vmcnt(10)\n"
"ds_write_b128 v52, a[44:47] offset:50176\nQmvaa 236,112,28\nQk 122,152,153\nv_min_u32 v122, -2, v122\nQmvaa 80,116,28\nQtv 4,46,20736\nQk 123,154,155\nQmvaa 112,120,28\n"
"Qtv 6,46,25856\nv_min_u32 v123, -2, v123\nQmvaa 144,124,28\nQda 80,53,29696\nv_add_u32 v75, 1, v75\nQmvaa 176,128,28\nds_write_b32 v73, v75\nbuffer_store_dwordx4 v[120:123], v60, s[24:27], s68 offen offset:2048 sc1\n"
"s_waitcnt lgkmcnt(7)\nQmvaa 92,132,72\nQtv 8,46,20992\nQk 184,184,185\nQmvaa 124,136,72\nQtv 10,46,26112\nv_min_u32 v184, -2, v184\nQmvaa 156,140,72\n"
"Qtv 12,46,21248\nQk 185,186,187\nQmvaa 188,144,72\nQtv 14,46,26368\nv_min_u32 v185, -2, v185\nQmvaa 220,148,72\ns_waitcnt vmcnt(10)\nds_write_b128 v52, a[208:211] offset:0\n"
"Qmvaa 252,132,76\nQk 186,216,217\nv_min_u32 v186, -2, v186\nQmvaa 100,136,76\nQtv 16,46,21504\nQk 187,218,219\nQmvaa 132,140,76\nds_read_b32 v69, v74\n"
"v_min_u32 v187, -2, v187\nQmvaa 164,144,76\nQda 48,53,33792\nbuffer_store_dwordx4 v[184:187], v60, s[24:27], s68 offen offset:3072 sc1\nQmvaa 196,148,76\nQtv 18,46,26624\nQk 248,248,249\ns_waitcnt lgkmcnt(10)\n"
"Qmvaa 228,132,80\nv_min_u32 v248, -2, v248\nQk 249,250,251\nQmvaa 108,136,80\ns_waitcnt lgkmcnt(1)\nv_cmp_gt_u32 vcc, v75, v69\nQmvaa 140,140,80\ns_cbranch_vccnz .Lso60r0\n"
".Lro60r0:\nv_min_u32 v249, -2, v249\nQmvaa 172,144,80\nQtv 20,51,20480\nQk 250,116,117\nQmvaa 204,148,80\ns_waitcnt vmcnt(10)\nds_write_b128 v52, a[212:215] offset:4096\n"
"Qmvaa 236,132,48\nv_min_u32 v250, -2, v250\nQk 251,118,119\nQmvaa 80,136,48\nQtv 22,51,25600\nv_min_u32 v251, -2, v251\nQmvaa 112,140,48\nQtv 24,51,20736\n"
"s_add_u32 s68, s64, 4096\nQmvaa 144,144,48\nQda 52,53,37888\nbuffer_store_dwordx4 v[248:251], v60, s[24:27], s68 offen offset:0 sc1\nQmvaa 176,148,48\nQtv 26,51,25856\nQk 96,96,97\ns_waitcnt lgkmcnt(6)\n"
"Qmvva 92,0,32\nv_min_u32 v96, -2, v96\nQk 97,98,99\nQmvva 124,4,32\nQtv 28,51,20992\nv_min_u32 v97, -2, v97\nQmvva 156,8,32\nQtv 30,51,26112\n"
"Qk 98,128,129\nQmvva 188,12,32\nQtv 32,51,21248\nv_min_u32 v98, -2, v98\nQmvva 220,16,32\ns_waitcnt vmcnt(10)\nds_write_b128 v52, a[216:219] offset:8192\nQmvva 252,0,36\n"
"Qk 99,130,131\nv_min_u32 v99, -2, v99\nQmvva 100,4,36\nbuffer_store_dwordx4 v[96:99], v60, s[24:27], s68 offen offset:1024 sc1\nQk 160,160,161\nQmvva 132,8,36\nQtv 34,51,26368\nv_min_u32 v160, -2, v160\n"
"Qmvva 164,12,36\nQda 56,53,41984\nQk 161,162,163\nQmvva 196,16,36\nQtv 36,51,21504\nv_min_u32 v161, -2, v161\nQmvva 228,0,40\nQtv 38,51,26624\n"
"Qk 162,192,193\nQmvva 108,4,40\nv_min_u32 v162, -2, v162\nQk 163,194,195\nQmvva 140,8,40\nQta 112,45,0\nv_min_u32 v163, -2, v163\nQmvva 172,12,40\n"
"Qta 114,45,5120\nv_add_u32 v75, 1, v75\nQmvva 204,16,40\nds_write_b32 v73, v75\nbuffer_store_dwordx4 v[160:163], v60, s[24:27], s68 offen offset:2048 sc1\nQmvva 236,0,44\ns_waitcnt vmcnt(11)\nds_write_b128 v52, a[220:223] offset:12288\n"
"Qmvva 80,4,44\nQk 224,224,225\nv_min_u32 v224, -2, v224\nQmvva 112,8,44\nQta 116,45,256\nQk 225,226,227\nQmvva 144,12,44\nQda 60,53,46080\n"
"v_min_u32 v225, -2, v225\nQmvva 176,16,44\nQta 118,45,5376\ns_add_u32 s68, s64, 14336\ns_waitcnt lgkmcnt(7)\nQmvva 92,20,52\nQta 120,45,512\nbuffer_store_dwordx4 v[224:227], v60, s[24:27], s68 offen offset:0 sc1\n"
"Qmvva 124,24,52\nQta 122,45,5632\nQmvva 156,28,52\nds_read_b32 v69, v74\nQmvva 188,32,52\nQta 124,45,768\nQmvva 220,36,52\ns_waitcnt vmcnt(11)\n"
"ds_write_b128 v52, a[224:227] offset:16384\nQmvva 252,20,56\nQmvva 100,24,56\ns_waitcnt lgkmcnt(2)\nv_cmp_gt_u32 vcc, v75, v69\nQmvva 132,28,56\ns_cbranch_vccnz .Lso61r0\n.Lro61r0:\n"
"Qmvva 164,32,56\nQda 64,53,50176\nQmvva 196,36,56\nQta 126,45,5888\ns_waitcnt lgkmcnt(1)\nQmvva 228,20,60\nQta 128,45,1024\nQmvva 108,24,60\n"
"Qta 130,45,6144\nQmvva 140,28,60\nQta 132,47,0\nQmvva 172,32,60\nQta 134,47,5120\nQmvva 204,36,60\ns_waitcnt vmcnt(10)\nds_write_b128 v52, a[228:231] offset:20480\n"
"Qmvva 236,20,64\nQmvva 80,24,64\nQta 136,47,256\nQmvva 112,28,64\nQta 138,47,5376\nQmvva 144,32,64\nQda 68,53,0\nQmvva 176,36,64\n"
"s_waitcnt lgkmcnt(6)\nQmvaa 92,112,208\nQta 140,47,512\nQmvaa 124,116,208\nQta 142,47,5632\nQmvaa 156,120,208\nQta 144,47,768\nQmvaa 188,124,208\n"
"Qmvaa 220,128,208\ns_waitcnt vmcnt(9)\nds_write_b128 v52, a[232:235] offset:25600\nQmvaa 252,112,212\nQmvaa 100,116,212\nQta 146,47,5888\nQmvaa 132,120,212\nQta 148,47,1024\n"
"Qmvaa 164,124,212\nQda 72,53,4096\nv_add_u32 v75, 1, v75\nQmvaa 196,128,212\nds_write_b32 v73, v75\nQmvaa 228,112,216\nQta 150,47,6144\nQmvaa 108,116,216\n"
"Qmvaa 140,120,216\nQtv 0,45,20480\nQmvaa 172,124,216\nQtv 2,45,25600\nQmvaa 204,128,216\ns_waitcnt vmcnt(8)\nds_write_b128 v52, a[236:239] offset:29696\nQmvaa 236,112,220\n"
"Qmvaa 80,116,220\nQtv 4,45,20736\nQmvaa 112,120,220\nds_read_b32 v69, v74\nQmvaa 144,124,220\nQda 76,53,8192\nQmvaa 176,128,220\nQtv 6,45,25856\n"
"s_waitcnt lgkmcnt(15)\nQmvaa 92,132,68\nQmvaa 124,136,68\ns_waitcnt lgkmcnt(2)\nv_cmp_gt_u32 vcc, v75, v69\nQmvaa 156,140,68\ns_cbranch_vccnz .Lso62r0\n.Lro62r0:\n"
"Qmvaa 188,144,68\nQtv 8,45,20992\nQmvaa 220,148,68\nQtv 10,45,26112\nQmvaa 252,132,72\nQtv 12,45,21248\nQmvaa 100,136,72\nQmvaa 132,140,72\n"
"Qtv 14,45,26368\nQmvaa 164,144,72\nQda 80,53,12288\nQmvaa 196,148,72\nQtv 16,45,21504\ns_waitcnt lgkmcnt(1)\nQmvaa 228,132,76\nQtv 18,45,26624\n"
"Qmvaa 108,136,76\nQmvaa 140,140,76\nQtv 20,47,20480\nQmvaa 172,144,76\nQtv 22,47,25600\nQmvaa 204,148,76\nQmvaa 236,132,80\nQtv 24,47,20736\n"
"Qmvaa 80,136,80\nQmvaa 112,140,80\nQtv 26,47,25856\nQmvaa 144,144,80\nQda 48,53,16384\nQmvaa 176,148,80\nQtv 28,47,20992\ns_waitcnt lgkmcnt(6)\n"
"Qmvva 92,0,224\nQmvva 124,4,224\nQtv 30,47,26112\nQmvva 156,8,224\nQmvva 188,12,224\nQtv 32,47,21248\nv_add_u32 v75, 1, v75\nQmvva 220,16,224\n"
"ds_write_b32 v73, v75\nQmvva 252,0,228\nQtv 34,47,26368\nQmvva 100,4,228\nQmvva 132,8,228\nQtv 36,47,21504\nQmvva 164,12,228\nQda 52,53,20480\n"
"Qmvva 196,16,228\nQmvva 228,0,232\nQtv 38,47,26624\nQmvva 108,4,232\nQmvva 140,8,232\nds_read_b32 v69, v74\nQmvva 172,12,232\nQmvva 204,16,232\n"
"Qmvva 236,0,236\nQmvva 80,4,236\ns_waitcnt lgkmcnt(0)\nv_cmp_gt_u32 vcc, v75, v69\nQmvva 112,8,236\ns_cbranch_vccnz .Lso63r0\n.Lro63r0:\nQmvva 144,12,236\n"
"Qda 56,53,25600\nQmvva 176,16,236\ns_waitcnt lgkmcnt(2)\nQmvva 92,20,48\ns_add_u32 s69, s98, 0\nbuffer_load_dwordx4 v[240:243], v60, s[24:27], s69 offen offset:0 sc1\nQmvva 124,24,48\nQmvva 156,28,48\n"
"buffer_load_dwordx4 v[88:91], v60, s[24:27], s69 offen offset:1024 sc1\nQmvva 188,32,48\nQmvva 220,36,48\nbuffer_load_dwordx4 v[152:155], v60, s[24:27], s69 offen offset:2048 sc1\nQmvva 252,20,52\nQmvva 100,24,52\nbuffer_load_dwordx4 v[216:219], v60, s[24:27], s69 offen offset:3072 sc1\nQmvva 132,28,52\n"
"Qmvva 164,32,52\nQda 60,53,29696\nQmvva 196,36,52\ns_add_u32 s69, s98, 4096\nbuffer_load_dwordx4 v[116:119], v60, s[24:27], s69 offen offset:0 sc1\ns_waitcnt lgkmcnt(0)\nQmvva 228,20,56\nQmvva 108,24,56\n"
"buffer_load_dwordx4 v[128:131], v60, s[24:27], s69 offen offset:1024 sc1\nQmvva 140,28,56\nQmvva 172,32,56\nbuffer_load_dwordx4 v[192:195], v60, s[24:27], s69 offen offset:2048 sc1\nQmvva 204,36,56\nQmvva 236,20,60\ns_add_u32 s69, s98, 14336\nbuffer_load_dwordx4 v[148:151], v60, s[24:27], s69 offen offset:0 sc1\n"
"Qmvva 80,24,60\nQmvva 112,28,60\nbuffer_load_dwordx4 v[180:183], v60, s[24:27], s69 offen offset:1024 sc1\nQmvva 144,32,60\nQmvva 176,36,60\nbuffer_load_dwordx4 v[104:107], v60, s[24:27], s69 offen offset:2048 sc1\ns_barrier\ns_mov_b32 s97, 0\n"
".Lrx0:\ns_waitcnt vmcnt(0)\nv_max_u32 v40, v240, v241\nv_max3_u32 v40, v40, v242, v243\nv_max3_u32 v40, v40, v88, v89\nv_max3_u32 v40, v40, v90, v91\nv_max3_u32 v40, v40, v152, v153\nv_max3_u32 v40, v40, v154, v155\n"
"v_max3_u32 v40, v40, v216, v217\nv_max3_u32 v40, v40, v218, v219\nv_max3_u32 v40, v40, v116, v117\nv_max3_u32 v40, v40, v118, v119\nv_max3_u32 v40, v40, v128, v129\nv_max3_u32 v40, v40, v130, v131\nv_max3_u32 v40, v40, v192, v193\nv_max3_u32 v40, v40, v194, v195\n"
"v_max3_u32 v40, v40, v148, v149\nv_max3_u32 v40, v40, v150, v151\nv_max3_u32 v40, v40, v180, v181\nv_max3_u32 v40, v40, v182, v183\nv_max3_u32 v40, v40, v104, v105\nv_max3_u32 v40, v40, v106, v107\nv_cmp_eq_u32 vcc, -1, v40\ns_cmp_eq_u64 vcc, 0\n"
"s_cbranch_scc1 .Lrx0_ok\ns_add_u32 s97, s97, 1\ns_cmp_gt_u32 s97, 1048576\ns_cbranch_scc1 .Lrx0_ok\ns_add_u32 s69, s98, 0\nbuffer_load_dwordx4 v[240:243], v60, s[24:27], s69 offen offset:0 sc1\nbuffer_load_dwordx4 v[88:91], v60, s[24:27], s69 offen offset:1024 sc1\nbuffer_load_dwordx4 v[152:155], v60, s[24:27], s69 offen offset:2048 sc1\n"
"buffer_load_dwordx4 v[216:219], v60, s[24:27], s69 offen offset:3072 sc1\ns_add_u32 s69, s98, 4096\nbuffer_load_dwordx4 v[116:119], v60, s[24:27], s69 offen offset:0 sc1\nbuffer_load_dwordx4 v[128:131], v60, s[24:27], s69 offen offset:1024 sc1\nbuffer_load_dwordx4 v[192:195], v60, s[24:27], s69 offen offset:2048 sc1\ns_add_u32 s69, s98, 14336\nbuffer_load_dwordx4 v[148:151], v60, s[24:27], s69 offen offset:0 sc1\nbuffer_load_dwordx4 v[180:183], v60, s[24:27], s69 offen offset:1024 sc1\n"
"buffer_load_dwordx4 v[104:107], v60, s[24:27], s69 offen offset:2048 sc1\ns_branch .Lrx0\n.Lrx0_ok:\nv_lshlrev_b32 v41, 16, v240\nv_and_b32 v42, 0xffff0000, v240\nQa 92,92,41\nQa 93,93,42\nv_lshlrev_b32 v43, 16, v241\n"
"v_and_b32 v44, 0xffff0000, v241\nQa 94,94,43\nQa 95,95,44\nQk 92,92,93\nQk 93,94,95\nds_write_b64 v61, v[92:93] offset:0\nv_lshlrev_b32 v41, 16, v242\nv_and_b32 v42, 0xffff0000, v242\n"
"Qa 124,124,41\nQa 125,125,42\nv_lshlrev_b32 v43, 16, v243\nv_and_b32 v44, 0xffff0000, v243\nQa 126,126,43\nQa 127,127,44\nQk 124,124,125\nQk 125,126,127\n"
"ds_write_b64 v61, v[124:125] offset:32\nv_lshlrev_b32 v41, 16, v88\nv_and_b32 v42, 0xffff0000, v88\nQa 156,156,41\nQa 157,157,42\nv_lshlrev_b32 v43, 16, v89\nv_and_b32 v44, 0xffff0000, v89\nQa 158,158,43\n"
"Qa 159,159,44\nQk 156,156,157\nQk 157,158,159\nds_write_b64 v61, v[156:157] offset:64\nv_lshlrev_b32 v41, 16, v90\nv_and_b32 v42, 0xffff0000, v90\nQa 188,188,41\nQa 189,189,42\n"
"v_lshlrev_b32 v43, 16, v91\nv_and_b32 v44, 0xffff0000, v91\nQa 190,190,43\nQa 191,191,44\nQk 188,188,189\nQk 189,190,191\nds_write_b64 v61, v[188:189] offset:96\nv_lshlrev_b32 v41, 16, v152\n"
"v_and_b32 v42, 0xffff0000, v152\nQa 220,220,41\nQa 221,221,42\nv_lshlrev_b32 v43, 16, v153\nv_and_b32 v44, 0xffff0000, v153\nQa 222,222,43\nQa 223,223,44\nQk 220,220,221\n"
"Qk 221,222,223\nds_write_b64 v61, v[220:221] offset:128\nv_lshlrev_b32 v41, 16, v154\nv_and_b32 v42, 0xffff0000, v154\nQa 252,252,41\nQa 253,253,42\nv_lshlrev_b32 v43, 16, v155\nv_and_b32 v44, 0xffff0000, v155\n"
"Qa 254,254,43\nQa 255,255,44\nQk 252,252,253\nQk 253,254,255\nds_write_b64 v61, v[252:253] offset:5376\nv_lshlrev_b32 v41, 16, v216\nv_and_b32 v42, 0xffff0000, v216\nQa 100,100,41\n"
"Qa 101,101,42\nv_lshlrev_b32 v43, 16, v217\nv_and_b32 v44, 0xffff0000, v217\nQa 102,102,43\nQa 103,103,44\nQk 100,100,101\nQk 101,102,103\nds_write_b64 v61, v[100:101] offset:5408\n"
"v_lshlrev_b32 v41, 16, v218\nv_and_b32 v42, 0xffff0000, v218\nQa 132,132,41\nQa 133,133,42\nv_lshlrev_b32 v43, 16, v219\nv_and_b32 v44, 0xffff0000, v219\nQa 134,134,43\nQa 135,135,44\n"
"Qk 132,132,133\nQk 133,134,135\nds_write_b64 v61, v[132:133] offset:5440\nv_lshlrev_b32 v41, 16, v116\nv_and_b32 v42, 0xffff0000, v116\nQa 164,164,41\nQa 165,165,42\nv_lshlrev_b32 v43, 16, v117\n"
"v_and_b32 v44, 0xffff0000, v117\nQa 166,166,43\nQa 167,167,44\nQk 164,164,165\nQk 165,166,167\nds_write_b64 v61, v[164:165] offset:5472\nv_lshlrev_b32 v41, 16, v118\nv_and_b32 v42, 0xffff0000, v118\n"
"Qa 196,196,41\nQa 197,197,42\nv_lshlrev_b32 v43, 16, v119\nv_and_b32 v44, 0xffff0000, v119\nQa 198,198,43\nQa 199,199,44\nQk 196,196,197\nQk 197,198,199\n"
"ds_write_b64 v61, v[196:197] offset:5504\nv_lshlrev_b32 v41, 16, v128\nv_and_b32 v42, 0xffff0000, v128\nQa 228,228,41\nQa 229,229,42\nv_lshlrev_b32 v43, 16, v129\nv_and_b32 v44, 0xffff0000, v129\nQa 230,230,43\n"
"Qa 231,231,44\nQk 228,228,229\nQk 229,230,231\nds_write_b64 v61, v[228:229] offset:10752\nv_lshlrev_b32 v41, 16, v130\nv_and_b32 v42, 0xffff0000, v130\nQa 108,108,41\nQa 109,109,42\n"
"v_lshlrev_b32 v43, 16, v131\nv_and_b32 v44, 0xffff0000, v131\n.Lpg23:\ns_cbranch_execz .Lpg27\nQa 110,110,43\nQa 111,111,44\nQk 108,108,109\nQk 109,110,111\n"
"ds_write_b64 v61, v[108:109] offset:10784\nv_lshlrev_b32 v41, 16, v192\nv_and_b32 v42, 0xffff0000, v192\nQa 140,140,41\nQa 141,141,42\nv_lshlrev_b32 v43, 16, v193\nv_and_b32 v44, 0xffff0000, v193\nQa 142,142,43\n"
"Qa 143,143,44\nQk 140,140,141\nQk 141,142,143\nds_write_b64 v61, v[140:141] offset:10816\nv_lshlrev_b32 v41, 16, v194\nv_and_b32 v42, 0xffff0000, v194\nQa 172,172,41\nQa 173,173,42\n"
"v_lshlrev_b32 v43, 16, v195\nv_and_b32 v44, 0xffff0000, v195\nQa 174,174,43\nQa 175,175,44\nQk 172,172,173\nQk 173,174,175\nds_write_b64 v61, v[172:173] offset:10848\nv_lshlrev_b32 v41, 16, v148\n"
"v_and_b32 v42, 0xffff0000, v148\nQa 204,204,41\nQa 205,205,42\nv_lshlrev_b32 v43, 16, v149\nv_and_b32 v44, 0xffff0000, v149\nQa 206,206,43\nQa 207,207,44\nQk 204,204,205\n"
"Qk 205,206,207\nds_write_b64 v61, v[204:205] offset:10880\nv_lshlrev_b32 v41, 16, v150\nv_and_b32 v42, 0xffff0000, v150\nQa 236,236,41\nQa 237,237,42\nv_lshlrev_b32 v43, 16, v151\nv_and_b32 v44, 0xffff0000, v151\n"
"Qa 238,238,43\nQa 239,239,44\nQk 236,236,237\nQk 237,238,239\nds_write_b64 v61, v[236:237] offset:16128\nv_lshlrev_b32 v41, 16, v180\nv_and_b32 v42, 0xffff0000, v180\nQa 80,80,41\n"
"Qa 81,81,42\nv_lshlrev_b32 v43, 16, v181\nv_and_b32 v44, 0xffff0000, v181\nQa 82,82,43\nQa 83,83,44\nQk 80,80,81\nQk 81,82,83\nds_write_b64 v61, v[80:81] offset:16160\n"
"v_lshlrev_b32 v41, 16, v182\nv_and_b32 v42, 0xffff0000, v182\nQa 112,112,41\nQa 113,113,42\nv_lshlrev_b32 v43, 16, v183\nv_and_b32 v44, 0xffff0000, v183\nQa 114,114,43\nQa 115,115,44\n"
"Qk 112,112,113\nQk 113,114,115\nds_write_b64 v61, v[112:113] offset:16192\nv_lshlrev_b32 v41, 16, v104\nv_and_b32 v42, 0xffff0000, v104\nQa 144,144,41\nQa 145,145,42\nv_lshlrev_b32 v43, 16, v105\n"
"v_and_b32 v44, 0xffff0000, v105\nQa 146,146,43\nQa 147,147,44\nQk 144,144,145\nQk 145,146,147\nds_write_b64 v61, v[144:145] offset:16224\nv_lshlrev_b32 v41, 16, v106\nv_and_b32 v42, 0xffff0000, v106\n"
"Qa 176,176,41\nQa 177,177,42\nv_lshlrev_b32 v43, 16, v107\nv_and_b32 v44, 0xffff0000, v107\nQa 178,178,43\nQa 179,179,44\nQk 176,176,177\nQk 177,178,179\n"
"ds_write_b64 v61, v[176:177] offset:16256\ns_branch .Lfin\n.Lrole1:\n.Lpg24:\ns_cbranch_execz .Ltr8\ns_waitcnt lgkmcnt(2)\nQmvaa 92,132,56\ns_waitcnt vmcnt(26)\n"
"ds_write_b128 v52, a[172:175] offset:0\nQmvaa 124,136,56\nQmvaa 156,140,56\nQtv 0,45,0\nQmvaa 188,144,56\nQda 72,53,33792\nQmvaa 220,148,56\nQtv 2,45,5120\n"
"Qmvaa 252,132,60\nQtv 4,45,256\ns_mov_b32 s94, 835584\nQmvaa 100,136,60\nbuffer_load_dwordx4 a[220:223], v60, s[48:51], s94 offen offset:0 sc1\nQmvaa 132,140,60\nds_read_b32 v69, v74\nQmvaa 164,144,60\n"
"Qtv 6,45,5376\nQmvaa 196,148,60\ns_waitcnt vmcnt(26)\nds_write_b128 v52, a[0:3] offset:4096\ns_waitcnt lgkmcnt(15)\nQmvaa 228,132,64\nQmvaa 108,136,64\ns_waitcnt lgkmcnt(2)\n"
"v_cmp_gt_u32 vcc, v75, v69\nQmvaa 140,140,64\ns_cbranch_vccnz .Lso49r1\n.Lro49r1:\nQmvaa 172,144,64\nQda 76,53,37888\ns_add_u32 m0, s96, 86016\ns_mov_b32 s95, 1761280\n"
"Qmvaa 204,148,64\nbuffer_load_dwordx4 v40, s[52:55], s95 offen lds\nQmvaa 236,132,68\nQtv 8,45,512\nQmvaa 80,136,68\nbuffer_load_dwordx4 a[224:227], v60, s[48:51], s94 offen offset:1024 sc1\nQmvaa 112,140,68\nQtv 10,45,5632\n"
"Qmvaa 144,144,68\nQtv 12,45,768\nQmvaa 176,148,68\ns_waitcnt vmcnt(27)\nds_write_b128 v52, a[4:7] offset:8192\ns_waitcnt lgkmcnt(4)\nQmvaa 208,132,72\nQmvaa 240,136,72\n"
"buffer_load_dwordx4 v41, s[52:55], s95 offen offset:1024 lds\nQmvaa 84,140,72\nQtv 14,45,5888\nQmvaa 88,144,72\nQda 80,53,41984\nQmvaa 120,148,72\nQtv 16,45,1024\nQmvaa 152,132,76\n"
"Qtv 18,45,6144\nQmvaa 184,136,76\nbuffer_load_dwordx4 a[228:231], v60, s[48:51], s94 offen offset:2048 sc1\nQmvaa 216,140,76\nQmvaa 248,144,76\nbuffer_load_dwordx4 v42, s[52:55], s95 offen offset:2048 lds\nQmvaa 116,148,76\ns_waitcnt vmcnt(28)\n"
"ds_write_b128 v52, a[8:11] offset:12288\ns_waitcnt lgkmcnt(3)\nQmvaa 96,132,80\nQmvaa 128,136,80\nQmvaa 160,140,80\nQmvaa 192,144,80\nQda 48,53,46080\nv_add_u32 v75, 1, v75\n"
"Qmvaa 224,148,80\ns_waitcnt vmcnt(34)\nds_write_b32 v73, v75\ns_waitcnt lgkmcnt(3)\nQmvva 92,0,252\nbuffer_load_dwordx4 v43, s[52:55], s95 offen offset:3072 lds\ns_mov_b32 s94, 501760\nQmvva 124,4,252\n"
"buffer_load_dwordx4 a[232:235], v60, s[48:51], s94 offen offset:0 sc1\nQmvva 156,8,252\nQtv 20,47,0\nQmvva 188,12,252\nQtv 22,47,5120\nQmvva 220,16,252\ns_waitcnt vmcnt(28)\nds_write_b128 v52, a[12:15] offset:16384\n"
"Qmvva 252,0,168\nQmvva 100,4,168\nQtv 24,47,256\nQmvva 132,8,168\nds_read_b32 v69, v74\nQmvva 164,12,168\nQda 52,53,50176\ns_add_u32 m0, s96, 90112\n"
"Qmvva 196,16,168\nbuffer_load_dwordx4 v44, s[52:55], s95 offen lds\nQmvva 228,0,172\nQtv 26,47,5376\nQmvva 108,4,172\ns_waitcnt lgkmcnt(2)\nv_cmp_gt_u32 vcc, v75, v69\nQmvva 140,8,172\n"
"s_cbranch_vccnz .Lso50r1\n.Lro50r1:\nQmvva 172,12,172\nbuffer_load_dwordx4 a[236:239], v60, s[48:51], s94 offen offset:1024 sc1\nQmvva 204,16,172\ns_waitcnt vmcnt(27)\nds_write_b128 v52, a[16:19] offset:20480\nQmvva 236,0,0\n"
"Qmvva 80,4,0\nQtv 28,47,512\nQmvva 112,8,0\nQmvva 144,12,0\nQda 56,53,0\nQmvva 176,16,0\nQtv 30,47,5632\nQmvva 208,0,4\n"
"Qtv 32,47,768\nQmvva 240,4,4\nbuffer_load_dwordx4 a[240:243], v60, s[48:51], s94 offen offset:2048 sc1\nQmvva 84,8,4\nQtv 34,47,5888\nQmvva 88,12,4\nQtv 36,47,1024\nQmvva 120,16,4\n"
"s_waitcnt vmcnt(25)\nds_write_b128 v52, a[20:23] offset:25600\nQmvva 152,0,8\nQmvva 184,4,8\nQtv 38,47,6144\nQmvva 216,8,8\nQmvva 248,12,8\nQda 60,53,4096\n"
"Qmvva 116,16,8\nQmvva 96,0,12\nQmvva 128,4,12\nbuffer_load_dwordx4 a[244:247], v60, s[48:51], s94 offen offset:3072 sc1\nQmvva 160,8,12\nQmvva 192,12,12\nv_add_u32 v75, 1, v75\nQmvva 224,16,12\n"
"ds_write_b32 v73, v75\ns_waitcnt lgkmcnt(2)\nQmvva 92,20,48\ns_waitcnt vmcnt(25)\nds_write_b128 v52, a[24:27] offset:29696\nQmvva 124,24,48\nQmvva 156,28,48\nQta 112,45,20480\n"
"Qmvva 188,32,48\nQda 64,53,8192\nQmvva 220,36,48\nQta 114,45,25600\nQmvva 252,20,52\nQta 116,45,20736\ns_mov_b32 s94, 616448\nQmvva 100,24,52\n"
"buffer_load_dwordx4 a[248:251], v60, s[48:51], s94 offen offset:0 sc1\nQmvva 132,28,52\nds_read_b32 v69, v74\nQmvva 164,32,52\nQta 118,45,25856\nQmvva 196,36,52\ns_waitcnt vmcnt(25)\nds_write_b128 v52, a[28:31] offset:33792\n"
"s_waitcnt lgkmcnt(15)\nQmvva 228,20,56\nQmvva 108,24,56\ns_waitcnt lgkmcnt(2)\nv_cmp_gt_u32 vcc, v75, v69\nQmvva 140,28,56\ns_cbranch_vccnz .Lso51r1\n.Lro51r1:\n"
"Qmvva 172,32,56\nQda 68,53,12288\ns_add_u32 m0, s96, 4096\ns_mov_b32 s95, 2088960\nQmvva 204,36,56\nbuffer_load_dwordx4 v40, s[52:55], s95 offen lds\nQmvva 236,20,60\nQta 120,45,20992\n"
"Qmvva 80,24,60\nbuffer_load_dwordx4 a[252:255], v60, s[48:51], s94 offen offset:1024 sc1\nQmvva 112,28,60\nQta 122,45,26112\nQmvva 144,32,60\nQta 124,45,21248\nQmvva 176,36,60\ns_waitcnt vmcnt(26)\n"
"ds_write_b128 v52, a[32:35] offset:37888\ns_waitcnt lgkmcnt(4)\nQmvva 208,20,64\nQmvva 240,24,64\nbuffer_load_dwordx4 v41, s[52:55], s95 offen offset:1024 lds\nQmvva 84,28,64\nQta 126,45,26368\nQmvva 88,32,64\n"
"Qda 72,53,16384\nQmvva 120,36,64\nQta 128,45,21504\nQmvva 152,20,68\nQta 130,45,26624\nQmvva 184,24,68\nbuffer_load_dwordx4 a[168:171], v60, s[48:51], s94 offen offset:2048 sc1\nQmvva 216,28,68\n"
"Qmvva 248,32,68\nbuffer_load_dwordx4 v42, s[52:55], s95 offen offset:2048 lds\nQmvva 116,36,68\ns_waitcnt vmcnt(27)\nds_write_b128 v52, a[36:39] offset:41984\ns_waitcnt lgkmcnt(3)\nQmvva 96,20,72\nQmvva 128,24,72\n"
"Qmvva 160,28,72\nQmvva 192,32,72\nQda 76,53,20480\nv_add_u32 v75, 1, v75\nQmvva 224,36,72\ns_waitcnt vmcnt(33)\nds_write_b32 v73, v75\ns_waitcnt lgkmcnt(3)\n"
"Qmvaa 92,112,16\nbuffer_load_dwordx4 v43, s[52:55], s95 offen offset:3072 lds\nQmvaa 124,116,16\nbuffer_load_dwordx4 a[172:175], v60, s[48:51], s94 offen offset:3072 sc1\nQmvaa 156,120,16\nQta 132,47,20480\nQmvaa 188,124,16\nQta 134,47,25600\n"
"Qmvaa 220,128,16\ns_waitcnt vmcnt(27)\nds_write_b128 v52, a[40:43] offset:46080\nQmvaa 252,112,20\nQmvaa 100,116,20\nQta 136,47,20736\nQmvaa 132,120,20\nds_read_b32 v69, v74\n"
"Qmvaa 164,124,20\nQda 80,53,25600\ns_add_u32 m0, s96, 8192\nQmvaa 196,128,20\nbuffer_load_dwordx4 v44, s[52:55], s95 offen lds\nQmvaa 228,112,24\nQta 138,47,25856\nQmvaa 108,116,24\n"
"s_waitcnt lgkmcnt(2)\nv_cmp_gt_u32 vcc, v75, v69\nQmvaa 140,120,24\ns_mov_b32 s94, 731136\ns_cbranch_vccnz .Lso52r1\n.Lro52r1:\nQmvaa 172,124,24\nbuffer_load_dwordx4 a[0:3], v60, s[48:51], s94 offen offset:0 sc1\n"
"Qmvaa 204,128,24\ns_waitcnt vmcnt(27)\nds_write_b128 v52, a[44:47] offset:50176\nQmvaa 236,112,28\nQmvaa 80,116,28\nQta 140,47,20992\nQmvaa 112,120,28\nQmvaa 144,124,28\n"
"Qda 48,53,29696\nQmvaa 176,128,28\nQta 142,47,26112\nQmvaa 208,112,32\nQta 144,47,21248\nQmvaa 240,116,32\nbuffer_load_dwordx4 a[4:7], v60, s[48:51], s94 offen offset:1024 sc1\nQmvaa 84,120,32\n"
"Qta 146,47,26368\nQmvaa 88,124,32\nQta 148,47,21504\nQmvaa 120,128,32\ns_waitcnt vmcnt(25)\nds_write_b128 v52, a[208:211] offset:0\nQmvaa 152,112,36\nQmvaa 184,116,36\n"
"Qta 150,47,26624\nQmvaa 216,120,36\nQmvaa 248,124,36\nQda 52,53,33792\nQmvaa 116,128,36\nQmvaa 96,112,40\nQmvaa 128,116,40\nbuffer_load_dwordx4 a[8:11], v60, s[48:51], s94 offen offset:2048 sc1\n"
"Qmvaa 160,120,40\nQmvaa 192,124,40\nv_add_u32 v75, 1, v75\nQmvaa 224,128,40\nds_write_b32 v73, v75\ns_waitcnt lgkmcnt(2)\nQmvaa 92,132,76\ns_waitcnt vmcnt(25)\n"
"ds_write_b128 v52, a[212:215] offset:4096\nQmvaa 124,136,76\nQmvaa 156,140,76\nQtv 0,45,40960\nQmvaa 188,144,76\nQda 56,53,37888\nQmvaa 220,148,76\nQtv 2,45,46080\n"
"Qmvaa 252,132,80\nQtv 4,45,41216\nQmvaa 100,136,80\nbuffer_load_dwordx4 a[12:15], v60, s[48:51], s94 offen offset:3072 sc1\nQmvaa 132,140,80\nds_read_b32 v69, v74\nQmvaa 164,144,80\nQtv 6,45,46336\n"
"Qmvaa 196,148,80\ns_waitcnt vmcnt(25)\nds_write_b128 v52, a[216:219] offset:8192\ns_waitcnt lgkmcnt(15)\nQmvaa 228,132,48\nQmvaa 108,136,48\ns_waitcnt lgkmcnt(2)\nv_cmp_gt_u32 vcc, v75, v69\n"
"Qmvaa 140,140,48\ns_cbranch_vccnz .Lso53r1\n.Lro53r1:\nQmvaa 172,144,48\nQda 60,53,41984\ns_add_u32 m0, s96, 24576\ns_mov_b32 s95, 2416640\nQmvaa 204,148,48\n"
"buffer_load_dwordx4 v40, s[52:55], s95 offen lds\nQmvaa 236,132,52\nQtv 8,45,41472\ns_mov_b32 s94, 845824\nQmvaa 80,136,52\nbuffer_load_dwordx4 a[16:19], v60, s[48:51], s94 offen offset:0 sc1\nQmvaa 112,140,52\nQtv 10,45,46592\n"
"Qmvaa 144,144,52\nQtv 12,45,41728\nQmvaa 176,148,52\ns_waitcnt vmcnt(26)\nds_write_b128 v52, a[220:223] offset:12288\ns_waitcnt lgkmcnt(4)\nQmvaa 208,132,56\nQmvaa 240,136,56\n"
"buffer_load_dwordx4 v41, s[52:55], s95 offen offset:1024 lds\nQmvaa 84,140,56\nQtv 14,45,46848\nQmvaa 88,144,56\nQda 64,53,46080\nQmvaa 120,148,56\nQtv 16,45,41984\nQmvaa 152,132,60\n"
"Qtv 18,45,47104\nQmvaa 184,136,60\nbuffer_load_dwordx4 a[20:23], v60, s[48:51], s94 offen offset:1024 sc1\nQmvaa 216,140,60\nQmvaa 248,144,60\nbuffer_load_dwordx4 v42, s[52:55], s95 offen offset:2048 lds\nQmvaa 116,148,60\ns_waitcnt vmcnt(27)\n"
"ds_write_b128 v52, a[224:227] offset:16384\ns_waitcnt lgkmcnt(3)\nQmvaa 96,132,64\nQmvaa 128,136,64\nQmvaa 160,140,64\nQmvaa 192,144,64\nQda 68,53,50176\nv_add_u32 v75, 1, v75\n"
"Qmvaa 224,148,64\ns_waitcnt vmcnt(33)\nds_write_b32 v73, v75\ns_waitcnt lgkmcnt(3)\nQmvva 92,0,44\nbuffer_load_dwordx4 v43, s[52:55], s95 offen offset:3072 lds\nQmvva 124,4,44\nbuffer_load_dwordx4 a[24:27], v60, s[48:51], s94 offen offset:2048 sc1\n"
"Qmvva 156,8,44\nQtv 20,47,40960\nQmvva 188,12,44\nQtv 22,47,46080\nQmvva 220,16,44\ns_waitcnt vmcnt(27)\nds_write_b128 v52, a[228:231] offset:20480\nQmvva 252,0,208\n"
"Qmvva 100,4,208\nQtv 24,47,41216\nQmvva 132,8,208\nds_read_b32 v69, v74\nQmvva 164,12,208\nQda 72,53,0\ns_add_u32 m0, s96, 28672\nQmvva 196,16,208\n"
"buffer_load_dwordx4 v44, s[52:55], s95 offen lds\nQmvva 228,0,212\nQtv 26,47,46336\nQmvva 108,4,212\ns_waitcnt lgkmcnt(2)\nv_cmp_gt_u32 vcc, v75, v69\nQmvva 140,8,212\ns_cbranch_vccnz .Lso54r1\n"
".Lro54r1:\nQmvva 172,12,212\nbuffer_load_dwordx4 a[28:31], v60, s[48:51], s94 offen offset:3072 sc1\nQmvva 204,16,212\ns_waitcnt vmcnt(26)\nds_write_b128 v52, a[232:235] offset:25600\nQmvva 236,0,216\nQmvva 80,4,216\n"
"Qtv 28,47,41472\nQmvva 112,8,216\nQmvva 144,12,216\nQda 76,53,4096\nQmvva 176,16,216\nQtv 30,47,46592\nQmvva 208,0,220\nQtv 32,47,41728\n"
"s_mov_b32 s94, 505856\nQmvva 240,4,220\nbuffer_load_dwordx4 a[32:35], v60, s[48:51], s94 offen offset:0 sc1\nQmvva 84,8,220\nQtv 34,47,46848\nQmvva 88,12,220\nQtv 36,47,41984\nQmvva 120,16,220\n"
"s_waitcnt vmcnt(25)\nds_write_b128 v52, a[236:239] offset:29696\nQmvva 152,0,224\nQmvva 184,4,224\nQtv 38,47,47104\nQmvva 216,8,224\nQmvva 248,12,224\nQda 80,53,8192\n"
"Qmvva 116,16,224\nQmvva 96,0,228\nQmvva 128,4,228\nbuffer_load_dwordx4 a[36:39], v60, s[48:51], s94 offen offset:1024 sc1\nQmvva 160,8,228\nQmvva 192,12,228\nv_add_u32 v75, 1, v75\nQmvva 224,16,228\n"
"s_waitcnt vmcnt(27)\nds_write_b32 v73, v75\ns_waitcnt lgkmcnt(2)\nQmvva 92,20,68\ns_waitcnt vmcnt(25)\nds_write_b128 v52, a[240:243] offset:33792\nQmvva 124,24,68\nQmvva 156,28,68\n"
"Qta 112,46,0\nQmvva 188,32,68\nQda 48,53,12288\nQmvva 220,36,68\nQta 114,46,5120\nQmvva 252,20,72\nQta 116,46,256\nQmvva 100,24,72\n"
"buffer_load_dwordx4 a[40:43], v60, s[48:51], s94 offen offset:2048 sc1\nQmvva 132,28,72\nds_read_b32 v69, v74\nQmvva 164,32,72\nQmvva 196,36,72\ns_waitcnt vmcnt(25)\nds_write_b128 v52, a[244:247] offset:37888\ns_waitcnt lgkmcnt(15)\n"
"Qmvva 228,20,76\nQmvva 108,24,76\ns_waitcnt lgkmcnt(1)\nv_cmp_gt_u32 vcc, v75, v69\nQmvva 140,28,76\ns_cbranch_vccnz .Lso55r1\n.Lro55r1:\nQmvva 172,32,76\n"
"Qda 52,53,16384\nQmvva 204,36,76\nQta 118,46,5376\nQmvva 236,20,80\nQta 120,46,512\ns_mov_b32 s94, 620544\nQmvva 80,24,80\nbuffer_load_dwordx4 a[44:47], v60, s[48:51], s94 offen offset:0 sc1\n"
"Qmvva 112,28,80\nQmvva 144,32,80\n.Lpg25:\ns_cbranch_execz .Ltr8\nQta 122,46,5632\nQmvva 176,36,80\ns_waitcnt vmcnt(25)\nds_write_b128 v52, a[248:251] offset:41984\n"
"s_waitcnt lgkmcnt(4)\nQmvva 208,20,48\nQmvva 240,24,48\nQta 124,46,768\nQmvva 84,28,48\nQta 126,46,5888\nQmvva 88,32,48\nQda 56,53,20480\n"
"Qmvva 120,36,48\nQmvva 152,20,52\nQta 128,46,1024\nQmvva 184,24,52\nbuffer_load_dwordx4 a[208:211], v60, s[48:51], s94 offen offset:1024 sc1\nQmvva 216,28,52\nQta 130,46,6144\nQmvva 248,32,52\n"
"Qta 132,51,0\nQta 134,51,5120\nQmvva 116,36,52\ns_waitcnt vmcnt(24)\nds_write_b128 v52, a[252:255] offset:46080\ns_waitcnt lgkmcnt(5)\nQmvva 96,20,56\nQmvva 128,24,56\n"
"Qta 136,51,256\nQmvva 160,28,56\nQta 138,51,5376\nQmvva 192,32,56\nQda 60,53,25600\nv_add_u32 v75, 1, v75\nQmvva 224,36,56\nds_write_b32 v73, v75\n"
"s_waitcnt lgkmcnt(7)\nQmvaa 92,112,232\nQta 140,51,512\nQmvaa 124,116,232\nbuffer_load_dwordx4 a[212:215], v60, s[48:51], s94 offen offset:2048 sc1\nQmvaa 156,120,232\nQta 142,51,5632\nQmvaa 188,124,232\n"
"Qta 144,51,768\nQmvaa 220,128,232\ns_waitcnt vmcnt(23)\nds_write_b128 v52, a[168:171] offset:50176\nQmvaa 252,112,236\nQmvaa 100,116,236\nQta 146,51,5888\nQmvaa 132,120,236\n"
"ds_read_b32 v69, v74\nQmvaa 164,124,236\nQda 64,53,29696\nQmvaa 196,128,236\nQta 148,51,1024\nQmvaa 228,112,240\nQta 150,51,6144\nQmvaa 108,116,240\n"
"s_waitcnt lgkmcnt(3)\nv_cmp_gt_u32 vcc, v75, v69\nQmvaa 140,120,240\ns_mov_b32 s94, 735232\ns_cbranch_vccnz .Lso56r1\n.Lro56r1:\nQmvaa 172,124,240\nbuffer_load_dwordx4 a[216:219], v60, s[48:51], s94 offen offset:0 sc1\n"
"Qmvaa 204,128,240\ns_waitcnt vmcnt(21)\nds_write_b128 v52, a[172:175] offset:0\nQmvaa 236,112,244\nQmvaa 80,116,244\nQtv 0,46,20480\nQmvaa 112,120,244\nQtv 2,46,25600\n"
"Qmvaa 144,124,244\nQda 68,53,33792\nQmvaa 176,128,244\nQtv 4,46,20736\ns_waitcnt lgkmcnt(5)\nQmvaa 92,132,60\nQtv 6,46,25856\nQmvaa 124,136,60\n"
"buffer_load_dwordx4 a[220:223], v60, s[48:51], s94 offen offset:1024 sc1\nQmvaa 156,140,60\nQtv 8,46,20992\nQmvaa 188,144,60\nQtv 10,46,26112\nQmvaa 220,148,60\ns_waitcnt vmcnt(20)\nds_write_b128 v52, a[0:3] offset:4096\n"
"Qmvaa 252,132,64\nQmvaa 100,136,64\nQtv 12,46,21248\nQmvaa 132,140,64\nQtv 14,46,26368\nQmvaa 164,144,64\nQda 72,53,37888\nQmvaa 196,148,64\n"
"Qtv 16,46,21504\ns_waitcnt lgkmcnt(1)\nQmvaa 228,132,68\nQtv 18,46,26624\nQmvaa 108,136,68\nbuffer_load_dwordx4 a[224:227], v60, s[48:51], s94 offen offset:2048 sc1\nQmvaa 140,140,68\nQtv 20,51,20480\n"
"Qmvaa 172,144,68\nQtv 22,51,25600\nv_add_u32 v75, 1, v75\nQmvaa 204,148,68\ns_waitcnt vmcnt(22)\nds_write_b32 v73, v75\nQmvaa 236,132,72\ns_waitcnt vmcnt(20)\n"
"ds_write_b128 v52, a[4:7] offset:8192\nQmvaa 80,136,72\nQmvaa 112,140,72\nQtv 24,51,20736\nQmvaa 144,144,72\nQda 76,53,41984\nQmvaa 176,148,72\nQtv 26,51,25856\n"
"s_waitcnt lgkmcnt(7)\nQmvva 92,0,248\nQtv 28,51,20992\ns_mov_b32 s94, 849920\nQmvva 124,4,248\nbuffer_load_dwordx4 a[228:231], v60, s[48:51], s94 offen offset:0 sc1\nQmvva 156,8,248\nds_read_b32 v69, v74\n"
"Qmvva 188,12,248\nQtv 30,51,26112\nQmvva 220,16,248\ns_waitcnt vmcnt(20)\nds_write_b128 v52, a[8:11] offset:12288\nQmvva 252,0,252\nQmvva 100,4,252\ns_waitcnt lgkmcnt(2)\n"
"v_cmp_gt_u32 vcc, v75, v69\nQmvva 132,8,252\ns_cbranch_vccnz .Lso57r1\n.Lro57r1:\nQmvva 164,12,252\nQda 80,53,46080\nQmvva 196,16,252\nQtv 32,51,21248\n"
"Qmvva 228,0,168\nQtv 34,51,26368\nQmvva 108,4,168\nbuffer_load_dwordx4 a[232:235], v60, s[48:51], s94 offen offset:1024 sc1\nQmvva 140,8,168\nQtv 36,51,21504\nQmvva 172,12,168\nQtv 38,51,26624\n"
"Qmvva 204,16,168\ns_waitcnt vmcnt(20)\nds_write_b128 v52, a[12:15] offset:16384\nQmvva 236,0,172\nQmvva 80,4,172\nQta 112,45,0\nQmvva 112,8,172\nQta 114,45,5120\n"
"Qmvva 144,12,172\nQda 48,53,50176\nQmvva 176,16,172\nQta 116,45,256\ns_waitcnt lgkmcnt(5)\nQmvva 92,20,76\nQta 118,45,5376\nQmvva 124,24,76\n"
"buffer_load_dwordx4 a[236:239], v60, s[48:51], s94 offen offset:2048 sc1\nQmvva 156,28,76\nQta 120,45,512\nQmvva 188,32,76\nQta 122,45,5632\nQmvva 220,36,76\ns_waitcnt vmcnt(19)\nds_write_b128 v52, a[16:19] offset:20480\n"
"Qmvva 252,20,80\nQmvva 100,24,80\nQta 124,45,768\nQmvva 132,28,80\nQta 126,45,5888\nQmvva 164,32,80\nQda 52,53,0\nv_add_u32 v75, 1, v75\n"
"Qmvva 196,36,80\ns_waitcnt vmcnt(13)\nds_write_b32 v73, v75\ns_waitcnt lgkmcnt(1)\nQmvva 228,20,48\nQta 128,45,1024\nQmvva 108,24,48\nQta 130,45,6144\n"
"Qmvva 140,28,48\nQta 132,47,0\nQmvva 172,32,48\nQta 134,47,5120\nQmvva 204,36,48\ns_waitcnt vmcnt(17)\nds_write_b128 v52, a[20:23] offset:25600\nQmvva 236,20,52\n"
"Qmvva 80,24,52\nQta 136,47,256\nQmvva 112,28,52\nds_read_b32 v69, v74\nQmvva 144,32,52\nQda 56,53,4096\nQmvva 176,36,52\nQta 138,47,5376\n"
"s_waitcnt lgkmcnt(15)\nQmvaa 92,112,0\nQta 140,47,512\nQmvaa 124,116,0\ns_waitcnt lgkmcnt(3)\nv_cmp_gt_u32 vcc, v75, v69\nQmvaa 156,120,0\ns_cbranch_vccnz .Lso58r1\n"
".Lro58r1:\nQmvaa 188,124,0\nQta 142,47,5632\nQmvaa 220,128,0\ns_waitcnt vmcnt(14)\nds_write_b128 v52, a[24:27] offset:29696\nQmvaa 252,112,4\nQmvaa 100,116,4\n"
"Qta 144,47,768\nQmvaa 132,120,4\nQta 146,47,5888\nQmvaa 164,124,4\nQda 60,53,8192\nQmvaa 196,128,4\nQta 148,47,1024\nQmvaa 228,112,8\n"
"Qta 150,47,6144\nQmvaa 108,116,8\nQmvaa 140,120,8\nQtv 0,45,20480\nQmvaa 172,124,8\nQtv 2,45,25600\nQmvaa 204,128,8\ns_waitcnt vmcnt(12)\n"
"ds_write_b128 v52, a[28:31] offset:33792\nQmvaa 236,112,12\nQmvaa 80,116,12\nQtv 4,45,20736\nQmvaa 112,120,12\nQtv 6,45,25856\nQmvaa 144,124,12\nQda 64,53,12288\n"
"Qmvaa 176,128,12\ns_waitcnt lgkmcnt(6)\nQmvaa 92,132,56\nQtv 8,45,20992\nQmvaa 124,136,56\nQtv 10,45,26112\nQmvaa 156,140,56\nQtv 12,45,21248\n"
"Qmvaa 188,144,56\nv_add_u32 v75, 1, v75\nQmvaa 220,148,56\nds_write_b32 v73, v75\nQmvaa 252,132,60\ns_waitcnt vmcnt(11)\nds_write_b128 v52, a[32:35] offset:37888\nQmvaa 100,136,60\n"
"Qmvaa 132,140,60\nQtv 14,45,26368\nQmvaa 164,144,60\nQda 68,53,16384\nQmvaa 196,148,60\nQtv 16,45,21504\ns_waitcnt lgkmcnt(1)\nQmvaa 228,132,64\n"
"Qtv 18,45,26624\nQmvaa 108,136,64\nQmvaa 140,140,64\nds_read_b32 v69, v74\nQmvaa 172,144,64\nQtv 20,47,20480\nQmvaa 204,148,64\ns_waitcnt vmcnt(10)\n"
"ds_write_b128 v52, a[36:39] offset:41984\nQmvaa 236,132,68\nQmvaa 80,136,68\ns_waitcnt lgkmcnt(2)\nv_cmp_gt_u32 vcc, v75, v69\nQmvaa 112,140,68\ns_cbranch_vccnz .Lso59r1\n.Lro59r1:\n"
"Qmvaa 144,144,68\nQda 72,53,20480\nQmvaa 176,148,68\nQtv 22,47,25600\ns_waitcnt lgkmcnt(5)\nQmvva 92,0,16\nQtv 24,47,20736\nQmvva 124,4,16\n"
"Qtv 26,47,25856\nQmvva 156,8,16\nQtv 28,47,20992\nQmvva 188,12,16\nQtv 30,47,26112\nQmvva 220,16,16\ns_waitcnt vmcnt(9)\nds_write_b128 v52, a[40:43] offset:46080\n"
"Qmvva 252,0,20\nQmvva 100,4,20\nQtv 32,47,21248\nQmvva 132,8,20\nQtv 34,47,26368\nQmvva 164,12,20\nQda 76,53,25600\nQmvva 196,16,20\n"
"Qtv 36,47,21504\nQmvva 228,0,24\nQtv 38,47,26624\nQmvva 108,4,24\nQmvva 140,8,24\nQta 112,46,0\nQmvva 172,12,24\nQta 114,46,5120\n"
"Qmvva 204,16,24\ns_waitcnt vmcnt(8)\nds_write_b128 v52, a[44:47] offset:50176\nQmvva 236,0,28\nQmvva 80,4,28\nQta 116,46,256\nQmvva 112,8,28\nQta 118,46,5376\n"
"Qmvva 144,12,28\nQda 80,53,29696\nv_add_u32 v75, 1, v75\nQmvva 176,16,28\nds_write_b32 v73, v75\ns_waitcnt lgkmcnt(7)\nQmvva 92,20,72\nQta 120,46,512\n"
"s_add_u32 s68, s64, 0\nQmvva 124,24,72\nQta 122,46,5632\nQmvva 156,28,72\nQta 124,46,768\nQmvva 188,32,72\nQta 126,46,5888\nQmvva 220,36,72\n"
"s_waitcnt vmcnt(7)\nds_write_b128 v52, a[208:211] offset:0\nQmvva 252,20,76\nQmvva 100,24,76\nQta 128,46,1024\nQk 92,92,93\nQmvva 132,28,76\nds_read_b32 v69, v74\n"
"v_min_u32 v92, -2, v92\nQmvva 164,32,76\nQda 48,53,33792\nQk 93,94,95\nQmvva 196,36,76\nQta 130,46,6144\nv_min_u32 v93, -2, v93\ns_waitcnt lgkmcnt(10)\n"
"Qmvva 228,20,80\nQk 94,124,125\nv_min_u32 v94, -2, v94\nQmvva 108,24,80\ns_waitcnt lgkmcnt(1)\nv_cmp_gt_u32 vcc, v75, v69\nQmvva 140,28,80\ns_cbranch_vccnz .Lso60r1\n"
".Lro60r1:\nQk 95,126,127\nQmvva 172,32,80\nQta 132,51,0\nv_min_u32 v95, -2, v95\nQmvva 204,36,80\ns_waitcnt vmcnt(6)\nds_write_b128 v52, a[212:215] offset:4096\n"
"Qmvva 236,20,48\nbuffer_store_dwordx4 v[92:95], v60, s[24:27], s68 offen offset:0 sc1\nQk 156,156,157\nQmvva 80,24,48\nQta 134,51,5120\nv_min_u32 v156, -2, v156\nQmvva 112,28,48\nQta 136,51,256\n"
"Qk 157,158,159\nQmvva 144,32,48\nQda 52,53,37888\nv_min_u32 v157, -2, v157\nQmvva 176,36,48\nQta 138,51,5376\nQk 158,188,189\ns_waitcnt lgkmcnt(6)\n"
"Qmvaa 208,112,32\nQta 140,51,512\nv_min_u32 v158, -2, v158\nQmvaa 240,116,32\nQk 159,190,191\nv_min_u32 v159, -2, v159\nQmvaa 84,120,32\nQta 142,51,5632\n"
"buffer_store_dwordx4 v[156:159], v60, s[24:27], s68 offen offset:1024 sc1\nQmvaa 88,124,32\nQta 144,51,768\nQk 220,220,221\nQmvaa 120,128,32\ns_waitcnt vmcnt(7)\nds_write_b128 v52, a[216:219] offset:8192\nQmvaa 152,112,36\n"
"v_min_u32 v220, -2, v220\nQk 221,222,223\nQmvaa 184,116,36\nQta 146,51,5888\nv_min_u32 v221, -2, v221\nQmvaa 216,120,36\nQta 148,51,1024\nQk 222,252,253\n"
"Qmvaa 248,124,36\nQda 56,53,41984\nv_min_u32 v222, -2, v222\nQmvaa 116,128,36\nQta 150,51,6144\nQk 223,254,255\nQmvaa 96,112,40\nQtv 0,46,20480\n"
"Qtv 2,46,25600\nQmvaa 128,116,40\nQtv 4,46,20736\nv_min_u32 v223, -2, v223\nQmvaa 160,120,40\nQtv 6,46,25856\nbuffer_store_dwordx4 v[220:223], v60, s[24:27], s68 offen offset:2048 sc1\nQmvaa 192,124,40\n"
"Qtv 8,46,20992\nv_add_u32 v75, 1, v75\nQmvaa 224,128,40\nds_write_b32 v73, v75\nQk 100,100,101\ns_waitcnt lgkmcnt(6)\nQmvaa 208,132,52\ns_waitcnt vmcnt(7)\n"
"ds_write_b128 v52, a[220:223] offset:12288\nQmvaa 240,136,52\nv_min_u32 v100, -2, v100\nQk 101,102,103\nQmvaa 84,140,52\nQtv 10,46,26112\nv_min_u32 v101, -2, v101\nQmvaa 88,144,52\n"
"Qda 60,53,46080\nQk 102,132,133\nQmvaa 120,148,52\nQtv 12,46,21248\nv_min_u32 v102, -2, v102\nQmvaa 152,132,56\nQtv 14,46,26368\nQk 103,134,135\n"
"Qmvaa 184,136,56\nQtv 16,46,21504\nv_min_u32 v103, -2, v103\nQmvaa 216,140,56\nds_read_b32 v69, v74\nbuffer_store_dwordx4 v[100:103], v60, s[24:27], s68 offen offset:3072 sc1\nQmvaa 248,144,56\nQtv 18,46,26624\n"
"Qk 164,164,165\nQmvaa 116,148,56\ns_waitcnt vmcnt(7)\nds_write_b128 v52, a[224:227] offset:16384\ns_waitcnt lgkmcnt(6)\nQmvaa 96,132,60\nv_min_u32 v164, -2, v164\nQk 165,166,167\n"
"Qmvaa 128,136,60\ns_waitcnt lgkmcnt(2)\nv_cmp_gt_u32 vcc, v75, v69\nQmvaa 160,140,60\ns_cbranch_vccnz .Lso61r1\n.Lro61r1:\nv_min_u32 v165, -2, v165\nQmvaa 192,144,60\n"
"Qda 64,53,50176\nQk 166,196,197\nQmvaa 224,148,60\nQtv 20,51,20480\nQtv 22,51,25600\ns_waitcnt lgkmcnt(4)\nQmvva 208,0,44\nQtv 24,51,20736\n"
"v_min_u32 v166, -2, v166\nQmvva 240,4,44\nQtv 26,51,25856\nQk 167,198,199\nQmvva 84,8,44\nQtv 28,51,20992\nv_min_u32 v167, -2, v167\nQmvva 88,12,44\n"
"Qtv 30,51,26112\ns_add_u32 s68, s64, 4096\nQmvva 120,16,44\ns_waitcnt vmcnt(6)\nds_write_b128 v52, a[228:231] offset:20480\nQmvva 152,0,208\nbuffer_store_dwordx4 v[164:167], v60, s[24:27], s68 offen offset:0 sc1\nQk 228,228,229\n"
"Qmvva 184,4,208\nQtv 32,51,21248\nv_min_u32 v228, -2, v228\nQmvva 216,8,208\nQtv 34,51,26368\nQk 229,230,231\nQmvva 248,12,208\nQda 68,53,0\n"
"v_min_u32 v229, -2, v229\nQmvva 116,16,208\nQtv 36,51,21504\nQk 230,108,109\nQmvva 96,0,212\nQtv 38,51,26624\nv_min_u32 v230, -2, v230\nQmvva 128,4,212\n"
"Qta 112,45,0\nQta 114,45,5120\nQmvva 160,8,212\nQta 116,45,256\nQk 231,110,111\nQmvva 192,12,212\nQta 118,45,5376\nv_min_u32 v231, -2, v231\n"
"Qmvva 224,16,212\ns_waitcnt vmcnt(6)\nds_write_b128 v52, a[232:235] offset:25600\ns_waitcnt lgkmcnt(5)\nQmvva 208,20,64\nbuffer_store_dwordx4 v[228:231], v60, s[24:27], s68 offen offset:1024 sc1\nQk 140,140,141\nQmvva 240,24,64\n"
"Qta 120,45,512\nv_min_u32 v140, -2, v140\nQmvva 84,28,64\nQta 122,45,5632\nQk 141,142,143\nQmvva 88,32,64\nQda 72,53,4096\nv_add_u32 v75, 1, v75\n"
"Qmvva 120,36,64\nds_write_b32 v73, v75\nv_min_u32 v141, -2, v141\nQmvva 152,20,68\nQta 124,45,768\nQk 142,172,173\nQmvva 184,24,68\nQta 126,45,5888\n"
"v_min_u32 v142, -2, v142\nQmvva 216,28,68\nQta 128,45,1024\nQk 143,174,175\nQmvva 248,32,68\nQta 130,45,6144\nv_min_u32 v143, -2, v143\nQmvva 116,36,68\n"
"s_waitcnt vmcnt(6)\nds_write_b128 v52, a[236:239] offset:29696\ns_waitcnt lgkmcnt(6)\n.Lpg26:\ns_cbranch_execz .Ltr8\nQmvva 96,20,72\nbuffer_store_dwordx4 v[140:143], v60, s[24:27], s68 offen offset:2048 sc1\nQk 204,204,205\n"
"Qmvva 128,24,72\nQta 132,47,0\nv_min_u32 v204, -2, v204\nQmvva 160,28,72\nds_read_b32 v69, v74\nQk 205,206,207\nQmvva 192,32,72\nQda 76,53,8192\n"
"v_min_u32 v205, -2, v205\nQmvva 224,36,72\nQta 134,47,5120\nQk 206,236,237\ns_waitcnt lgkmcnt(14)\nQmvaa 208,112,216\nQta 136,47,256\nv_min_u32 v206, -2, v206\n"
"Qmvaa 240,116,216\ns_waitcnt lgkmcnt(3)\nv_cmp_gt_u32 vcc, v75, v69\nQmvaa 84,120,216\ns_cbranch_vccnz .Lso62r1\n.Lro62r1:\nQk 207,238,239\nQmvaa 88,124,216\n"
"Qta 138,47,5376\nv_min_u32 v207, -2, v207\nQmvaa 120,128,216\nQta 140,47,512\ns_add_u32 s68, s64, 14336\nQmvaa 152,112,220\nQta 142,47,5632\nbuffer_store_dwordx4 v[204:207], v60, s[24:27], s68 offen offset:0 sc1\n"
"Qmvaa 184,116,220\nQta 144,47,768\nQk 80,80,81\nQmvaa 216,120,220\nQta 146,47,5888\nv_min_u32 v80, -2, v80\nQmvaa 248,124,220\nQda 80,53,12288\n"
"Qk 81,82,83\nQmvaa 116,128,220\nQta 148,47,1024\nv_min_u32 v81, -2, v81\nQmvaa 96,112,224\nQta 150,47,6144\nQk 82,112,113\nQmvaa 128,116,224\n"
"Qtv 0,45,20480\nv_min_u32 v82, -2, v82\nQmvaa 160,120,224\nQtv 2,45,25600\nQk 83,114,115\nQmvaa 192,124,224\nQtv 4,45,20736\nv_min_u32 v83, -2, v83\n"
"Qmvaa 224,128,224\nQtv 6,45,25856\nbuffer_store_dwordx4 v[80:83], v60, s[24:27], s68 offen offset:1024 sc1\ns_waitcnt lgkmcnt(4)\nQmvaa 208,132,76\nQtv 8,45,20992\nQk 144,144,145\nQmvaa 240,136,76\n"
"Qtv 10,45,26112\nv_min_u32 v144, -2, v144\nQmvaa 84,140,76\nQtv 12,45,21248\nQk 145,146,147\nQmvaa 88,144,76\nQda 48,53,16384\nv_min_u32 v145, -2, v145\n"
"Qmvaa 120,148,76\nQtv 14,45,26368\nQk 146,176,177\nQmvaa 152,132,80\nQtv 16,45,21504\nv_min_u32 v146, -2, v146\nQmvaa 184,136,80\nQtv 18,45,26624\n"
"Qk 147,178,179\nQmvaa 216,140,80\nQtv 20,47,20480\nv_min_u32 v147, -2, v147\nQmvaa 248,144,80\nQtv 22,47,25600\nv_add_u32 v75, 1, v75\nQmvaa 116,148,80\n"
"ds_write_b32 v73, v75\nbuffer_store_dwordx4 v[144:147], v60, s[24:27], s68 offen offset:2048 sc1\ns_waitcnt lgkmcnt(6)\nQmvaa 96,132,48\nQtv 24,47,20736\nQmvaa 128,136,48\nQtv 26,47,25856\nQmvaa 160,140,48\n"
"Qtv 28,47,20992\nQmvaa 192,144,48\nQda 52,53,20480\nQmvaa 224,148,48\nQtv 30,47,26112\ns_waitcnt lgkmcnt(8)\nQmvva 208,0,228\nQtv 32,47,21248\n"
"Qmvva 240,4,228\nQtv 34,47,26368\nQmvva 84,8,228\nds_read_b32 v69, v74\nQmvva 88,12,228\nQtv 36,47,21504\nQmvva 120,16,228\nQtv 38,47,26624\n"
"Qmvva 152,0,232\nQmvva 184,4,232\ns_waitcnt lgkmcnt(2)\nv_cmp_gt_u32 vcc, v75, v69\nQmvva 216,8,232\ns_cbranch_vccnz .Lso63r1\n.Lro63r1:\nQmvva 248,12,232\n"
"Qda 56,53,25600\nQmvva 116,16,232\nQmvva 96,0,236\ns_add_u32 s69, s98, 0\nbuffer_load_dwordx4 v[124:127], v60, s[24:27], s69 offen offset:0 sc1\nQmvva 128,4,236\nQmvva 160,8,236\nbuffer_load_dwordx4 v[188:191], v60, s[24:27], s69 offen offset:1024 sc1\n"
"Qmvva 192,12,236\nQmvva 224,16,236\nbuffer_load_dwordx4 v[252:255], v60, s[24:27], s69 offen offset:2048 sc1\ns_waitcnt lgkmcnt(0)\nQmvva 208,20,52\nQmvva 240,24,52\nbuffer_load_dwordx4 v[132:135], v60, s[24:27], s69 offen offset:3072 sc1\nQmvva 84,28,52\n"
"Qmvva 88,32,52\nQda 60,53,29696\nQmvva 120,36,52\ns_add_u32 s69, s98, 4096\nbuffer_load_dwordx4 v[196:199], v60, s[24:27], s69 offen offset:0 sc1\nQmvva 152,20,56\nQmvva 184,24,56\nbuffer_load_dwordx4 v[108:111], v60, s[24:27], s69 offen offset:1024 sc1\n"
"Qmvva 216,28,56\nQmvva 248,32,56\nbuffer_load_dwordx4 v[172:175], v60, s[24:27], s69 offen offset:2048 sc1\nQmvva 116,36,56\ns_waitcnt lgkmcnt(0)\nQmvva 96,20,60\ns_add_u32 s69, s98, 14336\nbuffer_load_dwordx4 v[236:239], v60, s[24:27], s69 offen offset:0 sc1\n"
"Qmvva 128,24,60\nQmvva 160,28,60\nQmvva 192,32,60\nQmvva 224,36,60\ns_barrier\ns_mov_b32 s97, 0\n.Lrx1:\ns_waitcnt vmcnt(0)\n"
"v_max_u32 v40, v124, v125\nv_max3_u32 v40, v40, v126, v127\nv_max3_u32 v40, v40, v188, v189\nv_max3_u32 v40, v40, v190, v191\nv_max3_u32 v40, v40, v252, v253\nv_max3_u32 v40, v40, v254, v255\nv_max3_u32 v40, v40, v132, v133\nv_max3_u32 v40, v40, v134, v135\n"
"v_max3_u32 v40, v40, v196, v197\nv_max3_u32 v40, v40, v198, v199\nv_max3_u32 v40, v40, v108, v109\nv_max3_u32 v40, v40, v110, v111\nv_max3_u32 v40, v40, v172, v173\nv_max3_u32 v40, v40, v174, v175\nv_max3_u32 v40, v40, v236, v237\nv_cmp_eq_u32 vcc, -1, v40\n"
"s_cmp_eq_u64 vcc, 0\ns_cbranch_scc1 .Lrx1_ok\ns_add_u32 s97, s97, 1\ns_cmp_gt_u32 s97, 1048576\ns_cbranch_scc1 .Lrx1_ok\ns_add_u32 s69, s98, 0\nbuffer_load_dwordx4 v[124:127], v60, s[24:27], s69 offen offset:0 sc1\nbuffer_load_dwordx4 v[188:191], v60, s[24:27], s69 offen offset:1024 sc1\n"
"buffer_load_dwordx4 v[252:255], v60, s[24:27], s69 offen offset:2048 sc1\nbuffer_load_dwordx4 v[132:135], v60, s[24:27], s69 offen offset:3072 sc1\ns_add_u32 s69, s98, 4096\nbuffer_load_dwordx4 v[196:199], v60, s[24:27], s69 offen offset:0 sc1\nbuffer_load_dwordx4 v[108:111], v60, s[24:27], s69 offen offset:1024 sc1\nbuffer_load_dwordx4 v[172:175], v60, s[24:27], s69 offen offset:2048 sc1\ns_add_u32 s69, s98, 14336\nbuffer_load_dwordx4 v[236:239], v60, s[24:27], s69 offen offset:0 sc1\n"
"s_branch .Lrx1\n.Lrx1_ok:\nv_lshlrev_b32 v41, 16, v124\nv_and_b32 v42, 0xffff0000, v124\nQa 208,208,41\nQa 209,209,42\nv_lshlrev_b32 v43, 16, v125\nv_and_b32 v44, 0xffff0000, v125\n"
"Qa 210,210,43\nQa 211,211,44\nQk 208,208,209\nQk 209,210,211\nds_write_b64 v61, v[208:209] offset:21504\nv_lshlrev_b32 v41, 16, v126\nv_and_b32 v42, 0xffff0000, v126\nQa 240,240,41\n"
"Qa 241,241,42\nv_lshlrev_b32 v43, 16, v127\nv_and_b32 v44, 0xffff0000, v127\nQa 242,242,43\nQa 243,243,44\nQk 240,240,241\nQk 241,242,243\nds_write_b64 v61, v[240:241] offset:21536\n"
"v_lshlrev_b32 v41, 16, v188\nv_and_b32 v42, 0xffff0000, v188\nQa 84,84,41\nQa 85,85,42\nv_lshlrev_b32 v43, 16, v189\nv_and_b32 v44, 0xffff0000, v189\nQa 86,86,43\nQa 87,87,44\n"
"Qk 84,84,85\nQk 85,86,87\nds_write_b64 v61, v[84:85] offset:21568\nv_lshlrev_b32 v41, 16, v190\nv_and_b32 v42, 0xffff0000, v190\nQa 88,88,41\nQa 89,89,42\nv_lshlrev_b32 v43, 16, v191\n"
"v_and_b32 v44, 0xffff0000, v191\nQa 90,90,43\nQa 91,91,44\nQk 88,88,89\nQk 89,90,91\nds_write_b64 v61, v[88:89] offset:21600\nv_lshlrev_b32 v41, 16, v252\nv_and_b32 v42, 0xffff0000, v252\n"
"Qa 120,120,41\nQa 121,121,42\nv_lshlrev_b32 v43, 16, v253\nv_and_b32 v44, 0xffff0000, v253\nQa 122,122,43\nQa 123,123,44\nQk 120,120,121\nQk 121,122,123\n"
"ds_write_b64 v61, v[120:121] offset:21632\nv_lshlrev_b32 v41, 16, v254\nv_and_b32 v42, 0xffff0000, v254\nQa 152,152,41\nQa 153,153,42\nv_lshlrev_b32 v43, 16, v255\nv_and_b32 v44, 0xffff0000, v255\nQa 154,154,43\n"
"Qa 155,155,44\nQk 152,152,153\nQk 153,154,155\nds_write_b64 v61, v[152:153] offset:26880\nv_lshlrev_b32 v41, 16, v132\nv_and_b32 v42, 0xffff0000, v132\nQa 184,184,41\nQa 185,185,42\n"
"v_lshlrev_b32 v43, 16, v133\nv_and_b32 v44, 0xffff0000, v133\nQa 186,186,43\nQa 187,187,44\nQk 184,184,185\nQk 185,186,187\nds_write_b64 v61, v[184:185] offset:26912\nv_lshlrev_b32 v41, 16, v134\n"
"v_and_b32 v42, 0xffff0000, v134\nQa 216,216,41\nQa 217,217,42\nv_lshlrev_b32 v43, 16, v135\nv_and_b32 v44, 0xffff0000, v135\nQa 218,218,43\nQa 219,219,44\nQk 216,216,217\n"
"Qk 217,218,219\nds_write_b64 v61, v[216:217] offset:26944\nv_lshlrev_b32 v41, 16, v196\nv_and_b32 v42, 0xffff0000, v196\nQa 248,248,41\nQa 249,249,42\nv_lshlrev_b32 v43, 16, v197\nv_and_b32 v44, 0xffff0000, v197\n"
"Qa 250,250,43\nQa 251,251,44\nQk 248,248,249\nQk 249,250,251\nds_write_b64 v61, v[248:249] offset:26976\nv_lshlrev_b32 v41, 16, v198\nv_and_b32 v42, 0xffff0000, v198\nQa 116,116,41\n"
"Qa 117,117,42\nv_lshlrev_b32 v43, 16, v199\nv_and_b32 v44, 0xffff0000, v199\nQa 118,118,43\nQa 119,119,44\nQk 116,116,117\nQk 117,118,119\nds_write_b64 v61, v[116:117] offset:27008\n"
"v_lshlrev_b32 v41, 16, v108\nv_and_b32 v42, 0xffff0000, v108\nQa 96,96,41\nQa 97,97,42\nv_lshlrev_b32 v43, 16, v109\nv_and_b32 v44, 0xffff0000, v109\nQa 98,98,43\nQa 99,99,44\n"
"Qk 96,96,97\nQk 97,98,99\nds_write_b64 v61, v[96:97] offset:32256\nv_lshlrev_b32 v41, 16, v110\nv_and_b32 v42, 0xffff0000, v110\nQa 128,128,41\nQa 129,129,42\nv_lshlrev_b32 v43, 16, v111\n"
"v_and_b32 v44, 0xffff0000, v111\nQa 130,130,43\nQa 131,131,44\nQk 128,128,129\nQk 129,130,131\nds_write_b64 v61, v[128:129] offset:32288\nv_lshlrev_b32 v41, 16, v172\nv_and_b32 v42, 0xffff0000, v172\n"
"Qa 160,160,41\nQa 161,161,42\nv_lshlrev_b32 v43, 16, v173\nv_and_b32 v44, 0xffff0000, v173\nQa 162,162,43\nQa 163,163,44\nQk 160,160,161\nQk 161,162,163\n"
"ds_write_b64 v61, v[160:161] offset:32320\nv_lshlrev_b32 v41, 16, v174\nv_and_b32 v42, 0xffff0000, v174\nQa 192,192,41\nQa 193,193,42\nv_lshlrev_b32 v43, 16, v175\nv_and_b32 v44, 0xffff0000, v175\nQa 194,194,43\n"
"Qa 195,195,44\nQk 192,192,193\nQk 193,194,195\nds_write_b64 v61, v[192:193] offset:32352\nv_lshlrev_b32 v41, 16, v236\nv_and_b32 v42, 0xffff0000, v236\nQa 224,224,41\nQa 225,225,42\n"
"v_lshlrev_b32 v43, 16, v237\nv_and_b32 v44, 0xffff0000, v237\nQa 226,226,43\nQa 227,227,44\nQk 224,224,225\nQk 225,226,227\nds_write_b64 v61, v[224:225] offset:32384\n.Lfin:\n"
".Lpg27:\ns_cbranch_execz .Ltr8\ns_waitcnt lgkmcnt(0)\ns_barrier\nQdv 0,62,0\nQdv 4,62,5376\nQdv 8,62,10752\nQdv 12,62,16128\n"
"Qdv 16,62,21504\nQdv 20,62,26880\nQdv 24,62,32256\nQdv 28,70,0\nQdv 32,70,21504\ns_add_u32 s68, s65, 0\ns_waitcnt lgkmcnt(8)\nbuffer_store_dwordx4 v[0:3], v63, s[40:43], s68 offen sc1\n"
"s_add_u32 s68, s65, 40960\ns_waitcnt lgkmcnt(7)\nbuffer_store_dwordx4 v[4:7], v63, s[40:43], s68 offen sc1\ns_add_u32 s68, s65, 81920\ns_waitcnt lgkmcnt(6)\nbuffer_store_dwordx4 v[8:11], v63, s[40:43], s68 offen sc1\ns_add_u32 s68, s65, 122880\ns_waitcnt lgkmcnt(5)\n"
"buffer_store_dwordx4 v[12:15], v63, s[40:43], s68 offen sc1\ns_add_u32 s68, s60, 163840\ns_waitcnt lgkmcnt(4)\nbuffer_store_dwordx4 v[16:19], v63, s[40:43], s68 offen sc1\ns_add_u32 s68, s60, 204800\ns_waitcnt lgkmcnt(3)\nbuffer_store_dwordx4 v[20:23], v63, s[40:43], s68 offen sc1\ns_add_u32 s68, s60, 245760\n"
"s_waitcnt lgkmcnt(2)\nbuffer_store_dwordx4 v[24:27], v63, s[40:43], s68 offen sc1\ns_add_u32 s68, s60, 163840\ns_cmp_eq_u32 s70, 3\ns_cselect_b32 s68, 0x7ffff000, s68\ns_waitcnt lgkmcnt(1)\nbuffer_store_dwordx4 v[28:31], v71, s[40:43], s65 offen sc1\ns_waitcnt lgkmcnt(0)\n"
"buffer_store_dwordx4 v[32:35], v71, s[40:43], s68 offen sc1\ns_waitcnt vmcnt(0) lgkmcnt(0)\ns_branch .Lexit\n.Lend:\n.Lfb:\ns_waitcnt vmcnt(0) lgkmcnt(0)\nv_lshrrev_b32 v68, 4, v60\nv_and_b32 v69, 15, v68\n"
"v_lshrrev_b32 v68, 4, v68\nv_lshlrev_b32 v66, 8, v69\nv_lshl_add_u32 v66, v68, 4, v66\nv_add_u32 v66, 1024, v66\nv_lshlrev_b32 v67, 10, v68\nv_lshl_add_u32 v67, v69, 1, v67\nv_add_u32 v67, 1024, v67\nv_mov_b32 v68, 0x3f803f80\n"
"v_accvgpr_write_b32 a204, v68\nv_accvgpr_write_b32 a205, v68\nv_accvgpr_write_b32 a206, v68\nv_accvgpr_write_b32 a207, v68\nv_mov_b32 v24, 0xff800000\nv_mov_b32 v28, 0xff800000\nv_mov_b32 v32, 0xff800000\nv_mov_b32 v36, 0xff800000\n"
"v_mov_b32 v40, 0xff800000\nv_mov_b32 v44, 0xff800000\nv_mov_b32 v48, 0xff800000\ns_lshl_b32 s89, s67, 21\ns_mov_b32 s66, 0\n.Lfb1:\ns_add_u32 s68, s89, 4096\nbuffer_load_dwordx4 a[112:115], v66, s[40:43], s89 offen offset:0\n"
"buffer_load_dwordx4 a[116:119], v66, s[40:43], s89 offen offset:64\nbuffer_load_dwordx4 a[120:123], v66, s[40:43], s89 offen offset:128\nbuffer_load_dwordx4 a[124:127], v66, s[40:43], s89 offen offset:192\nbuffer_load_dwordx4 a[128:131], v66, s[40:43], s68 offen offset:0\nbuffer_load_dwordx4 a[132:135], v66, s[40:43], s68 offen offset:64\nbuffer_load_dwordx4 a[136:139], v66, s[40:43], s68 offen offset:128\nbuffer_load_dwordx4 a[140:143], v66, s[40:43], s68 offen offset:192\ns_waitcnt vmcnt(0)\n"
"Qzvaa 0,112,0\nQmvaa 0,116,4\nQmvaa 0,120,8\nQmvaa 0,124,12\nQzvaa 4,128,0\nQmvaa 4,132,4\nQmvaa 4,136,8\nQmvaa 4,140,12\n"
"s_nop 7\nv_max3_f32 v24, v24, v0, v1\nv_max3_f32 v24, v24, v2, v3\nv_max3_f32 v24, v24, v4, v5\nv_max3_f32 v24, v24, v6, v7\nQzvaa 0,112,16\nQmvaa 0,116,20\nQmvaa 0,120,24\n"
"Qmvaa 0,124,28\nQzvaa 4,128,16\nQmvaa 4,132,20\nQmvaa 4,136,24\nQmvaa 4,140,28\ns_nop 7\nv_max3_f32 v28, v28, v0, v1\nv_max3_f32 v28, v28, v2, v3\n"
"v_max3_f32 v28, v28, v4, v5\nv_max3_f32 v28, v28, v6, v7\nQzvaa 0,112,32\nQmvaa 0,116,36\nQmvaa 0,120,40\nQmvaa 0,124,44\nQzvaa 4,128,32\nQmvaa 4,132,36\n"
"Qmvaa 4,136,40\nQmvaa 4,140,44\ns_nop 7\nv_max3_f32 v32, v32, v0, v1\nv_max3_f32 v32, v32, v2, v3\nv_max3_f32 v32, v32, v4, v5\nv_max3_f32 v32, v32, v6, v7\nQzvaa 0,112,48\n"
"Qmvaa 0,116,52\nQmvaa 0,120,56\nQmvaa 0,124,60\nQzvaa 4,128,48\nQmvaa 4,132,52\nQmvaa 4,136,56\nQmvaa 4,140,60\ns_nop 7\n"
"v_max3_f32 v36, v36, v0, v1\nv_max3_f32 v36, v36, v2, v3\nv_max3_f32 v36, v36, v4, v5\nv_max3_f32 v36, v36, v6, v7\nQzvaa 0,112,64\nQmvaa 0,116,68\nQmvaa 0,120,72\nQmvaa 0,124,76\n"
"Qzvaa 4,128,64\nQmvaa 4,132,68\nQmvaa 4,136,72\nQmvaa 4,140,76\ns_nop 7\nv_max3_f32 v40, v40, v0, v1\nv_max3_f32 v40, v40, v2, v3\nv_max3_f32 v40, v40, v4, v5\n"
"v_max3_f32 v40, v40, v6, v7\nQzvaa 0,112,80\nQmvaa 0,116,84\nQmvaa 0,120,88\nQmvaa 0,124,92\nQzvaa 4,128,80\nQmvaa 4,132,84\nQmvaa 4,136,88\n"
"Qmvaa 4,140,92\ns_nop 7\nv_max3_f32 v44, v44, v0, v1\nv_max3_f32 v44, v44, v2, v3\nv_max3_f32 v44, v44, v4, v5\nv_max3_f32 v44, v44, v6, v7\nQzvaa 0,112,96\nQmvaa 0,116,100\n"
"Qmvaa 0,120,104\nQmvaa 0,124,108\nQzvaa 4,128,96\nQmvaa 4,132,100\nQmvaa 4,136,104\nQmvaa 4,140,108\ns_nop 7\nv_max3_f32 v48, v48, v0, v1\n"
"v_max3_f32 v48, v48, v2, v3\nv_max3_f32 v48, v48, v4, v5\nv_max3_f32 v48, v48, v6, v7\ns_add_u32 s89, s89, 8192\ns_add_u32 s66, s66, 1\ns_cmp_lt_u32 s66, 32\ns_cbranch_scc1 .Lfb1\nv_mov_b32 v25, v24\n"
"s_nop 1\nv_permlane32_swap_b32 v24, v25\nv_max_f32 v24, v24, v25\nv_mov_b32 v25, v24\ns_nop 1\nv_permlane16_swap_b32 v24, v25\nv_max_f32 v24, v24, v25\nv_sub_f32 v24, 0, v24\n"
"v_mov_b32 v25, v24\nv_mov_b32 v26, v24\nv_mov_b32 v27, v24\nv_mov_b32 v29, v28\ns_nop 1\nv_permlane32_swap_b32 v28, v29\nv_max_f32 v28, v28, v29\nv_mov_b32 v29, v28\n"
"s_nop 1\nv_permlane16_swap_b32 v28, v29\nv_max_f32 v28, v28, v29\nv_sub_f32 v28, 0, v28\nv_mov_b32 v29, v28\nv_mov_b32 v30, v28\nv_mov_b32 v31, v28\nv_mov_b32 v33, v32\n"
"s_nop 1\nv_permlane32_swap_b32 v32, v33\nv_max_f32 v32, v32, v33\nv_mov_b32 v33, v32\ns_nop 1\nv_permlane16_swap_b32 v32, v33\nv_max_f32 v32, v32, v33\nv_sub_f32 v32, 0, v32\n"
"v_mov_b32 v33, v32\nv_mov_b32 v34, v32\nv_mov_b32 v35, v32\nv_mov_b32 v37, v36\ns_nop 1\nv_permlane32_swap_b32 v36, v37\nv_max_f32 v36, v36, v37\nv_mov_b32 v37, v36\n"
"s_nop 1\nv_permlane16_swap_b32 v36, v37\nv_max_f32 v36, v36, v37\nv_sub_f32 v36, 0, v36\nv_mov_b32 v37, v36\nv_mov_b32 v38, v36\nv_mov_b32 v39, v36\nv_mov_b32 v41, v40\n"
"s_nop 1\nv_permlane32_swap_b32 v40, v41\nv_max_f32 v40, v40, v41\nv_mov_b32 v41, v40\ns_nop 1\nv_permlane16_swap_b32 v40, v41\nv_max_f32 v40, v40, v41\nv_sub_f32 v40, 0, v40\n"
"v_mov_b32 v41, v40\nv_mov_b32 v42, v40\nv_mov_b32 v43, v40\nv_mov_b32 v45, v44\ns_nop 1\nv_permlane32_swap_b32 v44, v45\nv_max_f32 v44, v44, v45\nv_mov_b32 v45, v44\n"
"s_nop 1\nv_permlane16_swap_b32 v44, v45\nv_max_f32 v44, v44, v45\nv_sub_f32 v44, 0, v44\nv_mov_b32 v45, v44\nv_mov_b32 v46, v44\nv_mov_b32 v47, v44\nv_mov_b32 v49, v48\n"
"s_nop 1\nv_permlane32_swap_b32 v48, v49\nv_max_f32 v48, v48, v49\nv_mov_b32 v49, v48\ns_nop 1\nv_permlane16_swap_b32 v48, v49\nv_max_f32 v48, v48, v49\nv_sub_f32 v48, 0, v48\n"
"v_mov_b32 v49, v48\nv_mov_b32 v50, v48\nv_mov_b32 v51, v48\nv_mov_b32 v68, 0\nv_mov_b32 v80, 0\nv_mov_b32 v81, 0\nv_mov_b32 v82, 0\nv_mov_b32 v83, 0\n"
"v_mov_b32 v84, 0\nv_mov_b32 v85, 0\nv_mov_b32 v86, 0\nv_mov_b32 v87, 0\nv_mov_b32 v88, 0\nv_mov_b32 v89, 0\nv_mov_b32 v90, 0\nv_mov_b32 v91, 0\n"
"v_mov_b32 v92, 0\nv_mov_b32 v93, 0\nv_mov_b32 v94, 0\nv_mov_b32 v95, 0\nv_mov_b32 v96, 0\nv_mov_b32 v97, 0\nv_mov_b32 v98, 0\nv_mov_b32 v99, 0\n"
"v_mov_b32 v100, 0\nv_mov_b32 v101, 0\nv_mov_b32 v102, 0\nv_mov_b32 v103, 0\nv_mov_b32 v104, 0\nv_mov_b32 v105, 0\nv_mov_b32 v106, 0\nv_mov_b32 v107, 0\n"
"v_mov_b32 v108, 0\nv_mov_b32 v109, 0\nv_mov_b32 v110, 0\nv_mov_b32 v111, 0\nv_accvgpr_write_b32 a176, v68\nv_accvgpr_write_b32 a177, v68\nv_accvgpr_write_b32 a178, v68\nv_accvgpr_write_b32 a179, v68\n"
"v_mov_b32 v112, 0\nv_mov_b32 v113, 0\nv_mov_b32 v114, 0\nv_mov_b32 v115, 0\nv_mov_b32 v116, 0\nv_mov_b32 v117, 0\nv_mov_b32 v118, 0\nv_mov_b32 v119, 0\n"
"v_mov_b32 v120, 0\nv_mov_b32 v121, 0\nv_mov_b32 v122, 0\nv_mov_b32 v123, 0\nv_mov_b32 v124, 0\nv_mov_b32 v125, 0\nv_mov_b32 v126, 0\nv_mov_b32 v127, 0\n"
"v_mov_b32 v128, 0\nv_mov_b32 v129, 0\nv_mov_b32 v130, 0\nv_mov_b32 v131, 0\nv_mov_b32 v132, 0\nv_mov_b32 v133, 0\nv_mov_b32 v134, 0\nv_mov_b32 v135, 0\n"
"v_mov_b32 v136, 0\nv_mov_b32 v137, 0\nv_mov_b32 v138, 0\nv_mov_b32 v139, 0\nv_mov_b32 v140, 0\nv_mov_b32 v141, 0\nv_mov_b32 v142, 0\nv_mov_b32 v143, 0\n"
"v_accvgpr_write_b32 a180, v68\nv_accvgpr_write_b32 a181, v68\nv_accvgpr_write_b32 a182, v68\nv_accvgpr_write_b32 a183, v68\nv_mov_b32 v144, 0\nv_mov_b32 v145, 0\nv_mov_b32 v146, 0\nv_mov_b32 v147, 0\n"
"v_mov_b32 v148, 0\nv_mov_b32 v149, 0\nv_mov_b32 v150, 0\nv_mov_b32 v151, 0\nv_mov_b32 v152, 0\nv_mov_b32 v153, 0\nv_mov_b32 v154, 0\nv_mov_b32 v155, 0\n"
"v_mov_b32 v156, 0\nv_mov_b32 v157, 0\nv_mov_b32 v158, 0\nv_mov_b32 v159, 0\nv_mov_b32 v160, 0\nv_mov_b32 v161, 0\nv_mov_b32 v162, 0\nv_mov_b32 v163, 0\n"
"v_mov_b32 v164, 0\nv_mov_b32 v165, 0\nv_mov_b32 v166, 0\nv_mov_b32 v167, 0\nv_mov_b32 v168, 0\nv_mov_b32 v169, 0\nv_mov_b32 v170, 0\nv_mov_b32 v171, 0\n"
"v_mov_b32 v172, 0\nv_mov_b32 v173, 0\nv_mov_b32 v174, 0\nv_mov_b32 v175, 0\nv_accvgpr_write_b32 a184, v68\nv_accvgpr_write_b32 a185, v68\nv_accvgpr_write_b32 a186, v68\nv_accvgpr_write_b32 a187, v68\n"
"v_mov_b32 v176, 0\nv_mov_b32 v177, 0\nv_mov_b32 v178, 0\nv_mov_b32 v179, 0\nv_mov_b32 v180, 0\nv_mov_b32 v181, 0\nv_mov_b32 v182, 0\nv_mov_b32 v183, 0\n"
"v_mov_b32 v184, 0\nv_mov_b32 v185, 0\nv_mov_b32 v186, 0\nv_mov_b32 v187, 0\nv_mov_b32 v188, 0\nv_mov_b32 v189, 0\nv_mov_b32 v190, 0\nv_mov_b32 v191, 0\n"
"v_mov_b32 v192, 0\nv_mov_b32 v193, 0\nv_mov_b32 v194, 0\nv_mov_b32 v195, 0\nv_mov_b32 v196, 0\nv_mov_b32 v197, 0\nv_mov_b32 v198, 0\nv_mov_b32 v199, 0\n"
"v_mov_b32 v200, 0\nv_mov_b32 v201, 0\nv_mov_b32 v202, 0\nv_mov_b32 v203, 0\nv_mov_b32 v204, 0\nv_mov_b32 v205, 0\nv_mov_b32 v206, 0\nv_mov_b32 v207, 0\n"
"v_accvgpr_write_b32 a188, v68\nv_accvgpr_write_b32 a189, v68\nv_accvgpr_write_b32 a190, v68\nv_accvgpr_write_b32 a191, v68\nv_mov_b32 v208, 0\nv_mov_b32 v209, 0\nv_mov_b32 v210, 0\nv_mov_b32 v211, 0\n"
"v_mov_b32 v212, 0\nv_mov_b32 v213, 0\nv_mov_b32 v214, 0\nv_mov_b32 v215, 0\nv_mov_b32 v216, 0\nv_mov_b32 v217, 0\nv_mov_b32 v218, 0\nv_mov_b32 v219, 0\n"
"v_mov_b32 v220, 0\nv_mov_b32 v221, 0\nv_mov_b32 v222, 0\nv_mov_b32 v223, 0\nv_mov_b32 v224, 0\nv_mov_b32 v225, 0\nv_mov_b32 v226, 0\nv_mov_b32 v227, 0\n"
"v_mov_b32 v228, 0\nv_mov_b32 v229, 0\nv_mov_b32 v230, 0\nv_mov_b32 v231, 0\nv_mov_b32 v232, 0\nv_mov_b32 v233, 0\nv_mov_b32 v234, 0\nv_mov_b32 v235, 0\n"
"v_mov_b32 v236, 0\nv_mov_b32 v237, 0\nv_mov_b32 v238, 0\nv_mov_b32 v239, 0\nv_accvgpr_write_b32 a192, v68\nv_accvgpr_write_b32 a193, v68\nv_accvgpr_write_b32 a194, v68\nv_accvgpr_write_b32 a195, v68\n"
"v_mov_b32 v240, 0\nv_mov_b32 v241, 0\nv_mov_b32 v242, 0\nv_mov_b32 v243, 0\nv_mov_b32 v244, 0\nv_mov_b32 v245, 0\nv_mov_b32 v246, 0\nv_mov_b32 v247, 0\n"
"v_mov_b32 v248, 0\nv_mov_b32 v249, 0\nv_mov_b32 v250, 0\nv_mov_b32 v251, 0\nv_mov_b32 v252, 0\nv_mov_b32 v253, 0\nv_mov_b32 v254, 0\nv_mov_b32 v255, 0\n"
"v_accvgpr_write_b32 a208, v68\nv_accvgpr_write_b32 a209, v68\nv_accvgpr_write_b32 a210, v68\nv_accvgpr_write_b32 a211, v68\nv_accvgpr_write_b32 a212, v68\nv_accvgpr_write_b32 a213, v68\nv_accvgpr_write_b32 a214, v68\nv_accvgpr_write_b32 a215, v68\n"
"v_accvgpr_write_b32 a216, v68\nv_accvgpr_write_b32 a217, v68\nv_accvgpr_write_b32 a218, v68\nv_accvgpr_write_b32 a219, v68\nv_accvgpr_write_b32 a220, v68\nv_accvgpr_write_b32 a221, v68\nv_accvgpr_write_b32 a222, v68\nv_accvgpr_write_b32 a223, v68\n"
"v_accvgpr_write_b32 a196, v68\nv_accvgpr_write_b32 a197, v68\nv_accvgpr_write_b32 a198, v68\nv_accvgpr_write_b32 a199, v68\nv_accvgpr_write_b32 a224, v68\nv_accvgpr_write_b32 a225, v68\nv_accvgpr_write_b32 a226, v68\nv_accvgpr_write_b32 a227, v68\n"
"v_accvgpr_write_b32 a228, v68\nv_accvgpr_write_b32 a229, v68\nv_accvgpr_write_b32 a230, v68\nv_accvgpr_write_b32 a231, v68\nv_accvgpr_write_b32 a232, v68\nv_accvgpr_write_b32 a233, v68\nv_accvgpr_write_b32 a234, v68\nv_accvgpr_write_b32 a235, v68\n"
"v_accvgpr_write_b32 a236, v68\nv_accvgpr_write_b32 a237, v68\nv_accvgpr_write_b32 a238, v68\nv_accvgpr_write_b32 a239, v68\nv_accvgpr_write_b32 a240, v68\nv_accvgpr_write_b32 a241, v68\nv_accvgpr_write_b32 a242, v68\nv_accvgpr_write_b32 a243, v68\n"
"v_accvgpr_write_b32 a244, v68\nv_accvgpr_write_b32 a245, v68\nv_accvgpr_write_b32 a246, v68\nv_accvgpr_write_b32 a247, v68\nv_accvgpr_write_b32 a248, v68\nv_accvgpr_write_b32 a249, v68\nv_accvgpr_write_b32 a250, v68\nv_accvgpr_write_b32 a251, v68\n"
"v_accvgpr_write_b32 a252, v68\nv_accvgpr_write_b32 a253, v68\nv_accvgpr_write_b32 a254, v68\nv_accvgpr_write_b32 a255, v68\nv_accvgpr_write_b32 a200, v68\nv_accvgpr_write_b32 a201, v68\nv_accvgpr_write_b32 a202, v68\nv_accvgpr_write_b32 a203, v68\n"
"s_lshl_b32 s89, s67, 21\ns_mov_b32 s66, 0\n.Lfb2:\ns_add_u32 s68, s89, 4096\nbuffer_load_dwordx4 a[112:115], v66, s[40:43], s89 offen offset:0\nbuffer_load_dwordx4 a[116:119], v66, s[40:43], s89 offen offset:64\nbuffer_load_dwordx4 a[120:123], v66, s[40:43], s89 offen offset:128\nbuffer_load_dwordx4 a[124:127], v66, s[40:43], s89 offen offset:192\n"
"buffer_load_dwordx4 a[128:131], v66, s[40:43], s68 offen offset:0\nbuffer_load_dwordx4 a[132:135], v66, s[40:43], s68 offen offset:64\nbuffer_load_dwordx4 a[136:139], v66, s[40:43], s68 offen offset:128\nbuffer_load_dwordx4 a[140:143], v66, s[40:43], s68 offen offset:192\nbuffer_load_ushort v8, v67, s[44:47], s89 offen offset:0\nbuffer_load_ushort v9, v67, s[44:47], s89 offen offset:256\nbuffer_load_ushort v10, v67, s[44:47], s89 offen offset:512\nbuffer_load_ushort v11, v67, s[44:47], s89 offen offset:768\n"
"buffer_load_ushort v12, v67, s[44:47], s68 offen offset:0\nbuffer_load_ushort v13, v67, s[44:47], s68 offen offset:256\nbuffer_load_ushort v14, v67, s[44:47], s68 offen offset:512\nbuffer_load_ushort v15, v67, s[44:47], s68 offen offset:768\ns_waitcnt vmcnt(0)\nv_lshl_or_b32 v8, v9, 16, v8\nv_accvgpr_write_b32 a144, v8\nv_lshl_or_b32 v10, v11, 16, v10\n"
"v_accvgpr_write_b32 a145, v10\nv_lshl_or_b32 v12, v13, 16, v12\nv_accvgpr_write_b32 a146, v12\nv_lshl_or_b32 v14, v15, 16, v14\nv_accvgpr_write_b32 a147, v14\nbuffer_load_ushort v8, v67, s[44:47], s89 offen offset:32\nbuffer_load_ushort v9, v67, s[44:47], s89 offen offset:288\nbuffer_load_ushort v10, v67, s[44:47], s89 offen offset:544\n"
"buffer_load_ushort v11, v67, s[44:47], s89 offen offset:800\nbuffer_load_ushort v12, v67, s[44:47], s68 offen offset:32\nbuffer_load_ushort v13, v67, s[44:47], s68 offen offset:288\nbuffer_load_ushort v14, v67, s[44:47], s68 offen offset:544\nbuffer_load_ushort v15, v67, s[44:47], s68 offen offset:800\ns_waitcnt vmcnt(0)\nv_lshl_or_b32 v8, v9, 16, v8\nv_accvgpr_write_b32 a148, v8\n"
"v_lshl_or_b32 v10, v11, 16, v10\nv_accvgpr_write_b32 a149, v10\nv_lshl_or_b32 v12, v13, 16, v12\nv_accvgpr_write_b32 a150, v12\nv_lshl_or_b32 v14, v15, 16, v14\nv_accvgpr_write_b32 a151, v14\nbuffer_load_ushort v8, v67, s[44:47], s89 offen offset:64\nbuffer_load_ushort v9, v67, s[44:47], s89 offen offset:320\n"
"buffer_load_ushort v10, v67, s[44:47], s89 offen offset:576\nbuffer_load_ushort v11, v67, s[44:47], s89 offen offset:832\nbuffer_load_ushort v12, v67, s[44:47], s68 offen offset:64\nbuffer_load_ushort v13, v67, s[44:47], s68 offen offset:320\nbuffer_load_ushort v14, v67, s[44:47], s68 offen offset:576\nbuffer_load_ushort v15, v67, s[44:47], s68 offen offset:832\ns_waitcnt vmcnt(0)\nv_lshl_or_b32 v8, v9, 16, v8\n"
"v_accvgpr_write_b32 a152, v8\nv_lshl_or_b32 v10, v11, 16, v10\nv_accvgpr_write_b32 a153, v10\nv_lshl_or_b32 v12, v13, 16, v12\nv_accvgpr_write_b32 a154, v12\nv_lshl_or_b32 v14, v15, 16, v14\nv_accvgpr_write_b32 a155, v14\nbuffer_load_ushort v8, v67, s[44:47], s89 offen offset:96\n"
"buffer_load_ushort v9, v67, s[44:47], s89 offen offset:352\nbuffer_load_ushort v10, v67, s[44:47], s89 offen offset:608\nbuffer_load_ushort v11, v67, s[44:47], s89 offen offset:864\nbuffer_load_ushort v12, v67, s[44:47], s68 offen offset:96\nbuffer_load_ushort v13, v67, s[44:47], s68 offen offset:352\nbuffer_load_ushort v14, v67, s[44:47], s68 offen offset:608\nbuffer_load_ushort v15, v67, s[44:47], s68 offen offset:864\ns_waitcnt vmcnt(0)\n"
"v_lshl_or_b32 v8, v9, 16, v8\nv_accvgpr_write_b32 a156, v8\nv_lshl_or_b32 v10, v11, 16, v10\nv_accvgpr_write_b32 a157, v10\nv_lshl_or_b32 v12, v13, 16, v12\nv_accvgpr_write_b32 a158, v12\nv_lshl_or_b32 v14, v15, 16, v14\nv_accvgpr_write_b32 a159, v14\n"
"buffer_load_ushort v8, v67, s[44:47], s89 offen offset:128\nbuffer_load_ushort v9, v67, s[44:47], s89 offen offset:384\nbuffer_load_ushort v10, v67, s[44:47], s89 offen offset:640\nbuffer_load_ushort v11, v67, s[44:47], s89 offen offset:896\nbuffer_load_ushort v12, v67, s[44:47], s68 offen offset:128\nbuffer_load_ushort v13, v67, s[44:47], s68 offen offset:384\nbuffer_load_ushort v14, v67, s[44:47], s68 offen offset:640\nbuffer_load_ushort v15, v67, s[44:47], s68 offen offset:896\n"
"s_waitcnt vmcnt(0)\nv_lshl_or_b32 v8, v9, 16, v8\nv_accvgpr_write_b32 a160, v8\nv_lshl_or_b32 v10, v11, 16, v10\nv_accvgpr_write_b32 a161, v10\nv_lshl_or_b32 v12, v13, 16, v12\nv_accvgpr_write_b32 a162, v12\nv_lshl_or_b32 v14, v15, 16, v14\n"
"v_accvgpr_write_b32 a163, v14\nbuffer_load_ushort v8, v67, s[44:47], s89 offen offset:160\nbuffer_load_ushort v9, v67, s[44:47], s89 offen offset:416\nbuffer_load_ushort v10, v67, s[44:47], s89 offen offset:672\nbuffer_load_ushort v11, v67, s[44:47], s89 offen offset:928\nbuffer_load_ushort v12, v67, s[44:47], s68 offen offset:160\nbuffer_load_ushort v13, v67, s[44:47], s68 offen offset:416\nbuffer_load_ushort v14, v67, s[44:47], s68 offen offset:672\n"
"buffer_load_ushort v15, v67, s[44:47], s68 offen offset:928\ns_waitcnt vmcnt(0)\nv_lshl_or_b32 v8, v9, 16, v8\nv_accvgpr_write_b32 a164, v8\nv_lshl_or_b32 v10, v11, 16, v10\nv_accvgpr_write_b32 a165, v10\nv_lshl_or_b32 v12, v13, 16, v12\nv_accvgpr_write_b32 a166, v12\n"
"v_lshl_or_b32 v14, v15, 16, v14\nv_accvgpr_write_b32 a167, v14\nbuffer_load_ushort v8, v67, s[44:47], s89 offen offset:192\nbuffer_load_ushort v9, v67, s[44:47], s89 offen offset:448\nbuffer_load_ushort v10, v67, s[44:47], s89 offen offset:704\nbuffer_load_ushort v11, v67, s[44:47], s89 offen offset:960\nbuffer_load_ushort v12, v67, s[44:47], s68 offen offset:192\nbuffer_load_ushort v13, v67, s[44:47], s68 offen offset:448\n"
"buffer_load_ushort v14, v67, s[44:47], s68 offen offset:704\nbuffer_load_ushort v15, v67, s[44:47], s68 offen offset:960\ns_waitcnt vmcnt(0)\nv_lshl_or_b32 v8, v9, 16, v8\nv_accvgpr_write_b32 a168, v8\nv_lshl_or_b32 v10, v11, 16, v10\nv_accvgpr_write_b32 a169, v10\nv_lshl_or_b32 v12, v13, 16, v12\n"
"v_accvgpr_write_b32 a170, v12\nv_lshl_or_b32 v14, v15, 16, v14\nv_accvgpr_write_b32 a171, v14\nbuffer_load_ushort v8, v67, s[44:47], s89 offen offset:224\nbuffer_load_ushort v9, v67, s[44:47], s89 offen offset:480\nbuffer_load_ushort v10, v67, s[44:47], s89 offen offset:736\nbuffer_load_ushort v11, v67, s[44:47], s89 offen offset:992\nbuffer_load_ushort v12, v67, s[44:47], s68 offen offset:224\n"
"buffer_load_ushort v13, v67, s[44:47], s68 offen offset:480\nbuffer_load_ushort v14, v67, s[44:47], s68 offen offset:736\nbuffer_load_ushort v15, v67, s[44:47], s68 offen offset:992\ns_waitcnt vmcnt(0)\nv_lshl_or_b32 v8, v9, 16, v8\nv_accvgpr_write_b32 a172, v8\nv_lshl_or_b32 v10, v11, 16, v10\nv_accvgpr_write_b32 a173, v10\n"
"v_lshl_or_b32 v12, v13, 16, v12\nv_accvgpr_write_b32 a174, v12\nv_lshl_or_b32 v14, v15, 16, v14\nv_accvgpr_write_b32 a175, v14\nQcvaa 0,112,0,24\nQmvaa 0,116,4\nQmvaa 0,120,8\nQmvaa 0,124,12\n"
"Qcvaa 4,128,0,24\nQmvaa 4,132,4\nQmvaa 4,136,8\nQmvaa 4,140,12\ns_nop 7\nQe 0\nQe 1\nQe 2\n"
"Qe 3\nQe 4\nQe 5\nQe 6\nQe 7\ns_nop 0\nQk 16,0,1\nQk 17,2,3\n"
"Qk 18,4,5\nQk 19,6,7\ns_nop 1\nQmvav 80,144,16\nQmvav 84,148,16\nQmvav 88,152,16\nQmvav 92,156,16\nQmvav 96,160,16\n"
"Qmvav 100,164,16\nQmvav 104,168,16\nQmvav 108,172,16\nQmaav 176,204,16\nQcvaa 0,112,16,28\nQmvaa 0,116,20\nQmvaa 0,120,24\nQmvaa 0,124,28\n"
"Qcvaa 4,128,16,28\nQmvaa 4,132,20\nQmvaa 4,136,24\nQmvaa 4,140,28\ns_nop 7\nQe 0\nQe 1\nQe 2\n"
"Qe 3\nQe 4\nQe 5\nQe 6\nQe 7\ns_nop 0\nQk 16,0,1\nQk 17,2,3\n"
"Qk 18,4,5\nQk 19,6,7\ns_nop 1\nQmvav 112,144,16\nQmvav 116,148,16\nQmvav 120,152,16\nQmvav 124,156,16\nQmvav 128,160,16\n"
"Qmvav 132,164,16\nQmvav 136,168,16\nQmvav 140,172,16\nQmaav 180,204,16\nQcvaa 0,112,32,32\nQmvaa 0,116,36\nQmvaa 0,120,40\nQmvaa 0,124,44\n"
"Qcvaa 4,128,32,32\nQmvaa 4,132,36\nQmvaa 4,136,40\nQmvaa 4,140,44\ns_nop 7\nQe 0\nQe 1\nQe 2\n"
"Qe 3\nQe 4\nQe 5\nQe 6\nQe 7\ns_nop 0\nQk 16,0,1\nQk 17,2,3\n"
"Qk 18,4,5\nQk 19,6,7\ns_nop 1\nQmvav 144,144,16\nQmvav 148,148,16\nQmvav 152,152,16\nQmvav 156,156,16\nQmvav 160,160,16\n"
"Qmvav 164,164,16\nQmvav 168,168,16\nQmvav 172,172,16\nQmaav 184,204,16\nQcvaa 0,112,48,36\nQmvaa 0,116,52\nQmvaa 0,120,56\nQmvaa 0,124,60\n"
"Qcvaa 4,128,48,36\nQmvaa 4,132,52\nQmvaa 4,136,56\nQmvaa 4,140,60\ns_nop 7\nQe 0\nQe 1\nQe 2\n"
"Qe 3\nQe 4\nQe 5\nQe 6\nQe 7\ns_nop 0\nQk 16,0,1\nQk 17,2,3\n"
"Qk 18,4,5\nQk 19,6,7\ns_nop 1\nQmvav 176,144,16\nQmvav 180,148,16\nQmvav 184,152,16\nQmvav 188,156,16\nQmvav 192,160,16\n"
"Qmvav 196,164,16\nQmvav 200,168,16\nQmvav 204,172,16\nQmaav 188,204,16\nQcvaa 0,112,64,40\nQmvaa 0,116,68\nQmvaa 0,120,72\nQmvaa 0,124,76\n"
"Qcvaa 4,128,64,40\nQmvaa 4,132,68\nQmvaa 4,136,72\nQmvaa 4,140,76\ns_nop 7\nQe 0\nQe 1\nQe 2\n"
"Qe 3\nQe 4\nQe 5\nQe 6\nQe 7\ns_nop 0\nQk 16,0,1\nQk 17,2,3\n"
"Qk 18,4,5\nQk 19,6,7\ns_nop 1\nQmvav 208,144,16\nQmvav 212,148,16\nQmvav 216,152,16\nQmvav 220,156,16\nQmvav 224,160,16\n"
"Qmvav 228,164,16\nQmvav 232,168,16\nQmvav 236,172,16\nQmaav 192,204,16\nQcvaa 0,112,80,44\nQmvaa 0,116,84\nQmvaa 0,120,88\nQmvaa 0,124,92\n"
"Qcvaa 4,128,80,44\nQmvaa 4,132,84\nQmvaa 4,136,88\nQmvaa 4,140,92\ns_nop 7\nQe 0\nQe 1\nQe 2\n"
"Qe 3\nQe 4\nQe 5\nQe 6\nQe 7\ns_nop 0\nQk 16,0,1\nQk 17,2,3\n"
"Qk 18,4,5\nQk 19,6,7\ns_nop 1\nQmvav 240,144,16\nQmvav 244,148,16\nQmvav 248,152,16\nQmvav 252,156,16\nQmaav 208,160,16\n"
"Qmaav 212,164,16\nQmaav 216,168,16\nQmaav 220,172,16\nQmaav 196,204,16\nQcvaa 0,112,96,48\nQmvaa 0,116,100\nQmvaa 0,120,104\nQmvaa 0,124,108\n"
"Qcvaa 4,128,96,48\nQmvaa 4,132,100\nQmvaa 4,136,104\nQmvaa 4,140,108\ns_nop 7\nQe 0\nQe 1\nQe 2\n"
"Qe 3\nQe 4\nQe 5\nQe 6\nQe 7\ns_nop 0\nQk 16,0,1\nQk 17,2,3\n"
"Qk 18,4,5\nQk 19,6,7\ns_nop 1\nQmaav 224,144,16\nQmaav 228,148,16\nQmaav 232,152,16\nQmaav 236,156,16\nQmaav 240,160,16\n"
"Qmaav 244,164,16\nQmaav 248,168,16\nQmaav 252,172,16\nQmaav 200,204,16\ns_add_u32 s89, s89, 8192\ns_add_u32 s66, s66, 1\ns_cmp_lt_u32 s66, 32\ns_cbranch_scc1 .Lfb2\n"
"s_nop 7\nv_accvgpr_read_b32 v24, a176\nv_accvgpr_read_b32 v28, a180\nv_accvgpr_read_b32 v32, a184\nv_accvgpr_read_b32 v36, a188\nv_accvgpr_read_b32 v40, a192\nv_accvgpr_read_b32 v44, a196\nv_accvgpr_read_b32 v48, a200\n"
"s_cmp_eq_u32 s67, 0\ns_cbranch_scc1 .Lfbret0\ns_branch .Lfbret1\n.Lf2_w:\ns_mov_b32 s97, 0\n.Lf2_l:\ns_sleep 1\nbuffer_load_dword v78, v76, s[24:27], 0 offen sc1\n"
"s_waitcnt vmcnt(0)\ns_add_u32 s97, s97, 1\ns_cmp_gt_u32 s97, 1048576\ns_cbranch_scc1 .Lf2_r\nv_sub_u32 v66, v78, v65\nv_cmp_gt_u32 vcc, 2, v66\ns_and_b64 vcc, vcc, s[28:29]\ns_cbranch_scc1 .Lf2_l\n"
"s_branch .Lf2_r\n.Lf3_w:\ns_mov_b32 s97, 0\n.Lf3_l:\ns_sleep 1\nbuffer_load_dword v78, v76, s[24:27], 0 offen sc1\ns_waitcnt vmcnt(0)\ns_add_u32 s97, s97, 1\n"
"s_cmp_gt_u32 s97, 1048576\ns_cbranch_scc1 .Lf3_r\nv_sub_u32 v66, v78, v65\nv_cmp_gt_u32 vcc, 3, v66\ns_and_b64 vcc, vcc, s[28:29]\ns_cbranch_scc1 .Lf3_l\ns_branch .Lf3_r\n.Lf4_w:\n"
"s_mov_b32 s97, 0\n.Lf4_l:\ns_sleep 1\nbuffer_load_dword v78, v76, s[24:27], 0 offen sc1\ns_waitcnt vmcnt(0)\ns_add_u32 s97, s97, 1\ns_cmp_gt_u32 s97, 1048576\ns_cbranch_scc1 .Lf4_r\n"
"v_sub_u32 v66, v78, v65\nv_cmp_gt_u32 vcc, 4, v66\ns_and_b64 vcc, vcc, s[28:29]\ns_cbranch_scc1 .Lf4_l\ns_branch .Lf4_r\n.Lf5_w:\ns_mov_b32 s97, 0\n.Lf5_l:\n"
"s_sleep 1\nbuffer_load_dword v78, v76, s[24:27], 0 offen sc1\ns_waitcnt vmcnt(0)\ns_add_u32 s97, s97, 1\ns_cmp_gt_u32 s97, 1048576\ns_cbranch_scc1 .Lf5_r\nv_sub_u32 v66, v78, v65\nv_cmp_gt_u32 vcc, 5, v66\n"
"s_and_b64 vcc, vcc, s[28:29]\ns_cbranch_scc1 .Lf5_l\ns_branch .Lf5_r\n.Lexit:\n"
  :: "s"((unsigned)kp), "s"((unsigned)(kp >> 32)), "s"((unsigned)vp), "s"((unsigned)(vp >> 32)), "s"((unsigned)xp), "s"((unsigned)(xp >> 32)),
     "s"((unsigned)wp), "s"((unsigned)(wp >> 32)), "s"((unsigned)op), "s"((unsigned)(op >> 32)), "s"(ldsb), "s"(wid), "s"(head0),
     "s"((unsigned)dp), "s"((unsigned)(dp >> 32)), "s"((unsigned)wop), "s"((unsigned)(wop >> 32)), "s"((unsigned)yp), "s"((unsigned)(yp >> 32)),
     "s"(hg), "s"(b * 8u + hg)
  : "v0", "v1", "v2", "v3", "v4", "v5", "v6", "v7", "v8", "v9", "v10", "v11", "v12", "v13", "v14", "v15", "v16", "v17", "v18", "v19", "v20", "v21", "v22", "v23", "v24", "v25", "v26", "v27", "v28", "v29", "v30", "v31", "v32", "v33", "v34", "v35", "v36", "v37", "v38", "v39", "v40", "v41", "v42", "v43", "v44", "v45", "v46", "v47", "v48", "v49", "v50", "v51", "v52", "v53", "v54", "v55", "v56", "v57", "v58", "v59", "v60", "v61", "v62", "v63", "v64", "v65", "v66", "v67", "v68", "v69", "v70", "v71", "v72", "v73", "v74", "v75", "v76", "v77", "v78", "v79", "v80", "v81", "v82", "v83", "v84", "v85", "v86", "v87", "v88", "v89", "v90", "v91", "v92", "v93", "v94", "v95", "v96", "v97", "v98", "v99", "v100", "v101", "v102", "v103", "v104", "v105", "v106", "v107", "v108", "v109", "v110", "v111", "v112", "v113", "v114", "v115", "v116", "v117", "v118", "v119", "v120", "v121", "v122", "v123", "v124", "v125", "v126", "v127", "v128", "v129", "v130", "v131", "v132", "v133", "v134", "v135", "v136", "v137", "v138", "v139", "v140", "v141", "v142", "v143", "v144", "v145", "v146", "v147", "v148", "v149", "v150", "v151", "v152", "v153", "v154", "v155", "v156", "v157", "v158", "v159", "v160", "v161", "v162", "v163", "v164", "v165", "v166", "v167", "v168", "v169", "v170", "v171", "v172", "v173", "v174", "v175", "v176", "v177", "v178", "v179", "v180", "v181", "v182", "v183", "v184", "v185", "v186", "v187", "v188", "v189", "v190", "v191", "v192", "v193", "v194", "v195", "v196", "v197", "v198", "v199", "v200", "v201", "v202", "v203", "v204", "v205", "v206", "v207", "v208", "v209", "v210", "v211", "v212", "v213", "v214", "v215", "v216", "v217", "v218", "v219", "v220", "v221", "v222", "v223", "v224", "v225", "v226", "v227", "v228", "v229", "v230", "v231", "v232", "v233", "v234", "v235", "v236", "v237", "v238", "v239", "v240", "v241", "v242", "v243", "v244", "v245", "v246", "v247", "v248", "v249", "v250", "v251", "v252", "v253", "v254", "v255", "a0", "a1", "a2", "a3", "a4", "a5", "a6", "a7", "a8", "a9", "a10", "a11", "a12", "a13", "a14", "a15", "a16", "a17", "a18", "a19", "a20", "a21", "a22", "a23", "a24", "a25", "a26", "a27", "a28", "a29", "a30", "a31", "a32", "a33", "a34", "a35", "a36", "a37", "a38", "a39", "a40", "a41", "a42", "a43", "a44", "a45", "a46", "a47", "a48", "a49", "a50", "a51", "a52", "a53", "a54", "a55", "a56", "a57", "a58", "a59", "a60", "a61", "a62", "a63", "a64", "a65", "a66", "a67", "a68", "a69", "a70", "a71", "a72", "a73", "a74", "a75", "a76", "a77", "a78", "a79", "a80", "a81", "a82", "a83", "a84", "a85", "a86", "a87", "a88", "a89", "a90", "a91", "a92", "a93", "a94", "a95", "a96", "a97", "a98", "a99", "a100", "a101", "a102", "a103", "a104", "a105", "a106", "a107", "a108", "a109", "a110", "a111", "a112", "a113", "a114", "a115", "a116", "a117", "a118", "a119", "a120", "a121", "a122", "a123", "a124", "a125", "a126", "a127", "a128", "a129", "a130", "a131", "a132", "a133", "a134", "a135", "a136", "a137", "a138", "a139", "a140", "a141", "a142", "a143", "a144", "a145", "a146", "a147", "a148", "a149", "a150", "a151", "a152", "a153", "a154", "a155", "a156", "a157", "a158", "a159", "a160", "a161", "a162", "a163", "a164", "a165", "a166", "a167", "a168", "a169", "a170", "a171", "a172", "a173", "a174", "a175", "a176", "a177", "a178", "a179", "a180", "a181", "a182", "a183", "a184", "a185", "a186", "a187", "a188", "a189", "a190", "a191", "a192", "a193", "a194", "a195", "a196", "a197", "a198", "a199", "a200", "a201", "a202", "a203", "a204", "a205", "a206", "a207", "a208", "a209", "a210", "a211", "a212", "a213", "a214", "a215", "a216", "a217", "a218", "a219", "a220", "a221", "a222", "a223", "a224", "a225", "a226", "a227", "a228", "a229", "a230", "a231", "a232", "a233", "a234", "a235", "a236", "a237", "a238", "a239", "a240", "a241", "a242", "a243", "a244", "a245", "a246", "a247", "a248", "a249", "a250", "a251", "a252", "a253", "a254", "a255", "s24", "s25", "s26", "s27", "s28", "s29", "s30", "s31", "s40", "s41", "s42", "s43", "s44", "s45", "s46", "s47", "s48", "s49", "s50", "s51", "s52", "s53", "s54", "s55", "s56", "s57", "s58", "s59", "s60", "s61", "s62", "s63", "s64", "s65", "s66", "s67", "s68", "s69", "s70", "s71", "s72", "s73", "s74", "s75", "s76", "s77", "s78", "s79", "s80", "s81", "s82", "s83", "s84", "s85", "s86", "s87", "s88", "s89", "s90", "s91", "s92", "s93", "s94", "s95", "s96", "s97", "s98", "s99", "vcc", "scc", "memory");
}

extern "C" void kernel_launch(void* const* d_in, const int* in_sizes, int n_in, void* d_out, int out_size, void* d_ws, size_t ws_size, hipStream_t stream) {
  typedef const unsigned short* P;
  (void)hipFuncSetAttribute((const void*)mha2_asm, hipFuncAttributeMaxDynamicSharedMemorySize, 163840);
  hipLaunchKernelGGL(mha2_asm, dim3(256), dim3(256), 163840, stream, (P)d_in[0], (P)d_in[5], (P)d_in[3], (P)d_in[8], (P)d_in[1], (P)d_in[6], (P)d_in[2], (P)d_in[7],
                     (P)d_in[4], (P)d_in[9], (char*)d_ws, (unsigned short*)d_out);
}
```

```cpp
#include <hip/hip_runtime.h>
#include <stdint.h>

__global__ __launch_bounds__(256, 1) __attribute__((aligned(4096)))
void mha2_asm(const unsigned short* __restrict__ X0, const unsigned short* __restrict__ X1, const unsigned short* __restrict__ W0, const unsigned short* __restrict__ W1,
              const unsigned short* __restrict__ K0, const unsigned short* __restrict__ K1,
              const unsigned short* __restrict__ V0, const unsigned short* __restrict__ V1,
              const unsigned short* __restrict__ WO0, const unsigned short* __restrict__ WO1, char* ws, unsigned short* out) {
  extern __shared__ __attribute__((aligned(16))) char lds[];
  const unsigned c = blockIdx.x;
  const unsigned xcd = c & 7u, idx = c >> 3;
  const unsigned hg = idx & 7u, bb = xcd * 4u + (idx >> 3);
  const unsigned bk = bb >> 4, b = bb & 15u;
  const unsigned wid = __builtin_amdgcn_readfirstlane(threadIdx.x >> 6);
  const unsigned head0 = (hg >> 2) * 16u + (hg & 3u) * 2u + (wid >> 1);
  const unsigned long long kp = (unsigned long long)(bk ? K1 : K0) + ((unsigned long long)b * 32u + head0) * 262144ull - 1024ull;
  const unsigned long long vp = (unsigned long long)(bk ? V1 : V0) + ((unsigned long long)b * 32u + head0) * 262144ull - 1024ull;
  const unsigned long long xp = (unsigned long long)(bk ? X1 : X0) + (unsigned long long)b * (224u * 1280u);
  const unsigned long long wp = (unsigned long long)(bk ? W1 : W0);
  const unsigned long long op = (unsigned long long)ws + 0ull + (unsigned long long)bb * 2295808ull;
  const unsigned ldsb = (unsigned)(size_t)lds;
  const unsigned long long wop = (unsigned long long)(bk ? WO1 : WO0) - 4096ull;
  const unsigned long long yp = (unsigned long long)out + (unsigned long long)b * (224ull * 2560ull) + bk * 1280u + (hg & 3u) * 320u;
  const unsigned long long dp = (unsigned long long)ws + (100ull << 20) + (c * 4u + wid) * 256u;
  asm volatile(
".macro Qta d,v,o\nds_read_b64_tr_b16 a[\\d:\\d+1], v\\v offset:\\o\n.endm\n.macro Qdv d,v,o\nds_read_b128 v[\\d:\\d+3], v\\v offset:\\o\n.endm\n.macro Qda d,v,o\nds_read_b128 a[\\d:\\d+3], v\\v offset:\\o\n"
".endm\n.macro Qzvav d,a,b\nv_mfma_f32_16x16x32_bf16 v[\\d:\\d+3], a[\\a:\\a+3], v[\\b:\\b+3], 0\n.endm\n.macro Qmvaa d,a,b\nv_mfma_f32_16x16x32_bf16 v[\\d:\\d+3], a[\\a:\\a+3], a[\\b:\\b+3], v[\\d:\\d+3]\n.endm\n.macro Qzaav d,a,b\n"
"v_mfma_f32_16x16x32_bf16 a[\\d:\\d+3], a[\\a:\\a+3], v[\\b:\\b+3], 0\n.endm\n.macro Qmaaa d,a,b\nv_mfma_f32_16x16x32_bf16 a[\\d:\\d+3], a[\\a:\\a+3], a[\\b:\\b+3], a[\\d:\\d+3]\n.endm\n.macro Qmvav d,a,b\nv_mfma_f32_16x16x32_bf16 v[\\d:\\d+3], a[\\a:\\a+3], v[\\b:\\b+3], v[\\d:\\d+3]\n.endm\n"
".macro Qmaav d,a,b\nv_mfma_f32_16x16x32_bf16 a[\\d:\\d+3], a[\\a:\\a+3], v[\\b:\\b+3], a[\\d:\\d+3]\n.endm\n.macro Qk d,a,b\nv_cvt_pk_bf16_f32 v\\d, v\\a, v\\b\n.endm\n.macro Qzvaa d,a,b\nv_mfma_f32_16x16x32_bf16 v[\\d:\\d+3], a[\\a:\\a+3], a[\\b:\\b+3], 0\n"
".endm\n.macro Qa d,a,b\nv_add_f32 v\\d, v\\a, v\\b\n.endm\n.macro Qe d\nv_exp_f32 v\\d, v\\d\n.endm\n.macro Qcvaa d,a,b,c\n"
"v_mfma_f32_16x16x32_bf16 v[\\d:\\d+3], a[\\a:\\a+3], a[\\b:\\b+3], v[\\c:\\c+3]\n.endm\n.macro Qtv d,v,o\nds_read_b64_tr_b16 v[\\d:\\d+1], v\\v offset:\\o\n.endm\n.macro Qzava d,a,b\nv_mfma_f32_16x16x32_bf16 a[\\d:\\d+3], v[\\a:\\a+3], a[\\b:\\b+3], 0\n.endm\n"
".macro Qmava d,a,b\nv_mfma_f32_16x16x32_bf16 a[\\d:\\d+3], v[\\a:\\a+3], a[\\b:\\b+3], a[\\d:\\d+3]\n.endm\n.macro Qmvva d,a,b\nv_mfma_f32_16x16x32_bf16 v[\\d:\\d+3], v[\\a:\\a+3], a[\\b:\\b+3], v[\\d:\\d+3]\n.endm\n.p2align 7\nv_mbcnt_lo_u32_b32 v0, -1, 0\n"
"v_mbcnt_hi_u32_b32 v0, -1, v0\nv_lshlrev_b32 v60, 4, v0\ns_add_u32 m0, %10, 160768\ns_getpc_b64 s[56:57]\n.Lpc0:\ns_and_b32 s56, s56, 0xffffff80\ns_and_b32 s57, s57, 0xffff\ns_mov_b32 s58, .Lend-.Lpc0\n"
"s_mov_b32 s59, 0x20000\ns_and_b32 s94, %20, 31\ns_lshl_b32 s94, s94, 2\ns_add_u32 s94, s94, %11\ns_lshl_b32 s94, s94, 10\nbuffer_load_dwordx4 v60, s[56:59], s94 offen lds\ns_mov_b32 s52, %6\ns_and_b32 s53, %7, 0xffff\n"
"s_mov_b32 s54, 5242880\ns_mov_b32 s55, 0x20000\ns_mov_b32 s48, %4\ns_and_b32 s49, %5, 0xffff\ns_mov_b32 s50, 286720\ns_mov_b32 s51, 0x20000\ns_mov_b32 s70, %11\ns_and_b32 s92, s70, 1\n"
"s_lshr_b32 s93, s70, 1\nv_lshrrev_b32 v2, 4, v0\ns_lshl_b32 s68, s92, 18\nv_and_b32 v4, 3, v0\nv_lshlrev_b32 v4, 4, v4\nv_and_b32 v1, 1, v2\nv_lshl_add_u32 v4, v1, 6, v4\nv_lshrrev_b32 v1, 5, v0\n"
"v_lshl_add_u32 v4, v1, 15, v4\nv_bfe_u32 v1, v0, 2, 2\nv_lshl_add_u32 v4, v1, 13, v4\nv_add_u32 v62, s68, v4\ns_lshl_b32 s87, %12, 8\ns_mul_i32 s62, s70, 4128\ns_add_u32 s62, s62, %10\ns_add_u32 s62, s62, 33024\n"
"s_add_u32 m0, s62, 0\ns_add_u32 s66, s87, 0\ns_nop 0\nbuffer_load_dwordx4 v62, s[52:55], s66 offen lds\ns_add_u32 s66, s87, 64504\ns_nop 0\nbuffer_load_dwordx4 v62, s[52:55], s66 offen offset:1032 lds\ns_add_u32 s66, s87, 129008\n"
"s_nop 0\nbuffer_load_dwordx4 v62, s[52:55], s66 offen offset:2064 lds\ns_add_u32 s66, s87, 193512\ns_nop 0\nbuffer_load_dwordx4 v62, s[52:55], s66 offen offset:3096 lds\ns_lshl_b32 s69, s92, 1\nv_lshrrev_b32 v3, 5, v0\nv_and_b32 v4, 3, v0\n"
"v_xor_b32 v4, s69, v4\nv_lshlrev_b32 v4, 4, v4\nv_lshl_add_u32 v4, v3, 6, v4\nv_bfe_u32 v1, v0, 2, 3\nv_mul_u32_u24 v64, 1280, v1\nv_add_u32 v64, v64, v4\ns_mul_i32 s86, s93, 143360\ns_mul_i32 s68, s92, 10240\n"
"s_add_u32 s86, s86, s68\ns_mul_i32 s63, s92, 14336\ns_mul_i32 s68, s93, 7168\ns_add_u32 s63, s63, s68\ns_add_u32 s63, s63, %10\ns_add_u32 s63, s63, 99072\ns_add_u32 m0, s63, 33024\ns_add_u32 s97, s86, 0\n"
"s_nop 0\nbuffer_load_dwordx4 v64, s[48:51], s97 offen lds\ns_add_u32 s97, s86, 19456\ns_nop 0\nbuffer_load_dwordx4 v64, s[48:51], s97 offen offset:1024 lds\ns_add_u32 s97, s86, 38912\ns_nop 0\nbuffer_load_dwordx4 v64, s[48:51], s97 offen offset:2048 lds\n"
"s_add_u32 s97, s86, 58368\ns_nop 0\nbuffer_load_dwordx4 v64, s[48:51], s97 offen offset:3072 lds\ns_add_u32 m0, s63, 37120\ns_add_u32 s97, s86, 81920\ns_nop 0\nbuffer_load_dwordx4 v64, s[48:51], s97 offen lds\ns_add_u32 s97, s86, 101376\n"
"s_nop 0\nbuffer_load_dwordx4 v64, s[48:51], s97 offen offset:1024 lds\ns_add_u32 s97, s86, 120832\ns_nop 0\nbuffer_load_dwordx4 v64, s[48:51], s97 offen offset:2048 lds\ns_add_u32 m0, s62, 16512\ns_add_u32 s66, s87, 128\ns_nop 0\n"
"buffer_load_dwordx4 v62, s[52:55], s66 offen lds\ns_mov_b32 s24, %8\ns_and_b32 s25, %9, 0xffff\ns_mov_b32 s26, 2295808\ns_mov_b32 s27, 0x20000\nv_and_b32 v5, 7, v0\nv_lshlrev_b32 v5, 7, v5\ns_add_u32 s66, s87, 64632\n"
"s_nop 0\nbuffer_load_dwordx4 v62, s[52:55], s66 offen offset:1032 lds\nv_add_u32 v76, 2293760, v5\nbuffer_load_dword v65, v76, s[24:27], 0 offen sc1\ns_mov_b32 s40, %0\ns_and_b32 s41, %1, 0xffff\ns_mov_b32 s42, 2360320\ns_mov_b32 s43, 0x20000\n"
"s_add_u32 s66, s87, 129136\ns_nop 0\nbuffer_load_dwordx4 v62, s[52:55], s66 offen offset:2064 lds\ns_mov_b32 s44, %2\ns_and_b32 s45, %3, 0xffff\ns_mov_b32 s46, 2360320\ns_mov_b32 s47, 0x20000\ns_mov_b32 s56, %8\n"
"s_and_b32 s57, %9, 0xffff\ns_add_u32 s66, s87, 193640\ns_nop 0\nbuffer_load_dwordx4 v62, s[52:55], s66 offen offset:3096 lds\ns_mov_b32 s58, 0\ns_mov_b32 s59, 0x20000\ns_mov_b32 s60, 0\ns_mov_b32 s90, 896\n"
"s_mov_b32 s67, 0\ns_mov_b32 s72, 0x3e0293ee\ns_mov_b64 exec, 0\ns_cmp_eq_u32 s70, 0\ns_cbranch_scc1 .Lpg0\ns_cmp_eq_u32 s70, 1\ns_cbranch_scc1 .Lpg1\ns_cmp_eq_u32 s70, 2\n"
"s_cbranch_scc1 .Lpg2\ns_branch .Lpg3\n.Ltr0:\ns_mov_b64 exec, -1\ns_lshl_b32 s68, s92, 12\ns_add_u32 s88, s68, 128\nv_lshl_add_u32 v4, v1, 8, v4\nv_add_u32 v69, s88, v4\n"
"v_and_b32 v4, 1, v0\nv_lshlrev_b32 v4, 4, v4\ns_add_u32 m0, s63, 0\ns_add_u32 s97, s86, 128\ns_nop 0\nbuffer_load_dwordx4 v64, s[48:51], s97 offen lds\nv_lshl_add_u32 v4, v2, 5, v4\nv_bfe_u32 v1, v0, 1, 3\n"
"v_lshl_add_u32 v4, v1, 8, v4\nv_add_u32 v72, s88, v4\ns_lshl_b32 s69, s93, 14\ns_add_u32 s69, s69, %10\ns_add_u32 s97, s86, 19584\ns_nop 0\nbuffer_load_dwordx4 v64, s[48:51], s97 offen offset:1024 lds\ns_add_u32 s61, s69, s68\n"
"v_bfe_u32 v3, v0, 3, 1\nv_lshlrev_b32 v4, 1, v3\nv_xor_b32 v4, v4, v2\nv_and_b32 v1, 7, v0\nv_lshl_add_u32 v4, v1, 2, v4\ns_add_u32 s97, s86, 39040\ns_nop 0\nbuffer_load_dwordx4 v64, s[48:51], s97 offen offset:2048 lds\n"
"v_lshlrev_b32 v4, 4, v4\nv_lshl_add_u32 v1, v3, 12, v4\nv_add_u32 v68, s69, v1\ns_mul_i32 s68, s92, 7168\ns_add_u32 s68, s68, %10\ns_add_u32 s68, s68, 99072\ns_add_u32 s97, s86, 58496\ns_nop 0\n"
"buffer_load_dwordx4 v64, s[48:51], s97 offen offset:3072 lds\nv_mul_u32_u24 v1, 14336, v3\nv_add3_u32 v63, v1, v4, s68\nv_lshrrev_b32 v3, 5, v0\nv_and_b32 v4, 31, v0\nv_lshlrev_b32 v4, 3, v4\nv_lshl_add_u32 v4, v3, 12, v4\ns_add_u32 m0, s63, 4096\n"
"s_add_u32 s97, s86, 82048\ns_nop 0\nbuffer_load_dwordx4 v64, s[48:51], s97 offen lds\nv_add_u32 v4, 8192, v4\nv_add_u32 v70, s69, v4\nv_add_u32 v71, 66048, v70\ns_mul_i32 s68, s93, 8256\ns_add_u32 s68, s68, %10\n"
"s_add_u32 s68, s68, 33024\ns_add_u32 s97, s86, 101504\ns_nop 0\nbuffer_load_dwordx4 v64, s[48:51], s97 offen offset:1024 lds\nv_mul_u32_u24 v3, 1032, v2\nv_and_b32 v4, 15, v0\nv_lshlrev_b32 v4, 4, v4\nv_add3_u32 v61, v3, v4, s68\n"
"s_lshl_b32 s68, s70, 2\ns_add_u32 s69, %10, 131840\ns_add_u32 s97, s86, 120960\ns_nop 0\nbuffer_load_dwordx4 v64, s[48:51], s97 offen offset:2048 lds\ns_add_u32 s68, s68, s69\nv_mov_b32 v73, s68\nv_and_b32 v3, 3, v0\n"
"v_lshl_add_u32 v74, v3, 2, s69\nv_mov_b32 v75, 0\nds_write_b32 v73, v75\ns_add_u32 m0, s62, 33024\ns_add_u32 s66, s87, 524288\ns_nop 0\nbuffer_load_dwordx4 v62, s[52:55], s66 offen lds\ns_waitcnt lgkmcnt(0)\n"
"s_mul_i32 s91, s70, 6144\ns_add_u32 s91, s91, %10\ns_mul_i32 s96, s70, 5120\ns_add_u32 s96, s96, %10\nv_and_b32 v3, 1, v0\ns_add_u32 s66, s87, 588792\ns_nop 0\nbuffer_load_dwordx4 v62, s[52:55], s66 offen offset:1032 lds\n"
"v_mul_u32_u24 v3, 0xaaaa, v3\nv_or_b32 v77, 0x4444, v3\ns_and_b32 s68, %19, 4\ns_lshl_b32 s28, 15, s68\ns_mov_b32 s29, 0\ns_and_b32 s69, %19, 3\ns_add_u32 s66, s87, 653296\ns_nop 0\n"
"buffer_load_dwordx4 v62, s[52:55], s66 offen offset:2064 lds\ns_lshl_b32 s69, s69, 2\ns_lshr_b32 s68, s68, 1\ns_add_u32 s69, s69, s68\ns_add_u32 s69, s69, s93\ns_mul_i32 s98, s69, 28672\ns_mul_i32 s68, s92, 7168\ns_add_u32 s66, s87, 717800\n"
"s_nop 0\nbuffer_load_dwordx4 v62, s[52:55], s66 offen offset:3096 lds\ns_add_u32 s98, s98, s68\ns_add_u32 s98, s98, 1835008\ns_mov_b32 s71, s98\ns_add_u32 s58, s98, 17408\ns_and_b32 s68, %19, 3\ns_lshl_b32 s99, 3072, s68\n"
"s_mov_b64 exec, 0\ns_cmp_eq_u32 s70, 0\ns_cbranch_scc1 .Lpg4\ns_cmp_eq_u32 s70, 1\ns_cbranch_scc1 .Lpg5\ns_cmp_eq_u32 s70, 2\ns_cbranch_scc1 .Lpg6\ns_branch .Lpg7\n"
".Ltr9:\ns_mov_b64 exec, -1\ns_or_b32 s99, s99, 0x302\ns_mov_b32 s94, 0x7ffff000\ns_mul_i32 s68, %20, 40960\ns_mul_i32 s69, s93, 20480\ns_add_u32 s68, s68, s69\ns_lshl_b32 s69, s92, 10\n"
"s_add_u32 m0, s62, 49536\ns_add_u32 s66, s87, 524416\ns_nop 0\nbuffer_load_dwordx4 v62, s[52:55], s66 offen lds\ns_add_u32 s68, s68, s69\ns_add_u32 s68, s68, 2048\ns_add_u32 s94, %6, s68\ns_addc_u32 s95, %7, 0\n"
"s_and_b32 s95, s95, 0xffff\ns_mov_b32 s75, 17408\ns_add_u32 s66, s87, 588920\ns_nop 0\nbuffer_load_dwordx4 v62, s[52:55], s66 offen offset:1032 lds\nv_mov_b32 v3, -1\nv_accvgpr_write_b32 a0, v3\nv_accvgpr_write_b32 a1, v3\n"
"v_accvgpr_write_b32 a2, v3\nv_accvgpr_write_b32 a3, v3\nv_accvgpr_write_b32 a16, v3\ns_add_u32 s66, s87, 653424\ns_nop 0\nbuffer_load_dwordx4 v62, s[52:55], s66 offen offset:2064 lds\nv_accvgpr_write_b32 a17, v3\nv_accvgpr_write_b32 a18, v3\n"
"v_accvgpr_write_b32 a19, v3\nv_accvgpr_write_b32 a32, v3\nv_accvgpr_write_b32 a33, v3\nv_accvgpr_write_b32 a34, v3\ns_add_u32 s66, s87, 717928\ns_nop 0\nbuffer_load_dwordx4 v62, s[52:55], s66 offen offset:3096 lds\nv_accvgpr_write_b32 a35, v3\n"
"v_accvgpr_write_b32 a48, v3\nv_accvgpr_write_b32 a49, v3\nv_accvgpr_write_b32 a50, v3\nv_accvgpr_write_b32 a51, v3\nv_accvgpr_write_b32 a64, v3\ns_mov_b64 exec, 0\ns_cmp_eq_u32 s70, 0\ns_cbranch_scc1 .Lpg8\n"
"s_cmp_eq_u32 s70, 1\ns_cbranch_scc1 .Lpg9\ns_cmp_eq_u32 s70, 2\ns_cbranch_scc1 .Lpg10\ns_branch .Lpg11\n.Ltr10:\ns_mov_b64 exec, -1\nv_accvgpr_write_b32 a65, v3\n"
"v_accvgpr_write_b32 a66, v3\nv_accvgpr_write_b32 a67, v3\nv_accvgpr_write_b32 a80, v3\nv_accvgpr_write_b32 a81, v3\nv_accvgpr_write_b32 a82, v3\nv_accvgpr_write_b32 a83, v3\nv_accvgpr_write_b32 a96, v3\nv_accvgpr_write_b32 a97, v3\n"
"v_accvgpr_write_b32 a98, v3\nv_accvgpr_write_b32 a99, v3\nv_accvgpr_write_b32 a4, v3\nv_accvgpr_write_b32 a5, v3\nv_accvgpr_write_b32 a6, v3\nv_accvgpr_write_b32 a7, v3\nv_accvgpr_write_b32 a20, v3\nv_accvgpr_write_b32 a21, v3\n"
"v_accvgpr_write_b32 a22, v3\nv_accvgpr_write_b32 a23, v3\nv_accvgpr_write_b32 a36, v3\nv_accvgpr_write_b32 a37, v3\nv_accvgpr_write_b32 a38, v3\nv_accvgpr_write_b32 a39, v3\n.Lround:\ns_cmp_lg_u32 s67, 0\n"
"s_cselect_b64 vcc, -1, 0\ns_waitcnt vmcnt(15)\ns_barrier\nQta 112,61,0\nQta 114,61,512\nQdv 24,63,33024\nQta 116,61,4128\nQta 118,61,4640\n"
"Qdv 28,63,34048\nQta 120,61,8\nQta 122,61,520\nQdv 32,63,35072\nQta 124,61,4136\nQta 126,61,4648\nQdv 36,63,36096\nQta 128,61,256\n"
"Qta 130,61,768\nQdv 40,63,37120\nQta 132,61,4384\nQta 134,61,4896\nQdv 44,63,38144\nQta 136,61,264\nQta 138,61,776\nQdv 48,63,39168\n"
"Qta 140,61,4392\nQta 142,61,4904\nQda 176,63,33536\nQda 180,63,34560\nQda 184,63,35584\nQda 188,63,36608\nQda 192,63,37632\nQda 196,63,38656\n"
"Qda 200,63,39680\ns_waitcnt lgkmcnt(6)\nQzvav 80,112,24\nQmvaa 80,116,176\ns_add_u32 s68, s71, 0\nQzvav 84,120,24\nbuffer_store_dwordx4 a[0:3], v60, s[56:59], s68 offen offset:0 sc1\nQmvaa 84,124,176\n"
"Qzvav 88,128,24\ns_add_u32 s68, s71, 14336\nQmvaa 88,132,176\nbuffer_store_dwordx4 a[4:7], v60, s[56:59], s68 offen offset:0 sc1\nQzvav 92,136,24\nQmvaa 92,140,176\ns_waitcnt lgkmcnt(0)\nQzvav 112,112,28\n"
"s_waitcnt vmcnt(17)\ns_barrier\nQmvaa 112,116,180\nQta 144,61,16512\ns_add_u32 m0, s62, 0\ns_add_u32 s69, s87, 1048576\nQzvav 116,120,28\nbuffer_load_dwordx4 v62, s[52:55], s69 offen lds\n"
"Qmvaa 116,124,180\nQta 146,61,17024\nQzvav 120,128,28\nQmvaa 120,132,180\nQta 148,61,20640\ns_add_u32 s69, s87, 1113080\nQzvav 124,136,28\nbuffer_load_dwordx4 v62, s[52:55], s69 offen offset:1032 lds\n"
"Qmvaa 124,140,180\nQta 150,61,21152\nQzvav 144,112,32\nQmvaa 144,116,184\nQta 152,61,16520\ns_add_u32 s69, s87, 1177584\nQzvav 148,120,32\nbuffer_load_dwordx4 v62, s[52:55], s69 offen offset:2064 lds\n"
"Qmvaa 148,124,184\nQta 154,61,17032\nQzvav 152,128,32\nQmvaa 152,132,184\nQta 156,61,20648\ns_add_u32 s69, s87, 1242088\nQzvav 156,136,32\nbuffer_load_dwordx4 v62, s[52:55], s69 offen offset:3096 lds\n"
"Qmvaa 156,140,184\nQta 158,61,21160\nQzvav 176,112,36\nQmvaa 176,116,188\nQta 160,61,16768\ns_add_u32 m0, s63, 33024\ns_add_u32 s88, s86, 256\nQzvav 180,120,36\n"
"buffer_load_dwordx4 v64, s[48:51], s88 offen lds\nQmvaa 180,124,188\nQta 162,61,17280\nQzvav 184,128,36\nQmvaa 184,132,188\nQta 164,61,20896\ns_add_u32 s88, s86, 19712\nQzvav 188,136,36\n"
"buffer_load_dwordx4 v64, s[48:51], s88 offen offset:1024 lds\nQmvaa 188,140,188\nQta 166,61,21408\nQzvav 208,112,40\nQmvaa 208,116,192\nQta 168,61,16776\ns_add_u32 s88, s86, 39168\nQzvav 212,120,40\n"
"buffer_load_dwordx4 v64, s[48:51], s88 offen offset:2048 lds\nQmvaa 212,124,192\nQta 170,61,17288\nQzvav 216,128,40\nQmvaa 216,132,192\nQta 172,61,20904\ns_add_u32 s88, s86, 58624\nQzvav 220,136,40\n"
"buffer_load_dwordx4 v64, s[48:51], s88 offen offset:3072 lds\nQmvaa 220,140,192\nQta 174,61,21416\nQzvav 240,112,44\nQmvaa 240,116,196\ns_add_u32 m0, s63, 37120\ns_add_u32 s88, s86, 82176\nQzvav 244,120,44\n"
"buffer_load_dwordx4 v64, s[48:51], s88 offen lds\nQmvaa 244,124,196\nQzvav 248,128,44\nQmvaa 248,132,196\ns_add_u32 s88, s86, 101632\nQzvav 252,136,44\nbuffer_load_dwordx4 v64, s[48:51], s88 offen offset:1024 lds\nQmvaa 252,140,196\n"
"Qzaav 224,112,48\nQmaaa 224,116,200\ns_add_u32 s88, s86, 121088\nQzaav 228,120,48\nbuffer_load_dwordx4 v64, s[48:51], s88 offen offset:2048 lds\nQmaaa 228,124,200\nQzaav 232,128,48\nQmaaa 232,132,200\n"
"Qzaav 236,136,48\nQmaaa 236,140,200\ns_waitcnt lgkmcnt(0)\nQzvav 96,144,24\nQmvaa 96,148,176\ns_add_u32 s68, s71, 28672\nQzvav 100,152,24\nbuffer_store_dwordx4 a[8:11], v60, s[56:59], s68 offen offset:0 sc1\n"
"Qmvaa 100,156,176\nQzvav 104,160,24\ns_add_u32 s68, s71, 43008\nQmvaa 104,164,176\nbuffer_store_dwordx4 a[12:15], v60, s[56:59], s68 offen offset:0 sc1\nQzvav 108,168,24\nQmvaa 108,172,176\ns_waitcnt lgkmcnt(0)\n"
"Qzvav 128,144,28\ns_waitcnt vmcnt(15)\ns_barrier\nQmvaa 128,148,180\nQdv 24,63,0\ns_add_u32 m0, s62, 16512\ns_add_u32 s69, s87, 1048704\nQzvav 132,152,28\n"
"buffer_load_dwordx4 v62, s[52:55], s69 offen lds\nQmvaa 132,156,180\nQda 176,63,512\nQzvav 136,160,28\nQta 112,61,33024\nQmvaa 136,164,180\ns_add_u32 s69, s87, 1113208\nQzvav 140,168,28\n"
"buffer_load_dwordx4 v62, s[52:55], s69 offen offset:1032 lds\nQmvaa 140,172,180\nQdv 28,63,1024\nQzvav 160,144,32\nQda 180,63,1536\nQmvaa 160,148,184\nQta 114,61,33536\ns_add_u32 s69, s87, 1177712\n"
"Qzvav 164,152,32\nbuffer_load_dwordx4 v62, s[52:55], s69 offen offset:2064 lds\nQmvaa 164,156,184\nQta 116,61,37152\nQzvav 168,160,32\nQmvaa 168,164,184\nQta 118,61,37664\ns_add_u32 s69, s87, 1242216\n"
"Qzvav 172,168,32\nbuffer_load_dwordx4 v62, s[52:55], s69 offen offset:3096 lds\nQmvaa 172,172,184\nQdv 32,63,2048\nQzvav 192,144,36\nQda 184,63,2560\nQmvaa 192,148,188\nQta 120,61,33032\n"
"s_add_u32 m0, s61, 0\nQzvav 196,152,36\ns_cbranch_vccnz .Ld0q0\nbuffer_load_dwordx4 v69, s[40:43], s90 offen nt lds\n.Ld0q0:\nQmvaa 196,156,188\nQta 122,61,33544\nQzvav 200,160,36\n"
"Qmvaa 200,164,188\nQta 124,61,37160\nQzvav 204,168,36\nQdv 36,63,3072\nQmvaa 204,172,188\nQda 188,63,3584\nQzvav 224,144,40\nQta 126,61,37672\n"
"Qmvaa 224,148,192\nQzvav 228,152,40\nQta 128,61,33280\nQmvaa 228,156,192\nQzvav 232,160,40\nQta 130,61,33792\n.Lpg0:\ns_cbranch_execz .Ltr0\n"
"Qmvaa 232,164,192\nQzvav 236,168,40\nQdv 40,63,4096\nQmvaa 236,172,192\nQda 192,63,4608\nQzaav 208,144,44\nQta 132,61,37408\nQmaaa 208,148,196\n"
"Qzaav 212,152,44\nQta 134,61,37920\nQmaaa 212,156,196\nQzaav 216,160,44\nQta 136,61,33288\nQmaaa 216,164,196\nQzaav 220,168,44\nQdv 44,63,5120\n"
"Qmaaa 220,172,196\nQda 196,63,5632\nQzaav 240,144,48\nQta 138,61,33800\nQmaaa 240,148,200\nQzaav 244,152,48\nQta 140,61,37416\nQmaaa 244,156,200\n"
"Qzaav 248,160,48\nQta 142,61,37928\nQmaaa 248,164,200\nQzaav 252,168,48\nQdv 48,63,6144\nQmaaa 252,172,200\nQda 200,63,6656\ns_waitcnt lgkmcnt(2)\n"
"Qmvav 80,112,24\nQmvaa 80,116,176\ns_add_u32 s68, s71, 0\nQmvav 84,120,24\nbuffer_store_dwordx4 a[16:19], v60, s[56:59], s68 offen offset:1024 sc1\nQmvaa 84,124,176\nQmvav 88,128,24\ns_add_u32 s68, s71, 14336\n"
"Qmvaa 88,132,176\nbuffer_store_dwordx4 a[20:23], v60, s[56:59], s68 offen offset:1024 sc1\nQmvav 92,136,24\nQmvaa 92,140,176\ns_waitcnt lgkmcnt(0)\nQmvav 112,112,28\ns_waitcnt vmcnt(22)\ns_barrier\n"
"Qmvaa 112,116,180\nQta 144,61,49536\ns_add_u32 m0, s62, 33024\ns_add_u32 s69, s87, 1572864\nQmvav 116,120,28\nbuffer_load_dwordx4 v62, s[52:55], s69 offen lds\nQmvaa 116,124,180\nQta 146,61,50048\n"
"Qmvav 120,128,28\nQmvaa 120,132,180\nQta 148,61,53664\ns_add_u32 s69, s87, 1637368\nQmvav 124,136,28\nbuffer_load_dwordx4 v62, s[52:55], s69 offen offset:1032 lds\nQmvaa 124,140,180\nQta 150,61,54176\n"
"Qmvav 144,112,32\nQmvaa 144,116,184\nQta 152,61,49544\ns_add_u32 s69, s87, 1701872\nQmvav 148,120,32\nbuffer_load_dwordx4 v62, s[52:55], s69 offen offset:2064 lds\nQmvaa 148,124,184\nQta 154,61,50056\n"
"Qmvav 152,128,32\nQmvaa 152,132,184\nQta 156,61,53672\ns_add_u32 s69, s87, 1766376\nQmvav 156,136,32\nbuffer_load_dwordx4 v62, s[52:55], s69 offen offset:3096 lds\nQmvaa 156,140,184\nQta 158,61,54184\n"
"Qmvav 176,112,36\nQmvaa 176,116,188\nQta 160,61,49792\ns_add_u32 m0, s63, 0\ns_add_u32 s88, s86, 384\nQmvav 180,120,36\nbuffer_load_dwordx4 v64, s[48:51], s88 offen lds\nQmvaa 180,124,188\n"
"Qta 162,61,50304\nQmvav 184,128,36\nQmvaa 184,132,188\nQta 164,61,53920\ns_add_u32 s88, s86, 19840\nQmvav 188,136,36\nbuffer_load_dwordx4 v64, s[48:51], s88 offen offset:1024 lds\nQmvaa 188,140,188\n"
"Qta 166,61,54432\nQmvav 208,112,40\nQmvaa 208,116,192\nQta 168,61,49800\ns_add_u32 s88, s86, 39296\nQmvav 212,120,40\nbuffer_load_dwordx4 v64, s[48:51], s88 offen offset:2048 lds\nQmvaa 212,124,192\n"
"Qta 170,61,50312\nQmvav 216,128,40\nQmvaa 216,132,192\nQta 172,61,53928\ns_add_u32 s88, s86, 58752\nQmvav 220,136,40\nbuffer_load_dwordx4 v64, s[48:51], s88 offen offset:3072 lds\nQmvaa 220,140,192\n"
"Qta 174,61,54440\nQmvav 240,112,44\nQmvaa 240,116,196\ns_add_u32 m0, s63, 4096\ns_add_u32 s88, s86, 82304\nQmvav 244,120,44\nbuffer_load_dwordx4 v64, s[48:51], s88 offen lds\nQmvaa 244,124,196\n"
"Qmvav 248,128,44\nQmvaa 248,132,196\ns_add_u32 s88, s86, 101760\nQmvav 252,136,44\nbuffer_load_dwordx4 v64, s[48:51], s88 offen offset:1024 lds\nQmvaa 252,140,196\nQmaav 224,112,48\nQmaaa 224,116,200\n"
"s_add_u32 s88, s86, 121216\nQmaav 228,120,48\nbuffer_load_dwordx4 v64, s[48:51], s88 offen offset:2048 lds\nQmaaa 228,124,200\nQmaav 232,128,48\nQmaaa 232,132,200\nQmaav 236,136,48\nQmaaa 236,140,200\n"
"s_waitcnt lgkmcnt(0)\nQmvav 96,144,24\nQmvaa 96,148,176\ns_add_u32 s68, s71, 28672\nQmvav 100,152,24\nbuffer_store_dwordx4 a[24:27], v60, s[56:59], s68 offen offset:1024 sc1\nQmvaa 100,156,176\nQmvav 104,160,24\n"
"s_add_u32 s68, s71, 43008\nQmvaa 104,164,176\nbuffer_store_dwordx4 a[28:31], v60, s[56:59], s68 offen offset:1024 sc1\nQmvav 108,168,24\nQmvaa 108,172,176\ns_waitcnt lgkmcnt(0)\nQmvav 128,144,28\ns_waitcnt vmcnt(21)\n"
"s_barrier\nQmvaa 128,148,180\nQdv 24,63,33024\ns_add_u32 m0, s62, 49536\ns_add_u32 s69, s87, 1572992\nQmvav 132,152,28\nbuffer_load_dwordx4 v62, s[52:55], s69 offen lds\nQmvaa 132,156,180\n"
"Qda 176,63,33536\nQmvav 136,160,28\nQta 112,61,0\nQmvaa 136,164,180\ns_add_u32 s69, s87, 1637496\nQmvav 140,168,28\nbuffer_load_dwordx4 v62, s[52:55], s69 offen offset:1032 lds\nQmvaa 140,172,180\n"
"Qdv 28,63,34048\nQmvav 160,144,32\nQda 180,63,34560\nQmvaa 160,148,184\nQta 114,61,512\ns_add_u32 s69, s87, 1702000\nQmvav 164,152,32\nbuffer_load_dwordx4 v62, s[52:55], s69 offen offset:2064 lds\n"
"Qmvaa 164,156,184\nQta 116,61,4128\nQmvav 168,160,32\nQmvaa 168,164,184\nQta 118,61,4640\ns_add_u32 s69, s87, 1766504\nQmvav 172,168,32\nbuffer_load_dwordx4 v62, s[52:55], s69 offen offset:3096 lds\n"
"Qmvaa 172,172,184\nQdv 32,63,35072\nQmvav 192,144,36\nQda 184,63,35584\nQmvaa 192,148,188\nQta 120,61,8\ns_add_u32 m0, s61, 0\nQmvav 196,152,36\n"
"s_cbranch_vccnz .Ld0q1\nbuffer_load_dwordx4 v69, s[40:43], s60 offen offset:1024 nt lds\n.Ld0q1:\nQmvaa 196,156,188\nQta 122,61,520\nQmvav 200,160,36\nQmvaa 200,164,188\nQta 124,61,4136\n"
"Qmvav 204,168,36\nQdv 36,63,36096\nQmvaa 204,172,188\nQda 188,63,36608\nQmvav 224,144,40\nQta 126,61,4648\nQmvaa 224,148,192\nQmvav 228,152,40\n"
"Qta 128,61,256\nQmvaa 228,156,192\nQmvav 232,160,40\nQta 130,61,768\nQmvaa 232,164,192\nQmvav 236,168,40\nQdv 40,63,37120\nQmvaa 236,172,192\n"
"Qda 192,63,37632\nQmaav 208,144,44\nQta 132,61,4384\nQmaaa 208,148,196\nQmaav 212,152,44\nQta 134,61,4896\nQmaaa 212,156,196\nQmaav 216,160,44\n"
"Qta 136,61,264\nQmaaa 216,164,196\nQmaav 220,168,44\nQdv 44,63,38144\nQmaaa 220,172,196\nQda 196,63,38656\nQmaav 240,144,48\nQta 138,61,776\n"
"Qmaaa 240,148,200\nQmaav 244,152,48\nQta 140,61,4392\nQmaaa 244,156,200\nQmaav 248,160,48\nQta 142,61,4904\nQmaaa 248,164,200\nQmaav 252,168,48\n"
"Qdv 48,63,39168\nQmaaa 252,172,200\nQda 200,63,39680\ns_waitcnt lgkmcnt(2)\nQmvav 80,112,24\nQmvaa 80,116,176\ns_add_u32 s68, s71, 0\nQmvav 84,120,24\n"
"buffer_store_dwordx4 a[32:35], v60, s[56:59], s68 offen offset:2048 sc1\nQmvaa 84,124,176\nQmvav 88,128,24\ns_add_u32 s68, s71, 14336\nQmvaa 88,132,176\nbuffer_store_dwordx4 a[36:39], v60, s[56:59], s68 offen offset:2048 sc1\nQmvav 92,136,24\nQmvaa 92,140,176\n"
"s_waitcnt lgkmcnt(0)\nQmvav 112,112,28\ns_waitcnt vmcnt(21)\ns_barrier\nQmvaa 112,116,180\nQta 144,61,16512\ns_add_u32 m0, s62, 0\ns_add_u32 s69, s87, 2097152\n"
"Qmvav 116,120,28\nbuffer_load_dwordx4 v62, s[52:55], s69 offen lds\nQmvaa 116,124,180\nQta 146,61,17024\nQmvav 120,128,28\nQmvaa 120,132,180\nQta 148,61,20640\ns_add_u32 s69, s87, 2161656\n"
"Qmvav 124,136,28\nbuffer_load_dwordx4 v62, s[52:55], s69 offen offset:1032 lds\nQmvaa 124,140,180\nQta 150,61,21152\nQmvav 144,112,32\nQmvaa 144,116,184\nQta 152,61,16520\ns_add_u32 s69, s87, 2226160\n"
"Qmvav 148,120,32\nbuffer_load_dwordx4 v62, s[52:55], s69 offen offset:2064 lds\nQmvaa 148,124,184\nQta 154,61,17032\nQmvav 152,128,32\nQmvaa 152,132,184\nQta 156,61,20648\ns_add_u32 s69, s87, 2290664\n"
"Qmvav 156,136,32\nbuffer_load_dwordx4 v62, s[52:55], s69 offen offset:3096 lds\nQmvaa 156,140,184\nQta 158,61,21160\nQmvav 176,112,36\nQmvaa 176,116,188\nQta 160,61,16768\ns_add_u32 m0, s63, 33024\n"
"s_add_u32 s88, s86, 512\nQmvav 180,120,36\nbuffer_load_dwordx4 v64, s[48:51], s88 offen lds\nQmvaa 180,124,188\nQta 162,61,17280\nQmvav 184,128,36\nQmvaa 184,132,188\nQta 164,61,20896\n"
"s_add_u32 s88, s86, 19968\nQmvav 188,136,36\nbuffer_load_dwordx4 v64, s[48:51], s88 offen offset:1024 lds\nQmvaa 188,140,188\nQta 166,61,21408\nQmvav 208,112,40\nQmvaa 208,116,192\nQta 168,61,16776\n"
"s_add_u32 s88, s86, 39424\nQmvav 212,120,40\nbuffer_load_dwordx4 v64, s[48:51], s88 offen offset:2048 lds\nQmvaa 212,124,192\nQta 170,61,17288\nQmvav 216,128,40\nQmvaa 216,132,192\nQta 172,61,20904\n"
"s_add_u32 s88, s86, 58880\nQmvav 220,136,40\nbuffer_load_dwordx4 v64, s[48:51], s88 offen offset:3072 lds\nQmvaa 220,140,192\nQta 174,61,21416\nQmvav 240,112,44\nQmvaa 240,116,196\ns_add_u32 m0, s63, 37120\n"
"s_add_u32 s88, s86, 82432\nQmvav 244,120,44\nbuffer_load_dwordx4 v64, s[48:51], s88 offen lds\nQmvaa 244,124,196\nQmvav 248,128,44\nQmvaa 248,132,196\ns_add_u32 s88, s86, 101888\nQmvav 252,136,44\n"
"buffer_load_dwordx4 v64, s[48:51], s88 offen offset:1024 lds\nQmvaa 252,140,196\nQmaav 224,112,48\nQmaaa 224,116,200\ns_add_u32 s88, s86, 121344\nQmaav 228,120,48\nbuffer_load_dwordx4 v64, s[48:51], s88 offen offset:2048 lds\nQmaaa 228,124,200\n"
"Qmaav 232,128,48\nQmaaa 232,132,200\nQmaav 236,136,48\nQmaaa 236,140,200\ns_waitcnt lgkmcnt(0)\nQmvav 96,144,24\nQmvaa 96,148,176\ns_add_u32 s68, s71, 28672\n"
"Qmvav 100,152,24\nbuffer_store_dwordx4 a[40:43], v60, s[56:59], s68 offen offset:2048 sc1\nQmvaa 100,156,176\nQmvav 104,160,24\ns_add_u32 s68, s71, 43008\nQmvaa 104,164,176\nbuffer_store_dwordx4 a[44:47], v60, s[56:59], s68 offen offset:2048 sc1\nQmvav 108,168,24\n"
"Qmvaa 108,172,176\ns_waitcnt lgkmcnt(0)\nQmvav 128,144,28\ns_waitcnt vmcnt(21)\ns_barrier\nQmvaa 128,148,180\nQdv 24,63,0\ns_add_u32 m0, s62, 16512\n"
"s_add_u32 s69, s87, 2097280\nQmvav 132,152,28\nbuffer_load_dwordx4 v62, s[52:55], s69 offen lds\nQmvaa 132,156,180\nQda 176,63,512\nQmvav 136,160,28\nQta 112,61,33024\nQmvaa 136,164,180\n"
"s_add_u32 s69, s87, 2161784\nQmvav 140,168,28\nbuffer_load_dwordx4 v62, s[52:55], s69 offen offset:1032 lds\nQmvaa 140,172,180\nQdv 28,63,1024\nQmvav 160,144,32\nQda 180,63,1536\nQmvaa 160,148,184\n"
"Qta 114,61,33536\ns_add_u32 s69, s87, 2226288\nQmvav 164,152,32\nbuffer_load_dwordx4 v62, s[52:55], s69 offen offset:2064 lds\nQmvaa 164,156,184\nQta 116,61,37152\nQmvav 168,160,32\nQmvaa 168,164,184\n"
"Qta 118,61,37664\ns_add_u32 s69, s87, 2290792\nQmvav 172,168,32\nbuffer_load_dwordx4 v62, s[52:55], s69 offen offset:3096 lds\nQmvaa 172,172,184\nQdv 32,63,2048\nQmvav 192,144,36\nQda 184,63,2560\n"
"Qmvaa 192,148,188\nQta 120,61,33032\ns_add_u32 m0, s61, 0\nQmvav 196,152,36\ns_cbranch_vccnz .Ld0q2\nbuffer_load_dwordx4 v69, s[40:43], s90 offen offset:2048 nt lds\n.Ld0q2:\nQmvaa 196,156,188\n"
"Qta 122,61,33544\nQmvav 200,160,36\nQmvaa 200,164,188\nQta 124,61,37160\nQmvav 204,168,36\nQdv 36,63,3072\nQmvaa 204,172,188\nQda 188,63,3584\n"
"Qmvav 224,144,40\nQta 126,61,37672\nQmvaa 224,148,192\nQmvav 228,152,40\nQta 128,61,33280\nQmvaa 228,156,192\nQmvav 232,160,40\nQta 130,61,33792\n"
"Qmvaa 232,164,192\nQmvav 236,168,40\nQdv 40,63,4096\nQmvaa 236,172,192\nQda 192,63,4608\nQmaav 208,144,44\nQta 132,61,37408\nQmaaa 208,148,196\n"
"Qmaav 212,152,44\nQta 134,61,37920\nQmaaa 212,156,196\nQmaav 216,160,44\nQta 136,61,33288\nQmaaa 216,164,196\nQmaav 220,168,44\nQdv 44,63,5120\n"
"Qmaaa 220,172,196\nQda 196,63,5632\nQmaav 240,144,48\nQta 138,61,33800\nQmaaa 240,148,200\nQmaav 244,152,48\nQta 140,61,37416\nQmaaa 244,156,200\n"
"Qmaav 248,160,48\nQta 142,61,37928\nQmaaa 248,164,200\nQmaav 252,168,48\nQdv 48,63,6144\nQmaaa 252,172,200\nQda 200,63,6656\ns_waitcnt lgkmcnt(2)\n"
"Qmvav 80,112,24\nQmvaa 80,116,176\ns_add_u32 s68, s71, 0\nQmvav 84,120,24\nbuffer_store_dwordx4 a[48:51], v60, s[56:59], s68 offen offset:3072 sc1\nQmvaa 84,124,176\nQmvav 88,128,24\ns_add_u32 s68, s71, 14336\n"
"Qmvaa 88,132,176\nbuffer_store_dwordx4 a[52:55], v60, s[56:59], s68 offen offset:3072 sc1\nQmvav 92,136,24\nQmvaa 92,140,176\ns_waitcnt lgkmcnt(0)\nQmvav 112,112,28\ns_waitcnt vmcnt(21)\ns_barrier\n"
"Qmvaa 112,116,180\nQta 144,61,49536\ns_add_u32 m0, s62, 33024\ns_add_u32 s69, s87, 2621440\nQmvav 116,120,28\nbuffer_load_dwordx4 v62, s[52:55], s69 offen lds\nQmvaa 116,124,180\nQta 146,61,50048\n"
"Qmvav 120,128,28\nQmvaa 120,132,180\nQta 148,61,53664\ns_add_u32 s69, s87, 2685944\nQmvav 124,136,28\nbuffer_load_dwordx4 v62, s[52:55], s69 offen offset:1032 lds\nQmvaa 124,140,180\nQta 150,61,54176\n"
"Qmvav 144,112,32\nQmvaa 144,116,184\nQta 152,61,49544\ns_add_u32 s69, s87, 2750448\nQmvav 148,120,32\nbuffer_load_dwordx4 v62, s[52:55], s69 offen offset:2064 lds\nQmvaa 148,124,184\nQta 154,61,50056\n"
"Qmvav 152,128,32\nQmvaa 152,132,184\nQta 156,61,53672\ns_add_u32 s69, s87, 2814952\nQmvav 156,136,32\nbuffer_load_dwordx4 v62, s[52:55], s69 offen offset:3096 lds\nQmvaa 156,140,184\nQta 158,61,54184\n"
"Qmvav 176,112,36\nQmvaa 176,116,188\nQta 160,61,49792\ns_add_u32 m0, s63, 0\ns_add_u32 s88, s86, 640\nQmvav 180,120,36\nbuffer_load_dwordx4 v64, s[48:51], s88 offen lds\nQmvaa 180,124,188\n"
"Qta 162,61,50304\nQmvav 184,128,36\nQmvaa 184,132,188\nQta 164,61,53920\ns_add_u32 s88, s86, 20096\nQmvav 188,136,36\nbuffer_load_dwordx4 v64, s[48:51], s88 offen offset:1024 lds\nQmvaa 188,140,188\n"
"Qta 166,61,54432\nQmvav 208,112,40\nQmvaa 208,116,192\nQta 168,61,49800\ns_add_u32 s88, s86, 39552\nQmvav 212,120,40\nbuffer_load_dwordx4 v64, s[48:51], s88 offen offset:2048 lds\nQmvaa 212,124,192\n"
"Qta 170,61,50312\n.Lpg1:\ns_cbranch_execz .Ltr0\nQmvav 216,128,40\nQmvaa 216,132,192\nQta 172,61,53928\ns_add_u32 s88, s86, 59008\nQmvav 220,136,40\n"
"buffer_load_dwordx4 v64, s[48:51], s88 offen offset:3072 lds\nQmvaa 220,140,192\nQta 174,61,54440\nQmvav 240,112,44\nQmvaa 240,116,196\ns_add_u32 m0, s63, 4096\ns_add_u32 s88, s86, 82560\nQmvav 244,120,44\n"
"buffer_load_dwordx4 v64, s[48:51], s88 offen lds\nQmvaa 244,124,196\nQmvav 248,128,44\nQmvaa 248,132,196\ns_add_u32 s88, s86, 102016\nQmvav 252,136,44\nbuffer_load_dwordx4 v64, s[48:51], s88 offen offset:1024 lds\nQmvaa 252,140,196\n"
"Qmaav 224,112,48\nQmaaa 224,116,200\ns_add_u32 s88, s86, 121472\nQmaav 228,120,48\nbuffer_load_dwordx4 v64, s[48:51], s88 offen offset:2048 lds\nQmaaa 228,124,200\nQmaav 232,128,48\nQmaaa 232,132,200\n"
"Qmaav 236,136,48\nQmaaa 236,140,200\ns_waitcnt lgkmcnt(0)\nQmvav 96,144,24\nQmvaa 96,148,176\ns_add_u32 s68, s71, 28672\nQmvav 100,152,24\nbuffer_store_dwordx4 a[56:59], v60, s[56:59], s68 offen offset:3072 sc1\n"
"Qmvaa 100,156,176\nQmvav 104,160,24\ns_add_u32 s68, s71, 43008\nQmvaa 104,164,176\nbuffer_store_dwordx4 a[60:63], v60, s[56:59], s68 offen offset:3072 sc1\nQmvav 108,168,24\nQmvaa 108,172,176\ns_waitcnt lgkmcnt(0)\n"
"Qmvav 128,144,28\ns_waitcnt vmcnt(21)\ns_barrier\nQmvaa 128,148,180\nQdv 24,63,33024\ns_add_u32 m0, s62, 49536\ns_add_u32 s69, s87, 2621568\nQmvav 132,152,28\n"
"buffer_load_dwordx4 v62, s[52:55], s69 offen lds\nQmvaa 132,156,180\nQda 176,63,33536\nQmvav 136,160,28\nQta 112,61,0\nQmvaa 136,164,180\ns_add_u32 s69, s87, 2686072\nQmvav 140,168,28\n"
"buffer_load_dwordx4 v62, s[52:55], s69 offen offset:1032 lds\nQmvaa 140,172,180\nQdv 28,63,34048\nQmvav 160,144,32\nQda 180,63,34560\nQmvaa 160,148,184\nQta 114,61,512\ns_add_u32 s69, s87, 2750576\n"
"Qmvav 164,152,32\nbuffer_load_dwordx4 v62, s[52:55], s69 offen offset:2064 lds\nQmvaa 164,156,184\nQta 116,61,4128\nQmvav 168,160,32\nQmvaa 168,164,184\nQta 118,61,4640\ns_add_u32 s69, s87, 2815080\n"
"Qmvav 172,168,32\nbuffer_load_dwordx4 v62, s[52:55], s69 offen offset:3096 lds\nQmvaa 172,172,184\nQdv 32,63,35072\nQmvav 192,144,36\nQda 184,63,35584\nQmvaa 192,148,188\nQta 120,61,8\n"
"s_add_u32 m0, s61, 0\nQmvav 196,152,36\ns_cbranch_vccnz .Ld0q3\nbuffer_load_dwordx4 v69, s[40:43], s60 offen offset:3072 nt lds\n.Ld0q3:\nQmvaa 196,156,188\nQta 122,61,520\nQmvav 200,160,36\n"
"Qmvaa 200,164,188\nQta 124,61,4136\nQmvav 204,168,36\nQdv 36,63,36096\nQmvaa 204,172,188\nQda 188,63,36608\nQmvav 224,144,40\nQta 126,61,4648\n"
"Qmvaa 224,148,192\nQmvav 228,152,40\nQta 128,61,256\nQmvaa 228,156,192\nQmvav 232,160,40\nQta 130,61,768\nQmvaa 232,164,192\nQmvav 236,168,40\n"
"Qdv 40,63,37120\nQmvaa 236,172,192\nQda 192,63,37632\nQmaav 208,144,44\nQta 132,61,4384\nQmaaa 208,148,196\nQmaav 212,152,44\nQta 134,61,4896\n"
"Qmaaa 212,156,196\nQmaav 216,160,44\nQta 136,61,264\nQmaaa 216,164,196\nQmaav 220,168,44\nQdv 44,63,38144\nQmaaa 220,172,196\nQda 196,63,38656\n"
"Qmaav 240,144,48\nQta 138,61,776\nQmaaa 240,148,200\nQmaav 244,152,48\nQta 140,61,4392\nQmaaa 244,156,200\nQmaav 248,160,48\nQta 142,61,4904\n"
"Qmaaa 248,164,200\nQmaav 252,168,48\nQdv 48,63,39168\nQmaaa 252,172,200\nQda 200,63,39680\ns_waitcnt lgkmcnt(2)\nQmvav 80,112,24\nQmvaa 80,116,176\n"
"s_add_u32 s68, s71, 4096\nQmvav 84,120,24\nbuffer_store_dwordx4 a[64:67], v60, s[56:59], s68 offen offset:0 sc1\nQmvaa 84,124,176\nQmvav 88,128,24\ns_add_u32 s68, s71, 18432\nQmvaa 88,132,176\nbuffer_store_dwordx4 a[68:71], v60, s[56:59], s68 offen offset:0 sc1\n"
"Qmvav 92,136,24\nQmvaa 92,140,176\ns_waitcnt lgkmcnt(0)\nQmvav 112,112,28\ns_waitcnt vmcnt(21)\ns_barrier\nQmvaa 112,116,180\nQta 144,61,16512\n"
"s_add_u32 m0, s62, 0\ns_add_u32 s69, s87, 3145728\nQmvav 116,120,28\nbuffer_load_dwordx4 v62, s[52:55], s69 offen lds\nQmvaa 116,124,180\nQta 146,61,17024\nQmvav 120,128,28\nQmvaa 120,132,180\n"
"Qta 148,61,20640\ns_add_u32 s69, s87, 3210232\nQmvav 124,136,28\nbuffer_load_dwordx4 v62, s[52:55], s69 offen offset:1032 lds\nQmvaa 124,140,180\nQta 150,61,21152\nQmvav 144,112,32\nQmvaa 144,116,184\n"
"Qta 152,61,16520\ns_add_u32 s69, s87, 3274736\nQmvav 148,120,32\nbuffer_load_dwordx4 v62, s[52:55], s69 offen offset:2064 lds\nQmvaa 148,124,184\nQta 154,61,17032\nQmvav 152,128,32\nQmvaa 152,132,184\n"
"Qta 156,61,20648\ns_add_u32 s69, s87, 3339240\nQmvav 156,136,32\nbuffer_load_dwordx4 v62, s[52:55], s69 offen offset:3096 lds\nQmvaa 156,140,184\nQta 158,61,21160\nQmvav 176,112,36\nQmvaa 176,116,188\n"
"Qta 160,61,16768\ns_add_u32 m0, s63, 33024\ns_add_u32 s88, s86, 768\nQmvav 180,120,36\nbuffer_load_dwordx4 v64, s[48:51], s88 offen lds\nQmvaa 180,124,188\nQta 162,61,17280\nQmvav 184,128,36\n"
"Qmvaa 184,132,188\nQta 164,61,20896\ns_add_u32 s88, s86, 20224\nQmvav 188,136,36\nbuffer_load_dwordx4 v64, s[48:51], s88 offen offset:1024 lds\nQmvaa 188,140,188\nQta 166,61,21408\nQmvav 208,112,40\n"
"Qmvaa 208,116,192\nQta 168,61,16776\ns_add_u32 s88, s86, 39680\nQmvav 212,120,40\nbuffer_load_dwordx4 v64, s[48:51], s88 offen offset:2048 lds\nQmvaa 212,124,192\nQta 170,61,17288\nQmvav 216,128,40\n"
"Qmvaa 216,132,192\nQta 172,61,20904\ns_add_u32 s88, s86, 59136\nQmvav 220,136,40\nbuffer_load_dwordx4 v64, s[48:51], s88 offen offset:3072 lds\nQmvaa 220,140,192\nQta 174,61,21416\nQmvav 240,112,44\n"
"Qmvaa 240,116,196\ns_add_u32 m0, s63, 37120\ns_add_u32 s88, s86, 82688\nQmvav 244,120,44\nbuffer_load_dwordx4 v64, s[48:51], s88 offen lds\nQmvaa 244,124,196\nQmvav 248,128,44\nQmvaa 248,132,196\n"
"s_add_u32 s88, s86, 102144\nQmvav 252,136,44\nbuffer_load_dwordx4 v64, s[48:51], s88 offen offset:1024 lds\nQmvaa 252,140,196\nQmaav 224,112,48\nQmaaa 224,116,200\ns_add_u32 s88, s86, 121600\nQmaav 228,120,48\n"
"buffer_load_dwordx4 v64, s[48:51], s88 offen offset:2048 lds\nQmaaa 228,124,200\nQmaav 232,128,48\nQmaaa 232,132,200\nQmaav 236,136,48\nQmaaa 236,140,200\ns_waitcnt lgkmcnt(0)\nQmvav 96,144,24\n"
"Qmvaa 96,148,176\ns_add_u32 s68, s71, 32768\nQmvav 100,152,24\nbuffer_store_dwordx4 a[72:75], v60, s[56:59], s68 offen offset:0 sc1\nQmvaa 100,156,176\nQmvav 104,160,24\ns_add_u32 s68, s71, 47104\nQmvaa 104,164,176\n"
"buffer_store_dwordx4 a[76:79], v60, s[56:59], s68 offen offset:0 sc1\nQmvav 108,168,24\nQmvaa 108,172,176\ns_waitcnt lgkmcnt(0)\nQmvav 128,144,28\ns_waitcnt vmcnt(21)\ns_barrier\nQmvaa 128,148,180\n"
"Qdv 24,63,0\ns_add_u32 m0, s62, 16512\ns_add_u32 s69, s87, 3145856\nQmvav 132,152,28\nbuffer_load_dwordx4 v62, s[52:55], s69 offen lds\nQmvaa 132,156,180\nQda 176,63,512\nQmvav 136,160,28\n"
"Qta 112,61,33024\nQmvaa 136,164,180\ns_add_u32 s69, s87, 3210360\nQmvav 140,168,28\nbuffer_load_dwordx4 v62, s[52:55], s69 offen offset:1032 lds\nQmvaa 140,172,180\nQdv 28,63,1024\nQmvav 160,144,32\n"
"Qda 180,63,1536\nQmvaa 160,148,184\nQta 114,61,33536\ns_add_u32 s69, s87, 3274864\nQmvav 164,152,32\nbuffer_load_dwordx4 v62, s[52:55], s69 offen offset:2064 lds\nQmvaa 164,156,184\nQta 116,61,37152\n"
"Qmvav 168,160,32\nQmvaa 168,164,184\nQta 118,61,37664\ns_add_u32 s69, s87, 3339368\nQmvav 172,168,32\nbuffer_load_dwordx4 v62, s[52:55], s69 offen offset:3096 lds\nQmvaa 172,172,184\nQdv 32,63,2048\n"
"Qmvav 192,144,36\nQda 184,63,2560\nQmvaa 192,148,188\nQta 120,61,33032\ns_add_u32 m0, s61, 8192\nQmvav 196,152,36\ns_cbranch_vccnz .Ld0q4\nbuffer_load_dwordx4 v72, s[44:47], s90 offen nt lds\n"
".Ld0q4:\nQmvaa 196,156,188\nQta 122,61,33544\nQmvav 200,160,36\nQmvaa 200,164,188\nQta 124,61,37160\nQmvav 204,168,36\nQdv 36,63,3072\n"
"Qmvaa 204,172,188\nQda 188,63,3584\nQmvav 224,144,40\nQta 126,61,37672\nQmvaa 224,148,192\nQmvav 228,152,40\nQta 128,61,33280\nQmvaa 228,156,192\n"
"Qmvav 232,160,40\nQta 130,61,33792\nQmvaa 232,164,192\nQmvav 236,168,40\nQdv 40,63,4096\nQmvaa 236,172,192\nQda 192,63,4608\nQmaav 208,144,44\n"
"Qta 132,61,37408\nQmaaa 208,148,196\nQmaav 212,152,44\nQta 134,61,37920\nQmaaa 212,156,196\nQmaav 216,160,44\nQta 136,61,33288\nQmaaa 216,164,196\n"
"Qmaav 220,168,44\nQdv 44,63,5120\nQmaaa 220,172,196\nQda 196,63,5632\nQmaav 240,144,48\nQta 138,61,33800\nQmaaa 240,148,200\nQmaav 244,152,48\n"
"Qta 140,61,37416\nQmaaa 244,156,200\nQmaav 248,160,48\nQta 142,61,37928\nQmaaa 248,164,200\nQmaav 252,168,48\nQdv 48,63,6144\nQmaaa 252,172,200\n"
"Qda 200,63,6656\ns_waitcnt lgkmcnt(2)\nQmvav 80,112,24\nQmvaa 80,116,176\ns_add_u32 s68, s71, 4096\nQmvav 84,120,24\nbuffer_store_dwordx4 a[80:83], v60, s[56:59], s68 offen offset:1024 sc1\nQmvaa 84,124,176\n"
"Qmvav 88,128,24\ns_add_u32 s68, s71, 18432\nQmvaa 88,132,176\nbuffer_store_dwordx4 a[84:87], v60, s[56:59], s68 offen offset:1024 sc1\nQmvav 92,136,24\nQmvaa 92,140,176\ns_waitcnt lgkmcnt(0)\nQmvav 112,112,28\n"
"s_waitcnt vmcnt(21)\ns_barrier\nQmvaa 112,116,180\nQta 144,61,49536\ns_add_u32 m0, s62, 33024\ns_add_u32 s69, s87, 3670016\nQmvav 116,120,28\nbuffer_load_dwordx4 v62, s[52:55], s69 offen lds\n"
"Qmvaa 116,124,180\nQta 146,61,50048\nQmvav 120,128,28\nQmvaa 120,132,180\nQta 148,61,53664\ns_add_u32 s69, s87, 3734520\nQmvav 124,136,28\nbuffer_load_dwordx4 v62, s[52:55], s69 offen offset:1032 lds\n"
"Qmvaa 124,140,180\nQta 150,61,54176\nQmvav 144,112,32\nQmvaa 144,116,184\nQta 152,61,49544\ns_add_u32 s69, s87, 3799024\nQmvav 148,120,32\nbuffer_load_dwordx4 v62, s[52:55], s69 offen offset:2064 lds\n"
"Qmvaa 148,124,184\nQta 154,61,50056\nQmvav 152,128,32\nQmvaa 152,132,184\nQta 156,61,53672\ns_add_u32 s69, s87, 3863528\nQmvav 156,136,32\nbuffer_load_dwordx4 v62, s[52:55], s69 offen offset:3096 lds\n"
"Qmvaa 156,140,184\nQta 158,61,54184\nQmvav 176,112,36\nQmvaa 176,116,188\nQta 160,61,49792\ns_add_u32 m0, s63, 0\ns_add_u32 s88, s86, 896\nQmvav 180,120,36\n"
"buffer_load_dwordx4 v64, s[48:51], s88 offen lds\nQmvaa 180,124,188\nQta 162,61,50304\nQmvav 184,128,36\nQmvaa 184,132,188\nQta 164,61,53920\ns_add_u32 s88, s86, 20352\nQmvav 188,136,36\n"
"buffer_load_dwordx4 v64, s[48:51], s88 offen offset:1024 lds\nQmvaa 188,140,188\nQta 166,61,54432\nQmvav 208,112,40\nQmvaa 208,116,192\nQta 168,61,49800\ns_add_u32 s88, s86, 39808\nQmvav 212,120,40\n"
"buffer_load_dwordx4 v64, s[48:51], s88 offen offset:2048 lds\nQmvaa 212,124,192\nQta 170,61,50312\nQmvav 216,128,40\nQmvaa 216,132,192\nQta 172,61,53928\ns_add_u32 s88, s86, 59264\nQmvav 220,136,40\n"
"buffer_load_dwordx4 v64, s[48:51], s88 offen offset:3072 lds\nQmvaa 220,140,192\nQta 174,61,54440\nQmvav 240,112,44\nQmvaa 240,116,196\ns_add_u32 m0, s63, 4096\ns_add_u32 s88, s86, 82816\nQmvav 244,120,44\n"
"buffer_load_dwordx4 v64, s[48:51], s88 offen lds\nQmvaa 244,124,196\nQmvav 248,128,44\nQmvaa 248,132,196\ns_add_u32 s88, s86, 102272\nQmvav 252,136,44\nbuffer_load_dwordx4 v64, s[48:51], s88 offen offset:1024 lds\nQmvaa 252,140,196\n"
"Qmaav 224,112,48\nQmaaa 224,116,200\ns_add_u32 s88, s86, 121728\nQmaav 228,120,48\nbuffer_load_dwordx4 v64, s[48:51], s88 offen offset:2048 lds\nQmaaa 228,124,200\nQmaav 232,128,48\nQmaaa 232,132,200\n"
"Qmaav 236,136,48\nQmaaa 236,140,200\ns_waitcnt lgkmcnt(0)\nQmvav 96,144,24\nQmvaa 96,148,176\ns_add_u32 s68, s71, 32768\nQmvav 100,152,24\nbuffer_store_dwordx4 a[88:91], v60, s[56:59], s68 offen offset:1024 sc1\n"
"Qmvaa 100,156,176\nQmvav 104,160,24\ns_add_u32 s68, s71, 47104\nQmvaa 104,164,176\nbuffer_store_dwordx4 a[92:95], v60, s[56:59], s68 offen offset:1024 sc1\nQmvav 108,168,24\nQmvaa 108,172,176\ns_waitcnt lgkmcnt(0)\n"
"Qmvav 128,144,28\ns_waitcnt vmcnt(21)\ns_barrier\nQmvaa 128,148,180\nQdv 24,63,33024\ns_add_u32 m0, s62, 49536\ns_add_u32 s69, s87, 3670144\nQmvav 132,152,28\n"
"buffer_load_dwordx4 v62, s[52:55], s69 offen lds\nQmvaa 132,156,180\nQda 176,63,33536\nQmvav 136,160,28\nQta 112,61,0\nQmvaa 136,164,180\ns_add_u32 s69, s87, 3734648\nQmvav 140,168,28\n"
"buffer_load_dwordx4 v62, s[52:55], s69 offen offset:1032 lds\nQmvaa 140,172,180\nQdv 28,63,34048\nQmvav 160,144,32\nQda 180,63,34560\nQmvaa 160,148,184\nQta 114,61,512\ns_add_u32 s69, s87, 3799152\n"
"Qmvav 164,152,32\nbuffer_load_dwordx4 v62, s[52:55], s69 offen offset:2064 lds\nQmvaa 164,156,184\nQta 116,61,4128\nQmvav 168,160,32\nQmvaa 168,164,184\nQta 118,61,4640\ns_add_u32 s69, s87, 3863656\n"
"Qmvav 172,168,32\nbuffer_load_dwordx4 v62, s[52:55], s69 offen offset:3096 lds\nQmvaa 172,172,184\nQdv 32,63,35072\nQmvav 192,144,36\nQda 184,63,35584\nQmvaa 192,148,188\nQta 120,61,8\n"
"s_add_u32 m0, s61, 8192\nQmvav 196,152,36\ns_cbranch_vccnz .Ld0q5\nbuffer_load_dwordx4 v72, s[44:47], s60 offen offset:1024 nt lds\n.Ld0q5:\nQmvaa 196,156,188\nQta 122,61,520\nQmvav 200,160,36\n"
"Qmvaa 200,164,188\nQta 124,61,4136\nQmvav 204,168,36\nQdv 36,63,36096\nQmvaa 204,172,188\nQda 188,63,36608\nQmvav 224,144,40\nQta 126,61,4648\n"
"Qmvaa 224,148,192\nQmvav 228,152,40\n.Lpg2:\ns_cbranch_execz .Ltr0\nQta 128,61,256\nQmvaa 228,156,192\nQmvav 232,160,40\nQta 130,61,768\n"
"Qmvaa 232,164,192\nQmvav 236,168,40\nQdv 40,63,37120\nQmvaa 236,172,192\nQda 192,63,37632\nQmaav 208,144,44\nQta 132,61,4384\nQmaaa 208,148,196\n"
"Qmaav 212,152,44\nQta 134,61,4896\nQmaaa 212,156,196\nQmaav 216,160,44\nQta 136,61,264\nQmaaa 216,164,196\nQmaav 220,168,44\nQdv 44,63,38144\n"
"Qmaaa 220,172,196\nQda 196,63,38656\nQmaav 240,144,48\nQta 138,61,776\nQmaaa 240,148,200\nQmaav 244,152,48\nQta 140,61,4392\nQmaaa 244,156,200\n"
"Qmaav 248,160,48\nQta 142,61,4904\nQmaaa 248,164,200\nQmaav 252,168,48\nQdv 48,63,39168\nQmaaa 252,172,200\nQda 200,63,39680\ns_waitcnt lgkmcnt(2)\n"
"Qmvav 80,112,24\nQmvaa 80,116,176\ns_add_u32 s68, s71, 4096\nQmvav 84,120,24\nbuffer_store_dwordx4 a[96:99], v60, s[56:59], s68 offen offset:2048 sc1\nQmvaa 84,124,176\nQmvav 88,128,24\ns_add_u32 s68, s71, 18432\n"
"Qmvaa 88,132,176\nbuffer_store_dwordx4 a[100:103], v60, s[56:59], s68 offen offset:2048 sc1\nQmvav 92,136,24\nQmvaa 92,140,176\ns_waitcnt lgkmcnt(0)\nQmvav 112,112,28\ns_waitcnt vmcnt(21)\ns_barrier\n"
"Qmvaa 112,116,180\nQta 144,61,16512\ns_add_u32 m0, s62, 0\ns_add_u32 s69, s87, 4194304\nQmvav 116,120,28\nbuffer_load_dwordx4 v62, s[52:55], s69 offen lds\nQmvaa 116,124,180\nQta 146,61,17024\n"
"Qmvav 120,128,28\nQmvaa 120,132,180\nQta 148,61,20640\ns_add_u32 s69, s87, 4258808\nQmvav 124,136,28\nbuffer_load_dwordx4 v62, s[52:55], s69 offen offset:1032 lds\nQmvaa 124,140,180\nQta 150,61,21152\n"
"Qmvav 144,112,32\nQmvaa 144,116,184\nQta 152,61,16520\ns_add_u32 s69, s87, 4323312\nQmvav 148,120,32\nbuffer_load_dwordx4 v62, s[52:55], s69 offen offset:2064 lds\nQmvaa 148,124,184\nQta 154,61,17032\n"
"Qmvav 152,128,32\nQmvaa 152,132,184\nQta 156,61,20648\ns_add_u32 s69, s87, 4387816\nQmvav 156,136,32\nbuffer_load_dwordx4 v62, s[52:55], s69 offen offset:3096 lds\nQmvaa 156,140,184\nQta 158,61,21160\n"
"Qmvav 176,112,36\nQmvaa 176,116,188\nQta 160,61,16768\ns_add_u32 m0, s63, 33024\ns_add_u32 s88, s86, 1024\nQmvav 180,120,36\nbuffer_load_dwordx4 v64, s[48:51], s88 offen lds\nQmvaa 180,124,188\n"
"Qta 162,61,17280\nQmvav 184,128,36\nQmvaa 184,132,188\nQta 164,61,20896\ns_add_u32 s88, s86, 20480\nQmvav 188,136,36\nbuffer_load_dwordx4 v64, s[48:51], s88 offen offset:1024 lds\nQmvaa 188,140,188\n"
"Qta 166,61,21408\nQmvav 208,112,40\nQmvaa 208,116,192\nQta 168,61,16776\ns_add_u32 s88, s86, 39936\nQmvav 212,120,40\nbuffer_load_dwordx4 v64, s[48:51], s88 offen offset:2048 lds\nQmvaa 212,124,192\n"
"Qta 170,61,17288\nQmvav 216,128,40\nQmvaa 216,132,192\nQta 172,61,20904\ns_add_u32 s88, s86, 59392\nQmvav 220,136,40\nbuffer_load_dwordx4 v64, s[48:51], s88 offen offset:3072 lds\nQmvaa 220,140,192\n"
"Qta 174,61,21416\nQmvav 240,112,44\nQmvaa 240,116,196\ns_add_u32 m0, s63, 37120\ns_add_u32 s88, s86, 82944\nQmvav 244,120,44\nbuffer_load_dwordx4 v64, s[48:51], s88 offen lds\nQmvaa 244,124,196\n"
"Qmvav 248,128,44\nQmvaa 248,132,196\ns_add_u32 s88, s86, 102400\nQmvav 252,136,44\nbuffer_load_dwordx4 v64, s[48:51], s88 offen offset:1024 lds\nQmvaa 252,140,196\nQmaav 224,112,48\nQmaaa 224,116,200\n"
"s_add_u32 s88, s86, 121856\nQmaav 228,120,48\nbuffer_load_dwordx4 v64, s[48:51], s88 offen offset:2048 lds\nQmaaa 228,124,200\nQmaav 232,128,48\nQmaaa 232,132,200\nQmaav 236,136,48\nQmaaa 236,140,200\n"
"s_waitcnt lgkmcnt(0)\nQmvav 96,144,24\nQmvaa 96,148,176\ns_add_u32 s68, s71, 32768\nQmvav 100,152,24\nbuffer_store_dwordx4 a[104:107], v60, s[56:59], s68 offen offset:2048 sc1\nQmvaa 100,156,176\nQmvav 104,160,24\n"
"s_add_u32 s68, s71, 47104\nQmvaa 104,164,176\nbuffer_store_dwordx4 a[108:111], v60, s[56:59], s68 offen offset:2048 sc1\nQmvav 108,168,24\nQmvaa 108,172,176\ns_waitcnt lgkmcnt(0)\nQmvav 128,144,28\ns_waitcnt vmcnt(21)\n"
"s_barrier\nQmvaa 128,148,180\nQdv 24,63,0\ns_add_u32 m0, s62, 16512\ns_add_u32 s69, s87, 4194432\nQmvav 132,152,28\nbuffer_load_dwordx4 v62, s[52:55], s69 offen lds\nQmvaa 132,156,180\n"
"Qda 176,63,512\nQmvav 136,160,28\nQta 112,61,33024\nQmvaa 136,164,180\ns_add_u32 s69, s87, 4258936\nQmvav 140,168,28\nbuffer_load_dwordx4 v62, s[52:55], s69 offen offset:1032 lds\nQmvaa 140,172,180\n"
"Qdv 28,63,1024\nQmvav 160,144,32\nQda 180,63,1536\nQmvaa 160,148,184\nQta 114,61,33536\ns_add_u32 s69, s87, 4323440\nQmvav 164,152,32\nbuffer_load_dwordx4 v62, s[52:55], s69 offen offset:2064 lds\n"
"Qmvaa 164,156,184\nQta 116,61,37152\nQmvav 168,160,32\nQmvaa 168,164,184\nQta 118,61,37664\ns_add_u32 s69, s87, 4387944\nQmvav 172,168,32\nbuffer_load_dwordx4 v62, s[52:55], s69 offen offset:3096 lds\n"
"Qmvaa 172,172,184\nQdv 32,63,2048\nQmvav 192,144,36\nQda 184,63,2560\nQmvaa 192,148,188\nQta 120,61,33032\ns_add_u32 m0, s61, 8192\nQmvav 196,152,36\n"
"s_cbranch_vccnz .Ld0q6\nbuffer_load_dwordx4 v72, s[44:47], s90 offen offset:2048 nt lds\n.Ld0q6:\nQmvaa 196,156,188\nQta 122,61,33544\nQmvav 200,160,36\nQmvaa 200,164,188\nQta 124,61,37160\n"
"Qmvav 204,168,36\nQdv 36,63,3072\nQmvaa 204,172,188\nQda 188,63,3584\nQmvav 224,144,40\nQta 126,61,37672\nQmvaa 224,148,192\nQmvav 228,152,40\n"
"Qta 128,61,33280\nQmvaa 228,156,192\nQmvav 232,160,40\nQta 130,61,33792\nQmvaa 232,164,192\nQmvav 236,168,40\nQdv 40,63,4096\nQmvaa 236,172,192\n"
"Qda 192,63,4608\nQmaav 208,144,44\nQta 132,61,37408\nQmaaa 208,148,196\nQmaav 212,152,44\nQta 134,61,37920\nQmaaa 212,156,196\nQmaav 216,160,44\n"
"Qta 136,61,33288\nQmaaa 216,164,196\nQmaav 220,168,44\nQdv 44,63,5120\nQmaaa 220,172,196\nQda 196,63,5632\nQmaav 240,144,48\nQta 138,61,33800\n"
"Qmaaa 240,148,200\nQmaav 244,152,48\nQta 140,61,37416\nQmaaa 244,156,200\nQmaav 248,160,48\nQta 142,61,37928\nQmaaa 248,164,200\nQmaav 252,168,48\n"
"Qdv 48,63,6144\nQmaaa 252,172,200\nQda 200,63,6656\ns_waitcnt lgkmcnt(2)\nQmvav 80,112,24\nQmvaa 80,116,176\nQmvav 84,120,24\nQmvaa 84,124,176\n"
"Qmvav 88,128,24\nQmvaa 88,132,176\nQmvav 92,136,24\nQmvaa 92,140,176\ns_waitcnt lgkmcnt(0)\nQmvav 112,112,28\ns_waitcnt vmcnt(19)\ns_barrier\n"
"Qmvaa 112,116,180\nQta 144,61,49536\ns_add_u32 m0, s62, 33024\ns_add_u32 s69, s87, 4718592\nQmvav 116,120,28\nbuffer_load_dwordx4 v62, s[52:55], s69 offen lds\nQmvaa 116,124,180\nQta 146,61,50048\n"
"Qmvav 120,128,28\nQmvaa 120,132,180\nQta 148,61,53664\ns_add_u32 s69, s87, 4783096\nQmvav 124,136,28\nbuffer_load_dwordx4 v62, s[52:55], s69 offen offset:1032 lds\nQmvaa 124,140,180\nQta 150,61,54176\n"
"Qmvav 144,112,32\nQmvaa 144,116,184\nQta 152,61,49544\ns_add_u32 s69, s87, 4847600\nQmvav 148,120,32\nbuffer_load_dwordx4 v62, s[52:55], s69 offen offset:2064 lds\nQmvaa 148,124,184\nQta 154,61,50056\n"
"Qmvav 152,128,32\nQmvaa 152,132,184\nQta 156,61,53672\ns_add_u32 s69, s87, 4912104\nQmvav 156,136,32\nbuffer_load_dwordx4 v62, s[52:55], s69 offen offset:3096 lds\nQmvaa 156,140,184\nQta 158,61,54184\n"
"Qmvav 176,112,36\nQmvaa 176,116,188\nQta 160,61,49792\ns_add_u32 m0, s63, 0\ns_add_u32 s88, s86, 1152\nQmvav 180,120,36\nbuffer_load_dwordx4 v64, s[48:51], s88 offen lds\nQmvaa 180,124,188\n"
"Qta 162,61,50304\nQmvav 184,128,36\nQmvaa 184,132,188\nQta 164,61,53920\ns_add_u32 s88, s86, 20608\nQmvav 188,136,36\nbuffer_load_dwordx4 v64, s[48:51], s88 offen offset:1024 lds\nQmvaa 188,140,188\n"
"Qta 166,61,54432\nQmvav 208,112,40\nQmvaa 208,116,192\nQta 168,61,49800\ns_add_u32 s88, s86, 40064\nQmvav 212,120,40\nbuffer_load_dwordx4 v64, s[48:51], s88 offen offset:2048 lds\nQmvaa 212,124,192\n"
"Qta 170,61,50312\nQmvav 216,128,40\nQmvaa 216,132,192\nQta 172,61,53928\ns_add_u32 s88, s86, 59520\nQmvav 220,136,40\nbuffer_load_dwordx4 v64, s[48:51], s88 offen offset:3072 lds\nQmvaa 220,140,192\n"
"Qta 174,61,54440\nQmvav 240,112,44\nQmvaa 240,116,196\ns_add_u32 m0, s63, 4096\ns_add_u32 s88, s86, 83072\nQmvav 244,120,44\nbuffer_load_dwordx4 v64, s[48:51], s88 offen lds\nQmvaa 244,124,196\n"
"Qmvav 248,128,44\nQmvaa 248,132,196\ns_add_u32 s88, s86, 102528\nQmvav 252,136,44\nbuffer_load_dwordx4 v64, s[48:51], s88 offen offset:1024 lds\nQmvaa 252,140,196\nQmaav 224,112,48\nQmaaa 224,116,200\n"
"s_add_u32 s88, s86, 121984\nQmaav 228,120,48\nbuffer_load_dwordx4 v64, s[48:51], s88 offen offset:2048 lds\nQmaaa 228,124,200\nQmaav 232,128,48\nQmaaa 232,132,200\nQmaav 236,136,48\nQmaaa 236,140,200\n"
"s_waitcnt lgkmcnt(0)\nQmvav 96,144,24\nQmvaa 96,148,176\nQmvav 100,152,24\nQmvaa 100,156,176\nQmvav 104,160,24\nQmvaa 104,164,176\nQmvav 108,168,24\n"
"Qmvaa 108,172,176\ns_waitcnt lgkmcnt(0)\nQmvav 128,144,28\ns_waitcnt vmcnt(17)\ns_barrier\nQmvaa 128,148,180\nQdv 24,63,33024\ns_add_u32 m0, s62, 49536\n"
"s_add_u32 s69, s87, 4718720\nQmvav 132,152,28\nbuffer_load_dwordx4 v62, s[52:55], s69 offen lds\nQmvaa 132,156,180\nQda 176,63,33536\nQmvav 136,160,28\nQta 112,61,0\nQmvaa 136,164,180\n"
"s_add_u32 s69, s87, 4783224\nQmvav 140,168,28\nbuffer_load_dwordx4 v62, s[52:55], s69 offen offset:1032 lds\nQmvaa 140,172,180\nQdv 28,63,34048\nQmvav 160,144,32\nQda 180,63,34560\nQmvaa 160,148,184\n"
"Qta 114,61,512\ns_add_u32 s69, s87, 4847728\nQmvav 164,152,32\nbuffer_load_dwordx4 v62, s[52:55], s69 offen offset:2064 lds\nQmvaa 164,156,184\nQta 116,61,4128\nQmvav 168,160,32\nQmvaa 168,164,184\n"
"Qta 118,61,4640\ns_add_u32 s69, s87, 4912232\nQmvav 172,168,32\nbuffer_load_dwordx4 v62, s[52:55], s69 offen offset:3096 lds\nQmvaa 172,172,184\nQdv 32,63,35072\nQmvav 192,144,36\nQda 184,63,35584\n"
"Qmvaa 192,148,188\nQta 120,61,8\ns_add_u32 m0, s61, 8192\nQmvav 196,152,36\ns_cbranch_vccnz .Ld0q7\nbuffer_load_dwordx4 v72, s[44:47], s60 offen offset:3072 nt lds\ns_add_u32 s60, s60, 8192\ns_add_u32 s90, s60, 896\n"
".Ld0q7:\nQmvaa 196,156,188\nQta 122,61,520\nQmvav 200,160,36\nQmvaa 200,164,188\nQta 124,61,4136\nQmvav 204,168,36\nQdv 36,63,36096\n"
"Qmvaa 204,172,188\nQda 188,63,36608\nQmvav 224,144,40\nQta 126,61,4648\nQmvaa 224,148,192\nQmvav 228,152,40\nQta 128,61,256\nQmvaa 228,156,192\n"
"Qmvav 232,160,40\nQta 130,61,768\nQmvaa 232,164,192\nQmvav 236,168,40\nQdv 40,63,37120\nQmvaa 236,172,192\nQda 192,63,37632\nQmaav 208,144,44\n"
"Qta 132,61,4384\nQmaaa 208,148,196\nQmaav 212,152,44\nQta 134,61,4896\nQmaaa 212,156,196\nQmaav 216,160,44\nQta 136,61,264\nQmaaa 216,164,196\n"
"Qmaav 220,168,44\nQdv 44,63,38144\nQmaaa 220,172,196\nQda 196,63,38656\nQmaav 240,144,48\nQta 138,61,776\nQmaaa 240,148,200\nQmaav 244,152,48\n"
"Qta 140,61,4392\nQmaaa 244,156,200\nQmaav 248,160,48\nQta 142,61,4904\nQmaaa 248,164,200\nQmaav 252,168,48\nQdv 48,63,39168\nQmaaa 252,172,200\n"
"Qda 200,63,39680\ns_waitcnt lgkmcnt(2)\nQmvav 80,112,24\nQmvaa 80,116,176\nQmvav 84,120,24\nQmvaa 84,124,176\nQmvav 88,128,24\nQmvaa 88,132,176\n"
"Qmvav 92,136,24\nQmvaa 92,140,176\ns_waitcnt lgkmcnt(0)\nQmvav 112,112,28\ns_waitcnt vmcnt(15)\ns_barrier\nQmvaa 112,116,180\nQta 144,61,16512\n"
"s_mov_b32 s56, s94\ns_mov_b32 s57, s95\ns_mov_b32 s58, s75\ns_mov_b32 s94, 0\ns_add_u32 m0, %10, 160768\nQmvav 116,120,28\nbuffer_load_dwordx4 v60, s[56:59], s94 offen lds\nQmvaa 116,124,180\n"
"Qta 146,61,17024\nQmvav 120,128,28\nQmvaa 120,132,180\nQta 148,61,20640\ns_add_u32 s94, s94, 4096\nQmvav 124,136,28\nbuffer_load_dwordx4 v60, s[56:59], s94 offen lds\nQmvaa 124,140,180\n"
"Qta 150,61,21152\nQmvav 144,112,32\nQmvaa 144,116,184\nQta 152,61,16520\ns_add_u32 s94, s94, 4096\nQmvav 148,120,32\nbuffer_load_dwordx4 v60, s[56:59], s94 offen lds\nQmvaa 148,124,184\n"
"Qta 154,61,17032\nQmvav 152,128,32\nQmvaa 152,132,184\nQta 156,61,20648\ns_add_u32 s94, s94, 4096\nQmvav 156,136,32\nbuffer_load_dwordx4 v60, s[56:59], s94 offen lds\nQmvaa 156,140,184\n"
"Qta 158,61,21160\nQmvav 176,112,36\nQmvaa 176,116,188\nQta 160,61,16768\ns_add_u32 s94, s94, 4096\nQmvav 180,120,36\nbuffer_load_dwordx4 v60, s[56:59], s94 offen lds\nQmvaa 180,124,188\n"
"Qta 162,61,17280\nQmvav 184,128,36\nQmvaa 184,132,188\nQta 164,61,20896\ns_add_u32 s94, s94, 4096\nQmvav 188,136,36\nbuffer_load_dwordx4 v60, s[56:59], s94 offen lds\nQmvaa 188,140,188\n"
"Qta 166,61,21408\nQmvav 208,112,40\nQmvaa 208,116,192\nQta 168,61,16776\ns_add_u32 s94, s94, 4096\nQmvav 212,120,40\nbuffer_load_dwordx4 v60, s[56:59], s94 offen lds\nQmvaa 212,124,192\n"
"Qta 170,61,17288\nQmvav 216,128,40\nQmvaa 216,132,192\nQta 172,61,20904\n.Lpg3:\ns_cbranch_execz .Ltr0\ns_add_u32 s94, s94, 4096\nQmvav 220,136,40\n"
"buffer_load_dwordx4 v60, s[56:59], s94 offen lds\nQmvaa 220,140,192\nQta 174,61,21416\nQmvav 240,112,44\nQmvaa 240,116,196\ns_add_u32 s94, s94, 4096\nQmvav 244,120,44\nbuffer_load_dwordx4 v60, s[56:59], s94 offen lds\n"
"Qmvaa 244,124,196\nQmvav 248,128,44\nQmvaa 248,132,196\ns_add_u32 s94, s94, 4096\nQmvav 252,136,44\nbuffer_load_dwordx4 v60, s[56:59], s94 offen lds\nQmvaa 252,140,196\nQmaav 224,112,48\n"
"Qmaaa 224,116,200\nQmaav 228,120,48\nQmaaa 228,124,200\nQmaav 232,128,48\nQmaaa 232,132,200\nQmaav 236,136,48\nQmaaa 236,140,200\ns_waitcnt lgkmcnt(0)\n"
"Qmvav 96,144,24\nQmvaa 96,148,176\nQmvav 100,152,24\nQmvaa 100,156,176\nQmvav 104,160,24\nQmvaa 104,164,176\nQmvav 108,168,24\nQmvaa 108,172,176\n"
"s_waitcnt lgkmcnt(0)\nQmvav 128,144,28\ns_waitcnt vmcnt(14)\ns_barrier\nQmvaa 128,148,180\nQdv 24,63,0\ns_add_u32 m0, s61, 33024\nQmvav 132,152,28\n"
"buffer_load_dwordx4 v69, s[40:43], s90 offen nt lds\nQmvaa 132,156,180\nQda 176,63,512\nQmvav 136,160,28\nQta 112,61,33024\nQmvaa 136,164,180\nQmvav 140,168,28\nbuffer_load_dwordx4 v69, s[40:43], s60 offen offset:1024 nt lds\n"
"Qmvaa 140,172,180\nQdv 28,63,1024\nQmvav 160,144,32\nQda 180,63,1536\nQmvaa 160,148,184\nQta 114,61,33536\nQmvav 164,152,32\nbuffer_load_dwordx4 v69, s[40:43], s90 offen offset:2048 nt lds\n"
"Qmvaa 164,156,184\nQta 116,61,37152\nQmvav 168,160,32\nQmvaa 168,164,184\nQta 118,61,37664\nQmvav 172,168,32\nbuffer_load_dwordx4 v69, s[40:43], s60 offen offset:3072 nt lds\nQmvaa 172,172,184\n"
"Qdv 32,63,2048\nQmvav 192,144,36\nQda 184,63,2560\nQmvaa 192,148,188\nQta 120,61,33032\ns_add_u32 m0, s61, 41216\nQmvav 196,152,36\nbuffer_load_dwordx4 v72, s[44:47], s90 offen nt lds\n"
"Qmvaa 196,156,188\nQta 122,61,33544\nQmvav 200,160,36\nQmvaa 200,164,188\nQta 124,61,37160\nQmvav 204,168,36\nbuffer_load_dwordx4 v72, s[44:47], s60 offen offset:1024 nt lds\nQmvaa 204,172,188\n"
"Qdv 36,63,3072\nQmvav 224,144,40\nQda 188,63,3584\nQmvaa 224,148,192\nQta 126,61,37672\nQmvav 228,152,40\nbuffer_load_dwordx4 v72, s[44:47], s90 offen offset:2048 nt lds\nQmvaa 228,156,192\n"
"Qta 128,61,33280\nQmvav 232,160,40\nQmvaa 232,164,192\nQta 130,61,33792\nQmvav 236,168,40\nbuffer_load_dwordx4 v72, s[44:47], s60 offen offset:3072 nt lds\ns_add_u32 s60, s60, 8192\ns_add_u32 s90, s60, 896\n"
"Qmvaa 236,172,192\nQdv 40,63,4096\nQmaav 208,144,44\nQda 192,63,4608\nQmaaa 208,148,196\nQta 132,61,37408\nQmaav 212,152,44\nQmaaa 212,156,196\n"
"Qta 134,61,37920\nQmaav 216,160,44\nQmaaa 216,164,196\nQta 136,61,33288\nQmaav 220,168,44\nQdv 44,63,5120\nQmaaa 220,172,196\nQda 196,63,5632\n"
"Qmaav 240,144,48\nQta 138,61,33800\nQmaaa 240,148,200\nQmaav 244,152,48\nQta 140,61,37416\nQmaaa 244,156,200\nQmaav 248,160,48\nQta 142,61,37928\n"
"Qmaaa 248,164,200\nQmaav 252,168,48\nQdv 48,63,6144\nQmaaa 252,172,200\nQda 200,63,6656\ns_waitcnt lgkmcnt(2)\nQmvav 80,112,24\nQmvaa 80,116,176\n"
"Qmvav 84,120,24\nQmvaa 84,124,176\nQmvav 88,128,24\nQmvaa 88,132,176\nQmvav 92,136,24\nQmvaa 92,140,176\ns_waitcnt lgkmcnt(0)\nQmvav 112,112,28\n"
"s_waitcnt vmcnt(18)\ns_barrier\nQmvaa 112,116,180\nQta 144,61,49536\nQmvav 116,120,28\nQmvaa 116,124,180\nQta 146,61,50048\nQmvav 120,128,28\n"
"Qmvaa 120,132,180\nQta 148,61,53664\nQmvav 124,136,28\nQmvaa 124,140,180\nQta 150,61,54176\nQmvav 144,112,32\nQmvaa 144,116,184\nQta 152,61,49544\n"
"Qmvav 148,120,32\nQmvaa 148,124,184\nQta 154,61,50056\nQmvav 152,128,32\nQmvaa 152,132,184\nQta 156,61,53672\nQmvav 156,136,32\nQmvaa 156,140,184\n"
"Qta 158,61,54184\nQmvav 176,112,36\nQmvaa 176,116,188\nQta 160,61,49792\nQmvav 180,120,36\nQmvaa 180,124,188\nQta 162,61,50304\nQmvav 184,128,36\n"
"Qmvaa 184,132,188\nQta 164,61,53920\nQmvav 188,136,36\nQmvaa 188,140,188\nQta 166,61,54432\nQmvav 208,112,40\nQmvaa 208,116,192\nQta 168,61,49800\n"
"Qmvav 212,120,40\nQmvaa 212,124,192\nQta 170,61,50312\nQmvav 216,128,40\nQmvaa 216,132,192\nQta 172,61,53928\nQmvav 220,136,40\nQmvaa 220,140,192\n"
"Qta 174,61,54440\nQmvav 240,112,44\nQmvaa 240,116,196\nQmvav 244,120,44\nQmvaa 244,124,196\nQmvav 248,128,44\nQmvaa 248,132,196\nQmvav 252,136,44\n"
"Qmvaa 252,140,196\nQmaav 224,112,48\nQmaaa 224,116,200\nQmaav 228,120,48\nQmaaa 228,124,200\nQmaav 232,128,48\nQmaaa 232,132,200\nQmaav 236,136,48\n"
"Qmaaa 236,140,200\ns_waitcnt lgkmcnt(0)\nQmvav 96,144,24\nQmvaa 96,148,176\nQmvav 100,152,24\nQmvaa 100,156,176\nQmvav 104,160,24\nQmvaa 104,164,176\n"
"Qmvav 108,168,24\nQmvaa 108,172,176\ns_waitcnt lgkmcnt(0)\nQmvav 128,144,28\ns_waitcnt vmcnt(18)\ns_barrier\nQmvaa 128,148,180\nQda 112,68,0\n"
"s_add_u32 m0, s61, 66048\nv_accvgpr_write_b32 a176, 0\nQmvav 132,152,28\nbuffer_load_dwordx4 v69, s[40:43], s90 offen nt lds\nQmvaa 132,156,180\nQda 116,68,512\nv_accvgpr_write_b32 a177, 0\nQmvav 136,160,28\n"
"Qmvaa 136,164,180\nQda 120,68,1024\nv_accvgpr_write_b32 a178, 0\nQmvav 140,168,28\nbuffer_load_dwordx4 v69, s[40:43], s60 offen offset:1024 nt lds\nQmvaa 140,172,180\nQda 124,68,1536\nv_accvgpr_write_b32 a179, 0\n"
"Qmvav 160,144,32\nQmvaa 160,148,184\nQda 128,68,2048\nv_accvgpr_write_b32 a180, 0\nQmvav 164,152,32\nbuffer_load_dwordx4 v69, s[40:43], s90 offen offset:2048 nt lds\nQmvaa 164,156,184\nQda 132,68,2560\n"
"v_accvgpr_write_b32 a181, 0\nQmvav 168,160,32\nQmvaa 168,164,184\nQda 136,68,3072\nv_accvgpr_write_b32 a182, 0\nQmvav 172,168,32\nbuffer_load_dwordx4 v69, s[40:43], s60 offen offset:3072 nt lds\nQmvaa 172,172,184\n"
"Qda 140,68,3584\nv_accvgpr_write_b32 a183, 0\nQmvav 192,144,36\nQmvaa 192,148,188\ns_add_u32 m0, s61, 74240\nv_accvgpr_write_b32 a184, 0\nQmvav 196,152,36\nbuffer_load_dwordx4 v72, s[44:47], s90 offen nt lds\n"
"Qmvaa 196,156,188\nv_accvgpr_write_b32 a185, 0\nQmvav 200,160,36\nQmvaa 200,164,188\nv_accvgpr_write_b32 a186, 0\nQmvav 204,168,36\nbuffer_load_dwordx4 v72, s[44:47], s60 offen offset:1024 nt lds\nQmvaa 204,172,188\n"
"v_accvgpr_write_b32 a187, 0\nQmvav 224,144,40\nQmvaa 224,148,192\nv_accvgpr_write_b32 a188, 0\nQmvav 228,152,40\nbuffer_load_dwordx4 v72, s[44:47], s90 offen offset:2048 nt lds\nQmvaa 228,156,192\nv_accvgpr_write_b32 a189, 0\n"
"Qmvav 232,160,40\nQmvaa 232,164,192\nv_accvgpr_write_b32 a190, 0\nQmvav 236,168,40\nbuffer_load_dwordx4 v72, s[44:47], s60 offen offset:3072 nt lds\ns_add_u32 s60, s60, 8192\ns_add_u32 s90, s60, 896\nQmvaa 236,172,192\n"
"v_accvgpr_write_b32 a191, 0\nQmaav 208,144,44\nQmaaa 208,148,196\nv_accvgpr_write_b32 a192, 0\nQmaav 212,152,44\nQmaaa 212,156,196\nv_accvgpr_write_b32 a193, 0\nQmaav 216,160,44\n"
"Qmaaa 216,164,196\nv_accvgpr_write_b32 a194, 0\nQmaav 220,168,44\nQmaaa 220,172,196\nv_accvgpr_write_b32 a195, 0\nQmaav 240,144,48\nQmaaa 240,148,200\nv_accvgpr_write_b32 a196, 0\n"
"Qmaav 244,152,48\nQmaaa 244,156,200\nv_accvgpr_write_b32 a197, 0\nQmaav 248,160,48\nQmaaa 248,164,200\nv_accvgpr_write_b32 a198, 0\nQmaav 252,168,48\nQmaaa 252,172,200\n"
"v_accvgpr_write_b32 a199, 0\nQta 148,70,1024\nQta 150,70,3072\nQta 156,70,1280\nQta 158,70,3328\nQta 164,70,1536\nQta 166,70,3584\nQta 172,70,1792\n"
"Qta 174,70,3840\nv_pk_mul_f32 v[80:81], v[80:81], s[72:73] op_sel_hi:[1,0]\nv_pk_mul_f32 v[82:83], v[82:83], s[72:73] op_sel_hi:[1,0]\nv_pk_mul_f32 v[84:85], v[84:85], s[72:73] op_sel_hi:[1,0]\nv_pk_mul_f32 v[86:87], v[86:87], s[72:73] op_sel_hi:[1,0]\nQk 80,80,81\nQk 81,82,83\nQk 82,84,85\n"
"Qk 83,86,87\nv_accvgpr_write_b32 a0, v80\nv_accvgpr_write_b32 a1, v81\nv_accvgpr_write_b32 a2, v82\nv_accvgpr_write_b32 a3, v83\nv_pk_mul_f32 v[88:89], v[88:89], s[72:73] op_sel_hi:[1,0]\nv_pk_mul_f32 v[90:91], v[90:91], s[72:73] op_sel_hi:[1,0]\nv_pk_mul_f32 v[92:93], v[92:93], s[72:73] op_sel_hi:[1,0]\n"
"v_pk_mul_f32 v[94:95], v[94:95], s[72:73] op_sel_hi:[1,0]\nQk 88,88,89\nQk 89,90,91\nQk 90,92,93\nQk 91,94,95\nv_accvgpr_write_b32 a4, v88\nv_accvgpr_write_b32 a5, v89\nv_accvgpr_write_b32 a6, v90\n"
"v_accvgpr_write_b32 a7, v91\nv_pk_mul_f32 v[96:97], v[96:97], s[72:73] op_sel_hi:[1,0]\nv_pk_mul_f32 v[98:99], v[98:99], s[72:73] op_sel_hi:[1,0]\nv_pk_mul_f32 v[100:101], v[100:101], s[72:73] op_sel_hi:[1,0]\nv_pk_mul_f32 v[102:103], v[102:103], s[72:73] op_sel_hi:[1,0]\nQk 96,96,97\nQk 97,98,99\nQk 98,100,101\n"
"Qk 99,102,103\nv_accvgpr_write_b32 a8, v96\nv_accvgpr_write_b32 a9, v97\nv_accvgpr_write_b32 a10, v98\nv_accvgpr_write_b32 a11, v99\nv_pk_mul_f32 v[104:105], v[104:105], s[72:73] op_sel_hi:[1,0]\nv_pk_mul_f32 v[106:107], v[106:107], s[72:73] op_sel_hi:[1,0]\nv_pk_mul_f32 v[108:109], v[108:109], s[72:73] op_sel_hi:[1,0]\n"
"v_pk_mul_f32 v[110:111], v[110:111], s[72:73] op_sel_hi:[1,0]\nQk 104,104,105\nQk 105,106,107\nQk 106,108,109\nQk 107,110,111\nv_accvgpr_write_b32 a12, v104\nv_accvgpr_write_b32 a13, v105\nv_accvgpr_write_b32 a14, v106\n"
"v_accvgpr_write_b32 a15, v107\nv_pk_mul_f32 v[112:113], v[112:113], s[72:73] op_sel_hi:[1,0]\nv_pk_mul_f32 v[114:115], v[114:115], s[72:73] op_sel_hi:[1,0]\nv_pk_mul_f32 v[116:117], v[116:117], s[72:73] op_sel_hi:[1,0]\nv_pk_mul_f32 v[118:119], v[118:119], s[72:73] op_sel_hi:[1,0]\nQk 112,112,113\nQk 113,114,115\nQk 114,116,117\n"
"Qk 115,118,119\nv_accvgpr_write_b32 a16, v112\nv_accvgpr_write_b32 a17, v113\nv_accvgpr_write_b32 a18, v114\nv_accvgpr_write_b32 a19, v115\nv_pk_mul_f32 v[120:121], v[120:121], s[72:73] op_sel_hi:[1,0]\nv_pk_mul_f32 v[122:123], v[122:123], s[72:73] op_sel_hi:[1,0]\nv_pk_mul_f32 v[124:125], v[124:125], s[72:73] op_sel_hi:[1,0]\n"
"v_pk_mul_f32 v[126:127], v[126:127], s[72:73] op_sel_hi:[1,0]\nQk 120,120,121\nQk 121,122,123\nQk 122,124,125\nQk 123,126,127\nv_accvgpr_write_b32 a20, v120\nv_accvgpr_write_b32 a21, v121\nv_accvgpr_write_b32 a22, v122\n"
"v_accvgpr_write_b32 a23, v123\nv_pk_mul_f32 v[128:129], v[128:129], s[72:73] op_sel_hi:[1,0]\nv_pk_mul_f32 v[130:131], v[130:131], s[72:73] op_sel_hi:[1,0]\nv_pk_mul_f32 v[132:133], v[132:133], s[72:73] op_sel_hi:[1,0]\nv_pk_mul_f32 v[134:135], v[134:135], s[72:73] op_sel_hi:[1,0]\nQk 128,128,129\nQk 129,130,131\nQk 130,132,133\n"
"Qk 131,134,135\nv_accvgpr_write_b32 a24, v128\nv_accvgpr_write_b32 a25, v129\nv_accvgpr_write_b32 a26, v130\nv_accvgpr_write_b32 a27, v131\nv_pk_mul_f32 v[136:137], v[136:137], s[72:73] op_sel_hi:[1,0]\nv_pk_mul_f32 v[138:139], v[138:139], s[72:73] op_sel_hi:[1,0]\nv_pk_mul_f32 v[140:141], v[140:141], s[72:73] op_sel_hi:[1,0]\n"
"v_pk_mul_f32 v[142:143], v[142:143], s[72:73] op_sel_hi:[1,0]\nQk 136,136,137\nQk 137,138,139\nQk 138,140,141\nQk 139,142,143\nv_accvgpr_write_b32 a28, v136\nv_accvgpr_write_b32 a29, v137\nv_accvgpr_write_b32 a30, v138\n"
"v_accvgpr_write_b32 a31, v139\nv_pk_mul_f32 v[144:145], v[144:145], s[72:73] op_sel_hi:[1,0]\nv_pk_mul_f32 v[146:147], v[146:147], s[72:73] op_sel_hi:[1,0]\nv_pk_mul_f32 v[148:149], v[148:149], s[72:73] op_sel_hi:[1,0]\nv_pk_mul_f32 v[150:151], v[150:151], s[72:73] op_sel_hi:[1,0]\nQk 144,144,145\nQk 145,146,147\nQk 146,148,149\n"
"Qk 147,150,151\nv_accvgpr_write_b32 a32, v144\nv_accvgpr_write_b32 a33, v145\nv_accvgpr_write_b32 a34, v146\nv_accvgpr_write_b32 a35, v147\nv_pk_mul_f32 v[152:153], v[152:153], s[72:73] op_sel_hi:[1,0]\nv_pk_mul_f32 v[154:155], v[154:155], s[72:73] op_sel_hi:[1,0]\nv_pk_mul_f32 v[156:157], v[156:157], s[72:73] op_sel_hi:[1,0]\n"
"v_pk_mul_f32 v[158:159], v[158:159], s[72:73] op_sel_hi:[1,0]\nQk 152,152,153\nQk 153,154,155\nQk 154,156,157\nQk 155,158,159\nv_accvgpr_write_b32 a36, v152\nv_accvgpr_write_b32 a37, v153\nv_accvgpr_write_b32 a38, v154\n"
"v_accvgpr_write_b32 a39, v155\nv_pk_mul_f32 v[160:161], v[160:161], s[72:73] op_sel_hi:[1,0]\nv_pk_mul_f32 v[162:163], v[162:163], s[72:73] op_sel_hi:[1,0]\nv_pk_mul_f32 v[164:165], v[164:165], s[72:73] op_sel_hi:[1,0]\nv_pk_mul_f32 v[166:167], v[166:167], s[72:73] op_sel_hi:[1,0]\nQk 160,160,161\nQk 161,162,163\nQk 162,164,165\n"
"Qk 163,166,167\nv_accvgpr_write_b32 a40, v160\nv_accvgpr_write_b32 a41, v161\nv_accvgpr_write_b32 a42, v162\nv_accvgpr_write_b32 a43, v163\nv_pk_mul_f32 v[168:169], v[168:169], s[72:73] op_sel_hi:[1,0]\nv_pk_mul_f32 v[170:171], v[170:171], s[72:73] op_sel_hi:[1,0]\nv_pk_mul_f32 v[172:173], v[172:173], s[72:73] op_sel_hi:[1,0]\n"
"v_pk_mul_f32 v[174:175], v[174:175], s[72:73] op_sel_hi:[1,0]\nQk 168,168,169\nQk 169,170,171\nQk 170,172,173\nQk 171,174,175\nv_accvgpr_write_b32 a44, v168\nv_accvgpr_write_b32 a45, v169\nv_accvgpr_write_b32 a46, v170\n"
"v_accvgpr_write_b32 a47, v171\nv_pk_mul_f32 v[176:177], v[176:177], s[72:73] op_sel_hi:[1,0]\nv_pk_mul_f32 v[178:179], v[178:179], s[72:73] op_sel_hi:[1,0]\nv_pk_mul_f32 v[180:181], v[180:181], s[72:73] op_sel_hi:[1,0]\nv_pk_mul_f32 v[182:183], v[182:183], s[72:73] op_sel_hi:[1,0]\nQk 176,176,177\nQk 177,178,179\nQk 178,180,181\n"
"Qk 179,182,183\nv_accvgpr_write_b32 a48, v176\nv_accvgpr_write_b32 a49, v177\nv_accvgpr_write_b32 a50, v178\nv_accvgpr_write_b32 a51, v179\nv_pk_mul_f32 v[184:185], v[184:185], s[72:73] op_sel_hi:[1,0]\nv_pk_mul_f32 v[186:187], v[186:187], s[72:73] op_sel_hi:[1,0]\nv_pk_mul_f32 v[188:189], v[188:189], s[72:73] op_sel_hi:[1,0]\n"
"v_pk_mul_f32 v[190:191], v[190:191], s[72:73] op_sel_hi:[1,0]\nQk 184,184,185\nQk 185,186,187\nQk 186,188,189\nQk 187,190,191\nv_accvgpr_write_b32 a52, v184\nv_accvgpr_write_b32 a53, v185\nv_accvgpr_write_b32 a54, v186\n"
"v_accvgpr_write_b32 a55, v187\nv_pk_mul_f32 v[192:193], v[192:193], s[72:73] op_sel_hi:[1,0]\nv_pk_mul_f32 v[194:195], v[194:195], s[72:73] op_sel_hi:[1,0]\nv_pk_mul_f32 v[196:197], v[196:197], s[72:73] op_sel_hi:[1,0]\nv_pk_mul_f32 v[198:199], v[198:199], s[72:73] op_sel_hi:[1,0]\nQk 192,192,193\nQk 193,194,195\nQk 194,196,197\n"
"Qk 195,198,199\nv_accvgpr_write_b32 a56, v192\nv_accvgpr_write_b32 a57, v193\nv_accvgpr_write_b32 a58, v194\nv_accvgpr_write_b32 a59, v195\nv_pk_mul_f32 v[200:201], v[200:201], s[72:73] op_sel_hi:[1,0]\nv_pk_mul_f32 v[202:203], v[202:203], s[72:73] op_sel_hi:[1,0]\nv_pk_mul_f32 v[204:205], v[204:205], s[72:73] op_sel_hi:[1,0]\n"
"v_pk_mul_f32 v[206:207], v[206:207], s[72:73] op_sel_hi:[1,0]\nQk 200,200,201\nQk 201,202,203\nQk 202,204,205\nQk 203,206,207\nv_accvgpr_write_b32 a60, v200\nv_accvgpr_write_b32 a61, v201\nv_accvgpr_write_b32 a62, v202\n"
"v_accvgpr_write_b32 a63, v203\nv_pk_mul_f32 v[208:209], v[208:209], s[72:73] op_sel_hi:[1,0]\nv_pk_mul_f32 v[210:211], v[210:211], s[72:73] op_sel_hi:[1,0]\nv_pk_mul_f32 v[212:213], v[212:213], s[72:73] op_sel_hi:[1,0]\nv_pk_mul_f32 v[214:215], v[214:215], s[72:73] op_sel_hi:[1,0]\nQk 208,208,209\nQk 209,210,211\nQk 210,212,213\n"
"Qk 211,214,215\nv_accvgpr_write_b32 a64, v208\nv_accvgpr_write_b32 a65, v209\n.Lpg4:\ns_cbranch_execz .Ltr9\nv_accvgpr_write_b32 a66, v210\nv_accvgpr_write_b32 a67, v211\nv_pk_mul_f32 v[216:217], v[216:217], s[72:73] op_sel_hi:[1,0]\n"
"v_pk_mul_f32 v[218:219], v[218:219], s[72:73] op_sel_hi:[1,0]\nv_pk_mul_f32 v[220:221], v[220:221], s[72:73] op_sel_hi:[1,0]\nv_pk_mul_f32 v[222:223], v[222:223], s[72:73] op_sel_hi:[1,0]\nQk 216,216,217\nQk 217,218,219\nQk 218,220,221\nQk 219,222,223\nv_accvgpr_write_b32 a68, v216\n"
"v_accvgpr_write_b32 a69, v217\nv_accvgpr_write_b32 a70, v218\nv_accvgpr_write_b32 a71, v219\nv_pk_mul_f32 v[224:225], v[224:225], s[72:73] op_sel_hi:[1,0]\nv_pk_mul_f32 v[226:227], v[226:227], s[72:73] op_sel_hi:[1,0]\nv_pk_mul_f32 v[228:229], v[228:229], s[72:73] op_sel_hi:[1,0]\nv_pk_mul_f32 v[230:231], v[230:231], s[72:73] op_sel_hi:[1,0]\nQk 224,224,225\n"
"Qk 225,226,227\nQk 226,228,229\nQk 227,230,231\nv_accvgpr_write_b32 a72, v224\nv_accvgpr_write_b32 a73, v225\nv_accvgpr_write_b32 a74, v226\nv_accvgpr_write_b32 a75, v227\nv_pk_mul_f32 v[232:233], v[232:233], s[72:73] op_sel_hi:[1,0]\n"
"v_pk_mul_f32 v[234:235], v[234:235], s[72:73] op_sel_hi:[1,0]\nv_pk_mul_f32 v[236:237], v[236:237], s[72:73] op_sel_hi:[1,0]\nv_pk_mul_f32 v[238:239], v[238:239], s[72:73] op_sel_hi:[1,0]\nQk 232,232,233\nQk 233,234,235\nQk 234,236,237\nQk 235,238,239\nv_accvgpr_write_b32 a76, v232\n"
"v_accvgpr_write_b32 a77, v233\nv_accvgpr_write_b32 a78, v234\nv_accvgpr_write_b32 a79, v235\nv_pk_mul_f32 v[240:241], v[240:241], s[72:73] op_sel_hi:[1,0]\nv_pk_mul_f32 v[242:243], v[242:243], s[72:73] op_sel_hi:[1,0]\nv_pk_mul_f32 v[244:245], v[244:245], s[72:73] op_sel_hi:[1,0]\nv_pk_mul_f32 v[246:247], v[246:247], s[72:73] op_sel_hi:[1,0]\nQk 240,240,241\n"
"Qk 241,242,243\nQk 242,244,245\nQk 243,246,247\nv_accvgpr_write_b32 a80, v240\nv_accvgpr_write_b32 a81, v241\nv_accvgpr_write_b32 a82, v242\nv_accvgpr_write_b32 a83, v243\nv_pk_mul_f32 v[248:249], v[248:249], s[72:73] op_sel_hi:[1,0]\n"
"v_pk_mul_f32 v[250:251], v[250:251], s[72:73] op_sel_hi:[1,0]\nv_pk_mul_f32 v[252:253], v[252:253], s[72:73] op_sel_hi:[1,0]\nv_pk_mul_f32 v[254:255], v[254:255], s[72:73] op_sel_hi:[1,0]\nQk 248,248,249\nQk 249,250,251\nQk 250,252,253\nQk 251,254,255\nv_accvgpr_write_b32 a84, v248\n"
"v_accvgpr_write_b32 a85, v249\nv_accvgpr_write_b32 a86, v250\nv_accvgpr_write_b32 a87, v251\nv_accvgpr_read_b32 v0, a208\nv_accvgpr_read_b32 v1, a209\nv_accvgpr_read_b32 v2, a210\nv_accvgpr_read_b32 v3, a211\nv_accvgpr_read_b32 v4, a212\n"
"v_accvgpr_read_b32 v5, a213\nv_accvgpr_read_b32 v6, a214\nv_accvgpr_read_b32 v7, a215\nv_pk_mul_f32 v[0:1], v[0:1], s[72:73] op_sel_hi:[1,0]\nv_pk_mul_f32 v[2:3], v[2:3], s[72:73] op_sel_hi:[1,0]\nv_pk_mul_f32 v[4:5], v[4:5], s[72:73] op_sel_hi:[1,0]\nv_pk_mul_f32 v[6:7], v[6:7], s[72:73] op_sel_hi:[1,0]\nQk 0,0,1\n"
"Qk 1,2,3\nQk 2,4,5\nQk 3,6,7\nv_accvgpr_write_b32 a88, v0\nv_accvgpr_write_b32 a89, v1\nv_accvgpr_write_b32 a90, v2\nv_accvgpr_write_b32 a91, v3\nv_accvgpr_read_b32 v8, a216\n"
"v_accvgpr_read_b32 v9, a217\nv_accvgpr_read_b32 v10, a218\nv_accvgpr_read_b32 v11, a219\nv_accvgpr_read_b32 v12, a220\nv_accvgpr_read_b32 v13, a221\nv_accvgpr_read_b32 v14, a222\nv_accvgpr_read_b32 v15, a223\nv_pk_mul_f32 v[8:9], v[8:9], s[72:73] op_sel_hi:[1,0]\n"
"v_pk_mul_f32 v[10:11], v[10:11], s[72:73] op_sel_hi:[1,0]\nv_pk_mul_f32 v[12:13], v[12:13], s[72:73] op_sel_hi:[1,0]\nv_pk_mul_f32 v[14:15], v[14:15], s[72:73] op_sel_hi:[1,0]\nQk 8,8,9\nQk 9,10,11\nQk 10,12,13\nQk 11,14,15\nv_accvgpr_write_b32 a92, v8\n"
"v_accvgpr_write_b32 a93, v9\nv_accvgpr_write_b32 a94, v10\nv_accvgpr_write_b32 a95, v11\nv_accvgpr_read_b32 v0, a224\nv_accvgpr_read_b32 v1, a225\nv_accvgpr_read_b32 v2, a226\nv_accvgpr_read_b32 v3, a227\nv_accvgpr_read_b32 v4, a228\n"
"v_accvgpr_read_b32 v5, a229\nv_accvgpr_read_b32 v6, a230\nv_accvgpr_read_b32 v7, a231\nv_pk_mul_f32 v[0:1], v[0:1], s[72:73] op_sel_hi:[1,0]\nv_pk_mul_f32 v[2:3], v[2:3], s[72:73] op_sel_hi:[1,0]\nv_pk_mul_f32 v[4:5], v[4:5], s[72:73] op_sel_hi:[1,0]\nv_pk_mul_f32 v[6:7], v[6:7], s[72:73] op_sel_hi:[1,0]\nQk 0,0,1\n"
"Qk 1,2,3\nQk 2,4,5\nQk 3,6,7\nv_accvgpr_write_b32 a96, v0\nv_accvgpr_write_b32 a97, v1\nv_accvgpr_write_b32 a98, v2\nv_accvgpr_write_b32 a99, v3\nv_accvgpr_read_b32 v8, a232\n"
"v_accvgpr_read_b32 v9, a233\nv_accvgpr_read_b32 v10, a234\nv_accvgpr_read_b32 v11, a235\nv_accvgpr_read_b32 v12, a236\nv_accvgpr_read_b32 v13, a237\nv_accvgpr_read_b32 v14, a238\nv_accvgpr_read_b32 v15, a239\nv_pk_mul_f32 v[8:9], v[8:9], s[72:73] op_sel_hi:[1,0]\n"
"v_pk_mul_f32 v[10:11], v[10:11], s[72:73] op_sel_hi:[1,0]\nv_pk_mul_f32 v[12:13], v[12:13], s[72:73] op_sel_hi:[1,0]\nv_pk_mul_f32 v[14:15], v[14:15], s[72:73] op_sel_hi:[1,0]\nQk 8,8,9\nQk 9,10,11\nQk 10,12,13\nQk 11,14,15\nv_accvgpr_write_b32 a100, v8\n"
"v_accvgpr_write_b32 a101, v9\nv_accvgpr_write_b32 a102, v10\nv_accvgpr_write_b32 a103, v11\nv_accvgpr_read_b32 v0, a240\nv_accvgpr_read_b32 v1, a241\nv_accvgpr_read_b32 v2, a242\nv_accvgpr_read_b32 v3, a243\nv_accvgpr_read_b32 v4, a244\n"
"v_accvgpr_read_b32 v5, a245\nv_accvgpr_read_b32 v6, a246\nv_accvgpr_read_b32 v7, a247\nv_pk_mul_f32 v[0:1], v[0:1], s[72:73] op_sel_hi:[1,0]\nv_pk_mul_f32 v[2:3], v[2:3], s[72:73] op_sel_hi:[1,0]\nv_pk_mul_f32 v[4:5], v[4:5], s[72:73] op_sel_hi:[1,0]\nv_pk_mul_f32 v[6:7], v[6:7], s[72:73] op_sel_hi:[1,0]\nQk 0,0,1\n"
"Qk 1,2,3\nQk 2,4,5\nQk 3,6,7\nv_accvgpr_write_b32 a104, v0\nv_accvgpr_write_b32 a105, v1\nv_accvgpr_write_b32 a106, v2\nv_accvgpr_write_b32 a107, v3\nv_accvgpr_read_b32 v8, a248\n"
"v_accvgpr_read_b32 v9, a249\nv_accvgpr_read_b32 v10, a250\nv_accvgpr_read_b32 v11, a251\nv_accvgpr_read_b32 v12, a252\nv_accvgpr_read_b32 v13, a253\nv_accvgpr_read_b32 v14, a254\nv_accvgpr_read_b32 v15, a255\nv_pk_mul_f32 v[8:9], v[8:9], s[72:73] op_sel_hi:[1,0]\n"
"v_pk_mul_f32 v[10:11], v[10:11], s[72:73] op_sel_hi:[1,0]\nv_pk_mul_f32 v[12:13], v[12:13], s[72:73] op_sel_hi:[1,0]\nv_pk_mul_f32 v[14:15], v[14:15], s[72:73] op_sel_hi:[1,0]\nQk 8,8,9\nQk 9,10,11\nQk 10,12,13\nQk 11,14,15\nv_accvgpr_write_b32 a108, v8\n"
"v_accvgpr_write_b32 a109, v9\nv_accvgpr_write_b32 a110, v10\nv_accvgpr_write_b32 a111, v11\nv_mov_b32 v66, 0x3f803f80\nv_accvgpr_write_b32 a200, 0\nv_accvgpr_write_b32 a201, 0\nv_accvgpr_write_b32 a202, 0\nv_accvgpr_write_b32 a203, 0\n"
"s_mov_b32 vcc_lo, 0x33333333\ns_mov_b32 vcc_hi, 0xcccccccc\nv_cndmask_b32 v66, 0, v66, vcc\nv_accvgpr_write_b32 a204, v66\nv_accvgpr_write_b32 a205, v66\nv_accvgpr_write_b32 a206, v66\nv_accvgpr_write_b32 a207, v66\ns_add_u32 s87, s87, 2048\n"
"s_waitcnt lgkmcnt(8)\nQzvaa 0,112,0\nQmvaa 0,116,4\nQmvaa 0,120,8\nQmvaa 0,124,12\nQzvaa 4,128,0\nQmvaa 4,132,4\nQmvaa 4,136,8\n"
"Qmvaa 4,140,12\ns_nop 3\nv_max3_f32 v66, v0, v1, v2\ns_nop 2\nv_max3_f32 v66, v66, v3, v4\nv_max3_f32 v66, v66, v5, v6\nv_max_f32 v66, v66, v7\nQzvaa 8,112,16\n"
"v_mov_b32 v67, v66\nQmvaa 8,116,20\ns_nop 0\nv_permlane32_swap_b32 v66, v67\nv_max_f32 v66, v66, v67\nQmvaa 8,120,24\nv_mov_b32 v67, v66\nQmvaa 8,124,28\n"
"s_nop 0\nv_permlane16_swap_b32 v66, v67\nv_max_f32 v66, v66, v67\nQzvaa 12,128,16\nv_sub_f32 v56, 0xc1b00000, v66\nv_mov_b32 v57, v56\nQmvaa 12,132,20\nv_mov_b32 v58, v56\n"
"v_mov_b32 v59, v56\nQmvaa 12,136,24\nQa 0,0,56\nQa 1,1,56\nQmvaa 12,140,28\nQa 2,2,56\nQa 3,3,56\nQe 0\n"
"Qe 1\nQe 2\nQe 3\nQk 16,0,1\nQk 17,2,3\nQa 4,4,56\nQa 5,5,56\nQa 6,6,56\n"
"Qa 7,7,56\nQe 4\nQe 5\nQcvaa 0,112,32,56\nQe 6\nQmvaa 0,116,36\nQe 7\nQmvaa 0,120,40\n"
"Qk 18,4,5\nQk 19,6,7\nQmvaa 0,124,44\nQa 8,8,56\nQa 9,9,56\nQcvaa 4,128,32,56\nQa 10,10,56\nQa 11,11,56\n"
"Qmvaa 4,132,36\nQe 8\nQmvaa 4,136,40\nQe 9\nQmvaa 4,140,44\ns_waitcnt lgkmcnt(0)\nQk 20,8,9\nQzvav 96,148,16\n"
"Qe 10\nQzvav 100,156,16\nQe 11\nQzvav 104,164,16\nQk 21,10,11\nQa 12,12,56\nQzvav 108,172,16\nQa 13,13,56\n"
"Qa 14,14,56\nQcvaa 8,112,48,56\nQa 15,15,56\nQmvaa 8,116,52\nQe 12\nQmvaa 8,120,56\nQe 13\nQe 14\n"
"Qe 15\nQmvaa 8,124,60\nQk 22,12,13\nQk 23,14,15\nQcvaa 12,128,48,56\nQe 0\nQmvaa 12,132,52\nQe 1\n"
"v_smfmac_f32_16x16x64_bf16 a[176:179], a[204:207], v[16:23], v77\nQk 24,0,1\nQmvaa 12,136,56\nQe 2\nQmvaa 12,140,60\nQe 3\ns_add_u32 m0, s61, 99072\nQzvav 128,148,20\n"
"buffer_load_dwordx4 v69, s[40:43], s90 offen nt lds\nQzvav 132,156,20\nQk 25,2,3\nQzvav 136,164,20\nQe 4\nbuffer_load_dwordx4 v69, s[40:43], s60 offen offset:1024 nt lds\nQzvav 140,172,20\nQe 5\n"
"Qcvaa 0,112,64,56\nQe 6\nQmvaa 0,116,68\nQe 7\nQmvaa 0,120,72\nQk 26,4,5\nQk 27,6,7\nQmvaa 0,124,76\n"
"buffer_load_dwordx4 v69, s[40:43], s90 offen offset:2048 nt lds\nQcvaa 4,128,64,56\nQe 8\nQmvaa 4,132,68\nQe 9\nQmvaa 4,136,72\nQe 10\nQmvaa 4,140,76\n"
"Qe 11\nbuffer_load_dwordx4 v69, s[40:43], s60 offen offset:3072 nt lds\nQzvav 160,148,24\nQk 28,8,9\nQk 29,10,11\nQzvav 164,156,24\nQe 12\ns_add_u32 m0, s61, 107264\n"
"Qzvav 168,164,24\nbuffer_load_dwordx4 v72, s[44:47], s90 offen nt lds\nQzvav 172,172,24\nQe 13\nQcvaa 8,112,80,56\nQe 14\nQmvaa 8,116,84\nQe 15\n"
"Qmvaa 8,120,88\nbuffer_load_dwordx4 v72, s[44:47], s60 offen offset:1024 nt lds\nQmvaa 8,124,92\nQk 30,12,13\nQk 31,14,15\nQcvaa 12,128,80,56\nQe 0\nQmvaa 12,132,84\n"
"Qe 1\nv_smfmac_f32_16x16x64_bf16 a[180:183], a[204:207], v[24:31], v77\nQk 32,0,1\nQmvaa 12,136,88\nbuffer_load_dwordx4 v72, s[44:47], s90 offen offset:2048 nt lds\nQmvaa 12,140,92\nQe 2\nQzvav 192,148,28\n"
"Qe 3\ns_waitcnt vmcnt(15)\ns_barrier\nQzvav 196,156,28\nbuffer_load_dwordx4 v72, s[44:47], s60 offen offset:3072 nt lds\nQzvav 200,164,28\nQk 33,2,3\ns_add_u32 s60, s60, 8192\n"
"s_and_b32 s68, s60, 0x40000\ns_mul_i32 s68, s68, 7\ns_add_u32 s60, s60, s68\ns_add_u32 s90, s60, 896\nQzvav 204,172,28\nQe 4\nQcvaa 0,112,96,56\nQda 112,68,33024\n"
"Qmvaa 0,116,100\nQda 116,68,33536\nQmvaa 0,120,104\nQda 120,68,34048\nQe 5\nQe 6\nQe 7\nQmvaa 0,124,108\n"
"Qda 124,68,34560\nQk 34,4,5\nQk 35,6,7\nQcvaa 4,128,96,56\nQda 128,68,35072\nQmvaa 4,132,100\nQda 132,68,35584\nQmvaa 4,136,104\n"
"Qda 136,68,36096\nQmvaa 4,140,108\nQda 140,68,36608\nQzvav 224,148,32\ns_memrealtime s[74:75]\nQzvav 228,156,32\nQe 8\nQzvav 232,164,32\n"
"Qe 9\nQe 10\nQe 11\nQzvav 236,172,32\ns_waitcnt lgkmcnt(0)\nQk 36,8,9\nQk 37,10,11\nQcvaa 8,112,0,56\n"
"Qe 12\nQmvaa 8,116,4\nv_add_u32 v68, 66048, v68\nQmvaa 8,120,8\nQe 13\nQe 14\nQe 15\nQmvaa 8,124,12\n"
"Qk 38,12,13\nQk 39,14,15\nQcvaa 12,128,0,56\nQe 0\nQmvaa 12,132,4\nQe 1\nv_smfmac_f32_16x16x64_bf16 a[184:187], a[204:207], v[32:39], v77\nQk 40,0,1\n"
"Qmvaa 12,136,8\nQe 2\nQmvaa 12,140,12\nQe 3\ns_waitcnt lgkmcnt(0)\nQzaav 208,148,36\ns_mov_b32 s73, 140\nQk 41,2,3\n"
"s_add_u32 s77, s74, s73\nQzaav 212,156,36\ns_sub_u32 s73, s73, 4\ns_max_u32 s73, s73, 120\nQzaav 216,164,36\nQe 4\nQzaav 220,172,36\nQe 8\n"
"Qcvaa 0,112,16,56\nQe 9\nQmvaa 0,116,20\nQe 10\nQmvaa 0,120,24\nQe 11\nQe 5\nQe 6\n"
"Qe 7\nQmvaa 0,124,28\nQk 44,8,9\nQk 45,10,11\nQk 42,4,5\nQk 43,6,7\nQcvaa 4,128,16,56\nQe 12\n"
"Qmvaa 4,132,20\nQe 13\nQmvaa 4,136,24\nQe 14\nQmvaa 4,140,28\nQe 15\nQta 144,70,0\nQta 146,70,2048\n"
"Qzaav 240,148,40\nQta 148,70,33024\nQta 150,70,35072\nQta 152,70,256\nQta 154,70,2304\nQzaav 244,156,40\nQta 156,70,33280\nQta 158,70,35328\n"
"Qta 160,70,512\nQta 162,70,2560\nQzaav 248,164,40\nQta 164,70,33536\nQta 166,70,35584\nQta 168,70,768\nQta 170,70,2816\nQzaav 252,172,40\n"
"Qta 172,70,33792\nQta 174,70,35840\nQcvaa 8,112,32,56\nv_add_u32 v75, 1, v75\nQk 46,12,13\nQmvaa 8,116,36\ns_waitcnt vmcnt(8)\nds_write_b32 v73, v75\n"
"Qmvaa 8,120,40\nQk 47,14,15\nQmvaa 8,124,44\nQe 0\nQcvaa 12,128,32,56\nQe 1\nQmvaa 12,132,36\nQe 2\n"
"v_smfmac_f32_16x16x64_bf16 a[188:191], a[204:207], v[40:47], v77\nQk 48,0,1\nQmvaa 12,136,40\ns_waitcnt lgkmcnt(1)\nQmvaa 12,140,44\nQe 3\nQzvav 80,144,16\nQk 49,2,3\n"
"Qmvav 80,148,44\nQe 4\nQzvav 84,152,16\nQe 5\nQmvav 84,156,44\nQe 6\nQzvav 88,160,16\nQe 7\n"
"Qmvav 88,164,44\nQk 50,4,5\nQk 51,6,7\nQzvav 92,168,16\nQe 8\nQmvav 92,172,44\nQe 9\nQcvaa 0,112,48,56\n"
"Qe 10\nQmvaa 0,116,52\nds_read_b32 v66, v74\nQk 52,8,9\nQmvaa 0,120,56\nQe 11\nQmvaa 0,124,60\nQk 53,10,11\n"
"Qcvaa 4,128,48,56\nQe 12\nQmvaa 4,132,52\nQe 13\nQmvaa 4,136,56\ns_waitcnt lgkmcnt(0)\nv_cmp_gt_u32 vcc, v75, v66\nQmvaa 4,140,60\n"
"s_cbranch_vccnz .Lsa1\n.Lra1:\nQk 54,12,13\nQzvav 112,144,20\ns_add_u32 m0, s61, 0\nQmvav 112,148,48\nbuffer_load_dwordx4 v69, s[40:43], s90 offen nt lds\nQzvav 116,152,20\n"
"Qe 14\nQmvav 116,156,48\nQe 15\nQzvav 120,160,20\nQk 55,14,15\nQmvav 120,164,48\nQe 0\nQzvav 124,168,20\n"
"buffer_load_dwordx4 v69, s[40:43], s60 offen offset:1024 nt lds\nQmvav 124,172,48\nQe 1\nQcvaa 8,112,64,56\nQe 2\nQmvaa 8,116,68\nQe 3\nQmvaa 8,120,72\n"
"Qk 20,0,1\nQk 21,2,3\nQmvaa 8,124,76\nbuffer_load_dwordx4 v69, s[40:43], s90 offen offset:2048 nt lds\nQcvaa 12,128,64,56\nQe 4\nQmvaa 12,132,68\nQe 5\n"
"v_smfmac_f32_16x16x64_bf16 a[192:195], a[204:207], v[48:55], v77\nQk 22,4,5\nQmvaa 12,136,72\nQe 6\nQmvaa 12,140,76\nbuffer_load_dwordx4 v69, s[40:43], s60 offen offset:3072 nt lds\nQzvav 144,144,24\nQe 7\n"
"Qmvav 144,148,52\nQk 23,6,7\nQzvav 148,152,24\nQe 8\nQmvav 148,156,52\ns_add_u32 m0, s61, 8192\nQzvav 152,160,24\nbuffer_load_dwordx4 v72, s[44:47], s90 offen nt lds\n"
"Qmvav 152,164,52\nQe 9\nQzvav 156,168,24\n.Lpg5:\ns_cbranch_execz .Ltr9\nQe 10\nQmvav 156,172,52\nQe 11\n"
"Qcvaa 0,112,80,56\nQk 24,8,9\nQk 25,10,11\nQmvaa 0,116,84\nbuffer_load_dwordx4 v72, s[44:47], s60 offen offset:1024 nt lds\nQmvaa 0,120,88\nQe 12\nQmvaa 0,124,92\n"
"Qe 13\nQcvaa 4,128,80,56\nQe 14\nQmvaa 4,132,84\nQe 15\nQmvaa 4,136,88\nbuffer_load_dwordx4 v72, s[44:47], s90 offen offset:2048 nt lds\nQmvaa 4,140,92\n"
"Qk 26,12,13\nQk 27,14,15\nQzvav 176,144,28\nQmvav 176,148,20\nQzvav 180,152,28\nQmvav 180,156,20\nbuffer_load_dwordx4 v72, s[44:47], s60 offen offset:3072 nt lds\nQzvav 184,160,28\n"
"Qe 0\nQmvav 184,164,20\nQe 1\nQzvav 188,168,28\ns_add_u32 s60, s60, 8192\ns_add_u32 s90, s60, 896\nQmvav 188,172,20\nQe 2\n"
"Qcvaa 8,112,96,56\nQda 112,68,0\nQk 28,0,1\nQmvaa 8,116,100\nQda 116,68,512\nQmvaa 8,120,104\nQda 120,68,1024\nQmvaa 8,124,108\n"
"Qda 124,68,1536\nQcvaa 12,128,96,56\nQda 128,68,2048\nQmvaa 12,132,100\nQda 132,68,2560\nv_smfmac_f32_16x16x64_bf16 a[196:199], a[204:207], v[20:27], v77\nQmvaa 12,136,104\nQda 136,68,3072\n"
"Qmvaa 12,140,108\nQda 140,68,3584\nQzvav 208,144,32\ns_memrealtime s[74:75]\nQmvav 208,148,24\nQe 3\nQzvav 212,152,32\nQk 29,2,3\n"
"Qmvav 212,156,24\nQe 4\nQzvav 216,160,32\nQe 5\nQmvav 216,164,24\nQe 6\nQzvav 220,168,32\nQe 7\n"
"Qmvav 220,172,24\ns_waitcnt lgkmcnt(0)\nQk 30,4,5\nQcvaa 0,112,0,56\nQk 31,6,7\nQmvaa 0,116,4\nQe 8\nQmvaa 0,120,8\n"
"Qe 9\nQmvaa 0,124,12\nQe 10\nQcvaa 4,128,0,56\nQe 11\nQmvaa 4,132,4\nQk 32,8,9\nQk 33,10,11\n"
"Qmvaa 4,136,8\nQe 12\nQmvaa 4,140,12\nQe 13\nQzvav 240,144,36\nQe 14\nQmvav 240,148,28\ns_waitcnt lgkmcnt(0)\n"
"s_sub_u32 s75, s77, s74\nQzvav 244,152,36\ns_cmp_lt_u32 s75, 256\ns_cbranch_scc1 .Lsp1\n.Lrp1:\nQmvav 244,156,28\ns_add_u32 s77, s74, s73\nQk 34,12,13\n"
"Qzvav 248,160,36\ns_sub_u32 s73, s73, 4\nQmvav 248,164,28\ns_max_u32 s73, s73, 120\nQzvav 252,168,36\nQe 15\nQmvav 252,172,28\nQk 35,14,15\n"
"Qcvaa 8,112,16,56\nQe 0\nQmvaa 8,116,20\nQe 1\nQmvaa 8,120,24\nQe 2\nQmvaa 8,124,28\nQe 3\n"
"Qcvaa 12,128,16,56\nQk 36,0,1\nQk 37,2,3\nQmvaa 12,132,20\nQe 4\nv_smfmac_f32_16x16x64_bf16 a[200:203], a[204:207], v[28:35], v77\nQmvaa 12,136,24\nQe 5\n"
"Qmvaa 12,140,28\nQe 6\nQzaav 224,144,40\nQta 144,70,34048\nQta 146,70,36096\nQmaav 224,148,32\nQta 148,71,1024\nQta 150,71,3072\n"
"Qzaav 228,152,40\nQta 152,70,34304\nQta 154,70,36352\nQmaav 228,156,32\nQta 156,71,1280\nQta 158,71,3328\nQzaav 232,160,40\nQta 160,70,34560\n"
"Qta 162,70,36608\nQmaav 232,164,32\nQta 164,71,1536\nQta 166,71,3584\nQzaav 236,168,40\nQta 168,70,34816\nQta 170,70,36864\nQmaav 236,172,32\n"
"Qta 172,71,1792\nQta 174,71,3840\ns_lshl_b32 s66, s67, 3\n.Lloop:\ns_bitcmp1_b32 s99, s66\ns_cbranch_scc1 .Lhook\n.Lhookret:\nQcvaa 0,112,32,56\n"
"v_add_u32 v75, 1, v75\nQk 38,4,5\nQmvaa 0,116,36\ns_waitcnt vmcnt(8)\nds_write_b32 v73, v75\nQmvaa 0,120,40\nQe 7\nQmvaa 0,124,44\n"
"Qk 39,6,7\nQcvaa 4,128,32,56\nQe 8\nQmvaa 4,132,36\nQe 9\nQmvaa 4,136,40\nQe 10\nQmvaa 4,140,44\n"
"s_waitcnt lgkmcnt(1)\nQk 40,8,9\nQmvav 96,144,44\nQe 11\nQmvav 96,148,36\nQk 41,10,11\nQmvav 100,152,44\nQe 12\n"
"Qmvav 100,156,36\nQe 13\nQmvav 104,160,44\nQe 14\nQmvav 104,164,36\nQe 15\nQmvav 108,168,44\nQk 42,12,13\n"
"Qk 43,14,15\nQmvav 108,172,36\nQe 0\nQcvaa 8,112,48,56\nQe 1\nQmvaa 8,116,52\nQe 2\nQmvaa 8,120,56\n"
"ds_read_b32 v66, v74\nQk 44,0,1\nQmvaa 8,124,60\nQe 3\nQcvaa 12,128,48,56\nQk 45,2,3\nQmvaa 12,132,52\nQe 4\n"
"v_smfmac_f32_16x16x64_bf16 a[176:179], a[204:207], v[36:43], v77\nQmvaa 12,136,56\ns_waitcnt lgkmcnt(0)\nv_cmp_gt_u32 vcc, v75, v66\nQmvaa 12,140,60\ns_cbranch_vccnz .Lsa2\n.Lra2:\nQmvav 128,144,48\n"
"s_add_u32 m0, s61, 33024\nQmvav 128,148,40\nbuffer_load_dwordx4 v69, s[40:43], s90 offen nt lds\nQmvav 132,152,48\nQe 5\nQmvav 132,156,40\nQe 6\nQmvav 136,160,48\n"
"Qe 7\nQmvav 136,164,40\nQk 46,4,5\nQk 47,6,7\nQmvav 140,168,48\nbuffer_load_dwordx4 v69, s[40:43], s60 offen offset:1024 nt lds\nQmvav 140,172,40\nQe 8\n"
"Qcvaa 0,112,64,56\nQe 9\nQmvaa 0,116,68\nQe 10\nQmvaa 0,120,72\nQe 11\nQmvaa 0,124,76\nbuffer_load_dwordx4 v69, s[40:43], s90 offen offset:2048 nt lds\n"
"Qcvaa 4,128,64,56\nQk 48,8,9\nQk 49,10,11\nQmvaa 4,132,68\nQe 12\nQmvaa 4,136,72\nQe 13\nQmvaa 4,140,76\n"
"Qe 14\nQmvav 160,144,52\nbuffer_load_dwordx4 v69, s[40:43], s60 offen offset:3072 nt lds\nQmvav 160,148,44\nQe 15\nQmvav 164,152,52\nQk 50,12,13\nQk 51,14,15\n"
"Qmvav 164,156,44\nQe 0\nQmvav 168,160,52\ns_add_u32 m0, s61, 41216\nQmvav 168,164,44\nbuffer_load_dwordx4 v72, s[44:47], s90 offen nt lds\nQmvav 172,168,52\nQe 1\n"
"Qmvav 172,172,44\nQe 2\nQcvaa 8,112,80,56\nQe 3\nQmvaa 8,116,84\nQk 16,0,1\nQk 17,2,3\nQmvaa 8,120,88\n"
"buffer_load_dwordx4 v72, s[44:47], s60 offen offset:1024 nt lds\nQmvaa 8,124,92\nQe 4\nQcvaa 12,128,80,56\nQe 5\nQmvaa 12,132,84\nQe 6\nv_smfmac_f32_16x16x64_bf16 a[180:183], a[204:207], v[44:51], v77\n"
"Qk 18,4,5\nQmvaa 12,136,88\nbuffer_load_dwordx4 v72, s[44:47], s90 offen offset:2048 nt lds\nQmvaa 12,140,92\nQe 7\nQmvav 192,144,20\nQk 19,6,7\nQmvav 192,148,48\n"
"Qmvav 196,152,20\nQe 8\nQmvav 196,156,48\nbuffer_load_dwordx4 v72, s[44:47], s60 offen offset:3072 nt lds\nQmvav 200,160,20\nQe 9\nQmvav 200,164,48\nQe 10\n"
"Qmvav 204,168,20\ns_add_u32 s60, s60, 8192\ns_add_u32 s90, s60, 896\nQmvav 204,172,48\nQe 11\nQcvaa 0,112,96,56\nQda 112,68,33024\nQk 20,8,9\n"
"Qmvaa 0,116,100\nQda 116,68,33536\nQk 21,10,11\nQmvaa 0,120,104\nQda 120,68,34048\nQmvaa 0,124,108\nQda 124,68,34560\nQcvaa 4,128,96,56\n"
"Qda 128,68,35072\nQmvaa 4,132,100\nQda 132,68,35584\nQmvaa 4,136,104\nQda 136,68,36096\nQmvaa 4,140,108\nQda 140,68,36608\nQmvav 224,144,24\n"
"Qe 12\nQmvav 224,148,16\ns_memrealtime s[74:75]\nQmvav 228,152,24\nQe 13\nQmvav 228,156,16\nQe 14\nQmvav 232,160,24\n"
"Qe 15\nQmvav 232,164,16\nQk 22,12,13\nQk 23,14,15\nQmvav 236,168,24\nQe 0\nQmvav 236,172,16\ns_waitcnt lgkmcnt(0)\n"
"Qcvaa 8,112,0,56\nQe 1\nQmvaa 8,116,4\nv_subrev_u32 v68, 66048, v68\nQk 24,0,1\nQmvaa 8,120,8\nQe 2\nQmvaa 8,124,12\n"
"Qe 3\nQcvaa 12,128,0,56\nQk 25,2,3\nQmvaa 12,132,4\nQe 4\nv_smfmac_f32_16x16x64_bf16 a[184:187], a[204:207], v[16:23], v77\nQmvaa 12,136,8\nQe 5\n"
"Qmvaa 12,140,12\nQe 6\nQmaav 208,144,28\ns_waitcnt lgkmcnt(0)\ns_sub_u32 s75, s77, s74\nQmaav 208,148,20\ns_cmp_lt_u32 s75, 256\ns_cbranch_scc1 .Lsp2\n"
".Lrp2:\nQmaav 212,152,28\ns_add_u32 s77, s74, s73\nQk 26,4,5\nQmaav 212,156,20\ns_sub_u32 s73, s73, 4\nQmaav 216,160,28\ns_max_u32 s73, s73, 120\n"
"Qmaav 216,164,20\nQe 7\nQmaav 220,168,28\nQk 27,6,7\nQmaav 220,172,20\nQe 8\nQcvaa 0,112,16,56\nQe 9\n"
"Qmvaa 0,116,20\nQe 10\nQmvaa 0,120,24\nQe 11\nQmvaa 0,124,28\nQk 28,8,9\nQk 29,10,11\nQcvaa 4,128,16,56\n"
"Qe 12\nQmvaa 4,132,20\nQe 13\nQmvaa 4,136,24\nQe 14\nQmvaa 4,140,28\nQe 15\nQmaav 240,144,32\n"
"Qta 144,71,0\nQta 146,71,2048\nQmaav 240,148,24\nQta 148,71,33024\nQta 150,71,35072\nQmaav 244,152,32\nQta 152,71,256\nQta 154,71,2304\n"
"Qmaav 244,156,24\nQta 156,71,33280\nQta 158,71,35328\nQmaav 248,160,32\nQta 160,71,512\nQta 162,71,2560\nQmaav 248,164,24\nQta 164,71,33536\n"
"Qta 166,71,35584\nQmaav 252,168,32\nQta 168,71,768\nQta 170,71,2816\nQmaav 252,172,24\nQta 172,71,33792\nQta 174,71,35840\nQcvaa 8,112,32,56\n"
"v_add_u32 v75, 1, v75\nQk 30,12,13\nQmvaa 8,116,36\ns_waitcnt vmcnt(8)\nds_write_b32 v73, v75\nQmvaa 8,120,40\nQk 31,14,15\nQmvaa 8,124,44\n"
"Qe 0\nQcvaa 12,128,32,56\nQe 1\nQmvaa 12,132,36\nQe 2\nv_smfmac_f32_16x16x64_bf16 a[188:191], a[204:207], v[24:31], v77\nQk 32,0,1\nQmvaa 12,136,40\n"
"s_waitcnt lgkmcnt(1)\nQmvaa 12,140,44\nQe 3\nQmvav 80,144,36\nQk 33,2,3\nQmvav 80,148,28\nQe 4\nQmvav 84,152,36\n"
"Qe 5\nQmvav 84,156,28\nQe 6\nQmvav 88,160,36\nQe 7\nQmvav 88,164,28\nQk 34,4,5\nQk 35,6,7\n"
"Qmvav 92,168,36\nQe 8\nQmvav 92,172,28\nQe 9\nQcvaa 0,112,48,56\nQe 10\nQmvaa 0,116,52\nds_read_b32 v66, v74\n"
"Qk 36,8,9\nQmvaa 0,120,56\nQe 11\nQmvaa 0,124,60\nQk 37,10,11\nQcvaa 4,128,48,56\nQe 12\nQmvaa 4,132,52\n"
"Qe 13\nQmvaa 4,136,56\ns_waitcnt lgkmcnt(0)\nv_cmp_gt_u32 vcc, v75, v66\nQmvaa 4,140,60\ns_cbranch_vccnz .Lsa3\n.Lra3:\nQk 38,12,13\n"
"Qmvav 112,144,40\ns_add_u32 m0, s61, 66048\nQmvav 112,148,32\nbuffer_load_dwordx4 v69, s[40:43], s90 offen nt lds\nQmvav 116,152,40\nQe 14\nQmvav 116,156,32\nQe 15\n"
"Qmvav 120,160,40\nQk 39,14,15\nQmvav 120,164,32\nQe 0\nQmvav 124,168,40\nbuffer_load_dwordx4 v69, s[40:43], s60 offen offset:1024 nt lds\nQmvav 124,172,32\nQe 1\n"
"Qcvaa 8,112,64,56\nQe 2\nQmvaa 8,116,68\nQe 3\nQmvaa 8,120,72\nQk 40,0,1\nQk 41,2,3\nQmvaa 8,124,76\n"
"buffer_load_dwordx4 v69, s[40:43], s90 offen offset:2048 nt lds\nQcvaa 12,128,64,56\nQe 4\nQmvaa 12,132,68\nQe 5\nv_smfmac_f32_16x16x64_bf16 a[192:195], a[204:207], v[32:39], v77\nQk 42,4,5\nQmvaa 12,136,72\n"
"Qe 6\nQmvaa 12,140,76\nbuffer_load_dwordx4 v69, s[40:43], s60 offen offset:3072 nt lds\nQmvav 144,144,44\nQe 7\nQmvav 144,148,36\nQk 43,6,7\nQmvav 148,152,44\n"
"Qe 8\nQmvav 148,156,36\ns_add_u32 m0, s61, 74240\nQmvav 152,160,44\nbuffer_load_dwordx4 v72, s[44:47], s90 offen nt lds\nQmvav 152,164,36\nQe 9\nQmvav 156,168,44\n"
"Qe 10\nQmvav 156,172,36\nQe 11\nQcvaa 0,112,80,56\nQk 44,8,9\nQk 45,10,11\nQmvaa 0,116,84\nbuffer_load_dwordx4 v72, s[44:47], s60 offen offset:1024 nt lds\n"
"Qmvaa 0,120,88\nQe 12\nQmvaa 0,124,92\nQe 13\nQcvaa 4,128,80,56\nQe 14\nQmvaa 4,132,84\nQe 15\n"
"Qmvaa 4,136,88\nbuffer_load_dwordx4 v72, s[44:47], s90 offen offset:2048 nt lds\nQmvaa 4,140,92\nQk 46,12,13\nQk 47,14,15\nQmvav 176,144,48\nQmvav 176,148,40\nQmvav 180,152,48\n"
"Qmvav 180,156,40\nbuffer_load_dwordx4 v72, s[44:47], s60 offen offset:3072 nt lds\nQmvav 184,160,48\nQe 0\nQmvav 184,164,40\nQe 1\nQmvav 188,168,48\ns_add_u32 s60, s60, 8192\n"
"s_add_u32 s90, s60, 896\nQmvav 188,172,40\nQe 2\nQcvaa 8,112,96,56\nQda 112,68,0\nQk 48,0,1\nQmvaa 8,116,100\nQda 116,68,512\n"
"Qmvaa 8,120,104\nQda 120,68,1024\nQmvaa 8,124,108\nQda 124,68,1536\nQcvaa 12,128,96,56\nQda 128,68,2048\nQmvaa 12,132,100\nQda 132,68,2560\n"
"v_smfmac_f32_16x16x64_bf16 a[196:199], a[204:207], v[40:47], v77\nQmvaa 12,136,104\nQda 136,68,3072\nQmvaa 12,140,108\nQda 140,68,3584\nQmvav 208,144,16\ns_memrealtime s[74:75]\nQmvav 208,148,44\n"
"Qe 3\nQmvav 212,152,16\nQk 49,2,3\nQmvav 212,156,44\nQe 4\nQmvav 216,160,16\nQe 5\nQmvav 216,164,44\n"
"Qe 6\nQmvav 220,168,16\nQe 7\nQmvav 220,172,44\ns_waitcnt lgkmcnt(0)\nQk 50,4,5\nQcvaa 0,112,0,56\nQk 51,6,7\n"
"Qmvaa 0,116,4\nQe 8\nQmvaa 0,120,8\nQe 9\nQmvaa 0,124,12\nQe 10\nQcvaa 4,128,0,56\nQe 11\n"
"Qmvaa 4,132,4\n.Lpg6:\ns_cbranch_execz .Ltr9\nQk 52,8,9\nQk 53,10,11\nQmvaa 4,136,8\nQe 12\nQmvaa 4,140,12\n"
"Qe 13\nQmvav 240,144,20\nQe 14\nQmvav 240,148,48\ns_waitcnt lgkmcnt(0)\ns_sub_u32 s75, s77, s74\nQmvav 244,152,20\ns_cmp_lt_u32 s75, 256\n"
"s_cbranch_scc1 .Lsp3\n.Lrp3:\nQmvav 244,156,48\ns_add_u32 s77, s74, s73\nQk 54,12,13\nQmvav 248,160,20\ns_sub_u32 s73, s73, 4\nQmvav 248,164,48\n"
"s_max_u32 s73, s73, 120\nQmvav 252,168,20\nQe 15\nQmvav 252,172,48\nQk 55,14,15\nQcvaa 8,112,16,56\nQe 0\nQmvaa 8,116,20\n"
"Qe 1\nQmvaa 8,120,24\nQe 2\nQmvaa 8,124,28\nQe 3\nQcvaa 12,128,16,56\nQk 16,0,1\nQk 17,2,3\n"
"Qmvaa 12,132,20\nQe 4\nv_smfmac_f32_16x16x64_bf16 a[200:203], a[204:207], v[48:55], v77\nQmvaa 12,136,24\nQe 5\nQmvaa 12,140,28\nQe 6\nQmaav 224,144,24\n"
"Qta 144,71,34048\nQta 146,71,36096\nQmaav 224,148,52\nQta 148,70,1024\nQta 150,70,3072\nQmaav 228,152,24\nQta 152,71,34304\nQta 154,71,36352\n"
"Qmaav 228,156,52\nQta 156,70,1280\nQta 158,70,3328\nQmaav 232,160,24\nQta 160,71,34560\nQta 162,71,36608\nQmaav 232,164,52\nQta 164,70,1536\n"
"Qta 166,70,3584\nQmaav 236,168,24\nQta 168,71,34816\nQta 170,71,36864\nQmaav 236,172,52\nQta 172,70,1792\nQta 174,70,3840\nQcvaa 0,112,32,56\n"
"v_add_u32 v75, 1, v75\nQk 18,4,5\nQmvaa 0,116,36\ns_waitcnt vmcnt(8)\nds_write_b32 v73, v75\nQmvaa 0,120,40\nQe 7\nQmvaa 0,124,44\n"
"Qk 19,6,7\nQcvaa 4,128,32,56\nQe 8\nQmvaa 4,132,36\nQe 9\nQmvaa 4,136,40\nQe 10\nQmvaa 4,140,44\n"
"s_waitcnt lgkmcnt(1)\nQk 20,8,9\nQmvav 96,144,28\nQe 11\nQmvav 96,148,16\nQk 21,10,11\nQmvav 100,152,28\nQe 12\n"
"Qmvav 100,156,16\nQe 13\nQmvav 104,160,28\nQe 14\nQmvav 104,164,16\nQe 15\nQmvav 108,168,28\nQk 22,12,13\n"
"Qk 23,14,15\nQmvav 108,172,16\nQe 0\nQcvaa 8,112,48,56\nQe 1\nQmvaa 8,116,52\nQe 2\nQmvaa 8,120,56\n"
"ds_read_b32 v66, v74\nQk 24,0,1\nQmvaa 8,124,60\nQe 3\nQcvaa 12,128,48,56\nQk 25,2,3\nQmvaa 12,132,52\nQe 4\n"
"v_smfmac_f32_16x16x64_bf16 a[176:179], a[204:207], v[16:23], v77\nQmvaa 12,136,56\ns_waitcnt lgkmcnt(0)\nv_cmp_gt_u32 vcc, v75, v66\nQmvaa 12,140,60\ns_cbranch_vccnz .Lsa4\n.Lra4:\nQmvav 128,144,32\n"
"s_add_u32 m0, s61, 99072\nQmvav 128,148,20\nbuffer_load_dwordx4 v69, s[40:43], s90 offen nt lds\nQmvav 132,152,32\nQe 5\nQmvav 132,156,20\nQe 6\nQmvav 136,160,32\n"
"Qe 7\nQmvav 136,164,20\nQk 26,4,5\nQk 27,6,7\nQmvav 140,168,32\nbuffer_load_dwordx4 v69, s[40:43], s60 offen offset:1024 nt lds\nQmvav 140,172,20\nQe 8\n"
"Qcvaa 0,112,64,56\nQe 9\nQmvaa 0,116,68\nQe 10\nQmvaa 0,120,72\nQe 11\nQmvaa 0,124,76\nbuffer_load_dwordx4 v69, s[40:43], s90 offen offset:2048 nt lds\n"
"Qcvaa 4,128,64,56\nQk 28,8,9\nQk 29,10,11\nQmvaa 4,132,68\nQe 12\nQmvaa 4,136,72\nQe 13\nQmvaa 4,140,76\n"
"Qe 14\nQmvav 160,144,36\nbuffer_load_dwordx4 v69, s[40:43], s60 offen offset:3072 nt lds\nQmvav 160,148,24\nQe 15\nQmvav 164,152,36\nQk 30,12,13\nQk 31,14,15\n"
"Qmvav 164,156,24\nQe 0\nQmvav 168,160,36\ns_add_u32 m0, s61, 107264\nQmvav 168,164,24\nbuffer_load_dwordx4 v72, s[44:47], s90 offen nt lds\nQmvav 172,168,36\nQe 1\n"
"Qmvav 172,172,24\nQe 2\nQcvaa 8,112,80,56\nQe 3\nQmvaa 8,116,84\nQk 32,0,1\nQk 33,2,3\nQmvaa 8,120,88\n"
"buffer_load_dwordx4 v72, s[44:47], s60 offen offset:1024 nt lds\nQmvaa 8,124,92\nQe 4\nQcvaa 12,128,80,56\nQe 5\nQmvaa 12,132,84\nQe 6\nv_smfmac_f32_16x16x64_bf16 a[180:183], a[204:207], v[24:31], v77\n"
"Qk 34,4,5\nQmvaa 12,136,88\nbuffer_load_dwordx4 v72, s[44:47], s90 offen offset:2048 nt lds\nQmvaa 12,140,92\nQe 7\nQmvav 192,144,40\nQk 35,6,7\nQmvav 192,148,28\n"
"Qmvav 196,152,40\nQe 8\nQmvav 196,156,28\nbuffer_load_dwordx4 v72, s[44:47], s60 offen offset:3072 nt lds\nQmvav 200,160,40\nQe 9\nQmvav 200,164,28\nQe 10\n"
"Qmvav 204,168,40\ns_add_u32 s60, s60, 8192\ns_and_b32 s68, s60, 0x40000\ns_mul_i32 s68, s68, 7\ns_add_u32 s60, s60, s68\ns_add_u32 s90, s60, 896\nQmvav 204,172,28\nQe 11\n"
"Qcvaa 0,112,96,56\nQda 112,68,33024\nQk 36,8,9\nQmvaa 0,116,100\nQda 116,68,33536\nQk 37,10,11\nQmvaa 0,120,104\nQda 120,68,34048\n"
"Qmvaa 0,124,108\nQda 124,68,34560\nQcvaa 4,128,96,56\nQda 128,68,35072\nQmvaa 4,132,100\nQda 132,68,35584\nQmvaa 4,136,104\nQda 136,68,36096\n"
"Qmvaa 4,140,108\nQda 140,68,36608\nQmvav 224,144,44\nQe 12\nQmvav 224,148,32\ns_memrealtime s[74:75]\nQmvav 228,152,44\nQe 13\n"
"Qmvav 228,156,32\nQe 14\nQmvav 232,160,44\nQe 15\nQmvav 232,164,32\nQk 38,12,13\nQk 39,14,15\nQmvav 236,168,44\n"
"Qe 0\nQmvav 236,172,32\ns_waitcnt lgkmcnt(0)\nQcvaa 8,112,0,56\nQe 1\nQmvaa 8,116,4\nv_add_u32 v68, 66048, v68\nQk 40,0,1\n"
"Qmvaa 8,120,8\nQe 2\nQmvaa 8,124,12\nQe 3\nQcvaa 12,128,0,56\nQk 41,2,3\nQmvaa 12,132,4\nQe 4\n"
"v_smfmac_f32_16x16x64_bf16 a[184:187], a[204:207], v[32:39], v77\nQmvaa 12,136,8\nQe 5\nQmvaa 12,140,12\nQe 6\nQmaav 208,144,48\ns_waitcnt lgkmcnt(0)\ns_sub_u32 s75, s77, s74\n"
"Qmaav 208,148,36\ns_cmp_lt_u32 s75, 256\ns_cbranch_scc1 .Lsp4\n.Lrp4:\nQmaav 212,152,48\ns_add_u32 s77, s74, s73\nQk 42,4,5\nQmaav 212,156,36\n"
"s_sub_u32 s73, s73, 4\nQmaav 216,160,48\ns_max_u32 s73, s73, 120\nQmaav 216,164,36\nQe 7\nQmaav 220,168,48\nQk 43,6,7\nQmaav 220,172,36\n"
"Qe 8\nQcvaa 0,112,16,56\nQe 9\nQmvaa 0,116,20\nQe 10\nQmvaa 0,120,24\nQe 11\nQmvaa 0,124,28\n"
"Qk 44,8,9\nQk 45,10,11\nQcvaa 4,128,16,56\nQe 12\nQmvaa 4,132,20\nQe 13\nQmvaa 4,136,24\nQe 14\n"
"Qmvaa 4,140,28\nQe 15\nQmaav 240,144,52\nQta 144,70,0\nQta 146,70,2048\nQmaav 240,148,40\nQta 148,70,33024\nQta 150,70,35072\n"
"Qmaav 244,152,52\nQta 152,70,256\nQta 154,70,2304\nQmaav 244,156,40\nQta 156,70,33280\nQta 158,70,35328\nQmaav 248,160,52\nQta 160,70,512\n"
"Qta 162,70,2560\nQmaav 248,164,40\nQta 164,70,33536\nQta 166,70,35584\nQmaav 252,168,52\nQta 168,70,768\nQta 170,70,2816\nQmaav 252,172,40\n"
"Qta 172,70,33792\nQta 174,70,35840\nQcvaa 8,112,32,56\nv_add_u32 v75, 1, v75\nQk 46,12,13\nQmvaa 8,116,36\ns_waitcnt vmcnt(8)\nds_write_b32 v73, v75\n"
"Qmvaa 8,120,40\nQk 47,14,15\nQmvaa 8,124,44\nQe 0\nQcvaa 12,128,32,56\nQe 1\nQmvaa 12,132,36\nQe 2\n"
"v_smfmac_f32_16x16x64_bf16 a[188:191], a[204:207], v[40:47], v77\nQk 48,0,1\nQmvaa 12,136,40\ns_waitcnt lgkmcnt(1)\nQmvaa 12,140,44\nQe 3\nQmvav 80,144,16\nQk 49,2,3\n"
"Qmvav 80,148,44\nQe 4\nQmvav 84,152,16\nQe 5\nQmvav 84,156,44\nQe 6\nQmvav 88,160,16\nQe 7\n"
"Qmvav 88,164,44\nQk 50,4,5\nQk 51,6,7\nQmvav 92,168,16\nQe 8\nQmvav 92,172,44\nQe 9\nQcvaa 0,112,48,56\n"
"Qe 10\nQmvaa 0,116,52\nds_read_b32 v66, v74\nQk 52,8,9\nQmvaa 0,120,56\nQe 11\nQmvaa 0,124,60\nQk 53,10,11\n"
"Qcvaa 4,128,48,56\nQe 12\nQmvaa 4,132,52\nQe 13\nQmvaa 4,136,56\ns_waitcnt lgkmcnt(0)\nv_cmp_gt_u32 vcc, v75, v66\nQmvaa 4,140,60\n"
"s_cbranch_vccnz .Lsa5\n.Lra5:\nQk 54,12,13\nQmvav 112,144,20\ns_add_u32 m0, s61, 0\nQmvav 112,148,48\nbuffer_load_dwordx4 v69, s[40:43], s90 offen nt lds\nQmvav 116,152,20\n"
"Qe 14\nQmvav 116,156,48\nQe 15\nQmvav 120,160,20\nQk 55,14,15\nQmvav 120,164,48\nQe 0\nQmvav 124,168,20\n"
"buffer_load_dwordx4 v69, s[40:43], s60 offen offset:1024 nt lds\nQmvav 124,172,48\nQe 1\nQcvaa 8,112,64,56\nQe 2\nQmvaa 8,116,68\nQe 3\nQmvaa 8,120,72\n"
"Qk 20,0,1\nQk 21,2,3\nQmvaa 8,124,76\nbuffer_load_dwordx4 v69, s[40:43], s90 offen offset:2048 nt lds\nQcvaa 12,128,64,56\nQe 4\nQmvaa 12,132,68\nQe 5\n"
"v_smfmac_f32_16x16x64_bf16 a[192:195], a[204:207], v[48:55], v77\nQk 22,4,5\nQmvaa 12,136,72\nQe 6\nQmvaa 12,140,76\nbuffer_load_dwordx4 v69, s[40:43], s60 offen offset:3072 nt lds\nQmvav 144,144,24\nQe 7\n"
"Qmvav 144,148,52\nQk 23,6,7\nQmvav 148,152,24\nQe 8\nQmvav 148,156,52\ns_add_u32 m0, s61, 8192\nQmvav 152,160,24\nbuffer_load_dwordx4 v72, s[44:47], s90 offen nt lds\n"
"Qmvav 152,164,52\nQe 9\nQmvav 156,168,24\nQe 10\nQmvav 156,172,52\nQe 11\nQcvaa 0,112,80,56\nQk 24,8,9\n"
"Qk 25,10,11\nQmvaa 0,116,84\nbuffer_load_dwordx4 v72, s[44:47], s60 offen offset:1024 nt lds\nQmvaa 0,120,88\nQe 12\nQmvaa 0,124,92\nQe 13\nQcvaa 4,128,80,56\n"
"Qe 14\nQmvaa 4,132,84\nQe 15\nQmvaa 4,136,88\nbuffer_load_dwordx4 v72, s[44:47], s90 offen offset:2048 nt lds\nQmvaa 4,140,92\nQk 26,12,13\nQk 27,14,15\n"
"Qmvav 176,144,28\nQmvav 176,148,20\nQmvav 180,152,28\nQmvav 180,156,20\nbuffer_load_dwordx4 v72, s[44:47], s60 offen offset:3072 nt lds\nQmvav 184,160,28\nQe 0\nQmvav 184,164,20\n"
"Qe 1\nQmvav 188,168,28\ns_add_u32 s60, s60, 8192\ns_add_u32 s90, s60, 896\nQmvav 188,172,20\nQe 2\nQcvaa 8,112,96,56\nQda 112,68,0\n"
"Qk 28,0,1\nQmvaa 8,116,100\nQda 116,68,512\nQmvaa 8,120,104\nQda 120,68,1024\nQmvaa 8,124,108\nQda 124,68,1536\nQcvaa 12,128,96,56\n"
"Qda 128,68,2048\nQmvaa 12,132,100\nQda 132,68,2560\nv_smfmac_f32_16x16x64_bf16 a[196:199], a[204:207], v[20:27], v77\nQmvaa 12,136,104\nQda 136,68,3072\nQmvaa 12,140,108\nQda 140,68,3584\n"
"Qmvav 208,144,32\ns_memrealtime s[74:75]\nQmvav 208,148,24\nQe 3\nQmvav 212,152,32\nQk 29,2,3\nQmvav 212,156,24\nQe 4\n"
"Qmvav 216,160,32\nQe 5\nQmvav 216,164,24\nQe 6\nQmvav 220,168,32\nQe 7\nQmvav 220,172,24\ns_waitcnt lgkmcnt(0)\n"
"Qk 30,4,5\nQcvaa 0,112,0,56\nQk 31,6,7\nQmvaa 0,116,4\nQe 8\nQmvaa 0,120,8\nQe 9\nQmvaa 0,124,12\n"
"Qe 10\nQcvaa 4,128,0,56\nQe 11\nQmvaa 4,132,4\nQk 32,8,9\nQk 33,10,11\nQmvaa 4,136,8\nQe 12\n"
"Qmvaa 4,140,12\nQe 13\nQmvav 240,144,36\nQe 14\nQmvav 240,148,28\ns_waitcnt lgkmcnt(0)\ns_sub_u32 s75, s77, s74\nQmvav 244,152,36\n"
"s_cmp_lt_u32 s75, 256\ns_cbranch_scc1 .Lsp5\n.Lrp5:\nQmvav 244,156,28\ns_add_u32 s77, s74, s73\nQk 34,12,13\nQmvav 248,160,36\ns_sub_u32 s73, s73, 4\n"
"Qmvav 248,164,28\ns_max_u32 s73, s73, 120\nQmvav 252,168,36\nQe 15\nQmvav 252,172,28\nQk 35,14,15\nQcvaa 8,112,16,56\nQe 0\n"
"Qmvaa 8,116,20\nQe 1\nQmvaa 8,120,24\nQe 2\nQmvaa 8,124,28\nQe 3\nQcvaa 12,128,16,56\nQk 36,0,1\n"
"Qk 37,2,3\nQmvaa 12,132,20\nQe 4\nv_smfmac_f32_16x16x64_bf16 a[200:203], a[204:207], v[28:35], v77\nQmvaa 12,136,24\nQe 5\nQmvaa 12,140,28\nQe 6\n"
"Qmaav 224,144,40\nQta 144,70,34048\nQta 146,70,36096\nQmaav 224,148,32\nQta 148,71,1024\nQta 150,71,3072\nQmaav 228,152,40\nQta 152,70,34304\n"
"Qta 154,70,36352\nQmaav 228,156,32\nQta 156,71,1280\nQta 158,71,3328\nQmaav 232,160,40\nQta 160,70,34560\nQta 162,70,36608\nQmaav 232,164,32\n"
"Qta 164,71,1536\nQta 166,71,3584\nQmaav 236,168,40\nQta 168,70,34816\nQta 170,70,36864\nQmaav 236,172,32\nQta 172,71,1792\nQta 174,71,3840\n"
"s_add_u32 s66, s66, 1\ns_bitcmp1_b32 0x7f7f, s66\ns_cbranch_scc1 .Lloop\ns_cmp_lg_u32 s67, 0\ns_cbranch_scc1 .Ltail1\nQcvaa 0,112,32,56\nv_add_u32 v75, 1, v75\nQk 38,4,5\n"
"Qmvaa 0,116,36\ns_waitcnt vmcnt(8)\nds_write_b32 v73, v75\nQmvaa 0,120,40\nQe 7\nQmvaa 0,124,44\nQk 39,6,7\nQcvaa 4,128,32,56\n"
"Qe 8\n.Lpg7:\ns_cbranch_execz .Ltr9\nQmvaa 4,132,36\nQe 9\nQmvaa 4,136,40\nQe 10\nQmvaa 4,140,44\n"
"s_waitcnt lgkmcnt(1)\nQk 40,8,9\nQmvav 96,144,44\nQe 11\nQmvav 96,148,36\nQk 41,10,11\nQmvav 100,152,44\nQe 12\n"
"Qmvav 100,156,36\nQe 13\nQmvav 104,160,44\nQe 14\nQmvav 104,164,36\nQe 15\nQmvav 108,168,44\nQk 42,12,13\n"
"Qk 43,14,15\nQmvav 108,172,36\nQe 0\nQcvaa 8,112,48,56\nQe 1\nQmvaa 8,116,52\nQe 2\nQmvaa 8,120,56\n"
"ds_read_b32 v66, v74\nQk 44,0,1\nQmvaa 8,124,60\nQe 3\nQcvaa 12,128,48,56\nQk 45,2,3\nQmvaa 12,132,52\nQe 4\n"
"v_smfmac_f32_16x16x64_bf16 a[176:179], a[204:207], v[36:43], v77\nQmvaa 12,136,56\ns_waitcnt lgkmcnt(0)\nv_cmp_gt_u32 vcc, v75, v66\nQmvaa 12,140,60\ns_cbranch_vccnz .Lsa30\n.Lra30:\nQmvav 128,144,48\n"
"s_add_u32 m0, s62, 0\ns_add_u32 s69, s87, 0\nQmvav 128,148,40\nbuffer_load_dwordx4 v62, s[52:55], s69 offen lds\nQmvav 132,152,48\nQe 5\nQmvav 132,156,40\nQe 6\n"
"Qmvav 136,160,48\nQe 7\nQmvav 136,164,40\ns_add_u32 s69, s87, 64504\nQk 46,4,5\nQk 47,6,7\nQmvav 140,168,48\nbuffer_load_dwordx4 v62, s[52:55], s69 offen offset:1032 lds\n"
"Qmvav 140,172,40\nQe 8\nQcvaa 0,112,64,56\nQe 9\nQmvaa 0,116,68\nQe 10\nQmvaa 0,120,72\ns_add_u32 s69, s87, 129008\n"
"Qe 11\nQmvaa 0,124,76\nbuffer_load_dwordx4 v62, s[52:55], s69 offen offset:2064 lds\nQcvaa 4,128,64,56\nQk 48,8,9\nQk 49,10,11\nQmvaa 4,132,68\nQe 12\n"
"Qmvaa 4,136,72\nQe 13\nQmvaa 4,140,76\ns_add_u32 s69, s87, 193512\nQe 14\nQmvav 160,144,52\nbuffer_load_dwordx4 v62, s[52:55], s69 offen offset:3096 lds\nQmvav 160,148,44\n"
"Qe 15\nQmvav 164,152,52\nQk 50,12,13\nQk 51,14,15\nQmvav 164,156,44\nQe 0\nQmvav 168,160,52\ns_add_u32 m0, s62, 16512\n"
"s_add_u32 s69, s87, 128\nQmvav 168,164,44\nbuffer_load_dwordx4 v62, s[52:55], s69 offen lds\nQmvav 172,168,52\nQe 1\nQmvav 172,172,44\nQe 2\nQcvaa 8,112,80,56\n"
"Qe 3\nQmvaa 8,116,84\ns_add_u32 s69, s87, 64632\nQk 16,0,1\nQk 17,2,3\nQmvaa 8,120,88\nbuffer_load_dwordx4 v62, s[52:55], s69 offen offset:1032 lds\nQmvaa 8,124,92\n"
"Qe 4\nQcvaa 12,128,80,56\nQe 5\nQmvaa 12,132,84\nQe 6\nv_smfmac_f32_16x16x64_bf16 a[180:183], a[204:207], v[44:51], v77\ns_add_u32 s69, s87, 129136\nQk 18,4,5\n"
"Qmvaa 12,136,88\nbuffer_load_dwordx4 v62, s[52:55], s69 offen offset:2064 lds\nQmvaa 12,140,92\nQe 7\nQmvav 192,144,20\nQk 19,6,7\nQmvav 192,148,48\nQmvav 196,152,20\n"
"s_add_u32 s69, s87, 193640\nQe 8\nQmvav 196,156,48\nbuffer_load_dwordx4 v62, s[52:55], s69 offen offset:3096 lds\nQmvav 200,160,20\nQe 9\nQmvav 200,164,48\nQe 10\n"
"Qmvav 204,168,20\nQmvav 204,172,48\nQe 11\nQcvaa 0,112,96,56\nQda 112,68,33024\nQk 20,8,9\nQmvaa 0,116,100\nQda 116,68,33536\n"
"Qk 21,10,11\nQmvaa 0,120,104\nQda 120,68,34048\nQmvaa 0,124,108\nQda 124,68,34560\nQcvaa 4,128,96,56\nQda 128,68,35072\nQmvaa 4,132,100\n"
"Qda 132,68,35584\nQmvaa 4,136,104\nQda 136,68,36096\nQmvaa 4,140,108\nQda 140,68,36608\nQmvav 224,144,24\nQe 12\nQmvav 224,148,16\n"
"s_add_u32 m0, s63, 33024\ns_add_u32 s88, s86, 0\nQmvav 228,152,24\nbuffer_load_dwordx4 v64, s[48:51], s88 offen lds\nQe 13\nQmvav 228,156,16\nQe 14\nQmvav 232,160,24\n"
"Qe 15\nQmvav 232,164,16\ns_add_u32 s88, s86, 19456\nQk 22,12,13\nQk 23,14,15\nQmvav 236,168,24\nbuffer_load_dwordx4 v64, s[48:51], s88 offen offset:1024 lds\nQe 0\n"
"Qmvav 236,172,16\ns_waitcnt lgkmcnt(0)\nQcvaa 8,112,0,56\nQe 1\nQmvaa 8,116,4\nv_subrev_u32 v68, 66048, v68\ns_add_u32 s88, s86, 38912\nQk 24,0,1\n"
"Qmvaa 8,120,8\nbuffer_load_dwordx4 v64, s[48:51], s88 offen offset:2048 lds\nQe 2\nQmvaa 8,124,12\nQe 3\nQcvaa 12,128,0,56\nQk 25,2,3\nQmvaa 12,132,4\n"
"Qe 4\nv_smfmac_f32_16x16x64_bf16 a[184:187], a[204:207], v[16:23], v77\ns_add_u32 s88, s86, 58368\nQmvaa 12,136,8\nbuffer_load_dwordx4 v64, s[48:51], s88 offen offset:3072 lds\nQe 5\nQmvaa 12,140,12\nQe 6\n"
"Qmaav 208,144,28\ns_add_u32 m0, s63, 37120\ns_add_u32 s88, s86, 81920\nQmaav 208,148,20\nbuffer_load_dwordx4 v64, s[48:51], s88 offen lds\nQmaav 212,152,28\nQk 26,4,5\nQmaav 212,156,20\n"
"Qmaav 216,160,28\ns_add_u32 s88, s86, 101376\nQmaav 216,164,20\nbuffer_load_dwordx4 v64, s[48:51], s88 offen offset:1024 lds\nQe 7\nQmaav 220,168,28\nQk 27,6,7\nQmaav 220,172,20\n"
"Qe 8\nQcvaa 0,112,16,56\ns_add_u32 s88, s86, 120832\nQe 9\nQmvaa 0,116,20\nbuffer_load_dwordx4 v64, s[48:51], s88 offen offset:2048 lds\nQe 10\nQmvaa 0,120,24\n"
"Qe 11\nQmvaa 0,124,28\nQk 28,8,9\nQk 29,10,11\nQcvaa 4,128,16,56\nQe 12\nQmvaa 4,132,20\nQe 13\n"
"Qmvaa 4,136,24\nQe 14\nQmvaa 4,140,28\nQe 15\nQmaav 240,144,32\nQta 144,71,0\nQta 146,71,2048\nQmaav 240,148,24\n"
"Qta 148,71,33024\nQta 150,71,35072\nQmaav 244,152,32\nQta 152,71,256\nQta 154,71,2304\nQmaav 244,156,24\nQta 156,71,33280\nQta 158,71,35328\n"
"Qmaav 248,160,32\nQta 160,71,512\nQta 162,71,2560\nQmaav 248,164,24\nQta 164,71,33536\nQta 166,71,35584\nQmaav 252,168,32\nQta 168,71,768\n"
"Qta 170,71,2816\nQmaav 252,172,24\nQta 172,71,33792\nQta 174,71,35840\nQcvaa 8,112,32,56\nv_add_u32 v75, 1, v75\nQk 30,12,13\nQmvaa 8,116,36\n"
"ds_write_b32 v73, v75\nQmvaa 8,120,40\nQk 31,14,15\nQmvaa 8,124,44\nQe 0\nQcvaa 12,128,32,56\nQe 1\nQmvaa 12,132,36\n"
"Qe 2\nv_smfmac_f32_16x16x64_bf16 a[188:191], a[204:207], v[24:31], v77\nQk 32,0,1\nQmvaa 12,136,40\ns_waitcnt lgkmcnt(1)\nQmvaa 12,140,44\nQe 3\nQmvav 80,144,36\n"
"Qk 33,2,3\nQmvav 80,148,28\nQe 4\nQmvav 84,152,36\nQe 5\nQmvav 84,156,28\nQe 6\nQmvav 88,160,36\n"
"Qe 7\nQmvav 88,164,28\nQk 34,4,5\nQk 35,6,7\nQmvav 92,168,36\nQe 8\nQmvav 92,172,28\nQe 9\n"
"Qcvaa 0,112,48,56\nQe 10\nQmvaa 0,116,52\nds_read_b32 v66, v74\nQk 36,8,9\nQmvaa 0,120,56\nQe 11\nQmvaa 0,124,60\n"
"Qk 37,10,11\nQcvaa 4,128,48,56\nQe 12\nQmvaa 4,132,52\nQe 13\nQmvaa 4,136,56\ns_waitcnt lgkmcnt(0)\nv_cmp_gt_u32 vcc, v75, v66\n"
"Qmvaa 4,140,60\ns_cbranch_vccnz .Lsa31\n.Lra31:\nQk 38,12,13\nQmvav 112,144,40\ns_add_u32 m0, s62, 33024\ns_add_u32 s69, s87, 524288\nQmvav 112,148,32\n"
"buffer_load_dwordx4 v62, s[52:55], s69 offen lds\nQmvav 116,152,40\nQe 14\nQmvav 116,156,32\nQe 15\nQmvav 120,160,40\nQk 39,14,15\nQmvav 120,164,32\n"
"s_add_u32 s69, s87, 588792\nQe 0\nQmvav 124,168,40\nbuffer_load_dwordx4 v62, s[52:55], s69 offen offset:1032 lds\nQmvav 124,172,32\nQe 1\nQcvaa 8,112,64,56\nQe 2\n"
"Qmvaa 8,116,68\nQe 3\nQmvaa 8,120,72\ns_add_u32 s69, s87, 653296\nQk 40,0,1\nQk 41,2,3\nQmvaa 8,124,76\nbuffer_load_dwordx4 v62, s[52:55], s69 offen offset:2064 lds\n"
"Qcvaa 12,128,64,56\nQe 4\nQmvaa 12,132,68\nQe 5\nv_smfmac_f32_16x16x64_bf16 a[192:195], a[204:207], v[32:39], v77\nQk 42,4,5\nQmvaa 12,136,72\ns_add_u32 s69, s87, 717800\n"
"Qe 6\nQmvaa 12,140,76\nbuffer_load_dwordx4 v62, s[52:55], s69 offen offset:3096 lds\nQmvav 144,144,44\nQe 7\nQmvav 144,148,36\nQk 43,6,7\nQmvav 148,152,44\n"
"Qe 8\nQmvav 148,156,36\ns_add_u32 m0, s62, 49536\ns_add_u32 s69, s87, 524416\nQmvav 152,160,44\nbuffer_load_dwordx4 v62, s[52:55], s69 offen lds\nQmvav 152,164,36\nQe 9\n"
"Qmvav 156,168,44\nQe 10\nQmvav 156,172,36\nQe 11\nQcvaa 0,112,80,56\ns_add_u32 s69, s87, 588920\nQk 44,8,9\nQk 45,10,11\n"
"Qmvaa 0,116,84\nbuffer_load_dwordx4 v62, s[52:55], s69 offen offset:1032 lds\nQmvaa 0,120,88\nQe 12\nQmvaa 0,124,92\nQe 13\nQcvaa 4,128,80,56\nQe 14\n"
"Qmvaa 4,132,84\ns_add_u32 s69, s87, 653424\nQe 15\nQmvaa 4,136,88\nbuffer_load_dwordx4 v62, s[52:55], s69 offen offset:2064 lds\nQmvaa 4,140,92\nQk 46,12,13\nQk 47,14,15\n"
"Qmvav 176,144,48\nQmvav 176,148,40\nQmvav 180,152,48\ns_add_u32 s69, s87, 717928\nQmvav 180,156,40\nbuffer_load_dwordx4 v62, s[52:55], s69 offen offset:3096 lds\nQmvav 184,160,48\nQe 0\n"
"Qmvav 184,164,40\nQe 1\nQmvav 188,168,48\nQmvav 188,172,40\nQe 2\nQcvaa 8,112,96,56\nQk 48,0,1\nQmvaa 8,116,100\n"
"Qmvaa 8,120,104\nQmvaa 8,124,108\nQcvaa 12,128,96,56\nQmvaa 12,132,100\nv_smfmac_f32_16x16x64_bf16 a[196:199], a[204:207], v[40:47], v77\nQmvaa 12,136,104\nQmvaa 12,140,108\nQmvav 208,144,16\n"
"Qta 112,71,34048\nQmvav 208,148,44\nQta 114,71,36096\nQe 3\nQmvav 212,152,16\nQta 116,71,34304\nQk 49,2,3\nQmvav 212,156,44\n"
"Qta 118,71,36352\nQe 4\nQmvav 216,160,16\nQta 120,71,34560\nQe 5\nQmvav 216,164,44\nQta 122,71,36608\nQe 6\n"
"Qmvav 220,168,16\nQta 124,71,34816\nQe 7\nQmvav 220,172,44\nQta 126,71,36864\nQk 50,4,5\nQk 51,6,7\nQe 8\n"
"Qe 9\nQe 10\nQe 11\nQk 52,8,9\nQk 53,10,11\nQe 12\nQe 13\nQmvav 240,144,20\n"
"v_add_u32 v75, 1, v75\nQe 14\nQmvav 240,148,48\nds_write_b32 v73, v75\nQmvav 244,152,20\nQmvav 244,156,48\nQk 54,12,13\nQmvav 248,160,20\n"
"Qmvav 248,164,48\nQmvav 252,168,20\nQe 15\nQmvav 252,172,48\nQk 55,14,15\ns_nop 1\nv_smfmac_f32_16x16x64_bf16 a[200:203], a[204:207], v[48:55], v77\nQmaav 224,144,24\n"
"ds_read_b32 v66, v74\nv_accvgpr_read_b32 v0, a176\nQmaav 224,148,52\nv_accvgpr_read_b32 v1, a177\nv_accvgpr_read_b32 v6, a178\nQmaav 228,152,24\nv_accvgpr_read_b32 v7, a179\nv_accvgpr_read_b32 v8, a180\n"
"Qmaav 228,156,52\nv_accvgpr_read_b32 v9, a181\nv_accvgpr_read_b32 v14, a182\nQmaav 232,160,24\nv_accvgpr_read_b32 v15, a183\nv_accvgpr_read_b32 v16, a184\nQmaav 232,164,52\ns_waitcnt lgkmcnt(0)\n"
"v_cmp_gt_u32 vcc, v75, v66\nQmaav 236,168,24\ns_cbranch_vccnz .Lsa32\n.Lra32:\nv_accvgpr_read_b32 v17, a185\nQmaav 236,172,52\ns_waitcnt lgkmcnt(2)\nv_accvgpr_read_b32 v22, a186\n"
"Qmvav 96,112,28\ns_add_u32 m0, s63, 0\ns_add_u32 s88, s86, 128\nv_accvgpr_read_b32 v23, a187\nQmvav 100,116,28\nbuffer_load_dwordx4 v64, s[48:51], s88 offen lds\nQmvav 104,120,28\nv_accvgpr_read_b32 v24, a188\n"
"v_accvgpr_read_b32 v25, a189\nQmvav 108,124,28\nv_accvgpr_read_b32 v2, a190\nv_accvgpr_read_b32 v3, a191\nQmvav 128,112,32\ns_add_u32 s88, s86, 19584\nv_accvgpr_read_b32 v4, a192\nQmvav 132,116,32\n"
"buffer_load_dwordx4 v64, s[48:51], s88 offen offset:1024 lds\nQmvav 136,120,32\nv_accvgpr_read_b32 v5, a193\nv_accvgpr_read_b32 v10, a194\nQmvav 140,124,32\nv_accvgpr_read_b32 v11, a195\nv_accvgpr_read_b32 v12, a196\nQmvav 160,112,36\n"
"s_add_u32 s88, s86, 39040\nv_accvgpr_read_b32 v13, a197\nQmvav 164,116,36\nbuffer_load_dwordx4 v64, s[48:51], s88 offen offset:2048 lds\nQmvav 168,120,36\nv_accvgpr_read_b32 v18, a198\nv_accvgpr_read_b32 v19, a199\nQmvav 172,124,36\n"
"v_accvgpr_read_b32 v20, a200\nv_accvgpr_read_b32 v21, a201\nQmvav 192,112,40\ns_add_u32 s88, s86, 58496\nv_accvgpr_read_b32 v26, a202\nQmvav 196,116,40\nbuffer_load_dwordx4 v64, s[48:51], s88 offen offset:3072 lds\nQmvav 200,120,40\n"
"v_accvgpr_read_b32 v27, a203\nQa 8,8,9\nQmvav 204,124,40\nQa 10,10,11\nQa 8,8,10\nQmvav 224,112,44\ns_add_u32 m0, s63, 4096\ns_add_u32 s88, s86, 82048\n"
"Qa 16,16,17\nQmvav 228,116,44\nbuffer_load_dwordx4 v64, s[48:51], s88 offen lds\nQmvav 232,120,44\nQa 18,18,19\nQa 16,16,18\nQmvav 236,124,44\nQa 4,4,5\n"
"Qa 6,6,7\nQmaav 208,112,48\ns_add_u32 s88, s86, 101504\nQa 4,4,6\nQmaav 212,116,48\nbuffer_load_dwordx4 v64, s[48:51], s88 offen offset:1024 lds\nQmaav 216,120,48\nQa 12,12,13\n"
"Qa 14,14,15\nQmaav 220,124,48\nQa 12,12,14\nQa 0,0,1\nQmaav 240,112,52\ns_add_u32 s88, s86, 120960\nQa 2,2,3\nQmaav 244,116,52\n"
"buffer_load_dwordx4 v64, s[48:51], s88 offen offset:2048 lds\nQmaav 248,120,52\nQa 0,0,2\nQa 24,24,25\nQmaav 252,124,52\nQa 26,26,27\nQa 24,24,26\nQa 20,20,21\n"
"Qa 22,22,23\nQa 20,20,22\nv_max3_u32 v9, v8, v16, v4\nv_max3_u32 v9, v9, v12, v0\nv_max3_u32 v9, v9, v24, v20\nv_min3_u32 v11, v8, v16, v4\nv_min3_u32 v11, v11, v12, v0\nv_min3_u32 v11, v11, v24, v20\n"
"v_cmp_lt_u32 vcc, 0x44800000, v9\ns_mov_b64 s[74:75], vcc\nv_cmp_gt_u32 vcc, 0x2b800000, v11\ns_or_b64 s[74:75], s[74:75], vcc\ns_cmp_lg_u64 s[74:75], 0\ns_cbranch_scc1 .Lfb\n.Lfbret0:\nv_rcp_f32 v49, v24\n"
"v_rcp_f32 v48, v24\nv_rcp_f32 v45, v20\nv_rcp_f32 v44, v20\nv_rcp_f32 v41, v16\nv_rcp_f32 v40, v16\nv_rcp_f32 v37, v12\nv_rcp_f32 v36, v12\nv_rcp_f32 v33, v8\n"
"v_rcp_f32 v32, v8\nv_rcp_f32 v29, v4\nv_rcp_f32 v28, v4\nv_rcp_f32 v25, v0\nv_rcp_f32 v24, v0\ns_nop 0\nv_pk_mul_f32 v[80:81], v[80:81], v[24:25]\nv_pk_mul_f32 v[82:83], v[82:83], v[24:25]\n"
".Lpg8:\ns_cbranch_execz .Ltr10\nv_pk_mul_f32 v[84:85], v[84:85], v[24:25]\nv_pk_mul_f32 v[86:87], v[86:87], v[24:25]\nQk 80,80,81\nQk 81,82,83\nQk 82,84,85\nQk 83,86,87\n"
"v_accvgpr_write_b32 a0, v80\nv_accvgpr_write_b32 a1, v81\nv_accvgpr_write_b32 a2, v82\nv_accvgpr_write_b32 a3, v83\nv_pk_mul_f32 v[88:89], v[88:89], v[24:25]\nv_pk_mul_f32 v[90:91], v[90:91], v[24:25]\nv_pk_mul_f32 v[92:93], v[92:93], v[24:25]\nv_pk_mul_f32 v[94:95], v[94:95], v[24:25]\n"
"Qk 88,88,89\nQk 89,90,91\nQk 90,92,93\nQk 91,94,95\nv_accvgpr_write_b32 a4, v88\nv_accvgpr_write_b32 a5, v89\nv_accvgpr_write_b32 a6, v90\nv_accvgpr_write_b32 a7, v91\n"
"v_pk_mul_f32 v[96:97], v[96:97], v[24:25]\nv_pk_mul_f32 v[98:99], v[98:99], v[24:25]\nv_pk_mul_f32 v[100:101], v[100:101], v[24:25]\nv_pk_mul_f32 v[102:103], v[102:103], v[24:25]\nQk 96,96,97\nQk 97,98,99\nQk 98,100,101\nQk 99,102,103\n"
"v_accvgpr_write_b32 a8, v96\nv_accvgpr_write_b32 a9, v97\nv_accvgpr_write_b32 a10, v98\nv_accvgpr_write_b32 a11, v99\nv_pk_mul_f32 v[104:105], v[104:105], v[24:25]\nv_pk_mul_f32 v[106:107], v[106:107], v[24:25]\nv_pk_mul_f32 v[108:109], v[108:109], v[24:25]\nv_pk_mul_f32 v[110:111], v[110:111], v[24:25]\n"
"Qk 104,104,105\nQk 105,106,107\nQk 106,108,109\nQk 107,110,111\nv_accvgpr_write_b32 a12, v104\nv_accvgpr_write_b32 a13, v105\nv_accvgpr_write_b32 a14, v106\nv_accvgpr_write_b32 a15, v107\n"
"v_pk_mul_f32 v[112:113], v[112:113], v[28:29]\nv_pk_mul_f32 v[114:115], v[114:115], v[28:29]\nv_pk_mul_f32 v[116:117], v[116:117], v[28:29]\nv_pk_mul_f32 v[118:119], v[118:119], v[28:29]\nQk 112,112,113\nQk 113,114,115\nQk 114,116,117\nQk 115,118,119\n"
"v_accvgpr_write_b32 a16, v112\nv_accvgpr_write_b32 a17, v113\nv_accvgpr_write_b32 a18, v114\nv_accvgpr_write_b32 a19, v115\nv_pk_mul_f32 v[120:121], v[120:121], v[28:29]\nv_pk_mul_f32 v[122:123], v[122:123], v[28:29]\nv_pk_mul_f32 v[124:125], v[124:125], v[28:29]\nv_pk_mul_f32 v[126:127], v[126:127], v[28:29]\n"
"Qk 120,120,121\nQk 121,122,123\nQk 122,124,125\nQk 123,126,127\nv_accvgpr_write_b32 a20, v120\nv_accvgpr_write_b32 a21, v121\nv_accvgpr_write_b32 a22, v122\nv_accvgpr_write_b32 a23, v123\n"
"v_pk_mul_f32 v[128:129], v[128:129], v[28:29]\nv_pk_mul_f32 v[130:131], v[130:131], v[28:29]\nv_pk_mul_f32 v[132:133], v[132:133], v[28:29]\nv_pk_mul_f32 v[134:135], v[134:135], v[28:29]\nQk 128,128,129\nQk 129,130,131\nQk 130,132,133\nQk 131,134,135\n"
"v_accvgpr_write_b32 a24, v128\nv_accvgpr_write_b32 a25, v129\nv_accvgpr_write_b32 a26, v130\nv_accvgpr_write_b32 a27, v131\nv_pk_mul_f32 v[136:137], v[136:137], v[28:29]\nv_pk_mul_f32 v[138:139], v[138:139], v[28:29]\nv_pk_mul_f32 v[140:141], v[140:141], v[28:29]\nv_pk_mul_f32 v[142:143], v[142:143], v[28:29]\n"
"Qk 136,136,137\nQk 137,138,139\nQk 138,140,141\nQk 139,142,143\nv_accvgpr_write_b32 a28, v136\nv_accvgpr_write_b32 a29, v137\nv_accvgpr_write_b32 a30, v138\nv_accvgpr_write_b32 a31, v139\n"
"v_pk_mul_f32 v[144:145], v[144:145], v[32:33]\nv_pk_mul_f32 v[146:147], v[146:147], v[32:33]\nv_pk_mul_f32 v[148:149], v[148:149], v[32:33]\nv_pk_mul_f32 v[150:151], v[150:151], v[32:33]\nQk 144,144,145\nQk 145,146,147\nQk 146,148,149\nQk 147,150,151\n"
"v_accvgpr_write_b32 a32, v144\nv_accvgpr_write_b32 a33, v145\nv_accvgpr_write_b32 a34, v146\nv_accvgpr_write_b32 a35, v147\nv_pk_mul_f32 v[152:153], v[152:153], v[32:33]\nv_pk_mul_f32 v[154:155], v[154:155], v[32:33]\nv_pk_mul_f32 v[156:157], v[156:157], v[32:33]\nv_pk_mul_f32 v[158:159], v[158:159], v[32:33]\n"
"Qk 152,152,153\nQk 153,154,155\nQk 154,156,157\nQk 155,158,159\nv_accvgpr_write_b32 a36, v152\nv_accvgpr_write_b32 a37, v153\nv_accvgpr_write_b32 a38, v154\nv_accvgpr_write_b32 a39, v155\n"
"v_pk_mul_f32 v[160:161], v[160:161], v[32:33]\nv_pk_mul_f32 v[162:163], v[162:163], v[32:33]\nv_pk_mul_f32 v[164:165], v[164:165], v[32:33]\nv_pk_mul_f32 v[166:167], v[166:167], v[32:33]\nQk 160,160,161\nQk 161,162,163\nQk 162,164,165\nQk 163,166,167\n"
"v_accvgpr_write_b32 a40, v160\nv_accvgpr_write_b32 a41, v161\nv_accvgpr_write_b32 a42, v162\nv_accvgpr_write_b32 a43, v163\nv_pk_mul_f32 v[168:169], v[168:169], v[32:33]\nv_pk_mul_f32 v[170:171], v[170:171], v[32:33]\nv_pk_mul_f32 v[172:173], v[172:173], v[32:33]\nv_pk_mul_f32 v[174:175], v[174:175], v[32:33]\n"
"Qk 168,168,169\nQk 169,170,171\nQk 170,172,173\nQk 171,174,175\nv_accvgpr_write_b32 a44, v168\nv_accvgpr_write_b32 a45, v169\nv_accvgpr_write_b32 a46, v170\nv_accvgpr_write_b32 a47, v171\n"
"v_pk_mul_f32 v[176:177], v[176:177], v[36:37]\nv_pk_mul_f32 v[178:179], v[178:179], v[36:37]\nv_pk_mul_f32 v[180:181], v[180:181], v[36:37]\nv_pk_mul_f32 v[182:183], v[182:183], v[36:37]\nQk 176,176,177\nQk 177,178,179\nQk 178,180,181\nQk 179,182,183\n"
"v_accvgpr_write_b32 a48, v176\nv_accvgpr_write_b32 a49, v177\nv_accvgpr_write_b32 a50, v178\nv_accvgpr_write_b32 a51, v179\nv_pk_mul_f32 v[184:185], v[184:185], v[36:37]\nv_pk_mul_f32 v[186:187], v[186:187], v[36:37]\nv_pk_mul_f32 v[188:189], v[188:189], v[36:37]\nv_pk_mul_f32 v[190:191], v[190:191], v[36:37]\n"
"Qk 184,184,185\nQk 185,186,187\nQk 186,188,189\nQk 187,190,191\nv_accvgpr_write_b32 a52, v184\nv_accvgpr_write_b32 a53, v185\nv_accvgpr_write_b32 a54, v186\nv_accvgpr_write_b32 a55, v187\n"
"v_pk_mul_f32 v[192:193], v[192:193], v[36:37]\nv_pk_mul_f32 v[194:195], v[194:195], v[36:37]\nv_pk_mul_f32 v[196:197], v[196:197], v[36:37]\nv_pk_mul_f32 v[198:199], v[198:199], v[36:37]\nQk 192,192,193\nQk 193,194,195\nQk 194,196,197\nQk 195,198,199\n"
"v_accvgpr_write_b32 a56, v192\nv_accvgpr_write_b32 a57, v193\nv_accvgpr_write_b32 a58, v194\nv_accvgpr_write_b32 a59, v195\nv_pk_mul_f32 v[200:201], v[200:201], v[36:37]\nv_pk_mul_f32 v[202:203], v[202:203], v[36:37]\nv_pk_mul_f32 v[204:205], v[204:205], v[36:37]\nv_pk_mul_f32 v[206:207], v[206:207], v[36:37]\n"
"Qk 200,200,201\nQk 201,202,203\nQk 202,204,205\nQk 203,206,207\nv_accvgpr_write_b32 a60, v200\nv_accvgpr_write_b32 a61, v201\nv_accvgpr_write_b32 a62, v202\nv_accvgpr_write_b32 a63, v203\n"
"v_pk_mul_f32 v[208:209], v[208:209], v[40:41]\nv_pk_mul_f32 v[210:211], v[210:211], v[40:41]\nv_pk_mul_f32 v[212:213], v[212:213], v[40:41]\nv_pk_mul_f32 v[214:215], v[214:215], v[40:41]\nQk 208,208,209\nQk 209,210,211\nQk 210,212,213\nQk 211,214,215\n"
"v_accvgpr_write_b32 a64, v208\nv_accvgpr_write_b32 a65, v209\nv_accvgpr_write_b32 a66, v210\nv_accvgpr_write_b32 a67, v211\nv_pk_mul_f32 v[216:217], v[216:217], v[40:41]\nv_pk_mul_f32 v[218:219], v[218:219], v[40:41]\nv_pk_mul_f32 v[220:221], v[220:221], v[40:41]\nv_pk_mul_f32 v[222:223], v[222:223], v[40:41]\n"
"Qk 216,216,217\nQk 217,218,219\nQk 218,220,221\nQk 219,222,223\nv_accvgpr_write_b32 a68, v216\nv_accvgpr_write_b32 a69, v217\nv_accvgpr_write_b32 a70, v218\nv_accvgpr_write_b32 a71, v219\n"
"v_pk_mul_f32 v[224:225], v[224:225], v[40:41]\nv_pk_mul_f32 v[226:227], v[226:227], v[40:41]\nv_pk_mul_f32 v[228:229], v[228:229], v[40:41]\nv_pk_mul_f32 v[230:231], v[230:231], v[40:41]\nQk 224,224,225\nQk 225,226,227\nQk 226,228,229\nQk 227,230,231\n"
"v_accvgpr_write_b32 a72, v224\nv_accvgpr_write_b32 a73, v225\nv_accvgpr_write_b32 a74, v226\nv_accvgpr_write_b32 a75, v227\nv_pk_mul_f32 v[232:233], v[232:233], v[40:41]\nv_pk_mul_f32 v[234:235], v[234:235], v[40:41]\nv_pk_mul_f32 v[236:237], v[236:237], v[40:41]\nv_pk_mul_f32 v[238:239], v[238:239], v[40:41]\n"
"Qk 232,232,233\nQk 233,234,235\nQk 234,236,237\nQk 235,238,239\nv_accvgpr_write_b32 a76, v232\nv_accvgpr_write_b32 a77, v233\nv_accvgpr_write_b32 a78, v234\nv_accvgpr_write_b32 a79, v235\n"
"v_pk_mul_f32 v[240:241], v[240:241], v[44:45]\nv_pk_mul_f32 v[242:243], v[242:243], v[44:45]\nv_pk_mul_f32 v[244:245], v[244:245], v[44:45]\nv_pk_mul_f32 v[246:247], v[246:247], v[44:45]\nQk 240,240,241\nQk 241,242,243\nQk 242,244,245\nQk 243,246,247\n"
"v_accvgpr_write_b32 a80, v240\nv_accvgpr_write_b32 a81, v241\nv_accvgpr_write_b32 a82, v242\nv_accvgpr_write_b32 a83, v243\nv_pk_mul_f32 v[248:249], v[248:249], v[44:45]\nv_pk_mul_f32 v[250:251], v[250:251], v[44:45]\nv_pk_mul_f32 v[252:253], v[252:253], v[44:45]\nv_pk_mul_f32 v[254:255], v[254:255], v[44:45]\n"
"Qk 248,248,249\nQk 249,250,251\nQk 250,252,253\nQk 251,254,255\nv_accvgpr_write_b32 a84, v248\nv_accvgpr_write_b32 a85, v249\nv_accvgpr_write_b32 a86, v250\nv_accvgpr_write_b32 a87, v251\n"
"v_accvgpr_read_b32 v0, a208\nv_accvgpr_read_b32 v1, a209\nv_accvgpr_read_b32 v2, a210\nv_accvgpr_read_b32 v3, a211\nv_accvgpr_read_b32 v4, a212\nv_accvgpr_read_b32 v5, a213\nv_accvgpr_read_b32 v6, a214\nv_accvgpr_read_b32 v7, a215\n"
"v_pk_mul_f32 v[0:1], v[0:1], v[44:45]\nv_pk_mul_f32 v[2:3], v[2:3], v[44:45]\nv_pk_mul_f32 v[4:5], v[4:5], v[44:45]\nv_pk_mul_f32 v[6:7], v[6:7], v[44:45]\nQk 0,0,1\nQk 1,2,3\nQk 2,4,5\nQk 3,6,7\n"
"v_accvgpr_write_b32 a88, v0\nv_accvgpr_write_b32 a89, v1\nv_accvgpr_write_b32 a90, v2\nv_accvgpr_write_b32 a91, v3\nv_accvgpr_read_b32 v8, a216\nv_accvgpr_read_b32 v9, a217\nv_accvgpr_read_b32 v10, a218\nv_accvgpr_read_b32 v11, a219\n"
"v_accvgpr_read_b32 v12, a220\nv_accvgpr_read_b32 v13, a221\nv_accvgpr_read_b32 v14, a222\nv_accvgpr_read_b32 v15, a223\nv_pk_mul_f32 v[8:9], v[8:9], v[44:45]\nv_pk_mul_f32 v[10:11], v[10:11], v[44:45]\nv_pk_mul_f32 v[12:13], v[12:13], v[44:45]\nv_pk_mul_f32 v[14:15], v[14:15], v[44:45]\n"
"Qk 8,8,9\nQk 9,10,11\nQk 10,12,13\nQk 11,14,15\nv_accvgpr_write_b32 a92, v8\nv_accvgpr_write_b32 a93, v9\nv_accvgpr_write_b32 a94, v10\nv_accvgpr_write_b32 a95, v11\n"
"v_accvgpr_read_b32 v0, a224\nv_accvgpr_read_b32 v1, a225\nv_accvgpr_read_b32 v2, a226\nv_accvgpr_read_b32 v3, a227\nv_accvgpr_read_b32 v4, a228\nv_accvgpr_read_b32 v5, a229\nv_accvgpr_read_b32 v6, a230\nv_accvgpr_read_b32 v7, a231\n"
"v_pk_mul_f32 v[0:1], v[0:1], v[48:49]\nv_pk_mul_f32 v[2:3], v[2:3], v[48:49]\nv_pk_mul_f32 v[4:5], v[4:5], v[48:49]\nv_pk_mul_f32 v[6:7], v[6:7], v[48:49]\nQk 0,0,1\nQk 1,2,3\nQk 2,4,5\nQk 3,6,7\n"
"v_accvgpr_write_b32 a96, v0\nv_accvgpr_write_b32 a97, v1\nv_accvgpr_write_b32 a98, v2\nv_accvgpr_write_b32 a99, v3\nv_accvgpr_read_b32 v8, a232\nv_accvgpr_read_b32 v9, a233\nv_accvgpr_read_b32 v10, a234\nv_accvgpr_read_b32 v11, a235\n"
"v_accvgpr_read_b32 v12, a236\nv_accvgpr_read_b32 v13, a237\nv_accvgpr_read_b32 v14, a238\nv_accvgpr_read_b32 v15, a239\nv_pk_mul_f32 v[8:9], v[8:9], v[48:49]\nv_pk_mul_f32 v[10:11], v[10:11], v[48:49]\nv_pk_mul_f32 v[12:13], v[12:13], v[48:49]\nv_pk_mul_f32 v[14:15], v[14:15], v[48:49]\n"
"Qk 8,8,9\nQk 9,10,11\nQk 10,12,13\nQk 11,14,15\nv_accvgpr_write_b32 a100, v8\nv_accvgpr_write_b32 a101, v9\nv_accvgpr_write_b32 a102, v10\nv_accvgpr_write_b32 a103, v11\n"
"v_accvgpr_read_b32 v0, a240\nv_accvgpr_read_b32 v1, a241\nv_accvgpr_read_b32 v2, a242\nv_accvgpr_read_b32 v3, a243\nv_accvgpr_read_b32 v4, a244\nv_accvgpr_read_b32 v5, a245\nv_accvgpr_read_b32 v6, a246\nv_accvgpr_read_b32 v7, a247\n"
"v_pk_mul_f32 v[0:1], v[0:1], v[48:49]\nv_pk_mul_f32 v[2:3], v[2:3], v[48:49]\nv_pk_mul_f32 v[4:5], v[4:5], v[48:49]\nv_pk_mul_f32 v[6:7], v[6:7], v[48:49]\nQk 0,0,1\nQk 1,2,3\nQk 2,4,5\nQk 3,6,7\n"
"v_accvgpr_write_b32 a104, v0\nv_accvgpr_write_b32 a105, v1\nv_accvgpr_write_b32 a106, v2\nv_accvgpr_write_b32 a107, v3\nv_accvgpr_read_b32 v8, a248\nv_accvgpr_read_b32 v9, a249\nv_accvgpr_read_b32 v10, a250\nv_accvgpr_read_b32 v11, a251\n"
"v_accvgpr_read_b32 v12, a252\nv_accvgpr_read_b32 v13, a253\nv_accvgpr_read_b32 v14, a254\nv_accvgpr_read_b32 v15, a255\nv_pk_mul_f32 v[8:9], v[8:9], v[48:49]\nv_pk_mul_f32 v[10:11], v[10:11], v[48:49]\nv_pk_mul_f32 v[12:13], v[12:13], v[48:49]\nv_pk_mul_f32 v[14:15], v[14:15], v[48:49]\n"
"Qk 8,8,9\nQk 9,10,11\nQk 10,12,13\nQk 11,14,15\nv_accvgpr_write_b32 a108, v8\nv_accvgpr_write_b32 a109, v9\nv_accvgpr_write_b32 a110, v10\nv_accvgpr_write_b32 a111, v11\n"
"s_mov_b32 s56, %8\ns_and_b32 s57, %9, 0xffff\ns_mov_b32 s94, 0x7ffff000\ns_mul_i32 s68, %20, 40960\ns_lshl_b32 s69, s70, 10\ns_add_u32 s68, s68, s69\ns_add_u32 s68, s68, 4096\ns_add_u32 s94, %15, s68\n"
"s_addc_u32 s95, %16, 0\ns_and_b32 s95, s95, 0xffff\ns_mov_b32 s75, 37888\ns_mov_b32 s58, 1835008\ns_mov_b32 s67, 1\ns_mul_i32 s71, %12, 57344\ns_mul_i32 s68, s92, 7168\ns_add_u32 s71, s71, s68\n"
"s_mov_b64 exec, 0\ns_cmp_eq_u32 s70, 0\ns_cbranch_scc1 .Lpg12\ns_cmp_eq_u32 s70, 1\ns_cbranch_scc1 .Lpg13\ns_cmp_eq_u32 s70, 2\ns_cbranch_scc1 .Lpg14\ns_branch .Lpg15\n"
".Ltr6:\ns_mov_b64 exec, -1\ns_branch .Lround\n.Lhook:\n.Lpg9:\ns_cbranch_execz .Ltr10\ns_cmp_eq_u32 s66, 1\ns_cbranch_scc1 .Lh1\n"
"s_cmp_eq_u32 s66, 8\ns_cbranch_scc1 .Lh8\ns_cmp_eq_u32 s66, 9\ns_cbranch_scc1 .Lh9\ns_lshr_b32 s68, s99, s66\ns_cmp_eq_u32 s68, 1\ns_cbranch_scc1 .Lh12\ns_branch .Lh11\n"
".Lh1:\ns_cmp_eq_u32 s70, 0\ns_cselect_b32 s68, 128, 0\nv_mov_b32 v66, 0x7ffff000\nv_cmp_gt_u32 vcc, s68, v60\nv_not_b32 v67, v65\ns_lshl_b32 s68, %19, 2\nv_cndmask_b32 v66, v66, v76, vcc\n"
"v_readlane_b32 s30, v67, %19\nbuffer_store_dword v67, v66, s[24:27], s68 offen offset:1024 sc1\ns_branch .Lhookret\n.Lh8:\nv_bfe_u32 v66, v60, 4, 3\ns_lshl_b32 s68, %19, 7\nv_lshlrev_b32 v66, 2, v66\ns_add_u32 s68, s68, 2293760\n"
"buffer_load_dword v78, v66, s[24:27], s68 offen offset:1024 sc1\ns_branch .Lhookret\n.Lh9:\ns_waitcnt vmcnt(32)\ns_mov_b32 s97, 0\n.Lack_l:\nv_cmp_ne_u32 vcc, s30, v78\ns_cmp_eq_u64 vcc, 0\n"
"s_cbranch_scc1 .Lack_ok\ns_sleep 1\nv_bfe_u32 v66, v60, 4, 3\ns_lshl_b32 s68, %19, 7\nv_lshlrev_b32 v66, 2, v66\ns_add_u32 s68, s68, 2293760\nbuffer_load_dword v78, v66, s[24:27], s68 offen offset:1024 sc1\ns_waitcnt vmcnt(0)\n"
"s_add_u32 s97, s97, 1\ns_cmp_lt_u32 s97, 1048576\ns_cbranch_scc1 .Lack_l\n.Lack_ok:\ns_lshl_b32 s68, %19, 4\ns_cmp_eq_u32 s70, 0\ns_cselect_b32 s68, s68, -1\nv_mov_b32 v67, 0x7ffff000\n"
"v_cmp_eq_u32 vcc, s68, v60\ns_nop 1\nv_cndmask_b32 v67, v67, v76, vcc\nv_add_u32 v66, 1, v65\nbuffer_store_dword v66, v67, s[24:27], 0 offen sc1\ns_branch .Lhookret\n.Lh11:\nbuffer_load_dword v78, v76, s[24:27], 0 offen sc1\n"
"s_branch .Lhookret\n.Lh12:\ns_waitcnt vmcnt(32)\nv_sub_u32 v66, v78, v65\nv_cmp_gt_u32 vcc, 1, v66\ns_and_b64 vcc, vcc, s[28:29]\ns_cbranch_scc1 .Lf1_w\n.Lf1_r:\n"
"s_cmp_lg_u32 s70, 3\ns_cbranch_scc1 .Lf1_n\nbuffer_inv sc1\n.Lf1_n:\ns_lshr_b32 s88, %19, 2\ns_lshl_b32 s88, s88, 4\ns_add_u32 s88, s88, s93\ns_mul_i32 s68, s88, 57344\n"
"s_mul_i32 s69, s92, 7168\ns_add_u32 s68, s68, s69\ns_add_u32 s48, %8, s68\ns_addc_u32 s49, %9, 0\ns_and_b32 s49, s49, 0xffff\ns_sub_u32 s50, 1835008, s68\ns_mul_i32 s68, s88, 163840\ns_and_b32 s69, %19, 3\n"
"s_mul_i32 s69, s69, 320\ns_add_u32 s68, s68, s69\ns_add_u32 s52, %15, s68\ns_addc_u32 s53, %16, 0\ns_and_b32 s53, s53, 0xffff\ns_sub_u32 s54, 5246976, s68\ns_add_u32 s71, s71, 458752\ns_mul_i32 s69, s92, 10240\n"
"v_bfe_u32 v66, v60, 5, 3\nv_mul_u32_u24 v66, 1280, v66\nv_and_b32 v61, 16, v60\nv_add3_u32 v61, v61, v66, s69\nv_lshrrev_b32 v66, 8, v60\nv_lshl_add_u32 v61, v66, 5, v61\nv_lshrrev_b32 v66, 9, v60\nv_mul_u32_u24 v66, 20288, v66\n"
"v_add_u32 v62, v61, v66\ns_branch .Lhookret\n.Lf1_w:\ns_mov_b32 s97, 0\n.Lf1_l:\ns_sleep 1\nbuffer_load_dword v78, v76, s[24:27], 0 offen sc1\ns_waitcnt vmcnt(0)\n"
"s_add_u32 s97, s97, 1\ns_cmp_gt_u32 s97, 1048576\ns_cbranch_scc1 .Lf1_r\nv_sub_u32 v66, v78, v65\nv_cmp_gt_u32 vcc, 1, v66\ns_and_b64 vcc, vcc, s[28:29]\ns_cbranch_scc1 .Lf1_l\ns_branch .Lf1_r\n"
".Lsa1:\nds_read_b32 v66, v74\ns_waitcnt lgkmcnt(0)\nv_cmp_gt_u32 vcc, v75, v66\ns_cbranch_vccnz .Lsa1\ns_branch .Lra1\n.Lsp1:\ns_memrealtime s[74:75]\n"
"s_waitcnt lgkmcnt(0)\ns_sub_u32 s75, s77, s74\ns_cmp_lt_u32 s75, 256\ns_cbranch_scc1 .Lsp1\ns_mov_b32 s74, s77\ns_branch .Lrp1\n.Lsa2:\nds_read_b32 v66, v74\n"
"s_waitcnt lgkmcnt(0)\nv_cmp_gt_u32 vcc, v75, v66\ns_cbranch_vccnz .Lsa2\ns_branch .Lra2\n.Lsp2:\ns_memrealtime s[74:75]\ns_waitcnt lgkmcnt(0)\ns_sub_u32 s75, s77, s74\n"
"s_cmp_lt_u32 s75, 256\ns_cbranch_scc1 .Lsp2\ns_mov_b32 s74, s77\ns_branch .Lrp2\n.Lsa3:\nds_read_b32 v66, v74\ns_waitcnt lgkmcnt(0)\nv_cmp_gt_u32 vcc, v75, v66\n"
"s_cbranch_vccnz .Lsa3\ns_branch .Lra3\n.Lsp3:\ns_memrealtime s[74:75]\ns_waitcnt lgkmcnt(0)\ns_sub_u32 s75, s77, s74\ns_cmp_lt_u32 s75, 256\ns_cbranch_scc1 .Lsp3\n"
"s_mov_b32 s74, s77\ns_branch .Lrp3\n.Lsa4:\nds_read_b32 v66, v74\ns_waitcnt lgkmcnt(0)\nv_cmp_gt_u32 vcc, v75, v66\ns_cbranch_vccnz .Lsa4\ns_branch .Lra4\n"
".Lsp4:\ns_memrealtime s[74:75]\ns_waitcnt lgkmcnt(0)\ns_sub_u32 s75, s77, s74\ns_cmp_lt_u32 s75, 256\ns_cbranch_scc1 .Lsp4\ns_mov_b32 s74, s77\ns_branch .Lrp4\n"
".Lsa5:\nds_read_b32 v66, v74\ns_waitcnt lgkmcnt(0)\nv_cmp_gt_u32 vcc, v75, v66\ns_cbranch_vccnz .Lsa5\ns_branch .Lra5\n.Lsp5:\ns_memrealtime s[74:75]\n"
"s_waitcnt lgkmcnt(0)\ns_sub_u32 s75, s77, s74\ns_cmp_lt_u32 s75, 256\ns_cbranch_scc1 .Lsp5\ns_mov_b32 s74, s77\ns_branch .Lrp5\n.Lsa30:\nds_read_b32 v66, v74\n"
"s_waitcnt lgkmcnt(0)\nv_cmp_gt_u32 vcc, v75, v66\ns_cbranch_vccnz .Lsa30\ns_branch .Lra30\n.Lsa31:\nds_read_b32 v66, v74\ns_waitcnt lgkmcnt(0)\nv_cmp_gt_u32 vcc, v75, v66\n"
"s_cbranch_vccnz .Lsa31\ns_branch .Lra31\n.Lsa32:\nds_read_b32 v66, v74\n.Lpg10:\ns_cbranch_execz .Ltr10\ns_waitcnt lgkmcnt(0)\nv_cmp_gt_u32 vcc, v75, v66\n"
"s_cbranch_vccnz .Lsa32\ns_branch .Lra32\n.Lsa30x:\nds_read_b32 v66, v74\ns_waitcnt lgkmcnt(0)\nv_cmp_gt_u32 vcc, v75, v66\ns_cbranch_vccnz .Lsa30x\ns_branch .Lra30x\n"
".Lsa31x:\nds_read_b32 v66, v74\ns_waitcnt lgkmcnt(0)\nv_cmp_gt_u32 vcc, v75, v66\ns_cbranch_vccnz .Lsa31x\ns_branch .Lra31x\n.Lso0:\nds_read_b32 v66, v74\n"
"s_waitcnt lgkmcnt(0)\nv_cmp_gt_u32 vcc, v75, v66\ns_cbranch_vccnz .Lso0\ns_branch .Lro0\n.Lso1:\nds_read_b32 v66, v74\ns_waitcnt lgkmcnt(0)\nv_cmp_gt_u32 vcc, v75, v66\n"
"s_cbranch_vccnz .Lso1\ns_branch .Lro1\n.Lso2:\nds_read_b32 v66, v74\ns_waitcnt lgkmcnt(0)\nv_cmp_gt_u32 vcc, v75, v66\ns_cbranch_vccnz .Lso2\ns_branch .Lro2\n"
".Lso3:\nds_read_b32 v66, v74\ns_waitcnt lgkmcnt(0)\nv_cmp_gt_u32 vcc, v75, v66\ns_cbranch_vccnz .Lso3\ns_branch .Lro3\n.Lso4:\nds_read_b32 v66, v74\n"
"s_waitcnt lgkmcnt(0)\nv_cmp_gt_u32 vcc, v75, v66\ns_cbranch_vccnz .Lso4\ns_branch .Lro4\n.Lso5:\nds_read_b32 v66, v74\ns_waitcnt lgkmcnt(0)\nv_cmp_gt_u32 vcc, v75, v66\n"
"s_cbranch_vccnz .Lso5\ns_branch .Lro5\n.Lso6:\nds_read_b32 v66, v74\ns_waitcnt lgkmcnt(0)\nv_cmp_gt_u32 vcc, v75, v66\ns_cbranch_vccnz .Lso6\ns_branch .Lro6\n"
".Lso7:\nds_read_b32 v66, v74\ns_waitcnt lgkmcnt(0)\nv_cmp_gt_u32 vcc, v75, v66\ns_cbranch_vccnz .Lso7\ns_branch .Lro7\n.Lso8:\nds_read_b32 v66, v74\n"
"s_waitcnt lgkmcnt(0)\nv_cmp_gt_u32 vcc, v75, v66\ns_cbranch_vccnz .Lso8\ns_branch .Lro8\n.Lso9:\nds_read_b32 v66, v74\ns_waitcnt lgkmcnt(0)\nv_cmp_gt_u32 vcc, v75, v66\n"
"s_cbranch_vccnz .Lso9\ns_branch .Lro9\n.Lso10:\nds_read_b32 v66, v74\ns_waitcnt lgkmcnt(0)\nv_cmp_gt_u32 vcc, v75, v66\ns_cbranch_vccnz .Lso10\ns_branch .Lro10\n"
".Lso11:\nds_read_b32 v66, v74\ns_waitcnt lgkmcnt(0)\nv_cmp_gt_u32 vcc, v75, v66\ns_cbranch_vccnz .Lso11\ns_branch .Lro11\n.Lso12:\nds_read_b32 v66, v74\n"
"s_waitcnt lgkmcnt(0)\nv_cmp_gt_u32 vcc, v75, v66\ns_cbranch_vccnz .Lso12\ns_branch .Lro12\n.Lso13:\nds_read_b32 v66, v74\ns_waitcnt lgkmcnt(0)\nv_cmp_gt_u32 vcc, v75, v66\n"
"s_cbranch_vccnz .Lso13\ns_branch .Lro13\n.Lso14:\nds_read_b32 v66, v74\ns_waitcnt lgkmcnt(0)\nv_cmp_gt_u32 vcc, v75, v66\ns_cbranch_vccnz .Lso14\ns_branch .Lro14\n"
".Lso15:\nds_read_b32 v66, v74\ns_waitcnt lgkmcnt(0)\nv_cmp_gt_u32 vcc, v75, v66\ns_cbranch_vccnz .Lso15\ns_branch .Lro15\n.Lso16:\nds_read_b32 v66, v74\n"
"s_waitcnt lgkmcnt(0)\nv_cmp_gt_u32 vcc, v75, v66\ns_cbranch_vccnz .Lso16\ns_branch .Lro16\n.Lso17:\nds_read_b32 v66, v74\ns_waitcnt lgkmcnt(0)\nv_cmp_gt_u32 vcc, v75, v66\n"
"s_cbranch_vccnz .Lso17\ns_branch .Lro17\n.Lso18:\nds_read_b32 v66, v74\ns_waitcnt lgkmcnt(0)\nv_cmp_gt_u32 vcc, v75, v66\ns_cbranch_vccnz .Lso18\ns_branch .Lro18\n"
".Lso19:\nds_read_b32 v66, v74\ns_waitcnt lgkmcnt(0)\nv_cmp_gt_u32 vcc, v75, v66\ns_cbranch_vccnz .Lso19\ns_branch .Lro19\n.Lso20:\nds_read_b32 v66, v74\n"
"s_waitcnt lgkmcnt(0)\nv_cmp_gt_u32 vcc, v75, v66\ns_cbranch_vccnz .Lso20\ns_branch .Lro20\n.Lso21:\nds_read_b32 v66, v74\ns_waitcnt lgkmcnt(0)\nv_cmp_gt_u32 vcc, v75, v66\n"
"s_cbranch_vccnz .Lso21\ns_branch .Lro21\n.Lso22:\nds_read_b32 v66, v74\ns_waitcnt lgkmcnt(0)\nv_cmp_gt_u32 vcc, v75, v66\ns_cbranch_vccnz .Lso22\ns_branch .Lro22\n"
".Lso23:\nds_read_b32 v66, v74\ns_waitcnt lgkmcnt(0)\nv_cmp_gt_u32 vcc, v75, v66\ns_cbranch_vccnz .Lso23\ns_branch .Lro23\n.Lso24:\nds_read_b32 v66, v74\n"
"s_waitcnt lgkmcnt(0)\nv_cmp_gt_u32 vcc, v75, v66\ns_cbranch_vccnz .Lso24\ns_branch .Lro24\n.Lso25:\nds_read_b32 v66, v74\ns_waitcnt lgkmcnt(0)\nv_cmp_gt_u32 vcc, v75, v66\n"
"s_cbranch_vccnz .Lso25\ns_branch .Lro25\n.Lso26:\nds_read_b32 v66, v74\ns_waitcnt lgkmcnt(0)\nv_cmp_gt_u32 vcc, v75, v66\ns_cbranch_vccnz .Lso26\ns_branch .Lro26\n"
".Lso27:\nds_read_b32 v66, v74\ns_waitcnt lgkmcnt(0)\nv_cmp_gt_u32 vcc, v75, v66\ns_cbranch_vccnz .Lso27\ns_branch .Lro27\n.Lso28:\nds_read_b32 v66, v74\n"
"s_waitcnt lgkmcnt(0)\nv_cmp_gt_u32 vcc, v75, v66\ns_cbranch_vccnz .Lso28\ns_branch .Lro28\n.Lso29:\nds_read_b32 v66, v74\ns_waitcnt lgkmcnt(0)\nv_cmp_gt_u32 vcc, v75, v66\n"
"s_cbranch_vccnz .Lso29\ns_branch .Lro29\n.Lso30:\nds_read_b32 v66, v74\ns_waitcnt lgkmcnt(0)\nv_cmp_gt_u32 vcc, v75, v66\ns_cbranch_vccnz .Lso30\ns_branch .Lro30\n"
".Lso31:\nds_read_b32 v66, v74\ns_waitcnt lgkmcnt(0)\nv_cmp_gt_u32 vcc, v75, v66\ns_cbranch_vccnz .Lso31\ns_branch .Lro31\n.Lso32:\nds_read_b32 v66, v74\n"
"s_waitcnt lgkmcnt(0)\nv_cmp_gt_u32 vcc, v75, v66\ns_cbranch_vccnz .Lso32\ns_branch .Lro32\n.Lso33:\nds_read_b32 v66, v74\ns_waitcnt lgkmcnt(0)\nv_cmp_gt_u32 vcc, v75, v66\n"
"s_cbranch_vccnz .Lso33\ns_branch .Lro33\n.Lso34:\nds_read_b32 v66, v74\ns_waitcnt lgkmcnt(0)\nv_cmp_gt_u32 vcc, v75, v66\ns_cbranch_vccnz .Lso34\ns_branch .Lro34\n"
".Lso35:\nds_read_b32 v66, v74\ns_waitcnt lgkmcnt(0)\nv_cmp_gt_u32 vcc, v75, v66\ns_cbranch_vccnz .Lso35\ns_branch .Lro35\n.Lso36:\nds_read_b32 v66, v74\n"
"s_waitcnt lgkmcnt(0)\nv_cmp_gt_u32 vcc, v75, v66\ns_cbranch_vccnz .Lso36\ns_branch .Lro36\n.Lso37:\nds_read_b32 v66, v74\ns_waitcnt lgkmcnt(0)\nv_cmp_gt_u32 vcc, v75, v66\n"
"s_cbranch_vccnz .Lso37\ns_branch .Lro37\n.Lso38:\nds_read_b32 v66, v74\ns_waitcnt lgkmcnt(0)\nv_cmp_gt_u32 vcc, v75, v66\ns_cbranch_vccnz .Lso38\ns_branch .Lro38\n"
".Lso39:\nds_read_b32 v66, v74\ns_waitcnt lgkmcnt(0)\nv_cmp_gt_u32 vcc, v75, v66\ns_cbranch_vccnz .Lso39\ns_branch .Lro39\n.Lso40:\nds_read_b32 v66, v74\n"
"s_waitcnt lgkmcnt(0)\nv_cmp_gt_u32 vcc, v75, v66\ns_cbranch_vccnz .Lso40\ns_branch .Lro40\n.Lso41:\nds_read_b32 v66, v74\ns_waitcnt lgkmcnt(0)\nv_cmp_gt_u32 vcc, v75, v66\n"
"s_cbranch_vccnz .Lso41\ns_branch .Lro41\n.Lso42:\nds_read_b32 v66, v74\ns_waitcnt lgkmcnt(0)\nv_cmp_gt_u32 vcc, v75, v66\ns_cbranch_vccnz .Lso42\ns_branch .Lro42\n"
".Lso43:\nds_read_b32 v66, v74\ns_waitcnt lgkmcnt(0)\nv_cmp_gt_u32 vcc, v75, v66\ns_cbranch_vccnz .Lso43\ns_branch .Lro43\n.Lso44:\nds_read_b32 v66, v74\n"
"s_waitcnt lgkmcnt(0)\nv_cmp_gt_u32 vcc, v75, v66\ns_cbranch_vccnz .Lso44\ns_branch .Lro44\n.Lso45:\nds_read_b32 v66, v74\ns_waitcnt lgkmcnt(0)\nv_cmp_gt_u32 vcc, v75, v66\n"
"s_cbranch_vccnz .Lso45\ns_branch .Lro45\n.Lso46:\nds_read_b32 v66, v74\ns_waitcnt lgkmcnt(0)\nv_cmp_gt_u32 vcc, v75, v66\ns_cbranch_vccnz .Lso46\ns_branch .Lro46\n"
".Lso47:\nds_read_b32 v66, v74\ns_waitcnt lgkmcnt(0)\nv_cmp_gt_u32 vcc, v75, v66\ns_cbranch_vccnz .Lso47\ns_branch .Lro47\n.Lso48:\nds_read_b32 v66, v74\n"
"s_waitcnt lgkmcnt(0)\nv_cmp_gt_u32 vcc, v75, v66\ns_cbranch_vccnz .Lso48\ns_branch .Lro48\n.Lso49r0:\nds_read_b32 v66, v74\ns_waitcnt lgkmcnt(0)\nv_cmp_gt_u32 vcc, v75, v66\n"
"s_cbranch_vccnz .Lso49r0\ns_branch .Lro49r0\n.Lso50r0:\nds_read_b32 v66, v74\ns_waitcnt lgkmcnt(0)\nv_cmp_gt_u32 vcc, v75, v66\ns_cbranch_vccnz .Lso50r0\ns_branch .Lro50r0\n"
".Lso51r0:\nds_read_b32 v66, v74\ns_waitcnt lgkmcnt(0)\nv_cmp_gt_u32 vcc, v75, v66\ns_cbranch_vccnz .Lso51r0\ns_branch .Lro51r0\n.Lso52r0:\nds_read_b32 v66, v74\n"
"s_waitcnt lgkmcnt(0)\nv_cmp_gt_u32 vcc, v75, v66\ns_cbranch_vccnz .Lso52r0\ns_branch .Lro52r0\n.Lso53r0:\nds_read_b32 v66, v74\ns_waitcnt lgkmcnt(0)\nv_cmp_gt_u32 vcc, v75, v66\n"
"s_cbranch_vccnz .Lso53r0\ns_branch .Lro53r0\n.Lso54r0:\nds_read_b32 v66, v74\ns_waitcnt lgkmcnt(0)\nv_cmp_gt_u32 vcc, v75, v66\ns_cbranch_vccnz .Lso54r0\ns_branch .Lro54r0\n"
".Lso55r0:\nds_read_b32 v66, v74\ns_waitcnt lgkmcnt(0)\nv_cmp_gt_u32 vcc, v75, v66\ns_cbranch_vccnz .Lso55r0\ns_branch .Lro55r0\n.Lso56r0:\nds_read_b32 v66, v74\n"
"s_waitcnt lgkmcnt(0)\nv_cmp_gt_u32 vcc, v75, v66\ns_cbranch_vccnz .Lso56r0\ns_branch .Lro56r0\n.Lso57r0:\nds_read_b32 v66, v74\ns_waitcnt lgkmcnt(0)\nv_cmp_gt_u32 vcc, v75, v66\n"
"s_cbranch_vccnz .Lso57r0\ns_branch .Lro57r0\n.Lso58r0:\nds_read_b32 v66, v74\ns_waitcnt lgkmcnt(0)\nv_cmp_gt_u32 vcc, v75, v66\ns_cbranch_vccnz .Lso58r0\ns_branch .Lro58r0\n"
".Lso59r0:\nds_read_b32 v66, v74\ns_waitcnt lgkmcnt(0)\nv_cmp_gt_u32 vcc, v75, v66\ns_cbranch_vccnz .Lso59r0\ns_branch .Lro59r0\n.Lso60r0:\nds_read_b32 v66, v74\n"
"s_waitcnt lgkmcnt(0)\nv_cmp_gt_u32 vcc, v75, v66\ns_cbranch_vccnz .Lso60r0\ns_branch .Lro60r0\n.Lso61r0:\nds_read_b32 v66, v74\ns_waitcnt lgkmcnt(0)\nv_cmp_gt_u32 vcc, v75, v66\n"
"s_cbranch_vccnz .Lso61r0\ns_branch .Lro61r0\n.Lso62r0:\nds_read_b32 v66, v74\ns_waitcnt lgkmcnt(0)\nv_cmp_gt_u32 vcc, v75, v66\ns_cbranch_vccnz .Lso62r0\ns_branch .Lro62r0\n"
".Lso63r0:\nds_read_b32 v66, v74\ns_waitcnt lgkmcnt(0)\nv_cmp_gt_u32 vcc, v75, v66\ns_cbranch_vccnz .Lso63r0\ns_branch .Lro63r0\n.Lso49r1:\nds_read_b32 v66, v74\n"
"s_waitcnt lgkmcnt(0)\nv_cmp_gt_u32 vcc, v75, v66\ns_cbranch_vccnz .Lso49r1\ns_branch .Lro49r1\n.Lso50r1:\nds_read_b32 v66, v74\ns_waitcnt lgkmcnt(0)\nv_cmp_gt_u32 vcc, v75, v66\n"
"s_cbranch_vccnz .Lso50r1\ns_branch .Lro50r1\n.Lso51r1:\nds_read_b32 v66, v74\ns_waitcnt lgkmcnt(0)\nv_cmp_gt_u32 vcc, v75, v66\ns_cbranch_vccnz .Lso51r1\ns_branch .Lro51r1\n"
".Lso52r1:\nds_read_b32 v66, v74\ns_waitcnt lgkmcnt(0)\nv_cmp_gt_u32 vcc, v75, v66\ns_cbranch_vccnz .Lso52r1\ns_branch .Lro52r1\n.Lso53r1:\nds_read_b32 v66, v74\n"
"s_waitcnt lgkmcnt(0)\nv_cmp_gt_u32 vcc, v75, v66\ns_cbranch_vccnz .Lso53r1\ns_branch .Lro53r1\n.Lso54r1:\nds_read_b32 v66, v74\ns_waitcnt lgkmcnt(0)\nv_cmp_gt_u32 vcc, v75, v66\n"
"s_cbranch_vccnz .Lso54r1\ns_branch .Lro54r1\n.Lso55r1:\nds_read_b32 v66, v74\ns_waitcnt lgkmcnt(0)\nv_cmp_gt_u32 vcc, v75, v66\ns_cbranch_vccnz .Lso55r1\ns_branch .Lro55r1\n"
".Lso56r1:\nds_read_b32 v66, v74\ns_waitcnt lgkmcnt(0)\nv_cmp_gt_u32 vcc, v75, v66\ns_cbranch_vccnz .Lso56r1\ns_branch .Lro56r1\n.Lso57r1:\nds_read_b32 v66, v74\n"
"s_waitcnt lgkmcnt(0)\nv_cmp_gt_u32 vcc, v75, v66\ns_cbranch_vccnz .Lso57r1\ns_branch .Lro57r1\n.Lso58r1:\nds_read_b32 v66, v74\ns_waitcnt lgkmcnt(0)\nv_cmp_gt_u32 vcc, v75, v66\n"
"s_cbranch_vccnz .Lso58r1\ns_branch .Lro58r1\n.Lso59r1:\nds_read_b32 v66, v74\ns_waitcnt lgkmcnt(0)\nv_cmp_gt_u32 vcc, v75, v66\ns_cbranch_vccnz .Lso59r1\ns_branch .Lro59r1\n"
".Lso60r1:\nds_read_b32 v66, v74\ns_waitcnt lgkmcnt(0)\nv_cmp_gt_u32 vcc, v75, v66\ns_cbranch_vccnz .Lso60r1\ns_branch .Lro60r1\n.Lso61r1:\nds_read_b32 v66, v74\n"
"s_waitcnt lgkmcnt(0)\nv_cmp_gt_u32 vcc, v75, v66\ns_cbranch_vccnz .Lso61r1\ns_branch .Lro61r1\n.Lso62r1:\nds_read_b32 v66, v74\ns_waitcnt lgkmcnt(0)\nv_cmp_gt_u32 vcc, v75, v66\n"
"s_cbranch_vccnz .Lso62r1\ns_branch .Lro62r1\n.Lso63r1:\nds_read_b32 v66, v74\ns_waitcnt lgkmcnt(0)\nv_cmp_gt_u32 vcc, v75, v66\ns_cbranch_vccnz .Lso63r1\ns_branch .Lro63r1\n"
".Ltail1:\n.Lpg11:\ns_cbranch_execz .Ltr10\nQcvaa 0,112,32,56\nv_add_u32 v75, 1, v75\nQk 38,4,5\nQmvaa 0,116,36\ns_waitcnt vmcnt(0)\n"
"ds_write_b32 v73, v75\nQmvaa 0,120,40\nQe 7\nQmvaa 0,124,44\nQk 39,6,7\nQcvaa 4,128,32,56\nQe 8\nQmvaa 4,132,36\n"
"Qe 9\nQmvaa 4,136,40\nQe 10\nQmvaa 4,140,44\ns_waitcnt lgkmcnt(1)\nQk 40,8,9\nQmvav 96,144,44\nQe 11\n"
"Qmvav 96,148,36\nQk 41,10,11\nQmvav 100,152,44\nQe 12\nQmvav 100,156,36\nQe 13\nQmvav 104,160,44\nQe 14\n"
"Qmvav 104,164,36\nQe 15\nQmvav 108,168,44\nQk 42,12,13\nQk 43,14,15\nQmvav 108,172,36\nQe 0\nQcvaa 8,112,48,56\n"
"Qe 1\nQmvaa 8,116,52\nQe 2\nQmvaa 8,120,56\nds_read_b32 v66, v74\nQk 44,0,1\nQmvaa 8,124,60\nQe 3\n"
"Qcvaa 12,128,48,56\nQk 45,2,3\nQmvaa 12,132,52\nQe 4\nv_smfmac_f32_16x16x64_bf16 a[176:179], a[204:207], v[36:43], v77\nQmvaa 12,136,56\ns_waitcnt lgkmcnt(0)\nv_cmp_gt_u32 vcc, v75, v66\n"
"Qmvaa 12,140,60\ns_cbranch_vccnz .Lsa30x\n.Lra30x:\nQmvav 128,144,48\ns_add_u32 m0, s91, 132096\ns_mov_b32 s94, 0\nQmvav 128,148,40\nbuffer_load_dwordx4 v60, s[48:51], s94 offen lds\n"
"Qmvav 132,152,48\nQe 5\nQmvav 132,156,40\nQe 6\nQmvav 136,160,48\nQe 7\nQmvav 136,164,40\nQk 46,4,5\n"
"Qk 47,6,7\nQmvav 140,168,48\nbuffer_load_dwordx4 v60, s[48:51], s94 offen offset:1024 lds\nQmvav 140,172,40\nQe 8\nQcvaa 0,112,64,56\nQe 9\nQmvaa 0,116,68\n"
"Qe 10\nQmvaa 0,120,72\nQe 11\nQmvaa 0,124,76\nbuffer_load_dwordx4 v60, s[48:51], s94 offen offset:2048 lds\nQcvaa 4,128,64,56\nQk 48,8,9\nQk 49,10,11\n"
"Qmvaa 4,132,68\nQe 12\nQmvaa 4,136,72\nQe 13\nQmvaa 4,140,76\ns_mov_b32 s95, 1024\nQe 14\nQmvav 160,144,52\n"
"buffer_load_dwordx4 v61, s[52:55], s95 offen offset:3072 lds\nQmvav 160,148,44\nQe 15\nQmvav 164,152,52\nQk 50,12,13\nQk 51,14,15\nQmvav 164,156,44\nQe 0\n"
"Qmvav 168,160,52\ns_add_u32 m0, s91, 136192\ns_mov_b32 s95, 4224\nQmvav 168,164,44\nbuffer_load_dwordx4 v62, s[52:55], s95 offen lds\nQmvav 172,168,52\nQe 1\nQmvav 172,172,44\n"
"Qe 2\nQcvaa 8,112,80,56\nQe 3\nQmvaa 8,116,84\ns_mov_b32 s95, 23616\nQk 16,0,1\nQk 17,2,3\nQmvaa 8,120,88\n"
"buffer_load_dwordx4 v61, s[52:55], s95 offen offset:1024 lds\nQmvaa 8,124,92\nQe 4\nQcvaa 12,128,80,56\nQe 5\nQmvaa 12,132,84\nQe 6\nv_smfmac_f32_16x16x64_bf16 a[180:183], a[204:207], v[44:51], v77\n"
"s_add_u32 m0, s91, 0\ns_mov_b32 s94, 3072\nQk 18,4,5\nQmvaa 12,136,88\nbuffer_load_dwordx4 v60, s[48:51], s94 offen lds\nQmvaa 12,140,92\nQe 7\nQmvav 192,144,20\n"
"Qk 19,6,7\nQmvav 192,148,48\nQmvav 196,152,20\nQe 8\nQmvav 196,156,48\nbuffer_load_dwordx4 v60, s[48:51], s94 offen offset:1024 lds\nQmvav 200,160,20\nQe 9\n"
"Qmvav 200,164,48\nQe 10\nQmvav 204,168,20\nQmvav 204,172,48\nQe 11\nQcvaa 0,112,96,56\nQda 112,68,33024\nbuffer_load_dwordx4 v60, s[48:51], s94 offen offset:2048 lds\n"
"Qk 20,8,9\nQmvaa 0,116,100\nQda 116,68,33536\nQk 21,10,11\nQmvaa 0,120,104\nQda 120,68,34048\nQmvaa 0,124,108\nQda 124,68,34560\n"
"Qcvaa 4,128,96,56\nQda 128,68,35072\nQmvaa 4,132,100\nQda 132,68,35584\nbuffer_load_dwordx4 v60, s[48:51], s94 offen offset:3072 lds\nQmvaa 4,136,104\nQda 136,68,36096\nQmvaa 4,140,108\n"
"Qda 140,68,36608\nQmvav 224,144,24\nQe 12\nQmvav 224,148,16\ns_add_u32 m0, s91, 4096\ns_mov_b32 s95, 4288\nQmvav 228,152,24\nbuffer_load_dwordx4 v61, s[52:55], s95 offen lds\n"
"Qe 13\nQmvav 228,156,16\nQe 14\nQmvav 232,160,24\nQe 15\nQmvav 232,164,16\nQk 22,12,13\nQk 23,14,15\n"
"Qmvav 236,168,24\ns_mov_b32 s95, 23744\nQe 0\nQmvav 236,172,16\nbuffer_load_dwordx4 v61, s[52:55], s95 offen offset:1024 lds\ns_waitcnt lgkmcnt(0)\nQcvaa 8,112,0,56\nQe 1\n"
"Qmvaa 8,116,4\nv_subrev_u32 v68, 66048, v68\nQk 24,0,1\nQmvaa 8,120,8\nQe 2\nQmvaa 8,124,12\ns_add_u32 m0, s91, 24576\ns_mov_b32 s94, 14336\n"
"Qe 3\nQcvaa 12,128,0,56\nbuffer_load_dwordx4 v60, s[48:51], s94 offen lds\nQk 25,2,3\nQmvaa 12,132,4\nQe 4\nv_smfmac_f32_16x16x64_bf16 a[184:187], a[204:207], v[16:23], v77\nQmvaa 12,136,8\n"
"Qe 5\nQmvaa 12,140,12\nQe 6\nQmaav 208,144,28\nbuffer_load_dwordx4 v60, s[48:51], s94 offen offset:1024 lds\nQmaav 208,148,20\nQmaav 212,152,28\nQk 26,4,5\n"
"Qmaav 212,156,20\nQmaav 216,160,28\nQmaav 216,164,20\nbuffer_load_dwordx4 v60, s[48:51], s94 offen offset:2048 lds\nQe 7\nQmaav 220,168,28\nQk 27,6,7\nQmaav 220,172,20\n"
"Qe 8\nQcvaa 0,112,16,56\nQe 9\nQmvaa 0,116,20\ns_mov_b32 s95, 41984\nQe 10\nQmvaa 0,120,24\nbuffer_load_dwordx4 v61, s[52:55], s95 offen offset:3072 lds\n"
"Qe 11\nQmvaa 0,124,28\nQk 28,8,9\nQk 29,10,11\nQcvaa 4,128,16,56\nQe 12\nQmvaa 4,132,20\nQe 13\n"
"Qmvaa 4,136,24\ns_add_u32 m0, s91, 28672\ns_mov_b32 s95, 45184\nQe 14\nQmvaa 4,140,28\nbuffer_load_dwordx4 v62, s[52:55], s95 offen lds\nQe 15\nQmaav 240,144,32\n"
"Qta 144,71,0\nQta 146,71,2048\nQmaav 240,148,24\nQta 148,71,33024\nQta 150,71,35072\nQmaav 244,152,32\nQta 152,71,256\nQta 154,71,2304\n"
"Qmaav 244,156,24\nQta 156,71,33280\nQta 158,71,35328\ns_mov_b32 s95, 64576\nQmaav 248,160,32\nQta 160,71,512\nQta 162,71,2560\nbuffer_load_dwordx4 v61, s[52:55], s95 offen offset:1024 lds\n"
"Qmaav 248,164,24\nQta 164,71,33536\nQta 166,71,35584\nQmaav 252,168,32\nQta 168,71,768\nQta 170,71,2816\nQmaav 252,172,24\nQta 172,71,33792\n"
"Qta 174,71,35840\nQcvaa 8,112,32,56\nv_add_u32 v75, 1, v75\nQk 30,12,13\nQmvaa 8,116,36\nds_write_b32 v73, v75\nQmvaa 8,120,40\nQk 31,14,15\n"
"Qmvaa 8,124,44\nQe 0\nQcvaa 12,128,32,56\nQe 1\nQmvaa 12,132,36\nQe 2\nv_smfmac_f32_16x16x64_bf16 a[188:191], a[204:207], v[24:31], v77\nQk 32,0,1\n"
"Qmvaa 12,136,40\ns_waitcnt lgkmcnt(1)\nQmvaa 12,140,44\nQe 3\nQmvav 80,144,36\nQk 33,2,3\nQmvav 80,148,28\nQe 4\n"
"Qmvav 84,152,36\nQe 5\nQmvav 84,156,28\nQe 6\nQmvav 88,160,36\nQe 7\nQmvav 88,164,28\nQk 34,4,5\n"
"Qk 35,6,7\nQmvav 92,168,36\nQe 8\nQmvav 92,172,28\n.Lpg12:\ns_cbranch_execz .Ltr6\nQe 9\nQcvaa 0,112,48,56\n"
"Qe 10\nQmvaa 0,116,52\nds_read_b32 v66, v74\nQk 36,8,9\nQmvaa 0,120,56\nQe 11\nQmvaa 0,124,60\nQk 37,10,11\n"
"Qcvaa 4,128,48,56\nQe 12\nQmvaa 4,132,52\nQe 13\nQmvaa 4,136,56\ns_waitcnt lgkmcnt(0)\nv_cmp_gt_u32 vcc, v75, v66\nQmvaa 4,140,60\n"
"s_cbranch_vccnz .Lsa31x\n.Lra31x:\nQk 38,12,13\nQmvav 112,144,40\ns_add_u32 m0, s91, 49152\ns_mov_b32 s94, 17408\nQmvav 112,148,32\nbuffer_load_dwordx4 v60, s[48:51], s94 offen lds\n"
"Qmvav 116,152,40\nQe 14\nQmvav 116,156,32\nQe 15\nQmvav 120,160,40\nQk 39,14,15\nQmvav 120,164,32\nQe 0\n"
"Qmvav 124,168,40\nbuffer_load_dwordx4 v60, s[48:51], s94 offen offset:1024 lds\nQmvav 124,172,32\nQe 1\nQcvaa 8,112,64,56\nQe 2\nQmvaa 8,116,68\nQe 3\n"
"Qmvaa 8,120,72\nQk 40,0,1\nQk 41,2,3\nQmvaa 8,124,76\nbuffer_load_dwordx4 v60, s[48:51], s94 offen offset:2048 lds\nQcvaa 12,128,64,56\nQe 4\nQmvaa 12,132,68\n"
"Qe 5\nv_smfmac_f32_16x16x64_bf16 a[192:195], a[204:207], v[32:39], v77\nQk 42,4,5\nQmvaa 12,136,72\nQe 6\nQmvaa 12,140,76\nbuffer_load_dwordx4 v60, s[48:51], s94 offen offset:3072 lds\nQmvav 144,144,44\n"
"Qe 7\nQmvav 144,148,36\nQk 43,6,7\nQmvav 148,152,44\nQe 8\nQmvav 148,156,36\ns_add_u32 m0, s91, 53248\ns_mov_b32 s95, 45248\n"
"Qmvav 152,160,44\nbuffer_load_dwordx4 v61, s[52:55], s95 offen lds\nQmvav 152,164,36\nQe 9\nQmvav 156,168,44\nQe 10\nQmvav 156,172,36\nQe 11\n"
"Qcvaa 0,112,80,56\ns_mov_b32 s95, 64704\nQk 44,8,9\nQk 45,10,11\nQmvaa 0,116,84\nbuffer_load_dwordx4 v61, s[52:55], s95 offen offset:1024 lds\nQmvaa 0,120,88\nQe 12\n"
"Qmvaa 0,124,92\nQe 13\nQcvaa 4,128,80,56\nQe 14\nQmvaa 4,132,84\ns_add_u32 m0, s91, 73728\ns_mov_b32 s94, 28672\nQe 15\n"
"Qmvaa 4,136,88\nbuffer_load_dwordx4 v60, s[48:51], s94 offen lds\nQmvaa 4,140,92\nQk 46,12,13\nQk 47,14,15\nQmvav 176,144,48\nQmvav 176,148,40\nQmvav 180,152,48\n"
"Qmvav 180,156,40\nbuffer_load_dwordx4 v60, s[48:51], s94 offen offset:1024 lds\nQmvav 184,160,48\nQe 0\nQmvav 184,164,40\nQe 1\nQmvav 188,168,48\nQmvav 188,172,40\n"
"Qe 2\nQcvaa 8,112,96,56\nbuffer_load_dwordx4 v60, s[48:51], s94 offen offset:2048 lds\nQk 48,0,1\nQmvaa 8,116,100\nQmvaa 8,120,104\nQmvaa 8,124,108\nQcvaa 12,128,96,56\n"
"s_mov_b32 s95, 82944\nQmvaa 12,132,100\nbuffer_load_dwordx4 v61, s[52:55], s95 offen offset:3072 lds\nv_smfmac_f32_16x16x64_bf16 a[196:199], a[204:207], v[40:47], v77\nQmvaa 12,136,104\nQmvaa 12,140,108\nQmvav 208,144,16\nQta 112,71,34048\n"
"s_add_u32 m0, s91, 77824\ns_mov_b32 s95, 86144\nQmvav 208,148,44\nQta 114,71,36096\nbuffer_load_dwordx4 v62, s[52:55], s95 offen lds\nQe 3\nQmvav 212,152,16\nQta 116,71,34304\n"
"Qk 49,2,3\nQmvav 212,156,44\nQta 118,71,36352\nQe 4\nQmvav 216,160,16\nQta 120,71,34560\nQe 5\nQmvav 216,164,44\n"
"Qta 122,71,36608\ns_mov_b32 s95, 105536\nQe 6\nQmvav 220,168,16\nQta 124,71,34816\nbuffer_load_dwordx4 v61, s[52:55], s95 offen offset:1024 lds\nQe 7\nQmvav 220,172,44\n"
"Qta 126,71,36864\nQk 50,4,5\nQk 51,6,7\nQe 8\nQe 9\nQe 10\nQe 11\nQk 52,8,9\n"
"Qk 53,10,11\nQe 12\nQe 13\nQmvav 240,144,20\nQe 14\nQmvav 240,148,48\nQmvav 244,152,20\nQmvav 244,156,48\n"
"Qk 54,12,13\nQmvav 248,160,20\nQmvav 248,164,48\nQmvav 252,168,20\nQe 15\nQmvav 252,172,48\nQk 55,14,15\ns_nop 1\n"
"v_smfmac_f32_16x16x64_bf16 a[200:203], a[204:207], v[48:55], v77\nQmaav 224,144,24\nv_accvgpr_read_b32 v0, a176\nv_accvgpr_read_b32 v1, a177\nQmaav 224,148,52\nv_accvgpr_read_b32 v6, a178\nv_accvgpr_read_b32 v7, a179\nQmaav 228,152,24\n"
"v_accvgpr_read_b32 v8, a180\nv_accvgpr_read_b32 v9, a181\nQmaav 228,156,52\nv_accvgpr_read_b32 v14, a182\nv_accvgpr_read_b32 v15, a183\nQmaav 232,160,24\nv_accvgpr_read_b32 v16, a184\nv_accvgpr_read_b32 v17, a185\n"
"Qmaav 232,164,52\nv_accvgpr_read_b32 v22, a186\nv_accvgpr_read_b32 v23, a187\nQmaav 236,168,24\nv_accvgpr_read_b32 v2, a190\nv_accvgpr_read_b32 v3, a191\nQmaav 236,172,52\ns_waitcnt lgkmcnt(0)\n"
"v_accvgpr_read_b32 v24, a188\nQmvav 96,112,28\nv_accvgpr_read_b32 v25, a189\nv_accvgpr_read_b32 v4, a192\nQmvav 100,116,28\nv_accvgpr_read_b32 v5, a193\nv_accvgpr_read_b32 v10, a194\nQmvav 104,120,28\n"
"v_accvgpr_read_b32 v11, a195\nv_accvgpr_read_b32 v12, a196\nQmvav 108,124,28\nv_accvgpr_read_b32 v13, a197\nv_accvgpr_read_b32 v18, a198\nQmvav 128,112,32\nv_accvgpr_read_b32 v19, a199\nv_accvgpr_read_b32 v20, a200\n"
"Qmvav 132,116,32\nv_accvgpr_read_b32 v21, a201\nv_accvgpr_read_b32 v26, a202\nQmvav 136,120,32\nv_accvgpr_read_b32 v27, a203\nQa 8,8,9\nQmvav 140,124,32\nQa 10,10,11\n"
"Qa 8,8,10\nQmvav 160,112,36\nQa 16,16,17\nQa 18,18,19\nQmvav 164,116,36\nQa 16,16,18\nQa 4,4,5\nQmvav 168,120,36\n"
"Qa 6,6,7\nQa 4,4,6\nQmvav 172,124,36\nQa 12,12,13\nQa 14,14,15\nQmvav 192,112,40\nQa 12,12,14\nQa 0,0,1\n"
"Qmvav 196,116,40\nQa 2,2,3\nQa 0,0,2\nQmvav 200,120,40\nQa 24,24,25\nQa 26,26,27\nQmvav 204,124,40\nQa 24,24,26\n"
"Qa 20,20,21\nQmvav 224,112,44\nQa 22,22,23\nQa 20,20,22\nQmvav 228,116,44\nv_max3_u32 v9, v8, v16, v4\nv_max3_u32 v9, v9, v12, v0\nQmvav 232,120,44\n"
"v_max3_u32 v9, v9, v24, v20\nv_min3_u32 v11, v8, v16, v4\nQmvav 236,124,44\nv_min3_u32 v11, v11, v12, v0\nv_min3_u32 v11, v11, v24, v20\nQmaav 208,112,48\nQmaav 212,116,48\nQmaav 216,120,48\n"
"Qmaav 220,124,48\nQmaav 240,112,52\nQmaav 244,116,52\nQmaav 248,120,52\nQmaav 252,124,52\nv_cmp_lt_u32 vcc, 0x44800000, v9\ns_mov_b64 s[74:75], vcc\nv_cmp_gt_u32 vcc, 0x2b800000, v11\n"
"s_or_b64 s[74:75], s[74:75], vcc\ns_cmp_lg_u64 s[74:75], 0\ns_cbranch_scc1 .Lfb\n.Lfbret1:\nv_rcp_f32 v52, v0\nv_rcp_f32 v53, v4\nv_rcp_f32 v54, v8\nv_rcp_f32 v55, v12\n"
"v_rcp_f32 v56, v16\nv_rcp_f32 v57, v20\nv_rcp_f32 v58, v24\ns_mov_b32 s56, %8\ns_and_b32 s57, %9, 0xffff\ns_mov_b32 s58, 1835008\ns_lshl_b32 s68, %19, 4\ns_cmp_eq_u32 s70, 0\n"
"s_cselect_b32 s68, s68, -1\nv_mov_b32 v77, 0x7ffff000\nv_cmp_eq_u32 vcc, s68, v60\ns_nop 1\nv_cndmask_b32 v77, v77, v76, vcc\ns_mul_i32 s68, s92, 6144\ns_add_u32 s68, s68, %10\nv_add_u32 v45, s68, v60\n"
"v_add_u32 v46, 49152, v45\nv_add_u32 v47, 98304, v45\ns_lshl_b32 s68, s93, 8\ns_add_u32 s68, s68, %10\nv_lshrrev_b32 v66, 9, v60\nv_mul_u32_u24 v66, 6144, v66\nv_bfe_u32 v67, v60, 1, 8\nv_add3_u32 v48, v66, v67, s68\n"
"v_add_u32 v49, 49152, v48\nv_add_u32 v50, 98304, v48\ns_mov_b64 exec, 0\ns_cmp_eq_u32 s70, 0\ns_cbranch_scc1 .Lpg16\ns_cmp_eq_u32 s70, 1\ns_cbranch_scc1 .Lpg17\ns_cmp_eq_u32 s70, 2\n"
"s_cbranch_scc1 .Lpg18\ns_branch .Lpg19\n.Ltr8:\ns_mov_b64 exec, -1\ns_waitcnt vmcnt(18)\ns_barrier\nQda 112,47,33792\nQda 116,47,34816\n"
"Qda 120,47,35840\nQtv 0,50,36864\nQtv 2,50,38400\nQtv 4,50,37376\nQtv 6,50,38912\nQtv 8,50,37888\nQtv 10,50,39424\nQda 140,47,46080\n"
"Qda 144,47,47104\nQda 148,47,48128\nQtv 20,50,49152\nQtv 22,50,50688\nQtv 24,50,49664\nQtv 26,50,51200\nQtv 28,50,50176\nQtv 30,50,51712\n"
"Qtv 12,48,4096\nQtv 14,48,5120\ns_add_u32 m0, s91, 98304\ns_mov_b32 s94, 31744\nbuffer_load_dwordx4 v60, s[48:51], s94 offen lds\nQtv 32,48,16384\nbuffer_load_dwordx4 v60, s[48:51], s94 offen offset:1024 lds\nQtv 34,48,17408\n"
"buffer_load_dwordx4 v60, s[48:51], s94 offen offset:2048 lds\nv_add_u32 v75, 1, v75\ns_waitcnt vmcnt(15)\nds_write_b32 v73, v75\nv_mul_f32 v80, v52, v80\nv_mul_f32 v81, v52, v81\nv_mul_f32 v82, v52, v82\nv_mul_f32 v83, v52, v83\n"
"v_mul_f32 v84, v52, v84\nv_mul_f32 v85, v52, v85\nv_mul_f32 v86, v52, v86\nv_mul_f32 v87, v52, v87\nQk 80,80,81\nQk 81,82,83\nQk 82,84,85\nQk 83,86,87\n"
"v_mul_f32 v112, v53, v112\nv_mul_f32 v113, v53, v113\nv_mul_f32 v114, v53, v114\nv_mul_f32 v115, v53, v115\nv_mul_f32 v116, v53, v116\nv_mul_f32 v117, v53, v117\nv_mul_f32 v118, v53, v118\nv_mul_f32 v119, v53, v119\n"
"Qk 112,112,113\nQk 113,114,115\nQk 114,116,117\nQk 115,118,119\ns_waitcnt lgkmcnt(7)\nQzava 0,0,112\nbuffer_load_dwordx4 v60, s[48:51], s94 offen offset:3072 lds\nQmava 0,20,140\n"
"Qtv 16,48,4608\ns_add_u32 m0, s91, 102400\ns_mov_b32 s95, 86208\nQzava 20,0,116\nbuffer_load_dwordx4 v61, s[52:55], s95 offen lds\nQmava 20,20,144\nQtv 18,48,5632\ns_mov_b32 s95, 105664\n"
"Qzava 40,0,120\nbuffer_load_dwordx4 v61, s[52:55], s95 offen offset:1024 lds\nQmava 40,20,148\nQtv 36,48,16896\ns_and_b32 s66, %19, 3\nQzava 4,4,112\nQtv 38,48,17920\ns_mul_i32 s66, s66, 327680\n"
"Qmava 4,24,140\nds_read_b32 v66, v74\ns_add_u32 s66, s66, 1310720\nQzava 24,4,116\nQda 124,45,0\nv_add_u32 v44, s91, v60\nQmava 24,24,144\nQda 152,45,12288\n"
"v_mul_f32 v144, v54, v144\nQzava 44,4,120\nQda 128,45,1024\nv_mul_f32 v145, v54, v145\nQmava 44,24,148\ns_waitcnt lgkmcnt(3)\nv_cmp_gt_u32 vcc, v75, v66\nQzava 8,8,112\n"
"s_cbranch_vccnz .Lso0\n.Lro0:\nv_mul_f32 v146, v54, v146\nQmava 8,28,140\nQda 156,45,13312\ns_add_u32 m0, s91, 132096\ns_mov_b32 s94, 43008\nQzava 28,8,116\n"
"buffer_load_dwordx4 v60, s[48:51], s94 offen lds\nQmava 28,28,144\nQda 132,45,2048\nv_mul_f32 v147, v54, v147\nQzava 48,8,120\nQda 160,45,14336\nv_mul_f32 v148, v54, v148\nQmava 48,28,148\n"
"Qda 136,45,3072\nv_mul_f32 v149, v54, v149\ns_waitcnt lgkmcnt(8)\nQzava 12,12,112\nbuffer_load_dwordx4 v60, s[48:51], s94 offen offset:1024 lds\nQmava 12,32,140\nQda 164,45,15360\nv_mul_f32 v150, v54, v150\n"
"Qzava 32,12,116\nv_mul_f32 v151, v54, v151\nQk 144,144,145\nQmava 32,32,144\nQk 145,146,147\nQk 146,148,149\nQzava 52,12,120\nQk 147,150,151\n"
"v_mul_f32 v176, v55, v176\nQmava 52,32,148\nbuffer_load_dwordx4 v60, s[48:51], s94 offen offset:2048 lds\nQzava 16,16,112\nQda 112,45,24576\nv_mul_f32 v177, v55, v177\nQmava 16,36,140\nQda 140,45,36864\n"
"v_mul_f32 v178, v55, v178\nQzava 36,16,116\nQda 116,45,25600\nv_mul_f32 v179, v55, v179\nQmava 36,36,144\nQda 144,45,37888\ns_mov_b32 s95, 123904\nQzava 56,16,120\n"
"buffer_load_dwordx4 v61, s[52:55], s95 offen offset:3072 lds\nv_add_u32 v75, 1, v75\nQmava 56,36,148\ns_waitcnt vmcnt(16)\nds_write_b32 v73, v75\ns_waitcnt lgkmcnt(5)\nQzava 60,0,124\nQda 120,45,26624\n"
"s_add_u32 s88, s71, 0\nQmava 60,20,152\nbuffer_store_dwordx4 v[80:83], v60, s[56:59], s88 offen offset:0 sc1\ns_add_u32 m0, s91, 136192\ns_mov_b32 s95, 127104\nQzava 80,0,128\nbuffer_load_dwordx4 v62, s[52:55], s95 offen lds\nQmava 80,20,156\n"
"Qda 148,45,38912\nv_mul_f32 v180, v55, v180\nQzava 100,0,132\nv_mul_f32 v181, v55, v181\nv_mul_f32 v182, v55, v182\nQmava 100,20,160\nv_mul_f32 v183, v55, v183\nQk 176,176,177\n"
"Qzava 184,0,136\nQtv 0,48,27648\nQk 177,178,179\nQmava 184,20,164\nds_read_b32 v66, v74\ns_mov_b32 s95, 146496\nQzava 64,4,124\nbuffer_load_dwordx4 v61, s[52:55], s95 offen offset:1024 lds\n"
"Qmava 64,24,152\nQtv 2,48,29184\nQk 178,180,181\nQzava 84,4,128\nQtv 20,48,39936\nQk 179,182,183\nQmava 84,24,156\ns_waitcnt lgkmcnt(2)\n"
"v_cmp_gt_u32 vcc, v75, v66\nQzava 104,4,132\ns_cbranch_vccnz .Lso1\n.Lro1:\nv_mul_f32 v208, v56, v208\nQmava 104,24,160\nQtv 22,48,41472\ns_add_u32 m0, s91, 0\n"
"s_mov_b32 s94, 46080\nQzava 188,4,136\nbuffer_load_dwordx4 v60, s[48:51], s94 offen lds\nQmava 188,24,164\nQtv 4,48,28160\nv_mul_f32 v209, v56, v209\nQzava 68,8,124\nQtv 6,48,29696\n"
"v_mul_f32 v210, v56, v210\nQmava 68,28,152\nQtv 24,48,40448\nv_mul_f32 v211, v56, v211\nQzava 88,8,128\nQtv 26,48,41984\nv_mul_f32 v212, v56, v212\nQmava 88,28,156\n"
"v_mul_f32 v213, v56, v213\nv_mul_f32 v214, v56, v214\nQzava 108,8,132\nbuffer_load_dwordx4 v60, s[48:51], s94 offen offset:1024 lds\nQmava 108,28,160\nv_mul_f32 v215, v56, v215\nQk 208,208,209\nQzava 192,8,136\n"
"Qtv 8,48,28672\nQk 209,210,211\nQmava 192,28,164\nQtv 10,48,30208\nQk 210,212,213\nQzava 72,12,124\nQtv 28,48,40960\nQk 211,214,215\n"
"Qmava 72,32,152\nQtv 30,48,42496\nv_mul_f32 v240, v57, v240\nQzava 92,12,128\nbuffer_load_dwordx4 v60, s[48:51], s94 offen offset:2048 lds\nQmava 92,32,156\nv_mul_f32 v241, v57, v241\nv_mul_f32 v242, v57, v242\n"
"Qzava 176,12,132\nv_mul_f32 v243, v57, v243\nv_mul_f32 v244, v57, v244\nQmava 176,32,160\nv_mul_f32 v245, v57, v245\nv_mul_f32 v246, v57, v246\nQzava 196,12,136\nQtv 12,49,4096\n"
"v_mul_f32 v247, v57, v247\nQmava 196,32,164\nQtv 14,49,5120\nQk 240,240,241\nQzava 76,16,124\nQtv 32,49,16384\nQk 241,242,243\nQmava 76,36,152\n"
"buffer_load_dwordx4 v60, s[48:51], s94 offen offset:3072 lds\nQzava 96,16,128\nQtv 34,49,17408\nQk 242,244,245\nQmava 96,36,156\nQk 243,246,247\nv_accvgpr_read_b32 v84, a224\nQzava 180,16,132\n"
"v_accvgpr_read_b32 v85, a225\nv_accvgpr_read_b32 v86, a226\nQmava 180,36,160\nv_accvgpr_read_b32 v87, a227\nv_accvgpr_read_b32 v40, a228\nQzava 200,16,136\nQtv 16,49,4608\nv_add_u32 v75, 1, v75\n"
"Qmava 200,36,164\ns_waitcnt vmcnt(17)\nds_write_b32 v73, v75\ns_add_u32 m0, s91, 4096\ns_mov_b32 s95, 127168\ns_waitcnt lgkmcnt(10)\nQmava 0,0,112\nbuffer_load_dwordx4 v61, s[52:55], s95 offen lds\n"
"Qmava 0,20,140\nbuffer_store_dwordx4 v[112:115], v60, s[56:59], s88 offen offset:1024 sc1\nQmava 20,0,116\nQtv 18,49,5632\nv_accvgpr_read_b32 v41, a229\nQmava 20,20,144\nQtv 36,49,16896\nv_accvgpr_read_b32 v42, a230\n"
"Qmava 40,0,120\nQtv 38,49,17920\ns_mov_b32 s95, 146624\nQmava 40,20,148\nbuffer_load_dwordx4 v61, s[52:55], s95 offen offset:1024 lds\nQmava 4,4,112\nQda 124,46,0\nv_accvgpr_read_b32 v43, a231\n"
"Qmava 4,24,140\nds_read_b32 v66, v74\nv_mul_f32 v84, v58, v84\nQmava 24,4,116\nQda 152,46,12288\nv_mul_f32 v85, v58, v85\nQmava 24,24,144\n.Lpg13:\n"
"s_cbranch_execz .Ltr6\nQda 128,46,1024\nv_mul_f32 v86, v58, v86\nQmava 44,4,120\nQda 156,46,13312\nv_mul_f32 v87, v58, v87\nQmava 44,24,148\ns_waitcnt lgkmcnt(3)\n"
"v_cmp_gt_u32 vcc, v75, v66\nQmava 8,8,112\ns_cbranch_vccnz .Lso2\n.Lro2:\nv_mul_f32 v40, v58, v40\nQmava 8,28,140\nQda 132,46,2048\ns_add_u32 m0, s91, 24576\n"
"s_add_u32 s95, s66, 1024\nQmava 28,8,116\nbuffer_load_dwordx4 v61, s[52:55], s95 offen offset:3072 lds\nQmava 28,28,144\nQda 160,46,14336\nv_mul_f32 v41, v58, v41\nQmava 48,8,120\nQda 136,46,3072\n"
"v_mul_f32 v42, v58, v42\nQmava 48,28,148\nQda 164,46,15360\nv_mul_f32 v43, v58, v43\ns_waitcnt lgkmcnt(9)\nQmava 12,12,112\nQk 84,84,85\nQk 85,86,87\n"
"Qmava 12,32,140\nQk 86,40,41\nQk 87,42,43\nQmava 32,12,116\nv_mul_f32 v88, v52, v88\nv_mul_f32 v89, v52, v89\nQmava 32,32,144\nv_mul_f32 v90, v52, v90\n"
"v_mul_f32 v91, v52, v91\nQmava 52,12,120\ns_add_u32 m0, s91, 28672\ns_add_u32 s95, s66, 4224\nv_mul_f32 v92, v52, v92\nQmava 52,32,148\nbuffer_load_dwordx4 v62, s[52:55], s95 offen lds\nQmava 16,16,112\n"
"Qda 112,46,24576\nv_mul_f32 v93, v52, v93\nQmava 16,36,140\nQda 140,46,36864\nv_mul_f32 v94, v52, v94\nQmava 36,16,116\nQda 116,46,25600\nv_mul_f32 v95, v52, v95\n"
"Qmava 36,36,144\nQda 144,46,37888\nQk 88,88,89\nQmava 56,16,120\nQda 120,46,26624\nv_add_u32 v75, 1, v75\nQmava 56,36,148\ns_waitcnt vmcnt(16)\n"
"ds_write_b32 v73, v75\ns_waitcnt lgkmcnt(6)\nQmava 60,0,124\nQda 148,46,38912\nQk 89,90,91\nQmava 60,20,152\nbuffer_store_dwordx4 v[144:147], v60, s[56:59], s88 offen offset:2048 sc1\ns_add_u32 s95, s66, 23616\n"
"Qmava 80,0,128\nbuffer_load_dwordx4 v61, s[52:55], s95 offen offset:1024 lds\nQmava 80,20,156\nQk 90,92,93\nQk 91,94,95\nQmava 100,0,132\nv_mul_f32 v120, v53, v120\nv_mul_f32 v121, v53, v121\n"
"Qmava 100,20,160\nv_mul_f32 v122, v53, v122\nv_mul_f32 v123, v53, v123\nQmava 184,0,136\nQtv 0,49,27648\nv_mul_f32 v124, v53, v124\nQmava 184,20,164\nds_read_b32 v66, v74\n"
"v_mul_f32 v125, v53, v125\nQmava 64,4,124\nQtv 2,49,29184\nv_mul_f32 v126, v53, v126\nQmava 64,24,152\nQtv 20,49,39936\nv_mul_f32 v127, v53, v127\nQmava 84,4,128\n"
"Qtv 22,49,41472\nQk 120,120,121\nQmava 84,24,156\ns_waitcnt lgkmcnt(3)\nv_cmp_gt_u32 vcc, v75, v66\nQmava 104,4,132\ns_cbranch_vccnz .Lso3\n.Lro3:\n"
"Qk 121,122,123\nQmava 104,24,160\ns_add_u32 m0, s91, 53248\ns_add_u32 s95, s66, 4288\nQk 122,124,125\nQmava 188,4,136\nbuffer_load_dwordx4 v61, s[52:55], s95 offen lds\nQmava 188,24,164\n"
"ds_write_b128 v44, v[80:83] offset:24576\nQmava 68,8,124\nQtv 4,49,28160\nQk 123,126,127\nQmava 68,28,152\nQtv 6,49,29696\nv_mul_f32 v152, v54, v152\nQmava 88,8,128\n"
"Qtv 24,49,40448\nv_mul_f32 v153, v54, v153\nQmava 88,28,156\nQtv 26,49,41984\nv_mul_f32 v154, v54, v154\nQmava 108,8,132\nv_mul_f32 v155, v54, v155\nv_mul_f32 v156, v54, v156\n"
"Qmava 108,28,160\nv_mul_f32 v157, v54, v157\nv_mul_f32 v158, v54, v158\nQmava 192,8,136\nQtv 8,49,28672\nv_mul_f32 v159, v54, v159\nQmava 192,28,164\nQtv 10,49,30208\n"
"Qk 152,152,153\nQmava 72,12,124\nQtv 28,49,40960\nQk 153,154,155\nQmava 72,32,152\nQtv 30,49,42496\nQk 154,156,157\nQmava 92,12,128\n"
"ds_write_b128 v44, v[112:115] offset:25600\nQmava 92,32,156\nQk 155,158,159\nv_mul_f32 v184, v55, v184\nQmava 176,12,132\nv_mul_f32 v185, v55, v185\nv_mul_f32 v186, v55, v186\nQmava 176,32,160\n"
"v_mul_f32 v187, v55, v187\nv_mul_f32 v188, v55, v188\nQmava 196,12,136\nQtv 12,50,4096\nv_mul_f32 v189, v55, v189\nQmava 196,32,164\nQtv 14,50,5120\nv_mul_f32 v190, v55, v190\n"
"Qmava 76,16,124\nQtv 32,50,16384\ns_add_u32 s95, s66, 23744\nQmava 76,36,152\nbuffer_load_dwordx4 v61, s[52:55], s95 offen offset:1024 lds\nQmava 96,16,128\nQtv 34,50,17408\nv_mul_f32 v191, v55, v191\n"
"Qmava 96,36,156\nQk 184,184,185\nQk 185,186,187\nQmava 180,16,132\nQk 186,188,189\nQk 187,190,191\nQmava 180,36,160\nv_mul_f32 v216, v56, v216\n"
"v_mul_f32 v217, v56, v217\nQmava 200,16,136\nds_write_b128 v44, v[144:147] offset:26624\nv_add_u32 v75, 1, v75\nQmava 200,36,164\ns_waitcnt vmcnt(13)\nds_write_b32 v73, v75\ns_waitcnt lgkmcnt(11)\n"
"Qmava 0,0,112\nQtv 16,50,4608\nv_mul_f32 v218, v56, v218\nQmava 0,20,140\nbuffer_store_dwordx4 v[176:179], v60, s[56:59], s88 offen offset:3072 sc1\nQmava 20,0,116\nQtv 18,50,5632\nv_mul_f32 v219, v56, v219\n"
"Qmava 20,20,144\nQtv 36,50,16896\nv_mul_f32 v220, v56, v220\nQmava 40,0,120\nQtv 38,50,17920\nv_mul_f32 v221, v56, v221\nQmava 40,20,148\nQda 124,47,0\n"
"v_mul_f32 v222, v56, v222\nQmava 4,4,112\nQda 152,47,12288\nv_mul_f32 v223, v56, v223\nQmava 4,24,140\nds_read_b32 v66, v74\nQk 216,216,217\nQmava 24,4,116\n"
"Qda 128,47,1024\nQk 217,218,219\nQmava 24,24,144\nQda 156,47,13312\nQk 218,220,221\nQmava 44,4,120\nds_write_b128 v44, v[176:179] offset:49152\nQmava 44,24,148\n"
"s_waitcnt lgkmcnt(3)\nv_cmp_gt_u32 vcc, v75, v66\nQmava 8,8,112\ns_cbranch_vccnz .Lso4\n.Lro4:\nQk 219,222,223\nQmava 8,28,140\nQda 132,47,2048\n"
"s_add_u32 m0, s91, 73728\ns_mov_b32 s94, 114688\nQmava 28,8,116\nbuffer_load_dwordx4 v60, s[48:51], s94 offen lds\nQmava 28,28,144\nQda 160,47,14336\nv_mul_f32 v248, v57, v248\nQmava 48,8,120\n"
"Qda 136,47,3072\nv_mul_f32 v249, v57, v249\nQmava 48,28,148\nQda 164,47,15360\nv_mul_f32 v250, v57, v250\ns_waitcnt lgkmcnt(10)\nQmava 12,12,112\nbuffer_load_dwordx4 v60, s[48:51], s94 offen offset:1024 lds\n"
"Qmava 12,32,140\nv_mul_f32 v251, v57, v251\nv_mul_f32 v252, v57, v252\nQmava 32,12,116\nv_mul_f32 v253, v57, v253\nv_mul_f32 v254, v57, v254\nQmava 32,32,144\nv_mul_f32 v255, v57, v255\n"
"Qk 248,248,249\nQmava 52,12,120\nds_write_b128 v44, v[208:211] offset:50176\nQmava 52,32,148\nbuffer_load_dwordx4 v60, s[48:51], s94 offen offset:2048 lds\nQmava 16,16,112\nQda 112,47,33792\nQk 249,250,251\n"
"Qmava 16,36,140\nQda 140,47,46080\nQk 250,252,253\nQmava 36,16,116\nQda 116,47,34816\nQk 251,254,255\nQmava 36,36,144\nQda 144,47,47104\n"
"s_mov_b32 s95, 328704\nQmava 56,16,120\nbuffer_load_dwordx4 v61, s[52:55], s95 offen offset:3072 lds\nv_add_u32 v75, 1, v75\nQmava 56,36,148\ns_waitcnt vmcnt(11)\nds_write_b32 v73, v75\ns_waitcnt lgkmcnt(6)\n"
"Qmava 60,0,124\nQda 120,47,35840\ns_add_u32 s88, s71, 4096\nQmava 60,20,152\nbuffer_store_dwordx4 v[208:211], v60, s[56:59], s88 offen offset:0 sc1\ns_add_u32 m0, s91, 77824\ns_mov_b32 s95, 331904\nQmava 80,0,128\n"
"buffer_load_dwordx4 v62, s[52:55], s95 offen lds\nQmava 80,20,156\nQda 148,47,48128\nv_accvgpr_read_b32 v116, a232\nQmava 100,0,132\nds_write_b128 v44, v[240:243] offset:51200\nQmava 100,20,160\nv_accvgpr_read_b32 v117, a233\n"
"v_accvgpr_read_b32 v118, a234\nQmava 184,0,136\nQtv 0,50,36864\nv_accvgpr_read_b32 v119, a235\nQmava 184,20,164\nds_read_b32 v66, v74\ns_mov_b32 s95, 351296\nQmava 64,4,124\n"
"buffer_load_dwordx4 v61, s[52:55], s95 offen offset:1024 lds\nQmava 64,24,152\nQtv 2,50,38400\nv_accvgpr_read_b32 v40, a236\nQmava 84,4,128\nQtv 20,50,49152\nv_accvgpr_read_b32 v41, a237\nQmava 84,24,156\n"
"s_waitcnt lgkmcnt(2)\nv_cmp_gt_u32 vcc, v75, v66\nQmava 104,4,132\ns_cbranch_vccnz .Lso5\n.Lro5:\nv_accvgpr_read_b32 v42, a238\nQmava 104,24,160\nQtv 22,50,50688\n"
"s_add_u32 m0, s91, 98304\ns_mov_b32 s94, 117760\nQmava 188,4,136\nbuffer_load_dwordx4 v60, s[48:51], s94 offen lds\nQmava 188,24,164\nQtv 4,50,37376\nv_accvgpr_read_b32 v43, a239\nQmava 68,8,124\n"
"ds_write_b128 v44, v[84:87] offset:52224\nQmava 68,28,152\nQtv 6,50,38912\nv_mul_f32 v116, v58, v116\nQmava 88,8,128\nQtv 24,50,49664\nv_mul_f32 v117, v58, v117\nQmava 88,28,156\n"
"Qtv 26,50,51200\nv_mul_f32 v118, v58, v118\nQmava 108,8,132\nbuffer_load_dwordx4 v60, s[48:51], s94 offen offset:1024 lds\nQmava 108,28,160\nv_mul_f32 v119, v58, v119\nv_mul_f32 v40, v58, v40\nQmava 192,8,136\n"
"Qtv 8,50,37888\nv_mul_f32 v41, v58, v41\nQmava 192,28,164\nQtv 10,50,39424\nv_mul_f32 v42, v58, v42\nQmava 72,12,124\nQtv 28,50,50176\nv_mul_f32 v43, v58, v43\n"
"Qmava 72,32,152\nQtv 30,50,51712\nQk 116,116,117\nQmava 92,12,128\nbuffer_load_dwordx4 v60, s[48:51], s94 offen offset:2048 lds\nQmava 92,32,156\nQk 117,118,119\nQk 118,40,41\n"
"Qmava 176,12,132\nQk 119,42,43\nv_mul_f32 v96, v52, v96\nQmava 176,32,160\nv_mul_f32 v97, v52, v97\nv_mul_f32 v98, v52, v98\nQmava 196,12,136\nQtv 12,48,4096\n"
"v_mul_f32 v99, v52, v99\nQmava 196,32,164\nQtv 14,48,5120\nv_mul_f32 v100, v52, v100\nQmava 76,16,124\nQtv 32,48,16384\nv_mul_f32 v101, v52, v101\nQmava 76,36,152\n"
"buffer_load_dwordx4 v60, s[48:51], s94 offen offset:3072 lds\nQmava 96,16,128\nQtv 34,48,17408\nv_mul_f32 v102, v52, v102\nQmava 96,36,156\nv_mul_f32 v103, v52, v103\nQk 96,96,97\nQmava 180,16,132\n"
"Qk 97,98,99\nQk 98,100,101\nQmava 180,36,160\nQk 99,102,103\nv_mul_f32 v128, v53, v128\nQmava 200,16,136\nQtv 16,48,4608\nv_add_u32 v75, 1, v75\n"
"Qmava 200,36,164\ns_waitcnt vmcnt(14)\nds_write_b32 v73, v75\ns_add_u32 m0, s91, 102400\ns_mov_b32 s95, 331968\ns_waitcnt lgkmcnt(10)\nQmava 0,0,112\nbuffer_load_dwordx4 v61, s[52:55], s95 offen lds\n"
"Qmava 0,20,140\nbuffer_store_dwordx4 v[240:243], v60, s[56:59], s88 offen offset:1024 sc1\nQmava 20,0,116\nQtv 18,48,5632\nv_mul_f32 v129, v53, v129\nQmava 20,20,144\nQtv 36,48,16896\nv_mul_f32 v130, v53, v130\n"
"Qmava 40,0,120\nQtv 38,48,17920\ns_mov_b32 s95, 351424\nQmava 40,20,148\nbuffer_load_dwordx4 v61, s[52:55], s95 offen offset:1024 lds\nQmava 4,4,112\nQda 124,45,0\nv_mul_f32 v131, v53, v131\n"
"Qmava 4,24,140\nds_read_b32 v66, v74\nv_mul_f32 v132, v53, v132\nQmava 24,4,116\nQda 152,45,12288\nv_mul_f32 v133, v53, v133\nQmava 24,24,144\nQda 128,45,1024\n"
"v_mul_f32 v134, v53, v134\nQmava 44,4,120\nQda 156,45,13312\nv_mul_f32 v135, v53, v135\nQmava 44,24,148\ns_waitcnt lgkmcnt(3)\nv_cmp_gt_u32 vcc, v75, v66\nQmava 8,8,112\n"
"s_cbranch_vccnz .Lso6\n.Lro6:\nQk 128,128,129\nQmava 8,28,140\nQda 132,45,2048\ns_add_u32 m0, s91, 132096\ns_mov_b32 s94, 129024\nQmava 28,8,116\n"
"buffer_load_dwordx4 v60, s[48:51], s94 offen lds\nQmava 28,28,144\nQda 160,45,14336\nQk 129,130,131\nQmava 48,8,120\nQda 136,45,3072\nQk 130,132,133\nQmava 48,28,148\n"
"Qda 164,45,15360\nQk 131,134,135\ns_waitcnt lgkmcnt(9)\nQmava 12,12,112\nbuffer_load_dwordx4 v60, s[48:51], s94 offen offset:1024 lds\nQmava 12,32,140\nv_mul_f32 v160, v54, v160\nv_mul_f32 v161, v54, v161\n"
"Qmava 32,12,116\nv_mul_f32 v162, v54, v162\nv_mul_f32 v163, v54, v163\nQmava 32,32,144\nv_mul_f32 v164, v54, v164\nv_mul_f32 v165, v54, v165\nQmava 52,12,120\nv_mul_f32 v166, v54, v166\n"
"v_mul_f32 v167, v54, v167\nQmava 52,32,148\nbuffer_load_dwordx4 v60, s[48:51], s94 offen offset:2048 lds\nQmava 16,16,112\nQda 112,45,24576\nQk 160,160,161\nQmava 16,36,140\nQda 140,45,36864\n"
"Qk 161,162,163\nQmava 36,16,116\nQda 116,45,25600\nQk 162,164,165\nQmava 36,36,144\nQda 144,45,37888\ns_mov_b32 s95, 369664\nQmava 56,16,120\n"
"buffer_load_dwordx4 v61, s[52:55], s95 offen offset:3072 lds\nv_add_u32 v75, 1, v75\nQmava 56,36,148\ns_waitcnt vmcnt(19)\nds_write_b32 v73, v75\ns_waitcnt lgkmcnt(5)\nQmava 60,0,124\nQda 120,45,26624\n"
"Qk 163,166,167\nQmava 60,20,152\nbuffer_store_dwordx4 v[84:87], v60, s[56:59], s88 offen offset:2048 sc1\ns_add_u32 m0, s91, 136192\ns_mov_b32 s95, 372864\nQmava 80,0,128\nbuffer_load_dwordx4 v62, s[52:55], s95 offen lds\nQmava 80,20,156\n"
"Qda 148,45,38912\nv_mul_f32 v192, v55, v192\nQmava 100,0,132\nv_mul_f32 v193, v55, v193\nv_mul_f32 v194, v55, v194\nQmava 100,20,160\nv_mul_f32 v195, v55, v195\nv_mul_f32 v196, v55, v196\n"
"Qmava 184,0,136\nQtv 0,48,27648\nv_mul_f32 v197, v55, v197\nQmava 184,20,164\nds_read_b32 v66, v74\ns_mov_b32 s95, 392256\nQmava 64,4,124\nbuffer_load_dwordx4 v61, s[52:55], s95 offen offset:1024 lds\n"
"Qmava 64,24,152\nQtv 2,48,29184\nv_mul_f32 v198, v55, v198\nQmava 84,4,128\nQtv 20,48,39936\nv_mul_f32 v199, v55, v199\nQmava 84,24,156\ns_waitcnt lgkmcnt(2)\n"
"v_cmp_gt_u32 vcc, v75, v66\nQmava 104,4,132\ns_cbranch_vccnz .Lso7\n.Lro7:\nQk 192,192,193\nQmava 104,24,160\nQtv 22,48,41472\ns_add_u32 m0, s91, 0\n"
"s_mov_b32 s94, 132096\nQmava 188,4,136\nbuffer_load_dwordx4 v60, s[48:51], s94 offen lds\nQmava 188,24,164\nQtv 4,48,28160\nQk 193,194,195\nQmava 68,8,124\nQtv 6,48,29696\n"
"Qk 194,196,197\nQmava 68,28,152\nQtv 24,48,40448\nQk 195,198,199\nQmava 88,8,128\nQtv 26,48,41984\nv_mul_f32 v224, v56, v224\nQmava 88,28,156\n"
"v_mul_f32 v225, v56, v225\nv_mul_f32 v226, v56, v226\nQmava 108,8,132\nbuffer_load_dwordx4 v60, s[48:51], s94 offen offset:1024 lds\nQmava 108,28,160\nv_mul_f32 v227, v56, v227\nv_mul_f32 v228, v56, v228\nQmava 192,8,136\n"
"Qtv 8,48,28672\nv_mul_f32 v229, v56, v229\nQmava 192,28,164\nQtv 10,48,30208\nv_mul_f32 v230, v56, v230\nQmava 72,12,124\nQtv 28,48,40960\nv_mul_f32 v231, v56, v231\n"
"Qmava 72,32,152\nQtv 30,48,42496\nQk 224,224,225\nQmava 92,12,128\nbuffer_load_dwordx4 v60, s[48:51], s94 offen offset:2048 lds\nQmava 92,32,156\nQk 225,226,227\nQk 226,228,229\n"
"Qmava 176,12,132\nQk 227,230,231\nv_accvgpr_read_b32 v148, a208\nQmava 176,32,160\nv_accvgpr_read_b32 v149, a209\nv_accvgpr_read_b32 v150, a210\nQmava 196,12,136\nQtv 12,49,4096\n"
"v_accvgpr_read_b32 v151, a211\nQmava 196,32,164\nQtv 14,49,5120\nv_accvgpr_read_b32 v40, a212\nQmava 76,16,124\nQtv 32,49,16384\nv_accvgpr_read_b32 v41, a213\nQmava 76,36,152\n"
"buffer_load_dwordx4 v60, s[48:51], s94 offen offset:3072 lds\nQmava 96,16,128\nQtv 34,49,17408\nv_accvgpr_read_b32 v42, a214\nQmava 96,36,156\nv_accvgpr_read_b32 v43, a215\nv_mul_f32 v148, v57, v148\nQmava 180,16,132\n"
"v_mul_f32 v149, v57, v149\nv_mul_f32 v150, v57, v150\nQmava 180,36,160\nv_mul_f32 v151, v57, v151\nv_mul_f32 v40, v57, v40\nQmava 200,16,136\nQtv 16,49,4608\n.Lpg14:\n"
"s_cbranch_execz .Ltr6\nv_add_u32 v75, 1, v75\nQmava 200,36,164\ns_waitcnt vmcnt(18)\nds_write_b32 v73, v75\ns_add_u32 m0, s91, 4096\ns_mov_b32 s95, 372928\ns_waitcnt lgkmcnt(10)\n"
"Qmava 0,0,112\nbuffer_load_dwordx4 v61, s[52:55], s95 offen lds\ns_add_u32 s88, s71, 14336\nQmava 0,20,140\nbuffer_store_dwordx4 v[88:91], v60, s[56:59], s88 offen offset:0 sc1\nQmava 20,0,116\nQtv 18,49,5632\nv_mul_f32 v41, v57, v41\n"
"Qmava 20,20,144\nQtv 36,49,16896\nv_mul_f32 v42, v57, v42\nQmava 40,0,120\nQtv 38,49,17920\ns_mov_b32 s95, 392384\nQmava 40,20,148\nbuffer_load_dwordx4 v61, s[52:55], s95 offen offset:1024 lds\n"
"Qmava 4,4,112\nQda 124,46,0\nv_mul_f32 v43, v57, v43\nQmava 4,24,140\nds_read_b32 v66, v74\nQk 148,148,149\nQmava 24,4,116\nQda 152,46,12288\n"
"Qk 149,150,151\nQmava 24,24,144\nQda 128,46,1024\nQk 150,40,41\nQmava 44,4,120\nQda 156,46,13312\nQk 151,42,43\nQmava 44,24,148\n"
"s_waitcnt lgkmcnt(3)\nv_cmp_gt_u32 vcc, v75, v66\nQmava 8,8,112\ns_cbranch_vccnz .Lso8\n.Lro8:\nv_accvgpr_read_b32 v180, a240\nQmava 8,28,140\nQda 132,46,2048\n"
"s_add_u32 m0, s91, 24576\ns_add_u32 s95, s66, 41984\nQmava 28,8,116\nbuffer_load_dwordx4 v61, s[52:55], s95 offen offset:3072 lds\nQmava 28,28,144\nQda 160,46,14336\nv_accvgpr_read_b32 v181, a241\nQmava 48,8,120\n"
"Qda 136,46,3072\nv_accvgpr_read_b32 v182, a242\nQmava 48,28,148\nQda 164,46,15360\nv_accvgpr_read_b32 v183, a243\ns_waitcnt lgkmcnt(9)\nQmava 12,12,112\nv_accvgpr_read_b32 v40, a244\n"
"v_accvgpr_read_b32 v41, a245\nQmava 12,32,140\nv_accvgpr_read_b32 v42, a246\nv_accvgpr_read_b32 v43, a247\nQmava 32,12,116\nv_mul_f32 v180, v58, v180\nv_mul_f32 v181, v58, v181\nQmava 32,32,144\n"
"v_mul_f32 v182, v58, v182\nv_mul_f32 v183, v58, v183\nQmava 52,12,120\ns_add_u32 m0, s91, 28672\ns_add_u32 s95, s66, 45184\nv_mul_f32 v40, v58, v40\nQmava 52,32,148\nbuffer_load_dwordx4 v62, s[52:55], s95 offen lds\n"
"Qmava 16,16,112\nQda 112,46,24576\nv_mul_f32 v41, v58, v41\nQmava 16,36,140\nQda 140,46,36864\nv_mul_f32 v42, v58, v42\nQmava 36,16,116\nQda 116,46,25600\n"
"v_mul_f32 v43, v58, v43\nQmava 36,36,144\nQda 144,46,37888\nQk 180,180,181\nQmava 56,16,120\nQda 120,46,26624\nv_add_u32 v75, 1, v75\nQmava 56,36,148\n"
"s_waitcnt vmcnt(16)\nds_write_b32 v73, v75\ns_waitcnt lgkmcnt(6)\nQmava 60,0,124\nQda 148,46,38912\nQk 181,182,183\nQmava 60,20,152\nbuffer_store_dwordx4 v[120:123], v60, s[56:59], s88 offen offset:1024 sc1\n"
"s_add_u32 s95, s66, 64576\nQmava 80,0,128\nbuffer_load_dwordx4 v61, s[52:55], s95 offen offset:1024 lds\nQmava 80,20,156\nQk 182,40,41\nQk 183,42,43\nQmava 100,0,132\nv_mul_f32 v104, v52, v104\n"
"v_mul_f32 v105, v52, v105\nQmava 100,20,160\nv_mul_f32 v106, v52, v106\nv_mul_f32 v107, v52, v107\nQmava 184,0,136\nQtv 0,49,27648\nv_mul_f32 v108, v52, v108\nQmava 184,20,164\n"
"ds_read_b32 v66, v74\nv_mul_f32 v109, v52, v109\nQmava 64,4,124\nQtv 2,49,29184\nv_mul_f32 v110, v52, v110\nQmava 64,24,152\nQtv 20,49,39936\nv_mul_f32 v111, v52, v111\n"
"Qmava 84,4,128\nQtv 22,49,41472\nQk 104,104,105\nQmava 84,24,156\ns_waitcnt lgkmcnt(3)\nv_cmp_gt_u32 vcc, v75, v66\nQmava 104,4,132\ns_cbranch_vccnz .Lso9\n"
".Lro9:\nQk 105,106,107\nQmava 104,24,160\ns_add_u32 m0, s91, 53248\ns_add_u32 s95, s66, 45248\nQk 106,108,109\nQmava 188,4,136\nbuffer_load_dwordx4 v61, s[52:55], s95 offen lds\n"
"Qmava 188,24,164\nds_write_b128 v44, v[88:91] offset:24576\nQmava 68,8,124\nQtv 4,49,28160\nQk 107,110,111\nQmava 68,28,152\nQtv 6,49,29696\nv_mul_f32 v136, v53, v136\n"
"Qmava 88,8,128\nQtv 24,49,40448\nv_mul_f32 v137, v53, v137\nQmava 88,28,156\nQtv 26,49,41984\nv_mul_f32 v138, v53, v138\nQmava 108,8,132\nv_mul_f32 v139, v53, v139\n"
"v_mul_f32 v140, v53, v140\nQmava 108,28,160\nv_mul_f32 v141, v53, v141\nv_mul_f32 v142, v53, v142\nQmava 192,8,136\nQtv 8,49,28672\nv_mul_f32 v143, v53, v143\nQmava 192,28,164\n"
"Qtv 10,49,30208\nQk 136,136,137\nQmava 72,12,124\nQtv 28,49,40960\nQk 137,138,139\nQmava 72,32,152\nQtv 30,49,42496\nQk 138,140,141\n"
"Qmava 92,12,128\nds_write_b128 v44, v[120:123] offset:25600\nQmava 92,32,156\nQk 139,142,143\nv_mul_f32 v168, v54, v168\nQmava 176,12,132\nv_mul_f32 v169, v54, v169\nv_mul_f32 v170, v54, v170\n"
"Qmava 176,32,160\nv_mul_f32 v171, v54, v171\nv_mul_f32 v172, v54, v172\nQmava 196,12,136\nQtv 12,50,4096\nv_mul_f32 v173, v54, v173\nQmava 196,32,164\nQtv 14,50,5120\n"
"v_mul_f32 v174, v54, v174\nQmava 76,16,124\nQtv 32,50,16384\ns_add_u32 s95, s66, 64704\nQmava 76,36,152\nbuffer_load_dwordx4 v61, s[52:55], s95 offen offset:1024 lds\nQmava 96,16,128\nQtv 34,50,17408\n"
"v_mul_f32 v175, v54, v175\nQmava 96,36,156\nQk 168,168,169\nQk 169,170,171\nQmava 180,16,132\nQk 170,172,173\nQk 171,174,175\nQmava 180,36,160\n"
"v_mul_f32 v200, v55, v200\nv_mul_f32 v201, v55, v201\nQmava 200,16,136\nds_write_b128 v44, v[152:155] offset:26624\nv_add_u32 v75, 1, v75\nQmava 200,36,164\ns_waitcnt vmcnt(13)\nds_write_b32 v73, v75\n"
"s_waitcnt lgkmcnt(11)\nQmava 0,0,112\nQtv 16,50,4608\nv_mul_f32 v202, v55, v202\nQmava 0,20,140\nbuffer_store_dwordx4 v[152:155], v60, s[56:59], s88 offen offset:2048 sc1\nQmava 20,0,116\nQtv 18,50,5632\n"
"v_mul_f32 v203, v55, v203\nQmava 20,20,144\nQtv 36,50,16896\nv_mul_f32 v204, v55, v204\nQmava 40,0,120\nQtv 38,50,17920\nv_mul_f32 v205, v55, v205\nQmava 40,20,148\n"
"Qda 124,47,0\nv_mul_f32 v206, v55, v206\nQmava 4,4,112\nQda 152,47,12288\nv_mul_f32 v207, v55, v207\nQmava 4,24,140\nds_read_b32 v66, v74\nQk 200,200,201\n"
"Qmava 24,4,116\nQda 128,47,1024\nQk 201,202,203\nQmava 24,24,144\nQda 156,47,13312\nQk 202,204,205\nQmava 44,4,120\nds_write_b128 v44, v[184:187] offset:49152\n"
"Qmava 44,24,148\ns_waitcnt lgkmcnt(3)\nv_cmp_gt_u32 vcc, v75, v66\nQmava 8,8,112\ns_cbranch_vccnz .Lso10\n.Lro10:\nQk 203,206,207\nQmava 8,28,140\n"
"Qda 132,47,2048\ns_add_u32 m0, s91, 73728\ns_mov_b32 s94, 143360\nQmava 28,8,116\nbuffer_load_dwordx4 v60, s[48:51], s94 offen lds\nQmava 28,28,144\nQda 160,47,14336\nv_mul_f32 v232, v56, v232\n"
"Qmava 48,8,120\nQda 136,47,3072\nv_mul_f32 v233, v56, v233\nQmava 48,28,148\nQda 164,47,15360\nv_mul_f32 v234, v56, v234\ns_waitcnt lgkmcnt(10)\nQmava 12,12,112\n"
"buffer_load_dwordx4 v60, s[48:51], s94 offen offset:1024 lds\nQmava 12,32,140\nv_mul_f32 v235, v56, v235\nv_mul_f32 v236, v56, v236\nQmava 32,12,116\nv_mul_f32 v237, v56, v237\nv_mul_f32 v238, v56, v238\nQmava 32,32,144\n"
"v_mul_f32 v239, v56, v239\nQk 232,232,233\nQmava 52,12,120\nds_write_b128 v44, v[216:219] offset:50176\nQmava 52,32,148\nbuffer_load_dwordx4 v60, s[48:51], s94 offen offset:2048 lds\nQmava 16,16,112\nQda 112,47,33792\n"
"Qk 233,234,235\nQmava 16,36,140\nQda 140,47,46080\nQk 234,236,237\nQmava 36,16,116\nQda 116,47,34816\nQk 235,238,239\nQmava 36,36,144\n"
"Qda 144,47,47104\ns_mov_b32 s95, 410624\nQmava 56,16,120\nbuffer_load_dwordx4 v61, s[52:55], s95 offen offset:3072 lds\nv_add_u32 v75, 1, v75\nQmava 56,36,148\ns_waitcnt vmcnt(11)\nds_write_b32 v73, v75\n"
"s_waitcnt lgkmcnt(6)\nQmava 60,0,124\nQda 120,47,35840\nv_accvgpr_read_b32 v212, a216\nQmava 60,20,152\nbuffer_store_dwordx4 v[184:187], v60, s[56:59], s88 offen offset:3072 sc1\ns_add_u32 m0, s91, 77824\ns_mov_b32 s95, 413824\n"
"Qmava 80,0,128\nbuffer_load_dwordx4 v62, s[52:55], s95 offen lds\nQmava 80,20,156\nQda 148,47,48128\nv_accvgpr_read_b32 v213, a217\nQmava 100,0,132\nds_write_b128 v44, v[248:251] offset:51200\nQmava 100,20,160\n"
"v_accvgpr_read_b32 v214, a218\nv_accvgpr_read_b32 v215, a219\nQmava 184,0,136\nQtv 0,50,36864\nv_accvgpr_read_b32 v40, a220\nQmava 184,20,164\nds_read_b32 v66, v74\ns_mov_b32 s95, 433216\n"
"Qmava 64,4,124\nbuffer_load_dwordx4 v61, s[52:55], s95 offen offset:1024 lds\nQmava 64,24,152\nQtv 2,50,38400\nv_accvgpr_read_b32 v41, a221\nQmava 84,4,128\nQtv 20,50,49152\nv_accvgpr_read_b32 v42, a222\n"
"Qmava 84,24,156\ns_waitcnt lgkmcnt(2)\nv_cmp_gt_u32 vcc, v75, v66\nQmava 104,4,132\ns_cbranch_vccnz .Lso11\n.Lro11:\nv_accvgpr_read_b32 v43, a223\nQmava 104,24,160\n"
"Qtv 22,50,50688\ns_add_u32 m0, s91, 98304\ns_mov_b32 s94, 146432\nQmava 188,4,136\nbuffer_load_dwordx4 v60, s[48:51], s94 offen lds\nQmava 188,24,164\nQtv 4,50,37376\nv_mul_f32 v212, v57, v212\n"
"Qmava 68,8,124\nds_write_b128 v44, v[116:119] offset:52224\nQmava 68,28,152\nQtv 6,50,38912\nv_mul_f32 v213, v57, v213\nQmava 88,8,128\nQtv 24,50,49664\nv_mul_f32 v214, v57, v214\n"
"Qmava 88,28,156\nQtv 26,50,51200\nv_mul_f32 v215, v57, v215\nQmava 108,8,132\nbuffer_load_dwordx4 v60, s[48:51], s94 offen offset:1024 lds\nQmava 108,28,160\nv_mul_f32 v40, v57, v40\nv_mul_f32 v41, v57, v41\n"
"Qmava 192,8,136\nQtv 8,50,37888\nv_mul_f32 v42, v57, v42\nQmava 192,28,164\nQtv 10,50,39424\nv_mul_f32 v43, v57, v43\nQmava 72,12,124\nQtv 28,50,50176\n"
"Qk 212,212,213\nQmava 72,32,152\nQtv 30,50,51712\nQk 213,214,215\nQmava 92,12,128\nbuffer_load_dwordx4 v60, s[48:51], s94 offen offset:2048 lds\nQmava 92,32,156\nQk 214,40,41\n"
"Qk 215,42,43\nQmava 176,12,132\nv_accvgpr_read_b32 v244, a248\nv_accvgpr_read_b32 v245, a249\nQmava 176,32,160\nv_accvgpr_read_b32 v246, a250\nv_accvgpr_read_b32 v247, a251\nQmava 196,12,136\n"
"Qtv 12,48,4096\nv_accvgpr_read_b32 v40, a252\nQmava 196,32,164\nQtv 14,48,5120\nv_accvgpr_read_b32 v41, a253\nQmava 76,16,124\nQtv 32,48,16384\nv_accvgpr_read_b32 v42, a254\n"
"Qmava 76,36,152\nbuffer_load_dwordx4 v60, s[48:51], s94 offen offset:3072 lds\nQmava 96,16,128\nQtv 34,48,17408\nv_accvgpr_read_b32 v43, a255\nQmava 96,36,156\nv_mul_f32 v244, v58, v244\nv_mul_f32 v245, v58, v245\n"
"Qmava 180,16,132\nv_mul_f32 v246, v58, v246\nv_mul_f32 v247, v58, v247\nQmava 180,36,160\nv_mul_f32 v40, v58, v40\nv_mul_f32 v41, v58, v41\nQmava 200,16,136\nQtv 16,48,4608\n"
"v_add_u32 v75, 1, v75\nQmava 200,36,164\ns_waitcnt vmcnt(14)\nds_write_b32 v73, v75\ns_add_u32 m0, s91, 102400\ns_mov_b32 s95, 413888\ns_waitcnt lgkmcnt(10)\nQmava 0,0,112\n"
"buffer_load_dwordx4 v61, s[52:55], s95 offen lds\ns_add_u32 s88, s71, 18432\nQmava 0,20,140\nbuffer_store_dwordx4 v[216:219], v60, s[56:59], s88 offen offset:0 sc1\nQmava 20,0,116\nQtv 18,48,5632\nv_mul_f32 v42, v58, v42\nQmava 20,20,144\n"
"Qtv 36,48,16896\nv_mul_f32 v43, v58, v43\nQmava 40,0,120\nQtv 38,48,17920\ns_mov_b32 s95, 433344\nQmava 40,20,148\nbuffer_load_dwordx4 v61, s[52:55], s95 offen offset:1024 lds\nQmava 4,4,112\n"
"Qda 124,45,0\nQk 244,244,245\nQmava 4,24,140\nds_read_b32 v66, v74\nQk 245,246,247\nQmava 24,4,116\nQda 152,45,12288\nQk 246,40,41\n"
"Qmava 24,24,144\nQda 128,45,1024\nQk 247,42,43\nQmava 44,4,120\nQda 156,45,13312\nv_add_u32 v67, 2, v65\nQmava 44,24,148\ns_waitcnt lgkmcnt(3)\n"
"v_cmp_gt_u32 vcc, v75, v66\nQmava 8,8,112\ns_cbranch_vccnz .Lso12\n.Lro12:\nQmava 8,28,140\nbuffer_store_dword v67, v77, s[24:27], 0 offen sc1\ns_add_u32 m0, s91, 132096\ns_mov_b32 s94, 157696\n"
"Qmava 28,8,116\nbuffer_load_dwordx4 v60, s[48:51], s94 offen lds\nQmava 28,28,144\nQda 132,45,2048\nQmava 48,8,120\nQda 160,45,14336\nQmava 48,28,148\nQda 136,45,3072\n"
"s_waitcnt lgkmcnt(8)\nQmava 12,12,112\nbuffer_load_dwordx4 v60, s[48:51], s94 offen offset:1024 lds\nQmava 12,32,140\nQda 164,45,15360\nQmava 32,12,116\nQmava 32,32,144\nQmava 52,12,120\n"
"Qmava 52,32,148\nbuffer_load_dwordx4 v60, s[48:51], s94 offen offset:2048 lds\nQmava 16,16,112\nQda 112,45,24576\nQmava 16,36,140\nQda 140,45,36864\nQmava 36,16,116\nQda 116,45,25600\n"
"Qmava 36,36,144\nQda 144,45,37888\ns_mov_b32 s95, 451584\nQmava 56,16,120\nbuffer_load_dwordx4 v61, s[52:55], s95 offen offset:3072 lds\nv_add_u32 v75, 1, v75\nQmava 56,36,148\ns_waitcnt vmcnt(20)\n"
"ds_write_b32 v73, v75\ns_waitcnt lgkmcnt(5)\nQmava 60,0,124\nQda 120,45,26624\nQmava 60,20,152\nbuffer_store_dwordx4 v[248:251], v60, s[56:59], s88 offen offset:1024 sc1\ns_add_u32 m0, s91, 136192\ns_mov_b32 s95, 454784\n"
"Qmava 80,0,128\nbuffer_load_dwordx4 v62, s[52:55], s95 offen lds\nQmava 80,20,156\nQda 148,45,38912\nQmava 100,0,132\nQmava 100,20,160\nQmava 184,0,136\nQtv 0,48,27648\n"
"Qmava 184,20,164\nds_read_b32 v66, v74\ns_mov_b32 s95, 474176\nQmava 64,4,124\nbuffer_load_dwordx4 v61, s[52:55], s95 offen offset:1024 lds\nQmava 64,24,152\nQtv 2,48,29184\nQmava 84,4,128\n"
"Qtv 20,48,39936\nQmava 84,24,156\ns_waitcnt lgkmcnt(2)\nv_cmp_gt_u32 vcc, v75, v66\nQmava 104,4,132\ns_cbranch_vccnz .Lso13\n.Lro13:\nQmava 104,24,160\n"
"Qtv 22,48,41472\ns_add_u32 m0, s91, 0\ns_mov_b32 s94, 160768\nQmava 188,4,136\nbuffer_load_dwordx4 v60, s[48:51], s94 offen lds\nQmava 188,24,164\nQtv 4,48,28160\nQmava 68,8,124\n"
"Qtv 6,48,29696\nQmava 68,28,152\nQtv 24,48,40448\nQmava 88,8,128\nQtv 26,48,41984\nQmava 88,28,156\nQmava 108,8,132\nbuffer_load_dwordx4 v60, s[48:51], s94 offen offset:1024 lds\n"
"Qmava 108,28,160\nQmava 192,8,136\nQtv 8,48,28672\nQmava 192,28,164\nQtv 10,48,30208\nQmava 72,12,124\nQtv 28,48,40960\nQmava 72,32,152\n"
"Qtv 30,48,42496\nQmava 92,12,128\nbuffer_load_dwordx4 v60, s[48:51], s94 offen offset:2048 lds\nQmava 92,32,156\nQmava 176,12,132\nQmava 176,32,160\nQmava 196,12,136\nQtv 12,49,4096\n"
"Qmava 196,32,164\nQtv 14,49,5120\nQmava 76,16,124\nQtv 32,49,16384\nQmava 76,36,152\nbuffer_load_dwordx4 v60, s[48:51], s94 offen offset:3072 lds\nQmava 96,16,128\nQtv 34,49,17408\n"
"Qmava 96,36,156\nQmava 180,16,132\nQmava 180,36,160\nQmava 200,16,136\n.Lpg15:\ns_cbranch_execz .Ltr6\nQtv 16,49,4608\nv_add_u32 v75, 1, v75\n"
"Qmava 200,36,164\ns_waitcnt vmcnt(19)\nds_write_b32 v73, v75\ns_add_u32 m0, s91, 4096\ns_mov_b32 s95, 454848\ns_waitcnt lgkmcnt(10)\nQmava 0,0,112\nbuffer_load_dwordx4 v61, s[52:55], s95 offen lds\n"
"Qmava 0,20,140\nbuffer_store_dwordx4 v[116:119], v60, s[56:59], s88 offen offset:2048 sc1\nQmava 20,0,116\nQtv 18,49,5632\nQmava 20,20,144\nQtv 36,49,16896\nQmava 40,0,120\nQtv 38,49,17920\n"
"s_mov_b32 s95, 474304\nQmava 40,20,148\nbuffer_load_dwordx4 v61, s[52:55], s95 offen offset:1024 lds\nQmava 4,4,112\nQda 124,46,0\ns_add_u32 s68, %19, 1\nQmava 4,24,140\nds_read_b32 v66, v74\n"
"s_and_b32 s68, s68, 3\nQmava 24,4,116\nQda 152,46,12288\nv_accvgpr_read_b32 v92, a0\nQmava 24,24,144\nQda 128,46,1024\nv_accvgpr_read_b32 v93, a1\nQmava 44,4,120\n"
"Qda 156,46,13312\nv_accvgpr_read_b32 v94, a2\nQmava 44,24,148\ns_waitcnt lgkmcnt(3)\nv_cmp_gt_u32 vcc, v75, v66\nQmava 8,8,112\ns_cbranch_vccnz .Lso14\n.Lro14:\n"
"v_accvgpr_read_b32 v95, a3\nQmava 8,28,140\nQda 132,46,2048\ns_add_u32 m0, s91, 24576\ns_add_u32 s95, s66, 82944\nQmava 28,8,116\nbuffer_load_dwordx4 v61, s[52:55], s95 offen offset:3072 lds\nQmava 28,28,144\n"
"Qda 160,46,14336\nv_accvgpr_read_b32 v124, a4\nQmava 48,8,120\nQda 136,46,3072\nv_accvgpr_read_b32 v125, a5\nQmava 48,28,148\nQda 164,46,15360\nv_accvgpr_read_b32 v126, a6\n"
"s_waitcnt lgkmcnt(9)\nQmava 12,12,112\nv_accvgpr_read_b32 v127, a7\ns_lshl_b32 s68, s68, 3\nQmava 12,32,140\ns_add_u32 s68, s68, 32\ns_mul_i32 s67, s68, 40960\nQmava 32,12,116\n"
"v_accvgpr_read_b32 v156, a8\nv_accvgpr_read_b32 v157, a9\nQmava 32,32,144\nv_accvgpr_read_b32 v158, a10\nv_accvgpr_read_b32 v159, a11\nQmava 52,12,120\ns_add_u32 m0, s91, 28672\ns_add_u32 s95, s66, 86144\n"
"s_mul_i32 s74, s68, 14336\nQmava 52,32,148\nbuffer_load_dwordx4 v62, s[52:55], s95 offen lds\nQmava 16,16,112\nQda 112,46,24576\ns_add_u32 s68, %19, 2\nQmava 16,36,140\nQda 140,46,36864\n"
"s_and_b32 s68, s68, 3\nQmava 36,16,116\nQda 116,46,25600\nv_accvgpr_read_b32 v188, a12\nQmava 36,36,144\nQda 144,46,37888\nv_accvgpr_read_b32 v189, a13\nQmava 56,16,120\n"
"Qda 120,46,26624\nv_add_u32 v75, 1, v75\nQmava 56,36,148\ns_waitcnt vmcnt(17)\nds_write_b32 v73, v75\ns_waitcnt lgkmcnt(6)\nQmava 60,0,124\nQda 148,46,38912\n"
"s_add_u32 s88, s71, 28672\nQmava 60,20,152\nbuffer_store_dwordx4 v[96:99], v60, s[56:59], s88 offen offset:0 sc1\ns_add_u32 s95, s66, 105536\nQmava 80,0,128\nbuffer_load_dwordx4 v61, s[52:55], s95 offen offset:1024 lds\nQmava 80,20,156\nv_accvgpr_read_b32 v190, a14\n"
"v_accvgpr_read_b32 v191, a15\nQmava 100,0,132\ns_lshl_b32 s68, s68, 3\ns_add_u32 s68, s68, 32\nQmava 100,20,160\ns_mul_i32 s72, s68, 40960\ns_mul_i32 s75, s68, 14336\nQmava 184,0,136\n"
"Qtv 0,49,27648\ns_add_u32 s68, %19, 3\nQmava 184,20,164\nds_read_b32 v66, v74\ns_and_b32 s68, s68, 3\nQmava 64,4,124\nQtv 2,49,29184\ns_lshl_b32 s68, s68, 3\n"
"Qmava 64,24,152\nQtv 20,49,39936\ns_add_u32 s68, s68, 32\nQmava 84,4,128\nQtv 22,49,41472\ns_mul_i32 s73, s68, 40960\nQmava 84,24,156\ns_waitcnt lgkmcnt(3)\n"
"v_cmp_gt_u32 vcc, v75, v66\nQmava 104,4,132\ns_cbranch_vccnz .Lso15\n.Lro15:\ns_mul_i32 s77, s68, 14336\nQmava 104,24,160\ns_add_u32 m0, s91, 53248\ns_add_u32 s95, s66, 86208\n"
"v_and_b32 v54, 512, v60\nQmava 188,4,136\nbuffer_load_dwordx4 v61, s[52:55], s95 offen lds\nQmava 188,24,164\nds_write_b128 v44, v[96:99] offset:24576\nQmava 68,8,124\nQtv 4,49,28160\nv_lshlrev_b32 v54, 1, v54\n"
"Qmava 68,28,152\nQtv 6,49,29696\nv_sub_u32 v54, v48, v54\nQmava 88,8,128\nQtv 24,49,40448\ns_mul_i32 s68, s93, 10240\nQmava 88,28,156\nQtv 26,49,41984\n"
"s_add_u32 s68, s68, 4096\nQmava 108,8,132\nv_add_u32 v40, s68, v54\nv_add_u32 v41, 61440, v40\nQmava 108,28,160\ns_sub_u32 s68, 18432, s68\nv_add_u32 v42, s68, v54\nQmava 192,8,136\n"
"Qtv 8,49,28672\nv_add_u32 v43, 61440, v42\nQmava 192,28,164\nQtv 10,49,30208\ns_lshl_b32 s68, s70, 10\nQmava 72,12,124\nQtv 28,49,40960\ns_add_u32 s69, %10, 106496\n"
"Qmava 72,32,152\nQtv 30,49,42496\nv_add_u32 v54, s69, v60\nQmava 92,12,128\nds_write_b128 v44, v[128:131] offset:25600\nQmava 92,32,156\nv_add_u32 v52, s68, v54\ns_xor_b32 s68, s68, 2048\n"
"Qmava 176,12,132\nv_add_u32 v53, s68, v54\nQmava 176,32,160\nQmava 196,12,136\nQtv 12,50,4096\nQmava 196,32,164\nQtv 14,50,5120\nQmava 76,16,124\n"
"Qtv 32,50,16384\ns_add_u32 s95, s66, 105664\nQmava 76,36,152\nbuffer_load_dwordx4 v61, s[52:55], s95 offen offset:1024 lds\nQmava 96,16,128\nQtv 34,50,17408\nQmava 96,36,156\nQmava 180,16,132\n"
"Qmava 180,36,160\nQmava 200,16,136\nds_write_b128 v44, v[160:163] offset:26624\nv_add_u32 v75, 1, v75\nQmava 200,36,164\ns_waitcnt vmcnt(13)\nds_write_b32 v73, v75\ns_waitcnt lgkmcnt(11)\n"
"Qmvva 92,0,112\nQtv 16,50,4608\nQmvva 92,20,140\nbuffer_store_dwordx4 v[128:131], v60, s[56:59], s88 offen offset:1024 sc1\nQmava 20,0,116\nQtv 18,50,5632\nQmava 20,20,144\nQtv 36,50,16896\n"
"Qmava 40,0,120\nQtv 38,50,17920\nQmava 40,20,148\nQda 124,47,0\nQmvva 124,4,112\nQda 152,47,12288\nQmvva 124,24,140\nds_read_b32 v66, v74\n"
"Qmava 24,4,116\nQda 128,47,1024\nQmava 24,24,144\nQda 156,47,13312\nQmava 44,4,120\nds_write_b128 v44, v[192:195] offset:49152\nQmava 44,24,148\ns_waitcnt lgkmcnt(3)\n"
"v_cmp_gt_u32 vcc, v75, v66\nQmvva 156,8,112\ns_cbranch_vccnz .Lso16\n.Lro16:\nv_accvgpr_read_b32 v252, a20\nQmvva 156,28,140\nQda 132,47,2048\ns_add_u32 m0, s91, 73728\n"
"s_mov_b32 s94, 229376\nQmava 28,8,116\nbuffer_load_dwordx4 v60, s[48:51], s94 offen lds\nQmava 28,28,144\nQda 160,47,14336\nv_accvgpr_read_b32 v253, a21\nQmava 48,8,120\nQda 136,47,3072\n"
"v_accvgpr_read_b32 v254, a22\nQmava 48,28,148\nQda 164,47,15360\nv_accvgpr_read_b32 v255, a23\ns_waitcnt lgkmcnt(10)\nQmvva 188,12,112\nbuffer_load_dwordx4 v60, s[48:51], s94 offen offset:1024 lds\nQmvva 188,32,140\n"
"v_accvgpr_read_b32 v100, a24\nv_accvgpr_read_b32 v101, a25\nQmava 32,12,116\nv_accvgpr_read_b32 v102, a26\nv_accvgpr_read_b32 v103, a27\nQmava 32,32,144\nQmava 52,12,120\nds_write_b128 v44, v[224:227] offset:50176\n"
"Qmava 52,32,148\nbuffer_load_dwordx4 v60, s[48:51], s94 offen offset:2048 lds\nQmava 16,16,112\nQda 112,47,33792\nv_accvgpr_read_b32 v132, a28\nQmava 16,36,140\nQda 140,47,46080\nv_accvgpr_read_b32 v133, a29\n"
"Qmava 36,16,116\nQda 116,47,34816\nv_accvgpr_read_b32 v134, a30\nQmava 36,36,144\nQda 144,47,47104\ns_mov_b32 s95, 656384\nQmava 56,16,120\nbuffer_load_dwordx4 v61, s[52:55], s95 offen offset:3072 lds\n"
"v_add_u32 v75, 1, v75\nQmava 56,36,148\ns_waitcnt vmcnt(11)\nds_write_b32 v73, v75\ns_waitcnt lgkmcnt(6)\nQmava 60,0,124\nQda 120,47,35840\nv_accvgpr_read_b32 v135, a31\n"
"Qmava 60,20,152\nbuffer_store_dwordx4 v[160:163], v60, s[56:59], s88 offen offset:2048 sc1\ns_add_u32 m0, s91, 77824\ns_mov_b32 s95, 659584\nQmava 80,0,128\nbuffer_load_dwordx4 v62, s[52:55], s95 offen lds\nQmava 80,20,156\nQda 148,47,48128\n"
"v_accvgpr_read_b32 v220, a16\nQmava 100,0,132\nds_write_b128 v44, v[148:151] offset:51200\nQmava 100,20,160\nv_accvgpr_read_b32 v221, a17\nv_accvgpr_read_b32 v222, a18\nQmava 184,0,136\nQtv 0,50,36864\n"
"v_accvgpr_read_b32 v223, a19\nQmava 184,20,164\nds_read_b32 v66, v74\ns_mov_b32 s95, 678976\nQmava 64,4,124\nbuffer_load_dwordx4 v61, s[52:55], s95 offen offset:1024 lds\nQmava 64,24,152\nQtv 2,50,38400\n"
"Qmava 84,4,128\nQtv 20,50,49152\nQmava 84,24,156\ns_waitcnt lgkmcnt(2)\nv_cmp_gt_u32 vcc, v75, v66\nQmava 104,4,132\ns_cbranch_vccnz .Lso17\n.Lro17:\n"
"Qmava 104,24,160\nQtv 22,50,50688\ns_add_u32 m0, s91, 98304\ns_mov_b32 s94, 232448\nQmava 188,4,136\nbuffer_load_dwordx4 v60, s[48:51], s94 offen lds\nQmava 188,24,164\nQtv 4,50,37376\n"
"Qmava 68,8,124\nds_write_b128 v44, v[180:183] offset:52224\nQmava 68,28,152\nQtv 6,50,38912\nQmava 88,8,128\nQtv 24,50,49664\nQmava 88,28,156\nQtv 26,50,51200\n"
"Qmava 108,8,132\nbuffer_load_dwordx4 v60, s[48:51], s94 offen offset:1024 lds\nQmava 108,28,160\nQmava 192,8,136\nQtv 8,50,37888\nQmava 192,28,164\nQtv 10,50,39424\nQmava 72,12,124\n"
"Qtv 28,50,50176\nQmava 72,32,152\nQtv 30,50,51712\nQmava 92,12,128\nbuffer_load_dwordx4 v60, s[48:51], s94 offen offset:2048 lds\nQmava 92,32,156\nQmava 176,12,132\nQmava 176,32,160\n"
"Qmava 196,12,136\nQtv 12,48,4096\nQmava 196,32,164\nQtv 14,48,5120\nQmava 76,16,124\nQtv 32,48,16384\nQmava 76,36,152\nbuffer_load_dwordx4 v60, s[48:51], s94 offen offset:3072 lds\n"
"Qmava 96,16,128\nQtv 34,48,17408\nQmava 96,36,156\nQmava 180,16,132\nQmava 180,36,160\nQmava 200,16,136\nQtv 16,48,4608\nv_add_u32 v75, 1, v75\n"
"Qmava 200,36,164\ns_waitcnt vmcnt(14)\nds_write_b32 v73, v75\ns_add_u32 m0, s91, 102400\ns_mov_b32 s95, 659648\ns_waitcnt lgkmcnt(10)\nQmvva 92,0,112\nbuffer_load_dwordx4 v61, s[52:55], s95 offen lds\n"
"Qmvva 92,20,140\nbuffer_store_dwordx4 v[192:195], v60, s[56:59], s88 offen offset:3072 sc1\nQmvva 252,0,116\nQtv 18,48,5632\nQmvva 252,20,144\nQtv 36,48,16896\nQmava 40,0,120\nQtv 38,48,17920\n"
"s_mov_b32 s95, 679104\nQmava 40,20,148\nbuffer_load_dwordx4 v61, s[52:55], s95 offen offset:1024 lds\nQmvva 124,4,112\nQda 124,45,0\nQmvva 124,24,140\nds_read_b32 v66, v74\nQmvva 100,4,116\n"
"Qda 152,45,12288\nQmvva 100,24,144\nQda 128,45,1024\nQmava 44,4,120\nQda 156,45,13312\nQmava 44,24,148\ns_waitcnt lgkmcnt(3)\nv_cmp_gt_u32 vcc, v75, v66\n"
"Qmvva 156,8,112\ns_cbranch_vccnz .Lso18\n.Lro18:\nv_accvgpr_read_b32 v228, a40\nQmvva 156,28,140\nQda 132,45,2048\ns_add_u32 m0, s91, 132096\ns_mov_b32 s94, 243712\n"
"Qmvva 132,8,116\nbuffer_load_dwordx4 v60, s[48:51], s94 offen lds\nQmvva 132,28,144\nQda 160,45,14336\nv_accvgpr_read_b32 v229, a41\nQmava 48,8,120\nQda 136,45,3072\nv_accvgpr_read_b32 v230, a42\n"
"Qmava 48,28,148\nQda 164,45,15360\nv_accvgpr_read_b32 v231, a43\ns_waitcnt lgkmcnt(9)\nQmvva 188,12,112\nbuffer_load_dwordx4 v60, s[48:51], s94 offen offset:1024 lds\nQmvva 188,32,140\nv_accvgpr_read_b32 v108, a44\n"
"v_accvgpr_read_b32 v109, a45\nQmava 32,12,116\nv_accvgpr_read_b32 v110, a46\nv_accvgpr_read_b32 v111, a47\nQmava 32,32,144\nQmava 52,12,120\nQmava 52,32,148\nbuffer_load_dwordx4 v60, s[48:51], s94 offen offset:2048 lds\n"
"Qmvva 220,16,112\nQda 112,45,24576\nQmvva 220,36,140\nQda 140,45,36864\nQmava 36,16,116\nQda 116,45,25600\nQmava 36,36,144\nQda 144,45,37888\n"
"s_mov_b32 s95, 697344\nQmava 56,16,120\nbuffer_load_dwordx4 v61, s[52:55], s95 offen offset:3072 lds\nv_add_u32 v75, 1, v75\nQmava 56,36,148\ns_waitcnt vmcnt(19)\nds_write_b32 v73, v75\ns_waitcnt lgkmcnt(5)\n"
"Qmava 60,0,124\nQda 120,45,26624\ns_add_u32 s88, s71, 32768\nQmava 60,20,152\nbuffer_store_dwordx4 v[224:227], v60, s[56:59], s88 offen offset:0 sc1\ns_add_u32 m0, s91, 136192\ns_mov_b32 s95, 700544\nQmava 80,0,128\n"
"buffer_load_dwordx4 v62, s[52:55], s95 offen lds\nQmava 80,20,156\nQda 148,45,38912\nv_accvgpr_read_b32 v164, a32\nQmava 100,0,132\nv_accvgpr_read_b32 v165, a33\nv_accvgpr_read_b32 v166, a34\nQmava 100,20,160\n"
"v_accvgpr_read_b32 v167, a35\nv_accvgpr_read_b32 v196, a36\nQmava 184,0,136\nQtv 0,48,27648\nv_accvgpr_read_b32 v197, a37\nQmava 184,20,164\nds_read_b32 v66, v74\ns_mov_b32 s95, 719936\n"
"Qmava 64,4,124\nbuffer_load_dwordx4 v61, s[52:55], s95 offen offset:1024 lds\nQmava 64,24,152\nQtv 2,48,29184\nv_accvgpr_read_b32 v198, a38\nQmava 84,4,128\nQtv 20,48,39936\nv_add_u32 v67, 3, v65\n"
"Qmava 84,24,156\ns_waitcnt lgkmcnt(2)\nv_cmp_gt_u32 vcc, v75, v66\nQmava 104,4,132\ns_cbranch_vccnz .Lso19\n.Lro19:\nv_accvgpr_read_b32 v199, a39\nQmava 104,24,160\n"
"buffer_store_dword v67, v77, s[24:27], 0 offen sc1\ns_add_u32 m0, s91, 0\ns_mov_b32 s94, 246784\nQmava 188,4,136\nbuffer_load_dwordx4 v60, s[48:51], s94 offen lds\nQmava 188,24,164\nQtv 22,48,41472\nQmava 68,8,124\n"
"Qtv 4,48,28160\nQmava 68,28,152\nQtv 6,48,29696\nQmava 88,8,128\nQtv 24,48,40448\nQmava 88,28,156\nQtv 26,48,41984\nQmava 108,8,132\n"
"buffer_load_dwordx4 v60, s[48:51], s94 offen offset:1024 lds\nQmava 108,28,160\nQmava 192,8,136\nQtv 8,48,28672\nQmava 192,28,164\nQtv 10,48,30208\nQmava 72,12,124\nQtv 28,48,40960\n"
"Qmava 72,32,152\nQtv 30,48,42496\nQmava 92,12,128\nbuffer_load_dwordx4 v60, s[48:51], s94 offen offset:2048 lds\nQmava 92,32,156\nQmava 176,12,132\nQmava 176,32,160\nQmava 196,12,136\n"
"Qtv 12,49,4096\nQmava 196,32,164\nQtv 14,49,5120\nQmava 76,16,124\nQtv 32,49,16384\nQmava 76,36,152\nbuffer_load_dwordx4 v60, s[48:51], s94 offen offset:3072 lds\nQmava 96,16,128\n"
"Qtv 34,49,17408\nQmava 96,36,156\nQmava 180,16,132\nQmava 180,36,160\nQmava 200,16,136\nQtv 16,49,4608\nv_add_u32 v75, 1, v75\nQmava 200,36,164\n"
"s_waitcnt vmcnt(19)\nds_write_b32 v73, v75\ns_add_u32 m0, s91, 4096\ns_mov_b32 s95, 700608\ns_waitcnt lgkmcnt(10)\nQmvva 92,0,112\nbuffer_load_dwordx4 v61, s[52:55], s95 offen lds\nQmvva 92,20,140\n"
"buffer_store_dwordx4 v[148:151], v60, s[56:59], s88 offen offset:1024 sc1\nQmvva 252,0,116\nQtv 18,49,5632\nQmvva 252,20,144\nQtv 36,49,16896\nQmvva 228,0,120\nQtv 38,49,17920\ns_mov_b32 s95, 720064\n"
"Qmvva 228,20,148\nbuffer_load_dwordx4 v61, s[52:55], s95 offen offset:1024 lds\nQmvva 124,4,112\nQda 124,46,0\n.Lpg16:\ns_cbranch_execz .Lpg20\nQmvva 124,24,140\nds_read_b32 v66, v74\n"
"Qmvva 100,4,116\nQda 152,46,12288\nQmvva 100,24,144\nQda 128,46,1024\nQmvva 108,4,120\nQda 156,46,13312\nQmvva 108,24,148\ns_waitcnt lgkmcnt(3)\n"
"v_cmp_gt_u32 vcc, v75, v66\nQmvva 156,8,112\ns_cbranch_vccnz .Lso20\n.Lro20:\nQmvva 156,28,140\nQda 132,46,2048\ns_add_u32 m0, s91, 24576\ns_add_u32 s95, s66, 123904\n"
"Qmvva 132,8,116\nbuffer_load_dwordx4 v61, s[52:55], s95 offen offset:3072 lds\nQmvva 132,28,144\nQda 160,46,14336\nQmava 48,8,120\nQda 136,46,3072\nQmava 48,28,148\nQda 164,46,15360\n"
"s_waitcnt lgkmcnt(9)\nQmvva 188,12,112\nQmvva 188,32,140\nQmvva 164,12,116\nQmvva 164,32,144\nQmava 52,12,120\ns_add_u32 m0, s91, 28672\ns_add_u32 s95, s66, 127104\n"
"Qmava 52,32,148\nbuffer_load_dwordx4 v62, s[52:55], s95 offen lds\nQmvva 220,16,112\nQda 112,46,24576\nv_accvgpr_read_b32 v140, a48\nQmvva 220,36,140\nQda 140,46,36864\nv_accvgpr_read_b32 v141, a49\n"
"Qmvva 196,16,116\nQda 116,46,25600\nv_accvgpr_read_b32 v142, a50\nQmvva 196,36,144\nQda 144,46,37888\nv_accvgpr_read_b32 v143, a51\nQmava 56,16,120\nQda 120,46,26624\n"
"v_add_u32 v75, 1, v75\nQmava 56,36,148\ns_waitcnt vmcnt(17)\nds_write_b32 v73, v75\ns_waitcnt lgkmcnt(6)\nQmava 60,0,124\nQda 148,46,38912\nv_accvgpr_read_b32 v172, a52\n"
"Qmava 60,20,152\nbuffer_store_dwordx4 v[180:183], v60, s[56:59], s88 offen offset:2048 sc1\ns_add_u32 s95, s66, 146496\nQmava 80,0,128\nbuffer_load_dwordx4 v61, s[52:55], s95 offen offset:1024 lds\nQmava 80,20,156\nv_accvgpr_read_b32 v173, a53\nv_accvgpr_read_b32 v174, a54\n"
"Qmava 100,0,132\nv_accvgpr_read_b32 v175, a55\nQmava 100,20,160\nQmava 184,0,136\nQtv 0,49,27648\nv_accvgpr_read_b32 v204, a56\nQmava 184,20,164\nds_read_b32 v66, v74\n"
"v_accvgpr_read_b32 v205, a57\nQmava 64,4,124\nQtv 2,49,29184\nv_accvgpr_read_b32 v206, a58\nQmava 64,24,152\nQtv 20,49,39936\nv_accvgpr_read_b32 v207, a59\nQmava 84,4,128\n"
"Qtv 22,49,41472\nv_accvgpr_read_b32 v236, a60\nQmava 84,24,156\ns_waitcnt lgkmcnt(3)\nv_cmp_gt_u32 vcc, v75, v66\nQmava 104,4,132\ns_cbranch_vccnz .Lso21\n.Lro21:\n"
"v_accvgpr_read_b32 v237, a61\nQmava 104,24,160\ns_add_u32 m0, s91, 53248\ns_add_u32 s95, s66, 127168\nv_accvgpr_read_b32 v238, a62\nQmava 188,4,136\nbuffer_load_dwordx4 v61, s[52:55], s95 offen lds\nQmava 188,24,164\n"
"ds_write_b128 v44, v[104:107] offset:24576\nQmava 68,8,124\nQtv 4,49,28160\nv_accvgpr_read_b32 v239, a63\nQmava 68,28,152\nQtv 6,49,29696\nQmava 88,8,128\nQtv 24,49,40448\n"
"Qmava 88,28,156\nQtv 26,49,41984\nQmava 108,8,132\nQmava 108,28,160\nQmava 192,8,136\nQtv 8,49,28672\nQmava 192,28,164\nQtv 10,49,30208\n"
"Qmava 72,12,124\nQtv 28,49,40960\nQmava 72,32,152\nQtv 30,49,42496\nQmava 92,12,128\nds_write_b128 v44, v[136:139] offset:25600\nQmava 92,32,156\nQmava 176,12,132\n"
"Qmava 176,32,160\nQmava 196,12,136\nQtv 12,50,4096\nQmava 196,32,164\nQtv 14,50,5120\nQmava 76,16,124\nQtv 32,50,16384\ns_add_u32 s95, s66, 146624\n"
"Qmava 76,36,152\nbuffer_load_dwordx4 v61, s[52:55], s95 offen offset:1024 lds\nQmava 96,16,128\nQtv 34,50,17408\nQmava 96,36,156\nQmava 180,16,132\nQmava 180,36,160\nQmava 200,16,136\n"
"ds_write_b128 v44, v[168:171] offset:26624\nv_add_u32 v75, 1, v75\nQmava 200,36,164\ns_waitcnt vmcnt(14)\nds_write_b32 v73, v75\ns_waitcnt lgkmcnt(11)\nQmvva 92,0,112\nQtv 16,50,4608\n"
"s_add_u32 s88, s71, 43008\nQmvva 92,20,140\nbuffer_store_dwordx4 v[104:107], v60, s[56:59], s88 offen offset:0 sc1\nQmvva 252,0,116\nQtv 18,50,5632\nQmvva 252,20,144\nQtv 36,50,16896\nQmvva 228,0,120\n"
"Qtv 38,50,17920\nQmvva 228,20,148\nQda 124,47,0\nQmvva 124,4,112\nQda 152,47,12288\nQmvva 124,24,140\nds_read_b32 v66, v74\nQmvva 100,4,116\n"
"Qda 128,47,1024\nQmvva 100,24,144\nQda 156,47,13312\nQmvva 108,4,120\nds_write_b128 v44, v[200:203] offset:49152\nQmvva 108,24,148\ns_waitcnt lgkmcnt(3)\nv_cmp_gt_u32 vcc, v75, v66\n"
"Qmvva 156,8,112\ns_cbranch_vccnz .Lso22\n.Lro22:\nQmvva 156,28,140\nQda 132,47,2048\ns_add_u32 m0, s91, 73728\ns_mov_b32 s94, 258048\nQmvva 132,8,116\n"
"buffer_load_dwordx4 v60, s[48:51], s94 offen lds\nQmvva 132,28,144\nQda 160,47,14336\nQmvva 140,8,120\nQda 136,47,3072\nQmvva 140,28,148\nQda 164,47,15360\ns_waitcnt lgkmcnt(10)\n"
"Qmvva 188,12,112\nbuffer_load_dwordx4 v60, s[48:51], s94 offen offset:1024 lds\nQmvva 188,32,140\nQmvva 164,12,116\nQmvva 164,32,144\nQmvva 172,12,120\nds_write_b128 v44, v[232:235] offset:50176\nQmvva 172,32,148\n"
"buffer_load_dwordx4 v60, s[48:51], s94 offen offset:2048 lds\nQmvva 220,16,112\nQda 112,47,33792\nQmvva 220,36,140\nQda 140,47,46080\nQmvva 196,16,116\nQda 116,47,34816\nQmvva 196,36,144\n"
"Qda 144,47,47104\ns_mov_b32 s95, 738304\nQmvva 204,16,120\nbuffer_load_dwordx4 v61, s[52:55], s95 offen offset:3072 lds\nv_add_u32 v75, 1, v75\nQmvva 204,36,148\ns_waitcnt vmcnt(11)\nds_write_b32 v73, v75\n"
"s_waitcnt lgkmcnt(6)\nQmvva 236,0,124\nQda 120,47,35840\nQmvva 236,20,152\nbuffer_store_dwordx4 v[136:139], v60, s[56:59], s88 offen offset:1024 sc1\ns_add_u32 m0, s91, 77824\ns_mov_b32 s95, 741504\nQmava 80,0,128\n"
"buffer_load_dwordx4 v62, s[52:55], s95 offen lds\nQmava 80,20,156\nQda 148,47,48128\nQmava 100,0,132\nds_write_b128 v44, v[212:215] offset:51200\nQmava 100,20,160\nQmava 184,0,136\nQtv 0,50,36864\n"
"Qmava 184,20,164\nds_read_b32 v66, v74\ns_mov_b32 s95, 760896\nQmava 64,4,124\nbuffer_load_dwordx4 v61, s[52:55], s95 offen offset:1024 lds\nQmava 64,24,152\nQtv 2,50,38400\nQmava 84,4,128\n"
"Qtv 20,50,49152\nQmava 84,24,156\ns_waitcnt lgkmcnt(2)\nv_cmp_gt_u32 vcc, v75, v66\nQmava 104,4,132\ns_cbranch_vccnz .Lso23\n.Lro23:\nQmava 104,24,160\n"
"Qtv 22,50,50688\ns_add_u32 m0, s91, 98304\ns_mov_b32 s94, 261120\nQmava 188,4,136\nbuffer_load_dwordx4 v60, s[48:51], s94 offen lds\nQmava 188,24,164\nQtv 4,50,37376\nQmava 68,8,124\n"
"ds_write_b128 v44, v[244:247] offset:52224\nQmava 68,28,152\nQtv 6,50,38912\nv_accvgpr_read_b32 v80, a64\nQmava 88,8,128\nQtv 24,50,49664\nv_accvgpr_read_b32 v81, a65\nQmava 88,28,156\n"
"Qtv 26,50,51200\nv_accvgpr_read_b32 v82, a66\nQmava 108,8,132\nbuffer_load_dwordx4 v60, s[48:51], s94 offen offset:1024 lds\nQmava 108,28,160\nv_accvgpr_read_b32 v83, a67\nQmava 192,8,136\nQtv 8,50,37888\n"
"Qmava 192,28,164\nQtv 10,50,39424\nQmava 72,12,124\nQtv 28,50,50176\nv_accvgpr_read_b32 v112, a68\nQmava 72,32,152\nQtv 30,50,51712\nv_accvgpr_read_b32 v113, a69\n"
"Qmava 92,12,128\nbuffer_load_dwordx4 v60, s[48:51], s94 offen offset:2048 lds\nQmava 92,32,156\nv_accvgpr_read_b32 v114, a70\nv_accvgpr_read_b32 v115, a71\nQmava 176,12,132\nQmava 176,32,160\nQmava 196,12,136\n"
"Qtv 12,48,4096\nQmava 196,32,164\nQtv 14,48,5120\nQmava 76,16,124\nQtv 32,48,16384\nv_accvgpr_read_b32 v144, a72\nQmava 76,36,152\nbuffer_load_dwordx4 v60, s[48:51], s94 offen offset:3072 lds\n"
"Qmava 96,16,128\nQtv 34,48,17408\nv_accvgpr_read_b32 v145, a73\nQmava 96,36,156\nv_accvgpr_read_b32 v146, a74\nv_accvgpr_read_b32 v147, a75\nQmava 180,16,132\nQmava 180,36,160\n"
"Qmava 200,16,136\nQtv 16,48,4608\nv_add_u32 v75, 1, v75\nQmava 200,36,164\ns_waitcnt vmcnt(14)\nds_write_b32 v73, v75\ns_add_u32 m0, s91, 102400\ns_mov_b32 s95, 741568\n"
"s_waitcnt lgkmcnt(10)\nQmvva 92,0,112\nbuffer_load_dwordx4 v61, s[52:55], s95 offen lds\nQmvva 92,20,140\nbuffer_store_dwordx4 v[168:171], v60, s[56:59], s88 offen offset:2048 sc1\nQmvva 252,0,116\nQtv 18,48,5632\nv_accvgpr_read_b32 v176, a76\n"
"Qmvva 252,20,144\nQtv 36,48,16896\nv_accvgpr_read_b32 v177, a77\nQmvva 228,0,120\nQtv 38,48,17920\ns_mov_b32 s95, 761024\nQmvva 228,20,148\nbuffer_load_dwordx4 v61, s[52:55], s95 offen offset:1024 lds\n"
"Qmvva 124,4,112\nQda 124,45,0\nv_accvgpr_read_b32 v178, a78\nQmvva 124,24,140\nds_read_b32 v66, v74\nv_accvgpr_read_b32 v179, a79\nQmvva 100,4,116\nQda 152,45,12288\n"
"Qmvva 100,24,144\nQda 128,45,1024\nQmvva 108,4,120\nQda 156,45,13312\nQmvva 108,24,148\ns_waitcnt lgkmcnt(3)\nv_cmp_gt_u32 vcc, v75, v66\nQmvva 156,8,112\n"
"s_cbranch_vccnz .Lso24\n.Lro24:\nQmvva 156,28,140\nQda 132,45,2048\ns_add_u32 m0, s91, 132096\ns_mov_b32 s94, 272384\nQmvva 132,8,116\nbuffer_load_dwordx4 v60, s[48:51], s94 offen lds\n"
"Qmvva 132,28,144\nQda 160,45,14336\nQmvva 140,8,120\nQda 136,45,3072\nQmvva 140,28,148\nQda 164,45,15360\ns_waitcnt lgkmcnt(9)\nQmvva 188,12,112\n"
"buffer_load_dwordx4 v60, s[48:51], s94 offen offset:1024 lds\nQmvva 188,32,140\nQmvva 164,12,116\nQmvva 164,32,144\nQmvva 172,12,120\nQmvva 172,32,148\nbuffer_load_dwordx4 v60, s[48:51], s94 offen offset:2048 lds\nQmvva 220,16,112\n"
"Qda 112,45,24576\nQmvva 220,36,140\nQda 140,45,36864\nQmvva 196,16,116\nQda 116,45,25600\nQmvva 196,36,144\nQda 144,45,37888\ns_mov_b32 s95, 779264\n"
"Qmvva 204,16,120\nbuffer_load_dwordx4 v61, s[52:55], s95 offen offset:3072 lds\nv_add_u32 v75, 1, v75\nQmvva 204,36,148\ns_waitcnt vmcnt(19)\nds_write_b32 v73, v75\ns_waitcnt lgkmcnt(5)\nQmvva 236,0,124\n"
"Qda 120,45,26624\nQmvva 236,20,152\nbuffer_store_dwordx4 v[200:203], v60, s[56:59], s88 offen offset:3072 sc1\ns_add_u32 m0, s91, 136192\ns_mov_b32 s95, 782464\nQmava 80,0,128\nbuffer_load_dwordx4 v62, s[52:55], s95 offen lds\nQmava 80,20,156\n"
"Qda 148,45,38912\nQmava 100,0,132\nQmava 100,20,160\nQmava 184,0,136\nQtv 0,48,27648\nQmava 184,20,164\nds_read_b32 v66, v74\ns_mov_b32 s95, 801856\n"
"Qmvva 80,4,124\nbuffer_load_dwordx4 v61, s[52:55], s95 offen offset:1024 lds\nQmvva 80,24,152\nQtv 2,48,29184\nQmava 84,4,128\nQtv 20,48,39936\nv_accvgpr_read_b32 v208, a80\nQmava 84,24,156\n"
"s_waitcnt lgkmcnt(2)\nv_cmp_gt_u32 vcc, v75, v66\nQmava 104,4,132\ns_cbranch_vccnz .Lso25\n.Lro25:\nv_accvgpr_read_b32 v209, a81\nQmava 104,24,160\nQtv 22,48,41472\n"
"s_add_u32 m0, s91, 0\ns_mov_b32 s94, 275456\nQmava 188,4,136\nbuffer_load_dwordx4 v60, s[48:51], s94 offen lds\nQmava 188,24,164\nQtv 4,48,28160\nv_accvgpr_read_b32 v210, a82\nQmvva 112,8,124\n"
"Qtv 6,48,29696\nv_accvgpr_read_b32 v211, a83\nQmvva 112,28,152\nQtv 24,48,40448\nQmava 88,8,128\nQtv 26,48,41984\nv_accvgpr_read_b32 v240, a84\nQmava 88,28,156\n"
"v_accvgpr_read_b32 v241, a85\nv_accvgpr_read_b32 v242, a86\nQmava 108,8,132\nbuffer_load_dwordx4 v60, s[48:51], s94 offen offset:1024 lds\nQmava 108,28,160\nv_accvgpr_read_b32 v243, a87\nQmava 192,8,136\nQtv 8,48,28672\n"
"Qmava 192,28,164\nQtv 10,48,30208\nQmvva 144,12,124\nQtv 28,48,40960\nQmvva 144,32,152\nQtv 30,48,42496\nQmava 92,12,128\nbuffer_load_dwordx4 v60, s[48:51], s94 offen offset:2048 lds\n"
"Qmava 92,32,156\nv_accvgpr_read_b32 v84, a88\nv_accvgpr_read_b32 v85, a89\nQmava 176,12,132\nv_accvgpr_read_b32 v86, a90\nv_accvgpr_read_b32 v87, a91\nQmava 176,32,160\nQmava 196,12,136\n"
"Qtv 12,49,4096\nQmava 196,32,164\nQtv 14,49,5120\nQmvva 176,16,124\nQtv 32,49,16384\nQmvva 176,36,152\nbuffer_load_dwordx4 v60, s[48:51], s94 offen offset:3072 lds\nQmava 96,16,128\n"
"Qtv 34,49,17408\nv_accvgpr_read_b32 v88, a92\nQmava 96,36,156\nv_accvgpr_read_b32 v89, a93\nv_accvgpr_read_b32 v90, a94\nQmava 180,16,132\nv_accvgpr_read_b32 v91, a95\nQmava 180,36,160\n"
"Qmava 200,16,136\nQtv 16,49,4608\nv_add_u32 v75, 1, v75\nQmava 200,36,164\ns_waitcnt vmcnt(18)\nds_write_b32 v73, v75\ns_add_u32 m0, s91, 4096\ns_mov_b32 s95, 782528\n"
"s_waitcnt lgkmcnt(10)\nQmvva 92,0,112\nbuffer_load_dwordx4 v61, s[52:55], s95 offen lds\ns_add_u32 s88, s71, 47104\nQmvva 92,20,140\nbuffer_store_dwordx4 v[232:235], v60, s[56:59], s88 offen offset:0 sc1\nQmvva 252,0,116\nQtv 18,49,5632\n"
"Qmvva 252,20,144\nQtv 36,49,16896\nQmvva 228,0,120\nQtv 38,49,17920\ns_mov_b32 s95, 801984\nQmvva 228,20,148\nbuffer_load_dwordx4 v61, s[52:55], s95 offen offset:1024 lds\nQmvva 124,4,112\n"
"Qda 124,46,0\nQmvva 124,24,140\nds_read_b32 v66, v74\nQmvva 100,4,116\nQda 152,46,12288\nQmvva 100,24,144\nQda 128,46,1024\nQmvva 108,4,120\n"
"Qda 156,46,13312\nv_add_u32 v67, 4, v65\nQmvva 108,24,148\ns_waitcnt lgkmcnt(3)\nv_cmp_gt_u32 vcc, v75, v66\nQmvva 156,8,112\ns_cbranch_vccnz .Lso26\n.Lro26:\n"
"Qmvva 156,28,140\nbuffer_store_dword v67, v77, s[24:27], 0 offen sc1\ns_add_u32 m0, s91, 24576\ns_mov_b32 s94, 344064\nQmvva 132,8,116\nbuffer_load_dwordx4 v60, s[48:51], s94 offen lds\nQmvva 132,28,144\nQda 132,46,2048\n"
"Qmvva 140,8,120\nQda 160,46,14336\nQmvva 140,28,148\nQda 136,46,3072\ns_waitcnt lgkmcnt(8)\nQmvva 188,12,112\nbuffer_load_dwordx4 v60, s[48:51], s94 offen offset:1024 lds\nQmvva 188,32,140\n"
"Qda 164,46,15360\nQmvva 164,12,116\nQmvva 164,32,144\nQmvva 172,12,120\nQmvva 172,32,148\nbuffer_load_dwordx4 v60, s[48:51], s94 offen offset:2048 lds\nQmvva 220,16,112\nQda 112,46,24576\n"
"Qmvva 220,36,140\nQda 140,46,36864\nQmvva 196,16,116\nQda 116,46,25600\nQmvva 196,36,144\nQda 144,46,37888\ns_mov_b32 s95, 984064\nQmvva 204,16,120\n"
"buffer_load_dwordx4 v61, s[52:55], s95 offen offset:3072 lds\nv_add_u32 v75, 1, v75\nQmvva 204,36,148\n.Lpg17:\ns_cbranch_execz .Lpg21\ns_waitcnt vmcnt(19)\nds_write_b32 v73, v75\ns_waitcnt lgkmcnt(5)\n"
"Qmvva 236,0,124\nQda 120,46,26624\nQmvva 236,20,152\nbuffer_store_dwordx4 v[212:215], v60, s[56:59], s88 offen offset:1024 sc1\ns_add_u32 m0, s91, 28672\ns_mov_b32 s95, 987264\nQmvva 208,0,128\nbuffer_load_dwordx4 v62, s[52:55], s95 offen lds\n"
"Qmvva 208,20,156\nQda 148,46,38912\nQmava 100,0,132\nQmava 100,20,160\nQmava 184,0,136\nQtv 0,49,27648\nQmava 184,20,164\nds_read_b32 v66, v74\n"
"s_mov_b32 s95, 1006656\nQmvva 80,4,124\nbuffer_load_dwordx4 v61, s[52:55], s95 offen offset:1024 lds\nQmvva 80,24,152\nQtv 2,49,29184\nQmvva 240,4,128\nQtv 20,49,39936\nQmvva 240,24,156\n"
"s_waitcnt lgkmcnt(2)\nv_cmp_gt_u32 vcc, v75, v66\nQmava 104,4,132\ns_cbranch_vccnz .Lso27\n.Lro27:\nv_accvgpr_read_b32 v152, a100\nQmava 104,24,160\nQtv 22,49,41472\n"
"s_add_u32 m0, s91, 49152\ns_mov_b32 s94, 347136\nQmava 188,4,136\nbuffer_load_dwordx4 v60, s[48:51], s94 offen lds\nQmava 188,24,164\nQtv 4,49,28160\nv_accvgpr_read_b32 v153, a101\nQmvva 112,8,124\n"
"Qtv 6,49,29696\nv_accvgpr_read_b32 v154, a102\nQmvva 112,28,152\nQtv 24,49,40448\nv_accvgpr_read_b32 v155, a103\nQmvva 84,8,128\nQtv 26,49,41984\nQmvva 84,28,156\n"
"Qmava 108,8,132\nbuffer_load_dwordx4 v60, s[48:51], s94 offen offset:1024 lds\nQmava 108,28,160\nv_accvgpr_read_b32 v184, a104\nv_accvgpr_read_b32 v185, a105\nQmava 192,8,136\nQtv 8,49,28672\nv_accvgpr_read_b32 v186, a106\n"
"Qmava 192,28,164\nQtv 10,49,30208\nv_accvgpr_read_b32 v187, a107\nQmvva 144,12,124\nQtv 28,49,40960\nQmvva 144,32,152\nQtv 30,49,42496\nQmvva 88,12,128\n"
"buffer_load_dwordx4 v60, s[48:51], s94 offen offset:2048 lds\nQmvva 88,32,156\nQmava 176,12,132\nv_accvgpr_read_b32 v216, a108\nv_accvgpr_read_b32 v217, a109\nQmava 176,32,160\nv_accvgpr_read_b32 v218, a110\nv_accvgpr_read_b32 v219, a111\n"
"Qmava 196,12,136\nQtv 12,50,4096\nQmava 196,32,164\nQtv 14,50,5120\nQmvva 176,16,124\nQtv 32,50,16384\nQmvva 176,36,152\nbuffer_load_dwordx4 v60, s[48:51], s94 offen offset:3072 lds\n"
"Qmava 96,16,128\nQtv 34,50,17408\nQmava 96,36,156\nQmava 180,16,132\nQmava 180,36,160\nQmava 200,16,136\nQtv 16,50,4608\nv_add_u32 v75, 1, v75\n"
"Qmava 200,36,164\ns_waitcnt vmcnt(19)\nds_write_b32 v73, v75\ns_add_u32 m0, s91, 53248\ns_mov_b32 s95, 987328\ns_waitcnt lgkmcnt(10)\nQmvva 92,0,112\nbuffer_load_dwordx4 v61, s[52:55], s95 offen lds\n"
"Qmvva 92,20,140\nbuffer_store_dwordx4 v[244:247], v60, s[56:59], s88 offen offset:2048 sc1\nQmvva 252,0,116\nQtv 18,50,5632\nv_accvgpr_read_b32 v120, a96\nQmvva 252,20,144\nQtv 36,50,16896\nv_accvgpr_read_b32 v121, a97\n"
"Qmvva 228,0,120\nQtv 38,50,17920\ns_mov_b32 s95, 1006784\nQmvva 228,20,148\nbuffer_load_dwordx4 v61, s[52:55], s95 offen offset:1024 lds\nQmvva 124,4,112\nQda 124,47,0\nv_accvgpr_read_b32 v122, a98\n"
"Qmvva 124,24,140\nds_read_b32 v66, v74\nv_accvgpr_read_b32 v123, a99\nQmvva 100,4,116\nQda 152,47,12288\ns_mov_b32 s40, %17\nQmvva 100,24,144\nQda 128,47,1024\n"
"s_and_b32 s41, %18, 0xffff\nQmvva 108,4,120\nQda 156,47,13312\ns_mov_b32 s42, 571200\nQmvva 108,24,148\ns_waitcnt lgkmcnt(3)\nv_cmp_gt_u32 vcc, v75, v66\nQmvva 156,8,112\n"
"s_cbranch_vccnz .Lso28\n.Lro28:\ns_lshr_b32 s68, %19, 2\nQmvva 156,28,140\nQda 132,47,2048\ns_add_u32 m0, s91, 73728\ns_mov_b32 s94, 358400\nQmvva 132,8,116\n"
"buffer_load_dwordx4 v60, s[48:51], s94 offen lds\nQmvva 132,28,144\nQda 160,47,14336\ns_mul_i32 s65, s68, 286720\nQmvva 140,8,120\nQda 136,47,3072\ns_sub_u32 s60, 286720, s65\nQmvva 140,28,148\n"
"Qda 164,47,15360\ns_mul_i32 s68, s68, 114688\ns_waitcnt lgkmcnt(9)\nQmvva 188,12,112\nbuffer_load_dwordx4 v60, s[48:51], s94 offen offset:1024 lds\nQmvva 188,32,140\ns_add_u32 s64, s98, 57344\ns_sub_u32 s64, s64, s68\n"
"Qmvva 164,12,116\ns_lshl_b32 s68, s93, 5\ns_add_u32 s68, s68, %10\nQmvva 164,32,144\nv_bfe_u32 v54, v60, 4, 4\nv_mul_u32_u24 v54, 336, v54\nQmvva 172,12,120\nv_lshrrev_b32 v55, 8, v60\n"
"v_lshlrev_b32 v56, 3, v55\nQmvva 172,32,148\nbuffer_load_dwordx4 v60, s[48:51], s94 offen offset:2048 lds\nQmvva 220,16,112\nQda 112,47,33792\nv_add3_u32 v63, v54, v56, s68\nQmvva 220,36,140\nQda 140,47,46080\n"
"s_lshl_b32 s68, s70, 2\nQmvva 196,16,116\nQda 116,47,34816\nv_add_u32 v55, s68, v55\nQmvva 196,36,144\nQda 144,47,47104\ns_mov_b32 s95, 1025024\nQmvva 204,16,120\n"
"buffer_load_dwordx4 v61, s[52:55], s95 offen offset:3072 lds\nv_add_u32 v75, 1, v75\nQmvva 204,36,148\ns_waitcnt vmcnt(19)\nds_write_b32 v73, v75\ns_waitcnt lgkmcnt(5)\nQmvva 236,0,124\nQda 120,47,35840\n"
"s_and_b32 s69, %19, 1\nQmvva 236,20,152\nQda 148,47,48128\ns_add_u32 m0, s91, 77824\ns_mov_b32 s95, 1028224\nQmvva 208,0,128\nbuffer_load_dwordx4 v62, s[52:55], s95 offen lds\nQmvva 208,20,156\n"
"s_lshl_b32 s68, s69, 6\nv_and_b32 v54, 255, v60\nQmvva 152,0,132\nv_add_u32 v54, s68, v54\nv_mul_u32_u24 v56, 336, v55\nQmvva 152,20,160\nv_add3_u32 v64, v54, v56, %10\nv_mul_u32_u24 v56, 2560, v55\n"
"Qmava 184,0,136\nQtv 0,50,36864\nv_add_u32 v70, v54, v56\nQmava 184,20,164\nds_read_b32 v66, v74\ns_mov_b32 s95, 1047616\nQmvva 80,4,124\nbuffer_load_dwordx4 v61, s[52:55], s95 offen offset:1024 lds\n"
"Qmvva 80,24,152\nQtv 2,50,38400\ns_lshl_b32 s68, s70, 4\nQmvva 240,4,128\nQtv 20,50,49152\nv_lshrrev_b32 v55, 6, v60\nQmvva 240,24,156\ns_waitcnt lgkmcnt(2)\n"
"v_cmp_gt_u32 vcc, v75, v66\nQmvva 184,4,132\ns_cbranch_vccnz .Lso29\n.Lro29:\nv_add_u32 v55, s68, v55\nQmvva 184,24,160\nQtv 22,50,50688\ns_add_u32 m0, s91, 98304\n"
"s_mov_b32 s94, 361472\nQmava 188,4,136\nbuffer_load_dwordx4 v60, s[48:51], s94 offen lds\nQmava 188,24,164\nQtv 4,50,37376\nv_accvgpr_read_b32 v96, a184\nQmvva 112,8,124\nQtv 6,50,38912\n"
"v_accvgpr_read_b32 v97, a185\nQmvva 112,28,152\nQtv 24,50,49664\nv_accvgpr_read_b32 v98, a186\nQmvva 84,8,128\nQtv 26,50,51200\nv_accvgpr_read_b32 v99, a187\nQmvva 84,28,156\n"
"s_xor_b32 s69, s69, 1\ns_lshl_b32 s68, s69, 8\nQmvva 216,8,132\nbuffer_load_dwordx4 v60, s[48:51], s94 offen offset:1024 lds\nQmvva 216,28,160\nv_and_b32 v54, 63, v60\nv_add_u32 v54, s68, v54\nQmava 192,8,136\n"
"Qtv 8,50,37888\nv_accvgpr_read_b32 v128, a188\nQmava 192,28,164\nQtv 10,50,39424\nv_accvgpr_read_b32 v129, a189\nQmvva 144,12,124\nQtv 28,50,50176\nv_accvgpr_read_b32 v130, a190\n"
"Qmvva 144,32,152\nQtv 30,50,51712\nv_accvgpr_read_b32 v131, a191\nQmvva 88,12,128\nbuffer_load_dwordx4 v60, s[48:51], s94 offen offset:2048 lds\nQmvva 88,32,156\nv_mul_u32_u24 v56, 336, v55\nv_add3_u32 v71, v54, v56, %10\n"
"Qmava 176,12,132\nv_mul_u32_u24 v56, 2560, v55\nv_add_u32 v72, v54, v56\nQmava 176,32,160\nQmava 196,12,136\nQtv 12,48,4096\nQmava 196,32,164\nQtv 14,48,5120\n"
"Qmvva 176,16,124\nQtv 32,48,16384\nQmvva 176,36,152\nbuffer_load_dwordx4 v60, s[48:51], s94 offen offset:3072 lds\nQmvva 120,16,128\nQtv 34,48,17408\nQmvva 120,36,156\nQmava 180,16,132\n"
"v_accvgpr_read_b32 v248, a176\nv_accvgpr_read_b32 v249, a177\nQmava 180,36,160\nv_accvgpr_read_b32 v250, a178\nv_accvgpr_read_b32 v251, a179\nQmava 200,16,136\nQtv 16,48,4608\nv_add_u32 v75, 1, v75\n"
"Qmava 200,36,164\ns_waitcnt vmcnt(17)\nds_write_b32 v73, v75\ns_add_u32 m0, s91, 102400\ns_mov_b32 s95, 1028288\ns_waitcnt lgkmcnt(10)\nQmvva 92,0,112\nbuffer_load_dwordx4 v61, s[52:55], s95 offen lds\n"
"Qmvva 92,20,140\nQtv 18,48,5632\nQmvva 252,0,116\nQtv 36,48,16896\nQmvva 252,20,144\nQtv 38,48,17920\nQmvva 228,0,120\nQda 124,45,0\n"
"s_mov_b32 s95, 1047744\nQmvva 228,20,148\nbuffer_load_dwordx4 v61, s[52:55], s95 offen offset:1024 lds\nQmvva 124,4,112\nQda 152,45,12288\nv_accvgpr_read_b32 v116, a180\nQmvva 124,24,140\nds_read_b32 v66, v74\n"
"v_accvgpr_read_b32 v117, a181\nQmvva 100,4,116\nQda 128,45,1024\nv_accvgpr_read_b32 v118, a182\nQmvva 100,24,144\nQda 156,45,13312\nv_accvgpr_read_b32 v119, a183\nQmvva 108,4,120\n"
"Qda 132,45,2048\nQmvva 108,24,148\ns_waitcnt lgkmcnt(3)\nv_cmp_gt_u32 vcc, v75, v66\nQmvva 156,8,112\ns_cbranch_vccnz .Lso30\n.Lro30:\nQmvva 156,28,140\n"
"Qda 160,45,14336\ns_add_u32 m0, s91, 132096\ns_mov_b32 s94, 372736\nQmvva 132,8,116\nbuffer_load_dwordx4 v60, s[48:51], s94 offen lds\nQmvva 132,28,144\nQda 136,45,3072\nQmvva 140,8,120\n"
"Qda 164,45,15360\nQmvva 140,28,148\ns_waitcnt lgkmcnt(9)\nQmvva 188,12,112\nbuffer_load_dwordx4 v60, s[48:51], s94 offen offset:1024 lds\nQmvva 188,32,140\nQmvva 164,12,116\nQmvva 164,32,144\n"
"Qmvva 172,12,120\nQmvva 172,32,148\nbuffer_load_dwordx4 v60, s[48:51], s94 offen offset:2048 lds\nQmvva 220,16,112\nQda 112,45,24576\nQmvva 220,36,140\nQda 140,45,36864\nQmvva 196,16,116\n"
"Qda 116,45,25600\nQmvva 196,36,144\nQda 144,45,37888\ns_mov_b32 s95, 1065984\nQmvva 204,16,120\nbuffer_load_dwordx4 v61, s[52:55], s95 offen offset:3072 lds\nv_add_u32 v75, 1, v75\nQmvva 204,36,148\n"
"s_waitcnt vmcnt(16)\nds_write_b32 v73, v75\ns_waitcnt lgkmcnt(5)\nQmvva 236,0,124\nbuffer_load_dword v78, v76, s[24:27], 0 offen sc1\nQmvva 236,20,152\nQda 120,45,26624\ns_add_u32 m0, s91, 136192\n"
"s_mov_b32 s95, 1069184\nQmvva 208,0,128\nbuffer_load_dwordx4 v62, s[52:55], s95 offen lds\nQmvva 208,20,156\nQda 148,45,38912\nQmvva 152,0,132\nQmvva 152,20,160\nQmvva 96,0,136\n"
"Qtv 0,48,27648\nQmvva 96,20,164\nds_read_b32 v66, v74\ns_mov_b32 s95, 1088576\nQmvva 80,4,124\nbuffer_load_dwordx4 v61, s[52:55], s95 offen offset:1024 lds\nQmvva 80,24,152\nQtv 2,48,29184\n"
"Qmvva 240,4,128\nQtv 20,48,39936\nQmvva 240,24,156\ns_waitcnt lgkmcnt(2)\nv_cmp_gt_u32 vcc, v75, v66\nQmvva 184,4,132\ns_cbranch_vccnz .Lso31\n.Lro31:\n"
"Qmvva 184,24,160\nQtv 22,48,41472\ns_add_u32 m0, s91, 0\ns_mov_b32 s94, 375808\nQmvva 128,4,136\nbuffer_load_dwordx4 v60, s[48:51], s94 offen lds\nQmvva 128,24,164\nQtv 4,48,28160\n"
"Qmvva 112,8,124\nQtv 6,48,29696\nQmvva 112,28,152\nQtv 24,48,40448\nQmvva 84,8,128\nQtv 26,48,41984\nQmvva 84,28,156\nQmvva 216,8,132\n"
"buffer_load_dwordx4 v60, s[48:51], s94 offen offset:1024 lds\nQmvva 216,28,160\nQmava 192,8,136\nQtv 8,48,28672\nQmava 192,28,164\nQtv 10,48,30208\nQmvva 144,12,124\nQtv 28,48,40960\n"
"Qmvva 144,32,152\nQtv 30,48,42496\nQmvva 88,12,128\nbuffer_load_dwordx4 v60, s[48:51], s94 offen offset:2048 lds\nQmvva 88,32,156\nQmvva 248,12,132\nQmvva 248,32,160\nQmava 196,12,136\n"
"Qtv 12,49,4096\nv_accvgpr_read_b32 v160, a192\nQmava 196,32,164\nQtv 14,49,5120\nv_accvgpr_read_b32 v161, a193\nQmvva 176,16,124\nQtv 32,49,16384\nv_accvgpr_read_b32 v162, a194\n"
"Qmvva 176,36,152\nbuffer_load_dwordx4 v60, s[48:51], s94 offen offset:3072 lds\ns_mov_b32 s31, 387072\nQmvva 120,16,128\nbuffer_load_dwordx4 a[208:211], v60, s[48:51], s31 offen offset:0 sc1\nQmvva 120,36,156\nQtv 34,49,17408\nv_accvgpr_read_b32 v163, a195\n"
"Qmvva 116,16,132\nQmvva 116,36,160\nQmava 200,16,136\nQtv 16,49,4608\nv_add_u32 v75, 1, v75\nQmava 200,36,164\ns_waitcnt vmcnt(18)\nds_write_b32 v73, v75\n"
"s_add_u32 m0, s91, 4096\ns_mov_b32 s95, 1069248\ns_waitcnt lgkmcnt(10)\nQmvva 92,0,112\nbuffer_load_dwordx4 v61, s[52:55], s95 offen lds\nQmvva 92,20,140\nQtv 18,49,5632\nv_accvgpr_read_b32 v192, a196\n"
"Qmvva 252,0,116\nQtv 36,49,16896\nv_accvgpr_read_b32 v193, a197\nQmvva 252,20,144\nQtv 38,49,17920\nv_accvgpr_read_b32 v194, a198\nQmvva 228,0,120\nbuffer_load_dwordx4 a[212:215], v60, s[48:51], s31 offen offset:1024 sc1\n"
"s_mov_b32 s95, 1088704\nQmvva 228,20,148\nbuffer_load_dwordx4 v61, s[52:55], s95 offen offset:1024 lds\nQmvva 124,4,112\nQda 124,46,0\nv_lshrrev_b32 v62, 9, v60\nv_mul_u32_u24 v62, 20160, v62\nv_add_u32 v62, v61, v62\n"
"Qmvva 124,24,140\nds_read_b32 v66, v74\nv_accvgpr_read_b32 v195, a199\nQmvva 100,4,116\nQda 152,46,12288\nv_accvgpr_read_b32 v224, a200\nQmvva 100,24,144\nQda 128,46,1024\n"
"v_accvgpr_read_b32 v225, a201\nQmvva 108,4,120\nQda 156,46,13312\nv_accvgpr_read_b32 v226, a202\nQmvva 108,24,148\ns_waitcnt lgkmcnt(3)\nv_cmp_gt_u32 vcc, v75, v66\nQmvva 156,8,112\n"
"s_cbranch_vccnz .Lso32\n.Lro32:\nv_accvgpr_read_b32 v227, a203\nQmvva 156,28,140\nQda 132,46,2048\nQmvva 132,8,116\nbuffer_load_dwordx4 a[216:219], v60, s[48:51], s31 offen offset:2048 sc1\ns_add_u32 m0, s96, 24576\n"
"s_mov_b32 s95, 1110016\nQmvva 132,28,144\nbuffer_load_dwordx4 v61, s[52:55], s95 offen lds\nQmvva 140,8,120\nQda 160,46,14336\nQmvva 140,28,148\nQda 136,46,3072\ns_waitcnt lgkmcnt(8)\n"
"Qmvva 188,12,112\nQda 164,46,15360\nQmvva 188,32,140\nQmvva 164,12,116\nQmvva 164,32,144\nQmvva 172,12,120\nQmvva 172,32,148\nQmvva 220,16,112\n"
"buffer_load_dwordx4 a[220:223], v60, s[48:51], s31 offen offset:3072 sc1\ns_mov_b32 s95, 1109120\nQmvva 220,36,140\nbuffer_load_dwordx4 v61, s[52:55], s95 offen offset:1024 lds\nQmvva 196,16,116\nQda 112,46,24576\nQmvva 196,36,144\nQda 140,46,36864\n"
"Qmvva 204,16,120\nQda 116,46,25600\nv_add_u32 v75, 1, v75\nQmvva 204,36,148\ns_waitcnt vmcnt(19)\nds_write_b32 v73, v75\ns_waitcnt lgkmcnt(4)\nQmvva 236,0,124\n"
"s_waitcnt vmcnt(14)\nv_sub_u32 v66, v78, v65\nv_cmp_gt_u32 vcc, 2, v66\ns_and_b64 vcc, vcc, s[28:29]\ns_cbranch_scc1 .Lf2_w\n.Lf2_r:\nQmvva 236,20,152\nQda 144,46,37888\n"
"Qmvva 208,0,128\nQda 120,46,26624\nQmvva 208,20,156\nQda 148,46,38912\ns_mov_b32 s31, 391168\nQmvva 152,0,132\nbuffer_load_dwordx4 a[224:227], v60, s[48:51], s31 offen offset:0 sc1\ns_mov_b32 s95, 1108224\n"
"Qmvva 152,20,160\nbuffer_load_dwordx4 v62, s[52:55], s95 offen offset:2048 lds\nQmvva 96,0,136\nQtv 0,49,27648\nQmvva 96,20,164\nds_read_b32 v66, v74\nQmvva 80,4,124\n.Lpg18:\n"
"s_cbranch_execz .Lpg22\nQtv 2,49,29184\nQmvva 80,24,152\nQtv 20,49,39936\nQmvva 240,4,128\nQtv 22,49,41472\nv_add_u32 v67, 5, v65\nQmvva 240,24,156\n"
"s_waitcnt lgkmcnt(3)\nv_cmp_gt_u32 vcc, v75, v66\nQmvva 184,4,132\ns_cbranch_vccnz .Lso33\n.Lro33:\nQmvva 184,24,160\nbuffer_store_dword v67, v77, s[24:27], 0 offen sc1\nQmvva 128,4,136\n"
"buffer_load_dwordx4 a[228:231], v60, s[48:51], s31 offen offset:1024 sc1\ns_mov_b32 s95, 1127488\nQmvva 128,24,164\nbuffer_load_dwordx4 v61, s[52:55], s95 offen offset:3072 lds\nQmvva 112,8,124\nQtv 4,49,28160\nQmvva 112,28,152\nQtv 6,49,29696\n"
"Qmvva 84,8,128\nQtv 24,49,40448\nQmvva 84,28,156\nQtv 26,49,41984\nQmvva 216,8,132\nQmvva 216,28,160\nQmvva 160,8,136\nQtv 8,49,28672\n"
"Qmvva 160,28,164\nQtv 10,49,30208\nQmvva 144,12,124\nbuffer_load_dwordx4 a[232:235], v60, s[48:51], s31 offen offset:2048 sc1\ns_add_u32 m0, s96, 28672\ns_mov_b32 s95, 1130688\nQmvva 144,32,152\nbuffer_load_dwordx4 v61, s[52:55], s95 offen lds\n"
"Qmvva 88,12,128\nQtv 28,49,40960\nQmvva 88,32,156\nQtv 30,49,42496\nQmvva 248,12,132\nQmvva 248,32,160\nQmvva 192,12,136\nQtv 12,50,4096\n"
"Qmvva 192,32,164\nQtv 14,50,5120\nQmvva 176,16,124\nQtv 32,50,16384\nQmvva 176,36,152\nQtv 34,50,17408\ns_add_u32 s31, s74, 0\nQmvva 120,16,128\n"
"buffer_load_dwordx4 a[236:239], v60, s[48:51], s31 offen offset:0 sc1\ns_add_u32 m0, s96, 45056\ns_add_u32 s95, s67, 4096\nQmvva 120,36,156\nbuffer_load_dwordx4 v61, s[52:55], s95 offen lds\nQmvva 116,16,132\nQmvva 116,36,160\nQmvva 224,16,136\n"
"Qtv 16,50,4608\nv_add_u32 v75, 1, v75\nQmvva 224,36,164\ns_waitcnt vmcnt(21)\nds_write_b32 v73, v75\ns_waitcnt lgkmcnt(10)\nQmvva 92,0,112\nQtv 18,50,5632\n"
"Qmvva 92,20,140\nQtv 36,50,16896\nQmvva 252,0,116\nQtv 38,50,17920\nQmvva 252,20,144\nQda 124,47,0\nQmvva 228,0,120\nbuffer_load_dwordx4 a[240:243], v60, s[48:51], s31 offen offset:1024 sc1\n"
"s_add_u32 s95, s67, 3200\nQmvva 228,20,148\nbuffer_load_dwordx4 v61, s[52:55], s95 offen offset:1024 lds\nQmvva 124,4,112\nQda 152,47,12288\nQmvva 124,24,140\nds_read_b32 v66, v74\nQmvva 100,4,116\n"
"Qda 128,47,1024\nQmvva 100,24,144\nQda 156,47,13312\nQmvva 108,4,120\nQda 132,47,2048\nQmvva 108,24,148\ns_waitcnt lgkmcnt(3)\nv_cmp_gt_u32 vcc, v75, v66\n"
"Qmvva 156,8,112\ns_cbranch_vccnz .Lso34\n.Lro34:\nQmvva 156,28,140\nQda 160,47,14336\nQmvva 132,8,116\nbuffer_load_dwordx4 a[244:247], v60, s[48:51], s31 offen offset:2048 sc1\ns_add_u32 s95, s67, 2304\n"
"Qmvva 132,28,144\nbuffer_load_dwordx4 v62, s[52:55], s95 offen offset:2048 lds\nQmvva 140,8,120\nQda 136,47,3072\nQmvva 140,28,148\nQda 164,47,15360\ns_waitcnt lgkmcnt(9)\nQmvva 188,12,112\n"
"Qmvva 188,32,140\nQmvva 164,12,116\nQmvva 164,32,144\nQmvva 172,12,120\nQmvva 172,32,148\nQmvva 220,16,112\nbuffer_load_dwordx4 a[248:251], v60, s[48:51], s31 offen offset:3072 sc1\ns_add_u32 s95, s67, 21568\n"
"Qmvva 220,36,140\nbuffer_load_dwordx4 v61, s[52:55], s95 offen offset:3072 lds\nQmvva 196,16,116\nQda 112,47,33792\nQmvva 196,36,144\nQda 140,47,46080\nQmvva 204,16,120\nQda 116,47,34816\n"
"v_add_u32 v75, 1, v75\nQmvva 204,36,148\ns_waitcnt vmcnt(19)\nds_write_b32 v73, v75\ns_waitcnt lgkmcnt(4)\nQmvva 236,0,124\nQda 144,47,47104\nQmvva 236,20,152\n"
"Qda 120,47,35840\nQmvva 208,0,128\nQda 148,47,48128\nQmvva 208,20,156\ns_add_u32 s31, s74, 4096\nQmvva 152,0,132\nbuffer_load_dwordx4 a[252:255], v60, s[48:51], s31 offen offset:0 sc1\ns_add_u32 m0, s96, 49152\n"
"s_add_u32 s95, s67, 24768\nQmvva 152,20,160\nbuffer_load_dwordx4 v61, s[52:55], s95 offen lds\nQmvva 96,0,136\nQtv 0,50,36864\nQmvva 96,20,164\nds_read_b32 v66, v74\nQmvva 80,4,124\n"
"Qtv 2,50,38400\nQmvva 80,24,152\nQtv 20,50,49152\nQmvva 240,4,128\nQtv 22,50,50688\nQmvva 240,24,156\ns_waitcnt lgkmcnt(3)\nv_cmp_gt_u32 vcc, v75, v66\n"
"Qmvva 184,4,132\ns_cbranch_vccnz .Lso35\n.Lro35:\nQmvva 184,24,160\nQmvva 128,4,136\nbuffer_load_dwordx4 a[168:171], v60, s[48:51], s31 offen offset:1024 sc1\ns_add_u32 m0, s96, 65536\ns_add_u32 s95, s72, 4096\n"
"Qmvva 128,24,164\nbuffer_load_dwordx4 v61, s[52:55], s95 offen lds\nQmvva 112,8,124\nQtv 4,50,37376\nQmvva 112,28,152\nQtv 6,50,38912\nQmvva 84,8,128\nQtv 24,50,49664\n"
"Qmvva 84,28,156\nQtv 26,50,51200\nQmvva 216,8,132\nQmvva 216,28,160\nQmvva 160,8,136\nQtv 8,50,37888\nQmvva 160,28,164\nQtv 10,50,39424\n"
"Qmvva 144,12,124\nbuffer_load_dwordx4 a[172:175], v60, s[48:51], s31 offen offset:2048 sc1\ns_add_u32 s95, s72, 3200\nQmvva 144,32,152\nbuffer_load_dwordx4 v61, s[52:55], s95 offen offset:1024 lds\nQmvva 88,12,128\nQtv 28,50,50176\nQmvva 88,32,156\n"
"Qtv 30,50,51712\nQmvva 248,12,132\nQmvva 248,32,160\nQmvva 192,12,136\nQtv 12,48,4096\nQmvva 192,32,164\nQtv 14,48,5120\nQmvva 176,16,124\n"
"Qtv 32,48,16384\nQmvva 176,36,152\nQtv 34,48,17408\ns_add_u32 s31, s75, 0\nQmvva 120,16,128\nbuffer_load_dwordx4 a[0:3], v60, s[48:51], s31 offen offset:0 sc1\ns_add_u32 s95, s72, 2304\nQmvva 120,36,156\n"
"buffer_load_dwordx4 v62, s[52:55], s95 offen offset:2048 lds\nQmvva 116,16,132\nQmvva 116,36,160\nQmvva 224,16,136\nQtv 16,48,4608\nv_add_u32 v75, 1, v75\nQmvva 224,36,164\ns_waitcnt vmcnt(16)\n"
"ds_write_b32 v73, v75\ns_waitcnt lgkmcnt(10)\nQmvva 92,0,112\nQtv 18,48,5632\nQmvva 92,20,140\nQtv 36,48,16896\nQmvva 252,0,116\nQtv 38,48,17920\n"
"Qmvva 252,20,144\nQda 124,45,0\nQmvva 228,0,120\nbuffer_load_dwordx4 a[4:7], v60, s[48:51], s31 offen offset:1024 sc1\ns_add_u32 s95, s72, 21568\nQmvva 228,20,148\nbuffer_load_dwordx4 v61, s[52:55], s95 offen offset:3072 lds\nQmvva 124,4,112\n"
"Qda 152,45,12288\nQmvva 124,24,140\nds_read_b32 v66, v74\nQmvva 100,4,116\nQda 128,45,1024\nQmvva 100,24,144\nQda 156,45,13312\nQmvva 108,4,120\n"
"Qda 132,45,2048\nQmvva 108,24,148\ns_waitcnt lgkmcnt(3)\nv_cmp_gt_u32 vcc, v75, v66\nQmvva 156,8,112\ns_cbranch_vccnz .Lso36\n.Lro36:\nQmvva 156,28,140\n"
"s_waitcnt vmcnt(32)\nds_write_b128 v52, a[208:211] offset:0\nQmvva 132,8,116\nQmvva 132,28,144\nbuffer_load_dwordx4 a[8:11], v60, s[48:51], s31 offen offset:2048 sc1\ns_add_u32 m0, s96, 69632\ns_add_u32 s95, s72, 24768\nQmvva 140,8,120\n"
"buffer_load_dwordx4 v61, s[52:55], s95 offen lds\nQmvva 140,28,148\ns_waitcnt vmcnt(32)\nds_write_b128 v52, a[212:215] offset:4096\ns_waitcnt lgkmcnt(8)\nQmvva 188,12,112\nQmvva 188,32,140\nQda 160,45,14336\n"
"Qmvva 164,12,116\nQda 136,45,3072\nQmvva 164,32,144\nQda 164,45,15360\nQmvva 172,12,120\nQmvva 172,32,148\nQmvva 220,16,112\nbuffer_load_dwordx4 a[12:15], v60, s[48:51], s31 offen offset:3072 sc1\n"
"Qmvva 220,36,140\ns_add_u32 m0, s96, 86016\ns_add_u32 s95, s73, 4096\nQmvva 196,16,116\nbuffer_load_dwordx4 v61, s[52:55], s95 offen lds\nQmvva 196,36,144\ns_waitcnt vmcnt(32)\nds_write_b128 v52, a[216:219] offset:8192\n"
"Qmvva 204,16,120\nv_add_u32 v75, 1, v75\nQmvva 204,36,148\ns_waitcnt vmcnt(12)\nds_write_b32 v73, v75\ns_waitcnt lgkmcnt(2)\nQmvva 236,0,124\nbuffer_load_dword v78, v76, s[24:27], 0 offen sc1\n"
"Qmvva 236,20,152\nQta 84,40,20480\nQta 86,40,23040\nQmvva 208,0,128\nQta 176,42,20480\nQmvva 208,20,156\nQta 178,42,23040\ns_add_u32 s31, s75, 4096\n"
"Qmvva 152,0,132\nbuffer_load_dwordx4 a[16:19], v60, s[48:51], s31 offen offset:0 sc1\nQmvva 152,20,160\nQta 88,40,20992\ns_add_u32 s95, s73, 3200\nQmvva 96,0,136\nbuffer_load_dwordx4 v61, s[52:55], s95 offen offset:1024 lds\nQmvva 96,20,164\n"
"ds_read_b32 v66, v74\nQmvva 80,4,124\ns_waitcnt vmcnt(33)\nds_write_b128 v52, a[220:223] offset:12288\nQmvva 80,24,152\nQmvva 240,4,128\nQta 90,40,23552\nQmvva 240,24,156\n"
"s_waitcnt lgkmcnt(2)\nv_cmp_gt_u32 vcc, v75, v66\nQmvva 184,4,132\ns_cbranch_vccnz .Lso37\n.Lro37:\nQmvva 184,24,160\nQta 180,42,20992\nQmvva 128,4,136\n"
"buffer_load_dwordx4 a[20:23], v60, s[48:51], s31 offen offset:1024 sc1\nQmvva 128,24,164\nQta 182,42,23552\ns_add_u32 s95, s73, 2304\nQmvva 112,8,124\nbuffer_load_dwordx4 v62, s[52:55], s95 offen offset:2048 lds\nQmvva 112,28,152\ns_waitcnt vmcnt(33)\n"
"ds_write_b128 v52, a[224:227] offset:16384\nQmvva 84,8,128\nQmvva 84,28,156\nQta 92,40,21504\nQmvva 216,8,132\nQta 94,40,24064\nQmvva 216,28,160\nQta 184,42,21504\n"
"Qta 186,42,24064\nQmvva 160,8,136\nQta 96,40,22016\nQmvva 160,28,164\nQta 98,40,24576\nQmvva 144,12,124\nbuffer_load_dwordx4 a[24:27], v60, s[48:51], s31 offen offset:2048 sc1\nQmvva 144,32,152\n"
"Qta 188,42,22016\ns_add_u32 s95, s73, 21568\nQmvva 88,12,128\nbuffer_load_dwordx4 v61, s[52:55], s95 offen offset:3072 lds\nQmvva 88,32,156\ns_waitcnt vmcnt(32)\nds_write_b128 v52, a[228:231] offset:20480\nQmvva 248,12,132\n"
"Qmvva 248,32,160\nQta 190,42,24576\nQmvva 192,12,136\nQta 100,40,22528\nQmvva 192,32,164\nQda 48,53,0\nQmvva 176,16,124\nQta 102,40,25088\n"
"Qmvva 176,36,152\nQta 192,42,22528\ns_add_u32 s31, s77, 0\nQmvva 120,16,128\nbuffer_load_dwordx4 a[28:31], v60, s[48:51], s31 offen offset:0 sc1\nQmvva 120,36,156\nQta 194,42,25088\ns_add_u32 m0, s96, 90112\n"
"s_add_u32 s95, s73, 24768\nQmvva 116,16,132\nbuffer_load_dwordx4 v61, s[52:55], s95 offen lds\nQmvva 116,36,160\ns_waitcnt vmcnt(32)\nds_write_b128 v52, a[232:235] offset:25600\nQmvva 224,16,136\nv_add_u32 v75, 1, v75\n"
"Qmvva 224,36,164\nds_write_b32 v73, v75\ns_waitcnt lgkmcnt(2)\nQmvaa 92,84,208\nQtv 0,40,40960\nQmvaa 92,176,48\nQda 52,53,4096\nQmvaa 124,88,208\n"
"Qtv 2,40,43520\nQmvaa 124,180,48\nQtv 20,42,40960\nQmvaa 156,92,208\nbuffer_load_dwordx4 a[32:35], v60, s[48:51], s31 offen offset:1024 sc1\nQmvaa 156,184,48\nQtv 22,42,43520\ns_add_u32 m0, s96, 4096\n"
"s_add_u32 s95, s67, 45056\nQmvaa 188,96,208\nbuffer_load_dwordx4 v61, s[52:55], s95 offen lds\nQmvaa 188,188,48\nds_read_b32 v66, v74\nQmvaa 220,100,208\ns_waitcnt vmcnt(32)\nds_write_b128 v52, a[236:239] offset:29696\n"
"Qmvaa 220,192,48\ns_waitcnt lgkmcnt(5)\nQmvaa 252,84,212\nQtv 4,40,41472\nQmvaa 252,176,52\ns_waitcnt lgkmcnt(2)\nv_cmp_gt_u32 vcc, v75, v66\nQmvaa 100,88,212\n"
"s_cbranch_vccnz .Lso38\n.Lro38:\nQmvaa 100,180,52\nQda 56,53,8192\nQmvaa 132,92,212\nbuffer_load_dwordx4 a[36:39], v60, s[48:51], s31 offen offset:2048 sc1\nQmvaa 132,184,52\nQtv 6,40,44032\n"
"s_add_u32 s95, s67, 44160\nQmvaa 164,96,212\nbuffer_load_dwordx4 v61, s[52:55], s95 offen offset:1024 lds\nQmvaa 164,188,52\ns_waitcnt vmcnt(32)\nds_write_b128 v52, a[240:243] offset:33792\nQmvaa 196,100,212\nQmvaa 196,192,52\n"
"Qtv 24,42,41472\ns_waitcnt lgkmcnt(3)\nQmvaa 228,84,216\nQmvaa 228,176,56\nQda 60,53,12288\nQmvaa 108,88,216\nQtv 26,42,44032\nQmvaa 108,180,56\n"
"Qtv 8,40,41984\nQmvaa 140,92,216\nbuffer_load_dwordx4 a[40:43], v60, s[48:51], s31 offen offset:3072 sc1\nQmvaa 140,184,56\nQtv 10,40,44544\ns_add_u32 s95, s67, 43264\nQmvaa 172,96,216\nbuffer_load_dwordx4 v62, s[52:55], s95 offen offset:2048 lds\n"
"Qmvaa 172,188,56\ns_waitcnt vmcnt(32)\nds_write_b128 v52, a[244:247] offset:37888\nQmvaa 204,100,216\nv_add_u32 v75, 1, v75\nQmvaa 204,192,56\ns_waitcnt vmcnt(17)\nds_write_b32 v73, v75\n"
"s_waitcnt lgkmcnt(5)\nQmvaa 236,84,220\ns_waitcnt vmcnt(14)\nv_sub_u32 v66, v78, v65\nv_cmp_gt_u32 vcc, 3, v66\ns_and_b64 vcc, vcc, s[28:29]\ns_cbranch_scc1 .Lf3_w\n.Lf3_r:\n"
"Qmvaa 236,176,60\nQda 48,53,16384\nQmvaa 80,88,220\nQtv 28,42,41984\nQmvaa 80,180,60\nQtv 30,42,44544\ns_add_u32 s31, s77, 4096\nQmvaa 112,92,220\n"
"buffer_load_dwordx4 a[44:47], v60, s[48:51], s31 offen offset:0 sc1\nQmvaa 112,184,60\nQtv 12,40,42496\ns_add_u32 s95, s67, 62528\nQmvaa 144,96,220\nbuffer_load_dwordx4 v61, s[52:55], s95 offen offset:3072 lds\nQmvaa 144,188,60\nds_read_b32 v66, v74\n"
"Qmvaa 176,100,220\ns_waitcnt vmcnt(32)\nds_write_b128 v52, a[248:251] offset:41984\nQmvaa 176,192,60\ns_waitcnt lgkmcnt(5)\nQmvaa 208,84,224\nQtv 14,40,45056\nQmvaa 208,176,48\n"
"s_waitcnt lgkmcnt(2)\nv_cmp_gt_u32 vcc, v75, v66\nQmvaa 240,88,224\ns_cbranch_vccnz .Lso39\n.Lro39:\nQmvaa 240,180,48\nQda 52,53,20480\nQmvaa 84,92,224\n"
"buffer_load_dwordx4 a[208:211], v60, s[48:51], s31 offen offset:1024 sc1\nQmvaa 84,184,48\ns_add_u32 m0, s96, 8192\ns_add_u32 s95, s67, 65728\nQmvaa 88,96,224\nbuffer_load_dwordx4 v61, s[52:55], s95 offen lds\nQmvaa 88,188,48\ns_waitcnt vmcnt(32)\n"
"ds_write_b128 v52, a[252:255] offset:46080\nQmvaa 120,100,224\nQmvaa 120,192,48\nQtv 32,42,42496\ns_waitcnt lgkmcnt(2)\nQmvaa 152,84,228\nQtv 34,42,45056\nQmvaa 152,176,52\n"
"Qda 56,53,25600\nQmvaa 184,88,228\nQtv 16,40,43008\nQmvaa 184,180,52\nQtv 18,40,45568\nQmvaa 216,92,228\nbuffer_load_dwordx4 a[212:215], v60, s[48:51], s31 offen offset:2048 sc1\nQmvaa 216,184,52\n"
"Qtv 36,42,43008\ns_add_u32 m0, s96, 24576\ns_add_u32 s95, s72, 45056\nQmvaa 248,96,228\nbuffer_load_dwordx4 v61, s[52:55], s95 offen lds\nQmvaa 248,188,52\ns_waitcnt vmcnt(32)\nds_write_b128 v52, a[168:171] offset:50176\n"
"Qmvaa 116,100,228\nQmvaa 116,192,52\nQtv 38,42,45568\ns_waitcnt lgkmcnt(5)\nQmvaa 96,84,232\nQmvaa 96,176,56\nQda 60,53,29696\nQmvaa 128,88,232\n"
"Qta 112,41,0\nQmvaa 128,180,56\nQta 114,41,2560\ns_add_u32 s31, s74, 14336\nQmvaa 160,92,232\nbuffer_load_dwordx4 a[216:219], v60, s[48:51], s31 offen offset:0 sc1\nQmvaa 160,184,56\nQta 132,43,0\n"
"s_add_u32 s95, s72, 44160\nQmvaa 192,96,232\nbuffer_load_dwordx4 v61, s[52:55], s95 offen offset:1024 lds\nQmvaa 192,188,56\ns_waitcnt vmcnt(32)\nds_write_b128 v52, a[172:175] offset:0\nQmvaa 224,100,232\nv_add_u32 v75, 1, v75\n"
"Qmvaa 224,192,56\nds_write_b32 v73, v75\ns_waitcnt lgkmcnt(5)\nQmvva 92,0,236\nQmvva 92,20,60\nQda 48,53,33792\nQmvva 124,4,236\nQta 134,43,2560\n"
"Qmvva 124,24,60\nQta 116,41,512\nQmvva 156,8,236\n.Lpg19:\ns_cbranch_execz .Lpg23\nbuffer_load_dwordx4 a[220:223], v60, s[48:51], s31 offen offset:1024 sc1\nQmvva 156,28,60\nQta 118,41,3072\n"
"s_add_u32 s95, s72, 43264\nQmvva 188,12,236\nbuffer_load_dwordx4 v62, s[52:55], s95 offen offset:2048 lds\nQmvva 188,32,60\nds_read_b32 v66, v74\nQmvva 220,16,236\ns_waitcnt vmcnt(32)\nds_write_b128 v52, a[0:3] offset:4096\n"
"Qmvva 220,36,60\ns_waitcnt lgkmcnt(5)\nQmvva 252,0,240\nQmvva 252,20,48\ns_waitcnt lgkmcnt(1)\nv_cmp_gt_u32 vcc, v75, v66\nQmvva 100,4,240\ns_cbranch_vccnz .Lso40\n"
".Lro40:\nQmvva 100,24,48\nQda 52,53,37888\nQmvva 132,8,240\nbuffer_load_dwordx4 a[224:227], v60, s[48:51], s31 offen offset:2048 sc1\nQmvva 132,28,48\nQta 136,43,512\ns_add_u32 s95, s72, 62528\n"
"Qmvva 164,12,240\nbuffer_load_dwordx4 v61, s[52:55], s95 offen offset:3072 lds\nQmvva 164,32,48\ns_waitcnt vmcnt(32)\nds_write_b128 v52, a[4:7] offset:8192\nQmvva 196,16,240\nQmvva 196,36,48\nQta 138,43,3072\n"
"s_waitcnt lgkmcnt(3)\nQmvva 228,0,244\nQta 120,41,1024\nQmvva 228,20,52\nQda 56,53,41984\nQmvva 108,4,244\nQmvva 108,24,52\nQta 122,41,3584\n"
"Qmvva 140,8,244\nbuffer_load_dwordx4 a[228:231], v60, s[48:51], s31 offen offset:3072 sc1\nQmvva 140,28,52\nQta 140,43,1024\ns_add_u32 m0, s96, 28672\ns_add_u32 s95, s72, 65728\nQmvva 172,12,244\nbuffer_load_dwordx4 v61, s[52:55], s95 offen lds\n"
"Qmvva 172,32,52\ns_waitcnt vmcnt(32)\nds_write_b128 v52, a[8:11] offset:12288\nQmvva 204,16,244\nv_add_u32 v75, 1, v75\nQmvva 204,36,52\ns_waitcnt vmcnt(20)\nds_write_b32 v73, v75\n"
"s_waitcnt lgkmcnt(4)\nQmvva 236,0,248\nQta 142,43,3584\nQmvva 236,20,56\nQda 60,53,46080\nQmvva 80,4,248\nQmvva 80,24,56\nQta 124,41,1536\n"
"s_add_u32 s31, s74, 18432\nQmvva 112,8,248\nbuffer_load_dwordx4 a[232:235], v60, s[48:51], s31 offen offset:0 sc1\nQmvva 112,28,56\nQta 126,41,4096\ns_add_u32 m0, s96, 45056\ns_add_u32 s95, s73, 45056\nQmvva 144,12,248\n"
"buffer_load_dwordx4 v61, s[52:55], s95 offen lds\nQmvva 144,32,56\nds_read_b32 v66, v74\nQmvva 176,16,248\ns_waitcnt vmcnt(32)\nds_write_b128 v52, a[12:15] offset:16384\nQmvva 176,36,56\ns_waitcnt lgkmcnt(4)\n"
"Qmvva 208,0,252\nQta 144,43,1536\nQmvva 208,20,60\ns_waitcnt lgkmcnt(2)\nv_cmp_gt_u32 vcc, v75, v66\nQmvva 240,4,252\ns_cbranch_vccnz .Lso41\n.Lro41:\n"
"Qmvva 240,24,60\nQda 48,53,50176\nQmvva 84,8,252\nbuffer_load_dwordx4 a[236:239], v60, s[48:51], s31 offen offset:1024 sc1\nQmvva 84,28,60\ns_add_u32 s95, s73, 44160\nQmvva 88,12,252\nbuffer_load_dwordx4 v61, s[52:55], s95 offen offset:1024 lds\n"
"Qmvva 88,32,60\ns_waitcnt vmcnt(31)\nds_write_b128 v52, a[16:19] offset:20480\nQmvva 120,16,252\nQmvva 120,36,60\nQta 146,43,4096\ns_waitcnt lgkmcnt(2)\nQmvva 152,0,168\n"
"Qta 128,41,2048\nQmvva 152,20,48\nQda 52,53,0\nQmvva 184,4,168\nQta 130,41,4608\nQmvva 184,24,48\nQmvva 216,8,168\nbuffer_load_dwordx4 a[240:243], v60, s[48:51], s31 offen offset:2048 sc1\n"
"Qmvva 216,28,48\nQta 148,43,2048\ns_add_u32 s95, s73, 43264\nQmvva 248,12,168\nbuffer_load_dwordx4 v62, s[52:55], s95 offen offset:2048 lds\nQmvva 248,32,48\ns_waitcnt vmcnt(31)\nds_write_b128 v52, a[20:23] offset:25600\n"
"Qmvva 116,16,168\nQmvva 116,36,48\nQta 150,43,4608\ns_waitcnt lgkmcnt(4)\nQmvva 96,0,172\nQmvva 96,20,52\nQda 56,53,4096\nQmvva 128,4,172\n"
"Qta 84,41,20480\nQmvva 128,24,52\nQta 86,41,23040\ns_add_u32 s31, s75, 14336\nQmvva 160,8,172\nbuffer_load_dwordx4 a[244:247], v60, s[48:51], s31 offen offset:0 sc1\nQmvva 160,28,52\nQta 176,43,20480\n"
"s_add_u32 s95, s73, 62528\nQmvva 192,12,172\nbuffer_load_dwordx4 v61, s[52:55], s95 offen offset:3072 lds\nQmvva 192,32,52\ns_waitcnt vmcnt(31)\nds_write_b128 v52, a[24:27] offset:29696\nQmvva 224,16,172\nv_add_u32 v75, 1, v75\n"
"Qmvva 224,36,52\nds_write_b32 v73, v75\ns_waitcnt lgkmcnt(5)\nQmvaa 92,112,0\nQta 178,43,23040\nQmvaa 92,132,56\nQda 60,53,8192\nQmvaa 124,116,0\n"
"Qmvaa 124,136,56\nQta 88,41,20992\nQmvaa 156,120,0\nbuffer_load_dwordx4 a[248:251], v60, s[48:51], s31 offen offset:1024 sc1\nQmvaa 156,140,56\nQta 90,41,23552\ns_add_u32 m0, s96, 49152\ns_add_u32 s95, s73, 65728\n"
"Qmvaa 188,124,0\nbuffer_load_dwordx4 v61, s[52:55], s95 offen lds\nQmvaa 188,144,56\nds_read_b32 v66, v74\nQmvaa 220,128,0\ns_waitcnt vmcnt(31)\nds_write_b128 v52, a[28:31] offset:33792\nQmvaa 220,148,56\n"
"s_waitcnt lgkmcnt(4)\nQmvaa 252,112,4\nQta 180,43,20992\nQmvaa 252,132,60\ns_waitcnt lgkmcnt(2)\nv_cmp_gt_u32 vcc, v75, v66\nQmvaa 100,116,4\ns_cbranch_vccnz .Lso42\n"
".Lro42:\nQmvaa 100,136,60\nQda 48,53,12288\nQmvaa 132,120,4\nbuffer_load_dwordx4 a[252:255], v60, s[48:51], s31 offen offset:2048 sc1\nQmvaa 132,140,60\ns_add_u32 m0, s96, 65536\ns_add_u32 s95, s67, 86016\n"
"Qmvaa 164,124,4\nbuffer_load_dwordx4 v61, s[52:55], s95 offen lds\nQmvaa 164,144,60\ns_waitcnt vmcnt(31)\nds_write_b128 v52, a[32:35] offset:37888\nQmvaa 196,128,4\nQmvaa 196,148,60\nQta 182,43,23552\n"
"s_waitcnt lgkmcnt(2)\nQmvaa 228,112,8\nQta 92,41,21504\nQmvaa 228,132,48\nQda 52,53,16384\nQmvaa 108,116,8\nQta 94,41,24064\nQmvaa 108,136,48\n"
"Qmvaa 140,120,8\nbuffer_load_dwordx4 a[168:171], v60, s[48:51], s31 offen offset:3072 sc1\nQmvaa 140,140,48\nQta 184,43,21504\ns_add_u32 s95, s67, 85120\nQmvaa 172,124,8\nbuffer_load_dwordx4 v61, s[52:55], s95 offen offset:1024 lds\nQmvaa 172,144,48\n"
"s_waitcnt vmcnt(31)\nds_write_b128 v52, a[36:39] offset:41984\nQmvaa 204,128,8\nv_add_u32 v75, 1, v75\nQmvaa 204,148,48\ns_waitcnt vmcnt(24)\nds_write_b32 v73, v75\ns_waitcnt lgkmcnt(4)\n"
"Qmvaa 236,112,12\nbuffer_load_dword v78, v76, s[24:27], 0 offen sc1\nQmvaa 236,132,52\nQda 56,53,20480\nQmvaa 80,116,12\nQta 186,43,24064\nQmvaa 80,136,52\nQta 96,41,22016\n"
"s_add_u32 s31, s75, 18432\nQmvaa 112,120,12\nbuffer_load_dwordx4 a[172:175], v60, s[48:51], s31 offen offset:0 sc1\nQmvaa 112,140,52\nQta 98,41,24576\ns_add_u32 s95, s67, 84224\nQmvaa 144,124,12\nbuffer_load_dwordx4 v62, s[52:55], s95 offen offset:2048 lds\n"
"Qmvaa 144,144,52\nds_read_b32 v66, v74\nQmvaa 176,128,12\ns_waitcnt vmcnt(32)\nds_write_b128 v52, a[40:43] offset:46080\nQmvaa 176,148,52\ns_waitcnt lgkmcnt(5)\nQmvaa 208,112,16\n"
"Qmvaa 208,132,56\ns_waitcnt lgkmcnt(1)\nv_cmp_gt_u32 vcc, v75, v66\nQmvaa 240,116,16\ns_cbranch_vccnz .Lso43\n.Lro43:\nQmvaa 240,136,56\nQda 60,53,25600\n"
"Qmvaa 84,120,16\nbuffer_load_dwordx4 a[0:3], v60, s[48:51], s31 offen offset:1024 sc1\nQmvaa 84,140,56\nQta 188,43,22016\ns_add_u32 s95, s67, 103488\nQmvaa 88,124,16\nbuffer_load_dwordx4 v61, s[52:55], s95 offen offset:3072 lds\nQmvaa 88,144,56\n"
"s_waitcnt vmcnt(32)\nds_write_b128 v52, a[44:47] offset:50176\nQmvaa 120,128,16\nQmvaa 120,148,56\nQta 190,43,24576\ns_waitcnt lgkmcnt(3)\nQmvaa 152,112,20\nQta 100,41,22528\n"
"Qmvaa 152,132,60\nQda 48,53,29696\nQmvaa 184,116,20\nQmvaa 184,136,60\nQta 102,41,25088\nQmvaa 216,120,20\nbuffer_load_dwordx4 a[4:7], v60, s[48:51], s31 offen offset:2048 sc1\nQmvaa 216,140,60\n"
"Qta 192,43,22528\ns_add_u32 m0, s96, 69632\ns_add_u32 s95, s67, 106688\nQmvaa 248,124,20\nbuffer_load_dwordx4 v61, s[52:55], s95 offen lds\nQmvaa 248,144,60\ns_waitcnt vmcnt(32)\nds_write_b128 v52, a[208:211] offset:0\n"
"Qmvaa 116,128,20\nQmvaa 116,148,60\nQta 194,43,25088\ns_waitcnt lgkmcnt(4)\nQmvaa 96,112,24\nQmvaa 96,132,48\nQda 52,53,33792\nQmvaa 128,116,24\n"
"Qtv 0,40,0\nQmvaa 128,136,48\nQtv 2,40,2560\ns_add_u32 s31, s77, 14336\nQmvaa 160,120,24\nbuffer_load_dwordx4 a[8:11], v60, s[48:51], s31 offen offset:0 sc1\nQmvaa 160,140,48\nQtv 20,42,0\n"
"Qmvaa 192,124,24\nQmvaa 192,144,48\ns_waitcnt vmcnt(31)\nds_write_b128 v52, a[212:215] offset:4096\nQmvaa 224,128,24\nv_add_u32 v75, 1, v75\nQmvaa 224,148,48\nds_write_b32 v73, v75\n"
"s_waitcnt lgkmcnt(5)\nQmvaa 92,84,28\nQtv 22,42,2560\nQmvaa 92,176,52\nQda 56,53,37888\nQmvaa 124,88,28\nQtv 4,40,512\nQmvaa 124,180,52\n"
"Qmvaa 156,92,28\nbuffer_load_dwordx4 a[12:15], v60, s[48:51], s31 offen offset:1024 sc1\nQmvaa 156,184,52\nQtv 6,40,3072\nQmvaa 188,96,28\nQtv 24,42,512\nQmvaa 188,188,52\nds_read_b32 v66, v74\n"
"Qmvaa 220,100,28\ns_waitcnt vmcnt(30)\nds_write_b128 v52, a[216:219] offset:8192\nQmvaa 220,192,52\ns_waitcnt lgkmcnt(5)\nQmvaa 252,84,32\nQtv 26,42,3072\nQmvaa 252,176,56\n"
"s_waitcnt lgkmcnt(2)\nv_cmp_gt_u32 vcc, v75, v66\nQmvaa 100,88,32\ns_cbranch_vccnz .Lso44\n.Lro44:\nQmvaa 100,180,56\nQda 60,53,41984\nQmvaa 132,92,32\n"
"buffer_load_dwordx4 a[16:19], v60, s[48:51], s31 offen offset:2048 sc1\ns_add_u32 m0, s96, 86016\ns_add_u32 s95, s72, 86016\nQmvaa 132,184,56\nbuffer_load_dwordx4 v61, s[52:55], s95 offen lds\nQmvaa 164,96,32\nQmvaa 164,188,56\ns_waitcnt vmcnt(30)\n"
"ds_write_b128 v52, a[220:223] offset:12288\nQmvaa 196,100,32\nQmvaa 196,192,56\nQtv 8,40,1024\ns_waitcnt lgkmcnt(2)\nQmvaa 228,84,36\nQtv 10,40,3584\nQmvaa 228,176,60\n"
"Qda 48,53,46080\nQmvaa 108,88,36\nQmvaa 108,180,60\nQtv 28,42,1024\nQmvaa 140,92,36\nbuffer_load_dwordx4 a[20:23], v60, s[48:51], s31 offen offset:3072 sc1\ns_add_u32 s95, s72, 85120\nQmvaa 140,184,60\n"
"buffer_load_dwordx4 v61, s[52:55], s95 offen offset:1024 lds\nQmvaa 172,96,36\nQtv 30,42,3584\nQmvaa 172,188,60\ns_waitcnt vmcnt(30)\nds_write_b128 v52, a[224:227] offset:16384\nQmvaa 204,100,36\nv_add_u32 v75, 1, v75\n"
"Qmvaa 204,192,60\ns_waitcnt vmcnt(27)\nds_write_b32 v73, v75\ns_waitcnt lgkmcnt(4)\nQmvaa 236,84,40\ns_waitcnt vmcnt(12)\nv_sub_u32 v66, v78, v65\nv_cmp_gt_u32 vcc, 4, v66\n"
"s_and_b64 vcc, vcc, s[28:29]\ns_cbranch_scc1 .Lf4_w\n.Lf4_r:\nQmvaa 236,176,48\nQda 52,53,50176\nQmvaa 80,88,40\nQtv 12,40,1536\nQmvaa 80,180,48\n"
"s_add_u32 s31, s77, 18432\nQmvaa 112,92,40\nbuffer_load_dwordx4 a[24:27], v60, s[48:51], s31 offen offset:0 sc1\ns_add_u32 s95, s72, 84224\nQmvaa 112,184,48\nbuffer_load_dwordx4 v62, s[52:55], s95 offen offset:2048 lds\nQmvaa 144,96,40\nQtv 14,40,4096\n"
"Qmvaa 144,188,48\nds_read_b32 v66, v74\nQmvaa 176,100,40\ns_waitcnt vmcnt(30)\nds_write_b128 v52, a[228:231] offset:20480\nQmvaa 176,192,48\ns_waitcnt lgkmcnt(4)\nQmvaa 208,84,44\n"
"Qtv 32,42,1536\nQmvaa 208,176,52\ns_waitcnt lgkmcnt(2)\nv_cmp_gt_u32 vcc, v75, v66\nQmvaa 240,88,44\ns_cbranch_vccnz .Lso45\n.Lro45:\nQmvaa 240,180,52\n"
"Qda 56,53,0\nQmvaa 84,92,44\nbuffer_load_dwordx4 a[28:31], v60, s[48:51], s31 offen offset:1024 sc1\ns_add_u32 s95, s72, 103488\nQmvaa 84,184,52\nbuffer_load_dwordx4 v61, s[52:55], s95 offen offset:3072 lds\nQmvaa 88,96,44\nQmvaa 88,188,52\n"
"s_waitcnt vmcnt(30)\nds_write_b128 v52, a[232:235] offset:25600\nQmvaa 120,100,44\nQmvaa 120,192,52\nQtv 34,42,4096\ns_waitcnt lgkmcnt(2)\nQmvaa 152,84,208\nQtv 16,40,2048\n"
"Qmvaa 152,176,56\nQda 60,53,4096\nQmvaa 184,88,208\nQtv 18,40,4608\nQmvaa 184,180,56\nQmvaa 216,92,208\nbuffer_load_dwordx4 a[32:35], v60, s[48:51], s31 offen offset:2048 sc1\ns_add_u32 m0, s96, 90112\n"
"s_add_u32 s95, s72, 106688\nQmvaa 216,184,56\nbuffer_load_dwordx4 v61, s[52:55], s95 offen lds\nQmvaa 248,96,208\nQtv 36,42,2048\nQmvaa 248,188,56\ns_waitcnt vmcnt(30)\nds_write_b128 v52, a[236:239] offset:29696\n"
"Qmvaa 116,100,208\nQmvaa 116,192,56\nQtv 38,42,4608\ns_waitcnt lgkmcnt(4)\nQmvaa 96,84,212\nQmvaa 96,176,60\nQda 48,53,8192\nQmvaa 128,88,212\n"
"Qta 112,40,20480\nQmvaa 128,180,60\nQta 114,40,23040\ns_add_u32 s31, s74, 28672\nQmvaa 160,92,212\nbuffer_load_dwordx4 a[36:39], v60, s[48:51], s31 offen offset:0 sc1\nQmvaa 160,184,60\nQta 132,42,20480\n"
"Qmvaa 192,96,212\nQmvaa 192,188,60\ns_waitcnt vmcnt(29)\nds_write_b128 v52, a[240:243] offset:33792\nQmvaa 224,100,212\nv_add_u32 v75, 1, v75\nQmvaa 224,192,60\nds_write_b32 v73, v75\n"
"s_waitcnt lgkmcnt(5)\nQmvva 92,0,216\nbuffer_load_dword v78, v76, s[24:27], 0 offen sc1\nQmvva 92,20,48\nQda 52,53,12288\nQmvva 124,4,216\nQta 134,42,23040\nQmvva 124,24,48\n"
"Qta 116,40,20992\nQmvva 156,8,216\nbuffer_load_dwordx4 a[40:43], v60, s[48:51], s31 offen offset:1024 sc1\nQmvva 156,28,48\nQmvva 188,12,216\nQta 118,40,23552\nQmvva 188,32,48\nds_read_b32 v66, v74\n"
"Qmvva 220,16,216\ns_waitcnt vmcnt(29)\nds_write_b128 v52, a[244:247] offset:37888\nQmvva 220,36,48\ns_waitcnt lgkmcnt(5)\nQmvva 252,0,220\nQta 136,42,20992\nQmvva 252,20,52\n"
"s_waitcnt lgkmcnt(2)\nv_cmp_gt_u32 vcc, v75, v66\nQmvva 100,4,220\ns_cbranch_vccnz .Lso46\n.Lro46:\nQmvva 100,24,52\nQda 56,53,16384\nQmvva 132,8,220\n"
"buffer_load_dwordx4 a[44:47], v60, s[48:51], s31 offen offset:2048 sc1\ns_add_u32 m0, s96, 4096\ns_add_u32 s95, s73, 86016\nQmvva 132,28,52\nbuffer_load_dwordx4 v61, s[52:55], s95 offen lds\nQmvva 164,12,220\nQta 138,42,23552\nQmvva 164,32,52\n"
"s_waitcnt vmcnt(29)\nds_write_b128 v52, a[248:251] offset:41984\nQmvva 196,16,220\nQmvva 196,36,52\ns_waitcnt lgkmcnt(2)\nQmvva 228,0,224\nQta 120,40,21504\nQmvva 228,20,56\n"
"Qda 60,53,20480\nQmvva 108,4,224\nQta 122,40,24064\nQmvva 108,24,56\nQmvva 140,8,224\nbuffer_load_dwordx4 a[208:211], v60, s[48:51], s31 offen offset:3072 sc1\ns_add_u32 s95, s73, 85120\nQmvva 140,28,56\n"
"buffer_load_dwordx4 v61, s[52:55], s95 offen offset:1024 lds\nQmvva 172,12,224\nQta 140,42,21504\nQmvva 172,32,56\ns_waitcnt vmcnt(29)\nds_write_b128 v52, a[252:255] offset:46080\nQmvva 204,16,224\nv_add_u32 v75, 1, v75\n"
"Qmvva 204,36,56\ns_waitcnt vmcnt(30)\nds_write_b32 v73, v75\ns_waitcnt lgkmcnt(4)\nQmvva 236,0,228\nQta 142,42,24064\nQmvva 236,20,60\nQda 48,53,25600\n"
"Qmvva 80,4,228\nQta 124,40,22016\nQmvva 80,24,60\ns_add_u32 s31, s74, 32768\nQmvva 112,8,228\nbuffer_load_dwordx4 a[212:215], v60, s[48:51], s31 offen offset:0 sc1\ns_add_u32 s95, s73, 84224\nQmvva 112,28,60\n"
"buffer_load_dwordx4 v62, s[52:55], s95 offen offset:2048 lds\nQmvva 144,12,228\nQta 126,40,24576\nQmvva 144,32,60\nds_read_b32 v66, v74\nQmvva 176,16,228\ns_waitcnt vmcnt(29)\nds_write_b128 v52, a[168:171] offset:50176\n"
"Qmvva 176,36,60\ns_waitcnt lgkmcnt(4)\nQmvva 208,0,232\nQta 144,42,22016\nQmvva 208,20,48\ns_waitcnt lgkmcnt(2)\nv_cmp_gt_u32 vcc, v75, v66\nQmvva 240,4,232\n"
"s_cbranch_vccnz .Lso47\n.Lro47:\nQmvva 240,24,48\n.Lpg20:\ns_cbranch_execz .Lpg24\nQda 52,53,29696\nQmvva 84,8,232\nbuffer_load_dwordx4 a[216:219], v60, s[48:51], s31 offen offset:1024 sc1\n"
"s_add_u32 s95, s73, 103488\nQmvva 84,28,48\nbuffer_load_dwordx4 v61, s[52:55], s95 offen offset:3072 lds\nQmvva 88,12,232\nQmvva 88,32,48\ns_waitcnt vmcnt(28)\nds_write_b128 v52, a[172:175] offset:0\nQmvva 120,16,232\n"
"Qmvva 120,36,48\nQta 146,42,24576\ns_waitcnt lgkmcnt(2)\nQmvva 152,0,236\nQta 128,40,22528\nQmvva 152,20,52\nQda 56,53,33792\nQmvva 184,4,236\n"
"Qta 130,40,25088\nQmvva 184,24,52\nQmvva 216,8,236\nbuffer_load_dwordx4 a[220:223], v60, s[48:51], s31 offen offset:2048 sc1\ns_add_u32 m0, s96, 8192\ns_add_u32 s95, s73, 106688\nQmvva 216,28,52\nbuffer_load_dwordx4 v61, s[52:55], s95 offen lds\n"
"Qmvva 248,12,236\nQta 148,42,22528\nQmvva 248,32,52\ns_waitcnt vmcnt(28)\nds_write_b128 v52, a[0:3] offset:4096\nQmvva 116,16,236\nQmvva 116,36,52\nQta 150,42,25088\n"
"s_waitcnt lgkmcnt(4)\nQmvva 96,0,240\nQmvva 96,20,56\nQda 60,53,37888\nQmvva 128,4,240\nQta 84,40,40960\nQmvva 128,24,56\nQta 86,40,43520\n"
"s_add_u32 s31, s75, 28672\nQmvva 160,8,240\nbuffer_load_dwordx4 a[224:227], v60, s[48:51], s31 offen offset:0 sc1\nQmvva 160,28,56\nQta 176,42,40960\nQmvva 192,12,240\nQmvva 192,32,56\ns_waitcnt vmcnt(27)\n"
"ds_write_b128 v52, a[4:7] offset:8192\nQmvva 224,16,240\nv_add_u32 v75, 1, v75\nQmvva 224,36,56\nds_write_b32 v73, v75\ns_waitcnt lgkmcnt(5)\nQmvaa 92,112,244\ns_waitcnt vmcnt(12)\n"
"v_sub_u32 v66, v78, v65\nv_cmp_gt_u32 vcc, 5, v66\ns_and_b64 vcc, vcc, s[28:29]\ns_cbranch_scc1 .Lf5_w\n.Lf5_r:\nQmvaa 92,132,60\nQda 48,53,41984\nQmvaa 124,116,244\n"
"Qta 178,42,43520\nQmvaa 124,136,60\nQta 88,40,41472\nQmvaa 156,120,244\nbuffer_load_dwordx4 a[228:231], v60, s[48:51], s31 offen offset:1024 sc1\nQmvaa 156,140,60\nQmvaa 188,124,244\nQta 90,40,44032\n"
"Qmvaa 188,144,60\nds_read_b32 v66, v74\nQmvaa 220,128,244\ns_waitcnt vmcnt(26)\nds_write_b128 v52, a[8:11] offset:12288\nQmvaa 220,148,60\ns_waitcnt lgkmcnt(5)\nQmvaa 252,112,248\n"
"Qta 180,42,41472\nQmvaa 252,132,48\ns_waitcnt lgkmcnt(2)\nv_cmp_gt_u32 vcc, v75, v66\nQmvaa 100,116,248\ns_cbranch_vccnz .Lso48\n.Lro48:\nQmvaa 100,136,48\n"
"Qda 52,53,46080\nQmvaa 132,120,248\nbuffer_load_dwordx4 a[232:235], v60, s[48:51], s31 offen offset:2048 sc1\ns_add_u32 m0, s96, 24576\ns_add_u32 s95, s67, 126976\nQmvaa 132,140,48\nbuffer_load_dwordx4 v61, s[52:55], s95 offen lds\nQmvaa 164,124,248\n"
"Qta 182,42,44032\nQmvaa 164,144,48\ns_waitcnt vmcnt(27)\nds_write_b128 v52, a[12:15] offset:16384\nQmvaa 196,128,248\nQmvaa 196,148,48\ns_waitcnt lgkmcnt(2)\nQmvaa 228,112,252\n"
"Qta 92,40,41984\nQmvaa 228,132,52\nQda 56,53,50176\nQmvaa 108,116,252\nQta 94,40,44544\nQmvaa 108,136,52\nQmvaa 140,120,252\nbuffer_load_dwordx4 a[236:239], v60, s[48:51], s31 offen offset:3072 sc1\n"
"s_add_u32 s95, s67, 126080\nQmvaa 140,140,52\nbuffer_load_dwordx4 v61, s[52:55], s95 offen offset:1024 lds\nQmvaa 172,124,252\nQta 184,42,41984\nQmvaa 172,144,52\ns_waitcnt vmcnt(28)\nds_write_b128 v52, a[16:19] offset:20480\n"
"Qmvaa 204,128,252\nv_add_u32 v75, 1, v75\nQmvaa 204,148,52\ns_waitcnt vmcnt(31)\nds_write_b32 v73, v75\ns_lshr_b32 s68, %19, 2\ns_xor_b32 s68, s68, s92\ns_cbranch_scc1 .Lrole1\n"
"s_waitcnt lgkmcnt(4)\nQmvaa 236,112,168\nQta 186,42,44544\nQmvaa 236,132,56\nQda 60,53,0\nQmvaa 80,116,168\nQta 96,40,42496\nQmvaa 80,136,56\n"
"s_add_u32 s31, s75, 32768\nQmvaa 112,120,168\nbuffer_load_dwordx4 a[240:243], v60, s[48:51], s31 offen offset:0 sc1\ns_add_u32 s95, s67, 125184\nQmvaa 112,140,56\nbuffer_load_dwordx4 v62, s[52:55], s95 offen offset:2048 lds\nQmvaa 144,124,168\nQta 98,40,45056\n"
"Qmvaa 144,144,56\nds_read_b32 v66, v74\nQmvaa 176,128,168\ns_waitcnt vmcnt(28)\nds_write_b128 v52, a[20:23] offset:25600\nQmvaa 176,148,56\ns_waitcnt lgkmcnt(4)\nQmvaa 208,112,172\n"
"Qta 188,42,42496\nQmvaa 208,132,60\ns_waitcnt lgkmcnt(2)\nv_cmp_gt_u32 vcc, v75, v66\nQmvaa 240,116,172\ns_cbranch_vccnz .Lso49r0\n.Lro49r0:\nQmvaa 240,136,60\n"
"Qda 48,53,4096\nQmvaa 84,120,172\nbuffer_load_dwordx4 a[244:247], v60, s[48:51], s31 offen offset:1024 sc1\ns_add_u32 s95, s67, 144448\nQmvaa 84,140,60\nbuffer_load_dwordx4 v61, s[52:55], s95 offen offset:3072 lds\nQmvaa 88,124,172\nQmvaa 88,144,60\n"
"s_waitcnt vmcnt(28)\nds_write_b128 v52, a[24:27] offset:29696\nQmvaa 120,128,172\nQmvaa 120,148,60\nQta 190,42,45056\ns_waitcnt lgkmcnt(2)\nQmvaa 152,112,0\nQta 100,40,43008\n"
"Qmvaa 152,132,48\nQda 52,53,8192\nQmvaa 184,116,0\nQta 102,40,45568\nQmvaa 184,136,48\nQmvaa 216,120,0\nbuffer_load_dwordx4 a[248:251], v60, s[48:51], s31 offen offset:2048 sc1\ns_add_u32 m0, s96, 28672\n"
"s_add_u32 s95, s67, 147648\nQmvaa 216,140,48\nbuffer_load_dwordx4 v61, s[52:55], s95 offen lds\nQmvaa 248,124,0\nQta 192,42,43008\nQmvaa 248,144,48\ns_waitcnt vmcnt(28)\nds_write_b128 v52, a[28:31] offset:33792\n"
"Qmvaa 116,128,0\nQmvaa 116,148,48\nQta 194,42,45568\ns_waitcnt lgkmcnt(4)\nQmvaa 96,112,4\nQmvaa 96,132,52\nQda 56,53,12288\nQmvaa 128,116,4\n"
"Qtv 0,41,0\nQmvaa 128,136,52\nQtv 2,41,2560\ns_add_u32 s31, s77, 32768\nQmvaa 160,120,4\nbuffer_load_dwordx4 a[252:255], v60, s[48:51], s31 offen offset:0 sc1\nQmvaa 160,140,52\nQtv 20,43,0\n"
"Qmvaa 192,124,4\nQmvaa 192,144,52\ns_waitcnt vmcnt(27)\nds_write_b128 v52, a[32:35] offset:37888\nQmvaa 224,128,4\nv_add_u32 v75, 1, v75\nQmvaa 224,148,52\nds_write_b32 v73, v75\n"
"s_waitcnt lgkmcnt(5)\nQmvaa 92,84,8\nQtv 22,43,2560\nQmvaa 92,176,56\nQda 60,53,16384\nQmvaa 124,88,8\nQtv 4,41,512\nQmvaa 124,180,56\n"
"Qmvaa 156,92,8\nbuffer_load_dwordx4 a[168:171], v60, s[48:51], s31 offen offset:1024 sc1\nQmvaa 156,184,56\nQtv 6,41,3072\nQmvaa 188,96,8\nQtv 24,43,512\nQmvaa 188,188,56\nds_read_b32 v66, v74\n"
"Qmvaa 220,100,8\ns_waitcnt vmcnt(26)\nds_write_b128 v52, a[36:39] offset:41984\nQmvaa 220,192,56\ns_waitcnt lgkmcnt(5)\nQmvaa 252,84,12\nQmvaa 252,176,60\ns_waitcnt lgkmcnt(1)\n"
"v_cmp_gt_u32 vcc, v75, v66\nQmvaa 100,88,12\ns_cbranch_vccnz .Lso50r0\n.Lro50r0:\nQmvaa 100,180,60\nQda 48,53,20480\nQmvaa 132,92,12\nbuffer_load_dwordx4 a[172:175], v60, s[48:51], s31 offen offset:2048 sc1\n"
"s_add_u32 m0, s96, 45056\ns_add_u32 s95, s72, 126976\nQmvaa 132,184,60\nbuffer_load_dwordx4 v61, s[52:55], s95 offen lds\nQmvaa 164,96,12\nQtv 26,43,3072\nQmvaa 164,188,60\ns_waitcnt vmcnt(26)\n"
"ds_write_b128 v52, a[40:43] offset:46080\nQmvaa 196,100,12\nQmvaa 196,192,60\nQtv 8,41,1024\ns_waitcnt lgkmcnt(3)\nQmvaa 228,84,16\nQmvaa 228,176,48\nQda 52,53,25600\n"
"Qmvaa 108,88,16\nQtv 10,41,3584\nQmvaa 108,180,48\nQtv 28,43,1024\ns_add_u32 s31, s74, 47104\nQmvaa 140,92,16\nbuffer_load_dwordx4 a[0:3], v60, s[48:51], s31 offen offset:0 sc1\ns_add_u32 s95, s72, 126080\n"
"Qmvaa 140,184,48\nbuffer_load_dwordx4 v61, s[52:55], s95 offen offset:1024 lds\nQmvaa 172,96,16\nQtv 30,43,3584\nQmvaa 172,188,48\ns_waitcnt vmcnt(27)\nds_write_b128 v52, a[44:47] offset:50176\nQmvaa 204,100,16\n"
"v_add_u32 v75, 1, v75\nQmvaa 204,192,48\ns_waitcnt vmcnt(31)\nds_write_b32 v73, v75\ns_waitcnt lgkmcnt(5)\nQmvaa 236,84,20\nQmvaa 236,176,52\nQda 56,53,29696\n"
"Qmvaa 80,88,20\nQtv 12,41,1536\nQmvaa 80,180,52\nQtv 14,41,4096\nQmvaa 112,92,20\nbuffer_load_dwordx4 a[4:7], v60, s[48:51], s31 offen offset:1024 sc1\ns_add_u32 s95, s72, 125184\nQmvaa 112,184,52\n"
"buffer_load_dwordx4 v62, s[52:55], s95 offen offset:2048 lds\nQmvaa 144,96,20\nQmvaa 144,188,52\nds_read_b32 v66, v74\nQmvaa 176,100,20\ns_waitcnt vmcnt(27)\nds_write_b128 v52, a[208:211] offset:0\nQmvaa 176,192,52\n"
"s_waitcnt lgkmcnt(4)\nQmvaa 208,84,24\nQtv 32,43,1536\nQmvaa 208,176,56\ns_waitcnt lgkmcnt(2)\nv_cmp_gt_u32 vcc, v75, v66\nQmvaa 240,88,24\ns_cbranch_vccnz .Lso51r0\n"
".Lro51r0:\nQmvaa 240,180,56\nQda 60,53,33792\nQmvaa 84,92,24\nbuffer_load_dwordx4 a[8:11], v60, s[48:51], s31 offen offset:2048 sc1\ns_add_u32 s95, s72, 144448\nQmvaa 84,184,56\nbuffer_load_dwordx4 v61, s[52:55], s95 offen offset:3072 lds\n"
"Qmvaa 88,96,24\nQtv 34,43,4096\nQmvaa 88,188,56\ns_waitcnt vmcnt(27)\nds_write_b128 v52, a[212:215] offset:4096\nQmvaa 120,100,24\nQmvaa 120,192,56\ns_waitcnt lgkmcnt(2)\n"
"Qmvaa 152,84,28\nQtv 16,41,2048\nQmvaa 152,176,60\nQda 48,53,37888\nQmvaa 184,88,28\nQtv 18,41,4608\nQmvaa 184,180,60\ns_add_u32 s31, s75, 47104\n"
"Qmvaa 216,92,28\nbuffer_load_dwordx4 a[12:15], v60, s[48:51], s31 offen offset:0 sc1\ns_add_u32 m0, s96, 49152\ns_add_u32 s95, s72, 147648\nQmvaa 216,184,60\nbuffer_load_dwordx4 v61, s[52:55], s95 offen lds\nQmvaa 248,96,28\nQtv 36,43,2048\n"
"Qmvaa 248,188,60\ns_waitcnt vmcnt(27)\nds_write_b128 v52, a[216:219] offset:8192\nQmvaa 116,100,28\nQmvaa 116,192,60\nQtv 38,43,4608\ns_waitcnt lgkmcnt(4)\nQmvaa 96,84,32\n"
"Qmvaa 96,176,48\nQda 52,53,41984\nQmvaa 128,88,32\nQta 112,41,20480\nQmvaa 128,180,48\nQta 114,41,23040\nQmvaa 160,92,32\nbuffer_load_dwordx4 a[16:19], v60, s[48:51], s31 offen offset:1024 sc1\n"
"Qmvaa 160,184,48\nQta 132,43,20480\nQmvaa 192,96,32\nQmvaa 192,188,48\ns_waitcnt vmcnt(26)\nds_write_b128 v52, a[220:223] offset:12288\nQmvaa 224,100,32\nv_add_u32 v75, 1, v75\n"
"Qmvaa 224,192,48\nds_write_b32 v73, v75\ns_waitcnt lgkmcnt(5)\nQmvva 92,0,36\nQta 134,43,23040\nQmvva 92,20,52\nQda 56,53,46080\nQmvva 124,4,36\n"
"Qta 116,41,20992\nQmvva 124,24,52\nQmvva 156,8,36\nbuffer_load_dwordx4 a[20:23], v60, s[48:51], s31 offen offset:2048 sc1\nQmvva 156,28,52\nQta 118,41,23552\nQmvva 188,12,36\nQta 136,43,20992\n"
"Qmvva 188,32,52\nds_read_b32 v66, v74\nQmvva 220,16,36\ns_waitcnt vmcnt(25)\nds_write_b128 v52, a[224:227] offset:16384\nQmvva 220,36,52\ns_waitcnt lgkmcnt(5)\nQmvva 252,0,40\n"
"Qmvva 252,20,56\ns_waitcnt lgkmcnt(1)\nv_cmp_gt_u32 vcc, v75, v66\nQmvva 100,4,40\ns_cbranch_vccnz .Lso52r0\n.Lro52r0:\nQmvva 100,24,56\nQda 60,53,50176\n"
"s_add_u32 s31, s77, 47104\nQmvva 132,8,40\nbuffer_load_dwordx4 a[24:27], v60, s[48:51], s31 offen offset:0 sc1\ns_add_u32 m0, s96, 65536\ns_add_u32 s95, s73, 126976\nQmvva 132,28,56\nbuffer_load_dwordx4 v61, s[52:55], s95 offen lds\nQmvva 164,12,40\n"
"Qta 138,43,23552\nQmvva 164,32,56\ns_waitcnt vmcnt(26)\nds_write_b128 v52, a[228:231] offset:20480\nQmvva 196,16,40\nQmvva 196,36,56\nQta 120,41,21504\ns_waitcnt lgkmcnt(3)\n"
"Qmvva 228,0,44\nQmvva 228,20,60\nQda 48,53,0\nQmvva 108,4,44\nQta 122,41,24064\nQmvva 108,24,60\nQta 140,43,21504\nQmvva 140,8,44\n"
"buffer_load_dwordx4 a[28:31], v60, s[48:51], s31 offen offset:1024 sc1\ns_add_u32 s95, s73, 126080\nQmvva 140,28,60\nbuffer_load_dwordx4 v61, s[52:55], s95 offen offset:1024 lds\nQmvva 172,12,44\nQta 142,43,24064\nQmvva 172,32,60\ns_waitcnt vmcnt(27)\n"
"ds_write_b128 v52, a[232:235] offset:25600\nQmvva 204,16,44\nv_add_u32 v75, 1, v75\nQmvva 204,36,60\ns_waitcnt vmcnt(30)\nds_write_b32 v73, v75\ns_waitcnt lgkmcnt(5)\nQmvva 236,0,208\n"
"Qmvva 236,20,48\nQda 52,53,4096\nQmvva 80,4,208\nQta 124,41,22016\nQmvva 80,24,48\nQta 126,41,24576\nQmvva 112,8,208\nbuffer_load_dwordx4 a[32:35], v60, s[48:51], s31 offen offset:2048 sc1\n"
"s_add_u32 s95, s73, 125184\nQmvva 112,28,48\nbuffer_load_dwordx4 v62, s[52:55], s95 offen offset:2048 lds\nQmvva 144,12,208\nQmvva 144,32,48\nds_read_b32 v66, v74\nQmvva 176,16,208\ns_waitcnt vmcnt(27)\n"
"ds_write_b128 v52, a[236:239] offset:29696\nQmvva 176,36,48\ns_waitcnt lgkmcnt(4)\nQmvva 208,0,212\nQta 144,43,22016\nQmvva 208,20,52\ns_waitcnt lgkmcnt(2)\nv_cmp_gt_u32 vcc, v75, v66\n"
"Qmvva 240,4,212\ns_cbranch_vccnz .Lso53r0\n.Lro53r0:\nQmvva 240,24,52\nQda 56,53,8192\ns_add_u32 s31, s77, 28672\nQmvva 84,8,212\nbuffer_load_dwordx4 a[36:39], v60, s[48:51], s31 offen offset:0 sc1\n"
"s_add_u32 s95, s73, 144448\nQmvva 84,28,52\nbuffer_load_dwordx4 v61, s[52:55], s95 offen offset:3072 lds\nQmvva 88,12,212\nQta 146,43,24576\nQmvva 88,32,52\ns_waitcnt vmcnt(27)\nds_write_b128 v52, a[240:243] offset:33792\n"
"Qmvva 120,16,212\nQmvva 120,36,52\ns_waitcnt lgkmcnt(2)\nQmvva 152,0,216\nQta 128,41,22528\nQmvva 152,20,56\nQda 60,53,12288\nQmvva 184,4,216\n"
"Qta 130,41,25088\nQmvva 184,24,56\nQmvva 216,8,216\nbuffer_load_dwordx4 a[40:43], v60, s[48:51], s31 offen offset:1024 sc1\ns_add_u32 m0, s96, 69632\ns_add_u32 s95, s73, 147648\nQmvva 216,28,56\nbuffer_load_dwordx4 v61, s[52:55], s95 offen lds\n"
"Qmvva 248,12,216\nQta 148,43,22528\nQmvva 248,32,56\ns_waitcnt vmcnt(27)\nds_write_b128 v52, a[244:247] offset:37888\nQmvva 116,16,216\nQmvva 116,36,56\nQta 150,43,25088\n"
"s_waitcnt lgkmcnt(4)\nQmvva 96,0,220\nQmvva 96,20,60\nQda 48,53,16384\nQmvva 128,4,220\nQta 84,40,0\nQmvva 128,24,60\nQta 86,40,2560\n"
"Qmvva 160,8,220\nbuffer_load_dwordx4 a[44:47], v60, s[48:51], s31 offen offset:2048 sc1\nQmvva 160,28,60\nQmvva 192,12,220\nQta 176,42,0\nQmvva 192,32,60\ns_waitcnt vmcnt(26)\nds_write_b128 v52, a[248:251] offset:41984\n"
"Qmvva 224,16,220\nv_add_u32 v75, 1, v75\nQmvva 224,36,60\nds_write_b32 v73, v75\ns_waitcnt lgkmcnt(5)\nQmvaa 92,112,224\nQmvaa 92,132,48\nQda 52,53,20480\n"
"Qmvaa 124,116,224\nQta 178,42,2560\nQmvaa 124,136,48\nQta 88,40,512\nQmvaa 156,120,224\nbuffer_load_dwordx4 a[208:211], v60, s[48:51], s31 offen offset:3072 sc1\nQmvaa 156,140,48\nQmvaa 188,124,224\n"
"Qta 90,40,3072\nQmvaa 188,144,48\nds_read_b32 v66, v74\nQmvaa 220,128,224\ns_waitcnt vmcnt(25)\nds_write_b128 v52, a[252:255] offset:46080\nQmvaa 220,148,48\ns_waitcnt lgkmcnt(5)\n"
"Qmvaa 252,112,228\nQmvaa 252,132,52\ns_waitcnt lgkmcnt(1)\nv_cmp_gt_u32 vcc, v75, v66\nQmvaa 100,116,228\ns_cbranch_vccnz .Lso54r0\n.Lro54r0:\nQmvaa 100,136,52\n"
"Qda 56,53,25600\ns_add_u32 s31, s74, 43008\nQmvaa 132,120,228\nbuffer_load_dwordx4 a[212:215], v60, s[48:51], s31 offen offset:0 sc1\nQmvaa 132,140,52\nQta 180,42,512\nQmvaa 164,124,228\nQta 182,42,3072\n"
"Qmvaa 164,144,52\n.Lpg21:\ns_cbranch_execz .Lpg25\ns_waitcnt vmcnt(25)\nds_write_b128 v52, a[168:171] offset:50176\nQmvaa 196,128,228\nQmvaa 196,148,52\ns_waitcnt lgkmcnt(3)\n"
"Qmvaa 228,112,232\nQta 92,40,1024\nQmvaa 228,132,56\nQda 60,53,29696\nQmvaa 108,116,232\nQmvaa 108,136,56\nQta 94,40,3584\nQmvaa 140,120,232\n"
"buffer_load_dwordx4 a[216:219], v60, s[48:51], s31 offen offset:1024 sc1\nQmvaa 140,140,56\nQmvaa 172,124,232\nQta 184,42,1024\nQmvaa 172,144,56\ns_waitcnt vmcnt(25)\nds_write_b128 v52, a[172:175] offset:0\nQmvaa 204,128,232\n"
"v_add_u32 v75, 1, v75\nQmvaa 204,148,56\ns_waitcnt vmcnt(28)\nds_write_b32 v73, v75\ns_waitcnt lgkmcnt(4)\nQmvaa 236,112,236\nQta 186,42,3584\nQmvaa 236,132,60\n"
"Qda 48,53,33792\nQmvaa 80,116,236\nQmvaa 80,136,60\nQta 96,40,1536\nQmvaa 112,120,236\nbuffer_load_dwordx4 a[220:223], v60, s[48:51], s31 offen offset:2048 sc1\nQmvaa 112,140,60\nQmvaa 144,124,236\n"
"Qta 98,40,4096\nQmvaa 144,144,60\nds_read_b32 v66, v74\nQmvaa 176,128,236\ns_waitcnt vmcnt(24)\nds_write_b128 v52, a[0:3] offset:4096\nQmvaa 176,148,60\ns_waitcnt lgkmcnt(4)\n"
"Qmvaa 208,112,240\nQta 188,42,1536\nQmvaa 208,132,48\ns_waitcnt lgkmcnt(2)\nv_cmp_gt_u32 vcc, v75, v66\nQmvaa 240,116,240\ns_cbranch_vccnz .Lso55r0\n.Lro55r0:\n"
"Qmvaa 240,136,48\nQda 52,53,37888\nQmvaa 84,120,240\nbuffer_load_dwordx4 a[224:227], v60, s[48:51], s31 offen offset:3072 sc1\nQmvaa 84,140,48\nQmvaa 88,124,240\nQta 190,42,4096\nQmvaa 88,144,48\n"
"s_waitcnt vmcnt(23)\nds_write_b128 v52, a[4:7] offset:8192\nQmvaa 120,128,240\nQmvaa 120,148,48\ns_waitcnt lgkmcnt(2)\nQmvaa 152,112,244\nQta 100,40,2048\nQmvaa 152,132,52\n"
"Qda 56,53,41984\nQmvaa 184,116,244\nQta 102,40,4608\nQmvaa 184,136,52\ns_add_u32 s31, s75, 43008\nQmvaa 216,120,244\nbuffer_load_dwordx4 a[228:231], v60, s[48:51], s31 offen offset:0 sc1\nQmvaa 216,140,52\n"
"Qta 192,42,2048\nQmvaa 248,124,244\nQmvaa 248,144,52\ns_waitcnt vmcnt(22)\nds_write_b128 v52, a[8:11] offset:12288\nQmvaa 116,128,244\nQmvaa 116,148,52\nQta 194,42,4608\n"
"s_waitcnt lgkmcnt(4)\nQmvaa 96,112,248\nQmvaa 96,132,56\nQda 60,53,46080\nQmvaa 128,116,248\nQtv 0,40,20480\nQtv 2,40,23040\nQmvaa 128,136,56\n"
"Qtv 20,42,20480\nQmvaa 160,120,248\nbuffer_load_dwordx4 a[232:235], v60, s[48:51], s31 offen offset:1024 sc1\nQmvaa 160,140,56\nQtv 22,42,23040\nQmvaa 192,124,248\nQtv 4,40,20992\nQmvaa 192,144,56\n"
"s_waitcnt vmcnt(21)\nds_write_b128 v52, a[12:15] offset:16384\nQmvaa 224,128,248\nv_add_u32 v75, 1, v75\nQmvaa 224,148,56\ns_waitcnt vmcnt(20)\nds_write_b32 v73, v75\ns_waitcnt lgkmcnt(7)\n"
"Qmvaa 208,84,252\nQtv 6,40,23552\nQmvaa 208,176,60\nQda 48,53,50176\nQmvaa 240,88,252\nQtv 24,42,20992\nQtv 26,42,23552\nQmvaa 240,180,60\n"
"Qtv 8,40,21504\nQmvaa 84,92,252\nbuffer_load_dwordx4 a[236:239], v60, s[48:51], s31 offen offset:2048 sc1\nQmvaa 84,184,60\nQtv 10,40,24064\nQmvaa 88,96,252\nQtv 28,42,21504\nQmvaa 88,188,60\n"
"ds_read_b32 v66, v74\nQmvaa 120,100,252\ns_waitcnt vmcnt(20)\nds_write_b128 v52, a[16:19] offset:20480\nQmvaa 120,192,60\ns_waitcnt lgkmcnt(7)\nQmvaa 152,84,168\nQtv 30,42,24064\n"
"Qmvaa 152,176,48\ns_waitcnt lgkmcnt(2)\nv_cmp_gt_u32 vcc, v75, v66\nQmvaa 184,88,168\ns_cbranch_vccnz .Lso56r0\n.Lro56r0:\nQmvaa 184,180,48\nQda 52,53,0\n"
"Qmvaa 216,92,168\nbuffer_load_dwordx4 a[240:243], v60, s[48:51], s31 offen offset:3072 sc1\nQmvaa 216,184,48\nQtv 12,40,22016\nQmvaa 248,96,168\nQtv 14,40,24576\nQtv 32,42,22016\nQmvaa 248,188,48\n"
"s_waitcnt vmcnt(20)\nds_write_b128 v52, a[20:23] offset:25600\nQmvaa 116,100,168\nQmvaa 116,192,48\nQtv 34,42,24576\ns_waitcnt lgkmcnt(5)\nQmvaa 96,84,172\nQtv 16,40,22528\n"
"Qmvaa 96,176,52\nQda 56,53,4096\nQmvaa 128,88,172\nQtv 18,40,25088\nQmvaa 128,180,52\nQtv 36,42,22528\ns_add_u32 s31, s77, 43008\nQmvaa 160,92,172\n"
"buffer_load_dwordx4 a[244:247], v60, s[48:51], s31 offen offset:0 sc1\nQmvaa 160,184,52\nQtv 38,42,25088\nQmvaa 192,96,172\nQta 112,40,40960\nQta 114,40,43520\nQmvaa 192,188,52\ns_waitcnt vmcnt(20)\n"
"ds_write_b128 v52, a[24:27] offset:29696\nQmvaa 224,100,172\nv_add_u32 v75, 1, v75\nQmvaa 224,192,52\ns_waitcnt vmcnt(11)\nds_write_b32 v73, v75\ns_waitcnt lgkmcnt(4)\nQmvva 208,0,0\n"
"Qta 132,42,40960\nQmvva 208,20,56\nQda 60,53,8192\nQmvva 240,4,0\nQta 134,42,43520\nQta 116,40,41472\nQmvva 240,24,56\nQta 118,40,44032\n"
"Qmvva 84,8,0\nbuffer_load_dwordx4 a[248:251], v60, s[48:51], s31 offen offset:1024 sc1\nQmvva 84,28,56\nQta 136,42,41472\nQta 138,42,44032\nQmvva 88,12,0\nQta 120,40,41984\nQmvva 88,32,56\n"
"ds_read_b32 v66, v74\nQmvva 120,16,0\ns_waitcnt vmcnt(19)\nds_write_b128 v52, a[28:31] offset:33792\nQmvva 120,36,56\ns_waitcnt lgkmcnt(8)\nQmvva 152,0,4\nQta 122,40,44544\n"
"Qmvva 152,20,60\ns_waitcnt lgkmcnt(2)\nv_cmp_gt_u32 vcc, v75, v66\nQmvva 184,4,4\ns_cbranch_vccnz .Lso57r0\n.Lro57r0:\nQmvva 184,24,60\nQda 48,53,12288\n"
"Qmvva 216,8,4\nbuffer_load_dwordx4 a[252:255], v60, s[48:51], s31 offen offset:2048 sc1\nQmvva 216,28,60\nQta 140,42,41984\nQta 142,42,44544\nQmvva 248,12,4\nQta 124,40,42496\nQmvva 248,32,60\n"
"s_waitcnt vmcnt(18)\nds_write_b128 v52, a[32:35] offset:37888\nQmvva 116,16,4\nQmvva 116,36,60\nQta 126,40,45056\nQta 144,42,42496\ns_waitcnt lgkmcnt(6)\nQmvva 96,0,8\n"
"Qta 146,42,45056\nQmvva 96,20,48\nQda 52,53,16384\nQmvva 128,4,8\nQta 128,40,43008\nQta 130,40,45568\nQmvva 128,24,48\nQta 148,42,43008\n"
"Qmvva 160,8,8\nbuffer_load_dwordx4 a[168:171], v60, s[48:51], s31 offen offset:3072 sc1\nQmvva 160,28,48\nQta 150,42,45568\nQmvva 192,12,8\nQta 84,41,0\nQta 86,41,2560\nQmvva 192,32,48\n"
"s_waitcnt vmcnt(17)\nds_write_b128 v52, a[36:39] offset:41984\nQmvva 224,16,8\nQmvva 224,36,48\nQta 176,43,0\ns_waitcnt lgkmcnt(4)\nQmvaa 208,112,12\nQta 178,43,2560\n"
"Qmvaa 208,132,52\nQda 56,53,20480\nQmvaa 240,116,12\nQta 88,41,512\nQmvaa 240,136,52\nQta 90,41,3072\nQmvaa 84,120,12\nQta 180,43,512\n"
"Qta 182,43,3072\nQmvaa 84,140,52\nQta 92,41,1024\nQmvaa 88,124,12\nQta 94,41,3584\nQmvaa 88,144,52\ns_waitcnt vmcnt(15)\nds_write_b128 v52, a[40:43] offset:46080\n"
"Qmvaa 120,128,12\nv_add_u32 v75, 1, v75\nQmvaa 120,148,52\nds_write_b32 v73, v75\ns_waitcnt lgkmcnt(8)\nQmvaa 152,112,16\nQta 184,43,1024\nQmvaa 152,132,56\n"
"Qda 60,53,25600\nQmvaa 184,116,16\nQta 186,43,3584\nQmvaa 184,136,56\nQta 96,41,1536\nQmvaa 216,120,16\nQta 98,41,4096\nQta 188,43,1536\n"
"Qmvaa 216,140,56\nQta 190,43,4096\nQmvaa 248,124,16\nQta 100,41,2048\nQmvaa 248,144,56\nds_read_b32 v66, v74\nQmvaa 116,128,16\ns_waitcnt vmcnt(13)\n"
"ds_write_b128 v52, a[44:47] offset:50176\nQmvaa 116,148,56\ns_waitcnt lgkmcnt(8)\nQmvaa 96,112,20\nQta 102,41,4608\nQmvaa 96,132,60\ns_waitcnt lgkmcnt(2)\nv_cmp_gt_u32 vcc, v75, v66\n"
"Qmvaa 128,116,20\ns_cbranch_vccnz .Lso58r0\n.Lro58r0:\nQmvaa 128,136,60\nQda 48,53,29696\nQmvaa 160,120,20\nQta 192,43,2048\nQmvaa 160,140,60\n"
"Qta 194,43,4608\nQmvaa 192,124,20\nQtv 0,40,0\nQtv 2,40,2560\nQmvaa 192,144,60\ns_waitcnt vmcnt(12)\nds_write_b128 v52, a[208:211] offset:0\nQmvaa 224,128,20\n"
"Qmvaa 224,148,60\nQtv 20,42,0\ns_waitcnt lgkmcnt(4)\nQmvaa 208,84,24\nQtv 22,42,2560\nQmvaa 208,176,48\nQda 52,53,33792\ns_add_u32 s68, s64, 0\n"
"Qmvaa 240,88,24\nQtv 4,40,512\nQmvaa 240,180,48\nQtv 6,40,3072\nQmvaa 84,92,24\nQtv 24,42,512\nQtv 26,42,3072\nQmvaa 84,184,48\n"
"Qtv 8,40,1024\nQmvaa 88,96,24\nQtv 10,40,3584\nQmvaa 88,188,48\ns_waitcnt vmcnt(11)\nds_write_b128 v52, a[212:215] offset:4096\nQmvaa 120,100,24\nv_add_u32 v75, 1, v75\n"
"Qk 208,208,209\nQmvaa 120,192,48\nds_write_b32 v73, v75\nv_min_u32 v208, -2, v208\ns_waitcnt lgkmcnt(8)\nQmvaa 152,84,28\nQtv 28,42,1024\nQk 209,210,211\n"
"Qmvaa 152,176,52\nQda 56,53,37888\nv_min_u32 v209, -2, v209\nQmvaa 184,88,28\nQtv 30,42,3584\nQk 210,240,241\nQmvaa 184,180,52\nQtv 12,40,1536\n"
"v_min_u32 v210, -2, v210\nQmvaa 216,92,28\nQtv 14,40,4096\nQtv 32,42,1536\nQmvaa 216,184,52\nQtv 34,42,4096\nQk 211,242,243\nQmvaa 248,96,28\n"
"Qtv 16,40,2048\nv_min_u32 v211, -2, v211\nQmvaa 248,188,52\nds_read_b32 v66, v74\nbuffer_store_dwordx4 v[208:211], v60, s[24:27], s68 offen offset:0 sc1\nQmvaa 116,100,28\ns_waitcnt vmcnt(11)\nds_write_b128 v52, a[216:219] offset:8192\n"
"Qmvaa 116,192,52\nQk 84,84,85\nv_min_u32 v84, -2, v84\ns_waitcnt lgkmcnt(8)\nQmvaa 96,84,32\nQtv 18,40,4608\nQk 85,86,87\nQmvaa 96,176,56\n"
"s_waitcnt lgkmcnt(2)\nv_cmp_gt_u32 vcc, v75, v66\nQmvaa 128,88,32\ns_cbranch_vccnz .Lso59r0\n.Lro59r0:\nv_min_u32 v85, -2, v85\nQmvaa 128,180,56\nQda 60,53,41984\n"
"Qk 86,88,89\nQmvaa 160,92,32\nQtv 36,42,2048\nv_min_u32 v86, -2, v86\nQmvaa 160,184,56\nQtv 38,42,4608\nQk 87,90,91\nQmvaa 192,96,32\n"
"Qta 112,40,20480\nv_min_u32 v87, -2, v87\nQmvaa 192,188,56\ns_waitcnt vmcnt(10)\nds_write_b128 v52, a[220:223] offset:12288\nQmvaa 224,100,32\nbuffer_store_dwordx4 v[84:87], v60, s[24:27], s68 offen offset:1024 sc1\nQk 120,120,121\n"
"Qmvaa 224,192,56\nQta 114,40,23040\nv_min_u32 v120, -2, v120\ns_waitcnt lgkmcnt(3)\nQmvva 92,0,36\nQta 132,42,20480\nQk 121,122,123\nQmvva 92,20,60\n"
"Qda 48,53,46080\nv_min_u32 v121, -2, v121\nQmvva 124,4,36\nQta 134,42,23040\nQk 122,152,153\nQmvva 124,24,60\nQta 116,40,20992\nv_min_u32 v122, -2, v122\n"
"Qmvva 156,8,36\nQta 118,40,23552\nQk 123,154,155\nQmvva 156,28,60\nQta 136,42,20992\nv_min_u32 v123, -2, v123\nQmvva 188,12,36\nQta 138,42,23552\n"
"buffer_store_dwordx4 v[120:123], v60, s[24:27], s68 offen offset:2048 sc1\nQmvva 188,32,60\ns_waitcnt vmcnt(11)\nds_write_b128 v52, a[224:227] offset:16384\nQmvva 220,16,36\nQk 184,184,185\nv_min_u32 v184, -2, v184\nQmvva 220,36,60\n"
"Qta 120,40,21504\nQk 185,186,187\ns_waitcnt lgkmcnt(7)\nQmvva 252,0,40\nQta 122,40,24064\nv_min_u32 v185, -2, v185\nQmvva 252,20,48\nQda 52,53,50176\n"
"Qk 186,216,217\nQmvva 100,4,40\nv_min_u32 v186, -2, v186\nQk 187,218,219\nQmvva 100,24,48\nQta 140,42,21504\nv_min_u32 v187, -2, v187\nQmvva 132,8,40\n"
"Qta 142,42,24064\nbuffer_store_dwordx4 v[184:187], v60, s[24:27], s68 offen offset:3072 sc1\nQmvva 132,28,48\nQta 124,40,22016\nQk 248,248,249\nQmvva 164,12,40\nQta 126,40,24576\nv_min_u32 v248, -2, v248\n"
"Qmvva 164,32,48\ns_waitcnt vmcnt(11)\nds_write_b128 v52, a[228:231] offset:20480\nQmvva 196,16,40\nv_add_u32 v75, 1, v75\nQk 249,250,251\nQmvva 196,36,48\nds_write_b32 v73, v75\n"
"v_min_u32 v249, -2, v249\ns_waitcnt lgkmcnt(6)\nQmvva 228,0,44\nQta 144,42,22016\nQk 250,116,117\nQmvva 228,20,52\nQda 56,53,0\nv_min_u32 v250, -2, v250\n"
"Qmvva 108,4,44\nQta 146,42,24576\nQk 251,118,119\nQmvva 108,24,52\nQta 128,40,22528\nv_min_u32 v251, -2, v251\nQmvva 140,8,44\nQta 130,40,25088\n"
"s_add_u32 s68, s64, 4096\nQmvva 140,28,52\nQta 148,42,22528\nbuffer_store_dwordx4 v[248:251], v60, s[24:27], s68 offen offset:0 sc1\nQmvva 172,12,44\nQta 150,42,25088\nQk 96,96,97\nQmvva 172,32,52\n"
"ds_read_b32 v66, v74\nv_min_u32 v96, -2, v96\nQmvva 204,16,44\ns_waitcnt vmcnt(11)\nds_write_b128 v52, a[232:235] offset:25600\nQmvva 204,36,52\nQk 97,98,99\nv_min_u32 v97, -2, v97\n"
"s_waitcnt lgkmcnt(7)\nQmvva 236,0,208\nQk 98,128,129\nv_min_u32 v98, -2, v98\nQmvva 236,20,56\ns_waitcnt lgkmcnt(1)\nv_cmp_gt_u32 vcc, v75, v66\nQmvva 80,4,208\n"
"s_cbranch_vccnz .Lso60r0\n.Lro60r0:\nQk 99,130,131\nQmvva 80,24,56\nQda 60,53,4096\nv_min_u32 v99, -2, v99\nQmvva 112,8,208\nQta 84,40,40960\n"
"buffer_store_dwordx4 v[96:99], v60, s[24:27], s68 offen offset:1024 sc1\nQmvva 112,28,56\nQta 86,40,43520\nQk 160,160,161\nQmvva 144,12,208\nQta 176,42,40960\nv_min_u32 v160, -2, v160\nQmvva 144,32,56\n"
"s_waitcnt vmcnt(11)\nds_write_b128 v52, a[236:239] offset:29696\nQmvva 176,16,208\nQk 161,162,163\nv_min_u32 v161, -2, v161\nQmvva 176,36,56\nQta 178,42,43520\nQk 162,192,193\n"
"s_waitcnt lgkmcnt(5)\nQmvaa 92,112,212\nQta 88,40,41472\nv_min_u32 v162, -2, v162\nQmvaa 92,132,60\nQda 48,53,8192\nQk 163,194,195\nQmvaa 124,116,212\n"
"Qta 90,40,44032\nv_min_u32 v163, -2, v163\nQmvaa 124,136,60\nQta 180,42,41472\nbuffer_store_dwordx4 v[160:163], v60, s[24:27], s68 offen offset:2048 sc1\nQmvaa 156,120,212\nQta 182,42,44032\nQk 224,224,225\n"
"Qmvaa 156,140,60\nQta 92,40,41984\nv_min_u32 v224, -2, v224\nQmvaa 188,124,212\nQta 94,40,44544\nQk 225,226,227\nQmvaa 188,144,60\ns_waitcnt vmcnt(11)\n"
"ds_write_b128 v52, a[240:243] offset:33792\nQmvaa 220,128,212\nv_add_u32 v75, 1, v75\nv_min_u32 v225, -2, v225\nQmvaa 220,148,60\nds_write_b32 v73, v75\ns_add_u32 s68, s64, 14336\ns_waitcnt lgkmcnt(7)\n"
"Qmvaa 252,112,216\nbuffer_store_dwordx4 v[224:227], v60, s[24:27], s68 offen offset:0 sc1\nQmvaa 252,132,48\nQda 52,53,12288\nQmvaa 100,116,216\n.Lpg22:\ns_cbranch_execz .Lpg26\nQta 184,42,41984\n"
"Qmvaa 100,136,48\nQta 186,42,44544\nQmvaa 132,120,216\nQta 96,40,42496\nQmvaa 132,140,48\nQta 98,40,45056\nQmvaa 164,124,216\nQta 188,42,42496\n"
"Qmvaa 164,144,48\nds_read_b32 v66, v74\nQmvaa 196,128,216\ns_waitcnt vmcnt(11)\nds_write_b128 v52, a[244:247] offset:37888\nQmvaa 196,148,48\ns_waitcnt lgkmcnt(7)\nQmvaa 228,112,220\n"
"Qta 190,42,45056\nQmvaa 228,132,52\ns_waitcnt lgkmcnt(2)\nv_cmp_gt_u32 vcc, v75, v66\nQmvaa 108,116,220\ns_cbranch_vccnz .Lso61r0\n.Lro61r0:\nQmvaa 108,136,52\n"
"Qda 56,53,16384\nQmvaa 140,120,220\nQta 100,40,43008\nQmvaa 140,140,52\nQta 102,40,45568\nQmvaa 172,124,220\nQta 192,42,43008\nQmvaa 172,144,52\n"
"s_waitcnt vmcnt(10)\nds_write_b128 v52, a[248:251] offset:41984\nQmvaa 204,128,220\nQmvaa 204,148,52\nQta 194,42,45568\ns_waitcnt lgkmcnt(5)\nQmvaa 236,112,224\nQmvaa 236,132,56\n"
"Qda 60,53,20480\nQmvaa 80,116,224\nQtv 0,41,0\nQmvaa 80,136,56\nQtv 2,41,2560\nQmvaa 112,120,224\nQtv 20,43,0\nQmvaa 112,140,56\n"
"Qmvaa 144,124,224\nQtv 22,43,2560\nQmvaa 144,144,56\ns_waitcnt vmcnt(9)\nds_write_b128 v52, a[252:255] offset:46080\nQmvaa 176,128,224\nQmvaa 176,148,56\nQtv 4,41,512\n"
"s_waitcnt lgkmcnt(6)\nQmvaa 92,84,228\nQmvaa 92,176,60\nQda 48,53,25600\nQmvaa 124,88,228\nQtv 6,41,3072\nQmvaa 124,180,60\nQtv 24,43,512\n"
"Qmvaa 156,92,228\nQtv 26,43,3072\nQmvaa 156,184,60\nQmvaa 188,96,228\nQtv 8,41,1024\nQmvaa 188,188,60\ns_waitcnt vmcnt(8)\nds_write_b128 v52, a[168:171] offset:50176\n"
"Qmvaa 220,100,228\nv_add_u32 v75, 1, v75\nQmvaa 220,192,60\nds_write_b32 v73, v75\ns_waitcnt lgkmcnt(6)\nQmvaa 252,84,232\nQtv 10,41,3584\nQmvaa 252,176,48\n"
"Qda 52,53,29696\nQmvaa 100,88,232\nQmvaa 100,180,48\nQtv 28,43,1024\nQmvaa 132,92,232\nQtv 30,43,3584\nQmvaa 132,184,48\nQtv 12,41,1536\n"
"Qmvaa 164,96,232\nQmvaa 164,188,48\nds_read_b32 v66, v74\nQmvaa 196,100,232\nQtv 14,41,4096\nQmvaa 196,192,48\nQtv 32,43,1536\ns_waitcnt lgkmcnt(6)\n"
"Qmvaa 228,84,236\nQmvaa 228,176,52\ns_waitcnt lgkmcnt(2)\nv_cmp_gt_u32 vcc, v75, v66\nQmvaa 108,88,236\ns_cbranch_vccnz .Lso62r0\n.Lro62r0:\nQmvaa 108,180,52\n"
"Qda 56,53,33792\nQmvaa 140,92,236\nQtv 34,43,4096\nQmvaa 140,184,52\nQtv 16,41,2048\nQmvaa 172,96,236\nQtv 18,41,4608\nQmvaa 172,188,52\n"
"Qmvaa 204,100,236\nQtv 36,43,2048\nQmvaa 204,192,52\nQtv 38,43,4608\ns_waitcnt lgkmcnt(5)\nQmvaa 236,84,240\nQmvaa 236,176,56\nQda 60,53,37888\n"
"Qmvaa 80,88,240\nQmvaa 80,180,56\nQmvaa 112,92,240\nQmvaa 112,184,56\nQmvaa 144,96,240\nQmvaa 144,188,56\nQmvaa 176,100,240\nv_add_u32 v75, 1, v75\n"
"Qmvaa 176,192,56\nds_write_b32 v73, v75\ns_waitcnt lgkmcnt(1)\nQmvva 92,0,244\nQmvva 92,20,60\nQda 48,53,41984\nQmvva 124,4,244\nQmvva 124,24,60\n"
"Qmvva 156,8,244\nQmvva 156,28,60\nQmvva 188,12,244\nQmvva 188,32,60\nds_read_b32 v66, v74\nQmvva 220,16,244\nQmvva 220,36,60\ns_waitcnt lgkmcnt(1)\n"
"Qmvva 252,0,248\nQmvva 252,20,48\ns_waitcnt lgkmcnt(0)\nv_cmp_gt_u32 vcc, v75, v66\nQmvva 100,4,248\ns_cbranch_vccnz .Lso63r0\n.Lro63r0:\nQmvva 100,24,48\n"
"Qda 52,53,46080\nQmvva 132,8,248\nQmvva 132,28,48\nQmvva 164,12,248\nQmvva 164,32,48\nQmvva 196,16,248\nQmvva 196,36,48\ns_waitcnt lgkmcnt(0)\n"
"Qmvva 228,0,252\ns_add_u32 s69, s98, 0\nbuffer_load_dwordx4 v[240:243], v60, s[24:27], s69 offen offset:0 sc1\nQmvva 228,20,52\nQda 56,53,50176\nQmvva 108,4,252\nbuffer_load_dwordx4 v[88:91], v60, s[24:27], s69 offen offset:1024 sc1\nQmvva 108,24,52\n"
"Qmvva 140,8,252\nbuffer_load_dwordx4 v[152:155], v60, s[24:27], s69 offen offset:2048 sc1\nQmvva 140,28,52\nQmvva 172,12,252\nbuffer_load_dwordx4 v[216:219], v60, s[24:27], s69 offen offset:3072 sc1\nQmvva 172,32,52\nQmvva 204,16,252\ns_add_u32 s69, s98, 4096\n"
"buffer_load_dwordx4 v[116:119], v60, s[24:27], s69 offen offset:0 sc1\nQmvva 204,36,52\ns_waitcnt lgkmcnt(0)\nQmvva 236,0,168\nbuffer_load_dwordx4 v[128:131], v60, s[24:27], s69 offen offset:1024 sc1\nQmvva 236,20,56\nQmvva 80,4,168\nbuffer_load_dwordx4 v[192:195], v60, s[24:27], s69 offen offset:2048 sc1\n"
"Qmvva 80,24,56\nQmvva 112,8,168\ns_add_u32 s69, s98, 14336\nbuffer_load_dwordx4 v[148:151], v60, s[24:27], s69 offen offset:0 sc1\nQmvva 112,28,56\nQmvva 144,12,168\nbuffer_load_dwordx4 v[180:183], v60, s[24:27], s69 offen offset:1024 sc1\nQmvva 144,32,56\n"
"Qmvva 176,16,168\nbuffer_load_dwordx4 v[104:107], v60, s[24:27], s69 offen offset:2048 sc1\nQmvva 176,36,56\ns_barrier\ns_mov_b32 s97, 0\n.Lrx0:\ns_waitcnt vmcnt(0)\nv_max_u32 v40, v240, v241\n"
"v_max3_u32 v40, v40, v242, v243\nv_max3_u32 v40, v40, v88, v89\nv_max3_u32 v40, v40, v90, v91\nv_max3_u32 v40, v40, v152, v153\nv_max3_u32 v40, v40, v154, v155\nv_max3_u32 v40, v40, v216, v217\nv_max3_u32 v40, v40, v218, v219\nv_max3_u32 v40, v40, v116, v117\n"
"v_max3_u32 v40, v40, v118, v119\nv_max3_u32 v40, v40, v128, v129\nv_max3_u32 v40, v40, v130, v131\nv_max3_u32 v40, v40, v192, v193\nv_max3_u32 v40, v40, v194, v195\nv_max3_u32 v40, v40, v148, v149\nv_max3_u32 v40, v40, v150, v151\nv_max3_u32 v40, v40, v180, v181\n"
"v_max3_u32 v40, v40, v182, v183\nv_max3_u32 v40, v40, v104, v105\nv_max3_u32 v40, v40, v106, v107\nv_cmp_eq_u32 vcc, -1, v40\ns_cmp_eq_u64 vcc, 0\ns_cbranch_scc1 .Lrx0_ok\ns_add_u32 s97, s97, 1\ns_cmp_gt_u32 s97, 1048576\n"
"s_cbranch_scc1 .Lrx0_ok\ns_add_u32 s69, s98, 0\nbuffer_load_dwordx4 v[240:243], v60, s[24:27], s69 offen offset:0 sc1\nbuffer_load_dwordx4 v[88:91], v60, s[24:27], s69 offen offset:1024 sc1\nbuffer_load_dwordx4 v[152:155], v60, s[24:27], s69 offen offset:2048 sc1\nbuffer_load_dwordx4 v[216:219], v60, s[24:27], s69 offen offset:3072 sc1\ns_add_u32 s69, s98, 4096\nbuffer_load_dwordx4 v[116:119], v60, s[24:27], s69 offen offset:0 sc1\n"
"buffer_load_dwordx4 v[128:131], v60, s[24:27], s69 offen offset:1024 sc1\nbuffer_load_dwordx4 v[192:195], v60, s[24:27], s69 offen offset:2048 sc1\ns_add_u32 s69, s98, 14336\nbuffer_load_dwordx4 v[148:151], v60, s[24:27], s69 offen offset:0 sc1\nbuffer_load_dwordx4 v[180:183], v60, s[24:27], s69 offen offset:1024 sc1\nbuffer_load_dwordx4 v[104:107], v60, s[24:27], s69 offen offset:2048 sc1\ns_branch .Lrx0\n.Lrx0_ok:\n"
"v_lshlrev_b32 v41, 16, v240\nv_and_b32 v42, 0xffff0000, v240\nQa 92,92,41\nQa 93,93,42\nv_lshlrev_b32 v43, 16, v241\nv_and_b32 v44, 0xffff0000, v241\nQa 94,94,43\nQa 95,95,44\n"
"Qk 92,92,93\nQk 93,94,95\nds_write_b64 v63, v[92:93] offset:0\nv_lshlrev_b32 v41, 16, v242\nv_and_b32 v42, 0xffff0000, v242\nQa 124,124,41\nQa 125,125,42\nv_lshlrev_b32 v43, 16, v243\n"
"v_and_b32 v44, 0xffff0000, v243\nQa 126,126,43\nQa 127,127,44\nQk 124,124,125\nQk 125,126,127\nds_write_b64 v63, v[124:125] offset:64\nv_lshlrev_b32 v41, 16, v88\nv_and_b32 v42, 0xffff0000, v88\n"
"Qa 156,156,41\nQa 157,157,42\nv_lshlrev_b32 v43, 16, v89\nv_and_b32 v44, 0xffff0000, v89\nQa 158,158,43\nQa 159,159,44\nQk 156,156,157\nQk 157,158,159\n"
"ds_write_b64 v63, v[156:157] offset:128\nv_lshlrev_b32 v41, 16, v90\nv_and_b32 v42, 0xffff0000, v90\nQa 188,188,41\nQa 189,189,42\nv_lshlrev_b32 v43, 16, v91\nv_and_b32 v44, 0xffff0000, v91\nQa 190,190,43\n"
"Qa 191,191,44\nQk 188,188,189\nQk 189,190,191\nds_write_b64 v63, v[188:189] offset:192\nv_lshlrev_b32 v41, 16, v152\nv_and_b32 v42, 0xffff0000, v152\nQa 220,220,41\nQa 221,221,42\n"
"v_lshlrev_b32 v43, 16, v153\nv_and_b32 v44, 0xffff0000, v153\nQa 222,222,43\nQa 223,223,44\nQk 220,220,221\nQk 221,222,223\nds_write_b64 v63, v[220:221] offset:256\nv_lshlrev_b32 v41, 16, v154\n"
"v_and_b32 v42, 0xffff0000, v154\nQa 252,252,41\nQa 253,253,42\nv_lshlrev_b32 v43, 16, v155\nv_and_b32 v44, 0xffff0000, v155\nQa 254,254,43\nQa 255,255,44\nQk 252,252,253\n"
"Qk 253,254,255\nds_write_b64 v63, v[252:253] offset:5376\nv_lshlrev_b32 v41, 16, v216\nv_and_b32 v42, 0xffff0000, v216\nQa 100,100,41\nQa 101,101,42\nv_lshlrev_b32 v43, 16, v217\nv_and_b32 v44, 0xffff0000, v217\n"
"Qa 102,102,43\nQa 103,103,44\nQk 100,100,101\nQk 101,102,103\nds_write_b64 v63, v[100:101] offset:5440\nv_lshlrev_b32 v41, 16, v218\nv_and_b32 v42, 0xffff0000, v218\nQa 132,132,41\n"
"Qa 133,133,42\nv_lshlrev_b32 v43, 16, v219\nv_and_b32 v44, 0xffff0000, v219\nQa 134,134,43\nQa 135,135,44\nQk 132,132,133\nQk 133,134,135\nds_write_b64 v63, v[132:133] offset:5504\n"
"v_lshlrev_b32 v41, 16, v116\nv_and_b32 v42, 0xffff0000, v116\nQa 164,164,41\nQa 165,165,42\nv_lshlrev_b32 v43, 16, v117\nv_and_b32 v44, 0xffff0000, v117\nQa 166,166,43\nQa 167,167,44\n"
"Qk 164,164,165\nQk 165,166,167\nds_write_b64 v63, v[164:165] offset:5568\nv_lshlrev_b32 v41, 16, v118\nv_and_b32 v42, 0xffff0000, v118\nQa 196,196,41\nQa 197,197,42\nv_lshlrev_b32 v43, 16, v119\n"
"v_and_b32 v44, 0xffff0000, v119\nQa 198,198,43\nQa 199,199,44\nQk 196,196,197\nQk 197,198,199\nds_write_b64 v63, v[196:197] offset:5632\nv_lshlrev_b32 v41, 16, v128\nv_and_b32 v42, 0xffff0000, v128\n"
"Qa 228,228,41\nQa 229,229,42\nv_lshlrev_b32 v43, 16, v129\nv_and_b32 v44, 0xffff0000, v129\nQa 230,230,43\nQa 231,231,44\nQk 228,228,229\nQk 229,230,231\n"
"ds_write_b64 v63, v[228:229] offset:10752\nv_lshlrev_b32 v41, 16, v130\nv_and_b32 v42, 0xffff0000, v130\nQa 108,108,41\nQa 109,109,42\nv_lshlrev_b32 v43, 16, v131\nv_and_b32 v44, 0xffff0000, v131\nQa 110,110,43\n"
"Qa 111,111,44\nQk 108,108,109\nQk 109,110,111\nds_write_b64 v63, v[108:109] offset:10816\nv_lshlrev_b32 v41, 16, v192\nv_and_b32 v42, 0xffff0000, v192\nQa 140,140,41\nQa 141,141,42\n"
"v_lshlrev_b32 v43, 16, v193\nv_and_b32 v44, 0xffff0000, v193\nQa 142,142,43\nQa 143,143,44\nQk 140,140,141\nQk 141,142,143\nds_write_b64 v63, v[140:141] offset:10880\nv_lshlrev_b32 v41, 16, v194\n"
"v_and_b32 v42, 0xffff0000, v194\nQa 172,172,41\nQa 173,173,42\nv_lshlrev_b32 v43, 16, v195\nv_and_b32 v44, 0xffff0000, v195\nQa 174,174,43\nQa 175,175,44\nQk 172,172,173\n"
"Qk 173,174,175\nds_write_b64 v63, v[172:173] offset:10944\nv_lshlrev_b32 v41, 16, v148\nv_and_b32 v42, 0xffff0000, v148\nQa 204,204,41\nQa 205,205,42\nv_lshlrev_b32 v43, 16, v149\nv_and_b32 v44, 0xffff0000, v149\n"
"Qa 206,206,43\nQa 207,207,44\nQk 204,204,205\nQk 205,206,207\nds_write_b64 v63, v[204:205] offset:11008\nv_lshlrev_b32 v41, 16, v150\nv_and_b32 v42, 0xffff0000, v150\nQa 236,236,41\n"
"Qa 237,237,42\nv_lshlrev_b32 v43, 16, v151\nv_and_b32 v44, 0xffff0000, v151\nQa 238,238,43\nQa 239,239,44\nQk 236,236,237\nQk 237,238,239\nds_write_b64 v63, v[236:237] offset:16128\n"
"v_lshlrev_b32 v41, 16, v180\nv_and_b32 v42, 0xffff0000, v180\nQa 80,80,41\nQa 81,81,42\nv_lshlrev_b32 v43, 16, v181\nv_and_b32 v44, 0xffff0000, v181\nQa 82,82,43\nQa 83,83,44\n"
"Qk 80,80,81\nQk 81,82,83\nds_write_b64 v63, v[80:81] offset:16192\nv_lshlrev_b32 v41, 16, v182\nv_and_b32 v42, 0xffff0000, v182\nQa 112,112,41\nQa 113,113,42\nv_lshlrev_b32 v43, 16, v183\n"
"v_and_b32 v44, 0xffff0000, v183\nQa 114,114,43\nQa 115,115,44\nQk 112,112,113\nQk 113,114,115\nds_write_b64 v63, v[112:113] offset:16256\nv_lshlrev_b32 v41, 16, v104\nv_and_b32 v42, 0xffff0000, v104\n"
"Qa 144,144,41\nQa 145,145,42\nv_lshlrev_b32 v43, 16, v105\nv_and_b32 v44, 0xffff0000, v105\nQa 146,146,43\nQa 147,147,44\nQk 144,144,145\nQk 145,146,147\n"
"ds_write_b64 v63, v[144:145] offset:16320\nv_lshlrev_b32 v41, 16, v106\nv_and_b32 v42, 0xffff0000, v106\nQa 176,176,41\nQa 177,177,42\nv_lshlrev_b32 v43, 16, v107\nv_and_b32 v44, 0xffff0000, v107\nQa 178,178,43\n"
"Qa 179,179,44\nQk 176,176,177\nQk 177,178,179\nds_write_b64 v63, v[176:177] offset:16384\ns_branch .Lfin\n.Lrole1:\n.Lpg23:\ns_cbranch_execz .Lpg27\n"
"s_waitcnt lgkmcnt(4)\nQmvaa 236,112,168\nQta 186,42,44544\nQmvaa 236,132,56\nQda 60,53,0\nQmvaa 80,116,168\nQta 96,40,42496\nQmvaa 80,136,56\n"
"s_add_u32 s31, s75, 32768\nQmvaa 112,120,168\nbuffer_load_dwordx4 a[240:243], v60, s[48:51], s31 offen offset:0 sc1\ns_add_u32 s95, s67, 125184\nQmvaa 112,140,56\nbuffer_load_dwordx4 v62, s[52:55], s95 offen offset:2048 lds\nQmvaa 144,124,168\nQta 98,40,45056\n"
"Qmvaa 144,144,56\nds_read_b32 v66, v74\nQmvaa 176,128,168\ns_waitcnt vmcnt(28)\nds_write_b128 v52, a[20:23] offset:25600\nQmvaa 176,148,56\ns_waitcnt lgkmcnt(4)\nQmvaa 208,112,172\n"
"Qta 188,42,42496\nQmvaa 208,132,60\ns_waitcnt lgkmcnt(2)\nv_cmp_gt_u32 vcc, v75, v66\nQmvaa 240,116,172\ns_cbranch_vccnz .Lso49r1\n.Lro49r1:\nQmvaa 240,136,60\n"
"Qda 48,53,4096\nQmvaa 84,120,172\nbuffer_load_dwordx4 a[244:247], v60, s[48:51], s31 offen offset:1024 sc1\ns_add_u32 s95, s67, 144448\nQmvaa 84,140,60\nbuffer_load_dwordx4 v61, s[52:55], s95 offen offset:3072 lds\nQmvaa 88,124,172\nQmvaa 88,144,60\n"
"s_waitcnt vmcnt(28)\nds_write_b128 v52, a[24:27] offset:29696\nQmvaa 120,128,172\nQmvaa 120,148,60\nQta 190,42,45056\ns_waitcnt lgkmcnt(2)\nQmvaa 152,112,0\nQta 100,40,43008\n"
"Qmvaa 152,132,48\nQda 52,53,8192\nQmvaa 184,116,0\nQta 102,40,45568\nQmvaa 184,136,48\nQmvaa 216,120,0\nbuffer_load_dwordx4 a[248:251], v60, s[48:51], s31 offen offset:2048 sc1\ns_add_u32 m0, s96, 28672\n"
"s_add_u32 s95, s67, 147648\nQmvaa 216,140,48\nbuffer_load_dwordx4 v61, s[52:55], s95 offen lds\nQmvaa 248,124,0\nQta 192,42,43008\nQmvaa 248,144,48\ns_waitcnt vmcnt(28)\nds_write_b128 v52, a[28:31] offset:33792\n"
"Qmvaa 116,128,0\nQmvaa 116,148,48\nQta 194,42,45568\ns_waitcnt lgkmcnt(4)\nQmvaa 96,112,4\nQmvaa 96,132,52\nQda 56,53,12288\nQmvaa 128,116,4\n"
"Qtv 0,41,0\nQmvaa 128,136,52\nQtv 2,41,2560\ns_add_u32 s31, s77, 28672\nQmvaa 160,120,4\nbuffer_load_dwordx4 a[252:255], v60, s[48:51], s31 offen offset:0 sc1\nQmvaa 160,140,52\nQtv 20,43,0\n"
"Qmvaa 192,124,4\nQmvaa 192,144,52\ns_waitcnt vmcnt(27)\nds_write_b128 v52, a[32:35] offset:37888\nQmvaa 224,128,4\nv_add_u32 v75, 1, v75\nQmvaa 224,148,52\nds_write_b32 v73, v75\n"
"s_waitcnt lgkmcnt(5)\nQmvaa 92,84,8\nQtv 22,43,2560\nQmvaa 92,176,56\nQda 60,53,16384\nQmvaa 124,88,8\nQtv 4,41,512\nQmvaa 124,180,56\n"
"Qmvaa 156,92,8\nbuffer_load_dwordx4 a[168:171], v60, s[48:51], s31 offen offset:1024 sc1\nQmvaa 156,184,56\nQtv 6,41,3072\nQmvaa 188,96,8\nQtv 24,43,512\nQmvaa 188,188,56\nds_read_b32 v66, v74\n"
"Qmvaa 220,100,8\ns_waitcnt vmcnt(26)\nds_write_b128 v52, a[36:39] offset:41984\nQmvaa 220,192,56\ns_waitcnt lgkmcnt(5)\nQmvaa 252,84,12\nQmvaa 252,176,60\ns_waitcnt lgkmcnt(1)\n"
"v_cmp_gt_u32 vcc, v75, v66\nQmvaa 100,88,12\ns_cbranch_vccnz .Lso50r1\n.Lro50r1:\nQmvaa 100,180,60\nQda 48,53,20480\nQmvaa 132,92,12\nbuffer_load_dwordx4 a[172:175], v60, s[48:51], s31 offen offset:2048 sc1\n"
"s_add_u32 m0, s96, 45056\ns_add_u32 s95, s72, 126976\nQmvaa 132,184,60\nbuffer_load_dwordx4 v61, s[52:55], s95 offen lds\nQmvaa 164,96,12\nQtv 26,43,3072\nQmvaa 164,188,60\ns_waitcnt vmcnt(26)\n"
"ds_write_b128 v52, a[40:43] offset:46080\nQmvaa 196,100,12\nQmvaa 196,192,60\nQtv 8,41,1024\ns_waitcnt lgkmcnt(3)\nQmvaa 228,84,16\nQmvaa 228,176,48\nQda 52,53,25600\n"
"Qmvaa 108,88,16\nQtv 10,41,3584\nQmvaa 108,180,48\nQtv 28,43,1024\nQmvaa 140,92,16\nbuffer_load_dwordx4 a[0:3], v60, s[48:51], s31 offen offset:3072 sc1\ns_add_u32 s95, s72, 126080\nQmvaa 140,184,48\n"
"buffer_load_dwordx4 v61, s[52:55], s95 offen offset:1024 lds\n.Lpg24:\ns_cbranch_execz .Ltr8\nQmvaa 172,96,16\nQtv 30,43,3584\nQmvaa 172,188,48\ns_waitcnt vmcnt(27)\nds_write_b128 v52, a[44:47] offset:50176\n"
"Qmvaa 204,100,16\nv_add_u32 v75, 1, v75\nQmvaa 204,192,48\ns_waitcnt vmcnt(31)\nds_write_b32 v73, v75\ns_waitcnt lgkmcnt(5)\nQmvaa 236,84,20\nQmvaa 236,176,52\n"
"Qda 56,53,29696\nQmvaa 80,88,20\nQtv 12,41,1536\nQmvaa 80,180,52\nQtv 14,41,4096\ns_add_u32 s31, s74, 43008\nQmvaa 112,92,20\nbuffer_load_dwordx4 a[4:7], v60, s[48:51], s31 offen offset:0 sc1\n"
"s_add_u32 s95, s72, 125184\nQmvaa 112,184,52\nbuffer_load_dwordx4 v62, s[52:55], s95 offen offset:2048 lds\nQmvaa 144,96,20\nQmvaa 144,188,52\nds_read_b32 v66, v74\nQmvaa 176,100,20\ns_waitcnt vmcnt(27)\n"
"ds_write_b128 v52, a[208:211] offset:0\nQmvaa 176,192,52\ns_waitcnt lgkmcnt(4)\nQmvaa 208,84,24\nQtv 32,43,1536\nQmvaa 208,176,56\ns_waitcnt lgkmcnt(2)\nv_cmp_gt_u32 vcc, v75, v66\n"
"Qmvaa 240,88,24\ns_cbranch_vccnz .Lso51r1\n.Lro51r1:\nQmvaa 240,180,56\nQda 60,53,33792\nQmvaa 84,92,24\nbuffer_load_dwordx4 a[8:11], v60, s[48:51], s31 offen offset:1024 sc1\ns_add_u32 s95, s72, 144448\n"
"Qmvaa 84,184,56\nbuffer_load_dwordx4 v61, s[52:55], s95 offen offset:3072 lds\nQmvaa 88,96,24\nQtv 34,43,4096\nQmvaa 88,188,56\ns_waitcnt vmcnt(27)\nds_write_b128 v52, a[212:215] offset:4096\nQmvaa 120,100,24\n"
"Qmvaa 120,192,56\ns_waitcnt lgkmcnt(2)\nQmvaa 152,84,28\nQtv 16,41,2048\nQmvaa 152,176,60\nQda 48,53,37888\nQmvaa 184,88,28\nQtv 18,41,4608\n"
"Qmvaa 184,180,60\nQmvaa 216,92,28\nbuffer_load_dwordx4 a[12:15], v60, s[48:51], s31 offen offset:2048 sc1\ns_add_u32 m0, s96, 49152\ns_add_u32 s95, s72, 147648\nQmvaa 216,184,60\nbuffer_load_dwordx4 v61, s[52:55], s95 offen lds\nQmvaa 248,96,28\n"
"Qtv 36,43,2048\nQmvaa 248,188,60\ns_waitcnt vmcnt(27)\nds_write_b128 v52, a[216:219] offset:8192\nQmvaa 116,100,28\nQmvaa 116,192,60\nQtv 38,43,4608\ns_waitcnt lgkmcnt(4)\n"
"Qmvaa 96,84,32\nQmvaa 96,176,48\nQda 52,53,41984\nQmvaa 128,88,32\nQta 112,41,20480\nQmvaa 128,180,48\nQta 114,41,23040\nQmvaa 160,92,32\n"
"buffer_load_dwordx4 a[16:19], v60, s[48:51], s31 offen offset:3072 sc1\nQmvaa 160,184,48\nQta 132,43,20480\nQmvaa 192,96,32\nQmvaa 192,188,48\ns_waitcnt vmcnt(26)\nds_write_b128 v52, a[220:223] offset:12288\nQmvaa 224,100,32\n"
"v_add_u32 v75, 1, v75\nQmvaa 224,192,48\nds_write_b32 v73, v75\ns_waitcnt lgkmcnt(5)\nQmvva 92,0,36\nQta 134,43,23040\nQmvva 92,20,52\nQda 56,53,46080\n"
"Qmvva 124,4,36\nQta 116,41,20992\nQmvva 124,24,52\ns_add_u32 s31, s75, 43008\nQmvva 156,8,36\nbuffer_load_dwordx4 a[20:23], v60, s[48:51], s31 offen offset:0 sc1\nQmvva 156,28,52\nQta 118,41,23552\n"
"Qmvva 188,12,36\nQta 136,43,20992\nQmvva 188,32,52\nds_read_b32 v66, v74\nQmvva 220,16,36\ns_waitcnt vmcnt(25)\nds_write_b128 v52, a[224:227] offset:16384\nQmvva 220,36,52\n"
"s_waitcnt lgkmcnt(5)\nQmvva 252,0,40\nQmvva 252,20,56\ns_waitcnt lgkmcnt(1)\nv_cmp_gt_u32 vcc, v75, v66\nQmvva 100,4,40\ns_cbranch_vccnz .Lso52r1\n.Lro52r1:\n"
"Qmvva 100,24,56\nQda 60,53,50176\nQmvva 132,8,40\nbuffer_load_dwordx4 a[24:27], v60, s[48:51], s31 offen offset:1024 sc1\ns_add_u32 m0, s96, 65536\ns_add_u32 s95, s73, 126976\nQmvva 132,28,56\nbuffer_load_dwordx4 v61, s[52:55], s95 offen lds\n"
"Qmvva 164,12,40\nQta 138,43,23552\nQmvva 164,32,56\ns_waitcnt vmcnt(26)\nds_write_b128 v52, a[228:231] offset:20480\nQmvva 196,16,40\nQmvva 196,36,56\nQta 120,41,21504\n"
"s_waitcnt lgkmcnt(3)\nQmvva 228,0,44\nQmvva 228,20,60\nQda 48,53,0\nQmvva 108,4,44\nQta 122,41,24064\nQmvva 108,24,60\nQta 140,43,21504\n"
"Qmvva 140,8,44\nbuffer_load_dwordx4 a[28:31], v60, s[48:51], s31 offen offset:2048 sc1\ns_add_u32 s95, s73, 126080\nQmvva 140,28,60\nbuffer_load_dwordx4 v61, s[52:55], s95 offen offset:1024 lds\nQmvva 172,12,44\nQta 142,43,24064\nQmvva 172,32,60\n"
"s_waitcnt vmcnt(27)\nds_write_b128 v52, a[232:235] offset:25600\nQmvva 204,16,44\nv_add_u32 v75, 1, v75\nQmvva 204,36,60\ns_waitcnt vmcnt(30)\nds_write_b32 v73, v75\ns_waitcnt lgkmcnt(5)\n"
"Qmvva 236,0,208\nQmvva 236,20,48\nQda 52,53,4096\nQmvva 80,4,208\nQta 124,41,22016\nQmvva 80,24,48\nQta 126,41,24576\nQmvva 112,8,208\n"
"buffer_load_dwordx4 a[32:35], v60, s[48:51], s31 offen offset:3072 sc1\ns_add_u32 s95, s73, 125184\nQmvva 112,28,48\nbuffer_load_dwordx4 v62, s[52:55], s95 offen offset:2048 lds\nQmvva 144,12,208\nQmvva 144,32,48\nds_read_b32 v66, v74\nQmvva 176,16,208\n"
"s_waitcnt vmcnt(27)\nds_write_b128 v52, a[236:239] offset:29696\nQmvva 176,36,48\ns_waitcnt lgkmcnt(4)\nQmvva 208,0,212\nQta 144,43,22016\nQmvva 208,20,52\ns_waitcnt lgkmcnt(2)\n"
"v_cmp_gt_u32 vcc, v75, v66\nQmvva 240,4,212\ns_cbranch_vccnz .Lso53r1\n.Lro53r1:\nQmvva 240,24,52\nQda 56,53,8192\ns_add_u32 s31, s77, 43008\nQmvva 84,8,212\n"
"buffer_load_dwordx4 a[36:39], v60, s[48:51], s31 offen offset:0 sc1\ns_add_u32 s95, s73, 144448\nQmvva 84,28,52\nbuffer_load_dwordx4 v61, s[52:55], s95 offen offset:3072 lds\nQmvva 88,12,212\nQta 146,43,24576\nQmvva 88,32,52\ns_waitcnt vmcnt(27)\n"
"ds_write_b128 v52, a[240:243] offset:33792\nQmvva 120,16,212\nQmvva 120,36,52\ns_waitcnt lgkmcnt(2)\nQmvva 152,0,216\nQta 128,41,22528\nQmvva 152,20,56\nQda 60,53,12288\n"
"Qmvva 184,4,216\nQta 130,41,25088\nQmvva 184,24,56\nQmvva 216,8,216\nbuffer_load_dwordx4 a[40:43], v60, s[48:51], s31 offen offset:1024 sc1\ns_add_u32 m0, s96, 69632\ns_add_u32 s95, s73, 147648\nQmvva 216,28,56\n"
"buffer_load_dwordx4 v61, s[52:55], s95 offen lds\nQmvva 248,12,216\nQta 148,43,22528\nQmvva 248,32,56\ns_waitcnt vmcnt(27)\nds_write_b128 v52, a[244:247] offset:37888\nQmvva 116,16,216\nQmvva 116,36,56\n"
"Qta 150,43,25088\ns_waitcnt lgkmcnt(4)\nQmvva 96,0,220\nQmvva 96,20,60\nQda 48,53,16384\nQmvva 128,4,220\nQta 84,40,0\nQmvva 128,24,60\n"
"Qta 86,40,2560\nQmvva 160,8,220\nbuffer_load_dwordx4 a[44:47], v60, s[48:51], s31 offen offset:2048 sc1\nQmvva 160,28,60\nQmvva 192,12,220\nQta 176,42,0\nQmvva 192,32,60\ns_waitcnt vmcnt(26)\n"
"ds_write_b128 v52, a[248:251] offset:41984\nQmvva 224,16,220\nv_add_u32 v75, 1, v75\nQmvva 224,36,60\nds_write_b32 v73, v75\ns_waitcnt lgkmcnt(5)\nQmvaa 92,112,224\nQmvaa 92,132,48\n"
"Qda 52,53,20480\nQmvaa 124,116,224\nQta 178,42,2560\nQmvaa 124,136,48\nQta 88,40,512\nQmvaa 156,120,224\nbuffer_load_dwordx4 a[208:211], v60, s[48:51], s31 offen offset:3072 sc1\nQmvaa 156,140,48\n"
"Qmvaa 188,124,224\nQta 90,40,3072\nQmvaa 188,144,48\nds_read_b32 v66, v74\nQmvaa 220,128,224\ns_waitcnt vmcnt(25)\nds_write_b128 v52, a[252:255] offset:46080\nQmvaa 220,148,48\n"
"s_waitcnt lgkmcnt(5)\nQmvaa 252,112,228\nQmvaa 252,132,52\ns_waitcnt lgkmcnt(1)\nv_cmp_gt_u32 vcc, v75, v66\nQmvaa 100,116,228\ns_cbranch_vccnz .Lso54r1\n.Lro54r1:\n"
"Qmvaa 100,136,52\nQda 56,53,25600\ns_add_u32 s31, s77, 32768\nQmvaa 132,120,228\nbuffer_load_dwordx4 a[212:215], v60, s[48:51], s31 offen offset:0 sc1\nQmvaa 132,140,52\nQta 180,42,512\nQmvaa 164,124,228\n"
"Qta 182,42,3072\nQmvaa 164,144,52\ns_waitcnt vmcnt(25)\nds_write_b128 v52, a[168:171] offset:50176\nQmvaa 196,128,228\nQmvaa 196,148,52\ns_waitcnt lgkmcnt(3)\nQmvaa 228,112,232\n"
"Qta 92,40,1024\nQmvaa 228,132,56\nQda 60,53,29696\nQmvaa 108,116,232\nQmvaa 108,136,56\nQta 94,40,3584\nQmvaa 140,120,232\nbuffer_load_dwordx4 a[216:219], v60, s[48:51], s31 offen offset:1024 sc1\n"
"Qmvaa 140,140,56\nQmvaa 172,124,232\nQta 184,42,1024\nQmvaa 172,144,56\ns_waitcnt vmcnt(25)\nds_write_b128 v52, a[172:175] offset:0\nQmvaa 204,128,232\nv_add_u32 v75, 1, v75\n"
"Qmvaa 204,148,56\ns_waitcnt vmcnt(28)\nds_write_b32 v73, v75\ns_waitcnt lgkmcnt(4)\nQmvaa 236,112,236\nQta 186,42,3584\nQmvaa 236,132,60\nQda 48,53,33792\n"
"Qmvaa 80,116,236\nQmvaa 80,136,60\nQta 96,40,1536\nQmvaa 112,120,236\nbuffer_load_dwordx4 a[220:223], v60, s[48:51], s31 offen offset:2048 sc1\nQmvaa 112,140,60\nQmvaa 144,124,236\nQta 98,40,4096\n"
"Qmvaa 144,144,60\nds_read_b32 v66, v74\nQmvaa 176,128,236\ns_waitcnt vmcnt(24)\nds_write_b128 v52, a[0:3] offset:4096\nQmvaa 176,148,60\ns_waitcnt lgkmcnt(4)\nQmvaa 208,112,240\n"
"Qta 188,42,1536\nQmvaa 208,132,48\ns_waitcnt lgkmcnt(2)\nv_cmp_gt_u32 vcc, v75, v66\nQmvaa 240,116,240\ns_cbranch_vccnz .Lso55r1\n.Lro55r1:\nQmvaa 240,136,48\n"
"Qda 52,53,37888\ns_add_u32 s31, s74, 47104\nQmvaa 84,120,240\nbuffer_load_dwordx4 a[224:227], v60, s[48:51], s31 offen offset:0 sc1\nQmvaa 84,140,48\nQmvaa 88,124,240\nQta 190,42,4096\nQmvaa 88,144,48\n"
"s_waitcnt vmcnt(23)\nds_write_b128 v52, a[4:7] offset:8192\nQmvaa 120,128,240\nQmvaa 120,148,48\ns_waitcnt lgkmcnt(2)\nQmvaa 152,112,244\nQta 100,40,2048\nQmvaa 152,132,52\n"
"Qda 56,53,41984\nQmvaa 184,116,244\nQta 102,40,4608\nQmvaa 184,136,52\nQmvaa 216,120,244\nbuffer_load_dwordx4 a[228:231], v60, s[48:51], s31 offen offset:1024 sc1\nQmvaa 216,140,52\nQta 192,42,2048\n"
"Qmvaa 248,124,244\nQmvaa 248,144,52\ns_waitcnt vmcnt(22)\nds_write_b128 v52, a[8:11] offset:12288\nQmvaa 116,128,244\nQmvaa 116,148,52\nQta 194,42,4608\ns_waitcnt lgkmcnt(4)\n"
"Qmvaa 96,112,248\nQmvaa 96,132,56\nQda 60,53,46080\nQmvaa 128,116,248\nQtv 0,40,20480\nQmvaa 128,136,56\nQtv 2,40,23040\nQmvaa 160,120,248\n"
"buffer_load_dwordx4 a[232:235], v60, s[48:51], s31 offen offset:2048 sc1\nQmvaa 160,140,56\nQtv 20,42,20480\nQmvaa 192,124,248\nQtv 22,42,23040\nQmvaa 192,144,56\ns_waitcnt vmcnt(21)\nds_write_b128 v52, a[12:15] offset:16384\n"
"Qmvaa 224,128,248\nv_add_u32 v75, 1, v75\nQmvaa 224,148,56\ns_waitcnt vmcnt(20)\nds_write_b32 v73, v75\ns_waitcnt lgkmcnt(6)\nQmvaa 92,84,252\nQtv 4,40,20992\n"
"Qmvaa 92,176,60\nQda 48,53,50176\nQmvaa 124,88,252\nQtv 6,40,23552\nQmvaa 124,180,60\nQtv 24,42,20992\ns_add_u32 s31, s75, 47104\nQmvaa 156,92,252\n"
"buffer_load_dwordx4 a[236:239], v60, s[48:51], s31 offen offset:0 sc1\nQmvaa 156,184,60\nQtv 26,42,23552\nQmvaa 188,96,252\nQtv 8,40,21504\nQmvaa 188,188,60\nds_read_b32 v66, v74\nQmvaa 220,100,252\n"
"s_waitcnt vmcnt(20)\nds_write_b128 v52, a[16:19] offset:20480\nQmvaa 220,192,60\ns_waitcnt lgkmcnt(6)\nQmvaa 252,84,168\nQtv 10,40,24064\nQmvaa 252,176,48\ns_waitcnt lgkmcnt(2)\n"
"v_cmp_gt_u32 vcc, v75, v66\nQmvaa 100,88,168\ns_cbranch_vccnz .Lso56r1\n.Lro56r1:\nQmvaa 100,180,48\nQda 52,53,0\nQmvaa 132,92,168\nbuffer_load_dwordx4 a[240:243], v60, s[48:51], s31 offen offset:1024 sc1\n"
"Qmvaa 132,184,48\nQmvaa 164,96,168\nQtv 28,42,21504\nQmvaa 164,188,48\ns_waitcnt vmcnt(20)\nds_write_b128 v52, a[20:23] offset:25600\nQmvaa 196,100,168\nQmvaa 196,192,48\n"
"Qtv 30,42,24064\ns_waitcnt lgkmcnt(3)\nQmvaa 228,84,172\nQtv 12,40,22016\nQmvaa 228,176,52\nQda 56,53,4096\nQmvaa 108,88,172\nQtv 14,40,24576\n"
"Qmvaa 108,180,52\nQtv 32,42,22016\nQmvaa 140,92,172\nbuffer_load_dwordx4 a[244:247], v60, s[48:51], s31 offen offset:2048 sc1\nQmvaa 140,184,52\nQtv 34,42,24576\nQmvaa 172,96,172\nQtv 16,40,22528\n"
"Qmvaa 172,188,52\ns_waitcnt vmcnt(20)\nds_write_b128 v52, a[24:27] offset:29696\nQmvaa 204,100,172\nv_add_u32 v75, 1, v75\nQmvaa 204,192,52\ns_waitcnt vmcnt(11)\nds_write_b32 v73, v75\n"
"s_waitcnt lgkmcnt(6)\nQmvaa 236,84,0\nQtv 18,40,25088\nQmvaa 236,176,56\nQda 60,53,8192\nQmvaa 80,88,0\nQtv 36,42,22528\nQmvaa 80,180,56\n"
"Qtv 38,42,25088\ns_add_u32 s31, s77, 47104\nQmvaa 112,92,0\nbuffer_load_dwordx4 a[248:251], v60, s[48:51], s31 offen offset:0 sc1\nQmvaa 112,184,56\nQta 112,40,40960\nQmvaa 144,96,0\nQta 114,40,43520\n"
"Qmvaa 144,188,56\nds_read_b32 v66, v74\nQmvaa 176,100,0\ns_waitcnt vmcnt(19)\nds_write_b128 v52, a[28:31] offset:33792\nQmvaa 176,192,56\ns_waitcnt lgkmcnt(6)\nQmvva 92,0,4\n"
"Qta 132,42,40960\nQmvva 92,20,60\ns_waitcnt lgkmcnt(2)\nv_cmp_gt_u32 vcc, v75, v66\nQmvva 124,4,4\ns_cbranch_vccnz .Lso57r1\n.Lro57r1:\nQmvva 124,24,60\n"
"Qda 48,53,12288\nQmvva 156,8,4\nbuffer_load_dwordx4 a[252:255], v60, s[48:51], s31 offen offset:1024 sc1\nQmvva 156,28,60\nQta 134,42,43520\nQmvva 188,12,4\nQta 116,40,41472\nQmvva 188,32,60\n"
"s_waitcnt vmcnt(18)\nds_write_b128 v52, a[32:35] offset:37888\nQmvva 220,16,4\nQmvva 220,36,60\nQta 118,40,44032\ns_waitcnt lgkmcnt(4)\nQmvva 252,0,8\nQta 136,42,41472\n"
"Qmvva 252,20,48\nQda 52,53,16384\nQmvva 100,4,8\nQmvva 100,24,48\nQta 138,42,44032\nQmvva 132,8,8\nbuffer_load_dwordx4 a[168:171], v60, s[48:51], s31 offen offset:2048 sc1\nQmvva 132,28,48\n"
"Qta 120,40,41984\nQmvva 164,12,8\nQta 122,40,44544\nQmvva 164,32,48\ns_waitcnt vmcnt(17)\nds_write_b128 v52, a[36:39] offset:41984\nQmvva 196,16,8\nQmvva 196,36,48\n"
"Qta 140,42,41984\ns_waitcnt lgkmcnt(5)\nQmvva 228,0,12\nQta 142,42,44544\nQmvva 228,20,52\nQda 56,53,20480\nQmvva 108,4,12\nQta 124,40,42496\n"
"Qmvva 108,24,52\nQta 126,40,45056\nQmvva 140,8,12\nQmvva 140,28,52\nQta 144,42,42496\nQmvva 172,12,12\nQta 146,42,45056\nQmvva 172,32,52\n"
"s_waitcnt vmcnt(15)\nds_write_b128 v52, a[40:43] offset:46080\nQmvva 204,16,12\nv_add_u32 v75, 1, v75\nQmvva 204,36,52\nds_write_b32 v73, v75\ns_waitcnt lgkmcnt(6)\nQmvva 236,0,16\n"
"Qta 128,40,43008\nQmvva 236,20,56\nQda 60,53,25600\nQmvva 80,4,16\nQta 130,40,45568\nQmvva 80,24,56\nQta 148,42,43008\nQmvva 112,8,16\n"
"Qta 150,42,45568\nQmvva 112,28,56\nQta 84,41,0\nQmvva 144,12,16\n.Lpg25:\ns_cbranch_execz .Ltr8\nQta 86,41,2560\nQmvva 144,32,56\n"
"ds_read_b32 v66, v74\nQmvva 176,16,16\ns_waitcnt vmcnt(13)\nds_write_b128 v52, a[44:47] offset:50176\nQmvva 176,36,56\ns_waitcnt lgkmcnt(7)\nQmvaa 92,112,20\nQta 176,43,0\n"
"Qmvaa 92,132,60\ns_waitcnt lgkmcnt(2)\nv_cmp_gt_u32 vcc, v75, v66\nQmvaa 124,116,20\ns_cbranch_vccnz .Lso58r1\n.Lro58r1:\nQmvaa 124,136,60\nQda 48,53,29696\n"
"Qmvaa 156,120,20\nQta 178,43,2560\nQmvaa 156,140,60\nQta 88,41,512\nQmvaa 188,124,20\nQta 90,41,3072\nQmvaa 188,144,60\ns_waitcnt vmcnt(12)\n"
"ds_write_b128 v52, a[208:211] offset:0\nQmvaa 220,128,20\nQmvaa 220,148,60\nQta 180,43,512\ns_waitcnt lgkmcnt(5)\nQmvaa 252,112,24\nQta 182,43,3072\nQmvaa 252,132,48\n"
"Qda 52,53,33792\nQmvaa 100,116,24\nQta 92,41,1024\nQmvaa 100,136,48\nQta 94,41,3584\nQmvaa 132,120,24\nQta 184,43,1024\nQmvaa 132,140,48\n"
"Qta 186,43,3584\nQmvaa 164,124,24\nQta 96,41,1536\nQmvaa 164,144,48\ns_waitcnt vmcnt(11)\nds_write_b128 v52, a[212:215] offset:4096\nQmvaa 196,128,24\nv_add_u32 v75, 1, v75\n"
"Qmvaa 196,148,48\nds_write_b32 v73, v75\ns_waitcnt lgkmcnt(7)\nQmvaa 228,112,28\nQta 98,41,4096\nQmvaa 228,132,52\nQda 56,53,37888\nQmvaa 108,116,28\n"
"Qta 188,43,1536\nQmvaa 108,136,52\nQta 190,43,4096\nQmvaa 140,120,28\nQta 100,41,2048\nQmvaa 140,140,52\nQta 102,41,4608\nQmvaa 172,124,28\n"
"Qta 192,43,2048\nQmvaa 172,144,52\nds_read_b32 v66, v74\nQmvaa 204,128,28\ns_waitcnt vmcnt(10)\nds_write_b128 v52, a[216:219] offset:8192\nQmvaa 204,148,52\ns_waitcnt lgkmcnt(7)\n"
"Qmvaa 236,112,32\nQta 194,43,4608\nQmvaa 236,132,56\ns_waitcnt lgkmcnt(2)\nv_cmp_gt_u32 vcc, v75, v66\nQmvaa 80,116,32\ns_cbranch_vccnz .Lso59r1\n.Lro59r1:\n"
"Qmvaa 80,136,56\nQda 60,53,41984\nQmvaa 112,120,32\nQtv 0,40,0\nQmvaa 112,140,56\nQtv 2,40,2560\nQmvaa 144,124,32\nQtv 20,42,0\n"
"Qmvaa 144,144,56\ns_waitcnt vmcnt(9)\nds_write_b128 v52, a[220:223] offset:12288\nQmvaa 176,128,32\nQmvaa 176,148,56\nQtv 22,42,2560\ns_waitcnt lgkmcnt(5)\nQmvaa 92,84,36\n"
"Qtv 4,40,512\nQmvaa 92,176,60\nQda 48,53,46080\ns_add_u32 s68, s64, 0\nQmvaa 124,88,36\nQmvaa 124,180,60\nQtv 6,40,3072\nQmvaa 156,92,36\n"
"Qtv 24,42,512\nQmvaa 156,184,60\nQtv 26,42,3072\nQmvaa 188,96,36\nQtv 8,40,1024\nQmvaa 188,188,60\ns_waitcnt vmcnt(8)\nds_write_b128 v52, a[224:227] offset:16384\n"
"Qmvaa 220,100,36\nQk 92,92,93\nv_min_u32 v92, -2, v92\nQmvaa 220,192,60\nQtv 10,40,3584\nQk 93,94,95\ns_waitcnt lgkmcnt(6)\nQmvaa 252,84,40\n"
"v_min_u32 v93, -2, v93\nQk 94,124,125\nQmvaa 252,176,48\nQda 52,53,50176\nv_min_u32 v94, -2, v94\nQmvaa 100,88,40\nQtv 28,42,1024\nQk 95,126,127\n"
"Qmvaa 100,180,48\nQtv 30,42,3584\nv_min_u32 v95, -2, v95\nQmvaa 132,92,40\nQtv 12,40,1536\nbuffer_store_dwordx4 v[92:95], v60, s[24:27], s68 offen offset:0 sc1\nQmvaa 132,184,48\nQtv 14,40,4096\n"
"Qk 156,156,157\nQmvaa 164,96,40\nQtv 32,42,1536\nv_min_u32 v156, -2, v156\nQmvaa 164,188,48\ns_waitcnt vmcnt(8)\nds_write_b128 v52, a[228:231] offset:20480\nQmvaa 196,100,40\n"
"v_add_u32 v75, 1, v75\nQk 157,158,159\nQmvaa 196,192,48\nds_write_b32 v73, v75\nv_min_u32 v157, -2, v157\ns_waitcnt lgkmcnt(7)\nQmvaa 228,84,44\nQk 158,188,189\n"
"v_min_u32 v158, -2, v158\nQmvaa 228,176,52\nQda 56,53,0\nQk 159,190,191\nQmvaa 108,88,44\nQtv 34,42,4096\nv_min_u32 v159, -2, v159\nQmvaa 108,180,52\n"
"Qtv 16,40,2048\nbuffer_store_dwordx4 v[156:159], v60, s[24:27], s68 offen offset:1024 sc1\nQmvaa 140,92,44\nQtv 18,40,4608\nQk 220,220,221\nQmvaa 140,184,52\nQtv 36,42,2048\nv_min_u32 v220, -2, v220\n"
"Qmvaa 172,96,44\nQtv 38,42,4608\nQk 221,222,223\nQmvaa 172,188,52\nds_read_b32 v66, v74\nv_min_u32 v221, -2, v221\nQmvaa 204,100,44\ns_waitcnt vmcnt(8)\n"
"ds_write_b128 v52, a[232:235] offset:25600\nQmvaa 204,192,52\nQk 222,252,253\nv_min_u32 v222, -2, v222\ns_waitcnt lgkmcnt(7)\nQmvaa 236,84,208\nQk 223,254,255\nv_min_u32 v223, -2, v223\n"
"Qmvaa 236,176,56\ns_waitcnt lgkmcnt(1)\nv_cmp_gt_u32 vcc, v75, v66\nQmvaa 80,88,208\nbuffer_store_dwordx4 v[220:223], v60, s[24:27], s68 offen offset:2048 sc1\ns_cbranch_vccnz .Lso60r1\n.Lro60r1:\nQmvaa 80,180,56\n"
"Qda 60,53,4096\nQk 100,100,101\nQmvaa 112,92,208\nQta 112,40,20480\nQta 114,40,23040\nQmvaa 112,184,56\nQta 132,42,20480\nv_min_u32 v100, -2, v100\n"
"Qmvaa 144,96,208\nQta 134,42,23040\nQk 101,102,103\nQmvaa 144,188,56\ns_waitcnt vmcnt(8)\nds_write_b128 v52, a[236:239] offset:29696\nQmvaa 176,100,208\nv_min_u32 v101, -2, v101\n"
"Qk 102,132,133\nQmvaa 176,192,56\nQta 116,40,20992\nv_min_u32 v102, -2, v102\ns_waitcnt lgkmcnt(6)\nQmvva 208,0,212\nQta 118,40,23552\nQk 103,134,135\n"
"Qmvva 208,20,60\nQda 48,53,8192\nv_min_u32 v103, -2, v103\nQmvva 240,4,212\nQta 136,42,20992\nbuffer_store_dwordx4 v[100:103], v60, s[24:27], s68 offen offset:3072 sc1\nQmvva 240,24,60\nQta 138,42,23552\n"
"Qk 164,164,165\nQmvva 84,8,212\nQta 120,40,21504\nv_min_u32 v164, -2, v164\nQmvva 84,28,60\nQta 122,40,24064\nQk 165,166,167\nQmvva 88,12,212\n"
"Qta 140,42,21504\nv_min_u32 v165, -2, v165\nQmvva 88,32,60\ns_waitcnt vmcnt(8)\nds_write_b128 v52, a[240:243] offset:33792\nQmvva 120,16,212\nv_add_u32 v75, 1, v75\nQk 166,196,197\n"
"Qmvva 120,36,60\nds_write_b32 v73, v75\nv_min_u32 v166, -2, v166\ns_waitcnt lgkmcnt(7)\nQmvva 152,0,216\nQta 142,42,24064\nQk 167,198,199\nQmvva 152,20,48\n"
"Qda 52,53,12288\nv_min_u32 v167, -2, v167\nQmvva 184,4,216\nQta 124,40,22016\ns_add_u32 s68, s64, 4096\nQmvva 184,24,48\nQta 126,40,24576\nbuffer_store_dwordx4 v[164:167], v60, s[24:27], s68 offen offset:0 sc1\n"
"Qmvva 216,8,216\nQta 144,42,22016\nQk 228,228,229\nQmvva 216,28,48\nQta 146,42,24576\nv_min_u32 v228, -2, v228\nQmvva 248,12,216\nQta 128,40,22528\n"
"Qk 229,230,231\nQmvva 248,32,48\nds_read_b32 v66, v74\nv_min_u32 v229, -2, v229\nQmvva 116,16,216\ns_waitcnt vmcnt(8)\nds_write_b128 v52, a[244:247] offset:37888\nQmvva 116,36,48\n"
"Qk 230,108,109\nv_min_u32 v230, -2, v230\ns_waitcnt lgkmcnt(7)\nQmvva 96,0,220\nQta 130,40,25088\nQk 231,110,111\nQmvva 96,20,52\ns_waitcnt lgkmcnt(2)\n"
"v_cmp_gt_u32 vcc, v75, v66\nQmvva 128,4,220\ns_cbranch_vccnz .Lso61r1\n.Lro61r1:\nv_min_u32 v231, -2, v231\nQmvva 128,24,52\nQda 56,53,16384\nbuffer_store_dwordx4 v[228:231], v60, s[24:27], s68 offen offset:1024 sc1\n"
"Qmvva 160,8,220\nQta 148,42,22528\nQk 140,140,141\nQmvva 160,28,52\nQta 150,42,25088\nv_min_u32 v140, -2, v140\nQmvva 192,12,220\nQta 84,40,40960\n"
"Qk 141,142,143\nQmvva 192,32,52\ns_waitcnt vmcnt(8)\nds_write_b128 v52, a[248:251] offset:41984\nQmvva 224,16,220\nv_min_u32 v141, -2, v141\nQk 142,172,173\nQmvva 224,36,52\n"
"Qta 86,40,43520\nv_min_u32 v142, -2, v142\ns_waitcnt lgkmcnt(3)\nQmvaa 208,112,224\nQta 176,42,40960\nQk 143,174,175\nQmvaa 208,132,56\nQda 60,53,20480\n"
"v_min_u32 v143, -2, v143\nQmvaa 240,116,224\nQta 178,42,43520\nbuffer_store_dwordx4 v[140:143], v60, s[24:27], s68 offen offset:2048 sc1\nQmvaa 240,136,56\nQta 88,40,41472\nQk 204,204,205\nQmvaa 84,120,224\n"
"Qta 90,40,44032\nv_min_u32 v204, -2, v204\nQmvaa 84,140,56\nQta 180,42,41472\nQk 205,206,207\nQmvaa 88,124,224\nQta 182,42,44032\nv_min_u32 v205, -2, v205\n"
"Qmvaa 88,144,56\ns_waitcnt vmcnt(8)\nds_write_b128 v52, a[252:255] offset:46080\nQmvaa 120,128,224\nQk 206,236,237\nv_min_u32 v206, -2, v206\nQmvaa 120,148,56\nQta 92,40,41984\n"
"Qk 207,238,239\ns_waitcnt lgkmcnt(7)\nQmvaa 152,112,228\nQta 94,40,44544\nv_min_u32 v207, -2, v207\nQmvaa 152,132,60\nQda 48,53,25600\ns_add_u32 s68, s64, 14336\n"
"Qmvaa 184,116,228\nQta 184,42,41984\nbuffer_store_dwordx4 v[204:207], v60, s[24:27], s68 offen offset:0 sc1\nQmvaa 184,136,60\nQta 186,42,44544\nQk 80,80,81\nQmvaa 216,120,228\nQta 96,40,42496\n"
"v_min_u32 v80, -2, v80\nQmvaa 216,140,60\nQta 98,40,45056\nQk 81,82,83\nQmvaa 248,124,228\nQta 188,42,42496\nv_min_u32 v81, -2, v81\nQmvaa 248,144,60\n"
"s_waitcnt vmcnt(8)\nds_write_b128 v52, a[168:171] offset:50176\nQmvaa 116,128,228\nv_add_u32 v75, 1, v75\nQk 82,112,113\nQmvaa 116,148,60\nds_write_b32 v73, v75\nv_min_u32 v82, -2, v82\n"
"s_waitcnt lgkmcnt(7)\nQmvaa 96,112,232\nQta 190,42,45056\nQk 83,114,115\nQmvaa 96,132,48\nQda 52,53,29696\nv_min_u32 v83, -2, v83\nQmvaa 128,116,232\n"
"Qta 100,40,43008\nbuffer_store_dwordx4 v[80:83], v60, s[24:27], s68 offen offset:1024 sc1\nQmvaa 128,136,48\nQta 102,40,45568\nQk 144,144,145\nQmvaa 160,120,232\nQta 192,42,43008\nv_min_u32 v144, -2, v144\n"
"Qmvaa 160,140,48\nQta 194,42,45568\nQk 145,146,147\nQmvaa 192,124,232\nQtv 0,41,0\nv_min_u32 v145, -2, v145\nQmvaa 192,144,48\nds_read_b32 v66, v74\n"
"Qk 146,176,177\nQmvaa 224,128,232\nQtv 2,41,2560\nv_min_u32 v146, -2, v146\nQmvaa 224,148,48\nQtv 20,43,0\nQk 147,178,179\ns_waitcnt lgkmcnt(8)\n"
"Qmvaa 208,84,236\nQtv 22,43,2560\nv_min_u32 v147, -2, v147\nQmvaa 208,176,52\ns_waitcnt lgkmcnt(3)\nv_cmp_gt_u32 vcc, v75, v66\nQmvaa 240,88,236\nbuffer_store_dwordx4 v[144:147], v60, s[24:27], s68 offen offset:2048 sc1\n"
"s_cbranch_vccnz .Lso62r1\n.Lro62r1:\nQmvaa 240,180,52\nQda 56,53,33792\nQmvaa 84,92,236\nQtv 4,41,512\nQmvaa 84,184,52\nQtv 6,41,3072\n"
"Qmvaa 88,96,236\nQtv 24,43,512\nQmvaa 88,188,52\nQmvaa 120,100,236\nQtv 26,43,3072\nQmvaa 120,192,52\nQtv 8,41,1024\ns_waitcnt lgkmcnt(5)\n"
"Qmvaa 152,84,240\nQtv 10,41,3584\nQmvaa 152,176,56\nQda 60,53,37888\nQmvaa 184,88,240\nQtv 28,43,1024\nQmvaa 184,180,56\nQtv 30,43,3584\n"
"Qmvaa 216,92,240\nQtv 12,41,1536\nQmvaa 216,184,56\nQtv 14,41,4096\nQmvaa 248,96,240\nQmvaa 248,188,56\nQtv 32,43,1536\nQmvaa 116,100,240\n"
"Qtv 34,43,4096\nv_add_u32 v75, 1, v75\nQmvaa 116,192,56\nds_write_b32 v73, v75\ns_waitcnt lgkmcnt(7)\nQmvaa 96,84,244\nQtv 16,41,2048\nQmvaa 96,176,60\n"
"Qda 48,53,41984\nQmvaa 128,88,244\nQtv 18,41,4608\nQmvaa 128,180,60\nQtv 36,43,2048\nQmvaa 160,92,244\nQtv 38,43,4608\nQmvaa 160,184,60\n"
"Qmvaa 192,96,244\nQmvaa 192,188,60\nds_read_b32 v66, v74\nQmvaa 224,100,244\nQmvaa 224,192,60\ns_waitcnt lgkmcnt(4)\nQmvva 208,0,248\nQmvva 208,20,48\n"
"s_waitcnt lgkmcnt(0)\nv_cmp_gt_u32 vcc, v75, v66\nQmvva 240,4,248\ns_cbranch_vccnz .Lso63r1\n.Lro63r1:\nQmvva 240,24,48\nQda 52,53,46080\nQmvva 84,8,248\n"
"Qmvva 84,28,48\nQmvva 88,12,248\nQmvva 88,32,48\nQmvva 120,16,248\nQmvva 120,36,48\ns_waitcnt lgkmcnt(0)\nQmvva 152,0,252\ns_add_u32 s69, s98, 0\n"
"buffer_load_dwordx4 v[124:127], v60, s[24:27], s69 offen offset:0 sc1\nQmvva 152,20,52\nQda 56,53,50176\nQmvva 184,4,252\nbuffer_load_dwordx4 v[188:191], v60, s[24:27], s69 offen offset:1024 sc1\nQmvva 184,24,52\nQmvva 216,8,252\nbuffer_load_dwordx4 v[252:255], v60, s[24:27], s69 offen offset:2048 sc1\n"
"Qmvva 216,28,52\nQmvva 248,12,252\nbuffer_load_dwordx4 v[132:135], v60, s[24:27], s69 offen offset:3072 sc1\nQmvva 248,32,52\nQmvva 116,16,252\ns_add_u32 s69, s98, 4096\nbuffer_load_dwordx4 v[196:199], v60, s[24:27], s69 offen offset:0 sc1\nQmvva 116,36,52\n"
"s_waitcnt lgkmcnt(0)\nQmvva 96,0,168\nbuffer_load_dwordx4 v[108:111], v60, s[24:27], s69 offen offset:1024 sc1\nQmvva 96,20,56\nQmvva 128,4,168\nbuffer_load_dwordx4 v[172:175], v60, s[24:27], s69 offen offset:2048 sc1\nQmvva 128,24,56\nQmvva 160,8,168\n"
"s_add_u32 s69, s98, 14336\nbuffer_load_dwordx4 v[236:239], v60, s[24:27], s69 offen offset:0 sc1\nQmvva 160,28,56\nQmvva 192,12,168\nQmvva 192,32,56\nQmvva 224,16,168\nQmvva 224,36,56\ns_barrier\n"
"s_mov_b32 s97, 0\n.Lrx1:\ns_waitcnt vmcnt(0)\nv_max_u32 v40, v124, v125\nv_max3_u32 v40, v40, v126, v127\nv_max3_u32 v40, v40, v188, v189\nv_max3_u32 v40, v40, v190, v191\nv_max3_u32 v40, v40, v252, v253\n"
"v_max3_u32 v40, v40, v254, v255\nv_max3_u32 v40, v40, v132, v133\nv_max3_u32 v40, v40, v134, v135\nv_max3_u32 v40, v40, v196, v197\nv_max3_u32 v40, v40, v198, v199\nv_max3_u32 v40, v40, v108, v109\nv_max3_u32 v40, v40, v110, v111\nv_max3_u32 v40, v40, v172, v173\n"
"v_max3_u32 v40, v40, v174, v175\nv_max3_u32 v40, v40, v236, v237\nv_cmp_eq_u32 vcc, -1, v40\ns_cmp_eq_u64 vcc, 0\ns_cbranch_scc1 .Lrx1_ok\ns_add_u32 s97, s97, 1\ns_cmp_gt_u32 s97, 1048576\ns_cbranch_scc1 .Lrx1_ok\n"
"s_add_u32 s69, s98, 0\nbuffer_load_dwordx4 v[124:127], v60, s[24:27], s69 offen offset:0 sc1\nbuffer_load_dwordx4 v[188:191], v60, s[24:27], s69 offen offset:1024 sc1\nbuffer_load_dwordx4 v[252:255], v60, s[24:27], s69 offen offset:2048 sc1\nbuffer_load_dwordx4 v[132:135], v60, s[24:27], s69 offen offset:3072 sc1\ns_add_u32 s69, s98, 4096\nbuffer_load_dwordx4 v[196:199], v60, s[24:27], s69 offen offset:0 sc1\nbuffer_load_dwordx4 v[108:111], v60, s[24:27], s69 offen offset:1024 sc1\n"
"buffer_load_dwordx4 v[172:175], v60, s[24:27], s69 offen offset:2048 sc1\ns_add_u32 s69, s98, 14336\nbuffer_load_dwordx4 v[236:239], v60, s[24:27], s69 offen offset:0 sc1\ns_branch .Lrx1\n.Lrx1_ok:\nv_lshlrev_b32 v41, 16, v124\nv_and_b32 v42, 0xffff0000, v124\nQa 208,208,41\n"
"Qa 209,209,42\nv_lshlrev_b32 v43, 16, v125\nv_and_b32 v44, 0xffff0000, v125\nQa 210,210,43\nQa 211,211,44\nQk 208,208,209\nQk 209,210,211\nds_write_b64 v63, v[208:209] offset:21504\n"
"v_lshlrev_b32 v41, 16, v126\nv_and_b32 v42, 0xffff0000, v126\nQa 240,240,41\nQa 241,241,42\nv_lshlrev_b32 v43, 16, v127\nv_and_b32 v44, 0xffff0000, v127\nQa 242,242,43\nQa 243,243,44\n"
"Qk 240,240,241\nQk 241,242,243\nds_write_b64 v63, v[240:241] offset:21568\nv_lshlrev_b32 v41, 16, v188\nv_and_b32 v42, 0xffff0000, v188\nQa 84,84,41\nQa 85,85,42\nv_lshlrev_b32 v43, 16, v189\n"
"v_and_b32 v44, 0xffff0000, v189\nQa 86,86,43\nQa 87,87,44\nQk 84,84,85\nQk 85,86,87\nds_write_b64 v63, v[84:85] offset:21632\n.Lpg26:\ns_cbranch_execz .Ltr8\n"
"v_lshlrev_b32 v41, 16, v190\nv_and_b32 v42, 0xffff0000, v190\nQa 88,88,41\nQa 89,89,42\nv_lshlrev_b32 v43, 16, v191\nv_and_b32 v44, 0xffff0000, v191\nQa 90,90,43\nQa 91,91,44\n"
"Qk 88,88,89\nQk 89,90,91\nds_write_b64 v63, v[88:89] offset:21696\nv_lshlrev_b32 v41, 16, v252\nv_and_b32 v42, 0xffff0000, v252\nQa 120,120,41\nQa 121,121,42\nv_lshlrev_b32 v43, 16, v253\n"
"v_and_b32 v44, 0xffff0000, v253\nQa 122,122,43\nQa 123,123,44\nQk 120,120,121\nQk 121,122,123\nds_write_b64 v63, v[120:121] offset:21760\nv_lshlrev_b32 v41, 16, v254\nv_and_b32 v42, 0xffff0000, v254\n"
"Qa 152,152,41\nQa 153,153,42\nv_lshlrev_b32 v43, 16, v255\nv_and_b32 v44, 0xffff0000, v255\nQa 154,154,43\nQa 155,155,44\nQk 152,152,153\nQk 153,154,155\n"
"ds_write_b64 v63, v[152:153] offset:26880\nv_lshlrev_b32 v41, 16, v132\nv_and_b32 v42, 0xffff0000, v132\nQa 184,184,41\nQa 185,185,42\nv_lshlrev_b32 v43, 16, v133\nv_and_b32 v44, 0xffff0000, v133\nQa 186,186,43\n"
"Qa 187,187,44\nQk 184,184,185\nQk 185,186,187\nds_write_b64 v63, v[184:185] offset:26944\nv_lshlrev_b32 v41, 16, v134\nv_and_b32 v42, 0xffff0000, v134\nQa 216,216,41\nQa 217,217,42\n"
"v_lshlrev_b32 v43, 16, v135\nv_and_b32 v44, 0xffff0000, v135\nQa 218,218,43\nQa 219,219,44\nQk 216,216,217\nQk 217,218,219\nds_write_b64 v63, v[216:217] offset:27008\nv_lshlrev_b32 v41, 16, v196\n"
"v_and_b32 v42, 0xffff0000, v196\nQa 248,248,41\nQa 249,249,42\nv_lshlrev_b32 v43, 16, v197\nv_and_b32 v44, 0xffff0000, v197\nQa 250,250,43\nQa 251,251,44\nQk 248,248,249\n"
"Qk 249,250,251\nds_write_b64 v63, v[248:249] offset:27072\nv_lshlrev_b32 v41, 16, v198\nv_and_b32 v42, 0xffff0000, v198\nQa 116,116,41\nQa 117,117,42\nv_lshlrev_b32 v43, 16, v199\nv_and_b32 v44, 0xffff0000, v199\n"
"Qa 118,118,43\nQa 119,119,44\nQk 116,116,117\nQk 117,118,119\nds_write_b64 v63, v[116:117] offset:27136\nv_lshlrev_b32 v41, 16, v108\nv_and_b32 v42, 0xffff0000, v108\nQa 96,96,41\n"
"Qa 97,97,42\nv_lshlrev_b32 v43, 16, v109\nv_and_b32 v44, 0xffff0000, v109\nQa 98,98,43\nQa 99,99,44\nQk 96,96,97\nQk 97,98,99\nds_write_b64 v63, v[96:97] offset:32256\n"
"v_lshlrev_b32 v41, 16, v110\nv_and_b32 v42, 0xffff0000, v110\nQa 128,128,41\nQa 129,129,42\nv_lshlrev_b32 v43, 16, v111\nv_and_b32 v44, 0xffff0000, v111\nQa 130,130,43\nQa 131,131,44\n"
"Qk 128,128,129\nQk 129,130,131\nds_write_b64 v63, v[128:129] offset:32320\nv_lshlrev_b32 v41, 16, v172\nv_and_b32 v42, 0xffff0000, v172\nQa 160,160,41\nQa 161,161,42\nv_lshlrev_b32 v43, 16, v173\n"
"v_and_b32 v44, 0xffff0000, v173\nQa 162,162,43\nQa 163,163,44\nQk 160,160,161\nQk 161,162,163\nds_write_b64 v63, v[160:161] offset:32384\nv_lshlrev_b32 v41, 16, v174\nv_and_b32 v42, 0xffff0000, v174\n"
"Qa 192,192,41\nQa 193,193,42\nv_lshlrev_b32 v43, 16, v175\nv_and_b32 v44, 0xffff0000, v175\nQa 194,194,43\nQa 195,195,44\nQk 192,192,193\nQk 193,194,195\n"
"ds_write_b64 v63, v[192:193] offset:32448\nv_lshlrev_b32 v41, 16, v236\nv_and_b32 v42, 0xffff0000, v236\nQa 224,224,41\nQa 225,225,42\nv_lshlrev_b32 v43, 16, v237\nv_and_b32 v44, 0xffff0000, v237\nQa 226,226,43\n"
"Qa 227,227,44\nQk 224,224,225\nQk 225,226,227\nds_write_b64 v63, v[224:225] offset:32512\n.Lfin:\n.Lpg27:\ns_cbranch_execz .Ltr8\ns_waitcnt lgkmcnt(0)\n"
"s_barrier\nQdv 0,64,0\nQdv 4,64,5376\nQdv 8,64,10752\nQdv 12,64,16128\nQdv 16,64,21504\nQdv 20,64,26880\nQdv 24,64,32256\n"
"Qdv 28,71,0\nQdv 32,71,21504\ns_add_u32 s68, s65, 0\ns_waitcnt lgkmcnt(8)\nbuffer_store_dwordx4 v[0:3], v70, s[40:43], s68 offen sc1\ns_add_u32 s68, s65, 40960\ns_waitcnt lgkmcnt(7)\nbuffer_store_dwordx4 v[4:7], v70, s[40:43], s68 offen sc1\n"
"s_add_u32 s68, s65, 81920\ns_waitcnt lgkmcnt(6)\nbuffer_store_dwordx4 v[8:11], v70, s[40:43], s68 offen sc1\ns_add_u32 s68, s65, 122880\ns_waitcnt lgkmcnt(5)\nbuffer_store_dwordx4 v[12:15], v70, s[40:43], s68 offen sc1\ns_add_u32 s68, s60, 163840\ns_waitcnt lgkmcnt(4)\n"
"buffer_store_dwordx4 v[16:19], v70, s[40:43], s68 offen sc1\ns_add_u32 s68, s60, 204800\ns_waitcnt lgkmcnt(3)\nbuffer_store_dwordx4 v[20:23], v70, s[40:43], s68 offen sc1\ns_add_u32 s68, s60, 245760\ns_waitcnt lgkmcnt(2)\nbuffer_store_dwordx4 v[24:27], v70, s[40:43], s68 offen sc1\ns_add_u32 s68, s60, 163840\n"
"s_cmp_eq_u32 s70, 3\ns_cselect_b32 s68, 0x7ffff000, s68\ns_waitcnt lgkmcnt(1)\nbuffer_store_dwordx4 v[28:31], v72, s[40:43], s65 offen sc1\ns_waitcnt lgkmcnt(0)\nbuffer_store_dwordx4 v[32:35], v72, s[40:43], s68 offen sc1\ns_waitcnt vmcnt(0) lgkmcnt(0)\ns_branch .Lexit\n"
".Lend:\n.Lfb:\ns_waitcnt vmcnt(0) lgkmcnt(0)\nv_lshrrev_b32 v52, 4, v60\nv_and_b32 v53, 15, v52\nv_lshrrev_b32 v52, 4, v52\nv_lshlrev_b32 v66, 8, v53\nv_lshl_add_u32 v66, v52, 4, v66\n"
"v_add_u32 v66, 1024, v66\nv_lshlrev_b32 v67, 10, v52\nv_lshl_add_u32 v67, v53, 1, v67\nv_add_u32 v67, 1024, v67\nv_mov_b32 v52, 0x3f803f80\nv_accvgpr_write_b32 a204, v52\nv_accvgpr_write_b32 a205, v52\nv_accvgpr_write_b32 a206, v52\n"
"v_accvgpr_write_b32 a207, v52\nv_mov_b32 v24, 0xff800000\nv_mov_b32 v28, 0xff800000\nv_mov_b32 v32, 0xff800000\nv_mov_b32 v36, 0xff800000\nv_mov_b32 v40, 0xff800000\nv_mov_b32 v44, 0xff800000\nv_mov_b32 v48, 0xff800000\n"
"s_lshl_b32 s89, s67, 21\ns_mov_b32 s66, 0\n.Lfb1:\ns_add_u32 s68, s89, 4096\nbuffer_load_dwordx4 a[112:115], v66, s[40:43], s89 offen offset:0\nbuffer_load_dwordx4 a[116:119], v66, s[40:43], s89 offen offset:64\nbuffer_load_dwordx4 a[120:123], v66, s[40:43], s89 offen offset:128\nbuffer_load_dwordx4 a[124:127], v66, s[40:43], s89 offen offset:192\n"
"buffer_load_dwordx4 a[128:131], v66, s[40:43], s68 offen offset:0\nbuffer_load_dwordx4 a[132:135], v66, s[40:43], s68 offen offset:64\nbuffer_load_dwordx4 a[136:139], v66, s[40:43], s68 offen offset:128\nbuffer_load_dwordx4 a[140:143], v66, s[40:43], s68 offen offset:192\ns_waitcnt vmcnt(0)\nQzvaa 0,112,0\nQmvaa 0,116,4\nQmvaa 0,120,8\n"
"Qmvaa 0,124,12\nQzvaa 4,128,0\nQmvaa 4,132,4\nQmvaa 4,136,8\nQmvaa 4,140,12\ns_nop 7\nv_max3_f32 v24, v24, v0, v1\nv_max3_f32 v24, v24, v2, v3\n"
"v_max3_f32 v24, v24, v4, v5\nv_max3_f32 v24, v24, v6, v7\nQzvaa 0,112,16\nQmvaa 0,116,20\nQmvaa 0,120,24\nQmvaa 0,124,28\nQzvaa 4,128,16\nQmvaa 4,132,20\n"
"Qmvaa 4,136,24\nQmvaa 4,140,28\ns_nop 7\nv_max3_f32 v28, v28, v0, v1\nv_max3_f32 v28, v28, v2, v3\nv_max3_f32 v28, v28, v4, v5\nv_max3_f32 v28, v28, v6, v7\nQzvaa 0,112,32\n"
"Qmvaa 0,116,36\nQmvaa 0,120,40\nQmvaa 0,124,44\nQzvaa 4,128,32\nQmvaa 4,132,36\nQmvaa 4,136,40\nQmvaa 4,140,44\ns_nop 7\n"
"v_max3_f32 v32, v32, v0, v1\nv_max3_f32 v32, v32, v2, v3\nv_max3_f32 v32, v32, v4, v5\nv_max3_f32 v32, v32, v6, v7\nQzvaa 0,112,48\nQmvaa 0,116,52\nQmvaa 0,120,56\nQmvaa 0,124,60\n"
"Qzvaa 4,128,48\nQmvaa 4,132,52\nQmvaa 4,136,56\nQmvaa 4,140,60\ns_nop 7\nv_max3_f32 v36, v36, v0, v1\nv_max3_f32 v36, v36, v2, v3\nv_max3_f32 v36, v36, v4, v5\n"
"v_max3_f32 v36, v36, v6, v7\nQzvaa 0,112,64\nQmvaa 0,116,68\nQmvaa 0,120,72\nQmvaa 0,124,76\nQzvaa 4,128,64\nQmvaa 4,132,68\nQmvaa 4,136,72\n"
"Qmvaa 4,140,76\ns_nop 7\nv_max3_f32 v40, v40, v0, v1\nv_max3_f32 v40, v40, v2, v3\nv_max3_f32 v40, v40, v4, v5\nv_max3_f32 v40, v40, v6, v7\nQzvaa 0,112,80\nQmvaa 0,116,84\n"
"Qmvaa 0,120,88\nQmvaa 0,124,92\nQzvaa 4,128,80\nQmvaa 4,132,84\nQmvaa 4,136,88\nQmvaa 4,140,92\ns_nop 7\nv_max3_f32 v44, v44, v0, v1\n"
"v_max3_f32 v44, v44, v2, v3\nv_max3_f32 v44, v44, v4, v5\nv_max3_f32 v44, v44, v6, v7\nQzvaa 0,112,96\nQmvaa 0,116,100\nQmvaa 0,120,104\nQmvaa 0,124,108\nQzvaa 4,128,96\n"
"Qmvaa 4,132,100\nQmvaa 4,136,104\nQmvaa 4,140,108\ns_nop 7\nv_max3_f32 v48, v48, v0, v1\nv_max3_f32 v48, v48, v2, v3\nv_max3_f32 v48, v48, v4, v5\nv_max3_f32 v48, v48, v6, v7\n"
"s_add_u32 s89, s89, 8192\ns_add_u32 s66, s66, 1\ns_cmp_lt_u32 s66, 32\ns_cbranch_scc1 .Lfb1\nv_mov_b32 v25, v24\ns_nop 1\nv_permlane32_swap_b32 v24, v25\nv_max_f32 v24, v24, v25\n"
"v_mov_b32 v25, v24\ns_nop 1\nv_permlane16_swap_b32 v24, v25\nv_max_f32 v24, v24, v25\nv_sub_f32 v24, 0, v24\nv_mov_b32 v25, v24\nv_mov_b32 v26, v24\nv_mov_b32 v27, v24\n"
"v_mov_b32 v29, v28\ns_nop 1\nv_permlane32_swap_b32 v28, v29\nv_max_f32 v28, v28, v29\nv_mov_b32 v29, v28\ns_nop 1\nv_permlane16_swap_b32 v28, v29\nv_max_f32 v28, v28, v29\n"
"v_sub_f32 v28, 0, v28\nv_mov_b32 v29, v28\nv_mov_b32 v30, v28\nv_mov_b32 v31, v28\nv_mov_b32 v33, v32\ns_nop 1\nv_permlane32_swap_b32 v32, v33\nv_max_f32 v32, v32, v33\n"
"v_mov_b32 v33, v32\ns_nop 1\nv_permlane16_swap_b32 v32, v33\nv_max_f32 v32, v32, v33\nv_sub_f32 v32, 0, v32\nv_mov_b32 v33, v32\nv_mov_b32 v34, v32\nv_mov_b32 v35, v32\n"
"v_mov_b32 v37, v36\ns_nop 1\nv_permlane32_swap_b32 v36, v37\nv_max_f32 v36, v36, v37\nv_mov_b32 v37, v36\ns_nop 1\nv_permlane16_swap_b32 v36, v37\nv_max_f32 v36, v36, v37\n"
"v_sub_f32 v36, 0, v36\nv_mov_b32 v37, v36\nv_mov_b32 v38, v36\nv_mov_b32 v39, v36\nv_mov_b32 v41, v40\ns_nop 1\nv_permlane32_swap_b32 v40, v41\nv_max_f32 v40, v40, v41\n"
"v_mov_b32 v41, v40\ns_nop 1\nv_permlane16_swap_b32 v40, v41\nv_max_f32 v40, v40, v41\nv_sub_f32 v40, 0, v40\nv_mov_b32 v41, v40\nv_mov_b32 v42, v40\nv_mov_b32 v43, v40\n"
"v_mov_b32 v45, v44\ns_nop 1\nv_permlane32_swap_b32 v44, v45\nv_max_f32 v44, v44, v45\nv_mov_b32 v45, v44\ns_nop 1\nv_permlane16_swap_b32 v44, v45\nv_max_f32 v44, v44, v45\n"
"v_sub_f32 v44, 0, v44\nv_mov_b32 v45, v44\nv_mov_b32 v46, v44\nv_mov_b32 v47, v44\nv_mov_b32 v49, v48\ns_nop 1\nv_permlane32_swap_b32 v48, v49\nv_max_f32 v48, v48, v49\n"
"v_mov_b32 v49, v48\ns_nop 1\nv_permlane16_swap_b32 v48, v49\nv_max_f32 v48, v48, v49\nv_sub_f32 v48, 0, v48\nv_mov_b32 v49, v48\nv_mov_b32 v50, v48\nv_mov_b32 v51, v48\n"
"v_mov_b32 v52, 0\nv_mov_b32 v80, 0\nv_mov_b32 v81, 0\nv_mov_b32 v82, 0\nv_mov_b32 v83, 0\nv_mov_b32 v84, 0\nv_mov_b32 v85, 0\nv_mov_b32 v86, 0\n"
"v_mov_b32 v87, 0\nv_mov_b32 v88, 0\nv_mov_b32 v89, 0\nv_mov_b32 v90, 0\nv_mov_b32 v91, 0\nv_mov_b32 v92, 0\nv_mov_b32 v93, 0\nv_mov_b32 v94, 0\n"
"v_mov_b32 v95, 0\nv_mov_b32 v96, 0\nv_mov_b32 v97, 0\nv_mov_b32 v98, 0\nv_mov_b32 v99, 0\nv_mov_b32 v100, 0\nv_mov_b32 v101, 0\nv_mov_b32 v102, 0\n"
"v_mov_b32 v103, 0\nv_mov_b32 v104, 0\nv_mov_b32 v105, 0\nv_mov_b32 v106, 0\nv_mov_b32 v107, 0\nv_mov_b32 v108, 0\nv_mov_b32 v109, 0\nv_mov_b32 v110, 0\n"
"v_mov_b32 v111, 0\nv_accvgpr_write_b32 a176, v52\nv_accvgpr_write_b32 a177, v52\nv_accvgpr_write_b32 a178, v52\nv_accvgpr_write_b32 a179, v52\nv_mov_b32 v112, 0\nv_mov_b32 v113, 0\nv_mov_b32 v114, 0\n"
"v_mov_b32 v115, 0\nv_mov_b32 v116, 0\nv_mov_b32 v117, 0\nv_mov_b32 v118, 0\nv_mov_b32 v119, 0\nv_mov_b32 v120, 0\nv_mov_b32 v121, 0\nv_mov_b32 v122, 0\n"
"v_mov_b32 v123, 0\nv_mov_b32 v124, 0\nv_mov_b32 v125, 0\nv_mov_b32 v126, 0\nv_mov_b32 v127, 0\nv_mov_b32 v128, 0\nv_mov_b32 v129, 0\nv_mov_b32 v130, 0\n"
"v_mov_b32 v131, 0\nv_mov_b32 v132, 0\nv_mov_b32 v133, 0\nv_mov_b32 v134, 0\nv_mov_b32 v135, 0\nv_mov_b32 v136, 0\nv_mov_b32 v137, 0\nv_mov_b32 v138, 0\n"
"v_mov_b32 v139, 0\nv_mov_b32 v140, 0\nv_mov_b32 v141, 0\nv_mov_b32 v142, 0\nv_mov_b32 v143, 0\nv_accvgpr_write_b32 a180, v52\nv_accvgpr_write_b32 a181, v52\nv_accvgpr_write_b32 a182, v52\n"
"v_accvgpr_write_b32 a183, v52\nv_mov_b32 v144, 0\nv_mov_b32 v145, 0\nv_mov_b32 v146, 0\nv_mov_b32 v147, 0\nv_mov_b32 v148, 0\nv_mov_b32 v149, 0\nv_mov_b32 v150, 0\n"
"v_mov_b32 v151, 0\nv_mov_b32 v152, 0\nv_mov_b32 v153, 0\nv_mov_b32 v154, 0\nv_mov_b32 v155, 0\nv_mov_b32 v156, 0\nv_mov_b32 v157, 0\nv_mov_b32 v158, 0\n"
"v_mov_b32 v159, 0\nv_mov_b32 v160, 0\nv_mov_b32 v161, 0\nv_mov_b32 v162, 0\nv_mov_b32 v163, 0\nv_mov_b32 v164, 0\nv_mov_b32 v165, 0\nv_mov_b32 v166, 0\n"
"v_mov_b32 v167, 0\nv_mov_b32 v168, 0\nv_mov_b32 v169, 0\nv_mov_b32 v170, 0\nv_mov_b32 v171, 0\nv_mov_b32 v172, 0\nv_mov_b32 v173, 0\nv_mov_b32 v174, 0\n"
"v_mov_b32 v175, 0\nv_accvgpr_write_b32 a184, v52\nv_accvgpr_write_b32 a185, v52\nv_accvgpr_write_b32 a186, v52\nv_accvgpr_write_b32 a187, v52\nv_mov_b32 v176, 0\nv_mov_b32 v177, 0\nv_mov_b32 v178, 0\n"
"v_mov_b32 v179, 0\nv_mov_b32 v180, 0\nv_mov_b32 v181, 0\nv_mov_b32 v182, 0\nv_mov_b32 v183, 0\nv_mov_b32 v184, 0\nv_mov_b32 v185, 0\nv_mov_b32 v186, 0\n"
"v_mov_b32 v187, 0\nv_mov_b32 v188, 0\nv_mov_b32 v189, 0\nv_mov_b32 v190, 0\nv_mov_b32 v191, 0\nv_mov_b32 v192, 0\nv_mov_b32 v193, 0\nv_mov_b32 v194, 0\n"
"v_mov_b32 v195, 0\nv_mov_b32 v196, 0\nv_mov_b32 v197, 0\nv_mov_b32 v198, 0\nv_mov_b32 v199, 0\nv_mov_b32 v200, 0\nv_mov_b32 v201, 0\nv_mov_b32 v202, 0\n"
"v_mov_b32 v203, 0\nv_mov_b32 v204, 0\nv_mov_b32 v205, 0\nv_mov_b32 v206, 0\nv_mov_b32 v207, 0\nv_accvgpr_write_b32 a188, v52\nv_accvgpr_write_b32 a189, v52\nv_accvgpr_write_b32 a190, v52\n"
"v_accvgpr_write_b32 a191, v52\nv_mov_b32 v208, 0\nv_mov_b32 v209, 0\nv_mov_b32 v210, 0\nv_mov_b32 v211, 0\nv_mov_b32 v212, 0\nv_mov_b32 v213, 0\nv_mov_b32 v214, 0\n"
"v_mov_b32 v215, 0\nv_mov_b32 v216, 0\nv_mov_b32 v217, 0\nv_mov_b32 v218, 0\nv_mov_b32 v219, 0\nv_mov_b32 v220, 0\nv_mov_b32 v221, 0\nv_mov_b32 v222, 0\n"
"v_mov_b32 v223, 0\nv_mov_b32 v224, 0\nv_mov_b32 v225, 0\nv_mov_b32 v226, 0\nv_mov_b32 v227, 0\nv_mov_b32 v228, 0\nv_mov_b32 v229, 0\nv_mov_b32 v230, 0\n"
"v_mov_b32 v231, 0\nv_mov_b32 v232, 0\nv_mov_b32 v233, 0\nv_mov_b32 v234, 0\nv_mov_b32 v235, 0\nv_mov_b32 v236, 0\nv_mov_b32 v237, 0\nv_mov_b32 v238, 0\n"
"v_mov_b32 v239, 0\nv_accvgpr_write_b32 a192, v52\nv_accvgpr_write_b32 a193, v52\nv_accvgpr_write_b32 a194, v52\nv_accvgpr_write_b32 a195, v52\nv_mov_b32 v240, 0\nv_mov_b32 v241, 0\nv_mov_b32 v242, 0\n"
"v_mov_b32 v243, 0\nv_mov_b32 v244, 0\nv_mov_b32 v245, 0\nv_mov_b32 v246, 0\nv_mov_b32 v247, 0\nv_mov_b32 v248, 0\nv_mov_b32 v249, 0\nv_mov_b32 v250, 0\n"
"v_mov_b32 v251, 0\nv_mov_b32 v252, 0\nv_mov_b32 v253, 0\nv_mov_b32 v254, 0\nv_mov_b32 v255, 0\nv_accvgpr_write_b32 a208, v52\nv_accvgpr_write_b32 a209, v52\nv_accvgpr_write_b32 a210, v52\n"
"v_accvgpr_write_b32 a211, v52\nv_accvgpr_write_b32 a212, v52\nv_accvgpr_write_b32 a213, v52\nv_accvgpr_write_b32 a214, v52\nv_accvgpr_write_b32 a215, v52\nv_accvgpr_write_b32 a216, v52\nv_accvgpr_write_b32 a217, v52\nv_accvgpr_write_b32 a218, v52\n"
"v_accvgpr_write_b32 a219, v52\nv_accvgpr_write_b32 a220, v52\nv_accvgpr_write_b32 a221, v52\nv_accvgpr_write_b32 a222, v52\nv_accvgpr_write_b32 a223, v52\nv_accvgpr_write_b32 a196, v52\nv_accvgpr_write_b32 a197, v52\nv_accvgpr_write_b32 a198, v52\n"
"v_accvgpr_write_b32 a199, v52\nv_accvgpr_write_b32 a224, v52\nv_accvgpr_write_b32 a225, v52\nv_accvgpr_write_b32 a226, v52\nv_accvgpr_write_b32 a227, v52\nv_accvgpr_write_b32 a228, v52\nv_accvgpr_write_b32 a229, v52\nv_accvgpr_write_b32 a230, v52\n"
"v_accvgpr_write_b32 a231, v52\nv_accvgpr_write_b32 a232, v52\nv_accvgpr_write_b32 a233, v52\nv_accvgpr_write_b32 a234, v52\nv_accvgpr_write_b32 a235, v52\nv_accvgpr_write_b32 a236, v52\nv_accvgpr_write_b32 a237, v52\nv_accvgpr_write_b32 a238, v52\n"
"v_accvgpr_write_b32 a239, v52\nv_accvgpr_write_b32 a240, v52\nv_accvgpr_write_b32 a241, v52\nv_accvgpr_write_b32 a242, v52\nv_accvgpr_write_b32 a243, v52\nv_accvgpr_write_b32 a244, v52\nv_accvgpr_write_b32 a245, v52\nv_accvgpr_write_b32 a246, v52\n"
"v_accvgpr_write_b32 a247, v52\nv_accvgpr_write_b32 a248, v52\nv_accvgpr_write_b32 a249, v52\nv_accvgpr_write_b32 a250, v52\nv_accvgpr_write_b32 a251, v52\nv_accvgpr_write_b32 a252, v52\nv_accvgpr_write_b32 a253, v52\nv_accvgpr_write_b32 a254, v52\n"
"v_accvgpr_write_b32 a255, v52\nv_accvgpr_write_b32 a200, v52\nv_accvgpr_write_b32 a201, v52\nv_accvgpr_write_b32 a202, v52\nv_accvgpr_write_b32 a203, v52\ns_lshl_b32 s89, s67, 21\ns_mov_b32 s66, 0\n.Lfb2:\n"
"s_add_u32 s68, s89, 4096\nbuffer_load_dwordx4 a[112:115], v66, s[40:43], s89 offen offset:0\nbuffer_load_dwordx4 a[116:119], v66, s[40:43], s89 offen offset:64\nbuffer_load_dwordx4 a[120:123], v66, s[40:43], s89 offen offset:128\nbuffer_load_dwordx4 a[124:127], v66, s[40:43], s89 offen offset:192\nbuffer_load_dwordx4 a[128:131], v66, s[40:43], s68 offen offset:0\nbuffer_load_dwordx4 a[132:135], v66, s[40:43], s68 offen offset:64\nbuffer_load_dwordx4 a[136:139], v66, s[40:43], s68 offen offset:128\n"
"buffer_load_dwordx4 a[140:143], v66, s[40:43], s68 offen offset:192\nbuffer_load_ushort v8, v67, s[44:47], s89 offen offset:0\nbuffer_load_ushort v9, v67, s[44:47], s89 offen offset:256\nbuffer_load_ushort v10, v67, s[44:47], s89 offen offset:512\nbuffer_load_ushort v11, v67, s[44:47], s89 offen offset:768\nbuffer_load_ushort v12, v67, s[44:47], s68 offen offset:0\nbuffer_load_ushort v13, v67, s[44:47], s68 offen offset:256\nbuffer_load_ushort v14, v67, s[44:47], s68 offen offset:512\n"
"buffer_load_ushort v15, v67, s[44:47], s68 offen offset:768\ns_waitcnt vmcnt(0)\nv_lshl_or_b32 v8, v9, 16, v8\nv_accvgpr_write_b32 a144, v8\nv_lshl_or_b32 v10, v11, 16, v10\nv_accvgpr_write_b32 a145, v10\nv_lshl_or_b32 v12, v13, 16, v12\nv_accvgpr_write_b32 a146, v12\n"
"v_lshl_or_b32 v14, v15, 16, v14\nv_accvgpr_write_b32 a147, v14\nbuffer_load_ushort v8, v67, s[44:47], s89 offen offset:32\nbuffer_load_ushort v9, v67, s[44:47], s89 offen offset:288\nbuffer_load_ushort v10, v67, s[44:47], s89 offen offset:544\nbuffer_load_ushort v11, v67, s[44:47], s89 offen offset:800\nbuffer_load_ushort v12, v67, s[44:47], s68 offen offset:32\nbuffer_load_ushort v13, v67, s[44:47], s68 offen offset:288\n"
"buffer_load_ushort v14, v67, s[44:47], s68 offen offset:544\nbuffer_load_ushort v15, v67, s[44:47], s68 offen offset:800\ns_waitcnt vmcnt(0)\nv_lshl_or_b32 v8, v9, 16, v8\nv_accvgpr_write_b32 a148, v8\nv_lshl_or_b32 v10, v11, 16, v10\nv_accvgpr_write_b32 a149, v10\nv_lshl_or_b32 v12, v13, 16, v12\n"
"v_accvgpr_write_b32 a150, v12\nv_lshl_or_b32 v14, v15, 16, v14\nv_accvgpr_write_b32 a151, v14\nbuffer_load_ushort v8, v67, s[44:47], s89 offen offset:64\nbuffer_load_ushort v9, v67, s[44:47], s89 offen offset:320\nbuffer_load_ushort v10, v67, s[44:47], s89 offen offset:576\nbuffer_load_ushort v11, v67, s[44:47], s89 offen offset:832\nbuffer_load_ushort v12, v67, s[44:47], s68 offen offset:64\n"
"buffer_load_ushort v13, v67, s[44:47], s68 offen offset:320\nbuffer_load_ushort v14, v67, s[44:47], s68 offen offset:576\nbuffer_load_ushort v15, v67, s[44:47], s68 offen offset:832\ns_waitcnt vmcnt(0)\nv_lshl_or_b32 v8, v9, 16, v8\nv_accvgpr_write_b32 a152, v8\nv_lshl_or_b32 v10, v11, 16, v10\nv_accvgpr_write_b32 a153, v10\n"
"v_lshl_or_b32 v12, v13, 16, v12\nv_accvgpr_write_b32 a154, v12\nv_lshl_or_b32 v14, v15, 16, v14\nv_accvgpr_write_b32 a155, v14\nbuffer_load_ushort v8, v67, s[44:47], s89 offen offset:96\nbuffer_load_ushort v9, v67, s[44:47], s89 offen offset:352\nbuffer_load_ushort v10, v67, s[44:47], s89 offen offset:608\nbuffer_load_ushort v11, v67, s[44:47], s89 offen offset:864\n"
"buffer_load_ushort v12, v67, s[44:47], s68 offen offset:96\nbuffer_load_ushort v13, v67, s[44:47], s68 offen offset:352\nbuffer_load_ushort v14, v67, s[44:47], s68 offen offset:608\nbuffer_load_ushort v15, v67, s[44:47], s68 offen offset:864\ns_waitcnt vmcnt(0)\nv_lshl_or_b32 v8, v9, 16, v8\nv_accvgpr_write_b32 a156, v8\nv_lshl_or_b32 v10, v11, 16, v10\n"
"v_accvgpr_write_b32 a157, v10\nv_lshl_or_b32 v12, v13, 16, v12\nv_accvgpr_write_b32 a158, v12\nv_lshl_or_b32 v14, v15, 16, v14\nv_accvgpr_write_b32 a159, v14\nbuffer_load_ushort v8, v67, s[44:47], s89 offen offset:128\nbuffer_load_ushort v9, v67, s[44:47], s89 offen offset:384\nbuffer_load_ushort v10, v67, s[44:47], s89 offen offset:640\n"
"buffer_load_ushort v11, v67, s[44:47], s89 offen offset:896\nbuffer_load_ushort v12, v67, s[44:47], s68 offen offset:128\nbuffer_load_ushort v13, v67, s[44:47], s68 offen offset:384\nbuffer_load_ushort v14, v67, s[44:47], s68 offen offset:640\nbuffer_load_ushort v15, v67, s[44:47], s68 offen offset:896\ns_waitcnt vmcnt(0)\nv_lshl_or_b32 v8, v9, 16, v8\nv_accvgpr_write_b32 a160, v8\n"
"v_lshl_or_b32 v10, v11, 16, v10\nv_accvgpr_write_b32 a161, v10\nv_lshl_or_b32 v12, v13, 16, v12\nv_accvgpr_write_b32 a162, v12\nv_lshl_or_b32 v14, v15, 16, v14\nv_accvgpr_write_b32 a163, v14\nbuffer_load_ushort v8, v67, s[44:47], s89 offen offset:160\nbuffer_load_ushort v9, v67, s[44:47], s89 offen offset:416\n"
"buffer_load_ushort v10, v67, s[44:47], s89 offen offset:672\nbuffer_load_ushort v11, v67, s[44:47], s89 offen offset:928\nbuffer_load_ushort v12, v67, s[44:47], s68 offen offset:160\nbuffer_load_ushort v13, v67, s[44:47], s68 offen offset:416\nbuffer_load_ushort v14, v67, s[44:47], s68 offen offset:672\nbuffer_load_ushort v15, v67, s[44:47], s68 offen offset:928\ns_waitcnt vmcnt(0)\nv_lshl_or_b32 v8, v9, 16, v8\n"
"v_accvgpr_write_b32 a164, v8\nv_lshl_or_b32 v10, v11, 16, v10\nv_accvgpr_write_b32 a165, v10\nv_lshl_or_b32 v12, v13, 16, v12\nv_accvgpr_write_b32 a166, v12\nv_lshl_or_b32 v14, v15, 16, v14\nv_accvgpr_write_b32 a167, v14\nbuffer_load_ushort v8, v67, s[44:47], s89 offen offset:192\n"
"buffer_load_ushort v9, v67, s[44:47], s89 offen offset:448\nbuffer_load_ushort v10, v67, s[44:47], s89 offen offset:704\nbuffer_load_ushort v11, v67, s[44:47], s89 offen offset:960\nbuffer_load_ushort v12, v67, s[44:47], s68 offen offset:192\nbuffer_load_ushort v13, v67, s[44:47], s68 offen offset:448\nbuffer_load_ushort v14, v67, s[44:47], s68 offen offset:704\nbuffer_load_ushort v15, v67, s[44:47], s68 offen offset:960\ns_waitcnt vmcnt(0)\n"
"v_lshl_or_b32 v8, v9, 16, v8\nv_accvgpr_write_b32 a168, v8\nv_lshl_or_b32 v10, v11, 16, v10\nv_accvgpr_write_b32 a169, v10\nv_lshl_or_b32 v12, v13, 16, v12\nv_accvgpr_write_b32 a170, v12\nv_lshl_or_b32 v14, v15, 16, v14\nv_accvgpr_write_b32 a171, v14\n"
"buffer_load_ushort v8, v67, s[44:47], s89 offen offset:224\nbuffer_load_ushort v9, v67, s[44:47], s89 offen offset:480\nbuffer_load_ushort v10, v67, s[44:47], s89 offen offset:736\nbuffer_load_ushort v11, v67, s[44:47], s89 offen offset:992\nbuffer_load_ushort v12, v67, s[44:47], s68 offen offset:224\nbuffer_load_ushort v13, v67, s[44:47], s68 offen offset:480\nbuffer_load_ushort v14, v67, s[44:47], s68 offen offset:736\nbuffer_load_ushort v15, v67, s[44:47], s68 offen offset:992\n"
"s_waitcnt vmcnt(0)\nv_lshl_or_b32 v8, v9, 16, v8\nv_accvgpr_write_b32 a172, v8\nv_lshl_or_b32 v10, v11, 16, v10\nv_accvgpr_write_b32 a173, v10\nv_lshl_or_b32 v12, v13, 16, v12\nv_accvgpr_write_b32 a174, v12\nv_lshl_or_b32 v14, v15, 16, v14\n"
"v_accvgpr_write_b32 a175, v14\nQcvaa 0,112,0,24\nQmvaa 0,116,4\nQmvaa 0,120,8\nQmvaa 0,124,12\nQcvaa 4,128,0,24\nQmvaa 4,132,4\nQmvaa 4,136,8\n"
"Qmvaa 4,140,12\ns_nop 7\nQe 0\nQe 1\nQe 2\nQe 3\nQe 4\nQe 5\n"
"Qe 6\nQe 7\ns_nop 0\nQk 16,0,1\nQk 17,2,3\nQk 18,4,5\nQk 19,6,7\ns_nop 1\n"
"Qmvav 80,144,16\nQmvav 84,148,16\nQmvav 88,152,16\nQmvav 92,156,16\nQmvav 96,160,16\nQmvav 100,164,16\nQmvav 104,168,16\nQmvav 108,172,16\n"
"Qmaav 176,204,16\nQcvaa 0,112,16,28\nQmvaa 0,116,20\nQmvaa 0,120,24\nQmvaa 0,124,28\nQcvaa 4,128,16,28\nQmvaa 4,132,20\nQmvaa 4,136,24\n"
"Qmvaa 4,140,28\ns_nop 7\nQe 0\nQe 1\nQe 2\nQe 3\nQe 4\nQe 5\n"
"Qe 6\nQe 7\ns_nop 0\nQk 16,0,1\nQk 17,2,3\nQk 18,4,5\nQk 19,6,7\ns_nop 1\n"
"Qmvav 112,144,16\nQmvav 116,148,16\nQmvav 120,152,16\nQmvav 124,156,16\nQmvav 128,160,16\nQmvav 132,164,16\nQmvav 136,168,16\nQmvav 140,172,16\n"
"Qmaav 180,204,16\nQcvaa 0,112,32,32\nQmvaa 0,116,36\nQmvaa 0,120,40\nQmvaa 0,124,44\nQcvaa 4,128,32,32\nQmvaa 4,132,36\nQmvaa 4,136,40\n"
"Qmvaa 4,140,44\ns_nop 7\nQe 0\nQe 1\nQe 2\nQe 3\nQe 4\nQe 5\n"
"Qe 6\nQe 7\ns_nop 0\nQk 16,0,1\nQk 17,2,3\nQk 18,4,5\nQk 19,6,7\ns_nop 1\n"
"Qmvav 144,144,16\nQmvav 148,148,16\nQmvav 152,152,16\nQmvav 156,156,16\nQmvav 160,160,16\nQmvav 164,164,16\nQmvav 168,168,16\nQmvav 172,172,16\n"
"Qmaav 184,204,16\nQcvaa 0,112,48,36\nQmvaa 0,116,52\nQmvaa 0,120,56\nQmvaa 0,124,60\nQcvaa 4,128,48,36\nQmvaa 4,132,52\nQmvaa 4,136,56\n"
"Qmvaa 4,140,60\ns_nop 7\nQe 0\nQe 1\nQe 2\nQe 3\nQe 4\nQe 5\n"
"Qe 6\nQe 7\ns_nop 0\nQk 16,0,1\nQk 17,2,3\nQk 18,4,5\nQk 19,6,7\ns_nop 1\n"
"Qmvav 176,144,16\nQmvav 180,148,16\nQmvav 184,152,16\nQmvav 188,156,16\nQmvav 192,160,16\nQmvav 196,164,16\nQmvav 200,168,16\nQmvav 204,172,16\n"
"Qmaav 188,204,16\nQcvaa 0,112,64,40\nQmvaa 0,116,68\nQmvaa 0,120,72\nQmvaa 0,124,76\nQcvaa 4,128,64,40\nQmvaa 4,132,68\nQmvaa 4,136,72\n"
"Qmvaa 4,140,76\ns_nop 7\nQe 0\nQe 1\nQe 2\nQe 3\nQe 4\nQe 5\n"
"Qe 6\nQe 7\ns_nop 0\nQk 16,0,1\nQk 17,2,3\nQk 18,4,5\nQk 19,6,7\ns_nop 1\n"
"Qmvav 208,144,16\nQmvav 212,148,16\nQmvav 216,152,16\nQmvav 220,156,16\nQmvav 224,160,16\nQmvav 228,164,16\nQmvav 232,168,16\nQmvav 236,172,16\n"
"Qmaav 192,204,16\nQcvaa 0,112,80,44\nQmvaa 0,116,84\nQmvaa 0,120,88\nQmvaa 0,124,92\nQcvaa 4,128,80,44\nQmvaa 4,132,84\nQmvaa 4,136,88\n"
"Qmvaa 4,140,92\ns_nop 7\nQe 0\nQe 1\nQe 2\nQe 3\nQe 4\nQe 5\n"
"Qe 6\nQe 7\ns_nop 0\nQk 16,0,1\nQk 17,2,3\nQk 18,4,5\nQk 19,6,7\ns_nop 1\n"
"Qmvav 240,144,16\nQmvav 244,148,16\nQmvav 248,152,16\nQmvav 252,156,16\nQmaav 208,160,16\nQmaav 212,164,16\nQmaav 216,168,16\nQmaav 220,172,16\n"
"Qmaav 196,204,16\nQcvaa 0,112,96,48\nQmvaa 0,116,100\nQmvaa 0,120,104\nQmvaa 0,124,108\nQcvaa 4,128,96,48\nQmvaa 4,132,100\nQmvaa 4,136,104\n"
"Qmvaa 4,140,108\ns_nop 7\nQe 0\nQe 1\nQe 2\nQe 3\nQe 4\nQe 5\n"
"Qe 6\nQe 7\ns_nop 0\nQk 16,0,1\nQk 17,2,3\nQk 18,4,5\nQk 19,6,7\ns_nop 1\n"
"Qmaav 224,144,16\nQmaav 228,148,16\nQmaav 232,152,16\nQmaav 236,156,16\nQmaav 240,160,16\nQmaav 244,164,16\nQmaav 248,168,16\nQmaav 252,172,16\n"
"Qmaav 200,204,16\ns_add_u32 s89, s89, 8192\ns_add_u32 s66, s66, 1\ns_cmp_lt_u32 s66, 32\ns_cbranch_scc1 .Lfb2\ns_nop 7\nv_accvgpr_read_b32 v0, a176\nv_accvgpr_read_b32 v4, a180\n"
"v_accvgpr_read_b32 v8, a184\nv_accvgpr_read_b32 v12, a188\nv_accvgpr_read_b32 v16, a192\nv_accvgpr_read_b32 v20, a196\nv_accvgpr_read_b32 v24, a200\ns_cmp_eq_u32 s67, 0\ns_cbranch_scc1 .Lfbret0\ns_branch .Lfbret1\n"
".Lf2_w:\ns_mov_b32 s97, 0\n.Lf2_l:\ns_sleep 1\nbuffer_load_dword v78, v76, s[24:27], 0 offen sc1\ns_waitcnt vmcnt(0)\ns_add_u32 s97, s97, 1\ns_cmp_gt_u32 s97, 1048576\n"
"s_cbranch_scc1 .Lf2_r\nv_sub_u32 v66, v78, v65\nv_cmp_gt_u32 vcc, 2, v66\ns_and_b64 vcc, vcc, s[28:29]\ns_cbranch_scc1 .Lf2_l\ns_branch .Lf2_r\n.Lf3_w:\ns_mov_b32 s97, 0\n"
".Lf3_l:\ns_sleep 1\nbuffer_load_dword v78, v76, s[24:27], 0 offen sc1\ns_waitcnt vmcnt(0)\ns_add_u32 s97, s97, 1\ns_cmp_gt_u32 s97, 1048576\ns_cbranch_scc1 .Lf3_r\nv_sub_u32 v66, v78, v65\n"
"v_cmp_gt_u32 vcc, 3, v66\ns_and_b64 vcc, vcc, s[28:29]\ns_cbranch_scc1 .Lf3_l\ns_branch .Lf3_r\n.Lf4_w:\ns_mov_b32 s97, 0\n.Lf4_l:\ns_sleep 1\n"
"buffer_load_dword v78, v76, s[24:27], 0 offen sc1\ns_waitcnt vmcnt(0)\ns_add_u32 s97, s97, 1\ns_cmp_gt_u32 s97, 1048576\ns_cbranch_scc1 .Lf4_r\nv_sub_u32 v66, v78, v65\nv_cmp_gt_u32 vcc, 4, v66\ns_and_b64 vcc, vcc, s[28:29]\n"
"s_cbranch_scc1 .Lf4_l\ns_branch .Lf4_r\n.Lf5_w:\ns_mov_b32 s97, 0\n.Lf5_l:\ns_sleep 1\nbuffer_load_dword v78, v76, s[24:27], 0 offen sc1\ns_waitcnt vmcnt(0)\n"
"s_add_u32 s97, s97, 1\ns_cmp_gt_u32 s97, 1048576\ns_cbranch_scc1 .Lf5_r\nv_sub_u32 v66, v78, v65\nv_cmp_gt_u32 vcc, 5, v66\ns_and_b64 vcc, vcc, s[28:29]\ns_cbranch_scc1 .Lf5_l\ns_branch .Lf5_r\n"
".Lexit:\n"
  :: "s"((unsigned)kp), "s"((unsigned)(kp >> 32)), "s"((unsigned)vp), "s"((unsigned)(vp >> 32)), "s"((unsigned)xp), "s"((unsigned)(xp >> 32)),
     "s"((unsigned)wp), "s"((unsigned)(wp >> 32)), "s"((unsigned)op), "s"((unsigned)(op >> 32)), "s"(ldsb), "s"(wid), "s"(head0),
     "s"((unsigned)dp), "s"((unsigned)(dp >> 32)), "s"((unsigned)wop), "s"((unsigned)(wop >> 32)), "s"((unsigned)yp), "s"((unsigned)(yp >> 32)),
     "s"(hg), "s"(b * 8u + hg)
  : "v0", "v1", "v2", "v3", "v4", "v5", "v6", "v7", "v8", "v9", "v10", "v11", "v12", "v13", "v14", "v15", "v16", "v17", "v18", "v19", "v20", "v21", "v22", "v23", "v24", "v25", "v26", "v27", "v28", "v29", "v30", "v31", "v32", "v33", "v34", "v35", "v36", "v37", "v38", "v39", "v40", "v41", "v42", "v43", "v44", "v45", "v46", "v47", "v48", "v49", "v50", "v51", "v52", "v53", "v54", "v55", "v56", "v57", "v58", "v59", "v60", "v61", "v62", "v63", "v64", "v65", "v66", "v67", "v68", "v69", "v70", "v71", "v72", "v73", "v74", "v75", "v76", "v77", "v78", "v79", "v80", "v81", "v82", "v83", "v84", "v85", "v86", "v87", "v88", "v89", "v90", "v91", "v92", "v93", "v94", "v95", "v96", "v97", "v98", "v99", "v100", "v101", "v102", "v103", "v104", "v105", "v106", "v107", "v108", "v109", "v110", "v111", "v112", "v113", "v114", "v115", "v116", "v117", "v118", "v119", "v120", "v121", "v122", "v123", "v124", "v125", "v126", "v127", "v128", "v129", "v130", "v131", "v132", "v133", "v134", "v135", "v136", "v137", "v138", "v139", "v140", "v141", "v142", "v143", "v144", "v145", "v146", "v147", "v148", "v149", "v150", "v151", "v152", "v153", "v154", "v155", "v156", "v157", "v158", "v159", "v160", "v161", "v162", "v163", "v164", "v165", "v166", "v167", "v168", "v169", "v170", "v171", "v172", "v173", "v174", "v175", "v176", "v177", "v178", "v179", "v180", "v181", "v182", "v183", "v184", "v185", "v186", "v187", "v188", "v189", "v190", "v191", "v192", "v193", "v194", "v195", "v196", "v197", "v198", "v199", "v200", "v201", "v202", "v203", "v204", "v205", "v206", "v207", "v208", "v209", "v210", "v211", "v212", "v213", "v214", "v215", "v216", "v217", "v218", "v219", "v220", "v221", "v222", "v223", "v224", "v225", "v226", "v227", "v228", "v229", "v230", "v231", "v232", "v233", "v234", "v235", "v236", "v237", "v238", "v239", "v240", "v241", "v242", "v243", "v244", "v245", "v246", "v247", "v248", "v249", "v250", "v251", "v252", "v253", "v254", "v255", "a0", "a1", "a2", "a3", "a4", "a5", "a6", "a7", "a8", "a9", "a10", "a11", "a12", "a13", "a14", "a15", "a16", "a17", "a18", "a19", "a20", "a21", "a22", "a23", "a24", "a25", "a26", "a27", "a28", "a29", "a30", "a31", "a32", "a33", "a34", "a35", "a36", "a37", "a38", "a39", "a40", "a41", "a42", "a43", "a44", "a45", "a46", "a47", "a48", "a49", "a50", "a51", "a52", "a53", "a54", "a55", "a56", "a57", "a58", "a59", "a60", "a61", "a62", "a63", "a64", "a65", "a66", "a67", "a68", "a69", "a70", "a71", "a72", "a73", "a74", "a75", "a76", "a77", "a78", "a79", "a80", "a81", "a82", "a83", "a84", "a85", "a86", "a87", "a88", "a89", "a90", "a91", "a92", "a93", "a94", "a95", "a96", "a97", "a98", "a99", "a100", "a101", "a102", "a103", "a104", "a105", "a106", "a107", "a108", "a109", "a110", "a111", "a112", "a113", "a114", "a115", "a116", "a117", "a118", "a119", "a120", "a121", "a122", "a123", "a124", "a125", "a126", "a127", "a128", "a129", "a130", "a131", "a132", "a133", "a134", "a135", "a136", "a137", "a138", "a139", "a140", "a141", "a142", "a143", "a144", "a145", "a146", "a147", "a148", "a149", "a150", "a151", "a152", "a153", "a154", "a155", "a156", "a157", "a158", "a159", "a160", "a161", "a162", "a163", "a164", "a165", "a166", "a167", "a168", "a169", "a170", "a171", "a172", "a173", "a174", "a175", "a176", "a177", "a178", "a179", "a180", "a181", "a182", "a183", "a184", "a185", "a186", "a187", "a188", "a189", "a190", "a191", "a192", "a193", "a194", "a195", "a196", "a197", "a198", "a199", "a200", "a201", "a202", "a203", "a204", "a205", "a206", "a207", "a208", "a209", "a210", "a211", "a212", "a213", "a214", "a215", "a216", "a217", "a218", "a219", "a220", "a221", "a222", "a223", "a224", "a225", "a226", "a227", "a228", "a229", "a230", "a231", "a232", "a233", "a234", "a235", "a236", "a237", "a238", "a239", "a240", "a241", "a242", "a243", "a244", "a245", "a246", "a247", "a248", "a249", "a250", "a251", "a252", "a253", "a254", "a255", "s24", "s25", "s26", "s27", "s28", "s29", "s30", "s31", "s40", "s41", "s42", "s43", "s44", "s45", "s46", "s47", "s48", "s49", "s50", "s51", "s52", "s53", "s54", "s55", "s56", "s57", "s58", "s59", "s60", "s61", "s62", "s63", "s64", "s65", "s66", "s67", "s68", "s69", "s70", "s71", "s72", "s73", "s74", "s75", "s76", "s77", "s78", "s79", "s80", "s81", "s82", "s83", "s84", "s85", "s86", "s87", "s88", "s89", "s90", "s91", "s92", "s93", "s94", "s95", "s96", "s97", "s98", "s99", "vcc", "scc", "memory");
}

extern "C" void kernel_launch(void* const* d_in, const int* in_sizes, int n_in, void* d_out, int out_size, void* d_ws, size_t ws_size, hipStream_t stream) {
  typedef const unsigned short* P;
  (void)hipFuncSetAttribute((const void*)mha2_asm, hipFuncAttributeMaxDynamicSharedMemorySize, 163840);
  hipLaunchKernelGGL(mha2_asm, dim3(256), dim3(256), 163840, stream, (P)d_in[0], (P)d_in[5], (P)d_in[3], (P)d_in[8], (P)d_in[1], (P)d_in[6], (P)d_in[2], (P)d_in[7],
                     (P)d_in[4], (P)d_in[9], (char*)d_ws, (unsigned short*)d_out);
}
```

```cpp
#include <hip/hip_runtime.h>
#include <stdint.h>

__global__ __launch_bounds__(256, 1) __attribute__((aligned(4096)))
void mha2_asm(const unsigned short* __restrict__ X0, const unsigned short* __restrict__ X1, const unsigned short* __restrict__ W0, const unsigned short* __restrict__ W1,
              const unsigned short* __restrict__ K0, const unsigned short* __restrict__ K1,
              const unsigned short* __restrict__ V0, const unsigned short* __restrict__ V1,
              const unsigned short* __restrict__ WO0, const unsigned short* __restrict__ WO1, char* ws, unsigned short* out) {
  extern __shared__ __attribute__((aligned(16))) char lds[];
  const unsigned c = blockIdx.x;
  const unsigned xcd = c & 7u, idx = c >> 3;
  const unsigned hg = idx & 7u, bb = xcd * 4u + (idx >> 3);
  const unsigned bk = bb >> 4, b = bb & 15u;
  const unsigned wid = __builtin_amdgcn_readfirstlane(threadIdx.x >> 6);
  const unsigned head0 = (hg >> 2) * 16u + (hg & 3u) * 2u + (wid >> 1);
  const unsigned long long kp = (unsigned long long)(bk ? K1 : K0) + ((unsigned long long)b * 32u + head0) * 262144ull - 1024ull;
  const unsigned long long vp = (unsigned long long)(bk ? V1 : V0) + ((unsigned long long)b * 32u + head0) * 262144ull - 1024ull;
  const unsigned long long xp = (unsigned long long)(bk ? X1 : X0) + (unsigned long long)b * (224u * 1280u);
  const unsigned long long wp = (unsigned long long)(bk ? W1 : W0);
  const unsigned long long op = (unsigned long long)ws + 0ull + (unsigned long long)bb * 2295808ull;
  const unsigned ldsb = (unsigned)(size_t)lds;
  const unsigned long long wop = (unsigned long long)(bk ? WO1 : WO0) - 4096ull;
  const unsigned long long yp = (unsigned long long)out + (unsigned long long)b * (224ull * 2560ull) + bk * 1280u + (hg & 3u) * 320u;
  const unsigned long long dp = (unsigned long long)ws + (100ull << 20) + (c * 4u + wid) * 256u;
  asm volatile(
".macro Qta d,v,o\nds_read_b64_tr_b16 a[\\d:\\d+1], v\\v offset:\\o\n.endm\n.macro Qdv d,v,o\nds_read_b128 v[\\d:\\d+3], v\\v offset:\\o\n.endm\n.macro Qda d,v,o\nds_read_b128 a[\\d:\\d+3], v\\v offset:\\o\n"
".endm\n.macro Qzvav d,a,b\nv_mfma_f32_16x16x32_bf16 v[\\d:\\d+3], a[\\a:\\a+3], v[\\b:\\b+3], 0\n.endm\n.macro Qmvaa d,a,b\nv_mfma_f32_16x16x32_bf16 v[\\d:\\d+3], a[\\a:\\a+3], a[\\b:\\b+3], v[\\d:\\d+3]\n.endm\n.macro Qzaav d,a,b\n"
"v_mfma_f32_16x16x32_bf16 a[\\d:\\d+3], a[\\a:\\a+3], v[\\b:\\b+3], 0\n.endm\n.macro Qmaaa d,a,b\nv_mfma_f32_16x16x32_bf16 a[\\d:\\d+3], a[\\a:\\a+3], a[\\b:\\b+3], a[\\d:\\d+3]\n.endm\n.macro Qmvav d,a,b\nv_mfma_f32_16x16x32_bf16 v[\\d:\\d+3], a[\\a:\\a+3], v[\\b:\\b+3], v[\\d:\\d+3]\n.endm\n"
".macro Qmaav d,a,b\nv_mfma_f32_16x16x32_bf16 a[\\d:\\d+3], a[\\a:\\a+3], v[\\b:\\b+3], a[\\d:\\d+3]\n.endm\n.macro Qk d,a,b\nv_cvt_pk_bf16_f32 v\\d, v\\a, v\\b\n.endm\n.macro Qzvaa d,a,b\nv_mfma_f32_16x16x32_bf16 v[\\d:\\d+3], a[\\a:\\a+3], a[\\b:\\b+3], 0\n"
".endm\n.macro Qa d,a,b\nv_add_f32 v\\d, v\\a, v\\b\n.endm\n.macro Qe d\nv_exp_f32 v\\d, v\\d\n.endm\n.macro Qcvaa d,a,b,c\n"
"v_mfma_f32_16x16x32_bf16 v[\\d:\\d+3], a[\\a:\\a+3], a[\\b:\\b+3], v[\\c:\\c+3]\n.endm\n.macro Qtv d,v,o\nds_read_b64_tr_b16 v[\\d:\\d+1], v\\v offset:\\o\n.endm\n.macro Qzava d,a,b\nv_mfma_f32_16x16x32_bf16 a[\\d:\\d+3], v[\\a:\\a+3], a[\\b:\\b+3], 0\n.endm\n"
".macro Qmava d,a,b\nv_mfma_f32_16x16x32_bf16 a[\\d:\\d+3], v[\\a:\\a+3], a[\\b:\\b+3], a[\\d:\\d+3]\n.endm\n.macro Qmvva d,a,b\nv_mfma_f32_16x16x32_bf16 v[\\d:\\d+3], v[\\a:\\a+3], a[\\b:\\b+3], v[\\d:\\d+3]\n.endm\n.macro Qmvvv d,a,b\nv_mfma_f32_16x16x32_bf16 v[\\d:\\d+3], v[\\a:\\a+3], v[\\b:\\b+3], v[\\d:\\d+3]\n"
".endm\n.p2align 7\nv_mbcnt_lo_u32_b32 v0, -1, 0\nv_mbcnt_hi_u32_b32 v0, -1, v0\nv_lshlrev_b32 v60, 4, v0\ns_add_u32 m0, %10, 160768\ns_getpc_b64 s[56:57]\n.Lpc0:\n"
"s_and_b32 s56, s56, 0xffffff80\ns_and_b32 s57, s57, 0xffff\ns_mov_b32 s58, .Lend-.Lpc0\ns_mov_b32 s59, 0x20000\ns_and_b32 s94, %20, 31\ns_lshl_b32 s94, s94, 2\ns_add_u32 s94, s94, %11\ns_lshl_b32 s94, s94, 10\n"
"buffer_load_dwordx4 v60, s[56:59], s94 offen lds\ns_mov_b32 s52, %6\ns_and_b32 s53, %7, 0xffff\ns_mov_b32 s54, 5242880\ns_mov_b32 s55, 0x20000\ns_mov_b32 s48, %4\ns_and_b32 s49, %5, 0xffff\ns_mov_b32 s50, 286720\n"
"s_mov_b32 s51, 0x20000\ns_mov_b32 s70, %11\ns_and_b32 s92, s70, 1\ns_lshr_b32 s93, s70, 1\nv_lshrrev_b32 v2, 4, v0\ns_lshl_b32 s68, s92, 18\nv_and_b32 v4, 3, v0\nv_lshlrev_b32 v4, 4, v4\n"
"v_and_b32 v1, 1, v2\nv_lshl_add_u32 v4, v1, 6, v4\nv_lshrrev_b32 v1, 5, v0\nv_lshl_add_u32 v4, v1, 15, v4\nv_bfe_u32 v1, v0, 2, 2\nv_lshl_add_u32 v4, v1, 13, v4\nv_add_u32 v62, s68, v4\ns_lshl_b32 s87, %12, 8\n"
"s_mul_i32 s62, s70, 4128\ns_add_u32 s62, s62, %10\ns_add_u32 s62, s62, 33024\ns_add_u32 m0, s62, 0\ns_add_u32 s66, s87, 0\ns_nop 0\nbuffer_load_dwordx4 v62, s[52:55], s66 offen lds\ns_add_u32 s66, s87, 64504\n"
"s_nop 0\nbuffer_load_dwordx4 v62, s[52:55], s66 offen offset:1032 lds\ns_add_u32 s66, s87, 129008\ns_nop 0\nbuffer_load_dwordx4 v62, s[52:55], s66 offen offset:2064 lds\ns_add_u32 s66, s87, 193512\ns_nop 0\nbuffer_load_dwordx4 v62, s[52:55], s66 offen offset:3096 lds\n"
"s_lshl_b32 s69, s92, 1\nv_lshrrev_b32 v3, 5, v0\nv_and_b32 v4, 3, v0\nv_xor_b32 v4, s69, v4\nv_lshlrev_b32 v4, 4, v4\nv_lshl_add_u32 v4, v3, 6, v4\nv_bfe_u32 v1, v0, 2, 3\nv_mul_u32_u24 v64, 1280, v1\n"
"v_add_u32 v64, v64, v4\ns_mul_i32 s86, s93, 143360\ns_mul_i32 s68, s92, 10240\ns_add_u32 s86, s86, s68\ns_mul_i32 s63, s92, 14336\ns_mul_i32 s68, s93, 7168\ns_add_u32 s63, s63, s68\ns_add_u32 s63, s63, %10\n"
"s_add_u32 s63, s63, 99072\ns_add_u32 m0, s63, 33024\ns_add_u32 s97, s86, 0\ns_nop 0\nbuffer_load_dwordx4 v64, s[48:51], s97 offen lds\ns_add_u32 s97, s86, 19456\ns_nop 0\nbuffer_load_dwordx4 v64, s[48:51], s97 offen offset:1024 lds\n"
"s_add_u32 s97, s86, 38912\ns_nop 0\nbuffer_load_dwordx4 v64, s[48:51], s97 offen offset:2048 lds\ns_add_u32 s97, s86, 58368\ns_nop 0\nbuffer_load_dwordx4 v64, s[48:51], s97 offen offset:3072 lds\ns_add_u32 m0, s63, 37120\ns_add_u32 s97, s86, 81920\n"
"s_nop 0\nbuffer_load_dwordx4 v64, s[48:51], s97 offen lds\ns_add_u32 s97, s86, 101376\ns_nop 0\nbuffer_load_dwordx4 v64, s[48:51], s97 offen offset:1024 lds\ns_add_u32 s97, s86, 120832\ns_nop 0\nbuffer_load_dwordx4 v64, s[48:51], s97 offen offset:2048 lds\n"
"s_add_u32 m0, s62, 16512\ns_add_u32 s66, s87, 128\ns_nop 0\nbuffer_load_dwordx4 v62, s[52:55], s66 offen lds\ns_mov_b32 s24, %8\ns_and_b32 s25, %9, 0xffff\ns_mov_b32 s26, 2295808\ns_mov_b32 s27, 0x20000\n"
"v_and_b32 v5, 7, v0\nv_lshlrev_b32 v5, 7, v5\ns_add_u32 s66, s87, 64632\ns_nop 0\nbuffer_load_dwordx4 v62, s[52:55], s66 offen offset:1032 lds\nv_add_u32 v76, 2293760, v5\ns_lshl_b32 s68, s70, 2\ns_add_u32 s68, s68, 4\n"
"v_bfe_u32 v5, v0, 3, 1\nv_mad_u32_u24 v76, v5, s68, v76\nbuffer_load_dword v65, v76, s[24:27], 0 offen sc1\ns_add_u32 s66, s87, 129136\ns_nop 0\nbuffer_load_dwordx4 v62, s[52:55], s66 offen offset:2064 lds\ns_mov_b32 s40, %0\ns_and_b32 s41, %1, 0xffff\n"
"s_mov_b32 s42, 2360320\ns_mov_b32 s43, 0x20000\ns_mov_b32 s44, %2\ns_and_b32 s45, %3, 0xffff\ns_add_u32 s66, s87, 193640\ns_nop 0\nbuffer_load_dwordx4 v62, s[52:55], s66 offen offset:3096 lds\ns_mov_b32 s46, 2360320\n"
"s_mov_b32 s47, 0x20000\ns_mov_b32 s56, %8\ns_and_b32 s57, %9, 0xffff\ns_mov_b32 s58, 0\ns_mov_b32 s59, 0x20000\ns_mov_b64 exec, 0\ns_cmp_eq_u32 s70, 0\ns_cbranch_scc1 .Lpg0\n"
"s_cmp_eq_u32 s70, 1\ns_cbranch_scc1 .Lpg1\ns_cmp_eq_u32 s70, 2\ns_cbranch_scc1 .Lpg2\ns_branch .Lpg3\n.Ltr0:\ns_mov_b64 exec, -1\ns_mov_b32 s60, 0\n"
"s_mov_b32 s90, 896\ns_mov_b32 s67, 0\ns_mov_b32 s72, 0x3e0293ee\ns_lshl_b32 s68, s92, 12\ns_add_u32 s88, s68, 128\ns_add_u32 m0, s63, 0\ns_add_u32 s97, s86, 128\ns_nop 0\n"
"buffer_load_dwordx4 v64, s[48:51], s97 offen lds\nv_lshl_add_u32 v4, v1, 8, v4\nv_add_u32 v69, s88, v4\nv_and_b32 v4, 1, v0\nv_lshlrev_b32 v4, 4, v4\nv_lshl_add_u32 v4, v2, 5, v4\nv_bfe_u32 v1, v0, 1, 3\ns_add_u32 s97, s86, 19584\n"
"s_nop 0\nbuffer_load_dwordx4 v64, s[48:51], s97 offen offset:1024 lds\nv_lshl_add_u32 v4, v1, 8, v4\nv_add_u32 v72, s88, v4\ns_lshl_b32 s69, s93, 14\ns_add_u32 s69, s69, %10\ns_add_u32 s61, s69, s68\nv_bfe_u32 v3, v0, 3, 1\n"
"s_add_u32 s97, s86, 39040\ns_nop 0\nbuffer_load_dwordx4 v64, s[48:51], s97 offen offset:2048 lds\nv_lshlrev_b32 v4, 1, v3\nv_xor_b32 v4, v4, v2\nv_and_b32 v1, 7, v0\nv_lshl_add_u32 v4, v1, 2, v4\nv_lshlrev_b32 v4, 4, v4\n"
"v_lshl_add_u32 v1, v3, 12, v4\ns_add_u32 s97, s86, 58496\ns_nop 0\nbuffer_load_dwordx4 v64, s[48:51], s97 offen offset:3072 lds\nv_add_u32 v68, s69, v1\ns_mul_i32 s68, s92, 7168\ns_add_u32 s68, s68, %10\ns_add_u32 s68, s68, 99072\n"
"v_mul_u32_u24 v1, 14336, v3\nv_add3_u32 v63, v1, v4, s68\ns_add_u32 m0, s63, 4096\ns_add_u32 s97, s86, 82048\ns_nop 0\nbuffer_load_dwordx4 v64, s[48:51], s97 offen lds\nv_lshrrev_b32 v3, 5, v0\nv_and_b32 v4, 31, v0\n"
"v_lshlrev_b32 v4, 3, v4\nv_lshl_add_u32 v4, v3, 12, v4\nv_add_u32 v4, 8192, v4\nv_add_u32 v70, s69, v4\ns_add_u32 s97, s86, 101504\ns_nop 0\nbuffer_load_dwordx4 v64, s[48:51], s97 offen offset:1024 lds\nv_add_u32 v71, 66048, v70\n"
"s_mul_i32 s68, s93, 8256\ns_add_u32 s68, s68, %10\ns_add_u32 s68, s68, 33024\nv_mul_u32_u24 v3, 1032, v2\nv_and_b32 v4, 15, v0\ns_add_u32 s97, s86, 120960\ns_nop 0\nbuffer_load_dwordx4 v64, s[48:51], s97 offen offset:2048 lds\n"
"v_lshlrev_b32 v4, 4, v4\nv_add3_u32 v61, v3, v4, s68\ns_lshl_b32 s68, s70, 2\ns_add_u32 s69, %10, 131840\ns_add_u32 s68, s68, s69\nv_mov_b32 v73, s68\ns_add_u32 m0, s62, 33024\ns_add_u32 s66, s87, 524288\n"
"s_nop 0\nbuffer_load_dwordx4 v62, s[52:55], s66 offen lds\nv_and_b32 v3, 3, v0\nv_lshl_add_u32 v74, v3, 2, s69\nv_mov_b32 v75, 0\nds_write_b32 v73, v75\ns_waitcnt lgkmcnt(0)\ns_mul_i32 s91, s70, 6144\n"
"s_add_u32 s66, s87, 588792\ns_nop 0\nbuffer_load_dwordx4 v62, s[52:55], s66 offen offset:1032 lds\ns_add_u32 s91, s91, %10\ns_mul_i32 s96, s70, 5120\ns_add_u32 s96, s96, %10\nv_and_b32 v3, 1, v0\nv_mul_u32_u24 v3, 0xaaaa, v3\n"
"v_or_b32 v77, 0x4444, v3\ns_add_u32 s66, s87, 653296\ns_nop 0\nbuffer_load_dwordx4 v62, s[52:55], s66 offen offset:2064 lds\ns_and_b32 s68, %19, 4\ns_lshl_b32 s28, 15, s68\ns_mov_b32 s29, 0\ns_and_b32 s69, %19, 3\n"
"s_lshl_b32 s69, s69, 2\ns_lshr_b32 s68, s68, 1\ns_add_u32 s66, s87, 717800\ns_nop 0\nbuffer_load_dwordx4 v62, s[52:55], s66 offen offset:3096 lds\ns_add_u32 s69, s69, s68\ns_add_u32 s69, s69, s93\ns_mul_i32 s98, s69, 28672\n"
"s_mul_i32 s68, s92, 7168\ns_add_u32 s98, s98, s68\ns_add_u32 s98, s98, 1835008\ns_mov_b64 exec, 0\ns_cmp_eq_u32 s70, 0\ns_cbranch_scc1 .Lpg4\ns_cmp_eq_u32 s70, 1\ns_cbranch_scc1 .Lpg5\n"
"s_cmp_eq_u32 s70, 2\ns_cbranch_scc1 .Lpg6\ns_branch .Lpg7\n.Ltr9:\ns_mov_b64 exec, -1\ns_mov_b32 s71, 0\ns_and_b32 s68, %19, 3\ns_lshl_b32 s99, 3072, s68\n"
"s_or_b32 s99, s99, 0x302\ns_mov_b32 s94, 0x7ffff000\ns_mul_i32 s68, %20, 40960\ns_add_u32 m0, s62, 49536\ns_add_u32 s66, s87, 524416\ns_nop 0\nbuffer_load_dwordx4 v62, s[52:55], s66 offen lds\ns_mul_i32 s69, s93, 20480\n"
"s_add_u32 s68, s68, s69\ns_lshl_b32 s69, s92, 10\ns_add_u32 s68, s68, s69\ns_add_u32 s68, s68, 2048\ns_add_u32 s94, %6, s68\ns_addc_u32 s95, %7, 0\ns_add_u32 s66, s87, 588920\ns_nop 0\n"
"buffer_load_dwordx4 v62, s[52:55], s66 offen offset:1032 lds\ns_and_b32 s95, s95, 0xffff\ns_mov_b32 s75, 17408\ns_add_u32 s66, s87, 653424\ns_nop 0\nbuffer_load_dwordx4 v62, s[52:55], s66 offen offset:2064 lds\ns_add_u32 s66, s87, 717928\ns_nop 0\n"
"buffer_load_dwordx4 v62, s[52:55], s66 offen offset:3096 lds\ns_mov_b64 exec, 0\ns_cmp_eq_u32 s70, 0\ns_cbranch_scc1 .Lpg8\ns_cmp_eq_u32 s70, 1\ns_cbranch_scc1 .Lpg9\ns_cmp_eq_u32 s70, 2\ns_cbranch_scc1 .Lpg10\n"
"s_branch .Lpg11\n.Ltr10:\ns_mov_b64 exec, -1\n.Lround:\ns_cmp_lg_u32 s67, 0\ns_cselect_b64 vcc, -1, 0\ns_waitcnt vmcnt(15)\ns_barrier\n"
"Qta 112,61,0\nQta 114,61,512\nQdv 24,63,33024\nQta 116,61,4128\nQta 118,61,4640\nQdv 28,63,34048\nQta 120,61,8\nQta 122,61,520\n"
"Qdv 32,63,35072\nQta 124,61,4136\nQta 126,61,4648\nQdv 36,63,36096\nQta 128,61,256\nQta 130,61,768\nQdv 40,63,37120\nQta 132,61,4384\n"
"Qta 134,61,4896\nQdv 44,63,38144\nQta 136,61,264\nQta 138,61,776\nQdv 48,63,39168\nQta 140,61,4392\nQta 142,61,4904\nQda 176,63,33536\n"
"Qda 180,63,34560\nQda 184,63,35584\nQda 188,63,36608\nQda 192,63,37632\nQda 196,63,38656\nQda 200,63,39680\ns_waitcnt lgkmcnt(6)\nQzvav 80,112,24\n"
"Qmvaa 80,116,176\ns_add_u32 s68, s71, 0\nQzvav 84,120,24\nbuffer_store_dwordx4 a[0:3], v60, s[56:59], s68 offen offset:0 sc1\nQmvaa 84,124,176\nQzvav 88,128,24\ns_add_u32 s68, s71, 14336\nQmvaa 88,132,176\n"
"buffer_store_dwordx4 a[4:7], v60, s[56:59], s68 offen offset:0 sc1\nQzvav 92,136,24\nQmvaa 92,140,176\ns_waitcnt lgkmcnt(0)\nQzvav 112,112,28\ns_waitcnt vmcnt(17)\ns_barrier\nQmvaa 112,116,180\n"
"Qta 144,61,16512\ns_add_u32 m0, s62, 0\ns_add_u32 s69, s87, 1048576\nQzvav 116,120,28\nbuffer_load_dwordx4 v62, s[52:55], s69 offen lds\nQmvaa 116,124,180\nQta 146,61,17024\nQzvav 120,128,28\n"
"Qmvaa 120,132,180\nQta 148,61,20640\ns_add_u32 s69, s87, 1113080\nQzvav 124,136,28\nbuffer_load_dwordx4 v62, s[52:55], s69 offen offset:1032 lds\nQmvaa 124,140,180\nQta 150,61,21152\nQzvav 144,112,32\n"
"Qmvaa 144,116,184\nQta 152,61,16520\ns_add_u32 s69, s87, 1177584\nQzvav 148,120,32\nbuffer_load_dwordx4 v62, s[52:55], s69 offen offset:2064 lds\nQmvaa 148,124,184\nQta 154,61,17032\nQzvav 152,128,32\n"
"Qmvaa 152,132,184\nQta 156,61,20648\ns_add_u32 s69, s87, 1242088\nQzvav 156,136,32\nbuffer_load_dwordx4 v62, s[52:55], s69 offen offset:3096 lds\nQmvaa 156,140,184\nQta 158,61,21160\nQzvav 176,112,36\n"
"Qmvaa 176,116,188\nQta 160,61,16768\ns_add_u32 m0, s63, 33024\ns_add_u32 s88, s86, 256\nQzvav 180,120,36\nbuffer_load_dwordx4 v64, s[48:51], s88 offen lds\nQmvaa 180,124,188\nQta 162,61,17280\n"
"Qzvav 184,128,36\nQmvaa 184,132,188\nQta 164,61,20896\ns_add_u32 s88, s86, 19712\nQzvav 188,136,36\nbuffer_load_dwordx4 v64, s[48:51], s88 offen offset:1024 lds\nQmvaa 188,140,188\nQta 166,61,21408\n"
"Qzvav 208,112,40\nQmvaa 208,116,192\nQta 168,61,16776\ns_add_u32 s88, s86, 39168\nQzvav 212,120,40\nbuffer_load_dwordx4 v64, s[48:51], s88 offen offset:2048 lds\nQmvaa 212,124,192\nQta 170,61,17288\n"
"Qzvav 216,128,40\nQmvaa 216,132,192\nQta 172,61,20904\ns_add_u32 s88, s86, 58624\nQzvav 220,136,40\nbuffer_load_dwordx4 v64, s[48:51], s88 offen offset:3072 lds\nQmvaa 220,140,192\nQta 174,61,21416\n"
"Qzvav 240,112,44\nQmvaa 240,116,196\ns_add_u32 m0, s63, 37120\ns_add_u32 s88, s86, 82176\nQzvav 244,120,44\nbuffer_load_dwordx4 v64, s[48:51], s88 offen lds\nQmvaa 244,124,196\nQzvav 248,128,44\n"
"Qmvaa 248,132,196\ns_add_u32 s88, s86, 101632\nQzvav 252,136,44\nbuffer_load_dwordx4 v64, s[48:51], s88 offen offset:1024 lds\nQmvaa 252,140,196\nQzaav 224,112,48\nQmaaa 224,116,200\ns_add_u32 s88, s86, 121088\n"
"Qzaav 228,120,48\nbuffer_load_dwordx4 v64, s[48:51], s88 offen offset:2048 lds\nQmaaa 228,124,200\nQzaav 232,128,48\nQmaaa 232,132,200\nQzaav 236,136,48\nQmaaa 236,140,200\ns_waitcnt lgkmcnt(0)\n"
"Qzvav 96,144,24\nQmvaa 96,148,176\ns_add_u32 s68, s71, 28672\nQzvav 100,152,24\nbuffer_store_dwordx4 a[8:11], v60, s[56:59], s68 offen offset:0 sc1\nQmvaa 100,156,176\nQzvav 104,160,24\ns_add_u32 s68, s71, 43008\n"
"Qmvaa 104,164,176\nbuffer_store_dwordx4 a[12:15], v60, s[56:59], s68 offen offset:0 sc1\nQzvav 108,168,24\nQmvaa 108,172,176\ns_waitcnt lgkmcnt(0)\nQzvav 128,144,28\ns_waitcnt vmcnt(15)\ns_barrier\n"
"Qmvaa 128,148,180\nQdv 24,63,0\ns_add_u32 m0, s62, 16512\ns_add_u32 s69, s87, 1048704\nQzvav 132,152,28\nbuffer_load_dwordx4 v62, s[52:55], s69 offen lds\nQmvaa 132,156,180\nQda 176,63,512\n"
"Qzvav 136,160,28\nQta 112,61,33024\nQmvaa 136,164,180\ns_add_u32 s69, s87, 1113208\nQzvav 140,168,28\nbuffer_load_dwordx4 v62, s[52:55], s69 offen offset:1032 lds\nQmvaa 140,172,180\nQdv 28,63,1024\n"
"Qzvav 160,144,32\nQda 180,63,1536\nQmvaa 160,148,184\nQta 114,61,33536\ns_add_u32 s69, s87, 1177712\nQzvav 164,152,32\nbuffer_load_dwordx4 v62, s[52:55], s69 offen offset:2064 lds\nQmvaa 164,156,184\n"
"Qta 116,61,37152\nQzvav 168,160,32\nQmvaa 168,164,184\nQta 118,61,37664\ns_add_u32 s69, s87, 1242216\nQzvav 172,168,32\nbuffer_load_dwordx4 v62, s[52:55], s69 offen offset:3096 lds\nQmvaa 172,172,184\n"
"Qdv 32,63,2048\nQzvav 192,144,36\nQda 184,63,2560\nQmvaa 192,148,188\nQta 120,61,33032\ns_add_u32 m0, s61, 0\nQzvav 196,152,36\ns_cbranch_vccnz .Ld0q0\n"
"buffer_load_dwordx4 v69, s[40:43], s90 offen nt lds\n.Ld0q0:\nQmvaa 196,156,188\nQta 122,61,33544\nQzvav 200,160,36\nQmvaa 200,164,188\nQta 124,61,37160\nQzvav 204,168,36\n"
"Qdv 36,63,3072\nQmvaa 204,172,188\nQda 188,63,3584\nQzvav 224,144,40\nQta 126,61,37672\nQmvaa 224,148,192\nQzvav 228,152,40\nQta 128,61,33280\n"
"Qmvaa 228,156,192\nQzvav 232,160,40\nQta 130,61,33792\nQmvaa 232,164,192\nQzvav 236,168,40\nQdv 40,63,4096\nQmvaa 236,172,192\nQda 192,63,4608\n"
"Qzaav 208,144,44\nQta 132,61,37408\nQmaaa 208,148,196\nQzaav 212,152,44\nQta 134,61,37920\nQmaaa 212,156,196\nQzaav 216,160,44\nQta 136,61,33288\n"
"Qmaaa 216,164,196\nQzaav 220,168,44\nQdv 44,63,5120\nQmaaa 220,172,196\nQda 196,63,5632\nQzaav 240,144,48\nQta 138,61,33800\nQmaaa 240,148,200\n"
"Qzaav 244,152,48\nQta 140,61,37416\nQmaaa 244,156,200\nQzaav 248,160,48\nQta 142,61,37928\nQmaaa 248,164,200\nQzaav 252,168,48\nQdv 48,63,6144\n"
"Qmaaa 252,172,200\nQda 200,63,6656\ns_waitcnt lgkmcnt(2)\nQmvav 80,112,24\nQmvaa 80,116,176\ns_add_u32 s68, s71, 0\nQmvav 84,120,24\nbuffer_store_dwordx4 a[16:19], v60, s[56:59], s68 offen offset:1024 sc1\n"
"Qmvaa 84,124,176\nQmvav 88,128,24\ns_add_u32 s68, s71, 14336\n.Lpg0:\ns_cbranch_execz .Ltr0\nQmvaa 88,132,176\nbuffer_store_dwordx4 a[20:23], v60, s[56:59], s68 offen offset:1024 sc1\nQmvav 92,136,24\n"
"Qmvaa 92,140,176\ns_waitcnt lgkmcnt(0)\nQmvav 112,112,28\ns_waitcnt vmcnt(22)\ns_barrier\nQmvaa 112,116,180\nQta 144,61,49536\ns_add_u32 m0, s62, 33024\n"
"s_add_u32 s69, s87, 1572864\nQmvav 116,120,28\nbuffer_load_dwordx4 v62, s[52:55], s69 offen lds\nQmvaa 116,124,180\nQta 146,61,50048\nQmvav 120,128,28\nQmvaa 120,132,180\nQta 148,61,53664\n"
"s_add_u32 s69, s87, 1637368\nQmvav 124,136,28\nbuffer_load_dwordx4 v62, s[52:55], s69 offen offset:1032 lds\nQmvaa 124,140,180\nQta 150,61,54176\nQmvav 144,112,32\nQmvaa 144,116,184\nQta 152,61,49544\n"
"s_add_u32 s69, s87, 1701872\nQmvav 148,120,32\nbuffer_load_dwordx4 v62, s[52:55], s69 offen offset:2064 lds\nQmvaa 148,124,184\nQta 154,61,50056\nQmvav 152,128,32\nQmvaa 152,132,184\nQta 156,61,53672\n"
"s_add_u32 s69, s87, 1766376\nQmvav 156,136,32\nbuffer_load_dwordx4 v62, s[52:55], s69 offen offset:3096 lds\nQmvaa 156,140,184\nQta 158,61,54184\nQmvav 176,112,36\nQmvaa 176,116,188\nQta 160,61,49792\n"
"s_add_u32 m0, s63, 0\ns_add_u32 s88, s86, 384\nQmvav 180,120,36\nbuffer_load_dwordx4 v64, s[48:51], s88 offen lds\nQmvaa 180,124,188\nQta 162,61,50304\nQmvav 184,128,36\nQmvaa 184,132,188\n"
"Qta 164,61,53920\ns_add_u32 s88, s86, 19840\nQmvav 188,136,36\nbuffer_load_dwordx4 v64, s[48:51], s88 offen offset:1024 lds\nQmvaa 188,140,188\nQta 166,61,54432\nQmvav 208,112,40\nQmvaa 208,116,192\n"
"Qta 168,61,49800\ns_add_u32 s88, s86, 39296\nQmvav 212,120,40\nbuffer_load_dwordx4 v64, s[48:51], s88 offen offset:2048 lds\nQmvaa 212,124,192\nQta 170,61,50312\nQmvav 216,128,40\nQmvaa 216,132,192\n"
"Qta 172,61,53928\ns_add_u32 s88, s86, 58752\nQmvav 220,136,40\nbuffer_load_dwordx4 v64, s[48:51], s88 offen offset:3072 lds\nQmvaa 220,140,192\nQta 174,61,54440\nQmvav 240,112,44\nQmvaa 240,116,196\n"
"s_add_u32 m0, s63, 4096\ns_add_u32 s88, s86, 82304\nQmvav 244,120,44\nbuffer_load_dwordx4 v64, s[48:51], s88 offen lds\nQmvaa 244,124,196\nQmvav 248,128,44\nQmvaa 248,132,196\ns_add_u32 s88, s86, 101760\n"
"Qmvav 252,136,44\nbuffer_load_dwordx4 v64, s[48:51], s88 offen offset:1024 lds\nQmvaa 252,140,196\nQmaav 224,112,48\nQmaaa 224,116,200\ns_add_u32 s88, s86, 121216\nQmaav 228,120,48\nbuffer_load_dwordx4 v64, s[48:51], s88 offen offset:2048 lds\n"
"Qmaaa 228,124,200\nQmaav 232,128,48\nQmaaa 232,132,200\nQmaav 236,136,48\nQmaaa 236,140,200\ns_waitcnt lgkmcnt(0)\nQmvav 96,144,24\nQmvaa 96,148,176\n"
"s_add_u32 s68, s71, 28672\nQmvav 100,152,24\nbuffer_store_dwordx4 a[24:27], v60, s[56:59], s68 offen offset:1024 sc1\nQmvaa 100,156,176\nQmvav 104,160,24\ns_add_u32 s68, s71, 43008\nQmvaa 104,164,176\nbuffer_store_dwordx4 a[28:31], v60, s[56:59], s68 offen offset:1024 sc1\n"
"Qmvav 108,168,24\nQmvaa 108,172,176\ns_waitcnt lgkmcnt(0)\nQmvav 128,144,28\ns_waitcnt vmcnt(21)\ns_barrier\nQmvaa 128,148,180\nQdv 24,63,33024\n"
"s_add_u32 m0, s62, 49536\ns_add_u32 s69, s87, 1572992\nQmvav 132,152,28\nbuffer_load_dwordx4 v62, s[52:55], s69 offen lds\nQmvaa 132,156,180\nQda 176,63,33536\nQmvav 136,160,28\nQta 112,61,0\n"
"Qmvaa 136,164,180\ns_add_u32 s69, s87, 1637496\nQmvav 140,168,28\nbuffer_load_dwordx4 v62, s[52:55], s69 offen offset:1032 lds\nQmvaa 140,172,180\nQdv 28,63,34048\nQmvav 160,144,32\nQda 180,63,34560\n"
"Qmvaa 160,148,184\nQta 114,61,512\ns_add_u32 s69, s87, 1702000\nQmvav 164,152,32\nbuffer_load_dwordx4 v62, s[52:55], s69 offen offset:2064 lds\nQmvaa 164,156,184\nQta 116,61,4128\nQmvav 168,160,32\n"
"Qmvaa 168,164,184\nQta 118,61,4640\ns_add_u32 s69, s87, 1766504\nQmvav 172,168,32\nbuffer_load_dwordx4 v62, s[52:55], s69 offen offset:3096 lds\nQmvaa 172,172,184\nQdv 32,63,35072\nQmvav 192,144,36\n"
"Qda 184,63,35584\nQmvaa 192,148,188\nQta 120,61,8\ns_add_u32 m0, s61, 0\nQmvav 196,152,36\ns_cbranch_vccnz .Ld0q1\nbuffer_load_dwordx4 v69, s[40:43], s60 offen offset:1024 nt lds\n.Ld0q1:\n"
"Qmvaa 196,156,188\nQta 122,61,520\nQmvav 200,160,36\nQmvaa 200,164,188\nQta 124,61,4136\nQmvav 204,168,36\nQdv 36,63,36096\nQmvaa 204,172,188\n"
"Qda 188,63,36608\nQmvav 224,144,40\nQta 126,61,4648\nQmvaa 224,148,192\nQmvav 228,152,40\nQta 128,61,256\nQmvaa 228,156,192\nQmvav 232,160,40\n"
"Qta 130,61,768\nQmvaa 232,164,192\nQmvav 236,168,40\nQdv 40,63,37120\nQmvaa 236,172,192\nQda 192,63,37632\nQmaav 208,144,44\nQta 132,61,4384\n"
"Qmaaa 208,148,196\nQmaav 212,152,44\nQta 134,61,4896\nQmaaa 212,156,196\nQmaav 216,160,44\nQta 136,61,264\nQmaaa 216,164,196\nQmaav 220,168,44\n"
"Qdv 44,63,38144\nQmaaa 220,172,196\nQda 196,63,38656\nQmaav 240,144,48\nQta 138,61,776\nQmaaa 240,148,200\nQmaav 244,152,48\nQta 140,61,4392\n"
"Qmaaa 244,156,200\nQmaav 248,160,48\nQta 142,61,4904\nQmaaa 248,164,200\nQmaav 252,168,48\nQdv 48,63,39168\nQmaaa 252,172,200\nQda 200,63,39680\n"
"s_waitcnt lgkmcnt(2)\nQmvav 80,112,24\nQmvaa 80,116,176\ns_add_u32 s68, s71, 0\nQmvav 84,120,24\nbuffer_store_dwordx4 a[32:35], v60, s[56:59], s68 offen offset:2048 sc1\nQmvaa 84,124,176\nQmvav 88,128,24\n"
"s_add_u32 s68, s71, 14336\nQmvaa 88,132,176\nbuffer_store_dwordx4 a[36:39], v60, s[56:59], s68 offen offset:2048 sc1\nQmvav 92,136,24\nQmvaa 92,140,176\ns_waitcnt lgkmcnt(0)\nQmvav 112,112,28\ns_waitcnt vmcnt(21)\n"
"s_barrier\nQmvaa 112,116,180\nQta 144,61,16512\ns_add_u32 m0, s62, 0\ns_add_u32 s69, s87, 2097152\nQmvav 116,120,28\nbuffer_load_dwordx4 v62, s[52:55], s69 offen lds\nQmvaa 116,124,180\n"
"Qta 146,61,17024\nQmvav 120,128,28\nQmvaa 120,132,180\nQta 148,61,20640\ns_add_u32 s69, s87, 2161656\nQmvav 124,136,28\nbuffer_load_dwordx4 v62, s[52:55], s69 offen offset:1032 lds\nQmvaa 124,140,180\n"
"Qta 150,61,21152\nQmvav 144,112,32\nQmvaa 144,116,184\nQta 152,61,16520\ns_add_u32 s69, s87, 2226160\nQmvav 148,120,32\nbuffer_load_dwordx4 v62, s[52:55], s69 offen offset:2064 lds\nQmvaa 148,124,184\n"
"Qta 154,61,17032\nQmvav 152,128,32\nQmvaa 152,132,184\nQta 156,61,20648\ns_add_u32 s69, s87, 2290664\nQmvav 156,136,32\nbuffer_load_dwordx4 v62, s[52:55], s69 offen offset:3096 lds\nQmvaa 156,140,184\n"
"Qta 158,61,21160\nQmvav 176,112,36\nQmvaa 176,116,188\nQta 160,61,16768\ns_add_u32 m0, s63, 33024\ns_add_u32 s88, s86, 512\nQmvav 180,120,36\nbuffer_load_dwordx4 v64, s[48:51], s88 offen lds\n"
"Qmvaa 180,124,188\nQta 162,61,17280\nQmvav 184,128,36\nQmvaa 184,132,188\nQta 164,61,20896\ns_add_u32 s88, s86, 19968\nQmvav 188,136,36\nbuffer_load_dwordx4 v64, s[48:51], s88 offen offset:1024 lds\n"
"Qmvaa 188,140,188\nQta 166,61,21408\nQmvav 208,112,40\nQmvaa 208,116,192\nQta 168,61,16776\ns_add_u32 s88, s86, 39424\nQmvav 212,120,40\nbuffer_load_dwordx4 v64, s[48:51], s88 offen offset:2048 lds\n"
"Qmvaa 212,124,192\nQta 170,61,17288\nQmvav 216,128,40\nQmvaa 216,132,192\nQta 172,61,20904\ns_add_u32 s88, s86, 58880\nQmvav 220,136,40\nbuffer_load_dwordx4 v64, s[48:51], s88 offen offset:3072 lds\n"
"Qmvaa 220,140,192\nQta 174,61,21416\nQmvav 240,112,44\nQmvaa 240,116,196\ns_add_u32 m0, s63, 37120\ns_add_u32 s88, s86, 82432\nQmvav 244,120,44\nbuffer_load_dwordx4 v64, s[48:51], s88 offen lds\n"
"Qmvaa 244,124,196\nQmvav 248,128,44\nQmvaa 248,132,196\ns_add_u32 s88, s86, 101888\nQmvav 252,136,44\nbuffer_load_dwordx4 v64, s[48:51], s88 offen offset:1024 lds\nQmvaa 252,140,196\nQmaav 224,112,48\n"
"Qmaaa 224,116,200\ns_add_u32 s88, s86, 121344\nQmaav 228,120,48\nbuffer_load_dwordx4 v64, s[48:51], s88 offen offset:2048 lds\nQmaaa 228,124,200\nQmaav 232,128,48\nQmaaa 232,132,200\nQmaav 236,136,48\n"
"Qmaaa 236,140,200\ns_waitcnt lgkmcnt(0)\nQmvav 96,144,24\nQmvaa 96,148,176\ns_add_u32 s68, s71, 28672\nQmvav 100,152,24\nbuffer_store_dwordx4 a[40:43], v60, s[56:59], s68 offen offset:2048 sc1\nQmvaa 100,156,176\n"
"Qmvav 104,160,24\ns_add_u32 s68, s71, 43008\nQmvaa 104,164,176\nbuffer_store_dwordx4 a[44:47], v60, s[56:59], s68 offen offset:2048 sc1\nQmvav 108,168,24\nQmvaa 108,172,176\ns_waitcnt lgkmcnt(0)\nQmvav 128,144,28\n"
"s_waitcnt vmcnt(21)\ns_barrier\nQmvaa 128,148,180\nQdv 24,63,0\ns_add_u32 m0, s62, 16512\ns_add_u32 s69, s87, 2097280\nQmvav 132,152,28\nbuffer_load_dwordx4 v62, s[52:55], s69 offen lds\n"
"Qmvaa 132,156,180\nQda 176,63,512\nQmvav 136,160,28\nQta 112,61,33024\nQmvaa 136,164,180\ns_add_u32 s69, s87, 2161784\nQmvav 140,168,28\nbuffer_load_dwordx4 v62, s[52:55], s69 offen offset:1032 lds\n"
"Qmvaa 140,172,180\nQdv 28,63,1024\nQmvav 160,144,32\nQda 180,63,1536\nQmvaa 160,148,184\nQta 114,61,33536\ns_add_u32 s69, s87, 2226288\nQmvav 164,152,32\n"
"buffer_load_dwordx4 v62, s[52:55], s69 offen offset:2064 lds\nQmvaa 164,156,184\nQta 116,61,37152\nQmvav 168,160,32\nQmvaa 168,164,184\nQta 118,61,37664\ns_add_u32 s69, s87, 2290792\nQmvav 172,168,32\n"
"buffer_load_dwordx4 v62, s[52:55], s69 offen offset:3096 lds\nQmvaa 172,172,184\nQdv 32,63,2048\nQmvav 192,144,36\nQda 184,63,2560\nQmvaa 192,148,188\nQta 120,61,33032\ns_add_u32 m0, s61, 0\n"
"Qmvav 196,152,36\ns_cbranch_vccnz .Ld0q2\nbuffer_load_dwordx4 v69, s[40:43], s90 offen offset:2048 nt lds\n.Ld0q2:\nQmvaa 196,156,188\nQta 122,61,33544\nQmvav 200,160,36\nQmvaa 200,164,188\n"
"Qta 124,61,37160\nQmvav 204,168,36\nQdv 36,63,3072\nQmvaa 204,172,188\nQda 188,63,3584\nQmvav 224,144,40\nQta 126,61,37672\nQmvaa 224,148,192\n"
"Qmvav 228,152,40\nQta 128,61,33280\nQmvaa 228,156,192\nQmvav 232,160,40\nQta 130,61,33792\nQmvaa 232,164,192\nQmvav 236,168,40\nQdv 40,63,4096\n"
"Qmvaa 236,172,192\nQda 192,63,4608\nQmaav 208,144,44\nQta 132,61,37408\nQmaaa 208,148,196\nQmaav 212,152,44\nQta 134,61,37920\nQmaaa 212,156,196\n"
"Qmaav 216,160,44\nQta 136,61,33288\nQmaaa 216,164,196\nQmaav 220,168,44\nQdv 44,63,5120\nQmaaa 220,172,196\nQda 196,63,5632\nQmaav 240,144,48\n"
"Qta 138,61,33800\nQmaaa 240,148,200\nQmaav 244,152,48\nQta 140,61,37416\nQmaaa 244,156,200\nQmaav 248,160,48\nQta 142,61,37928\nQmaaa 248,164,200\n"
"Qmaav 252,168,48\nQdv 48,63,6144\nQmaaa 252,172,200\nQda 200,63,6656\ns_waitcnt lgkmcnt(2)\nQmvav 80,112,24\nQmvaa 80,116,176\ns_add_u32 s68, s71, 0\n"
"Qmvav 84,120,24\nbuffer_store_dwordx4 a[48:51], v60, s[56:59], s68 offen offset:3072 sc1\nQmvaa 84,124,176\nQmvav 88,128,24\ns_add_u32 s68, s71, 14336\nQmvaa 88,132,176\nbuffer_store_dwordx4 a[52:55], v60, s[56:59], s68 offen offset:3072 sc1\nQmvav 92,136,24\n"
"Qmvaa 92,140,176\ns_waitcnt lgkmcnt(0)\nQmvav 112,112,28\ns_waitcnt vmcnt(21)\ns_barrier\nQmvaa 112,116,180\nQta 144,61,49536\ns_add_u32 m0, s62, 33024\n"
"s_add_u32 s69, s87, 2621440\nQmvav 116,120,28\nbuffer_load_dwordx4 v62, s[52:55], s69 offen lds\nQmvaa 116,124,180\nQta 146,61,50048\nQmvav 120,128,28\nQmvaa 120,132,180\nQta 148,61,53664\n"
"s_add_u32 s69, s87, 2685944\nQmvav 124,136,28\nbuffer_load_dwordx4 v62, s[52:55], s69 offen offset:1032 lds\nQmvaa 124,140,180\nQta 150,61,54176\nQmvav 144,112,32\nQmvaa 144,116,184\nQta 152,61,49544\n"
"s_add_u32 s69, s87, 2750448\nQmvav 148,120,32\nbuffer_load_dwordx4 v62, s[52:55], s69 offen offset:2064 lds\nQmvaa 148,124,184\nQta 154,61,50056\nQmvav 152,128,32\nQmvaa 152,132,184\nQta 156,61,53672\n"
"s_add_u32 s69, s87, 2814952\nQmvav 156,136,32\nbuffer_load_dwordx4 v62, s[52:55], s69 offen offset:3096 lds\nQmvaa 156,140,184\nQta 158,61,54184\nQmvav 176,112,36\nQmvaa 176,116,188\nQta 160,61,49792\n"
"s_add_u32 m0, s63, 0\ns_add_u32 s88, s86, 640\nQmvav 180,120,36\nbuffer_load_dwordx4 v64, s[48:51], s88 offen lds\nQmvaa 180,124,188\nQta 162,61,50304\nQmvav 184,128,36\nQmvaa 184,132,188\n"
"Qta 164,61,53920\ns_add_u32 s88, s86, 20096\nQmvav 188,136,36\nbuffer_load_dwordx4 v64, s[48:51], s88 offen offset:1024 lds\nQmvaa 188,140,188\nQta 166,61,54432\nQmvav 208,112,40\nQmvaa 208,116,192\n"
"Qta 168,61,49800\ns_add_u32 s88, s86, 39552\nQmvav 212,120,40\nbuffer_load_dwordx4 v64, s[48:51], s88 offen offset:2048 lds\nQmvaa 212,124,192\nQta 170,61,50312\nQmvav 216,128,40\nQmvaa 216,132,192\n"
"Qta 172,61,53928\ns_add_u32 s88, s86, 59008\nQmvav 220,136,40\nbuffer_load_dwordx4 v64, s[48:51], s88 offen offset:3072 lds\nQmvaa 220,140,192\nQta 174,61,54440\nQmvav 240,112,44\nQmvaa 240,116,196\n"
"s_add_u32 m0, s63, 4096\ns_add_u32 s88, s86, 82560\nQmvav 244,120,44\nbuffer_load_dwordx4 v64, s[48:51], s88 offen lds\nQmvaa 244,124,196\nQmvav 248,128,44\nQmvaa 248,132,196\ns_add_u32 s88, s86, 102016\n"
"Qmvav 252,136,44\nbuffer_load_dwordx4 v64, s[48:51], s88 offen offset:1024 lds\nQmvaa 252,140,196\nQmaav 224,112,48\nQmaaa 224,116,200\ns_add_u32 s88, s86, 121472\nQmaav 228,120,48\nbuffer_load_dwordx4 v64, s[48:51], s88 offen offset:2048 lds\n"
"Qmaaa 228,124,200\nQmaav 232,128,48\nQmaaa 232,132,200\nQmaav 236,136,48\nQmaaa 236,140,200\ns_waitcnt lgkmcnt(0)\nQmvav 96,144,24\nQmvaa 96,148,176\n"
"s_add_u32 s68, s71, 28672\nQmvav 100,152,24\nbuffer_store_dwordx4 a[56:59], v60, s[56:59], s68 offen offset:3072 sc1\nQmvaa 100,156,176\nQmvav 104,160,24\n.Lpg1:\ns_cbranch_execz .Ltr0\ns_add_u32 s68, s71, 43008\n"
"Qmvaa 104,164,176\nbuffer_store_dwordx4 a[60:63], v60, s[56:59], s68 offen offset:3072 sc1\nQmvav 108,168,24\nQmvaa 108,172,176\ns_waitcnt lgkmcnt(0)\nQmvav 128,144,28\ns_waitcnt vmcnt(21)\ns_barrier\n"
"Qmvaa 128,148,180\nQdv 24,63,33024\ns_add_u32 m0, s62, 49536\ns_add_u32 s69, s87, 2621568\nQmvav 132,152,28\nbuffer_load_dwordx4 v62, s[52:55], s69 offen lds\nQmvaa 132,156,180\nQda 176,63,33536\n"
"Qmvav 136,160,28\nQta 112,61,0\nQmvaa 136,164,180\ns_add_u32 s69, s87, 2686072\nQmvav 140,168,28\nbuffer_load_dwordx4 v62, s[52:55], s69 offen offset:1032 lds\nQmvaa 140,172,180\nQdv 28,63,34048\n"
"Qmvav 160,144,32\nQda 180,63,34560\nQmvaa 160,148,184\nQta 114,61,512\ns_add_u32 s69, s87, 2750576\nQmvav 164,152,32\nbuffer_load_dwordx4 v62, s[52:55], s69 offen offset:2064 lds\nQmvaa 164,156,184\n"
"Qta 116,61,4128\nQmvav 168,160,32\nQmvaa 168,164,184\nQta 118,61,4640\ns_add_u32 s69, s87, 2815080\nQmvav 172,168,32\nbuffer_load_dwordx4 v62, s[52:55], s69 offen offset:3096 lds\nQmvaa 172,172,184\n"
"Qdv 32,63,35072\nQmvav 192,144,36\nQda 184,63,35584\nQmvaa 192,148,188\nQta 120,61,8\ns_add_u32 m0, s61, 0\nQmvav 196,152,36\ns_cbranch_vccnz .Ld0q3\n"
"buffer_load_dwordx4 v69, s[40:43], s60 offen offset:3072 nt lds\n.Ld0q3:\nQmvaa 196,156,188\nQta 122,61,520\nQmvav 200,160,36\nQmvaa 200,164,188\nQta 124,61,4136\nQmvav 204,168,36\n"
"Qdv 36,63,36096\nQmvaa 204,172,188\nQda 188,63,36608\nQmvav 224,144,40\nQta 126,61,4648\nQmvaa 224,148,192\nQmvav 228,152,40\nQta 128,61,256\n"
"Qmvaa 228,156,192\nQmvav 232,160,40\nQta 130,61,768\nQmvaa 232,164,192\nQmvav 236,168,40\nQdv 40,63,37120\nQmvaa 236,172,192\nQda 192,63,37632\n"
"Qmaav 208,144,44\nQta 132,61,4384\nQmaaa 208,148,196\nQmaav 212,152,44\nQta 134,61,4896\nQmaaa 212,156,196\nQmaav 216,160,44\nQta 136,61,264\n"
"Qmaaa 216,164,196\nQmaav 220,168,44\nQdv 44,63,38144\nQmaaa 220,172,196\nQda 196,63,38656\nQmaav 240,144,48\nQta 138,61,776\nQmaaa 240,148,200\n"
"Qmaav 244,152,48\nQta 140,61,4392\nQmaaa 244,156,200\nQmaav 248,160,48\nQta 142,61,4904\nQmaaa 248,164,200\nQmaav 252,168,48\nQdv 48,63,39168\n"
"Qmaaa 252,172,200\nQda 200,63,39680\ns_waitcnt lgkmcnt(2)\nQmvav 80,112,24\nQmvaa 80,116,176\ns_add_u32 s68, s71, 4096\nQmvav 84,120,24\nbuffer_store_dwordx4 a[64:67], v60, s[56:59], s68 offen offset:0 sc1\n"
"Qmvaa 84,124,176\nQmvav 88,128,24\ns_add_u32 s68, s71, 18432\nQmvaa 88,132,176\nbuffer_store_dwordx4 a[68:71], v60, s[56:59], s68 offen offset:0 sc1\nQmvav 92,136,24\nQmvaa 92,140,176\ns_waitcnt lgkmcnt(0)\n"
"Qmvav 112,112,28\ns_waitcnt vmcnt(21)\ns_barrier\nQmvaa 112,116,180\nQta 144,61,16512\ns_add_u32 m0, s62, 0\ns_add_u32 s69, s87, 3145728\nQmvav 116,120,28\n"
"buffer_load_dwordx4 v62, s[52:55], s69 offen lds\nQmvaa 116,124,180\nQta 146,61,17024\nQmvav 120,128,28\nQmvaa 120,132,180\nQta 148,61,20640\ns_add_u32 s69, s87, 3210232\nQmvav 124,136,28\n"
"buffer_load_dwordx4 v62, s[52:55], s69 offen offset:1032 lds\nQmvaa 124,140,180\nQta 150,61,21152\nQmvav 144,112,32\nQmvaa 144,116,184\nQta 152,61,16520\ns_add_u32 s69, s87, 3274736\nQmvav 148,120,32\n"
"buffer_load_dwordx4 v62, s[52:55], s69 offen offset:2064 lds\nQmvaa 148,124,184\nQta 154,61,17032\nQmvav 152,128,32\nQmvaa 152,132,184\nQta 156,61,20648\ns_add_u32 s69, s87, 3339240\nQmvav 156,136,32\n"
"buffer_load_dwordx4 v62, s[52:55], s69 offen offset:3096 lds\nQmvaa 156,140,184\nQta 158,61,21160\nQmvav 176,112,36\nQmvaa 176,116,188\nQta 160,61,16768\ns_add_u32 m0, s63, 33024\ns_add_u32 s88, s86, 768\n"
"Qmvav 180,120,36\nbuffer_load_dwordx4 v64, s[48:51], s88 offen lds\nQmvaa 180,124,188\nQta 162,61,17280\nQmvav 184,128,36\nQmvaa 184,132,188\nQta 164,61,20896\ns_add_u32 s88, s86, 20224\n"
"Qmvav 188,136,36\nbuffer_load_dwordx4 v64, s[48:51], s88 offen offset:1024 lds\nQmvaa 188,140,188\nQta 166,61,21408\nQmvav 208,112,40\nQmvaa 208,116,192\nQta 168,61,16776\ns_add_u32 s88, s86, 39680\n"
"Qmvav 212,120,40\nbuffer_load_dwordx4 v64, s[48:51], s88 offen offset:2048 lds\nQmvaa 212,124,192\nQta 170,61,17288\nQmvav 216,128,40\nQmvaa 216,132,192\nQta 172,61,20904\ns_add_u32 s88, s86, 59136\n"
"Qmvav 220,136,40\nbuffer_load_dwordx4 v64, s[48:51], s88 offen offset:3072 lds\nQmvaa 220,140,192\nQta 174,61,21416\nQmvav 240,112,44\nQmvaa 240,116,196\ns_add_u32 m0, s63, 37120\ns_add_u32 s88, s86, 82688\n"
"Qmvav 244,120,44\nbuffer_load_dwordx4 v64, s[48:51], s88 offen lds\nQmvaa 244,124,196\nQmvav 248,128,44\nQmvaa 248,132,196\ns_add_u32 s88, s86, 102144\nQmvav 252,136,44\nbuffer_load_dwordx4 v64, s[48:51], s88 offen offset:1024 lds\n"
"Qmvaa 252,140,196\nQmaav 224,112,48\nQmaaa 224,116,200\ns_add_u32 s88, s86, 121600\nQmaav 228,120,48\nbuffer_load_dwordx4 v64, s[48:51], s88 offen offset:2048 lds\nQmaaa 228,124,200\nQmaav 232,128,48\n"
"Qmaaa 232,132,200\nQmaav 236,136,48\nQmaaa 236,140,200\ns_waitcnt lgkmcnt(0)\nQmvav 96,144,24\nQmvaa 96,148,176\ns_add_u32 s68, s71, 32768\nQmvav 100,152,24\n"
"buffer_store_dwordx4 a[72:75], v60, s[56:59], s68 offen offset:0 sc1\nQmvaa 100,156,176\nQmvav 104,160,24\ns_add_u32 s68, s71, 47104\nQmvaa 104,164,176\nbuffer_store_dwordx4 a[76:79], v60, s[56:59], s68 offen offset:0 sc1\nQmvav 108,168,24\nQmvaa 108,172,176\n"
"s_waitcnt lgkmcnt(0)\nQmvav 128,144,28\ns_waitcnt vmcnt(21)\ns_barrier\nQmvaa 128,148,180\nQdv 24,63,0\ns_add_u32 m0, s62, 16512\ns_add_u32 s69, s87, 3145856\n"
"Qmvav 132,152,28\nbuffer_load_dwordx4 v62, s[52:55], s69 offen lds\nQmvaa 132,156,180\nQda 176,63,512\nQmvav 136,160,28\nQta 112,61,33024\nQmvaa 136,164,180\ns_add_u32 s69, s87, 3210360\n"
"Qmvav 140,168,28\nbuffer_load_dwordx4 v62, s[52:55], s69 offen offset:1032 lds\nQmvaa 140,172,180\nQdv 28,63,1024\nQmvav 160,144,32\nQda 180,63,1536\nQmvaa 160,148,184\nQta 114,61,33536\n"
"s_add_u32 s69, s87, 3274864\nQmvav 164,152,32\nbuffer_load_dwordx4 v62, s[52:55], s69 offen offset:2064 lds\nQmvaa 164,156,184\nQta 116,61,37152\nQmvav 168,160,32\nQmvaa 168,164,184\nQta 118,61,37664\n"
"s_add_u32 s69, s87, 3339368\nQmvav 172,168,32\nbuffer_load_dwordx4 v62, s[52:55], s69 offen offset:3096 lds\nQmvaa 172,172,184\nQdv 32,63,2048\nQmvav 192,144,36\nQda 184,63,2560\nQmvaa 192,148,188\n"
"Qta 120,61,33032\ns_add_u32 m0, s61, 8192\nQmvav 196,152,36\ns_cbranch_vccnz .Ld0q4\nbuffer_load_dwordx4 v72, s[44:47], s90 offen nt lds\n.Ld0q4:\nQmvaa 196,156,188\nQta 122,61,33544\n"
"Qmvav 200,160,36\nQmvaa 200,164,188\nQta 124,61,37160\nQmvav 204,168,36\nQdv 36,63,3072\nQmvaa 204,172,188\nQda 188,63,3584\nQmvav 224,144,40\n"
"Qta 126,61,37672\nQmvaa 224,148,192\nQmvav 228,152,40\nQta 128,61,33280\nQmvaa 228,156,192\nQmvav 232,160,40\nQta 130,61,33792\nQmvaa 232,164,192\n"
"Qmvav 236,168,40\nQdv 40,63,4096\nQmvaa 236,172,192\nQda 192,63,4608\nQmaav 208,144,44\nQta 132,61,37408\nQmaaa 208,148,196\nQmaav 212,152,44\n"
"Qta 134,61,37920\nQmaaa 212,156,196\nQmaav 216,160,44\nQta 136,61,33288\nQmaaa 216,164,196\nQmaav 220,168,44\nQdv 44,63,5120\nQmaaa 220,172,196\n"
"Qda 196,63,5632\nQmaav 240,144,48\nQta 138,61,33800\nQmaaa 240,148,200\nQmaav 244,152,48\nQta 140,61,37416\nQmaaa 244,156,200\nQmaav 248,160,48\n"
"Qta 142,61,37928\nQmaaa 248,164,200\nQmaav 252,168,48\nQdv 48,63,6144\nQmaaa 252,172,200\nQda 200,63,6656\ns_waitcnt lgkmcnt(2)\nQmvav 80,112,24\n"
"Qmvaa 80,116,176\ns_add_u32 s68, s71, 4096\nQmvav 84,120,24\nbuffer_store_dwordx4 a[80:83], v60, s[56:59], s68 offen offset:1024 sc1\nQmvaa 84,124,176\nQmvav 88,128,24\ns_add_u32 s68, s71, 18432\nQmvaa 88,132,176\n"
"buffer_store_dwordx4 a[84:87], v60, s[56:59], s68 offen offset:1024 sc1\nQmvav 92,136,24\nQmvaa 92,140,176\ns_waitcnt lgkmcnt(0)\nQmvav 112,112,28\ns_waitcnt vmcnt(21)\ns_barrier\nQmvaa 112,116,180\n"
"Qta 144,61,49536\ns_add_u32 m0, s62, 33024\ns_add_u32 s69, s87, 3670016\nQmvav 116,120,28\nbuffer_load_dwordx4 v62, s[52:55], s69 offen lds\nQmvaa 116,124,180\nQta 146,61,50048\nQmvav 120,128,28\n"
"Qmvaa 120,132,180\nQta 148,61,53664\ns_add_u32 s69, s87, 3734520\nQmvav 124,136,28\nbuffer_load_dwordx4 v62, s[52:55], s69 offen offset:1032 lds\nQmvaa 124,140,180\nQta 150,61,54176\nQmvav 144,112,32\n"
"Qmvaa 144,116,184\nQta 152,61,49544\ns_add_u32 s69, s87, 3799024\nQmvav 148,120,32\nbuffer_load_dwordx4 v62, s[52:55], s69 offen offset:2064 lds\nQmvaa 148,124,184\nQta 154,61,50056\nQmvav 152,128,32\n"
"Qmvaa 152,132,184\nQta 156,61,53672\ns_add_u32 s69, s87, 3863528\nQmvav 156,136,32\nbuffer_load_dwordx4 v62, s[52:55], s69 offen offset:3096 lds\nQmvaa 156,140,184\nQta 158,61,54184\nQmvav 176,112,36\n"
"Qmvaa 176,116,188\nQta 160,61,49792\ns_add_u32 m0, s63, 0\ns_add_u32 s88, s86, 896\nQmvav 180,120,36\nbuffer_load_dwordx4 v64, s[48:51], s88 offen lds\nQmvaa 180,124,188\nQta 162,61,50304\n"
"Qmvav 184,128,36\nQmvaa 184,132,188\nQta 164,61,53920\ns_add_u32 s88, s86, 20352\nQmvav 188,136,36\nbuffer_load_dwordx4 v64, s[48:51], s88 offen offset:1024 lds\nQmvaa 188,140,188\nQta 166,61,54432\n"
"Qmvav 208,112,40\nQmvaa 208,116,192\nQta 168,61,49800\ns_add_u32 s88, s86, 39808\nQmvav 212,120,40\nbuffer_load_dwordx4 v64, s[48:51], s88 offen offset:2048 lds\nQmvaa 212,124,192\nQta 170,61,50312\n"
"Qmvav 216,128,40\nQmvaa 216,132,192\nQta 172,61,53928\ns_add_u32 s88, s86, 59264\nQmvav 220,136,40\nbuffer_load_dwordx4 v64, s[48:51], s88 offen offset:3072 lds\nQmvaa 220,140,192\nQta 174,61,54440\n"
"Qmvav 240,112,44\nQmvaa 240,116,196\ns_add_u32 m0, s63, 4096\ns_add_u32 s88, s86, 82816\nQmvav 244,120,44\nbuffer_load_dwordx4 v64, s[48:51], s88 offen lds\nQmvaa 244,124,196\nQmvav 248,128,44\n"
"Qmvaa 248,132,196\ns_add_u32 s88, s86, 102272\nQmvav 252,136,44\nbuffer_load_dwordx4 v64, s[48:51], s88 offen offset:1024 lds\nQmvaa 252,140,196\nQmaav 224,112,48\nQmaaa 224,116,200\ns_add_u32 s88, s86, 121728\n"
"Qmaav 228,120,48\nbuffer_load_dwordx4 v64, s[48:51], s88 offen offset:2048 lds\nQmaaa 228,124,200\nQmaav 232,128,48\nQmaaa 232,132,200\nQmaav 236,136,48\nQmaaa 236,140,200\ns_waitcnt lgkmcnt(0)\n"
"Qmvav 96,144,24\nQmvaa 96,148,176\ns_add_u32 s68, s71, 32768\nQmvav 100,152,24\nbuffer_store_dwordx4 a[88:91], v60, s[56:59], s68 offen offset:1024 sc1\nQmvaa 100,156,176\nQmvav 104,160,24\ns_add_u32 s68, s71, 47104\n"
"Qmvaa 104,164,176\nbuffer_store_dwordx4 a[92:95], v60, s[56:59], s68 offen offset:1024 sc1\nQmvav 108,168,24\nQmvaa 108,172,176\ns_waitcnt lgkmcnt(0)\nQmvav 128,144,28\ns_waitcnt vmcnt(21)\ns_barrier\n"
"Qmvaa 128,148,180\nQdv 24,63,33024\ns_add_u32 m0, s62, 49536\ns_add_u32 s69, s87, 3670144\nQmvav 132,152,28\nbuffer_load_dwordx4 v62, s[52:55], s69 offen lds\nQmvaa 132,156,180\nQda 176,63,33536\n"
"Qmvav 136,160,28\nQta 112,61,0\nQmvaa 136,164,180\ns_add_u32 s69, s87, 3734648\nQmvav 140,168,28\nbuffer_load_dwordx4 v62, s[52:55], s69 offen offset:1032 lds\nQmvaa 140,172,180\nQdv 28,63,34048\n"
"Qmvav 160,144,32\nQda 180,63,34560\nQmvaa 160,148,184\nQta 114,61,512\ns_add_u32 s69, s87, 3799152\nQmvav 164,152,32\nbuffer_load_dwordx4 v62, s[52:55], s69 offen offset:2064 lds\nQmvaa 164,156,184\n"
"Qta 116,61,4128\nQmvav 168,160,32\nQmvaa 168,164,184\nQta 118,61,4640\ns_add_u32 s69, s87, 3863656\nQmvav 172,168,32\nbuffer_load_dwordx4 v62, s[52:55], s69 offen offset:3096 lds\nQmvaa 172,172,184\n"
"Qdv 32,63,35072\nQmvav 192,144,36\nQda 184,63,35584\nQmvaa 192,148,188\nQta 120,61,8\ns_add_u32 m0, s61, 8192\nQmvav 196,152,36\ns_cbranch_vccnz .Ld0q5\n"
"buffer_load_dwordx4 v72, s[44:47], s60 offen offset:1024 nt lds\n.Ld0q5:\nQmvaa 196,156,188\nQta 122,61,520\nQmvav 200,160,36\nQmvaa 200,164,188\nQta 124,61,4136\nQmvav 204,168,36\n"
"Qdv 36,63,36096\nQmvaa 204,172,188\nQda 188,63,36608\nQmvav 224,144,40\nQta 126,61,4648\nQmvaa 224,148,192\nQmvav 228,152,40\nQta 128,61,256\n"
"Qmvaa 228,156,192\nQmvav 232,160,40\nQta 130,61,768\nQmvaa 232,164,192\nQmvav 236,168,40\nQdv 40,63,37120\nQmvaa 236,172,192\nQda 192,63,37632\n"
"Qmaav 208,144,44\nQta 132,61,4384\nQmaaa 208,148,196\nQmaav 212,152,44\nQta 134,61,4896\nQmaaa 212,156,196\nQmaav 216,160,44\nQta 136,61,264\n"
"Qmaaa 216,164,196\nQmaav 220,168,44\nQdv 44,63,38144\nQmaaa 220,172,196\nQda 196,63,38656\nQmaav 240,144,48\nQta 138,61,776\nQmaaa 240,148,200\n"
"Qmaav 244,152,48\nQta 140,61,4392\nQmaaa 244,156,200\nQmaav 248,160,48\nQta 142,61,4904\nQmaaa 248,164,200\nQmaav 252,168,48\nQdv 48,63,39168\n"
"Qmaaa 252,172,200\nQda 200,63,39680\ns_waitcnt lgkmcnt(2)\nQmvav 80,112,24\nQmvaa 80,116,176\ns_add_u32 s68, s71, 4096\n.Lpg2:\ns_cbranch_execz .Ltr0\n"
"Qmvav 84,120,24\nbuffer_store_dwordx4 a[96:99], v60, s[56:59], s68 offen offset:2048 sc1\nQmvaa 84,124,176\nQmvav 88,128,24\ns_add_u32 s68, s71, 18432\nQmvaa 88,132,176\nbuffer_store_dwordx4 a[100:103], v60, s[56:59], s68 offen offset:2048 sc1\nQmvav 92,136,24\n"
"Qmvaa 92,140,176\ns_waitcnt lgkmcnt(0)\nQmvav 112,112,28\ns_waitcnt vmcnt(21)\ns_barrier\nQmvaa 112,116,180\nQta 144,61,16512\ns_add_u32 m0, s62, 0\n"
"s_add_u32 s69, s87, 4194304\nQmvav 116,120,28\nbuffer_load_dwordx4 v62, s[52:55], s69 offen lds\nQmvaa 116,124,180\nQta 146,61,17024\nQmvav 120,128,28\nQmvaa 120,132,180\nQta 148,61,20640\n"
"s_add_u32 s69, s87, 4258808\nQmvav 124,136,28\nbuffer_load_dwordx4 v62, s[52:55], s69 offen offset:1032 lds\nQmvaa 124,140,180\nQta 150,61,21152\nQmvav 144,112,32\nQmvaa 144,116,184\nQta 152,61,16520\n"
"s_add_u32 s69, s87, 4323312\nQmvav 148,120,32\nbuffer_load_dwordx4 v62, s[52:55], s69 offen offset:2064 lds\nQmvaa 148,124,184\nQta 154,61,17032\nQmvav 152,128,32\nQmvaa 152,132,184\nQta 156,61,20648\n"
"s_add_u32 s69, s87, 4387816\nQmvav 156,136,32\nbuffer_load_dwordx4 v62, s[52:55], s69 offen offset:3096 lds\nQmvaa 156,140,184\nQta 158,61,21160\nQmvav 176,112,36\nQmvaa 176,116,188\nQta 160,61,16768\n"
"s_add_u32 m0, s63, 33024\ns_add_u32 s88, s86, 1024\nQmvav 180,120,36\nbuffer_load_dwordx4 v64, s[48:51], s88 offen lds\nQmvaa 180,124,188\nQta 162,61,17280\nQmvav 184,128,36\nQmvaa 184,132,188\n"
"Qta 164,61,20896\ns_add_u32 s88, s86, 20480\nQmvav 188,136,36\nbuffer_load_dwordx4 v64, s[48:51], s88 offen offset:1024 lds\nQmvaa 188,140,188\nQta 166,61,21408\nQmvav 208,112,40\nQmvaa 208,116,192\n"
"Qta 168,61,16776\ns_add_u32 s88, s86, 39936\nQmvav 212,120,40\nbuffer_load_dwordx4 v64, s[48:51], s88 offen offset:2048 lds\nQmvaa 212,124,192\nQta 170,61,17288\nQmvav 216,128,40\nQmvaa 216,132,192\n"
"Qta 172,61,20904\ns_add_u32 s88, s86, 59392\nQmvav 220,136,40\nbuffer_load_dwordx4 v64, s[48:51], s88 offen offset:3072 lds\nQmvaa 220,140,192\nQta 174,61,21416\nQmvav 240,112,44\nQmvaa 240,116,196\n"
"s_add_u32 m0, s63, 37120\ns_add_u32 s88, s86, 82944\nQmvav 244,120,44\nbuffer_load_dwordx4 v64, s[48:51], s88 offen lds\nQmvaa 244,124,196\nQmvav 248,128,44\nQmvaa 248,132,196\ns_add_u32 s88, s86, 102400\n"
"Qmvav 252,136,44\nbuffer_load_dwordx4 v64, s[48:51], s88 offen offset:1024 lds\nQmvaa 252,140,196\nQmaav 224,112,48\nQmaaa 224,116,200\ns_add_u32 s88, s86, 121856\nQmaav 228,120,48\nbuffer_load_dwordx4 v64, s[48:51], s88 offen offset:2048 lds\n"
"Qmaaa 228,124,200\nQmaav 232,128,48\nQmaaa 232,132,200\nQmaav 236,136,48\nQmaaa 236,140,200\ns_waitcnt lgkmcnt(0)\nQmvav 96,144,24\nQmvaa 96,148,176\n"
"s_add_u32 s68, s71, 32768\nQmvav 100,152,24\nbuffer_store_dwordx4 a[104:107], v60, s[56:59], s68 offen offset:2048 sc1\nQmvaa 100,156,176\nQmvav 104,160,24\ns_add_u32 s68, s71, 47104\nQmvaa 104,164,176\nbuffer_store_dwordx4 a[108:111], v60, s[56:59], s68 offen offset:2048 sc1\n"
"Qmvav 108,168,24\nQmvaa 108,172,176\ns_waitcnt lgkmcnt(0)\nQmvav 128,144,28\ns_waitcnt vmcnt(21)\ns_barrier\nQmvaa 128,148,180\nQdv 24,63,0\n"
"s_add_u32 m0, s62, 16512\ns_add_u32 s69, s87, 4194432\nQmvav 132,152,28\nbuffer_load_dwordx4 v62, s[52:55], s69 offen lds\nQmvaa 132,156,180\nQda 176,63,512\nQmvav 136,160,28\nQta 112,61,33024\n"
"Qmvaa 136,164,180\ns_add_u32 s69, s87, 4258936\nQmvav 140,168,28\nbuffer_load_dwordx4 v62, s[52:55], s69 offen offset:1032 lds\nQmvaa 140,172,180\nQdv 28,63,1024\nQmvav 160,144,32\nQda 180,63,1536\n"
"Qmvaa 160,148,184\nQta 114,61,33536\ns_add_u32 s69, s87, 4323440\nQmvav 164,152,32\nbuffer_load_dwordx4 v62, s[52:55], s69 offen offset:2064 lds\nQmvaa 164,156,184\nQta 116,61,37152\nQmvav 168,160,32\n"
"Qmvaa 168,164,184\nQta 118,61,37664\ns_add_u32 s69, s87, 4387944\nQmvav 172,168,32\nbuffer_load_dwordx4 v62, s[52:55], s69 offen offset:3096 lds\nQmvaa 172,172,184\nQdv 32,63,2048\nQmvav 192,144,36\n"
"Qda 184,63,2560\nQmvaa 192,148,188\nQta 120,61,33032\ns_add_u32 m0, s61, 8192\nQmvav 196,152,36\ns_cbranch_vccnz .Ld0q6\nbuffer_load_dwordx4 v72, s[44:47], s90 offen offset:2048 nt lds\n.Ld0q6:\n"
"Qmvaa 196,156,188\nQta 122,61,33544\nQmvav 200,160,36\nQmvaa 200,164,188\nQta 124,61,37160\nQmvav 204,168,36\nQdv 36,63,3072\nQmvaa 204,172,188\n"
"Qda 188,63,3584\nQmvav 224,144,40\nQta 126,61,37672\nQmvaa 224,148,192\nQmvav 228,152,40\nQta 128,61,33280\nQmvaa 228,156,192\nQmvav 232,160,40\n"
"Qta 130,61,33792\nQmvaa 232,164,192\nQmvav 236,168,40\nQdv 40,63,4096\nQmvaa 236,172,192\nQda 192,63,4608\nQmaav 208,144,44\nQta 132,61,37408\n"
"Qmaaa 208,148,196\nQmaav 212,152,44\nQta 134,61,37920\nQmaaa 212,156,196\nQmaav 216,160,44\nQta 136,61,33288\nQmaaa 216,164,196\nQmaav 220,168,44\n"
"Qdv 44,63,5120\nQmaaa 220,172,196\nQda 196,63,5632\nQmaav 240,144,48\nQta 138,61,33800\nQmaaa 240,148,200\nQmaav 244,152,48\nQta 140,61,37416\n"
"Qmaaa 244,156,200\nQmaav 248,160,48\nQta 142,61,37928\nQmaaa 248,164,200\nQmaav 252,168,48\nQdv 48,63,6144\nQmaaa 252,172,200\nQda 200,63,6656\n"
"s_waitcnt lgkmcnt(2)\nQmvav 80,112,24\nQmvaa 80,116,176\nQmvav 84,120,24\nQmvaa 84,124,176\nQmvav 88,128,24\nQmvaa 88,132,176\nQmvav 92,136,24\n"
"Qmvaa 92,140,176\ns_waitcnt lgkmcnt(0)\nQmvav 112,112,28\ns_waitcnt vmcnt(19)\ns_barrier\nQmvaa 112,116,180\nQta 144,61,49536\ns_add_u32 m0, s62, 33024\n"
"s_add_u32 s69, s87, 4718592\nQmvav 116,120,28\nbuffer_load_dwordx4 v62, s[52:55], s69 offen lds\nQmvaa 116,124,180\nQta 146,61,50048\nQmvav 120,128,28\nQmvaa 120,132,180\nQta 148,61,53664\n"
"s_add_u32 s69, s87, 4783096\nQmvav 124,136,28\nbuffer_load_dwordx4 v62, s[52:55], s69 offen offset:1032 lds\nQmvaa 124,140,180\nQta 150,61,54176\nQmvav 144,112,32\nQmvaa 144,116,184\nQta 152,61,49544\n"
"s_add_u32 s69, s87, 4847600\nQmvav 148,120,32\nbuffer_load_dwordx4 v62, s[52:55], s69 offen offset:2064 lds\nQmvaa 148,124,184\nQta 154,61,50056\nQmvav 152,128,32\nQmvaa 152,132,184\nQta 156,61,53672\n"
"s_add_u32 s69, s87, 4912104\nQmvav 156,136,32\nbuffer_load_dwordx4 v62, s[52:55], s69 offen offset:3096 lds\nQmvaa 156,140,184\nQta 158,61,54184\nQmvav 176,112,36\nQmvaa 176,116,188\nQta 160,61,49792\n"
"s_add_u32 m0, s63, 0\ns_add_u32 s88, s86, 1152\nQmvav 180,120,36\nbuffer_load_dwordx4 v64, s[48:51], s88 offen lds\nQmvaa 180,124,188\nQta 162,61,50304\nQmvav 184,128,36\nQmvaa 184,132,188\n"
"Qta 164,61,53920\ns_add_u32 s88, s86, 20608\nQmvav 188,136,36\nbuffer_load_dwordx4 v64, s[48:51], s88 offen offset:1024 lds\nQmvaa 188,140,188\nQta 166,61,54432\nQmvav 208,112,40\nQmvaa 208,116,192\n"
"Qta 168,61,49800\ns_add_u32 s88, s86, 40064\nQmvav 212,120,40\nbuffer_load_dwordx4 v64, s[48:51], s88 offen offset:2048 lds\nQmvaa 212,124,192\nQta 170,61,50312\nQmvav 216,128,40\nQmvaa 216,132,192\n"
"Qta 172,61,53928\ns_add_u32 s88, s86, 59520\nQmvav 220,136,40\nbuffer_load_dwordx4 v64, s[48:51], s88 offen offset:3072 lds\nQmvaa 220,140,192\nQta 174,61,54440\nQmvav 240,112,44\nQmvaa 240,116,196\n"
"s_add_u32 m0, s63, 4096\ns_add_u32 s88, s86, 83072\nQmvav 244,120,44\nbuffer_load_dwordx4 v64, s[48:51], s88 offen lds\nQmvaa 244,124,196\nQmvav 248,128,44\nQmvaa 248,132,196\ns_add_u32 s88, s86, 102528\n"
"Qmvav 252,136,44\nbuffer_load_dwordx4 v64, s[48:51], s88 offen offset:1024 lds\nQmvaa 252,140,196\nQmaav 224,112,48\nQmaaa 224,116,200\ns_add_u32 s88, s86, 121984\nQmaav 228,120,48\nbuffer_load_dwordx4 v64, s[48:51], s88 offen offset:2048 lds\n"
"Qmaaa 228,124,200\nQmaav 232,128,48\nQmaaa 232,132,200\nQmaav 236,136,48\nQmaaa 236,140,200\ns_waitcnt lgkmcnt(0)\nQmvav 96,144,24\nQmvaa 96,148,176\n"
"Qmvav 100,152,24\nQmvaa 100,156,176\nQmvav 104,160,24\nQmvaa 104,164,176\nQmvav 108,168,24\nQmvaa 108,172,176\ns_waitcnt lgkmcnt(0)\nQmvav 128,144,28\n"
"s_waitcnt vmcnt(17)\ns_barrier\nQmvaa 128,148,180\nQdv 24,63,33024\ns_add_u32 m0, s62, 49536\ns_add_u32 s69, s87, 4718720\nQmvav 132,152,28\nbuffer_load_dwordx4 v62, s[52:55], s69 offen lds\n"
"Qmvaa 132,156,180\nQda 176,63,33536\nQmvav 136,160,28\nQta 112,61,0\nQmvaa 136,164,180\ns_add_u32 s69, s87, 4783224\nQmvav 140,168,28\nbuffer_load_dwordx4 v62, s[52:55], s69 offen offset:1032 lds\n"
"Qmvaa 140,172,180\nQdv 28,63,34048\nQmvav 160,144,32\nQda 180,63,34560\nQmvaa 160,148,184\nQta 114,61,512\ns_add_u32 s69, s87, 4847728\nQmvav 164,152,32\n"
"buffer_load_dwordx4 v62, s[52:55], s69 offen offset:2064 lds\nQmvaa 164,156,184\nQta 116,61,4128\nQmvav 168,160,32\nQmvaa 168,164,184\nQta 118,61,4640\ns_add_u32 s69, s87, 4912232\nQmvav 172,168,32\n"
"buffer_load_dwordx4 v62, s[52:55], s69 offen offset:3096 lds\nQmvaa 172,172,184\nQdv 32,63,35072\nQmvav 192,144,36\nQda 184,63,35584\nQmvaa 192,148,188\nQta 120,61,8\ns_add_u32 m0, s61, 8192\n"
"Qmvav 196,152,36\ns_cbranch_vccnz .Ld0q7\nbuffer_load_dwordx4 v72, s[44:47], s60 offen offset:3072 nt lds\ns_add_u32 s60, s60, 8192\ns_add_u32 s90, s60, 896\n.Ld0q7:\nQmvaa 196,156,188\nQta 122,61,520\n"
"Qmvav 200,160,36\nQmvaa 200,164,188\nQta 124,61,4136\nQmvav 204,168,36\nQdv 36,63,36096\nQmvaa 204,172,188\nQda 188,63,36608\nQmvav 224,144,40\n"
"Qta 126,61,4648\nQmvaa 224,148,192\nQmvav 228,152,40\nQta 128,61,256\nQmvaa 228,156,192\nQmvav 232,160,40\nQta 130,61,768\nQmvaa 232,164,192\n"
"Qmvav 236,168,40\nQdv 40,63,37120\nQmvaa 236,172,192\nQda 192,63,37632\nQmaav 208,144,44\nQta 132,61,4384\nQmaaa 208,148,196\nQmaav 212,152,44\n"
"Qta 134,61,4896\nQmaaa 212,156,196\nQmaav 216,160,44\nQta 136,61,264\nQmaaa 216,164,196\nQmaav 220,168,44\nQdv 44,63,38144\nQmaaa 220,172,196\n"
"Qda 196,63,38656\nQmaav 240,144,48\nQta 138,61,776\nQmaaa 240,148,200\nQmaav 244,152,48\nQta 140,61,4392\nQmaaa 244,156,200\nQmaav 248,160,48\n"
"Qta 142,61,4904\nQmaaa 248,164,200\nQmaav 252,168,48\nQdv 48,63,39168\nQmaaa 252,172,200\nQda 200,63,39680\ns_waitcnt lgkmcnt(2)\nQmvav 80,112,24\n"
"Qmvaa 80,116,176\nQmvav 84,120,24\nQmvaa 84,124,176\nQmvav 88,128,24\nQmvaa 88,132,176\nQmvav 92,136,24\nQmvaa 92,140,176\ns_waitcnt lgkmcnt(0)\n"
"Qmvav 112,112,28\ns_waitcnt vmcnt(15)\ns_barrier\nQmvaa 112,116,180\nQta 144,61,16512\ns_mov_b32 s56, s94\ns_mov_b32 s57, s95\ns_mov_b32 s58, s75\n"
"s_mov_b32 s94, 0\ns_add_u32 m0, %10, 160768\nQmvav 116,120,28\nbuffer_load_dwordx4 v60, s[56:59], s94 offen lds\nQmvaa 116,124,180\nQta 146,61,17024\nQmvav 120,128,28\nQmvaa 120,132,180\n"
"Qta 148,61,20640\ns_add_u32 s94, s94, 4096\nQmvav 124,136,28\nbuffer_load_dwordx4 v60, s[56:59], s94 offen lds\nQmvaa 124,140,180\nQta 150,61,21152\nQmvav 144,112,32\nQmvaa 144,116,184\n"
"Qta 152,61,16520\ns_add_u32 s94, s94, 4096\nQmvav 148,120,32\nbuffer_load_dwordx4 v60, s[56:59], s94 offen lds\nQmvaa 148,124,184\nQta 154,61,17032\nQmvav 152,128,32\nQmvaa 152,132,184\n"
"Qta 156,61,20648\ns_add_u32 s94, s94, 4096\nQmvav 156,136,32\nbuffer_load_dwordx4 v60, s[56:59], s94 offen lds\nQmvaa 156,140,184\nQta 158,61,21160\nQmvav 176,112,36\nQmvaa 176,116,188\n"
"Qta 160,61,16768\ns_add_u32 s94, s94, 4096\nQmvav 180,120,36\nbuffer_load_dwordx4 v60, s[56:59], s94 offen lds\nQmvaa 180,124,188\nQta 162,61,17280\nQmvav 184,128,36\nQmvaa 184,132,188\n"
"Qta 164,61,20896\ns_add_u32 s94, s94, 4096\nQmvav 188,136,36\nbuffer_load_dwordx4 v60, s[56:59], s94 offen lds\nQmvaa 188,140,188\nQta 166,61,21408\nQmvav 208,112,40\nQmvaa 208,116,192\n"
"Qta 168,61,16776\ns_add_u32 s94, s94, 4096\nQmvav 212,120,40\nbuffer_load_dwordx4 v60, s[56:59], s94 offen lds\nQmvaa 212,124,192\nQta 170,61,17288\nQmvav 216,128,40\nQmvaa 216,132,192\n"
"Qta 172,61,20904\ns_add_u32 s94, s94, 4096\nQmvav 220,136,40\nbuffer_load_dwordx4 v60, s[56:59], s94 offen lds\nQmvaa 220,140,192\nQta 174,61,21416\nQmvav 240,112,44\nQmvaa 240,116,196\n"
"s_add_u32 s94, s94, 4096\nQmvav 244,120,44\nbuffer_load_dwordx4 v60, s[56:59], s94 offen lds\nQmvaa 244,124,196\nQmvav 248,128,44\nQmvaa 248,132,196\ns_add_u32 s94, s94, 4096\nQmvav 252,136,44\n"
"buffer_load_dwordx4 v60, s[56:59], s94 offen lds\nQmvaa 252,140,196\nQmaav 224,112,48\nQmaaa 224,116,200\nQmaav 228,120,48\nQmaaa 228,124,200\nQmaav 232,128,48\nQmaaa 232,132,200\n"
"Qmaav 236,136,48\nQmaaa 236,140,200\ns_waitcnt lgkmcnt(0)\nQmvav 96,144,24\nQmvaa 96,148,176\nQmvav 100,152,24\nQmvaa 100,156,176\nQmvav 104,160,24\n"
"Qmvaa 104,164,176\nQmvav 108,168,24\nQmvaa 108,172,176\ns_waitcnt lgkmcnt(0)\nQmvav 128,144,28\ns_waitcnt vmcnt(14)\ns_barrier\nQmvaa 128,148,180\n"
"Qdv 24,63,0\n.Lpg3:\ns_cbranch_execz .Ltr0\ns_add_u32 m0, s61, 33024\nQmvav 132,152,28\nbuffer_load_dwordx4 v69, s[40:43], s90 offen nt lds\nQmvaa 132,156,180\nQda 176,63,512\n"
"Qmvav 136,160,28\nQta 112,61,33024\nQmvaa 136,164,180\nQmvav 140,168,28\nbuffer_load_dwordx4 v69, s[40:43], s60 offen offset:1024 nt lds\nQmvaa 140,172,180\nQdv 28,63,1024\nQmvav 160,144,32\n"
"Qda 180,63,1536\nQmvaa 160,148,184\nQta 114,61,33536\nQmvav 164,152,32\nbuffer_load_dwordx4 v69, s[40:43], s90 offen offset:2048 nt lds\nQmvaa 164,156,184\nQta 116,61,37152\nQmvav 168,160,32\n"
"Qmvaa 168,164,184\nQta 118,61,37664\nQmvav 172,168,32\nbuffer_load_dwordx4 v69, s[40:43], s60 offen offset:3072 nt lds\nQmvaa 172,172,184\nQdv 32,63,2048\nQmvav 192,144,36\nQda 184,63,2560\n"
"Qmvaa 192,148,188\nQta 120,61,33032\ns_add_u32 m0, s61, 41216\nQmvav 196,152,36\nbuffer_load_dwordx4 v72, s[44:47], s90 offen nt lds\nQmvaa 196,156,188\nQta 122,61,33544\nQmvav 200,160,36\n"
"Qmvaa 200,164,188\nQta 124,61,37160\nQmvav 204,168,36\nbuffer_load_dwordx4 v72, s[44:47], s60 offen offset:1024 nt lds\nQmvaa 204,172,188\nQdv 36,63,3072\nQmvav 224,144,40\nQda 188,63,3584\n"
"Qmvaa 224,148,192\nQta 126,61,37672\nQmvav 228,152,40\nbuffer_load_dwordx4 v72, s[44:47], s90 offen offset:2048 nt lds\nQmvaa 228,156,192\nQta 128,61,33280\nQmvav 232,160,40\nQmvaa 232,164,192\n"
"Qta 130,61,33792\nQmvav 236,168,40\nbuffer_load_dwordx4 v72, s[44:47], s60 offen offset:3072 nt lds\ns_add_u32 s60, s60, 8192\ns_add_u32 s90, s60, 896\nQmvaa 236,172,192\nQdv 40,63,4096\nQmaav 208,144,44\n"
"Qda 192,63,4608\nQmaaa 208,148,196\nQta 132,61,37408\nQmaav 212,152,44\nQmaaa 212,156,196\nQta 134,61,37920\nQmaav 216,160,44\nQmaaa 216,164,196\n"
"Qta 136,61,33288\nQmaav 220,168,44\nQdv 44,63,5120\nQmaaa 220,172,196\nQda 196,63,5632\nQmaav 240,144,48\nQta 138,61,33800\nQmaaa 240,148,200\n"
"Qmaav 244,152,48\nQta 140,61,37416\nQmaaa 244,156,200\nQmaav 248,160,48\nQta 142,61,37928\nQmaaa 248,164,200\nQmaav 252,168,48\nQdv 48,63,6144\n"
"Qmaaa 252,172,200\nQda 200,63,6656\ns_waitcnt lgkmcnt(2)\nQmvav 80,112,24\nQmvaa 80,116,176\nQmvav 84,120,24\nQmvaa 84,124,176\nQmvav 88,128,24\n"
"Qmvaa 88,132,176\nQmvav 92,136,24\nQmvaa 92,140,176\ns_waitcnt lgkmcnt(0)\nQmvav 112,112,28\ns_waitcnt vmcnt(18)\ns_barrier\nQmvaa 112,116,180\n"
"Qta 144,61,49536\nQmvav 116,120,28\nQmvaa 116,124,180\nQta 146,61,50048\nQmvav 120,128,28\nQmvaa 120,132,180\nQta 148,61,53664\nQmvav 124,136,28\n"
"Qmvaa 124,140,180\nQta 150,61,54176\nQmvav 144,112,32\nQmvaa 144,116,184\nQta 152,61,49544\nQmvav 148,120,32\nQmvaa 148,124,184\nQta 154,61,50056\n"
"Qmvav 152,128,32\nQmvaa 152,132,184\nQta 156,61,53672\nQmvav 156,136,32\nQmvaa 156,140,184\nQta 158,61,54184\nQmvav 176,112,36\nQmvaa 176,116,188\n"
"Qta 160,61,49792\nQmvav 180,120,36\nQmvaa 180,124,188\nQta 162,61,50304\nQmvav 184,128,36\nQmvaa 184,132,188\nQta 164,61,53920\nQmvav 188,136,36\n"
"Qmvaa 188,140,188\nQta 166,61,54432\nQmvav 208,112,40\nQmvaa 208,116,192\nQta 168,61,49800\nQmvav 212,120,40\nQmvaa 212,124,192\nQta 170,61,50312\n"
"Qmvav 216,128,40\nQmvaa 216,132,192\nQta 172,61,53928\nQmvav 220,136,40\nQmvaa 220,140,192\nQta 174,61,54440\nQmvav 240,112,44\nQmvaa 240,116,196\n"
"Qmvav 244,120,44\nQmvaa 244,124,196\nQmvav 248,128,44\nQmvaa 248,132,196\nQmvav 252,136,44\nQmvaa 252,140,196\nQmaav 224,112,48\nQmaaa 224,116,200\n"
"Qmaav 228,120,48\nQmaaa 228,124,200\nQmaav 232,128,48\nQmaaa 232,132,200\nQmaav 236,136,48\nQmaaa 236,140,200\ns_waitcnt lgkmcnt(0)\nQmvav 96,144,24\n"
"Qmvaa 96,148,176\nQmvav 100,152,24\nQmvaa 100,156,176\nQmvav 104,160,24\nQmvaa 104,164,176\nQmvav 108,168,24\nQmvaa 108,172,176\ns_waitcnt lgkmcnt(0)\n"
"Qmvav 128,144,28\ns_waitcnt vmcnt(18)\ns_barrier\nQmvaa 128,148,180\nQda 112,68,0\ns_add_u32 m0, s61, 66048\nv_accvgpr_write_b32 a176, 0\nQmvav 132,152,28\n"
"buffer_load_dwordx4 v69, s[40:43], s90 offen nt lds\nQmvaa 132,156,180\nQda 116,68,512\nv_accvgpr_write_b32 a177, 0\nQmvav 136,160,28\nQmvaa 136,164,180\nQda 120,68,1024\nv_accvgpr_write_b32 a178, 0\n"
"Qmvav 140,168,28\nbuffer_load_dwordx4 v69, s[40:43], s60 offen offset:1024 nt lds\nQmvaa 140,172,180\nQda 124,68,1536\nv_accvgpr_write_b32 a179, 0\nQmvav 160,144,32\nQmvaa 160,148,184\nQda 128,68,2048\n"
"v_accvgpr_write_b32 a180, 0\nQmvav 164,152,32\nbuffer_load_dwordx4 v69, s[40:43], s90 offen offset:2048 nt lds\nQmvaa 164,156,184\nQda 132,68,2560\nv_accvgpr_write_b32 a181, 0\nQmvav 168,160,32\nQmvaa 168,164,184\n"
"Qda 136,68,3072\nv_accvgpr_write_b32 a182, 0\nQmvav 172,168,32\nbuffer_load_dwordx4 v69, s[40:43], s60 offen offset:3072 nt lds\nQmvaa 172,172,184\nQda 140,68,3584\nv_accvgpr_write_b32 a183, 0\nQmvav 192,144,36\n"
"Qmvaa 192,148,188\ns_add_u32 m0, s61, 74240\nv_accvgpr_write_b32 a184, 0\nQmvav 196,152,36\nbuffer_load_dwordx4 v72, s[44:47], s90 offen nt lds\nQmvaa 196,156,188\nv_accvgpr_write_b32 a185, 0\nQmvav 200,160,36\n"
"Qmvaa 200,164,188\nv_accvgpr_write_b32 a186, 0\nQmvav 204,168,36\nbuffer_load_dwordx4 v72, s[44:47], s60 offen offset:1024 nt lds\nQmvaa 204,172,188\nv_accvgpr_write_b32 a187, 0\nQmvav 224,144,40\nQmvaa 224,148,192\n"
"v_accvgpr_write_b32 a188, 0\nQmvav 228,152,40\nbuffer_load_dwordx4 v72, s[44:47], s90 offen offset:2048 nt lds\nQmvaa 228,156,192\nv_accvgpr_write_b32 a189, 0\nQmvav 232,160,40\nQmvaa 232,164,192\nv_accvgpr_write_b32 a190, 0\n"
"Qmvav 236,168,40\nbuffer_load_dwordx4 v72, s[44:47], s60 offen offset:3072 nt lds\ns_add_u32 s60, s60, 8192\ns_add_u32 s90, s60, 896\nQmvaa 236,172,192\nv_accvgpr_write_b32 a191, 0\nQmaav 208,144,44\nQmaaa 208,148,196\n"
"v_accvgpr_write_b32 a192, 0\nQmaav 212,152,44\nQmaaa 212,156,196\nv_accvgpr_write_b32 a193, 0\nQmaav 216,160,44\nQmaaa 216,164,196\nv_accvgpr_write_b32 a194, 0\nQmaav 220,168,44\n"
"Qmaaa 220,172,196\nv_accvgpr_write_b32 a195, 0\nQmaav 240,144,48\nQmaaa 240,148,200\nv_accvgpr_write_b32 a196, 0\nQmaav 244,152,48\nQmaaa 244,156,200\nv_accvgpr_write_b32 a197, 0\n"
"Qmaav 248,160,48\nQmaaa 248,164,200\nv_accvgpr_write_b32 a198, 0\nQmaav 252,168,48\nQmaaa 252,172,200\nv_accvgpr_write_b32 a199, 0\nQta 148,70,1024\nQta 150,70,3072\n"
"Qta 156,70,1280\nQta 158,70,3328\nQta 164,70,1536\nQta 166,70,3584\nQta 172,70,1792\nQta 174,70,3840\nv_pk_mul_f32 v[80:81], v[80:81], s[72:73] op_sel_hi:[1,0]\nv_pk_mul_f32 v[82:83], v[82:83], s[72:73] op_sel_hi:[1,0]\n"
"v_pk_mul_f32 v[84:85], v[84:85], s[72:73] op_sel_hi:[1,0]\nv_pk_mul_f32 v[86:87], v[86:87], s[72:73] op_sel_hi:[1,0]\nQk 80,80,81\nQk 81,82,83\nQk 82,84,85\nQk 83,86,87\nv_accvgpr_write_b32 a0, v80\nv_accvgpr_write_b32 a1, v81\n"
"v_accvgpr_write_b32 a2, v82\nv_accvgpr_write_b32 a3, v83\nv_pk_mul_f32 v[88:89], v[88:89], s[72:73] op_sel_hi:[1,0]\nv_pk_mul_f32 v[90:91], v[90:91], s[72:73] op_sel_hi:[1,0]\nv_pk_mul_f32 v[92:93], v[92:93], s[72:73] op_sel_hi:[1,0]\nv_pk_mul_f32 v[94:95], v[94:95], s[72:73] op_sel_hi:[1,0]\nQk 88,88,89\nQk 89,90,91\n"
"Qk 90,92,93\nQk 91,94,95\nv_accvgpr_write_b32 a4, v88\nv_accvgpr_write_b32 a5, v89\nv_accvgpr_write_b32 a6, v90\nv_accvgpr_write_b32 a7, v91\nv_pk_mul_f32 v[96:97], v[96:97], s[72:73] op_sel_hi:[1,0]\nv_pk_mul_f32 v[98:99], v[98:99], s[72:73] op_sel_hi:[1,0]\n"
"v_pk_mul_f32 v[100:101], v[100:101], s[72:73] op_sel_hi:[1,0]\nv_pk_mul_f32 v[102:103], v[102:103], s[72:73] op_sel_hi:[1,0]\nQk 96,96,97\nQk 97,98,99\nQk 98,100,101\nQk 99,102,103\nv_accvgpr_write_b32 a8, v96\nv_accvgpr_write_b32 a9, v97\n"
"v_accvgpr_write_b32 a10, v98\nv_accvgpr_write_b32 a11, v99\nv_pk_mul_f32 v[104:105], v[104:105], s[72:73] op_sel_hi:[1,0]\nv_pk_mul_f32 v[106:107], v[106:107], s[72:73] op_sel_hi:[1,0]\nv_pk_mul_f32 v[108:109], v[108:109], s[72:73] op_sel_hi:[1,0]\nv_pk_mul_f32 v[110:111], v[110:111], s[72:73] op_sel_hi:[1,0]\nQk 104,104,105\nQk 105,106,107\n"
"Qk 106,108,109\nQk 107,110,111\nv_accvgpr_write_b32 a12, v104\nv_accvgpr_write_b32 a13, v105\nv_accvgpr_write_b32 a14, v106\nv_accvgpr_write_b32 a15, v107\nv_pk_mul_f32 v[112:113], v[112:113], s[72:73] op_sel_hi:[1,0]\nv_pk_mul_f32 v[114:115], v[114:115], s[72:73] op_sel_hi:[1,0]\n"
"v_pk_mul_f32 v[116:117], v[116:117], s[72:73] op_sel_hi:[1,0]\nv_pk_mul_f32 v[118:119], v[118:119], s[72:73] op_sel_hi:[1,0]\nQk 112,112,113\nQk 113,114,115\nQk 114,116,117\nQk 115,118,119\nv_accvgpr_write_b32 a16, v112\nv_accvgpr_write_b32 a17, v113\n"
"v_accvgpr_write_b32 a18, v114\nv_accvgpr_write_b32 a19, v115\nv_pk_mul_f32 v[120:121], v[120:121], s[72:73] op_sel_hi:[1,0]\nv_pk_mul_f32 v[122:123], v[122:123], s[72:73] op_sel_hi:[1,0]\nv_pk_mul_f32 v[124:125], v[124:125], s[72:73] op_sel_hi:[1,0]\nv_pk_mul_f32 v[126:127], v[126:127], s[72:73] op_sel_hi:[1,0]\nQk 120,120,121\nQk 121,122,123\n"
"Qk 122,124,125\nQk 123,126,127\nv_accvgpr_write_b32 a20, v120\nv_accvgpr_write_b32 a21, v121\nv_accvgpr_write_b32 a22, v122\nv_accvgpr_write_b32 a23, v123\nv_pk_mul_f32 v[128:129], v[128:129], s[72:73] op_sel_hi:[1,0]\nv_pk_mul_f32 v[130:131], v[130:131], s[72:73] op_sel_hi:[1,0]\n"
"v_pk_mul_f32 v[132:133], v[132:133], s[72:73] op_sel_hi:[1,0]\nv_pk_mul_f32 v[134:135], v[134:135], s[72:73] op_sel_hi:[1,0]\nQk 128,128,129\nQk 129,130,131\nQk 130,132,133\nQk 131,134,135\nv_accvgpr_write_b32 a24, v128\nv_accvgpr_write_b32 a25, v129\n"
"v_accvgpr_write_b32 a26, v130\nv_accvgpr_write_b32 a27, v131\nv_pk_mul_f32 v[136:137], v[136:137], s[72:73] op_sel_hi:[1,0]\nv_pk_mul_f32 v[138:139], v[138:139], s[72:73] op_sel_hi:[1,0]\nv_pk_mul_f32 v[140:141], v[140:141], s[72:73] op_sel_hi:[1,0]\nv_pk_mul_f32 v[142:143], v[142:143], s[72:73] op_sel_hi:[1,0]\nQk 136,136,137\nQk 137,138,139\n"
"Qk 138,140,141\nQk 139,142,143\nv_accvgpr_write_b32 a28, v136\nv_accvgpr_write_b32 a29, v137\nv_accvgpr_write_b32 a30, v138\nv_accvgpr_write_b32 a31, v139\nv_pk_mul_f32 v[144:145], v[144:145], s[72:73] op_sel_hi:[1,0]\nv_pk_mul_f32 v[146:147], v[146:147], s[72:73] op_sel_hi:[1,0]\n"
"v_pk_mul_f32 v[148:149], v[148:149], s[72:73] op_sel_hi:[1,0]\nv_pk_mul_f32 v[150:151], v[150:151], s[72:73] op_sel_hi:[1,0]\nQk 144,144,145\nQk 145,146,147\nQk 146,148,149\nQk 147,150,151\nv_accvgpr_write_b32 a32, v144\nv_accvgpr_write_b32 a33, v145\n"
"v_accvgpr_write_b32 a34, v146\nv_accvgpr_write_b32 a35, v147\nv_pk_mul_f32 v[152:153], v[152:153], s[72:73] op_sel_hi:[1,0]\nv_pk_mul_f32 v[154:155], v[154:155], s[72:73] op_sel_hi:[1,0]\nv_pk_mul_f32 v[156:157], v[156:157], s[72:73] op_sel_hi:[1,0]\nv_pk_mul_f32 v[158:159], v[158:159], s[72:73] op_sel_hi:[1,0]\nQk 152,152,153\nQk 153,154,155\n"
"Qk 154,156,157\nQk 155,158,159\nv_accvgpr_write_b32 a36, v152\nv_accvgpr_write_b32 a37, v153\nv_accvgpr_write_b32 a38, v154\nv_accvgpr_write_b32 a39, v155\nv_pk_mul_f32 v[160:161], v[160:161], s[72:73] op_sel_hi:[1,0]\nv_pk_mul_f32 v[162:163], v[162:163], s[72:73] op_sel_hi:[1,0]\n"
"v_pk_mul_f32 v[164:165], v[164:165], s[72:73] op_sel_hi:[1,0]\nv_pk_mul_f32 v[166:167], v[166:167], s[72:73] op_sel_hi:[1,0]\nQk 160,160,161\nQk 161,162,163\nQk 162,164,165\nQk 163,166,167\nv_accvgpr_write_b32 a40, v160\nv_accvgpr_write_b32 a41, v161\n"
"v_accvgpr_write_b32 a42, v162\nv_accvgpr_write_b32 a43, v163\nv_pk_mul_f32 v[168:169], v[168:169], s[72:73] op_sel_hi:[1,0]\nv_pk_mul_f32 v[170:171], v[170:171], s[72:73] op_sel_hi:[1,0]\nv_pk_mul_f32 v[172:173], v[172:173], s[72:73] op_sel_hi:[1,0]\nv_pk_mul_f32 v[174:175], v[174:175], s[72:73] op_sel_hi:[1,0]\nQk 168,168,169\nQk 169,170,171\n"
"Qk 170,172,173\nQk 171,174,175\nv_accvgpr_write_b32 a44, v168\nv_accvgpr_write_b32 a45, v169\nv_accvgpr_write_b32 a46, v170\nv_accvgpr_write_b32 a47, v171\nv_pk_mul_f32 v[176:177], v[176:177], s[72:73] op_sel_hi:[1,0]\nv_pk_mul_f32 v[178:179], v[178:179], s[72:73] op_sel_hi:[1,0]\n"
"v_pk_mul_f32 v[180:181], v[180:181], s[72:73] op_sel_hi:[1,0]\nv_pk_mul_f32 v[182:183], v[182:183], s[72:73] op_sel_hi:[1,0]\nQk 176,176,177\nQk 177,178,179\nQk 178,180,181\nQk 179,182,183\nv_accvgpr_write_b32 a48, v176\nv_accvgpr_write_b32 a49, v177\n"
"v_accvgpr_write_b32 a50, v178\nv_accvgpr_write_b32 a51, v179\nv_pk_mul_f32 v[184:185], v[184:185], s[72:73] op_sel_hi:[1,0]\nv_pk_mul_f32 v[186:187], v[186:187], s[72:73] op_sel_hi:[1,0]\nv_pk_mul_f32 v[188:189], v[188:189], s[72:73] op_sel_hi:[1,0]\nv_pk_mul_f32 v[190:191], v[190:191], s[72:73] op_sel_hi:[1,0]\nQk 184,184,185\nQk 185,186,187\n"
"Qk 186,188,189\nQk 187,190,191\nv_accvgpr_write_b32 a52, v184\nv_accvgpr_write_b32 a53, v185\nv_accvgpr_write_b32 a54, v186\nv_accvgpr_write_b32 a55, v187\nv_pk_mul_f32 v[192:193], v[192:193], s[72:73] op_sel_hi:[1,0]\nv_pk_mul_f32 v[194:195], v[194:195], s[72:73] op_sel_hi:[1,0]\n"
"v_pk_mul_f32 v[196:197], v[196:197], s[72:73] op_sel_hi:[1,0]\nv_pk_mul_f32 v[198:199], v[198:199], s[72:73] op_sel_hi:[1,0]\nQk 192,192,193\nQk 193,194,195\nQk 194,196,197\nQk 195,198,199\nv_accvgpr_write_b32 a56, v192\nv_accvgpr_write_b32 a57, v193\n"
"v_accvgpr_write_b32 a58, v194\nv_accvgpr_write_b32 a59, v195\nv_pk_mul_f32 v[200:201], v[200:201], s[72:73] op_sel_hi:[1,0]\nv_pk_mul_f32 v[202:203], v[202:203], s[72:73] op_sel_hi:[1,0]\nv_pk_mul_f32 v[204:205], v[204:205], s[72:73] op_sel_hi:[1,0]\nv_pk_mul_f32 v[206:207], v[206:207], s[72:73] op_sel_hi:[1,0]\nQk 200,200,201\nQk 201,202,203\n"
"Qk 202,204,205\nQk 203,206,207\nv_accvgpr_write_b32 a60, v200\nv_accvgpr_write_b32 a61, v201\nv_accvgpr_write_b32 a62, v202\nv_accvgpr_write_b32 a63, v203\nv_pk_mul_f32 v[208:209], v[208:209], s[72:73] op_sel_hi:[1,0]\nv_pk_mul_f32 v[210:211], v[210:211], s[72:73] op_sel_hi:[1,0]\n"
"v_pk_mul_f32 v[212:213], v[212:213], s[72:73] op_sel_hi:[1,0]\nv_pk_mul_f32 v[214:215], v[214:215], s[72:73] op_sel_hi:[1,0]\nQk 208,208,209\nQk 209,210,211\nQk 210,212,213\nQk 211,214,215\nv_accvgpr_write_b32 a64, v208\nv_accvgpr_write_b32 a65, v209\n"
"v_accvgpr_write_b32 a66, v210\nv_accvgpr_write_b32 a67, v211\nv_pk_mul_f32 v[216:217], v[216:217], s[72:73] op_sel_hi:[1,0]\nv_pk_mul_f32 v[218:219], v[218:219], s[72:73] op_sel_hi:[1,0]\nv_pk_mul_f32 v[220:221], v[220:221], s[72:73] op_sel_hi:[1,0]\nv_pk_mul_f32 v[222:223], v[222:223], s[72:73] op_sel_hi:[1,0]\nQk 216,216,217\nQk 217,218,219\n"
"Qk 218,220,221\nQk 219,222,223\nv_accvgpr_write_b32 a68, v216\nv_accvgpr_write_b32 a69, v217\nv_accvgpr_write_b32 a70, v218\nv_accvgpr_write_b32 a71, v219\nv_pk_mul_f32 v[224:225], v[224:225], s[72:73] op_sel_hi:[1,0]\nv_pk_mul_f32 v[226:227], v[226:227], s[72:73] op_sel_hi:[1,0]\n"
"v_pk_mul_f32 v[228:229], v[228:229], s[72:73] op_sel_hi:[1,0]\nv_pk_mul_f32 v[230:231], v[230:231], s[72:73] op_sel_hi:[1,0]\nQk 224,224,225\nQk 225,226,227\nQk 226,228,229\nQk 227,230,231\nv_accvgpr_write_b32 a72, v224\nv_accvgpr_write_b32 a73, v225\n"
"v_accvgpr_write_b32 a74, v226\nv_accvgpr_write_b32 a75, v227\nv_pk_mul_f32 v[232:233], v[232:233], s[72:73] op_sel_hi:[1,0]\nv_pk_mul_f32 v[234:235], v[234:235], s[72:73] op_sel_hi:[1,0]\nv_pk_mul_f32 v[236:237], v[236:237], s[72:73] op_sel_hi:[1,0]\nv_pk_mul_f32 v[238:239], v[238:239], s[72:73] op_sel_hi:[1,0]\nQk 232,232,233\nQk 233,234,235\n"
"Qk 234,236,237\nQk 235,238,239\nv_accvgpr_write_b32 a76, v232\nv_accvgpr_write_b32 a77, v233\nv_accvgpr_write_b32 a78, v234\nv_accvgpr_write_b32 a79, v235\nv_pk_mul_f32 v[240:241], v[240:241], s[72:73] op_sel_hi:[1,0]\n.Lpg4:\n"
"s_cbranch_execz .Ltr9\nv_pk_mul_f32 v[242:243], v[242:243], s[72:73] op_sel_hi:[1,0]\nv_pk_mul_f32 v[244:245], v[244:245], s[72:73] op_sel_hi:[1,0]\nv_pk_mul_f32 v[246:247], v[246:247], s[72:73] op_sel_hi:[1,0]\nQk 240,240,241\nQk 241,242,243\nQk 242,244,245\nQk 243,246,247\n"
"v_accvgpr_write_b32 a80, v240\nv_accvgpr_write_b32 a81, v241\nv_accvgpr_write_b32 a82, v242\nv_accvgpr_write_b32 a83, v243\nv_pk_mul_f32 v[248:249], v[248:249], s[72:73] op_sel_hi:[1,0]\nv_pk_mul_f32 v[250:251], v[250:251], s[72:73] op_sel_hi:[1,0]\nv_pk_mul_f32 v[252:253], v[252:253], s[72:73] op_sel_hi:[1,0]\nv_pk_mul_f32 v[254:255], v[254:255], s[72:73] op_sel_hi:[1,0]\n"
"Qk 248,248,249\nQk 249,250,251\nQk 250,252,253\nQk 251,254,255\nv_accvgpr_write_b32 a84, v248\nv_accvgpr_write_b32 a85, v249\nv_accvgpr_write_b32 a86, v250\nv_accvgpr_write_b32 a87, v251\n"
"v_accvgpr_read_b32 v0, a208\nv_accvgpr_read_b32 v1, a209\nv_accvgpr_read_b32 v2, a210\nv_accvgpr_read_b32 v3, a211\nv_accvgpr_read_b32 v4, a212\nv_accvgpr_read_b32 v5, a213\nv_accvgpr_read_b32 v6, a214\nv_accvgpr_read_b32 v7, a215\n"
"v_pk_mul_f32 v[0:1], v[0:1], s[72:73] op_sel_hi:[1,0]\nv_pk_mul_f32 v[2:3], v[2:3], s[72:73] op_sel_hi:[1,0]\nv_pk_mul_f32 v[4:5], v[4:5], s[72:73] op_sel_hi:[1,0]\nv_pk_mul_f32 v[6:7], v[6:7], s[72:73] op_sel_hi:[1,0]\nQk 0,0,1\nQk 1,2,3\nQk 2,4,5\nQk 3,6,7\n"
"v_accvgpr_write_b32 a88, v0\nv_accvgpr_write_b32 a89, v1\nv_accvgpr_write_b32 a90, v2\nv_accvgpr_write_b32 a91, v3\nv_accvgpr_read_b32 v8, a216\nv_accvgpr_read_b32 v9, a217\nv_accvgpr_read_b32 v10, a218\nv_accvgpr_read_b32 v11, a219\n"
"v_accvgpr_read_b32 v12, a220\nv_accvgpr_read_b32 v13, a221\nv_accvgpr_read_b32 v14, a222\nv_accvgpr_read_b32 v15, a223\nv_pk_mul_f32 v[8:9], v[8:9], s[72:73] op_sel_hi:[1,0]\nv_pk_mul_f32 v[10:11], v[10:11], s[72:73] op_sel_hi:[1,0]\nv_pk_mul_f32 v[12:13], v[12:13], s[72:73] op_sel_hi:[1,0]\nv_pk_mul_f32 v[14:15], v[14:15], s[72:73] op_sel_hi:[1,0]\n"
"Qk 8,8,9\nQk 9,10,11\nQk 10,12,13\nQk 11,14,15\nv_accvgpr_write_b32 a92, v8\nv_accvgpr_write_b32 a93, v9\nv_accvgpr_write_b32 a94, v10\nv_accvgpr_write_b32 a95, v11\n"
"v_accvgpr_read_b32 v0, a224\nv_accvgpr_read_b32 v1, a225\nv_accvgpr_read_b32 v2, a226\nv_accvgpr_read_b32 v3, a227\nv_accvgpr_read_b32 v4, a228\nv_accvgpr_read_b32 v5, a229\nv_accvgpr_read_b32 v6, a230\nv_accvgpr_read_b32 v7, a231\n"
"v_pk_mul_f32 v[0:1], v[0:1], s[72:73] op_sel_hi:[1,0]\nv_pk_mul_f32 v[2:3], v[2:3], s[72:73] op_sel_hi:[1,0]\nv_pk_mul_f32 v[4:5], v[4:5], s[72:73] op_sel_hi:[1,0]\nv_pk_mul_f32 v[6:7], v[6:7], s[72:73] op_sel_hi:[1,0]\nQk 0,0,1\nQk 1,2,3\nQk 2,4,5\nQk 3,6,7\n"
"v_accvgpr_write_b32 a96, v0\nv_accvgpr_write_b32 a97, v1\nv_accvgpr_write_b32 a98, v2\nv_accvgpr_write_b32 a99, v3\nv_accvgpr_read_b32 v8, a232\nv_accvgpr_read_b32 v9, a233\nv_accvgpr_read_b32 v10, a234\nv_accvgpr_read_b32 v11, a235\n"
"v_accvgpr_read_b32 v12, a236\nv_accvgpr_read_b32 v13, a237\nv_accvgpr_read_b32 v14, a238\nv_accvgpr_read_b32 v15, a239\nv_pk_mul_f32 v[8:9], v[8:9], s[72:73] op_sel_hi:[1,0]\nv_pk_mul_f32 v[10:11], v[10:11], s[72:73] op_sel_hi:[1,0]\nv_pk_mul_f32 v[12:13], v[12:13], s[72:73] op_sel_hi:[1,0]\nv_pk_mul_f32 v[14:15], v[14:15], s[72:73] op_sel_hi:[1,0]\n"
"Qk 8,8,9\nQk 9,10,11\nQk 10,12,13\nQk 11,14,15\nv_accvgpr_write_b32 a100, v8\nv_accvgpr_write_b32 a101, v9\nv_accvgpr_write_b32 a102, v10\nv_accvgpr_write_b32 a103, v11\n"
"v_accvgpr_read_b32 v0, a240\nv_accvgpr_read_b32 v1, a241\nv_accvgpr_read_b32 v2, a242\nv_accvgpr_read_b32 v3, a243\nv_accvgpr_read_b32 v4, a244\nv_accvgpr_read_b32 v5, a245\nv_accvgpr_read_b32 v6, a246\nv_accvgpr_read_b32 v7, a247\n"
"v_pk_mul_f32 v[0:1], v[0:1], s[72:73] op_sel_hi:[1,0]\nv_pk_mul_f32 v[2:3], v[2:3], s[72:73] op_sel_hi:[1,0]\nv_pk_mul_f32 v[4:5], v[4:5], s[72:73] op_sel_hi:[1,0]\nv_pk_mul_f32 v[6:7], v[6:7], s[72:73] op_sel_hi:[1,0]\nQk 0,0,1\nQk 1,2,3\nQk 2,4,5\nQk 3,6,7\n"
"v_accvgpr_write_b32 a104, v0\nv_accvgpr_write_b32 a105, v1\nv_accvgpr_write_b32 a106, v2\nv_accvgpr_write_b32 a107, v3\nv_accvgpr_read_b32 v8, a248\nv_accvgpr_read_b32 v9, a249\nv_accvgpr_read_b32 v10, a250\nv_accvgpr_read_b32 v11, a251\n"
"v_accvgpr_read_b32 v12, a252\nv_accvgpr_read_b32 v13, a253\nv_accvgpr_read_b32 v14, a254\nv_accvgpr_read_b32 v15, a255\nv_pk_mul_f32 v[8:9], v[8:9], s[72:73] op_sel_hi:[1,0]\nv_pk_mul_f32 v[10:11], v[10:11], s[72:73] op_sel_hi:[1,0]\nv_pk_mul_f32 v[12:13], v[12:13], s[72:73] op_sel_hi:[1,0]\nv_pk_mul_f32 v[14:15], v[14:15], s[72:73] op_sel_hi:[1,0]\n"
"Qk 8,8,9\nQk 9,10,11\nQk 10,12,13\nQk 11,14,15\nv_accvgpr_write_b32 a108, v8\nv_accvgpr_write_b32 a109, v9\nv_accvgpr_write_b32 a110, v10\nv_accvgpr_write_b32 a111, v11\n"
"v_mov_b32 v66, 0x3f803f80\nv_accvgpr_write_b32 a200, 0\nv_accvgpr_write_b32 a201, 0\nv_accvgpr_write_b32 a202, 0\nv_accvgpr_write_b32 a203, 0\ns_mov_b32 vcc_lo, 0x33333333\ns_mov_b32 vcc_hi, 0xcccccccc\nv_cndmask_b32 v66, 0, v66, vcc\n"
"v_accvgpr_write_b32 a204, v66\nv_accvgpr_write_b32 a205, v66\nv_accvgpr_write_b32 a206, v66\nv_accvgpr_write_b32 a207, v66\ns_add_u32 s87, s87, 2048\ns_waitcnt lgkmcnt(8)\nQzvaa 0,112,0\nQmvaa 0,116,4\n"
"Qmvaa 0,120,8\nQmvaa 0,124,12\nQzvaa 4,128,0\nQmvaa 4,132,4\nQmvaa 4,136,8\nQmvaa 4,140,12\ns_nop 3\nv_max3_f32 v66, v0, v1, v2\n"
"s_nop 2\nv_max3_f32 v66, v66, v3, v4\nv_max3_f32 v66, v66, v5, v6\nv_max_f32 v66, v66, v7\nQzvaa 8,112,16\nv_mov_b32 v67, v66\nQmvaa 8,116,20\ns_nop 0\n"
"v_permlane32_swap_b32 v66, v67\nv_max_f32 v66, v66, v67\nQmvaa 8,120,24\nv_mov_b32 v67, v66\nQmvaa 8,124,28\ns_nop 0\nv_permlane16_swap_b32 v66, v67\nv_max_f32 v66, v66, v67\n"
"Qzvaa 12,128,16\nv_sub_f32 v56, 0xc1b00000, v66\nv_mov_b32 v57, v56\nQmvaa 12,132,20\nv_mov_b32 v58, v56\nv_mov_b32 v59, v56\nQmvaa 12,136,24\nQa 0,0,56\n"
"Qa 1,1,56\nQmvaa 12,140,28\nQa 2,2,56\nQa 3,3,56\nQe 0\nQe 1\nQe 2\nQe 3\n"
"Qk 16,0,1\nQk 17,2,3\nQa 4,4,56\nQa 5,5,56\nQa 6,6,56\nQa 7,7,56\nQe 4\nQe 5\n"
"Qcvaa 0,112,32,56\nQe 6\nQmvaa 0,116,36\nQe 7\nQmvaa 0,120,40\nQk 18,4,5\nQk 19,6,7\nQmvaa 0,124,44\n"
"Qa 8,8,56\nQa 9,9,56\nQcvaa 4,128,32,56\nQa 10,10,56\nQa 11,11,56\nQmvaa 4,132,36\nQe 8\nQmvaa 4,136,40\n"
"Qe 9\nQmvaa 4,140,44\ns_waitcnt lgkmcnt(0)\nQk 20,8,9\nQzvav 96,148,16\nQe 10\nQzvav 100,156,16\nQe 11\n"
"Qzvav 104,164,16\nQk 21,10,11\nQa 12,12,56\nQzvav 108,172,16\nQa 13,13,56\nQa 14,14,56\nQcvaa 8,112,48,56\nQa 15,15,56\n"
"Qmvaa 8,116,52\nQe 12\nQmvaa 8,120,56\nQe 13\nQe 14\nQe 15\nQmvaa 8,124,60\nQk 22,12,13\n"
"Qk 23,14,15\nQcvaa 12,128,48,56\nQe 0\nQmvaa 12,132,52\nQe 1\nv_smfmac_f32_16x16x64_bf16 a[176:179], a[204:207], v[16:23], v77\nQk 24,0,1\nQmvaa 12,136,56\n"
"Qe 2\nQmvaa 12,140,60\nQe 3\ns_add_u32 m0, s61, 99072\nQzvav 128,148,20\nbuffer_load_dwordx4 v69, s[40:43], s90 offen nt lds\nQzvav 132,156,20\nQk 25,2,3\n"
"Qzvav 136,164,20\nQe 4\nbuffer_load_dwordx4 v69, s[40:43], s60 offen offset:1024 nt lds\nQzvav 140,172,20\nQe 5\nQcvaa 0,112,64,56\nQe 6\nQmvaa 0,116,68\n"
"Qe 7\nQmvaa 0,120,72\nQk 26,4,5\nQk 27,6,7\nQmvaa 0,124,76\nbuffer_load_dwordx4 v69, s[40:43], s90 offen offset:2048 nt lds\nQcvaa 4,128,64,56\nQe 8\n"
"Qmvaa 4,132,68\nQe 9\nQmvaa 4,136,72\nQe 10\nQmvaa 4,140,76\nQe 11\nbuffer_load_dwordx4 v69, s[40:43], s60 offen offset:3072 nt lds\nQzvav 160,148,24\n"
"Qk 28,8,9\nQk 29,10,11\nQzvav 164,156,24\nQe 12\ns_add_u32 m0, s61, 107264\nQzvav 168,164,24\nbuffer_load_dwordx4 v72, s[44:47], s90 offen nt lds\nQzvav 172,172,24\n"
"Qe 13\nQcvaa 8,112,80,56\nQe 14\nQmvaa 8,116,84\nQe 15\nQmvaa 8,120,88\nbuffer_load_dwordx4 v72, s[44:47], s60 offen offset:1024 nt lds\nQmvaa 8,124,92\n"
"Qk 30,12,13\nQk 31,14,15\nQcvaa 12,128,80,56\nQe 0\nQmvaa 12,132,84\nQe 1\nv_smfmac_f32_16x16x64_bf16 a[180:183], a[204:207], v[24:31], v77\nQk 32,0,1\n"
"Qmvaa 12,136,88\nbuffer_load_dwordx4 v72, s[44:47], s90 offen offset:2048 nt lds\nQmvaa 12,140,92\nQe 2\nQzvav 192,148,28\nQe 3\ns_waitcnt vmcnt(15)\ns_barrier\n"
"Qzvav 196,156,28\nbuffer_load_dwordx4 v72, s[44:47], s60 offen offset:3072 nt lds\nQzvav 200,164,28\nQk 33,2,3\ns_add_u32 s60, s60, 8192\ns_and_b32 s68, s60, 0x40000\ns_mul_i32 s68, s68, 7\ns_add_u32 s60, s60, s68\n"
"s_add_u32 s90, s60, 896\nQzvav 204,172,28\nQe 4\nQcvaa 0,112,96,56\nQda 112,68,33024\nQmvaa 0,116,100\nQda 116,68,33536\nQmvaa 0,120,104\n"
"Qda 120,68,34048\nQe 5\nQe 6\nQe 7\nQmvaa 0,124,108\nQda 124,68,34560\nQk 34,4,5\nQk 35,6,7\n"
"Qcvaa 4,128,96,56\nQda 128,68,35072\nQmvaa 4,132,100\nQda 132,68,35584\nQmvaa 4,136,104\nQda 136,68,36096\nQmvaa 4,140,108\nQda 140,68,36608\n"
"Qzvav 224,148,32\ns_memrealtime s[74:75]\nQzvav 228,156,32\nQe 8\nQzvav 232,164,32\nQe 9\nQe 10\nQe 11\n"
"Qzvav 236,172,32\ns_waitcnt lgkmcnt(0)\nQk 36,8,9\nQk 37,10,11\nQcvaa 8,112,0,56\nQe 12\nQmvaa 8,116,4\nv_add_u32 v68, 66048, v68\n"
"Qmvaa 8,120,8\nQe 13\nQe 14\nQe 15\nQmvaa 8,124,12\nQk 38,12,13\nQk 39,14,15\nQcvaa 12,128,0,56\n"
"Qe 0\nQmvaa 12,132,4\nQe 1\nv_smfmac_f32_16x16x64_bf16 a[184:187], a[204:207], v[32:39], v77\nQk 40,0,1\nQmvaa 12,136,8\nQe 2\nQmvaa 12,140,12\n"
"Qe 3\ns_waitcnt lgkmcnt(0)\nQzaav 208,148,36\ns_mov_b32 s73, 140\nQk 41,2,3\ns_add_u32 s77, s74, s73\nQzaav 212,156,36\ns_sub_u32 s73, s73, 4\n"
"s_max_u32 s73, s73, 120\nQzaav 216,164,36\nQe 4\nQzaav 220,172,36\nQe 8\nQcvaa 0,112,16,56\nQe 9\nQmvaa 0,116,20\n"
"Qe 10\nQmvaa 0,120,24\nQe 11\nQe 5\nQe 6\nQe 7\nQmvaa 0,124,28\nQk 44,8,9\n"
"Qk 45,10,11\nQk 42,4,5\nQk 43,6,7\nQcvaa 4,128,16,56\nQe 12\nQmvaa 4,132,20\nQe 13\nQmvaa 4,136,24\n"
"Qe 14\nQmvaa 4,140,28\nQe 15\nQta 144,70,0\nQta 146,70,2048\nQzaav 240,148,40\nQta 148,70,33024\nQta 150,70,35072\n"
"Qta 152,70,256\nQta 154,70,2304\nQzaav 244,156,40\nQta 156,70,33280\nQta 158,70,35328\nQta 160,70,512\nQta 162,70,2560\nQzaav 248,164,40\n"
"Qta 164,70,33536\nQta 166,70,35584\nQta 168,70,768\nQta 170,70,2816\nQzaav 252,172,40\nQta 172,70,33792\nQta 174,70,35840\nQcvaa 8,112,32,56\n"
"v_add_u32 v75, 1, v75\nQk 46,12,13\nQmvaa 8,116,36\ns_waitcnt vmcnt(8)\nds_write_b32 v73, v75\nQmvaa 8,120,40\nQk 47,14,15\nQmvaa 8,124,44\n"
"Qe 0\nQcvaa 12,128,32,56\nQe 1\nQmvaa 12,132,36\nQe 2\nv_smfmac_f32_16x16x64_bf16 a[188:191], a[204:207], v[40:47], v77\nQk 48,0,1\nQmvaa 12,136,40\n"
"s_waitcnt lgkmcnt(1)\nQmvaa 12,140,44\nQe 3\nQzvav 80,144,16\nQk 49,2,3\nQmvav 80,148,44\nQe 4\nQzvav 84,152,16\n"
"Qe 5\nQmvav 84,156,44\nQe 6\nQzvav 88,160,16\nQe 7\nQmvav 88,164,44\nQk 50,4,5\nQk 51,6,7\n"
"Qzvav 92,168,16\nQe 8\nQmvav 92,172,44\nQe 9\nQcvaa 0,112,48,56\nQe 10\nQmvaa 0,116,52\nds_read_b32 v66, v74\n"
"Qk 52,8,9\nQmvaa 0,120,56\nQe 11\nQmvaa 0,124,60\nQk 53,10,11\nQcvaa 4,128,48,56\nQe 12\nQmvaa 4,132,52\n"
"Qe 13\nQmvaa 4,136,56\ns_waitcnt lgkmcnt(0)\nv_cmp_gt_u32 vcc, v75, v66\nQmvaa 4,140,60\ns_cbranch_vccnz .Lsa1\n.Lra1:\nQk 54,12,13\n"
"Qzvav 112,144,20\ns_add_u32 m0, s61, 0\nQmvav 112,148,48\nbuffer_load_dwordx4 v69, s[40:43], s90 offen nt lds\nQzvav 116,152,20\nQe 14\nQmvav 116,156,48\nQe 15\n"
"Qzvav 120,160,20\nQk 55,14,15\nQmvav 120,164,48\nQe 0\nQzvav 124,168,20\nbuffer_load_dwordx4 v69, s[40:43], s60 offen offset:1024 nt lds\nQmvav 124,172,48\nQe 1\n"
"Qcvaa 8,112,64,56\nQe 2\nQmvaa 8,116,68\nQe 3\nQmvaa 8,120,72\nQk 20,0,1\nQk 21,2,3\nQmvaa 8,124,76\n"
"buffer_load_dwordx4 v69, s[40:43], s90 offen offset:2048 nt lds\nQcvaa 12,128,64,56\nQe 4\nQmvaa 12,132,68\nQe 5\nv_smfmac_f32_16x16x64_bf16 a[192:195], a[204:207], v[48:55], v77\nQk 22,4,5\nQmvaa 12,136,72\n"
"Qe 6\nQmvaa 12,140,76\nbuffer_load_dwordx4 v69, s[40:43], s60 offen offset:3072 nt lds\nQzvav 144,144,24\nQe 7\nQmvav 144,148,52\nQk 23,6,7\nQzvav 148,152,24\n"
"Qe 8\nQmvav 148,156,52\ns_add_u32 m0, s61, 8192\nQzvav 152,160,24\nbuffer_load_dwordx4 v72, s[44:47], s90 offen nt lds\nQmvav 152,164,52\nQe 9\nQzvav 156,168,24\n"
"Qe 10\nQmvav 156,172,52\nQe 11\nQcvaa 0,112,80,56\nQk 24,8,9\nQk 25,10,11\nQmvaa 0,116,84\nbuffer_load_dwordx4 v72, s[44:47], s60 offen offset:1024 nt lds\n"
"Qmvaa 0,120,88\nQe 12\nQmvaa 0,124,92\nQe 13\nQcvaa 4,128,80,56\nQe 14\nQmvaa 4,132,84\nQe 15\n"
"Qmvaa 4,136,88\nbuffer_load_dwordx4 v72, s[44:47], s90 offen offset:2048 nt lds\nQmvaa 4,140,92\nQk 26,12,13\nQk 27,14,15\nQzvav 176,144,28\nQmvav 176,148,20\nQzvav 180,152,28\n"
"Qmvav 180,156,20\nbuffer_load_dwordx4 v72, s[44:47], s60 offen offset:3072 nt lds\nQzvav 184,160,28\nQe 0\nQmvav 184,164,20\nQe 1\nQzvav 188,168,28\ns_add_u32 s60, s60, 8192\n"
"s_add_u32 s90, s60, 896\nQmvav 188,172,20\nQe 2\nQcvaa 8,112,96,56\nQda 112,68,0\nQk 28,0,1\nQmvaa 8,116,100\nQda 116,68,512\n"
"Qmvaa 8,120,104\nQda 120,68,1024\nQmvaa 8,124,108\n.Lpg5:\ns_cbranch_execz .Ltr9\nQda 124,68,1536\nQcvaa 12,128,96,56\nQda 128,68,2048\n"
"Qmvaa 12,132,100\nQda 132,68,2560\nv_smfmac_f32_16x16x64_bf16 a[196:199], a[204:207], v[20:27], v77\nQmvaa 12,136,104\nQda 136,68,3072\nQmvaa 12,140,108\nQda 140,68,3584\nQzvav 208,144,32\n"
"s_memrealtime s[74:75]\nQmvav 208,148,24\nQe 3\nQzvav 212,152,32\nQk 29,2,3\nQmvav 212,156,24\nQe 4\nQzvav 216,160,32\n"
"Qe 5\nQmvav 216,164,24\nQe 6\nQzvav 220,168,32\nQe 7\nQmvav 220,172,24\ns_waitcnt lgkmcnt(0)\nQk 30,4,5\n"
"Qcvaa 0,112,0,56\nQk 31,6,7\nQmvaa 0,116,4\nQe 8\nQmvaa 0,120,8\nQe 9\nQmvaa 0,124,12\nQe 10\n"
"Qcvaa 4,128,0,56\nQe 11\nQmvaa 4,132,4\nQk 32,8,9\nQk 33,10,11\nQmvaa 4,136,8\nQe 12\nQmvaa 4,140,12\n"
"Qe 13\nQzvav 240,144,36\nQe 14\nQmvav 240,148,28\ns_waitcnt lgkmcnt(0)\ns_sub_u32 s75, s77, s74\nQzvav 244,152,36\ns_cmp_lt_u32 s75, 256\n"
"s_cbranch_scc1 .Lsp1\n.Lrp1:\nQmvav 244,156,28\ns_add_u32 s77, s74, s73\nQk 34,12,13\nQzvav 248,160,36\ns_sub_u32 s73, s73, 4\nQmvav 248,164,28\n"
"s_max_u32 s73, s73, 120\nQzvav 252,168,36\nQe 15\nQmvav 252,172,28\nQk 35,14,15\nQcvaa 8,112,16,56\nQe 0\nQmvaa 8,116,20\n"
"Qe 1\nQmvaa 8,120,24\nQe 2\nQmvaa 8,124,28\nQe 3\nQcvaa 12,128,16,56\nQk 36,0,1\nQk 37,2,3\n"
"Qmvaa 12,132,20\nQe 4\nv_smfmac_f32_16x16x64_bf16 a[200:203], a[204:207], v[28:35], v77\nQmvaa 12,136,24\nQe 5\nQmvaa 12,140,28\nQe 6\nQzaav 224,144,40\n"
"Qta 144,70,34048\nQta 146,70,36096\nQmaav 224,148,32\nQta 148,71,1024\nQta 150,71,3072\nQzaav 228,152,40\nQta 152,70,34304\nQta 154,70,36352\n"
"Qmaav 228,156,32\nQta 156,71,1280\nQta 158,71,3328\nQzaav 232,160,40\nQta 160,70,34560\nQta 162,70,36608\nQmaav 232,164,32\nQta 164,71,1536\n"
"Qta 166,71,3584\nQzaav 236,168,40\nQta 168,70,34816\nQta 170,70,36864\nQmaav 236,172,32\nQta 172,71,1792\nQta 174,71,3840\ns_lshl_b32 s66, s67, 3\n"
".Lloop:\ns_bitcmp1_b32 s99, s66\ns_cbranch_scc1 .Lhook\n.Lhookret:\nQcvaa 0,112,32,56\nv_add_u32 v75, 1, v75\nQk 38,4,5\nQmvaa 0,116,36\n"
"s_waitcnt vmcnt(8)\nds_write_b32 v73, v75\nQmvaa 0,120,40\nQe 7\nQmvaa 0,124,44\nQk 39,6,7\nQcvaa 4,128,32,56\nQe 8\n"
"Qmvaa 4,132,36\nQe 9\nQmvaa 4,136,40\nQe 10\nQmvaa 4,140,44\ns_waitcnt lgkmcnt(1)\nQk 40,8,9\nQmvav 96,144,44\n"
"Qe 11\nQmvav 96,148,36\nQk 41,10,11\nQmvav 100,152,44\nQe 12\nQmvav 100,156,36\nQe 13\nQmvav 104,160,44\n"
"Qe 14\nQmvav 104,164,36\nQe 15\nQmvav 108,168,44\nQk 42,12,13\nQk 43,14,15\nQmvav 108,172,36\nQe 0\n"
"Qcvaa 8,112,48,56\nQe 1\nQmvaa 8,116,52\nQe 2\nQmvaa 8,120,56\nds_read_b32 v66, v74\nQk 44,0,1\nQmvaa 8,124,60\n"
"Qe 3\nQcvaa 12,128,48,56\nQk 45,2,3\nQmvaa 12,132,52\nQe 4\nv_smfmac_f32_16x16x64_bf16 a[176:179], a[204:207], v[36:43], v77\nQmvaa 12,136,56\ns_waitcnt lgkmcnt(0)\n"
"v_cmp_gt_u32 vcc, v75, v66\nQmvaa 12,140,60\ns_cbranch_vccnz .Lsa2\n.Lra2:\nQmvav 128,144,48\ns_add_u32 m0, s61, 33024\nQmvav 128,148,40\nbuffer_load_dwordx4 v69, s[40:43], s90 offen nt lds\n"
"Qmvav 132,152,48\nQe 5\nQmvav 132,156,40\nQe 6\nQmvav 136,160,48\nQe 7\nQmvav 136,164,40\nQk 46,4,5\n"
"Qk 47,6,7\nQmvav 140,168,48\nbuffer_load_dwordx4 v69, s[40:43], s60 offen offset:1024 nt lds\nQmvav 140,172,40\nQe 8\nQcvaa 0,112,64,56\nQe 9\nQmvaa 0,116,68\n"
"Qe 10\nQmvaa 0,120,72\nQe 11\nQmvaa 0,124,76\nbuffer_load_dwordx4 v69, s[40:43], s90 offen offset:2048 nt lds\nQcvaa 4,128,64,56\nQk 48,8,9\nQk 49,10,11\n"
"Qmvaa 4,132,68\nQe 12\nQmvaa 4,136,72\nQe 13\nQmvaa 4,140,76\nQe 14\nQmvav 160,144,52\nbuffer_load_dwordx4 v69, s[40:43], s60 offen offset:3072 nt lds\n"
"Qmvav 160,148,44\nQe 15\nQmvav 164,152,52\nQk 50,12,13\nQk 51,14,15\nQmvav 164,156,44\nQe 0\nQmvav 168,160,52\n"
"s_add_u32 m0, s61, 41216\nQmvav 168,164,44\nbuffer_load_dwordx4 v72, s[44:47], s90 offen nt lds\nQmvav 172,168,52\nQe 1\nQmvav 172,172,44\nQe 2\nQcvaa 8,112,80,56\n"
"Qe 3\nQmvaa 8,116,84\nQk 16,0,1\nQk 17,2,3\nQmvaa 8,120,88\nbuffer_load_dwordx4 v72, s[44:47], s60 offen offset:1024 nt lds\nQmvaa 8,124,92\nQe 4\n"
"Qcvaa 12,128,80,56\nQe 5\nQmvaa 12,132,84\nQe 6\nv_smfmac_f32_16x16x64_bf16 a[180:183], a[204:207], v[44:51], v77\nQk 18,4,5\nQmvaa 12,136,88\nbuffer_load_dwordx4 v72, s[44:47], s90 offen offset:2048 nt lds\n"
"Qmvaa 12,140,92\nQe 7\nQmvav 192,144,20\nQk 19,6,7\nQmvav 192,148,48\nQmvav 196,152,20\nQe 8\nQmvav 196,156,48\n"
"buffer_load_dwordx4 v72, s[44:47], s60 offen offset:3072 nt lds\nQmvav 200,160,20\nQe 9\nQmvav 200,164,48\nQe 10\nQmvav 204,168,20\ns_add_u32 s60, s60, 8192\ns_add_u32 s90, s60, 896\n"
"Qmvav 204,172,48\nQe 11\nQcvaa 0,112,96,56\nQda 112,68,33024\nQk 20,8,9\nQmvaa 0,116,100\nQda 116,68,33536\nQk 21,10,11\n"
"Qmvaa 0,120,104\nQda 120,68,34048\nQmvaa 0,124,108\nQda 124,68,34560\nQcvaa 4,128,96,56\nQda 128,68,35072\nQmvaa 4,132,100\nQda 132,68,35584\n"
"Qmvaa 4,136,104\nQda 136,68,36096\nQmvaa 4,140,108\nQda 140,68,36608\nQmvav 224,144,24\nQe 12\nQmvav 224,148,16\ns_memrealtime s[74:75]\n"
"Qmvav 228,152,24\nQe 13\nQmvav 228,156,16\nQe 14\nQmvav 232,160,24\nQe 15\nQmvav 232,164,16\nQk 22,12,13\n"
"Qk 23,14,15\nQmvav 236,168,24\nQe 0\nQmvav 236,172,16\ns_waitcnt lgkmcnt(0)\nQcvaa 8,112,0,56\nQe 1\nQmvaa 8,116,4\n"
"v_subrev_u32 v68, 66048, v68\nQk 24,0,1\nQmvaa 8,120,8\nQe 2\nQmvaa 8,124,12\nQe 3\nQcvaa 12,128,0,56\nQk 25,2,3\n"
"Qmvaa 12,132,4\nQe 4\nv_smfmac_f32_16x16x64_bf16 a[184:187], a[204:207], v[16:23], v77\nQmvaa 12,136,8\nQe 5\nQmvaa 12,140,12\nQe 6\nQmaav 208,144,28\n"
"s_waitcnt lgkmcnt(0)\ns_sub_u32 s75, s77, s74\nQmaav 208,148,20\ns_cmp_lt_u32 s75, 256\ns_cbranch_scc1 .Lsp2\n.Lrp2:\nQmaav 212,152,28\ns_add_u32 s77, s74, s73\n"
"Qk 26,4,5\nQmaav 212,156,20\ns_sub_u32 s73, s73, 4\nQmaav 216,160,28\ns_max_u32 s73, s73, 120\nQmaav 216,164,20\nQe 7\nQmaav 220,168,28\n"
"Qk 27,6,7\nQmaav 220,172,20\nQe 8\nQcvaa 0,112,16,56\nQe 9\nQmvaa 0,116,20\nQe 10\nQmvaa 0,120,24\n"
"Qe 11\nQmvaa 0,124,28\nQk 28,8,9\nQk 29,10,11\nQcvaa 4,128,16,56\nQe 12\nQmvaa 4,132,20\nQe 13\n"
"Qmvaa 4,136,24\nQe 14\nQmvaa 4,140,28\nQe 15\nQmaav 240,144,32\nQta 144,71,0\nQta 146,71,2048\nQmaav 240,148,24\n"
"Qta 148,71,33024\nQta 150,71,35072\nQmaav 244,152,32\nQta 152,71,256\nQta 154,71,2304\nQmaav 244,156,24\nQta 156,71,33280\nQta 158,71,35328\n"
"Qmaav 248,160,32\nQta 160,71,512\nQta 162,71,2560\nQmaav 248,164,24\nQta 164,71,33536\nQta 166,71,35584\nQmaav 252,168,32\nQta 168,71,768\n"
"Qta 170,71,2816\nQmaav 252,172,24\nQta 172,71,33792\nQta 174,71,35840\nQcvaa 8,112,32,56\nv_add_u32 v75, 1, v75\nQk 30,12,13\nQmvaa 8,116,36\n"
"s_waitcnt vmcnt(8)\nds_write_b32 v73, v75\nQmvaa 8,120,40\nQk 31,14,15\nQmvaa 8,124,44\nQe 0\nQcvaa 12,128,32,56\nQe 1\n"
"Qmvaa 12,132,36\nQe 2\nv_smfmac_f32_16x16x64_bf16 a[188:191], a[204:207], v[24:31], v77\nQk 32,0,1\nQmvaa 12,136,40\ns_waitcnt lgkmcnt(1)\nQmvaa 12,140,44\nQe 3\n"
"Qmvav 80,144,36\nQk 33,2,3\nQmvav 80,148,28\nQe 4\nQmvav 84,152,36\nQe 5\nQmvav 84,156,28\nQe 6\n"
"Qmvav 88,160,36\nQe 7\nQmvav 88,164,28\nQk 34,4,5\nQk 35,6,7\nQmvav 92,168,36\nQe 8\nQmvav 92,172,28\n"
"Qe 9\nQcvaa 0,112,48,56\nQe 10\nQmvaa 0,116,52\nds_read_b32 v66, v74\nQk 36,8,9\nQmvaa 0,120,56\nQe 11\n"
"Qmvaa 0,124,60\nQk 37,10,11\nQcvaa 4,128,48,56\nQe 12\nQmvaa 4,132,52\nQe 13\nQmvaa 4,136,56\ns_waitcnt lgkmcnt(0)\n"
"v_cmp_gt_u32 vcc, v75, v66\nQmvaa 4,140,60\ns_cbranch_vccnz .Lsa3\n.Lra3:\nQk 38,12,13\nQmvav 112,144,40\ns_add_u32 m0, s61, 66048\nQmvav 112,148,32\n"
"buffer_load_dwordx4 v69, s[40:43], s90 offen nt lds\nQmvav 116,152,40\nQe 14\nQmvav 116,156,32\nQe 15\nQmvav 120,160,40\nQk 39,14,15\nQmvav 120,164,32\n"
"Qe 0\nQmvav 124,168,40\nbuffer_load_dwordx4 v69, s[40:43], s60 offen offset:1024 nt lds\nQmvav 124,172,32\nQe 1\nQcvaa 8,112,64,56\nQe 2\nQmvaa 8,116,68\n"
"Qe 3\nQmvaa 8,120,72\nQk 40,0,1\nQk 41,2,3\nQmvaa 8,124,76\nbuffer_load_dwordx4 v69, s[40:43], s90 offen offset:2048 nt lds\nQcvaa 12,128,64,56\nQe 4\n"
"Qmvaa 12,132,68\nQe 5\nv_smfmac_f32_16x16x64_bf16 a[192:195], a[204:207], v[32:39], v77\nQk 42,4,5\nQmvaa 12,136,72\nQe 6\nQmvaa 12,140,76\nbuffer_load_dwordx4 v69, s[40:43], s60 offen offset:3072 nt lds\n"
"Qmvav 144,144,44\nQe 7\nQmvav 144,148,36\nQk 43,6,7\nQmvav 148,152,44\nQe 8\nQmvav 148,156,36\ns_add_u32 m0, s61, 74240\n"
"Qmvav 152,160,44\nbuffer_load_dwordx4 v72, s[44:47], s90 offen nt lds\nQmvav 152,164,36\nQe 9\nQmvav 156,168,44\nQe 10\nQmvav 156,172,36\nQe 11\n"
"Qcvaa 0,112,80,56\nQk 44,8,9\nQk 45,10,11\nQmvaa 0,116,84\nbuffer_load_dwordx4 v72, s[44:47], s60 offen offset:1024 nt lds\nQmvaa 0,120,88\nQe 12\nQmvaa 0,124,92\n"
"Qe 13\nQcvaa 4,128,80,56\nQe 14\nQmvaa 4,132,84\nQe 15\nQmvaa 4,136,88\nbuffer_load_dwordx4 v72, s[44:47], s90 offen offset:2048 nt lds\nQmvaa 4,140,92\n"
"Qk 46,12,13\nQk 47,14,15\nQmvav 176,144,48\nQmvav 176,148,40\nQmvav 180,152,48\nQmvav 180,156,40\nbuffer_load_dwordx4 v72, s[44:47], s60 offen offset:3072 nt lds\nQmvav 184,160,48\n"
"Qe 0\nQmvav 184,164,40\nQe 1\nQmvav 188,168,48\ns_add_u32 s60, s60, 8192\ns_add_u32 s90, s60, 896\nQmvav 188,172,40\nQe 2\n"
"Qcvaa 8,112,96,56\nQda 112,68,0\nQk 48,0,1\nQmvaa 8,116,100\nQda 116,68,512\nQmvaa 8,120,104\nQda 120,68,1024\nQmvaa 8,124,108\n"
"Qda 124,68,1536\nQcvaa 12,128,96,56\nQda 128,68,2048\nQmvaa 12,132,100\nQda 132,68,2560\nv_smfmac_f32_16x16x64_bf16 a[196:199], a[204:207], v[40:47], v77\nQmvaa 12,136,104\nQda 136,68,3072\n"
"Qmvaa 12,140,108\nQda 140,68,3584\nQmvav 208,144,16\ns_memrealtime s[74:75]\nQmvav 208,148,44\nQe 3\nQmvav 212,152,16\nQk 49,2,3\n"
"Qmvav 212,156,44\nQe 4\nQmvav 216,160,16\nQe 5\nQmvav 216,164,44\nQe 6\nQmvav 220,168,16\nQe 7\n"
"Qmvav 220,172,44\ns_waitcnt lgkmcnt(0)\nQk 50,4,5\nQcvaa 0,112,0,56\nQk 51,6,7\nQmvaa 0,116,4\nQe 8\nQmvaa 0,120,8\n"
"Qe 9\nQmvaa 0,124,12\nQe 10\nQcvaa 4,128,0,56\nQe 11\nQmvaa 4,132,4\nQk 52,8,9\nQk 53,10,11\n"
"Qmvaa 4,136,8\nQe 12\nQmvaa 4,140,12\nQe 13\nQmvav 240,144,20\nQe 14\nQmvav 240,148,48\ns_waitcnt lgkmcnt(0)\n"
"s_sub_u32 s75, s77, s74\nQmvav 244,152,20\ns_cmp_lt_u32 s75, 256\ns_cbranch_scc1 .Lsp3\n.Lrp3:\nQmvav 244,156,48\ns_add_u32 s77, s74, s73\nQk 54,12,13\n"
"Qmvav 248,160,20\ns_sub_u32 s73, s73, 4\nQmvav 248,164,48\ns_max_u32 s73, s73, 120\nQmvav 252,168,20\nQe 15\nQmvav 252,172,48\nQk 55,14,15\n"
"Qcvaa 8,112,16,56\nQe 0\nQmvaa 8,116,20\nQe 1\nQmvaa 8,120,24\nQe 2\nQmvaa 8,124,28\nQe 3\n"
"Qcvaa 12,128,16,56\nQk 16,0,1\nQk 17,2,3\nQmvaa 12,132,20\nQe 4\nv_smfmac_f32_16x16x64_bf16 a[200:203], a[204:207], v[48:55], v77\nQmvaa 12,136,24\nQe 5\n"
"Qmvaa 12,140,28\nQe 6\nQmaav 224,144,24\nQta 144,71,34048\nQta 146,71,36096\nQmaav 224,148,52\n.Lpg6:\ns_cbranch_execz .Ltr9\n"
"Qta 148,70,1024\nQta 150,70,3072\nQmaav 228,152,24\nQta 152,71,34304\nQta 154,71,36352\nQmaav 228,156,52\nQta 156,70,1280\nQta 158,70,3328\n"
"Qmaav 232,160,24\nQta 160,71,34560\nQta 162,71,36608\nQmaav 232,164,52\nQta 164,70,1536\nQta 166,70,3584\nQmaav 236,168,24\nQta 168,71,34816\n"
"Qta 170,71,36864\nQmaav 236,172,52\nQta 172,70,1792\nQta 174,70,3840\nQcvaa 0,112,32,56\nv_add_u32 v75, 1, v75\nQk 18,4,5\nQmvaa 0,116,36\n"
"s_waitcnt vmcnt(8)\nds_write_b32 v73, v75\nQmvaa 0,120,40\nQe 7\nQmvaa 0,124,44\nQk 19,6,7\nQcvaa 4,128,32,56\nQe 8\n"
"Qmvaa 4,132,36\nQe 9\nQmvaa 4,136,40\nQe 10\nQmvaa 4,140,44\ns_waitcnt lgkmcnt(1)\nQk 20,8,9\nQmvav 96,144,28\n"
"Qe 11\nQmvav 96,148,16\nQk 21,10,11\nQmvav 100,152,28\nQe 12\nQmvav 100,156,16\nQe 13\nQmvav 104,160,28\n"
"Qe 14\nQmvav 104,164,16\nQe 15\nQmvav 108,168,28\nQk 22,12,13\nQk 23,14,15\nQmvav 108,172,16\nQe 0\n"
"Qcvaa 8,112,48,56\nQe 1\nQmvaa 8,116,52\nQe 2\nQmvaa 8,120,56\nds_read_b32 v66, v74\nQk 24,0,1\nQmvaa 8,124,60\n"
"Qe 3\nQcvaa 12,128,48,56\nQk 25,2,3\nQmvaa 12,132,52\nQe 4\nv_smfmac_f32_16x16x64_bf16 a[176:179], a[204:207], v[16:23], v77\nQmvaa 12,136,56\ns_waitcnt lgkmcnt(0)\n"
"v_cmp_gt_u32 vcc, v75, v66\nQmvaa 12,140,60\ns_cbranch_vccnz .Lsa4\n.Lra4:\nQmvav 128,144,32\ns_add_u32 m0, s61, 99072\nQmvav 128,148,20\nbuffer_load_dwordx4 v69, s[40:43], s90 offen nt lds\n"
"Qmvav 132,152,32\nQe 5\nQmvav 132,156,20\nQe 6\nQmvav 136,160,32\nQe 7\nQmvav 136,164,20\nQk 26,4,5\n"
"Qk 27,6,7\nQmvav 140,168,32\nbuffer_load_dwordx4 v69, s[40:43], s60 offen offset:1024 nt lds\nQmvav 140,172,20\nQe 8\nQcvaa 0,112,64,56\nQe 9\nQmvaa 0,116,68\n"
"Qe 10\nQmvaa 0,120,72\nQe 11\nQmvaa 0,124,76\nbuffer_load_dwordx4 v69, s[40:43], s90 offen offset:2048 nt lds\nQcvaa 4,128,64,56\nQk 28,8,9\nQk 29,10,11\n"
"Qmvaa 4,132,68\nQe 12\nQmvaa 4,136,72\nQe 13\nQmvaa 4,140,76\nQe 14\nQmvav 160,144,36\nbuffer_load_dwordx4 v69, s[40:43], s60 offen offset:3072 nt lds\n"
"Qmvav 160,148,24\nQe 15\nQmvav 164,152,36\nQk 30,12,13\nQk 31,14,15\nQmvav 164,156,24\nQe 0\nQmvav 168,160,36\n"
"s_add_u32 m0, s61, 107264\nQmvav 168,164,24\nbuffer_load_dwordx4 v72, s[44:47], s90 offen nt lds\nQmvav 172,168,36\nQe 1\nQmvav 172,172,24\nQe 2\nQcvaa 8,112,80,56\n"
"Qe 3\nQmvaa 8,116,84\nQk 32,0,1\nQk 33,2,3\nQmvaa 8,120,88\nbuffer_load_dwordx4 v72, s[44:47], s60 offen offset:1024 nt lds\nQmvaa 8,124,92\nQe 4\n"
"Qcvaa 12,128,80,56\nQe 5\nQmvaa 12,132,84\nQe 6\nv_smfmac_f32_16x16x64_bf16 a[180:183], a[204:207], v[24:31], v77\nQk 34,4,5\nQmvaa 12,136,88\nbuffer_load_dwordx4 v72, s[44:47], s90 offen offset:2048 nt lds\n"
"Qmvaa 12,140,92\nQe 7\nQmvav 192,144,40\nQk 35,6,7\nQmvav 192,148,28\nQmvav 196,152,40\nQe 8\nQmvav 196,156,28\n"
"buffer_load_dwordx4 v72, s[44:47], s60 offen offset:3072 nt lds\nQmvav 200,160,40\nQe 9\nQmvav 200,164,28\nQe 10\nQmvav 204,168,40\ns_add_u32 s60, s60, 8192\ns_and_b32 s68, s60, 0x40000\n"
"s_mul_i32 s68, s68, 7\ns_add_u32 s60, s60, s68\ns_add_u32 s90, s60, 896\nQmvav 204,172,28\nQe 11\nQcvaa 0,112,96,56\nQda 112,68,33024\nQk 36,8,9\n"
"Qmvaa 0,116,100\nQda 116,68,33536\nQk 37,10,11\nQmvaa 0,120,104\nQda 120,68,34048\nQmvaa 0,124,108\nQda 124,68,34560\nQcvaa 4,128,96,56\n"
"Qda 128,68,35072\nQmvaa 4,132,100\nQda 132,68,35584\nQmvaa 4,136,104\nQda 136,68,36096\nQmvaa 4,140,108\nQda 140,68,36608\nQmvav 224,144,44\n"
"Qe 12\nQmvav 224,148,32\ns_memrealtime s[74:75]\nQmvav 228,152,44\nQe 13\nQmvav 228,156,32\nQe 14\nQmvav 232,160,44\n"
"Qe 15\nQmvav 232,164,32\nQk 38,12,13\nQk 39,14,15\nQmvav 236,168,44\nQe 0\nQmvav 236,172,32\ns_waitcnt lgkmcnt(0)\n"
"Qcvaa 8,112,0,56\nQe 1\nQmvaa 8,116,4\nv_add_u32 v68, 66048, v68\nQk 40,0,1\nQmvaa 8,120,8\nQe 2\nQmvaa 8,124,12\n"
"Qe 3\nQcvaa 12,128,0,56\nQk 41,2,3\nQmvaa 12,132,4\nQe 4\nv_smfmac_f32_16x16x64_bf16 a[184:187], a[204:207], v[32:39], v77\nQmvaa 12,136,8\nQe 5\n"
"Qmvaa 12,140,12\nQe 6\nQmaav 208,144,48\ns_waitcnt lgkmcnt(0)\ns_sub_u32 s75, s77, s74\nQmaav 208,148,36\ns_cmp_lt_u32 s75, 256\ns_cbranch_scc1 .Lsp4\n"
".Lrp4:\nQmaav 212,152,48\ns_add_u32 s77, s74, s73\nQk 42,4,5\nQmaav 212,156,36\ns_sub_u32 s73, s73, 4\nQmaav 216,160,48\ns_max_u32 s73, s73, 120\n"
"Qmaav 216,164,36\nQe 7\nQmaav 220,168,48\nQk 43,6,7\nQmaav 220,172,36\nQe 8\nQcvaa 0,112,16,56\nQe 9\n"
"Qmvaa 0,116,20\nQe 10\nQmvaa 0,120,24\nQe 11\nQmvaa 0,124,28\nQk 44,8,9\nQk 45,10,11\nQcvaa 4,128,16,56\n"
"Qe 12\nQmvaa 4,132,20\nQe 13\nQmvaa 4,136,24\nQe 14\nQmvaa 4,140,28\nQe 15\nQmaav 240,144,52\n"
"Qta 144,70,0\nQta 146,70,2048\nQmaav 240,148,40\nQta 148,70,33024\nQta 150,70,35072\nQmaav 244,152,52\nQta 152,70,256\nQta 154,70,2304\n"
"Qmaav 244,156,40\nQta 156,70,33280\nQta 158,70,35328\nQmaav 248,160,52\nQta 160,70,512\nQta 162,70,2560\nQmaav 248,164,40\nQta 164,70,33536\n"
"Qta 166,70,35584\nQmaav 252,168,52\nQta 168,70,768\nQta 170,70,2816\nQmaav 252,172,40\nQta 172,70,33792\nQta 174,70,35840\nQcvaa 8,112,32,56\n"
"v_add_u32 v75, 1, v75\nQk 46,12,13\nQmvaa 8,116,36\ns_waitcnt vmcnt(8)\nds_write_b32 v73, v75\nQmvaa 8,120,40\nQk 47,14,15\nQmvaa 8,124,44\n"
"Qe 0\nQcvaa 12,128,32,56\nQe 1\nQmvaa 12,132,36\nQe 2\nv_smfmac_f32_16x16x64_bf16 a[188:191], a[204:207], v[40:47], v77\nQk 48,0,1\nQmvaa 12,136,40\n"
"s_waitcnt lgkmcnt(1)\nQmvaa 12,140,44\nQe 3\nQmvav 80,144,16\nQk 49,2,3\nQmvav 80,148,44\nQe 4\nQmvav 84,152,16\n"
"Qe 5\nQmvav 84,156,44\nQe 6\nQmvav 88,160,16\nQe 7\nQmvav 88,164,44\nQk 50,4,5\nQk 51,6,7\n"
"Qmvav 92,168,16\nQe 8\nQmvav 92,172,44\nQe 9\nQcvaa 0,112,48,56\nQe 10\nQmvaa 0,116,52\nds_read_b32 v66, v74\n"
"Qk 52,8,9\nQmvaa 0,120,56\nQe 11\nQmvaa 0,124,60\nQk 53,10,11\nQcvaa 4,128,48,56\nQe 12\nQmvaa 4,132,52\n"
"Qe 13\nQmvaa 4,136,56\ns_waitcnt lgkmcnt(0)\nv_cmp_gt_u32 vcc, v75, v66\nQmvaa 4,140,60\ns_cbranch_vccnz .Lsa5\n.Lra5:\nQk 54,12,13\n"
"Qmvav 112,144,20\ns_add_u32 m0, s61, 0\nQmvav 112,148,48\nbuffer_load_dwordx4 v69, s[40:43], s90 offen nt lds\nQmvav 116,152,20\nQe 14\nQmvav 116,156,48\nQe 15\n"
"Qmvav 120,160,20\nQk 55,14,15\nQmvav 120,164,48\nQe 0\nQmvav 124,168,20\nbuffer_load_dwordx4 v69, s[40:43], s60 offen offset:1024 nt lds\nQmvav 124,172,48\nQe 1\n"
"Qcvaa 8,112,64,56\nQe 2\nQmvaa 8,116,68\nQe 3\nQmvaa 8,120,72\nQk 20,0,1\nQk 21,2,3\nQmvaa 8,124,76\n"
"buffer_load_dwordx4 v69, s[40:43], s90 offen offset:2048 nt lds\nQcvaa 12,128,64,56\nQe 4\nQmvaa 12,132,68\nQe 5\nv_smfmac_f32_16x16x64_bf16 a[192:195], a[204:207], v[48:55], v77\nQk 22,4,5\nQmvaa 12,136,72\n"
"Qe 6\nQmvaa 12,140,76\nbuffer_load_dwordx4 v69, s[40:43], s60 offen offset:3072 nt lds\nQmvav 144,144,24\nQe 7\nQmvav 144,148,52\nQk 23,6,7\nQmvav 148,152,24\n"
"Qe 8\nQmvav 148,156,52\ns_add_u32 m0, s61, 8192\nQmvav 152,160,24\nbuffer_load_dwordx4 v72, s[44:47], s90 offen nt lds\nQmvav 152,164,52\nQe 9\nQmvav 156,168,24\n"
"Qe 10\nQmvav 156,172,52\nQe 11\nQcvaa 0,112,80,56\nQk 24,8,9\nQk 25,10,11\nQmvaa 0,116,84\nbuffer_load_dwordx4 v72, s[44:47], s60 offen offset:1024 nt lds\n"
"Qmvaa 0,120,88\nQe 12\nQmvaa 0,124,92\nQe 13\nQcvaa 4,128,80,56\nQe 14\nQmvaa 4,132,84\nQe 15\n"
"Qmvaa 4,136,88\nbuffer_load_dwordx4 v72, s[44:47], s90 offen offset:2048 nt lds\nQmvaa 4,140,92\nQk 26,12,13\nQk 27,14,15\nQmvav 176,144,28\nQmvav 176,148,20\nQmvav 180,152,28\n"
"Qmvav 180,156,20\nbuffer_load_dwordx4 v72, s[44:47], s60 offen offset:3072 nt lds\nQmvav 184,160,28\nQe 0\nQmvav 184,164,20\nQe 1\nQmvav 188,168,28\ns_add_u32 s60, s60, 8192\n"
"s_add_u32 s90, s60, 896\nQmvav 188,172,20\nQe 2\nQcvaa 8,112,96,56\nQda 112,68,0\nQk 28,0,1\nQmvaa 8,116,100\nQda 116,68,512\n"
"Qmvaa 8,120,104\nQda 120,68,1024\nQmvaa 8,124,108\nQda 124,68,1536\nQcvaa 12,128,96,56\nQda 128,68,2048\nQmvaa 12,132,100\nQda 132,68,2560\n"
"v_smfmac_f32_16x16x64_bf16 a[196:199], a[204:207], v[20:27], v77\nQmvaa 12,136,104\nQda 136,68,3072\nQmvaa 12,140,108\nQda 140,68,3584\nQmvav 208,144,32\ns_memrealtime s[74:75]\nQmvav 208,148,24\n"
"Qe 3\nQmvav 212,152,32\nQk 29,2,3\nQmvav 212,156,24\nQe 4\nQmvav 216,160,32\nQe 5\nQmvav 216,164,24\n"
"Qe 6\nQmvav 220,168,32\nQe 7\nQmvav 220,172,24\ns_waitcnt lgkmcnt(0)\nQk 30,4,5\nQcvaa 0,112,0,56\nQk 31,6,7\n"
"Qmvaa 0,116,4\nQe 8\nQmvaa 0,120,8\nQe 9\nQmvaa 0,124,12\nQe 10\nQcvaa 4,128,0,56\nQe 11\n"
"Qmvaa 4,132,4\nQk 32,8,9\nQk 33,10,11\nQmvaa 4,136,8\nQe 12\nQmvaa 4,140,12\nQe 13\nQmvav 240,144,36\n"
"Qe 14\nQmvav 240,148,28\ns_waitcnt lgkmcnt(0)\ns_sub_u32 s75, s77, s74\nQmvav 244,152,36\ns_cmp_lt_u32 s75, 256\ns_cbranch_scc1 .Lsp5\n.Lrp5:\n"
"Qmvav 244,156,28\ns_add_u32 s77, s74, s73\nQk 34,12,13\nQmvav 248,160,36\ns_sub_u32 s73, s73, 4\nQmvav 248,164,28\ns_max_u32 s73, s73, 120\nQmvav 252,168,36\n"
"Qe 15\nQmvav 252,172,28\nQk 35,14,15\nQcvaa 8,112,16,56\nQe 0\nQmvaa 8,116,20\nQe 1\nQmvaa 8,120,24\n"
"Qe 2\nQmvaa 8,124,28\nQe 3\nQcvaa 12,128,16,56\nQk 36,0,1\nQk 37,2,3\nQmvaa 12,132,20\nQe 4\n"
"v_smfmac_f32_16x16x64_bf16 a[200:203], a[204:207], v[28:35], v77\nQmvaa 12,136,24\nQe 5\nQmvaa 12,140,28\nQe 6\nQmaav 224,144,40\nQta 144,70,34048\nQta 146,70,36096\n"
"Qmaav 224,148,32\nQta 148,71,1024\nQta 150,71,3072\nQmaav 228,152,40\nQta 152,70,34304\nQta 154,70,36352\nQmaav 228,156,32\nQta 156,71,1280\n"
"Qta 158,71,3328\nQmaav 232,160,40\nQta 160,70,34560\nQta 162,70,36608\nQmaav 232,164,32\nQta 164,71,1536\nQta 166,71,3584\nQmaav 236,168,40\n"
"Qta 168,70,34816\nQta 170,70,36864\nQmaav 236,172,32\nQta 172,71,1792\nQta 174,71,3840\ns_add_u32 s66, s66, 1\ns_bitcmp1_b32 0x7f7f, s66\ns_cbranch_scc1 .Lloop\n"
"s_cmp_lg_u32 s67, 0\ns_cbranch_scc1 .Ltail1\nQcvaa 0,112,32,56\nv_add_u32 v75, 1, v75\nQk 38,4,5\nQmvaa 0,116,36\ns_waitcnt vmcnt(8)\nds_write_b32 v73, v75\n"
"Qmvaa 0,120,40\nQe 7\nQmvaa 0,124,44\nQk 39,6,7\nQcvaa 4,128,32,56\nQe 8\nQmvaa 4,132,36\nQe 9\n"
"Qmvaa 4,136,40\nQe 10\nQmvaa 4,140,44\ns_waitcnt lgkmcnt(1)\nQk 40,8,9\nQmvav 96,144,44\nQe 11\nQmvav 96,148,36\n"
"Qk 41,10,11\nQmvav 100,152,44\nQe 12\nQmvav 100,156,36\nQe 13\nQmvav 104,160,44\nQe 14\nQmvav 104,164,36\n"
"Qe 15\nQmvav 108,168,44\nQk 42,12,13\nQk 43,14,15\nQmvav 108,172,36\nQe 0\nQcvaa 8,112,48,56\nQe 1\n"
"Qmvaa 8,116,52\nQe 2\nQmvaa 8,120,56\nds_read_b32 v66, v74\nQk 44,0,1\nQmvaa 8,124,60\nQe 3\nQcvaa 12,128,48,56\n"
"Qk 45,2,3\nQmvaa 12,132,52\nQe 4\nv_smfmac_f32_16x16x64_bf16 a[176:179], a[204:207], v[36:43], v77\nQmvaa 12,136,56\ns_waitcnt lgkmcnt(0)\nv_cmp_gt_u32 vcc, v75, v66\nQmvaa 12,140,60\n"
"s_cbranch_vccnz .Lsa30\n.Lra30:\nQmvav 128,144,48\ns_add_u32 m0, s62, 0\ns_add_u32 s69, s87, 0\nQmvav 128,148,40\nbuffer_load_dwordx4 v62, s[52:55], s69 offen lds\n.Lpg7:\n"
"s_cbranch_execz .Ltr9\nQmvav 132,152,48\nQe 5\nQmvav 132,156,40\nQe 6\nQmvav 136,160,48\nQe 7\nQmvav 136,164,40\n"
"s_add_u32 s69, s87, 64504\nQk 46,4,5\nQk 47,6,7\nQmvav 140,168,48\nbuffer_load_dwordx4 v62, s[52:55], s69 offen offset:1032 lds\nQmvav 140,172,40\nQe 8\nQcvaa 0,112,64,56\n"
"Qe 9\nQmvaa 0,116,68\nQe 10\nQmvaa 0,120,72\ns_add_u32 s69, s87, 129008\nQe 11\nQmvaa 0,124,76\nbuffer_load_dwordx4 v62, s[52:55], s69 offen offset:2064 lds\n"
"Qcvaa 4,128,64,56\nQk 48,8,9\nQk 49,10,11\nQmvaa 4,132,68\nQe 12\nQmvaa 4,136,72\nQe 13\nQmvaa 4,140,76\n"
"s_add_u32 s69, s87, 193512\nQe 14\nQmvav 160,144,52\nbuffer_load_dwordx4 v62, s[52:55], s69 offen offset:3096 lds\nQmvav 160,148,44\nQe 15\nQmvav 164,152,52\nQk 50,12,13\n"
"Qk 51,14,15\nQmvav 164,156,44\nQe 0\nQmvav 168,160,52\ns_add_u32 m0, s62, 16512\ns_add_u32 s69, s87, 128\nQmvav 168,164,44\nbuffer_load_dwordx4 v62, s[52:55], s69 offen lds\n"
"Qmvav 172,168,52\nQe 1\nQmvav 172,172,44\nQe 2\nQcvaa 8,112,80,56\nQe 3\nQmvaa 8,116,84\ns_add_u32 s69, s87, 64632\n"
"Qk 16,0,1\nQk 17,2,3\nQmvaa 8,120,88\nbuffer_load_dwordx4 v62, s[52:55], s69 offen offset:1032 lds\nQmvaa 8,124,92\nQe 4\nQcvaa 12,128,80,56\nQe 5\n"
"Qmvaa 12,132,84\nQe 6\nv_smfmac_f32_16x16x64_bf16 a[180:183], a[204:207], v[44:51], v77\ns_add_u32 s69, s87, 129136\nQk 18,4,5\nQmvaa 12,136,88\nbuffer_load_dwordx4 v62, s[52:55], s69 offen offset:2064 lds\nQmvaa 12,140,92\n"
"Qe 7\nQmvav 192,144,20\nQk 19,6,7\nQmvav 192,148,48\nQmvav 196,152,20\ns_add_u32 s69, s87, 193640\nQe 8\nQmvav 196,156,48\n"
"buffer_load_dwordx4 v62, s[52:55], s69 offen offset:3096 lds\nQmvav 200,160,20\nQe 9\nQmvav 200,164,48\nQe 10\nQmvav 204,168,20\nQmvav 204,172,48\nQe 11\n"
"Qcvaa 0,112,96,56\nQda 112,68,33024\nQk 20,8,9\nQmvaa 0,116,100\nQda 116,68,33536\nQk 21,10,11\nQmvaa 0,120,104\nQda 120,68,34048\n"
"Qmvaa 0,124,108\nQda 124,68,34560\nQcvaa 4,128,96,56\nQda 128,68,35072\nQmvaa 4,132,100\nQda 132,68,35584\nQmvaa 4,136,104\nQda 136,68,36096\n"
"Qmvaa 4,140,108\nQda 140,68,36608\nQmvav 224,144,24\nQe 12\nQmvav 224,148,16\ns_add_u32 m0, s63, 33024\ns_add_u32 s88, s86, 0\nQmvav 228,152,24\n"
"buffer_load_dwordx4 v64, s[48:51], s88 offen lds\nQe 13\nQmvav 228,156,16\nQe 14\nQmvav 232,160,24\nQe 15\nQmvav 232,164,16\ns_add_u32 s88, s86, 19456\n"
"Qk 22,12,13\nQk 23,14,15\nQmvav 236,168,24\nbuffer_load_dwordx4 v64, s[48:51], s88 offen offset:1024 lds\nQe 0\nQmvav 236,172,16\ns_waitcnt lgkmcnt(0)\nQcvaa 8,112,0,56\n"
"Qe 1\nQmvaa 8,116,4\nv_subrev_u32 v68, 66048, v68\ns_add_u32 s88, s86, 38912\nQk 24,0,1\nQmvaa 8,120,8\nbuffer_load_dwordx4 v64, s[48:51], s88 offen offset:2048 lds\nQe 2\n"
"Qmvaa 8,124,12\nQe 3\nQcvaa 12,128,0,56\nQk 25,2,3\nQmvaa 12,132,4\nQe 4\nv_smfmac_f32_16x16x64_bf16 a[184:187], a[204:207], v[16:23], v77\ns_add_u32 s88, s86, 58368\n"
"Qmvaa 12,136,8\nbuffer_load_dwordx4 v64, s[48:51], s88 offen offset:3072 lds\nQe 5\nQmvaa 12,140,12\nQe 6\nQmaav 208,144,28\ns_add_u32 m0, s63, 37120\ns_add_u32 s88, s86, 81920\n"
"Qmaav 208,148,20\nbuffer_load_dwordx4 v64, s[48:51], s88 offen lds\nQmaav 212,152,28\nQk 26,4,5\nQmaav 212,156,20\nQmaav 216,160,28\ns_add_u32 s88, s86, 101376\nQmaav 216,164,20\n"
"buffer_load_dwordx4 v64, s[48:51], s88 offen offset:1024 lds\nQe 7\nQmaav 220,168,28\nQk 27,6,7\nQmaav 220,172,20\nQe 8\nQcvaa 0,112,16,56\ns_add_u32 s88, s86, 120832\n"
"Qe 9\nQmvaa 0,116,20\nbuffer_load_dwordx4 v64, s[48:51], s88 offen offset:2048 lds\nQe 10\nQmvaa 0,120,24\nQe 11\nQmvaa 0,124,28\nQk 28,8,9\n"
"Qk 29,10,11\nQcvaa 4,128,16,56\nQe 12\nQmvaa 4,132,20\nQe 13\nQmvaa 4,136,24\nQe 14\nQmvaa 4,140,28\n"
"Qe 15\nQmaav 240,144,32\nQta 144,71,0\nQta 146,71,2048\nQmaav 240,148,24\nQta 148,71,33024\nQta 150,71,35072\nQmaav 244,152,32\n"
"Qta 152,71,256\nQta 154,71,2304\nQmaav 244,156,24\nQta 156,71,33280\nQta 158,71,35328\nQmaav 248,160,32\nQta 160,71,512\nQta 162,71,2560\n"
"Qmaav 248,164,24\nQta 164,71,33536\nQta 166,71,35584\nQmaav 252,168,32\nQta 168,71,768\nQta 170,71,2816\nQmaav 252,172,24\nQta 172,71,33792\n"
"Qta 174,71,35840\nQcvaa 8,112,32,56\nv_add_u32 v75, 1, v75\nQk 30,12,13\nQmvaa 8,116,36\nds_write_b32 v73, v75\nQmvaa 8,120,40\nQk 31,14,15\n"
"Qmvaa 8,124,44\nQe 0\nQcvaa 12,128,32,56\nQe 1\nQmvaa 12,132,36\nQe 2\nv_smfmac_f32_16x16x64_bf16 a[188:191], a[204:207], v[24:31], v77\nQk 32,0,1\n"
"Qmvaa 12,136,40\ns_waitcnt lgkmcnt(1)\nQmvaa 12,140,44\nQe 3\nQmvav 80,144,36\nQk 33,2,3\nQmvav 80,148,28\nQe 4\n"
"Qmvav 84,152,36\nQe 5\nQmvav 84,156,28\nQe 6\nQmvav 88,160,36\nQe 7\nQmvav 88,164,28\nQk 34,4,5\n"
"Qk 35,6,7\nQmvav 92,168,36\nQe 8\nQmvav 92,172,28\nQe 9\nQcvaa 0,112,48,56\nQe 10\nQmvaa 0,116,52\n"
"ds_read_b32 v66, v74\nQk 36,8,9\nQmvaa 0,120,56\nQe 11\nQmvaa 0,124,60\nQk 37,10,11\nQcvaa 4,128,48,56\nQe 12\n"
"Qmvaa 4,132,52\nQe 13\nQmvaa 4,136,56\ns_waitcnt lgkmcnt(0)\nv_cmp_gt_u32 vcc, v75, v66\nQmvaa 4,140,60\ns_cbranch_vccnz .Lsa31\n.Lra31:\n"
"Qk 38,12,13\nQmvav 112,144,40\ns_add_u32 m0, s62, 33024\ns_add_u32 s69, s87, 524288\nQmvav 112,148,32\nbuffer_load_dwordx4 v62, s[52:55], s69 offen lds\nQmvav 116,152,40\nQe 14\n"
"Qmvav 116,156,32\nQe 15\nQmvav 120,160,40\nQk 39,14,15\nQmvav 120,164,32\ns_add_u32 s69, s87, 588792\nQe 0\nQmvav 124,168,40\n"
"buffer_load_dwordx4 v62, s[52:55], s69 offen offset:1032 lds\nQmvav 124,172,32\nQe 1\nQcvaa 8,112,64,56\nQe 2\nQmvaa 8,116,68\nQe 3\nQmvaa 8,120,72\n"
"s_add_u32 s69, s87, 653296\nQk 40,0,1\nQk 41,2,3\nQmvaa 8,124,76\nbuffer_load_dwordx4 v62, s[52:55], s69 offen offset:2064 lds\nQcvaa 12,128,64,56\nQe 4\nQmvaa 12,132,68\n"
"Qe 5\nv_smfmac_f32_16x16x64_bf16 a[192:195], a[204:207], v[32:39], v77\nQk 42,4,5\nQmvaa 12,136,72\ns_add_u32 s69, s87, 717800\nQe 6\nQmvaa 12,140,76\nbuffer_load_dwordx4 v62, s[52:55], s69 offen offset:3096 lds\n"
"Qmvav 144,144,44\nQe 7\nQmvav 144,148,36\nQk 43,6,7\nQmvav 148,152,44\nQe 8\nQmvav 148,156,36\ns_add_u32 m0, s62, 49536\n"
"s_add_u32 s69, s87, 524416\nQmvav 152,160,44\nbuffer_load_dwordx4 v62, s[52:55], s69 offen lds\nQmvav 152,164,36\nQe 9\nQmvav 156,168,44\nQe 10\nQmvav 156,172,36\n"
"Qe 11\nQcvaa 0,112,80,56\ns_add_u32 s69, s87, 588920\nQk 44,8,9\nQk 45,10,11\nQmvaa 0,116,84\nbuffer_load_dwordx4 v62, s[52:55], s69 offen offset:1032 lds\nQmvaa 0,120,88\n"
"Qe 12\nQmvaa 0,124,92\nQe 13\nQcvaa 4,128,80,56\nQe 14\nQmvaa 4,132,84\ns_add_u32 s69, s87, 653424\nQe 15\n"
"Qmvaa 4,136,88\nbuffer_load_dwordx4 v62, s[52:55], s69 offen offset:2064 lds\nQmvaa 4,140,92\nQk 46,12,13\nQk 47,14,15\nQmvav 176,144,48\nQmvav 176,148,40\nQmvav 180,152,48\n"
"s_add_u32 s69, s87, 717928\nQmvav 180,156,40\nbuffer_load_dwordx4 v62, s[52:55], s69 offen offset:3096 lds\nQmvav 184,160,48\nQe 0\nQmvav 184,164,40\nQe 1\nQmvav 188,168,48\n"
"Qmvav 188,172,40\nQe 2\nQcvaa 8,112,96,56\nQk 48,0,1\nQmvaa 8,116,100\nQmvaa 8,120,104\nQmvaa 8,124,108\nQcvaa 12,128,96,56\n"
"Qmvaa 12,132,100\nv_smfmac_f32_16x16x64_bf16 a[196:199], a[204:207], v[40:47], v77\nQmvaa 12,136,104\nQmvaa 12,140,108\nQmvav 208,144,16\nQta 112,71,34048\nQmvav 208,148,44\nQta 114,71,36096\n"
"Qe 3\nQmvav 212,152,16\nQta 116,71,34304\nQk 49,2,3\nQmvav 212,156,44\nQta 118,71,36352\nQe 4\nQmvav 216,160,16\n"
"Qta 120,71,34560\nQe 5\nQmvav 216,164,44\nQta 122,71,36608\nQe 6\nQmvav 220,168,16\nQta 124,71,34816\nQe 7\n"
"Qmvav 220,172,44\nQta 126,71,36864\nQk 50,4,5\nQk 51,6,7\nQe 8\nQe 9\nQe 10\nQe 11\n"
"Qk 52,8,9\nQk 53,10,11\nQe 12\nQe 13\nQmvav 240,144,20\nv_add_u32 v75, 1, v75\nQe 14\nQmvav 240,148,48\n"
"ds_write_b32 v73, v75\nQmvav 244,152,20\nQmvav 244,156,48\nQk 54,12,13\nQmvav 248,160,20\nQmvav 248,164,48\nQmvav 252,168,20\nQe 15\n"
"Qmvav 252,172,48\nQk 55,14,15\ns_nop 1\nv_smfmac_f32_16x16x64_bf16 a[200:203], a[204:207], v[48:55], v77\nQmaav 224,144,24\nds_read_b32 v66, v74\nv_accvgpr_read_b32 v0, a176\nQmaav 224,148,52\n"
"v_accvgpr_read_b32 v1, a177\nv_accvgpr_read_b32 v6, a178\nQmaav 228,152,24\nv_accvgpr_read_b32 v7, a179\nv_accvgpr_read_b32 v8, a180\nQmaav 228,156,52\nv_accvgpr_read_b32 v9, a181\nv_accvgpr_read_b32 v14, a182\n"
"Qmaav 232,160,24\nv_accvgpr_read_b32 v15, a183\nv_accvgpr_read_b32 v16, a184\nQmaav 232,164,52\ns_waitcnt lgkmcnt(0)\nv_cmp_gt_u32 vcc, v75, v66\nQmaav 236,168,24\ns_cbranch_vccnz .Lsa32\n"
".Lra32:\nv_accvgpr_read_b32 v17, a185\nQmaav 236,172,52\ns_waitcnt lgkmcnt(2)\nv_accvgpr_read_b32 v22, a186\nQmvav 96,112,28\ns_add_u32 m0, s63, 0\ns_add_u32 s88, s86, 128\n"
"v_accvgpr_read_b32 v23, a187\nQmvav 100,116,28\nbuffer_load_dwordx4 v64, s[48:51], s88 offen lds\nQmvav 104,120,28\nv_accvgpr_read_b32 v24, a188\nv_accvgpr_read_b32 v25, a189\nQmvav 108,124,28\nv_accvgpr_read_b32 v2, a190\n"
"v_accvgpr_read_b32 v3, a191\nQmvav 128,112,32\ns_add_u32 s88, s86, 19584\nv_accvgpr_read_b32 v4, a192\nQmvav 132,116,32\nbuffer_load_dwordx4 v64, s[48:51], s88 offen offset:1024 lds\nQmvav 136,120,32\nv_accvgpr_read_b32 v5, a193\n"
"v_accvgpr_read_b32 v10, a194\nQmvav 140,124,32\nv_accvgpr_read_b32 v11, a195\nv_accvgpr_read_b32 v12, a196\nQmvav 160,112,36\ns_add_u32 s88, s86, 39040\nv_accvgpr_read_b32 v13, a197\nQmvav 164,116,36\n"
"buffer_load_dwordx4 v64, s[48:51], s88 offen offset:2048 lds\nQmvav 168,120,36\nv_accvgpr_read_b32 v18, a198\nv_accvgpr_read_b32 v19, a199\nQmvav 172,124,36\nv_accvgpr_read_b32 v20, a200\nv_accvgpr_read_b32 v21, a201\nQmvav 192,112,40\n"
"s_add_u32 s88, s86, 58496\nv_accvgpr_read_b32 v26, a202\nQmvav 196,116,40\nbuffer_load_dwordx4 v64, s[48:51], s88 offen offset:3072 lds\nQmvav 200,120,40\nv_accvgpr_read_b32 v27, a203\nQa 8,8,9\nQmvav 204,124,40\n"
"Qa 10,10,11\nQa 8,8,10\nQmvav 224,112,44\ns_add_u32 m0, s63, 4096\ns_add_u32 s88, s86, 82048\nQa 16,16,17\nQmvav 228,116,44\nbuffer_load_dwordx4 v64, s[48:51], s88 offen lds\n"
"Qmvav 232,120,44\nQa 18,18,19\nQa 16,16,18\nQmvav 236,124,44\nQa 4,4,5\nQa 6,6,7\nQmaav 208,112,48\ns_add_u32 s88, s86, 101504\n"
"Qa 4,4,6\nQmaav 212,116,48\nbuffer_load_dwordx4 v64, s[48:51], s88 offen offset:1024 lds\nQmaav 216,120,48\nQa 12,12,13\nQa 14,14,15\nQmaav 220,124,48\nQa 12,12,14\n"
"Qa 0,0,1\nQmaav 240,112,52\ns_add_u32 s88, s86, 120960\nQa 2,2,3\nQmaav 244,116,52\nbuffer_load_dwordx4 v64, s[48:51], s88 offen offset:2048 lds\nQmaav 248,120,52\nQa 0,0,2\n"
"Qa 24,24,25\nQmaav 252,124,52\nQa 26,26,27\nQa 24,24,26\nQa 20,20,21\nQa 22,22,23\nQa 20,20,22\nv_max3_u32 v9, v8, v16, v4\n"
"v_max3_u32 v9, v9, v12, v0\nv_max3_u32 v9, v9, v24, v20\nv_min3_u32 v11, v8, v16, v4\nv_min3_u32 v11, v11, v12, v0\nv_min3_u32 v11, v11, v24, v20\nv_cmp_lt_u32 vcc, 0x44800000, v9\ns_mov_b64 s[74:75], vcc\nv_cmp_gt_u32 vcc, 0x2b800000, v11\n"
"s_or_b64 s[74:75], s[74:75], vcc\ns_cmp_lg_u64 s[74:75], 0\ns_cbranch_scc1 .Lfb\n.Lfbret0:\nv_rcp_f32 v49, v24\nv_rcp_f32 v48, v24\nv_rcp_f32 v45, v20\nv_rcp_f32 v44, v20\n"
"v_rcp_f32 v41, v16\nv_rcp_f32 v40, v16\nv_rcp_f32 v37, v12\nv_rcp_f32 v36, v12\nv_rcp_f32 v33, v8\nv_rcp_f32 v32, v8\nv_rcp_f32 v29, v4\nv_rcp_f32 v28, v4\n"
"v_rcp_f32 v25, v0\nv_rcp_f32 v24, v0\ns_nop 0\nv_pk_mul_f32 v[80:81], v[80:81], v[24:25]\nv_pk_mul_f32 v[82:83], v[82:83], v[24:25]\nv_pk_mul_f32 v[84:85], v[84:85], v[24:25]\nv_pk_mul_f32 v[86:87], v[86:87], v[24:25]\nQk 80,80,81\n"
"Qk 81,82,83\nQk 82,84,85\nQk 83,86,87\nv_accvgpr_write_b32 a0, v80\nv_accvgpr_write_b32 a1, v81\nv_accvgpr_write_b32 a2, v82\nv_accvgpr_write_b32 a3, v83\nv_pk_mul_f32 v[88:89], v[88:89], v[24:25]\n"
"v_pk_mul_f32 v[90:91], v[90:91], v[24:25]\nv_pk_mul_f32 v[92:93], v[92:93], v[24:25]\nv_pk_mul_f32 v[94:95], v[94:95], v[24:25]\nQk 88,88,89\nQk 89,90,91\nQk 90,92,93\nQk 91,94,95\nv_accvgpr_write_b32 a4, v88\n"
"v_accvgpr_write_b32 a5, v89\nv_accvgpr_write_b32 a6, v90\nv_accvgpr_write_b32 a7, v91\nv_pk_mul_f32 v[96:97], v[96:97], v[24:25]\nv_pk_mul_f32 v[98:99], v[98:99], v[24:25]\nv_pk_mul_f32 v[100:101], v[100:101], v[24:25]\nv_pk_mul_f32 v[102:103], v[102:103], v[24:25]\nQk 96,96,97\n"
"Qk 97,98,99\nQk 98,100,101\nQk 99,102,103\nv_accvgpr_write_b32 a8, v96\nv_accvgpr_write_b32 a9, v97\nv_accvgpr_write_b32 a10, v98\nv_accvgpr_write_b32 a11, v99\nv_pk_mul_f32 v[104:105], v[104:105], v[24:25]\n"
"v_pk_mul_f32 v[106:107], v[106:107], v[24:25]\nv_pk_mul_f32 v[108:109], v[108:109], v[24:25]\nv_pk_mul_f32 v[110:111], v[110:111], v[24:25]\nQk 104,104,105\n.Lpg8:\ns_cbranch_execz .Ltr10\nQk 105,106,107\nQk 106,108,109\n"
"Qk 107,110,111\nv_accvgpr_write_b32 a12, v104\nv_accvgpr_write_b32 a13, v105\nv_accvgpr_write_b32 a14, v106\nv_accvgpr_write_b32 a15, v107\nv_pk_mul_f32 v[112:113], v[112:113], v[28:29]\nv_pk_mul_f32 v[114:115], v[114:115], v[28:29]\nv_pk_mul_f32 v[116:117], v[116:117], v[28:29]\n"
"v_pk_mul_f32 v[118:119], v[118:119], v[28:29]\nQk 112,112,113\nQk 113,114,115\nQk 114,116,117\nQk 115,118,119\nv_accvgpr_write_b32 a16, v112\nv_accvgpr_write_b32 a17, v113\nv_accvgpr_write_b32 a18, v114\n"
"v_accvgpr_write_b32 a19, v115\nv_pk_mul_f32 v[120:121], v[120:121], v[28:29]\nv_pk_mul_f32 v[122:123], v[122:123], v[28:29]\nv_pk_mul_f32 v[124:125], v[124:125], v[28:29]\nv_pk_mul_f32 v[126:127], v[126:127], v[28:29]\nQk 120,120,121\nQk 121,122,123\nQk 122,124,125\n"
"Qk 123,126,127\nv_accvgpr_write_b32 a20, v120\nv_accvgpr_write_b32 a21, v121\nv_accvgpr_write_b32 a22, v122\nv_accvgpr_write_b32 a23, v123\nv_pk_mul_f32 v[128:129], v[128:129], v[28:29]\nv_pk_mul_f32 v[130:131], v[130:131], v[28:29]\nv_pk_mul_f32 v[132:133], v[132:133], v[28:29]\n"
"v_pk_mul_f32 v[134:135], v[134:135], v[28:29]\nQk 128,128,129\nQk 129,130,131\nQk 130,132,133\nQk 131,134,135\nv_accvgpr_write_b32 a24, v128\nv_accvgpr_write_b32 a25, v129\nv_accvgpr_write_b32 a26, v130\n"
"v_accvgpr_write_b32 a27, v131\nv_pk_mul_f32 v[136:137], v[136:137], v[28:29]\nv_pk_mul_f32 v[138:139], v[138:139], v[28:29]\nv_pk_mul_f32 v[140:141], v[140:141], v[28:29]\nv_pk_mul_f32 v[142:143], v[142:143], v[28:29]\nQk 136,136,137\nQk 137,138,139\nQk 138,140,141\n"
"Qk 139,142,143\nv_accvgpr_write_b32 a28, v136\nv_accvgpr_write_b32 a29, v137\nv_accvgpr_write_b32 a30, v138\nv_accvgpr_write_b32 a31, v139\nv_pk_mul_f32 v[144:145], v[144:145], v[32:33]\nv_pk_mul_f32 v[146:147], v[146:147], v[32:33]\nv_pk_mul_f32 v[148:149], v[148:149], v[32:33]\n"
"v_pk_mul_f32 v[150:151], v[150:151], v[32:33]\nQk 144,144,145\nQk 145,146,147\nQk 146,148,149\nQk 147,150,151\nv_accvgpr_write_b32 a32, v144\nv_accvgpr_write_b32 a33, v145\nv_accvgpr_write_b32 a34, v146\n"
"v_accvgpr_write_b32 a35, v147\nv_pk_mul_f32 v[152:153], v[152:153], v[32:33]\nv_pk_mul_f32 v[154:155], v[154:155], v[32:33]\nv_pk_mul_f32 v[156:157], v[156:157], v[32:33]\nv_pk_mul_f32 v[158:159], v[158:159], v[32:33]\nQk 152,152,153\nQk 153,154,155\nQk 154,156,157\n"
"Qk 155,158,159\nv_accvgpr_write_b32 a36, v152\nv_accvgpr_write_b32 a37, v153\nv_accvgpr_write_b32 a38, v154\nv_accvgpr_write_b32 a39, v155\nv_pk_mul_f32 v[160:161], v[160:161], v[32:33]\nv_pk_mul_f32 v[162:163], v[162:163], v[32:33]\nv_pk_mul_f32 v[164:165], v[164:165], v[32:33]\n"
"v_pk_mul_f32 v[166:167], v[166:167], v[32:33]\nQk 160,160,161\nQk 161,162,163\nQk 162,164,165\nQk 163,166,167\nv_accvgpr_write_b32 a40, v160\nv_accvgpr_write_b32 a41, v161\nv_accvgpr_write_b32 a42, v162\n"
"v_accvgpr_write_b32 a43, v163\nv_pk_mul_f32 v[168:169], v[168:169], v[32:33]\nv_pk_mul_f32 v[170:171], v[170:171], v[32:33]\nv_pk_mul_f32 v[172:173], v[172:173], v[32:33]\nv_pk_mul_f32 v[174:175], v[174:175], v[32:33]\nQk 168,168,169\nQk 169,170,171\nQk 170,172,173\n"
"Qk 171,174,175\nv_accvgpr_write_b32 a44, v168\nv_accvgpr_write_b32 a45, v169\nv_accvgpr_write_b32 a46, v170\nv_accvgpr_write_b32 a47, v171\nv_pk_mul_f32 v[176:177], v[176:177], v[36:37]\nv_pk_mul_f32 v[178:179], v[178:179], v[36:37]\nv_pk_mul_f32 v[180:181], v[180:181], v[36:37]\n"
"v_pk_mul_f32 v[182:183], v[182:183], v[36:37]\nQk 176,176,177\nQk 177,178,179\nQk 178,180,181\nQk 179,182,183\nv_accvgpr_write_b32 a48, v176\nv_accvgpr_write_b32 a49, v177\nv_accvgpr_write_b32 a50, v178\n"
"v_accvgpr_write_b32 a51, v179\nv_pk_mul_f32 v[184:185], v[184:185], v[36:37]\nv_pk_mul_f32 v[186:187], v[186:187], v[36:37]\nv_pk_mul_f32 v[188:189], v[188:189], v[36:37]\nv_pk_mul_f32 v[190:191], v[190:191], v[36:37]\nQk 184,184,185\nQk 185,186,187\nQk 186,188,189\n"
"Qk 187,190,191\nv_accvgpr_write_b32 a52, v184\nv_accvgpr_write_b32 a53, v185\nv_accvgpr_write_b32 a54, v186\nv_accvgpr_write_b32 a55, v187\nv_pk_mul_f32 v[192:193], v[192:193], v[36:37]\nv_pk_mul_f32 v[194:195], v[194:195], v[36:37]\nv_pk_mul_f32 v[196:197], v[196:197], v[36:37]\n"
"v_pk_mul_f32 v[198:199], v[198:199], v[36:37]\nQk 192,192,193\nQk 193,194,195\nQk 194,196,197\nQk 195,198,199\nv_accvgpr_write_b32 a56, v192\nv_accvgpr_write_b32 a57, v193\nv_accvgpr_write_b32 a58, v194\n"
"v_accvgpr_write_b32 a59, v195\nv_pk_mul_f32 v[200:201], v[200:201], v[36:37]\nv_pk_mul_f32 v[202:203], v[202:203], v[36:37]\nv_pk_mul_f32 v[204:205], v[204:205], v[36:37]\nv_pk_mul_f32 v[206:207], v[206:207], v[36:37]\nQk 200,200,201\nQk 201,202,203\nQk 202,204,205\n"
"Qk 203,206,207\nv_accvgpr_write_b32 a60, v200\nv_accvgpr_write_b32 a61, v201\nv_accvgpr_write_b32 a62, v202\nv_accvgpr_write_b32 a63, v203\nv_pk_mul_f32 v[208:209], v[208:209], v[40:41]\nv_pk_mul_f32 v[210:211], v[210:211], v[40:41]\nv_pk_mul_f32 v[212:213], v[212:213], v[40:41]\n"
"v_pk_mul_f32 v[214:215], v[214:215], v[40:41]\nQk 208,208,209\nQk 209,210,211\nQk 210,212,213\nQk 211,214,215\nv_accvgpr_write_b32 a64, v208\nv_accvgpr_write_b32 a65, v209\nv_accvgpr_write_b32 a66, v210\n"
"v_accvgpr_write_b32 a67, v211\nv_pk_mul_f32 v[216:217], v[216:217], v[40:41]\nv_pk_mul_f32 v[218:219], v[218:219], v[40:41]\nv_pk_mul_f32 v[220:221], v[220:221], v[40:41]\nv_pk_mul_f32 v[222:223], v[222:223], v[40:41]\nQk 216,216,217\nQk 217,218,219\nQk 218,220,221\n"
"Qk 219,222,223\nv_accvgpr_write_b32 a68, v216\nv_accvgpr_write_b32 a69, v217\nv_accvgpr_write_b32 a70, v218\nv_accvgpr_write_b32 a71, v219\nv_pk_mul_f32 v[224:225], v[224:225], v[40:41]\nv_pk_mul_f32 v[226:227], v[226:227], v[40:41]\nv_pk_mul_f32 v[228:229], v[228:229], v[40:41]\n"
"v_pk_mul_f32 v[230:231], v[230:231], v[40:41]\nQk 224,224,225\nQk 225,226,227\nQk 226,228,229\nQk 227,230,231\nv_accvgpr_write_b32 a72, v224\nv_accvgpr_write_b32 a73, v225\nv_accvgpr_write_b32 a74, v226\n"
"v_accvgpr_write_b32 a75, v227\nv_pk_mul_f32 v[232:233], v[232:233], v[40:41]\nv_pk_mul_f32 v[234:235], v[234:235], v[40:41]\nv_pk_mul_f32 v[236:237], v[236:237], v[40:41]\nv_pk_mul_f32 v[238:239], v[238:239], v[40:41]\nQk 232,232,233\nQk 233,234,235\nQk 234,236,237\n"
"Qk 235,238,239\nv_accvgpr_write_b32 a76, v232\nv_accvgpr_write_b32 a77, v233\nv_accvgpr_write_b32 a78, v234\nv_accvgpr_write_b32 a79, v235\nv_pk_mul_f32 v[240:241], v[240:241], v[44:45]\nv_pk_mul_f32 v[242:243], v[242:243], v[44:45]\nv_pk_mul_f32 v[244:245], v[244:245], v[44:45]\n"
"v_pk_mul_f32 v[246:247], v[246:247], v[44:45]\nQk 240,240,241\nQk 241,242,243\nQk 242,244,245\nQk 243,246,247\nv_accvgpr_write_b32 a80, v240\nv_accvgpr_write_b32 a81, v241\nv_accvgpr_write_b32 a82, v242\n"
"v_accvgpr_write_b32 a83, v243\nv_pk_mul_f32 v[248:249], v[248:249], v[44:45]\nv_pk_mul_f32 v[250:251], v[250:251], v[44:45]\nv_pk_mul_f32 v[252:253], v[252:253], v[44:45]\nv_pk_mul_f32 v[254:255], v[254:255], v[44:45]\nQk 248,248,249\nQk 249,250,251\nQk 250,252,253\n"
"Qk 251,254,255\nv_accvgpr_write_b32 a84, v248\nv_accvgpr_write_b32 a85, v249\nv_accvgpr_write_b32 a86, v250\nv_accvgpr_write_b32 a87, v251\nv_accvgpr_read_b32 v0, a208\nv_accvgpr_read_b32 v1, a209\nv_accvgpr_read_b32 v2, a210\n"
"v_accvgpr_read_b32 v3, a211\nv_accvgpr_read_b32 v4, a212\nv_accvgpr_read_b32 v5, a213\nv_accvgpr_read_b32 v6, a214\nv_accvgpr_read_b32 v7, a215\nv_pk_mul_f32 v[0:1], v[0:1], v[44:45]\nv_pk_mul_f32 v[2:3], v[2:3], v[44:45]\nv_pk_mul_f32 v[4:5], v[4:5], v[44:45]\n"
"v_pk_mul_f32 v[6:7], v[6:7], v[44:45]\nQk 0,0,1\nQk 1,2,3\nQk 2,4,5\nQk 3,6,7\nv_accvgpr_write_b32 a88, v0\nv_accvgpr_write_b32 a89, v1\nv_accvgpr_write_b32 a90, v2\n"
"v_accvgpr_write_b32 a91, v3\nv_accvgpr_read_b32 v8, a216\nv_accvgpr_read_b32 v9, a217\nv_accvgpr_read_b32 v10, a218\nv_accvgpr_read_b32 v11, a219\nv_accvgpr_read_b32 v12, a220\nv_accvgpr_read_b32 v13, a221\nv_accvgpr_read_b32 v14, a222\n"
"v_accvgpr_read_b32 v15, a223\nv_pk_mul_f32 v[8:9], v[8:9], v[44:45]\nv_pk_mul_f32 v[10:11], v[10:11], v[44:45]\nv_pk_mul_f32 v[12:13], v[12:13], v[44:45]\nv_pk_mul_f32 v[14:15], v[14:15], v[44:45]\nQk 8,8,9\nQk 9,10,11\nQk 10,12,13\n"
"Qk 11,14,15\nv_accvgpr_write_b32 a92, v8\nv_accvgpr_write_b32 a93, v9\nv_accvgpr_write_b32 a94, v10\nv_accvgpr_write_b32 a95, v11\nv_accvgpr_read_b32 v0, a224\nv_accvgpr_read_b32 v1, a225\nv_accvgpr_read_b32 v2, a226\n"
"v_accvgpr_read_b32 v3, a227\nv_accvgpr_read_b32 v4, a228\nv_accvgpr_read_b32 v5, a229\nv_accvgpr_read_b32 v6, a230\nv_accvgpr_read_b32 v7, a231\nv_pk_mul_f32 v[0:1], v[0:1], v[48:49]\nv_pk_mul_f32 v[2:3], v[2:3], v[48:49]\nv_pk_mul_f32 v[4:5], v[4:5], v[48:49]\n"
"v_pk_mul_f32 v[6:7], v[6:7], v[48:49]\nQk 0,0,1\nQk 1,2,3\nQk 2,4,5\nQk 3,6,7\nv_accvgpr_write_b32 a96, v0\nv_accvgpr_write_b32 a97, v1\nv_accvgpr_write_b32 a98, v2\n"
"v_accvgpr_write_b32 a99, v3\nv_accvgpr_read_b32 v8, a232\nv_accvgpr_read_b32 v9, a233\nv_accvgpr_read_b32 v10, a234\nv_accvgpr_read_b32 v11, a235\nv_accvgpr_read_b32 v12, a236\nv_accvgpr_read_b32 v13, a237\nv_accvgpr_read_b32 v14, a238\n"
"v_accvgpr_read_b32 v15, a239\nv_pk_mul_f32 v[8:9], v[8:9], v[48:49]\nv_pk_mul_f32 v[10:11], v[10:11], v[48:49]\nv_pk_mul_f32 v[12:13], v[12:13], v[48:49]\nv_pk_mul_f32 v[14:15], v[14:15], v[48:49]\nQk 8,8,9\nQk 9,10,11\nQk 10,12,13\n"
"Qk 11,14,15\nv_accvgpr_write_b32 a100, v8\nv_accvgpr_write_b32 a101, v9\nv_accvgpr_write_b32 a102, v10\nv_accvgpr_write_b32 a103, v11\nv_accvgpr_read_b32 v0, a240\nv_accvgpr_read_b32 v1, a241\nv_accvgpr_read_b32 v2, a242\n"
"v_accvgpr_read_b32 v3, a243\nv_accvgpr_read_b32 v4, a244\nv_accvgpr_read_b32 v5, a245\nv_accvgpr_read_b32 v6, a246\nv_accvgpr_read_b32 v7, a247\nv_pk_mul_f32 v[0:1], v[0:1], v[48:49]\nv_pk_mul_f32 v[2:3], v[2:3], v[48:49]\nv_pk_mul_f32 v[4:5], v[4:5], v[48:49]\n"
"v_pk_mul_f32 v[6:7], v[6:7], v[48:49]\nQk 0,0,1\nQk 1,2,3\nQk 2,4,5\nQk 3,6,7\nv_accvgpr_write_b32 a104, v0\nv_accvgpr_write_b32 a105, v1\nv_accvgpr_write_b32 a106, v2\n"
"v_accvgpr_write_b32 a107, v3\nv_accvgpr_read_b32 v8, a248\nv_accvgpr_read_b32 v9, a249\nv_accvgpr_read_b32 v10, a250\nv_accvgpr_read_b32 v11, a251\nv_accvgpr_read_b32 v12, a252\nv_accvgpr_read_b32 v13, a253\nv_accvgpr_read_b32 v14, a254\n"
"v_accvgpr_read_b32 v15, a255\nv_pk_mul_f32 v[8:9], v[8:9], v[48:49]\nv_pk_mul_f32 v[10:11], v[10:11], v[48:49]\nv_pk_mul_f32 v[12:13], v[12:13], v[48:49]\nv_pk_mul_f32 v[14:15], v[14:15], v[48:49]\nQk 8,8,9\nQk 9,10,11\nQk 10,12,13\n"
"Qk 11,14,15\nv_accvgpr_write_b32 a108, v8\nv_accvgpr_write_b32 a109, v9\nv_accvgpr_write_b32 a110, v10\nv_accvgpr_write_b32 a111, v11\ns_mov_b32 s56, %8\ns_and_b32 s57, %9, 0xffff\ns_mov_b32 s94, 0x7ffff000\n"
"s_mul_i32 s68, %20, 40960\ns_lshl_b32 s69, s70, 10\ns_add_u32 s68, s68, s69\ns_add_u32 s68, s68, 4096\ns_add_u32 s94, %15, s68\ns_addc_u32 s95, %16, 0\ns_and_b32 s95, s95, 0xffff\ns_mov_b32 s75, 37888\n"
"s_mov_b32 s58, 1835008\ns_mov_b32 s67, 1\ns_mul_i32 s71, %12, 57344\ns_mul_i32 s68, s92, 7168\ns_add_u32 s71, s71, s68\ns_mov_b64 exec, 0\ns_cmp_eq_u32 s70, 0\ns_cbranch_scc1 .Lpg12\n"
"s_cmp_eq_u32 s70, 1\ns_cbranch_scc1 .Lpg13\ns_cmp_eq_u32 s70, 2\ns_cbranch_scc1 .Lpg14\ns_branch .Lpg15\n.Ltr6:\ns_mov_b64 exec, -1\ns_branch .Lround\n"
".Lhook:\n.Lpg9:\ns_cbranch_execz .Ltr10\ns_cmp_eq_u32 s66, 1\ns_cbranch_scc1 .Lh1\ns_cmp_eq_u32 s66, 8\ns_cbranch_scc1 .Lh8\ns_cmp_eq_u32 s66, 9\n"
"s_cbranch_scc1 .Lh9\ns_lshr_b32 s68, s99, s66\ns_cmp_eq_u32 s68, 1\ns_cbranch_scc1 .Lh12\ns_branch .Lh11\n.Lh1:\ns_cmp_eq_u32 s70, 0\ns_cselect_b32 s68, 128, 0\n"
"v_mov_b32 v66, 0x7ffff000\nv_cmp_gt_u32 vcc, s68, v60\nv_not_b32 v67, v65\ns_lshl_b32 s68, %19, 2\nv_cndmask_b32 v66, v66, v76, vcc\nv_readlane_b32 s30, v67, %19\nbuffer_store_dword v67, v66, s[24:27], s68 offen offset:1024 sc1\ns_branch .Lhookret\n"
".Lh8:\nv_bfe_u32 v66, v60, 4, 3\ns_lshl_b32 s68, %19, 7\nv_lshlrev_b32 v66, 2, v66\ns_add_u32 s68, s68, 2293760\nbuffer_load_dword v78, v66, s[24:27], s68 offen offset:1024 sc1\ns_branch .Lhookret\n.Lh9:\n"
"s_waitcnt vmcnt(32)\ns_mov_b32 s97, 0\n.Lack_l:\nv_cmp_ne_u32 vcc, s30, v78\ns_cmp_eq_u64 vcc, 0\ns_cbranch_scc1 .Lack_ok\ns_sleep 1\nv_bfe_u32 v66, v60, 4, 3\n"
"s_lshl_b32 s68, %19, 7\nv_lshlrev_b32 v66, 2, v66\ns_add_u32 s68, s68, 2293760\nbuffer_load_dword v78, v66, s[24:27], s68 offen offset:1024 sc1\ns_waitcnt vmcnt(0)\ns_add_u32 s97, s97, 1\ns_cmp_lt_u32 s97, 1048576\ns_cbranch_scc1 .Lack_l\n"
".Lack_ok:\ns_lshl_b32 s68, %19, 4\ns_cmp_eq_u32 s70, 0\ns_cselect_b32 s68, s68, -1\nv_mov_b32 v67, 0x7ffff000\nv_cmp_eq_u32 vcc, s68, v60\ns_nop 1\nv_cndmask_b32 v67, v67, v76, vcc\n"
"v_add_u32 v66, 1, v65\nbuffer_store_dword v66, v67, s[24:27], 0 offen sc1\ns_branch .Lhookret\n.Lh11:\nbuffer_load_dword v78, v76, s[24:27], 0 offen sc1\ns_branch .Lhookret\n.Lh12:\ns_waitcnt vmcnt(32)\n"
"v_sub_u32 v66, v78, v65\nv_cmp_gt_u32 vcc, 1, v66\ns_and_b64 vcc, vcc, s[28:29]\ns_cbranch_scc1 .Lf1_w\n.Lf1_r:\ns_cmp_lg_u32 s70, 3\ns_cbranch_scc1 .Lf1_n\nbuffer_inv sc1\n"
".Lf1_n:\ns_lshr_b32 s88, %19, 2\ns_lshl_b32 s88, s88, 4\ns_add_u32 s88, s88, s93\ns_mul_i32 s68, s88, 57344\ns_mul_i32 s69, s92, 7168\ns_add_u32 s68, s68, s69\ns_add_u32 s48, %8, s68\n"
"s_addc_u32 s49, %9, 0\ns_and_b32 s49, s49, 0xffff\ns_sub_u32 s50, 1835008, s68\ns_mul_i32 s68, s88, 163840\ns_and_b32 s69, %19, 3\ns_mul_i32 s69, s69, 320\ns_add_u32 s68, s68, s69\ns_add_u32 s52, %15, s68\n"
"s_addc_u32 s53, %16, 0\ns_and_b32 s53, s53, 0xffff\ns_sub_u32 s54, 5246976, s68\ns_add_u32 s71, s71, 458752\ns_mul_i32 s69, s92, 10240\nv_bfe_u32 v66, v60, 5, 3\nv_mul_u32_u24 v66, 1280, v66\nv_and_b32 v61, 16, v60\n"
"v_add3_u32 v61, v61, v66, s69\nv_lshrrev_b32 v66, 8, v60\nv_lshl_add_u32 v61, v66, 5, v61\nv_lshrrev_b32 v66, 9, v60\nv_mul_u32_u24 v66, 20288, v66\nv_add_u32 v62, v61, v66\ns_branch .Lhookret\n.Lf1_w:\n"
"s_mov_b32 s97, 0\n.Lf1_l:\ns_sleep 1\nbuffer_load_dword v78, v76, s[24:27], 0 offen sc1\ns_waitcnt vmcnt(0)\ns_add_u32 s97, s97, 1\ns_cmp_gt_u32 s97, 1048576\ns_cbranch_scc1 .Lf1_r\n"
"v_sub_u32 v66, v78, v65\nv_cmp_gt_u32 vcc, 1, v66\ns_and_b64 vcc, vcc, s[28:29]\ns_cbranch_scc1 .Lf1_l\ns_branch .Lf1_r\n.Lsa1:\nds_read_b32 v66, v74\ns_waitcnt lgkmcnt(0)\n"
"v_cmp_gt_u32 vcc, v75, v66\ns_cbranch_vccnz .Lsa1\ns_branch .Lra1\n.Lsp1:\ns_memrealtime s[74:75]\ns_waitcnt lgkmcnt(0)\ns_sub_u32 s75, s77, s74\ns_cmp_lt_u32 s75, 256\n"
"s_cbranch_scc1 .Lsp1\ns_mov_b32 s74, s77\ns_branch .Lrp1\n.Lsa2:\nds_read_b32 v66, v74\ns_waitcnt lgkmcnt(0)\nv_cmp_gt_u32 vcc, v75, v66\ns_cbranch_vccnz .Lsa2\n"
"s_branch .Lra2\n.Lsp2:\ns_memrealtime s[74:75]\ns_waitcnt lgkmcnt(0)\ns_sub_u32 s75, s77, s74\ns_cmp_lt_u32 s75, 256\ns_cbranch_scc1 .Lsp2\ns_mov_b32 s74, s77\n"
"s_branch .Lrp2\n.Lsa3:\nds_read_b32 v66, v74\ns_waitcnt lgkmcnt(0)\nv_cmp_gt_u32 vcc, v75, v66\ns_cbranch_vccnz .Lsa3\ns_branch .Lra3\n.Lsp3:\n"
"s_memrealtime s[74:75]\ns_waitcnt lgkmcnt(0)\ns_sub_u32 s75, s77, s74\ns_cmp_lt_u32 s75, 256\ns_cbranch_scc1 .Lsp3\ns_mov_b32 s74, s77\ns_branch .Lrp3\n.Lsa4:\n"
"ds_read_b32 v66, v74\ns_waitcnt lgkmcnt(0)\nv_cmp_gt_u32 vcc, v75, v66\ns_cbranch_vccnz .Lsa4\ns_branch .Lra4\n.Lsp4:\ns_memrealtime s[74:75]\ns_waitcnt lgkmcnt(0)\n"
"s_sub_u32 s75, s77, s74\ns_cmp_lt_u32 s75, 256\ns_cbranch_scc1 .Lsp4\ns_mov_b32 s74, s77\ns_branch .Lrp4\n.Lsa5:\nds_read_b32 v66, v74\ns_waitcnt lgkmcnt(0)\n"
"v_cmp_gt_u32 vcc, v75, v66\ns_cbranch_vccnz .Lsa5\ns_branch .Lra5\n.Lsp5:\ns_memrealtime s[74:75]\ns_waitcnt lgkmcnt(0)\ns_sub_u32 s75, s77, s74\ns_cmp_lt_u32 s75, 256\n"
"s_cbranch_scc1 .Lsp5\ns_mov_b32 s74, s77\ns_branch .Lrp5\n.Lsa30:\nds_read_b32 v66, v74\ns_waitcnt lgkmcnt(0)\nv_cmp_gt_u32 vcc, v75, v66\ns_cbranch_vccnz .Lsa30\n"
"s_branch .Lra30\n.Lsa31:\nds_read_b32 v66, v74\ns_waitcnt lgkmcnt(0)\nv_cmp_gt_u32 vcc, v75, v66\ns_cbranch_vccnz .Lsa31\ns_branch .Lra31\n.Lsa32:\n"
"ds_read_b32 v66, v74\ns_waitcnt lgkmcnt(0)\nv_cmp_gt_u32 vcc, v75, v66\ns_cbranch_vccnz .Lsa32\ns_branch .Lra32\n.Lsa30x:\nds_read_b32 v66, v74\ns_waitcnt lgkmcnt(0)\n"
"v_cmp_gt_u32 vcc, v75, v66\ns_cbranch_vccnz .Lsa30x\ns_branch .Lra30x\n.Lsa31x:\nds_read_b32 v66, v74\ns_waitcnt lgkmcnt(0)\nv_cmp_gt_u32 vcc, v75, v66\ns_cbranch_vccnz .Lsa31x\n"
"s_branch .Lra31x\n.Lso0:\nds_read_b32 v66, v74\ns_waitcnt lgkmcnt(0)\nv_cmp_gt_u32 vcc, v75, v66\ns_cbranch_vccnz .Lso0\ns_branch .Lro0\n.Lso1:\n"
"ds_read_b32 v66, v74\ns_waitcnt lgkmcnt(0)\nv_cmp_gt_u32 vcc, v75, v66\ns_cbranch_vccnz .Lso1\ns_branch .Lro1\n.Lso2:\nds_read_b32 v66, v74\ns_waitcnt lgkmcnt(0)\n"
"v_cmp_gt_u32 vcc, v75, v66\ns_cbranch_vccnz .Lso2\ns_branch .Lro2\n.Lso3:\nds_read_b32 v66, v74\ns_waitcnt lgkmcnt(0)\nv_cmp_gt_u32 vcc, v75, v66\ns_cbranch_vccnz .Lso3\n"
"s_branch .Lro3\n.Lso4:\nds_read_b32 v66, v74\ns_waitcnt lgkmcnt(0)\nv_cmp_gt_u32 vcc, v75, v66\ns_cbranch_vccnz .Lso4\ns_branch .Lro4\n.Lso5:\n"
"ds_read_b32 v66, v74\ns_waitcnt lgkmcnt(0)\nv_cmp_gt_u32 vcc, v75, v66\ns_cbranch_vccnz .Lso5\ns_branch .Lro5\n.Lso6:\nds_read_b32 v66, v74\ns_waitcnt lgkmcnt(0)\n"
"v_cmp_gt_u32 vcc, v75, v66\ns_cbranch_vccnz .Lso6\ns_branch .Lro6\n.Lso7:\nds_read_b32 v66, v74\ns_waitcnt lgkmcnt(0)\nv_cmp_gt_u32 vcc, v75, v66\ns_cbranch_vccnz .Lso7\n"
"s_branch .Lro7\n.Lso8:\nds_read_b32 v66, v74\ns_waitcnt lgkmcnt(0)\nv_cmp_gt_u32 vcc, v75, v66\ns_cbranch_vccnz .Lso8\ns_branch .Lro8\n.Lso9:\n"
"ds_read_b32 v66, v74\ns_waitcnt lgkmcnt(0)\nv_cmp_gt_u32 vcc, v75, v66\ns_cbranch_vccnz .Lso9\ns_branch .Lro9\n.Lpg10:\ns_cbranch_execz .Ltr10\n.Lso10:\n"
"ds_read_b32 v66, v74\ns_waitcnt lgkmcnt(0)\nv_cmp_gt_u32 vcc, v75, v66\ns_cbranch_vccnz .Lso10\ns_branch .Lro10\n.Lso11:\nds_read_b32 v66, v74\ns_waitcnt lgkmcnt(0)\n"
"v_cmp_gt_u32 vcc, v75, v66\ns_cbranch_vccnz .Lso11\ns_branch .Lro11\n.Lso12:\nds_read_b32 v66, v74\ns_waitcnt lgkmcnt(0)\nv_cmp_gt_u32 vcc, v75, v66\ns_cbranch_vccnz .Lso12\n"
"s_branch .Lro12\n.Lso13:\nds_read_b32 v66, v74\ns_waitcnt lgkmcnt(0)\nv_cmp_gt_u32 vcc, v75, v66\ns_cbranch_vccnz .Lso13\ns_branch .Lro13\n.Lso14:\n"
"ds_read_b32 v66, v74\ns_waitcnt lgkmcnt(0)\nv_cmp_gt_u32 vcc, v75, v66\ns_cbranch_vccnz .Lso14\ns_branch .Lro14\n.Lso15:\nds_read_b32 v66, v74\ns_waitcnt lgkmcnt(0)\n"
"v_cmp_gt_u32 vcc, v75, v66\ns_cbranch_vccnz .Lso15\ns_branch .Lro15\n.Lso16:\nds_read_b32 v66, v74\ns_waitcnt lgkmcnt(0)\nv_cmp_gt_u32 vcc, v75, v66\ns_cbranch_vccnz .Lso16\n"
"s_branch .Lro16\n.Lso17:\nds_read_b32 v66, v74\ns_waitcnt lgkmcnt(0)\nv_cmp_gt_u32 vcc, v75, v66\ns_cbranch_vccnz .Lso17\ns_branch .Lro17\n.Lso18:\n"
"ds_read_b32 v66, v74\ns_waitcnt lgkmcnt(0)\nv_cmp_gt_u32 vcc, v75, v66\ns_cbranch_vccnz .Lso18\ns_branch .Lro18\n.Lso19:\nds_read_b32 v66, v74\ns_waitcnt lgkmcnt(0)\n"
"v_cmp_gt_u32 vcc, v75, v66\ns_cbranch_vccnz .Lso19\ns_branch .Lro19\n.Lso20:\nds_read_b32 v66, v74\ns_waitcnt lgkmcnt(0)\nv_cmp_gt_u32 vcc, v75, v66\ns_cbranch_vccnz .Lso20\n"
"s_branch .Lro20\n.Lso21:\nds_read_b32 v66, v74\ns_waitcnt lgkmcnt(0)\nv_cmp_gt_u32 vcc, v75, v66\ns_cbranch_vccnz .Lso21\ns_branch .Lro21\n.Lso22:\n"
"ds_read_b32 v66, v74\ns_waitcnt lgkmcnt(0)\nv_cmp_gt_u32 vcc, v75, v66\ns_cbranch_vccnz .Lso22\ns_branch .Lro22\n.Lso23:\nds_read_b32 v66, v74\ns_waitcnt lgkmcnt(0)\n"
"v_cmp_gt_u32 vcc, v75, v66\ns_cbranch_vccnz .Lso23\ns_branch .Lro23\n.Lso24:\nds_read_b32 v66, v74\ns_waitcnt lgkmcnt(0)\nv_cmp_gt_u32 vcc, v75, v66\ns_cbranch_vccnz .Lso24\n"
"s_branch .Lro24\n.Lso25:\nds_read_b32 v66, v74\ns_waitcnt lgkmcnt(0)\nv_cmp_gt_u32 vcc, v75, v66\ns_cbranch_vccnz .Lso25\ns_branch .Lro25\n.Lso26:\n"
"ds_read_b32 v66, v74\ns_waitcnt lgkmcnt(0)\nv_cmp_gt_u32 vcc, v75, v66\ns_cbranch_vccnz .Lso26\ns_branch .Lro26\n.Lso27:\nds_read_b32 v66, v74\ns_waitcnt lgkmcnt(0)\n"
"v_cmp_gt_u32 vcc, v75, v66\ns_cbranch_vccnz .Lso27\ns_branch .Lro27\n.Lso28:\nds_read_b32 v66, v74\ns_waitcnt lgkmcnt(0)\nv_cmp_gt_u32 vcc, v75, v66\ns_cbranch_vccnz .Lso28\n"
"s_branch .Lro28\n.Lso29:\nds_read_b32 v66, v74\ns_waitcnt lgkmcnt(0)\nv_cmp_gt_u32 vcc, v75, v66\ns_cbranch_vccnz .Lso29\ns_branch .Lro29\n.Lso30:\n"
"ds_read_b32 v66, v74\ns_waitcnt lgkmcnt(0)\nv_cmp_gt_u32 vcc, v75, v66\ns_cbranch_vccnz .Lso30\ns_branch .Lro30\n.Lso31:\nds_read_b32 v66, v74\ns_waitcnt lgkmcnt(0)\n"
"v_cmp_gt_u32 vcc, v75, v66\ns_cbranch_vccnz .Lso31\ns_branch .Lro31\n.Lso32:\nds_read_b32 v66, v74\ns_waitcnt lgkmcnt(0)\nv_cmp_gt_u32 vcc, v75, v66\ns_cbranch_vccnz .Lso32\n"
"s_branch .Lro32\n.Lso33:\nds_read_b32 v66, v74\ns_waitcnt lgkmcnt(0)\nv_cmp_gt_u32 vcc, v75, v66\ns_cbranch_vccnz .Lso33\ns_branch .Lro33\n.Lso34:\n"
"ds_read_b32 v66, v74\ns_waitcnt lgkmcnt(0)\nv_cmp_gt_u32 vcc, v75, v66\ns_cbranch_vccnz .Lso34\ns_branch .Lro34\n.Lso35:\nds_read_b32 v66, v74\ns_waitcnt lgkmcnt(0)\n"
"v_cmp_gt_u32 vcc, v75, v66\ns_cbranch_vccnz .Lso35\ns_branch .Lro35\n.Lso36:\nds_read_b32 v66, v74\ns_waitcnt lgkmcnt(0)\nv_cmp_gt_u32 vcc, v75, v66\ns_cbranch_vccnz .Lso36\n"
"s_branch .Lro36\n.Lso37:\nds_read_b32 v66, v74\ns_waitcnt lgkmcnt(0)\nv_cmp_gt_u32 vcc, v75, v66\ns_cbranch_vccnz .Lso37\ns_branch .Lro37\n.Lso38:\n"
"ds_read_b32 v66, v74\ns_waitcnt lgkmcnt(0)\nv_cmp_gt_u32 vcc, v75, v66\ns_cbranch_vccnz .Lso38\ns_branch .Lro38\n.Lso39:\nds_read_b32 v66, v74\ns_waitcnt lgkmcnt(0)\n"
"v_cmp_gt_u32 vcc, v75, v66\ns_cbranch_vccnz .Lso39\ns_branch .Lro39\n.Lso40:\nds_read_b32 v66, v74\ns_waitcnt lgkmcnt(0)\nv_cmp_gt_u32 vcc, v75, v66\ns_cbranch_vccnz .Lso40\n"
"s_branch .Lro40\n.Lso41:\nds_read_b32 v66, v74\ns_waitcnt lgkmcnt(0)\nv_cmp_gt_u32 vcc, v75, v66\ns_cbranch_vccnz .Lso41\ns_branch .Lro41\n.Lso42:\n"
"ds_read_b32 v66, v74\ns_waitcnt lgkmcnt(0)\nv_cmp_gt_u32 vcc, v75, v66\ns_cbranch_vccnz .Lso42\ns_branch .Lro42\n.Lso43:\nds_read_b32 v66, v74\ns_waitcnt lgkmcnt(0)\n"
"v_cmp_gt_u32 vcc, v75, v66\ns_cbranch_vccnz .Lso43\ns_branch .Lro43\n.Lso44:\nds_read_b32 v66, v74\ns_waitcnt lgkmcnt(0)\nv_cmp_gt_u32 vcc, v75, v66\ns_cbranch_vccnz .Lso44\n"
"s_branch .Lro44\n.Lso45:\nds_read_b32 v66, v74\ns_waitcnt lgkmcnt(0)\nv_cmp_gt_u32 vcc, v75, v66\ns_cbranch_vccnz .Lso45\ns_branch .Lro45\n.Lso46:\n"
"ds_read_b32 v66, v74\ns_waitcnt lgkmcnt(0)\nv_cmp_gt_u32 vcc, v75, v66\ns_cbranch_vccnz .Lso46\ns_branch .Lro46\n.Lso47r0:\nds_read_b32 v66, v74\ns_waitcnt lgkmcnt(0)\n"
"v_cmp_gt_u32 vcc, v75, v66\ns_cbranch_vccnz .Lso47r0\ns_branch .Lro47r0\n.Lso48r0:\nds_read_b32 v66, v74\ns_waitcnt lgkmcnt(0)\nv_cmp_gt_u32 vcc, v75, v66\ns_cbranch_vccnz .Lso48r0\n"
"s_branch .Lro48r0\n.Lso49r0:\nds_read_b32 v66, v74\ns_waitcnt lgkmcnt(0)\nv_cmp_gt_u32 vcc, v75, v66\ns_cbranch_vccnz .Lso49r0\ns_branch .Lro49r0\n.Lso50r0:\n"
"ds_read_b32 v66, v74\ns_waitcnt lgkmcnt(0)\nv_cmp_gt_u32 vcc, v75, v66\ns_cbranch_vccnz .Lso50r0\ns_branch .Lro50r0\n.Lso51r0:\nds_read_b32 v66, v74\ns_waitcnt lgkmcnt(0)\n"
"v_cmp_gt_u32 vcc, v75, v66\ns_cbranch_vccnz .Lso51r0\ns_branch .Lro51r0\n.Lso52r0:\nds_read_b32 v66, v74\ns_waitcnt lgkmcnt(0)\nv_cmp_gt_u32 vcc, v75, v66\ns_cbranch_vccnz .Lso52r0\n"
"s_branch .Lro52r0\n.Lso53r0:\nds_read_b32 v66, v74\ns_waitcnt lgkmcnt(0)\nv_cmp_gt_u32 vcc, v75, v66\ns_cbranch_vccnz .Lso53r0\ns_branch .Lro53r0\n.Lso54r0:\n"
"ds_read_b32 v66, v74\ns_waitcnt lgkmcnt(0)\nv_cmp_gt_u32 vcc, v75, v66\ns_cbranch_vccnz .Lso54r0\ns_branch .Lro54r0\n.Lso55r0:\nds_read_b32 v66, v74\ns_waitcnt lgkmcnt(0)\n"
"v_cmp_gt_u32 vcc, v75, v66\ns_cbranch_vccnz .Lso55r0\ns_branch .Lro55r0\n.Lso56r0:\nds_read_b32 v66, v74\ns_waitcnt lgkmcnt(0)\nv_cmp_gt_u32 vcc, v75, v66\ns_cbranch_vccnz .Lso56r0\n"
"s_branch .Lro56r0\n.Lso57r0:\nds_read_b32 v66, v74\ns_waitcnt lgkmcnt(0)\nv_cmp_gt_u32 vcc, v75, v66\ns_cbranch_vccnz .Lso57r0\ns_branch .Lro57r0\n.Lso58r0:\n"
"ds_read_b32 v66, v74\ns_waitcnt lgkmcnt(0)\nv_cmp_gt_u32 vcc, v75, v66\ns_cbranch_vccnz .Lso58r0\ns_branch .Lro58r0\n.Lso59r0:\nds_read_b32 v66, v74\ns_waitcnt lgkmcnt(0)\n"
"v_cmp_gt_u32 vcc, v75, v66\ns_cbranch_vccnz .Lso59r0\ns_branch .Lro59r0\n.Lso60r0:\nds_read_b32 v66, v74\ns_waitcnt lgkmcnt(0)\nv_cmp_gt_u32 vcc, v75, v66\ns_cbranch_vccnz .Lso60r0\n"
"s_branch .Lro60r0\n.Lso61r0:\nds_read_b32 v66, v74\ns_waitcnt lgkmcnt(0)\nv_cmp_gt_u32 vcc, v75, v66\ns_cbranch_vccnz .Lso61r0\ns_branch .Lro61r0\n.Lso62r0:\n"
"ds_read_b32 v66, v74\ns_waitcnt lgkmcnt(0)\nv_cmp_gt_u32 vcc, v75, v66\ns_cbranch_vccnz .Lso62r0\ns_branch .Lro62r0\n.Lso63r0:\nds_read_b32 v66, v74\ns_waitcnt lgkmcnt(0)\n"
"v_cmp_gt_u32 vcc, v75, v66\ns_cbranch_vccnz .Lso63r0\ns_branch .Lro63r0\n.Lso47r1:\nds_read_b32 v66, v74\ns_waitcnt lgkmcnt(0)\nv_cmp_gt_u32 vcc, v75, v66\ns_cbranch_vccnz .Lso47r1\n"
"s_branch .Lro47r1\n.Lso48r1:\nds_read_b32 v66, v74\ns_waitcnt lgkmcnt(0)\nv_cmp_gt_u32 vcc, v75, v66\ns_cbranch_vccnz .Lso48r1\ns_branch .Lro48r1\n.Lso49r1:\n"
"ds_read_b32 v66, v74\ns_waitcnt lgkmcnt(0)\nv_cmp_gt_u32 vcc, v75, v66\ns_cbranch_vccnz .Lso49r1\ns_branch .Lro49r1\n.Lso50r1:\nds_read_b32 v66, v74\ns_waitcnt lgkmcnt(0)\n"
"v_cmp_gt_u32 vcc, v75, v66\ns_cbranch_vccnz .Lso50r1\ns_branch .Lro50r1\n.Lso51r1:\nds_read_b32 v66, v74\ns_waitcnt lgkmcnt(0)\nv_cmp_gt_u32 vcc, v75, v66\ns_cbranch_vccnz .Lso51r1\n"
"s_branch .Lro51r1\n.Lso52r1:\nds_read_b32 v66, v74\ns_waitcnt lgkmcnt(0)\nv_cmp_gt_u32 vcc, v75, v66\ns_cbranch_vccnz .Lso52r1\ns_branch .Lro52r1\n.Lso53r1:\n"
"ds_read_b32 v66, v74\ns_waitcnt lgkmcnt(0)\nv_cmp_gt_u32 vcc, v75, v66\ns_cbranch_vccnz .Lso53r1\ns_branch .Lro53r1\n.Lso54r1:\nds_read_b32 v66, v74\ns_waitcnt lgkmcnt(0)\n"
"v_cmp_gt_u32 vcc, v75, v66\ns_cbranch_vccnz .Lso54r1\ns_branch .Lro54r1\n.Lso55r1:\nds_read_b32 v66, v74\ns_waitcnt lgkmcnt(0)\nv_cmp_gt_u32 vcc, v75, v66\ns_cbranch_vccnz .Lso55r1\n"
"s_branch .Lro55r1\n.Lso56r1:\nds_read_b32 v66, v74\ns_waitcnt lgkmcnt(0)\nv_cmp_gt_u32 vcc, v75, v66\ns_cbranch_vccnz .Lso56r1\ns_branch .Lro56r1\n.Lso57r1:\n"
"ds_read_b32 v66, v74\ns_waitcnt lgkmcnt(0)\nv_cmp_gt_u32 vcc, v75, v66\ns_cbranch_vccnz .Lso57r1\ns_branch .Lro57r1\n.Lso58r1:\nds_read_b32 v66, v74\ns_waitcnt lgkmcnt(0)\n"
"v_cmp_gt_u32 vcc, v75, v66\ns_cbranch_vccnz .Lso58r1\ns_branch .Lro58r1\n.Lso59r1:\nds_read_b32 v66, v74\ns_waitcnt lgkmcnt(0)\nv_cmp_gt_u32 vcc, v75, v66\ns_cbranch_vccnz .Lso59r1\n"
"s_branch .Lro59r1\n.Lso60r1:\nds_read_b32 v66, v74\ns_waitcnt lgkmcnt(0)\nv_cmp_gt_u32 vcc, v75, v66\ns_cbranch_vccnz .Lso60r1\ns_branch .Lro60r1\n.Lso61r1:\n"
"ds_read_b32 v66, v74\ns_waitcnt lgkmcnt(0)\nv_cmp_gt_u32 vcc, v75, v66\ns_cbranch_vccnz .Lso61r1\ns_branch .Lro61r1\n.Lso62r1:\nds_read_b32 v66, v74\ns_waitcnt lgkmcnt(0)\n"
"v_cmp_gt_u32 vcc, v75, v66\ns_cbranch_vccnz .Lso62r1\ns_branch .Lro62r1\n.Lso63r1:\nds_read_b32 v66, v74\ns_waitcnt lgkmcnt(0)\nv_cmp_gt_u32 vcc, v75, v66\ns_cbranch_vccnz .Lso63r1\n"
"s_branch .Lro63r1\n.Ltail1:\n.Lpg11:\ns_cbranch_execz .Ltr10\nQcvaa 0,112,32,56\nv_add_u32 v75, 1, v75\nQk 38,4,5\nQmvaa 0,116,36\n"
"s_waitcnt vmcnt(0)\nds_write_b32 v73, v75\nQmvaa 0,120,40\nQe 7\nQmvaa 0,124,44\nQk 39,6,7\nQcvaa 4,128,32,56\nQe 8\n"
"Qmvaa 4,132,36\nQe 9\nQmvaa 4,136,40\nQe 10\nQmvaa 4,140,44\ns_waitcnt lgkmcnt(1)\nQk 40,8,9\nQmvav 96,144,44\n"
"Qe 11\nQmvav 96,148,36\nQk 41,10,11\nQmvav 100,152,44\nQe 12\nQmvav 100,156,36\nQe 13\nQmvav 104,160,44\n"
"Qe 14\nQmvav 104,164,36\nQe 15\nQmvav 108,168,44\nQk 42,12,13\nQk 43,14,15\nQmvav 108,172,36\nQe 0\n"
"Qcvaa 8,112,48,56\nQe 1\nQmvaa 8,116,52\nQe 2\nQmvaa 8,120,56\nds_read_b32 v66, v74\nQk 44,0,1\nQmvaa 8,124,60\n"
"Qe 3\nQcvaa 12,128,48,56\nQk 45,2,3\nQmvaa 12,132,52\nQe 4\nv_smfmac_f32_16x16x64_bf16 a[176:179], a[204:207], v[36:43], v77\nQmvaa 12,136,56\ns_waitcnt lgkmcnt(0)\n"
"v_cmp_gt_u32 vcc, v75, v66\nQmvaa 12,140,60\ns_cbranch_vccnz .Lsa30x\n.Lra30x:\nQmvav 128,144,48\ns_add_u32 m0, s91, 132096\ns_mov_b32 s94, 0\nQmvav 128,148,40\n"
"buffer_load_dwordx4 v60, s[48:51], s94 offen lds\nQmvav 132,152,48\nQe 5\nQmvav 132,156,40\nQe 6\nQmvav 136,160,48\nQe 7\nQmvav 136,164,40\n"
"Qk 46,4,5\nQk 47,6,7\nQmvav 140,168,48\nbuffer_load_dwordx4 v60, s[48:51], s94 offen offset:1024 lds\nQmvav 140,172,40\nQe 8\nQcvaa 0,112,64,56\nQe 9\n"
"Qmvaa 0,116,68\nQe 10\nQmvaa 0,120,72\nQe 11\nQmvaa 0,124,76\nbuffer_load_dwordx4 v60, s[48:51], s94 offen offset:2048 lds\nQcvaa 4,128,64,56\nQk 48,8,9\n"
"Qk 49,10,11\nQmvaa 4,132,68\nQe 12\nQmvaa 4,136,72\nQe 13\nQmvaa 4,140,76\ns_mov_b32 s95, 1024\nQe 14\n"
"Qmvav 160,144,52\nbuffer_load_dwordx4 v61, s[52:55], s95 offen offset:3072 lds\nQmvav 160,148,44\nQe 15\nQmvav 164,152,52\nQk 50,12,13\nQk 51,14,15\nQmvav 164,156,44\n"
"Qe 0\nQmvav 168,160,52\ns_add_u32 m0, s91, 136192\ns_mov_b32 s95, 4224\nQmvav 168,164,44\nbuffer_load_dwordx4 v62, s[52:55], s95 offen lds\nQmvav 172,168,52\nQe 1\n"
"Qmvav 172,172,44\nQe 2\nQcvaa 8,112,80,56\nQe 3\nQmvaa 8,116,84\ns_mov_b32 s95, 23616\nQk 16,0,1\nQk 17,2,3\n"
"Qmvaa 8,120,88\nbuffer_load_dwordx4 v61, s[52:55], s95 offen offset:1024 lds\nQmvaa 8,124,92\nQe 4\nQcvaa 12,128,80,56\nQe 5\nQmvaa 12,132,84\nQe 6\n"
"v_smfmac_f32_16x16x64_bf16 a[180:183], a[204:207], v[44:51], v77\ns_add_u32 m0, s91, 0\ns_mov_b32 s94, 3072\nQk 18,4,5\nQmvaa 12,136,88\nbuffer_load_dwordx4 v60, s[48:51], s94 offen lds\nQmvaa 12,140,92\nQe 7\n"
"Qmvav 192,144,20\nQk 19,6,7\nQmvav 192,148,48\nQmvav 196,152,20\nQe 8\nQmvav 196,156,48\nbuffer_load_dwordx4 v60, s[48:51], s94 offen offset:1024 lds\nQmvav 200,160,20\n"
"Qe 9\nQmvav 200,164,48\nQe 10\nQmvav 204,168,20\nQmvav 204,172,48\nQe 11\nQcvaa 0,112,96,56\nQda 112,68,33024\n"
"buffer_load_dwordx4 v60, s[48:51], s94 offen offset:2048 lds\nQk 20,8,9\nQmvaa 0,116,100\nQda 116,68,33536\nQk 21,10,11\nQmvaa 0,120,104\nQda 120,68,34048\nQmvaa 0,124,108\n"
"Qda 124,68,34560\nQcvaa 4,128,96,56\nQda 128,68,35072\nQmvaa 4,132,100\nQda 132,68,35584\nbuffer_load_dwordx4 v60, s[48:51], s94 offen offset:3072 lds\nQmvaa 4,136,104\nQda 136,68,36096\n"
"Qmvaa 4,140,108\nQda 140,68,36608\nQmvav 224,144,24\nQe 12\nQmvav 224,148,16\ns_add_u32 m0, s91, 4096\ns_mov_b32 s95, 4288\nQmvav 228,152,24\n"
"buffer_load_dwordx4 v61, s[52:55], s95 offen lds\nQe 13\nQmvav 228,156,16\nQe 14\nQmvav 232,160,24\nQe 15\nQmvav 232,164,16\nQk 22,12,13\n"
"Qk 23,14,15\nQmvav 236,168,24\ns_mov_b32 s95, 23744\nQe 0\nQmvav 236,172,16\nbuffer_load_dwordx4 v61, s[52:55], s95 offen offset:1024 lds\ns_waitcnt lgkmcnt(0)\nQcvaa 8,112,0,56\n"
"Qe 1\nQmvaa 8,116,4\nv_subrev_u32 v68, 66048, v68\nQk 24,0,1\nQmvaa 8,120,8\nQe 2\nQmvaa 8,124,12\ns_add_u32 m0, s91, 24576\n"
"s_mov_b32 s94, 14336\nQe 3\nQcvaa 12,128,0,56\nbuffer_load_dwordx4 v60, s[48:51], s94 offen lds\nQk 25,2,3\nQmvaa 12,132,4\nQe 4\nv_smfmac_f32_16x16x64_bf16 a[184:187], a[204:207], v[16:23], v77\n"
"Qmvaa 12,136,8\nQe 5\nQmvaa 12,140,12\nQe 6\nQmaav 208,144,28\nbuffer_load_dwordx4 v60, s[48:51], s94 offen offset:1024 lds\nQmaav 208,148,20\nQmaav 212,152,28\n"
"Qk 26,4,5\nQmaav 212,156,20\nQmaav 216,160,28\nQmaav 216,164,20\nbuffer_load_dwordx4 v60, s[48:51], s94 offen offset:2048 lds\nQe 7\nQmaav 220,168,28\nQk 27,6,7\n"
"Qmaav 220,172,20\nQe 8\nQcvaa 0,112,16,56\nQe 9\nQmvaa 0,116,20\ns_mov_b32 s95, 41984\nQe 10\nQmvaa 0,120,24\n"
"buffer_load_dwordx4 v61, s[52:55], s95 offen offset:3072 lds\nQe 11\nQmvaa 0,124,28\nQk 28,8,9\nQk 29,10,11\nQcvaa 4,128,16,56\nQe 12\nQmvaa 4,132,20\n"
"Qe 13\nQmvaa 4,136,24\ns_add_u32 m0, s91, 28672\ns_mov_b32 s95, 45184\nQe 14\nQmvaa 4,140,28\nbuffer_load_dwordx4 v62, s[52:55], s95 offen lds\nQe 15\n"
"Qmaav 240,144,32\nQta 144,71,0\nQta 146,71,2048\nQmaav 240,148,24\nQta 148,71,33024\nQta 150,71,35072\nQmaav 244,152,32\nQta 152,71,256\n"
"Qta 154,71,2304\nQmaav 244,156,24\nQta 156,71,33280\nQta 158,71,35328\ns_mov_b32 s95, 64576\nQmaav 248,160,32\nQta 160,71,512\nQta 162,71,2560\n"
"buffer_load_dwordx4 v61, s[52:55], s95 offen offset:1024 lds\nQmaav 248,164,24\nQta 164,71,33536\nQta 166,71,35584\nQmaav 252,168,32\nQta 168,71,768\nQta 170,71,2816\nQmaav 252,172,24\n"
"Qta 172,71,33792\nQta 174,71,35840\nQcvaa 8,112,32,56\nv_add_u32 v75, 1, v75\nQk 30,12,13\nQmvaa 8,116,36\nds_write_b32 v73, v75\nQmvaa 8,120,40\n"
"Qk 31,14,15\nQmvaa 8,124,44\nQe 0\nQcvaa 12,128,32,56\nQe 1\nQmvaa 12,132,36\nQe 2\nv_smfmac_f32_16x16x64_bf16 a[188:191], a[204:207], v[24:31], v77\n"
"Qk 32,0,1\nQmvaa 12,136,40\ns_waitcnt lgkmcnt(1)\nQmvaa 12,140,44\nQe 3\nQmvav 80,144,36\nQk 33,2,3\nQmvav 80,148,28\n"
"Qe 4\nQmvav 84,152,36\nQe 5\nQmvav 84,156,28\nQe 6\nQmvav 88,160,36\nQe 7\nQmvav 88,164,28\n"
"Qk 34,4,5\nQk 35,6,7\nQmvav 92,168,36\nQe 8\nQmvav 92,172,28\nQe 9\nQcvaa 0,112,48,56\nQe 10\n"
"Qmvaa 0,116,52\nds_read_b32 v66, v74\nQk 36,8,9\nQmvaa 0,120,56\nQe 11\nQmvaa 0,124,60\nQk 37,10,11\nQcvaa 4,128,48,56\n"
"Qe 12\nQmvaa 4,132,52\nQe 13\nQmvaa 4,136,56\ns_waitcnt lgkmcnt(0)\nv_cmp_gt_u32 vcc, v75, v66\nQmvaa 4,140,60\ns_cbranch_vccnz .Lsa31x\n"
".Lra31x:\nQk 38,12,13\nQmvav 112,144,40\ns_add_u32 m0, s91, 49152\ns_mov_b32 s94, 17408\nQmvav 112,148,32\nbuffer_load_dwordx4 v60, s[48:51], s94 offen lds\nQmvav 116,152,40\n"
"Qe 14\nQmvav 116,156,32\nQe 15\nQmvav 120,160,40\nQk 39,14,15\nQmvav 120,164,32\nQe 0\nQmvav 124,168,40\n"
"buffer_load_dwordx4 v60, s[48:51], s94 offen offset:1024 lds\nQmvav 124,172,32\nQe 1\nQcvaa 8,112,64,56\nQe 2\n.Lpg12:\ns_cbranch_execz .Ltr6\nQmvaa 8,116,68\n"
"Qe 3\nQmvaa 8,120,72\nQk 40,0,1\nQk 41,2,3\nQmvaa 8,124,76\nbuffer_load_dwordx4 v60, s[48:51], s94 offen offset:2048 lds\nQcvaa 12,128,64,56\nQe 4\n"
"Qmvaa 12,132,68\nQe 5\nv_smfmac_f32_16x16x64_bf16 a[192:195], a[204:207], v[32:39], v77\nQk 42,4,5\nQmvaa 12,136,72\nQe 6\nQmvaa 12,140,76\nbuffer_load_dwordx4 v60, s[48:51], s94 offen offset:3072 lds\n"
"Qmvav 144,144,44\nQe 7\nQmvav 144,148,36\nQk 43,6,7\nQmvav 148,152,44\nQe 8\nQmvav 148,156,36\ns_add_u32 m0, s91, 53248\n"
"s_mov_b32 s95, 45248\nQmvav 152,160,44\nbuffer_load_dwordx4 v61, s[52:55], s95 offen lds\nQmvav 152,164,36\nQe 9\nQmvav 156,168,44\nQe 10\nQmvav 156,172,36\n"
"Qe 11\nQcvaa 0,112,80,56\ns_mov_b32 s95, 64704\nQk 44,8,9\nQk 45,10,11\nQmvaa 0,116,84\nbuffer_load_dwordx4 v61, s[52:55], s95 offen offset:1024 lds\nQmvaa 0,120,88\n"
"Qe 12\nQmvaa 0,124,92\nQe 13\nQcvaa 4,128,80,56\nQe 14\nQmvaa 4,132,84\ns_add_u32 m0, s91, 73728\ns_mov_b32 s94, 28672\n"
"Qe 15\nQmvaa 4,136,88\nbuffer_load_dwordx4 v60, s[48:51], s94 offen lds\nQmvaa 4,140,92\nQk 46,12,13\nQk 47,14,15\nQmvav 176,144,48\nQmvav 176,148,40\n"
"Qmvav 180,152,48\nQmvav 180,156,40\nbuffer_load_dwordx4 v60, s[48:51], s94 offen offset:1024 lds\nQmvav 184,160,48\nQe 0\nQmvav 184,164,40\nQe 1\nQmvav 188,168,48\n"
"Qmvav 188,172,40\nQe 2\nQcvaa 8,112,96,56\nbuffer_load_dwordx4 v60, s[48:51], s94 offen offset:2048 lds\nQk 48,0,1\nQmvaa 8,116,100\nQmvaa 8,120,104\nQmvaa 8,124,108\n"
"Qcvaa 12,128,96,56\ns_mov_b32 s95, 82944\nQmvaa 12,132,100\nbuffer_load_dwordx4 v61, s[52:55], s95 offen offset:3072 lds\nv_smfmac_f32_16x16x64_bf16 a[196:199], a[204:207], v[40:47], v77\nQmvaa 12,136,104\nQmvaa 12,140,108\nQmvav 208,144,16\n"
"Qta 112,71,34048\ns_add_u32 m0, s91, 77824\ns_mov_b32 s95, 86144\nQmvav 208,148,44\nQta 114,71,36096\nbuffer_load_dwordx4 v62, s[52:55], s95 offen lds\nQe 3\nQmvav 212,152,16\n"
"Qta 116,71,34304\nQk 49,2,3\nQmvav 212,156,44\nQta 118,71,36352\nQe 4\nQmvav 216,160,16\nQta 120,71,34560\nQe 5\n"
"Qmvav 216,164,44\nQta 122,71,36608\ns_mov_b32 s95, 105536\nQe 6\nQmvav 220,168,16\nQta 124,71,34816\nbuffer_load_dwordx4 v61, s[52:55], s95 offen offset:1024 lds\nQe 7\n"
"Qmvav 220,172,44\nQta 126,71,36864\nQk 50,4,5\nQk 51,6,7\nQe 8\nQe 9\nQe 10\nQe 11\n"
"Qk 52,8,9\nQk 53,10,11\nQe 12\nQe 13\nQmvav 240,144,20\nQe 14\nQmvav 240,148,48\nQmvav 244,152,20\n"
"Qmvav 244,156,48\nQk 54,12,13\nQmvav 248,160,20\nQmvav 248,164,48\nQmvav 252,168,20\nQe 15\nQmvav 252,172,48\nQk 55,14,15\n"
"s_nop 1\nv_smfmac_f32_16x16x64_bf16 a[200:203], a[204:207], v[48:55], v77\nQmaav 224,144,24\nv_accvgpr_read_b32 v0, a176\nv_accvgpr_read_b32 v1, a177\nQmaav 224,148,52\nv_accvgpr_read_b32 v6, a178\nv_accvgpr_read_b32 v7, a179\n"
"Qmaav 228,152,24\nv_accvgpr_read_b32 v8, a180\nv_accvgpr_read_b32 v9, a181\nQmaav 228,156,52\nv_accvgpr_read_b32 v14, a182\nv_accvgpr_read_b32 v15, a183\nQmaav 232,160,24\nv_accvgpr_read_b32 v16, a184\n"
"v_accvgpr_read_b32 v17, a185\nQmaav 232,164,52\nv_accvgpr_read_b32 v22, a186\nv_accvgpr_read_b32 v23, a187\nQmaav 236,168,24\nv_accvgpr_read_b32 v2, a190\nv_accvgpr_read_b32 v3, a191\nQmaav 236,172,52\n"
"s_waitcnt lgkmcnt(0)\nv_accvgpr_read_b32 v24, a188\nQmvav 96,112,28\nv_accvgpr_read_b32 v25, a189\nv_accvgpr_read_b32 v4, a192\nQmvav 100,116,28\nv_accvgpr_read_b32 v5, a193\nv_accvgpr_read_b32 v10, a194\n"
"Qmvav 104,120,28\nv_accvgpr_read_b32 v11, a195\nv_accvgpr_read_b32 v12, a196\nQmvav 108,124,28\nv_accvgpr_read_b32 v13, a197\nv_accvgpr_read_b32 v18, a198\nQmvav 128,112,32\nv_accvgpr_read_b32 v19, a199\n"
"v_accvgpr_read_b32 v20, a200\nQmvav 132,116,32\nv_accvgpr_read_b32 v21, a201\nv_accvgpr_read_b32 v26, a202\nQmvav 136,120,32\nv_accvgpr_read_b32 v27, a203\nQa 8,8,9\nQmvav 140,124,32\n"
"Qa 10,10,11\nQa 8,8,10\nQmvav 160,112,36\nQa 16,16,17\nQa 18,18,19\nQmvav 164,116,36\nQa 16,16,18\nQa 4,4,5\n"
"Qmvav 168,120,36\nQa 6,6,7\nQa 4,4,6\nQmvav 172,124,36\nQa 12,12,13\nQa 14,14,15\nQmvav 192,112,40\nQa 12,12,14\n"
"Qa 0,0,1\nQmvav 196,116,40\nQa 2,2,3\nQa 0,0,2\nQmvav 200,120,40\nQa 24,24,25\nQa 26,26,27\nQmvav 204,124,40\n"
"Qa 24,24,26\nQa 20,20,21\nQmvav 224,112,44\nQa 22,22,23\nQa 20,20,22\nQmvav 228,116,44\nv_max3_u32 v9, v8, v16, v4\nv_max3_u32 v9, v9, v12, v0\n"
"Qmvav 232,120,44\nv_max3_u32 v9, v9, v24, v20\nv_min3_u32 v11, v8, v16, v4\nQmvav 236,124,44\nv_min3_u32 v11, v11, v12, v0\nv_min3_u32 v11, v11, v24, v20\nQmaav 208,112,48\nQmaav 212,116,48\n"
"Qmaav 216,120,48\nQmaav 220,124,48\nQmaav 240,112,52\nQmaav 244,116,52\nQmaav 248,120,52\nQmaav 252,124,52\nv_cmp_lt_u32 vcc, 0x44800000, v9\ns_mov_b64 s[74:75], vcc\n"
"v_cmp_gt_u32 vcc, 0x2b800000, v11\ns_or_b64 s[74:75], s[74:75], vcc\ns_cmp_lg_u64 s[74:75], 0\ns_cbranch_scc1 .Lfb\n.Lfbret1:\nv_rcp_f32 v52, v0\nv_rcp_f32 v53, v4\nv_rcp_f32 v54, v8\n"
"v_rcp_f32 v55, v12\nv_rcp_f32 v56, v16\nv_rcp_f32 v57, v20\nv_rcp_f32 v58, v24\ns_mov_b32 s56, %8\ns_and_b32 s57, %9, 0xffff\ns_mov_b32 s58, 1835008\ns_lshl_b32 s68, %19, 4\n"
"s_cmp_eq_u32 s70, 0\ns_cselect_b32 s68, s68, -1\nv_mov_b32 v77, 0x7ffff000\nv_cmp_eq_u32 vcc, s68, v60\ns_nop 1\nv_cndmask_b32 v77, v77, v76, vcc\ns_mul_i32 s68, s92, 6144\ns_add_u32 s68, s68, %10\n"
"v_add_u32 v45, s68, v60\nv_add_u32 v46, 49152, v45\nv_add_u32 v47, 98304, v45\ns_lshl_b32 s68, s93, 8\ns_add_u32 s68, s68, %10\nv_lshrrev_b32 v66, 9, v60\nv_mul_u32_u24 v66, 6144, v66\nv_bfe_u32 v67, v60, 1, 8\n"
"v_add3_u32 v48, v66, v67, s68\nv_add_u32 v49, 49152, v48\nv_add_u32 v50, 98304, v48\ns_mov_b64 exec, 0\ns_cmp_eq_u32 s70, 0\ns_cbranch_scc1 .Lpg16\ns_cmp_eq_u32 s70, 1\ns_cbranch_scc1 .Lpg17\n"
"s_cmp_eq_u32 s70, 2\ns_cbranch_scc1 .Lpg18\ns_branch .Lpg19\n.Ltr8:\ns_mov_b64 exec, -1\ns_waitcnt vmcnt(18)\ns_barrier\nQda 112,47,33792\n"
"Qda 116,47,34816\nQda 120,47,35840\nQtv 0,50,36864\nQtv 2,50,38400\nQtv 4,50,37376\nQtv 6,50,38912\nQtv 8,50,37888\nQtv 10,50,39424\n"
"Qda 140,47,46080\nQda 144,47,47104\nQda 148,47,48128\nQtv 20,50,49152\nQtv 22,50,50688\nQtv 24,50,49664\nQtv 26,50,51200\nQtv 28,50,50176\n"
"Qtv 30,50,51712\nQtv 12,48,4096\nQtv 14,48,5120\ns_add_u32 m0, s91, 98304\ns_mov_b32 s94, 31744\nbuffer_load_dwordx4 v60, s[48:51], s94 offen lds\nQtv 32,48,16384\nbuffer_load_dwordx4 v60, s[48:51], s94 offen offset:1024 lds\n"
"Qtv 34,48,17408\nbuffer_load_dwordx4 v60, s[48:51], s94 offen offset:2048 lds\nv_add_u32 v75, 1, v75\ns_waitcnt vmcnt(15)\nds_write_b32 v73, v75\nv_mul_f32 v80, v52, v80\nv_mul_f32 v81, v52, v81\nv_mul_f32 v82, v52, v82\n"
"v_mul_f32 v83, v52, v83\nv_mul_f32 v84, v52, v84\nv_mul_f32 v85, v52, v85\nv_mul_f32 v86, v52, v86\nv_mul_f32 v87, v52, v87\nQk 80,80,81\nQk 81,82,83\nQk 82,84,85\n"
"Qk 83,86,87\nv_mul_f32 v112, v53, v112\nv_mul_f32 v113, v53, v113\nv_mul_f32 v114, v53, v114\nv_mul_f32 v115, v53, v115\nv_mul_f32 v116, v53, v116\nv_mul_f32 v117, v53, v117\nv_mul_f32 v118, v53, v118\n"
"v_mul_f32 v119, v53, v119\nQk 112,112,113\nQk 113,114,115\nQk 114,116,117\nQk 115,118,119\ns_waitcnt lgkmcnt(7)\nQzava 0,0,112\nbuffer_load_dwordx4 v60, s[48:51], s94 offen offset:3072 lds\n"
"Qmava 0,20,140\nQtv 16,48,4608\ns_add_u32 m0, s91, 102400\ns_mov_b32 s95, 86208\nQzava 20,0,116\nbuffer_load_dwordx4 v61, s[52:55], s95 offen lds\nQmava 20,20,144\nQtv 18,48,5632\n"
"s_mov_b32 s95, 105664\nQzava 40,0,120\nbuffer_load_dwordx4 v61, s[52:55], s95 offen offset:1024 lds\nQmava 40,20,148\nQtv 36,48,16896\ns_and_b32 s66, %19, 3\nQzava 4,4,112\nQtv 38,48,17920\n"
"s_mul_i32 s66, s66, 327680\nQmava 4,24,140\nds_read_b32 v66, v74\ns_add_u32 s66, s66, 1310720\nQzava 24,4,116\nQda 124,45,0\nv_add_u32 v44, s91, v60\nQmava 24,24,144\n"
"Qda 152,45,12288\nv_mul_f32 v144, v54, v144\nQzava 44,4,120\nQda 128,45,1024\nv_mul_f32 v145, v54, v145\nQmava 44,24,148\ns_waitcnt lgkmcnt(3)\nv_cmp_gt_u32 vcc, v75, v66\n"
"Qzava 8,8,112\ns_cbranch_vccnz .Lso0\n.Lro0:\nv_mul_f32 v146, v54, v146\nQmava 8,28,140\nQda 156,45,13312\ns_add_u32 m0, s91, 132096\ns_mov_b32 s94, 43008\n"
"Qzava 28,8,116\nbuffer_load_dwordx4 v60, s[48:51], s94 offen lds\nQmava 28,28,144\nQda 132,45,2048\nv_mul_f32 v147, v54, v147\nQzava 48,8,120\nQda 160,45,14336\nv_mul_f32 v148, v54, v148\n"
"Qmava 48,28,148\nQda 136,45,3072\nv_mul_f32 v149, v54, v149\ns_waitcnt lgkmcnt(8)\nQzava 12,12,112\nbuffer_load_dwordx4 v60, s[48:51], s94 offen offset:1024 lds\nQmava 12,32,140\nQda 164,45,15360\n"
"v_mul_f32 v150, v54, v150\nQzava 32,12,116\nv_mul_f32 v151, v54, v151\nQk 144,144,145\nQmava 32,32,144\nQk 145,146,147\nQk 146,148,149\nQzava 52,12,120\n"
"Qk 147,150,151\nv_mul_f32 v176, v55, v176\nQmava 52,32,148\nbuffer_load_dwordx4 v60, s[48:51], s94 offen offset:2048 lds\nQzava 16,16,112\nQda 112,45,24576\nv_mul_f32 v177, v55, v177\nQmava 16,36,140\n"
"Qda 140,45,36864\nv_mul_f32 v178, v55, v178\nQzava 36,16,116\nQda 116,45,25600\nv_mul_f32 v179, v55, v179\nQmava 36,36,144\nQda 144,45,37888\ns_mov_b32 s95, 123904\n"
"Qzava 56,16,120\nbuffer_load_dwordx4 v61, s[52:55], s95 offen offset:3072 lds\nv_add_u32 v75, 1, v75\nQmava 56,36,148\ns_waitcnt vmcnt(16)\nds_write_b32 v73, v75\ns_waitcnt lgkmcnt(5)\nQzava 60,0,124\n"
"Qda 120,45,26624\ns_add_u32 s88, s71, 0\nQmava 60,20,152\nbuffer_store_dwordx4 v[80:83], v60, s[56:59], s88 offen offset:0 sc1\ns_add_u32 m0, s91, 136192\ns_mov_b32 s95, 127104\nQzava 80,0,128\nbuffer_load_dwordx4 v62, s[52:55], s95 offen lds\n"
"Qmava 80,20,156\nQda 148,45,38912\nv_mul_f32 v180, v55, v180\nQzava 100,0,132\nv_mul_f32 v181, v55, v181\nv_mul_f32 v182, v55, v182\nQmava 100,20,160\nv_mul_f32 v183, v55, v183\n"
"Qk 176,176,177\nQzava 184,0,136\nQtv 0,48,27648\nQk 177,178,179\nQmava 184,20,164\nds_read_b32 v66, v74\ns_mov_b32 s95, 146496\nQzava 64,4,124\n"
"buffer_load_dwordx4 v61, s[52:55], s95 offen offset:1024 lds\nQmava 64,24,152\nQtv 2,48,29184\nQk 178,180,181\nQzava 84,4,128\nQtv 20,48,39936\nQk 179,182,183\nQmava 84,24,156\n"
"s_waitcnt lgkmcnt(2)\nv_cmp_gt_u32 vcc, v75, v66\nQzava 104,4,132\ns_cbranch_vccnz .Lso1\n.Lro1:\nv_mul_f32 v208, v56, v208\nQmava 104,24,160\nQtv 22,48,41472\n"
"s_add_u32 m0, s91, 0\ns_mov_b32 s94, 46080\nQzava 188,4,136\nbuffer_load_dwordx4 v60, s[48:51], s94 offen lds\nQmava 188,24,164\nQtv 4,48,28160\nv_mul_f32 v209, v56, v209\nQzava 68,8,124\n"
"Qtv 6,48,29696\nv_mul_f32 v210, v56, v210\nQmava 68,28,152\nQtv 24,48,40448\nv_mul_f32 v211, v56, v211\nQzava 88,8,128\nQtv 26,48,41984\nv_mul_f32 v212, v56, v212\n"
"Qmava 88,28,156\nv_mul_f32 v213, v56, v213\nv_mul_f32 v214, v56, v214\nQzava 108,8,132\nbuffer_load_dwordx4 v60, s[48:51], s94 offen offset:1024 lds\nQmava 108,28,160\nv_mul_f32 v215, v56, v215\nQk 208,208,209\n"
"Qzava 192,8,136\nQtv 8,48,28672\nQk 209,210,211\nQmava 192,28,164\nQtv 10,48,30208\nQk 210,212,213\nQzava 72,12,124\nQtv 28,48,40960\n"
"Qk 211,214,215\nQmava 72,32,152\nQtv 30,48,42496\nv_mul_f32 v240, v57, v240\nQzava 92,12,128\nbuffer_load_dwordx4 v60, s[48:51], s94 offen offset:2048 lds\nQmava 92,32,156\nv_mul_f32 v241, v57, v241\n"
"v_mul_f32 v242, v57, v242\nQzava 176,12,132\nv_mul_f32 v243, v57, v243\nv_mul_f32 v244, v57, v244\nQmava 176,32,160\nv_mul_f32 v245, v57, v245\nv_mul_f32 v246, v57, v246\nQzava 196,12,136\n"
"Qtv 12,49,4096\nv_mul_f32 v247, v57, v247\nQmava 196,32,164\nQtv 14,49,5120\nQk 240,240,241\nQzava 76,16,124\nQtv 32,49,16384\nQk 241,242,243\n"
"Qmava 76,36,152\nbuffer_load_dwordx4 v60, s[48:51], s94 offen offset:3072 lds\nQzava 96,16,128\nQtv 34,49,17408\nQk 242,244,245\nQmava 96,36,156\nQk 243,246,247\nv_accvgpr_read_b32 v84, a224\n"
"Qzava 180,16,132\nv_accvgpr_read_b32 v85, a225\nv_accvgpr_read_b32 v86, a226\nQmava 180,36,160\nv_accvgpr_read_b32 v87, a227\nv_accvgpr_read_b32 v40, a228\nQzava 200,16,136\nQtv 16,49,4608\n"
"v_add_u32 v75, 1, v75\nQmava 200,36,164\ns_waitcnt vmcnt(17)\nds_write_b32 v73, v75\ns_add_u32 m0, s91, 4096\ns_mov_b32 s95, 127168\ns_waitcnt lgkmcnt(10)\nQmava 0,0,112\n"
"buffer_load_dwordx4 v61, s[52:55], s95 offen lds\nQmava 0,20,140\nbuffer_store_dwordx4 v[112:115], v60, s[56:59], s88 offen offset:1024 sc1\nQmava 20,0,116\nQtv 18,49,5632\nv_accvgpr_read_b32 v41, a229\nQmava 20,20,144\nQtv 36,49,16896\n"
"v_accvgpr_read_b32 v42, a230\nQmava 40,0,120\nQtv 38,49,17920\ns_mov_b32 s95, 146624\nQmava 40,20,148\nbuffer_load_dwordx4 v61, s[52:55], s95 offen offset:1024 lds\nQmava 4,4,112\nQda 124,46,0\n"
"v_accvgpr_read_b32 v43, a231\nQmava 4,24,140\nds_read_b32 v66, v74\nv_mul_f32 v84, v58, v84\nQmava 24,4,116\nQda 152,46,12288\nv_mul_f32 v85, v58, v85\nQmava 24,24,144\n"
"Qda 128,46,1024\nv_mul_f32 v86, v58, v86\nQmava 44,4,120\nQda 156,46,13312\nv_mul_f32 v87, v58, v87\nQmava 44,24,148\ns_waitcnt lgkmcnt(3)\nv_cmp_gt_u32 vcc, v75, v66\n"
"Qmava 8,8,112\ns_cbranch_vccnz .Lso2\n.Lro2:\nv_mul_f32 v40, v58, v40\nQmava 8,28,140\nQda 132,46,2048\ns_add_u32 m0, s91, 24576\ns_add_u32 s95, s66, 1024\n"
"Qmava 28,8,116\nbuffer_load_dwordx4 v61, s[52:55], s95 offen offset:3072 lds\nQmava 28,28,144\nQda 160,46,14336\nv_mul_f32 v41, v58, v41\nQmava 48,8,120\nQda 136,46,3072\nv_mul_f32 v42, v58, v42\n"
"Qmava 48,28,148\nQda 164,46,15360\nv_mul_f32 v43, v58, v43\ns_waitcnt lgkmcnt(9)\nQmava 12,12,112\nQk 84,84,85\nQk 85,86,87\nQmava 12,32,140\n"
"Qk 86,40,41\nQk 87,42,43\nQmava 32,12,116\nv_mul_f32 v88, v52, v88\nv_mul_f32 v89, v52, v89\nQmava 32,32,144\nv_mul_f32 v90, v52, v90\nv_mul_f32 v91, v52, v91\n"
"Qmava 52,12,120\n.Lpg13:\ns_cbranch_execz .Ltr6\ns_add_u32 m0, s91, 28672\ns_add_u32 s95, s66, 4224\nv_mul_f32 v92, v52, v92\nQmava 52,32,148\nbuffer_load_dwordx4 v62, s[52:55], s95 offen lds\n"
"Qmava 16,16,112\nQda 112,46,24576\nv_mul_f32 v93, v52, v93\nQmava 16,36,140\nQda 140,46,36864\nv_mul_f32 v94, v52, v94\nQmava 36,16,116\nQda 116,46,25600\n"
"v_mul_f32 v95, v52, v95\nQmava 36,36,144\nQda 144,46,37888\nQk 88,88,89\nQmava 56,16,120\nQda 120,46,26624\nv_add_u32 v75, 1, v75\nQmava 56,36,148\n"
"s_waitcnt vmcnt(16)\nds_write_b32 v73, v75\ns_waitcnt lgkmcnt(6)\nQmava 60,0,124\nQda 148,46,38912\nQk 89,90,91\nQmava 60,20,152\nbuffer_store_dwordx4 v[144:147], v60, s[56:59], s88 offen offset:2048 sc1\n"
"s_add_u32 s95, s66, 23616\nQmava 80,0,128\nbuffer_load_dwordx4 v61, s[52:55], s95 offen offset:1024 lds\nQmava 80,20,156\nQk 90,92,93\nQk 91,94,95\nQmava 100,0,132\nv_mul_f32 v120, v53, v120\n"
"v_mul_f32 v121, v53, v121\nQmava 100,20,160\nv_mul_f32 v122, v53, v122\nv_mul_f32 v123, v53, v123\nQmava 184,0,136\nQtv 0,49,27648\nv_mul_f32 v124, v53, v124\nQmava 184,20,164\n"
"ds_read_b32 v66, v74\nv_mul_f32 v125, v53, v125\nQmava 64,4,124\nQtv 2,49,29184\nv_mul_f32 v126, v53, v126\nQmava 64,24,152\nQtv 20,49,39936\nv_mul_f32 v127, v53, v127\n"
"Qmava 84,4,128\nQtv 22,49,41472\nQk 120,120,121\nQmava 84,24,156\ns_waitcnt lgkmcnt(3)\nv_cmp_gt_u32 vcc, v75, v66\nQmava 104,4,132\ns_cbranch_vccnz .Lso3\n"
".Lro3:\nQk 121,122,123\nQmava 104,24,160\ns_add_u32 m0, s91, 53248\ns_add_u32 s95, s66, 4288\nQk 122,124,125\nQmava 188,4,136\nbuffer_load_dwordx4 v61, s[52:55], s95 offen lds\n"
"Qmava 188,24,164\nds_write_b128 v44, v[80:83] offset:24576\nQmava 68,8,124\nQtv 4,49,28160\nQk 123,126,127\nQmava 68,28,152\nQtv 6,49,29696\nv_mul_f32 v152, v54, v152\n"
"Qmava 88,8,128\nQtv 24,49,40448\nv_mul_f32 v153, v54, v153\nQmava 88,28,156\nQtv 26,49,41984\nv_mul_f32 v154, v54, v154\nQmava 108,8,132\nv_mul_f32 v155, v54, v155\n"
"v_mul_f32 v156, v54, v156\nQmava 108,28,160\nv_mul_f32 v157, v54, v157\nv_mul_f32 v158, v54, v158\nQmava 192,8,136\nQtv 8,49,28672\nv_mul_f32 v159, v54, v159\nQmava 192,28,164\n"
"Qtv 10,49,30208\nQk 152,152,153\nQmava 72,12,124\nQtv 28,49,40960\nQk 153,154,155\nQmava 72,32,152\nQtv 30,49,42496\nQk 154,156,157\n"
"Qmava 92,12,128\nds_write_b128 v44, v[112:115] offset:25600\nQmava 92,32,156\nQk 155,158,159\nv_mul_f32 v184, v55, v184\nQmava 176,12,132\nv_mul_f32 v185, v55, v185\nv_mul_f32 v186, v55, v186\n"
"Qmava 176,32,160\nv_mul_f32 v187, v55, v187\nv_mul_f32 v188, v55, v188\nQmava 196,12,136\nQtv 12,50,4096\nv_mul_f32 v189, v55, v189\nQmava 196,32,164\nQtv 14,50,5120\n"
"v_mul_f32 v190, v55, v190\nQmava 76,16,124\nQtv 32,50,16384\ns_add_u32 s95, s66, 23744\nQmava 76,36,152\nbuffer_load_dwordx4 v61, s[52:55], s95 offen offset:1024 lds\nQmava 96,16,128\nQtv 34,50,17408\n"
"v_mul_f32 v191, v55, v191\nQmava 96,36,156\nQk 184,184,185\nQk 185,186,187\nQmava 180,16,132\nQk 186,188,189\nQk 187,190,191\nQmava 180,36,160\n"
"v_mul_f32 v216, v56, v216\nv_mul_f32 v217, v56, v217\nQmava 200,16,136\nds_write_b128 v44, v[144:147] offset:26624\nv_add_u32 v75, 1, v75\nQmava 200,36,164\ns_waitcnt vmcnt(13)\nds_write_b32 v73, v75\n"
"s_waitcnt lgkmcnt(11)\nQmava 0,0,112\nQtv 16,50,4608\nv_mul_f32 v218, v56, v218\nQmava 0,20,140\nbuffer_store_dwordx4 v[176:179], v60, s[56:59], s88 offen offset:3072 sc1\nQmava 20,0,116\nQtv 18,50,5632\n"
"v_mul_f32 v219, v56, v219\nQmava 20,20,144\nQtv 36,50,16896\nv_mul_f32 v220, v56, v220\nQmava 40,0,120\nQtv 38,50,17920\nv_mul_f32 v221, v56, v221\nQmava 40,20,148\n"
"Qda 124,47,0\nv_mul_f32 v222, v56, v222\nQmava 4,4,112\nQda 152,47,12288\nv_mul_f32 v223, v56, v223\nQmava 4,24,140\nds_read_b32 v66, v74\nQk 216,216,217\n"
"Qmava 24,4,116\nQda 128,47,1024\nQk 217,218,219\nQmava 24,24,144\nQda 156,47,13312\nQk 218,220,221\nQmava 44,4,120\nds_write_b128 v44, v[176:179] offset:49152\n"
"Qmava 44,24,148\ns_waitcnt lgkmcnt(3)\nv_cmp_gt_u32 vcc, v75, v66\nQmava 8,8,112\ns_cbranch_vccnz .Lso4\n.Lro4:\nQk 219,222,223\nQmava 8,28,140\n"
"Qda 132,47,2048\ns_add_u32 m0, s91, 73728\ns_mov_b32 s94, 114688\nQmava 28,8,116\nbuffer_load_dwordx4 v60, s[48:51], s94 offen lds\nQmava 28,28,144\nQda 160,47,14336\nv_mul_f32 v248, v57, v248\n"
"Qmava 48,8,120\nQda 136,47,3072\nv_mul_f32 v249, v57, v249\nQmava 48,28,148\nQda 164,47,15360\nv_mul_f32 v250, v57, v250\ns_waitcnt lgkmcnt(10)\nQmava 12,12,112\n"
"buffer_load_dwordx4 v60, s[48:51], s94 offen offset:1024 lds\nQmava 12,32,140\nv_mul_f32 v251, v57, v251\nv_mul_f32 v252, v57, v252\nQmava 32,12,116\nv_mul_f32 v253, v57, v253\nv_mul_f32 v254, v57, v254\nQmava 32,32,144\n"
"v_mul_f32 v255, v57, v255\nQk 248,248,249\nQmava 52,12,120\nds_write_b128 v44, v[208:211] offset:50176\nQmava 52,32,148\nbuffer_load_dwordx4 v60, s[48:51], s94 offen offset:2048 lds\nQmava 16,16,112\nQda 112,47,33792\n"
"Qk 249,250,251\nQmava 16,36,140\nQda 140,47,46080\nQk 250,252,253\nQmava 36,16,116\nQda 116,47,34816\nQk 251,254,255\nQmava 36,36,144\n"
"Qda 144,47,47104\ns_mov_b32 s95, 328704\nQmava 56,16,120\nbuffer_load_dwordx4 v61, s[52:55], s95 offen offset:3072 lds\nv_add_u32 v75, 1, v75\nQmava 56,36,148\ns_waitcnt vmcnt(11)\nds_write_b32 v73, v75\n"
"s_waitcnt lgkmcnt(6)\nQmava 60,0,124\nQda 120,47,35840\ns_add_u32 s88, s71, 4096\nQmava 60,20,152\nbuffer_store_dwordx4 v[208:211], v60, s[56:59], s88 offen offset:0 sc1\ns_add_u32 m0, s91, 77824\ns_mov_b32 s95, 331904\n"
"Qmava 80,0,128\nbuffer_load_dwordx4 v62, s[52:55], s95 offen lds\nQmava 80,20,156\nQda 148,47,48128\nv_accvgpr_read_b32 v116, a232\nQmava 100,0,132\nds_write_b128 v44, v[240:243] offset:51200\nQmava 100,20,160\n"
"v_accvgpr_read_b32 v117, a233\nv_accvgpr_read_b32 v118, a234\nQmava 184,0,136\nQtv 0,50,36864\nv_accvgpr_read_b32 v119, a235\nQmava 184,20,164\nds_read_b32 v66, v74\ns_mov_b32 s95, 351296\n"
"Qmava 64,4,124\nbuffer_load_dwordx4 v61, s[52:55], s95 offen offset:1024 lds\nQmava 64,24,152\nQtv 2,50,38400\nv_accvgpr_read_b32 v40, a236\nQmava 84,4,128\nQtv 20,50,49152\nv_accvgpr_read_b32 v41, a237\n"
"Qmava 84,24,156\ns_waitcnt lgkmcnt(2)\nv_cmp_gt_u32 vcc, v75, v66\nQmava 104,4,132\ns_cbranch_vccnz .Lso5\n.Lro5:\nv_accvgpr_read_b32 v42, a238\nQmava 104,24,160\n"
"Qtv 22,50,50688\ns_add_u32 m0, s91, 98304\ns_mov_b32 s94, 117760\nQmava 188,4,136\nbuffer_load_dwordx4 v60, s[48:51], s94 offen lds\nQmava 188,24,164\nQtv 4,50,37376\nv_accvgpr_read_b32 v43, a239\n"
"Qmava 68,8,124\nds_write_b128 v44, v[84:87] offset:52224\nQmava 68,28,152\nQtv 6,50,38912\nv_mul_f32 v116, v58, v116\nQmava 88,8,128\nQtv 24,50,49664\nv_mul_f32 v117, v58, v117\n"
"Qmava 88,28,156\nQtv 26,50,51200\nv_mul_f32 v118, v58, v118\nQmava 108,8,132\nbuffer_load_dwordx4 v60, s[48:51], s94 offen offset:1024 lds\nQmava 108,28,160\nv_mul_f32 v119, v58, v119\nv_mul_f32 v40, v58, v40\n"
"Qmava 192,8,136\nQtv 8,50,37888\nv_mul_f32 v41, v58, v41\nQmava 192,28,164\nQtv 10,50,39424\nv_mul_f32 v42, v58, v42\nQmava 72,12,124\nQtv 28,50,50176\n"
"v_mul_f32 v43, v58, v43\nQmava 72,32,152\nQtv 30,50,51712\nQk 116,116,117\nQmava 92,12,128\nbuffer_load_dwordx4 v60, s[48:51], s94 offen offset:2048 lds\nQmava 92,32,156\nQk 117,118,119\n"
"Qk 118,40,41\nQmava 176,12,132\nQk 119,42,43\nv_mul_f32 v96, v52, v96\nQmava 176,32,160\nv_mul_f32 v97, v52, v97\nv_mul_f32 v98, v52, v98\nQmava 196,12,136\n"
"Qtv 12,48,4096\nv_mul_f32 v99, v52, v99\nQmava 196,32,164\nQtv 14,48,5120\nv_mul_f32 v100, v52, v100\nQmava 76,16,124\nQtv 32,48,16384\nv_mul_f32 v101, v52, v101\n"
"Qmava 76,36,152\nbuffer_load_dwordx4 v60, s[48:51], s94 offen offset:3072 lds\nQmava 96,16,128\nQtv 34,48,17408\nv_mul_f32 v102, v52, v102\nQmava 96,36,156\nv_mul_f32 v103, v52, v103\nQk 96,96,97\n"
"Qmava 180,16,132\nQk 97,98,99\nQk 98,100,101\nQmava 180,36,160\nQk 99,102,103\nv_mul_f32 v128, v53, v128\nQmava 200,16,136\nQtv 16,48,4608\n"
"v_add_u32 v75, 1, v75\nQmava 200,36,164\ns_waitcnt vmcnt(14)\nds_write_b32 v73, v75\ns_add_u32 m0, s91, 102400\ns_mov_b32 s95, 331968\ns_waitcnt lgkmcnt(10)\nQmava 0,0,112\n"
"buffer_load_dwordx4 v61, s[52:55], s95 offen lds\nQmava 0,20,140\nbuffer_store_dwordx4 v[240:243], v60, s[56:59], s88 offen offset:1024 sc1\nQmava 20,0,116\nQtv 18,48,5632\nv_mul_f32 v129, v53, v129\nQmava 20,20,144\nQtv 36,48,16896\n"
"v_mul_f32 v130, v53, v130\nQmava 40,0,120\nQtv 38,48,17920\ns_mov_b32 s95, 351424\nQmava 40,20,148\nbuffer_load_dwordx4 v61, s[52:55], s95 offen offset:1024 lds\nQmava 4,4,112\nQda 124,45,0\n"
"v_mul_f32 v131, v53, v131\nQmava 4,24,140\nds_read_b32 v66, v74\nv_mul_f32 v132, v53, v132\nQmava 24,4,116\nQda 152,45,12288\nv_mul_f32 v133, v53, v133\nQmava 24,24,144\n"
"Qda 128,45,1024\nv_mul_f32 v134, v53, v134\nQmava 44,4,120\nQda 156,45,13312\nv_mul_f32 v135, v53, v135\nQmava 44,24,148\ns_waitcnt lgkmcnt(3)\nv_cmp_gt_u32 vcc, v75, v66\n"
"Qmava 8,8,112\ns_cbranch_vccnz .Lso6\n.Lro6:\nQk 128,128,129\nQmava 8,28,140\nQda 132,45,2048\ns_add_u32 m0, s91, 132096\ns_mov_b32 s94, 129024\n"
"Qmava 28,8,116\nbuffer_load_dwordx4 v60, s[48:51], s94 offen lds\nQmava 28,28,144\nQda 160,45,14336\nQk 129,130,131\nQmava 48,8,120\nQda 136,45,3072\nQk 130,132,133\n"
"Qmava 48,28,148\nQda 164,45,15360\nQk 131,134,135\ns_waitcnt lgkmcnt(9)\nQmava 12,12,112\nbuffer_load_dwordx4 v60, s[48:51], s94 offen offset:1024 lds\nQmava 12,32,140\nv_mul_f32 v160, v54, v160\n"
"v_mul_f32 v161, v54, v161\nQmava 32,12,116\nv_mul_f32 v162, v54, v162\nv_mul_f32 v163, v54, v163\nQmava 32,32,144\nv_mul_f32 v164, v54, v164\nv_mul_f32 v165, v54, v165\nQmava 52,12,120\n"
"v_mul_f32 v166, v54, v166\nv_mul_f32 v167, v54, v167\nQmava 52,32,148\nbuffer_load_dwordx4 v60, s[48:51], s94 offen offset:2048 lds\nQmava 16,16,112\nQda 112,45,24576\nQk 160,160,161\nQmava 16,36,140\n"
"Qda 140,45,36864\nQk 161,162,163\nQmava 36,16,116\nQda 116,45,25600\nQk 162,164,165\nQmava 36,36,144\nQda 144,45,37888\ns_mov_b32 s95, 369664\n"
"Qmava 56,16,120\nbuffer_load_dwordx4 v61, s[52:55], s95 offen offset:3072 lds\nv_add_u32 v75, 1, v75\nQmava 56,36,148\ns_waitcnt vmcnt(19)\nds_write_b32 v73, v75\ns_waitcnt lgkmcnt(5)\nQmava 60,0,124\n"
"Qda 120,45,26624\nQk 163,166,167\nQmava 60,20,152\nbuffer_store_dwordx4 v[84:87], v60, s[56:59], s88 offen offset:2048 sc1\ns_add_u32 m0, s91, 136192\ns_mov_b32 s95, 372864\nQmava 80,0,128\nbuffer_load_dwordx4 v62, s[52:55], s95 offen lds\n"
"Qmava 80,20,156\nQda 148,45,38912\nv_mul_f32 v192, v55, v192\nQmava 100,0,132\nv_mul_f32 v193, v55, v193\nv_mul_f32 v194, v55, v194\nQmava 100,20,160\nv_mul_f32 v195, v55, v195\n"
"v_mul_f32 v196, v55, v196\nQmava 184,0,136\nQtv 0,48,27648\nv_mul_f32 v197, v55, v197\nQmava 184,20,164\nds_read_b32 v66, v74\ns_mov_b32 s95, 392256\nQmava 64,4,124\n"
"buffer_load_dwordx4 v61, s[52:55], s95 offen offset:1024 lds\nQmava 64,24,152\nQtv 2,48,29184\nv_mul_f32 v198, v55, v198\nQmava 84,4,128\nQtv 20,48,39936\nv_mul_f32 v199, v55, v199\nQmava 84,24,156\n"
"s_waitcnt lgkmcnt(2)\nv_cmp_gt_u32 vcc, v75, v66\nQmava 104,4,132\ns_cbranch_vccnz .Lso7\n.Lro7:\nQk 192,192,193\nQmava 104,24,160\nQtv 22,48,41472\n"
"s_add_u32 m0, s91, 0\ns_mov_b32 s94, 132096\nQmava 188,4,136\nbuffer_load_dwordx4 v60, s[48:51], s94 offen lds\nQmava 188,24,164\nQtv 4,48,28160\nQk 193,194,195\nQmava 68,8,124\n"
"Qtv 6,48,29696\nQk 194,196,197\nQmava 68,28,152\nQtv 24,48,40448\nQk 195,198,199\nQmava 88,8,128\nQtv 26,48,41984\nv_mul_f32 v224, v56, v224\n"
"Qmava 88,28,156\nv_mul_f32 v225, v56, v225\nv_mul_f32 v226, v56, v226\nQmava 108,8,132\nbuffer_load_dwordx4 v60, s[48:51], s94 offen offset:1024 lds\nQmava 108,28,160\nv_mul_f32 v227, v56, v227\nv_mul_f32 v228, v56, v228\n"
"Qmava 192,8,136\nQtv 8,48,28672\nv_mul_f32 v229, v56, v229\nQmava 192,28,164\nQtv 10,48,30208\nv_mul_f32 v230, v56, v230\nQmava 72,12,124\nQtv 28,48,40960\n"
"v_mul_f32 v231, v56, v231\nQmava 72,32,152\nQtv 30,48,42496\nQk 224,224,225\nQmava 92,12,128\nbuffer_load_dwordx4 v60, s[48:51], s94 offen offset:2048 lds\nQmava 92,32,156\nQk 225,226,227\n"
"Qk 226,228,229\nQmava 176,12,132\nQk 227,230,231\nv_accvgpr_read_b32 v148, a208\nQmava 176,32,160\nv_accvgpr_read_b32 v149, a209\nv_accvgpr_read_b32 v150, a210\nQmava 196,12,136\n"
"Qtv 12,49,4096\nv_accvgpr_read_b32 v151, a211\nQmava 196,32,164\nQtv 14,49,5120\nv_accvgpr_read_b32 v40, a212\nQmava 76,16,124\nQtv 32,49,16384\nv_accvgpr_read_b32 v41, a213\n"
"Qmava 76,36,152\nbuffer_load_dwordx4 v60, s[48:51], s94 offen offset:3072 lds\nQmava 96,16,128\nQtv 34,49,17408\nv_accvgpr_read_b32 v42, a214\nQmava 96,36,156\nv_accvgpr_read_b32 v43, a215\nv_mul_f32 v148, v57, v148\n"
"Qmava 180,16,132\nv_mul_f32 v149, v57, v149\nv_mul_f32 v150, v57, v150\nQmava 180,36,160\nv_mul_f32 v151, v57, v151\nv_mul_f32 v40, v57, v40\nQmava 200,16,136\nQtv 16,49,4608\n"
"v_add_u32 v75, 1, v75\nQmava 200,36,164\ns_waitcnt vmcnt(18)\nds_write_b32 v73, v75\ns_add_u32 m0, s91, 4096\ns_mov_b32 s95, 372928\ns_waitcnt lgkmcnt(10)\nQmava 0,0,112\n"
"buffer_load_dwordx4 v61, s[52:55], s95 offen lds\ns_add_u32 s88, s71, 14336\nQmava 0,20,140\nbuffer_store_dwordx4 v[88:91], v60, s[56:59], s88 offen offset:0 sc1\nQmava 20,0,116\nQtv 18,49,5632\nv_mul_f32 v41, v57, v41\nQmava 20,20,144\n"
"Qtv 36,49,16896\nv_mul_f32 v42, v57, v42\nQmava 40,0,120\nQtv 38,49,17920\ns_mov_b32 s95, 392384\nQmava 40,20,148\nbuffer_load_dwordx4 v61, s[52:55], s95 offen offset:1024 lds\nQmava 4,4,112\n"
"Qda 124,46,0\nv_mul_f32 v43, v57, v43\nQmava 4,24,140\nds_read_b32 v66, v74\nQk 148,148,149\nQmava 24,4,116\nQda 152,46,12288\nQk 149,150,151\n"
"Qmava 24,24,144\nQda 128,46,1024\nQk 150,40,41\n.Lpg14:\ns_cbranch_execz .Ltr6\nQmava 44,4,120\nQda 156,46,13312\nQk 151,42,43\n"
"Qmava 44,24,148\ns_waitcnt lgkmcnt(3)\nv_cmp_gt_u32 vcc, v75, v66\nQmava 8,8,112\ns_cbranch_vccnz .Lso8\n.Lro8:\nv_accvgpr_read_b32 v180, a240\nQmava 8,28,140\n"
"Qda 132,46,2048\ns_add_u32 m0, s91, 24576\ns_add_u32 s95, s66, 41984\nQmava 28,8,116\nbuffer_load_dwordx4 v61, s[52:55], s95 offen offset:3072 lds\nQmava 28,28,144\nQda 160,46,14336\nv_accvgpr_read_b32 v181, a241\n"
"Qmava 48,8,120\nQda 136,46,3072\nv_accvgpr_read_b32 v182, a242\nQmava 48,28,148\nQda 164,46,15360\nv_accvgpr_read_b32 v183, a243\ns_waitcnt lgkmcnt(9)\nQmava 12,12,112\n"
"v_accvgpr_read_b32 v40, a244\nv_accvgpr_read_b32 v41, a245\nQmava 12,32,140\nv_accvgpr_read_b32 v42, a246\nv_accvgpr_read_b32 v43, a247\nQmava 32,12,116\nv_mul_f32 v180, v58, v180\nv_mul_f32 v181, v58, v181\n"
"Qmava 32,32,144\nv_mul_f32 v182, v58, v182\nv_mul_f32 v183, v58, v183\nQmava 52,12,120\ns_add_u32 m0, s91, 28672\ns_add_u32 s95, s66, 45184\nv_mul_f32 v40, v58, v40\nQmava 52,32,148\n"
"buffer_load_dwordx4 v62, s[52:55], s95 offen lds\nQmava 16,16,112\nQda 112,46,24576\nv_mul_f32 v41, v58, v41\nQmava 16,36,140\nQda 140,46,36864\nv_mul_f32 v42, v58, v42\nQmava 36,16,116\n"
"Qda 116,46,25600\nv_mul_f32 v43, v58, v43\nQmava 36,36,144\nQda 144,46,37888\nQk 180,180,181\nQmava 56,16,120\nQda 120,46,26624\nv_add_u32 v75, 1, v75\n"
"Qmava 56,36,148\ns_waitcnt vmcnt(16)\nds_write_b32 v73, v75\ns_waitcnt lgkmcnt(6)\nQmava 60,0,124\nQda 148,46,38912\nQk 181,182,183\nQmava 60,20,152\n"
"buffer_store_dwordx4 v[120:123], v60, s[56:59], s88 offen offset:1024 sc1\ns_add_u32 s95, s66, 64576\nQmava 80,0,128\nbuffer_load_dwordx4 v61, s[52:55], s95 offen offset:1024 lds\nQmava 80,20,156\nQk 182,40,41\nQk 183,42,43\nQmava 100,0,132\n"
"v_mul_f32 v104, v52, v104\nv_mul_f32 v105, v52, v105\nQmava 100,20,160\nv_mul_f32 v106, v52, v106\nv_mul_f32 v107, v52, v107\nQmava 184,0,136\nQtv 0,49,27648\nv_mul_f32 v108, v52, v108\n"
"Qmava 184,20,164\nds_read_b32 v66, v74\nv_mul_f32 v109, v52, v109\nQmava 64,4,124\nQtv 2,49,29184\nv_mul_f32 v110, v52, v110\nQmava 64,24,152\nQtv 20,49,39936\n"
"v_mul_f32 v111, v52, v111\nQmava 84,4,128\nQtv 22,49,41472\nQk 104,104,105\nQmava 84,24,156\ns_waitcnt lgkmcnt(3)\nv_cmp_gt_u32 vcc, v75, v66\nQmava 104,4,132\n"
"s_cbranch_vccnz .Lso9\n.Lro9:\nQk 105,106,107\nQmava 104,24,160\ns_add_u32 m0, s91, 53248\ns_add_u32 s95, s66, 45248\nQk 106,108,109\nQmava 188,4,136\n"
"buffer_load_dwordx4 v61, s[52:55], s95 offen lds\nQmava 188,24,164\nds_write_b128 v44, v[88:91] offset:24576\nQmava 68,8,124\nQtv 4,49,28160\nQk 107,110,111\nQmava 68,28,152\nQtv 6,49,29696\n"
"v_mul_f32 v136, v53, v136\nQmava 88,8,128\nQtv 24,49,40448\nv_mul_f32 v137, v53, v137\nQmava 88,28,156\nQtv 26,49,41984\nv_mul_f32 v138, v53, v138\nQmava 108,8,132\n"
"v_mul_f32 v139, v53, v139\nv_mul_f32 v140, v53, v140\nQmava 108,28,160\nv_mul_f32 v141, v53, v141\nv_mul_f32 v142, v53, v142\nQmava 192,8,136\nQtv 8,49,28672\nv_mul_f32 v143, v53, v143\n"
"Qmava 192,28,164\nQtv 10,49,30208\nQk 136,136,137\nQmava 72,12,124\nQtv 28,49,40960\nQk 137,138,139\nQmava 72,32,152\nQtv 30,49,42496\n"
"Qk 138,140,141\nQmava 92,12,128\nds_write_b128 v44, v[120:123] offset:25600\nQmava 92,32,156\nQk 139,142,143\nv_mul_f32 v168, v54, v168\nQmava 176,12,132\nv_mul_f32 v169, v54, v169\n"
"v_mul_f32 v170, v54, v170\nQmava 176,32,160\nv_mul_f32 v171, v54, v171\nv_mul_f32 v172, v54, v172\nQmava 196,12,136\nQtv 12,50,4096\nv_mul_f32 v173, v54, v173\nQmava 196,32,164\n"
"Qtv 14,50,5120\nv_mul_f32 v174, v54, v174\nQmava 76,16,124\nQtv 32,50,16384\ns_add_u32 s95, s66, 64704\nQmava 76,36,152\nbuffer_load_dwordx4 v61, s[52:55], s95 offen offset:1024 lds\nQmava 96,16,128\n"
"Qtv 34,50,17408\nv_mul_f32 v175, v54, v175\nQmava 96,36,156\nQk 168,168,169\nQk 169,170,171\nQmava 180,16,132\nQk 170,172,173\nQk 171,174,175\n"
"Qmava 180,36,160\nv_mul_f32 v200, v55, v200\nv_mul_f32 v201, v55, v201\nQmava 200,16,136\nds_write_b128 v44, v[152:155] offset:26624\nv_add_u32 v75, 1, v75\nQmava 200,36,164\ns_waitcnt vmcnt(13)\n"
"ds_write_b32 v73, v75\ns_waitcnt lgkmcnt(11)\nQmava 0,0,112\nQtv 16,50,4608\nv_mul_f32 v202, v55, v202\nQmava 0,20,140\nbuffer_store_dwordx4 v[152:155], v60, s[56:59], s88 offen offset:2048 sc1\nQmava 20,0,116\n"
"Qtv 18,50,5632\nv_mul_f32 v203, v55, v203\nQmava 20,20,144\nQtv 36,50,16896\nv_mul_f32 v204, v55, v204\nQmava 40,0,120\nQtv 38,50,17920\nv_mul_f32 v205, v55, v205\n"
"Qmava 40,20,148\nQda 124,47,0\nv_mul_f32 v206, v55, v206\nQmava 4,4,112\nQda 152,47,12288\nv_mul_f32 v207, v55, v207\nQmava 4,24,140\nds_read_b32 v66, v74\n"
"Qk 200,200,201\nQmava 24,4,116\nQda 128,47,1024\nQk 201,202,203\nQmava 24,24,144\nQda 156,47,13312\nQk 202,204,205\nQmava 44,4,120\n"
"ds_write_b128 v44, v[184:187] offset:49152\nQmava 44,24,148\ns_waitcnt lgkmcnt(3)\nv_cmp_gt_u32 vcc, v75, v66\nQmava 8,8,112\ns_cbranch_vccnz .Lso10\n.Lro10:\nQk 203,206,207\n"
"Qmava 8,28,140\nQda 132,47,2048\ns_add_u32 m0, s91, 73728\ns_mov_b32 s94, 143360\nQmava 28,8,116\nbuffer_load_dwordx4 v60, s[48:51], s94 offen lds\nQmava 28,28,144\nQda 160,47,14336\n"
"v_mul_f32 v232, v56, v232\nQmava 48,8,120\nQda 136,47,3072\nv_mul_f32 v233, v56, v233\nQmava 48,28,148\nQda 164,47,15360\nv_mul_f32 v234, v56, v234\ns_waitcnt lgkmcnt(10)\n"
"Qmava 12,12,112\nbuffer_load_dwordx4 v60, s[48:51], s94 offen offset:1024 lds\nQmava 12,32,140\nv_mul_f32 v235, v56, v235\nv_mul_f32 v236, v56, v236\nQmava 32,12,116\nv_mul_f32 v237, v56, v237\nv_mul_f32 v238, v56, v238\n"
"Qmava 32,32,144\nv_mul_f32 v239, v56, v239\nQk 232,232,233\nQmava 52,12,120\nds_write_b128 v44, v[216:219] offset:50176\nQmava 52,32,148\nbuffer_load_dwordx4 v60, s[48:51], s94 offen offset:2048 lds\nQmava 16,16,112\n"
"Qda 112,47,33792\nQk 233,234,235\nQmava 16,36,140\nQda 140,47,46080\nQk 234,236,237\nQmava 36,16,116\nQda 116,47,34816\nQk 235,238,239\n"
"Qmava 36,36,144\nQda 144,47,47104\ns_mov_b32 s95, 410624\nQmava 56,16,120\nbuffer_load_dwordx4 v61, s[52:55], s95 offen offset:3072 lds\nv_add_u32 v75, 1, v75\nQmava 56,36,148\ns_waitcnt vmcnt(11)\n"
"ds_write_b32 v73, v75\ns_waitcnt lgkmcnt(6)\nQmava 60,0,124\nQda 120,47,35840\nv_accvgpr_read_b32 v212, a216\nQmava 60,20,152\nbuffer_store_dwordx4 v[184:187], v60, s[56:59], s88 offen offset:3072 sc1\ns_add_u32 m0, s91, 77824\n"
"s_mov_b32 s95, 413824\nQmava 80,0,128\nbuffer_load_dwordx4 v62, s[52:55], s95 offen lds\nQmava 80,20,156\nQda 148,47,48128\nv_accvgpr_read_b32 v213, a217\nQmava 100,0,132\nds_write_b128 v44, v[248:251] offset:51200\n"
"Qmava 100,20,160\nv_accvgpr_read_b32 v214, a218\nv_accvgpr_read_b32 v215, a219\nQmava 184,0,136\nQtv 0,50,36864\nv_accvgpr_read_b32 v40, a220\nQmava 184,20,164\nds_read_b32 v66, v74\n"
"s_mov_b32 s95, 433216\nQmava 64,4,124\nbuffer_load_dwordx4 v61, s[52:55], s95 offen offset:1024 lds\nQmava 64,24,152\nQtv 2,50,38400\nv_accvgpr_read_b32 v41, a221\nQmava 84,4,128\nQtv 20,50,49152\n"
"v_accvgpr_read_b32 v42, a222\nQmava 84,24,156\ns_waitcnt lgkmcnt(2)\nv_cmp_gt_u32 vcc, v75, v66\nQmava 104,4,132\ns_cbranch_vccnz .Lso11\n.Lro11:\nv_accvgpr_read_b32 v43, a223\n"
"Qmava 104,24,160\nQtv 22,50,50688\ns_add_u32 m0, s91, 98304\ns_mov_b32 s94, 146432\nQmava 188,4,136\nbuffer_load_dwordx4 v60, s[48:51], s94 offen lds\nQmava 188,24,164\nQtv 4,50,37376\n"
"v_mul_f32 v212, v57, v212\nQmava 68,8,124\nds_write_b128 v44, v[116:119] offset:52224\nQmava 68,28,152\nQtv 6,50,38912\nv_mul_f32 v213, v57, v213\nQmava 88,8,128\nQtv 24,50,49664\n"
"v_mul_f32 v214, v57, v214\nQmava 88,28,156\nQtv 26,50,51200\nv_mul_f32 v215, v57, v215\nQmava 108,8,132\nbuffer_load_dwordx4 v60, s[48:51], s94 offen offset:1024 lds\nQmava 108,28,160\nv_mul_f32 v40, v57, v40\n"
"v_mul_f32 v41, v57, v41\nQmava 192,8,136\nQtv 8,50,37888\nv_mul_f32 v42, v57, v42\nQmava 192,28,164\nQtv 10,50,39424\nv_mul_f32 v43, v57, v43\nQmava 72,12,124\n"
"Qtv 28,50,50176\nQk 212,212,213\nQmava 72,32,152\nQtv 30,50,51712\nQk 213,214,215\nQmava 92,12,128\nbuffer_load_dwordx4 v60, s[48:51], s94 offen offset:2048 lds\nQmava 92,32,156\n"
"Qk 214,40,41\nQk 215,42,43\nQmava 176,12,132\nv_accvgpr_read_b32 v244, a248\nv_accvgpr_read_b32 v245, a249\nQmava 176,32,160\nv_accvgpr_read_b32 v246, a250\nv_accvgpr_read_b32 v247, a251\n"
"Qmava 196,12,136\nQtv 12,48,4096\nv_accvgpr_read_b32 v40, a252\nQmava 196,32,164\nQtv 14,48,5120\nv_accvgpr_read_b32 v41, a253\nQmava 76,16,124\nQtv 32,48,16384\n"
"v_accvgpr_read_b32 v42, a254\nQmava 76,36,152\nbuffer_load_dwordx4 v60, s[48:51], s94 offen offset:3072 lds\nQmava 96,16,128\nQtv 34,48,17408\nv_accvgpr_read_b32 v43, a255\nQmava 96,36,156\nv_mul_f32 v244, v58, v244\n"
"v_mul_f32 v245, v58, v245\nQmava 180,16,132\nv_mul_f32 v246, v58, v246\nv_mul_f32 v247, v58, v247\nQmava 180,36,160\nv_mul_f32 v40, v58, v40\nv_mul_f32 v41, v58, v41\nQmava 200,16,136\n"
"Qtv 16,48,4608\nv_add_u32 v75, 1, v75\nQmava 200,36,164\ns_waitcnt vmcnt(14)\nds_write_b32 v73, v75\ns_add_u32 m0, s91, 102400\ns_mov_b32 s95, 413888\ns_waitcnt lgkmcnt(10)\n"
"Qmava 0,0,112\nbuffer_load_dwordx4 v61, s[52:55], s95 offen lds\ns_add_u32 s88, s71, 18432\nQmava 0,20,140\nbuffer_store_dwordx4 v[216:219], v60, s[56:59], s88 offen offset:0 sc1\nQmava 20,0,116\nQtv 18,48,5632\nv_mul_f32 v42, v58, v42\n"
"Qmava 20,20,144\nQtv 36,48,16896\nv_mul_f32 v43, v58, v43\nQmava 40,0,120\nQtv 38,48,17920\ns_mov_b32 s95, 433344\nQmava 40,20,148\nbuffer_load_dwordx4 v61, s[52:55], s95 offen offset:1024 lds\n"
"Qmava 4,4,112\nQda 124,45,0\nQk 244,244,245\nQmava 4,24,140\nds_read_b32 v66, v74\nQk 245,246,247\nQmava 24,4,116\nQda 152,45,12288\n"
"Qk 246,40,41\nQmava 24,24,144\nQda 128,45,1024\nQk 247,42,43\nQmava 44,4,120\nQda 156,45,13312\nv_add_u32 v67, 2, v65\nQmava 44,24,148\n"
"s_waitcnt lgkmcnt(3)\nv_cmp_gt_u32 vcc, v75, v66\nQmava 8,8,112\ns_cbranch_vccnz .Lso12\n.Lro12:\nQmava 8,28,140\nbuffer_store_dword v67, v77, s[24:27], 0 offen sc1\ns_add_u32 m0, s91, 132096\n"
"s_mov_b32 s94, 157696\nQmava 28,8,116\nbuffer_load_dwordx4 v60, s[48:51], s94 offen lds\nQmava 28,28,144\nQda 132,45,2048\nQmava 48,8,120\nQda 160,45,14336\nQmava 48,28,148\n"
"Qda 136,45,3072\ns_waitcnt lgkmcnt(8)\nQmava 12,12,112\nbuffer_load_dwordx4 v60, s[48:51], s94 offen offset:1024 lds\nQmava 12,32,140\nQda 164,45,15360\nQmava 32,12,116\nQmava 32,32,144\n"
"Qmava 52,12,120\nQmava 52,32,148\nbuffer_load_dwordx4 v60, s[48:51], s94 offen offset:2048 lds\nQmava 16,16,112\nQda 112,45,24576\nQmava 16,36,140\nQda 140,45,36864\nQmava 36,16,116\n"
"Qda 116,45,25600\nQmava 36,36,144\nQda 144,45,37888\ns_mov_b32 s95, 451584\nQmava 56,16,120\nbuffer_load_dwordx4 v61, s[52:55], s95 offen offset:3072 lds\nv_add_u32 v75, 1, v75\nQmava 56,36,148\n"
"s_waitcnt vmcnt(20)\nds_write_b32 v73, v75\ns_waitcnt lgkmcnt(5)\nQmava 60,0,124\nQda 120,45,26624\nQmava 60,20,152\nbuffer_store_dwordx4 v[248:251], v60, s[56:59], s88 offen offset:1024 sc1\ns_add_u32 m0, s91, 136192\n"
"s_mov_b32 s95, 454784\nQmava 80,0,128\nbuffer_load_dwordx4 v62, s[52:55], s95 offen lds\nQmava 80,20,156\nQda 148,45,38912\nQmava 100,0,132\nQmava 100,20,160\nQmava 184,0,136\n"
"Qtv 0,48,27648\nQmava 184,20,164\nds_read_b32 v66, v74\ns_mov_b32 s95, 474176\nQmava 64,4,124\nbuffer_load_dwordx4 v61, s[52:55], s95 offen offset:1024 lds\nQmava 64,24,152\nQtv 2,48,29184\n"
"Qmava 84,4,128\nQtv 20,48,39936\nQmava 84,24,156\ns_waitcnt lgkmcnt(2)\nv_cmp_gt_u32 vcc, v75, v66\nQmava 104,4,132\ns_cbranch_vccnz .Lso13\n.Lro13:\n"
"Qmava 104,24,160\nQtv 22,48,41472\ns_add_u32 m0, s91, 0\ns_mov_b32 s94, 160768\nQmava 188,4,136\nbuffer_load_dwordx4 v60, s[48:51], s94 offen lds\nQmava 188,24,164\nQtv 4,48,28160\n"
"Qmava 68,8,124\nQtv 6,48,29696\nQmava 68,28,152\nQtv 24,48,40448\nQmava 88,8,128\nQtv 26,48,41984\nQmava 88,28,156\nQmava 108,8,132\n"
"buffer_load_dwordx4 v60, s[48:51], s94 offen offset:1024 lds\nQmava 108,28,160\nQmava 192,8,136\nQtv 8,48,28672\nQmava 192,28,164\nQtv 10,48,30208\nQmava 72,12,124\nQtv 28,48,40960\n"
"Qmava 72,32,152\nQtv 30,48,42496\nQmava 92,12,128\nbuffer_load_dwordx4 v60, s[48:51], s94 offen offset:2048 lds\nQmava 92,32,156\nQmava 176,12,132\nQmava 176,32,160\nQmava 196,12,136\n"
"Qtv 12,49,4096\nQmava 196,32,164\nQtv 14,49,5120\nQmava 76,16,124\nQtv 32,49,16384\nQmava 76,36,152\nbuffer_load_dwordx4 v60, s[48:51], s94 offen offset:3072 lds\nQmava 96,16,128\n"
"Qtv 34,49,17408\nQmava 96,36,156\nQmava 180,16,132\nQmava 180,36,160\nQmava 200,16,136\nQtv 16,49,4608\nv_add_u32 v75, 1, v75\nQmava 200,36,164\n"
"s_waitcnt vmcnt(19)\nds_write_b32 v73, v75\ns_add_u32 m0, s91, 4096\ns_mov_b32 s95, 454848\ns_waitcnt lgkmcnt(10)\nQmava 0,0,112\nbuffer_load_dwordx4 v61, s[52:55], s95 offen lds\nQmava 0,20,140\n"
"buffer_store_dwordx4 v[116:119], v60, s[56:59], s88 offen offset:2048 sc1\nQmava 20,0,116\nQtv 18,49,5632\nQmava 20,20,144\nQtv 36,49,16896\nQmava 40,0,120\nQtv 38,49,17920\ns_mov_b32 s95, 474304\n"
"Qmava 40,20,148\nbuffer_load_dwordx4 v61, s[52:55], s95 offen offset:1024 lds\nQmava 4,4,112\nQda 124,46,0\ns_add_u32 s68, %19, 1\nQmava 4,24,140\nds_read_b32 v66, v74\ns_and_b32 s68, s68, 3\n"
"Qmava 24,4,116\nQda 152,46,12288\nv_accvgpr_read_b32 v92, a0\nQmava 24,24,144\nQda 128,46,1024\nv_accvgpr_read_b32 v93, a1\nQmava 44,4,120\nQda 156,46,13312\n"
".Lpg15:\ns_cbranch_execz .Ltr6\nv_accvgpr_read_b32 v94, a2\nQmava 44,24,148\ns_waitcnt lgkmcnt(3)\nv_cmp_gt_u32 vcc, v75, v66\nQmava 8,8,112\ns_cbranch_vccnz .Lso14\n"
".Lro14:\nv_accvgpr_read_b32 v95, a3\nQmava 8,28,140\nQda 132,46,2048\ns_add_u32 m0, s91, 24576\ns_add_u32 s95, s66, 82944\nQmava 28,8,116\nbuffer_load_dwordx4 v61, s[52:55], s95 offen offset:3072 lds\n"
"Qmava 28,28,144\nQda 160,46,14336\nv_accvgpr_read_b32 v124, a4\nQmava 48,8,120\nQda 136,46,3072\nv_accvgpr_read_b32 v125, a5\nQmava 48,28,148\nQda 164,46,15360\n"
"v_accvgpr_read_b32 v126, a6\ns_waitcnt lgkmcnt(9)\nQmava 12,12,112\nv_accvgpr_read_b32 v127, a7\ns_lshl_b32 s68, s68, 3\nQmava 12,32,140\ns_add_u32 s68, s68, 32\ns_mul_i32 s67, s68, 40960\n"
"Qmava 32,12,116\nv_accvgpr_read_b32 v156, a8\nv_accvgpr_read_b32 v157, a9\nQmava 32,32,144\nv_accvgpr_read_b32 v158, a10\nv_accvgpr_read_b32 v159, a11\nQmava 52,12,120\ns_add_u32 m0, s91, 28672\n"
"s_add_u32 s95, s66, 86144\ns_mul_i32 s74, s68, 14336\nQmava 52,32,148\nbuffer_load_dwordx4 v62, s[52:55], s95 offen lds\nQmava 16,16,112\nQda 112,46,24576\ns_add_u32 s68, %19, 2\nQmava 16,36,140\n"
"Qda 140,46,36864\ns_and_b32 s68, s68, 3\nQmava 36,16,116\nQda 116,46,25600\nv_accvgpr_read_b32 v188, a12\nQmava 36,36,144\nQda 144,46,37888\nv_accvgpr_read_b32 v189, a13\n"
"Qmava 56,16,120\nQda 120,46,26624\nv_add_u32 v75, 1, v75\nQmava 56,36,148\ns_waitcnt vmcnt(17)\nds_write_b32 v73, v75\ns_waitcnt lgkmcnt(6)\nQmava 60,0,124\n"
"Qda 148,46,38912\ns_add_u32 s88, s71, 28672\nQmava 60,20,152\nbuffer_store_dwordx4 v[96:99], v60, s[56:59], s88 offen offset:0 sc1\ns_add_u32 s95, s66, 105536\nQmava 80,0,128\nbuffer_load_dwordx4 v61, s[52:55], s95 offen offset:1024 lds\nQmava 80,20,156\n"
"v_accvgpr_read_b32 v190, a14\nv_accvgpr_read_b32 v191, a15\nQmava 100,0,132\ns_lshl_b32 s68, s68, 3\ns_add_u32 s68, s68, 32\nQmava 100,20,160\ns_mul_i32 s72, s68, 40960\ns_mul_i32 s75, s68, 14336\n"
"Qmava 184,0,136\nQtv 0,49,27648\ns_add_u32 s68, %19, 3\nQmava 184,20,164\nds_read_b32 v66, v74\ns_and_b32 s68, s68, 3\nQmava 64,4,124\nQtv 2,49,29184\n"
"s_lshl_b32 s68, s68, 3\nQmava 64,24,152\nQtv 20,49,39936\ns_add_u32 s68, s68, 32\nQmava 84,4,128\nQtv 22,49,41472\ns_mul_i32 s73, s68, 40960\nQmava 84,24,156\n"
"s_waitcnt lgkmcnt(3)\nv_cmp_gt_u32 vcc, v75, v66\nQmava 104,4,132\ns_cbranch_vccnz .Lso15\n.Lro15:\ns_mul_i32 s77, s68, 14336\nQmava 104,24,160\ns_add_u32 m0, s91, 53248\n"
"s_add_u32 s95, s66, 86208\nv_and_b32 v54, 512, v60\nQmava 188,4,136\nbuffer_load_dwordx4 v61, s[52:55], s95 offen lds\nQmava 188,24,164\nds_write_b128 v44, v[96:99] offset:24576\nQmava 68,8,124\nQtv 4,49,28160\n"
"v_lshlrev_b32 v54, 1, v54\nQmava 68,28,152\nQtv 6,49,29696\nv_sub_u32 v54, v48, v54\nQmava 88,8,128\nQtv 24,49,40448\ns_mul_i32 s68, s93, 10240\nQmava 88,28,156\n"
"Qtv 26,49,41984\ns_add_u32 s68, s68, 4096\nQmava 108,8,132\nv_add_u32 v40, s68, v54\nv_add_u32 v41, 61440, v40\nQmava 108,28,160\ns_sub_u32 s68, 18432, s68\nv_add_u32 v42, s68, v54\n"
"Qmava 192,8,136\nQtv 8,49,28672\nv_add_u32 v43, 61440, v42\nQmava 192,28,164\nQtv 10,49,30208\ns_lshl_b32 s68, s70, 10\nQmava 72,12,124\nQtv 28,49,40960\n"
"s_add_u32 s69, %10, 106496\nQmava 72,32,152\nQtv 30,49,42496\nv_add_u32 v54, s69, v60\nQmava 92,12,128\nds_write_b128 v44, v[128:131] offset:25600\nQmava 92,32,156\nv_add_u32 v52, s68, v54\n"
"s_xor_b32 s68, s68, 2048\nQmava 176,12,132\nv_add_u32 v53, s68, v54\nQmava 176,32,160\nQmava 196,12,136\nQtv 12,50,4096\nQmava 196,32,164\nQtv 14,50,5120\n"
"Qmava 76,16,124\nQtv 32,50,16384\ns_add_u32 s95, s66, 105664\nQmava 76,36,152\nbuffer_load_dwordx4 v61, s[52:55], s95 offen offset:1024 lds\nQmava 96,16,128\nQtv 34,50,17408\nQmava 96,36,156\n"
"Qmava 180,16,132\nQmava 180,36,160\nQmava 200,16,136\nds_write_b128 v44, v[160:163] offset:26624\nv_add_u32 v75, 1, v75\nQmava 200,36,164\ns_waitcnt vmcnt(13)\nds_write_b32 v73, v75\n"
"s_waitcnt lgkmcnt(11)\nQmvva 92,0,112\nQtv 16,50,4608\nQmvva 92,20,140\nbuffer_store_dwordx4 v[128:131], v60, s[56:59], s88 offen offset:1024 sc1\nQmava 20,0,116\nQtv 18,50,5632\nQmava 20,20,144\n"
"Qtv 36,50,16896\nQmava 40,0,120\nQtv 38,50,17920\nQmava 40,20,148\nQda 124,47,0\nQmvva 124,4,112\nQda 152,47,12288\nQmvva 124,24,140\n"
"ds_read_b32 v66, v74\nQmava 24,4,116\nQda 128,47,1024\nQmava 24,24,144\nQda 156,47,13312\nQmava 44,4,120\nds_write_b128 v44, v[192:195] offset:49152\nQmava 44,24,148\n"
"s_waitcnt lgkmcnt(3)\nv_cmp_gt_u32 vcc, v75, v66\nQmvva 156,8,112\ns_cbranch_vccnz .Lso16\n.Lro16:\nv_accvgpr_read_b32 v252, a20\nQmvva 156,28,140\nQda 132,47,2048\n"
"s_add_u32 m0, s91, 73728\ns_mov_b32 s94, 229376\nQmava 28,8,116\nbuffer_load_dwordx4 v60, s[48:51], s94 offen lds\nQmava 28,28,144\nQda 160,47,14336\nv_accvgpr_read_b32 v253, a21\nQmava 48,8,120\n"
"Qda 136,47,3072\nv_accvgpr_read_b32 v254, a22\nQmava 48,28,148\nQda 164,47,15360\nv_accvgpr_read_b32 v255, a23\ns_waitcnt lgkmcnt(10)\nQmvva 188,12,112\nbuffer_load_dwordx4 v60, s[48:51], s94 offen offset:1024 lds\n"
"Qmvva 188,32,140\nv_accvgpr_read_b32 v100, a24\nv_accvgpr_read_b32 v101, a25\nQmava 32,12,116\nv_accvgpr_read_b32 v102, a26\nv_accvgpr_read_b32 v103, a27\nQmava 32,32,144\nQmava 52,12,120\n"
"ds_write_b128 v44, v[224:227] offset:50176\nQmava 52,32,148\nbuffer_load_dwordx4 v60, s[48:51], s94 offen offset:2048 lds\nQmava 16,16,112\nQda 112,47,33792\nv_accvgpr_read_b32 v132, a28\nQmava 16,36,140\nQda 140,47,46080\n"
"v_accvgpr_read_b32 v133, a29\nQmava 36,16,116\nQda 116,47,34816\nv_accvgpr_read_b32 v134, a30\nQmava 36,36,144\nQda 144,47,47104\ns_mov_b32 s95, 656384\nQmava 56,16,120\n"
"buffer_load_dwordx4 v61, s[52:55], s95 offen offset:3072 lds\nv_add_u32 v75, 1, v75\nQmava 56,36,148\ns_waitcnt vmcnt(11)\nds_write_b32 v73, v75\ns_waitcnt lgkmcnt(6)\nQmava 60,0,124\nQda 120,47,35840\n"
"v_accvgpr_read_b32 v135, a31\nQmava 60,20,152\nbuffer_store_dwordx4 v[160:163], v60, s[56:59], s88 offen offset:2048 sc1\ns_add_u32 m0, s91, 77824\ns_mov_b32 s95, 659584\nQmava 80,0,128\nbuffer_load_dwordx4 v62, s[52:55], s95 offen lds\nQmava 80,20,156\n"
"Qda 148,47,48128\nv_accvgpr_read_b32 v220, a16\nQmava 100,0,132\nds_write_b128 v44, v[148:151] offset:51200\nQmava 100,20,160\nv_accvgpr_read_b32 v221, a17\nv_accvgpr_read_b32 v222, a18\nQmava 184,0,136\n"
"Qtv 0,50,36864\nv_accvgpr_read_b32 v223, a19\nQmava 184,20,164\nds_read_b32 v66, v74\ns_mov_b32 s95, 678976\nQmava 64,4,124\nbuffer_load_dwordx4 v61, s[52:55], s95 offen offset:1024 lds\nQmava 64,24,152\n"
"Qtv 2,50,38400\nQmava 84,4,128\nQtv 20,50,49152\nQmava 84,24,156\ns_waitcnt lgkmcnt(2)\nv_cmp_gt_u32 vcc, v75, v66\nQmava 104,4,132\ns_cbranch_vccnz .Lso17\n"
".Lro17:\nQmava 104,24,160\nQtv 22,50,50688\ns_add_u32 m0, s91, 98304\ns_mov_b32 s94, 232448\nQmava 188,4,136\nbuffer_load_dwordx4 v60, s[48:51], s94 offen lds\nQmava 188,24,164\n"
"Qtv 4,50,37376\nQmava 68,8,124\nds_write_b128 v44, v[180:183] offset:52224\nQmava 68,28,152\nQtv 6,50,38912\nQmava 88,8,128\nQtv 24,50,49664\nQmava 88,28,156\n"
"Qtv 26,50,51200\nQmava 108,8,132\nbuffer_load_dwordx4 v60, s[48:51], s94 offen offset:1024 lds\nQmava 108,28,160\nQmava 192,8,136\nQtv 8,50,37888\nQmava 192,28,164\nQtv 10,50,39424\n"
"Qmava 72,12,124\nQtv 28,50,50176\nQmava 72,32,152\nQtv 30,50,51712\nQmava 92,12,128\nbuffer_load_dwordx4 v60, s[48:51], s94 offen offset:2048 lds\nQmava 92,32,156\nQmava 176,12,132\n"
"Qmava 176,32,160\nQmava 196,12,136\nQtv 12,48,4096\nQmava 196,32,164\nQtv 14,48,5120\nQmava 76,16,124\nQtv 32,48,16384\nQmava 76,36,152\n"
"buffer_load_dwordx4 v60, s[48:51], s94 offen offset:3072 lds\nQmava 96,16,128\nQtv 34,48,17408\nQmava 96,36,156\nQmava 180,16,132\nQmava 180,36,160\nQmava 200,16,136\nQtv 16,48,4608\n"
"v_add_u32 v75, 1, v75\nQmava 200,36,164\ns_waitcnt vmcnt(14)\nds_write_b32 v73, v75\ns_add_u32 m0, s91, 102400\ns_mov_b32 s95, 659648\ns_waitcnt lgkmcnt(10)\nQmvva 92,0,112\n"
"buffer_load_dwordx4 v61, s[52:55], s95 offen lds\nQmvva 92,20,140\nbuffer_store_dwordx4 v[192:195], v60, s[56:59], s88 offen offset:3072 sc1\nQmvva 252,0,116\nQtv 18,48,5632\nQmvva 252,20,144\nQtv 36,48,16896\nQmava 40,0,120\n"
"Qtv 38,48,17920\ns_mov_b32 s95, 679104\nQmava 40,20,148\nbuffer_load_dwordx4 v61, s[52:55], s95 offen offset:1024 lds\nQmvva 124,4,112\nQda 124,45,0\nQmvva 124,24,140\nds_read_b32 v66, v74\n"
"Qmvva 100,4,116\nQda 152,45,12288\nQmvva 100,24,144\nQda 128,45,1024\nQmava 44,4,120\nQda 156,45,13312\nQmava 44,24,148\ns_waitcnt lgkmcnt(3)\n"
"v_cmp_gt_u32 vcc, v75, v66\nQmvva 156,8,112\ns_cbranch_vccnz .Lso18\n.Lro18:\nv_accvgpr_read_b32 v228, a40\nQmvva 156,28,140\nQda 132,45,2048\ns_add_u32 m0, s91, 132096\n"
"s_mov_b32 s94, 243712\nQmvva 132,8,116\nbuffer_load_dwordx4 v60, s[48:51], s94 offen lds\nQmvva 132,28,144\nQda 160,45,14336\nv_accvgpr_read_b32 v229, a41\nQmava 48,8,120\nQda 136,45,3072\n"
"v_accvgpr_read_b32 v230, a42\nQmava 48,28,148\nQda 164,45,15360\nv_accvgpr_read_b32 v231, a43\ns_waitcnt lgkmcnt(9)\nQmvva 188,12,112\nbuffer_load_dwordx4 v60, s[48:51], s94 offen offset:1024 lds\nQmvva 188,32,140\n"
"v_accvgpr_read_b32 v108, a44\nv_accvgpr_read_b32 v109, a45\nQmava 32,12,116\nv_accvgpr_read_b32 v110, a46\nv_accvgpr_read_b32 v111, a47\nQmava 32,32,144\nQmava 52,12,120\nQmava 52,32,148\n"
"buffer_load_dwordx4 v60, s[48:51], s94 offen offset:2048 lds\nQmvva 220,16,112\nQda 112,45,24576\nQmvva 220,36,140\nQda 140,45,36864\nQmava 36,16,116\nQda 116,45,25600\nQmava 36,36,144\n"
"Qda 144,45,37888\ns_mov_b32 s95, 697344\nQmava 56,16,120\nbuffer_load_dwordx4 v61, s[52:55], s95 offen offset:3072 lds\nv_add_u32 v75, 1, v75\nQmava 56,36,148\ns_waitcnt vmcnt(19)\nds_write_b32 v73, v75\n"
"s_waitcnt lgkmcnt(5)\nQmava 60,0,124\nQda 120,45,26624\ns_add_u32 s88, s71, 32768\nQmava 60,20,152\nbuffer_store_dwordx4 v[224:227], v60, s[56:59], s88 offen offset:0 sc1\ns_add_u32 m0, s91, 136192\ns_mov_b32 s95, 700544\n"
"Qmava 80,0,128\nbuffer_load_dwordx4 v62, s[52:55], s95 offen lds\nQmava 80,20,156\nQda 148,45,38912\nv_accvgpr_read_b32 v164, a32\nQmava 100,0,132\nv_accvgpr_read_b32 v165, a33\nv_accvgpr_read_b32 v166, a34\n"
"Qmava 100,20,160\nv_accvgpr_read_b32 v167, a35\nv_accvgpr_read_b32 v196, a36\nQmava 184,0,136\nQtv 0,48,27648\nv_accvgpr_read_b32 v197, a37\nQmava 184,20,164\nds_read_b32 v66, v74\n"
"s_mov_b32 s95, 719936\nQmava 64,4,124\nbuffer_load_dwordx4 v61, s[52:55], s95 offen offset:1024 lds\nQmava 64,24,152\nQtv 2,48,29184\nv_accvgpr_read_b32 v198, a38\nQmava 84,4,128\nQtv 20,48,39936\n"
"v_add_u32 v67, 3, v65\nQmava 84,24,156\ns_waitcnt lgkmcnt(2)\nv_cmp_gt_u32 vcc, v75, v66\nQmava 104,4,132\ns_cbranch_vccnz .Lso19\n.Lro19:\nv_accvgpr_read_b32 v199, a39\n"
"Qmava 104,24,160\nbuffer_store_dword v67, v77, s[24:27], 0 offen sc1\ns_add_u32 m0, s91, 0\ns_mov_b32 s94, 246784\nQmava 188,4,136\nbuffer_load_dwordx4 v60, s[48:51], s94 offen lds\nQmava 188,24,164\nQtv 22,48,41472\n"
"Qmava 68,8,124\nQtv 4,48,28160\nQmava 68,28,152\nQtv 6,48,29696\nQmava 88,8,128\nQtv 24,48,40448\nQmava 88,28,156\nQtv 26,48,41984\n"
"Qmava 108,8,132\nbuffer_load_dwordx4 v60, s[48:51], s94 offen offset:1024 lds\nQmava 108,28,160\nQmava 192,8,136\nQtv 8,48,28672\nQmava 192,28,164\nQtv 10,48,30208\nQmava 72,12,124\n"
"Qtv 28,48,40960\nQmava 72,32,152\nQtv 30,48,42496\nQmava 92,12,128\nbuffer_load_dwordx4 v60, s[48:51], s94 offen offset:2048 lds\nQmava 92,32,156\nQmava 176,12,132\nQmava 176,32,160\n"
"Qmava 196,12,136\nQtv 12,49,4096\nQmava 196,32,164\nQtv 14,49,5120\nQmava 76,16,124\nQtv 32,49,16384\nQmava 76,36,152\nbuffer_load_dwordx4 v60, s[48:51], s94 offen offset:3072 lds\n"
"Qmava 96,16,128\nQtv 34,49,17408\nQmava 96,36,156\nQmava 180,16,132\nQmava 180,36,160\nQmava 200,16,136\nQtv 16,49,4608\nv_add_u32 v75, 1, v75\n"
"Qmava 200,36,164\ns_waitcnt vmcnt(19)\nds_write_b32 v73, v75\ns_add_u32 m0, s91, 4096\ns_mov_b32 s95, 700608\ns_waitcnt lgkmcnt(10)\nQmvva 92,0,112\nbuffer_load_dwordx4 v61, s[52:55], s95 offen lds\n"
"Qmvva 92,20,140\nbuffer_store_dwordx4 v[148:151], v60, s[56:59], s88 offen offset:1024 sc1\nQmvva 252,0,116\nQtv 18,49,5632\nQmvva 252,20,144\nQtv 36,49,16896\nQmvva 228,0,120\nQtv 38,49,17920\n"
"s_mov_b32 s95, 720064\nQmvva 228,20,148\nbuffer_load_dwordx4 v61, s[52:55], s95 offen offset:1024 lds\nQmvva 124,4,112\nQda 124,46,0\nQmvva 124,24,140\nds_read_b32 v66, v74\nQmvva 100,4,116\n"
"Qda 152,46,12288\nQmvva 100,24,144\nQda 128,46,1024\nQmvva 108,4,120\nQda 156,46,13312\nQmvva 108,24,148\ns_waitcnt lgkmcnt(3)\nv_cmp_gt_u32 vcc, v75, v66\n"
"Qmvva 156,8,112\ns_cbranch_vccnz .Lso20\n.Lro20:\nQmvva 156,28,140\nQda 132,46,2048\ns_add_u32 m0, s91, 24576\ns_add_u32 s95, s66, 123904\nQmvva 132,8,116\n"
"buffer_load_dwordx4 v61, s[52:55], s95 offen offset:3072 lds\nQmvva 132,28,144\nQda 160,46,14336\nQmava 48,8,120\nQda 136,46,3072\nQmava 48,28,148\nQda 164,46,15360\ns_waitcnt lgkmcnt(9)\n"
"Qmvva 188,12,112\nQmvva 188,32,140\nQmvva 164,12,116\nQmvva 164,32,144\nQmava 52,12,120\ns_add_u32 m0, s91, 28672\ns_add_u32 s95, s66, 127104\nQmava 52,32,148\n"
".Lpg16:\ns_cbranch_execz .Lpg20\nbuffer_load_dwordx4 v62, s[52:55], s95 offen lds\nQmvva 220,16,112\nQda 112,46,24576\nv_accvgpr_read_b32 v140, a48\nQmvva 220,36,140\nQda 140,46,36864\n"
"v_accvgpr_read_b32 v141, a49\nQmvva 196,16,116\nQda 116,46,25600\nv_accvgpr_read_b32 v142, a50\nQmvva 196,36,144\nQda 144,46,37888\nv_accvgpr_read_b32 v143, a51\nQmava 56,16,120\n"
"Qda 120,46,26624\nv_add_u32 v75, 1, v75\nQmava 56,36,148\ns_waitcnt vmcnt(17)\nds_write_b32 v73, v75\ns_waitcnt lgkmcnt(6)\nQmava 60,0,124\nQda 148,46,38912\n"
"v_accvgpr_read_b32 v172, a52\nQmava 60,20,152\nbuffer_store_dwordx4 v[180:183], v60, s[56:59], s88 offen offset:2048 sc1\ns_add_u32 s95, s66, 146496\nQmava 80,0,128\nbuffer_load_dwordx4 v61, s[52:55], s95 offen offset:1024 lds\nQmava 80,20,156\nv_accvgpr_read_b32 v173, a53\n"
"v_accvgpr_read_b32 v174, a54\nQmava 100,0,132\nv_accvgpr_read_b32 v175, a55\nQmava 100,20,160\nQmava 184,0,136\nQtv 0,49,27648\nv_accvgpr_read_b32 v204, a56\nQmava 184,20,164\n"
"ds_read_b32 v66, v74\nv_accvgpr_read_b32 v205, a57\nQmava 64,4,124\nQtv 2,49,29184\nv_accvgpr_read_b32 v206, a58\nQmava 64,24,152\nQtv 20,49,39936\nv_accvgpr_read_b32 v207, a59\n"
"Qmava 84,4,128\nQtv 22,49,41472\nv_accvgpr_read_b32 v236, a60\nQmava 84,24,156\ns_waitcnt lgkmcnt(3)\nv_cmp_gt_u32 vcc, v75, v66\nQmava 104,4,132\ns_cbranch_vccnz .Lso21\n"
".Lro21:\nv_accvgpr_read_b32 v237, a61\nQmava 104,24,160\ns_add_u32 m0, s91, 53248\ns_add_u32 s95, s66, 127168\nv_accvgpr_read_b32 v238, a62\nQmava 188,4,136\nbuffer_load_dwordx4 v61, s[52:55], s95 offen lds\n"
"Qmava 188,24,164\nds_write_b128 v44, v[104:107] offset:24576\nQmava 68,8,124\nQtv 4,49,28160\nv_accvgpr_read_b32 v239, a63\nQmava 68,28,152\nQtv 6,49,29696\nQmava 88,8,128\n"
"Qtv 24,49,40448\nQmava 88,28,156\nQtv 26,49,41984\nQmava 108,8,132\nQmava 108,28,160\nQmava 192,8,136\nQtv 8,49,28672\nQmava 192,28,164\n"
"Qtv 10,49,30208\nQmava 72,12,124\nQtv 28,49,40960\nQmava 72,32,152\nQtv 30,49,42496\nQmava 92,12,128\nds_write_b128 v44, v[136:139] offset:25600\nQmava 92,32,156\n"
"Qmava 176,12,132\nQmava 176,32,160\nQmava 196,12,136\nQtv 12,50,4096\nQmava 196,32,164\nQtv 14,50,5120\nQmava 76,16,124\nQtv 32,50,16384\n"
"s_add_u32 s95, s66, 146624\nQmava 76,36,152\nbuffer_load_dwordx4 v61, s[52:55], s95 offen offset:1024 lds\nQmava 96,16,128\nQtv 34,50,17408\nQmava 96,36,156\nQmava 180,16,132\nQmava 180,36,160\n"
"Qmava 200,16,136\nds_write_b128 v44, v[168:171] offset:26624\nv_add_u32 v75, 1, v75\nQmava 200,36,164\ns_waitcnt vmcnt(14)\nds_write_b32 v73, v75\ns_waitcnt lgkmcnt(11)\nQmvva 92,0,112\n"
"Qtv 16,50,4608\ns_add_u32 s88, s71, 43008\nQmvva 92,20,140\nbuffer_store_dwordx4 v[104:107], v60, s[56:59], s88 offen offset:0 sc1\nQmvva 252,0,116\nQtv 18,50,5632\nQmvva 252,20,144\nQtv 36,50,16896\n"
"Qmvva 228,0,120\nQtv 38,50,17920\nQmvva 228,20,148\nQda 124,47,0\nQmvva 124,4,112\nQda 152,47,12288\nQmvva 124,24,140\nds_read_b32 v66, v74\n"
"Qmvva 100,4,116\nQda 128,47,1024\nQmvva 100,24,144\nQda 156,47,13312\nQmvva 108,4,120\nds_write_b128 v44, v[200:203] offset:49152\nQmvva 108,24,148\ns_waitcnt lgkmcnt(3)\n"
"v_cmp_gt_u32 vcc, v75, v66\nQmvva 156,8,112\ns_cbranch_vccnz .Lso22\n.Lro22:\nQmvva 156,28,140\nQda 132,47,2048\ns_add_u32 m0, s91, 73728\ns_mov_b32 s94, 258048\n"
"Qmvva 132,8,116\nbuffer_load_dwordx4 v60, s[48:51], s94 offen lds\nQmvva 132,28,144\nQda 160,47,14336\nQmvva 140,8,120\nQda 136,47,3072\nQmvva 140,28,148\nQda 164,47,15360\n"
"s_waitcnt lgkmcnt(10)\nQmvva 188,12,112\nbuffer_load_dwordx4 v60, s[48:51], s94 offen offset:1024 lds\nQmvva 188,32,140\nQmvva 164,12,116\nQmvva 164,32,144\nQmvva 172,12,120\nds_write_b128 v44, v[232:235] offset:50176\n"
"Qmvva 172,32,148\nbuffer_load_dwordx4 v60, s[48:51], s94 offen offset:2048 lds\nQmvva 220,16,112\nQda 112,47,33792\nQmvva 220,36,140\nQda 140,47,46080\nQmvva 196,16,116\nQda 116,47,34816\n"
"Qmvva 196,36,144\nQda 144,47,47104\ns_mov_b32 s95, 738304\nQmvva 204,16,120\nbuffer_load_dwordx4 v61, s[52:55], s95 offen offset:3072 lds\nv_add_u32 v75, 1, v75\nQmvva 204,36,148\ns_waitcnt vmcnt(11)\n"
"ds_write_b32 v73, v75\ns_waitcnt lgkmcnt(6)\nQmvva 236,0,124\nQda 120,47,35840\nQmvva 236,20,152\nbuffer_store_dwordx4 v[136:139], v60, s[56:59], s88 offen offset:1024 sc1\ns_add_u32 m0, s91, 77824\ns_mov_b32 s95, 741504\n"
"Qmava 80,0,128\nbuffer_load_dwordx4 v62, s[52:55], s95 offen lds\nQmava 80,20,156\nQda 148,47,48128\nQmava 100,0,132\nds_write_b128 v44, v[212:215] offset:51200\nQmava 100,20,160\nQmava 184,0,136\n"
"Qtv 0,50,36864\nQmava 184,20,164\nds_read_b32 v66, v74\ns_mov_b32 s95, 760896\nQmava 64,4,124\nbuffer_load_dwordx4 v61, s[52:55], s95 offen offset:1024 lds\nQmava 64,24,152\nQtv 2,50,38400\n"
"Qmava 84,4,128\nQtv 20,50,49152\nQmava 84,24,156\ns_waitcnt lgkmcnt(2)\nv_cmp_gt_u32 vcc, v75, v66\nQmava 104,4,132\ns_cbranch_vccnz .Lso23\n.Lro23:\n"
"Qmava 104,24,160\nQtv 22,50,50688\ns_add_u32 m0, s91, 98304\ns_mov_b32 s94, 261120\nQmava 188,4,136\nbuffer_load_dwordx4 v60, s[48:51], s94 offen lds\nQmava 188,24,164\nQtv 4,50,37376\n"
"Qmava 68,8,124\nds_write_b128 v44, v[244:247] offset:52224\nQmava 68,28,152\nQtv 6,50,38912\nv_accvgpr_read_b32 v80, a64\nQmava 88,8,128\nQtv 24,50,49664\nv_accvgpr_read_b32 v81, a65\n"
"Qmava 88,28,156\nQtv 26,50,51200\nv_accvgpr_read_b32 v82, a66\nQmava 108,8,132\nbuffer_load_dwordx4 v60, s[48:51], s94 offen offset:1024 lds\nQmava 108,28,160\nv_accvgpr_read_b32 v83, a67\nQmava 192,8,136\n"
"Qtv 8,50,37888\nQmava 192,28,164\nQtv 10,50,39424\nQmava 72,12,124\nQtv 28,50,50176\nv_accvgpr_read_b32 v112, a68\nQmava 72,32,152\nQtv 30,50,51712\n"
"v_accvgpr_read_b32 v113, a69\nQmava 92,12,128\nbuffer_load_dwordx4 v60, s[48:51], s94 offen offset:2048 lds\nQmava 92,32,156\nv_accvgpr_read_b32 v114, a70\nv_accvgpr_read_b32 v115, a71\nQmava 176,12,132\nQmava 176,32,160\n"
"Qmava 196,12,136\nQtv 12,48,4096\nQmava 196,32,164\nQtv 14,48,5120\nQmava 76,16,124\nQtv 32,48,16384\nv_accvgpr_read_b32 v144, a72\nQmava 76,36,152\n"
"buffer_load_dwordx4 v60, s[48:51], s94 offen offset:3072 lds\nQmava 96,16,128\nQtv 34,48,17408\nv_accvgpr_read_b32 v145, a73\nQmava 96,36,156\nv_accvgpr_read_b32 v146, a74\nv_accvgpr_read_b32 v147, a75\nQmava 180,16,132\n"
"Qmava 180,36,160\nQmava 200,16,136\nQtv 16,48,4608\nv_add_u32 v75, 1, v75\nQmava 200,36,164\ns_waitcnt vmcnt(14)\nds_write_b32 v73, v75\ns_add_u32 m0, s91, 102400\n"
"s_mov_b32 s95, 741568\ns_waitcnt lgkmcnt(10)\nQmvva 92,0,112\nbuffer_load_dwordx4 v61, s[52:55], s95 offen lds\nQmvva 92,20,140\nbuffer_store_dwordx4 v[168:171], v60, s[56:59], s88 offen offset:2048 sc1\nQmvva 252,0,116\nQtv 18,48,5632\n"
"v_accvgpr_read_b32 v176, a76\nQmvva 252,20,144\nQtv 36,48,16896\nv_accvgpr_read_b32 v177, a77\nQmvva 228,0,120\nQtv 38,48,17920\ns_mov_b32 s95, 761024\nQmvva 228,20,148\n"
"buffer_load_dwordx4 v61, s[52:55], s95 offen offset:1024 lds\nQmvva 124,4,112\nQda 124,45,0\nv_accvgpr_read_b32 v178, a78\nQmvva 124,24,140\nds_read_b32 v66, v74\nv_accvgpr_read_b32 v179, a79\nQmvva 100,4,116\n"
"Qda 152,45,12288\nQmvva 100,24,144\nQda 128,45,1024\nQmvva 108,4,120\nQda 156,45,13312\nQmvva 108,24,148\ns_waitcnt lgkmcnt(3)\nv_cmp_gt_u32 vcc, v75, v66\n"
"Qmvva 156,8,112\ns_cbranch_vccnz .Lso24\n.Lro24:\nQmvva 156,28,140\nQda 132,45,2048\ns_add_u32 m0, s91, 132096\ns_mov_b32 s94, 272384\nQmvva 132,8,116\n"
"buffer_load_dwordx4 v60, s[48:51], s94 offen lds\nQmvva 132,28,144\nQda 160,45,14336\nQmvva 140,8,120\nQda 136,45,3072\nQmvva 140,28,148\nQda 164,45,15360\ns_waitcnt lgkmcnt(9)\n"
"Qmvva 188,12,112\nbuffer_load_dwordx4 v60, s[48:51], s94 offen offset:1024 lds\nQmvva 188,32,140\nQmvva 164,12,116\nQmvva 164,32,144\nQmvva 172,12,120\nQmvva 172,32,148\nbuffer_load_dwordx4 v60, s[48:51], s94 offen offset:2048 lds\n"
"Qmvva 220,16,112\nQda 112,45,24576\nQmvva 220,36,140\nQda 140,45,36864\nQmvva 196,16,116\nQda 116,45,25600\nQmvva 196,36,144\nQda 144,45,37888\n"
"s_mov_b32 s95, 779264\nQmvva 204,16,120\nbuffer_load_dwordx4 v61, s[52:55], s95 offen offset:3072 lds\nv_add_u32 v75, 1, v75\nQmvva 204,36,148\ns_waitcnt vmcnt(19)\nds_write_b32 v73, v75\ns_waitcnt lgkmcnt(5)\n"
"Qmvva 236,0,124\nQda 120,45,26624\nQmvva 236,20,152\nbuffer_store_dwordx4 v[200:203], v60, s[56:59], s88 offen offset:3072 sc1\ns_add_u32 m0, s91, 136192\ns_mov_b32 s95, 782464\nQmava 80,0,128\nbuffer_load_dwordx4 v62, s[52:55], s95 offen lds\n"
"Qmava 80,20,156\nQda 148,45,38912\nQmava 100,0,132\nQmava 100,20,160\nQmava 184,0,136\nQtv 0,48,27648\nQmava 184,20,164\nds_read_b32 v66, v74\n"
"s_mov_b32 s95, 801856\nQmvva 80,4,124\nbuffer_load_dwordx4 v61, s[52:55], s95 offen offset:1024 lds\nQmvva 80,24,152\nQtv 2,48,29184\nQmava 84,4,128\nQtv 20,48,39936\nv_accvgpr_read_b32 v208, a80\n"
"Qmava 84,24,156\ns_waitcnt lgkmcnt(2)\nv_cmp_gt_u32 vcc, v75, v66\nQmava 104,4,132\ns_cbranch_vccnz .Lso25\n.Lro25:\nv_accvgpr_read_b32 v209, a81\nQmava 104,24,160\n"
"Qtv 22,48,41472\ns_add_u32 m0, s91, 0\ns_mov_b32 s94, 275456\nQmava 188,4,136\nbuffer_load_dwordx4 v60, s[48:51], s94 offen lds\nQmava 188,24,164\nQtv 4,48,28160\nv_accvgpr_read_b32 v210, a82\n"
"Qmvva 112,8,124\nQtv 6,48,29696\nv_accvgpr_read_b32 v211, a83\nQmvva 112,28,152\nQtv 24,48,40448\nQmava 88,8,128\nQtv 26,48,41984\nv_accvgpr_read_b32 v240, a84\n"
"Qmava 88,28,156\nv_accvgpr_read_b32 v241, a85\nv_accvgpr_read_b32 v242, a86\nQmava 108,8,132\nbuffer_load_dwordx4 v60, s[48:51], s94 offen offset:1024 lds\nQmava 108,28,160\nv_accvgpr_read_b32 v243, a87\nQmava 192,8,136\n"
"Qtv 8,48,28672\nQmava 192,28,164\nQtv 10,48,30208\nQmvva 144,12,124\nQtv 28,48,40960\nQmvva 144,32,152\nQtv 30,48,42496\nQmava 92,12,128\n"
"buffer_load_dwordx4 v60, s[48:51], s94 offen offset:2048 lds\nQmava 92,32,156\nv_accvgpr_read_b32 v84, a88\nv_accvgpr_read_b32 v85, a89\nQmava 176,12,132\nv_accvgpr_read_b32 v86, a90\nv_accvgpr_read_b32 v87, a91\nQmava 176,32,160\n"
"Qmava 196,12,136\nQtv 12,49,4096\nQmava 196,32,164\nQtv 14,49,5120\nQmvva 176,16,124\nQtv 32,49,16384\nQmvva 176,36,152\nbuffer_load_dwordx4 v60, s[48:51], s94 offen offset:3072 lds\n"
"Qmava 96,16,128\nQtv 34,49,17408\nv_accvgpr_read_b32 v88, a92\nQmava 96,36,156\nv_accvgpr_read_b32 v89, a93\nv_accvgpr_read_b32 v90, a94\nQmava 180,16,132\nv_accvgpr_read_b32 v91, a95\n"
"Qmava 180,36,160\nQmava 200,16,136\nQtv 16,49,4608\nv_add_u32 v75, 1, v75\nQmava 200,36,164\ns_waitcnt vmcnt(18)\nds_write_b32 v73, v75\ns_add_u32 m0, s91, 4096\n"
"s_mov_b32 s95, 782528\ns_waitcnt lgkmcnt(10)\nQmvva 92,0,112\nbuffer_load_dwordx4 v61, s[52:55], s95 offen lds\ns_add_u32 s88, s71, 47104\nQmvva 92,20,140\nbuffer_store_dwordx4 v[232:235], v60, s[56:59], s88 offen offset:0 sc1\nQmvva 252,0,116\n"
"Qtv 18,49,5632\nQmvva 252,20,144\nQtv 36,49,16896\nQmvva 228,0,120\nQtv 38,49,17920\ns_mov_b32 s95, 801984\nQmvva 228,20,148\nbuffer_load_dwordx4 v61, s[52:55], s95 offen offset:1024 lds\n"
"Qmvva 124,4,112\nQda 124,46,0\nQmvva 124,24,140\nds_read_b32 v66, v74\nQmvva 100,4,116\nQda 152,46,12288\nQmvva 100,24,144\nQda 128,46,1024\n"
"Qmvva 108,4,120\nQda 156,46,13312\nv_add_u32 v67, 4, v65\nQmvva 108,24,148\ns_waitcnt lgkmcnt(3)\nv_cmp_gt_u32 vcc, v75, v66\nQmvva 156,8,112\ns_cbranch_vccnz .Lso26\n"
".Lro26:\nQmvva 156,28,140\nbuffer_store_dword v67, v77, s[24:27], 0 offen sc1\ns_add_u32 m0, s91, 24576\ns_mov_b32 s94, 344064\nQmvva 132,8,116\nbuffer_load_dwordx4 v60, s[48:51], s94 offen lds\nQmvva 132,28,144\n"
"Qda 132,46,2048\nQmvva 140,8,120\nQda 160,46,14336\nQmvva 140,28,148\nQda 136,46,3072\ns_waitcnt lgkmcnt(8)\nQmvva 188,12,112\nbuffer_load_dwordx4 v60, s[48:51], s94 offen offset:1024 lds\n"
"Qmvva 188,32,140\nQda 164,46,15360\nQmvva 164,12,116\nQmvva 164,32,144\nQmvva 172,12,120\nQmvva 172,32,148\nbuffer_load_dwordx4 v60, s[48:51], s94 offen offset:2048 lds\nQmvva 220,16,112\n"
"Qda 112,46,24576\nQmvva 220,36,140\nQda 140,46,36864\nQmvva 196,16,116\nQda 116,46,25600\nQmvva 196,36,144\nQda 144,46,37888\ns_mov_b32 s95, 984064\n"
"Qmvva 204,16,120\nbuffer_load_dwordx4 v61, s[52:55], s95 offen offset:3072 lds\nv_add_u32 v75, 1, v75\nQmvva 204,36,148\ns_waitcnt vmcnt(19)\nds_write_b32 v73, v75\ns_waitcnt lgkmcnt(5)\nQmvva 236,0,124\n"
"Qda 120,46,26624\nQmvva 236,20,152\nbuffer_store_dwordx4 v[212:215], v60, s[56:59], s88 offen offset:1024 sc1\ns_add_u32 m0, s91, 28672\ns_mov_b32 s95, 987264\nQmvva 208,0,128\nbuffer_load_dwordx4 v62, s[52:55], s95 offen lds\nQmvva 208,20,156\n"
"Qda 148,46,38912\nQmava 100,0,132\nQmava 100,20,160\nQmava 184,0,136\nQtv 0,49,27648\nQmava 184,20,164\nds_read_b32 v66, v74\ns_mov_b32 s95, 1006656\n"
"Qmvva 80,4,124\nbuffer_load_dwordx4 v61, s[52:55], s95 offen offset:1024 lds\nQmvva 80,24,152\nQtv 2,49,29184\nQmvva 240,4,128\nQtv 20,49,39936\nQmvva 240,24,156\ns_waitcnt lgkmcnt(2)\n"
"v_cmp_gt_u32 vcc, v75, v66\nQmava 104,4,132\ns_cbranch_vccnz .Lso27\n.Lro27:\nv_accvgpr_read_b32 v152, a100\nQmava 104,24,160\nQtv 22,49,41472\ns_add_u32 m0, s91, 49152\n"
".Lpg17:\ns_cbranch_execz .Lpg21\ns_mov_b32 s94, 347136\nQmava 188,4,136\nbuffer_load_dwordx4 v60, s[48:51], s94 offen lds\nQmava 188,24,164\nQtv 4,49,28160\nv_accvgpr_read_b32 v153, a101\n"
"Qmvva 112,8,124\nQtv 6,49,29696\nv_accvgpr_read_b32 v154, a102\nQmvva 112,28,152\nQtv 24,49,40448\nv_accvgpr_read_b32 v155, a103\nQmvva 84,8,128\nQtv 26,49,41984\n"
"Qmvva 84,28,156\nQmava 108,8,132\nbuffer_load_dwordx4 v60, s[48:51], s94 offen offset:1024 lds\nQmava 108,28,160\nv_accvgpr_read_b32 v184, a104\nv_accvgpr_read_b32 v185, a105\nQmava 192,8,136\nQtv 8,49,28672\n"
"v_accvgpr_read_b32 v186, a106\nQmava 192,28,164\nQtv 10,49,30208\nv_accvgpr_read_b32 v187, a107\nQmvva 144,12,124\nQtv 28,49,40960\nQmvva 144,32,152\nQtv 30,49,42496\n"
"Qmvva 88,12,128\nbuffer_load_dwordx4 v60, s[48:51], s94 offen offset:2048 lds\nQmvva 88,32,156\nQmava 176,12,132\nv_accvgpr_read_b32 v216, a108\nv_accvgpr_read_b32 v217, a109\nQmava 176,32,160\nv_accvgpr_read_b32 v218, a110\n"
"v_accvgpr_read_b32 v219, a111\nQmava 196,12,136\nQtv 12,50,4096\nQmava 196,32,164\nQtv 14,50,5120\nQmvva 176,16,124\nQtv 32,50,16384\nQmvva 176,36,152\n"
"buffer_load_dwordx4 v60, s[48:51], s94 offen offset:3072 lds\nQmava 96,16,128\nQtv 34,50,17408\nQmava 96,36,156\nQmava 180,16,132\nQmava 180,36,160\nQmava 200,16,136\nQtv 16,50,4608\n"
"v_add_u32 v75, 1, v75\nQmava 200,36,164\ns_waitcnt vmcnt(19)\nds_write_b32 v73, v75\ns_add_u32 m0, s91, 53248\ns_mov_b32 s95, 987328\ns_waitcnt lgkmcnt(10)\nQmvva 92,0,112\n"
"buffer_load_dwordx4 v61, s[52:55], s95 offen lds\nQmvva 92,20,140\nbuffer_store_dwordx4 v[244:247], v60, s[56:59], s88 offen offset:2048 sc1\nQmvva 252,0,116\nQtv 18,50,5632\nv_accvgpr_read_b32 v120, a96\nQmvva 252,20,144\nQtv 36,50,16896\n"
"v_accvgpr_read_b32 v121, a97\nQmvva 228,0,120\nQtv 38,50,17920\ns_mov_b32 s95, 1006784\nQmvva 228,20,148\nbuffer_load_dwordx4 v61, s[52:55], s95 offen offset:1024 lds\nQmvva 124,4,112\nQda 124,47,0\n"
"v_accvgpr_read_b32 v122, a98\nQmvva 124,24,140\nds_read_b32 v66, v74\nv_accvgpr_read_b32 v123, a99\nQmvva 100,4,116\nQda 152,47,12288\ns_mov_b32 s40, %17\nQmvva 100,24,144\n"
"Qda 128,47,1024\ns_and_b32 s41, %18, 0xffff\nQmvva 108,4,120\nQda 156,47,13312\ns_mov_b32 s42, 571200\nQmvva 108,24,148\ns_waitcnt lgkmcnt(3)\nv_cmp_gt_u32 vcc, v75, v66\n"
"Qmvva 156,8,112\ns_cbranch_vccnz .Lso28\n.Lro28:\ns_lshr_b32 s68, %19, 2\nQmvva 156,28,140\nQda 132,47,2048\ns_add_u32 m0, s91, 73728\ns_mov_b32 s94, 358400\n"
"Qmvva 132,8,116\nbuffer_load_dwordx4 v60, s[48:51], s94 offen lds\nQmvva 132,28,144\nQda 160,47,14336\ns_mul_i32 s65, s68, 286720\nQmvva 140,8,120\nQda 136,47,3072\ns_sub_u32 s60, 286720, s65\n"
"Qmvva 140,28,148\nQda 164,47,15360\ns_mul_i32 s68, s68, 114688\ns_waitcnt lgkmcnt(9)\nQmvva 188,12,112\nbuffer_load_dwordx4 v60, s[48:51], s94 offen offset:1024 lds\nQmvva 188,32,140\ns_add_u32 s64, s98, 57344\n"
"s_sub_u32 s64, s64, s68\nQmvva 164,12,116\ns_lshl_b32 s68, s93, 5\ns_add_u32 s68, s68, %10\nQmvva 164,32,144\nv_bfe_u32 v54, v60, 4, 4\nv_mul_u32_u24 v54, 336, v54\nQmvva 172,12,120\n"
"v_lshrrev_b32 v55, 8, v60\nv_lshlrev_b32 v56, 3, v55\nQmvva 172,32,148\nbuffer_load_dwordx4 v60, s[48:51], s94 offen offset:2048 lds\nQmvva 220,16,112\nQda 112,47,33792\nv_add3_u32 v63, v54, v56, s68\nQmvva 220,36,140\n"
"Qda 140,47,46080\ns_lshl_b32 s68, s70, 2\nQmvva 196,16,116\nQda 116,47,34816\nv_add_u32 v55, s68, v55\nQmvva 196,36,144\nQda 144,47,47104\ns_mov_b32 s95, 1025024\n"
"Qmvva 204,16,120\nbuffer_load_dwordx4 v61, s[52:55], s95 offen offset:3072 lds\nv_add_u32 v75, 1, v75\nQmvva 204,36,148\ns_waitcnt vmcnt(19)\nds_write_b32 v73, v75\ns_waitcnt lgkmcnt(5)\nQmvva 236,0,124\n"
"Qda 120,47,35840\ns_and_b32 s69, %19, 1\nQmvva 236,20,152\nQda 148,47,48128\ns_add_u32 m0, s91, 77824\ns_mov_b32 s95, 1028224\nQmvva 208,0,128\nbuffer_load_dwordx4 v62, s[52:55], s95 offen lds\n"
"Qmvva 208,20,156\ns_lshl_b32 s68, s69, 6\nv_and_b32 v54, 255, v60\nQmvva 152,0,132\nv_add_u32 v54, s68, v54\nv_mul_u32_u24 v56, 336, v55\nQmvva 152,20,160\nv_add3_u32 v64, v54, v56, %10\n"
"v_mul_u32_u24 v56, 2560, v55\nQmava 184,0,136\nQtv 0,50,36864\nv_add_u32 v70, v54, v56\nQmava 184,20,164\nds_read_b32 v66, v74\ns_mov_b32 s95, 1047616\nQmvva 80,4,124\n"
"buffer_load_dwordx4 v61, s[52:55], s95 offen offset:1024 lds\nQmvva 80,24,152\nQtv 2,50,38400\ns_lshl_b32 s68, s70, 4\nQmvva 240,4,128\nQtv 20,50,49152\nv_lshrrev_b32 v55, 6, v60\nQmvva 240,24,156\n"
"s_waitcnt lgkmcnt(2)\nv_cmp_gt_u32 vcc, v75, v66\nQmvva 184,4,132\ns_cbranch_vccnz .Lso29\n.Lro29:\nv_add_u32 v55, s68, v55\nQmvva 184,24,160\nQtv 22,50,50688\n"
"s_add_u32 m0, s91, 98304\ns_mov_b32 s94, 361472\nQmava 188,4,136\nbuffer_load_dwordx4 v60, s[48:51], s94 offen lds\nQmava 188,24,164\nQtv 4,50,37376\nv_accvgpr_read_b32 v96, a184\nQmvva 112,8,124\n"
"Qtv 6,50,38912\nv_accvgpr_read_b32 v97, a185\nQmvva 112,28,152\nQtv 24,50,49664\nv_accvgpr_read_b32 v98, a186\nQmvva 84,8,128\nQtv 26,50,51200\nv_accvgpr_read_b32 v99, a187\n"
"Qmvva 84,28,156\ns_xor_b32 s69, s69, 1\ns_lshl_b32 s68, s69, 8\nQmvva 216,8,132\nbuffer_load_dwordx4 v60, s[48:51], s94 offen offset:1024 lds\nQmvva 216,28,160\nv_and_b32 v54, 63, v60\nv_add_u32 v54, s68, v54\n"
"Qmava 192,8,136\nQtv 8,50,37888\nv_accvgpr_read_b32 v128, a188\nQmava 192,28,164\nQtv 10,50,39424\nv_accvgpr_read_b32 v129, a189\nQmvva 144,12,124\nQtv 28,50,50176\n"
"v_accvgpr_read_b32 v130, a190\nQmvva 144,32,152\nQtv 30,50,51712\nv_accvgpr_read_b32 v131, a191\nQmvva 88,12,128\nbuffer_load_dwordx4 v60, s[48:51], s94 offen offset:2048 lds\nQmvva 88,32,156\nv_mul_u32_u24 v56, 336, v55\n"
"v_add3_u32 v71, v54, v56, %10\nQmava 176,12,132\nv_mul_u32_u24 v56, 2560, v55\nv_add_u32 v72, v54, v56\nQmava 176,32,160\nQmava 196,12,136\nQtv 12,48,4096\nQmava 196,32,164\n"
"Qtv 14,48,5120\nQmvva 176,16,124\nQtv 32,48,16384\nQmvva 176,36,152\nbuffer_load_dwordx4 v60, s[48:51], s94 offen offset:3072 lds\nQmvva 120,16,128\nQtv 34,48,17408\nQmvva 120,36,156\n"
"Qmava 180,16,132\nv_accvgpr_read_b32 v248, a176\nv_accvgpr_read_b32 v249, a177\nQmava 180,36,160\nv_accvgpr_read_b32 v250, a178\nv_accvgpr_read_b32 v251, a179\nQmava 200,16,136\nQtv 16,48,4608\n"
"v_add_u32 v75, 1, v75\nQmava 200,36,164\ns_waitcnt vmcnt(17)\nds_write_b32 v73, v75\ns_add_u32 m0, s91, 102400\ns_mov_b32 s95, 1028288\ns_waitcnt lgkmcnt(10)\nQmvva 92,0,112\n"
"buffer_load_dwordx4 v61, s[52:55], s95 offen lds\nQmvva 92,20,140\nQtv 18,48,5632\nQmvva 252,0,116\nQtv 36,48,16896\nQmvva 252,20,144\nQtv 38,48,17920\nQmvva 228,0,120\n"
"Qda 124,45,0\ns_mov_b32 s95, 1047744\nQmvva 228,20,148\nbuffer_load_dwordx4 v61, s[52:55], s95 offen offset:1024 lds\nQmvva 124,4,112\nQda 152,45,12288\nv_accvgpr_read_b32 v116, a180\nQmvva 124,24,140\n"
"ds_read_b32 v66, v74\nv_accvgpr_read_b32 v117, a181\nQmvva 100,4,116\nQda 128,45,1024\nv_accvgpr_read_b32 v118, a182\nQmvva 100,24,144\nQda 156,45,13312\nv_accvgpr_read_b32 v119, a183\n"
"Qmvva 108,4,120\nQda 132,45,2048\nQmvva 108,24,148\ns_waitcnt lgkmcnt(3)\nv_cmp_gt_u32 vcc, v75, v66\nQmvva 156,8,112\ns_cbranch_vccnz .Lso30\n.Lro30:\n"
"Qmvva 156,28,140\nQda 160,45,14336\ns_add_u32 m0, s91, 132096\ns_mov_b32 s94, 372736\nQmvva 132,8,116\nbuffer_load_dwordx4 v60, s[48:51], s94 offen lds\nQmvva 132,28,144\nQda 136,45,3072\n"
"Qmvva 140,8,120\nQda 164,45,15360\nQmvva 140,28,148\ns_waitcnt lgkmcnt(9)\nQmvva 188,12,112\nbuffer_load_dwordx4 v60, s[48:51], s94 offen offset:1024 lds\nQmvva 188,32,140\nQmvva 164,12,116\n"
"Qmvva 164,32,144\nQmvva 172,12,120\nQmvva 172,32,148\nbuffer_load_dwordx4 v60, s[48:51], s94 offen offset:2048 lds\nQmvva 220,16,112\nQda 112,45,24576\nQmvva 220,36,140\nQda 140,45,36864\n"
"Qmvva 196,16,116\nQda 116,45,25600\nQmvva 196,36,144\nQda 144,45,37888\ns_mov_b32 s95, 1065984\nQmvva 204,16,120\nbuffer_load_dwordx4 v61, s[52:55], s95 offen offset:3072 lds\nv_add_u32 v75, 1, v75\n"
"Qmvva 204,36,148\ns_waitcnt vmcnt(16)\nds_write_b32 v73, v75\ns_waitcnt lgkmcnt(5)\nQmvva 236,0,124\nbuffer_load_dword v78, v76, s[24:27], 0 offen sc1\nQmvva 236,20,152\nQda 120,45,26624\n"
"s_add_u32 m0, s91, 136192\ns_mov_b32 s95, 1069184\nQmvva 208,0,128\nbuffer_load_dwordx4 v62, s[52:55], s95 offen lds\nQmvva 208,20,156\nQda 148,45,38912\nQmvva 152,0,132\nQmvva 152,20,160\n"
"Qmvva 96,0,136\nQtv 0,48,27648\nQmvva 96,20,164\nds_read_b32 v66, v74\ns_mov_b32 s95, 1088576\nQmvva 80,4,124\nbuffer_load_dwordx4 v61, s[52:55], s95 offen offset:1024 lds\nQmvva 80,24,152\n"
"Qtv 2,48,29184\nQmvva 240,4,128\nQtv 20,48,39936\nQmvva 240,24,156\ns_waitcnt lgkmcnt(2)\nv_cmp_gt_u32 vcc, v75, v66\nQmvva 184,4,132\ns_cbranch_vccnz .Lso31\n"
".Lro31:\nQmvva 184,24,160\nQtv 22,48,41472\ns_add_u32 m0, s91, 0\ns_mov_b32 s94, 375808\nQmvva 128,4,136\nbuffer_load_dwordx4 v60, s[48:51], s94 offen lds\nQmvva 128,24,164\n"
"Qtv 4,48,28160\nQmvva 112,8,124\nQtv 6,48,29696\nQmvva 112,28,152\nQtv 24,48,40448\nQmvva 84,8,128\nQtv 26,48,41984\nQmvva 84,28,156\n"
"Qmvva 216,8,132\nbuffer_load_dwordx4 v60, s[48:51], s94 offen offset:1024 lds\nQmvva 216,28,160\nQmava 192,8,136\nQtv 8,48,28672\nQmava 192,28,164\nQtv 10,48,30208\nQmvva 144,12,124\n"
"Qtv 28,48,40960\nQmvva 144,32,152\nQtv 30,48,42496\nQmvva 88,12,128\nbuffer_load_dwordx4 v60, s[48:51], s94 offen offset:2048 lds\nQmvva 88,32,156\nQmvva 248,12,132\nQmvva 248,32,160\n"
"Qmava 196,12,136\nQtv 12,49,4096\nv_accvgpr_read_b32 v160, a192\nQmava 196,32,164\nQtv 14,49,5120\nv_accvgpr_read_b32 v161, a193\nQmvva 176,16,124\nQtv 32,49,16384\n"
"v_accvgpr_read_b32 v162, a194\nQmvva 176,36,152\nbuffer_load_dwordx4 v60, s[48:51], s94 offen offset:3072 lds\ns_mov_b32 s31, 387072\nQmvva 120,16,128\nbuffer_load_dwordx4 a[208:211], v60, s[48:51], s31 offen offset:0 sc1\nQmvva 120,36,156\nQtv 34,49,17408\n"
"v_accvgpr_read_b32 v163, a195\nQmvva 116,16,132\nQmvva 116,36,160\nQmava 200,16,136\nQtv 16,49,4608\nv_add_u32 v75, 1, v75\nQmava 200,36,164\ns_waitcnt vmcnt(18)\n"
"ds_write_b32 v73, v75\ns_add_u32 m0, s91, 4096\ns_mov_b32 s95, 1069248\ns_waitcnt lgkmcnt(10)\nQmvva 92,0,112\nbuffer_load_dwordx4 v61, s[52:55], s95 offen lds\nQmvva 92,20,140\nQtv 18,49,5632\n"
"v_accvgpr_read_b32 v192, a196\nQmvva 252,0,116\nQtv 36,49,16896\nv_accvgpr_read_b32 v193, a197\nQmvva 252,20,144\nQtv 38,49,17920\nv_accvgpr_read_b32 v194, a198\nQmvva 228,0,120\n"
"buffer_load_dwordx4 a[212:215], v60, s[48:51], s31 offen offset:1024 sc1\ns_mov_b32 s95, 1088704\nQmvva 228,20,148\nbuffer_load_dwordx4 v61, s[52:55], s95 offen offset:1024 lds\nQmvva 124,4,112\nQda 124,46,0\nv_lshrrev_b32 v62, 9, v60\nv_mul_u32_u24 v62, 20160, v62\n"
"v_add_u32 v62, v61, v62\nQmvva 124,24,140\nds_read_b32 v66, v74\nv_accvgpr_read_b32 v195, a199\nQmvva 100,4,116\nQda 152,46,12288\nv_accvgpr_read_b32 v224, a200\nQmvva 100,24,144\n"
"Qda 128,46,1024\nv_accvgpr_read_b32 v225, a201\nQmvva 108,4,120\nQda 156,46,13312\nv_accvgpr_read_b32 v226, a202\nQmvva 108,24,148\ns_waitcnt lgkmcnt(3)\nv_cmp_gt_u32 vcc, v75, v66\n"
"Qmvva 156,8,112\ns_cbranch_vccnz .Lso32\n.Lro32:\nv_accvgpr_read_b32 v227, a203\nQmvva 156,28,140\nQda 132,46,2048\nQmvva 132,8,116\nbuffer_load_dwordx4 a[216:219], v60, s[48:51], s31 offen offset:2048 sc1\n"
"s_add_u32 m0, s96, 24576\ns_mov_b32 s95, 1110016\nQmvva 132,28,144\nbuffer_load_dwordx4 v61, s[52:55], s95 offen lds\nQmvva 140,8,120\nQda 160,46,14336\nQmvva 140,28,148\nQda 136,46,3072\n"
"s_waitcnt lgkmcnt(8)\nQmvva 188,12,112\nQda 164,46,15360\nQmvva 188,32,140\nQmvva 164,12,116\nQmvva 164,32,144\nQmvva 172,12,120\nQmvva 172,32,148\n"
"Qmvva 220,16,112\nbuffer_load_dwordx4 a[220:223], v60, s[48:51], s31 offen offset:3072 sc1\ns_mov_b32 s95, 1109120\nQmvva 220,36,140\nbuffer_load_dwordx4 v61, s[52:55], s95 offen offset:1024 lds\nQmvva 196,16,116\nQda 112,46,24576\nQmvva 196,36,144\n"
"Qda 140,46,36864\nQmvva 204,16,120\nQda 116,46,25600\nv_add_u32 v75, 1, v75\nQmvva 204,36,148\ns_waitcnt vmcnt(19)\nds_write_b32 v73, v75\ns_waitcnt lgkmcnt(4)\n"
"Qmvva 236,0,124\ns_waitcnt vmcnt(14)\nv_sub_u32 v66, v78, v65\nv_cmp_gt_u32 vcc, 2, v66\ns_and_b64 vcc, vcc, s[28:29]\ns_cbranch_scc1 .Lf2_w\n.Lf2_r:\nQmvva 236,20,152\n"
"Qda 144,46,37888\nQmvva 208,0,128\nQda 120,46,26624\nQmvva 208,20,156\nQda 148,46,38912\ns_mov_b32 s31, 391168\nQmvva 152,0,132\nbuffer_load_dwordx4 a[224:227], v60, s[48:51], s31 offen offset:0 sc1\n"
"s_mov_b32 s95, 1108224\nQmvva 152,20,160\nbuffer_load_dwordx4 v62, s[52:55], s95 offen offset:2048 lds\nQmvva 96,0,136\nQtv 0,49,27648\nQmvva 96,20,164\nds_read_b32 v66, v74\nQmvva 80,4,124\n"
"Qtv 2,49,29184\nQmvva 80,24,152\nQtv 20,49,39936\nQmvva 240,4,128\nQtv 22,49,41472\nv_add_u32 v67, 5, v65\nQmvva 240,24,156\ns_waitcnt lgkmcnt(3)\n"
"v_cmp_gt_u32 vcc, v75, v66\nQmvva 184,4,132\ns_cbranch_vccnz .Lso33\n.Lro33:\nQmvva 184,24,160\nbuffer_store_dword v67, v77, s[24:27], 0 offen sc1\nQmvva 128,4,136\nbuffer_load_dwordx4 a[228:231], v60, s[48:51], s31 offen offset:1024 sc1\n"
"s_mov_b32 s95, 1127488\nQmvva 128,24,164\nbuffer_load_dwordx4 v61, s[52:55], s95 offen offset:3072 lds\nQmvva 112,8,124\nQtv 4,49,28160\nQmvva 112,28,152\nQtv 6,49,29696\nQmvva 84,8,128\n"
"Qtv 24,49,40448\nQmvva 84,28,156\nQtv 26,49,41984\nQmvva 216,8,132\nQmvva 216,28,160\nQmvva 160,8,136\nQtv 8,49,28672\nQmvva 160,28,164\n"
"Qtv 10,49,30208\nQmvva 144,12,124\nbuffer_load_dwordx4 a[232:235], v60, s[48:51], s31 offen offset:2048 sc1\n.Lpg18:\ns_cbranch_execz .Lpg22\ns_add_u32 m0, s96, 28672\ns_mov_b32 s95, 1130688\nQmvva 144,32,152\n"
"buffer_load_dwordx4 v61, s[52:55], s95 offen lds\nQmvva 88,12,128\nQtv 28,49,40960\nQmvva 88,32,156\nQtv 30,49,42496\nQmvva 248,12,132\nQmvva 248,32,160\nQmvva 192,12,136\n"
"Qtv 12,50,4096\nQmvva 192,32,164\nQtv 14,50,5120\nQmvva 176,16,124\nQtv 32,50,16384\nQmvva 176,36,152\nQtv 34,50,17408\ns_add_u32 s31, s74, 0\n"
"Qmvva 120,16,128\nbuffer_load_dwordx4 a[236:239], v60, s[48:51], s31 offen offset:0 sc1\ns_add_u32 m0, s96, 45056\ns_add_u32 s95, s67, 4096\nQmvva 120,36,156\nbuffer_load_dwordx4 v61, s[52:55], s95 offen lds\nQmvva 116,16,132\nQmvva 116,36,160\n"
"Qmvva 224,16,136\nQtv 16,50,4608\nv_add_u32 v75, 1, v75\nQmvva 224,36,164\ns_waitcnt vmcnt(21)\nds_write_b32 v73, v75\ns_waitcnt lgkmcnt(10)\nQmvva 92,0,112\n"
"Qtv 18,50,5632\nQmvva 92,20,140\nQtv 36,50,16896\nQmvva 252,0,116\nQtv 38,50,17920\nQmvva 252,20,144\nQda 124,47,0\nQmvva 228,0,120\n"
"buffer_load_dwordx4 a[240:243], v60, s[48:51], s31 offen offset:1024 sc1\ns_add_u32 s95, s67, 3200\nQmvva 228,20,148\nbuffer_load_dwordx4 v61, s[52:55], s95 offen offset:1024 lds\nQmvva 124,4,112\nQda 152,47,12288\nQmvva 124,24,140\nds_read_b32 v66, v74\n"
"Qmvva 100,4,116\nQda 128,47,1024\nQmvva 100,24,144\nQda 156,47,13312\nQmvva 108,4,120\nQda 132,47,2048\nQmvva 108,24,148\ns_waitcnt lgkmcnt(3)\n"
"v_cmp_gt_u32 vcc, v75, v66\nQmvva 156,8,112\ns_cbranch_vccnz .Lso34\n.Lro34:\nQmvva 156,28,140\nQda 160,47,14336\nQmvva 132,8,116\nbuffer_load_dwordx4 a[244:247], v60, s[48:51], s31 offen offset:2048 sc1\n"
"s_add_u32 s95, s67, 2304\nQmvva 132,28,144\nbuffer_load_dwordx4 v62, s[52:55], s95 offen offset:2048 lds\nQmvva 140,8,120\nQda 136,47,3072\nQmvva 140,28,148\nQda 164,47,15360\ns_waitcnt lgkmcnt(9)\n"
"Qmvva 188,12,112\nQmvva 188,32,140\nQmvva 164,12,116\nQmvva 164,32,144\nQmvva 172,12,120\nQmvva 172,32,148\nQmvva 220,16,112\nbuffer_load_dwordx4 a[248:251], v60, s[48:51], s31 offen offset:3072 sc1\n"
"s_add_u32 s95, s67, 21568\nQmvva 220,36,140\nbuffer_load_dwordx4 v61, s[52:55], s95 offen offset:3072 lds\nQmvva 196,16,116\nQda 112,47,33792\nQmvva 196,36,144\nQda 140,47,46080\nQmvva 204,16,120\n"
"Qda 116,47,34816\nv_add_u32 v75, 1, v75\nQmvva 204,36,148\ns_waitcnt vmcnt(19)\nds_write_b32 v73, v75\ns_waitcnt lgkmcnt(4)\nQmvva 236,0,124\nQda 144,47,47104\n"
"Qmvva 236,20,152\nQda 120,47,35840\nQmvva 208,0,128\nQda 148,47,48128\nQmvva 208,20,156\ns_add_u32 s31, s74, 4096\nQmvva 152,0,132\nbuffer_load_dwordx4 a[252:255], v60, s[48:51], s31 offen offset:0 sc1\n"
"s_add_u32 m0, s96, 49152\ns_add_u32 s95, s67, 24768\nQmvva 152,20,160\nbuffer_load_dwordx4 v61, s[52:55], s95 offen lds\nQmvva 96,0,136\nQtv 0,50,36864\nQmvva 96,20,164\nds_read_b32 v66, v74\n"
"Qmvva 80,4,124\nQtv 2,50,38400\nQmvva 80,24,152\nQtv 20,50,49152\nQmvva 240,4,128\nQtv 22,50,50688\nQmvva 240,24,156\ns_waitcnt lgkmcnt(3)\n"
"v_cmp_gt_u32 vcc, v75, v66\nQmvva 184,4,132\ns_cbranch_vccnz .Lso35\n.Lro35:\nQmvva 184,24,160\nQmvva 128,4,136\nbuffer_load_dwordx4 a[168:171], v60, s[48:51], s31 offen offset:1024 sc1\ns_add_u32 m0, s96, 65536\n"
"s_add_u32 s95, s72, 4096\nQmvva 128,24,164\nbuffer_load_dwordx4 v61, s[52:55], s95 offen lds\nQmvva 112,8,124\nQtv 4,50,37376\nQmvva 112,28,152\nQtv 6,50,38912\nQmvva 84,8,128\n"
"Qtv 24,50,49664\nQmvva 84,28,156\nQtv 26,50,51200\nQmvva 216,8,132\nQmvva 216,28,160\nQmvva 160,8,136\nQtv 8,50,37888\nQmvva 160,28,164\n"
"Qtv 10,50,39424\nQmvva 144,12,124\nbuffer_load_dwordx4 a[172:175], v60, s[48:51], s31 offen offset:2048 sc1\ns_add_u32 s95, s72, 3200\nQmvva 144,32,152\nbuffer_load_dwordx4 v61, s[52:55], s95 offen offset:1024 lds\nQmvva 88,12,128\nQtv 28,50,50176\n"
"Qmvva 88,32,156\nQtv 30,50,51712\nQmvva 248,12,132\nQmvva 248,32,160\nQmvva 192,12,136\nQtv 12,48,4096\nQmvva 192,32,164\nQtv 14,48,5120\n"
"Qmvva 176,16,124\nQtv 32,48,16384\nQmvva 176,36,152\nQtv 34,48,17408\ns_add_u32 s31, s75, 0\nQmvva 120,16,128\nbuffer_load_dwordx4 a[0:3], v60, s[48:51], s31 offen offset:0 sc1\ns_add_u32 s95, s72, 2304\n"
"Qmvva 120,36,156\nbuffer_load_dwordx4 v62, s[52:55], s95 offen offset:2048 lds\nQmvva 116,16,132\nQmvva 116,36,160\nQmvva 224,16,136\nQtv 16,48,4608\nv_add_u32 v75, 1, v75\nQmvva 224,36,164\n"
"s_waitcnt vmcnt(16)\nds_write_b32 v73, v75\ns_waitcnt lgkmcnt(10)\nQmvva 92,0,112\nQtv 18,48,5632\nQmvva 92,20,140\nQtv 36,48,16896\nQmvva 252,0,116\n"
"Qtv 38,48,17920\nQmvva 252,20,144\nQda 124,45,0\nQmvva 228,0,120\nbuffer_load_dwordx4 a[4:7], v60, s[48:51], s31 offen offset:1024 sc1\ns_add_u32 s95, s72, 21568\nQmvva 228,20,148\nbuffer_load_dwordx4 v61, s[52:55], s95 offen offset:3072 lds\n"
"Qmvva 124,4,112\nQda 152,45,12288\nQmvva 124,24,140\nds_read_b32 v66, v74\nQmvva 100,4,116\nQda 128,45,1024\nQmvva 100,24,144\nQda 156,45,13312\n"
"Qmvva 108,4,120\nQda 132,45,2048\nQmvva 108,24,148\ns_waitcnt lgkmcnt(3)\nv_cmp_gt_u32 vcc, v75, v66\nQmvva 156,8,112\ns_cbranch_vccnz .Lso36\n.Lro36:\n"
"Qmvva 156,28,140\ns_waitcnt vmcnt(32)\nds_write_b128 v52, a[208:211] offset:0\nQmvva 132,8,116\nQmvva 132,28,144\nbuffer_load_dwordx4 a[8:11], v60, s[48:51], s31 offen offset:2048 sc1\ns_add_u32 m0, s96, 69632\ns_add_u32 s95, s72, 24768\n"
"Qmvva 140,8,120\nbuffer_load_dwordx4 v61, s[52:55], s95 offen lds\nQmvva 140,28,148\ns_waitcnt vmcnt(32)\nds_write_b128 v52, a[212:215] offset:4096\ns_waitcnt lgkmcnt(8)\nQmvva 188,12,112\nQmvva 188,32,140\n"
"Qda 160,45,14336\nQmvva 164,12,116\nQda 136,45,3072\nQmvva 164,32,144\nQda 164,45,15360\nQmvva 172,12,120\nQmvva 172,32,148\nQmvva 220,16,112\n"
"buffer_load_dwordx4 a[12:15], v60, s[48:51], s31 offen offset:3072 sc1\nQmvva 220,36,140\ns_add_u32 m0, s96, 86016\ns_add_u32 s95, s73, 4096\nQmvva 196,16,116\nbuffer_load_dwordx4 v61, s[52:55], s95 offen lds\nQmvva 196,36,144\ns_waitcnt vmcnt(32)\n"
"ds_write_b128 v52, a[216:219] offset:8192\nQmvva 204,16,120\nv_add_u32 v75, 1, v75\nQmvva 204,36,148\ns_waitcnt vmcnt(12)\nds_write_b32 v73, v75\ns_waitcnt lgkmcnt(2)\nQmvva 236,0,124\n"
"buffer_load_dword v78, v76, s[24:27], 0 offen sc1\nQmvva 236,20,152\nQta 84,40,20480\nQta 86,40,23040\nQmvva 208,0,128\nQta 176,42,20480\nQmvva 208,20,156\nQta 178,42,23040\n"
"s_add_u32 s31, s75, 4096\nQmvva 152,0,132\nbuffer_load_dwordx4 a[16:19], v60, s[48:51], s31 offen offset:0 sc1\nQmvva 152,20,160\nQta 88,40,20992\ns_add_u32 s95, s73, 3200\nQmvva 96,0,136\nbuffer_load_dwordx4 v61, s[52:55], s95 offen offset:1024 lds\n"
"Qmvva 96,20,164\nds_read_b32 v66, v74\nQmvva 80,4,124\ns_waitcnt vmcnt(33)\nds_write_b128 v52, a[220:223] offset:12288\nQmvva 80,24,152\nQmvva 240,4,128\nQta 90,40,23552\n"
"Qmvva 240,24,156\ns_waitcnt lgkmcnt(2)\nv_cmp_gt_u32 vcc, v75, v66\nQmvva 184,4,132\ns_cbranch_vccnz .Lso37\n.Lro37:\nQmvva 184,24,160\nQta 180,42,20992\n"
"Qmvva 128,4,136\nbuffer_load_dwordx4 a[20:23], v60, s[48:51], s31 offen offset:1024 sc1\nQmvva 128,24,164\nQta 182,42,23552\ns_add_u32 s95, s73, 2304\nQmvva 112,8,124\nbuffer_load_dwordx4 v62, s[52:55], s95 offen offset:2048 lds\nQmvva 112,28,152\n"
"s_waitcnt vmcnt(33)\nds_write_b128 v52, a[224:227] offset:16384\nQmvva 84,8,128\nQmvva 84,28,156\nQta 92,40,21504\nQmvva 216,8,132\nQta 94,40,24064\nQmvva 216,28,160\n"
"Qta 184,42,21504\nQta 186,42,24064\nQmvva 160,8,136\nQta 96,40,22016\nQmvva 160,28,164\nQta 98,40,24576\nQmvva 144,12,124\nbuffer_load_dwordx4 a[24:27], v60, s[48:51], s31 offen offset:2048 sc1\n"
"Qmvva 144,32,152\nQta 188,42,22016\ns_add_u32 s95, s73, 21568\nQmvva 88,12,128\nbuffer_load_dwordx4 v61, s[52:55], s95 offen offset:3072 lds\nQmvva 88,32,156\ns_waitcnt vmcnt(32)\nds_write_b128 v52, a[228:231] offset:20480\n"
"Qmvva 248,12,132\nQmvva 248,32,160\nQta 190,42,24576\nQmvva 192,12,136\nQta 100,40,22528\nQmvva 192,32,164\nQda 48,53,0\nQmvva 176,16,124\n"
"Qta 102,40,25088\nQmvva 176,36,152\nQta 192,42,22528\ns_add_u32 s31, s77, 0\nQmvva 120,16,128\nbuffer_load_dwordx4 a[28:31], v60, s[48:51], s31 offen offset:0 sc1\nQmvva 120,36,156\nQta 194,42,25088\n"
"s_add_u32 m0, s96, 90112\ns_add_u32 s95, s73, 24768\nQmvva 116,16,132\nbuffer_load_dwordx4 v61, s[52:55], s95 offen lds\nQmvva 116,36,160\ns_waitcnt vmcnt(32)\nds_write_b128 v52, a[232:235] offset:25600\nQmvva 224,16,136\n"
"v_add_u32 v75, 1, v75\nQmvva 224,36,164\nds_write_b32 v73, v75\ns_waitcnt lgkmcnt(2)\nQmvaa 92,84,208\nQtv 0,40,40960\nQmvaa 92,176,48\nQda 52,53,4096\n"
"Qmvaa 124,88,208\nQtv 2,40,43520\nQmvaa 124,180,48\nQtv 20,42,40960\nQmvaa 156,92,208\nbuffer_load_dwordx4 a[32:35], v60, s[48:51], s31 offen offset:1024 sc1\nQmvaa 156,184,48\nQtv 22,42,43520\n"
"s_add_u32 m0, s96, 4096\ns_add_u32 s95, s67, 45056\nQmvaa 188,96,208\nbuffer_load_dwordx4 v61, s[52:55], s95 offen lds\nQmvaa 188,188,48\nds_read_b32 v66, v74\nQmvaa 220,100,208\ns_waitcnt vmcnt(32)\n"
"ds_write_b128 v52, a[236:239] offset:29696\nQmvaa 220,192,48\ns_waitcnt lgkmcnt(5)\nQmvaa 252,84,212\nQtv 4,40,41472\nQmvaa 252,176,52\ns_waitcnt lgkmcnt(2)\nv_cmp_gt_u32 vcc, v75, v66\n"
"Qmvaa 100,88,212\ns_cbranch_vccnz .Lso38\n.Lro38:\nQmvaa 100,180,52\nQda 56,53,8192\nQmvaa 132,92,212\nbuffer_load_dwordx4 a[36:39], v60, s[48:51], s31 offen offset:2048 sc1\nQmvaa 132,184,52\n"
"Qtv 6,40,44032\ns_add_u32 s95, s67, 44160\nQmvaa 164,96,212\nbuffer_load_dwordx4 v61, s[52:55], s95 offen offset:1024 lds\nQmvaa 164,188,52\ns_waitcnt vmcnt(32)\nds_write_b128 v52, a[240:243] offset:33792\nQmvaa 196,100,212\n"
"Qmvaa 196,192,52\nQtv 24,42,41472\ns_waitcnt lgkmcnt(3)\nQmvaa 228,84,216\nQmvaa 228,176,56\nQda 60,53,12288\nQmvaa 108,88,216\nQtv 26,42,44032\n"
"Qmvaa 108,180,56\nQtv 8,40,41984\nQmvaa 140,92,216\nbuffer_load_dwordx4 a[40:43], v60, s[48:51], s31 offen offset:3072 sc1\nQmvaa 140,184,56\nQtv 10,40,44544\ns_add_u32 s95, s67, 43264\nQmvaa 172,96,216\n"
"buffer_load_dwordx4 v62, s[52:55], s95 offen offset:2048 lds\nQmvaa 172,188,56\ns_waitcnt vmcnt(32)\nds_write_b128 v52, a[244:247] offset:37888\nQmvaa 204,100,216\nv_add_u32 v75, 1, v75\nQmvaa 204,192,56\ns_waitcnt vmcnt(17)\n"
"ds_write_b32 v73, v75\ns_waitcnt lgkmcnt(5)\nQmvaa 236,84,220\ns_waitcnt vmcnt(14)\nv_sub_u32 v66, v78, v65\nv_cmp_gt_u32 vcc, 3, v66\ns_and_b64 vcc, vcc, s[28:29]\ns_cbranch_scc1 .Lf3_w\n"
".Lf3_r:\nQmvaa 236,176,60\nQda 48,53,16384\nQmvaa 80,88,220\nQtv 28,42,41984\nQmvaa 80,180,60\nQtv 30,42,44544\ns_add_u32 s31, s77, 4096\n"
"Qmvaa 112,92,220\nbuffer_load_dwordx4 a[44:47], v60, s[48:51], s31 offen offset:0 sc1\nQmvaa 112,184,60\nQtv 12,40,42496\ns_add_u32 s95, s67, 62528\nQmvaa 144,96,220\nbuffer_load_dwordx4 v61, s[52:55], s95 offen offset:3072 lds\nQmvaa 144,188,60\n"
"ds_read_b32 v66, v74\nQmvaa 176,100,220\ns_waitcnt vmcnt(32)\nds_write_b128 v52, a[248:251] offset:41984\nQmvaa 176,192,60\ns_waitcnt lgkmcnt(5)\nQmvaa 208,84,224\nQtv 14,40,45056\n"
"Qmvaa 208,176,48\ns_waitcnt lgkmcnt(2)\nv_cmp_gt_u32 vcc, v75, v66\nQmvaa 240,88,224\ns_cbranch_vccnz .Lso39\n.Lro39:\nQmvaa 240,180,48\nQda 52,53,20480\n"
"Qmvaa 84,92,224\nbuffer_load_dwordx4 a[208:211], v60, s[48:51], s31 offen offset:1024 sc1\nQmvaa 84,184,48\ns_add_u32 m0, s96, 8192\ns_add_u32 s95, s67, 65728\nQmvaa 88,96,224\nbuffer_load_dwordx4 v61, s[52:55], s95 offen lds\nQmvaa 88,188,48\n"
"s_waitcnt vmcnt(32)\nds_write_b128 v52, a[252:255] offset:46080\nQmvaa 120,100,224\nQmvaa 120,192,48\nQtv 32,42,42496\ns_waitcnt lgkmcnt(2)\nQmvaa 152,84,228\nQtv 34,42,45056\n"
"Qmvaa 152,176,52\nQda 56,53,25600\nQmvaa 184,88,228\nQtv 16,40,43008\nQmvaa 184,180,52\nQtv 18,40,45568\nQmvaa 216,92,228\nbuffer_load_dwordx4 a[212:215], v60, s[48:51], s31 offen offset:2048 sc1\n"
"Qmvaa 216,184,52\nQtv 36,42,43008\ns_add_u32 m0, s96, 24576\ns_add_u32 s95, s72, 45056\nQmvaa 248,96,228\nbuffer_load_dwordx4 v61, s[52:55], s95 offen lds\nQmvaa 248,188,52\ns_waitcnt vmcnt(32)\n"
"ds_write_b128 v52, a[168:171] offset:50176\nQmvaa 116,100,228\nQmvaa 116,192,52\nQtv 38,42,45568\ns_waitcnt lgkmcnt(5)\nQmvaa 96,84,232\nQmvaa 96,176,56\nQda 60,53,29696\n"
"Qmvaa 128,88,232\nQta 112,41,0\nQmvaa 128,180,56\nQta 114,41,2560\ns_add_u32 s31, s74, 14336\nQmvaa 160,92,232\nbuffer_load_dwordx4 a[216:219], v60, s[48:51], s31 offen offset:0 sc1\nQmvaa 160,184,56\n"
"Qta 132,43,0\ns_add_u32 s95, s72, 44160\nQmvaa 192,96,232\nbuffer_load_dwordx4 v61, s[52:55], s95 offen offset:1024 lds\nQmvaa 192,188,56\ns_waitcnt vmcnt(32)\nds_write_b128 v52, a[172:175] offset:0\nQmvaa 224,100,232\n"
"v_add_u32 v75, 1, v75\nQmvaa 224,192,56\nds_write_b32 v73, v75\ns_waitcnt lgkmcnt(5)\nQmvva 92,0,236\nv_bfe_u32 v48, v60, 6, 2\nv_lshrrev_b32 v49, 8, v60\nQmvva 92,20,60\n"
"Qda 48,53,33792\nv_and_b32 v44, 16, v60\nQmvva 124,4,236\nQta 134,43,2560\nv_mov_b32 v45, 0x3f80\nQmvva 124,24,60\nQta 116,41,512\nv_lshlrev_b32 v44, v44, v45\n"
"Qmvva 156,8,236\nbuffer_load_dwordx4 a[220:223], v60, s[48:51], s31 offen offset:1024 sc1\nQmvva 156,28,60\nQta 118,41,3072\ns_add_u32 s95, s72, 43264\nQmvva 188,12,236\nbuffer_load_dwordx4 v62, s[52:55], s95 offen offset:2048 lds\nQmvva 188,32,60\n"
"ds_read_b32 v66, v74\nv_xor_b32 v48, v48, v49\nQmvva 220,16,236\ns_waitcnt vmcnt(32)\nds_write_b128 v52, a[0:3] offset:4096\nQmvva 220,36,60\nv_bfe_u32 v49, v60, 5, 1\nv_lshl_or_b32 v48, v48, 1, v49\n"
"s_waitcnt lgkmcnt(5)\nQmvva 252,0,240\nv_xor_b32 v49, 1, v48\nv_min_u32 v48, 1, v48\nQmvva 252,20,48\ns_waitcnt lgkmcnt(1)\nv_cmp_gt_u32 vcc, v75, v66\nQmvva 100,4,240\n"
"s_cbranch_vccnz .Lso40\n.Lro40:\nv_min_u32 v49, 1, v49\nQmvva 100,24,48\nQda 52,53,37888\nv_add_u32 v48, -1, v48\nQmvva 132,8,240\nbuffer_load_dwordx4 a[224:227], v60, s[48:51], s31 offen offset:2048 sc1\n"
"Qmvva 132,28,48\nQta 136,43,512\ns_add_u32 s95, s72, 62528\nQmvva 164,12,240\n.Lpg19:\ns_cbranch_execz .Lpg23\nbuffer_load_dwordx4 v61, s[52:55], s95 offen offset:3072 lds\nQmvva 164,32,48\n"
"s_waitcnt vmcnt(32)\nds_write_b128 v52, a[4:7] offset:8192\nQmvva 196,16,240\nv_add_u32 v49, -1, v49\nv_and_b32 v46, v44, v48\nQmvva 196,36,48\nQta 138,43,3072\nv_and_b32 v47, v44, v49\n"
"s_waitcnt lgkmcnt(3)\nQmvva 228,0,244\nQta 120,41,1024\nv_mov_b32 v44, 0\nQmvva 228,20,52\nQda 56,53,41984\nv_mov_b32 v45, 0\nQmvva 108,4,244\n"
"v_mov_b32 v48, 0\nv_mov_b32 v49, 0\nQmvva 108,24,52\nQta 122,41,3584\nQmvva 140,8,244\nbuffer_load_dwordx4 a[228:231], v60, s[48:51], s31 offen offset:3072 sc1\nQmvva 140,28,52\nQta 140,43,1024\n"
"s_add_u32 m0, s96, 28672\ns_add_u32 s95, s72, 65728\nQmvva 172,12,244\nbuffer_load_dwordx4 v61, s[52:55], s95 offen lds\nQmvva 172,32,52\ns_waitcnt vmcnt(32)\nds_write_b128 v52, a[8:11] offset:12288\nQmvva 204,16,244\n"
"v_add_u32 v75, 1, v75\nQmvva 204,36,52\ns_waitcnt vmcnt(20)\nds_write_b32 v73, v75\ns_waitcnt lgkmcnt(4)\nQmvva 236,0,248\nQta 142,43,3584\nQmvva 236,20,56\n"
"Qda 60,53,46080\nQmvva 80,4,248\nQmvva 80,24,56\nQta 124,41,1536\ns_add_u32 s31, s74, 18432\nQmvva 112,8,248\nbuffer_load_dwordx4 a[232:235], v60, s[48:51], s31 offen offset:0 sc1\nQmvva 112,28,56\n"
"Qta 126,41,4096\ns_add_u32 m0, s96, 45056\ns_add_u32 s95, s73, 45056\nQmvva 144,12,248\nbuffer_load_dwordx4 v61, s[52:55], s95 offen lds\nQmvva 144,32,56\nds_read_b32 v66, v74\nQmvva 176,16,248\n"
"s_waitcnt vmcnt(32)\nds_write_b128 v52, a[12:15] offset:16384\nQmvva 176,36,56\ns_waitcnt lgkmcnt(4)\nQmvva 208,0,252\nQta 144,43,1536\nQmvva 208,20,60\ns_waitcnt lgkmcnt(2)\n"
"v_cmp_gt_u32 vcc, v75, v66\nQmvva 240,4,252\ns_cbranch_vccnz .Lso41\n.Lro41:\nQmvva 240,24,60\nQda 48,53,50176\nQmvva 84,8,252\nbuffer_load_dwordx4 a[236:239], v60, s[48:51], s31 offen offset:1024 sc1\n"
"Qmvva 84,28,60\ns_add_u32 s95, s73, 44160\nQmvva 88,12,252\nbuffer_load_dwordx4 v61, s[52:55], s95 offen offset:1024 lds\nQmvva 88,32,60\ns_waitcnt vmcnt(31)\nds_write_b128 v52, a[16:19] offset:20480\nQmvva 120,16,252\n"
"Qmvva 120,36,60\nQta 146,43,4096\ns_waitcnt lgkmcnt(2)\nQmvva 152,0,168\nQta 128,41,2048\nQmvva 152,20,48\nQda 52,53,0\nQmvva 184,4,168\n"
"Qta 130,41,4608\nQmvva 184,24,48\nQmvva 216,8,168\nbuffer_load_dwordx4 a[240:243], v60, s[48:51], s31 offen offset:2048 sc1\nQmvva 216,28,48\nQta 148,43,2048\ns_add_u32 s95, s73, 43264\nQmvva 248,12,168\n"
"buffer_load_dwordx4 v62, s[52:55], s95 offen offset:2048 lds\nQmvva 248,32,48\ns_waitcnt vmcnt(31)\nds_write_b128 v52, a[20:23] offset:25600\nQmvva 116,16,168\nQmvva 116,36,48\nQta 150,43,4608\ns_waitcnt lgkmcnt(4)\n"
"Qmvva 96,0,172\nQmvva 96,20,52\nQda 56,53,4096\nQmvva 128,4,172\nQta 84,41,20480\nQmvva 128,24,52\nQta 86,41,23040\ns_add_u32 s31, s75, 14336\n"
"Qmvva 160,8,172\nbuffer_load_dwordx4 a[244:247], v60, s[48:51], s31 offen offset:0 sc1\nQmvva 160,28,52\nQta 176,43,20480\ns_add_u32 s95, s73, 62528\nQmvva 192,12,172\nbuffer_load_dwordx4 v61, s[52:55], s95 offen offset:3072 lds\nQmvva 192,32,52\n"
"s_waitcnt vmcnt(31)\nds_write_b128 v52, a[24:27] offset:29696\nQmvva 224,16,172\nv_add_u32 v75, 1, v75\nQmvva 224,36,52\nds_write_b32 v73, v75\ns_waitcnt lgkmcnt(5)\nQmvaa 92,112,0\n"
"Qta 178,43,23040\nQmvaa 92,132,56\nQda 60,53,8192\nQmvaa 124,116,0\nQmvaa 124,136,56\nQta 88,41,20992\nQmvaa 156,120,0\nbuffer_load_dwordx4 a[248:251], v60, s[48:51], s31 offen offset:1024 sc1\n"
"Qmvaa 156,140,56\nQta 90,41,23552\ns_add_u32 m0, s96, 49152\ns_add_u32 s95, s73, 65728\nQmvaa 188,124,0\nbuffer_load_dwordx4 v61, s[52:55], s95 offen lds\nQmvaa 188,144,56\nds_read_b32 v66, v74\n"
"Qmvaa 220,128,0\ns_waitcnt vmcnt(31)\nds_write_b128 v52, a[28:31] offset:33792\nQmvaa 220,148,56\ns_waitcnt lgkmcnt(4)\nQmvaa 252,112,4\nQta 180,43,20992\nQmvaa 252,132,60\n"
"s_waitcnt lgkmcnt(2)\nv_cmp_gt_u32 vcc, v75, v66\nQmvaa 100,116,4\ns_cbranch_vccnz .Lso42\n.Lro42:\nQmvaa 100,136,60\nQda 48,53,12288\nQmvaa 132,120,4\n"
"buffer_load_dwordx4 a[252:255], v60, s[48:51], s31 offen offset:2048 sc1\nQmvaa 132,140,60\ns_add_u32 m0, s96, 65536\ns_add_u32 s95, s67, 86016\nQmvaa 164,124,4\nbuffer_load_dwordx4 v61, s[52:55], s95 offen lds\nQmvaa 164,144,60\ns_waitcnt vmcnt(31)\n"
"ds_write_b128 v52, a[32:35] offset:37888\nQmvaa 196,128,4\nQmvaa 196,148,60\nQta 182,43,23552\ns_waitcnt lgkmcnt(2)\nQmvaa 228,112,8\nQta 92,41,21504\nQmvaa 228,132,48\n"
"Qda 52,53,16384\nQmvaa 108,116,8\nQta 94,41,24064\nQmvaa 108,136,48\nQmvaa 140,120,8\nbuffer_load_dwordx4 a[168:171], v60, s[48:51], s31 offen offset:3072 sc1\nQmvaa 140,140,48\nQta 184,43,21504\n"
"s_add_u32 s95, s67, 85120\nQmvaa 172,124,8\nbuffer_load_dwordx4 v61, s[52:55], s95 offen offset:1024 lds\nQmvaa 172,144,48\ns_waitcnt vmcnt(31)\nds_write_b128 v52, a[36:39] offset:41984\nQmvaa 204,128,8\nv_add_u32 v75, 1, v75\n"
"Qmvaa 204,148,48\ns_waitcnt vmcnt(24)\nds_write_b32 v73, v75\ns_waitcnt lgkmcnt(4)\nQmvaa 236,112,12\nbuffer_load_dword v78, v76, s[24:27], 0 offen sc1\nQmvaa 236,132,52\nQda 56,53,20480\n"
"Qmvaa 80,116,12\nQta 186,43,24064\nQmvaa 80,136,52\nQta 96,41,22016\ns_add_u32 s31, s75, 18432\nQmvaa 112,120,12\nbuffer_load_dwordx4 a[172:175], v60, s[48:51], s31 offen offset:0 sc1\nQmvaa 112,140,52\n"
"Qta 98,41,24576\ns_add_u32 s95, s67, 84224\nQmvaa 144,124,12\nbuffer_load_dwordx4 v62, s[52:55], s95 offen offset:2048 lds\nQmvaa 144,144,52\nds_read_b32 v66, v74\nQmvaa 176,128,12\ns_waitcnt vmcnt(32)\n"
"ds_write_b128 v52, a[40:43] offset:46080\nQmvaa 176,148,52\ns_waitcnt lgkmcnt(5)\nQmvaa 208,112,16\nQmvaa 208,132,56\ns_waitcnt lgkmcnt(1)\nv_cmp_gt_u32 vcc, v75, v66\nQmvaa 240,116,16\n"
"s_cbranch_vccnz .Lso43\n.Lro43:\nQmvaa 240,136,56\nQda 60,53,25600\nQmvaa 84,120,16\nbuffer_load_dwordx4 a[0:3], v60, s[48:51], s31 offen offset:1024 sc1\nQmvaa 84,140,56\nQta 188,43,22016\n"
"s_add_u32 s95, s67, 103488\nQmvaa 88,124,16\nbuffer_load_dwordx4 v61, s[52:55], s95 offen offset:3072 lds\nQmvaa 88,144,56\ns_waitcnt vmcnt(32)\nds_write_b128 v52, a[44:47] offset:50176\nQmvaa 120,128,16\nQmvaa 120,148,56\n"
"Qta 190,43,24576\ns_waitcnt lgkmcnt(3)\nQmvaa 152,112,20\nQta 100,41,22528\nQmvaa 152,132,60\nQda 48,53,29696\nQmvaa 184,116,20\nQmvaa 184,136,60\n"
"Qta 102,41,25088\nQmvaa 216,120,20\nbuffer_load_dwordx4 a[4:7], v60, s[48:51], s31 offen offset:2048 sc1\nQmvaa 216,140,60\nQta 192,43,22528\ns_add_u32 m0, s96, 69632\ns_add_u32 s95, s67, 106688\nQmvaa 248,124,20\n"
"buffer_load_dwordx4 v61, s[52:55], s95 offen lds\nQmvaa 248,144,60\ns_waitcnt vmcnt(32)\nds_write_b128 v52, a[208:211] offset:0\nQmvaa 116,128,20\nQmvaa 116,148,60\nQta 194,43,25088\ns_waitcnt lgkmcnt(4)\n"
"Qmvaa 96,112,24\nQmvaa 96,132,48\nQda 52,53,33792\nQmvaa 128,116,24\nQtv 0,40,0\nQmvaa 128,136,48\nQtv 2,40,2560\ns_add_u32 s31, s77, 14336\n"
"Qmvaa 160,120,24\nbuffer_load_dwordx4 a[8:11], v60, s[48:51], s31 offen offset:0 sc1\nQmvaa 160,140,48\nQtv 20,42,0\nQmvaa 192,124,24\nQmvaa 192,144,48\ns_waitcnt vmcnt(31)\nds_write_b128 v52, a[212:215] offset:4096\n"
"Qmvaa 224,128,24\nv_add_u32 v75, 1, v75\nQmvaa 224,148,48\nds_write_b32 v73, v75\ns_waitcnt lgkmcnt(5)\nQmvaa 92,84,28\nbuffer_load_dword v78, v76, s[24:27], 0 offen sc1\nQmvaa 92,176,52\n"
"Qda 56,53,37888\nQmvaa 124,88,28\nQtv 22,42,2560\nQmvaa 124,180,52\nQtv 4,40,512\nQmvaa 156,92,28\nbuffer_load_dwordx4 a[12:15], v60, s[48:51], s31 offen offset:1024 sc1\nQmvaa 156,184,52\n"
"Qtv 6,40,3072\nQmvaa 188,96,28\nQmvaa 188,188,52\nds_read_b32 v66, v74\nQmvaa 220,100,28\ns_waitcnt vmcnt(31)\nds_write_b128 v52, a[216:219] offset:8192\nQmvaa 220,192,52\n"
"s_waitcnt lgkmcnt(5)\nQmvaa 252,84,32\nQtv 24,42,512\nQmvaa 252,176,56\ns_waitcnt lgkmcnt(2)\nv_cmp_gt_u32 vcc, v75, v66\nQmvaa 100,88,32\ns_cbranch_vccnz .Lso44\n"
".Lro44:\nQmvaa 100,180,56\nQda 60,53,41984\nQmvaa 132,92,32\nbuffer_load_dwordx4 a[16:19], v60, s[48:51], s31 offen offset:2048 sc1\ns_add_u32 m0, s96, 86016\ns_add_u32 s95, s72, 86016\nQmvaa 132,184,56\n"
"buffer_load_dwordx4 v61, s[52:55], s95 offen lds\nQmvaa 164,96,32\nQtv 26,42,3072\nQmvaa 164,188,56\ns_waitcnt vmcnt(31)\nds_write_b128 v52, a[220:223] offset:12288\nQmvaa 196,100,32\nQmvaa 196,192,56\n"
"Qtv 8,40,1024\ns_waitcnt lgkmcnt(3)\nQmvaa 228,84,36\nQmvaa 228,176,60\nQda 48,53,46080\nQmvaa 108,88,36\nQtv 10,40,3584\nQmvaa 108,180,60\n"
"Qtv 28,42,1024\nQmvaa 140,92,36\nbuffer_load_dwordx4 a[20:23], v60, s[48:51], s31 offen offset:3072 sc1\ns_add_u32 s95, s72, 85120\nQmvaa 140,184,60\nbuffer_load_dwordx4 v61, s[52:55], s95 offen offset:1024 lds\nQmvaa 172,96,36\nQtv 30,42,3584\n"
"Qmvaa 172,188,60\ns_waitcnt vmcnt(31)\nds_write_b128 v52, a[224:227] offset:16384\nQmvaa 204,100,36\nv_add_u32 v75, 1, v75\nQmvaa 204,192,60\ns_waitcnt vmcnt(28)\nds_write_b32 v73, v75\n"
"s_waitcnt lgkmcnt(5)\nQmvaa 236,84,40\ns_waitcnt vmcnt(13)\nv_sub_u32 v66, v78, v65\nv_cmp_gt_u32 vcc, 4, v66\ns_and_b64 vcc, vcc, s[28:29]\ns_cbranch_scc1 .Lf4_w\n.Lf4_r:\n"
"Qmvaa 236,176,48\nQda 52,53,50176\nQmvaa 80,88,40\nQmvaa 80,180,48\nQtv 12,40,1536\ns_add_u32 s31, s77, 18432\nQmvaa 112,92,40\nbuffer_load_dwordx4 a[24:27], v60, s[48:51], s31 offen offset:0 sc1\n"
"s_add_u32 s95, s72, 84224\nQmvaa 112,184,48\nbuffer_load_dwordx4 v62, s[52:55], s95 offen offset:2048 lds\nQmvaa 144,96,40\nQtv 14,40,4096\nQmvaa 144,188,48\nds_read_b32 v66, v74\nQmvaa 176,100,40\n"
"s_waitcnt vmcnt(31)\nds_write_b128 v52, a[228:231] offset:20480\nQmvaa 176,192,48\ns_waitcnt lgkmcnt(4)\nQmvaa 208,84,44\nQtv 32,42,1536\nQmvaa 208,176,52\ns_waitcnt lgkmcnt(2)\n"
"v_cmp_gt_u32 vcc, v75, v66\nQmvaa 240,88,44\ns_cbranch_vccnz .Lso45\n.Lro45:\nQmvaa 240,180,52\nQda 56,53,0\nQmvaa 84,92,44\nbuffer_load_dwordx4 a[28:31], v60, s[48:51], s31 offen offset:1024 sc1\n"
"s_add_u32 s95, s72, 103488\nQmvaa 84,184,52\nbuffer_load_dwordx4 v61, s[52:55], s95 offen offset:3072 lds\nQmvaa 88,96,44\nQmvaa 88,188,52\ns_waitcnt vmcnt(31)\nds_write_b128 v52, a[232:235] offset:25600\nQmvaa 120,100,44\n"
"Qmvaa 120,192,52\nQtv 34,42,4096\ns_waitcnt lgkmcnt(2)\nQmvaa 152,84,208\nQtv 16,40,2048\nQmvaa 152,176,56\nQda 60,53,4096\nQmvaa 184,88,208\n"
"Qtv 18,40,4608\nQmvaa 184,180,56\nQmvaa 216,92,208\nbuffer_load_dwordx4 a[32:35], v60, s[48:51], s31 offen offset:2048 sc1\ns_add_u32 m0, s96, 90112\ns_add_u32 s95, s72, 106688\nQmvaa 216,184,56\nbuffer_load_dwordx4 v61, s[52:55], s95 offen lds\n"
"Qmvaa 248,96,208\nQtv 36,42,2048\nQmvaa 248,188,56\ns_waitcnt vmcnt(31)\nds_write_b128 v52, a[236:239] offset:29696\nQmvaa 116,100,208\nQmvaa 116,192,56\nQtv 38,42,4608\n"
"s_waitcnt lgkmcnt(4)\nQmvaa 96,84,212\nQmvaa 96,176,60\nQda 48,53,8192\nQmvaa 128,88,212\nQta 112,40,20480\nQmvaa 128,180,60\nQta 114,40,23040\n"
"s_add_u32 s31, s74, 28672\nQmvaa 160,92,212\nbuffer_load_dwordx4 a[36:39], v60, s[48:51], s31 offen offset:0 sc1\nQmvaa 160,184,60\nQta 132,42,20480\nQmvaa 192,96,212\nQmvaa 192,188,60\ns_waitcnt vmcnt(30)\n"
"ds_write_b128 v52, a[240:243] offset:33792\nQmvaa 224,100,212\nv_add_u32 v75, 1, v75\nQmvaa 224,192,60\nds_write_b32 v73, v75\ns_waitcnt lgkmcnt(5)\nQmvva 92,0,216\ns_waitcnt vmcnt(12)\n"
"v_sub_u32 v66, v78, v65\nv_cmp_gt_u32 vcc, 5, v66\ns_and_b64 vcc, vcc, s[28:29]\ns_cbranch_scc1 .Lf5_w\n.Lf5_r:\nQmvva 92,20,48\nQda 52,53,12288\nQmvva 124,4,216\n"
"Qta 134,42,23040\nQmvva 124,24,48\nQta 116,40,20992\nQmvva 156,8,216\nbuffer_load_dwordx4 a[40:43], v60, s[48:51], s31 offen offset:1024 sc1\nQmvva 156,28,48\nQmvva 188,12,216\nQta 118,40,23552\n"
"Qmvva 188,32,48\nds_read_b32 v66, v74\nQmvva 220,16,216\ns_waitcnt vmcnt(29)\nds_write_b128 v52, a[244:247] offset:37888\nQmvva 220,36,48\ns_waitcnt lgkmcnt(5)\nQmvva 252,0,220\n"
"Qta 136,42,20992\nQmvva 252,20,52\ns_waitcnt lgkmcnt(2)\nv_cmp_gt_u32 vcc, v75, v66\nQmvva 100,4,220\ns_cbranch_vccnz .Lso46\n.Lro46:\nQmvva 100,24,52\n"
"Qda 56,53,16384\nQmvva 132,8,220\nbuffer_load_dwordx4 a[44:47], v60, s[48:51], s31 offen offset:2048 sc1\ns_add_u32 m0, s96, 4096\ns_add_u32 s95, s73, 86016\nQmvva 132,28,52\nbuffer_load_dwordx4 v61, s[52:55], s95 offen lds\nQmvva 164,12,220\n"
"Qta 138,42,23552\nQmvva 164,32,52\ns_waitcnt vmcnt(29)\nds_write_b128 v52, a[248:251] offset:41984\nQmvva 196,16,220\nQmvva 196,36,52\ns_waitcnt lgkmcnt(2)\nQmvva 228,0,224\n"
"Qta 120,40,21504\nQmvva 228,20,56\nQda 60,53,20480\nQmvva 108,4,224\nQta 122,40,24064\nQmvva 108,24,56\nQmvva 140,8,224\nbuffer_load_dwordx4 a[208:211], v60, s[48:51], s31 offen offset:3072 sc1\n"
"s_add_u32 s95, s73, 85120\nQmvva 140,28,56\nbuffer_load_dwordx4 v61, s[52:55], s95 offen offset:1024 lds\nQmvva 172,12,224\nQta 140,42,21504\nQmvva 172,32,56\ns_waitcnt vmcnt(29)\nds_write_b128 v52, a[252:255] offset:46080\n"
"Qmvva 204,16,224\nv_add_u32 v75, 1, v75\nQmvva 204,36,56\ns_waitcnt vmcnt(30)\nds_write_b32 v73, v75\ns_lshr_b32 s68, %19, 2\ns_xor_b32 s68, s68, s92\ns_cbranch_scc1 .Lrole1\n"
"s_waitcnt lgkmcnt(4)\nQmvva 236,0,228\nQta 142,42,24064\ns_lshl_b32 s46, %19, 4\nQmvva 236,20,60\nQda 48,53,25600\ns_add_u32 s46, s46, 128\nQmvva 80,4,228\n"
"Qta 124,40,22016\nv_mov_b32 v77, 0x7ffff000\nQmvva 80,24,60\ns_add_u32 s31, s74, 32768\nQmvva 112,8,228\nbuffer_load_dwordx4 a[212:215], v60, s[48:51], s31 offen offset:0 sc1\ns_add_u32 s95, s73, 84224\nQmvva 112,28,60\n"
"buffer_load_dwordx4 v62, s[52:55], s95 offen offset:2048 lds\nQmvva 144,12,228\nQta 126,40,24576\nQmvva 144,32,60\nds_read_b32 v66, v74\nQmvva 176,16,228\ns_waitcnt vmcnt(29)\nds_write_b128 v52, a[168:171] offset:50176\n"
"Qmvva 176,36,60\ns_waitcnt lgkmcnt(4)\nQmvva 208,0,232\nQta 144,42,22016\nQmvva 208,20,48\ns_waitcnt lgkmcnt(2)\nv_cmp_gt_u32 vcc, v75, v66\nQmvva 240,4,232\n"
"s_cbranch_vccnz .Lso47r0\n.Lro47r0:\nQmvva 240,24,48\nQda 52,53,29696\nQmvva 84,8,232\nbuffer_load_dwordx4 a[216:219], v60, s[48:51], s31 offen offset:1024 sc1\ns_add_u32 s95, s73, 103488\nQmvva 84,28,48\n"
"buffer_load_dwordx4 v61, s[52:55], s95 offen offset:3072 lds\nQmvva 88,12,232\nQmvva 88,32,48\ns_waitcnt vmcnt(28)\nds_write_b128 v52, a[172:175] offset:0\nQmvva 120,16,232\nQmvva 120,36,48\nQta 146,42,24576\n"
"s_waitcnt lgkmcnt(2)\nQmvva 152,0,236\n.Lpg20:\ns_cbranch_execz .Lpg24\nQta 128,40,22528\nQmvva 152,20,52\nQda 56,53,33792\nQmvva 184,4,236\n"
"Qta 130,40,25088\nQmvva 184,24,52\nQmvva 216,8,236\nbuffer_load_dwordx4 a[220:223], v60, s[48:51], s31 offen offset:2048 sc1\ns_add_u32 m0, s96, 8192\ns_add_u32 s95, s73, 106688\nQmvva 216,28,52\nbuffer_load_dwordx4 v61, s[52:55], s95 offen lds\n"
"Qmvva 248,12,236\nQta 148,42,22528\nQmvva 248,32,52\ns_waitcnt vmcnt(28)\nds_write_b128 v52, a[0:3] offset:4096\nQmvva 116,16,236\nQmvva 116,36,52\nQta 150,42,25088\n"
"s_waitcnt lgkmcnt(4)\nQmvva 96,0,240\nQmvva 96,20,56\nQda 60,53,37888\nQmvva 128,4,240\nQta 84,40,40960\nQmvva 128,24,56\nQta 86,40,43520\n"
"s_add_u32 s31, s75, 32768\nQmvva 160,8,240\nbuffer_load_dwordx4 a[224:227], v60, s[48:51], s31 offen offset:0 sc1\nQmvva 160,28,56\nQta 176,42,40960\nQmvva 192,12,240\nQmvva 192,32,56\ns_waitcnt vmcnt(27)\n"
"ds_write_b128 v52, a[4:7] offset:8192\nQmvva 224,16,240\nv_add_u32 v75, 1, v75\nQmvva 224,36,56\nds_write_b32 v73, v75\ns_waitcnt lgkmcnt(5)\nQmvaa 92,112,244\nQta 178,42,43520\n"
"Qmvaa 92,132,60\nQda 48,53,41984\nQmvaa 124,116,244\nQta 88,40,41472\nQmvaa 124,136,60\nQmvaa 156,120,244\nbuffer_load_dwordx4 a[228:231], v60, s[48:51], s31 offen offset:1024 sc1\nQmvaa 156,140,60\n"
"Qta 90,40,44032\nQmvaa 188,124,244\nQta 180,42,41472\nQmvaa 188,144,60\nds_read_b32 v66, v74\nQmvaa 220,128,244\ns_waitcnt vmcnt(26)\nds_write_b128 v52, a[8:11] offset:12288\n"
"Qmvaa 220,148,60\ns_waitcnt lgkmcnt(5)\nQmvaa 252,112,248\nQmvaa 252,132,48\ns_waitcnt lgkmcnt(1)\nv_cmp_gt_u32 vcc, v75, v66\nQmvaa 100,116,248\ns_cbranch_vccnz .Lso48r0\n"
".Lro48r0:\nQmvaa 100,136,48\nQda 52,53,46080\nQmvaa 132,120,248\nbuffer_load_dwordx4 a[232:235], v60, s[48:51], s31 offen offset:2048 sc1\ns_add_u32 m0, s96, 24576\ns_add_u32 s95, s67, 126976\nQmvaa 132,140,48\n"
"buffer_load_dwordx4 v61, s[52:55], s95 offen lds\nQmvaa 164,124,248\nQta 182,42,44032\nQmvaa 164,144,48\ns_waitcnt vmcnt(26)\nds_write_b128 v52, a[12:15] offset:16384\nQmvaa 196,128,248\nQmvaa 196,148,48\n"
"Qta 92,40,41984\ns_waitcnt lgkmcnt(3)\nQmvaa 228,112,252\nv_cmp_eq_u32 s[44:45], s46, v60\ns_nop 1\nv_cndmask_b32 v77, v77, v76, s[44:45]\nv_add_u32 v67, 1, v65\ns_xor_b32 s46, %19, 12\n"
"s_lshl_b32 s28, 1, s46\nQmvaa 228,132,52\nQda 56,53,50176\nQmvaa 108,116,252\nQta 94,40,44544\nQmvaa 108,136,52\nQta 184,42,41984\ns_add_u32 s31, s77, 32768\n"
"Qmvaa 140,120,252\nbuffer_load_dwordx4 a[236:239], v60, s[48:51], s31 offen offset:0 sc1\ns_add_u32 s95, s67, 126080\nQmvaa 140,140,52\nbuffer_load_dwordx4 v61, s[52:55], s95 offen offset:1024 lds\nQmvaa 172,124,252\nQta 186,42,44544\nQmvaa 172,144,52\n"
"s_waitcnt vmcnt(27)\nds_write_b128 v52, a[16:19] offset:20480\nQmvaa 204,128,252\nv_add_u32 v75, 1, v75\nQmvaa 204,148,52\ns_waitcnt vmcnt(31)\nds_write_b32 v73, v75\ns_waitcnt lgkmcnt(5)\n"
"Qmvaa 236,112,168\nQmvaa 236,132,56\nQda 60,53,0\nQmvaa 80,116,168\nQta 96,40,42496\nQmvaa 80,136,56\nQta 98,40,45056\nQmvaa 112,120,168\n"
"buffer_load_dwordx4 a[240:243], v60, s[48:51], s31 offen offset:1024 sc1\ns_add_u32 s95, s67, 125184\nQmvaa 112,140,56\nbuffer_load_dwordx4 v62, s[52:55], s95 offen offset:2048 lds\nQmvaa 144,124,168\nQmvaa 144,144,56\nds_read_b32 v66, v74\nQmvaa 176,128,168\n"
"s_waitcnt vmcnt(27)\nds_write_b128 v52, a[20:23] offset:25600\nQmvaa 176,148,56\ns_waitcnt lgkmcnt(4)\nQmvaa 208,112,172\nQta 188,42,42496\nQmvaa 208,132,60\ns_waitcnt lgkmcnt(2)\n"
"v_cmp_gt_u32 vcc, v75, v66\nQmvaa 240,116,172\ns_cbranch_vccnz .Lso49r0\n.Lro49r0:\nQmvaa 240,136,60\nQda 48,53,4096\nQmvaa 84,120,172\nbuffer_load_dwordx4 a[244:247], v60, s[48:51], s31 offen offset:2048 sc1\n"
"s_add_u32 s95, s67, 144448\nQmvaa 84,140,60\nbuffer_load_dwordx4 v61, s[52:55], s95 offen offset:3072 lds\nQmvaa 88,124,172\nQta 190,42,45056\nQmvaa 88,144,60\ns_waitcnt vmcnt(27)\nds_write_b128 v52, a[24:27] offset:29696\n"
"Qmvaa 120,128,172\nQmvaa 120,148,60\ns_waitcnt lgkmcnt(2)\nQmvaa 152,112,0\nQta 100,40,43008\nQmvaa 152,132,48\nQda 52,53,8192\nQmvaa 184,116,0\n"
"Qta 102,40,45568\nQmvaa 184,136,48\ns_add_u32 s31, s74, 47104\nQmvaa 216,120,0\nbuffer_load_dwordx4 a[248:251], v60, s[48:51], s31 offen offset:0 sc1\ns_add_u32 m0, s96, 28672\ns_add_u32 s95, s67, 147648\nQmvaa 216,140,48\n"
"buffer_load_dwordx4 v61, s[52:55], s95 offen lds\nQmvaa 248,124,0\nQta 192,42,43008\nQmvaa 248,144,48\ns_waitcnt vmcnt(27)\nds_write_b128 v52, a[28:31] offset:33792\nQmvaa 116,128,0\nQmvaa 116,148,48\n"
"Qta 194,42,45568\ns_waitcnt lgkmcnt(4)\nQmvaa 96,112,4\nQmvaa 96,132,52\nQda 56,53,12288\nQmvaa 128,116,4\nQtv 0,41,0\nQmvaa 128,136,52\n"
"Qtv 2,41,2560\nQmvaa 160,120,4\nbuffer_load_dwordx4 a[252:255], v60, s[48:51], s31 offen offset:1024 sc1\nQmvaa 160,140,52\nQtv 20,43,0\nQmvaa 192,124,4\nQmvaa 192,144,52\ns_waitcnt vmcnt(26)\n"
"ds_write_b128 v52, a[32:35] offset:37888\nQmvaa 224,128,4\nv_add_u32 v75, 1, v75\nQmvaa 224,148,52\nds_write_b32 v73, v75\ns_waitcnt lgkmcnt(5)\nQmvaa 92,84,8\nQtv 22,43,2560\n"
"Qmvaa 92,176,56\nQda 60,53,16384\nQmvaa 124,88,8\nQtv 4,41,512\nQmvaa 124,180,56\nQmvaa 156,92,8\nbuffer_load_dwordx4 a[168:171], v60, s[48:51], s31 offen offset:2048 sc1\nQmvaa 156,184,56\n"
"Qtv 6,41,3072\nQmvaa 188,96,8\nQtv 24,43,512\nQmvaa 188,188,56\nds_read_b32 v66, v74\nQmvaa 220,100,8\ns_waitcnt vmcnt(25)\nds_write_b128 v52, a[36:39] offset:41984\n"
"Qmvaa 220,192,56\ns_waitcnt lgkmcnt(5)\nQmvaa 252,84,12\nQmvaa 252,176,60\ns_waitcnt lgkmcnt(1)\nv_cmp_gt_u32 vcc, v75, v66\nQmvaa 100,88,12\ns_cbranch_vccnz .Lso50r0\n"
".Lro50r0:\nQmvaa 100,180,60\nQda 48,53,20480\ns_add_u32 s31, s75, 47104\nQmvaa 132,92,12\nbuffer_load_dwordx4 a[172:175], v60, s[48:51], s31 offen offset:0 sc1\ns_add_u32 m0, s96, 45056\ns_add_u32 s95, s72, 126976\n"
"Qmvaa 132,184,60\nbuffer_load_dwordx4 v61, s[52:55], s95 offen lds\nQmvaa 164,96,12\nQtv 26,43,3072\nQmvaa 164,188,60\ns_waitcnt vmcnt(26)\nds_write_b128 v52, a[40:43] offset:46080\nQmvaa 196,100,12\n"
"Qmvaa 196,192,60\nQtv 8,41,1024\ns_waitcnt lgkmcnt(3)\nQmvaa 228,84,16\nQmvaa 228,176,48\nQda 52,53,25600\nQmvaa 108,88,16\nQtv 10,41,3584\n"
"Qmvaa 108,180,48\nQtv 28,43,1024\nQmvaa 140,92,16\nbuffer_load_dwordx4 a[0:3], v60, s[48:51], s31 offen offset:1024 sc1\ns_add_u32 s95, s72, 126080\nQmvaa 140,184,48\nbuffer_load_dwordx4 v61, s[52:55], s95 offen offset:1024 lds\nQmvaa 172,96,16\n"
"Qtv 30,43,3584\nQmvaa 172,188,48\ns_waitcnt vmcnt(27)\nds_write_b128 v52, a[44:47] offset:50176\nQmvaa 204,100,16\nv_add_u32 v75, 1, v75\nQmvaa 204,192,48\ns_waitcnt vmcnt(30)\n"
"ds_write_b32 v73, v75\ns_waitcnt lgkmcnt(5)\nQmvaa 236,84,20\nQmvaa 236,176,52\nQda 56,53,29696\nQmvaa 80,88,20\nQtv 12,41,1536\nQmvaa 80,180,52\n"
"Qtv 14,41,4096\nQmvaa 112,92,20\nbuffer_load_dwordx4 a[4:7], v60, s[48:51], s31 offen offset:2048 sc1\ns_add_u32 s95, s72, 125184\nQmvaa 112,184,52\nbuffer_load_dwordx4 v62, s[52:55], s95 offen offset:2048 lds\nQmvaa 144,96,20\nQmvaa 144,188,52\n"
"ds_read_b32 v66, v74\nQmvaa 176,100,20\ns_waitcnt vmcnt(27)\nds_write_b128 v52, a[208:211] offset:0\nQmvaa 176,192,52\ns_waitcnt lgkmcnt(4)\nQmvaa 208,84,24\nQtv 32,43,1536\n"
"Qmvaa 208,176,56\ns_waitcnt lgkmcnt(2)\nv_cmp_gt_u32 vcc, v75, v66\nQmvaa 240,88,24\ns_cbranch_vccnz .Lso51r0\n.Lro51r0:\nQmvaa 240,180,56\nQda 60,53,33792\n"
"s_add_u32 s31, s77, 47104\nQmvaa 84,92,24\nbuffer_load_dwordx4 a[8:11], v60, s[48:51], s31 offen offset:0 sc1\ns_add_u32 s95, s72, 144448\nQmvaa 84,184,56\nbuffer_load_dwordx4 v61, s[52:55], s95 offen offset:3072 lds\nQmvaa 88,96,24\nQtv 34,43,4096\n"
"Qmvaa 88,188,56\ns_waitcnt vmcnt(27)\nds_write_b128 v52, a[212:215] offset:4096\nQmvaa 120,100,24\nQmvaa 120,192,56\ns_waitcnt lgkmcnt(2)\nQmvaa 152,84,28\nQtv 16,41,2048\n"
"Qmvaa 152,176,60\nQda 48,53,37888\nQmvaa 184,88,28\nQtv 18,41,4608\nQmvaa 184,180,60\nQmvaa 216,92,28\nbuffer_load_dwordx4 a[12:15], v60, s[48:51], s31 offen offset:1024 sc1\ns_add_u32 m0, s96, 49152\n"
"s_add_u32 s95, s72, 147648\nQmvaa 216,184,60\nbuffer_load_dwordx4 v61, s[52:55], s95 offen lds\nQmvaa 248,96,28\nQtv 36,43,2048\nQmvaa 248,188,60\ns_waitcnt vmcnt(27)\nds_write_b128 v52, a[216:219] offset:8192\n"
"Qmvaa 116,100,28\nQmvaa 116,192,60\nQtv 38,43,4608\ns_waitcnt lgkmcnt(4)\nQmvaa 96,84,32\nQmvaa 96,176,48\nQda 52,53,41984\nQmvaa 128,88,32\n"
"Qta 112,41,20480\nQmvaa 128,180,48\nQta 114,41,23040\nQmvaa 160,92,32\nbuffer_load_dwordx4 a[16:19], v60, s[48:51], s31 offen offset:2048 sc1\nQmvaa 160,184,48\nQta 132,43,20480\nQmvaa 192,96,32\n"
"Qmvaa 192,188,48\ns_waitcnt vmcnt(26)\nds_write_b128 v52, a[220:223] offset:12288\nQmvaa 224,100,32\nv_add_u32 v75, 1, v75\nQmvaa 224,192,48\nds_write_b32 v73, v75\ns_waitcnt lgkmcnt(5)\n"
"Qmvva 92,0,36\nQta 134,43,23040\nQmvva 92,20,52\nQda 56,53,46080\nQmvva 124,4,36\nQta 116,41,20992\nQmvva 124,24,52\ns_add_u32 s31, s75, 28672\n"
"Qmvva 156,8,36\nbuffer_load_dwordx4 a[20:23], v60, s[48:51], s31 offen offset:0 sc1\nQmvva 156,28,52\nQta 118,41,23552\nQmvva 188,12,36\nQta 136,43,20992\nQmvva 188,32,52\nds_read_b32 v66, v74\n"
"Qmvva 220,16,36\ns_waitcnt vmcnt(25)\nds_write_b128 v52, a[224:227] offset:16384\nQmvva 220,36,52\ns_waitcnt lgkmcnt(5)\nQmvva 252,0,40\nQmvva 252,20,56\ns_waitcnt lgkmcnt(1)\n"
"v_cmp_gt_u32 vcc, v75, v66\nQmvva 100,4,40\ns_cbranch_vccnz .Lso52r0\n.Lro52r0:\nQmvva 100,24,56\nQda 60,53,50176\nQmvva 132,8,40\nbuffer_load_dwordx4 a[24:27], v60, s[48:51], s31 offen offset:1024 sc1\n"
"s_add_u32 m0, s96, 65536\ns_add_u32 s95, s73, 126976\nQmvva 132,28,56\nbuffer_load_dwordx4 v61, s[52:55], s95 offen lds\nQmvva 164,12,40\nQta 138,43,23552\nQmvva 164,32,56\ns_waitcnt vmcnt(26)\n"
"ds_write_b128 v52, a[228:231] offset:20480\nQmvva 196,16,40\nQmvva 196,36,56\nQta 120,41,21504\ns_waitcnt lgkmcnt(3)\nQmvva 228,0,44\nQmvva 228,20,60\nQda 48,53,0\n"
"Qmvva 108,4,44\nQta 122,41,24064\nQmvva 108,24,60\nQta 140,43,21504\nQmvva 140,8,44\nbuffer_load_dwordx4 a[28:31], v60, s[48:51], s31 offen offset:2048 sc1\ns_add_u32 s95, s73, 126080\nQmvva 140,28,60\n"
"buffer_load_dwordx4 v61, s[52:55], s95 offen offset:1024 lds\nQmvva 172,12,44\nQta 142,43,24064\nQmvva 172,32,60\ns_waitcnt vmcnt(27)\nds_write_b128 v52, a[232:235] offset:25600\nQmvva 204,16,44\nv_add_u32 v75, 1, v75\n"
"Qmvva 204,36,60\ns_waitcnt vmcnt(30)\nds_write_b32 v73, v75\ns_waitcnt lgkmcnt(5)\nQmvva 236,0,208\nQmvva 236,20,48\nQda 52,53,4096\nQmvva 80,4,208\n"
"Qta 124,41,22016\nQmvva 80,24,48\nQta 126,41,24576\nQmvva 112,8,208\nbuffer_load_dwordx4 a[32:35], v60, s[48:51], s31 offen offset:3072 sc1\ns_add_u32 s95, s73, 125184\nQmvva 112,28,48\nbuffer_load_dwordx4 v62, s[52:55], s95 offen offset:2048 lds\n"
"Qmvva 144,12,208\nQmvva 144,32,48\nds_read_b32 v66, v74\nQmvva 176,16,208\ns_waitcnt vmcnt(27)\nds_write_b128 v52, a[236:239] offset:29696\nQmvva 176,36,48\ns_waitcnt lgkmcnt(4)\n"
"Qmvva 208,0,212\nQta 144,43,22016\nQmvva 208,20,52\ns_waitcnt lgkmcnt(2)\nv_cmp_gt_u32 vcc, v75, v66\nQmvva 240,4,212\ns_cbranch_vccnz .Lso53r0\n.Lro53r0:\n"
"Qmvva 240,24,52\nQda 56,53,8192\ns_add_u32 s31, s77, 28672\nQmvva 84,8,212\nbuffer_load_dwordx4 a[36:39], v60, s[48:51], s31 offen offset:0 sc1\ns_add_u32 s95, s73, 144448\nQmvva 84,28,52\nbuffer_load_dwordx4 v61, s[52:55], s95 offen offset:3072 lds\n"
"Qmvva 88,12,212\nQta 146,43,24576\nQmvva 88,32,52\ns_waitcnt vmcnt(27)\nds_write_b128 v52, a[240:243] offset:33792\nQmvva 120,16,212\nQmvva 120,36,52\ns_waitcnt lgkmcnt(2)\n"
"Qmvva 152,0,216\nQta 128,41,22528\nQmvva 152,20,56\nQda 60,53,12288\nQmvva 184,4,216\nQta 130,41,25088\nQmvva 184,24,56\nQmvva 216,8,216\n"
"buffer_load_dwordx4 a[40:43], v60, s[48:51], s31 offen offset:1024 sc1\ns_add_u32 m0, s96, 69632\ns_add_u32 s95, s73, 147648\nQmvva 216,28,56\nbuffer_load_dwordx4 v61, s[52:55], s95 offen lds\nQmvva 248,12,216\nQta 148,43,22528\nQmvva 248,32,56\n"
"s_waitcnt vmcnt(27)\nds_write_b128 v52, a[244:247] offset:37888\nQmvva 116,16,216\nQmvva 116,36,56\nQta 150,43,25088\ns_waitcnt lgkmcnt(4)\nQmvva 96,0,220\nQmvva 96,20,60\n"
"Qda 48,53,16384\nQmvva 128,4,220\nQta 84,40,0\nQta 86,40,2560\nQmvva 128,24,60\nQta 176,42,0\nQmvva 160,8,220\nbuffer_load_dwordx4 a[44:47], v60, s[48:51], s31 offen offset:2048 sc1\n"
"Qmvva 160,28,60\nQta 178,42,2560\nQmvva 192,12,220\nQta 88,40,512\nQmvva 192,32,60\ns_waitcnt vmcnt(26)\nds_write_b128 v52, a[248:251] offset:41984\nQmvva 224,16,220\n"
"v_add_u32 v75, 1, v75\nQmvva 224,36,60\ns_waitcnt vmcnt(25)\nds_write_b32 v73, v75\ns_waitcnt lgkmcnt(7)\nQmvaa 208,112,224\nQta 90,40,3072\nQmvaa 208,132,48\n"
"Qda 52,53,20480\nQmvaa 240,116,224\nQta 180,42,512\nQta 182,42,3072\nQmvaa 240,136,48\nQta 92,40,1024\nQmvaa 84,120,224\nbuffer_load_dwordx4 a[208:211], v60, s[48:51], s31 offen offset:3072 sc1\n"
"Qmvaa 84,140,48\nQta 94,40,3584\nQmvaa 88,124,224\nQta 184,42,1024\nQmvaa 88,144,48\nds_read_b32 v66, v74\nQmvaa 120,128,224\ns_waitcnt vmcnt(25)\n"
"ds_write_b128 v52, a[252:255] offset:46080\nQmvaa 120,148,48\ns_waitcnt lgkmcnt(7)\nQmvaa 152,112,228\nQta 186,42,3584\nQmvaa 152,132,52\ns_waitcnt lgkmcnt(2)\nv_cmp_gt_u32 vcc, v75, v66\n"
"Qmvaa 184,116,228\ns_cbranch_vccnz .Lso54r0\n.Lro54r0:\nQmvaa 184,136,52\nQda 56,53,25600\ns_add_u32 s31, s74, 43008\nQmvaa 216,120,228\nbuffer_load_dwordx4 a[212:215], v60, s[48:51], s31 offen offset:0 sc1\n"
"Qmvaa 216,140,52\nQta 96,40,1536\nQmvaa 248,124,228\nQta 98,40,4096\nQta 188,42,1536\nQmvaa 248,144,52\ns_waitcnt vmcnt(25)\nds_write_b128 v52, a[168:171] offset:50176\n"
"Qmvaa 116,128,228\nQmvaa 116,148,52\nQta 190,42,4096\ns_waitcnt lgkmcnt(5)\nQmvaa 96,112,232\nQta 100,40,2048\n.Lpg21:\ns_cbranch_execz .Lpg25\n"
"Qmvaa 96,132,56\nQda 60,53,29696\nQmvaa 128,116,232\nQta 102,40,4608\nQmvaa 128,136,56\nQta 192,42,2048\nQmvaa 160,120,232\nbuffer_load_dwordx4 a[216:219], v60, s[48:51], s31 offen offset:1024 sc1\n"
"Qmvaa 160,140,56\nQta 194,42,4608\nQmvaa 192,124,232\nQtv 0,40,20480\nQtv 2,40,23040\nQmvaa 192,144,56\ns_waitcnt vmcnt(25)\nds_write_b128 v52, a[172:175] offset:0\n"
"Qmvaa 224,128,232\nv_add_u32 v75, 1, v75\nQmvaa 224,148,56\ns_waitcnt vmcnt(4)\nds_write_b32 v73, v75\ns_waitcnt lgkmcnt(4)\nQmvaa 208,84,236\nQtv 20,42,20480\n"
"Qmvaa 208,176,60\nQda 48,53,33792\nQmvaa 240,88,236\nQtv 22,42,23040\nQtv 4,40,20992\nQmvaa 240,180,60\nQtv 6,40,23552\nQmvaa 84,92,236\n"
"buffer_load_dwordx4 a[220:223], v60, s[48:51], s31 offen offset:2048 sc1\nQmvaa 84,184,60\nQtv 24,42,20992\nQtv 26,42,23552\nQmvaa 88,96,236\nQtv 8,40,21504\nQmvaa 88,188,60\nds_read_b32 v66, v74\n"
"Qmvaa 120,100,236\ns_waitcnt vmcnt(24)\nds_write_b128 v52, a[0:3] offset:4096\nQmvaa 120,192,60\ns_waitcnt lgkmcnt(8)\nQmvaa 152,84,240\nQtv 10,40,24064\nQmvaa 152,176,48\n"
"s_waitcnt lgkmcnt(2)\nv_cmp_gt_u32 vcc, v75, v66\nQmvaa 184,88,240\ns_cbranch_vccnz .Lso55r0\n.Lro55r0:\nQmvaa 184,180,48\nQda 52,53,37888\nQmvaa 216,92,240\n"
"buffer_load_dwordx4 a[224:227], v60, s[48:51], s31 offen offset:3072 sc1\nQmvaa 216,184,48\nQtv 28,42,21504\nQtv 30,42,24064\nQmvaa 248,96,240\nQtv 12,40,22016\nQmvaa 248,188,48\ns_waitcnt vmcnt(23)\n"
"ds_write_b128 v52, a[4:7] offset:8192\nQmvaa 116,100,240\nQmvaa 116,192,48\nQtv 14,40,24576\nQtv 32,42,22016\ns_waitcnt lgkmcnt(6)\nQmvaa 96,84,244\nQtv 34,42,24576\n"
"Qmvaa 96,176,52\nQda 56,53,41984\nQmvaa 128,88,244\nQtv 16,40,22528\nQtv 18,40,25088\nQmvaa 128,180,52\nQtv 36,42,22528\ns_add_u32 s31, s75, 43008\n"
"Qmvaa 160,92,244\nbuffer_load_dwordx4 a[228:231], v60, s[48:51], s31 offen offset:0 sc1\nQmvaa 160,184,52\nQtv 38,42,25088\nQmvaa 192,96,244\nQta 112,40,40960\nQta 114,40,43520\nQmvaa 192,188,52\n"
"s_waitcnt vmcnt(22)\nds_write_b128 v52, a[8:11] offset:12288\nQmvaa 224,100,244\nQmvaa 224,192,52\nQta 132,42,40960\ns_waitcnt lgkmcnt(4)\nQmvva 208,0,248\nQta 134,42,43520\n"
"Qta 116,40,41472\nQmvva 208,20,56\nQda 60,53,46080\nQmvva 240,4,248\nQta 118,40,44032\nQmvva 240,24,56\nQta 136,42,41472\nQta 138,42,44032\n"
"Qmvva 84,8,248\nbuffer_load_dwordx4 a[232:235], v60, s[48:51], s31 offen offset:1024 sc1\nQmvva 84,28,56\nQta 120,40,41984\nQmvva 88,12,248\nQta 122,40,44544\nQmvva 88,32,56\ns_waitcnt vmcnt(21)\n"
"ds_write_b128 v52, a[12:15] offset:16384\nQmvva 120,16,248\nv_add_u32 v75, 1, v75\nQmvva 120,36,56\nds_write_b32 v73, v75\ns_waitcnt lgkmcnt(7)\nQmvva 152,0,252\nQta 140,42,41984\n"
"Qta 142,42,44544\nQmvva 152,20,60\nQda 48,53,50176\nQmvva 184,4,252\nQta 124,40,42496\nQmvva 184,24,60\nQta 126,40,45056\nQta 144,42,42496\n"
"Qmvva 216,8,252\nbuffer_load_dwordx4 a[236:239], v60, s[48:51], s31 offen offset:2048 sc1\nQmvva 216,28,60\nQta 146,42,45056\nQmvva 248,12,252\nQta 128,40,43008\nQta 130,40,45568\nQmvva 248,32,60\n"
"ds_read_b32 v66, v74\nQmvva 116,16,252\ns_waitcnt vmcnt(20)\nds_write_b128 v52, a[16:19] offset:20480\nQmvva 116,36,60\ns_waitcnt lgkmcnt(8)\nQmvva 96,0,168\nQta 148,42,43008\n"
"Qmvva 96,20,48\ns_waitcnt lgkmcnt(2)\nv_cmp_gt_u32 vcc, v75, v66\nQmvva 128,4,168\ns_cbranch_vccnz .Lso56r0\n.Lro56r0:\nQmvva 128,24,48\nQda 52,53,0\n"
"Qmvva 160,8,168\nbuffer_load_dwordx4 a[240:243], v60, s[48:51], s31 offen offset:3072 sc1\nQmvva 160,28,48\nQta 150,42,45568\nQmvva 192,12,168\nQta 84,41,0\nQta 86,41,2560\nQmvva 192,32,48\n"
"s_waitcnt vmcnt(20)\nds_write_b128 v52, a[20:23] offset:25600\nQmvva 224,16,168\nQmvva 224,36,48\nQta 176,43,0\ns_waitcnt lgkmcnt(4)\nQmvaa 208,112,172\nQta 178,43,2560\n"
"Qta 88,41,512\nQmvaa 208,132,52\nQda 56,53,4096\nQmvaa 240,116,172\nQta 90,41,3072\nQmvaa 240,136,52\nQta 180,43,512\nQta 182,43,3072\n"
"s_add_u32 s31, s77, 43008\nQmvaa 84,120,172\nbuffer_load_dwordx4 a[244:247], v60, s[48:51], s31 offen offset:0 sc1\nQmvaa 84,140,52\nQta 92,41,1024\nQmvaa 88,124,172\nQta 94,41,3584\nQmvaa 88,144,52\n"
"s_waitcnt vmcnt(20)\nds_write_b128 v52, a[24:27] offset:29696\nQmvaa 120,128,172\nv_add_u32 v75, 1, v75\nQmvaa 120,148,52\nds_write_b32 v73, v75\ns_waitcnt lgkmcnt(7)\nQmvaa 152,112,0\n"
"Qta 184,43,1024\nQta 186,43,3584\nQmvaa 152,132,56\nQda 60,53,8192\nQmvaa 184,116,0\nQta 96,41,1536\nQmvaa 184,136,56\nQta 98,41,4096\n"
"Qta 188,43,1536\nQmvaa 216,120,0\nbuffer_load_dwordx4 a[248:251], v60, s[48:51], s31 offen offset:1024 sc1\nQmvaa 216,140,56\nQta 190,43,4096\nQmvaa 248,124,0\nQta 100,41,2048\nQta 102,41,4608\n"
"Qmvaa 248,144,56\nds_read_b32 v66, v74\nQmvaa 116,128,0\ns_waitcnt vmcnt(19)\nds_write_b128 v52, a[28:31] offset:33792\nQmvaa 116,148,56\ns_waitcnt lgkmcnt(8)\nQmvaa 96,112,4\n"
"Qta 192,43,2048\nQmvaa 96,132,60\ns_waitcnt lgkmcnt(2)\nv_cmp_gt_u32 vcc, v75, v66\nQmvaa 128,116,4\ns_cbranch_vccnz .Lso57r0\n.Lro57r0:\nQmvaa 128,136,60\n"
"Qda 48,53,12288\nQmvaa 160,120,4\nbuffer_load_dwordx4 a[252:255], v60, s[48:51], s31 offen offset:2048 sc1\nQmvaa 160,140,60\nQta 194,43,4608\nQmvaa 192,124,4\nQtv 0,41,20480\nQtv 2,41,23040\n"
"Qmvaa 192,144,60\ns_waitcnt vmcnt(18)\nds_write_b128 v52, a[32:35] offset:37888\nQmvaa 224,128,4\nQmvaa 224,148,60\nQtv 20,43,20480\ns_waitcnt lgkmcnt(4)\nQmvaa 208,84,8\n"
"Qtv 22,43,23040\nQmvaa 208,176,48\nQda 52,53,16384\ns_add_u32 s68, s64, 0\nQmvaa 240,88,8\nQtv 4,41,20992\nQmvaa 240,180,48\nQtv 6,41,23552\n"
"Qmvaa 84,92,8\nbuffer_load_dwordx4 a[168:171], v60, s[48:51], s31 offen offset:3072 sc1\nQmvaa 84,184,48\nQtv 24,43,20992\nQk 208,208,209\nQmvaa 88,96,8\nQtv 26,43,23552\nQk 209,210,211\n"
"Qmvaa 88,188,48\ns_waitcnt vmcnt(17)\nds_write_b128 v52, a[36:39] offset:41984\nQmvaa 120,100,8\nQk 210,240,241\nQk 211,242,243\nQmvaa 120,192,48\nQtv 8,41,21504\n"
"buffer_store_dwordx4 v[208:211], v60, s[24:27], s68 offen offset:0 sc1\ns_waitcnt lgkmcnt(6)\nQmvaa 152,84,12\nQtv 10,41,24064\nQk 84,84,85\nQmvaa 152,176,52\nQda 56,53,20480\nQk 85,86,87\n"
"Qmvaa 184,88,12\nQtv 28,43,21504\nQk 86,88,89\nQmvaa 184,180,52\nQtv 30,43,24064\nQk 87,90,91\nQmvaa 216,92,12\nQtv 12,41,22016\n"
"buffer_store_dwordx4 v[84:87], v60, s[24:27], s68 offen offset:1024 sc1\nQmvaa 216,184,52\nQtv 14,41,24576\nQk 120,120,121\nQmvaa 248,96,12\nQtv 32,43,22016\nQk 121,122,123\nQmvaa 248,188,52\n"
"s_waitcnt vmcnt(17)\nds_write_b128 v52, a[40:43] offset:46080\nQmvaa 116,100,12\nv_add_u32 v75, 1, v75\nQk 122,152,153\nQmvaa 116,192,52\nds_write_b32 v73, v75\nQk 123,154,155\n"
"s_waitcnt lgkmcnt(7)\nQmvaa 96,84,16\nQtv 34,43,24576\nbuffer_store_dwordx4 v[120:123], v60, s[24:27], s68 offen offset:2048 sc1\nQmvaa 96,176,56\nQda 60,53,25600\nQk 184,184,185\nQmvaa 128,88,16\n"
"Qtv 16,41,22528\nQk 185,186,187\nQmvaa 128,180,56\nQtv 18,41,25088\nQk 186,216,217\nQmvaa 160,92,16\nQtv 36,43,22528\nQk 187,218,219\n"
"Qmvaa 160,184,56\nQtv 38,43,25088\nbuffer_store_dwordx4 v[184:187], v60, s[24:27], s68 offen offset:3072 sc1\nQmvaa 192,96,16\nQta 112,40,0\nQk 248,248,249\nQmvaa 192,188,56\nds_read_b32 v66, v74\n"
"Qk 249,250,251\nQmvaa 224,100,16\ns_waitcnt vmcnt(17)\nds_write_b128 v52, a[44:47] offset:50176\nQmvaa 224,192,56\nQk 250,116,117\nQk 251,118,119\ns_waitcnt lgkmcnt(7)\n"
"Qmvva 92,0,20\nQta 114,40,2560\ns_add_u32 s68, s64, 4096\nQmvva 92,20,60\ns_waitcnt lgkmcnt(2)\nv_cmp_gt_u32 vcc, v75, v66\nQmvva 124,4,20\nbuffer_store_dwordx4 v[248:251], v60, s[24:27], s68 offen offset:0 sc1\n"
"s_cbranch_vccnz .Lso58r0\n.Lro58r0:\nQmvva 124,24,60\nQda 48,53,29696\nQk 96,96,97\nQmvva 156,8,20\nQta 132,42,0\nQk 97,98,99\n"
"Qmvva 156,28,60\nQta 134,42,2560\nQk 98,128,129\nQmvva 188,12,20\nQta 116,40,512\nQk 99,130,131\nQmvva 188,32,60\ns_waitcnt vmcnt(17)\n"
"ds_write_b128 v52, a[208:211] offset:0\nQmvva 220,16,20\nbuffer_store_dwordx4 v[96:99], v60, s[24:27], s68 offen offset:1024 sc1\nQk 160,160,161\nQmvva 220,36,60\nQta 118,40,3072\nQk 161,162,163\ns_waitcnt lgkmcnt(5)\n"
"Qmvva 252,0,24\nQta 136,42,512\nQk 162,192,193\nQmvva 252,20,48\nQda 52,53,33792\nQk 163,194,195\nQmvva 100,4,24\nQta 138,42,3072\n"
"buffer_store_dwordx4 v[160:163], v60, s[24:27], s68 offen offset:2048 sc1\nQmvva 100,24,48\nQta 120,40,1024\nQk 224,224,225\nQmvva 132,8,24\nQta 122,40,3584\nQk 225,226,227\nQmvva 132,28,48\n"
"Qta 140,42,1024\nv_mov_b32 v226, 0\nQmvva 164,12,24\nQta 142,42,3584\nv_mov_b32 v227, 0\nQmvva 164,32,48\ns_waitcnt vmcnt(18)\nds_write_b128 v52, a[212:215] offset:4096\n"
"Qmvva 196,16,24\nv_add_u32 v75, 1, v75\ns_add_u32 s68, s64, 14336\nQmvva 196,36,48\nds_write_b32 v73, v75\nbuffer_store_dwordx4 v[224:227], v60, s[24:27], s68 offen offset:0 sc1\ns_waitcnt lgkmcnt(7)\nQmvva 228,0,28\n"
"Qta 124,40,1536\nQmvva 228,20,52\nQda 56,53,37888\nQmvva 108,4,28\nQta 126,40,4096\nQmvva 108,24,52\nQta 144,42,1536\nQmvva 140,8,28\n"
"Qta 146,42,4096\nQmvva 140,28,52\nQta 128,40,2048\nQmvva 172,12,28\nQta 130,40,4608\nQmvva 172,32,52\nds_read_b32 v66, v74\nQmvva 204,16,28\n"
"s_waitcnt vmcnt(18)\nds_write_b128 v52, a[216:219] offset:8192\nQmvva 204,36,52\ns_waitcnt lgkmcnt(7)\nQmvva 236,0,32\nQta 148,42,2048\nQmvva 236,20,56\ns_waitcnt lgkmcnt(2)\n"
"v_cmp_gt_u32 vcc, v75, v66\nQmvva 80,4,32\ns_cbranch_vccnz .Lso59r0\n.Lro59r0:\nQmvva 80,24,56\nQda 60,53,41984\nQmvva 112,8,32\nQta 150,42,4608\n"
"Qmvva 112,28,56\nQta 84,40,20480\nQmvva 144,12,32\nQta 86,40,23040\nQmvva 144,32,56\ns_waitcnt vmcnt(17)\nds_write_b128 v52, a[220:223] offset:12288\nQmvva 176,16,32\n"
"Qmvva 176,36,56\nQta 176,42,20480\ns_waitcnt lgkmcnt(4)\nQmvaa 92,112,36\nQta 178,42,23040\nQmvaa 92,132,60\nQda 48,53,46080\nQmvaa 124,116,36\n"
"Qta 88,40,20992\nQmvaa 124,136,60\nQta 90,40,23552\nQmvaa 156,120,36\nQta 180,42,20992\nQmvaa 156,140,60\nQmvaa 188,124,36\nQta 182,42,23552\n"
"Qmvaa 188,144,60\ns_waitcnt vmcnt(16)\nds_write_b128 v52, a[224:227] offset:16384\nQmvaa 220,128,36\nQmvaa 220,148,60\nQta 92,40,21504\ns_waitcnt lgkmcnt(6)\nQmvaa 252,112,40\n"
"Qta 94,40,24064\nQmvaa 252,132,48\nQda 52,53,50176\nQmvaa 100,116,40\nQta 184,42,21504\nQmvaa 100,136,48\nQta 186,42,24064\nQmvaa 132,120,40\n"
"Qta 96,40,22016\nQmvaa 132,140,48\nQta 98,40,24576\nQmvaa 164,124,40\nQmvaa 164,144,48\ns_waitcnt vmcnt(15)\nds_write_b128 v52, a[228:231] offset:20480\nQmvaa 196,128,40\n"
"v_add_u32 v75, 1, v75\nQmvaa 196,148,48\nds_write_b32 v73, v75\ns_waitcnt lgkmcnt(6)\nQmvaa 228,112,44\nQta 188,42,22016\nQmvaa 228,132,52\nQda 56,53,0\n"
"Qmvaa 108,116,44\nQta 190,42,24576\nQmvaa 108,136,52\nQta 100,40,22528\nQmvaa 140,120,44\nQta 102,40,25088\nQmvaa 140,140,52\nQta 192,42,22528\n"
"Qmvaa 172,124,44\nQta 194,42,25088\nQmvaa 172,144,52\nds_read_b32 v66, v74\nQmvaa 204,128,44\ns_waitcnt vmcnt(14)\nds_write_b128 v52, a[232:235] offset:25600\nQmvaa 204,148,52\n"
"s_waitcnt lgkmcnt(7)\nQmvaa 236,112,208\nQmvaa 236,132,56\ns_waitcnt lgkmcnt(1)\nv_cmp_gt_u32 vcc, v75, v66\nQmvaa 80,116,208\ns_cbranch_vccnz .Lso60r0\n.Lro60r0:\n"
"Qmvaa 80,136,56\nQda 60,53,4096\nQmvaa 112,120,208\nQtv 0,40,40960\nQmvaa 112,140,56\nQtv 2,40,43520\nQmvaa 144,124,208\nQtv 20,42,40960\n"
"Qmvaa 144,144,56\ns_waitcnt vmcnt(13)\nds_write_b128 v52, a[236:239] offset:29696\nQmvaa 176,128,208\nQmvaa 176,148,56\nQtv 22,42,43520\ns_waitcnt vmcnt(0)\nbuffer_store_dword v67, v77, s[24:27], 0 offen sc1\n"
"s_waitcnt lgkmcnt(5)\nQmvaa 92,84,212\nQtv 4,40,41472\nQmvaa 92,176,60\nQda 48,53,8192\nQmvaa 124,88,212\nQtv 6,40,44032\nQmvaa 124,180,60\n"
"Qtv 24,42,41472\nQmvaa 156,92,212\nQtv 26,42,44032\nQmvaa 156,184,60\nQtv 8,40,41984\nQmvaa 188,96,212\nQtv 10,40,44544\nQmvaa 188,188,60\n"
"s_waitcnt vmcnt(13)\nds_write_b128 v52, a[240:243] offset:33792\nQmvaa 220,100,212\nv_add_u32 v75, 1, v75\nQmvaa 220,192,60\nds_write_b32 v73, v75\ns_waitcnt lgkmcnt(7)\nQmvaa 252,84,216\n"
"Qmvaa 252,176,48\nQda 52,53,12288\nQmvaa 100,88,216\nQtv 28,42,41984\nQmvaa 100,180,48\nQtv 30,42,44544\nQmvaa 132,92,216\nQtv 12,40,42496\n"
"Qmvaa 132,184,48\nQtv 14,40,45056\n.Lpg22:\ns_cbranch_execz .Lpg26\nQmvaa 164,96,216\nQtv 32,42,42496\nQmvaa 164,188,48\nds_read_b32 v66, v74\n"
"Qmvaa 196,100,216\ns_waitcnt vmcnt(12)\nds_write_b128 v52, a[244:247] offset:37888\nQmvaa 196,192,48\ns_waitcnt lgkmcnt(7)\nQmvaa 228,84,220\nQtv 34,42,45056\nQmvaa 228,176,52\n"
"s_waitcnt lgkmcnt(2)\nv_cmp_gt_u32 vcc, v75, v66\nQmvaa 108,88,220\ns_cbranch_vccnz .Lso61r0\n.Lro61r0:\nQmvaa 108,180,52\nQda 56,53,16384\nQmvaa 140,92,220\n"
"Qtv 16,40,43008\nQmvaa 140,184,52\nQtv 18,40,45568\nQmvaa 172,96,220\nQtv 36,42,43008\nQmvaa 172,188,52\ns_waitcnt vmcnt(11)\nds_write_b128 v52, a[248:251] offset:41984\n"
"Qmvaa 204,100,220\nQmvaa 204,192,52\nQtv 38,42,45568\ns_waitcnt lgkmcnt(5)\nQmvaa 236,84,224\nQmvaa 236,176,56\nQda 60,53,20480\nQmvaa 80,88,224\n"
"Qta 112,41,0\nQmvaa 80,180,56\nQta 114,41,2560\nQmvaa 112,92,224\nQta 132,43,0\nQmvaa 112,184,56\nQmvaa 144,96,224\nQta 134,43,2560\n"
"Qmvaa 144,188,56\ns_waitcnt vmcnt(10)\nds_write_b128 v52, a[252:255] offset:46080\nQmvaa 176,100,224\nQmvaa 176,192,56\nQta 116,41,512\ns_waitcnt lgkmcnt(6)\nQmvva 92,0,228\n"
"Qta 118,41,3072\nQmvva 92,20,60\nQda 48,53,25600\nQmvva 124,4,228\nQmvva 124,24,60\nQta 136,43,512\nQmvva 156,8,228\nQta 138,43,3072\n"
"Qmvva 156,28,60\nQta 120,41,1024\nQmvva 188,12,228\nQmvva 188,32,60\ns_waitcnt vmcnt(9)\nds_write_b128 v52, a[168:171] offset:50176\nQmvva 220,16,228\nv_add_u32 v75, 1, v75\n"
"Qmvva 220,36,60\nds_write_b32 v73, v75\ns_waitcnt lgkmcnt(5)\nQmvva 252,0,232\nQta 122,41,3584\nQmvva 252,20,48\nQda 52,53,29696\nQmvva 100,4,232\n"
"Qta 140,43,1024\nQmvva 100,24,48\nQta 142,43,3584\nQmvva 132,8,232\nQmvva 132,28,48\nQta 124,41,1536\nQmvva 164,12,232\nQta 126,41,4096\n"
"Qmvva 164,32,48\nds_read_b32 v66, v74\nQmvva 196,16,232\nQta 144,43,1536\nQmvva 196,36,48\ns_waitcnt lgkmcnt(6)\nQmvva 228,0,236\nbuffer_load_dword v78, v76, s[24:27], 0 offen sc1\n"
"Qmvva 228,20,52\ns_waitcnt lgkmcnt(1)\nv_cmp_gt_u32 vcc, v75, v66\nQmvva 108,4,236\ns_cbranch_vccnz .Lso62r0\n.Lro62r0:\nQmvva 108,24,52\nQda 56,53,33792\n"
"Qmvva 140,8,236\nQta 146,43,4096\nQmvva 140,28,52\nQta 128,41,2048\nQmvva 172,12,236\nQta 130,41,4608\nQmvva 172,32,52\nQmvva 204,16,236\n"
"Qta 148,43,2048\nQmvva 204,36,52\nQta 150,43,4608\ns_waitcnt lgkmcnt(5)\nQmvva 236,0,240\nQmvva 236,20,56\nQda 60,53,37888\nQmvva 80,4,240\n"
"Qmvva 80,24,56\nQmvva 112,8,240\nQmvva 112,28,56\nQmvva 144,12,240\nQmvva 144,32,56\nQmvva 176,16,240\nv_add_u32 v75, 1, v75\nQmvva 176,36,56\n"
"ds_write_b32 v73, v75\ns_waitcnt lgkmcnt(1)\nQmvaa 92,112,244\nQmvaa 92,132,60\nQda 48,53,41984\nQmvaa 124,116,244\nQmvaa 124,136,60\nQmvaa 156,120,244\n"
"Qmvaa 156,140,60\nQmvaa 188,124,244\nQmvaa 188,144,60\nds_read_b32 v66, v74\nQmvaa 220,128,244\nQmvaa 220,148,60\ns_waitcnt lgkmcnt(1)\nQmvaa 252,112,248\n"
"Qmvaa 252,132,48\ns_waitcnt lgkmcnt(0)\nv_cmp_gt_u32 vcc, v75, v66\nQmvaa 100,116,248\ns_cbranch_vccnz .Lso63r0\n.Lro63r0:\nQmvaa 100,136,48\nQda 52,53,46080\n"
"Qmvaa 132,120,248\nQmvaa 132,140,48\ns_waitcnt vmcnt(0)\nv_sub_u32 v66, v78, v65\nv_cmp_gt_u32 vcc, 1, v66\ns_and_b64 vcc, vcc, s[28:29]\ns_cbranch_scc1 .Lx0_w\n.Lx0_r:\n"
"Qmvaa 164,124,248\ns_add_u32 s69, s98, 0\nbuffer_load_dwordx4 v[240:243], v60, s[24:27], s69 offen offset:0 sc1\nQmvaa 164,144,48\nQmvaa 196,128,248\nbuffer_load_dwordx4 v[88:91], v60, s[24:27], s69 offen offset:1024 sc1\nQmvaa 196,148,48\ns_waitcnt lgkmcnt(0)\n"
"Qmvaa 228,112,252\nbuffer_load_dwordx4 v[152:155], v60, s[24:27], s69 offen offset:2048 sc1\nQmvaa 228,132,52\nQda 56,53,50176\nQmvaa 108,116,252\nbuffer_load_dwordx4 v[216:219], v60, s[24:27], s69 offen offset:3072 sc1\nQmvaa 108,136,52\nQmvaa 140,120,252\n"
"s_add_u32 s69, s98, 4096\nbuffer_load_dwordx4 v[116:119], v60, s[24:27], s69 offen offset:0 sc1\nQmvaa 140,140,52\nQmvaa 172,124,252\nbuffer_load_dwordx4 v[128:131], v60, s[24:27], s69 offen offset:1024 sc1\nQmvaa 172,144,52\nQmvaa 204,128,252\nbuffer_load_dwordx4 v[192:195], v60, s[24:27], s69 offen offset:2048 sc1\n"
"Qmvaa 204,148,52\ns_waitcnt lgkmcnt(0)\nQmvaa 236,112,168\ns_add_u32 s69, s98, 14336\nbuffer_load_dwordx4 v[148:151], v60, s[24:27], s69 offen offset:0 sc1\nQmvaa 236,132,56\nQmvaa 80,116,168\nbuffer_load_dwordx4 v[180:183], v60, s[24:27], s69 offen offset:1024 sc1\n"
"Qmvaa 80,136,56\nQmvaa 112,120,168\nbuffer_load_dwordx4 v[104:107], v60, s[24:27], s69 offen offset:2048 sc1\nQmvaa 112,140,56\nQmvaa 144,124,168\nQmvaa 144,144,56\nQmvaa 176,128,168\nQmvaa 176,148,56\n"
"s_waitcnt vmcnt(9)\nQmvvv 92,46,240\nQmvvv 124,44,240\ns_waitcnt vmcnt(8)\nQmvvv 156,46,88\nQmvvv 188,44,88\ns_nop 3\nQk 92,92,93\n"
"Qk 93,94,95\nds_write_b64 v63, v[92:93] offset:0\ns_waitcnt vmcnt(7)\nQmvvv 220,46,152\nQk 124,124,125\nQk 125,126,127\nds_write_b64 v63, v[124:125] offset:64\nQmvvv 252,44,152\n"
"Qk 156,156,157\nQk 157,158,159\nds_write_b64 v63, v[156:157] offset:128\ns_waitcnt vmcnt(6)\nQmvvv 100,46,216\nQk 188,188,189\nQk 189,190,191\nds_write_b64 v63, v[188:189] offset:192\n"
"Qmvvv 132,44,216\nQk 220,220,221\nQk 221,222,223\nds_write_b64 v63, v[220:221] offset:256\ns_waitcnt vmcnt(5)\nQmvvv 164,46,116\nQk 252,252,253\nQk 253,254,255\n"
"ds_write_b64 v63, v[252:253] offset:5376\nQmvvv 196,44,116\nQk 100,100,101\nQk 101,102,103\nds_write_b64 v63, v[100:101] offset:5440\ns_waitcnt vmcnt(4)\nQmvvv 228,46,128\nQk 132,132,133\n"
"Qk 133,134,135\nds_write_b64 v63, v[132:133] offset:5504\nQmvvv 108,44,128\nQk 164,164,165\nQk 165,166,167\nds_write_b64 v63, v[164:165] offset:5568\ns_waitcnt vmcnt(3)\nQmvvv 140,46,192\n"
"Qk 196,196,197\nQk 197,198,199\nds_write_b64 v63, v[196:197] offset:5632\nQmvvv 172,44,192\nQk 228,228,229\nQk 229,230,231\nds_write_b64 v63, v[228:229] offset:10752\ns_waitcnt vmcnt(2)\n"
"Qmvvv 204,46,148\nQk 108,108,109\nQk 109,110,111\nds_write_b64 v63, v[108:109] offset:10816\nQmvvv 236,44,148\nQk 140,140,141\nQk 141,142,143\nds_write_b64 v63, v[140:141] offset:10880\n"
"s_waitcnt vmcnt(1)\nQmvvv 80,46,180\nQk 172,172,173\nQk 173,174,175\nds_write_b64 v63, v[172:173] offset:10944\nQmvvv 112,44,180\nQk 204,204,205\nQk 205,206,207\n"
"ds_write_b64 v63, v[204:205] offset:11008\ns_waitcnt vmcnt(0)\nQmvvv 144,46,104\nQk 236,236,237\nQk 237,238,239\nds_write_b64 v63, v[236:237] offset:16128\nQmvvv 176,44,104\nQk 80,80,81\n"
"Qk 81,82,83\nds_write_b64 v63, v[80:81] offset:16192\nQk 112,112,113\nQk 113,114,115\nds_write_b64 v63, v[112:113] offset:16256\nQk 144,144,145\nQk 145,146,147\nds_write_b64 v63, v[144:145] offset:16320\n"
"Qk 176,176,177\nQk 177,178,179\nds_write_b64 v63, v[176:177] offset:16384\ns_branch .Lfin\n.Lrole1:\n.Lpg23:\ns_cbranch_execz .Lpg27\ns_waitcnt lgkmcnt(4)\n"
"Qmvva 236,0,228\nQta 142,42,24064\ns_lshl_b32 s46, %19, 4\nQmvva 236,20,60\nQda 48,53,25600\ns_add_u32 s46, s46, 128\nQmvva 80,4,228\nQta 124,40,22016\n"
"v_mov_b32 v77, 0x7ffff000\nQmvva 80,24,60\ns_add_u32 s31, s74, 32768\nQmvva 112,8,228\nbuffer_load_dwordx4 a[212:215], v60, s[48:51], s31 offen offset:0 sc1\ns_add_u32 s95, s73, 84224\nQmvva 112,28,60\nbuffer_load_dwordx4 v62, s[52:55], s95 offen offset:2048 lds\n"
"Qmvva 144,12,228\nQta 126,40,24576\nQmvva 144,32,60\nds_read_b32 v66, v74\nQmvva 176,16,228\ns_waitcnt vmcnt(29)\nds_write_b128 v52, a[168:171] offset:50176\nQmvva 176,36,60\n"
"s_waitcnt lgkmcnt(4)\nQmvva 208,0,232\nQta 144,42,22016\nQmvva 208,20,48\ns_waitcnt lgkmcnt(2)\nv_cmp_gt_u32 vcc, v75, v66\nQmvva 240,4,232\ns_cbranch_vccnz .Lso47r1\n"
".Lro47r1:\nQmvva 240,24,48\nQda 52,53,29696\nQmvva 84,8,232\nbuffer_load_dwordx4 a[216:219], v60, s[48:51], s31 offen offset:1024 sc1\ns_add_u32 s95, s73, 103488\nQmvva 84,28,48\nbuffer_load_dwordx4 v61, s[52:55], s95 offen offset:3072 lds\n"
"Qmvva 88,12,232\nQmvva 88,32,48\ns_waitcnt vmcnt(28)\nds_write_b128 v52, a[172:175] offset:0\nQmvva 120,16,232\nQmvva 120,36,48\nQta 146,42,24576\ns_waitcnt lgkmcnt(2)\n"
"Qmvva 152,0,236\nQta 128,40,22528\nQmvva 152,20,52\nQda 56,53,33792\nQmvva 184,4,236\nQta 130,40,25088\nQmvva 184,24,52\nQmvva 216,8,236\n"
"buffer_load_dwordx4 a[220:223], v60, s[48:51], s31 offen offset:2048 sc1\ns_add_u32 m0, s96, 8192\ns_add_u32 s95, s73, 106688\nQmvva 216,28,52\nbuffer_load_dwordx4 v61, s[52:55], s95 offen lds\nQmvva 248,12,236\nQta 148,42,22528\nQmvva 248,32,52\n"
"s_waitcnt vmcnt(28)\nds_write_b128 v52, a[0:3] offset:4096\nQmvva 116,16,236\nQmvva 116,36,52\nQta 150,42,25088\ns_waitcnt lgkmcnt(4)\nQmvva 96,0,240\nQmvva 96,20,56\n"
"Qda 60,53,37888\nQmvva 128,4,240\nQta 84,40,40960\nQmvva 128,24,56\nQta 86,40,43520\ns_add_u32 s31, s75, 28672\nQmvva 160,8,240\nbuffer_load_dwordx4 a[224:227], v60, s[48:51], s31 offen offset:0 sc1\n"
"Qmvva 160,28,56\nQta 176,42,40960\nQmvva 192,12,240\nQmvva 192,32,56\ns_waitcnt vmcnt(27)\nds_write_b128 v52, a[4:7] offset:8192\nQmvva 224,16,240\nv_add_u32 v75, 1, v75\n"
"Qmvva 224,36,56\nds_write_b32 v73, v75\ns_waitcnt lgkmcnt(5)\nQmvaa 92,112,244\nQta 178,42,43520\nQmvaa 92,132,60\nQda 48,53,41984\nQmvaa 124,116,244\n"
"Qta 88,40,41472\nQmvaa 124,136,60\nQmvaa 156,120,244\nbuffer_load_dwordx4 a[228:231], v60, s[48:51], s31 offen offset:1024 sc1\nQmvaa 156,140,60\nQta 90,40,44032\nQmvaa 188,124,244\nQta 180,42,41472\n"
"Qmvaa 188,144,60\nds_read_b32 v66, v74\nQmvaa 220,128,244\ns_waitcnt vmcnt(26)\nds_write_b128 v52, a[8:11] offset:12288\nQmvaa 220,148,60\ns_waitcnt lgkmcnt(5)\nQmvaa 252,112,248\n"
"Qmvaa 252,132,48\ns_waitcnt lgkmcnt(1)\nv_cmp_gt_u32 vcc, v75, v66\nQmvaa 100,116,248\ns_cbranch_vccnz .Lso48r1\n.Lro48r1:\nQmvaa 100,136,48\nQda 52,53,46080\n"
"Qmvaa 132,120,248\nbuffer_load_dwordx4 a[232:235], v60, s[48:51], s31 offen offset:2048 sc1\ns_add_u32 m0, s96, 24576\ns_add_u32 s95, s67, 126976\nQmvaa 132,140,48\nbuffer_load_dwordx4 v61, s[52:55], s95 offen lds\nQmvaa 164,124,248\nQta 182,42,44032\n"
"Qmvaa 164,144,48\ns_waitcnt vmcnt(26)\nds_write_b128 v52, a[12:15] offset:16384\nQmvaa 196,128,248\nQmvaa 196,148,48\nQta 92,40,41984\ns_waitcnt lgkmcnt(3)\nQmvaa 228,112,252\n"
"v_cmp_eq_u32 s[44:45], s46, v60\ns_nop 1\nv_cndmask_b32 v77, v77, v76, s[44:45]\nv_add_u32 v67, 1, v65\ns_xor_b32 s46, %19, 12\ns_lshl_b32 s28, 1, s46\nQmvaa 228,132,52\nQda 56,53,50176\n"
"Qmvaa 108,116,252\nQta 94,40,44544\nQmvaa 108,136,52\nQta 184,42,41984\nQmvaa 140,120,252\nbuffer_load_dwordx4 a[236:239], v60, s[48:51], s31 offen offset:3072 sc1\ns_add_u32 s95, s67, 126080\nQmvaa 140,140,52\n"
"buffer_load_dwordx4 v61, s[52:55], s95 offen offset:1024 lds\nQmvaa 172,124,252\nQta 186,42,44544\nQmvaa 172,144,52\ns_waitcnt vmcnt(27)\nds_write_b128 v52, a[16:19] offset:20480\nQmvaa 204,128,252\nv_add_u32 v75, 1, v75\n"
"Qmvaa 204,148,52\ns_waitcnt vmcnt(31)\nds_write_b32 v73, v75\ns_waitcnt lgkmcnt(5)\nQmvaa 236,112,168\nQmvaa 236,132,56\nQda 60,53,0\nQmvaa 80,116,168\n"
"Qta 96,40,42496\nQmvaa 80,136,56\nQta 98,40,45056\ns_add_u32 s31, s77, 28672\nQmvaa 112,120,168\nbuffer_load_dwordx4 a[240:243], v60, s[48:51], s31 offen offset:0 sc1\ns_add_u32 s95, s67, 125184\nQmvaa 112,140,56\n"
"buffer_load_dwordx4 v62, s[52:55], s95 offen offset:2048 lds\nQmvaa 144,124,168\nQmvaa 144,144,56\nds_read_b32 v66, v74\nQmvaa 176,128,168\ns_waitcnt vmcnt(27)\nds_write_b128 v52, a[20:23] offset:25600\nQmvaa 176,148,56\n"
"s_waitcnt lgkmcnt(4)\nQmvaa 208,112,172\nQta 188,42,42496\nQmvaa 208,132,60\ns_waitcnt lgkmcnt(2)\nv_cmp_gt_u32 vcc, v75, v66\nQmvaa 240,116,172\ns_cbranch_vccnz .Lso49r1\n"
".Lro49r1:\nQmvaa 240,136,60\nQda 48,53,4096\nQmvaa 84,120,172\nbuffer_load_dwordx4 a[244:247], v60, s[48:51], s31 offen offset:1024 sc1\ns_add_u32 s95, s67, 144448\nQmvaa 84,140,60\nbuffer_load_dwordx4 v61, s[52:55], s95 offen offset:3072 lds\n"
"Qmvaa 88,124,172\nQta 190,42,45056\nQmvaa 88,144,60\ns_waitcnt vmcnt(27)\nds_write_b128 v52, a[24:27] offset:29696\nQmvaa 120,128,172\nQmvaa 120,148,60\ns_waitcnt lgkmcnt(2)\n"
"Qmvaa 152,112,0\nQta 100,40,43008\nQmvaa 152,132,48\nQda 52,53,8192\nQmvaa 184,116,0\nQta 102,40,45568\nQmvaa 184,136,48\nQmvaa 216,120,0\n"
"buffer_load_dwordx4 a[248:251], v60, s[48:51], s31 offen offset:2048 sc1\ns_add_u32 m0, s96, 28672\ns_add_u32 s95, s67, 147648\nQmvaa 216,140,48\nbuffer_load_dwordx4 v61, s[52:55], s95 offen lds\nQmvaa 248,124,0\nQta 192,42,43008\nQmvaa 248,144,48\n"
"s_waitcnt vmcnt(27)\nds_write_b128 v52, a[28:31] offset:33792\nQmvaa 116,128,0\nQmvaa 116,148,48\nQta 194,42,45568\ns_waitcnt lgkmcnt(4)\nQmvaa 96,112,4\nQmvaa 96,132,52\n"
"Qda 56,53,12288\nQmvaa 128,116,4\nQtv 0,41,0\nQmvaa 128,136,52\nQtv 2,41,2560\nQmvaa 160,120,4\nbuffer_load_dwordx4 a[252:255], v60, s[48:51], s31 offen offset:3072 sc1\nQmvaa 160,140,52\n"
"Qtv 20,43,0\nQmvaa 192,124,4\nQmvaa 192,144,52\ns_waitcnt vmcnt(26)\nds_write_b128 v52, a[32:35] offset:37888\nQmvaa 224,128,4\nv_add_u32 v75, 1, v75\nQmvaa 224,148,52\n"
"ds_write_b32 v73, v75\ns_waitcnt lgkmcnt(5)\nQmvaa 92,84,8\nQtv 22,43,2560\nQmvaa 92,176,56\nQda 60,53,16384\nQmvaa 124,88,8\nQtv 4,41,512\n"
"Qmvaa 124,180,56\ns_add_u32 s31, s74, 43008\nQmvaa 156,92,8\nbuffer_load_dwordx4 a[168:171], v60, s[48:51], s31 offen offset:0 sc1\nQmvaa 156,184,56\nQtv 6,41,3072\nQmvaa 188,96,8\nQtv 24,43,512\n"
"Qmvaa 188,188,56\nds_read_b32 v66, v74\nQmvaa 220,100,8\ns_waitcnt vmcnt(25)\nds_write_b128 v52, a[36:39] offset:41984\nQmvaa 220,192,56\ns_waitcnt lgkmcnt(5)\nQmvaa 252,84,12\n"
".Lpg24:\ns_cbranch_execz .Ltr8\nQmvaa 252,176,60\ns_waitcnt lgkmcnt(1)\nv_cmp_gt_u32 vcc, v75, v66\nQmvaa 100,88,12\ns_cbranch_vccnz .Lso50r1\n.Lro50r1:\n"
"Qmvaa 100,180,60\nQda 48,53,20480\nQmvaa 132,92,12\nbuffer_load_dwordx4 a[172:175], v60, s[48:51], s31 offen offset:1024 sc1\ns_add_u32 m0, s96, 45056\ns_add_u32 s95, s72, 126976\nQmvaa 132,184,60\nbuffer_load_dwordx4 v61, s[52:55], s95 offen lds\n"
"Qmvaa 164,96,12\nQtv 26,43,3072\nQmvaa 164,188,60\ns_waitcnt vmcnt(26)\nds_write_b128 v52, a[40:43] offset:46080\nQmvaa 196,100,12\nQmvaa 196,192,60\nQtv 8,41,1024\n"
"s_waitcnt lgkmcnt(3)\nQmvaa 228,84,16\nQmvaa 228,176,48\nQda 52,53,25600\nQmvaa 108,88,16\nQtv 10,41,3584\nQmvaa 108,180,48\nQtv 28,43,1024\n"
"Qmvaa 140,92,16\nbuffer_load_dwordx4 a[0:3], v60, s[48:51], s31 offen offset:2048 sc1\ns_add_u32 s95, s72, 126080\nQmvaa 140,184,48\nbuffer_load_dwordx4 v61, s[52:55], s95 offen offset:1024 lds\nQmvaa 172,96,16\nQtv 30,43,3584\nQmvaa 172,188,48\n"
"s_waitcnt vmcnt(27)\nds_write_b128 v52, a[44:47] offset:50176\nQmvaa 204,100,16\nv_add_u32 v75, 1, v75\nQmvaa 204,192,48\ns_waitcnt vmcnt(30)\nds_write_b32 v73, v75\ns_waitcnt lgkmcnt(5)\n"
"Qmvaa 236,84,20\nQmvaa 236,176,52\nQda 56,53,29696\nQmvaa 80,88,20\nQtv 12,41,1536\nQmvaa 80,180,52\nQtv 14,41,4096\nQmvaa 112,92,20\n"
"buffer_load_dwordx4 a[4:7], v60, s[48:51], s31 offen offset:3072 sc1\ns_add_u32 s95, s72, 125184\nQmvaa 112,184,52\nbuffer_load_dwordx4 v62, s[52:55], s95 offen offset:2048 lds\nQmvaa 144,96,20\nQmvaa 144,188,52\nds_read_b32 v66, v74\nQmvaa 176,100,20\n"
"s_waitcnt vmcnt(27)\nds_write_b128 v52, a[208:211] offset:0\nQmvaa 176,192,52\ns_waitcnt lgkmcnt(4)\nQmvaa 208,84,24\nQtv 32,43,1536\nQmvaa 208,176,56\ns_waitcnt lgkmcnt(2)\n"
"v_cmp_gt_u32 vcc, v75, v66\nQmvaa 240,88,24\ns_cbranch_vccnz .Lso51r1\n.Lro51r1:\nQmvaa 240,180,56\nQda 60,53,33792\ns_add_u32 s31, s75, 43008\nQmvaa 84,92,24\n"
"buffer_load_dwordx4 a[8:11], v60, s[48:51], s31 offen offset:0 sc1\ns_add_u32 s95, s72, 144448\nQmvaa 84,184,56\nbuffer_load_dwordx4 v61, s[52:55], s95 offen offset:3072 lds\nQmvaa 88,96,24\nQtv 34,43,4096\nQmvaa 88,188,56\ns_waitcnt vmcnt(27)\n"
"ds_write_b128 v52, a[212:215] offset:4096\nQmvaa 120,100,24\nQmvaa 120,192,56\ns_waitcnt lgkmcnt(2)\nQmvaa 152,84,28\nQtv 16,41,2048\nQmvaa 152,176,60\nQda 48,53,37888\n"
"Qmvaa 184,88,28\nQtv 18,41,4608\nQmvaa 184,180,60\nQmvaa 216,92,28\nbuffer_load_dwordx4 a[12:15], v60, s[48:51], s31 offen offset:1024 sc1\ns_add_u32 m0, s96, 49152\ns_add_u32 s95, s72, 147648\nQmvaa 216,184,60\n"
"buffer_load_dwordx4 v61, s[52:55], s95 offen lds\nQmvaa 248,96,28\nQtv 36,43,2048\nQmvaa 248,188,60\ns_waitcnt vmcnt(27)\nds_write_b128 v52, a[216:219] offset:8192\nQmvaa 116,100,28\nQmvaa 116,192,60\n"
"Qtv 38,43,4608\ns_waitcnt lgkmcnt(4)\nQmvaa 96,84,32\nQmvaa 96,176,48\nQda 52,53,41984\nQmvaa 128,88,32\nQta 112,41,20480\nQmvaa 128,180,48\n"
"Qta 114,41,23040\nQmvaa 160,92,32\nbuffer_load_dwordx4 a[16:19], v60, s[48:51], s31 offen offset:2048 sc1\nQmvaa 160,184,48\nQta 132,43,20480\nQmvaa 192,96,32\nQmvaa 192,188,48\ns_waitcnt vmcnt(26)\n"
"ds_write_b128 v52, a[220:223] offset:12288\nQmvaa 224,100,32\nv_add_u32 v75, 1, v75\nQmvaa 224,192,48\nds_write_b32 v73, v75\ns_waitcnt lgkmcnt(5)\nQmvva 92,0,36\nQta 134,43,23040\n"
"Qmvva 92,20,52\nQda 56,53,46080\nQmvva 124,4,36\nQta 116,41,20992\nQmvva 124,24,52\nQmvva 156,8,36\nbuffer_load_dwordx4 a[20:23], v60, s[48:51], s31 offen offset:3072 sc1\nQmvva 156,28,52\n"
"Qta 118,41,23552\nQmvva 188,12,36\nQta 136,43,20992\nQmvva 188,32,52\nds_read_b32 v66, v74\nQmvva 220,16,36\ns_waitcnt vmcnt(25)\nds_write_b128 v52, a[224:227] offset:16384\n"
"Qmvva 220,36,52\ns_waitcnt lgkmcnt(5)\nQmvva 252,0,40\nQmvva 252,20,56\ns_waitcnt lgkmcnt(1)\nv_cmp_gt_u32 vcc, v75, v66\nQmvva 100,4,40\ns_cbranch_vccnz .Lso52r1\n"
".Lro52r1:\nQmvva 100,24,56\nQda 60,53,50176\ns_add_u32 s31, s77, 43008\nQmvva 132,8,40\nbuffer_load_dwordx4 a[24:27], v60, s[48:51], s31 offen offset:0 sc1\ns_add_u32 m0, s96, 65536\ns_add_u32 s95, s73, 126976\n"
"Qmvva 132,28,56\nbuffer_load_dwordx4 v61, s[52:55], s95 offen lds\nQmvva 164,12,40\nQta 138,43,23552\nQmvva 164,32,56\ns_waitcnt vmcnt(26)\nds_write_b128 v52, a[228:231] offset:20480\nQmvva 196,16,40\n"
"Qmvva 196,36,56\nQta 120,41,21504\ns_waitcnt lgkmcnt(3)\nQmvva 228,0,44\nQmvva 228,20,60\nQda 48,53,0\nQmvva 108,4,44\nQta 122,41,24064\n"
"Qmvva 108,24,60\nQta 140,43,21504\nQmvva 140,8,44\nbuffer_load_dwordx4 a[28:31], v60, s[48:51], s31 offen offset:1024 sc1\ns_add_u32 s95, s73, 126080\nQmvva 140,28,60\nbuffer_load_dwordx4 v61, s[52:55], s95 offen offset:1024 lds\nQmvva 172,12,44\n"
"Qta 142,43,24064\nQmvva 172,32,60\ns_waitcnt vmcnt(27)\nds_write_b128 v52, a[232:235] offset:25600\nQmvva 204,16,44\nv_add_u32 v75, 1, v75\nQmvva 204,36,60\ns_waitcnt vmcnt(30)\n"
"ds_write_b32 v73, v75\ns_waitcnt lgkmcnt(5)\nQmvva 236,0,208\nQmvva 236,20,48\nQda 52,53,4096\nQmvva 80,4,208\nQta 124,41,22016\nQmvva 80,24,48\n"
"Qta 126,41,24576\nQmvva 112,8,208\nbuffer_load_dwordx4 a[32:35], v60, s[48:51], s31 offen offset:2048 sc1\ns_add_u32 s95, s73, 125184\nQmvva 112,28,48\nbuffer_load_dwordx4 v62, s[52:55], s95 offen offset:2048 lds\nQmvva 144,12,208\nQmvva 144,32,48\n"
"ds_read_b32 v66, v74\nQmvva 176,16,208\ns_waitcnt vmcnt(27)\nds_write_b128 v52, a[236:239] offset:29696\nQmvva 176,36,48\ns_waitcnt lgkmcnt(4)\nQmvva 208,0,212\nQta 144,43,22016\n"
"Qmvva 208,20,52\ns_waitcnt lgkmcnt(2)\nv_cmp_gt_u32 vcc, v75, v66\nQmvva 240,4,212\ns_cbranch_vccnz .Lso53r1\n.Lro53r1:\nQmvva 240,24,52\nQda 56,53,8192\n"
"Qmvva 84,8,212\nbuffer_load_dwordx4 a[36:39], v60, s[48:51], s31 offen offset:3072 sc1\ns_add_u32 s95, s73, 144448\nQmvva 84,28,52\nbuffer_load_dwordx4 v61, s[52:55], s95 offen offset:3072 lds\nQmvva 88,12,212\nQta 146,43,24576\nQmvva 88,32,52\n"
"s_waitcnt vmcnt(27)\nds_write_b128 v52, a[240:243] offset:33792\nQmvva 120,16,212\nQmvva 120,36,52\ns_waitcnt lgkmcnt(2)\nQmvva 152,0,216\nQta 128,41,22528\nQmvva 152,20,56\n"
"Qda 60,53,12288\nQmvva 184,4,216\nQta 130,41,25088\nQmvva 184,24,56\ns_add_u32 s31, s75, 32768\nQmvva 216,8,216\nbuffer_load_dwordx4 a[40:43], v60, s[48:51], s31 offen offset:0 sc1\ns_add_u32 m0, s96, 69632\n"
"s_add_u32 s95, s73, 147648\nQmvva 216,28,56\nbuffer_load_dwordx4 v61, s[52:55], s95 offen lds\nQmvva 248,12,216\nQta 148,43,22528\nQmvva 248,32,56\ns_waitcnt vmcnt(27)\nds_write_b128 v52, a[244:247] offset:37888\n"
"Qmvva 116,16,216\nQmvva 116,36,56\nQta 150,43,25088\ns_waitcnt lgkmcnt(4)\nQmvva 96,0,220\nQmvva 96,20,60\nQda 48,53,16384\nQmvva 128,4,220\n"
"Qta 84,40,0\nQmvva 128,24,60\nQta 86,40,2560\nQmvva 160,8,220\nbuffer_load_dwordx4 a[44:47], v60, s[48:51], s31 offen offset:1024 sc1\nQmvva 160,28,60\nQta 176,42,0\nQmvva 192,12,220\n"
"Qta 178,42,2560\nQmvva 192,32,60\ns_waitcnt vmcnt(26)\nds_write_b128 v52, a[248:251] offset:41984\nQmvva 224,16,220\nv_add_u32 v75, 1, v75\nQmvva 224,36,60\ns_waitcnt vmcnt(25)\n"
"ds_write_b32 v73, v75\ns_waitcnt lgkmcnt(6)\nQmvaa 92,112,224\nQta 88,40,512\nQmvaa 92,132,48\nQda 52,53,20480\nQmvaa 124,116,224\nQta 90,40,3072\n"
"Qmvaa 124,136,48\nQta 180,42,512\nQmvaa 156,120,224\nbuffer_load_dwordx4 a[208:211], v60, s[48:51], s31 offen offset:2048 sc1\nQmvaa 156,140,48\nQta 182,42,3072\nQmvaa 188,124,224\nQta 92,40,1024\n"
"Qmvaa 188,144,48\nds_read_b32 v66, v74\nQmvaa 220,128,224\ns_waitcnt vmcnt(25)\nds_write_b128 v52, a[252:255] offset:46080\nQmvaa 220,148,48\ns_waitcnt lgkmcnt(6)\nQmvaa 252,112,228\n"
"Qta 94,40,3584\nQmvaa 252,132,52\ns_waitcnt lgkmcnt(2)\nv_cmp_gt_u32 vcc, v75, v66\nQmvaa 100,116,228\ns_cbranch_vccnz .Lso54r1\n.Lro54r1:\nQmvaa 100,136,52\n"
"Qda 56,53,25600\ns_add_u32 s31, s77, 32768\nQmvaa 132,120,228\nbuffer_load_dwordx4 a[212:215], v60, s[48:51], s31 offen offset:0 sc1\nQmvaa 132,140,52\nQmvaa 164,124,228\nQta 184,42,1024\nQmvaa 164,144,52\n"
"s_waitcnt vmcnt(25)\nds_write_b128 v52, a[168:171] offset:50176\nQmvaa 196,128,228\nQmvaa 196,148,52\nQta 186,42,3584\ns_waitcnt lgkmcnt(3)\nQmvaa 228,112,232\nQta 96,40,1536\n"
"Qmvaa 228,132,56\nQda 60,53,29696\nQmvaa 108,116,232\nQta 98,40,4096\nQmvaa 108,136,56\nQta 188,42,1536\nQmvaa 140,120,232\nbuffer_load_dwordx4 a[216:219], v60, s[48:51], s31 offen offset:1024 sc1\n"
"Qmvaa 140,140,56\nQta 190,42,4096\nQmvaa 172,124,232\nQta 100,40,2048\nQmvaa 172,144,56\ns_waitcnt vmcnt(25)\nds_write_b128 v52, a[172:175] offset:0\nQmvaa 204,128,232\n"
"v_add_u32 v75, 1, v75\nQmvaa 204,148,56\ns_waitcnt vmcnt(16)\nds_write_b32 v73, v75\ns_waitcnt lgkmcnt(6)\nQmvaa 236,112,236\nQta 102,40,4608\nQmvaa 236,132,60\n"
"Qda 48,53,33792\nQmvaa 80,116,236\nQta 192,42,2048\nQmvaa 80,136,60\nQta 194,42,4608\nQmvaa 112,120,236\nbuffer_load_dwordx4 a[220:223], v60, s[48:51], s31 offen offset:2048 sc1\nQmvaa 112,140,60\n"
"Qtv 0,40,20480\nQmvaa 144,124,236\nQtv 2,40,23040\nQmvaa 144,144,60\nds_read_b32 v66, v74\nQmvaa 176,128,236\ns_waitcnt vmcnt(24)\nds_write_b128 v52, a[0:3] offset:4096\n"
"Qmvaa 176,148,60\ns_waitcnt lgkmcnt(6)\nQmvaa 92,84,240\nQtv 20,42,20480\nQmvaa 92,176,48\ns_waitcnt lgkmcnt(2)\nv_cmp_gt_u32 vcc, v75, v66\nQmvaa 124,88,240\n"
"s_cbranch_vccnz .Lso55r1\n.Lro55r1:\nQmvaa 124,180,48\nQda 52,53,37888\ns_add_u32 s31, s74, 47104\nQmvaa 156,92,240\nbuffer_load_dwordx4 a[224:227], v60, s[48:51], s31 offen offset:0 sc1\nQmvaa 156,184,48\n"
"Qtv 22,42,23040\nQmvaa 188,96,240\nQtv 4,40,20992\nQmvaa 188,188,48\ns_waitcnt vmcnt(23)\nds_write_b128 v52, a[4:7] offset:8192\nQmvaa 220,100,240\nQmvaa 220,192,48\n"
"Qtv 6,40,23552\ns_waitcnt lgkmcnt(4)\nQmvaa 252,84,244\nQtv 24,42,20992\nQmvaa 252,176,52\nQda 56,53,41984\nQmvaa 100,88,244\nQtv 26,42,23552\n"
"Qmvaa 100,180,52\nQtv 8,40,21504\nQmvaa 132,92,244\nbuffer_load_dwordx4 a[228:231], v60, s[48:51], s31 offen offset:1024 sc1\nQmvaa 132,184,52\nQtv 10,40,24064\nQmvaa 164,96,244\nQtv 28,42,21504\n"
"Qmvaa 164,188,52\ns_waitcnt vmcnt(22)\nds_write_b128 v52, a[8:11] offset:12288\nQmvaa 196,100,244\nQmvaa 196,192,52\nQtv 30,42,24064\ns_waitcnt lgkmcnt(6)\nQmvaa 228,84,248\n"
"Qtv 12,40,22016\nQmvaa 228,176,56\nQda 60,53,46080\nQmvaa 108,88,248\nQtv 14,40,24576\nQmvaa 108,180,56\nQtv 32,42,22016\nQmvaa 140,92,248\n"
"buffer_load_dwordx4 a[232:235], v60, s[48:51], s31 offen offset:2048 sc1\nQmvaa 140,184,56\nQtv 34,42,24576\nQmvaa 172,96,248\nQtv 16,40,22528\nQmvaa 172,188,56\ns_waitcnt vmcnt(21)\nds_write_b128 v52, a[12:15] offset:16384\n"
"Qmvaa 204,100,248\nv_add_u32 v75, 1, v75\nQmvaa 204,192,56\ns_waitcnt vmcnt(8)\nds_write_b32 v73, v75\ns_waitcnt lgkmcnt(6)\nQmvaa 236,84,252\nQtv 18,40,25088\n"
"Qmvaa 236,176,60\nQda 48,53,50176\nQmvaa 80,88,252\nQtv 36,42,22528\nQmvaa 80,180,60\nQtv 38,42,25088\ns_add_u32 s31, s75, 47104\nQmvaa 112,92,252\n"
"buffer_load_dwordx4 a[236:239], v60, s[48:51], s31 offen offset:0 sc1\nQmvaa 112,184,60\nQta 112,40,40960\nQta 114,40,43520\nQmvaa 144,96,252\nQta 132,42,40960\nQmvaa 144,188,60\nds_read_b32 v66, v74\n"
"Qmvaa 176,100,252\ns_waitcnt vmcnt(20)\nds_write_b128 v52, a[16:19] offset:20480\nQmvaa 176,192,60\ns_waitcnt lgkmcnt(7)\nQmvva 92,0,168\nQta 134,42,43520\nQmvva 92,20,48\n"
"s_waitcnt lgkmcnt(2)\nv_cmp_gt_u32 vcc, v75, v66\nQmvva 124,4,168\ns_cbranch_vccnz .Lso56r1\n.Lro56r1:\nQmvva 124,24,48\nQda 52,53,0\nQmvva 156,8,168\n"
"buffer_load_dwordx4 a[240:243], v60, s[48:51], s31 offen offset:1024 sc1\nQmvva 156,28,48\nQta 116,40,41472\nQmvva 188,12,168\nQta 118,40,44032\nQmvva 188,32,48\ns_waitcnt vmcnt(20)\nds_write_b128 v52, a[20:23] offset:25600\n"
"Qmvva 220,16,168\nQmvva 220,36,48\nQta 136,42,41472\ns_waitcnt lgkmcnt(4)\nQmvva 252,0,172\nQta 138,42,44032\nQmvva 252,20,52\nQda 56,53,4096\n"
"Qmvva 100,4,172\nQta 120,40,41984\nQmvva 100,24,52\nQta 122,40,44544\nQmvva 132,8,172\nbuffer_load_dwordx4 a[244:247], v60, s[48:51], s31 offen offset:2048 sc1\nQmvva 132,28,52\nQta 140,42,41984\n"
"Qta 142,42,44544\nQmvva 164,12,172\nQta 124,40,42496\nQmvva 164,32,52\ns_waitcnt vmcnt(20)\nds_write_b128 v52, a[24:27] offset:29696\nQmvva 196,16,172\nv_add_u32 v75, 1, v75\n"
"Qmvva 196,36,52\nds_write_b32 v73, v75\ns_waitcnt lgkmcnt(7)\nQmvva 228,0,0\nQta 126,40,45056\nQmvva 228,20,56\nQda 60,53,8192\nQmvva 108,4,0\n"
"Qta 144,42,42496\nQmvva 108,24,56\nQta 146,42,45056\ns_add_u32 s31, s77, 47104\nQmvva 140,8,0\nbuffer_load_dwordx4 a[248:251], v60, s[48:51], s31 offen offset:0 sc1\nQmvva 140,28,56\nQta 128,40,43008\n"
"Qmvva 172,12,0\nQta 130,40,45568\nQmvva 172,32,56\nds_read_b32 v66, v74\nQmvva 204,16,0\ns_waitcnt vmcnt(19)\nds_write_b128 v52, a[28:31] offset:33792\nQmvva 204,36,56\n"
"s_waitcnt lgkmcnt(6)\nQmvva 236,0,4\nQta 148,42,43008\nQmvva 236,20,60\ns_waitcnt lgkmcnt(2)\nv_cmp_gt_u32 vcc, v75, v66\nQmvva 80,4,4\ns_cbranch_vccnz .Lso57r1\n"
".Lro57r1:\nQmvva 80,24,60\nQda 48,53,12288\nQmvva 112,8,4\nbuffer_load_dwordx4 a[252:255], v60, s[48:51], s31 offen offset:1024 sc1\nQmvva 112,28,60\nQta 150,42,45568\nQmvva 144,12,4\n"
"Qta 84,41,0\nQmvva 144,32,60\ns_waitcnt vmcnt(18)\nds_write_b128 v52, a[32:35] offset:37888\nQmvva 176,16,4\nQmvva 176,36,60\nQta 86,41,2560\ns_waitcnt lgkmcnt(3)\n"
"Qmvaa 92,112,8\nQta 176,43,0\nQmvaa 92,132,48\nQda 52,53,16384\nQmvaa 124,116,8\n.Lpg25:\ns_cbranch_execz .Ltr8\nQta 178,43,2560\n"
"Qmvaa 124,136,48\nQta 88,41,512\nQmvaa 156,120,8\nbuffer_load_dwordx4 a[168:171], v60, s[48:51], s31 offen offset:2048 sc1\nQmvaa 156,140,48\nQta 90,41,3072\nQmvaa 188,124,8\nQta 180,43,512\n"
"Qmvaa 188,144,48\ns_waitcnt vmcnt(17)\nds_write_b128 v52, a[36:39] offset:41984\nQmvaa 220,128,8\nQmvaa 220,148,48\nQta 182,43,3072\ns_waitcnt lgkmcnt(6)\nQmvaa 252,112,12\n"
"Qta 92,41,1024\nQmvaa 252,132,52\nQda 56,53,20480\nQmvaa 100,116,12\nQta 94,41,3584\nQmvaa 100,136,52\nQta 184,43,1024\nQmvaa 132,120,12\n"
"Qta 186,43,3584\nQmvaa 132,140,52\nQta 96,41,1536\nQmvaa 164,124,12\nQta 98,41,4096\nQmvaa 164,144,52\ns_waitcnt vmcnt(15)\nds_write_b128 v52, a[40:43] offset:46080\n"
"Qmvaa 196,128,12\nv_add_u32 v75, 1, v75\nQmvaa 196,148,52\nds_write_b32 v73, v75\ns_waitcnt lgkmcnt(7)\nQmvaa 228,112,16\nQta 188,43,1536\nQmvaa 228,132,56\n"
"Qda 60,53,25600\nQmvaa 108,116,16\nQta 190,43,4096\nQmvaa 108,136,56\nQta 100,41,2048\nQmvaa 140,120,16\nQta 102,41,4608\nQmvaa 140,140,56\n"
"Qta 192,43,2048\nQmvaa 172,124,16\nQta 194,43,4608\nQmvaa 172,144,56\nds_read_b32 v66, v74\nQmvaa 204,128,16\ns_waitcnt vmcnt(13)\nds_write_b128 v52, a[44:47] offset:50176\n"
"Qmvaa 204,148,56\ns_waitcnt lgkmcnt(7)\nQmvaa 236,112,20\nQmvaa 236,132,60\ns_waitcnt lgkmcnt(1)\nv_cmp_gt_u32 vcc, v75, v66\nQmvaa 80,116,20\ns_cbranch_vccnz .Lso58r1\n"
".Lro58r1:\nQmvaa 80,136,60\nQda 48,53,29696\nQmvaa 112,120,20\nQtv 0,41,20480\nQmvaa 112,140,60\nQtv 2,41,23040\nQmvaa 144,124,20\n"
"Qtv 20,43,20480\nQmvaa 144,144,60\ns_waitcnt vmcnt(12)\nds_write_b128 v52, a[208:211] offset:0\nQmvaa 176,128,20\nQmvaa 176,148,60\nQtv 22,43,23040\ns_waitcnt lgkmcnt(5)\n"
"Qmvaa 92,84,24\nQtv 4,41,20992\nQmvaa 92,176,48\nQda 52,53,33792\ns_add_u32 s68, s64, 0\nQmvaa 124,88,24\nQtv 6,41,23552\nQmvaa 124,180,48\n"
"Qtv 24,43,20992\nQmvaa 156,92,24\nQtv 26,43,23552\nQk 92,92,93\nQmvaa 156,184,48\nQtv 8,41,21504\nQk 93,94,95\nQmvaa 188,96,24\n"
"Qtv 10,41,24064\nQk 94,124,125\nQmvaa 188,188,48\ns_waitcnt vmcnt(11)\nds_write_b128 v52, a[212:215] offset:4096\nQmvaa 220,100,24\nv_add_u32 v75, 1, v75\nQk 95,126,127\n"
"Qmvaa 220,192,48\nds_write_b32 v73, v75\nbuffer_store_dwordx4 v[92:95], v60, s[24:27], s68 offen offset:0 sc1\ns_waitcnt lgkmcnt(7)\nQmvaa 252,84,28\nQk 156,156,157\nQk 157,158,159\nQmvaa 252,176,52\n"
"Qda 56,53,37888\nQk 158,188,189\nQmvaa 100,88,28\nQtv 28,43,21504\nQk 159,190,191\nQmvaa 100,180,52\nQtv 30,43,24064\nbuffer_store_dwordx4 v[156:159], v60, s[24:27], s68 offen offset:1024 sc1\n"
"Qmvaa 132,92,28\nQtv 12,41,22016\nQk 220,220,221\nQmvaa 132,184,52\nQtv 14,41,24576\nQk 221,222,223\nQmvaa 164,96,28\nQtv 32,43,22016\n"
"Qk 222,252,253\nQmvaa 164,188,52\nds_read_b32 v66, v74\nQk 223,254,255\nQmvaa 196,100,28\ns_waitcnt vmcnt(12)\nds_write_b128 v52, a[216:219] offset:8192\nQmvaa 196,192,52\n"
"buffer_store_dwordx4 v[220:223], v60, s[24:27], s68 offen offset:2048 sc1\nQk 100,100,101\ns_waitcnt lgkmcnt(7)\nQmvaa 228,84,32\nQtv 34,43,24576\nQk 101,102,103\nQmvaa 228,176,56\ns_waitcnt lgkmcnt(2)\n"
"v_cmp_gt_u32 vcc, v75, v66\nQmvaa 108,88,32\ns_cbranch_vccnz .Lso59r1\n.Lro59r1:\nQk 102,132,133\nQmvaa 108,180,56\nQda 60,53,41984\nQk 103,134,135\n"
"Qmvaa 140,92,32\nQtv 16,41,22528\nbuffer_store_dwordx4 v[100:103], v60, s[24:27], s68 offen offset:3072 sc1\nQmvaa 140,184,56\nQtv 18,41,25088\nQk 164,164,165\nQmvaa 172,96,32\nQtv 36,43,22528\n"
"Qk 165,166,167\nQmvaa 172,188,56\ns_waitcnt vmcnt(13)\nds_write_b128 v52, a[220:223] offset:12288\nQmvaa 204,100,32\nQk 166,196,197\nQk 167,198,199\nQmvaa 204,192,56\n"
"Qtv 38,43,25088\ns_add_u32 s68, s64, 4096\ns_waitcnt lgkmcnt(5)\nQmvaa 236,84,36\nbuffer_store_dwordx4 v[164:167], v60, s[24:27], s68 offen offset:0 sc1\nQk 228,228,229\nQmvaa 236,176,60\nQda 48,53,46080\n"
"Qk 229,230,231\nQmvaa 80,88,36\nQta 112,40,0\nQk 230,108,109\nQmvaa 80,180,60\nQta 114,40,2560\nQk 231,110,111\nQmvaa 112,92,36\n"
"Qta 132,42,0\nbuffer_store_dwordx4 v[228:231], v60, s[24:27], s68 offen offset:1024 sc1\nQmvaa 112,184,60\nQta 134,42,2560\nQk 140,140,141\nQmvaa 144,96,36\nQta 116,40,512\nQk 141,142,143\n"
"Qmvaa 144,188,60\ns_waitcnt vmcnt(14)\nds_write_b128 v52, a[224:227] offset:16384\nQmvaa 176,100,36\nQk 142,172,173\nQk 143,174,175\nQmvaa 176,192,60\nQta 118,40,3072\n"
"buffer_store_dwordx4 v[140:143], v60, s[24:27], s68 offen offset:2048 sc1\ns_waitcnt lgkmcnt(7)\nQmvva 208,0,40\nQta 136,42,512\nQk 204,204,205\nQmvva 208,20,48\nQda 52,53,50176\nQk 205,206,207\n"
"Qmvva 240,4,40\nQta 138,42,3072\nQk 206,236,237\nQmvva 240,24,48\nQta 120,40,1024\nQk 207,238,239\nQmvva 84,8,40\nQta 122,40,3584\n"
"s_add_u32 s68, s64, 14336\nQmvva 84,28,48\nQta 140,42,1024\nbuffer_store_dwordx4 v[204:207], v60, s[24:27], s68 offen offset:0 sc1\nQmvva 88,12,40\nQta 142,42,3584\nQk 80,80,81\nQmvva 88,32,48\n"
"s_waitcnt vmcnt(15)\nds_write_b128 v52, a[228:231] offset:20480\nQmvva 120,16,40\nv_add_u32 v75, 1, v75\nQk 81,82,83\nQmvva 120,36,48\nds_write_b32 v73, v75\nQk 82,112,113\n"
"s_waitcnt lgkmcnt(7)\nQmvva 152,0,44\nQta 124,40,1536\nQk 83,114,115\nQmvva 152,20,52\nQda 56,53,0\nbuffer_store_dwordx4 v[80:83], v60, s[24:27], s68 offen offset:1024 sc1\nQmvva 184,4,44\n"
"Qta 126,40,4096\nQk 144,144,145\nQmvva 184,24,52\nQta 144,42,1536\nQk 145,146,147\nQmvva 216,8,44\nQta 146,42,4096\nQk 146,176,177\n"
"Qmvva 216,28,52\nQta 128,40,2048\nQk 147,178,179\nQmvva 248,12,44\nQta 130,40,4608\nbuffer_store_dwordx4 v[144:147], v60, s[24:27], s68 offen offset:2048 sc1\nQmvva 248,32,52\nds_read_b32 v66, v74\n"
"Qmvva 116,16,44\ns_waitcnt vmcnt(16)\nds_write_b128 v52, a[232:235] offset:25600\nQmvva 116,36,52\ns_waitcnt lgkmcnt(7)\nQmvva 96,0,208\nQta 148,42,2048\nQmvva 96,20,56\n"
"s_waitcnt lgkmcnt(2)\nv_cmp_gt_u32 vcc, v75, v66\nQmvva 128,4,208\ns_cbranch_vccnz .Lso60r1\n.Lro60r1:\nQmvva 128,24,56\nQda 60,53,4096\nQmvva 160,8,208\n"
"Qta 150,42,4608\nQmvva 160,28,56\nQta 84,40,20480\nQta 86,40,23040\nQmvva 192,12,208\nQta 176,42,20480\nQmvva 192,32,56\ns_waitcnt vmcnt(15)\n"
"ds_write_b128 v52, a[236:239] offset:29696\nQmvva 224,16,208\nQmvva 224,36,56\nQta 178,42,23040\ns_waitcnt lgkmcnt(5)\nQmvaa 208,112,212\nQta 88,40,20992\nQmvaa 208,132,60\n"
"Qda 48,53,8192\nQmvaa 240,116,212\nQta 90,40,23552\nQmvaa 240,136,60\nQta 180,42,20992\nQmvaa 84,120,212\nQta 182,42,23552\nQmvaa 84,140,60\n"
"Qta 92,40,21504\nQmvaa 88,124,212\nQta 94,40,24064\nQmvaa 88,144,60\ns_waitcnt vmcnt(14)\nds_write_b128 v52, a[240:243] offset:33792\nQmvaa 120,128,212\nv_add_u32 v75, 1, v75\n"
"Qmvaa 120,148,60\nds_write_b32 v73, v75\ns_waitcnt lgkmcnt(7)\nQmvaa 152,112,216\nQta 184,42,21504\nQta 186,42,24064\nQmvaa 152,132,48\nQda 52,53,12288\n"
"Qmvaa 184,116,216\nQta 96,40,22016\nQmvaa 184,136,48\nQta 98,40,24576\nQmvaa 216,120,216\nQta 188,42,22016\nQmvaa 216,140,48\nQta 190,42,24576\n"
"Qmvaa 248,124,216\nQta 100,40,22528\nQmvaa 248,144,48\nds_read_b32 v66, v74\nQmvaa 116,128,216\ns_waitcnt vmcnt(13)\nds_write_b128 v52, a[244:247] offset:37888\nQmvaa 116,148,48\n"
"s_waitcnt lgkmcnt(7)\nQmvaa 96,112,220\nQta 102,40,25088\nQmvaa 96,132,52\ns_waitcnt lgkmcnt(2)\nv_cmp_gt_u32 vcc, v75, v66\nQmvaa 128,116,220\ns_cbranch_vccnz .Lso61r1\n"
".Lro61r1:\nQmvaa 128,136,52\nQda 56,53,16384\nQmvaa 160,120,220\nQta 192,42,22528\nQmvaa 160,140,52\nQta 194,42,25088\nQmvaa 192,124,220\n"
"Qtv 0,40,40960\nQtv 2,40,43520\nQmvaa 192,144,52\ns_waitcnt vmcnt(12)\nds_write_b128 v52, a[248:251] offset:41984\nQmvaa 224,128,220\ns_waitcnt vmcnt(0)\nbuffer_store_dword v67, v77, s[24:27], 0 offen sc1\n"
"Qmvaa 224,148,52\nQtv 20,42,40960\ns_waitcnt lgkmcnt(4)\nQmvaa 208,84,224\nQtv 22,42,43520\nQmvaa 208,176,56\nQda 60,53,20480\nQmvaa 240,88,224\n"
"Qtv 4,40,41472\nQmvaa 240,180,56\nQtv 6,40,44032\nQmvaa 84,92,224\nQtv 24,42,41472\nQmvaa 84,184,56\nQtv 26,42,44032\nQmvaa 88,96,224\n"
"Qtv 8,40,41984\nQmvaa 88,188,56\ns_waitcnt vmcnt(12)\nds_write_b128 v52, a[252:255] offset:46080\nQmvaa 120,100,224\nQmvaa 120,192,56\nQtv 10,40,44544\ns_waitcnt lgkmcnt(7)\n"
"Qmvaa 152,84,228\nQtv 28,42,41984\nQmvaa 152,176,60\nQda 48,53,25600\nQmvaa 184,88,228\nQtv 30,42,44544\nQmvaa 184,180,60\nQtv 12,40,42496\n"
"Qmvaa 216,92,228\nQtv 14,40,45056\nQmvaa 216,184,60\nQtv 32,42,42496\nQmvaa 248,96,228\nQtv 34,42,45056\nQmvaa 248,188,60\ns_waitcnt vmcnt(11)\n"
"ds_write_b128 v52, a[168:171] offset:50176\nQmvaa 116,100,228\nv_add_u32 v75, 1, v75\nQmvaa 116,192,60\nds_write_b32 v73, v75\ns_waitcnt lgkmcnt(7)\nQmvaa 96,84,232\nbuffer_load_dword v78, v76, s[24:27], 0 offen sc1\n"
"Qmvaa 96,176,48\nQda 52,53,29696\nQmvaa 128,88,232\nQtv 16,40,43008\nQmvaa 128,180,48\nQtv 18,40,45568\nQmvaa 160,92,232\nQtv 36,42,43008\n"
"Qmvaa 160,184,48\nQtv 38,42,45568\nQmvaa 192,96,232\nQta 112,41,0\nQmvaa 192,188,48\nds_read_b32 v66, v74\nQmvaa 224,100,232\nQta 114,41,2560\n"
"Qmvaa 224,192,48\nQta 132,43,0\ns_waitcnt lgkmcnt(8)\nQmvva 208,0,236\nQta 134,43,2560\nQmvva 208,20,52\ns_waitcnt lgkmcnt(3)\nv_cmp_gt_u32 vcc, v75, v66\n"
"Qmvva 240,4,236\ns_cbranch_vccnz .Lso62r1\n.Lro62r1:\nQmvva 240,24,52\nQda 56,53,33792\nQmvva 84,8,236\nQta 116,41,512\nQmvva 84,28,52\n"
"Qta 118,41,3072\nQmvva 88,12,236\nQta 136,43,512\nQmvva 88,32,52\nQmvva 120,16,236\nQta 138,43,3072\nQmvva 120,36,52\nQta 120,41,1024\n"
"s_waitcnt lgkmcnt(5)\nQmvva 152,0,240\nQta 122,41,3584\nQmvva 152,20,56\nQda 60,53,37888\nQmvva 184,4,240\nQta 140,43,1024\nQmvva 184,24,56\n"
"Qta 142,43,3584\nQmvva 216,8,240\nQta 124,41,1536\nQmvva 216,28,56\nQta 126,41,4096\nQmvva 248,12,240\nQmvva 248,32,56\nQta 144,43,1536\n"
"Qmvva 116,16,240\nQta 146,43,4096\nv_add_u32 v75, 1, v75\nQmvva 116,36,56\nds_write_b32 v73, v75\ns_waitcnt lgkmcnt(7)\nQmvva 96,0,244\nQta 128,41,2048\n"
"Qmvva 96,20,60\nQda 48,53,41984\nQmvva 128,4,244\nQta 130,41,4608\nQmvva 128,24,60\nQta 148,43,2048\nQmvva 160,8,244\nQta 150,43,4608\n"
"Qmvva 160,28,60\nQmvva 192,12,244\nQmvva 192,32,60\nds_read_b32 v66, v74\nQmvva 224,16,244\nQmvva 224,36,60\ns_waitcnt lgkmcnt(4)\nQmvaa 208,112,248\n"
"Qmvaa 208,132,48\ns_waitcnt lgkmcnt(0)\nv_cmp_gt_u32 vcc, v75, v66\nQmvaa 240,116,248\ns_cbranch_vccnz .Lso63r1\n.Lro63r1:\nQmvaa 240,136,48\nQda 52,53,46080\n"
"Qmvaa 84,120,248\nQmvaa 84,140,48\ns_waitcnt vmcnt(0)\nv_sub_u32 v66, v78, v65\nv_cmp_gt_u32 vcc, 1, v66\ns_and_b64 vcc, vcc, s[28:29]\ns_cbranch_scc1 .Lx1_w\n.Lx1_r:\n"
"Qmvaa 88,124,248\ns_add_u32 s69, s98, 0\nbuffer_load_dwordx4 v[124:127], v60, s[24:27], s69 offen offset:0 sc1\nQmvaa 88,144,48\nQmvaa 120,128,248\nbuffer_load_dwordx4 v[188:191], v60, s[24:27], s69 offen offset:1024 sc1\nQmvaa 120,148,48\ns_waitcnt lgkmcnt(0)\n"
"Qmvaa 152,112,252\nbuffer_load_dwordx4 v[252:255], v60, s[24:27], s69 offen offset:2048 sc1\nQmvaa 152,132,52\nQda 56,53,50176\nQmvaa 184,116,252\nbuffer_load_dwordx4 v[132:135], v60, s[24:27], s69 offen offset:3072 sc1\nQmvaa 184,136,52\nQmvaa 216,120,252\n"
"s_add_u32 s69, s98, 4096\nbuffer_load_dwordx4 v[196:199], v60, s[24:27], s69 offen offset:0 sc1\nQmvaa 216,140,52\nQmvaa 248,124,252\nbuffer_load_dwordx4 v[108:111], v60, s[24:27], s69 offen offset:1024 sc1\nQmvaa 248,144,52\nQmvaa 116,128,252\nbuffer_load_dwordx4 v[172:175], v60, s[24:27], s69 offen offset:2048 sc1\n"
"Qmvaa 116,148,52\ns_waitcnt lgkmcnt(0)\nQmvaa 96,112,168\ns_add_u32 s69, s98, 14336\nbuffer_load_dwordx4 v[236:239], v60, s[24:27], s69 offen offset:0 sc1\nQmvaa 96,132,56\nQmvaa 128,116,168\nQmvaa 128,136,56\n"
"Qmvaa 160,120,168\nQmvaa 160,140,56\nQmvaa 192,124,168\nQmvaa 192,144,56\nQmvaa 224,128,168\nQmvaa 224,148,56\ns_waitcnt vmcnt(7)\nQmvvv 208,46,124\n"
"Qmvvv 240,44,124\ns_waitcnt vmcnt(6)\nQmvvv 84,46,188\nQmvvv 88,44,188\ns_nop 3\nQk 208,208,209\nQk 209,210,211\nds_write_b64 v63, v[208:209] offset:21504\n"
"s_waitcnt vmcnt(5)\nQmvvv 120,46,252\nQk 240,240,241\nQk 241,242,243\nds_write_b64 v63, v[240:241] offset:21568\nQmvvv 152,44,252\nQk 84,84,85\nQk 85,86,87\n"
"ds_write_b64 v63, v[84:85] offset:21632\ns_waitcnt vmcnt(4)\nQmvvv 184,46,132\n.Lpg26:\ns_cbranch_execz .Ltr8\nQk 88,88,89\nQk 89,90,91\nds_write_b64 v63, v[88:89] offset:21696\n"
"Qmvvv 216,44,132\nQk 120,120,121\nQk 121,122,123\nds_write_b64 v63, v[120:121] offset:21760\ns_waitcnt vmcnt(3)\nQmvvv 248,46,196\nQk 152,152,153\nQk 153,154,155\n"
"ds_write_b64 v63, v[152:153] offset:26880\nQmvvv 116,44,196\nQk 184,184,185\nQk 185,186,187\nds_write_b64 v63, v[184:185] offset:26944\ns_waitcnt vmcnt(2)\nQmvvv 96,46,108\nQk 216,216,217\n"
"Qk 217,218,219\nds_write_b64 v63, v[216:217] offset:27008\nQmvvv 128,44,108\nQk 248,248,249\nQk 249,250,251\nds_write_b64 v63, v[248:249] offset:27072\ns_waitcnt vmcnt(1)\nQmvvv 160,46,172\n"
"Qk 116,116,117\nQk 117,118,119\nds_write_b64 v63, v[116:117] offset:27136\nQmvvv 192,44,172\nQk 96,96,97\nQk 97,98,99\nds_write_b64 v63, v[96:97] offset:32256\ns_waitcnt vmcnt(0)\n"
"Qmvvv 224,46,236\nQk 128,128,129\nQk 129,130,131\nds_write_b64 v63, v[128:129] offset:32320\nQk 160,160,161\nQk 161,162,163\nds_write_b64 v63, v[160:161] offset:32384\nQk 192,192,193\n"
"Qk 193,194,195\nds_write_b64 v63, v[192:193] offset:32448\nQk 224,224,225\nQk 225,226,227\nds_write_b64 v63, v[224:225] offset:32512\n.Lfin:\n.Lpg27:\ns_cbranch_execz .Ltr8\n"
"s_waitcnt lgkmcnt(0)\ns_barrier\nQdv 0,64,0\nQdv 4,64,5376\nQdv 8,64,10752\nQdv 12,64,16128\nQdv 16,64,21504\nQdv 20,64,26880\n"
"Qdv 24,64,32256\nQdv 28,71,0\nQdv 32,71,21504\ns_add_u32 s68, s65, 0\ns_waitcnt lgkmcnt(8)\nbuffer_store_dwordx4 v[0:3], v70, s[40:43], s68 offen sc1\ns_add_u32 s68, s65, 40960\ns_waitcnt lgkmcnt(7)\n"
"buffer_store_dwordx4 v[4:7], v70, s[40:43], s68 offen sc1\ns_add_u32 s68, s65, 81920\ns_waitcnt lgkmcnt(6)\nbuffer_store_dwordx4 v[8:11], v70, s[40:43], s68 offen sc1\ns_add_u32 s68, s65, 122880\ns_waitcnt lgkmcnt(5)\nbuffer_store_dwordx4 v[12:15], v70, s[40:43], s68 offen sc1\ns_add_u32 s68, s60, 163840\n"
"s_waitcnt lgkmcnt(4)\nbuffer_store_dwordx4 v[16:19], v70, s[40:43], s68 offen sc1\ns_add_u32 s68, s60, 204800\ns_waitcnt lgkmcnt(3)\nbuffer_store_dwordx4 v[20:23], v70, s[40:43], s68 offen sc1\ns_add_u32 s68, s60, 245760\ns_waitcnt lgkmcnt(2)\nbuffer_store_dwordx4 v[24:27], v70, s[40:43], s68 offen sc1\n"
"s_add_u32 s68, s60, 163840\ns_cmp_eq_u32 s70, 3\ns_cselect_b32 s68, 0x7ffff000, s68\ns_waitcnt lgkmcnt(1)\nbuffer_store_dwordx4 v[28:31], v72, s[40:43], s65 offen sc1\ns_waitcnt lgkmcnt(0)\nbuffer_store_dwordx4 v[32:35], v72, s[40:43], s68 offen sc1\ns_waitcnt vmcnt(0) lgkmcnt(0)\n"
"s_branch .Lexit\n.Lend:\n.Lfb:\ns_waitcnt vmcnt(0) lgkmcnt(0)\nv_lshrrev_b32 v52, 4, v60\nv_and_b32 v53, 15, v52\nv_lshrrev_b32 v52, 4, v52\nv_lshlrev_b32 v66, 8, v53\n"
"v_lshl_add_u32 v66, v52, 4, v66\nv_add_u32 v66, 1024, v66\nv_lshlrev_b32 v67, 10, v52\nv_lshl_add_u32 v67, v53, 1, v67\nv_add_u32 v67, 1024, v67\nv_mov_b32 v52, 0x3f803f80\nv_accvgpr_write_b32 a204, v52\nv_accvgpr_write_b32 a205, v52\n"
"v_accvgpr_write_b32 a206, v52\nv_accvgpr_write_b32 a207, v52\nv_mov_b32 v24, 0xff800000\nv_mov_b32 v28, 0xff800000\nv_mov_b32 v32, 0xff800000\nv_mov_b32 v36, 0xff800000\nv_mov_b32 v40, 0xff800000\nv_mov_b32 v44, 0xff800000\n"
"v_mov_b32 v48, 0xff800000\ns_lshl_b32 s89, s67, 21\ns_mov_b32 s66, 0\n.Lfb1:\ns_add_u32 s68, s89, 4096\nbuffer_load_dwordx4 a[112:115], v66, s[40:43], s89 offen offset:0\nbuffer_load_dwordx4 a[116:119], v66, s[40:43], s89 offen offset:64\nbuffer_load_dwordx4 a[120:123], v66, s[40:43], s89 offen offset:128\n"
"buffer_load_dwordx4 a[124:127], v66, s[40:43], s89 offen offset:192\nbuffer_load_dwordx4 a[128:131], v66, s[40:43], s68 offen offset:0\nbuffer_load_dwordx4 a[132:135], v66, s[40:43], s68 offen offset:64\nbuffer_load_dwordx4 a[136:139], v66, s[40:43], s68 offen offset:128\nbuffer_load_dwordx4 a[140:143], v66, s[40:43], s68 offen offset:192\ns_waitcnt vmcnt(0)\nQzvaa 0,112,0\nQmvaa 0,116,4\n"
"Qmvaa 0,120,8\nQmvaa 0,124,12\nQzvaa 4,128,0\nQmvaa 4,132,4\nQmvaa 4,136,8\nQmvaa 4,140,12\ns_nop 7\nv_max3_f32 v24, v24, v0, v1\n"
"v_max3_f32 v24, v24, v2, v3\nv_max3_f32 v24, v24, v4, v5\nv_max3_f32 v24, v24, v6, v7\nQzvaa 0,112,16\nQmvaa 0,116,20\nQmvaa 0,120,24\nQmvaa 0,124,28\nQzvaa 4,128,16\n"
"Qmvaa 4,132,20\nQmvaa 4,136,24\nQmvaa 4,140,28\ns_nop 7\nv_max3_f32 v28, v28, v0, v1\nv_max3_f32 v28, v28, v2, v3\nv_max3_f32 v28, v28, v4, v5\nv_max3_f32 v28, v28, v6, v7\n"
"Qzvaa 0,112,32\nQmvaa 0,116,36\nQmvaa 0,120,40\nQmvaa 0,124,44\nQzvaa 4,128,32\nQmvaa 4,132,36\nQmvaa 4,136,40\nQmvaa 4,140,44\n"
"s_nop 7\nv_max3_f32 v32, v32, v0, v1\nv_max3_f32 v32, v32, v2, v3\nv_max3_f32 v32, v32, v4, v5\nv_max3_f32 v32, v32, v6, v7\nQzvaa 0,112,48\nQmvaa 0,116,52\nQmvaa 0,120,56\n"
"Qmvaa 0,124,60\nQzvaa 4,128,48\nQmvaa 4,132,52\nQmvaa 4,136,56\nQmvaa 4,140,60\ns_nop 7\nv_max3_f32 v36, v36, v0, v1\nv_max3_f32 v36, v36, v2, v3\n"
"v_max3_f32 v36, v36, v4, v5\nv_max3_f32 v36, v36, v6, v7\nQzvaa 0,112,64\nQmvaa 0,116,68\nQmvaa 0,120,72\nQmvaa 0,124,76\nQzvaa 4,128,64\nQmvaa 4,132,68\n"
"Qmvaa 4,136,72\nQmvaa 4,140,76\ns_nop 7\nv_max3_f32 v40, v40, v0, v1\nv_max3_f32 v40, v40, v2, v3\nv_max3_f32 v40, v40, v4, v5\nv_max3_f32 v40, v40, v6, v7\nQzvaa 0,112,80\n"
"Qmvaa 0,116,84\nQmvaa 0,120,88\nQmvaa 0,124,92\nQzvaa 4,128,80\nQmvaa 4,132,84\nQmvaa 4,136,88\nQmvaa 4,140,92\ns_nop 7\n"
"v_max3_f32 v44, v44, v0, v1\nv_max3_f32 v44, v44, v2, v3\nv_max3_f32 v44, v44, v4, v5\nv_max3_f32 v44, v44, v6, v7\nQzvaa 0,112,96\nQmvaa 0,116,100\nQmvaa 0,120,104\nQmvaa 0,124,108\n"
"Qzvaa 4,128,96\nQmvaa 4,132,100\nQmvaa 4,136,104\nQmvaa 4,140,108\ns_nop 7\nv_max3_f32 v48, v48, v0, v1\nv_max3_f32 v48, v48, v2, v3\nv_max3_f32 v48, v48, v4, v5\n"
"v_max3_f32 v48, v48, v6, v7\ns_add_u32 s89, s89, 8192\ns_add_u32 s66, s66, 1\ns_cmp_lt_u32 s66, 32\ns_cbranch_scc1 .Lfb1\nv_mov_b32 v25, v24\ns_nop 1\nv_permlane32_swap_b32 v24, v25\n"
"v_max_f32 v24, v24, v25\nv_mov_b32 v25, v24\ns_nop 1\nv_permlane16_swap_b32 v24, v25\nv_max_f32 v24, v24, v25\nv_sub_f32 v24, 0, v24\nv_mov_b32 v25, v24\nv_mov_b32 v26, v24\n"
"v_mov_b32 v27, v24\nv_mov_b32 v29, v28\ns_nop 1\nv_permlane32_swap_b32 v28, v29\nv_max_f32 v28, v28, v29\nv_mov_b32 v29, v28\ns_nop 1\nv_permlane16_swap_b32 v28, v29\n"
"v_max_f32 v28, v28, v29\nv_sub_f32 v28, 0, v28\nv_mov_b32 v29, v28\nv_mov_b32 v30, v28\nv_mov_b32 v31, v28\nv_mov_b32 v33, v32\ns_nop 1\nv_permlane32_swap_b32 v32, v33\n"
"v_max_f32 v32, v32, v33\nv_mov_b32 v33, v32\ns_nop 1\nv_permlane16_swap_b32 v32, v33\nv_max_f32 v32, v32, v33\nv_sub_f32 v32, 0, v32\nv_mov_b32 v33, v32\nv_mov_b32 v34, v32\n"
"v_mov_b32 v35, v32\nv_mov_b32 v37, v36\ns_nop 1\nv_permlane32_swap_b32 v36, v37\nv_max_f32 v36, v36, v37\nv_mov_b32 v37, v36\ns_nop 1\nv_permlane16_swap_b32 v36, v37\n"
"v_max_f32 v36, v36, v37\nv_sub_f32 v36, 0, v36\nv_mov_b32 v37, v36\nv_mov_b32 v38, v36\nv_mov_b32 v39, v36\nv_mov_b32 v41, v40\ns_nop 1\nv_permlane32_swap_b32 v40, v41\n"
"v_max_f32 v40, v40, v41\nv_mov_b32 v41, v40\ns_nop 1\nv_permlane16_swap_b32 v40, v41\nv_max_f32 v40, v40, v41\nv_sub_f32 v40, 0, v40\nv_mov_b32 v41, v40\nv_mov_b32 v42, v40\n"
"v_mov_b32 v43, v40\nv_mov_b32 v45, v44\ns_nop 1\nv_permlane32_swap_b32 v44, v45\nv_max_f32 v44, v44, v45\nv_mov_b32 v45, v44\ns_nop 1\nv_permlane16_swap_b32 v44, v45\n"
"v_max_f32 v44, v44, v45\nv_sub_f32 v44, 0, v44\nv_mov_b32 v45, v44\nv_mov_b32 v46, v44\nv_mov_b32 v47, v44\nv_mov_b32 v49, v48\ns_nop 1\nv_permlane32_swap_b32 v48, v49\n"
"v_max_f32 v48, v48, v49\nv_mov_b32 v49, v48\ns_nop 1\nv_permlane16_swap_b32 v48, v49\nv_max_f32 v48, v48, v49\nv_sub_f32 v48, 0, v48\nv_mov_b32 v49, v48\nv_mov_b32 v50, v48\n"
"v_mov_b32 v51, v48\nv_mov_b32 v52, 0\nv_mov_b32 v80, 0\nv_mov_b32 v81, 0\nv_mov_b32 v82, 0\nv_mov_b32 v83, 0\nv_mov_b32 v84, 0\nv_mov_b32 v85, 0\n"
"v_mov_b32 v86, 0\nv_mov_b32 v87, 0\nv_mov_b32 v88, 0\nv_mov_b32 v89, 0\nv_mov_b32 v90, 0\nv_mov_b32 v91, 0\nv_mov_b32 v92, 0\nv_mov_b32 v93, 0\n"
"v_mov_b32 v94, 0\nv_mov_b32 v95, 0\nv_mov_b32 v96, 0\nv_mov_b32 v97, 0\nv_mov_b32 v98, 0\nv_mov_b32 v99, 0\nv_mov_b32 v100, 0\nv_mov_b32 v101, 0\n"
"v_mov_b32 v102, 0\nv_mov_b32 v103, 0\nv_mov_b32 v104, 0\nv_mov_b32 v105, 0\nv_mov_b32 v106, 0\nv_mov_b32 v107, 0\nv_mov_b32 v108, 0\nv_mov_b32 v109, 0\n"
"v_mov_b32 v110, 0\nv_mov_b32 v111, 0\nv_accvgpr_write_b32 a176, v52\nv_accvgpr_write_b32 a177, v52\nv_accvgpr_write_b32 a178, v52\nv_accvgpr_write_b32 a179, v52\nv_mov_b32 v112, 0\nv_mov_b32 v113, 0\n"
"v_mov_b32 v114, 0\nv_mov_b32 v115, 0\nv_mov_b32 v116, 0\nv_mov_b32 v117, 0\nv_mov_b32 v118, 0\nv_mov_b32 v119, 0\nv_mov_b32 v120, 0\nv_mov_b32 v121, 0\n"
"v_mov_b32 v122, 0\nv_mov_b32 v123, 0\nv_mov_b32 v124, 0\nv_mov_b32 v125, 0\nv_mov_b32 v126, 0\nv_mov_b32 v127, 0\nv_mov_b32 v128, 0\nv_mov_b32 v129, 0\n"
"v_mov_b32 v130, 0\nv_mov_b32 v131, 0\nv_mov_b32 v132, 0\nv_mov_b32 v133, 0\nv_mov_b32 v134, 0\nv_mov_b32 v135, 0\nv_mov_b32 v136, 0\nv_mov_b32 v137, 0\n"
"v_mov_b32 v138, 0\nv_mov_b32 v139, 0\nv_mov_b32 v140, 0\nv_mov_b32 v141, 0\nv_mov_b32 v142, 0\nv_mov_b32 v143, 0\nv_accvgpr_write_b32 a180, v52\nv_accvgpr_write_b32 a181, v52\n"
"v_accvgpr_write_b32 a182, v52\nv_accvgpr_write_b32 a183, v52\nv_mov_b32 v144, 0\nv_mov_b32 v145, 0\nv_mov_b32 v146, 0\nv_mov_b32 v147, 0\nv_mov_b32 v148, 0\nv_mov_b32 v149, 0\n"
"v_mov_b32 v150, 0\nv_mov_b32 v151, 0\nv_mov_b32 v152, 0\nv_mov_b32 v153, 0\nv_mov_b32 v154, 0\nv_mov_b32 v155, 0\nv_mov_b32 v156, 0\nv_mov_b32 v157, 0\n"
"v_mov_b32 v158, 0\nv_mov_b32 v159, 0\nv_mov_b32 v160, 0\nv_mov_b32 v161, 0\nv_mov_b32 v162, 0\nv_mov_b32 v163, 0\nv_mov_b32 v164, 0\nv_mov_b32 v165, 0\n"
"v_mov_b32 v166, 0\nv_mov_b32 v167, 0\nv_mov_b32 v168, 0\nv_mov_b32 v169, 0\nv_mov_b32 v170, 0\nv_mov_b32 v171, 0\nv_mov_b32 v172, 0\nv_mov_b32 v173, 0\n"
"v_mov_b32 v174, 0\nv_mov_b32 v175, 0\nv_accvgpr_write_b32 a184, v52\nv_accvgpr_write_b32 a185, v52\nv_accvgpr_write_b32 a186, v52\nv_accvgpr_write_b32 a187, v52\nv_mov_b32 v176, 0\nv_mov_b32 v177, 0\n"
"v_mov_b32 v178, 0\nv_mov_b32 v179, 0\nv_mov_b32 v180, 0\nv_mov_b32 v181, 0\nv_mov_b32 v182, 0\nv_mov_b32 v183, 0\nv_mov_b32 v184, 0\nv_mov_b32 v185, 0\n"
"v_mov_b32 v186, 0\nv_mov_b32 v187, 0\nv_mov_b32 v188, 0\nv_mov_b32 v189, 0\nv_mov_b32 v190, 0\nv_mov_b32 v191, 0\nv_mov_b32 v192, 0\nv_mov_b32 v193, 0\n"
"v_mov_b32 v194, 0\nv_mov_b32 v195, 0\nv_mov_b32 v196, 0\nv_mov_b32 v197, 0\nv_mov_b32 v198, 0\nv_mov_b32 v199, 0\nv_mov_b32 v200, 0\nv_mov_b32 v201, 0\n"
"v_mov_b32 v202, 0\nv_mov_b32 v203, 0\nv_mov_b32 v204, 0\nv_mov_b32 v205, 0\nv_mov_b32 v206, 0\nv_mov_b32 v207, 0\nv_accvgpr_write_b32 a188, v52\nv_accvgpr_write_b32 a189, v52\n"
"v_accvgpr_write_b32 a190, v52\nv_accvgpr_write_b32 a191, v52\nv_mov_b32 v208, 0\nv_mov_b32 v209, 0\nv_mov_b32 v210, 0\nv_mov_b32 v211, 0\nv_mov_b32 v212, 0\nv_mov_b32 v213, 0\n"
"v_mov_b32 v214, 0\nv_mov_b32 v215, 0\nv_mov_b32 v216, 0\nv_mov_b32 v217, 0\nv_mov_b32 v218, 0\nv_mov_b32 v219, 0\nv_mov_b32 v220, 0\nv_mov_b32 v221, 0\n"
"v_mov_b32 v222, 0\nv_mov_b32 v223, 0\nv_mov_b32 v224, 0\nv_mov_b32 v225, 0\nv_mov_b32 v226, 0\nv_mov_b32 v227, 0\nv_mov_b32 v228, 0\nv_mov_b32 v229, 0\n"
"v_mov_b32 v230, 0\nv_mov_b32 v231, 0\nv_mov_b32 v232, 0\nv_mov_b32 v233, 0\nv_mov_b32 v234, 0\nv_mov_b32 v235, 0\nv_mov_b32 v236, 0\nv_mov_b32 v237, 0\n"
"v_mov_b32 v238, 0\nv_mov_b32 v239, 0\nv_accvgpr_write_b32 a192, v52\nv_accvgpr_write_b32 a193, v52\nv_accvgpr_write_b32 a194, v52\nv_accvgpr_write_b32 a195, v52\nv_mov_b32 v240, 0\nv_mov_b32 v241, 0\n"
"v_mov_b32 v242, 0\nv_mov_b32 v243, 0\nv_mov_b32 v244, 0\nv_mov_b32 v245, 0\nv_mov_b32 v246, 0\nv_mov_b32 v247, 0\nv_mov_b32 v248, 0\nv_mov_b32 v249, 0\n"
"v_mov_b32 v250, 0\nv_mov_b32 v251, 0\nv_mov_b32 v252, 0\nv_mov_b32 v253, 0\nv_mov_b32 v254, 0\nv_mov_b32 v255, 0\nv_accvgpr_write_b32 a208, v52\nv_accvgpr_write_b32 a209, v52\n"
"v_accvgpr_write_b32 a210, v52\nv_accvgpr_write_b32 a211, v52\nv_accvgpr_write_b32 a212, v52\nv_accvgpr_write_b32 a213, v52\nv_accvgpr_write_b32 a214, v52\nv_accvgpr_write_b32 a215, v52\nv_accvgpr_write_b32 a216, v52\nv_accvgpr_write_b32 a217, v52\n"
"v_accvgpr_write_b32 a218, v52\nv_accvgpr_write_b32 a219, v52\nv_accvgpr_write_b32 a220, v52\nv_accvgpr_write_b32 a221, v52\nv_accvgpr_write_b32 a222, v52\nv_accvgpr_write_b32 a223, v52\nv_accvgpr_write_b32 a196, v52\nv_accvgpr_write_b32 a197, v52\n"
"v_accvgpr_write_b32 a198, v52\nv_accvgpr_write_b32 a199, v52\nv_accvgpr_write_b32 a224, v52\nv_accvgpr_write_b32 a225, v52\nv_accvgpr_write_b32 a226, v52\nv_accvgpr_write_b32 a227, v52\nv_accvgpr_write_b32 a228, v52\nv_accvgpr_write_b32 a229, v52\n"
"v_accvgpr_write_b32 a230, v52\nv_accvgpr_write_b32 a231, v52\nv_accvgpr_write_b32 a232, v52\nv_accvgpr_write_b32 a233, v52\nv_accvgpr_write_b32 a234, v52\nv_accvgpr_write_b32 a235, v52\nv_accvgpr_write_b32 a236, v52\nv_accvgpr_write_b32 a237, v52\n"
"v_accvgpr_write_b32 a238, v52\nv_accvgpr_write_b32 a239, v52\nv_accvgpr_write_b32 a240, v52\nv_accvgpr_write_b32 a241, v52\nv_accvgpr_write_b32 a242, v52\nv_accvgpr_write_b32 a243, v52\nv_accvgpr_write_b32 a244, v52\nv_accvgpr_write_b32 a245, v52\n"
"v_accvgpr_write_b32 a246, v52\nv_accvgpr_write_b32 a247, v52\nv_accvgpr_write_b32 a248, v52\nv_accvgpr_write_b32 a249, v52\nv_accvgpr_write_b32 a250, v52\nv_accvgpr_write_b32 a251, v52\nv_accvgpr_write_b32 a252, v52\nv_accvgpr_write_b32 a253, v52\n"
"v_accvgpr_write_b32 a254, v52\nv_accvgpr_write_b32 a255, v52\nv_accvgpr_write_b32 a200, v52\nv_accvgpr_write_b32 a201, v52\nv_accvgpr_write_b32 a202, v52\nv_accvgpr_write_b32 a203, v52\ns_lshl_b32 s89, s67, 21\ns_mov_b32 s66, 0\n"
".Lfb2:\ns_add_u32 s68, s89, 4096\nbuffer_load_dwordx4 a[112:115], v66, s[40:43], s89 offen offset:0\nbuffer_load_dwordx4 a[116:119], v66, s[40:43], s89 offen offset:64\nbuffer_load_dwordx4 a[120:123], v66, s[40:43], s89 offen offset:128\nbuffer_load_dwordx4 a[124:127], v66, s[40:43], s89 offen offset:192\nbuffer_load_dwordx4 a[128:131], v66, s[40:43], s68 offen offset:0\nbuffer_load_dwordx4 a[132:135], v66, s[40:43], s68 offen offset:64\n"
"buffer_load_dwordx4 a[136:139], v66, s[40:43], s68 offen offset:128\nbuffer_load_dwordx4 a[140:143], v66, s[40:43], s68 offen offset:192\nbuffer_load_ushort v8, v67, s[44:47], s89 offen offset:0\nbuffer_load_ushort v9, v67, s[44:47], s89 offen offset:256\nbuffer_load_ushort v10, v67, s[44:47], s89 offen offset:512\nbuffer_load_ushort v11, v67, s[44:47], s89 offen offset:768\nbuffer_load_ushort v12, v67, s[44:47], s68 offen offset:0\nbuffer_load_ushort v13, v67, s[44:47], s68 offen offset:256\n"
"buffer_load_ushort v14, v67, s[44:47], s68 offen offset:512\nbuffer_load_ushort v15, v67, s[44:47], s68 offen offset:768\ns_waitcnt vmcnt(0)\nv_lshl_or_b32 v8, v9, 16, v8\nv_accvgpr_write_b32 a144, v8\nv_lshl_or_b32 v10, v11, 16, v10\nv_accvgpr_write_b32 a145, v10\nv_lshl_or_b32 v12, v13, 16, v12\n"
"v_accvgpr_write_b32 a146, v12\nv_lshl_or_b32 v14, v15, 16, v14\nv_accvgpr_write_b32 a147, v14\nbuffer_load_ushort v8, v67, s[44:47], s89 offen offset:32\nbuffer_load_ushort v9, v67, s[44:47], s89 offen offset:288\nbuffer_load_ushort v10, v67, s[44:47], s89 offen offset:544\nbuffer_load_ushort v11, v67, s[44:47], s89 offen offset:800\nbuffer_load_ushort v12, v67, s[44:47], s68 offen offset:32\n"
"buffer_load_ushort v13, v67, s[44:47], s68 offen offset:288\nbuffer_load_ushort v14, v67, s[44:47], s68 offen offset:544\nbuffer_load_ushort v15, v67, s[44:47], s68 offen offset:800\ns_waitcnt vmcnt(0)\nv_lshl_or_b32 v8, v9, 16, v8\nv_accvgpr_write_b32 a148, v8\nv_lshl_or_b32 v10, v11, 16, v10\nv_accvgpr_write_b32 a149, v10\n"
"v_lshl_or_b32 v12, v13, 16, v12\nv_accvgpr_write_b32 a150, v12\nv_lshl_or_b32 v14, v15, 16, v14\nv_accvgpr_write_b32 a151, v14\nbuffer_load_ushort v8, v67, s[44:47], s89 offen offset:64\nbuffer_load_ushort v9, v67, s[44:47], s89 offen offset:320\nbuffer_load_ushort v10, v67, s[44:47], s89 offen offset:576\nbuffer_load_ushort v11, v67, s[44:47], s89 offen offset:832\n"
"buffer_load_ushort v12, v67, s[44:47], s68 offen offset:64\nbuffer_load_ushort v13, v67, s[44:47], s68 offen offset:320\nbuffer_load_ushort v14, v67, s[44:47], s68 offen offset:576\nbuffer_load_ushort v15, v67, s[44:47], s68 offen offset:832\ns_waitcnt vmcnt(0)\nv_lshl_or_b32 v8, v9, 16, v8\nv_accvgpr_write_b32 a152, v8\nv_lshl_or_b32 v10, v11, 16, v10\n"
"v_accvgpr_write_b32 a153, v10\nv_lshl_or_b32 v12, v13, 16, v12\nv_accvgpr_write_b32 a154, v12\nv_lshl_or_b32 v14, v15, 16, v14\nv_accvgpr_write_b32 a155, v14\nbuffer_load_ushort v8, v67, s[44:47], s89 offen offset:96\nbuffer_load_ushort v9, v67, s[44:47], s89 offen offset:352\nbuffer_load_ushort v10, v67, s[44:47], s89 offen offset:608\n"
"buffer_load_ushort v11, v67, s[44:47], s89 offen offset:864\nbuffer_load_ushort v12, v67, s[44:47], s68 offen offset:96\nbuffer_load_ushort v13, v67, s[44:47], s68 offen offset:352\nbuffer_load_ushort v14, v67, s[44:47], s68 offen offset:608\nbuffer_load_ushort v15, v67, s[44:47], s68 offen offset:864\ns_waitcnt vmcnt(0)\nv_lshl_or_b32 v8, v9, 16, v8\nv_accvgpr_write_b32 a156, v8\n"
"v_lshl_or_b32 v10, v11, 16, v10\nv_accvgpr_write_b32 a157, v10\nv_lshl_or_b32 v12, v13, 16, v12\nv_accvgpr_write_b32 a158, v12\nv_lshl_or_b32 v14, v15, 16, v14\nv_accvgpr_write_b32 a159, v14\nbuffer_load_ushort v8, v67, s[44:47], s89 offen offset:128\nbuffer_load_ushort v9, v67, s[44:47], s89 offen offset:384\n"
"buffer_load_ushort v10, v67, s[44:47], s89 offen offset:640\nbuffer_load_ushort v11, v67, s[44:47], s89 offen offset:896\nbuffer_load_ushort v12, v67, s[44:47], s68 offen offset:128\nbuffer_load_ushort v13, v67, s[44:47], s68 offen offset:384\nbuffer_load_ushort v14, v67, s[44:47], s68 offen offset:640\nbuffer_load_ushort v15, v67, s[44:47], s68 offen offset:896\ns_waitcnt vmcnt(0)\nv_lshl_or_b32 v8, v9, 16, v8\n"
"v_accvgpr_write_b32 a160, v8\nv_lshl_or_b32 v10, v11, 16, v10\nv_accvgpr_write_b32 a161, v10\nv_lshl_or_b32 v12, v13, 16, v12\nv_accvgpr_write_b32 a162, v12\nv_lshl_or_b32 v14, v15, 16, v14\nv_accvgpr_write_b32 a163, v14\nbuffer_load_ushort v8, v67, s[44:47], s89 offen offset:160\n"
"buffer_load_ushort v9, v67, s[44:47], s89 offen offset:416\nbuffer_load_ushort v10, v67, s[44:47], s89 offen offset:672\nbuffer_load_ushort v11, v67, s[44:47], s89 offen offset:928\nbuffer_load_ushort v12, v67, s[44:47], s68 offen offset:160\nbuffer_load_ushort v13, v67, s[44:47], s68 offen offset:416\nbuffer_load_ushort v14, v67, s[44:47], s68 offen offset:672\nbuffer_load_ushort v15, v67, s[44:47], s68 offen offset:928\ns_waitcnt vmcnt(0)\n"
"v_lshl_or_b32 v8, v9, 16, v8\nv_accvgpr_write_b32 a164, v8\nv_lshl_or_b32 v10, v11, 16, v10\nv_accvgpr_write_b32 a165, v10\nv_lshl_or_b32 v12, v13, 16, v12\nv_accvgpr_write_b32 a166, v12\nv_lshl_or_b32 v14, v15, 16, v14\nv_accvgpr_write_b32 a167, v14\n"
"buffer_load_ushort v8, v67, s[44:47], s89 offen offset:192\nbuffer_load_ushort v9, v67, s[44:47], s89 offen offset:448\nbuffer_load_ushort v10, v67, s[44:47], s89 offen offset:704\nbuffer_load_ushort v11, v67, s[44:47], s89 offen offset:960\nbuffer_load_ushort v12, v67, s[44:47], s68 offen offset:192\nbuffer_load_ushort v13, v67, s[44:47], s68 offen offset:448\nbuffer_load_ushort v14, v67, s[44:47], s68 offen offset:704\nbuffer_load_ushort v15, v67, s[44:47], s68 offen offset:960\n"
"s_waitcnt vmcnt(0)\nv_lshl_or_b32 v8, v9, 16, v8\nv_accvgpr_write_b32 a168, v8\nv_lshl_or_b32 v10, v11, 16, v10\nv_accvgpr_write_b32 a169, v10\nv_lshl_or_b32 v12, v13, 16, v12\nv_accvgpr_write_b32 a170, v12\nv_lshl_or_b32 v14, v15, 16, v14\n"
"v_accvgpr_write_b32 a171, v14\nbuffer_load_ushort v8, v67, s[44:47], s89 offen offset:224\nbuffer_load_ushort v9, v67, s[44:47], s89 offen offset:480\nbuffer_load_ushort v10, v67, s[44:47], s89 offen offset:736\nbuffer_load_ushort v11, v67, s[44:47], s89 offen offset:992\nbuffer_load_ushort v12, v67, s[44:47], s68 offen offset:224\nbuffer_load_ushort v13, v67, s[44:47], s68 offen offset:480\nbuffer_load_ushort v14, v67, s[44:47], s68 offen offset:736\n"
"buffer_load_ushort v15, v67, s[44:47], s68 offen offset:992\ns_waitcnt vmcnt(0)\nv_lshl_or_b32 v8, v9, 16, v8\nv_accvgpr_write_b32 a172, v8\nv_lshl_or_b32 v10, v11, 16, v10\nv_accvgpr_write_b32 a173, v10\nv_lshl_or_b32 v12, v13, 16, v12\nv_accvgpr_write_b32 a174, v12\n"
"v_lshl_or_b32 v14, v15, 16, v14\nv_accvgpr_write_b32 a175, v14\nQcvaa 0,112,0,24\nQmvaa 0,116,4\nQmvaa 0,120,8\nQmvaa 0,124,12\nQcvaa 4,128,0,24\nQmvaa 4,132,4\n"
"Qmvaa 4,136,8\nQmvaa 4,140,12\ns_nop 7\nQe 0\nQe 1\nQe 2\nQe 3\nQe 4\n"
"Qe 5\nQe 6\nQe 7\ns_nop 0\nQk 16,0,1\nQk 17,2,3\nQk 18,4,5\nQk 19,6,7\n"
"s_nop 1\nQmvav 80,144,16\nQmvav 84,148,16\nQmvav 88,152,16\nQmvav 92,156,16\nQmvav 96,160,16\nQmvav 100,164,16\nQmvav 104,168,16\n"
"Qmvav 108,172,16\nQmaav 176,204,16\nQcvaa 0,112,16,28\nQmvaa 0,116,20\nQmvaa 0,120,24\nQmvaa 0,124,28\nQcvaa 4,128,16,28\nQmvaa 4,132,20\n"
"Qmvaa 4,136,24\nQmvaa 4,140,28\ns_nop 7\nQe 0\nQe 1\nQe 2\nQe 3\nQe 4\n"
"Qe 5\nQe 6\nQe 7\ns_nop 0\nQk 16,0,1\nQk 17,2,3\nQk 18,4,5\nQk 19,6,7\n"
"s_nop 1\nQmvav 112,144,16\nQmvav 116,148,16\nQmvav 120,152,16\nQmvav 124,156,16\nQmvav 128,160,16\nQmvav 132,164,16\nQmvav 136,168,16\n"
"Qmvav 140,172,16\nQmaav 180,204,16\nQcvaa 0,112,32,32\nQmvaa 0,116,36\nQmvaa 0,120,40\nQmvaa 0,124,44\nQcvaa 4,128,32,32\nQmvaa 4,132,36\n"
"Qmvaa 4,136,40\nQmvaa 4,140,44\ns_nop 7\nQe 0\nQe 1\nQe 2\nQe 3\nQe 4\n"
"Qe 5\nQe 6\nQe 7\ns_nop 0\nQk 16,0,1\nQk 17,2,3\nQk 18,4,5\nQk 19,6,7\n"
"s_nop 1\nQmvav 144,144,16\nQmvav 148,148,16\nQmvav 152,152,16\nQmvav 156,156,16\nQmvav 160,160,16\nQmvav 164,164,16\nQmvav 168,168,16\n"
"Qmvav 172,172,16\nQmaav 184,204,16\nQcvaa 0,112,48,36\nQmvaa 0,116,52\nQmvaa 0,120,56\nQmvaa 0,124,60\nQcvaa 4,128,48,36\nQmvaa 4,132,52\n"
"Qmvaa 4,136,56\nQmvaa 4,140,60\ns_nop 7\nQe 0\nQe 1\nQe 2\nQe 3\nQe 4\n"
"Qe 5\nQe 6\nQe 7\ns_nop 0\nQk 16,0,1\nQk 17,2,3\nQk 18,4,5\nQk 19,6,7\n"
"s_nop 1\nQmvav 176,144,16\nQmvav 180,148,16\nQmvav 184,152,16\nQmvav 188,156,16\nQmvav 192,160,16\nQmvav 196,164,16\nQmvav 200,168,16\n"
"Qmvav 204,172,16\nQmaav 188,204,16\nQcvaa 0,112,64,40\nQmvaa 0,116,68\nQmvaa 0,120,72\nQmvaa 0,124,76\nQcvaa 4,128,64,40\nQmvaa 4,132,68\n"
"Qmvaa 4,136,72\nQmvaa 4,140,76\ns_nop 7\nQe 0\nQe 1\nQe 2\nQe 3\nQe 4\n"
"Qe 5\nQe 6\nQe 7\ns_nop 0\nQk 16,0,1\nQk 17,2,3\nQk 18,4,5\nQk 19,6,7\n"
"s_nop 1\nQmvav 208,144,16\nQmvav 212,148,16\nQmvav 216,152,16\nQmvav 220,156,16\nQmvav 224,160,16\nQmvav 228,164,16\nQmvav 232,168,16\n"
"Qmvav 236,172,16\nQmaav 192,204,16\nQcvaa 0,112,80,44\nQmvaa 0,116,84\nQmvaa 0,120,88\nQmvaa 0,124,92\nQcvaa 4,128,80,44\nQmvaa 4,132,84\n"
"Qmvaa 4,136,88\nQmvaa 4,140,92\ns_nop 7\nQe 0\nQe 1\nQe 2\nQe 3\nQe 4\n"
"Qe 5\nQe 6\nQe 7\ns_nop 0\nQk 16,0,1\nQk 17,2,3\nQk 18,4,5\nQk 19,6,7\n"
"s_nop 1\nQmvav 240,144,16\nQmvav 244,148,16\nQmvav 248,152,16\nQmvav 252,156,16\nQmaav 208,160,16\nQmaav 212,164,16\nQmaav 216,168,16\n"
"Qmaav 220,172,16\nQmaav 196,204,16\nQcvaa 0,112,96,48\nQmvaa 0,116,100\nQmvaa 0,120,104\nQmvaa 0,124,108\nQcvaa 4,128,96,48\nQmvaa 4,132,100\n"
"Qmvaa 4,136,104\nQmvaa 4,140,108\ns_nop 7\nQe 0\nQe 1\nQe 2\nQe 3\nQe 4\n"
"Qe 5\nQe 6\nQe 7\ns_nop 0\nQk 16,0,1\nQk 17,2,3\nQk 18,4,5\nQk 19,6,7\n"
"s_nop 1\nQmaav 224,144,16\nQmaav 228,148,16\nQmaav 232,152,16\nQmaav 236,156,16\nQmaav 240,160,16\nQmaav 244,164,16\nQmaav 248,168,16\n"
"Qmaav 252,172,16\nQmaav 200,204,16\ns_add_u32 s89, s89, 8192\ns_add_u32 s66, s66, 1\ns_cmp_lt_u32 s66, 32\ns_cbranch_scc1 .Lfb2\ns_nop 7\nv_accvgpr_read_b32 v0, a176\n"
"v_accvgpr_read_b32 v4, a180\nv_accvgpr_read_b32 v8, a184\nv_accvgpr_read_b32 v12, a188\nv_accvgpr_read_b32 v16, a192\nv_accvgpr_read_b32 v20, a196\nv_accvgpr_read_b32 v24, a200\ns_cmp_eq_u32 s67, 0\ns_cbranch_scc1 .Lfbret0\n"
"s_branch .Lfbret1\n.Lf2_w:\ns_mov_b32 s97, 0\n.Lf2_l:\ns_sleep 1\nbuffer_load_dword v78, v76, s[24:27], 0 offen sc1\ns_waitcnt vmcnt(0)\ns_add_u32 s97, s97, 1\n"
"s_cmp_gt_u32 s97, 1048576\ns_cbranch_scc1 .Lf2_r\nv_sub_u32 v66, v78, v65\nv_cmp_gt_u32 vcc, 2, v66\ns_and_b64 vcc, vcc, s[28:29]\ns_cbranch_scc1 .Lf2_l\ns_branch .Lf2_r\n.Lf3_w:\n"
"s_mov_b32 s97, 0\n.Lf3_l:\ns_sleep 1\nbuffer_load_dword v78, v76, s[24:27], 0 offen sc1\ns_waitcnt vmcnt(0)\ns_add_u32 s97, s97, 1\ns_cmp_gt_u32 s97, 1048576\ns_cbranch_scc1 .Lf3_r\n"
"v_sub_u32 v66, v78, v65\nv_cmp_gt_u32 vcc, 3, v66\ns_and_b64 vcc, vcc, s[28:29]\ns_cbranch_scc1 .Lf3_l\ns_branch .Lf3_r\n.Lf4_w:\ns_mov_b32 s97, 0\n.Lf4_l:\n"
"s_sleep 1\nbuffer_load_dword v78, v76, s[24:27], 0 offen sc1\ns_waitcnt vmcnt(0)\ns_add_u32 s97, s97, 1\ns_cmp_gt_u32 s97, 1048576\ns_cbranch_scc1 .Lf4_r\nv_sub_u32 v66, v78, v65\nv_cmp_gt_u32 vcc, 4, v66\n"
"s_and_b64 vcc, vcc, s[28:29]\ns_cbranch_scc1 .Lf4_l\ns_branch .Lf4_r\n.Lf5_w:\ns_mov_b32 s97, 0\n.Lf5_l:\ns_sleep 1\nbuffer_load_dword v78, v76, s[24:27], 0 offen sc1\n"
"s_waitcnt vmcnt(0)\ns_add_u32 s97, s97, 1\ns_cmp_gt_u32 s97, 1048576\ns_cbranch_scc1 .Lf5_r\nv_sub_u32 v66, v78, v65\nv_cmp_gt_u32 vcc, 5, v66\ns_and_b64 vcc, vcc, s[28:29]\ns_cbranch_scc1 .Lf5_l\n"
"s_branch .Lf5_r\n.Lx0_w:\ns_mov_b32 s97, 0\n.Lx0_l:\ns_sleep 1\nbuffer_load_dword v78, v76, s[24:27], 0 offen sc1\ns_waitcnt vmcnt(0)\ns_add_u32 s97, s97, 1\n"
"s_cmp_gt_u32 s97, 1048576\ns_cbranch_scc1 .Lx0_r\nv_sub_u32 v66, v78, v65\nv_cmp_gt_u32 vcc, 1, v66\ns_and_b64 vcc, vcc, s[28:29]\ns_cbranch_scc1 .Lx0_l\ns_branch .Lx0_r\n.Lx1_w:\n"
"s_mov_b32 s97, 0\n.Lx1_l:\ns_sleep 1\nbuffer_load_dword v78, v76, s[24:27], 0 offen sc1\ns_waitcnt vmcnt(0)\ns_add_u32 s97, s97, 1\ns_cmp_gt_u32 s97, 1048576\ns_cbranch_scc1 .Lx1_r\n"
"v_sub_u32 v66, v78, v65\nv_cmp_gt_u32 vcc, 1, v66\ns_and_b64 vcc, vcc, s[28:29]\ns_cbranch_scc1 .Lx1_l\ns_branch .Lx1_r\n.Lexit:\n"
  :: "s"((unsigned)kp), "s"((unsigned)(kp >> 32)), "s"((unsigned)vp), "s"((unsigned)(vp >> 32)), "s"((unsigned)xp), "s"((unsigned)(xp >> 32)),
     "s"((unsigned)wp), "s"((unsigned)(wp >> 32)), "s"((unsigned)op), "s"((unsigned)(op >> 32)), "s"(ldsb), "s"(wid), "s"(head0),
     "s"((unsigned)dp), "s"((unsigned)(dp >> 32)), "s"((unsigned)wop), "s"((unsigned)(wop >> 32)), "s"((unsigned)yp), "s"((unsigned)(yp >> 32)),
     "s"(hg), "s"(b * 8u + hg)
  : "v0", "v1", "v2", "v3", "v4", "v5", "v6", "v7", "v8", "v9", "v10", "v11", "v12", "v13", "v14", "v15", "v16", "v17", "v18", "v19", "v20", "v21", "v22", "v23", "v24", "v25", "v26", "v27", "v28", "v29", "v30", "v31", "v32", "v33", "v34", "v35", "v36", "v37", "v38", "v39", "v40", "v41", "v42", "v43", "v44", "v45", "v46", "v47", "v48", "v49", "v50", "v51", "v52", "v53", "v54", "v55", "v56", "v57", "v58", "v59", "v60", "v61", "v62", "v63", "v64", "v65", "v66", "v67", "v68", "v69", "v70", "v71", "v72", "v73", "v74", "v75", "v76", "v77", "v78", "v79", "v80", "v81", "v82", "v83", "v84", "v85", "v86", "v87", "v88", "v89", "v90", "v91", "v92", "v93", "v94", "v95", "v96", "v97", "v98", "v99", "v100", "v101", "v102", "v103", "v104", "v105", "v106", "v107", "v108", "v109", "v110", "v111", "v112", "v113", "v114", "v115", "v116", "v117", "v118", "v119", "v120", "v121", "v122", "v123", "v124", "v125", "v126", "v127", "v128", "v129", "v130", "v131", "v132", "v133", "v134", "v135", "v136", "v137", "v138", "v139", "v140", "v141", "v142", "v143", "v144", "v145", "v146", "v147", "v148", "v149", "v150", "v151", "v152", "v153", "v154", "v155", "v156", "v157", "v158", "v159", "v160", "v161", "v162", "v163", "v164", "v165", "v166", "v167", "v168", "v169", "v170", "v171", "v172", "v173", "v174", "v175", "v176", "v177", "v178", "v179", "v180", "v181", "v182", "v183", "v184", "v185", "v186", "v187", "v188", "v189", "v190", "v191", "v192", "v193", "v194", "v195", "v196", "v197", "v198", "v199", "v200", "v201", "v202", "v203", "v204", "v205", "v206", "v207", "v208", "v209", "v210", "v211", "v212", "v213", "v214", "v215", "v216", "v217", "v218", "v219", "v220", "v221", "v222", "v223", "v224", "v225", "v226", "v227", "v228", "v229", "v230", "v231", "v232", "v233", "v234", "v235", "v236", "v237", "v238", "v239", "v240", "v241", "v242", "v243", "v244", "v245", "v246", "v247", "v248", "v249", "v250", "v251", "v252", "v253", "v254", "v255", "a0", "a1", "a2", "a3", "a4", "a5", "a6", "a7", "a8", "a9", "a10", "a11", "a12", "a13", "a14", "a15", "a16", "a17", "a18", "a19", "a20", "a21", "a22", "a23", "a24", "a25", "a26", "a27", "a28", "a29", "a30", "a31", "a32", "a33", "a34", "a35", "a36", "a37", "a38", "a39", "a40", "a41", "a42", "a43", "a44", "a45", "a46", "a47", "a48", "a49", "a50", "a51", "a52", "a53", "a54", "a55", "a56", "a57", "a58", "a59", "a60", "a61", "a62", "a63", "a64", "a65", "a66", "a67", "a68", "a69", "a70", "a71", "a72", "a73", "a74", "a75", "a76", "a77", "a78", "a79", "a80", "a81", "a82", "a83", "a84", "a85", "a86", "a87", "a88", "a89", "a90", "a91", "a92", "a93", "a94", "a95", "a96", "a97", "a98", "a99", "a100", "a101", "a102", "a103", "a104", "a105", "a106", "a107", "a108", "a109", "a110", "a111", "a112", "a113", "a114", "a115", "a116", "a117", "a118", "a119", "a120", "a121", "a122", "a123", "a124", "a125", "a126", "a127", "a128", "a129", "a130", "a131", "a132", "a133", "a134", "a135", "a136", "a137", "a138", "a139", "a140", "a141", "a142", "a143", "a144", "a145", "a146", "a147", "a148", "a149", "a150", "a151", "a152", "a153", "a154", "a155", "a156", "a157", "a158", "a159", "a160", "a161", "a162", "a163", "a164", "a165", "a166", "a167", "a168", "a169", "a170", "a171", "a172", "a173", "a174", "a175", "a176", "a177", "a178", "a179", "a180", "a181", "a182", "a183", "a184", "a185", "a186", "a187", "a188", "a189", "a190", "a191", "a192", "a193", "a194", "a195", "a196", "a197", "a198", "a199", "a200", "a201", "a202", "a203", "a204", "a205", "a206", "a207", "a208", "a209", "a210", "a211", "a212", "a213", "a214", "a215", "a216", "a217", "a218", "a219", "a220", "a221", "a222", "a223", "a224", "a225", "a226", "a227", "a228", "a229", "a230", "a231", "a232", "a233", "a234", "a235", "a236", "a237", "a238", "a239", "a240", "a241", "a242", "a243", "a244", "a245", "a246", "a247", "a248", "a249", "a250", "a251", "a252", "a253", "a254", "a255", "s24", "s25", "s26", "s27", "s28", "s29", "s30", "s31", "s40", "s41", "s42", "s43", "s44", "s45", "s46", "s47", "s48", "s49", "s50", "s51", "s52", "s53", "s54", "s55", "s56", "s57", "s58", "s59", "s60", "s61", "s62", "s63", "s64", "s65", "s66", "s67", "s68", "s69", "s70", "s71", "s72", "s73", "s74", "s75", "s76", "s77", "s78", "s79", "s80", "s81", "s82", "s83", "s84", "s85", "s86", "s87", "s88", "s89", "s90", "s91", "s92", "s93", "s94", "s95", "s96", "s97", "s98", "s99", "vcc", "scc", "memory");
}

extern "C" void kernel_launch(void* const* d_in, const int* in_sizes, int n_in, void* d_out, int out_size, void* d_ws, size_t ws_size, hipStream_t stream) {
  typedef const unsigned short* P;
  (void)hipFuncSetAttribute((const void*)mha2_asm, hipFuncAttributeMaxDynamicSharedMemorySize, 163840);
  hipLaunchKernelGGL(mha2_asm, dim3(256), dim3(256), 163840, stream, (P)d_in[0], (P)d_in[5], (P)d_in[3], (P)d_in[8], (P)d_in[1], (P)d_in[6], (P)d_in[2], (P)d_in[7],
                     (P)d_in[4], (P)d_in[9], (char*)d_ws, (unsigned short*)d_out);
}
```

```cpp
#include <hip/hip_runtime.h>
#include <stdint.h>

__global__ __launch_bounds__(256, 1) __attribute__((aligned(4096)))
void mha2_asm(const unsigned short* __restrict__ X0, const unsigned short* __restrict__ X1, const unsigned short* __restrict__ W0, const unsigned short* __restrict__ W1,
              const unsigned short* __restrict__ K0, const unsigned short* __restrict__ K1,
              const unsigned short* __restrict__ V0, const unsigned short* __restrict__ V1,
              const unsigned short* __restrict__ WO0, const unsigned short* __restrict__ WO1, char* ws, unsigned short* out) {
  extern __shared__ __attribute__((aligned(16))) char lds[];
  const unsigned c = blockIdx.x;
  const unsigned xcd = c & 7u, idx = c >> 3;
  const unsigned hg = idx & 7u, bb = xcd * 4u + (idx >> 3);
  const unsigned bk = bb >> 4, b = bb & 15u;
  const unsigned wid = __builtin_amdgcn_readfirstlane(threadIdx.x >> 6);
  const unsigned head0 = (hg >> 2) * 16u + (hg & 3u) * 2u + (wid >> 1);
  const unsigned long long kp = (unsigned long long)(bk ? K1 : K0) + ((unsigned long long)b * 32u + head0) * 262144ull - 1024ull;
  const unsigned long long vp = (unsigned long long)(bk ? V1 : V0) + ((unsigned long long)b * 32u + head0) * 262144ull - 1024ull;
  const unsigned long long xp = (unsigned long long)(bk ? X1 : X0) + (unsigned long long)b * (224u * 1280u);
  const unsigned long long wp = (unsigned long long)(bk ? W1 : W0);
  const unsigned long long op = (unsigned long long)ws + 0ull + (unsigned long long)bb * 2295808ull;
  const unsigned ldsb = (unsigned)(size_t)lds;
  const unsigned long long wop = (unsigned long long)(bk ? WO1 : WO0) - 4096ull;
  const unsigned long long yp = (unsigned long long)out + (unsigned long long)b * (224ull * 2560ull) + bk * 1280u + (hg & 3u) * 320u;
  const unsigned long long dp = (unsigned long long)ws + (100ull << 20) + (c * 4u + wid) * 256u;
  asm volatile(
".macro Qta d,v,o\nds_read_b64_tr_b16 a[\\d:\\d+1], v\\v offset:\\o\n.endm\n.macro Qdv d,v,o\nds_read_b128 v[\\d:\\d+3], v\\v offset:\\o\n.endm\n.macro Qda d,v,o\nds_read_b128 a[\\d:\\d+3], v\\v offset:\\o\n"
".endm\n.macro Qzvav d,a,b\nv_mfma_f32_16x16x32_bf16 v[\\d:\\d+3], a[\\a:\\a+3], v[\\b:\\b+3], 0\n.endm\n.macro Qmvaa d,a,b\nv_mfma_f32_16x16x32_bf16 v[\\d:\\d+3], a[\\a:\\a+3], a[\\b:\\b+3], v[\\d:\\d+3]\n.endm\n.macro Qzaav d,a,b\n"
"v_mfma_f32_16x16x32_bf16 a[\\d:\\d+3], a[\\a:\\a+3], v[\\b:\\b+3], 0\n.endm\n.macro Qmaaa d,a,b\nv_mfma_f32_16x16x32_bf16 a[\\d:\\d+3], a[\\a:\\a+3], a[\\b:\\b+3], a[\\d:\\d+3]\n.endm\n.macro Qmvav d,a,b\nv_mfma_f32_16x16x32_bf16 v[\\d:\\d+3], a[\\a:\\a+3], v[\\b:\\b+3], v[\\d:\\d+3]\n.endm\n"
".macro Qmaav d,a,b\nv_mfma_f32_16x16x32_bf16 a[\\d:\\d+3], a[\\a:\\a+3], v[\\b:\\b+3], a[\\d:\\d+3]\n.endm\n.macro Qk d,a,b\nv_cvt_pk_bf16_f32 v\\d, v\\a, v\\b\n.endm\n.macro Qzvaa d,a,b\nv_mfma_f32_16x16x32_bf16 v[\\d:\\d+3], a[\\a:\\a+3], a[\\b:\\b+3], 0\n"
".endm\n.macro Qa d,a,b\nv_add_f32 v\\d, v\\a, v\\b\n.endm\n.macro Qe d\nv_exp_f32 v\\d, v\\d\n.endm\n.macro Qcvaa d,a,b,c\n"
"v_mfma_f32_16x16x32_bf16 v[\\d:\\d+3], a[\\a:\\a+3], a[\\b:\\b+3], v[\\c:\\c+3]\n.endm\n.p2align 7\nv_mbcnt_lo_u32_b32 v0, -1, 0\nv_mbcnt_hi_u32_b32 v0, -1, v0\nv_lshlrev_b32 v60, 4, v0\ns_add_u32 m0, %10, 160768\ns_getpc_b64 s[56:57]\n"
".Lpc0:\ns_and_b32 s56, s56, 0xffffff80\ns_and_b32 s57, s57, 0xffff\ns_mov_b32 s58, .Lend-.Lpc0\ns_mov_b32 s59, 0x20000\ns_and_b32 s94, %20, 31\ns_lshl_b32 s94, s94, 2\ns_add_u32 s94, s94, %11\n"
"s_lshl_b32 s94, s94, 10\nbuffer_load_dwordx4 v60, s[56:59], s94 offen lds\ns_mov_b32 s52, %6\ns_and_b32 s53, %7, 0xffff\ns_mov_b32 s54, 5242880\ns_mov_b32 s55, 0x20000\ns_mov_b32 s48, %4\ns_and_b32 s49, %5, 0xffff\n"
"s_mov_b32 s50, 286720\ns_mov_b32 s51, 0x20000\ns_mov_b32 s70, %11\ns_and_b32 s92, s70, 1\ns_lshr_b32 s93, s70, 1\nv_lshrrev_b32 v2, 4, v0\ns_lshl_b32 s68, s92, 18\nv_and_b32 v4, 3, v0\n"
"v_lshlrev_b32 v4, 4, v4\nv_and_b32 v1, 1, v2\nv_lshl_add_u32 v4, v1, 6, v4\nv_lshrrev_b32 v1, 5, v0\nv_lshl_add_u32 v4, v1, 15, v4\nv_bfe_u32 v1, v0, 2, 2\nv_lshl_add_u32 v4, v1, 13, v4\nv_add_u32 v62, s68, v4\n"
"s_lshl_b32 s87, %12, 8\ns_mul_i32 s62, s70, 4128\ns_add_u32 s62, s62, %10\ns_add_u32 s62, s62, 33024\ns_add_u32 m0, s62, 0\ns_add_u32 s66, s87, 0\ns_nop 0\nbuffer_load_dwordx4 v62, s[52:55], s66 offen lds\n"
"s_add_u32 s66, s87, 64504\ns_nop 0\nbuffer_load_dwordx4 v62, s[52:55], s66 offen offset:1032 lds\ns_add_u32 s66, s87, 129008\ns_nop 0\nbuffer_load_dwordx4 v62, s[52:55], s66 offen offset:2064 lds\ns_add_u32 s66, s87, 193512\ns_nop 0\n"
"buffer_load_dwordx4 v62, s[52:55], s66 offen offset:3096 lds\ns_lshl_b32 s69, s92, 1\nv_lshrrev_b32 v3, 5, v0\nv_and_b32 v4, 3, v0\nv_xor_b32 v4, s69, v4\nv_lshlrev_b32 v4, 4, v4\nv_lshl_add_u32 v4, v3, 6, v4\nv_bfe_u32 v1, v0, 2, 3\n"
"v_mul_u32_u24 v64, 1280, v1\nv_add_u32 v64, v64, v4\ns_mul_i32 s86, s93, 143360\ns_mul_i32 s68, s92, 10240\ns_add_u32 s86, s86, s68\ns_mul_i32 s63, s92, 14336\ns_mul_i32 s68, s93, 7168\ns_add_u32 s63, s63, s68\n"
"s_add_u32 s63, s63, %10\ns_add_u32 s63, s63, 99072\ns_add_u32 m0, s63, 33024\ns_add_u32 s97, s86, 0\ns_nop 0\nbuffer_load_dwordx4 v64, s[48:51], s97 offen lds\ns_add_u32 s97, s86, 19456\ns_nop 0\n"
"buffer_load_dwordx4 v64, s[48:51], s97 offen offset:1024 lds\ns_add_u32 s97, s86, 38912\ns_nop 0\nbuffer_load_dwordx4 v64, s[48:51], s97 offen offset:2048 lds\ns_add_u32 s97, s86, 58368\ns_nop 0\nbuffer_load_dwordx4 v64, s[48:51], s97 offen offset:3072 lds\ns_add_u32 m0, s63, 37120\n"
"s_add_u32 s97, s86, 81920\ns_nop 0\nbuffer_load_dwordx4 v64, s[48:51], s97 offen lds\ns_add_u32 s97, s86, 101376\ns_nop 0\nbuffer_load_dwordx4 v64, s[48:51], s97 offen offset:1024 lds\ns_add_u32 s97, s86, 120832\ns_nop 0\n"
"buffer_load_dwordx4 v64, s[48:51], s97 offen offset:2048 lds\ns_add_u32 m0, s62, 16512\ns_add_u32 s66, s87, 128\ns_nop 0\nbuffer_load_dwordx4 v62, s[52:55], s66 offen lds\ns_mov_b32 s24, %8\ns_and_b32 s25, %9, 0xffff\ns_mov_b32 s26, 2295808\n"
"s_mov_b32 s27, 0x20000\nv_and_b32 v5, 7, v0\nv_lshlrev_b32 v5, 7, v5\ns_add_u32 s66, s87, 64632\ns_nop 0\nbuffer_load_dwordx4 v62, s[52:55], s66 offen offset:1032 lds\nv_add_u32 v76, 2293760, v5\ns_lshl_b32 s68, s70, 2\n"
"s_add_u32 s68, s68, 4\nv_bfe_u32 v5, v0, 3, 1\nv_mad_u32_u24 v76, v5, s68, v76\nbuffer_load_dword v65, v76, s[24:27], 0 offen sc1\ns_add_u32 s66, s87, 129136\ns_nop 0\nbuffer_load_dwordx4 v62, s[52:55], s66 offen offset:2064 lds\ns_mov_b32 s40, %0\n"
"s_and_b32 s41, %1, 0xffff\ns_mov_b32 s42, 2360320\ns_mov_b32 s43, 0x20000\ns_mov_b32 s44, %2\ns_and_b32 s45, %3, 0xffff\ns_add_u32 s66, s87, 193640\ns_nop 0\nbuffer_load_dwordx4 v62, s[52:55], s66 offen offset:3096 lds\n"
"s_mov_b32 s46, 2360320\ns_mov_b32 s47, 0x20000\ns_mov_b32 s56, %8\ns_and_b32 s57, %9, 0xffff\ns_mov_b32 s58, 0\ns_mov_b32 s59, 0x20000\ns_mov_b64 exec, 0\ns_cmp_eq_u32 s70, 0\n"
"s_cbranch_scc1 .Lpg0\ns_cmp_eq_u32 s70, 1\ns_cbranch_scc1 .Lpg1\ns_cmp_eq_u32 s70, 2\ns_cbranch_scc1 .Lpg2\ns_branch .Lpg3\n.Ltr0:\ns_mov_b64 exec, -1\n"
"s_mov_b32 s60, 0\ns_mov_b32 s90, 896\ns_mov_b32 s67, 0\ns_mov_b32 s72, 0x3e0293ee\ns_lshl_b32 s68, s92, 12\ns_add_u32 s88, s68, 128\ns_add_u32 m0, s63, 0\ns_add_u32 s97, s86, 128\n"
"s_nop 0\nbuffer_load_dwordx4 v64, s[48:51], s97 offen lds\nv_lshl_add_u32 v4, v1, 8, v4\nv_add_u32 v69, s88, v4\nv_and_b32 v4, 1, v0\nv_lshlrev_b32 v4, 4, v4\nv_lshl_add_u32 v4, v2, 5, v4\nv_bfe_u32 v1, v0, 1, 3\n"
"s_add_u32 s97, s86, 19584\ns_nop 0\nbuffer_load_dwordx4 v64, s[48:51], s97 offen offset:1024 lds\nv_lshl_add_u32 v4, v1, 8, v4\nv_add_u32 v72, s88, v4\ns_lshl_b32 s69, s93, 14\ns_add_u32 s69, s69, %10\ns_add_u32 s61, s69, s68\n"
"v_bfe_u32 v3, v0, 3, 1\ns_add_u32 s97, s86, 39040\ns_nop 0\nbuffer_load_dwordx4 v64, s[48:51], s97 offen offset:2048 lds\nv_lshlrev_b32 v4, 1, v3\nv_xor_b32 v4, v4, v2\nv_and_b32 v1, 7, v0\nv_lshl_add_u32 v4, v1, 2, v4\n"
"v_lshlrev_b32 v4, 4, v4\nv_lshl_add_u32 v1, v3, 12, v4\ns_add_u32 s97, s86, 58496\ns_nop 0\nbuffer_load_dwordx4 v64, s[48:51], s97 offen offset:3072 lds\nv_add_u32 v68, s69, v1\ns_mul_i32 s68, s92, 7168\ns_add_u32 s68, s68, %10\n"
"s_add_u32 s68, s68, 99072\nv_mul_u32_u24 v1, 14336, v3\nv_add3_u32 v63, v1, v4, s68\ns_add_u32 m0, s63, 4096\ns_add_u32 s97, s86, 82048\ns_nop 0\nbuffer_load_dwordx4 v64, s[48:51], s97 offen lds\nv_lshrrev_b32 v3, 5, v0\n"
"v_and_b32 v4, 31, v0\nv_lshlrev_b32 v4, 3, v4\nv_lshl_add_u32 v4, v3, 12, v4\nv_add_u32 v4, 8192, v4\nv_add_u32 v70, s69, v4\ns_add_u32 s97, s86, 101504\ns_nop 0\nbuffer_load_dwordx4 v64, s[48:51], s97 offen offset:1024 lds\n"
"v_add_u32 v71, 66048, v70\ns_mul_i32 s68, s93, 8256\ns_add_u32 s68, s68, %10\ns_add_u32 s68, s68, 33024\nv_mul_u32_u24 v3, 1032, v2\nv_and_b32 v4, 15, v0\ns_add_u32 s97, s86, 120960\ns_nop 0\n"
"buffer_load_dwordx4 v64, s[48:51], s97 offen offset:2048 lds\nv_lshlrev_b32 v4, 4, v4\nv_add3_u32 v61, v3, v4, s68\ns_lshl_b32 s68, s70, 2\ns_add_u32 s69, %10, 131840\ns_add_u32 s68, s68, s69\nv_mov_b32 v73, s68\ns_add_u32 m0, s62, 33024\n"
"s_add_u32 s66, s87, 524288\ns_nop 0\nbuffer_load_dwordx4 v62, s[52:55], s66 offen lds\nv_and_b32 v3, 3, v0\nv_lshl_add_u32 v74, v3, 2, s69\nv_mov_b32 v75, 0\nds_write_b32 v73, v75\ns_waitcnt lgkmcnt(0)\n"
"s_mul_i32 s91, s70, 7168\ns_add_u32 s66, s87, 588792\ns_nop 0\nbuffer_load_dwordx4 v62, s[52:55], s66 offen offset:1032 lds\ns_add_u32 s91, s91, %10\ns_add_u32 s91, s91, 98304\ns_lshl_b32 s96, s70, 11\ns_add_u32 s96, s96, %10\n"
"v_and_b32 v3, 1, v0\nv_mul_u32_u24 v3, 0xaaaa, v3\ns_add_u32 s66, s87, 653296\ns_nop 0\nbuffer_load_dwordx4 v62, s[52:55], s66 offen offset:2064 lds\nv_or_b32 v77, 0x4444, v3\ns_and_b32 s68, %19, 4\ns_lshl_b32 s28, 15, s68\n"
"s_mov_b32 s29, 0\ns_mov_b32 s71, 0\ns_and_b32 s68, %19, 3\ns_add_u32 s66, s87, 717800\ns_nop 0\nbuffer_load_dwordx4 v62, s[52:55], s66 offen offset:3096 lds\ns_lshl_b32 s99, 3072, s68\ns_or_b32 s99, s99, 0x302\n"
"s_mov_b32 s94, 0x7ffff000\ns_mul_i32 s68, %20, 40960\ns_mul_i32 s69, s93, 20480\ns_add_u32 s68, s68, s69\ns_mov_b64 exec, 0\ns_cmp_eq_u32 s70, 0\ns_cbranch_scc1 .Lpg4\ns_cmp_eq_u32 s70, 1\n"
"s_cbranch_scc1 .Lpg5\ns_cmp_eq_u32 s70, 2\ns_cbranch_scc1 .Lpg6\ns_branch .Lpg7\n.Ltr9:\ns_mov_b64 exec, -1\ns_lshl_b32 s69, s92, 10\ns_add_u32 s68, s68, s69\n"
"s_add_u32 s68, s68, 2048\ns_add_u32 s94, %6, s68\ns_addc_u32 s95, %7, 0\ns_and_b32 s95, s95, 0xffff\ns_add_u32 m0, s62, 49536\ns_add_u32 s66, s87, 524416\ns_nop 0\nbuffer_load_dwordx4 v62, s[52:55], s66 offen lds\n"
"s_mov_b32 s75, 17408\ns_add_u32 s66, s87, 588920\ns_nop 0\nbuffer_load_dwordx4 v62, s[52:55], s66 offen offset:1032 lds\ns_add_u32 s66, s87, 653424\ns_nop 0\nbuffer_load_dwordx4 v62, s[52:55], s66 offen offset:2064 lds\ns_add_u32 s66, s87, 717928\n"
"s_nop 0\nbuffer_load_dwordx4 v62, s[52:55], s66 offen offset:3096 lds\ns_mov_b64 exec, 0\ns_cmp_eq_u32 s70, 0\ns_cbranch_scc1 .Lpg8\ns_cmp_eq_u32 s70, 1\ns_cbranch_scc1 .Lpg9\ns_cmp_eq_u32 s70, 2\n"
"s_cbranch_scc1 .Lpg10\ns_branch .Lpg11\n.Ltr10:\ns_mov_b64 exec, -1\n.Lround:\ns_cmp_lg_u32 s67, 0\ns_cselect_b64 vcc, -1, 0\ns_waitcnt vmcnt(15)\n"
"s_barrier\nQta 112,61,0\nQta 114,61,512\nQdv 24,63,33024\nQta 116,61,4128\nQta 118,61,4640\nQdv 28,63,34048\nQta 120,61,8\n"
"Qta 122,61,520\nQdv 32,63,35072\nQta 124,61,4136\nQta 126,61,4648\nQdv 36,63,36096\nQta 128,61,256\nQta 130,61,768\nQdv 40,63,37120\n"
"Qta 132,61,4384\nQta 134,61,4896\nQdv 44,63,38144\nQta 136,61,264\nQta 138,61,776\nQdv 48,63,39168\nQta 140,61,4392\nQta 142,61,4904\n"
"Qda 176,63,33536\nQda 180,63,34560\nQda 184,63,35584\nQda 188,63,36608\nQda 192,63,37632\nQda 196,63,38656\nQda 200,63,39680\ns_waitcnt lgkmcnt(6)\n"
"Qzvav 80,112,24\nQmvaa 80,116,176\ns_add_u32 s68, s71, 0\nQzvav 84,120,24\ns_cbranch_vccz .Lst27\nbuffer_store_dwordx4 a[0:3], v60, s[56:59], s68 offen offset:0 sc1\n.Lst27:\nQmvaa 84,124,176\n"
"Qzvav 88,128,24\ns_add_u32 s68, s71, 14336\nQmvaa 88,132,176\ns_cbranch_vccz .Lst26\nbuffer_store_dwordx4 a[4:7], v60, s[56:59], s68 offen offset:0 sc1\n.Lst26:\nQzvav 92,136,24\nQmvaa 92,140,176\n"
"s_waitcnt lgkmcnt(0)\nQzvav 112,112,28\ns_waitcnt vmcnt(15)\ns_barrier\nQmvaa 112,116,180\nQta 144,61,16512\ns_add_u32 m0, s62, 0\ns_add_u32 s69, s87, 1048576\n"
"Qzvav 116,120,28\nbuffer_load_dwordx4 v62, s[52:55], s69 offen lds\nQmvaa 116,124,180\nQta 146,61,17024\nQzvav 120,128,28\nQmvaa 120,132,180\nQta 148,61,20640\ns_add_u32 s69, s87, 1113080\n"
"Qzvav 124,136,28\nbuffer_load_dwordx4 v62, s[52:55], s69 offen offset:1032 lds\nQmvaa 124,140,180\nQta 150,61,21152\nQzvav 144,112,32\nQmvaa 144,116,184\nQta 152,61,16520\ns_add_u32 s69, s87, 1177584\n"
"Qzvav 148,120,32\nbuffer_load_dwordx4 v62, s[52:55], s69 offen offset:2064 lds\nQmvaa 148,124,184\nQta 154,61,17032\nQzvav 152,128,32\nQmvaa 152,132,184\nQta 156,61,20648\ns_add_u32 s69, s87, 1242088\n"
"Qzvav 156,136,32\nbuffer_load_dwordx4 v62, s[52:55], s69 offen offset:3096 lds\nQmvaa 156,140,184\nQta 158,61,21160\nQzvav 176,112,36\nQmvaa 176,116,188\nQta 160,61,16768\ns_add_u32 m0, s63, 33024\n"
"s_add_u32 s88, s86, 256\nQzvav 180,120,36\nbuffer_load_dwordx4 v64, s[48:51], s88 offen lds\nQmvaa 180,124,188\nQta 162,61,17280\nQzvav 184,128,36\nQmvaa 184,132,188\nQta 164,61,20896\n"
"s_add_u32 s88, s86, 19712\nQzvav 188,136,36\nbuffer_load_dwordx4 v64, s[48:51], s88 offen offset:1024 lds\nQmvaa 188,140,188\nQta 166,61,21408\nQzvav 208,112,40\nQmvaa 208,116,192\nQta 168,61,16776\n"
"s_add_u32 s88, s86, 39168\nQzvav 212,120,40\nbuffer_load_dwordx4 v64, s[48:51], s88 offen offset:2048 lds\nQmvaa 212,124,192\nQta 170,61,17288\nQzvav 216,128,40\nQmvaa 216,132,192\nQta 172,61,20904\n"
"s_add_u32 s88, s86, 58624\nQzvav 220,136,40\nbuffer_load_dwordx4 v64, s[48:51], s88 offen offset:3072 lds\nQmvaa 220,140,192\nQta 174,61,21416\nQzvav 240,112,44\nQmvaa 240,116,196\ns_add_u32 m0, s63, 37120\n"
"s_add_u32 s88, s86, 82176\nQzvav 244,120,44\nbuffer_load_dwordx4 v64, s[48:51], s88 offen lds\nQmvaa 244,124,196\nQzvav 248,128,44\nQmvaa 248,132,196\ns_add_u32 s88, s86, 101632\nQzvav 252,136,44\n"
"buffer_load_dwordx4 v64, s[48:51], s88 offen offset:1024 lds\nQmvaa 252,140,196\nQzaav 224,112,48\nQmaaa 224,116,200\ns_add_u32 s88, s86, 121088\nQzaav 228,120,48\nbuffer_load_dwordx4 v64, s[48:51], s88 offen offset:2048 lds\nQmaaa 228,124,200\n"
"Qzaav 232,128,48\nQmaaa 232,132,200\nQzaav 236,136,48\nQmaaa 236,140,200\ns_waitcnt lgkmcnt(0)\nQzvav 96,144,24\nQmvaa 96,148,176\ns_add_u32 s68, s71, 28672\n"
"Qzvav 100,152,24\ns_cbranch_vccz .Lst25\nbuffer_store_dwordx4 a[8:11], v60, s[56:59], s68 offen offset:0 sc1\n.Lst25:\nQmvaa 100,156,176\nQzvav 104,160,24\ns_add_u32 s68, s71, 43008\nQmvaa 104,164,176\n"
"s_cbranch_vccz .Lst24\nbuffer_store_dwordx4 a[12:15], v60, s[56:59], s68 offen offset:0 sc1\n.Lst24:\nQzvav 108,168,24\nQmvaa 108,172,176\ns_waitcnt lgkmcnt(0)\nQzvav 128,144,28\ns_waitcnt vmcnt(15)\n"
"s_barrier\nQmvaa 128,148,180\nQdv 24,63,0\ns_add_u32 m0, s62, 16512\ns_add_u32 s69, s87, 1048704\nQzvav 132,152,28\nbuffer_load_dwordx4 v62, s[52:55], s69 offen lds\nQmvaa 132,156,180\n"
"Qda 176,63,512\nQzvav 136,160,28\nQta 112,61,33024\nQmvaa 136,164,180\ns_add_u32 s69, s87, 1113208\nQzvav 140,168,28\nbuffer_load_dwordx4 v62, s[52:55], s69 offen offset:1032 lds\nQmvaa 140,172,180\n"
"Qdv 28,63,1024\nQzvav 160,144,32\nQda 180,63,1536\nQmvaa 160,148,184\nQta 114,61,33536\ns_add_u32 s69, s87, 1177712\nQzvav 164,152,32\nbuffer_load_dwordx4 v62, s[52:55], s69 offen offset:2064 lds\n"
"Qmvaa 164,156,184\nQta 116,61,37152\nQzvav 168,160,32\nQmvaa 168,164,184\nQta 118,61,37664\ns_add_u32 s69, s87, 1242216\nQzvav 172,168,32\nbuffer_load_dwordx4 v62, s[52:55], s69 offen offset:3096 lds\n"
"Qmvaa 172,172,184\nQdv 32,63,2048\nQzvav 192,144,36\nQda 184,63,2560\nQmvaa 192,148,188\nQta 120,61,33032\ns_add_u32 m0, s61, 0\nQzvav 196,152,36\n"
"s_cbranch_vccnz .Ld0q0\nbuffer_load_dwordx4 v69, s[40:43], s90 offen nt lds\n.Ld0q0:\nQmvaa 196,156,188\nQta 122,61,33544\nQzvav 200,160,36\nQmvaa 200,164,188\nQta 124,61,37160\n"
"Qzvav 204,168,36\nQdv 36,63,3072\nQmvaa 204,172,188\nQda 188,63,3584\nQzvav 224,144,40\nQta 126,61,37672\nQmvaa 224,148,192\nQzvav 228,152,40\n"
"Qta 128,61,33280\nQmvaa 228,156,192\nQzvav 232,160,40\nQta 130,61,33792\nQmvaa 232,164,192\nQzvav 236,168,40\nQdv 40,63,4096\nQmvaa 236,172,192\n"
"Qda 192,63,4608\nQzaav 208,144,44\nQta 132,61,37408\nQmaaa 208,148,196\nQzaav 212,152,44\nQta 134,61,37920\nQmaaa 212,156,196\nQzaav 216,160,44\n"
"Qta 136,61,33288\nQmaaa 216,164,196\nQzaav 220,168,44\nQdv 44,63,5120\nQmaaa 220,172,196\nQda 196,63,5632\nQzaav 240,144,48\nQta 138,61,33800\n"
"Qmaaa 240,148,200\nQzaav 244,152,48\nQta 140,61,37416\nQmaaa 244,156,200\nQzaav 248,160,48\nQta 142,61,37928\nQmaaa 248,164,200\nQzaav 252,168,48\n"
"Qdv 48,63,6144\nQmaaa 252,172,200\nQda 200,63,6656\ns_waitcnt lgkmcnt(2)\nQmvav 80,112,24\nQmvaa 80,116,176\ns_add_u32 s68, s71, 0\nQmvav 84,120,24\n"
"s_cbranch_vccz .Lst23\nbuffer_store_dwordx4 a[16:19], v60, s[56:59], s68 offen offset:1024 sc1\n.Lst23:\nQmvaa 84,124,176\nQmvav 88,128,24\ns_add_u32 s68, s71, 14336\nQmvaa 88,132,176\ns_cbranch_vccz .Lst22\n"
"buffer_store_dwordx4 a[20:23], v60, s[56:59], s68 offen offset:1024 sc1\n.Lpg0:\ns_cbranch_execz .Ltr0\n.Lst22:\nQmvav 92,136,24\nQmvaa 92,140,176\ns_waitcnt lgkmcnt(0)\nQmvav 112,112,28\n"
"s_waitcnt vmcnt(16)\ns_barrier\nQmvaa 112,116,180\nQta 144,61,49536\ns_add_u32 m0, s62, 33024\ns_add_u32 s69, s87, 1572864\nQmvav 116,120,28\nbuffer_load_dwordx4 v62, s[52:55], s69 offen lds\n"
"Qmvaa 116,124,180\nQta 146,61,50048\nQmvav 120,128,28\nQmvaa 120,132,180\nQta 148,61,53664\ns_add_u32 s69, s87, 1637368\nQmvav 124,136,28\nbuffer_load_dwordx4 v62, s[52:55], s69 offen offset:1032 lds\n"
"Qmvaa 124,140,180\nQta 150,61,54176\nQmvav 144,112,32\nQmvaa 144,116,184\nQta 152,61,49544\ns_add_u32 s69, s87, 1701872\nQmvav 148,120,32\nbuffer_load_dwordx4 v62, s[52:55], s69 offen offset:2064 lds\n"
"Qmvaa 148,124,184\nQta 154,61,50056\nQmvav 152,128,32\nQmvaa 152,132,184\nQta 156,61,53672\ns_add_u32 s69, s87, 1766376\nQmvav 156,136,32\nbuffer_load_dwordx4 v62, s[52:55], s69 offen offset:3096 lds\n"
"Qmvaa 156,140,184\nQta 158,61,54184\nQmvav 176,112,36\nQmvaa 176,116,188\nQta 160,61,49792\ns_add_u32 m0, s63, 0\ns_add_u32 s88, s86, 384\nQmvav 180,120,36\n"
"buffer_load_dwordx4 v64, s[48:51], s88 offen lds\nQmvaa 180,124,188\nQta 162,61,50304\nQmvav 184,128,36\nQmvaa 184,132,188\nQta 164,61,53920\ns_add_u32 s88, s86, 19840\nQmvav 188,136,36\n"
"buffer_load_dwordx4 v64, s[48:51], s88 offen offset:1024 lds\nQmvaa 188,140,188\nQta 166,61,54432\nQmvav 208,112,40\nQmvaa 208,116,192\nQta 168,61,49800\ns_add_u32 s88, s86, 39296\nQmvav 212,120,40\n"
"buffer_load_dwordx4 v64, s[48:51], s88 offen offset:2048 lds\nQmvaa 212,124,192\nQta 170,61,50312\nQmvav 216,128,40\nQmvaa 216,132,192\nQta 172,61,53928\ns_add_u32 s88, s86, 58752\nQmvav 220,136,40\n"
"buffer_load_dwordx4 v64, s[48:51], s88 offen offset:3072 lds\nQmvaa 220,140,192\nQta 174,61,54440\nQmvav 240,112,44\nQmvaa 240,116,196\ns_add_u32 m0, s63, 4096\ns_add_u32 s88, s86, 82304\nQmvav 244,120,44\n"
"buffer_load_dwordx4 v64, s[48:51], s88 offen lds\nQmvaa 244,124,196\nQmvav 248,128,44\nQmvaa 248,132,196\ns_add_u32 s88, s86, 101760\nQmvav 252,136,44\nbuffer_load_dwordx4 v64, s[48:51], s88 offen offset:1024 lds\nQmvaa 252,140,196\n"
"Qmaav 224,112,48\nQmaaa 224,116,200\ns_add_u32 s88, s86, 121216\nQmaav 228,120,48\nbuffer_load_dwordx4 v64, s[48:51], s88 offen offset:2048 lds\nQmaaa 228,124,200\nQmaav 232,128,48\nQmaaa 232,132,200\n"
"Qmaav 236,136,48\nQmaaa 236,140,200\ns_waitcnt lgkmcnt(0)\nQmvav 96,144,24\nQmvaa 96,148,176\ns_add_u32 s68, s71, 28672\nQmvav 100,152,24\ns_cbranch_vccz .Lst21\n"
"buffer_store_dwordx4 a[24:27], v60, s[56:59], s68 offen offset:1024 sc1\n.Lst21:\nQmvaa 100,156,176\nQmvav 104,160,24\ns_add_u32 s68, s71, 43008\nQmvaa 104,164,176\ns_cbranch_vccz .Lst20\nbuffer_store_dwordx4 a[28:31], v60, s[56:59], s68 offen offset:1024 sc1\n"
".Lst20:\nQmvav 108,168,24\nQmvaa 108,172,176\ns_waitcnt lgkmcnt(0)\nQmvav 128,144,28\ns_waitcnt vmcnt(16)\ns_barrier\nQmvaa 128,148,180\n"
"Qdv 24,63,33024\ns_add_u32 m0, s62, 49536\ns_add_u32 s69, s87, 1572992\nQmvav 132,152,28\nbuffer_load_dwordx4 v62, s[52:55], s69 offen lds\nQmvaa 132,156,180\nQda 176,63,33536\nQmvav 136,160,28\n"
"Qta 112,61,0\nQmvaa 136,164,180\ns_add_u32 s69, s87, 1637496\nQmvav 140,168,28\nbuffer_load_dwordx4 v62, s[52:55], s69 offen offset:1032 lds\nQmvaa 140,172,180\nQdv 28,63,34048\nQmvav 160,144,32\n"
"Qda 180,63,34560\nQmvaa 160,148,184\nQta 114,61,512\ns_add_u32 s69, s87, 1702000\nQmvav 164,152,32\nbuffer_load_dwordx4 v62, s[52:55], s69 offen offset:2064 lds\nQmvaa 164,156,184\nQta 116,61,4128\n"
"Qmvav 168,160,32\nQmvaa 168,164,184\nQta 118,61,4640\ns_add_u32 s69, s87, 1766504\nQmvav 172,168,32\nbuffer_load_dwordx4 v62, s[52:55], s69 offen offset:3096 lds\nQmvaa 172,172,184\nQdv 32,63,35072\n"
"Qmvav 192,144,36\nQda 184,63,35584\nQmvaa 192,148,188\nQta 120,61,8\ns_add_u32 m0, s61, 0\nQmvav 196,152,36\ns_cbranch_vccnz .Ld0q1\nbuffer_load_dwordx4 v69, s[40:43], s60 offen offset:1024 nt lds\n"
".Ld0q1:\nQmvaa 196,156,188\nQta 122,61,520\nQmvav 200,160,36\nQmvaa 200,164,188\nQta 124,61,4136\nQmvav 204,168,36\nQdv 36,63,36096\n"
"Qmvaa 204,172,188\nQda 188,63,36608\nQmvav 224,144,40\nQta 126,61,4648\nQmvaa 224,148,192\nQmvav 228,152,40\nQta 128,61,256\nQmvaa 228,156,192\n"
"Qmvav 232,160,40\nQta 130,61,768\nQmvaa 232,164,192\nQmvav 236,168,40\nQdv 40,63,37120\nQmvaa 236,172,192\nQda 192,63,37632\nQmaav 208,144,44\n"
"Qta 132,61,4384\nQmaaa 208,148,196\nQmaav 212,152,44\nQta 134,61,4896\nQmaaa 212,156,196\nQmaav 216,160,44\nQta 136,61,264\nQmaaa 216,164,196\n"
"Qmaav 220,168,44\nQdv 44,63,38144\nQmaaa 220,172,196\nQda 196,63,38656\nQmaav 240,144,48\nQta 138,61,776\nQmaaa 240,148,200\nQmaav 244,152,48\n"
"Qta 140,61,4392\nQmaaa 244,156,200\nQmaav 248,160,48\nQta 142,61,4904\nQmaaa 248,164,200\nQmaav 252,168,48\nQdv 48,63,39168\nQmaaa 252,172,200\n"
"Qda 200,63,39680\ns_waitcnt lgkmcnt(2)\nQmvav 80,112,24\nQmvaa 80,116,176\ns_add_u32 s68, s71, 0\nQmvav 84,120,24\ns_cbranch_vccz .Lst19\nbuffer_store_dwordx4 a[32:35], v60, s[56:59], s68 offen offset:2048 sc1\n"
".Lst19:\nQmvaa 84,124,176\nQmvav 88,128,24\ns_add_u32 s68, s71, 14336\nQmvaa 88,132,176\ns_cbranch_vccz .Lst18\nbuffer_store_dwordx4 a[36:39], v60, s[56:59], s68 offen offset:2048 sc1\n.Lst18:\n"
"Qmvav 92,136,24\nQmvaa 92,140,176\ns_waitcnt lgkmcnt(0)\nQmvav 112,112,28\ns_waitcnt vmcnt(17)\ns_barrier\nQmvaa 112,116,180\nQta 144,61,16512\n"
"s_add_u32 m0, s62, 0\ns_add_u32 s69, s87, 2097152\nQmvav 116,120,28\nbuffer_load_dwordx4 v62, s[52:55], s69 offen lds\nQmvaa 116,124,180\nQta 146,61,17024\nQmvav 120,128,28\nQmvaa 120,132,180\n"
"Qta 148,61,20640\ns_add_u32 s69, s87, 2161656\nQmvav 124,136,28\nbuffer_load_dwordx4 v62, s[52:55], s69 offen offset:1032 lds\nQmvaa 124,140,180\nQta 150,61,21152\nQmvav 144,112,32\nQmvaa 144,116,184\n"
"Qta 152,61,16520\ns_add_u32 s69, s87, 2226160\nQmvav 148,120,32\nbuffer_load_dwordx4 v62, s[52:55], s69 offen offset:2064 lds\nQmvaa 148,124,184\nQta 154,61,17032\nQmvav 152,128,32\nQmvaa 152,132,184\n"
"Qta 156,61,20648\ns_add_u32 s69, s87, 2290664\nQmvav 156,136,32\nbuffer_load_dwordx4 v62, s[52:55], s69 offen offset:3096 lds\nQmvaa 156,140,184\nQta 158,61,21160\nQmvav 176,112,36\nQmvaa 176,116,188\n"
"Qta 160,61,16768\ns_add_u32 m0, s63, 33024\ns_add_u32 s88, s86, 512\nQmvav 180,120,36\nbuffer_load_dwordx4 v64, s[48:51], s88 offen lds\nQmvaa 180,124,188\nQta 162,61,17280\nQmvav 184,128,36\n"
"Qmvaa 184,132,188\nQta 164,61,20896\ns_add_u32 s88, s86, 19968\nQmvav 188,136,36\nbuffer_load_dwordx4 v64, s[48:51], s88 offen offset:1024 lds\nQmvaa 188,140,188\nQta 166,61,21408\nQmvav 208,112,40\n"
"Qmvaa 208,116,192\nQta 168,61,16776\ns_add_u32 s88, s86, 39424\nQmvav 212,120,40\nbuffer_load_dwordx4 v64, s[48:51], s88 offen offset:2048 lds\nQmvaa 212,124,192\nQta 170,61,17288\nQmvav 216,128,40\n"
"Qmvaa 216,132,192\nQta 172,61,20904\ns_add_u32 s88, s86, 58880\nQmvav 220,136,40\nbuffer_load_dwordx4 v64, s[48:51], s88 offen offset:3072 lds\nQmvaa 220,140,192\nQta 174,61,21416\nQmvav 240,112,44\n"
"Qmvaa 240,116,196\ns_add_u32 m0, s63, 37120\ns_add_u32 s88, s86, 82432\nQmvav 244,120,44\nbuffer_load_dwordx4 v64, s[48:51], s88 offen lds\nQmvaa 244,124,196\nQmvav 248,128,44\nQmvaa 248,132,196\n"
"s_add_u32 s88, s86, 101888\nQmvav 252,136,44\nbuffer_load_dwordx4 v64, s[48:51], s88 offen offset:1024 lds\nQmvaa 252,140,196\nQmaav 224,112,48\nQmaaa 224,116,200\ns_add_u32 s88, s86, 121344\nQmaav 228,120,48\n"
"buffer_load_dwordx4 v64, s[48:51], s88 offen offset:2048 lds\nQmaaa 228,124,200\nQmaav 232,128,48\nQmaaa 232,132,200\nQmaav 236,136,48\nQmaaa 236,140,200\ns_waitcnt lgkmcnt(0)\nQmvav 96,144,24\n"
"Qmvaa 96,148,176\ns_add_u32 s68, s71, 28672\nQmvav 100,152,24\ns_cbranch_vccz .Lst17\nbuffer_store_dwordx4 a[40:43], v60, s[56:59], s68 offen offset:2048 sc1\n.Lst17:\nQmvaa 100,156,176\nQmvav 104,160,24\n"
"s_add_u32 s68, s71, 43008\nQmvaa 104,164,176\ns_cbranch_vccz .Lst16\nbuffer_store_dwordx4 a[44:47], v60, s[56:59], s68 offen offset:2048 sc1\n.Lst16:\nQmvav 108,168,24\nQmvaa 108,172,176\ns_waitcnt lgkmcnt(0)\n"
"Qmvav 128,144,28\ns_waitcnt vmcnt(16)\ns_barrier\nQmvaa 128,148,180\nQdv 24,63,0\ns_add_u32 m0, s62, 16512\ns_add_u32 s69, s87, 2097280\nQmvav 132,152,28\n"
"buffer_load_dwordx4 v62, s[52:55], s69 offen lds\nQmvaa 132,156,180\nQda 176,63,512\nQmvav 136,160,28\nQta 112,61,33024\nQmvaa 136,164,180\ns_add_u32 s69, s87, 2161784\nQmvav 140,168,28\n"
"buffer_load_dwordx4 v62, s[52:55], s69 offen offset:1032 lds\nQmvaa 140,172,180\nQdv 28,63,1024\nQmvav 160,144,32\nQda 180,63,1536\nQmvaa 160,148,184\nQta 114,61,33536\ns_add_u32 s69, s87, 2226288\n"
"Qmvav 164,152,32\nbuffer_load_dwordx4 v62, s[52:55], s69 offen offset:2064 lds\nQmvaa 164,156,184\nQta 116,61,37152\nQmvav 168,160,32\nQmvaa 168,164,184\nQta 118,61,37664\ns_add_u32 s69, s87, 2290792\n"
"Qmvav 172,168,32\nbuffer_load_dwordx4 v62, s[52:55], s69 offen offset:3096 lds\nQmvaa 172,172,184\nQdv 32,63,2048\nQmvav 192,144,36\nQda 184,63,2560\nQmvaa 192,148,188\nQta 120,61,33032\n"
"s_add_u32 m0, s61, 0\nQmvav 196,152,36\ns_cbranch_vccnz .Ld0q2\nbuffer_load_dwordx4 v69, s[40:43], s90 offen offset:2048 nt lds\n.Ld0q2:\nQmvaa 196,156,188\nQta 122,61,33544\nQmvav 200,160,36\n"
"Qmvaa 200,164,188\nQta 124,61,37160\nQmvav 204,168,36\nQdv 36,63,3072\nQmvaa 204,172,188\nQda 188,63,3584\nQmvav 224,144,40\nQta 126,61,37672\n"
"Qmvaa 224,148,192\nQmvav 228,152,40\nQta 128,61,33280\nQmvaa 228,156,192\nQmvav 232,160,40\nQta 130,61,33792\nQmvaa 232,164,192\nQmvav 236,168,40\n"
"Qdv 40,63,4096\nQmvaa 236,172,192\nQda 192,63,4608\nQmaav 208,144,44\nQta 132,61,37408\nQmaaa 208,148,196\nQmaav 212,152,44\nQta 134,61,37920\n"
"Qmaaa 212,156,196\nQmaav 216,160,44\nQta 136,61,33288\nQmaaa 216,164,196\nQmaav 220,168,44\nQdv 44,63,5120\nQmaaa 220,172,196\nQda 196,63,5632\n"
"Qmaav 240,144,48\nQta 138,61,33800\nQmaaa 240,148,200\nQmaav 244,152,48\nQta 140,61,37416\nQmaaa 244,156,200\nQmaav 248,160,48\nQta 142,61,37928\n"
"Qmaaa 248,164,200\nQmaav 252,168,48\nQdv 48,63,6144\nQmaaa 252,172,200\nQda 200,63,6656\ns_waitcnt lgkmcnt(2)\nQmvav 80,112,24\nQmvaa 80,116,176\n"
"s_add_u32 s68, s71, 0\nQmvav 84,120,24\ns_cbranch_vccz .Lst15\nbuffer_store_dwordx4 a[48:51], v60, s[56:59], s68 offen offset:3072 sc1\n.Lst15:\nQmvaa 84,124,176\nQmvav 88,128,24\ns_add_u32 s68, s71, 14336\n"
"Qmvaa 88,132,176\ns_cbranch_vccz .Lst14\nbuffer_store_dwordx4 a[52:55], v60, s[56:59], s68 offen offset:3072 sc1\n.Lst14:\nQmvav 92,136,24\nQmvaa 92,140,176\ns_waitcnt lgkmcnt(0)\nQmvav 112,112,28\n"
"s_waitcnt vmcnt(17)\ns_barrier\nQmvaa 112,116,180\nQta 144,61,49536\ns_add_u32 m0, s62, 33024\ns_add_u32 s69, s87, 2621440\nQmvav 116,120,28\nbuffer_load_dwordx4 v62, s[52:55], s69 offen lds\n"
"Qmvaa 116,124,180\nQta 146,61,50048\nQmvav 120,128,28\nQmvaa 120,132,180\nQta 148,61,53664\ns_add_u32 s69, s87, 2685944\nQmvav 124,136,28\nbuffer_load_dwordx4 v62, s[52:55], s69 offen offset:1032 lds\n"
"Qmvaa 124,140,180\nQta 150,61,54176\nQmvav 144,112,32\nQmvaa 144,116,184\nQta 152,61,49544\ns_add_u32 s69, s87, 2750448\nQmvav 148,120,32\nbuffer_load_dwordx4 v62, s[52:55], s69 offen offset:2064 lds\n"
"Qmvaa 148,124,184\nQta 154,61,50056\nQmvav 152,128,32\nQmvaa 152,132,184\nQta 156,61,53672\ns_add_u32 s69, s87, 2814952\nQmvav 156,136,32\nbuffer_load_dwordx4 v62, s[52:55], s69 offen offset:3096 lds\n"
"Qmvaa 156,140,184\nQta 158,61,54184\nQmvav 176,112,36\nQmvaa 176,116,188\nQta 160,61,49792\ns_add_u32 m0, s63, 0\ns_add_u32 s88, s86, 640\nQmvav 180,120,36\n"
"buffer_load_dwordx4 v64, s[48:51], s88 offen lds\nQmvaa 180,124,188\nQta 162,61,50304\nQmvav 184,128,36\nQmvaa 184,132,188\nQta 164,61,53920\ns_add_u32 s88, s86, 20096\nQmvav 188,136,36\n"
"buffer_load_dwordx4 v64, s[48:51], s88 offen offset:1024 lds\nQmvaa 188,140,188\nQta 166,61,54432\nQmvav 208,112,40\nQmvaa 208,116,192\nQta 168,61,49800\ns_add_u32 s88, s86, 39552\nQmvav 212,120,40\n"
"buffer_load_dwordx4 v64, s[48:51], s88 offen offset:2048 lds\nQmvaa 212,124,192\nQta 170,61,50312\nQmvav 216,128,40\nQmvaa 216,132,192\nQta 172,61,53928\ns_add_u32 s88, s86, 59008\nQmvav 220,136,40\n"
"buffer_load_dwordx4 v64, s[48:51], s88 offen offset:3072 lds\nQmvaa 220,140,192\nQta 174,61,54440\nQmvav 240,112,44\nQmvaa 240,116,196\ns_add_u32 m0, s63, 4096\ns_add_u32 s88, s86, 82560\nQmvav 244,120,44\n"
"buffer_load_dwordx4 v64, s[48:51], s88 offen lds\nQmvaa 244,124,196\nQmvav 248,128,44\nQmvaa 248,132,196\ns_add_u32 s88, s86, 102016\nQmvav 252,136,44\nbuffer_load_dwordx4 v64, s[48:51], s88 offen offset:1024 lds\nQmvaa 252,140,196\n"
"Qmaav 224,112,48\nQmaaa 224,116,200\ns_add_u32 s88, s86, 121472\nQmaav 228,120,48\nbuffer_load_dwordx4 v64, s[48:51], s88 offen offset:2048 lds\nQmaaa 228,124,200\nQmaav 232,128,48\nQmaaa 232,132,200\n"
"Qmaav 236,136,48\nQmaaa 236,140,200\ns_waitcnt lgkmcnt(0)\nQmvav 96,144,24\nQmvaa 96,148,176\ns_add_u32 s68, s71, 28672\nQmvav 100,152,24\ns_cbranch_vccz .Lst13\n"
"buffer_store_dwordx4 a[56:59], v60, s[56:59], s68 offen offset:3072 sc1\n.Lpg1:\ns_cbranch_execz .Ltr0\n.Lst13:\nQmvaa 100,156,176\nQmvav 104,160,24\ns_add_u32 s68, s71, 43008\nQmvaa 104,164,176\n"
"s_cbranch_vccz .Lst12\nbuffer_store_dwordx4 a[60:63], v60, s[56:59], s68 offen offset:3072 sc1\n.Lst12:\nQmvav 108,168,24\nQmvaa 108,172,176\ns_waitcnt lgkmcnt(0)\nQmvav 128,144,28\ns_waitcnt vmcnt(16)\n"
"s_barrier\nQmvaa 128,148,180\nQdv 24,63,33024\ns_add_u32 m0, s62, 49536\ns_add_u32 s69, s87, 2621568\nQmvav 132,152,28\nbuffer_load_dwordx4 v62, s[52:55], s69 offen lds\nQmvaa 132,156,180\n"
"Qda 176,63,33536\nQmvav 136,160,28\nQta 112,61,0\nQmvaa 136,164,180\ns_add_u32 s69, s87, 2686072\nQmvav 140,168,28\nbuffer_load_dwordx4 v62, s[52:55], s69 offen offset:1032 lds\nQmvaa 140,172,180\n"
"Qdv 28,63,34048\nQmvav 160,144,32\nQda 180,63,34560\nQmvaa 160,148,184\nQta 114,61,512\ns_add_u32 s69, s87, 2750576\nQmvav 164,152,32\nbuffer_load_dwordx4 v62, s[52:55], s69 offen offset:2064 lds\n"
"Qmvaa 164,156,184\nQta 116,61,4128\nQmvav 168,160,32\nQmvaa 168,164,184\nQta 118,61,4640\ns_add_u32 s69, s87, 2815080\nQmvav 172,168,32\nbuffer_load_dwordx4 v62, s[52:55], s69 offen offset:3096 lds\n"
"Qmvaa 172,172,184\nQdv 32,63,35072\nQmvav 192,144,36\nQda 184,63,35584\nQmvaa 192,148,188\nQta 120,61,8\ns_add_u32 m0, s61, 0\nQmvav 196,152,36\n"
"s_cbranch_vccnz .Ld0q3\nbuffer_load_dwordx4 v69, s[40:43], s60 offen offset:3072 nt lds\n.Ld0q3:\nQmvaa 196,156,188\nQta 122,61,520\nQmvav 200,160,36\nQmvaa 200,164,188\nQta 124,61,4136\n"
"Qmvav 204,168,36\nQdv 36,63,36096\nQmvaa 204,172,188\nQda 188,63,36608\nQmvav 224,144,40\nQta 126,61,4648\nQmvaa 224,148,192\nQmvav 228,152,40\n"
"Qta 128,61,256\nQmvaa 228,156,192\nQmvav 232,160,40\nQta 130,61,768\nQmvaa 232,164,192\nQmvav 236,168,40\nQdv 40,63,37120\nQmvaa 236,172,192\n"
"Qda 192,63,37632\nQmaav 208,144,44\nQta 132,61,4384\nQmaaa 208,148,196\nQmaav 212,152,44\nQta 134,61,4896\nQmaaa 212,156,196\nQmaav 216,160,44\n"
"Qta 136,61,264\nQmaaa 216,164,196\nQmaav 220,168,44\nQdv 44,63,38144\nQmaaa 220,172,196\nQda 196,63,38656\nQmaav 240,144,48\nQta 138,61,776\n"
"Qmaaa 240,148,200\nQmaav 244,152,48\nQta 140,61,4392\nQmaaa 244,156,200\nQmaav 248,160,48\nQta 142,61,4904\nQmaaa 248,164,200\nQmaav 252,168,48\n"
"Qdv 48,63,39168\nQmaaa 252,172,200\nQda 200,63,39680\ns_waitcnt lgkmcnt(2)\nQmvav 80,112,24\nQmvaa 80,116,176\ns_add_u32 s68, s71, 4096\nQmvav 84,120,24\n"
"s_cbranch_vccz .Lst11\nbuffer_store_dwordx4 a[64:67], v60, s[56:59], s68 offen offset:0 sc1\n.Lst11:\nQmvaa 84,124,176\nQmvav 88,128,24\ns_add_u32 s68, s71, 18432\nQmvaa 88,132,176\ns_cbranch_vccz .Lst10\n"
"buffer_store_dwordx4 a[68:71], v60, s[56:59], s68 offen offset:0 sc1\n.Lst10:\nQmvav 92,136,24\nQmvaa 92,140,176\ns_waitcnt lgkmcnt(0)\nQmvav 112,112,28\ns_waitcnt vmcnt(17)\ns_barrier\n"
"Qmvaa 112,116,180\nQta 144,61,16512\ns_add_u32 m0, s62, 0\ns_add_u32 s69, s87, 3145728\nQmvav 116,120,28\nbuffer_load_dwordx4 v62, s[52:55], s69 offen lds\nQmvaa 116,124,180\nQta 146,61,17024\n"
"Qmvav 120,128,28\nQmvaa 120,132,180\nQta 148,61,20640\ns_add_u32 s69, s87, 3210232\nQmvav 124,136,28\nbuffer_load_dwordx4 v62, s[52:55], s69 offen offset:1032 lds\nQmvaa 124,140,180\nQta 150,61,21152\n"
"Qmvav 144,112,32\nQmvaa 144,116,184\nQta 152,61,16520\ns_add_u32 s69, s87, 3274736\nQmvav 148,120,32\nbuffer_load_dwordx4 v62, s[52:55], s69 offen offset:2064 lds\nQmvaa 148,124,184\nQta 154,61,17032\n"
"Qmvav 152,128,32\nQmvaa 152,132,184\nQta 156,61,20648\ns_add_u32 s69, s87, 3339240\nQmvav 156,136,32\nbuffer_load_dwordx4 v62, s[52:55], s69 offen offset:3096 lds\nQmvaa 156,140,184\nQta 158,61,21160\n"
"Qmvav 176,112,36\nQmvaa 176,116,188\nQta 160,61,16768\ns_add_u32 m0, s63, 33024\ns_add_u32 s88, s86, 768\nQmvav 180,120,36\nbuffer_load_dwordx4 v64, s[48:51], s88 offen lds\nQmvaa 180,124,188\n"
"Qta 162,61,17280\nQmvav 184,128,36\nQmvaa 184,132,188\nQta 164,61,20896\ns_add_u32 s88, s86, 20224\nQmvav 188,136,36\nbuffer_load_dwordx4 v64, s[48:51], s88 offen offset:1024 lds\nQmvaa 188,140,188\n"
"Qta 166,61,21408\nQmvav 208,112,40\nQmvaa 208,116,192\nQta 168,61,16776\ns_add_u32 s88, s86, 39680\nQmvav 212,120,40\nbuffer_load_dwordx4 v64, s[48:51], s88 offen offset:2048 lds\nQmvaa 212,124,192\n"
"Qta 170,61,17288\nQmvav 216,128,40\nQmvaa 216,132,192\nQta 172,61,20904\ns_add_u32 s88, s86, 59136\nQmvav 220,136,40\nbuffer_load_dwordx4 v64, s[48:51], s88 offen offset:3072 lds\nQmvaa 220,140,192\n"
"Qta 174,61,21416\nQmvav 240,112,44\nQmvaa 240,116,196\ns_add_u32 m0, s63, 37120\ns_add_u32 s88, s86, 82688\nQmvav 244,120,44\nbuffer_load_dwordx4 v64, s[48:51], s88 offen lds\nQmvaa 244,124,196\n"
"Qmvav 248,128,44\nQmvaa 248,132,196\ns_add_u32 s88, s86, 102144\nQmvav 252,136,44\nbuffer_load_dwordx4 v64, s[48:51], s88 offen offset:1024 lds\nQmvaa 252,140,196\nQmaav 224,112,48\nQmaaa 224,116,200\n"
"s_add_u32 s88, s86, 121600\nQmaav 228,120,48\nbuffer_load_dwordx4 v64, s[48:51], s88 offen offset:2048 lds\nQmaaa 228,124,200\nQmaav 232,128,48\nQmaaa 232,132,200\nQmaav 236,136,48\nQmaaa 236,140,200\n"
"s_waitcnt lgkmcnt(0)\nQmvav 96,144,24\nQmvaa 96,148,176\ns_add_u32 s68, s71, 32768\nQmvav 100,152,24\ns_cbranch_vccz .Lst9\nbuffer_store_dwordx4 a[72:75], v60, s[56:59], s68 offen offset:0 sc1\n.Lst9:\n"
"Qmvaa 100,156,176\nQmvav 104,160,24\ns_add_u32 s68, s71, 47104\nQmvaa 104,164,176\ns_cbranch_vccz .Lst8\nbuffer_store_dwordx4 a[76:79], v60, s[56:59], s68 offen offset:0 sc1\n.Lst8:\nQmvav 108,168,24\n"
"Qmvaa 108,172,176\ns_waitcnt lgkmcnt(0)\nQmvav 128,144,28\ns_waitcnt vmcnt(16)\ns_barrier\nQmvaa 128,148,180\nQdv 24,63,0\ns_add_u32 m0, s62, 16512\n"
"s_add_u32 s69, s87, 3145856\nQmvav 132,152,28\nbuffer_load_dwordx4 v62, s[52:55], s69 offen lds\nQmvaa 132,156,180\nQda 176,63,512\nQmvav 136,160,28\nQta 112,61,33024\nQmvaa 136,164,180\n"
"s_add_u32 s69, s87, 3210360\nQmvav 140,168,28\nbuffer_load_dwordx4 v62, s[52:55], s69 offen offset:1032 lds\nQmvaa 140,172,180\nQdv 28,63,1024\nQmvav 160,144,32\nQda 180,63,1536\nQmvaa 160,148,184\n"
"Qta 114,61,33536\ns_add_u32 s69, s87, 3274864\nQmvav 164,152,32\nbuffer_load_dwordx4 v62, s[52:55], s69 offen offset:2064 lds\nQmvaa 164,156,184\nQta 116,61,37152\nQmvav 168,160,32\nQmvaa 168,164,184\n"
"Qta 118,61,37664\ns_add_u32 s69, s87, 3339368\nQmvav 172,168,32\nbuffer_load_dwordx4 v62, s[52:55], s69 offen offset:3096 lds\nQmvaa 172,172,184\nQdv 32,63,2048\nQmvav 192,144,36\nQda 184,63,2560\n"
"Qmvaa 192,148,188\nQta 120,61,33032\ns_add_u32 m0, s61, 8192\nQmvav 196,152,36\ns_cbranch_vccnz .Ld0q4\nbuffer_load_dwordx4 v72, s[44:47], s90 offen nt lds\n.Ld0q4:\nQmvaa 196,156,188\n"
"Qta 122,61,33544\nQmvav 200,160,36\nQmvaa 200,164,188\nQta 124,61,37160\nQmvav 204,168,36\nQdv 36,63,3072\nQmvaa 204,172,188\nQda 188,63,3584\n"
"Qmvav 224,144,40\nQta 126,61,37672\nQmvaa 224,148,192\nQmvav 228,152,40\nQta 128,61,33280\nQmvaa 228,156,192\nQmvav 232,160,40\nQta 130,61,33792\n"
"Qmvaa 232,164,192\nQmvav 236,168,40\nQdv 40,63,4096\nQmvaa 236,172,192\nQda 192,63,4608\nQmaav 208,144,44\nQta 132,61,37408\nQmaaa 208,148,196\n"
"Qmaav 212,152,44\nQta 134,61,37920\nQmaaa 212,156,196\nQmaav 216,160,44\nQta 136,61,33288\nQmaaa 216,164,196\nQmaav 220,168,44\nQdv 44,63,5120\n"
"Qmaaa 220,172,196\nQda 196,63,5632\nQmaav 240,144,48\nQta 138,61,33800\nQmaaa 240,148,200\nQmaav 244,152,48\nQta 140,61,37416\nQmaaa 244,156,200\n"
"Qmaav 248,160,48\nQta 142,61,37928\nQmaaa 248,164,200\nQmaav 252,168,48\nQdv 48,63,6144\nQmaaa 252,172,200\nQda 200,63,6656\ns_waitcnt lgkmcnt(2)\n"
"Qmvav 80,112,24\nQmvaa 80,116,176\ns_add_u32 s68, s71, 4096\nQmvav 84,120,24\ns_cbranch_vccz .Lst7\nbuffer_store_dwordx4 a[80:83], v60, s[56:59], s68 offen offset:1024 sc1\n.Lst7:\nQmvaa 84,124,176\n"
"Qmvav 88,128,24\ns_add_u32 s68, s71, 18432\nQmvaa 88,132,176\ns_cbranch_vccz .Lst6\nbuffer_store_dwordx4 a[84:87], v60, s[56:59], s68 offen offset:1024 sc1\n.Lst6:\nQmvav 92,136,24\nQmvaa 92,140,176\n"
"s_waitcnt lgkmcnt(0)\nQmvav 112,112,28\ns_waitcnt vmcnt(17)\ns_barrier\nQmvaa 112,116,180\nQta 144,61,49536\ns_add_u32 m0, s62, 33024\ns_add_u32 s69, s87, 3670016\n"
"Qmvav 116,120,28\nbuffer_load_dwordx4 v62, s[52:55], s69 offen lds\nQmvaa 116,124,180\nQta 146,61,50048\nQmvav 120,128,28\nQmvaa 120,132,180\nQta 148,61,53664\ns_add_u32 s69, s87, 3734520\n"
"Qmvav 124,136,28\nbuffer_load_dwordx4 v62, s[52:55], s69 offen offset:1032 lds\nQmvaa 124,140,180\nQta 150,61,54176\nQmvav 144,112,32\nQmvaa 144,116,184\nQta 152,61,49544\ns_add_u32 s69, s87, 3799024\n"
"Qmvav 148,120,32\nbuffer_load_dwordx4 v62, s[52:55], s69 offen offset:2064 lds\nQmvaa 148,124,184\nQta 154,61,50056\nQmvav 152,128,32\nQmvaa 152,132,184\nQta 156,61,53672\ns_add_u32 s69, s87, 3863528\n"
"Qmvav 156,136,32\nbuffer_load_dwordx4 v62, s[52:55], s69 offen offset:3096 lds\nQmvaa 156,140,184\nQta 158,61,54184\nQmvav 176,112,36\nQmvaa 176,116,188\nQta 160,61,49792\ns_add_u32 m0, s63, 0\n"
"s_add_u32 s88, s86, 896\nQmvav 180,120,36\nbuffer_load_dwordx4 v64, s[48:51], s88 offen lds\nQmvaa 180,124,188\nQta 162,61,50304\nQmvav 184,128,36\nQmvaa 184,132,188\nQta 164,61,53920\n"
"s_add_u32 s88, s86, 20352\nQmvav 188,136,36\nbuffer_load_dwordx4 v64, s[48:51], s88 offen offset:1024 lds\nQmvaa 188,140,188\nQta 166,61,54432\nQmvav 208,112,40\nQmvaa 208,116,192\nQta 168,61,49800\n"
"s_add_u32 s88, s86, 39808\nQmvav 212,120,40\nbuffer_load_dwordx4 v64, s[48:51], s88 offen offset:2048 lds\nQmvaa 212,124,192\nQta 170,61,50312\nQmvav 216,128,40\nQmvaa 216,132,192\nQta 172,61,53928\n"
"s_add_u32 s88, s86, 59264\nQmvav 220,136,40\nbuffer_load_dwordx4 v64, s[48:51], s88 offen offset:3072 lds\nQmvaa 220,140,192\nQta 174,61,54440\nQmvav 240,112,44\nQmvaa 240,116,196\ns_add_u32 m0, s63, 4096\n"
"s_add_u32 s88, s86, 82816\nQmvav 244,120,44\nbuffer_load_dwordx4 v64, s[48:51], s88 offen lds\nQmvaa 244,124,196\nQmvav 248,128,44\nQmvaa 248,132,196\ns_add_u32 s88, s86, 102272\nQmvav 252,136,44\n"
"buffer_load_dwordx4 v64, s[48:51], s88 offen offset:1024 lds\nQmvaa 252,140,196\nQmaav 224,112,48\nQmaaa 224,116,200\ns_add_u32 s88, s86, 121728\nQmaav 228,120,48\nbuffer_load_dwordx4 v64, s[48:51], s88 offen offset:2048 lds\nQmaaa 228,124,200\n"
"Qmaav 232,128,48\nQmaaa 232,132,200\nQmaav 236,136,48\nQmaaa 236,140,200\ns_waitcnt lgkmcnt(0)\nQmvav 96,144,24\nQmvaa 96,148,176\ns_add_u32 s68, s71, 32768\n"
"Qmvav 100,152,24\ns_cbranch_vccz .Lst5\nbuffer_store_dwordx4 a[88:91], v60, s[56:59], s68 offen offset:1024 sc1\n.Lst5:\nQmvaa 100,156,176\nQmvav 104,160,24\ns_add_u32 s68, s71, 47104\nQmvaa 104,164,176\n"
"s_cbranch_vccz .Lst4\nbuffer_store_dwordx4 a[92:95], v60, s[56:59], s68 offen offset:1024 sc1\n.Lst4:\nQmvav 108,168,24\nQmvaa 108,172,176\ns_waitcnt lgkmcnt(0)\nQmvav 128,144,28\ns_waitcnt vmcnt(16)\n"
"s_barrier\nQmvaa 128,148,180\nQdv 24,63,33024\ns_add_u32 m0, s62, 49536\ns_add_u32 s69, s87, 3670144\nQmvav 132,152,28\nbuffer_load_dwordx4 v62, s[52:55], s69 offen lds\nQmvaa 132,156,180\n"
"Qda 176,63,33536\nQmvav 136,160,28\nQta 112,61,0\nQmvaa 136,164,180\ns_add_u32 s69, s87, 3734648\nQmvav 140,168,28\nbuffer_load_dwordx4 v62, s[52:55], s69 offen offset:1032 lds\nQmvaa 140,172,180\n"
"Qdv 28,63,34048\nQmvav 160,144,32\nQda 180,63,34560\nQmvaa 160,148,184\nQta 114,61,512\ns_add_u32 s69, s87, 3799152\nQmvav 164,152,32\nbuffer_load_dwordx4 v62, s[52:55], s69 offen offset:2064 lds\n"
"Qmvaa 164,156,184\nQta 116,61,4128\nQmvav 168,160,32\nQmvaa 168,164,184\nQta 118,61,4640\ns_add_u32 s69, s87, 3863656\nQmvav 172,168,32\nbuffer_load_dwordx4 v62, s[52:55], s69 offen offset:3096 lds\n"
"Qmvaa 172,172,184\nQdv 32,63,35072\nQmvav 192,144,36\nQda 184,63,35584\nQmvaa 192,148,188\nQta 120,61,8\ns_add_u32 m0, s61, 8192\nQmvav 196,152,36\n"
"s_cbranch_vccnz .Ld0q5\nbuffer_load_dwordx4 v72, s[44:47], s60 offen offset:1024 nt lds\n.Ld0q5:\nQmvaa 196,156,188\nQta 122,61,520\nQmvav 200,160,36\nQmvaa 200,164,188\nQta 124,61,4136\n"
"Qmvav 204,168,36\nQdv 36,63,36096\nQmvaa 204,172,188\nQda 188,63,36608\nQmvav 224,144,40\nQta 126,61,4648\nQmvaa 224,148,192\nQmvav 228,152,40\n"
"Qta 128,61,256\nQmvaa 228,156,192\nQmvav 232,160,40\nQta 130,61,768\nQmvaa 232,164,192\nQmvav 236,168,40\nQdv 40,63,37120\nQmvaa 236,172,192\n"
"Qda 192,63,37632\nQmaav 208,144,44\nQta 132,61,4384\nQmaaa 208,148,196\nQmaav 212,152,44\nQta 134,61,4896\nQmaaa 212,156,196\nQmaav 216,160,44\n"
"Qta 136,61,264\nQmaaa 216,164,196\nQmaav 220,168,44\nQdv 44,63,38144\nQmaaa 220,172,196\nQda 196,63,38656\nQmaav 240,144,48\nQta 138,61,776\n"
"Qmaaa 240,148,200\nQmaav 244,152,48\nQta 140,61,4392\nQmaaa 244,156,200\nQmaav 248,160,48\nQta 142,61,4904\nQmaaa 248,164,200\nQmaav 252,168,48\n"
".Lpg2:\ns_cbranch_execz .Ltr0\nQdv 48,63,39168\nQmaaa 252,172,200\nQda 200,63,39680\ns_waitcnt lgkmcnt(2)\nQmvav 80,112,24\nQmvaa 80,116,176\n"
"s_add_u32 s68, s71, 4096\nQmvav 84,120,24\ns_cbranch_vccz .Lst3\nbuffer_store_dwordx4 a[96:99], v60, s[56:59], s68 offen offset:2048 sc1\n.Lst3:\nQmvaa 84,124,176\nQmvav 88,128,24\ns_add_u32 s68, s71, 18432\n"
"Qmvaa 88,132,176\ns_cbranch_vccz .Lst2\nbuffer_store_dwordx4 a[100:103], v60, s[56:59], s68 offen offset:2048 sc1\n.Lst2:\nQmvav 92,136,24\nQmvaa 92,140,176\ns_waitcnt lgkmcnt(0)\nQmvav 112,112,28\n"
"s_waitcnt vmcnt(17)\ns_barrier\nQmvaa 112,116,180\nQta 144,61,16512\ns_add_u32 m0, s62, 0\ns_add_u32 s69, s87, 4194304\nQmvav 116,120,28\nbuffer_load_dwordx4 v62, s[52:55], s69 offen lds\n"
"Qmvaa 116,124,180\nQta 146,61,17024\nQmvav 120,128,28\nQmvaa 120,132,180\nQta 148,61,20640\ns_add_u32 s69, s87, 4258808\nQmvav 124,136,28\nbuffer_load_dwordx4 v62, s[52:55], s69 offen offset:1032 lds\n"
"Qmvaa 124,140,180\nQta 150,61,21152\nQmvav 144,112,32\nQmvaa 144,116,184\nQta 152,61,16520\ns_add_u32 s69, s87, 4323312\nQmvav 148,120,32\nbuffer_load_dwordx4 v62, s[52:55], s69 offen offset:2064 lds\n"
"Qmvaa 148,124,184\nQta 154,61,17032\nQmvav 152,128,32\nQmvaa 152,132,184\nQta 156,61,20648\ns_add_u32 s69, s87, 4387816\nQmvav 156,136,32\nbuffer_load_dwordx4 v62, s[52:55], s69 offen offset:3096 lds\n"
"Qmvaa 156,140,184\nQta 158,61,21160\nQmvav 176,112,36\nQmvaa 176,116,188\nQta 160,61,16768\ns_add_u32 m0, s63, 33024\ns_add_u32 s88, s86, 1024\nQmvav 180,120,36\n"
"buffer_load_dwordx4 v64, s[48:51], s88 offen lds\nQmvaa 180,124,188\nQta 162,61,17280\nQmvav 184,128,36\nQmvaa 184,132,188\nQta 164,61,20896\ns_add_u32 s88, s86, 20480\nQmvav 188,136,36\n"
"buffer_load_dwordx4 v64, s[48:51], s88 offen offset:1024 lds\nQmvaa 188,140,188\nQta 166,61,21408\nQmvav 208,112,40\nQmvaa 208,116,192\nQta 168,61,16776\ns_add_u32 s88, s86, 39936\nQmvav 212,120,40\n"
"buffer_load_dwordx4 v64, s[48:51], s88 offen offset:2048 lds\nQmvaa 212,124,192\nQta 170,61,17288\nQmvav 216,128,40\nQmvaa 216,132,192\nQta 172,61,20904\ns_add_u32 s88, s86, 59392\nQmvav 220,136,40\n"
"buffer_load_dwordx4 v64, s[48:51], s88 offen offset:3072 lds\nQmvaa 220,140,192\nQta 174,61,21416\nQmvav 240,112,44\nQmvaa 240,116,196\ns_add_u32 m0, s63, 37120\ns_add_u32 s88, s86, 82944\nQmvav 244,120,44\n"
"buffer_load_dwordx4 v64, s[48:51], s88 offen lds\nQmvaa 244,124,196\nQmvav 248,128,44\nQmvaa 248,132,196\ns_add_u32 s88, s86, 102400\nQmvav 252,136,44\nbuffer_load_dwordx4 v64, s[48:51], s88 offen offset:1024 lds\nQmvaa 252,140,196\n"
"Qmaav 224,112,48\nQmaaa 224,116,200\ns_add_u32 s88, s86, 121856\nQmaav 228,120,48\nbuffer_load_dwordx4 v64, s[48:51], s88 offen offset:2048 lds\nQmaaa 228,124,200\nQmaav 232,128,48\nQmaaa 232,132,200\n"
"Qmaav 236,136,48\nQmaaa 236,140,200\ns_waitcnt lgkmcnt(0)\nQmvav 96,144,24\nQmvaa 96,148,176\ns_add_u32 s68, s71, 32768\nQmvav 100,152,24\ns_cbranch_vccz .Lst1\n"
"buffer_store_dwordx4 a[104:107], v60, s[56:59], s68 offen offset:2048 sc1\n.Lst1:\nQmvaa 100,156,176\nQmvav 104,160,24\ns_add_u32 s68, s71, 47104\nQmvaa 104,164,176\ns_cbranch_vccz .Lst0\nbuffer_store_dwordx4 a[108:111], v60, s[56:59], s68 offen offset:2048 sc1\n"
".Lst0:\nQmvav 108,168,24\nQmvaa 108,172,176\ns_waitcnt lgkmcnt(0)\nQmvav 128,144,28\ns_waitcnt vmcnt(16)\ns_barrier\nQmvaa 128,148,180\n"
"Qdv 24,63,0\ns_add_u32 m0, s62, 16512\ns_add_u32 s69, s87, 4194432\nQmvav 132,152,28\nbuffer_load_dwordx4 v62, s[52:55], s69 offen lds\nQmvaa 132,156,180\nQda 176,63,512\nQmvav 136,160,28\n"
"Qta 112,61,33024\nQmvaa 136,164,180\ns_add_u32 s69, s87, 4258936\nQmvav 140,168,28\nbuffer_load_dwordx4 v62, s[52:55], s69 offen offset:1032 lds\nQmvaa 140,172,180\nQdv 28,63,1024\nQmvav 160,144,32\n"
"Qda 180,63,1536\nQmvaa 160,148,184\nQta 114,61,33536\ns_add_u32 s69, s87, 4323440\nQmvav 164,152,32\nbuffer_load_dwordx4 v62, s[52:55], s69 offen offset:2064 lds\nQmvaa 164,156,184\nQta 116,61,37152\n"
"Qmvav 168,160,32\nQmvaa 168,164,184\nQta 118,61,37664\ns_add_u32 s69, s87, 4387944\nQmvav 172,168,32\nbuffer_load_dwordx4 v62, s[52:55], s69 offen offset:3096 lds\nQmvaa 172,172,184\nQdv 32,63,2048\n"
"Qmvav 192,144,36\nQda 184,63,2560\nQmvaa 192,148,188\nQta 120,61,33032\ns_add_u32 m0, s61, 8192\nQmvav 196,152,36\ns_cbranch_vccnz .Ld0q6\nbuffer_load_dwordx4 v72, s[44:47], s90 offen offset:2048 nt lds\n"
".Ld0q6:\nQmvaa 196,156,188\nQta 122,61,33544\nQmvav 200,160,36\nQmvaa 200,164,188\nQta 124,61,37160\nQmvav 204,168,36\nQdv 36,63,3072\n"
"Qmvaa 204,172,188\nQda 188,63,3584\nQmvav 224,144,40\nQta 126,61,37672\nQmvaa 224,148,192\nQmvav 228,152,40\nQta 128,61,33280\nQmvaa 228,156,192\n"
"Qmvav 232,160,40\nQta 130,61,33792\nQmvaa 232,164,192\nQmvav 236,168,40\nQdv 40,63,4096\nQmvaa 236,172,192\nQda 192,63,4608\nQmaav 208,144,44\n"
"Qta 132,61,37408\nQmaaa 208,148,196\nQmaav 212,152,44\nQta 134,61,37920\nQmaaa 212,156,196\nQmaav 216,160,44\nQta 136,61,33288\nQmaaa 216,164,196\n"
"Qmaav 220,168,44\nQdv 44,63,5120\nQmaaa 220,172,196\nQda 196,63,5632\nQmaav 240,144,48\nQta 138,61,33800\nQmaaa 240,148,200\nQmaav 244,152,48\n"
"Qta 140,61,37416\nQmaaa 244,156,200\nQmaav 248,160,48\nQta 142,61,37928\nQmaaa 248,164,200\nQmaav 252,168,48\nQdv 48,63,6144\nQmaaa 252,172,200\n"
"Qda 200,63,6656\ns_waitcnt lgkmcnt(2)\nQmvav 80,112,24\nQmvaa 80,116,176\nQmvav 84,120,24\nQmvaa 84,124,176\nQmvav 88,128,24\nQmvaa 88,132,176\n"
"Qmvav 92,136,24\nQmvaa 92,140,176\ns_waitcnt lgkmcnt(0)\nQmvav 112,112,28\ns_waitcnt vmcnt(17)\ns_barrier\nQmvaa 112,116,180\nQta 144,61,49536\n"
"s_add_u32 m0, s62, 33024\ns_add_u32 s69, s87, 4718592\nQmvav 116,120,28\nbuffer_load_dwordx4 v62, s[52:55], s69 offen lds\nQmvaa 116,124,180\nQta 146,61,50048\nQmvav 120,128,28\nQmvaa 120,132,180\n"
"Qta 148,61,53664\ns_add_u32 s69, s87, 4783096\nQmvav 124,136,28\nbuffer_load_dwordx4 v62, s[52:55], s69 offen offset:1032 lds\nQmvaa 124,140,180\nQta 150,61,54176\nQmvav 144,112,32\nQmvaa 144,116,184\n"
"Qta 152,61,49544\ns_add_u32 s69, s87, 4847600\nQmvav 148,120,32\nbuffer_load_dwordx4 v62, s[52:55], s69 offen offset:2064 lds\nQmvaa 148,124,184\nQta 154,61,50056\nQmvav 152,128,32\nQmvaa 152,132,184\n"
"Qta 156,61,53672\ns_add_u32 s69, s87, 4912104\nQmvav 156,136,32\nbuffer_load_dwordx4 v62, s[52:55], s69 offen offset:3096 lds\nQmvaa 156,140,184\nQta 158,61,54184\nQmvav 176,112,36\nQmvaa 176,116,188\n"
"Qta 160,61,49792\ns_add_u32 m0, s63, 0\ns_add_u32 s88, s86, 1152\nQmvav 180,120,36\nbuffer_load_dwordx4 v64, s[48:51], s88 offen lds\nQmvaa 180,124,188\nQta 162,61,50304\nQmvav 184,128,36\n"
"Qmvaa 184,132,188\nQta 164,61,53920\ns_add_u32 s88, s86, 20608\nQmvav 188,136,36\nbuffer_load_dwordx4 v64, s[48:51], s88 offen offset:1024 lds\nQmvaa 188,140,188\nQta 166,61,54432\nQmvav 208,112,40\n"
"Qmvaa 208,116,192\nQta 168,61,49800\ns_add_u32 s88, s86, 40064\nQmvav 212,120,40\nbuffer_load_dwordx4 v64, s[48:51], s88 offen offset:2048 lds\nQmvaa 212,124,192\nQta 170,61,50312\nQmvav 216,128,40\n"
"Qmvaa 216,132,192\nQta 172,61,53928\ns_add_u32 s88, s86, 59520\nQmvav 220,136,40\nbuffer_load_dwordx4 v64, s[48:51], s88 offen offset:3072 lds\nQmvaa 220,140,192\nQta 174,61,54440\nQmvav 240,112,44\n"
"Qmvaa 240,116,196\ns_add_u32 m0, s63, 4096\ns_add_u32 s88, s86, 83072\nQmvav 244,120,44\nbuffer_load_dwordx4 v64, s[48:51], s88 offen lds\nQmvaa 244,124,196\nQmvav 248,128,44\nQmvaa 248,132,196\n"
"s_add_u32 s88, s86, 102528\nQmvav 252,136,44\nbuffer_load_dwordx4 v64, s[48:51], s88 offen offset:1024 lds\nQmvaa 252,140,196\nQmaav 224,112,48\nQmaaa 224,116,200\ns_add_u32 s88, s86, 121984\nQmaav 228,120,48\n"
"buffer_load_dwordx4 v64, s[48:51], s88 offen offset:2048 lds\nQmaaa 228,124,200\nQmaav 232,128,48\nQmaaa 232,132,200\nQmaav 236,136,48\nQmaaa 236,140,200\ns_waitcnt lgkmcnt(0)\nQmvav 96,144,24\n"
"Qmvaa 96,148,176\nQmvav 100,152,24\nQmvaa 100,156,176\nQmvav 104,160,24\nQmvaa 104,164,176\nQmvav 108,168,24\nQmvaa 108,172,176\ns_waitcnt lgkmcnt(0)\n"
"Qmvav 128,144,28\ns_waitcnt vmcnt(16)\ns_barrier\nQmvaa 128,148,180\nQdv 24,63,33024\ns_add_u32 m0, s62, 49536\ns_add_u32 s69, s87, 4718720\nQmvav 132,152,28\n"
"buffer_load_dwordx4 v62, s[52:55], s69 offen lds\nQmvaa 132,156,180\nQda 176,63,33536\nQmvav 136,160,28\nQta 112,61,0\nQmvaa 136,164,180\ns_add_u32 s69, s87, 4783224\nQmvav 140,168,28\n"
"buffer_load_dwordx4 v62, s[52:55], s69 offen offset:1032 lds\nQmvaa 140,172,180\nQdv 28,63,34048\nQmvav 160,144,32\nQda 180,63,34560\nQmvaa 160,148,184\nQta 114,61,512\ns_add_u32 s69, s87, 4847728\n"
"Qmvav 164,152,32\nbuffer_load_dwordx4 v62, s[52:55], s69 offen offset:2064 lds\nQmvaa 164,156,184\nQta 116,61,4128\nQmvav 168,160,32\nQmvaa 168,164,184\nQta 118,61,4640\ns_add_u32 s69, s87, 4912232\n"
"Qmvav 172,168,32\nbuffer_load_dwordx4 v62, s[52:55], s69 offen offset:3096 lds\nQmvaa 172,172,184\nQdv 32,63,35072\nQmvav 192,144,36\nQda 184,63,35584\nQmvaa 192,148,188\nQta 120,61,8\n"
"s_add_u32 m0, s61, 8192\nQmvav 196,152,36\ns_cbranch_vccnz .Ld0q7\nbuffer_load_dwordx4 v72, s[44:47], s60 offen offset:3072 nt lds\ns_add_u32 s60, s60, 8192\ns_add_u32 s90, s60, 896\n.Ld0q7:\nQmvaa 196,156,188\n"
"Qta 122,61,520\nQmvav 200,160,36\nQmvaa 200,164,188\nQta 124,61,4136\nQmvav 204,168,36\nQdv 36,63,36096\nQmvaa 204,172,188\nQda 188,63,36608\n"
"Qmvav 224,144,40\nQta 126,61,4648\nQmvaa 224,148,192\nQmvav 228,152,40\nQta 128,61,256\nQmvaa 228,156,192\nQmvav 232,160,40\nQta 130,61,768\n"
"Qmvaa 232,164,192\nQmvav 236,168,40\nQdv 40,63,37120\nQmvaa 236,172,192\nQda 192,63,37632\nQmaav 208,144,44\nQta 132,61,4384\nQmaaa 208,148,196\n"
"Qmaav 212,152,44\nQta 134,61,4896\nQmaaa 212,156,196\nQmaav 216,160,44\nQta 136,61,264\nQmaaa 216,164,196\nQmaav 220,168,44\nQdv 44,63,38144\n"
"Qmaaa 220,172,196\nQda 196,63,38656\nQmaav 240,144,48\nQta 138,61,776\nQmaaa 240,148,200\nQmaav 244,152,48\nQta 140,61,4392\nQmaaa 244,156,200\n"
"Qmaav 248,160,48\nQta 142,61,4904\nQmaaa 248,164,200\nQmaav 252,168,48\nQdv 48,63,39168\nQmaaa 252,172,200\nQda 200,63,39680\ns_waitcnt lgkmcnt(2)\n"
"Qmvav 80,112,24\nQmvaa 80,116,176\nQmvav 84,120,24\nQmvaa 84,124,176\nQmvav 88,128,24\nQmvaa 88,132,176\nQmvav 92,136,24\nQmvaa 92,140,176\n"
"s_waitcnt lgkmcnt(0)\nQmvav 112,112,28\ns_waitcnt vmcnt(15)\ns_barrier\nQmvaa 112,116,180\nQta 144,61,16512\ns_mov_b32 s56, s94\ns_mov_b32 s57, s95\n"
"s_mov_b32 s58, s75\ns_mov_b32 s94, 0\ns_add_u32 m0, %10, 160768\nQmvav 116,120,28\nbuffer_load_dwordx4 v60, s[56:59], s94 offen lds\nQmvaa 116,124,180\nQta 146,61,17024\nQmvav 120,128,28\n"
"Qmvaa 120,132,180\nQta 148,61,20640\ns_add_u32 s94, s94, 4096\nQmvav 124,136,28\nbuffer_load_dwordx4 v60, s[56:59], s94 offen lds\nQmvaa 124,140,180\nQta 150,61,21152\nQmvav 144,112,32\n"
"Qmvaa 144,116,184\nQta 152,61,16520\ns_add_u32 s94, s94, 4096\nQmvav 148,120,32\nbuffer_load_dwordx4 v60, s[56:59], s94 offen lds\nQmvaa 148,124,184\nQta 154,61,17032\nQmvav 152,128,32\n"
"Qmvaa 152,132,184\nQta 156,61,20648\ns_add_u32 s94, s94, 4096\nQmvav 156,136,32\nbuffer_load_dwordx4 v60, s[56:59], s94 offen lds\nQmvaa 156,140,184\nQta 158,61,21160\nQmvav 176,112,36\n"
"Qmvaa 176,116,188\nQta 160,61,16768\ns_add_u32 s94, s94, 4096\nQmvav 180,120,36\nbuffer_load_dwordx4 v60, s[56:59], s94 offen lds\nQmvaa 180,124,188\nQta 162,61,17280\nQmvav 184,128,36\n"
"Qmvaa 184,132,188\nQta 164,61,20896\ns_add_u32 s94, s94, 4096\nQmvav 188,136,36\nbuffer_load_dwordx4 v60, s[56:59], s94 offen lds\nQmvaa 188,140,188\nQta 166,61,21408\nQmvav 208,112,40\n"
"Qmvaa 208,116,192\nQta 168,61,16776\ns_add_u32 s94, s94, 4096\nQmvav 212,120,40\nbuffer_load_dwordx4 v60, s[56:59], s94 offen lds\nQmvaa 212,124,192\nQta 170,61,17288\nQmvav 216,128,40\n"
"Qmvaa 216,132,192\nQta 172,61,20904\ns_add_u32 s94, s94, 4096\nQmvav 220,136,40\nbuffer_load_dwordx4 v60, s[56:59], s94 offen lds\nQmvaa 220,140,192\nQta 174,61,21416\nQmvav 240,112,44\n"
"Qmvaa 240,116,196\ns_add_u32 s94, s94, 4096\nQmvav 244,120,44\nbuffer_load_dwordx4 v60, s[56:59], s94 offen lds\nQmvaa 244,124,196\nQmvav 248,128,44\nQmvaa 248,132,196\ns_add_u32 s94, s94, 4096\n"
"Qmvav 252,136,44\nbuffer_load_dwordx4 v60, s[56:59], s94 offen lds\nQmvaa 252,140,196\nQmaav 224,112,48\nQmaaa 224,116,200\nQmaav 228,120,48\nQmaaa 228,124,200\nQmaav 232,128,48\n"
"Qmaaa 232,132,200\nQmaav 236,136,48\nQmaaa 236,140,200\ns_waitcnt lgkmcnt(0)\nQmvav 96,144,24\nQmvaa 96,148,176\nQmvav 100,152,24\nQmvaa 100,156,176\n"
".Lpg3:\ns_cbranch_execz .Ltr0\nQmvav 104,160,24\nQmvaa 104,164,176\nQmvav 108,168,24\nQmvaa 108,172,176\ns_waitcnt lgkmcnt(0)\nQmvav 128,144,28\n"
"s_waitcnt vmcnt(14)\ns_barrier\nQmvaa 128,148,180\nQdv 24,63,0\ns_add_u32 m0, s61, 33024\nQmvav 132,152,28\nbuffer_load_dwordx4 v69, s[40:43], s90 offen nt lds\nQmvaa 132,156,180\n"
"Qda 176,63,512\nQmvav 136,160,28\nQta 112,61,33024\nQmvaa 136,164,180\nQmvav 140,168,28\nbuffer_load_dwordx4 v69, s[40:43], s60 offen offset:1024 nt lds\nQmvaa 140,172,180\nQdv 28,63,1024\n"
"Qmvav 160,144,32\nQda 180,63,1536\nQmvaa 160,148,184\nQta 114,61,33536\nQmvav 164,152,32\nbuffer_load_dwordx4 v69, s[40:43], s90 offen offset:2048 nt lds\nQmvaa 164,156,184\nQta 116,61,37152\n"
"Qmvav 168,160,32\nQmvaa 168,164,184\nQta 118,61,37664\nQmvav 172,168,32\nbuffer_load_dwordx4 v69, s[40:43], s60 offen offset:3072 nt lds\nQmvaa 172,172,184\nQdv 32,63,2048\nQmvav 192,144,36\n"
"Qda 184,63,2560\nQmvaa 192,148,188\nQta 120,61,33032\ns_add_u32 m0, s61, 41216\nQmvav 196,152,36\nbuffer_load_dwordx4 v72, s[44:47], s90 offen nt lds\nQmvaa 196,156,188\nQta 122,61,33544\n"
"Qmvav 200,160,36\nQmvaa 200,164,188\nQta 124,61,37160\nQmvav 204,168,36\nbuffer_load_dwordx4 v72, s[44:47], s60 offen offset:1024 nt lds\nQmvaa 204,172,188\nQdv 36,63,3072\nQmvav 224,144,40\n"
"Qda 188,63,3584\nQmvaa 224,148,192\nQta 126,61,37672\nQmvav 228,152,40\nbuffer_load_dwordx4 v72, s[44:47], s90 offen offset:2048 nt lds\nQmvaa 228,156,192\nQta 128,61,33280\nQmvav 232,160,40\n"
"Qmvaa 232,164,192\nQta 130,61,33792\nQmvav 236,168,40\nbuffer_load_dwordx4 v72, s[44:47], s60 offen offset:3072 nt lds\ns_add_u32 s60, s60, 8192\ns_add_u32 s90, s60, 896\nQmvaa 236,172,192\nQdv 40,63,4096\n"
"Qmaav 208,144,44\nQda 192,63,4608\nQmaaa 208,148,196\nQta 132,61,37408\nQmaav 212,152,44\nQmaaa 212,156,196\nQta 134,61,37920\nQmaav 216,160,44\n"
"Qmaaa 216,164,196\nQta 136,61,33288\nQmaav 220,168,44\nQdv 44,63,5120\nQmaaa 220,172,196\nQda 196,63,5632\nQmaav 240,144,48\nQta 138,61,33800\n"
"Qmaaa 240,148,200\nQmaav 244,152,48\nQta 140,61,37416\nQmaaa 244,156,200\nQmaav 248,160,48\nQta 142,61,37928\nQmaaa 248,164,200\nQmaav 252,168,48\n"
"Qdv 48,63,6144\nQmaaa 252,172,200\nQda 200,63,6656\ns_waitcnt lgkmcnt(2)\nQmvav 80,112,24\nQmvaa 80,116,176\nQmvav 84,120,24\nQmvaa 84,124,176\n"
"Qmvav 88,128,24\nQmvaa 88,132,176\nQmvav 92,136,24\nQmvaa 92,140,176\ns_waitcnt lgkmcnt(0)\nQmvav 112,112,28\ns_waitcnt vmcnt(18)\ns_barrier\n"
"Qmvaa 112,116,180\nQta 144,61,49536\nQmvav 116,120,28\nQmvaa 116,124,180\nQta 146,61,50048\nQmvav 120,128,28\nQmvaa 120,132,180\nQta 148,61,53664\n"
"Qmvav 124,136,28\nQmvaa 124,140,180\nQta 150,61,54176\nQmvav 144,112,32\nQmvaa 144,116,184\nQta 152,61,49544\nQmvav 148,120,32\nQmvaa 148,124,184\n"
"Qta 154,61,50056\nQmvav 152,128,32\nQmvaa 152,132,184\nQta 156,61,53672\nQmvav 156,136,32\nQmvaa 156,140,184\nQta 158,61,54184\nQmvav 176,112,36\n"
"Qmvaa 176,116,188\nQta 160,61,49792\nQmvav 180,120,36\nQmvaa 180,124,188\nQta 162,61,50304\nQmvav 184,128,36\nQmvaa 184,132,188\nQta 164,61,53920\n"
"Qmvav 188,136,36\nQmvaa 188,140,188\nQta 166,61,54432\nQmvav 208,112,40\nQmvaa 208,116,192\nQta 168,61,49800\nQmvav 212,120,40\nQmvaa 212,124,192\n"
"Qta 170,61,50312\nQmvav 216,128,40\nQmvaa 216,132,192\nQta 172,61,53928\nQmvav 220,136,40\nQmvaa 220,140,192\nQta 174,61,54440\nQmvav 240,112,44\n"
"Qmvaa 240,116,196\nQmvav 244,120,44\nQmvaa 244,124,196\nQmvav 248,128,44\nQmvaa 248,132,196\nQmvav 252,136,44\nQmvaa 252,140,196\nQmaav 224,112,48\n"
"Qmaaa 224,116,200\nQmaav 228,120,48\nQmaaa 228,124,200\nQmaav 232,128,48\nQmaaa 232,132,200\nQmaav 236,136,48\nQmaaa 236,140,200\ns_waitcnt lgkmcnt(0)\n"
"Qmvav 96,144,24\nQmvaa 96,148,176\nQmvav 100,152,24\nQmvaa 100,156,176\nQmvav 104,160,24\nQmvaa 104,164,176\nQmvav 108,168,24\nQmvaa 108,172,176\n"
"s_waitcnt lgkmcnt(0)\nQmvav 128,144,28\ns_waitcnt vmcnt(18)\ns_barrier\nQmvaa 128,148,180\nQda 112,68,0\ns_add_u32 m0, s61, 66048\nv_accvgpr_write_b32 a176, 0\n"
"Qmvav 132,152,28\nbuffer_load_dwordx4 v69, s[40:43], s90 offen nt lds\nQmvaa 132,156,180\nQda 116,68,512\nv_accvgpr_write_b32 a177, 0\nQmvav 136,160,28\nQmvaa 136,164,180\nQda 120,68,1024\n"
"v_accvgpr_write_b32 a178, 0\nQmvav 140,168,28\nbuffer_load_dwordx4 v69, s[40:43], s60 offen offset:1024 nt lds\nQmvaa 140,172,180\nQda 124,68,1536\nv_accvgpr_write_b32 a179, 0\nQmvav 160,144,32\nQmvaa 160,148,184\n"
"Qda 128,68,2048\nv_accvgpr_write_b32 a180, 0\nQmvav 164,152,32\nbuffer_load_dwordx4 v69, s[40:43], s90 offen offset:2048 nt lds\nQmvaa 164,156,184\nQda 132,68,2560\nv_accvgpr_write_b32 a181, 0\nQmvav 168,160,32\n"
"Qmvaa 168,164,184\nQda 136,68,3072\nv_accvgpr_write_b32 a182, 0\nQmvav 172,168,32\nbuffer_load_dwordx4 v69, s[40:43], s60 offen offset:3072 nt lds\nQmvaa 172,172,184\nQda 140,68,3584\nv_accvgpr_write_b32 a183, 0\n"
"Qmvav 192,144,36\nQmvaa 192,148,188\ns_add_u32 m0, s61, 74240\nv_accvgpr_write_b32 a184, 0\nQmvav 196,152,36\nbuffer_load_dwordx4 v72, s[44:47], s90 offen nt lds\nQmvaa 196,156,188\nv_accvgpr_write_b32 a185, 0\n"
"Qmvav 200,160,36\nQmvaa 200,164,188\nv_accvgpr_write_b32 a186, 0\nQmvav 204,168,36\nbuffer_load_dwordx4 v72, s[44:47], s60 offen offset:1024 nt lds\nQmvaa 204,172,188\nv_accvgpr_write_b32 a187, 0\nQmvav 224,144,40\n"
"Qmvaa 224,148,192\nv_accvgpr_write_b32 a188, 0\nQmvav 228,152,40\nbuffer_load_dwordx4 v72, s[44:47], s90 offen offset:2048 nt lds\nQmvaa 228,156,192\nv_accvgpr_write_b32 a189, 0\nQmvav 232,160,40\nQmvaa 232,164,192\n"
"v_accvgpr_write_b32 a190, 0\nQmvav 236,168,40\nbuffer_load_dwordx4 v72, s[44:47], s60 offen offset:3072 nt lds\ns_add_u32 s60, s60, 8192\ns_add_u32 s90, s60, 896\nQmvaa 236,172,192\nv_accvgpr_write_b32 a191, 0\nQmaav 208,144,44\n"
"Qmaaa 208,148,196\nv_accvgpr_write_b32 a192, 0\nQmaav 212,152,44\nQmaaa 212,156,196\nv_accvgpr_write_b32 a193, 0\nQmaav 216,160,44\nQmaaa 216,164,196\nv_accvgpr_write_b32 a194, 0\n"
"Qmaav 220,168,44\nQmaaa 220,172,196\nv_accvgpr_write_b32 a195, 0\nQmaav 240,144,48\nQmaaa 240,148,200\nv_accvgpr_write_b32 a196, 0\nQmaav 244,152,48\nQmaaa 244,156,200\n"
"v_accvgpr_write_b32 a197, 0\nQmaav 248,160,48\nQmaaa 248,164,200\nv_accvgpr_write_b32 a198, 0\nQmaav 252,168,48\nQmaaa 252,172,200\nv_accvgpr_write_b32 a199, 0\nQta 148,70,1024\n"
"Qta 150,70,3072\nQta 156,70,1280\nQta 158,70,3328\nQta 164,70,1536\nQta 166,70,3584\nQta 172,70,1792\nQta 174,70,3840\nv_pk_mul_f32 v[80:81], v[80:81], s[72:73] op_sel_hi:[1,0]\n"
"v_pk_mul_f32 v[82:83], v[82:83], s[72:73] op_sel_hi:[1,0]\nv_pk_mul_f32 v[84:85], v[84:85], s[72:73] op_sel_hi:[1,0]\nv_pk_mul_f32 v[86:87], v[86:87], s[72:73] op_sel_hi:[1,0]\nQk 80,80,81\nQk 81,82,83\nQk 82,84,85\nQk 83,86,87\nv_accvgpr_write_b32 a0, v80\n"
"v_accvgpr_write_b32 a1, v81\nv_accvgpr_write_b32 a2, v82\nv_accvgpr_write_b32 a3, v83\nv_pk_mul_f32 v[88:89], v[88:89], s[72:73] op_sel_hi:[1,0]\nv_pk_mul_f32 v[90:91], v[90:91], s[72:73] op_sel_hi:[1,0]\nv_pk_mul_f32 v[92:93], v[92:93], s[72:73] op_sel_hi:[1,0]\nv_pk_mul_f32 v[94:95], v[94:95], s[72:73] op_sel_hi:[1,0]\nQk 88,88,89\n"
"Qk 89,90,91\nQk 90,92,93\nQk 91,94,95\nv_accvgpr_write_b32 a4, v88\nv_accvgpr_write_b32 a5, v89\nv_accvgpr_write_b32 a6, v90\nv_accvgpr_write_b32 a7, v91\nv_pk_mul_f32 v[96:97], v[96:97], s[72:73] op_sel_hi:[1,0]\n"
"v_pk_mul_f32 v[98:99], v[98:99], s[72:73] op_sel_hi:[1,0]\nv_pk_mul_f32 v[100:101], v[100:101], s[72:73] op_sel_hi:[1,0]\nv_pk_mul_f32 v[102:103], v[102:103], s[72:73] op_sel_hi:[1,0]\nQk 96,96,97\nQk 97,98,99\nQk 98,100,101\nQk 99,102,103\nv_accvgpr_write_b32 a8, v96\n"
"v_accvgpr_write_b32 a9, v97\nv_accvgpr_write_b32 a10, v98\nv_accvgpr_write_b32 a11, v99\nv_pk_mul_f32 v[104:105], v[104:105], s[72:73] op_sel_hi:[1,0]\nv_pk_mul_f32 v[106:107], v[106:107], s[72:73] op_sel_hi:[1,0]\nv_pk_mul_f32 v[108:109], v[108:109], s[72:73] op_sel_hi:[1,0]\nv_pk_mul_f32 v[110:111], v[110:111], s[72:73] op_sel_hi:[1,0]\nQk 104,104,105\n"
"Qk 105,106,107\nQk 106,108,109\nQk 107,110,111\nv_accvgpr_write_b32 a12, v104\nv_accvgpr_write_b32 a13, v105\nv_accvgpr_write_b32 a14, v106\nv_accvgpr_write_b32 a15, v107\nv_pk_mul_f32 v[112:113], v[112:113], s[72:73] op_sel_hi:[1,0]\n"
"v_pk_mul_f32 v[114:115], v[114:115], s[72:73] op_sel_hi:[1,0]\nv_pk_mul_f32 v[116:117], v[116:117], s[72:73] op_sel_hi:[1,0]\nv_pk_mul_f32 v[118:119], v[118:119], s[72:73] op_sel_hi:[1,0]\nQk 112,112,113\nQk 113,114,115\nQk 114,116,117\nQk 115,118,119\nv_accvgpr_write_b32 a16, v112\n"
"v_accvgpr_write_b32 a17, v113\nv_accvgpr_write_b32 a18, v114\nv_accvgpr_write_b32 a19, v115\nv_pk_mul_f32 v[120:121], v[120:121], s[72:73] op_sel_hi:[1,0]\nv_pk_mul_f32 v[122:123], v[122:123], s[72:73] op_sel_hi:[1,0]\nv_pk_mul_f32 v[124:125], v[124:125], s[72:73] op_sel_hi:[1,0]\nv_pk_mul_f32 v[126:127], v[126:127], s[72:73] op_sel_hi:[1,0]\nQk 120,120,121\n"
"Qk 121,122,123\nQk 122,124,125\nQk 123,126,127\nv_accvgpr_write_b32 a20, v120\nv_accvgpr_write_b32 a21, v121\nv_accvgpr_write_b32 a22, v122\nv_accvgpr_write_b32 a23, v123\nv_pk_mul_f32 v[128:129], v[128:129], s[72:73] op_sel_hi:[1,0]\n"
"v_pk_mul_f32 v[130:131], v[130:131], s[72:73] op_sel_hi:[1,0]\nv_pk_mul_f32 v[132:133], v[132:133], s[72:73] op_sel_hi:[1,0]\nv_pk_mul_f32 v[134:135], v[134:135], s[72:73] op_sel_hi:[1,0]\nQk 128,128,129\nQk 129,130,131\nQk 130,132,133\nQk 131,134,135\nv_accvgpr_write_b32 a24, v128\n"
"v_accvgpr_write_b32 a25, v129\nv_accvgpr_write_b32 a26, v130\nv_accvgpr_write_b32 a27, v131\nv_pk_mul_f32 v[136:137], v[136:137], s[72:73] op_sel_hi:[1,0]\nv_pk_mul_f32 v[138:139], v[138:139], s[72:73] op_sel_hi:[1,0]\nv_pk_mul_f32 v[140:141], v[140:141], s[72:73] op_sel_hi:[1,0]\nv_pk_mul_f32 v[142:143], v[142:143], s[72:73] op_sel_hi:[1,0]\nQk 136,136,137\n"
"Qk 137,138,139\nQk 138,140,141\nQk 139,142,143\nv_accvgpr_write_b32 a28, v136\nv_accvgpr_write_b32 a29, v137\nv_accvgpr_write_b32 a30, v138\nv_accvgpr_write_b32 a31, v139\nv_pk_mul_f32 v[144:145], v[144:145], s[72:73] op_sel_hi:[1,0]\n"
"v_pk_mul_f32 v[146:147], v[146:147], s[72:73] op_sel_hi:[1,0]\nv_pk_mul_f32 v[148:149], v[148:149], s[72:73] op_sel_hi:[1,0]\nv_pk_mul_f32 v[150:151], v[150:151], s[72:73] op_sel_hi:[1,0]\nQk 144,144,145\nQk 145,146,147\nQk 146,148,149\nQk 147,150,151\nv_accvgpr_write_b32 a32, v144\n"
"v_accvgpr_write_b32 a33, v145\nv_accvgpr_write_b32 a34, v146\nv_accvgpr_write_b32 a35, v147\nv_pk_mul_f32 v[152:153], v[152:153], s[72:73] op_sel_hi:[1,0]\nv_pk_mul_f32 v[154:155], v[154:155], s[72:73] op_sel_hi:[1,0]\nv_pk_mul_f32 v[156:157], v[156:157], s[72:73] op_sel_hi:[1,0]\nv_pk_mul_f32 v[158:159], v[158:159], s[72:73] op_sel_hi:[1,0]\nQk 152,152,153\n"
"Qk 153,154,155\nQk 154,156,157\nQk 155,158,159\nv_accvgpr_write_b32 a36, v152\nv_accvgpr_write_b32 a37, v153\nv_accvgpr_write_b32 a38, v154\nv_accvgpr_write_b32 a39, v155\nv_pk_mul_f32 v[160:161], v[160:161], s[72:73] op_sel_hi:[1,0]\n"
"v_pk_mul_f32 v[162:163], v[162:163], s[72:73] op_sel_hi:[1,0]\nv_pk_mul_f32 v[164:165], v[164:165], s[72:73] op_sel_hi:[1,0]\nv_pk_mul_f32 v[166:167], v[166:167], s[72:73] op_sel_hi:[1,0]\nQk 160,160,161\nQk 161,162,163\nQk 162,164,165\nQk 163,166,167\nv_accvgpr_write_b32 a40, v160\n"
"v_accvgpr_write_b32 a41, v161\nv_accvgpr_write_b32 a42, v162\nv_accvgpr_write_b32 a43, v163\nv_pk_mul_f32 v[168:169], v[168:169], s[72:73] op_sel_hi:[1,0]\nv_pk_mul_f32 v[170:171], v[170:171], s[72:73] op_sel_hi:[1,0]\nv_pk_mul_f32 v[172:173], v[172:173], s[72:73] op_sel_hi:[1,0]\nv_pk_mul_f32 v[174:175], v[174:175], s[72:73] op_sel_hi:[1,0]\nQk 168,168,169\n"
"Qk 169,170,171\nQk 170,172,173\nQk 171,174,175\nv_accvgpr_write_b32 a44, v168\nv_accvgpr_write_b32 a45, v169\nv_accvgpr_write_b32 a46, v170\nv_accvgpr_write_b32 a47, v171\nv_pk_mul_f32 v[176:177], v[176:177], s[72:73] op_sel_hi:[1,0]\n"
"v_pk_mul_f32 v[178:179], v[178:179], s[72:73] op_sel_hi:[1,0]\nv_pk_mul_f32 v[180:181], v[180:181], s[72:73] op_sel_hi:[1,0]\nv_pk_mul_f32 v[182:183], v[182:183], s[72:73] op_sel_hi:[1,0]\nQk 176,176,177\nQk 177,178,179\nQk 178,180,181\nQk 179,182,183\nv_accvgpr_write_b32 a48, v176\n"
"v_accvgpr_write_b32 a49, v177\nv_accvgpr_write_b32 a50, v178\nv_accvgpr_write_b32 a51, v179\nv_pk_mul_f32 v[184:185], v[184:185], s[72:73] op_sel_hi:[1,0]\nv_pk_mul_f32 v[186:187], v[186:187], s[72:73] op_sel_hi:[1,0]\nv_pk_mul_f32 v[188:189], v[188:189], s[72:73] op_sel_hi:[1,0]\nv_pk_mul_f32 v[190:191], v[190:191], s[72:73] op_sel_hi:[1,0]\nQk 184,184,185\n"
"Qk 185,186,187\nQk 186,188,189\nQk 187,190,191\nv_accvgpr_write_b32 a52, v184\nv_accvgpr_write_b32 a53, v185\nv_accvgpr_write_b32 a54, v186\nv_accvgpr_write_b32 a55, v187\nv_pk_mul_f32 v[192:193], v[192:193], s[72:73] op_sel_hi:[1,0]\n"
"v_pk_mul_f32 v[194:195], v[194:195], s[72:73] op_sel_hi:[1,0]\nv_pk_mul_f32 v[196:197], v[196:197], s[72:73] op_sel_hi:[1,0]\nv_pk_mul_f32 v[198:199], v[198:199], s[72:73] op_sel_hi:[1,0]\nQk 192,192,193\nQk 193,194,195\nQk 194,196,197\nQk 195,198,199\nv_accvgpr_write_b32 a56, v192\n"
"v_accvgpr_write_b32 a57, v193\nv_accvgpr_write_b32 a58, v194\nv_accvgpr_write_b32 a59, v195\nv_pk_mul_f32 v[200:201], v[200:201], s[72:73] op_sel_hi:[1,0]\nv_pk_mul_f32 v[202:203], v[202:203], s[72:73] op_sel_hi:[1,0]\nv_pk_mul_f32 v[204:205], v[204:205], s[72:73] op_sel_hi:[1,0]\nv_pk_mul_f32 v[206:207], v[206:207], s[72:73] op_sel_hi:[1,0]\nQk 200,200,201\n"
"Qk 201,202,203\nQk 202,204,205\nQk 203,206,207\nv_accvgpr_write_b32 a60, v200\nv_accvgpr_write_b32 a61, v201\nv_accvgpr_write_b32 a62, v202\nv_accvgpr_write_b32 a63, v203\nv_pk_mul_f32 v[208:209], v[208:209], s[72:73] op_sel_hi:[1,0]\n"
"v_pk_mul_f32 v[210:211], v[210:211], s[72:73] op_sel_hi:[1,0]\nv_pk_mul_f32 v[212:213], v[212:213], s[72:73] op_sel_hi:[1,0]\nv_pk_mul_f32 v[214:215], v[214:215], s[72:73] op_sel_hi:[1,0]\nQk 208,208,209\nQk 209,210,211\nQk 210,212,213\nQk 211,214,215\nv_accvgpr_write_b32 a64, v208\n"
"v_accvgpr_write_b32 a65, v209\nv_accvgpr_write_b32 a66, v210\nv_accvgpr_write_b32 a67, v211\nv_pk_mul_f32 v[216:217], v[216:217], s[72:73] op_sel_hi:[1,0]\nv_pk_mul_f32 v[218:219], v[218:219], s[72:73] op_sel_hi:[1,0]\nv_pk_mul_f32 v[220:221], v[220:221], s[72:73] op_sel_hi:[1,0]\nv_pk_mul_f32 v[222:223], v[222:223], s[72:73] op_sel_hi:[1,0]\nQk 216,216,217\n"
"Qk 217,218,219\nQk 218,220,221\nQk 219,222,223\nv_accvgpr_write_b32 a68, v216\nv_accvgpr_write_b32 a69, v217\nv_accvgpr_write_b32 a70, v218\nv_accvgpr_write_b32 a71, v219\nv_pk_mul_f32 v[224:225], v[224:225], s[72:73] op_sel_hi:[1,0]\n"
"v_pk_mul_f32 v[226:227], v[226:227], s[72:73] op_sel_hi:[1,0]\nv_pk_mul_f32 v[228:229], v[228:229], s[72:73] op_sel_hi:[1,0]\nv_pk_mul_f32 v[230:231], v[230:231], s[72:73] op_sel_hi:[1,0]\nQk 224,224,225\nQk 225,226,227\nQk 226,228,229\nQk 227,230,231\nv_accvgpr_write_b32 a72, v224\n"
"v_accvgpr_write_b32 a73, v225\nv_accvgpr_write_b32 a74, v226\nv_accvgpr_write_b32 a75, v227\nv_pk_mul_f32 v[232:233], v[232:233], s[72:73] op_sel_hi:[1,0]\nv_pk_mul_f32 v[234:235], v[234:235], s[72:73] op_sel_hi:[1,0]\nv_pk_mul_f32 v[236:237], v[236:237], s[72:73] op_sel_hi:[1,0]\nv_pk_mul_f32 v[238:239], v[238:239], s[72:73] op_sel_hi:[1,0]\n.Lpg4:\n"
"s_cbranch_execz .Ltr9\nQk 232,232,233\nQk 233,234,235\nQk 234,236,237\nQk 235,238,239\nv_accvgpr_write_b32 a76, v232\nv_accvgpr_write_b32 a77, v233\nv_accvgpr_write_b32 a78, v234\n"
"v_accvgpr_write_b32 a79, v235\nv_pk_mul_f32 v[240:241], v[240:241], s[72:73] op_sel_hi:[1,0]\nv_pk_mul_f32 v[242:243], v[242:243], s[72:73] op_sel_hi:[1,0]\nv_pk_mul_f32 v[244:245], v[244:245], s[72:73] op_sel_hi:[1,0]\nv_pk_mul_f32 v[246:247], v[246:247], s[72:73] op_sel_hi:[1,0]\nQk 240,240,241\nQk 241,242,243\nQk 242,244,245\n"
"Qk 243,246,247\nv_accvgpr_write_b32 a80, v240\nv_accvgpr_write_b32 a81, v241\nv_accvgpr_write_b32 a82, v242\nv_accvgpr_write_b32 a83, v243\nv_pk_mul_f32 v[248:249], v[248:249], s[72:73] op_sel_hi:[1,0]\nv_pk_mul_f32 v[250:251], v[250:251], s[72:73] op_sel_hi:[1,0]\nv_pk_mul_f32 v[252:253], v[252:253], s[72:73] op_sel_hi:[1,0]\n"
"v_pk_mul_f32 v[254:255], v[254:255], s[72:73] op_sel_hi:[1,0]\nQk 248,248,249\nQk 249,250,251\nQk 250,252,253\nQk 251,254,255\nv_accvgpr_write_b32 a84, v248\nv_accvgpr_write_b32 a85, v249\nv_accvgpr_write_b32 a86, v250\n"
"v_accvgpr_write_b32 a87, v251\nv_accvgpr_read_b32 v0, a208\nv_accvgpr_read_b32 v1, a209\nv_accvgpr_read_b32 v2, a210\nv_accvgpr_read_b32 v3, a211\nv_accvgpr_read_b32 v4, a212\nv_accvgpr_read_b32 v5, a213\nv_accvgpr_read_b32 v6, a214\n"
"v_accvgpr_read_b32 v7, a215\nv_pk_mul_f32 v[0:1], v[0:1], s[72:73] op_sel_hi:[1,0]\nv_pk_mul_f32 v[2:3], v[2:3], s[72:73] op_sel_hi:[1,0]\nv_pk_mul_f32 v[4:5], v[4:5], s[72:73] op_sel_hi:[1,0]\nv_pk_mul_f32 v[6:7], v[6:7], s[72:73] op_sel_hi:[1,0]\nQk 0,0,1\nQk 1,2,3\nQk 2,4,5\n"
"Qk 3,6,7\nv_accvgpr_write_b32 a88, v0\nv_accvgpr_write_b32 a89, v1\nv_accvgpr_write_b32 a90, v2\nv_accvgpr_write_b32 a91, v3\nv_accvgpr_read_b32 v8, a216\nv_accvgpr_read_b32 v9, a217\nv_accvgpr_read_b32 v10, a218\n"
"v_accvgpr_read_b32 v11, a219\nv_accvgpr_read_b32 v12, a220\nv_accvgpr_read_b32 v13, a221\nv_accvgpr_read_b32 v14, a222\nv_accvgpr_read_b32 v15, a223\nv_pk_mul_f32 v[8:9], v[8:9], s[72:73] op_sel_hi:[1,0]\nv_pk_mul_f32 v[10:11], v[10:11], s[72:73] op_sel_hi:[1,0]\nv_pk_mul_f32 v[12:13], v[12:13], s[72:73] op_sel_hi:[1,0]\n"
"v_pk_mul_f32 v[14:15], v[14:15], s[72:73] op_sel_hi:[1,0]\nQk 8,8,9\nQk 9,10,11\nQk 10,12,13\nQk 11,14,15\nv_accvgpr_write_b32 a92, v8\nv_accvgpr_write_b32 a93, v9\nv_accvgpr_write_b32 a94, v10\n"
"v_accvgpr_write_b32 a95, v11\nv_accvgpr_read_b32 v0, a224\nv_accvgpr_read_b32 v1, a225\nv_accvgpr_read_b32 v2, a226\nv_accvgpr_read_b32 v3, a227\nv_accvgpr_read_b32 v4, a228\nv_accvgpr_read_b32 v5, a229\nv_accvgpr_read_b32 v6, a230\n"
"v_accvgpr_read_b32 v7, a231\nv_pk_mul_f32 v[0:1], v[0:1], s[72:73] op_sel_hi:[1,0]\nv_pk_mul_f32 v[2:3], v[2:3], s[72:73] op_sel_hi:[1,0]\nv_pk_mul_f32 v[4:5], v[4:5], s[72:73] op_sel_hi:[1,0]\nv_pk_mul_f32 v[6:7], v[6:7], s[72:73] op_sel_hi:[1,0]\nQk 0,0,1\nQk 1,2,3\nQk 2,4,5\n"
"Qk 3,6,7\nv_accvgpr_write_b32 a96, v0\nv_accvgpr_write_b32 a97, v1\nv_accvgpr_write_b32 a98, v2\nv_accvgpr_write_b32 a99, v3\nv_accvgpr_read_b32 v8, a232\nv_accvgpr_read_b32 v9, a233\nv_accvgpr_read_b32 v10, a234\n"
"v_accvgpr_read_b32 v11, a235\nv_accvgpr_read_b32 v12, a236\nv_accvgpr_read_b32 v13, a237\nv_accvgpr_read_b32 v14, a238\nv_accvgpr_read_b32 v15, a239\nv_pk_mul_f32 v[8:9], v[8:9], s[72:73] op_sel_hi:[1,0]\nv_pk_mul_f32 v[10:11], v[10:11], s[72:73] op_sel_hi:[1,0]\nv_pk_mul_f32 v[12:13], v[12:13], s[72:73] op_sel_hi:[1,0]\n"
"v_pk_mul_f32 v[14:15], v[14:15], s[72:73] op_sel_hi:[1,0]\nQk 8,8,9\nQk 9,10,11\nQk 10,12,13\nQk 11,14,15\nv_accvgpr_write_b32 a100, v8\nv_accvgpr_write_b32 a101, v9\nv_accvgpr_write_b32 a102, v10\n"
"v_accvgpr_write_b32 a103, v11\nv_accvgpr_read_b32 v0, a240\nv_accvgpr_read_b32 v1, a241\nv_accvgpr_read_b32 v2, a242\nv_accvgpr_read_b32 v3, a243\nv_accvgpr_read_b32 v4, a244\nv_accvgpr_read_b32 v5, a245\nv_accvgpr_read_b32 v6, a246\n"
"v_accvgpr_read_b32 v7, a247\nv_pk_mul_f32 v[0:1], v[0:1], s[72:73] op_sel_hi:[1,0]\nv_pk_mul_f32 v[2:3], v[2:3], s[72:73] op_sel_hi:[1,0]\nv_pk_mul_f32 v[4:5], v[4:5], s[72:73] op_sel_hi:[1,0]\nv_pk_mul_f32 v[6:7], v[6:7], s[72:73] op_sel_hi:[1,0]\nQk 0,0,1\nQk 1,2,3\nQk 2,4,5\n"
"Qk 3,6,7\nv_accvgpr_write_b32 a104, v0\nv_accvgpr_write_b32 a105, v1\nv_accvgpr_write_b32 a106, v2\nv_accvgpr_write_b32 a107, v3\nv_accvgpr_read_b32 v8, a248\nv_accvgpr_read_b32 v9, a249\nv_accvgpr_read_b32 v10, a250\n"
"v_accvgpr_read_b32 v11, a251\nv_accvgpr_read_b32 v12, a252\nv_accvgpr_read_b32 v13, a253\nv_accvgpr_read_b32 v14, a254\nv_accvgpr_read_b32 v15, a255\nv_pk_mul_f32 v[8:9], v[8:9], s[72:73] op_sel_hi:[1,0]\nv_pk_mul_f32 v[10:11], v[10:11], s[72:73] op_sel_hi:[1,0]\nv_pk_mul_f32 v[12:13], v[12:13], s[72:73] op_sel_hi:[1,0]\n"
"v_pk_mul_f32 v[14:15], v[14:15], s[72:73] op_sel_hi:[1,0]\nQk 8,8,9\nQk 9,10,11\nQk 10,12,13\nQk 11,14,15\nv_accvgpr_write_b32 a108, v8\nv_accvgpr_write_b32 a109, v9\nv_accvgpr_write_b32 a110, v10\n"
"v_accvgpr_write_b32 a111, v11\nv_mov_b32 v66, 0x3f803f80\nv_accvgpr_write_b32 a200, 0\nv_accvgpr_write_b32 a201, 0\nv_accvgpr_write_b32 a202, 0\nv_accvgpr_write_b32 a203, 0\ns_mov_b32 vcc_lo, 0x33333333\ns_mov_b32 vcc_hi, 0xcccccccc\n"
"v_cndmask_b32 v66, 0, v66, vcc\nv_accvgpr_write_b32 a204, v66\nv_accvgpr_write_b32 a205, v66\nv_accvgpr_write_b32 a206, v66\nv_accvgpr_write_b32 a207, v66\ns_add_u32 s87, s87, 2048\ns_waitcnt lgkmcnt(8)\nQzvaa 0,112,0\n"
"Qmvaa 0,116,4\nQmvaa 0,120,8\nQmvaa 0,124,12\nQzvaa 4,128,0\nQmvaa 4,132,4\nQmvaa 4,136,8\nQmvaa 4,140,12\ns_nop 3\n"
"v_max3_f32 v66, v0, v1, v2\ns_nop 2\nv_max3_f32 v66, v66, v3, v4\nv_max3_f32 v66, v66, v5, v6\nv_max_f32 v66, v66, v7\nQzvaa 8,112,16\nv_mov_b32 v67, v66\nQmvaa 8,116,20\n"
"s_nop 0\nv_permlane32_swap_b32 v66, v67\nv_max_f32 v66, v66, v67\nQmvaa 8,120,24\nv_mov_b32 v67, v66\nQmvaa 8,124,28\ns_nop 0\nv_permlane16_swap_b32 v66, v67\n"
"v_max_f32 v66, v66, v67\nQzvaa 12,128,16\nv_sub_f32 v56, 0xc1b00000, v66\nv_mov_b32 v57, v56\nQmvaa 12,132,20\nv_mov_b32 v58, v56\nv_mov_b32 v59, v56\nQmvaa 12,136,24\n"
"Qa 0,0,56\nQa 1,1,56\nQmvaa 12,140,28\nQa 2,2,56\nQa 3,3,56\nQe 0\nQe 1\nQe 2\n"
"Qe 3\nQk 16,0,1\nQk 17,2,3\nQa 4,4,56\nQa 5,5,56\nQa 6,6,56\nQa 7,7,56\nQe 4\n"
"Qe 5\nQcvaa 0,112,32,56\nQe 6\nQmvaa 0,116,36\nQe 7\nQmvaa 0,120,40\nQk 18,4,5\nQk 19,6,7\n"
"Qmvaa 0,124,44\nQa 8,8,56\nQa 9,9,56\nQcvaa 4,128,32,56\nQa 10,10,56\nQa 11,11,56\nQmvaa 4,132,36\nQe 8\n"
"Qmvaa 4,136,40\nQe 9\nQmvaa 4,140,44\ns_waitcnt lgkmcnt(0)\nQk 20,8,9\nQzvav 96,148,16\nQe 10\nQzvav 100,156,16\n"
"Qe 11\nQzvav 104,164,16\nQk 21,10,11\nQa 12,12,56\nQzvav 108,172,16\nQa 13,13,56\nQa 14,14,56\nQcvaa 8,112,48,56\n"
"Qa 15,15,56\nQmvaa 8,116,52\nQe 12\nQmvaa 8,120,56\nQe 13\nQe 14\nQe 15\nQmvaa 8,124,60\n"
"Qk 22,12,13\nQk 23,14,15\nQcvaa 12,128,48,56\nQe 0\nQmvaa 12,132,52\nQe 1\nv_smfmac_f32_16x16x64_bf16 a[176:179], a[204:207], v[16:23], v77\nQk 24,0,1\n"
"Qmvaa 12,136,56\nQe 2\nQmvaa 12,140,60\nQe 3\ns_add_u32 m0, s61, 99072\nQzvav 128,148,20\nbuffer_load_dwordx4 v69, s[40:43], s90 offen nt lds\nQzvav 132,156,20\n"
"Qk 25,2,3\nQzvav 136,164,20\nQe 4\nbuffer_load_dwordx4 v69, s[40:43], s60 offen offset:1024 nt lds\nQzvav 140,172,20\nQe 5\nQcvaa 0,112,64,56\nQe 6\n"
"Qmvaa 0,116,68\nQe 7\nQmvaa 0,120,72\nQk 26,4,5\nQk 27,6,7\nQmvaa 0,124,76\nbuffer_load_dwordx4 v69, s[40:43], s90 offen offset:2048 nt lds\nQcvaa 4,128,64,56\n"
"Qe 8\nQmvaa 4,132,68\nQe 9\nQmvaa 4,136,72\nQe 10\nQmvaa 4,140,76\nQe 11\nbuffer_load_dwordx4 v69, s[40:43], s60 offen offset:3072 nt lds\n"
"Qzvav 160,148,24\nQk 28,8,9\nQk 29,10,11\nQzvav 164,156,24\nQe 12\ns_add_u32 m0, s61, 107264\nQzvav 168,164,24\nbuffer_load_dwordx4 v72, s[44:47], s90 offen nt lds\n"
"Qzvav 172,172,24\nQe 13\nQcvaa 8,112,80,56\nQe 14\nQmvaa 8,116,84\nQe 15\nQmvaa 8,120,88\nbuffer_load_dwordx4 v72, s[44:47], s60 offen offset:1024 nt lds\n"
"Qmvaa 8,124,92\nQk 30,12,13\nQk 31,14,15\nQcvaa 12,128,80,56\nQe 0\nQmvaa 12,132,84\nQe 1\nv_smfmac_f32_16x16x64_bf16 a[180:183], a[204:207], v[24:31], v77\n"
"Qk 32,0,1\nQmvaa 12,136,88\nbuffer_load_dwordx4 v72, s[44:47], s90 offen offset:2048 nt lds\nQmvaa 12,140,92\nQe 2\nQzvav 192,148,28\nQe 3\ns_waitcnt vmcnt(15)\n"
"s_barrier\nQzvav 196,156,28\nbuffer_load_dwordx4 v72, s[44:47], s60 offen offset:3072 nt lds\nQzvav 200,164,28\nQk 33,2,3\ns_add_u32 s60, s60, 8192\ns_and_b32 s68, s60, 0x40000\ns_mul_i32 s68, s68, 7\n"
"s_add_u32 s60, s60, s68\ns_add_u32 s90, s60, 896\nQzvav 204,172,28\nQe 4\nQcvaa 0,112,96,56\nQda 112,68,33024\nQmvaa 0,116,100\nQda 116,68,33536\n"
"Qmvaa 0,120,104\nQda 120,68,34048\nQe 5\nQe 6\nQe 7\nQmvaa 0,124,108\nQda 124,68,34560\nQk 34,4,5\n"
"Qk 35,6,7\nQcvaa 4,128,96,56\nQda 128,68,35072\nQmvaa 4,132,100\nQda 132,68,35584\nQmvaa 4,136,104\nQda 136,68,36096\nQmvaa 4,140,108\n"
"Qda 140,68,36608\nQzvav 224,148,32\ns_memrealtime s[74:75]\nQzvav 228,156,32\nQe 8\nQzvav 232,164,32\nQe 9\nQe 10\n"
"Qe 11\nQzvav 236,172,32\ns_waitcnt lgkmcnt(0)\nQk 36,8,9\nQk 37,10,11\nQcvaa 8,112,0,56\nQe 12\nQmvaa 8,116,4\n"
"v_add_u32 v68, 66048, v68\nQmvaa 8,120,8\nQe 13\nQe 14\nQe 15\nQmvaa 8,124,12\nQk 38,12,13\nQk 39,14,15\n"
"Qcvaa 12,128,0,56\nQe 0\nQmvaa 12,132,4\nQe 1\nv_smfmac_f32_16x16x64_bf16 a[184:187], a[204:207], v[32:39], v77\nQk 40,0,1\nQmvaa 12,136,8\nQe 2\n"
"Qmvaa 12,140,12\nQe 3\ns_waitcnt lgkmcnt(0)\nQzaav 208,148,36\ns_mov_b32 s73, 140\nQk 41,2,3\ns_add_u32 s77, s74, s73\nQzaav 212,156,36\n"
"s_sub_u32 s73, s73, 4\ns_max_u32 s73, s73, 120\nQzaav 216,164,36\nQe 4\nQzaav 220,172,36\nQe 8\nQcvaa 0,112,16,56\nQe 9\n"
"Qmvaa 0,116,20\nQe 10\nQmvaa 0,120,24\nQe 11\nQe 5\nQe 6\nQe 7\nQmvaa 0,124,28\n"
"Qk 44,8,9\nQk 45,10,11\nQk 42,4,5\nQk 43,6,7\nQcvaa 4,128,16,56\nQe 12\nQmvaa 4,132,20\nQe 13\n"
"Qmvaa 4,136,24\nQe 14\nQmvaa 4,140,28\nQe 15\nQta 144,70,0\nQta 146,70,2048\nQzaav 240,148,40\nQta 148,70,33024\n"
"Qta 150,70,35072\nQta 152,70,256\nQta 154,70,2304\nQzaav 244,156,40\nQta 156,70,33280\nQta 158,70,35328\nQta 160,70,512\nQta 162,70,2560\n"
"Qzaav 248,164,40\nQta 164,70,33536\nQta 166,70,35584\nQta 168,70,768\nQta 170,70,2816\nQzaav 252,172,40\nQta 172,70,33792\nQta 174,70,35840\n"
"Qcvaa 8,112,32,56\nv_add_u32 v75, 1, v75\nQk 46,12,13\nQmvaa 8,116,36\ns_waitcnt vmcnt(8)\nds_write_b32 v73, v75\nQmvaa 8,120,40\nQk 47,14,15\n"
"Qmvaa 8,124,44\nQe 0\nQcvaa 12,128,32,56\nQe 1\nQmvaa 12,132,36\nQe 2\nv_smfmac_f32_16x16x64_bf16 a[188:191], a[204:207], v[40:47], v77\nQk 48,0,1\n"
"Qmvaa 12,136,40\ns_waitcnt lgkmcnt(1)\nQmvaa 12,140,44\nQe 3\nQzvav 80,144,16\nQk 49,2,3\nQmvav 80,148,44\nQe 4\n"
"Qzvav 84,152,16\nQe 5\nQmvav 84,156,44\nQe 6\nQzvav 88,160,16\nQe 7\nQmvav 88,164,44\nQk 50,4,5\n"
"Qk 51,6,7\nQzvav 92,168,16\nQe 8\nQmvav 92,172,44\nQe 9\nQcvaa 0,112,48,56\nQe 10\nQmvaa 0,116,52\n"
"ds_read_b32 v66, v74\nQk 52,8,9\nQmvaa 0,120,56\nQe 11\nQmvaa 0,124,60\nQk 53,10,11\nQcvaa 4,128,48,56\nQe 12\n"
"Qmvaa 4,132,52\nQe 13\nQmvaa 4,136,56\ns_waitcnt lgkmcnt(0)\nv_cmp_gt_u32 vcc, v75, v66\nQmvaa 4,140,60\ns_cbranch_vccnz .Lsa1\n.Lra1:\n"
"Qk 54,12,13\nQzvav 112,144,20\ns_add_u32 m0, s61, 0\nQmvav 112,148,48\nbuffer_load_dwordx4 v69, s[40:43], s90 offen nt lds\nQzvav 116,152,20\nQe 14\nQmvav 116,156,48\n"
"Qe 15\nQzvav 120,160,20\nQk 55,14,15\nQmvav 120,164,48\nQe 0\nQzvav 124,168,20\nbuffer_load_dwordx4 v69, s[40:43], s60 offen offset:1024 nt lds\nQmvav 124,172,48\n"
"Qe 1\nQcvaa 8,112,64,56\nQe 2\nQmvaa 8,116,68\nQe 3\nQmvaa 8,120,72\nQk 20,0,1\nQk 21,2,3\n"
"Qmvaa 8,124,76\nbuffer_load_dwordx4 v69, s[40:43], s90 offen offset:2048 nt lds\nQcvaa 12,128,64,56\nQe 4\nQmvaa 12,132,68\nQe 5\nv_smfmac_f32_16x16x64_bf16 a[192:195], a[204:207], v[48:55], v77\nQk 22,4,5\n"
"Qmvaa 12,136,72\nQe 6\nQmvaa 12,140,76\nbuffer_load_dwordx4 v69, s[40:43], s60 offen offset:3072 nt lds\nQzvav 144,144,24\nQe 7\nQmvav 144,148,52\nQk 23,6,7\n"
"Qzvav 148,152,24\nQe 8\nQmvav 148,156,52\ns_add_u32 m0, s61, 8192\nQzvav 152,160,24\nbuffer_load_dwordx4 v72, s[44:47], s90 offen nt lds\nQmvav 152,164,52\nQe 9\n"
"Qzvav 156,168,24\nQe 10\nQmvav 156,172,52\nQe 11\nQcvaa 0,112,80,56\nQk 24,8,9\nQk 25,10,11\nQmvaa 0,116,84\n"
"buffer_load_dwordx4 v72, s[44:47], s60 offen offset:1024 nt lds\nQmvaa 0,120,88\nQe 12\nQmvaa 0,124,92\nQe 13\nQcvaa 4,128,80,56\nQe 14\nQmvaa 4,132,84\n"
"Qe 15\nQmvaa 4,136,88\nbuffer_load_dwordx4 v72, s[44:47], s90 offen offset:2048 nt lds\nQmvaa 4,140,92\nQk 26,12,13\nQk 27,14,15\nQzvav 176,144,28\nQmvav 176,148,20\n"
"Qzvav 180,152,28\nQmvav 180,156,20\nbuffer_load_dwordx4 v72, s[44:47], s60 offen offset:3072 nt lds\nQzvav 184,160,28\nQe 0\nQmvav 184,164,20\nQe 1\nQzvav 188,168,28\n"
"s_add_u32 s60, s60, 8192\ns_add_u32 s90, s60, 896\nQmvav 188,172,20\n.Lpg5:\ns_cbranch_execz .Ltr9\nQe 2\nQcvaa 8,112,96,56\nQda 112,68,0\n"
"Qk 28,0,1\nQmvaa 8,116,100\nQda 116,68,512\nQmvaa 8,120,104\nQda 120,68,1024\nQmvaa 8,124,108\nQda 124,68,1536\nQcvaa 12,128,96,56\n"
"Qda 128,68,2048\nQmvaa 12,132,100\nQda 132,68,2560\nv_smfmac_f32_16x16x64_bf16 a[196:199], a[204:207], v[20:27], v77\nQmvaa 12,136,104\nQda 136,68,3072\nQmvaa 12,140,108\nQda 140,68,3584\n"
"Qzvav 208,144,32\ns_memrealtime s[74:75]\nQmvav 208,148,24\nQe 3\nQzvav 212,152,32\nQk 29,2,3\nQmvav 212,156,24\nQe 4\n"
"Qzvav 216,160,32\nQe 5\nQmvav 216,164,24\nQe 6\nQzvav 220,168,32\nQe 7\nQmvav 220,172,24\ns_waitcnt lgkmcnt(0)\n"
"Qk 30,4,5\nQcvaa 0,112,0,56\nQk 31,6,7\nQmvaa 0,116,4\nQe 8\nQmvaa 0,120,8\nQe 9\nQmvaa 0,124,12\n"
"Qe 10\nQcvaa 4,128,0,56\nQe 11\nQmvaa 4,132,4\nQk 32,8,9\nQk 33,10,11\nQmvaa 4,136,8\nQe 12\n"
"Qmvaa 4,140,12\nQe 13\nQzvav 240,144,36\nQe 14\nQmvav 240,148,28\ns_waitcnt lgkmcnt(0)\ns_sub_u32 s75, s77, s74\nQzvav 244,152,36\n"
"s_cmp_lt_u32 s75, 256\ns_cbranch_scc1 .Lsp1\n.Lrp1:\nQmvav 244,156,28\ns_add_u32 s77, s74, s73\nQk 34,12,13\nQzvav 248,160,36\ns_sub_u32 s73, s73, 4\n"
"Qmvav 248,164,28\ns_max_u32 s73, s73, 120\nQzvav 252,168,36\nQe 15\nQmvav 252,172,28\nQk 35,14,15\nQcvaa 8,112,16,56\nQe 0\n"
"Qmvaa 8,116,20\nQe 1\nQmvaa 8,120,24\nQe 2\nQmvaa 8,124,28\nQe 3\nQcvaa 12,128,16,56\nQk 36,0,1\n"
"Qk 37,2,3\nQmvaa 12,132,20\nQe 4\nv_smfmac_f32_16x16x64_bf16 a[200:203], a[204:207], v[28:35], v77\nQmvaa 12,136,24\nQe 5\nQmvaa 12,140,28\nQe 6\n"
"Qzaav 224,144,40\nQta 144,70,34048\nQta 146,70,36096\nQmaav 224,148,32\nQta 148,71,1024\nQta 150,71,3072\nQzaav 228,152,40\nQta 152,70,34304\n"
"Qta 154,70,36352\nQmaav 228,156,32\nQta 156,71,1280\nQta 158,71,3328\nQzaav 232,160,40\nQta 160,70,34560\nQta 162,70,36608\nQmaav 232,164,32\n"
"Qta 164,71,1536\nQta 166,71,3584\nQzaav 236,168,40\nQta 168,70,34816\nQta 170,70,36864\nQmaav 236,172,32\nQta 172,71,1792\nQta 174,71,3840\n"
"s_lshl_b32 s66, s67, 3\n.Lloop:\ns_bitcmp1_b32 s99, s66\ns_cbranch_scc1 .Lhook\n.Lhookret:\nQcvaa 0,112,32,56\nv_add_u32 v75, 1, v75\nQk 38,4,5\n"
"Qmvaa 0,116,36\ns_waitcnt vmcnt(8)\nds_write_b32 v73, v75\nQmvaa 0,120,40\nQe 7\nQmvaa 0,124,44\nQk 39,6,7\nQcvaa 4,128,32,56\n"
"Qe 8\nQmvaa 4,132,36\nQe 9\nQmvaa 4,136,40\nQe 10\nQmvaa 4,140,44\ns_waitcnt lgkmcnt(1)\nQk 40,8,9\n"
"Qmvav 96,144,44\nQe 11\nQmvav 96,148,36\nQk 41,10,11\nQmvav 100,152,44\nQe 12\nQmvav 100,156,36\nQe 13\n"
"Qmvav 104,160,44\nQe 14\nQmvav 104,164,36\nQe 15\nQmvav 108,168,44\nQk 42,12,13\nQk 43,14,15\nQmvav 108,172,36\n"
"Qe 0\nQcvaa 8,112,48,56\nQe 1\nQmvaa 8,116,52\nQe 2\nQmvaa 8,120,56\nds_read_b32 v66, v74\nQk 44,0,1\n"
"Qmvaa 8,124,60\nQe 3\nQcvaa 12,128,48,56\nQk 45,2,3\nQmvaa 12,132,52\nQe 4\nv_smfmac_f32_16x16x64_bf16 a[176:179], a[204:207], v[36:43], v77\nQmvaa 12,136,56\n"
"s_waitcnt lgkmcnt(0)\nv_cmp_gt_u32 vcc, v75, v66\nQmvaa 12,140,60\ns_cbranch_vccnz .Lsa2\n.Lra2:\nQmvav 128,144,48\ns_add_u32 m0, s61, 33024\nQmvav 128,148,40\n"
"buffer_load_dwordx4 v69, s[40:43], s90 offen nt lds\nQmvav 132,152,48\nQe 5\nQmvav 132,156,40\nQe 6\nQmvav 136,160,48\nQe 7\nQmvav 136,164,40\n"
"Qk 46,4,5\nQk 47,6,7\nQmvav 140,168,48\nbuffer_load_dwordx4 v69, s[40:43], s60 offen offset:1024 nt lds\nQmvav 140,172,40\nQe 8\nQcvaa 0,112,64,56\nQe 9\n"
"Qmvaa 0,116,68\nQe 10\nQmvaa 0,120,72\nQe 11\nQmvaa 0,124,76\nbuffer_load_dwordx4 v69, s[40:43], s90 offen offset:2048 nt lds\nQcvaa 4,128,64,56\nQk 48,8,9\n"
"Qk 49,10,11\nQmvaa 4,132,68\nQe 12\nQmvaa 4,136,72\nQe 13\nQmvaa 4,140,76\nQe 14\nQmvav 160,144,52\n"
"buffer_load_dwordx4 v69, s[40:43], s60 offen offset:3072 nt lds\nQmvav 160,148,44\nQe 15\nQmvav 164,152,52\nQk 50,12,13\nQk 51,14,15\nQmvav 164,156,44\nQe 0\n"
"Qmvav 168,160,52\ns_add_u32 m0, s61, 41216\nQmvav 168,164,44\nbuffer_load_dwordx4 v72, s[44:47], s90 offen nt lds\nQmvav 172,168,52\nQe 1\nQmvav 172,172,44\nQe 2\n"
"Qcvaa 8,112,80,56\nQe 3\nQmvaa 8,116,84\nQk 16,0,1\nQk 17,2,3\nQmvaa 8,120,88\nbuffer_load_dwordx4 v72, s[44:47], s60 offen offset:1024 nt lds\nQmvaa 8,124,92\n"
"Qe 4\nQcvaa 12,128,80,56\nQe 5\nQmvaa 12,132,84\nQe 6\nv_smfmac_f32_16x16x64_bf16 a[180:183], a[204:207], v[44:51], v77\nQk 18,4,5\nQmvaa 12,136,88\n"
"buffer_load_dwordx4 v72, s[44:47], s90 offen offset:2048 nt lds\nQmvaa 12,140,92\nQe 7\nQmvav 192,144,20\nQk 19,6,7\nQmvav 192,148,48\nQmvav 196,152,20\nQe 8\n"
"Qmvav 196,156,48\nbuffer_load_dwordx4 v72, s[44:47], s60 offen offset:3072 nt lds\nQmvav 200,160,20\nQe 9\nQmvav 200,164,48\nQe 10\nQmvav 204,168,20\ns_add_u32 s60, s60, 8192\n"
"s_add_u32 s90, s60, 896\nQmvav 204,172,48\nQe 11\nQcvaa 0,112,96,56\nQda 112,68,33024\nQk 20,8,9\nQmvaa 0,116,100\nQda 116,68,33536\n"
"Qk 21,10,11\nQmvaa 0,120,104\nQda 120,68,34048\nQmvaa 0,124,108\nQda 124,68,34560\nQcvaa 4,128,96,56\nQda 128,68,35072\nQmvaa 4,132,100\n"
"Qda 132,68,35584\nQmvaa 4,136,104\nQda 136,68,36096\nQmvaa 4,140,108\nQda 140,68,36608\nQmvav 224,144,24\nQe 12\nQmvav 224,148,16\n"
"s_memrealtime s[74:75]\nQmvav 228,152,24\nQe 13\nQmvav 228,156,16\nQe 14\nQmvav 232,160,24\nQe 15\nQmvav 232,164,16\n"
"Qk 22,12,13\nQk 23,14,15\nQmvav 236,168,24\nQe 0\nQmvav 236,172,16\ns_waitcnt lgkmcnt(0)\nQcvaa 8,112,0,56\nQe 1\n"
"Qmvaa 8,116,4\nv_subrev_u32 v68, 66048, v68\nQk 24,0,1\nQmvaa 8,120,8\nQe 2\nQmvaa 8,124,12\nQe 3\nQcvaa 12,128,0,56\n"
"Qk 25,2,3\nQmvaa 12,132,4\nQe 4\nv_smfmac_f32_16x16x64_bf16 a[184:187], a[204:207], v[16:23], v77\nQmvaa 12,136,8\nQe 5\nQmvaa 12,140,12\nQe 6\n"
"Qmaav 208,144,28\ns_waitcnt lgkmcnt(0)\ns_sub_u32 s75, s77, s74\nQmaav 208,148,20\ns_cmp_lt_u32 s75, 256\ns_cbranch_scc1 .Lsp2\n.Lrp2:\nQmaav 212,152,28\n"
"s_add_u32 s77, s74, s73\nQk 26,4,5\nQmaav 212,156,20\ns_sub_u32 s73, s73, 4\nQmaav 216,160,28\ns_max_u32 s73, s73, 120\nQmaav 216,164,20\nQe 7\n"
"Qmaav 220,168,28\nQk 27,6,7\nQmaav 220,172,20\nQe 8\nQcvaa 0,112,16,56\nQe 9\nQmvaa 0,116,20\nQe 10\n"
"Qmvaa 0,120,24\nQe 11\nQmvaa 0,124,28\nQk 28,8,9\nQk 29,10,11\nQcvaa 4,128,16,56\nQe 12\nQmvaa 4,132,20\n"
"Qe 13\nQmvaa 4,136,24\nQe 14\nQmvaa 4,140,28\nQe 15\nQmaav 240,144,32\nQta 144,71,0\nQta 146,71,2048\n"
"Qmaav 240,148,24\nQta 148,71,33024\nQta 150,71,35072\nQmaav 244,152,32\nQta 152,71,256\nQta 154,71,2304\nQmaav 244,156,24\nQta 156,71,33280\n"
"Qta 158,71,35328\nQmaav 248,160,32\nQta 160,71,512\nQta 162,71,2560\nQmaav 248,164,24\nQta 164,71,33536\nQta 166,71,35584\nQmaav 252,168,32\n"
"Qta 168,71,768\nQta 170,71,2816\nQmaav 252,172,24\nQta 172,71,33792\nQta 174,71,35840\nQcvaa 8,112,32,56\nv_add_u32 v75, 1, v75\nQk 30,12,13\n"
"Qmvaa 8,116,36\ns_waitcnt vmcnt(8)\nds_write_b32 v73, v75\nQmvaa 8,120,40\nQk 31,14,15\nQmvaa 8,124,44\nQe 0\nQcvaa 12,128,32,56\n"
"Qe 1\nQmvaa 12,132,36\nQe 2\nv_smfmac_f32_16x16x64_bf16 a[188:191], a[204:207], v[24:31], v77\nQk 32,0,1\nQmvaa 12,136,40\ns_waitcnt lgkmcnt(1)\nQmvaa 12,140,44\n"
"Qe 3\nQmvav 80,144,36\nQk 33,2,3\nQmvav 80,148,28\nQe 4\nQmvav 84,152,36\nQe 5\nQmvav 84,156,28\n"
"Qe 6\nQmvav 88,160,36\nQe 7\nQmvav 88,164,28\nQk 34,4,5\nQk 35,6,7\nQmvav 92,168,36\nQe 8\n"
"Qmvav 92,172,28\nQe 9\nQcvaa 0,112,48,56\nQe 10\nQmvaa 0,116,52\nds_read_b32 v66, v74\nQk 36,8,9\nQmvaa 0,120,56\n"
"Qe 11\nQmvaa 0,124,60\nQk 37,10,11\nQcvaa 4,128,48,56\nQe 12\nQmvaa 4,132,52\nQe 13\nQmvaa 4,136,56\n"
"s_waitcnt lgkmcnt(0)\nv_cmp_gt_u32 vcc, v75, v66\nQmvaa 4,140,60\ns_cbranch_vccnz .Lsa3\n.Lra3:\nQk 38,12,13\nQmvav 112,144,40\ns_add_u32 m0, s61, 66048\n"
"Qmvav 112,148,32\nbuffer_load_dwordx4 v69, s[40:43], s90 offen nt lds\nQmvav 116,152,40\nQe 14\nQmvav 116,156,32\nQe 15\nQmvav 120,160,40\nQk 39,14,15\n"
"Qmvav 120,164,32\nQe 0\nQmvav 124,168,40\nbuffer_load_dwordx4 v69, s[40:43], s60 offen offset:1024 nt lds\nQmvav 124,172,32\nQe 1\nQcvaa 8,112,64,56\nQe 2\n"
"Qmvaa 8,116,68\nQe 3\nQmvaa 8,120,72\nQk 40,0,1\nQk 41,2,3\nQmvaa 8,124,76\nbuffer_load_dwordx4 v69, s[40:43], s90 offen offset:2048 nt lds\nQcvaa 12,128,64,56\n"
"Qe 4\nQmvaa 12,132,68\nQe 5\nv_smfmac_f32_16x16x64_bf16 a[192:195], a[204:207], v[32:39], v77\nQk 42,4,5\nQmvaa 12,136,72\nQe 6\nQmvaa 12,140,76\n"
"buffer_load_dwordx4 v69, s[40:43], s60 offen offset:3072 nt lds\nQmvav 144,144,44\nQe 7\nQmvav 144,148,36\nQk 43,6,7\nQmvav 148,152,44\nQe 8\nQmvav 148,156,36\n"
"s_add_u32 m0, s61, 74240\nQmvav 152,160,44\nbuffer_load_dwordx4 v72, s[44:47], s90 offen nt lds\nQmvav 152,164,36\nQe 9\nQmvav 156,168,44\nQe 10\nQmvav 156,172,36\n"
"Qe 11\nQcvaa 0,112,80,56\nQk 44,8,9\nQk 45,10,11\nQmvaa 0,116,84\nbuffer_load_dwordx4 v72, s[44:47], s60 offen offset:1024 nt lds\nQmvaa 0,120,88\nQe 12\n"
"Qmvaa 0,124,92\nQe 13\nQcvaa 4,128,80,56\nQe 14\nQmvaa 4,132,84\nQe 15\nQmvaa 4,136,88\nbuffer_load_dwordx4 v72, s[44:47], s90 offen offset:2048 nt lds\n"
"Qmvaa 4,140,92\nQk 46,12,13\nQk 47,14,15\nQmvav 176,144,48\nQmvav 176,148,40\nQmvav 180,152,48\nQmvav 180,156,40\nbuffer_load_dwordx4 v72, s[44:47], s60 offen offset:3072 nt lds\n"
"Qmvav 184,160,48\nQe 0\nQmvav 184,164,40\nQe 1\nQmvav 188,168,48\ns_add_u32 s60, s60, 8192\ns_add_u32 s90, s60, 896\nQmvav 188,172,40\n"
"Qe 2\nQcvaa 8,112,96,56\nQda 112,68,0\nQk 48,0,1\nQmvaa 8,116,100\nQda 116,68,512\nQmvaa 8,120,104\nQda 120,68,1024\n"
"Qmvaa 8,124,108\nQda 124,68,1536\nQcvaa 12,128,96,56\nQda 128,68,2048\nQmvaa 12,132,100\nQda 132,68,2560\nv_smfmac_f32_16x16x64_bf16 a[196:199], a[204:207], v[40:47], v77\nQmvaa 12,136,104\n"
"Qda 136,68,3072\nQmvaa 12,140,108\nQda 140,68,3584\nQmvav 208,144,16\ns_memrealtime s[74:75]\nQmvav 208,148,44\nQe 3\nQmvav 212,152,16\n"
"Qk 49,2,3\nQmvav 212,156,44\nQe 4\nQmvav 216,160,16\nQe 5\nQmvav 216,164,44\nQe 6\nQmvav 220,168,16\n"
"Qe 7\nQmvav 220,172,44\ns_waitcnt lgkmcnt(0)\nQk 50,4,5\nQcvaa 0,112,0,56\nQk 51,6,7\nQmvaa 0,116,4\nQe 8\n"
"Qmvaa 0,120,8\nQe 9\nQmvaa 0,124,12\nQe 10\nQcvaa 4,128,0,56\nQe 11\nQmvaa 4,132,4\nQk 52,8,9\n"
"Qk 53,10,11\nQmvaa 4,136,8\nQe 12\nQmvaa 4,140,12\nQe 13\nQmvav 240,144,20\nQe 14\nQmvav 240,148,48\n"
"s_waitcnt lgkmcnt(0)\ns_sub_u32 s75, s77, s74\nQmvav 244,152,20\ns_cmp_lt_u32 s75, 256\ns_cbranch_scc1 .Lsp3\n.Lrp3:\nQmvav 244,156,48\ns_add_u32 s77, s74, s73\n"
"Qk 54,12,13\nQmvav 248,160,20\ns_sub_u32 s73, s73, 4\nQmvav 248,164,48\ns_max_u32 s73, s73, 120\nQmvav 252,168,20\nQe 15\nQmvav 252,172,48\n"
"Qk 55,14,15\nQcvaa 8,112,16,56\nQe 0\nQmvaa 8,116,20\nQe 1\nQmvaa 8,120,24\nQe 2\nQmvaa 8,124,28\n"
"Qe 3\nQcvaa 12,128,16,56\nQk 16,0,1\nQk 17,2,3\nQmvaa 12,132,20\n.Lpg6:\ns_cbranch_execz .Ltr9\nQe 4\n"
"v_smfmac_f32_16x16x64_bf16 a[200:203], a[204:207], v[48:55], v77\nQmvaa 12,136,24\nQe 5\nQmvaa 12,140,28\nQe 6\nQmaav 224,144,24\nQta 144,71,34048\nQta 146,71,36096\n"
"Qmaav 224,148,52\nQta 148,70,1024\nQta 150,70,3072\nQmaav 228,152,24\nQta 152,71,34304\nQta 154,71,36352\nQmaav 228,156,52\nQta 156,70,1280\n"
"Qta 158,70,3328\nQmaav 232,160,24\nQta 160,71,34560\nQta 162,71,36608\nQmaav 232,164,52\nQta 164,70,1536\nQta 166,70,3584\nQmaav 236,168,24\n"
"Qta 168,71,34816\nQta 170,71,36864\nQmaav 236,172,52\nQta 172,70,1792\nQta 174,70,3840\nQcvaa 0,112,32,56\nv_add_u32 v75, 1, v75\nQk 18,4,5\n"
"Qmvaa 0,116,36\ns_waitcnt vmcnt(8)\nds_write_b32 v73, v75\nQmvaa 0,120,40\nQe 7\nQmvaa 0,124,44\nQk 19,6,7\nQcvaa 4,128,32,56\n"
"Qe 8\nQmvaa 4,132,36\nQe 9\nQmvaa 4,136,40\nQe 10\nQmvaa 4,140,44\ns_waitcnt lgkmcnt(1)\nQk 20,8,9\n"
"Qmvav 96,144,28\nQe 11\nQmvav 96,148,16\nQk 21,10,11\nQmvav 100,152,28\nQe 12\nQmvav 100,156,16\nQe 13\n"
"Qmvav 104,160,28\nQe 14\nQmvav 104,164,16\nQe 15\nQmvav 108,168,28\nQk 22,12,13\nQk 23,14,15\nQmvav 108,172,16\n"
"Qe 0\nQcvaa 8,112,48,56\nQe 1\nQmvaa 8,116,52\nQe 2\nQmvaa 8,120,56\nds_read_b32 v66, v74\nQk 24,0,1\n"
"Qmvaa 8,124,60\nQe 3\nQcvaa 12,128,48,56\nQk 25,2,3\nQmvaa 12,132,52\nQe 4\nv_smfmac_f32_16x16x64_bf16 a[176:179], a[204:207], v[16:23], v77\nQmvaa 12,136,56\n"
"s_waitcnt lgkmcnt(0)\nv_cmp_gt_u32 vcc, v75, v66\nQmvaa 12,140,60\ns_cbranch_vccnz .Lsa4\n.Lra4:\nQmvav 128,144,32\ns_add_u32 m0, s61, 99072\nQmvav 128,148,20\n"
"buffer_load_dwordx4 v69, s[40:43], s90 offen nt lds\nQmvav 132,152,32\nQe 5\nQmvav 132,156,20\nQe 6\nQmvav 136,160,32\nQe 7\nQmvav 136,164,20\n"
"Qk 26,4,5\nQk 27,6,7\nQmvav 140,168,32\nbuffer_load_dwordx4 v69, s[40:43], s60 offen offset:1024 nt lds\nQmvav 140,172,20\nQe 8\nQcvaa 0,112,64,56\nQe 9\n"
"Qmvaa 0,116,68\nQe 10\nQmvaa 0,120,72\nQe 11\nQmvaa 0,124,76\nbuffer_load_dwordx4 v69, s[40:43], s90 offen offset:2048 nt lds\nQcvaa 4,128,64,56\nQk 28,8,9\n"
"Qk 29,10,11\nQmvaa 4,132,68\nQe 12\nQmvaa 4,136,72\nQe 13\nQmvaa 4,140,76\nQe 14\nQmvav 160,144,36\n"
"buffer_load_dwordx4 v69, s[40:43], s60 offen offset:3072 nt lds\nQmvav 160,148,24\nQe 15\nQmvav 164,152,36\nQk 30,12,13\nQk 31,14,15\nQmvav 164,156,24\nQe 0\n"
"Qmvav 168,160,36\ns_add_u32 m0, s61, 107264\nQmvav 168,164,24\nbuffer_load_dwordx4 v72, s[44:47], s90 offen nt lds\nQmvav 172,168,36\nQe 1\nQmvav 172,172,24\nQe 2\n"
"Qcvaa 8,112,80,56\nQe 3\nQmvaa 8,116,84\nQk 32,0,1\nQk 33,2,3\nQmvaa 8,120,88\nbuffer_load_dwordx4 v72, s[44:47], s60 offen offset:1024 nt lds\nQmvaa 8,124,92\n"
"Qe 4\nQcvaa 12,128,80,56\nQe 5\nQmvaa 12,132,84\nQe 6\nv_smfmac_f32_16x16x64_bf16 a[180:183], a[204:207], v[24:31], v77\nQk 34,4,5\nQmvaa 12,136,88\n"
"buffer_load_dwordx4 v72, s[44:47], s90 offen offset:2048 nt lds\nQmvaa 12,140,92\nQe 7\nQmvav 192,144,40\nQk 35,6,7\nQmvav 192,148,28\nQmvav 196,152,40\nQe 8\n"
"Qmvav 196,156,28\nbuffer_load_dwordx4 v72, s[44:47], s60 offen offset:3072 nt lds\nQmvav 200,160,40\nQe 9\nQmvav 200,164,28\nQe 10\nQmvav 204,168,40\ns_add_u32 s60, s60, 8192\n"
"s_and_b32 s68, s60, 0x40000\ns_mul_i32 s68, s68, 7\ns_add_u32 s60, s60, s68\ns_add_u32 s90, s60, 896\nQmvav 204,172,28\nQe 11\nQcvaa 0,112,96,56\nQda 112,68,33024\n"
"Qk 36,8,9\nQmvaa 0,116,100\nQda 116,68,33536\nQk 37,10,11\nQmvaa 0,120,104\nQda 120,68,34048\nQmvaa 0,124,108\nQda 124,68,34560\n"
"Qcvaa 4,128,96,56\nQda 128,68,35072\nQmvaa 4,132,100\nQda 132,68,35584\nQmvaa 4,136,104\nQda 136,68,36096\nQmvaa 4,140,108\nQda 140,68,36608\n"
"Qmvav 224,144,44\nQe 12\nQmvav 224,148,32\ns_memrealtime s[74:75]\nQmvav 228,152,44\nQe 13\nQmvav 228,156,32\nQe 14\n"
"Qmvav 232,160,44\nQe 15\nQmvav 232,164,32\nQk 38,12,13\nQk 39,14,15\nQmvav 236,168,44\nQe 0\nQmvav 236,172,32\n"
"s_waitcnt lgkmcnt(0)\nQcvaa 8,112,0,56\nQe 1\nQmvaa 8,116,4\nv_add_u32 v68, 66048, v68\nQk 40,0,1\nQmvaa 8,120,8\nQe 2\n"
"Qmvaa 8,124,12\nQe 3\nQcvaa 12,128,0,56\nQk 41,2,3\nQmvaa 12,132,4\nQe 4\nv_smfmac_f32_16x16x64_bf16 a[184:187], a[204:207], v[32:39], v77\nQmvaa 12,136,8\n"
"Qe 5\nQmvaa 12,140,12\nQe 6\nQmaav 208,144,48\ns_waitcnt lgkmcnt(0)\ns_sub_u32 s75, s77, s74\nQmaav 208,148,36\ns_cmp_lt_u32 s75, 256\n"
"s_cbranch_scc1 .Lsp4\n.Lrp4:\nQmaav 212,152,48\ns_add_u32 s77, s74, s73\nQk 42,4,5\nQmaav 212,156,36\ns_sub_u32 s73, s73, 4\nQmaav 216,160,48\n"
"s_max_u32 s73, s73, 120\nQmaav 216,164,36\nQe 7\nQmaav 220,168,48\nQk 43,6,7\nQmaav 220,172,36\nQe 8\nQcvaa 0,112,16,56\n"
"Qe 9\nQmvaa 0,116,20\nQe 10\nQmvaa 0,120,24\nQe 11\nQmvaa 0,124,28\nQk 44,8,9\nQk 45,10,11\n"
"Qcvaa 4,128,16,56\nQe 12\nQmvaa 4,132,20\nQe 13\nQmvaa 4,136,24\nQe 14\nQmvaa 4,140,28\nQe 15\n"
"Qmaav 240,144,52\nQta 144,70,0\nQta 146,70,2048\nQmaav 240,148,40\nQta 148,70,33024\nQta 150,70,35072\nQmaav 244,152,52\nQta 152,70,256\n"
"Qta 154,70,2304\nQmaav 244,156,40\nQta 156,70,33280\nQta 158,70,35328\nQmaav 248,160,52\nQta 160,70,512\nQta 162,70,2560\nQmaav 248,164,40\n"
"Qta 164,70,33536\nQta 166,70,35584\nQmaav 252,168,52\nQta 168,70,768\nQta 170,70,2816\nQmaav 252,172,40\nQta 172,70,33792\nQta 174,70,35840\n"
"Qcvaa 8,112,32,56\nv_add_u32 v75, 1, v75\nQk 46,12,13\nQmvaa 8,116,36\ns_waitcnt vmcnt(8)\nds_write_b32 v73, v75\nQmvaa 8,120,40\nQk 47,14,15\n"
"Qmvaa 8,124,44\nQe 0\nQcvaa 12,128,32,56\nQe 1\nQmvaa 12,132,36\nQe 2\nv_smfmac_f32_16x16x64_bf16 a[188:191], a[204:207], v[40:47], v77\nQk 48,0,1\n"
"Qmvaa 12,136,40\ns_waitcnt lgkmcnt(1)\nQmvaa 12,140,44\nQe 3\nQmvav 80,144,16\nQk 49,2,3\nQmvav 80,148,44\nQe 4\n"
"Qmvav 84,152,16\nQe 5\nQmvav 84,156,44\nQe 6\nQmvav 88,160,16\nQe 7\nQmvav 88,164,44\nQk 50,4,5\n"
"Qk 51,6,7\nQmvav 92,168,16\nQe 8\nQmvav 92,172,44\nQe 9\nQcvaa 0,112,48,56\nQe 10\nQmvaa 0,116,52\n"
"ds_read_b32 v66, v74\nQk 52,8,9\nQmvaa 0,120,56\nQe 11\nQmvaa 0,124,60\nQk 53,10,11\nQcvaa 4,128,48,56\nQe 12\n"
"Qmvaa 4,132,52\nQe 13\nQmvaa 4,136,56\ns_waitcnt lgkmcnt(0)\nv_cmp_gt_u32 vcc, v75, v66\nQmvaa 4,140,60\ns_cbranch_vccnz .Lsa5\n.Lra5:\n"
"Qk 54,12,13\nQmvav 112,144,20\ns_add_u32 m0, s61, 0\nQmvav 112,148,48\nbuffer_load_dwordx4 v69, s[40:43], s90 offen nt lds\nQmvav 116,152,20\nQe 14\nQmvav 116,156,48\n"
"Qe 15\nQmvav 120,160,20\nQk 55,14,15\nQmvav 120,164,48\nQe 0\nQmvav 124,168,20\nbuffer_load_dwordx4 v69, s[40:43], s60 offen offset:1024 nt lds\nQmvav 124,172,48\n"
"Qe 1\nQcvaa 8,112,64,56\nQe 2\nQmvaa 8,116,68\nQe 3\nQmvaa 8,120,72\nQk 20,0,1\nQk 21,2,3\n"
"Qmvaa 8,124,76\nbuffer_load_dwordx4 v69, s[40:43], s90 offen offset:2048 nt lds\nQcvaa 12,128,64,56\nQe 4\nQmvaa 12,132,68\nQe 5\nv_smfmac_f32_16x16x64_bf16 a[192:195], a[204:207], v[48:55], v77\nQk 22,4,5\n"
"Qmvaa 12,136,72\nQe 6\nQmvaa 12,140,76\nbuffer_load_dwordx4 v69, s[40:43], s60 offen offset:3072 nt lds\nQmvav 144,144,24\nQe 7\nQmvav 144,148,52\nQk 23,6,7\n"
"Qmvav 148,152,24\nQe 8\nQmvav 148,156,52\ns_add_u32 m0, s61, 8192\nQmvav 152,160,24\nbuffer_load_dwordx4 v72, s[44:47], s90 offen nt lds\nQmvav 152,164,52\nQe 9\n"
"Qmvav 156,168,24\nQe 10\nQmvav 156,172,52\nQe 11\nQcvaa 0,112,80,56\nQk 24,8,9\nQk 25,10,11\nQmvaa 0,116,84\n"
"buffer_load_dwordx4 v72, s[44:47], s60 offen offset:1024 nt lds\nQmvaa 0,120,88\nQe 12\nQmvaa 0,124,92\nQe 13\nQcvaa 4,128,80,56\nQe 14\nQmvaa 4,132,84\n"
"Qe 15\nQmvaa 4,136,88\nbuffer_load_dwordx4 v72, s[44:47], s90 offen offset:2048 nt lds\nQmvaa 4,140,92\nQk 26,12,13\nQk 27,14,15\nQmvav 176,144,28\nQmvav 176,148,20\n"
"Qmvav 180,152,28\nQmvav 180,156,20\nbuffer_load_dwordx4 v72, s[44:47], s60 offen offset:3072 nt lds\nQmvav 184,160,28\nQe 0\nQmvav 184,164,20\nQe 1\nQmvav 188,168,28\n"
"s_add_u32 s60, s60, 8192\ns_add_u32 s90, s60, 896\nQmvav 188,172,20\nQe 2\nQcvaa 8,112,96,56\nQda 112,68,0\nQk 28,0,1\nQmvaa 8,116,100\n"
"Qda 116,68,512\nQmvaa 8,120,104\nQda 120,68,1024\nQmvaa 8,124,108\nQda 124,68,1536\nQcvaa 12,128,96,56\nQda 128,68,2048\nQmvaa 12,132,100\n"
"Qda 132,68,2560\nv_smfmac_f32_16x16x64_bf16 a[196:199], a[204:207], v[20:27], v77\nQmvaa 12,136,104\nQda 136,68,3072\nQmvaa 12,140,108\nQda 140,68,3584\nQmvav 208,144,32\ns_memrealtime s[74:75]\n"
"Qmvav 208,148,24\nQe 3\nQmvav 212,152,32\nQk 29,2,3\nQmvav 212,156,24\nQe 4\nQmvav 216,160,32\nQe 5\n"
"Qmvav 216,164,24\nQe 6\nQmvav 220,168,32\nQe 7\nQmvav 220,172,24\ns_waitcnt lgkmcnt(0)\nQk 30,4,5\nQcvaa 0,112,0,56\n"
"Qk 31,6,7\nQmvaa 0,116,4\nQe 8\nQmvaa 0,120,8\nQe 9\nQmvaa 0,124,12\nQe 10\nQcvaa 4,128,0,56\n"
"Qe 11\nQmvaa 4,132,4\nQk 32,8,9\nQk 33,10,11\nQmvaa 4,136,8\nQe 12\nQmvaa 4,140,12\nQe 13\n"
"Qmvav 240,144,36\nQe 14\nQmvav 240,148,28\ns_waitcnt lgkmcnt(0)\ns_sub_u32 s75, s77, s74\nQmvav 244,152,36\ns_cmp_lt_u32 s75, 256\ns_cbranch_scc1 .Lsp5\n"
".Lrp5:\nQmvav 244,156,28\ns_add_u32 s77, s74, s73\nQk 34,12,13\nQmvav 248,160,36\ns_sub_u32 s73, s73, 4\nQmvav 248,164,28\ns_max_u32 s73, s73, 120\n"
"Qmvav 252,168,36\nQe 15\nQmvav 252,172,28\nQk 35,14,15\nQcvaa 8,112,16,56\nQe 0\nQmvaa 8,116,20\nQe 1\n"
"Qmvaa 8,120,24\nQe 2\nQmvaa 8,124,28\nQe 3\nQcvaa 12,128,16,56\nQk 36,0,1\nQk 37,2,3\nQmvaa 12,132,20\n"
"Qe 4\nv_smfmac_f32_16x16x64_bf16 a[200:203], a[204:207], v[28:35], v77\nQmvaa 12,136,24\nQe 5\nQmvaa 12,140,28\nQe 6\nQmaav 224,144,40\nQta 144,70,34048\n"
"Qta 146,70,36096\nQmaav 224,148,32\nQta 148,71,1024\nQta 150,71,3072\nQmaav 228,152,40\nQta 152,70,34304\nQta 154,70,36352\nQmaav 228,156,32\n"
"Qta 156,71,1280\nQta 158,71,3328\nQmaav 232,160,40\nQta 160,70,34560\nQta 162,70,36608\nQmaav 232,164,32\nQta 164,71,1536\nQta 166,71,3584\n"
"Qmaav 236,168,40\nQta 168,70,34816\nQta 170,70,36864\nQmaav 236,172,32\nQta 172,71,1792\nQta 174,71,3840\ns_add_u32 s66, s66, 1\ns_bitcmp1_b32 0x7f7f, s66\n"
"s_cbranch_scc1 .Lloop\ns_cmp_lg_u32 s67, 0\ns_cbranch_scc1 .Ltail1\nQcvaa 0,112,32,56\nv_add_u32 v75, 1, v75\nQk 38,4,5\nQmvaa 0,116,36\ns_waitcnt vmcnt(8)\n"
"ds_write_b32 v73, v75\nQmvaa 0,120,40\nQe 7\nQmvaa 0,124,44\nQk 39,6,7\nQcvaa 4,128,32,56\nQe 8\nQmvaa 4,132,36\n"
"Qe 9\nQmvaa 4,136,40\nQe 10\nQmvaa 4,140,44\ns_waitcnt lgkmcnt(1)\nQk 40,8,9\nQmvav 96,144,44\nQe 11\n"
"Qmvav 96,148,36\nQk 41,10,11\nQmvav 100,152,44\nQe 12\nQmvav 100,156,36\nQe 13\nQmvav 104,160,44\nQe 14\n"
"Qmvav 104,164,36\nQe 15\nQmvav 108,168,44\nQk 42,12,13\nQk 43,14,15\nQmvav 108,172,36\nQe 0\nQcvaa 8,112,48,56\n"
"Qe 1\nQmvaa 8,116,52\nQe 2\nQmvaa 8,120,56\nds_read_b32 v66, v74\nQk 44,0,1\nQmvaa 8,124,60\nQe 3\n"
"Qcvaa 12,128,48,56\nQk 45,2,3\nQmvaa 12,132,52\n.Lpg7:\ns_cbranch_execz .Ltr9\nQe 4\nv_smfmac_f32_16x16x64_bf16 a[176:179], a[204:207], v[36:43], v77\nQmvaa 12,136,56\n"
"s_waitcnt lgkmcnt(0)\nv_cmp_gt_u32 vcc, v75, v66\nQmvaa 12,140,60\ns_cbranch_vccnz .Lsa30\n.Lra30:\nQmvav 128,144,48\ns_add_u32 m0, s62, 0\ns_add_u32 s69, s87, 0\n"
"Qmvav 128,148,40\nbuffer_load_dwordx4 v62, s[52:55], s69 offen lds\nQmvav 132,152,48\nQe 5\nQmvav 132,156,40\nQe 6\nQmvav 136,160,48\nQe 7\n"
"Qmvav 136,164,40\ns_add_u32 s69, s87, 64504\nQk 46,4,5\nQk 47,6,7\nQmvav 140,168,48\nbuffer_load_dwordx4 v62, s[52:55], s69 offen offset:1032 lds\nQmvav 140,172,40\nQe 8\n"
"Qcvaa 0,112,64,56\nQe 9\nQmvaa 0,116,68\nQe 10\nQmvaa 0,120,72\ns_add_u32 s69, s87, 129008\nQe 11\nQmvaa 0,124,76\n"
"buffer_load_dwordx4 v62, s[52:55], s69 offen offset:2064 lds\nQcvaa 4,128,64,56\nQk 48,8,9\nQk 49,10,11\nQmvaa 4,132,68\nQe 12\nQmvaa 4,136,72\nQe 13\n"
"Qmvaa 4,140,76\ns_add_u32 s69, s87, 193512\nQe 14\nQmvav 160,144,52\nbuffer_load_dwordx4 v62, s[52:55], s69 offen offset:3096 lds\nQmvav 160,148,44\nQe 15\nQmvav 164,152,52\n"
"Qk 50,12,13\nQk 51,14,15\nQmvav 164,156,44\nQe 0\nQmvav 168,160,52\ns_add_u32 m0, s62, 16512\ns_add_u32 s69, s87, 128\nQmvav 168,164,44\n"
"buffer_load_dwordx4 v62, s[52:55], s69 offen lds\nQmvav 172,168,52\nQe 1\nQmvav 172,172,44\nQe 2\nQcvaa 8,112,80,56\nQe 3\nQmvaa 8,116,84\n"
"s_add_u32 s69, s87, 64632\nQk 16,0,1\nQk 17,2,3\nQmvaa 8,120,88\nbuffer_load_dwordx4 v62, s[52:55], s69 offen offset:1032 lds\nQmvaa 8,124,92\nQe 4\nQcvaa 12,128,80,56\n"
"Qe 5\nQmvaa 12,132,84\nQe 6\nv_smfmac_f32_16x16x64_bf16 a[180:183], a[204:207], v[44:51], v77\ns_add_u32 s69, s87, 129136\nQk 18,4,5\nQmvaa 12,136,88\nbuffer_load_dwordx4 v62, s[52:55], s69 offen offset:2064 lds\n"
"Qmvaa 12,140,92\nQe 7\nQmvav 192,144,20\nQk 19,6,7\nQmvav 192,148,48\nQmvav 196,152,20\ns_add_u32 s69, s87, 193640\nQe 8\n"
"Qmvav 196,156,48\nbuffer_load_dwordx4 v62, s[52:55], s69 offen offset:3096 lds\nQmvav 200,160,20\nQe 9\nQmvav 200,164,48\nQe 10\nQmvav 204,168,20\nQmvav 204,172,48\n"
"Qe 11\nQcvaa 0,112,96,56\nQda 112,68,33024\nQk 20,8,9\nQmvaa 0,116,100\nQda 116,68,33536\nQk 21,10,11\nQmvaa 0,120,104\n"
"Qda 120,68,34048\nQmvaa 0,124,108\nQda 124,68,34560\nQcvaa 4,128,96,56\nQda 128,68,35072\nQmvaa 4,132,100\nQda 132,68,35584\nQmvaa 4,136,104\n"
"Qda 136,68,36096\nQmvaa 4,140,108\nQda 140,68,36608\nQmvav 224,144,24\nQe 12\nQmvav 224,148,16\ns_add_u32 m0, s63, 33024\ns_add_u32 s88, s86, 0\n"
"Qmvav 228,152,24\nbuffer_load_dwordx4 v64, s[48:51], s88 offen lds\nQe 13\nQmvav 228,156,16\nQe 14\nQmvav 232,160,24\nQe 15\nQmvav 232,164,16\n"
"s_add_u32 s88, s86, 19456\nQk 22,12,13\nQk 23,14,15\nQmvav 236,168,24\nbuffer_load_dwordx4 v64, s[48:51], s88 offen offset:1024 lds\nQe 0\nQmvav 236,172,16\ns_waitcnt lgkmcnt(0)\n"
"Qcvaa 8,112,0,56\nQe 1\nQmvaa 8,116,4\nv_subrev_u32 v68, 66048, v68\ns_add_u32 s88, s86, 38912\nQk 24,0,1\nQmvaa 8,120,8\nbuffer_load_dwordx4 v64, s[48:51], s88 offen offset:2048 lds\n"
"Qe 2\nQmvaa 8,124,12\nQe 3\nQcvaa 12,128,0,56\nQk 25,2,3\nQmvaa 12,132,4\nQe 4\nv_smfmac_f32_16x16x64_bf16 a[184:187], a[204:207], v[16:23], v77\n"
"s_add_u32 s88, s86, 58368\nQmvaa 12,136,8\nbuffer_load_dwordx4 v64, s[48:51], s88 offen offset:3072 lds\nQe 5\nQmvaa 12,140,12\nQe 6\nQmaav 208,144,28\ns_add_u32 m0, s63, 37120\n"
"s_add_u32 s88, s86, 81920\nQmaav 208,148,20\nbuffer_load_dwordx4 v64, s[48:51], s88 offen lds\nQmaav 212,152,28\nQk 26,4,5\nQmaav 212,156,20\nQmaav 216,160,28\ns_add_u32 s88, s86, 101376\n"
"Qmaav 216,164,20\nbuffer_load_dwordx4 v64, s[48:51], s88 offen offset:1024 lds\nQe 7\nQmaav 220,168,28\nQk 27,6,7\nQmaav 220,172,20\nQe 8\nQcvaa 0,112,16,56\n"
"s_add_u32 s88, s86, 120832\nQe 9\nQmvaa 0,116,20\nbuffer_load_dwordx4 v64, s[48:51], s88 offen offset:2048 lds\nQe 10\nQmvaa 0,120,24\nQe 11\nQmvaa 0,124,28\n"
"Qk 28,8,9\nQk 29,10,11\nQcvaa 4,128,16,56\nQe 12\nQmvaa 4,132,20\nQe 13\nQmvaa 4,136,24\nQe 14\n"
"Qmvaa 4,140,28\nQe 15\nQmaav 240,144,32\nQta 144,71,0\nQta 146,71,2048\nQmaav 240,148,24\nQta 148,71,33024\nQta 150,71,35072\n"
"Qmaav 244,152,32\nQta 152,71,256\nQta 154,71,2304\nQmaav 244,156,24\nQta 156,71,33280\nQta 158,71,35328\nQmaav 248,160,32\nQta 160,71,512\n"
"Qta 162,71,2560\nQmaav 248,164,24\nQta 164,71,33536\nQta 166,71,35584\nQmaav 252,168,32\nQta 168,71,768\nQta 170,71,2816\nQmaav 252,172,24\n"
"Qta 172,71,33792\nQta 174,71,35840\nQcvaa 8,112,32,56\nv_add_u32 v75, 1, v75\nQk 30,12,13\nQmvaa 8,116,36\nds_write_b32 v73, v75\nQmvaa 8,120,40\n"
"Qk 31,14,15\nQmvaa 8,124,44\nQe 0\nQcvaa 12,128,32,56\nQe 1\nQmvaa 12,132,36\nQe 2\nv_smfmac_f32_16x16x64_bf16 a[188:191], a[204:207], v[24:31], v77\n"
"Qk 32,0,1\nQmvaa 12,136,40\ns_waitcnt lgkmcnt(1)\nQmvaa 12,140,44\nQe 3\nQmvav 80,144,36\nQk 33,2,3\nQmvav 80,148,28\n"
"Qe 4\nQmvav 84,152,36\nQe 5\nQmvav 84,156,28\nQe 6\nQmvav 88,160,36\nQe 7\nQmvav 88,164,28\n"
"Qk 34,4,5\nQk 35,6,7\nQmvav 92,168,36\nQe 8\nQmvav 92,172,28\nQe 9\nQcvaa 0,112,48,56\nQe 10\n"
"Qmvaa 0,116,52\nds_read_b32 v66, v74\nQk 36,8,9\nQmvaa 0,120,56\nQe 11\nQmvaa 0,124,60\nQk 37,10,11\nQcvaa 4,128,48,56\n"
"Qe 12\nQmvaa 4,132,52\nQe 13\nQmvaa 4,136,56\ns_waitcnt lgkmcnt(0)\nv_cmp_gt_u32 vcc, v75, v66\nQmvaa 4,140,60\ns_cbranch_vccnz .Lsa31\n"
".Lra31:\nQk 38,12,13\nQmvav 112,144,40\ns_add_u32 m0, s62, 33024\ns_add_u32 s69, s87, 524288\nQmvav 112,148,32\nbuffer_load_dwordx4 v62, s[52:55], s69 offen lds\nQmvav 116,152,40\n"
"Qe 14\nQmvav 116,156,32\nQe 15\nQmvav 120,160,40\nQk 39,14,15\nQmvav 120,164,32\ns_add_u32 s69, s87, 588792\nQe 0\n"
"Qmvav 124,168,40\nbuffer_load_dwordx4 v62, s[52:55], s69 offen offset:1032 lds\nQmvav 124,172,32\nQe 1\nQcvaa 8,112,64,56\nQe 2\nQmvaa 8,116,68\nQe 3\n"
"Qmvaa 8,120,72\ns_add_u32 s69, s87, 653296\nQk 40,0,1\nQk 41,2,3\nQmvaa 8,124,76\nbuffer_load_dwordx4 v62, s[52:55], s69 offen offset:2064 lds\nQcvaa 12,128,64,56\nQe 4\n"
"Qmvaa 12,132,68\nQe 5\nv_smfmac_f32_16x16x64_bf16 a[192:195], a[204:207], v[32:39], v77\nQk 42,4,5\nQmvaa 12,136,72\ns_add_u32 s69, s87, 717800\nQe 6\nQmvaa 12,140,76\n"
"buffer_load_dwordx4 v62, s[52:55], s69 offen offset:3096 lds\nQmvav 144,144,44\nQe 7\nQmvav 144,148,36\nQk 43,6,7\nQmvav 148,152,44\nQe 8\nQmvav 148,156,36\n"
"s_add_u32 m0, s62, 49536\ns_add_u32 s69, s87, 524416\nQmvav 152,160,44\nbuffer_load_dwordx4 v62, s[52:55], s69 offen lds\nQmvav 152,164,36\nQe 9\nQmvav 156,168,44\nQe 10\n"
"Qmvav 156,172,36\nQe 11\nQcvaa 0,112,80,56\ns_add_u32 s69, s87, 588920\nQk 44,8,9\nQk 45,10,11\nQmvaa 0,116,84\nbuffer_load_dwordx4 v62, s[52:55], s69 offen offset:1032 lds\n"
"Qmvaa 0,120,88\nQe 12\nQmvaa 0,124,92\nQe 13\nQcvaa 4,128,80,56\nQe 14\nQmvaa 4,132,84\ns_add_u32 s69, s87, 653424\n"
"Qe 15\nQmvaa 4,136,88\nbuffer_load_dwordx4 v62, s[52:55], s69 offen offset:2064 lds\nQmvaa 4,140,92\nQk 46,12,13\nQk 47,14,15\nQmvav 176,144,48\nQmvav 176,148,40\n"
"Qmvav 180,152,48\ns_add_u32 s69, s87, 717928\nQmvav 180,156,40\nbuffer_load_dwordx4 v62, s[52:55], s69 offen offset:3096 lds\nQmvav 184,160,48\nQe 0\nQmvav 184,164,40\nQe 1\n"
"Qmvav 188,168,48\nQmvav 188,172,40\nQe 2\nQcvaa 8,112,96,56\nQk 48,0,1\nQmvaa 8,116,100\nQmvaa 8,120,104\nQmvaa 8,124,108\n"
"Qcvaa 12,128,96,56\nQmvaa 12,132,100\nv_smfmac_f32_16x16x64_bf16 a[196:199], a[204:207], v[40:47], v77\nQmvaa 12,136,104\nQmvaa 12,140,108\nQmvav 208,144,16\nQta 112,71,34048\nQmvav 208,148,44\n"
"Qta 114,71,36096\nQe 3\nQmvav 212,152,16\nQta 116,71,34304\nQk 49,2,3\nQmvav 212,156,44\nQta 118,71,36352\nQe 4\n"
"Qmvav 216,160,16\nQta 120,71,34560\nQe 5\nQmvav 216,164,44\nQta 122,71,36608\nQe 6\nQmvav 220,168,16\nQta 124,71,34816\n"
"Qe 7\nQmvav 220,172,44\nQta 126,71,36864\nQk 50,4,5\nQk 51,6,7\nQe 8\nQe 9\nQe 10\n"
"Qe 11\nQk 52,8,9\nQk 53,10,11\nQe 12\nQe 13\nQmvav 240,144,20\nv_add_u32 v75, 1, v75\nQe 14\n"
"Qmvav 240,148,48\nds_write_b32 v73, v75\nQmvav 244,152,20\nQmvav 244,156,48\nQk 54,12,13\nQmvav 248,160,20\nQmvav 248,164,48\nQmvav 252,168,20\n"
"Qe 15\nQmvav 252,172,48\nQk 55,14,15\ns_nop 1\nv_smfmac_f32_16x16x64_bf16 a[200:203], a[204:207], v[48:55], v77\nQmaav 224,144,24\nds_read_b32 v66, v74\nv_accvgpr_read_b32 v0, a176\n"
"Qmaav 224,148,52\nv_accvgpr_read_b32 v1, a177\nv_accvgpr_read_b32 v6, a178\nQmaav 228,152,24\nv_accvgpr_read_b32 v7, a179\nv_accvgpr_read_b32 v8, a180\nQmaav 228,156,52\nv_accvgpr_read_b32 v9, a181\n"
"v_accvgpr_read_b32 v14, a182\nQmaav 232,160,24\nv_accvgpr_read_b32 v15, a183\nv_accvgpr_read_b32 v16, a184\nQmaav 232,164,52\ns_waitcnt lgkmcnt(0)\nv_cmp_gt_u32 vcc, v75, v66\nQmaav 236,168,24\n"
"s_cbranch_vccnz .Lsa32\n.Lra32:\nv_accvgpr_read_b32 v17, a185\nQmaav 236,172,52\ns_waitcnt lgkmcnt(2)\nv_accvgpr_read_b32 v22, a186\nQmvav 96,112,28\ns_add_u32 m0, s63, 0\n"
"s_add_u32 s88, s86, 128\nv_accvgpr_read_b32 v23, a187\nQmvav 100,116,28\nbuffer_load_dwordx4 v64, s[48:51], s88 offen lds\nQmvav 104,120,28\nv_accvgpr_read_b32 v24, a188\nv_accvgpr_read_b32 v25, a189\nQmvav 108,124,28\n"
"v_accvgpr_read_b32 v2, a190\nv_accvgpr_read_b32 v3, a191\nQmvav 128,112,32\ns_add_u32 s88, s86, 19584\nv_accvgpr_read_b32 v4, a192\nQmvav 132,116,32\nbuffer_load_dwordx4 v64, s[48:51], s88 offen offset:1024 lds\nQmvav 136,120,32\n"
"v_accvgpr_read_b32 v5, a193\nv_accvgpr_read_b32 v10, a194\nQmvav 140,124,32\nv_accvgpr_read_b32 v11, a195\nv_accvgpr_read_b32 v12, a196\nQmvav 160,112,36\ns_add_u32 s88, s86, 39040\nv_accvgpr_read_b32 v13, a197\n"
"Qmvav 164,116,36\nbuffer_load_dwordx4 v64, s[48:51], s88 offen offset:2048 lds\nQmvav 168,120,36\nv_accvgpr_read_b32 v18, a198\nv_accvgpr_read_b32 v19, a199\nQmvav 172,124,36\nv_accvgpr_read_b32 v20, a200\nv_accvgpr_read_b32 v21, a201\n"
"Qmvav 192,112,40\ns_add_u32 s88, s86, 58496\nv_accvgpr_read_b32 v26, a202\nQmvav 196,116,40\nbuffer_load_dwordx4 v64, s[48:51], s88 offen offset:3072 lds\nQmvav 200,120,40\nv_accvgpr_read_b32 v27, a203\nQa 8,8,9\n"
"Qmvav 204,124,40\nQa 10,10,11\nQa 8,8,10\nQmvav 224,112,44\ns_add_u32 m0, s63, 4096\ns_add_u32 s88, s86, 82048\nQa 16,16,17\nQmvav 228,116,44\n"
"buffer_load_dwordx4 v64, s[48:51], s88 offen lds\nQmvav 232,120,44\nQa 18,18,19\nQa 16,16,18\nQmvav 236,124,44\nQa 4,4,5\nQa 6,6,7\nQmaav 208,112,48\n"
"s_add_u32 s88, s86, 101504\nQa 4,4,6\nQmaav 212,116,48\nbuffer_load_dwordx4 v64, s[48:51], s88 offen offset:1024 lds\nQmaav 216,120,48\nQa 12,12,13\nQa 14,14,15\nQmaav 220,124,48\n"
"Qa 12,12,14\nQa 0,0,1\nQmaav 240,112,52\ns_add_u32 s88, s86, 120960\nQa 2,2,3\nQmaav 244,116,52\nbuffer_load_dwordx4 v64, s[48:51], s88 offen offset:2048 lds\nQmaav 248,120,52\n"
"Qa 0,0,2\nQa 24,24,25\nQmaav 252,124,52\nQa 26,26,27\nQa 24,24,26\nQa 20,20,21\nQa 22,22,23\nQa 20,20,22\n"
"v_max3_u32 v9, v8, v16, v4\nv_max3_u32 v9, v9, v12, v0\nv_max3_u32 v9, v9, v24, v20\nv_min3_u32 v11, v8, v16, v4\nv_min3_u32 v11, v11, v12, v0\nv_min3_u32 v11, v11, v24, v20\nv_cmp_lt_u32 vcc, 0x44800000, v9\ns_mov_b64 s[74:75], vcc\n"
"v_cmp_gt_u32 vcc, 0x2b800000, v11\ns_or_b64 s[74:75], s[74:75], vcc\ns_cmp_lg_u64 s[74:75], 0\ns_cbranch_scc1 .Lfb\n.Lfbret0:\nv_rcp_f32 v49, v24\nv_rcp_f32 v48, v24\nv_rcp_f32 v45, v20\n"
"v_rcp_f32 v44, v20\nv_rcp_f32 v41, v16\nv_rcp_f32 v40, v16\nv_rcp_f32 v37, v12\nv_rcp_f32 v36, v12\nv_rcp_f32 v33, v8\nv_rcp_f32 v32, v8\nv_rcp_f32 v29, v4\n"
"v_rcp_f32 v28, v4\nv_rcp_f32 v25, v0\nv_rcp_f32 v24, v0\ns_nop 0\nv_pk_mul_f32 v[80:81], v[80:81], v[24:25]\nv_pk_mul_f32 v[82:83], v[82:83], v[24:25]\nv_pk_mul_f32 v[84:85], v[84:85], v[24:25]\nv_pk_mul_f32 v[86:87], v[86:87], v[24:25]\n"
"Qk 80,80,81\nQk 81,82,83\nQk 82,84,85\nQk 83,86,87\nv_accvgpr_write_b32 a0, v80\nv_accvgpr_write_b32 a1, v81\nv_accvgpr_write_b32 a2, v82\nv_accvgpr_write_b32 a3, v83\n"
"v_pk_mul_f32 v[88:89], v[88:89], v[24:25]\nv_pk_mul_f32 v[90:91], v[90:91], v[24:25]\nv_pk_mul_f32 v[92:93], v[92:93], v[24:25]\nv_pk_mul_f32 v[94:95], v[94:95], v[24:25]\nQk 88,88,89\nQk 89,90,91\nQk 90,92,93\nQk 91,94,95\n"
"v_accvgpr_write_b32 a4, v88\nv_accvgpr_write_b32 a5, v89\nv_accvgpr_write_b32 a6, v90\nv_accvgpr_write_b32 a7, v91\nv_pk_mul_f32 v[96:97], v[96:97], v[24:25]\nv_pk_mul_f32 v[98:99], v[98:99], v[24:25]\nv_pk_mul_f32 v[100:101], v[100:101], v[24:25]\nv_pk_mul_f32 v[102:103], v[102:103], v[24:25]\n"
"Qk 96,96,97\nQk 97,98,99\nQk 98,100,101\nQk 99,102,103\n.Lpg8:\ns_cbranch_execz .Ltr10\nv_accvgpr_write_b32 a8, v96\nv_accvgpr_write_b32 a9, v97\n"
"v_accvgpr_write_b32 a10, v98\nv_accvgpr_write_b32 a11, v99\nv_pk_mul_f32 v[104:105], v[104:105], v[24:25]\nv_pk_mul_f32 v[106:107], v[106:107], v[24:25]\nv_pk_mul_f32 v[108:109], v[108:109], v[24:25]\nv_pk_mul_f32 v[110:111], v[110:111], v[24:25]\nQk 104,104,105\nQk 105,106,107\n"
"Qk 106,108,109\nQk 107,110,111\nv_accvgpr_write_b32 a12, v104\nv_accvgpr_write_b32 a13, v105\nv_accvgpr_write_b32 a14, v106\nv_accvgpr_write_b32 a15, v107\nv_pk_mul_f32 v[112:113], v[112:113], v[28:29]\nv_pk_mul_f32 v[114:115], v[114:115], v[28:29]\n"
"v_pk_mul_f32 v[116:117], v[116:117], v[28:29]\nv_pk_mul_f32 v[118:119], v[118:119], v[28:29]\nQk 112,112,113\nQk 113,114,115\nQk 114,116,117\nQk 115,118,119\nv_accvgpr_write_b32 a16, v112\nv_accvgpr_write_b32 a17, v113\n"
"v_accvgpr_write_b32 a18, v114\nv_accvgpr_write_b32 a19, v115\nv_pk_mul_f32 v[120:121], v[120:121], v[28:29]\nv_pk_mul_f32 v[122:123], v[122:123], v[28:29]\nv_pk_mul_f32 v[124:125], v[124:125], v[28:29]\nv_pk_mul_f32 v[126:127], v[126:127], v[28:29]\nQk 120,120,121\nQk 121,122,123\n"
"Qk 122,124,125\nQk 123,126,127\nv_accvgpr_write_b32 a20, v120\nv_accvgpr_write_b32 a21, v121\nv_accvgpr_write_b32 a22, v122\nv_accvgpr_write_b32 a23, v123\nv_pk_mul_f32 v[128:129], v[128:129], v[28:29]\nv_pk_mul_f32 v[130:131], v[130:131], v[28:29]\n"
"v_pk_mul_f32 v[132:133], v[132:133], v[28:29]\nv_pk_mul_f32 v[134:135], v[134:135], v[28:29]\nQk 128,128,129\nQk 129,130,131\nQk 130,132,133\nQk 131,134,135\nv_accvgpr_write_b32 a24, v128\nv_accvgpr_write_b32 a25, v129\n"
"v_accvgpr_write_b32 a26, v130\nv_accvgpr_write_b32 a27, v131\nv_pk_mul_f32 v[136:137], v[136:137], v[28:29]\nv_pk_mul_f32 v[138:139], v[138:139], v[28:29]\nv_pk_mul_f32 v[140:141], v[140:141], v[28:29]\nv_pk_mul_f32 v[142:143], v[142:143], v[28:29]\nQk 136,136,137\nQk 137,138,139\n"
"Qk 138,140,141\nQk 139,142,143\nv_accvgpr_write_b32 a28, v136\nv_accvgpr_write_b32 a29, v137\nv_accvgpr_write_b32 a30, v138\nv_accvgpr_write_b32 a31, v139\nv_pk_mul_f32 v[144:145], v[144:145], v[32:33]\nv_pk_mul_f32 v[146:147], v[146:147], v[32:33]\n"
"v_pk_mul_f32 v[148:149], v[148:149], v[32:33]\nv_pk_mul_f32 v[150:151], v[150:151], v[32:33]\nQk 144,144,145\nQk 145,146,147\nQk 146,148,149\nQk 147,150,151\nv_accvgpr_write_b32 a32, v144\nv_accvgpr_write_b32 a33, v145\n"
"v_accvgpr_write_b32 a34, v146\nv_accvgpr_write_b32 a35, v147\nv_pk_mul_f32 v[152:153], v[152:153], v[32:33]\nv_pk_mul_f32 v[154:155], v[154:155], v[32:33]\nv_pk_mul_f32 v[156:157], v[156:157], v[32:33]\nv_pk_mul_f32 v[158:159], v[158:159], v[32:33]\nQk 152,152,153\nQk 153,154,155\n"
"Qk 154,156,157\nQk 155,158,159\nv_accvgpr_write_b32 a36, v152\nv_accvgpr_write_b32 a37, v153\nv_accvgpr_write_b32 a38, v154\nv_accvgpr_write_b32 a39, v155\nv_pk_mul_f32 v[160:161], v[160:161], v[32:33]\nv_pk_mul_f32 v[162:163], v[162:163], v[32:33]\n"
"v_pk_mul_f32 v[164:165], v[164:165], v[32:33]\nv_pk_mul_f32 v[166:167], v[166:167], v[32:33]\nQk 160,160,161\nQk 161,162,163\nQk 162,164,165\nQk 163,166,167\nv_accvgpr_write_b32 a40, v160\nv_accvgpr_write_b32 a41, v161\n"
"v_accvgpr_write_b32 a42, v162\nv_accvgpr_write_b32 a43, v163\nv_pk_mul_f32 v[168:169], v[168:169], v[32:33]\nv_pk_mul_f32 v[170:171], v[170:171], v[32:33]\nv_pk_mul_f32 v[172:173], v[172:173], v[32:33]\nv_pk_mul_f32 v[174:175], v[174:175], v[32:33]\nQk 168,168,169\nQk 169,170,171\n"
"Qk 170,172,173\nQk 171,174,175\nv_accvgpr_write_b32 a44, v168\nv_accvgpr_write_b32 a45, v169\nv_accvgpr_write_b32 a46, v170\nv_accvgpr_write_b32 a47, v171\nv_pk_mul_f32 v[176:177], v[176:177], v[36:37]\nv_pk_mul_f32 v[178:179], v[178:179], v[36:37]\n"
"v_pk_mul_f32 v[180:181], v[180:181], v[36:37]\nv_pk_mul_f32 v[182:183], v[182:183], v[36:37]\nQk 176,176,177\nQk 177,178,179\nQk 178,180,181\nQk 179,182,183\nv_accvgpr_write_b32 a48, v176\nv_accvgpr_write_b32 a49, v177\n"
"v_accvgpr_write_b32 a50, v178\nv_accvgpr_write_b32 a51, v179\nv_pk_mul_f32 v[184:185], v[184:185], v[36:37]\nv_pk_mul_f32 v[186:187], v[186:187], v[36:37]\nv_pk_mul_f32 v[188:189], v[188:189], v[36:37]\nv_pk_mul_f32 v[190:191], v[190:191], v[36:37]\nQk 184,184,185\nQk 185,186,187\n"
"Qk 186,188,189\nQk 187,190,191\nv_accvgpr_write_b32 a52, v184\nv_accvgpr_write_b32 a53, v185\nv_accvgpr_write_b32 a54, v186\nv_accvgpr_write_b32 a55, v187\nv_pk_mul_f32 v[192:193], v[192:193], v[36:37]\nv_pk_mul_f32 v[194:195], v[194:195], v[36:37]\n"
"v_pk_mul_f32 v[196:197], v[196:197], v[36:37]\nv_pk_mul_f32 v[198:199], v[198:199], v[36:37]\nQk 192,192,193\nQk 193,194,195\nQk 194,196,197\nQk 195,198,199\nv_accvgpr_write_b32 a56, v192\nv_accvgpr_write_b32 a57, v193\n"
"v_accvgpr_write_b32 a58, v194\nv_accvgpr_write_b32 a59, v195\nv_pk_mul_f32 v[200:201], v[200:201], v[36:37]\nv_pk_mul_f32 v[202:203], v[202:203], v[36:37]\nv_pk_mul_f32 v[204:205], v[204:205], v[36:37]\nv_pk_mul_f32 v[206:207], v[206:207], v[36:37]\nQk 200,200,201\nQk 201,202,203\n"
"Qk 202,204,205\nQk 203,206,207\nv_accvgpr_write_b32 a60, v200\nv_accvgpr_write_b32 a61, v201\nv_accvgpr_write_b32 a62, v202\nv_accvgpr_write_b32 a63, v203\nv_pk_mul_f32 v[208:209], v[208:209], v[40:41]\nv_pk_mul_f32 v[210:211], v[210:211], v[40:41]\n"
"v_pk_mul_f32 v[212:213], v[212:213], v[40:41]\nv_pk_mul_f32 v[214:215], v[214:215], v[40:41]\nQk 208,208,209\nQk 209,210,211\nQk 210,212,213\nQk 211,214,215\nv_accvgpr_write_b32 a64, v208\nv_accvgpr_write_b32 a65, v209\n"
"v_accvgpr_write_b32 a66, v210\nv_accvgpr_write_b32 a67, v211\nv_pk_mul_f32 v[216:217], v[216:217], v[40:41]\nv_pk_mul_f32 v[218:219], v[218:219], v[40:41]\nv_pk_mul_f32 v[220:221], v[220:221], v[40:41]\nv_pk_mul_f32 v[222:223], v[222:223], v[40:41]\nQk 216,216,217\nQk 217,218,219\n"
"Qk 218,220,221\nQk 219,222,223\nv_accvgpr_write_b32 a68, v216\nv_accvgpr_write_b32 a69, v217\nv_accvgpr_write_b32 a70, v218\nv_accvgpr_write_b32 a71, v219\nv_pk_mul_f32 v[224:225], v[224:225], v[40:41]\nv_pk_mul_f32 v[226:227], v[226:227], v[40:41]\n"
"v_pk_mul_f32 v[228:229], v[228:229], v[40:41]\nv_pk_mul_f32 v[230:231], v[230:231], v[40:41]\nQk 224,224,225\nQk 225,226,227\nQk 226,228,229\nQk 227,230,231\nv_accvgpr_write_b32 a72, v224\nv_accvgpr_write_b32 a73, v225\n"
"v_accvgpr_write_b32 a74, v226\nv_accvgpr_write_b32 a75, v227\nv_pk_mul_f32 v[232:233], v[232:233], v[40:41]\nv_pk_mul_f32 v[234:235], v[234:235], v[40:41]\nv_pk_mul_f32 v[236:237], v[236:237], v[40:41]\nv_pk_mul_f32 v[238:239], v[238:239], v[40:41]\nQk 232,232,233\nQk 233,234,235\n"
"Qk 234,236,237\nQk 235,238,239\nv_accvgpr_write_b32 a76, v232\nv_accvgpr_write_b32 a77, v233\nv_accvgpr_write_b32 a78, v234\nv_accvgpr_write_b32 a79, v235\nv_pk_mul_f32 v[240:241], v[240:241], v[44:45]\nv_pk_mul_f32 v[242:243], v[242:243], v[44:45]\n"
"v_pk_mul_f32 v[244:245], v[244:245], v[44:45]\nv_pk_mul_f32 v[246:247], v[246:247], v[44:45]\nQk 240,240,241\nQk 241,242,243\nQk 242,244,245\nQk 243,246,247\nv_accvgpr_write_b32 a80, v240\nv_accvgpr_write_b32 a81, v241\n"
"v_accvgpr_write_b32 a82, v242\nv_accvgpr_write_b32 a83, v243\nv_pk_mul_f32 v[248:249], v[248:249], v[44:45]\nv_pk_mul_f32 v[250:251], v[250:251], v[44:45]\nv_pk_mul_f32 v[252:253], v[252:253], v[44:45]\nv_pk_mul_f32 v[254:255], v[254:255], v[44:45]\nQk 248,248,249\nQk 249,250,251\n"
"Qk 250,252,253\nQk 251,254,255\nv_accvgpr_write_b32 a84, v248\nv_accvgpr_write_b32 a85, v249\nv_accvgpr_write_b32 a86, v250\nv_accvgpr_write_b32 a87, v251\nv_accvgpr_read_b32 v0, a208\nv_accvgpr_read_b32 v1, a209\n"
"v_accvgpr_read_b32 v2, a210\nv_accvgpr_read_b32 v3, a211\nv_accvgpr_read_b32 v4, a212\nv_accvgpr_read_b32 v5, a213\nv_accvgpr_read_b32 v6, a214\nv_accvgpr_read_b32 v7, a215\nv_pk_mul_f32 v[0:1], v[0:1], v[44:45]\nv_pk_mul_f32 v[2:3], v[2:3], v[44:45]\n"
"v_pk_mul_f32 v[4:5], v[4:5], v[44:45]\nv_pk_mul_f32 v[6:7], v[6:7], v[44:45]\nQk 0,0,1\nQk 1,2,3\nQk 2,4,5\nQk 3,6,7\nv_accvgpr_write_b32 a88, v0\nv_accvgpr_write_b32 a89, v1\n"
"v_accvgpr_write_b32 a90, v2\nv_accvgpr_write_b32 a91, v3\nv_accvgpr_read_b32 v8, a216\nv_accvgpr_read_b32 v9, a217\nv_accvgpr_read_b32 v10, a218\nv_accvgpr_read_b32 v11, a219\nv_accvgpr_read_b32 v12, a220\nv_accvgpr_read_b32 v13, a221\n"
"v_accvgpr_read_b32 v14, a222\nv_accvgpr_read_b32 v15, a223\nv_pk_mul_f32 v[8:9], v[8:9], v[44:45]\nv_pk_mul_f32 v[10:11], v[10:11], v[44:45]\nv_pk_mul_f32 v[12:13], v[12:13], v[44:45]\nv_pk_mul_f32 v[14:15], v[14:15], v[44:45]\nQk 8,8,9\nQk 9,10,11\n"
"Qk 10,12,13\nQk 11,14,15\nv_accvgpr_write_b32 a92, v8\nv_accvgpr_write_b32 a93, v9\nv_accvgpr_write_b32 a94, v10\nv_accvgpr_write_b32 a95, v11\nv_accvgpr_read_b32 v0, a224\nv_accvgpr_read_b32 v1, a225\n"
"v_accvgpr_read_b32 v2, a226\nv_accvgpr_read_b32 v3, a227\nv_accvgpr_read_b32 v4, a228\nv_accvgpr_read_b32 v5, a229\nv_accvgpr_read_b32 v6, a230\nv_accvgpr_read_b32 v7, a231\nv_pk_mul_f32 v[0:1], v[0:1], v[48:49]\nv_pk_mul_f32 v[2:3], v[2:3], v[48:49]\n"
"v_pk_mul_f32 v[4:5], v[4:5], v[48:49]\nv_pk_mul_f32 v[6:7], v[6:7], v[48:49]\nQk 0,0,1\nQk 1,2,3\nQk 2,4,5\nQk 3,6,7\nv_accvgpr_write_b32 a96, v0\nv_accvgpr_write_b32 a97, v1\n"
"v_accvgpr_write_b32 a98, v2\nv_accvgpr_write_b32 a99, v3\nv_accvgpr_read_b32 v8, a232\nv_accvgpr_read_b32 v9, a233\nv_accvgpr_read_b32 v10, a234\nv_accvgpr_read_b32 v11, a235\nv_accvgpr_read_b32 v12, a236\nv_accvgpr_read_b32 v13, a237\n"
"v_accvgpr_read_b32 v14, a238\nv_accvgpr_read_b32 v15, a239\nv_pk_mul_f32 v[8:9], v[8:9], v[48:49]\nv_pk_mul_f32 v[10:11], v[10:11], v[48:49]\nv_pk_mul_f32 v[12:13], v[12:13], v[48:49]\nv_pk_mul_f32 v[14:15], v[14:15], v[48:49]\nQk 8,8,9\nQk 9,10,11\n"
"Qk 10,12,13\nQk 11,14,15\nv_accvgpr_write_b32 a100, v8\nv_accvgpr_write_b32 a101, v9\nv_accvgpr_write_b32 a102, v10\nv_accvgpr_write_b32 a103, v11\nv_accvgpr_read_b32 v0, a240\nv_accvgpr_read_b32 v1, a241\n"
"v_accvgpr_read_b32 v2, a242\nv_accvgpr_read_b32 v3, a243\nv_accvgpr_read_b32 v4, a244\nv_accvgpr_read_b32 v5, a245\nv_accvgpr_read_b32 v6, a246\nv_accvgpr_read_b32 v7, a247\nv_pk_mul_f32 v[0:1], v[0:1], v[48:49]\nv_pk_mul_f32 v[2:3], v[2:3], v[48:49]\n"
"v_pk_mul_f32 v[4:5], v[4:5], v[48:49]\nv_pk_mul_f32 v[6:7], v[6:7], v[48:49]\nQk 0,0,1\nQk 1,2,3\nQk 2,4,5\nQk 3,6,7\nv_accvgpr_write_b32 a104, v0\nv_accvgpr_write_b32 a105, v1\n"
"v_accvgpr_write_b32 a106, v2\nv_accvgpr_write_b32 a107, v3\nv_accvgpr_read_b32 v8, a248\nv_accvgpr_read_b32 v9, a249\nv_accvgpr_read_b32 v10, a250\nv_accvgpr_read_b32 v11, a251\nv_accvgpr_read_b32 v12, a252\nv_accvgpr_read_b32 v13, a253\n"
"v_accvgpr_read_b32 v14, a254\nv_accvgpr_read_b32 v15, a255\nv_pk_mul_f32 v[8:9], v[8:9], v[48:49]\nv_pk_mul_f32 v[10:11], v[10:11], v[48:49]\nv_pk_mul_f32 v[12:13], v[12:13], v[48:49]\nv_pk_mul_f32 v[14:15], v[14:15], v[48:49]\nQk 8,8,9\nQk 9,10,11\n"
"Qk 10,12,13\nQk 11,14,15\nv_accvgpr_write_b32 a108, v8\nv_accvgpr_write_b32 a109, v9\nv_accvgpr_write_b32 a110, v10\nv_accvgpr_write_b32 a111, v11\ns_mov_b32 s56, %8\ns_and_b32 s57, %9, 0xffff\n"
"s_mov_b32 s94, 0x7ffff000\ns_mul_i32 s68, %20, 40960\ns_lshl_b32 s69, s70, 10\ns_add_u32 s68, s68, s69\ns_add_u32 s68, s68, 4096\ns_add_u32 s94, %15, s68\ns_addc_u32 s95, %16, 0\ns_and_b32 s95, s95, 0xffff\n"
"s_mov_b32 s75, 37888\ns_mov_b32 s58, 1835008\ns_mov_b32 s67, 1\ns_mul_i32 s71, %12, 57344\ns_mul_i32 s68, s92, 7168\ns_add_u32 s71, s71, s68\ns_mov_b64 exec, 0\ns_cmp_eq_u32 s70, 0\n"
"s_cbranch_scc1 .Lpg12\ns_cmp_eq_u32 s70, 1\ns_cbranch_scc1 .Lpg13\ns_cmp_eq_u32 s70, 2\ns_cbranch_scc1 .Lpg14\ns_branch .Lpg15\n.Ltr6:\ns_mov_b64 exec, -1\n"
"s_branch .Lround\n.Lhook:\n.Lpg9:\ns_cbranch_execz .Ltr10\ns_cmp_eq_u32 s66, 1\ns_cbranch_scc1 .Lh1\ns_cmp_eq_u32 s66, 8\ns_cbranch_scc1 .Lh8\n"
"s_cmp_eq_u32 s66, 9\ns_cbranch_scc1 .Lh9\ns_lshr_b32 s68, s99, s66\ns_cmp_eq_u32 s68, 1\ns_cbranch_scc1 .Lh12\ns_branch .Lh11\n.Lh1:\ns_cmp_eq_u32 s70, 0\n"
"s_cselect_b32 s68, 128, 0\nv_mov_b32 v66, 0x7ffff000\nv_cmp_gt_u32 vcc, s68, v60\nv_not_b32 v67, v65\ns_lshl_b32 s68, %19, 2\nv_cndmask_b32 v66, v66, v76, vcc\nv_readlane_b32 s30, v67, %19\nbuffer_store_dword v67, v66, s[24:27], s68 offen offset:1024 sc1\n"
"s_branch .Lhookret\n.Lh8:\nv_bfe_u32 v66, v60, 4, 3\ns_lshl_b32 s68, %19, 7\nv_lshlrev_b32 v66, 2, v66\ns_add_u32 s68, s68, 2293760\nbuffer_load_dword v78, v66, s[24:27], s68 offen offset:1024 sc1\ns_branch .Lhookret\n"
".Lh9:\ns_waitcnt vmcnt(32)\ns_mov_b32 s97, 0\n.Lack_l:\nv_cmp_ne_u32 vcc, s30, v78\ns_cmp_eq_u64 vcc, 0\ns_cbranch_scc1 .Lack_ok\ns_sleep 1\n"
"v_bfe_u32 v66, v60, 4, 3\ns_lshl_b32 s68, %19, 7\nv_lshlrev_b32 v66, 2, v66\ns_add_u32 s68, s68, 2293760\nbuffer_load_dword v78, v66, s[24:27], s68 offen offset:1024 sc1\ns_waitcnt vmcnt(0)\ns_add_u32 s97, s97, 1\ns_cmp_lt_u32 s97, 1048576\n"
"s_cbranch_scc1 .Lack_l\n.Lack_ok:\ns_lshl_b32 s68, %19, 4\ns_cmp_eq_u32 s70, 0\ns_cselect_b32 s68, s68, -1\nv_mov_b32 v67, 0x7ffff000\nv_cmp_eq_u32 vcc, s68, v60\ns_nop 1\n"
"v_cndmask_b32 v67, v67, v76, vcc\nv_add_u32 v66, 1, v65\nbuffer_store_dword v66, v67, s[24:27], 0 offen sc1\ns_branch .Lhookret\n.Lh11:\nbuffer_load_dword v78, v76, s[24:27], 0 offen sc1\ns_branch .Lhookret\n.Lh12:\n"
"s_waitcnt vmcnt(32)\nv_sub_u32 v66, v78, v65\nv_cmp_gt_u32 vcc, 1, v66\ns_and_b64 vcc, vcc, s[28:29]\ns_cbranch_scc1 .Lf1_w\n.Lf1_r:\ns_cmp_lg_u32 s70, 3\ns_cbranch_scc1 .Lf1_n\n"
"buffer_inv sc1\n.Lf1_n:\ns_lshr_b32 s88, %19, 2\ns_lshl_b32 s88, s88, 4\ns_add_u32 s88, s88, s93\ns_mul_i32 s68, s88, 57344\ns_mul_i32 s69, s92, 7168\ns_add_u32 s68, s68, s69\n"
"s_add_u32 s48, %8, s68\ns_addc_u32 s49, %9, 0\ns_and_b32 s49, s49, 0xffff\ns_sub_u32 s50, 1835008, s68\ns_mul_i32 s68, s88, 163840\ns_and_b32 s69, %19, 3\ns_mul_i32 s69, s69, 320\ns_add_u32 s68, s68, s69\n"
"s_add_u32 s52, %15, s68\ns_addc_u32 s53, %16, 0\ns_and_b32 s53, s53, 0xffff\ns_sub_u32 s54, 5246976, s68\ns_add_u32 s71, s71, 458752\ns_and_b32 s87, %19, 3\ns_mul_i32 s87, s87, 327680\ns_add_u32 s87, s87, 1310720\n"
"s_mul_i32 s69, s92, 10240\nv_bfe_u32 v66, v60, 5, 3\nv_mul_u32_u24 v66, 1280, v66\nv_and_b32 v67, 16, v60\nv_add3_u32 v67, v67, v66, s69\ns_and_b32 s69, %19, 1\nv_lshrrev_b32 v66, 8, v60\ns_lshl_b32 s68, s93, 1\n"
"v_xor_b32 v66, s68, v66\nv_lshl_add_u32 v66, v66, 5, v67\ns_lshr_b32 s68, 128, s69\nv_add_u32 v61, s68, v66\ns_mul_i32 s68, s69, 192\nv_add_u32 v63, s68, v66\nv_bfe_u32 v66, v60, 8, 1\nv_xor_b32 v66, s93, v66\n"
"v_lshl_add_u32 v67, v66, 5, v67\nv_lshrrev_b32 v66, 9, v60\nv_mul_u32_u24 v66, 20480, v66\ns_xor_b32 s69, s69, 1\ns_lshl_b32 s69, s69, 8\nv_add3_u32 v62, v66, v67, s69\ns_branch .Lhookret\n.Lf1_w:\n"
"s_mov_b32 s97, 0\n.Lf1_l:\ns_sleep 1\nbuffer_load_dword v78, v76, s[24:27], 0 offen sc1\ns_waitcnt vmcnt(0)\ns_add_u32 s97, s97, 1\ns_cmp_gt_u32 s97, 1048576\ns_cbranch_scc1 .Lf1_r\n"
"v_sub_u32 v66, v78, v65\nv_cmp_gt_u32 vcc, 1, v66\ns_and_b64 vcc, vcc, s[28:29]\ns_cbranch_scc1 .Lf1_l\ns_branch .Lf1_r\n.Lsa1:\nds_read_b32 v66, v74\ns_waitcnt lgkmcnt(0)\n"
"v_cmp_gt_u32 vcc, v75, v66\ns_cbranch_vccnz .Lsa1\ns_branch .Lra1\n.Lsp1:\ns_memrealtime s[74:75]\ns_waitcnt lgkmcnt(0)\ns_sub_u32 s75, s77, s74\ns_cmp_lt_u32 s75, 256\n"
"s_cbranch_scc1 .Lsp1\ns_mov_b32 s74, s77\ns_branch .Lrp1\n.Lsa2:\nds_read_b32 v66, v74\ns_waitcnt lgkmcnt(0)\nv_cmp_gt_u32 vcc, v75, v66\ns_cbranch_vccnz .Lsa2\n"
"s_branch .Lra2\n.Lsp2:\ns_memrealtime s[74:75]\ns_waitcnt lgkmcnt(0)\ns_sub_u32 s75, s77, s74\ns_cmp_lt_u32 s75, 256\ns_cbranch_scc1 .Lsp2\ns_mov_b32 s74, s77\n"
"s_branch .Lrp2\n.Lsa3:\nds_read_b32 v66, v74\ns_waitcnt lgkmcnt(0)\nv_cmp_gt_u32 vcc, v75, v66\ns_cbranch_vccnz .Lsa3\ns_branch .Lra3\n.Lsp3:\n"
"s_memrealtime s[74:75]\ns_waitcnt lgkmcnt(0)\ns_sub_u32 s75, s77, s74\ns_cmp_lt_u32 s75, 256\ns_cbranch_scc1 .Lsp3\ns_mov_b32 s74, s77\ns_branch .Lrp3\n.Lsa4:\n"
"ds_read_b32 v66, v74\ns_waitcnt lgkmcnt(0)\nv_cmp_gt_u32 vcc, v75, v66\ns_cbranch_vccnz .Lsa4\ns_branch .Lra4\n.Lsp4:\ns_memrealtime s[74:75]\ns_waitcnt lgkmcnt(0)\n"
"s_sub_u32 s75, s77, s74\ns_cmp_lt_u32 s75, 256\ns_cbranch_scc1 .Lsp4\ns_mov_b32 s74, s77\ns_branch .Lrp4\n.Lsa5:\nds_read_b32 v66, v74\ns_waitcnt lgkmcnt(0)\n"
"v_cmp_gt_u32 vcc, v75, v66\ns_cbranch_vccnz .Lsa5\ns_branch .Lra5\n.Lsp5:\ns_memrealtime s[74:75]\ns_waitcnt lgkmcnt(0)\ns_sub_u32 s75, s77, s74\ns_cmp_lt_u32 s75, 256\n"
"s_cbranch_scc1 .Lsp5\ns_mov_b32 s74, s77\ns_branch .Lrp5\n.Lsa30:\nds_read_b32 v66, v74\ns_waitcnt lgkmcnt(0)\nv_cmp_gt_u32 vcc, v75, v66\ns_cbranch_vccnz .Lsa30\n"
"s_branch .Lra30\n.Lsa31:\nds_read_b32 v66, v74\ns_waitcnt lgkmcnt(0)\nv_cmp_gt_u32 vcc, v75, v66\ns_cbranch_vccnz .Lsa31\ns_branch .Lra31\n.Lsa32:\n"
"ds_read_b32 v66, v74\ns_waitcnt lgkmcnt(0)\nv_cmp_gt_u32 vcc, v75, v66\ns_cbranch_vccnz .Lsa32\ns_branch .Lra32\n.Lsa30x:\nds_read_b32 v66, v74\ns_waitcnt lgkmcnt(0)\n"
"v_cmp_gt_u32 vcc, v75, v66\ns_cbranch_vccnz .Lsa30x\ns_branch .Lra30x\n.Lsa31x:\nds_read_b32 v66, v74\ns_waitcnt lgkmcnt(0)\nv_cmp_gt_u32 vcc, v75, v66\ns_cbranch_vccnz .Lsa31x\n"
"s_branch .Lra31x\n.Lso40:\nds_read_b32 v66, v74\ns_waitcnt lgkmcnt(0)\nv_cmp_gt_u32 vcc, v75, v66\ns_cbranch_vccnz .Lso40\ns_branch .Lro40\n.Lso180:\n"
"ds_read_b32 v66, v74\ns_waitcnt lgkmcnt(0)\nv_cmp_gt_u32 vcc, v75, v66\ns_cbranch_vccnz .Lso180\ns_branch .Lro180\n.Lso320:\nds_read_b32 v66, v74\ns_waitcnt lgkmcnt(0)\n"
"v_cmp_gt_u32 vcc, v75, v66\ns_cbranch_vccnz .Lso320\ns_branch .Lro320\n.Lso460:\nds_read_b32 v66, v74\ns_waitcnt lgkmcnt(0)\n.Lpg10:\ns_cbranch_execz .Ltr10\n"
"v_cmp_gt_u32 vcc, v75, v66\ns_cbranch_vccnz .Lso460\ns_branch .Lro460\n.Lso600:\nds_read_b32 v66, v74\ns_waitcnt lgkmcnt(0)\nv_cmp_gt_u32 vcc, v75, v66\ns_cbranch_vccnz .Lso600\n"
"s_branch .Lro600\n.Lso740:\nds_read_b32 v66, v74\ns_waitcnt lgkmcnt(0)\nv_cmp_gt_u32 vcc, v75, v66\ns_cbranch_vccnz .Lso740\ns_branch .Lro740\n.Lso880:\n"
"ds_read_b32 v66, v74\ns_waitcnt lgkmcnt(0)\nv_cmp_gt_u32 vcc, v75, v66\ns_cbranch_vccnz .Lso880\ns_branch .Lro880\n.Lso1020:\nds_read_b32 v66, v74\ns_waitcnt lgkmcnt(0)\n"
"v_cmp_gt_u32 vcc, v75, v66\ns_cbranch_vccnz .Lso1020\ns_branch .Lro1020\n.Lso1160:\nds_read_b32 v66, v74\ns_waitcnt lgkmcnt(0)\nv_cmp_gt_u32 vcc, v75, v66\ns_cbranch_vccnz .Lso1160\n"
"s_branch .Lro1160\n.Lso1300:\nds_read_b32 v66, v74\ns_waitcnt lgkmcnt(0)\nv_cmp_gt_u32 vcc, v75, v66\ns_cbranch_vccnz .Lso1300\ns_branch .Lro1300\n.Lso1440:\n"
"ds_read_b32 v66, v74\ns_waitcnt lgkmcnt(0)\nv_cmp_gt_u32 vcc, v75, v66\ns_cbranch_vccnz .Lso1440\ns_branch .Lro1440\n.Lso1455:\nds_read_b32 v66, v74\ns_waitcnt lgkmcnt(0)\n"
"v_cmp_gt_u32 vcc, v75, v66\ns_cbranch_vccnz .Lso1455\ns_branch .Lro1455\n.Lso1525r0:\nds_read_b32 v66, v74\ns_waitcnt lgkmcnt(0)\nv_cmp_gt_u32 vcc, v75, v66\ns_cbranch_vccnz .Lso1525r0\n"
"s_branch .Lro1525r0\n.Lso1595r0:\nds_read_b32 v66, v74\ns_waitcnt lgkmcnt(0)\nv_cmp_gt_u32 vcc, v75, v66\ns_cbranch_vccnz .Lso1595r0\ns_branch .Lro1595r0\n.Lso1665r0:\n"
"ds_read_b32 v66, v74\ns_waitcnt lgkmcnt(0)\nv_cmp_gt_u32 vcc, v75, v66\ns_cbranch_vccnz .Lso1665r0\ns_branch .Lro1665r0\n.Lso1735r0:\nds_read_b32 v66, v74\ns_waitcnt lgkmcnt(0)\n"
"v_cmp_gt_u32 vcc, v75, v66\ns_cbranch_vccnz .Lso1735r0\ns_branch .Lro1735r0\n.Lso1805r0:\nds_read_b32 v66, v74\ns_waitcnt lgkmcnt(0)\nv_cmp_gt_u32 vcc, v75, v66\ns_cbranch_vccnz .Lso1805r0\n"
"s_branch .Lro1805r0\n.Lso1878r0:\nds_read_b32 v66, v74\ns_waitcnt lgkmcnt(0)\nv_cmp_gt_u32 vcc, v75, v66\ns_cbranch_vccnz .Lso1878r0\ns_branch .Lro1878r0\n.Lso1920r0:\n"
"ds_read_b32 v66, v74\ns_waitcnt lgkmcnt(0)\nv_cmp_gt_u32 vcc, v75, v66\ns_cbranch_vccnz .Lso1920r0\ns_branch .Lro1920r0\n.Lso1960r0:\nds_read_b32 v66, v74\ns_waitcnt lgkmcnt(0)\n"
"v_cmp_gt_u32 vcc, v75, v66\ns_cbranch_vccnz .Lso1960r0\ns_branch .Lro1960r0\n.Lso2046r0:\nds_read_b32 v66, v74\ns_waitcnt lgkmcnt(0)\nv_cmp_gt_u32 vcc, v75, v66\ns_cbranch_vccnz .Lso2046r0\n"
"s_branch .Lro2046r0\n.Lso2088r0:\nds_read_b32 v66, v74\ns_waitcnt lgkmcnt(0)\nv_cmp_gt_u32 vcc, v75, v66\ns_cbranch_vccnz .Lso2088r0\ns_branch .Lro2088r0\n.Lso2172r0:\n"
"ds_read_b32 v66, v74\ns_waitcnt lgkmcnt(0)\nv_cmp_gt_u32 vcc, v75, v66\ns_cbranch_vccnz .Lso2172r0\ns_branch .Lro2172r0\n.Lso2222r0:\nds_read_b32 v66, v74\ns_waitcnt lgkmcnt(0)\n"
"v_cmp_gt_u32 vcc, v75, v66\ns_cbranch_vccnz .Lso2222r0\ns_branch .Lro2222r0\n.Lso1525r1:\nds_read_b32 v66, v74\ns_waitcnt lgkmcnt(0)\nv_cmp_gt_u32 vcc, v75, v66\ns_cbranch_vccnz .Lso1525r1\n"
"s_branch .Lro1525r1\n.Lso1595r1:\nds_read_b32 v66, v74\ns_waitcnt lgkmcnt(0)\nv_cmp_gt_u32 vcc, v75, v66\ns_cbranch_vccnz .Lso1595r1\ns_branch .Lro1595r1\n.Lso1665r1:\n"
"ds_read_b32 v66, v74\ns_waitcnt lgkmcnt(0)\nv_cmp_gt_u32 vcc, v75, v66\ns_cbranch_vccnz .Lso1665r1\ns_branch .Lro1665r1\n.Lso1735r1:\nds_read_b32 v66, v74\ns_waitcnt lgkmcnt(0)\n"
"v_cmp_gt_u32 vcc, v75, v66\ns_cbranch_vccnz .Lso1735r1\ns_branch .Lro1735r1\n.Lso1805r1:\nds_read_b32 v66, v74\ns_waitcnt lgkmcnt(0)\nv_cmp_gt_u32 vcc, v75, v66\ns_cbranch_vccnz .Lso1805r1\n"
"s_branch .Lro1805r1\n.Lso1840r1:\nds_read_b32 v66, v74\ns_waitcnt lgkmcnt(0)\nv_cmp_gt_u32 vcc, v75, v66\ns_cbranch_vccnz .Lso1840r1\ns_branch .Lro1840r1\n.Lso1878r1:\n"
"ds_read_b32 v66, v74\ns_waitcnt lgkmcnt(0)\nv_cmp_gt_u32 vcc, v75, v66\ns_cbranch_vccnz .Lso1878r1\ns_branch .Lro1878r1\n.Lso1920r1:\nds_read_b32 v66, v74\ns_waitcnt lgkmcnt(0)\n"
"v_cmp_gt_u32 vcc, v75, v66\ns_cbranch_vccnz .Lso1920r1\ns_branch .Lro1920r1\n.Lso1945r1:\nds_read_b32 v66, v74\ns_waitcnt lgkmcnt(0)\nv_cmp_gt_u32 vcc, v75, v66\ns_cbranch_vccnz .Lso1945r1\n"
"s_branch .Lro1945r1\n.Lso2004r1:\nds_read_b32 v66, v74\ns_waitcnt lgkmcnt(0)\nv_cmp_gt_u32 vcc, v75, v66\ns_cbranch_vccnz .Lso2004r1\ns_branch .Lro2004r1\n.Lso2045r1:\n"
"ds_read_b32 v66, v74\ns_waitcnt lgkmcnt(0)\nv_cmp_gt_u32 vcc, v75, v66\ns_cbranch_vccnz .Lso2045r1\ns_branch .Lro2045r1\n.Lso2130r1:\nds_read_b32 v66, v74\ns_waitcnt lgkmcnt(0)\n"
"v_cmp_gt_u32 vcc, v75, v66\ns_cbranch_vccnz .Lso2130r1\ns_branch .Lro2130r1\n.Lso2172r1:\nds_read_b32 v66, v74\ns_waitcnt lgkmcnt(0)\nv_cmp_gt_u32 vcc, v75, v66\ns_cbranch_vccnz .Lso2172r1\n"
"s_branch .Lro2172r1\n.Lso2225r1:\nds_read_b32 v66, v74\ns_waitcnt lgkmcnt(0)\nv_cmp_gt_u32 vcc, v75, v66\ns_cbranch_vccnz .Lso2225r1\ns_branch .Lro2225r1\n.Ltail1:\n"
".Lpg11:\ns_cbranch_execz .Ltr10\nQcvaa 0,112,32,56\nv_add_u32 v75, 1, v75\nQk 38,4,5\nQmvaa 0,116,36\ns_waitcnt vmcnt(0)\nds_write_b32 v73, v75\n"
"Qmvaa 0,120,40\nQe 7\nQmvaa 0,124,44\nQk 39,6,7\nQcvaa 4,128,32,56\nQe 8\nQmvaa 4,132,36\nQe 9\n"
"Qmvaa 4,136,40\nQe 10\nQmvaa 4,140,44\ns_waitcnt lgkmcnt(1)\nQk 40,8,9\nQmvav 96,144,44\nQe 11\nQmvav 96,148,36\n"
"Qk 41,10,11\nQmvav 100,152,44\nQe 12\nQmvav 100,156,36\nQe 13\nQmvav 104,160,44\nQe 14\nQmvav 104,164,36\n"
"Qe 15\nQmvav 108,168,44\nQk 42,12,13\nQk 43,14,15\nQmvav 108,172,36\nQe 0\nQcvaa 8,112,48,56\nQe 1\n"
"Qmvaa 8,116,52\nQe 2\nQmvaa 8,120,56\nds_read_b32 v66, v74\nQk 44,0,1\nQmvaa 8,124,60\nQe 3\nQcvaa 12,128,48,56\n"
"Qk 45,2,3\nQmvaa 12,132,52\nQe 4\nv_smfmac_f32_16x16x64_bf16 a[176:179], a[204:207], v[36:43], v77\nQmvaa 12,136,56\ns_waitcnt lgkmcnt(0)\nv_cmp_gt_u32 vcc, v75, v66\nQmvaa 12,140,60\n"
"s_cbranch_vccnz .Lsa30x\n.Lra30x:\nQmvav 128,144,48\ns_add_u32 m0, s96, 0\ns_add_u32 s95, s87, 4096\nQmvav 128,148,40\nbuffer_load_dwordx4 v61, s[52:55], s95 offen lds\nQmvav 132,152,48\n"
"Qe 5\nQmvav 132,156,40\nQe 6\nQmvav 136,160,48\nQe 7\nQmvav 136,164,40\ns_add_u32 s95, s87, 23552\nQk 46,4,5\n"
"Qk 47,6,7\nQmvav 140,168,48\nbuffer_load_dwordx4 v61, s[52:55], s95 offen offset:1024 lds\nQmvav 140,172,40\nQe 8\nQcvaa 0,112,64,56\nQe 9\nQmvaa 0,116,68\n"
"Qe 10\nQmvaa 0,120,72\ns_add_u32 m0, s96, 8192\ns_add_u32 s95, s87, 45056\nQe 11\nQmvaa 0,124,76\nbuffer_load_dwordx4 v61, s[52:55], s95 offen lds\nQcvaa 4,128,64,56\n"
"Qk 48,8,9\nQk 49,10,11\nQmvaa 4,132,68\nQe 12\nQmvaa 4,136,72\nQe 13\nQmvaa 4,140,76\ns_add_u32 s95, s87, 64512\n"
"Qe 14\nQmvav 160,144,52\nbuffer_load_dwordx4 v61, s[52:55], s95 offen offset:1024 lds\nQmvav 160,148,44\nQe 15\nQmvav 164,152,52\nQk 50,12,13\nQk 51,14,15\n"
"Qmvav 164,156,44\nQe 0\nQmvav 168,160,52\ns_add_u32 m0, s96, 16384\ns_add_u32 s95, s87, 4096\nQmvav 168,164,44\nbuffer_load_dwordx4 v62, s[52:55], s95 offen lds\nQmvav 172,168,52\n"
"Qe 1\nQmvav 172,172,44\nQe 2\nQcvaa 8,112,80,56\nQe 3\nQmvaa 8,116,84\ns_add_u32 s95, s87, 44032\nQk 16,0,1\n"
"Qk 17,2,3\nQmvaa 8,120,88\nbuffer_load_dwordx4 v62, s[52:55], s95 offen offset:1024 lds\nQmvaa 8,124,92\nQe 4\nQcvaa 12,128,80,56\nQe 5\nQmvaa 12,132,84\n"
"Qe 6\nv_smfmac_f32_16x16x64_bf16 a[180:183], a[204:207], v[44:51], v77\ns_add_u32 m0, s96, 24576\ns_add_u32 s95, s87, 4096\nQk 18,4,5\nQmvaa 12,136,88\nbuffer_load_dwordx4 v63, s[52:55], s95 offen lds\nQmvaa 12,140,92\n"
"Qe 7\nQmvav 192,144,20\nQk 19,6,7\nQmvav 192,148,48\nQmvav 196,152,20\ns_add_u32 s95, s87, 23552\nQe 8\nQmvav 196,156,48\n"
"buffer_load_dwordx4 v63, s[52:55], s95 offen offset:1024 lds\nQmvav 200,160,20\nQe 9\nQmvav 200,164,48\nQe 10\nQmvav 204,168,20\nQmvav 204,172,48\ns_add_u32 m0, s96, 32768\n"
"s_add_u32 s95, s87, 45056\nQe 11\nQcvaa 0,112,96,56\nQda 112,68,33024\nbuffer_load_dwordx4 v63, s[52:55], s95 offen lds\nQk 20,8,9\nQmvaa 0,116,100\nQda 116,68,33536\n"
"Qk 21,10,11\nQmvaa 0,120,104\nQda 120,68,34048\nQmvaa 0,124,108\nQda 124,68,34560\nQcvaa 4,128,96,56\nQda 128,68,35072\ns_add_u32 s95, s87, 64512\n"
"Qmvaa 4,132,100\nQda 132,68,35584\nbuffer_load_dwordx4 v63, s[52:55], s95 offen offset:1024 lds\nQmvaa 4,136,104\nQda 136,68,36096\nQmvaa 4,140,108\nQda 140,68,36608\nQmvav 224,144,24\n"
"Qe 12\nQmvav 224,148,16\ns_add_u32 m0, s96, 40960\ns_mov_b32 s95, 4096\nQmvav 228,152,24\nbuffer_load_dwordx4 v61, s[52:55], s95 offen lds\nQe 13\nQmvav 228,156,16\n"
"Qe 14\nQmvav 232,160,24\nQe 15\nQmvav 232,164,16\nQk 22,12,13\nQk 23,14,15\nQmvav 236,168,24\ns_mov_b32 s95, 23552\n"
"Qe 0\nQmvav 236,172,16\nbuffer_load_dwordx4 v61, s[52:55], s95 offen offset:1024 lds\ns_waitcnt lgkmcnt(0)\nQcvaa 8,112,0,56\nQe 1\nQmvaa 8,116,4\nv_subrev_u32 v68, 66048, v68\n"
"Qk 24,0,1\nQmvaa 8,120,8\nQe 2\nQmvaa 8,124,12\ns_add_u32 m0, s96, 49152\ns_mov_b32 s95, 45056\nQe 3\nQcvaa 12,128,0,56\n"
"buffer_load_dwordx4 v61, s[52:55], s95 offen lds\nQk 25,2,3\nQmvaa 12,132,4\nQe 4\nv_smfmac_f32_16x16x64_bf16 a[184:187], a[204:207], v[16:23], v77\nQmvaa 12,136,8\nQe 5\nQmvaa 12,140,12\n"
"s_mov_b32 s95, 64512\nQe 6\nQmaav 208,144,28\nbuffer_load_dwordx4 v61, s[52:55], s95 offen offset:1024 lds\nQmaav 208,148,20\nQmaav 212,152,28\nQk 26,4,5\nQmaav 212,156,20\n"
"Qmaav 216,160,28\ns_add_u32 m0, s96, 57344\ns_mov_b32 s95, 4096\nQmaav 216,164,20\nbuffer_load_dwordx4 v62, s[52:55], s95 offen lds\nQe 7\nQmaav 220,168,28\nQk 27,6,7\n"
"Qmaav 220,172,20\nQe 8\nQcvaa 0,112,16,56\nQe 9\nQmvaa 0,116,20\ns_mov_b32 s95, 44032\nQe 10\nQmvaa 0,120,24\n"
"buffer_load_dwordx4 v62, s[52:55], s95 offen offset:1024 lds\nQe 11\nQmvaa 0,124,28\nQk 28,8,9\nQk 29,10,11\nQcvaa 4,128,16,56\nQe 12\nQmvaa 4,132,20\n"
"Qe 13\nQmvaa 4,136,24\ns_add_u32 m0, s96, 132096\ns_mov_b32 s95, 4096\nQe 14\nQmvaa 4,140,28\nbuffer_load_dwordx4 v63, s[52:55], s95 offen lds\nQe 15\n"
"Qmaav 240,144,32\nQta 144,71,0\nQta 146,71,2048\nQmaav 240,148,24\nQta 148,71,33024\nQta 150,71,35072\nQmaav 244,152,32\nQta 152,71,256\n"
"Qta 154,71,2304\nQmaav 244,156,24\nQta 156,71,33280\nQta 158,71,35328\ns_mov_b32 s95, 23552\nQmaav 248,160,32\nQta 160,71,512\nQta 162,71,2560\n"
"buffer_load_dwordx4 v63, s[52:55], s95 offen offset:1024 lds\nQmaav 248,164,24\nQta 164,71,33536\nQta 166,71,35584\nQmaav 252,168,32\nQta 168,71,768\nQta 170,71,2816\nQmaav 252,172,24\n"
"Qta 172,71,33792\nQta 174,71,35840\nQcvaa 8,112,32,56\nv_add_u32 v75, 1, v75\nQk 30,12,13\nQmvaa 8,116,36\nds_write_b32 v73, v75\nQmvaa 8,120,40\n"
"Qk 31,14,15\nQmvaa 8,124,44\nQe 0\nQcvaa 12,128,32,56\nQe 1\nQmvaa 12,132,36\nQe 2\nv_smfmac_f32_16x16x64_bf16 a[188:191], a[204:207], v[24:31], v77\n"
"Qk 32,0,1\nQmvaa 12,136,40\ns_waitcnt lgkmcnt(1)\nQmvaa 12,140,44\nQe 3\nQmvav 80,144,36\nQk 33,2,3\nQmvav 80,148,28\n"
"Qe 4\nQmvav 84,152,36\nQe 5\nQmvav 84,156,28\nQe 6\nQmvav 88,160,36\nQe 7\nQmvav 88,164,28\n"
"Qk 34,4,5\nQk 35,6,7\nQmvav 92,168,36\nQe 8\nQmvav 92,172,28\nQe 9\nQcvaa 0,112,48,56\nQe 10\n"
"Qmvaa 0,116,52\nds_read_b32 v66, v74\nQk 36,8,9\nQmvaa 0,120,56\nQe 11\nQmvaa 0,124,60\nQk 37,10,11\nQcvaa 4,128,48,56\n"
"Qe 12\nQmvaa 4,132,52\nQe 13\nQmvaa 4,136,56\ns_waitcnt lgkmcnt(0)\nv_cmp_gt_u32 vcc, v75, v66\nQmvaa 4,140,60\ns_cbranch_vccnz .Lsa31x\n"
".Lra31x:\nQk 38,12,13\nQmvav 112,144,40\ns_add_u32 m0, s96, 140288\ns_mov_b32 s95, 45056\nQmvav 112,148,32\nbuffer_load_dwordx4 v63, s[52:55], s95 offen lds\nQmvav 116,152,40\n"
"Qe 14\nQmvav 116,156,32\nQe 15\nQmvav 120,160,40\nQk 39,14,15\nQmvav 120,164,32\ns_mov_b32 s95, 64512\nQe 0\n"
"Qmvav 124,168,40\nbuffer_load_dwordx4 v63, s[52:55], s95 offen offset:1024 lds\nQmvav 124,172,32\nQe 1\nQcvaa 8,112,64,56\nQe 2\nQmvaa 8,116,68\nQe 3\n"
"Qmvaa 8,120,72\ns_add_u32 m0, s96, 148480\ns_add_u32 s95, s87, 86016\nQk 40,0,1\nQk 41,2,3\nQmvaa 8,124,76\nbuffer_load_dwordx4 v61, s[52:55], s95 offen lds\nQcvaa 12,128,64,56\n"
"Qe 4\nQmvaa 12,132,68\nQe 5\nv_smfmac_f32_16x16x64_bf16 a[192:195], a[204:207], v[32:39], v77\nQk 42,4,5\nQmvaa 12,136,72\ns_add_u32 s95, s87, 105472\nQe 6\n"
"Qmvaa 12,140,76\nbuffer_load_dwordx4 v61, s[52:55], s95 offen offset:1024 lds\nQmvav 144,144,44\nQe 7\nQmvav 144,148,36\nQk 43,6,7\nQmvav 148,152,44\nQe 8\n"
"Qmvav 148,156,36\ns_add_u32 m0, s96, 65536\ns_add_u32 s95, s87, 126976\nQmvav 152,160,44\nbuffer_load_dwordx4 v61, s[52:55], s95 offen lds\nQmvav 152,164,36\nQe 9\nQmvav 156,168,44\n"
"Qe 10\nQmvav 156,172,36\nQe 11\nQcvaa 0,112,80,56\ns_add_u32 s95, s87, 146432\nQk 44,8,9\nQk 45,10,11\nQmvaa 0,116,84\n"
"buffer_load_dwordx4 v61, s[52:55], s95 offen offset:1024 lds\nQmvaa 0,120,88\nQe 12\nQmvaa 0,124,92\nQe 13\nQcvaa 4,128,80,56\nQe 14\nQmvaa 4,132,84\n"
"s_add_u32 m0, s96, 73728\ns_add_u32 s95, s87, 86016\nQe 15\nQmvaa 4,136,88\nbuffer_load_dwordx4 v62, s[52:55], s95 offen lds\nQmvaa 4,140,92\nQk 46,12,13\nQk 47,14,15\n"
"Qmvav 176,144,48\nQmvav 176,148,40\nQmvav 180,152,48\ns_add_u32 s95, s87, 125952\nQmvav 180,156,40\nbuffer_load_dwordx4 v62, s[52:55], s95 offen offset:1024 lds\nQmvav 184,160,48\nQe 0\n"
"Qmvav 184,164,40\nQe 1\nQmvav 188,168,48\nQmvav 188,172,40\ns_add_u32 m0, s96, 81920\ns_add_u32 s95, s87, 86016\nQe 2\nQcvaa 8,112,96,56\n"
"buffer_load_dwordx4 v63, s[52:55], s95 offen lds\nQk 48,0,1\nQmvaa 8,116,100\nQmvaa 8,120,104\nQmvaa 8,124,108\nQcvaa 12,128,96,56\ns_add_u32 s95, s87, 105472\nQmvaa 12,132,100\n"
"buffer_load_dwordx4 v63, s[52:55], s95 offen offset:1024 lds\nv_smfmac_f32_16x16x64_bf16 a[196:199], a[204:207], v[40:47], v77\nQmvaa 12,136,104\nQmvaa 12,140,108\nQmvav 208,144,16\nQta 112,71,34048\ns_add_u32 m0, s96, 90112\ns_add_u32 s95, s87, 126976\n"
"Qmvav 208,148,44\nQta 114,71,36096\nbuffer_load_dwordx4 v63, s[52:55], s95 offen lds\nQe 3\nQmvav 212,152,16\nQta 116,71,34304\nQk 49,2,3\nQmvav 212,156,44\n"
"Qta 118,71,36352\nQe 4\nQmvav 216,160,16\nQta 120,71,34560\nQe 5\nQmvav 216,164,44\nQta 122,71,36608\ns_add_u32 s95, s87, 146432\n"
"Qe 6\nQmvav 220,168,16\n.Lpg12:\ns_cbranch_execz .Ltr6\nQta 124,71,34816\nbuffer_load_dwordx4 v63, s[52:55], s95 offen offset:1024 lds\nQe 7\nQmvav 220,172,44\n"
"Qta 126,71,36864\nQk 50,4,5\nQk 51,6,7\nQe 8\nQe 9\nQe 10\nQe 11\nQk 52,8,9\n"
"Qk 53,10,11\nQe 12\nQe 13\nQmvav 240,144,20\nQe 14\nQmvav 240,148,48\nQmvav 244,152,20\nQmvav 244,156,48\n"
"Qk 54,12,13\nQmvav 248,160,20\nQmvav 248,164,48\nQmvav 252,168,20\nQe 15\nQmvav 252,172,48\nQk 55,14,15\ns_nop 1\n"
"v_smfmac_f32_16x16x64_bf16 a[200:203], a[204:207], v[48:55], v77\nQmaav 224,144,24\nv_accvgpr_read_b32 v0, a176\nv_accvgpr_read_b32 v1, a177\nQmaav 224,148,52\nv_accvgpr_read_b32 v6, a178\nv_accvgpr_read_b32 v7, a179\nQmaav 228,152,24\n"
"v_accvgpr_read_b32 v8, a180\nv_accvgpr_read_b32 v9, a181\nQmaav 228,156,52\nv_accvgpr_read_b32 v14, a182\nv_accvgpr_read_b32 v15, a183\nQmaav 232,160,24\nv_accvgpr_read_b32 v16, a184\nv_accvgpr_read_b32 v17, a185\n"
"Qmaav 232,164,52\nv_accvgpr_read_b32 v22, a186\nv_accvgpr_read_b32 v23, a187\nQmaav 236,168,24\nv_accvgpr_read_b32 v2, a190\nv_accvgpr_read_b32 v3, a191\nQmaav 236,172,52\ns_waitcnt lgkmcnt(0)\n"
"v_accvgpr_read_b32 v24, a188\nQmvav 96,112,28\nv_accvgpr_read_b32 v25, a189\nv_accvgpr_read_b32 v4, a192\nQmvav 100,116,28\nv_accvgpr_read_b32 v5, a193\nv_accvgpr_read_b32 v10, a194\nQmvav 104,120,28\n"
"v_accvgpr_read_b32 v11, a195\nv_accvgpr_read_b32 v12, a196\nQmvav 108,124,28\nv_accvgpr_read_b32 v13, a197\nv_accvgpr_read_b32 v18, a198\nQmvav 128,112,32\nv_accvgpr_read_b32 v19, a199\nv_accvgpr_read_b32 v20, a200\n"
"Qmvav 132,116,32\nv_accvgpr_read_b32 v21, a201\nv_accvgpr_read_b32 v26, a202\nQmvav 136,120,32\nv_accvgpr_read_b32 v27, a203\nQa 8,8,9\nQmvav 140,124,32\nQa 10,10,11\n"
"Qa 8,8,10\nQmvav 160,112,36\nQa 16,16,17\nQa 18,18,19\nQmvav 164,116,36\nQa 16,16,18\nQa 4,4,5\nQmvav 168,120,36\n"
"Qa 6,6,7\nQa 4,4,6\nQmvav 172,124,36\nQa 12,12,13\nQa 14,14,15\nQmvav 192,112,40\nQa 12,12,14\nQa 0,0,1\n"
"Qmvav 196,116,40\nQa 2,2,3\nQa 0,0,2\nQmvav 200,120,40\nQa 24,24,25\nQa 26,26,27\nQmvav 204,124,40\nQa 24,24,26\n"
"Qa 20,20,21\nQmvav 224,112,44\nQa 22,22,23\nQa 20,20,22\nQmvav 228,116,44\nv_max3_u32 v9, v8, v16, v4\nv_max3_u32 v9, v9, v12, v0\nQmvav 232,120,44\n"
"v_max3_u32 v9, v9, v24, v20\nv_min3_u32 v11, v8, v16, v4\nQmvav 236,124,44\nv_min3_u32 v11, v11, v12, v0\nv_min3_u32 v11, v11, v24, v20\nQmaav 208,112,48\nQmaav 212,116,48\nQmaav 216,120,48\n"
"Qmaav 220,124,48\nQmaav 240,112,52\nQmaav 244,116,52\nQmaav 248,120,52\nQmaav 252,124,52\nv_cmp_lt_u32 vcc, 0x44800000, v9\ns_mov_b64 s[74:75], vcc\nv_cmp_gt_u32 vcc, 0x2b800000, v11\n"
"s_or_b64 s[74:75], s[74:75], vcc\ns_cmp_lg_u64 s[74:75], 0\ns_cbranch_scc1 .Lfb\n.Lfbret1:\nv_rcp_f32 v0, v0\nv_rcp_f32 v4, v4\nv_rcp_f32 v8, v8\nv_rcp_f32 v12, v12\n"
"v_rcp_f32 v16, v16\nv_rcp_f32 v20, v20\nv_rcp_f32 v24, v24\ns_mov_b32 s56, %8\ns_and_b32 s57, %9, 0xffff\ns_mov_b32 s58, 1835008\ns_lshl_b32 s68, %19, 4\ns_cmp_eq_u32 s70, 0\n"
"s_cselect_b32 s68, s68, -1\nv_mov_b32 v77, 0x7ffff000\nv_cmp_eq_u32 vcc, s68, v60\ns_nop 1\nv_cndmask_b32 v77, v77, v76, vcc\ns_lshl_b32 s68, s93, 12\ns_add_u32 s68, s68, %10\nv_lshrrev_b32 v66, 9, v60\n"
"v_lshlrev_b32 v66, 11, v66\nv_bfe_u32 v67, v60, 1, 8\nv_add3_u32 v68, v66, v67, s68\nv_add_u32 v69, 65536, v68\nv_add_u32 v70, 131072, v68\ns_mov_b64 exec, 0\ns_cmp_eq_u32 s70, 0\ns_cbranch_scc1 .Lpg16\n"
"s_cmp_eq_u32 s70, 1\ns_cbranch_scc1 .Lpg17\ns_cmp_eq_u32 s70, 2\ns_cbranch_scc1 .Lpg18\ns_branch .Lpg19\n.Ltr8:\ns_mov_b64 exec, -1\ns_mov_b32 s31, 0\n"
"v_pk_mul_f32 v[80:81], v[80:81], v[0:1] op_sel_hi:[1,0]\nv_pk_mul_f32 v[82:83], v[82:83], v[0:1] op_sel_hi:[1,0]\nv_pk_mul_f32 v[84:85], v[84:85], v[0:1] op_sel_hi:[1,0]\nv_pk_mul_f32 v[86:87], v[86:87], v[0:1] op_sel_hi:[1,0]\nQk 80,80,81\nQk 81,82,83\nQk 82,84,85\nQk 83,86,87\n"
"buffer_load_dwordx4 a[0:3], v60, s[48:51], s31 offen offset:0\nv_pk_mul_f32 v[112:113], v[112:113], v[4:5] op_sel_hi:[1,0]\nv_pk_mul_f32 v[114:115], v[114:115], v[4:5] op_sel_hi:[1,0]\nv_pk_mul_f32 v[116:117], v[116:117], v[4:5] op_sel_hi:[1,0]\nv_pk_mul_f32 v[118:119], v[118:119], v[4:5] op_sel_hi:[1,0]\nQk 112,112,113\nQk 113,114,115\nQk 114,116,117\n"
"Qk 115,118,119\nbuffer_load_dwordx4 a[4:7], v60, s[48:51], s31 offen offset:1024\nv_pk_mul_f32 v[144:145], v[144:145], v[8:9] op_sel_hi:[1,0]\nv_pk_mul_f32 v[146:147], v[146:147], v[8:9] op_sel_hi:[1,0]\nv_pk_mul_f32 v[148:149], v[148:149], v[8:9] op_sel_hi:[1,0]\nv_pk_mul_f32 v[150:151], v[150:151], v[8:9] op_sel_hi:[1,0]\nQk 144,144,145\nQk 145,146,147\n"
"Qk 146,148,149\nQk 147,150,151\nbuffer_load_dwordx4 a[8:11], v60, s[48:51], s31 offen offset:2048\nv_pk_mul_f32 v[176:177], v[176:177], v[12:13] op_sel_hi:[1,0]\nv_pk_mul_f32 v[178:179], v[178:179], v[12:13] op_sel_hi:[1,0]\nv_pk_mul_f32 v[180:181], v[180:181], v[12:13] op_sel_hi:[1,0]\nv_pk_mul_f32 v[182:183], v[182:183], v[12:13] op_sel_hi:[1,0]\nQk 176,176,177\n"
"Qk 177,178,179\nQk 178,180,181\nQk 179,182,183\nbuffer_load_dwordx4 a[12:15], v60, s[48:51], s31 offen offset:3072\ns_mov_b32 s31, 4096\nv_pk_mul_f32 v[208:209], v[208:209], v[16:17] op_sel_hi:[1,0]\nv_pk_mul_f32 v[210:211], v[210:211], v[16:17] op_sel_hi:[1,0]\nv_pk_mul_f32 v[212:213], v[212:213], v[16:17] op_sel_hi:[1,0]\n"
"v_pk_mul_f32 v[214:215], v[214:215], v[16:17] op_sel_hi:[1,0]\nQk 208,208,209\nQk 209,210,211\nQk 210,212,213\nQk 211,214,215\nbuffer_load_dwordx4 a[16:19], v60, s[48:51], s31 offen offset:0\nv_pk_mul_f32 v[240:241], v[240:241], v[20:21] op_sel_hi:[1,0]\nv_pk_mul_f32 v[242:243], v[242:243], v[20:21] op_sel_hi:[1,0]\n"
"v_pk_mul_f32 v[244:245], v[244:245], v[20:21] op_sel_hi:[1,0]\nv_pk_mul_f32 v[246:247], v[246:247], v[20:21] op_sel_hi:[1,0]\nQk 240,240,241\nQk 241,242,243\nQk 242,244,245\nQk 243,246,247\nbuffer_load_dwordx4 a[20:23], v60, s[48:51], s31 offen offset:1024\nv_accvgpr_read_b32 v84, a224\n"
"v_accvgpr_read_b32 v85, a225\nv_accvgpr_read_b32 v86, a226\nv_accvgpr_read_b32 v87, a227\nv_accvgpr_read_b32 v2, a228\nv_accvgpr_read_b32 v3, a229\nv_accvgpr_read_b32 v6, a230\nv_accvgpr_read_b32 v7, a231\nv_pk_mul_f32 v[84:85], v[84:85], v[24:25] op_sel_hi:[1,0]\n"
"v_pk_mul_f32 v[86:87], v[86:87], v[24:25] op_sel_hi:[1,0]\nv_pk_mul_f32 v[2:3], v[2:3], v[24:25] op_sel_hi:[1,0]\nv_pk_mul_f32 v[6:7], v[6:7], v[24:25] op_sel_hi:[1,0]\nQk 84,84,85\nQk 85,86,87\nQk 86,2,3\nQk 87,6,7\nbuffer_load_dwordx4 a[24:27], v60, s[48:51], s31 offen offset:2048\n"
"s_mov_b32 s31, 14336\nv_pk_mul_f32 v[88:89], v[88:89], v[0:1] op_sel_hi:[1,0]\nv_pk_mul_f32 v[90:91], v[90:91], v[0:1] op_sel_hi:[1,0]\nv_pk_mul_f32 v[92:93], v[92:93], v[0:1] op_sel_hi:[1,0]\nv_pk_mul_f32 v[94:95], v[94:95], v[0:1] op_sel_hi:[1,0]\nQk 88,88,89\nQk 89,90,91\nQk 90,92,93\n"
"Qk 91,94,95\nbuffer_load_dwordx4 a[28:31], v60, s[48:51], s31 offen offset:0\nv_pk_mul_f32 v[120:121], v[120:121], v[4:5] op_sel_hi:[1,0]\nv_pk_mul_f32 v[122:123], v[122:123], v[4:5] op_sel_hi:[1,0]\nv_pk_mul_f32 v[124:125], v[124:125], v[4:5] op_sel_hi:[1,0]\nv_pk_mul_f32 v[126:127], v[126:127], v[4:5] op_sel_hi:[1,0]\nQk 120,120,121\nQk 121,122,123\n"
"Qk 122,124,125\nQk 123,126,127\nbuffer_load_dwordx4 a[32:35], v60, s[48:51], s31 offen offset:1024\nv_pk_mul_f32 v[152:153], v[152:153], v[8:9] op_sel_hi:[1,0]\nv_pk_mul_f32 v[154:155], v[154:155], v[8:9] op_sel_hi:[1,0]\nv_pk_mul_f32 v[156:157], v[156:157], v[8:9] op_sel_hi:[1,0]\nv_pk_mul_f32 v[158:159], v[158:159], v[8:9] op_sel_hi:[1,0]\nQk 152,152,153\n"
"Qk 153,154,155\nQk 154,156,157\nQk 155,158,159\nbuffer_load_dwordx4 a[36:39], v60, s[48:51], s31 offen offset:2048\nv_pk_mul_f32 v[184:185], v[184:185], v[12:13] op_sel_hi:[1,0]\nv_pk_mul_f32 v[186:187], v[186:187], v[12:13] op_sel_hi:[1,0]\nv_pk_mul_f32 v[188:189], v[188:189], v[12:13] op_sel_hi:[1,0]\nv_pk_mul_f32 v[190:191], v[190:191], v[12:13] op_sel_hi:[1,0]\n"
"Qk 184,184,185\nQk 185,186,187\nQk 186,188,189\nQk 187,190,191\nbuffer_load_dwordx4 a[40:43], v60, s[48:51], s31 offen offset:3072\ns_mov_b32 s31, 18432\nv_pk_mul_f32 v[216:217], v[216:217], v[16:17] op_sel_hi:[1,0]\nv_pk_mul_f32 v[218:219], v[218:219], v[16:17] op_sel_hi:[1,0]\n"
"v_pk_mul_f32 v[220:221], v[220:221], v[16:17] op_sel_hi:[1,0]\nv_pk_mul_f32 v[222:223], v[222:223], v[16:17] op_sel_hi:[1,0]\nQk 216,216,217\nQk 217,218,219\nQk 218,220,221\nQk 219,222,223\nbuffer_load_dwordx4 a[44:47], v60, s[48:51], s31 offen offset:0\nv_pk_mul_f32 v[248:249], v[248:249], v[20:21] op_sel_hi:[1,0]\n"
"v_pk_mul_f32 v[250:251], v[250:251], v[20:21] op_sel_hi:[1,0]\nv_pk_mul_f32 v[252:253], v[252:253], v[20:21] op_sel_hi:[1,0]\nv_pk_mul_f32 v[254:255], v[254:255], v[20:21] op_sel_hi:[1,0]\nQk 248,248,249\nQk 249,250,251\nQk 250,252,253\nQk 251,254,255\nbuffer_load_dwordx4 a[48:51], v60, s[48:51], s31 offen offset:1024\n"
"v_accvgpr_read_b32 v116, a232\nv_accvgpr_read_b32 v117, a233\nv_accvgpr_read_b32 v118, a234\nv_accvgpr_read_b32 v119, a235\nv_accvgpr_read_b32 v10, a236\nv_accvgpr_read_b32 v11, a237\nv_accvgpr_read_b32 v14, a238\nv_accvgpr_read_b32 v15, a239\n"
"v_pk_mul_f32 v[116:117], v[116:117], v[24:25] op_sel_hi:[1,0]\nv_pk_mul_f32 v[118:119], v[118:119], v[24:25] op_sel_hi:[1,0]\nv_pk_mul_f32 v[10:11], v[10:11], v[24:25] op_sel_hi:[1,0]\nv_pk_mul_f32 v[14:15], v[14:15], v[24:25] op_sel_hi:[1,0]\nQk 116,116,117\nQk 117,118,119\nQk 118,10,11\nQk 119,14,15\n"
"buffer_load_dwordx4 a[52:55], v60, s[48:51], s31 offen offset:2048\ns_waitcnt vmcnt(36)\ns_barrier\nv_pk_mul_f32 v[96:97], v[96:97], v[0:1] op_sel_hi:[1,0]\nv_pk_mul_f32 v[98:99], v[98:99], v[0:1] op_sel_hi:[1,0]\nv_pk_mul_f32 v[100:101], v[100:101], v[0:1] op_sel_hi:[1,0]\nv_pk_mul_f32 v[102:103], v[102:103], v[0:1] op_sel_hi:[1,0]\nQk 96,96,97\n"
"Qk 97,98,99\nQk 98,100,101\nQk 99,102,103\nQta 56,68,0\nv_pk_mul_f32 v[128:129], v[128:129], v[4:5] op_sel_hi:[1,0]\nv_pk_mul_f32 v[130:131], v[130:131], v[4:5] op_sel_hi:[1,0]\nv_pk_mul_f32 v[132:133], v[132:133], v[4:5] op_sel_hi:[1,0]\nv_pk_mul_f32 v[134:135], v[134:135], v[4:5] op_sel_hi:[1,0]\n"
"Qk 128,128,129\nQk 129,130,131\nQk 130,132,133\nQk 131,134,135\nQta 58,68,1024\nv_pk_mul_f32 v[160:161], v[160:161], v[8:9] op_sel_hi:[1,0]\nv_pk_mul_f32 v[162:163], v[162:163], v[8:9] op_sel_hi:[1,0]\nv_pk_mul_f32 v[164:165], v[164:165], v[8:9] op_sel_hi:[1,0]\n"
"v_pk_mul_f32 v[166:167], v[166:167], v[8:9] op_sel_hi:[1,0]\nQk 160,160,161\nQk 161,162,163\nQk 162,164,165\nQk 163,166,167\nQta 60,68,8192\nv_pk_mul_f32 v[192:193], v[192:193], v[12:13] op_sel_hi:[1,0]\nv_pk_mul_f32 v[194:195], v[194:195], v[12:13] op_sel_hi:[1,0]\n"
"v_pk_mul_f32 v[196:197], v[196:197], v[12:13] op_sel_hi:[1,0]\nv_pk_mul_f32 v[198:199], v[198:199], v[12:13] op_sel_hi:[1,0]\nQk 192,192,193\nQk 193,194,195\nQk 194,196,197\nQk 195,198,199\nQta 62,68,9216\nv_pk_mul_f32 v[224:225], v[224:225], v[16:17] op_sel_hi:[1,0]\n"
"v_pk_mul_f32 v[226:227], v[226:227], v[16:17] op_sel_hi:[1,0]\nv_pk_mul_f32 v[228:229], v[228:229], v[16:17] op_sel_hi:[1,0]\nv_pk_mul_f32 v[230:231], v[230:231], v[16:17] op_sel_hi:[1,0]\nQk 224,224,225\nQk 225,226,227\nQk 226,228,229\nQk 227,230,231\nQta 64,68,256\n"
"v_accvgpr_read_b32 v148, a208\nv_accvgpr_read_b32 v149, a209\nv_accvgpr_read_b32 v150, a210\nv_accvgpr_read_b32 v151, a211\nv_accvgpr_read_b32 v10, a212\nv_accvgpr_read_b32 v11, a213\nv_accvgpr_read_b32 v14, a214\nv_accvgpr_read_b32 v15, a215\n"
"v_pk_mul_f32 v[148:149], v[148:149], v[20:21] op_sel_hi:[1,0]\nv_pk_mul_f32 v[150:151], v[150:151], v[20:21] op_sel_hi:[1,0]\nv_pk_mul_f32 v[10:11], v[10:11], v[20:21] op_sel_hi:[1,0]\nv_pk_mul_f32 v[14:15], v[14:15], v[20:21] op_sel_hi:[1,0]\nQk 148,148,149\nQk 149,150,151\nQk 150,10,11\nQk 151,14,15\n"
"Qta 66,68,1280\nv_accvgpr_read_b32 v180, a240\nv_accvgpr_read_b32 v181, a241\nv_accvgpr_read_b32 v182, a242\nv_accvgpr_read_b32 v183, a243\nv_accvgpr_read_b32 v18, a244\nv_accvgpr_read_b32 v19, a245\nv_accvgpr_read_b32 v22, a246\n"
"v_accvgpr_read_b32 v23, a247\nv_pk_mul_f32 v[180:181], v[180:181], v[24:25] op_sel_hi:[1,0]\nv_pk_mul_f32 v[182:183], v[182:183], v[24:25] op_sel_hi:[1,0]\nv_pk_mul_f32 v[18:19], v[18:19], v[24:25] op_sel_hi:[1,0]\nv_pk_mul_f32 v[22:23], v[22:23], v[24:25] op_sel_hi:[1,0]\nQk 180,180,181\nQk 181,182,183\nQk 182,18,19\n"
"Qk 183,22,23\nQta 68,68,8448\nv_pk_mul_f32 v[104:105], v[104:105], v[0:1] op_sel_hi:[1,0]\nv_pk_mul_f32 v[106:107], v[106:107], v[0:1] op_sel_hi:[1,0]\nv_pk_mul_f32 v[108:109], v[108:109], v[0:1] op_sel_hi:[1,0]\nv_pk_mul_f32 v[110:111], v[110:111], v[0:1] op_sel_hi:[1,0]\nQk 104,104,105\nQk 105,106,107\n"
"Qk 106,108,109\nQk 107,110,111\nQta 70,68,9472\nv_pk_mul_f32 v[136:137], v[136:137], v[4:5] op_sel_hi:[1,0]\nv_pk_mul_f32 v[138:139], v[138:139], v[4:5] op_sel_hi:[1,0]\nv_pk_mul_f32 v[140:141], v[140:141], v[4:5] op_sel_hi:[1,0]\nv_pk_mul_f32 v[142:143], v[142:143], v[4:5] op_sel_hi:[1,0]\nQk 136,136,137\n"
"Qk 137,138,139\nQk 138,140,141\nQk 139,142,143\nv_pk_mul_f32 v[168:169], v[168:169], v[8:9] op_sel_hi:[1,0]\nv_pk_mul_f32 v[170:171], v[170:171], v[8:9] op_sel_hi:[1,0]\nv_pk_mul_f32 v[172:173], v[172:173], v[8:9] op_sel_hi:[1,0]\nv_pk_mul_f32 v[174:175], v[174:175], v[8:9] op_sel_hi:[1,0]\nQk 168,168,169\n"
"Qk 169,170,171\nQk 170,172,173\nQk 171,174,175\nv_pk_mul_f32 v[200:201], v[200:201], v[12:13] op_sel_hi:[1,0]\nv_pk_mul_f32 v[202:203], v[202:203], v[12:13] op_sel_hi:[1,0]\nv_pk_mul_f32 v[204:205], v[204:205], v[12:13] op_sel_hi:[1,0]\nv_pk_mul_f32 v[206:207], v[206:207], v[12:13] op_sel_hi:[1,0]\nQk 200,200,201\n"
"Qk 201,202,203\nQk 202,204,205\nQk 203,206,207\nv_pk_mul_f32 v[232:233], v[232:233], v[16:17] op_sel_hi:[1,0]\nv_pk_mul_f32 v[234:235], v[234:235], v[16:17] op_sel_hi:[1,0]\nv_pk_mul_f32 v[236:237], v[236:237], v[16:17] op_sel_hi:[1,0]\nv_pk_mul_f32 v[238:239], v[238:239], v[16:17] op_sel_hi:[1,0]\nQk 232,232,233\n"
"Qk 233,234,235\nQk 234,236,237\nQk 235,238,239\nv_accvgpr_read_b32 v212, a216\nv_accvgpr_read_b32 v213, a217\nv_accvgpr_read_b32 v214, a218\nv_accvgpr_read_b32 v215, a219\nv_accvgpr_read_b32 v18, a220\n"
"v_accvgpr_read_b32 v19, a221\nv_accvgpr_read_b32 v22, a222\nv_accvgpr_read_b32 v23, a223\nv_pk_mul_f32 v[212:213], v[212:213], v[20:21] op_sel_hi:[1,0]\nv_pk_mul_f32 v[214:215], v[214:215], v[20:21] op_sel_hi:[1,0]\nv_pk_mul_f32 v[18:19], v[18:19], v[20:21] op_sel_hi:[1,0]\nv_pk_mul_f32 v[22:23], v[22:23], v[20:21] op_sel_hi:[1,0]\nQk 212,212,213\n"
"Qk 213,214,215\nQk 214,18,19\nQk 215,22,23\nv_accvgpr_read_b32 v244, a248\nv_accvgpr_read_b32 v245, a249\nv_accvgpr_read_b32 v246, a250\nv_accvgpr_read_b32 v247, a251\nv_accvgpr_read_b32 v2, a252\n"
"v_accvgpr_read_b32 v3, a253\nv_accvgpr_read_b32 v6, a254\nv_accvgpr_read_b32 v7, a255\nv_pk_mul_f32 v[244:245], v[244:245], v[24:25] op_sel_hi:[1,0]\nv_pk_mul_f32 v[246:247], v[246:247], v[24:25] op_sel_hi:[1,0]\nv_pk_mul_f32 v[2:3], v[2:3], v[24:25] op_sel_hi:[1,0]\nv_pk_mul_f32 v[6:7], v[6:7], v[24:25] op_sel_hi:[1,0]\nQk 244,244,245\n"
"Qk 245,246,247\nQk 246,2,3\nQk 247,6,7\ns_waitcnt lgkmcnt(4)\nQzaav 116,56,80\ns_add_u32 m0, s96, 98304\ns_mov_b32 s95, 86016\nQmaav 116,60,88\n"
"buffer_load_dwordx4 v61, s[52:55], s95 offen lds\nQzaav 120,56,112\nQmaav 120,60,120\nQzaav 124,56,144\nQta 72,68,16384\ns_mov_b32 s95, 105472\nQmaav 124,60,152\nbuffer_load_dwordx4 v61, s[52:55], s95 offen offset:1024 lds\n"
"Qzaav 128,56,176\nQmaav 128,60,184\nQta 74,68,16896\nQzaav 132,56,208\nQmaav 132,60,216\nQzaav 136,56,240\nQta 76,68,17408\nQmaav 136,60,248\n"
"s_add_u32 s88, s71, 0\nQzaav 140,56,84\nbuffer_store_dwordx4 v[80:83], v60, s[56:59], s88 offen offset:0 sc1\nQmaav 140,60,116\nQta 78,68,17920\ns_waitcnt lgkmcnt(4)\nQzaav 144,64,80\nQmaav 144,68,88\n"
"Qzaav 148,64,112\nQmaav 148,68,120\nQzaav 152,64,144\nQta 56,68,768\nQmaav 152,68,152\nQzaav 156,64,176\nQmaav 156,68,184\nQta 58,68,1792\n"
"s_add_u32 m0, s96, 106496\ns_mov_b32 s95, 126976\nQzaav 160,64,208\nbuffer_load_dwordx4 v61, s[52:55], s95 offen lds\nQmaav 160,68,216\nQzaav 164,64,240\nQta 60,68,8960\nQmaav 164,68,248\n"
"s_mov_b32 s95, 146432\nQzaav 168,64,84\nbuffer_load_dwordx4 v61, s[52:55], s95 offen offset:1024 lds\nQmaav 168,68,116\nQta 62,68,9984\ns_waitcnt lgkmcnt(4)\nQzaav 172,72,80\nQmaav 172,76,88\n"
"Qzaav 176,72,112\nQmaav 176,76,120\nQzaav 180,72,144\nQta 64,68,16640\nQmaav 180,76,152\nbuffer_store_dwordx4 v[112:115], v60, s[56:59], s88 offen offset:1024 sc1\nQzaav 184,72,176\nQmaav 184,76,184\n"
"Qta 66,68,17152\nQzaav 188,72,208\nQmaav 188,76,216\nQzaav 192,72,240\nQta 68,68,17664\nv_add_u32 v75, 1, v75\nQmaav 192,76,248\ns_waitcnt vmcnt(34)\n"
"ds_write_b32 v73, v75\nQzaav 196,72,84\nQmaav 196,76,116\nQta 70,68,18176\ns_waitcnt lgkmcnt(5)\nQzvav 28,56,80\ns_add_u32 m0, s96, 114688\ns_mov_b32 s95, 86016\n"
"Qmvav 28,60,88\nbuffer_load_dwordx4 v62, s[52:55], s95 offen lds\nQzvav 32,56,112\nQmvav 32,60,120\nQzvav 36,56,144\nQta 72,68,24576\nQmvav 36,60,152\nds_read_b32 v66, v74\n"
"s_mov_b32 s95, 125952\nQzvav 40,56,176\n.Lpg13:\ns_cbranch_execz .Ltr6\nbuffer_load_dwordx4 v62, s[52:55], s95 offen offset:1024 lds\nQmvav 40,60,184\nQta 74,68,25600\nQzvav 44,56,208\n"
"Qmvav 44,60,216\ns_waitcnt lgkmcnt(1)\nv_cmp_gt_u32 vcc, v75, v66\nQzvav 48,56,240\ns_cbranch_vccnz .Lso40\n.Lro40:\nQmvav 48,60,248\nQta 76,68,32768\n"
"Qzvav 52,56,84\nbuffer_store_dwordx4 v[144:147], v60, s[56:59], s88 offen offset:2048 sc1\nQmvav 52,60,116\ns_waitcnt lgkmcnt(4)\nQzvav 92,64,80\nQta 78,68,33792\nQmvav 92,68,88\nQzvav 124,64,112\n"
"Qmvav 124,68,120\nQzvav 156,64,144\nQta 56,68,24832\nQmvav 156,68,152\nQzvav 188,64,176\nQmvav 188,68,184\nQta 58,68,25856\ns_add_u32 m0, s96, 122880\n"
"s_mov_b32 s95, 86016\nQzvav 220,64,208\nbuffer_load_dwordx4 v63, s[52:55], s95 offen lds\nQmvav 220,68,216\nQzvav 252,64,240\nQta 60,68,33024\nQmvav 252,68,248\ns_mov_b32 s95, 105472\n"
"Qzvav 100,64,84\nbuffer_load_dwordx4 v63, s[52:55], s95 offen offset:1024 lds\nQmvav 100,68,116\nQta 62,68,34048\ns_waitcnt lgkmcnt(4)\nQzaav 200,72,80\nQmaav 200,76,88\nQzaav 204,72,112\n"
"buffer_store_dwordx4 v[176:179], v60, s[56:59], s88 offen offset:3072 sc1\nQmaav 204,76,120\nQzaav 208,72,144\nQta 64,68,25088\nQmaav 208,76,152\nQzaav 212,72,176\nQmaav 212,76,184\nQta 66,68,26112\n"
"Qzaav 216,72,208\nQmaav 216,76,216\nQzaav 220,72,240\nQta 68,68,33280\nQmaav 220,76,248\nQzaav 224,72,84\nQmaav 224,76,116\nQta 70,68,34304\n"
"s_waitcnt lgkmcnt(4)\nQzaav 228,56,80\nQmaav 228,60,88\nQzaav 232,56,112\nQmaav 232,60,120\nQzaav 236,56,144\nQta 72,68,25344\nQmaav 236,60,152\n"
"Qzaav 240,56,176\nQmaav 240,60,184\nQta 74,68,26368\ns_add_u32 s88, s71, 4096\nQzaav 244,56,208\nbuffer_store_dwordx4 v[208:211], v60, s[56:59], s88 offen offset:0 sc1\nQmaav 244,60,216\nQzaav 248,56,240\n"
"Qta 76,68,33536\nQmaav 248,60,248\nQzaav 252,56,84\nQmaav 252,60,116\nQta 78,68,34560\ns_waitcnt lgkmcnt(4)\nQzvav 132,64,80\nQmvav 132,68,88\n"
"Qzvav 164,64,112\nQmvav 164,68,120\nQzvav 196,64,144\nQta 56,68,512\nQmvav 196,68,152\nQzvav 228,64,176\nQmvav 228,68,184\nQta 58,68,1536\n"
"Qzvav 108,64,208\nQmvav 108,68,216\nQzvav 140,64,240\nQta 60,68,8704\nQmvav 140,68,248\nQzvav 172,64,84\nQmvav 172,68,116\nQta 62,68,9728\n"
"s_waitcnt lgkmcnt(4)\nQzvav 204,72,80\nbuffer_store_dwordx4 v[240:243], v60, s[56:59], s88 offen offset:1024 sc1\nQmvav 204,76,88\nQzvav 236,72,112\nQmvav 236,76,120\nQzaav 96,72,144\nQta 64,68,40960\n"
"Qmaav 96,76,152\nQzaav 100,72,176\nQmaav 100,76,184\nQta 66,68,41984\nQzaav 104,72,208\nQmaav 104,76,216\nQzaav 108,72,240\nQta 68,68,49152\n"
"Qmaav 108,76,248\nQzaav 112,72,84\nQmaav 112,76,116\nQta 70,68,50176\ns_waitcnt lgkmcnt(4)\nQzvav 0,56,80\nQmvav 0,60,88\nQzvav 4,56,112\n"
"Qmvav 4,60,120\nQzvav 8,56,144\nQta 72,68,41216\nQmvav 8,60,152\nQzvav 12,56,176\nbuffer_store_dwordx4 v[84:87], v60, s[56:59], s88 offen offset:2048 sc1\nQmvav 12,60,184\nQta 74,68,42240\n"
"Qzvav 16,56,208\nQmvav 16,60,216\nQzvav 20,56,240\nQta 76,68,49408\nQmvav 20,60,248\nQzvav 24,56,84\nQmvav 24,60,116\nQta 78,68,50432\n"
"s_waitcnt vmcnt(15)\ns_waitcnt lgkmcnt(4)\nQmaaa 116,64,0\nQmaaa 116,68,28\nQmaaa 120,64,4\nQmaaa 120,68,32\nQmaaa 124,64,8\nQta 56,68,41472\n"
"Qmaaa 124,68,36\nQmaaa 128,64,12\nQmaaa 128,68,40\nQta 58,68,42496\nQmaaa 132,64,16\nQmaaa 132,68,44\nQmaaa 136,64,20\nQta 60,68,49664\n"
"s_and_b32 s68, %19, 3\nQmaaa 136,68,48\ns_add_u32 s88, s71, 14336\nQmaaa 140,64,24\nbuffer_store_dwordx4 v[88:91], v60, s[56:59], s88 offen offset:0 sc1\nQmaaa 140,68,52\nQta 62,68,50688\ns_waitcnt lgkmcnt(4)\n"
"Qmaaa 144,72,0\ns_cmp_lt_u32 s68, 1\ns_cselect_b32 s68, 40, 32\nQmaaa 144,76,28\ns_mul_i32 s67, s68, 40960\ns_mul_i32 s74, s68, 14336\nQmaaa 148,72,4\ns_and_b32 s68, %19, 3\n"
"Qmaaa 148,76,32\ns_cmp_lt_u32 s68, 2\ns_cselect_b32 s68, 48, 40\nQmaaa 152,72,8\nQta 64,68,41728\ns_mul_i32 s72, s68, 40960\nQmaaa 152,76,36\ns_mul_i32 s75, s68, 14336\n"
"s_and_b32 s68, %19, 3\nQmaaa 156,72,12\ns_cmp_lt_u32 s68, 3\ns_cselect_b32 s68, 56, 48\nQmaaa 156,76,40\nQta 66,68,42752\ns_mul_i32 s73, s68, 40960\nQmaaa 160,72,16\n"
"s_mul_i32 s77, s68, 14336\nv_add_u32 v58, s91, v60\nQmaaa 160,76,44\ns_mul_i32 s68, s93, 28672\ns_sub_u32 s68, 14336, s68\nQmaaa 164,72,20\nQta 68,68,49920\nv_add_u32 v59, s68, v58\n"
"Qmaaa 164,76,48\nQmaaa 168,72,24\nQmaaa 168,76,52\nQta 70,68,50944\ns_waitcnt lgkmcnt(4)\nQmvaa 0,56,0\nQmvaa 0,60,28\nQmvaa 4,56,4\n"
"Qmvaa 4,60,32\nQmvaa 8,56,8\nQta 72,68,57344\nQmvaa 8,60,36\nbuffer_store_dwordx4 v[120:123], v60, s[56:59], s88 offen offset:1024 sc1\nQmvaa 12,56,12\nQmvaa 12,60,40\nQta 74,68,57856\n"
"Qmvaa 16,56,16\nQmvaa 16,60,44\nQmvaa 20,56,20\nQta 76,68,58368\nv_add_u32 v75, 1, v75\nQmvaa 20,60,48\ns_waitcnt vmcnt(35)\nds_write_b32 v73, v75\n"
"Qmvaa 24,56,24\nQmvaa 24,60,52\nQta 78,68,58880\ns_waitcnt lgkmcnt(5)\nQmvaa 28,64,0\nQmvaa 28,68,28\nQmvaa 32,64,4\nQmvaa 32,68,32\n"
"Qmvaa 36,64,8\nQta 56,68,57600\nQmvaa 36,68,36\nds_read_b32 v66, v74\nQmvaa 40,64,12\nQmvaa 40,68,40\nQta 58,68,58112\nQmvaa 44,64,16\n"
"Qmvaa 44,68,44\ns_waitcnt lgkmcnt(1)\nv_cmp_gt_u32 vcc, v75, v66\nQmvaa 48,64,20\ns_cbranch_vccnz .Lso180\n.Lro180:\nQmvaa 48,68,48\nQta 60,68,58624\n"
"Qmvaa 52,64,24\nbuffer_store_dwordx4 v[152:155], v60, s[56:59], s88 offen offset:2048 sc1\nQmvaa 52,68,52\ns_waitcnt lgkmcnt(4)\nQmaaa 172,72,0\nQta 62,68,59136\nQmaaa 172,76,28\nQmaaa 176,72,4\n"
"Qmaaa 176,76,32\nQmaaa 180,72,8\nQta 64,70,1024\nQmaaa 180,76,36\nQmaaa 184,72,12\nQmaaa 184,76,40\nQta 66,70,2048\ns_add_u32 m0, s96, 16384\n"
"s_mov_b32 s95, 126976\nQmaaa 188,72,16\nbuffer_load_dwordx4 v63, s[52:55], s95 offen lds\nQmaaa 188,76,44\nQmaaa 192,72,20\nQta 68,70,9216\nQmaaa 192,76,48\ns_mov_b32 s95, 146432\n"
"Qmaaa 196,72,24\nbuffer_load_dwordx4 v63, s[52:55], s95 offen offset:1024 lds\nQmaaa 196,76,52\nQta 70,70,10240\ns_waitcnt lgkmcnt(4)\nQmvaa 92,56,0\nQmvaa 92,60,28\nQmvaa 124,56,4\n"
"buffer_store_dwordx4 v[184:187], v60, s[56:59], s88 offen offset:3072 sc1\nQmvaa 124,60,32\nQmvaa 156,56,8\nQta 72,70,1280\nQmvaa 156,60,36\nQmvaa 188,56,12\nQmvaa 188,60,40\nQta 74,70,2304\n"
"Qmvaa 220,56,16\nQmvaa 220,60,44\nQmvaa 252,56,20\nQta 76,70,9472\nQmvaa 252,60,48\ns_add_u32 m0, s96, 24576\ns_mov_b32 s95, 331776\nQmvaa 100,56,24\n"
"buffer_load_dwordx4 v61, s[52:55], s95 offen lds\nQmvaa 100,60,52\nQta 78,70,10496\ns_waitcnt lgkmcnt(4)\nQmaaa 200,64,0\nQmaaa 200,68,28\ns_mov_b32 s95, 351232\nQmaaa 204,64,4\n"
"buffer_load_dwordx4 v61, s[52:55], s95 offen offset:1024 lds\nQmaaa 204,68,32\nQmaaa 208,64,8\nQta 56,70,1536\nQmaaa 208,68,36\nQmaaa 212,64,12\nQmaaa 212,68,40\nQta 58,70,2560\n"
"s_add_u32 s88, s71, 18432\nQmaaa 216,64,16\nbuffer_store_dwordx4 v[216:219], v60, s[56:59], s88 offen offset:0 sc1\nQmaaa 216,68,44\nQmaaa 220,64,20\nQta 60,70,9728\nQmaaa 220,68,48\nQmaaa 224,64,24\n"
"Qmaaa 224,68,52\nQta 62,70,10752\ns_waitcnt lgkmcnt(4)\nQmaaa 228,72,0\nQmaaa 228,76,28\nQmaaa 232,72,4\nQmaaa 232,76,32\nQmaaa 236,72,8\n"
"Qta 64,70,1792\ns_add_u32 m0, s96, 32768\ns_mov_b32 s95, 372736\nQmaaa 236,76,36\nbuffer_load_dwordx4 v61, s[52:55], s95 offen lds\nQmaaa 240,72,12\nQmaaa 240,76,40\nQta 66,70,2816\n"
"Qmaaa 244,72,16\ns_mov_b32 s95, 392192\nQmaaa 244,76,44\nbuffer_load_dwordx4 v61, s[52:55], s95 offen offset:1024 lds\nQmaaa 248,72,20\nQta 68,70,9984\nQmaaa 248,76,48\nQmaaa 252,72,24\n"
"Qmaaa 252,76,52\nQta 70,70,11008\ns_waitcnt lgkmcnt(4)\nQmvaa 132,56,0\nbuffer_store_dwordx4 v[248:251], v60, s[56:59], s88 offen offset:1024 sc1\nQmvaa 132,60,28\nQmvaa 164,56,4\nQmvaa 164,60,32\n"
"Qmvaa 196,56,8\nQta 72,70,17408\nQmvaa 196,60,36\nQmvaa 228,56,12\nQmvaa 228,60,40\nQta 74,70,18432\nQmvaa 108,56,16\nQmvaa 108,60,44\n"
"Qmvaa 140,56,20\nQta 76,69,0\ns_add_u32 m0, s96, 0\ns_mov_b32 s95, 331776\nQmvaa 140,60,48\nbuffer_load_dwordx4 v62, s[52:55], s95 offen lds\nQmvaa 172,56,24\nQmvaa 172,60,52\n"
"Qta 78,69,1024\ns_waitcnt lgkmcnt(4)\nQmvaa 204,64,0\ns_mov_b32 s95, 371712\nQmvaa 204,68,28\nbuffer_load_dwordx4 v62, s[52:55], s95 offen offset:1024 lds\nQmvaa 236,64,4\nQmvaa 236,68,32\n"
"Qmaaa 96,64,8\nQta 56,70,17664\nQmaaa 96,68,36\nQmaaa 100,64,12\nbuffer_store_dwordx4 v[116:119], v60, s[56:59], s88 offen offset:2048 sc1\nQmaaa 100,68,40\nQta 58,70,18688\nQmaaa 104,64,16\n"
"Qmaaa 104,68,44\nQmaaa 108,64,20\nQta 60,69,256\nQmaaa 108,68,48\nQmaaa 112,64,24\nQmaaa 112,68,52\nQta 62,69,1280\ns_waitcnt lgkmcnt(4)\n"
"Qmaav 116,72,96\nQmaav 116,76,104\ns_mov_b32 s31, 28672\nQmaav 120,72,128\nbuffer_load_dwordx4 a[0:3], v60, s[48:51], s31 offen offset:0\nQmaav 120,76,136\nQmaav 124,72,160\nQta 64,70,17920\n"
"Qmaav 124,76,168\nQmaav 128,72,192\nbuffer_load_dwordx4 a[4:7], v60, s[48:51], s31 offen offset:1024\nQmaav 128,76,200\nQta 66,70,18944\nQmaav 132,72,224\nQmaav 132,76,232\nQmaav 136,72,148\n"
"Qta 68,69,512\nQmaav 136,76,212\nbuffer_load_dwordx4 a[8:11], v60, s[48:51], s31 offen offset:2048\nQmaav 140,72,180\nQmaav 140,76,244\nQta 70,69,1536\ns_waitcnt lgkmcnt(4)\nQmaav 144,56,96\n"
"Qmaav 144,60,104\nbuffer_load_dwordx4 a[12:15], v60, s[48:51], s31 offen offset:3072\nQmaav 148,56,128\nQmaav 148,60,136\nQmaav 152,56,160\nQta 72,70,18176\ns_mov_b32 s31, 32768\nQmaav 152,60,168\n"
"buffer_load_dwordx4 a[16:19], v60, s[48:51], s31 offen offset:0\nQmaav 156,56,192\nQmaav 156,60,200\nQta 74,70,19200\nQmaav 160,56,224\nQmaav 160,60,232\nbuffer_load_dwordx4 a[20:23], v60, s[48:51], s31 offen offset:1024\nQmaav 164,56,148\n"
"Qta 76,69,768\nQmaav 164,60,212\nQmaav 168,56,180\nQmaav 168,60,244\nQta 78,69,1792\ns_waitcnt lgkmcnt(4)\nQmvav 0,64,96\nbuffer_load_dwordx4 a[24:27], v60, s[48:51], s31 offen offset:2048\n"
"Qmvav 0,68,104\nQmvav 4,64,128\nQmvav 4,68,136\nQmvav 8,64,160\nQta 56,69,8192\ns_mov_b32 s31, 43008\nQmvav 8,68,168\nbuffer_load_dwordx4 a[28:31], v60, s[48:51], s31 offen offset:0\n"
"Qmvav 12,64,192\nQmvav 12,68,200\nQta 58,69,8704\nQmvav 16,64,224\nQmvav 16,68,232\nbuffer_load_dwordx4 a[32:35], v60, s[48:51], s31 offen offset:1024\nQmvav 20,64,148\nQta 60,69,9216\n"
"v_add_u32 v75, 1, v75\nQmvav 20,68,212\ns_waitcnt vmcnt(31)\nds_write_b32 v73, v75\nQmvav 24,64,180\nQmvav 24,68,244\nQta 62,69,9728\ns_waitcnt lgkmcnt(5)\n"
"Qmvav 28,72,96\nbuffer_load_dwordx4 a[36:39], v60, s[48:51], s31 offen offset:2048\nQmvav 28,76,104\nQmvav 32,72,128\nQmvav 32,76,136\nQmvav 36,72,160\nQta 64,69,8448\nQmvav 36,76,168\n"
"ds_read_b32 v66, v74\nQmvav 40,72,192\nbuffer_load_dwordx4 a[40:43], v60, s[48:51], s31 offen offset:3072\nQmvav 40,76,200\nQta 66,69,8960\nQmvav 44,72,224\nQmvav 44,76,232\ns_waitcnt lgkmcnt(1)\n"
"v_cmp_gt_u32 vcc, v75, v66\nQmvav 48,72,148\ns_cbranch_vccnz .Lso320\n.Lro320:\nQmvav 48,76,212\nQta 68,69,9472\ns_mov_b32 s31, 47104\nQmvav 52,72,180\n"
"buffer_load_dwordx4 a[44:47], v60, s[48:51], s31 offen offset:0\nQmvav 52,76,244\ns_waitcnt lgkmcnt(4)\nQmaav 172,56,96\nQta 70,69,9984\nQmaav 172,60,104\nQmaav 176,56,128\nbuffer_load_dwordx4 a[48:51], v60, s[48:51], s31 offen offset:1024\n"
"Qmaav 176,60,136\nQmaav 180,56,160\nQta 72,69,16384\nQmaav 180,60,168\nQmaav 184,56,192\nbuffer_load_dwordx4 a[52:55], v60, s[48:51], s31 offen offset:2048\nQmaav 184,60,200\nQta 74,69,17408\n"
"Qmaav 188,56,224\nQmaav 188,60,232\nQmaav 192,56,148\nQta 76,69,24576\ns_mov_b32 s31, 114688\nQmaav 192,60,212\nbuffer_load_dwordx4 v[80:83], v60, s[48:51], s31 offen offset:0\nQmaav 196,56,180\n"
"Qmaav 196,60,244\nQta 78,69,25600\ns_waitcnt lgkmcnt(4)\nQmvav 92,64,96\nQmvav 92,68,104\nbuffer_load_dwordx4 v[112:115], v60, s[48:51], s31 offen offset:1024\nQmvav 124,64,128\nQmvav 124,68,136\n"
"Qmvav 156,64,160\nQta 56,69,16640\nQmvav 156,68,168\nbuffer_load_dwordx4 v[144:147], v60, s[48:51], s31 offen offset:2048\nQmvav 188,64,192\nQmvav 188,68,200\nQta 58,69,17664\nQmvav 220,64,224\n"
"Qmvav 220,68,232\nbuffer_load_dwordx4 v[176:179], v60, s[48:51], s31 offen offset:3072\nQmvav 252,64,148\nQta 60,69,24832\nQmvav 252,68,212\nQmvav 100,64,180\nQmvav 100,68,244\nQta 62,69,25856\n"
"s_mov_b32 s31, 118784\ns_waitcnt lgkmcnt(4)\nQmaav 200,72,96\nbuffer_load_dwordx4 v[208:211], v60, s[48:51], s31 offen offset:0\nQmaav 200,76,104\nQmaav 204,72,128\nQmaav 204,76,136\n.Lpg14:\n"
"s_cbranch_execz .Ltr6\nQmaav 208,72,160\nQta 64,69,16896\nQmaav 208,76,168\nbuffer_load_dwordx4 v[240:243], v60, s[48:51], s31 offen offset:1024\nQmaav 212,72,192\nQmaav 212,76,200\nQta 66,69,17920\n"
"Qmaav 216,72,224\nQmaav 216,76,232\nbuffer_load_dwordx4 v[84:87], v60, s[48:51], s31 offen offset:2048\nQmaav 220,72,148\nQta 68,69,25088\nQmaav 220,76,212\nQmaav 224,72,180\nQmaav 224,76,244\n"
"Qta 70,69,26112\ns_mov_b32 s31, 129024\ns_waitcnt lgkmcnt(4)\nQmaav 228,56,96\nbuffer_load_dwordx4 v[88:91], v60, s[48:51], s31 offen offset:0\nQmaav 228,60,104\nQmaav 232,56,128\nQmaav 232,60,136\n"
"Qmaav 236,56,160\nQta 72,69,17152\nQmaav 236,60,168\nbuffer_load_dwordx4 v[120:123], v60, s[48:51], s31 offen offset:1024\nQmaav 240,56,192\nQmaav 240,60,200\nQta 74,69,18176\nQmaav 244,56,224\n"
"Qmaav 244,60,232\nbuffer_load_dwordx4 v[152:155], v60, s[48:51], s31 offen offset:2048\nQmaav 248,56,148\nQta 76,69,25344\nQmaav 248,60,212\nQmaav 252,56,180\nQmaav 252,60,244\nQta 78,69,26368\n"
"s_waitcnt lgkmcnt(4)\nQmvav 132,64,96\nbuffer_load_dwordx4 v[184:187], v60, s[48:51], s31 offen offset:3072\nQmvav 132,68,104\nQmvav 164,64,128\nQmvav 164,68,136\nQmvav 196,64,160\nQta 56,69,32768\n"
"s_mov_b32 s31, 133120\nQmvav 196,68,168\nbuffer_load_dwordx4 v[216:219], v60, s[48:51], s31 offen offset:0\nQmvav 228,64,192\nQmvav 228,68,200\nQta 58,69,33792\nQmvav 108,64,224\nQmvav 108,68,232\n"
"buffer_load_dwordx4 v[248:251], v60, s[48:51], s31 offen offset:1024\nQmvav 140,64,148\nQta 60,69,40960\nQmvav 140,68,212\nQmvav 172,64,180\nQmvav 172,68,244\nQta 62,69,41984\ns_waitcnt lgkmcnt(4)\n"
"Qmvav 204,72,96\nbuffer_load_dwordx4 v[116:119], v60, s[48:51], s31 offen offset:2048\nQmvav 204,76,104\nQmvav 236,72,128\nQmvav 236,76,136\nQmaav 96,72,160\nQta 64,69,33024\ns_add_u32 m0, s96, 8192\n"
"s_mov_b32 s95, 331776\nQmaav 96,76,168\nbuffer_load_dwordx4 v63, s[52:55], s95 offen lds\nQmaav 100,72,192\nQmaav 100,76,200\nQta 66,69,34048\nQmaav 104,72,224\ns_mov_b32 s95, 351232\n"
"Qmaav 104,76,232\nbuffer_load_dwordx4 v63, s[52:55], s95 offen offset:1024 lds\nQmaav 108,72,148\nQta 68,69,41216\nQmaav 108,76,212\nQmaav 112,72,180\nQmaav 112,76,244\nQta 70,69,42240\n"
"s_add_u32 m0, s96, 40960\ns_mov_b32 s95, 372736\ns_waitcnt vmcnt(16)\ns_waitcnt lgkmcnt(4)\nQmaaa 116,56,0\nbuffer_load_dwordx4 v63, s[52:55], s95 offen lds\nQmaaa 116,60,28\nQmaaa 120,56,4\n"
"Qmaaa 120,60,32\nQmaaa 124,56,8\nQta 72,69,33280\ns_mov_b32 s95, 392192\nQmaaa 124,60,36\nbuffer_load_dwordx4 v63, s[52:55], s95 offen offset:1024 lds\nQmaaa 128,56,12\nQmaaa 128,60,40\n"
"Qta 74,69,34304\nQmaaa 132,56,16\nQmaaa 132,60,44\nQmaaa 136,56,20\nQta 76,69,41472\ns_add_u32 s88, s71, 28672\nQmaaa 136,60,48\nbuffer_store_dwordx4 v[96:99], v60, s[56:59], s88 offen offset:0 sc1\n"
"Qmaaa 140,56,24\nQmaaa 140,60,52\nQta 78,69,42496\ns_waitcnt lgkmcnt(4)\nQmaaa 144,64,0\nQmaaa 144,68,28\nQmaaa 148,64,4\nQmaaa 148,68,32\n"
"Qmaaa 152,64,8\nQta 56,69,33536\nQmaaa 152,68,36\nQmaaa 156,64,12\nQmaaa 156,68,40\nQta 58,69,34560\ns_add_u32 m0, s96, 49152\ns_mov_b32 s95, 413696\n"
"Qmaaa 160,64,16\nbuffer_load_dwordx4 v61, s[52:55], s95 offen lds\nQmaaa 160,68,44\nQmaaa 164,64,20\nQta 60,69,41728\nQmaaa 164,68,48\ns_mov_b32 s95, 433152\nQmaaa 168,64,24\n"
"buffer_load_dwordx4 v61, s[52:55], s95 offen offset:1024 lds\nQmaaa 168,68,52\nQta 62,69,42752\ns_waitcnt lgkmcnt(4)\nQmvaa 0,72,0\nQmvaa 0,76,28\nQmvaa 4,72,4\nbuffer_store_dwordx4 v[128:131], v60, s[56:59], s88 offen offset:1024 sc1\n"
"Qmvaa 4,76,32\nQmvaa 8,72,8\nQta 64,69,49152\nQmvaa 8,76,36\nQmvaa 12,72,12\nQmvaa 12,76,40\nQta 66,69,49664\nQmvaa 16,72,16\n"
"Qmvaa 16,76,44\nQmvaa 20,72,20\nQta 68,69,50176\nv_add_u32 v75, 1, v75\nQmvaa 20,76,48\ns_waitcnt vmcnt(36)\nds_write_b32 v73, v75\ns_add_u32 m0, s96, 57344\n"
"s_mov_b32 s95, 454656\nQmvaa 24,72,24\nbuffer_load_dwordx4 v61, s[52:55], s95 offen lds\nQmvaa 24,76,52\nQta 70,69,50688\ns_waitcnt lgkmcnt(5)\nQmvaa 28,56,0\nQmvaa 28,60,28\n"
"s_mov_b32 s95, 474112\nQmvaa 32,56,4\nbuffer_load_dwordx4 v61, s[52:55], s95 offen offset:1024 lds\nQmvaa 32,60,32\nQmvaa 36,56,8\nQta 72,69,49408\nQmvaa 36,60,36\nds_read_b32 v66, v74\n"
"Qmvaa 40,56,12\nQmvaa 40,60,40\nQta 74,69,49920\nQmvaa 44,56,16\nbuffer_store_dwordx4 v[160:163], v60, s[56:59], s88 offen offset:2048 sc1\nv_add_u32 v67, 2, v65\nQmvaa 44,60,44\ns_waitcnt lgkmcnt(1)\n"
"v_cmp_gt_u32 vcc, v75, v66\nQmvaa 48,56,20\ns_cbranch_vccnz .Lso460\n.Lro460:\nQmvaa 48,60,48\nQta 76,69,50432\nQmvaa 52,56,24\nbuffer_store_dword v67, v77, s[24:27], 0 offen sc1\n"
"Qmvaa 52,60,52\ns_waitcnt lgkmcnt(4)\nQmaaa 172,64,0\nQta 78,69,50944\nQmaaa 172,68,28\nQmaaa 176,64,4\nQmaaa 176,68,32\nQmaaa 180,64,8\n"
"Qta 56,69,57344\ns_add_u32 m0, s96, 132096\ns_mov_b32 s95, 413696\nQmaaa 180,68,36\nbuffer_load_dwordx4 v62, s[52:55], s95 offen lds\nQmaaa 184,64,12\nQmaaa 184,68,40\nQta 58,69,58368\n"
"Qmaaa 188,64,16\ns_mov_b32 s95, 453632\nQmaaa 188,68,44\nbuffer_load_dwordx4 v62, s[52:55], s95 offen offset:1024 lds\nQmaaa 192,64,20\nQta 60,68,16384\nQmaaa 192,68,48\nQmaaa 196,64,24\n"
"Qmaaa 196,68,52\nQta 62,68,17408\ns_waitcnt lgkmcnt(4)\nQmvaa 92,72,0\nbuffer_store_dwordx4 v[192:195], v60, s[56:59], s88 offen offset:3072 sc1\nQmvaa 92,76,28\nQmvaa 124,72,4\nQmvaa 124,76,32\n"
"Qmvaa 156,72,8\nQta 64,69,57600\nQmvaa 156,76,36\nQmvaa 188,72,12\nQmvaa 188,76,40\nQta 66,69,58624\nQmvaa 220,72,16\nQmvaa 220,76,44\n"
"Qmvaa 252,72,20\nQta 68,68,16640\ns_add_u32 m0, s96, 140288\ns_mov_b32 s95, 413696\nQmvaa 252,76,48\nbuffer_load_dwordx4 v63, s[52:55], s95 offen lds\nQmvaa 100,72,24\nQmvaa 100,76,52\n"
"Qta 70,68,17664\ns_waitcnt lgkmcnt(4)\nQmaaa 200,56,0\ns_mov_b32 s95, 433152\nQmaaa 200,60,28\nbuffer_load_dwordx4 v63, s[52:55], s95 offen offset:1024 lds\nQmaaa 204,56,4\nQmaaa 204,60,32\n"
"Qmaaa 208,56,8\nQta 72,69,57856\nQmaaa 208,60,36\ns_add_u32 s88, s71, 32768\nQmaaa 212,56,12\nbuffer_store_dwordx4 v[224:227], v60, s[56:59], s88 offen offset:0 sc1\nQmaaa 212,60,40\nQta 74,69,58880\n"
"Qmaaa 216,56,16\nQmaaa 216,60,44\nQmaaa 220,56,20\nQta 76,68,16896\nQmaaa 220,60,48\nQmaaa 224,56,24\nQmaaa 224,60,52\nQta 78,68,17920\n"
"s_waitcnt lgkmcnt(4)\nQmaaa 228,64,0\nQmaaa 228,68,28\ns_add_u32 m0, s96, 148480\ns_mov_b32 s95, 454656\nQmaaa 232,64,4\nbuffer_load_dwordx4 v63, s[52:55], s95 offen lds\nQmaaa 232,68,32\n"
"Qmaaa 236,64,8\nQta 56,69,58112\nQmaaa 236,68,36\ns_mov_b32 s95, 474112\nQmaaa 240,64,12\nbuffer_load_dwordx4 v63, s[52:55], s95 offen offset:1024 lds\nQmaaa 240,68,40\nQta 58,69,59136\n"
"Qmaaa 244,64,16\nQmaaa 244,68,44\nQmaaa 248,64,20\nQta 60,68,17152\nQmaaa 248,68,48\nQmaaa 252,64,24\nbuffer_store_dwordx4 v[148:151], v60, s[56:59], s88 offen offset:1024 sc1\nQmaaa 252,68,52\n"
"Qta 62,68,18176\ns_waitcnt lgkmcnt(4)\nQmvaa 132,72,0\nQmvaa 132,76,28\ns_mov_b32 s40, %17\ns_and_b32 s41, %18, 0xffff\nQmvaa 164,72,4\ns_mov_b32 s42, 571200\n"
"s_lshl_b32 s66, %19, 2\nQmvaa 164,76,32\ns_add_u32 s66, s66, s70\ns_mul_i32 s98, s66, 11264\nQmvaa 196,72,8\nQta 64,68,24576\ns_add_u32 s98, s98, 1835008\nQmvaa 196,76,36\n"
"s_xor_b32 s66, s66, 16\ns_mul_i32 s64, s66, 11264\nQmvaa 228,72,12\ns_add_u32 s64, s64, 1835008\ns_mul_i32 s66, s92, 33792\nQmvaa 228,76,40\nQta 66,68,25600\ns_add_u32 m0, s96, 65536\n"
"s_mov_b32 s95, 659456\nQmvaa 108,72,16\nbuffer_load_dwordx4 v61, s[52:55], s95 offen lds\nQmvaa 108,76,44\ns_add_u32 s66, s66, %10\ns_add_u32 s66, s66, 98304\nQmvaa 140,72,20\nQta 68,68,32768\n"
"s_mul_i32 s68, s93, 96\nQmvaa 140,76,48\ns_mov_b32 s95, 678912\ns_sub_u32 s68, s66, s68\nQmvaa 172,72,24\nbuffer_load_dwordx4 v61, s[52:55], s95 offen offset:1024 lds\nQmvaa 172,76,52\nQta 70,68,33792\n"
"s_add_u32 s68, s68, 96\ns_waitcnt lgkmcnt(4)\nQmvaa 204,56,0\nv_bfe_u32 v148, v60, 4, 4\nv_mul_u32_u24 v148, 208, v148\nQmvaa 204,60,28\nv_lshrrev_b32 v149, 8, v60\nv_lshl_add_u32 v148, v149, 3, v148\n"
"Qmvaa 236,56,4\nv_add_u32 v56, s68, v148\ns_lshl_b32 s68, s93, 3\nQmvaa 236,60,32\nv_lshrrev_b32 v148, 7, v60\nv_add_u32 v148, s68, v148\nQmaaa 96,56,8\nQta 72,68,24832\n"
"v_and_b32 v149, 127, v60\nQmaaa 96,60,36\nbuffer_store_dwordx4 v[180:183], v60, s[56:59], s88 offen offset:2048 sc1\nQmaaa 100,56,12\nv_and_b32 v150, 64, v60\nv_lshrrev_b32 v150, 1, v150\nQmaaa 100,60,40\nQta 74,68,25856\n"
"v_mul_u32_u24 v57, 208, v148\nQmaaa 104,56,16\nv_add3_u32 v57, v57, v149, s66\nv_add_u32 v57, v57, v150\nQmaaa 104,60,44\ns_and_b32 s65, %19, 1\ns_and_b32 s68, %19, 4\nQmaaa 108,56,20\n"
"Qta 76,68,33024\ns_lshl_b32 s68, s68, 2\nQmaaa 108,60,48\ns_lshl_b32 s69, s65, 3\ns_add_u32 s68, s68, s69\nQmaaa 112,56,24\ns_lshr_b32 s68, 0xc0004080, s68\ns_and_b32 s68, s68, 255\n"
"Qmaaa 112,60,52\nQta 78,68,34048\ns_mul_i32 s69, s92, 286720\ns_waitcnt vmcnt(24)\ns_waitcnt lgkmcnt(4)\nQmaav 116,64,80\ns_mov_b32 s31, 143360\ns_add_u32 s68, s68, s69\n"
"Qmaav 116,68,88\nbuffer_load_dwordx4 a[0:3], v60, s[48:51], s31 offen offset:0\nQmaav 120,64,112\nv_mul_u32_u24 v64, 2560, v148\nv_add3_u32 v64, v64, v149, s68\nQmaav 120,68,120\ns_lshl_b32 s68, s93, 4\nv_lshrrev_b32 v148, 6, v60\n"
"Qmaav 124,64,144\nQta 56,68,25088\nv_add_u32 v148, s68, v148\nQmaav 124,68,152\nbuffer_load_dwordx4 a[4:7], v60, s[48:51], s31 offen offset:1024\nQmaav 128,64,176\nv_and_b32 v149, 63, v60\nv_and_b32 v150, 32, v60\n"
"Qmaav 128,68,184\nQta 58,68,26112\nv_lshlrev_b32 v150, 1, v150\nQmaav 132,64,208\nv_mul_u32_u24 v71, 208, v148\nv_add3_u32 v71, v71, v149, s66\nQmaav 132,68,216\nbuffer_load_dwordx4 a[8:11], v60, s[48:51], s31 offen offset:2048\n"
"Qmaav 136,64,240\nQta 60,68,33280\nv_add_u32 v71, v71, v150\nQmaav 136,68,248\ns_xor_b32 s65, s65, 1\ns_lshl_b32 s65, s65, 8\nQmaav 140,64,84\ns_add_u32 s68, s65, s69\n"
"v_mul_u32_u24 v72, 2560, v148\nQmaav 140,68,116\nQta 62,68,34304\nv_add3_u32 v72, v72, v149, s68\ns_waitcnt lgkmcnt(4)\nQmaav 144,72,80\nbuffer_load_dwordx4 a[12:15], v60, s[48:51], s31 offen offset:3072\nQmaav 144,76,88\n"
"Qmaav 148,72,112\nQmaav 148,76,120\nQmaav 152,72,144\nQta 64,68,25344\ns_mov_b32 s31, 147456\nQmaav 152,76,152\nbuffer_load_dwordx4 a[16:19], v60, s[48:51], s31 offen offset:0\nQmaav 156,72,176\n"
"Qmaav 156,76,184\nQta 66,68,26368\nQmaav 160,72,208\nQmaav 160,76,216\nbuffer_load_dwordx4 a[20:23], v60, s[48:51], s31 offen offset:1024\nQmaav 164,72,240\nQta 68,68,33536\nQmaav 164,76,248\n"
"Qmaav 168,72,84\nQmaav 168,76,116\nQta 70,68,34560\ns_waitcnt lgkmcnt(4)\nQmvav 0,56,80\nbuffer_load_dwordx4 a[24:27], v60, s[48:51], s31 offen offset:2048\nQmvav 0,60,88\nQmvav 4,56,112\n"
"Qmvav 4,60,120\nQmvav 8,56,144\nQta 72,68,0\ns_mov_b32 s31, 157696\nQmvav 8,60,152\nbuffer_load_dwordx4 a[28:31], v60, s[48:51], s31 offen offset:0\nQmvav 12,56,176\nQmvav 12,60,184\n"
"Qta 74,68,512\nQmvav 16,56,208\nQmvav 16,60,216\nbuffer_load_dwordx4 a[32:35], v60, s[48:51], s31 offen offset:1024\nQmvav 20,56,240\nQta 76,68,1024\nv_add_u32 v75, 1, v75\nQmvav 20,60,248\n"
"s_waitcnt vmcnt(19)\nds_write_b32 v73, v75\nQmvav 24,56,84\nQmvav 24,60,116\nQta 78,68,1536\ns_waitcnt lgkmcnt(5)\nQmvav 28,64,80\nbuffer_load_dwordx4 a[36:39], v60, s[48:51], s31 offen offset:2048\n"
"Qmvav 28,68,88\nQmvav 32,64,112\nQmvav 32,68,120\nQmvav 36,64,144\nQta 56,68,256\nQmvav 36,68,152\nds_read_b32 v66, v74\nQmvav 40,64,176\n"
"buffer_load_dwordx4 a[40:43], v60, s[48:51], s31 offen offset:3072\nQmvav 40,68,184\nQta 58,68,768\nQmvav 44,64,208\nQmvav 44,68,216\ns_waitcnt lgkmcnt(1)\nv_cmp_gt_u32 vcc, v75, v66\nQmvav 48,64,240\n"
"s_cbranch_vccnz .Lso600\n.Lro600:\nQmvav 48,68,248\nQta 60,68,1280\ns_mov_b32 s31, 161792\nQmvav 52,64,84\nbuffer_load_dwordx4 a[44:47], v60, s[48:51], s31 offen offset:0\nQmvav 52,68,116\n"
"s_waitcnt lgkmcnt(4)\nQmaav 172,72,80\nQta 62,68,1792\nQmaav 172,76,88\nQmaav 176,72,112\nbuffer_load_dwordx4 a[48:51], v60, s[48:51], s31 offen offset:1024\nQmaav 176,76,120\nQmaav 180,72,144\n"
"Qta 64,68,8192\nQmaav 180,76,152\nQmaav 184,72,176\nbuffer_load_dwordx4 a[52:55], v60, s[48:51], s31 offen offset:2048\nQmaav 184,76,184\nQta 66,68,9216\nQmaav 188,72,208\nQmaav 188,76,216\n"
"Qmaav 192,72,240\nQta 68,68,40960\ns_add_u32 m0, s96, 73728\ns_mov_b32 s95, 700416\nQmaav 192,76,248\nbuffer_load_dwordx4 v61, s[52:55], s95 offen lds\nQmaav 196,72,84\nQmaav 196,76,116\n"
"Qta 70,68,41984\ns_waitcnt lgkmcnt(4)\nQmvav 92,56,80\ns_mov_b32 s95, 719872\nQmvav 92,60,88\nbuffer_load_dwordx4 v61, s[52:55], s95 offen offset:1024 lds\nQmvav 124,56,112\nQmvav 124,60,120\n"
"Qmvav 156,56,144\nQta 72,68,8448\ns_add_u32 m0, s96, 81920\ns_mov_b32 s95, 659456\nQmvav 156,60,152\nbuffer_load_dwordx4 v62, s[52:55], s95 offen lds\nQmvav 188,56,176\nQmvav 188,60,184\n"
"Qta 74,68,9472\nQmvav 220,56,208\ns_mov_b32 s95, 699392\nQmvav 220,60,216\nbuffer_load_dwordx4 v62, s[52:55], s95 offen offset:1024 lds\nQmvav 252,56,240\nQta 76,68,41216\nQmvav 252,60,248\n"
"Qmvav 100,56,84\nQmvav 100,60,116\nQta 78,68,42240\ns_add_u32 m0, s96, 90112\ns_mov_b32 s95, 659456\ns_waitcnt lgkmcnt(4)\nQmaav 200,64,80\nbuffer_load_dwordx4 v63, s[52:55], s95 offen lds\n"
"Qmaav 200,68,88\nQmaav 204,64,112\n.Lpg15:\ns_cbranch_execz .Ltr6\nQmaav 204,68,120\nQmaav 208,64,144\nQta 56,68,8704\ns_mov_b32 s95, 678912\n"
"Qmaav 208,68,152\nbuffer_load_dwordx4 v63, s[52:55], s95 offen offset:1024 lds\nQmaav 212,64,176\nQmaav 212,68,184\nQta 58,68,9728\nQmaav 216,64,208\ns_add_u32 m0, s96, 98304\ns_mov_b32 s95, 700416\n"
"Qmaav 216,68,216\nbuffer_load_dwordx4 v63, s[52:55], s95 offen lds\nQmaav 220,64,240\nQta 60,68,41472\nQmaav 220,68,248\nQmaav 224,64,84\nQmaav 224,68,116\nQta 62,68,42496\n"
"s_waitcnt lgkmcnt(4)\nQmaav 228,72,80\nQmaav 228,76,88\ns_add_u32 s88, s71, 43008\nQmaav 232,72,112\nbuffer_store_dwordx4 v[104:107], v60, s[56:59], s88 offen offset:0 sc1\nQmaav 232,76,120\nQmaav 236,72,144\n"
"Qta 64,68,8960\nQmaav 236,76,152\nQmaav 240,72,176\nQmaav 240,76,184\nQta 66,68,9984\nQmaav 244,72,208\nQmaav 244,76,216\nQmaav 248,72,240\n"
"Qta 68,68,41728\nQmaav 248,76,248\nQmaav 252,72,84\nQmaav 252,76,116\nQta 70,68,42752\ns_waitcnt lgkmcnt(4)\nQmvav 132,56,80\nbuffer_store_dwordx4 v[136:139], v60, s[56:59], s88 offen offset:1024 sc1\n"
"Qmvav 132,60,88\nQmvav 164,56,112\nQmvav 164,60,120\nQmvav 196,56,144\nQta 72,68,49152\nQmvav 196,60,152\nQmvav 228,56,176\nQmvav 228,60,184\n"
"Qta 74,68,50176\nQmvav 108,56,208\nQmvav 108,60,216\nQmvav 140,56,240\nQta 76,68,57344\ns_mov_b32 s95, 719872\nQmvav 140,60,248\nbuffer_load_dwordx4 v63, s[52:55], s95 offen offset:1024 lds\n"
"Qmvav 172,56,84\nQmvav 172,60,116\nQta 78,68,58368\ns_waitcnt lgkmcnt(4)\nQmvav 204,64,80\ns_mov_b32 s31, 229376\nQmvav 204,68,88\nbuffer_load_dwordx4 v[80:83], v60, s[48:51], s31 offen offset:0\n"
"Qmvav 236,64,112\nQmvav 236,68,120\nQmaav 96,64,144\nQta 56,68,49408\nQmaav 96,68,152\nbuffer_load_dwordx4 v[112:115], v60, s[48:51], s31 offen offset:1024\nQmaav 100,64,176\nQmaav 100,68,184\n"
"Qta 58,68,50432\nQmaav 104,64,208\nQmaav 104,68,216\nbuffer_load_dwordx4 v[144:147], v60, s[48:51], s31 offen offset:2048\nQmaav 108,64,240\nQta 60,68,57600\nQmaav 108,68,248\nQmaav 112,64,84\n"
"Qmaav 112,68,116\nQta 62,68,58624\ns_waitcnt vmcnt(13)\ns_waitcnt lgkmcnt(4)\nQmaaa 116,72,0\nbuffer_load_dwordx4 v[176:179], v60, s[48:51], s31 offen offset:3072\nQmaaa 116,76,28\nQmaaa 120,72,4\n"
"Qmaaa 120,76,32\nQmaaa 124,72,8\nQta 64,68,49664\ns_mov_b32 s31, 233472\nQmaaa 124,76,36\nbuffer_load_dwordx4 v[208:211], v60, s[48:51], s31 offen offset:0\nQmaaa 128,72,12\nQmaaa 128,76,40\n"
"Qta 66,68,50688\nQmaaa 132,72,16\nQmaaa 132,76,44\nbuffer_load_dwordx4 v[240:243], v60, s[48:51], s31 offen offset:1024\nQmaaa 136,72,20\nQta 68,68,57856\nQmaaa 136,76,48\nQmaaa 140,72,24\n"
"Qmaaa 140,76,52\nQta 70,68,58880\ns_waitcnt lgkmcnt(4)\nQmaaa 144,56,0\nbuffer_load_dwordx4 v[84:87], v60, s[48:51], s31 offen offset:2048\nQmaaa 144,60,28\nQmaaa 148,56,4\nQmaaa 148,60,32\n"
"Qmaaa 152,56,8\nQta 72,68,49920\ns_mov_b32 s31, 243712\nQmaaa 152,60,36\nbuffer_load_dwordx4 v[88:91], v60, s[48:51], s31 offen offset:0\nQmaaa 156,56,12\nQmaaa 156,60,40\nQta 74,68,50944\n"
"Qmaaa 160,56,16\nQmaaa 160,60,44\nbuffer_load_dwordx4 v[120:123], v60, s[48:51], s31 offen offset:1024\nQmaaa 164,56,20\nQta 76,68,58112\nQmaaa 164,60,48\nQmaaa 168,56,24\nQmaaa 168,60,52\n"
"Qta 78,68,59136\ns_waitcnt lgkmcnt(4)\nQmvaa 0,64,0\nbuffer_load_dwordx4 v[152:155], v60, s[48:51], s31 offen offset:2048\nQmvaa 0,68,28\nQmvaa 4,64,4\nQmvaa 4,68,32\nQmvaa 8,64,8\n"
"Qta 56,70,1024\nQmvaa 8,68,36\nbuffer_load_dwordx4 v[184:187], v60, s[48:51], s31 offen offset:3072\nQmvaa 12,64,12\nQmvaa 12,68,40\nQta 58,70,1536\nQmvaa 16,64,16\ns_mov_b32 s31, 247808\n"
"Qmvaa 16,68,44\nbuffer_load_dwordx4 v[216:219], v60, s[48:51], s31 offen offset:0\nQmvaa 20,64,20\nQta 60,70,2048\nv_add_u32 v75, 1, v75\nQmvaa 20,68,48\ns_waitcnt vmcnt(18)\nds_write_b32 v73, v75\n"
"Qmvaa 24,64,24\nQmvaa 24,68,52\nQta 62,70,2560\ns_waitcnt lgkmcnt(5)\nQmvaa 28,72,0\nbuffer_load_dwordx4 v[248:251], v60, s[48:51], s31 offen offset:1024\nQmvaa 28,76,28\nQmvaa 32,72,4\n"
"Qmvaa 32,76,32\nQmvaa 36,72,8\nQta 64,70,1280\nQmvaa 36,76,36\nds_read_b32 v66, v74\nQmvaa 40,72,12\nbuffer_load_dwordx4 v[116:119], v60, s[48:51], s31 offen offset:2048\nQmvaa 40,76,40\n"
"Qta 66,70,1792\nQmvaa 44,72,16\nQmvaa 44,76,44\ns_waitcnt lgkmcnt(1)\nv_cmp_gt_u32 vcc, v75, v66\nQmvaa 48,72,20\ns_cbranch_vccnz .Lso740\n.Lro740:\n"
"Qmvaa 48,76,48\nQta 68,70,2304\ns_add_u32 m0, s96, 106496\ns_mov_b32 s95, 741376\nQmvaa 52,72,24\nbuffer_load_dwordx4 v61, s[52:55], s95 offen lds\nQmvaa 52,76,52\ns_waitcnt lgkmcnt(4)\n"
"Qmaaa 172,56,0\nQta 70,70,2816\nQmaaa 172,60,28\ns_mov_b32 s95, 760832\nQmaaa 176,56,4\nbuffer_load_dwordx4 v61, s[52:55], s95 offen offset:1024 lds\nQmaaa 176,60,32\nQmaaa 180,56,8\n"
"Qta 72,70,9216\nQmaaa 180,60,36\ns_add_u32 m0, s96, 114688\ns_mov_b32 s95, 782336\nQmaaa 184,56,12\nbuffer_load_dwordx4 v61, s[52:55], s95 offen lds\nQmaaa 184,60,40\nQta 74,70,10240\n"
"Qmaaa 188,56,16\nQmaaa 188,60,44\nQmaaa 192,56,20\nQta 76,70,17408\ns_mov_b32 s95, 801792\nQmaaa 192,60,48\nbuffer_load_dwordx4 v61, s[52:55], s95 offen offset:1024 lds\nQmaaa 196,56,24\n"
"Qmaaa 196,60,52\nQta 78,70,18432\ns_waitcnt lgkmcnt(4)\nQmvaa 92,64,0\ns_add_u32 m0, s96, 122880\ns_mov_b32 s95, 741376\nQmvaa 92,68,28\nbuffer_load_dwordx4 v62, s[52:55], s95 offen lds\n"
"Qmvaa 124,64,4\nQmvaa 124,68,32\nQmvaa 156,64,8\nQta 56,70,9472\ns_mov_b32 s95, 781312\nQmvaa 156,68,36\nbuffer_load_dwordx4 v62, s[52:55], s95 offen offset:1024 lds\nQmvaa 188,64,12\n"
"Qmvaa 188,68,40\nQta 58,70,10496\nQmvaa 220,64,16\ns_add_u32 m0, s96, 16384\ns_mov_b32 s95, 741376\nQmvaa 220,68,44\nbuffer_load_dwordx4 v63, s[52:55], s95 offen lds\nQmvaa 252,64,20\n"
"Qta 60,70,17664\nQmvaa 252,68,48\nQmvaa 100,64,24\nQmvaa 100,68,52\nQta 62,70,18688\ns_mov_b32 s95, 760832\ns_waitcnt lgkmcnt(4)\nQmaaa 200,72,0\n"
"buffer_load_dwordx4 v63, s[52:55], s95 offen offset:1024 lds\nQmaaa 200,76,28\nQmaaa 204,72,4\nQmaaa 204,76,32\nQmaaa 208,72,8\nQta 64,70,9728\ns_add_u32 m0, s96, 24576\ns_mov_b32 s95, 782336\n"
"Qmaaa 208,76,36\nbuffer_load_dwordx4 v63, s[52:55], s95 offen lds\nQmaaa 212,72,12\nQmaaa 212,76,40\nQta 66,70,10752\nQmaaa 216,72,16\ns_mov_b32 s95, 801792\nQmaaa 216,76,44\n"
"buffer_load_dwordx4 v63, s[52:55], s95 offen offset:1024 lds\nQmaaa 220,72,20\nQta 68,70,17920\nQmaaa 220,76,48\nQmaaa 224,72,24\nQmaaa 224,76,52\nQta 70,70,18944\ns_waitcnt lgkmcnt(4)\n"
"Qmaaa 228,56,0\nQmaaa 228,60,28\nQmaaa 232,56,4\nbuffer_store_dwordx4 v[168:171], v60, s[56:59], s88 offen offset:2048 sc1\nQmaaa 232,60,32\nQmaaa 236,56,8\nQta 72,70,9984\nQmaaa 236,60,36\n"
"Qmaaa 240,56,12\nQmaaa 240,60,40\nQta 74,70,11008\nQmaaa 244,56,16\nQmaaa 244,60,44\nQmaaa 248,56,20\nQta 76,70,18176\nQmaaa 248,60,48\n"
"Qmaaa 252,56,24\nQmaaa 252,60,52\nQta 78,70,19200\ns_waitcnt lgkmcnt(4)\nQmvaa 132,64,0\nbuffer_store_dwordx4 v[200:203], v60, s[56:59], s88 offen offset:3072 sc1\nQmvaa 132,68,28\nQmvaa 164,64,4\n"
"Qmvaa 164,68,32\nQmvaa 196,64,8\nQta 56,69,0\nQmvaa 196,68,36\nQmvaa 228,64,12\nQmvaa 228,68,40\nQta 58,69,1024\nQmvaa 108,64,16\n"
"Qmvaa 108,68,44\nQmvaa 140,64,20\nQta 60,69,8192\ns_add_u32 m0, s96, 32768\ns_mov_b32 s95, 987136\nQmvaa 140,68,48\nbuffer_load_dwordx4 v61, s[52:55], s95 offen lds\nQmvaa 172,64,24\n"
"Qmvaa 172,68,52\nQta 62,69,9216\ns_waitcnt lgkmcnt(4)\nQmvaa 204,72,0\ns_mov_b32 s95, 1006592\nQmvaa 204,76,28\nbuffer_load_dwordx4 v61, s[52:55], s95 offen offset:1024 lds\nQmvaa 236,72,4\n"
"Qmvaa 236,76,32\nQmaaa 96,72,8\nQta 64,69,256\ns_mov_b32 s31, 258048\nQmaaa 96,76,36\nbuffer_load_dwordx4 a[0:3], v60, s[48:51], s31 offen offset:0\nQmaaa 100,72,12\nQmaaa 100,76,40\n"
"Qta 66,69,1280\nQmaaa 104,72,16\nQmaaa 104,76,44\nbuffer_load_dwordx4 a[4:7], v60, s[48:51], s31 offen offset:1024\nQmaaa 108,72,20\nQta 68,69,8448\nQmaaa 108,76,48\nQmaaa 112,72,24\n"
"Qmaaa 112,76,52\nQta 70,69,9472\ns_waitcnt vmcnt(16)\ns_waitcnt lgkmcnt(4)\nQmaav 116,56,80\nbuffer_load_dwordx4 a[8:11], v60, s[48:51], s31 offen offset:2048\nQmaav 116,60,88\nQmaav 120,56,112\n"
"Qmaav 120,60,120\nQmaav 124,56,144\nQta 72,69,512\nQmaav 124,60,152\nbuffer_load_dwordx4 a[12:15], v60, s[48:51], s31 offen offset:3072\nQmaav 128,56,176\nQmaav 128,60,184\nQta 74,69,1536\n"
"Qmaav 132,56,208\ns_mov_b32 s31, 262144\nQmaav 132,60,216\nbuffer_load_dwordx4 a[16:19], v60, s[48:51], s31 offen offset:0\nQmaav 136,56,240\nQta 76,69,8704\nQmaav 136,60,248\nQmaav 140,56,84\n"
"Qmaav 140,60,116\nQta 78,69,9728\ns_waitcnt lgkmcnt(4)\nQmaav 144,64,80\nbuffer_load_dwordx4 a[20:23], v60, s[48:51], s31 offen offset:1024\nQmaav 144,68,88\nQmaav 148,64,112\nQmaav 148,68,120\n"
"Qmaav 152,64,144\nQta 56,69,768\nQmaav 152,68,152\nbuffer_load_dwordx4 a[24:27], v60, s[48:51], s31 offen offset:2048\nQmaav 156,64,176\nQmaav 156,68,184\nQta 58,69,1792\nQmaav 160,64,208\n"
"s_mov_b32 s31, 272384\nQmaav 160,68,216\nbuffer_load_dwordx4 a[28:31], v60, s[48:51], s31 offen offset:0\nQmaav 164,64,240\nQta 60,69,8960\nQmaav 164,68,248\nQmaav 168,64,84\nQmaav 168,68,116\n"
"Qta 62,69,9984\ns_waitcnt lgkmcnt(4)\nQmvav 0,72,80\nbuffer_load_dwordx4 a[32:35], v60, s[48:51], s31 offen offset:1024\nQmvav 0,76,88\nQmvav 4,72,112\nQmvav 4,76,120\nQmvav 8,72,144\n"
"Qta 64,69,16384\nQmvav 8,76,152\nbuffer_load_dwordx4 a[36:39], v60, s[48:51], s31 offen offset:2048\nQmvav 12,72,176\nQmvav 12,76,184\nQta 66,69,16896\nQmvav 16,72,208\nQmvav 16,76,216\n"
"buffer_load_dwordx4 a[40:43], v60, s[48:51], s31 offen offset:3072\nQmvav 20,72,240\nQta 68,69,17408\nv_add_u32 v75, 1, v75\nQmvav 20,76,248\ns_waitcnt vmcnt(19)\nds_write_b32 v73, v75\nQmvav 24,72,84\n"
"Qmvav 24,76,116\nQta 70,69,17920\ns_mov_b32 s31, 276480\ns_waitcnt lgkmcnt(5)\nQmvav 28,56,80\nbuffer_load_dwordx4 a[44:47], v60, s[48:51], s31 offen offset:0\nQmvav 28,60,88\nQmvav 32,56,112\n"
"Qmvav 32,60,120\nQmvav 36,56,144\nQta 72,69,16640\nQmvav 36,60,152\nds_read_b32 v66, v74\nQmvav 40,56,176\nbuffer_load_dwordx4 a[48:51], v60, s[48:51], s31 offen offset:1024\nQmvav 40,60,184\n"
"Qta 74,69,17152\nQmvav 44,56,208\nQmvav 44,60,216\ns_waitcnt lgkmcnt(1)\nv_cmp_gt_u32 vcc, v75, v66\nQmvav 48,56,240\ns_cbranch_vccnz .Lso880\n.Lro880:\n"
"Qmvav 48,60,248\nQta 76,69,17664\nQmvav 52,56,84\nbuffer_load_dwordx4 a[52:55], v60, s[48:51], s31 offen offset:2048\nQmvav 52,60,116\ns_waitcnt lgkmcnt(4)\nQmaav 172,64,80\nQta 78,69,18176\n"
"Qmaav 172,68,88\ns_add_u32 m0, s96, 0\ns_mov_b32 s95, 1028096\nQmaav 176,64,112\nbuffer_load_dwordx4 v61, s[52:55], s95 offen lds\nQmaav 176,68,120\nQmaav 180,64,144\nQta 56,69,24576\n"
"Qmaav 180,68,152\ns_mov_b32 s95, 1047552\nQmaav 184,64,176\nbuffer_load_dwordx4 v61, s[52:55], s95 offen offset:1024 lds\nQmaav 184,68,184\nQta 58,69,25600\nQmaav 188,64,208\nQmaav 188,68,216\n"
"Qmaav 192,64,240\nQta 60,69,32768\ns_add_u32 m0, s96, 8192\ns_mov_b32 s95, 987136\nQmaav 192,68,248\nbuffer_load_dwordx4 v62, s[52:55], s95 offen lds\nQmaav 196,64,84\nQmaav 196,68,116\n"
"Qta 62,69,33792\ns_waitcnt lgkmcnt(4)\nQmvav 92,72,80\ns_mov_b32 s95, 1027072\nQmvav 92,76,88\nbuffer_load_dwordx4 v62, s[52:55], s95 offen offset:1024 lds\nQmvav 124,72,112\nQmvav 124,76,120\n"
"Qmvav 156,72,144\nQta 64,69,24832\ns_add_u32 m0, s96, 40960\ns_mov_b32 s95, 987136\nQmvav 156,76,152\nbuffer_load_dwordx4 v63, s[52:55], s95 offen lds\nQmvav 188,72,176\nQmvav 188,76,184\n"
"Qta 66,69,25856\nQmvav 220,72,208\ns_mov_b32 s95, 1006592\nQmvav 220,76,216\nbuffer_load_dwordx4 v63, s[52:55], s95 offen offset:1024 lds\nQmvav 252,72,240\nQta 68,69,33024\nQmvav 252,76,248\n"
"Qmvav 100,72,84\nQmvav 100,76,116\nQta 70,69,34048\ns_add_u32 m0, s96, 49152\ns_mov_b32 s95, 1028096\ns_waitcnt lgkmcnt(4)\nQmaav 200,56,80\nbuffer_load_dwordx4 v63, s[52:55], s95 offen lds\n"
"Qmaav 200,60,88\nQmaav 204,56,112\nQmaav 204,60,120\nQmaav 208,56,144\nQta 72,69,25088\ns_mov_b32 s95, 1047552\nQmaav 208,60,152\nbuffer_load_dwordx4 v63, s[52:55], s95 offen offset:1024 lds\n"
"Qmaav 212,56,176\nQmaav 212,60,184\nQta 74,69,26112\nQmaav 216,56,208\ns_add_u32 m0, s96, 57344\ns_mov_b32 s95, 1069056\nQmaav 216,60,216\nbuffer_load_dwordx4 v61, s[52:55], s95 offen lds\n"
"Qmaav 220,56,240\nQta 76,69,33280\nQmaav 220,60,248\nQmaav 224,56,84\nQmaav 224,60,116\nQta 78,69,34304\ns_waitcnt lgkmcnt(4)\nQmaav 228,64,80\n"
"Qmaav 228,68,88\ns_add_u32 s88, s71, 47104\nQmaav 232,64,112\nbuffer_store_dwordx4 v[232:235], v60, s[56:59], s88 offen offset:0 sc1\nQmaav 232,68,120\nQmaav 236,64,144\nQta 56,69,25344\nQmaav 236,68,152\n"
"Qmaav 240,64,176\nQmaav 240,68,184\nQta 58,69,26368\nQmaav 244,64,208\nQmaav 244,68,216\nQmaav 248,64,240\nQta 60,69,33536\n.Lpg16:\n"
"s_cbranch_execz .Lpg20\nQmaav 248,68,248\nQmaav 252,64,84\nQmaav 252,68,116\nQta 62,69,34560\ns_waitcnt lgkmcnt(4)\nQmvav 132,72,80\nbuffer_store_dwordx4 v[212:215], v60, s[56:59], s88 offen offset:1024 sc1\n"
"Qmvav 132,76,88\nQmvav 164,72,112\nQmvav 164,76,120\nQmvav 196,72,144\nQta 64,69,40960\nQmvav 196,76,152\nQmvav 228,72,176\nQmvav 228,76,184\n"
"Qta 66,69,41984\nQmvav 108,72,208\nQmvav 108,76,216\nQmvav 140,72,240\nQta 68,69,49152\ns_mov_b32 s95, 1088512\nQmvav 140,76,248\nbuffer_load_dwordx4 v61, s[52:55], s95 offen offset:1024 lds\n"
"Qmvav 172,72,84\nQmvav 172,76,116\nQta 70,69,50176\ns_waitcnt lgkmcnt(4)\nQmvav 204,56,80\ns_mov_b32 s31, 344064\nQmvav 204,60,88\nbuffer_load_dwordx4 v[80:83], v60, s[48:51], s31 offen offset:0\n"
"Qmvav 236,56,112\nQmvav 236,60,120\nQmaav 96,56,144\nQta 72,69,41216\nQmaav 96,60,152\nbuffer_load_dwordx4 v[112:115], v60, s[48:51], s31 offen offset:1024\nQmaav 100,56,176\nQmaav 100,60,184\n"
"Qta 74,69,42240\nQmaav 104,56,208\nQmaav 104,60,216\nbuffer_load_dwordx4 v[144:147], v60, s[48:51], s31 offen offset:2048\nQmaav 108,56,240\nQta 76,69,49408\nQmaav 108,60,248\nQmaav 112,56,84\n"
"v_accvgpr_read_b32 v96, a96\nv_accvgpr_read_b32 v97, a97\nQmaav 112,60,116\nQta 78,69,50432\nv_accvgpr_read_b32 v98, a98\ns_waitcnt vmcnt(15)\ns_waitcnt lgkmcnt(4)\nQmaaa 116,64,0\n"
"buffer_load_dwordx4 v[176:179], v60, s[48:51], s31 offen offset:3072\nQmaaa 116,68,28\nv_accvgpr_read_b32 v99, a99\nv_accvgpr_read_b32 v128, a100\nQmaaa 120,64,4\nv_accvgpr_read_b32 v129, a101\nv_accvgpr_read_b32 v130, a102\nQmaaa 120,68,32\n"
"v_accvgpr_read_b32 v131, a103\nv_accvgpr_read_b32 v160, a104\nQmaaa 124,64,8\nQta 56,69,41472\ns_mov_b32 s31, 348160\nQmaaa 124,68,36\nbuffer_load_dwordx4 v[208:211], v60, s[48:51], s31 offen offset:0\nQmaaa 128,64,12\n"
"v_accvgpr_read_b32 v161, a105\nv_accvgpr_read_b32 v162, a106\nQmaaa 128,68,40\nQta 58,69,42496\nv_accvgpr_read_b32 v163, a107\nQmaaa 132,64,16\nv_accvgpr_read_b32 v192, a108\nv_accvgpr_read_b32 v193, a109\n"
"Qmaaa 132,68,44\nbuffer_load_dwordx4 v[240:243], v60, s[48:51], s31 offen offset:1024\nQmaaa 136,64,20\nQta 60,69,49664\nv_accvgpr_read_b32 v194, a110\nQmaaa 136,68,48\nv_accvgpr_read_b32 v195, a111\nv_accvgpr_read_b32 v224, a112\n"
"Qmaaa 140,64,24\nv_accvgpr_read_b32 v225, a113\nv_accvgpr_read_b32 v226, a114\nQmaaa 140,68,52\nQta 62,69,50688\nv_accvgpr_read_b32 v227, a115\ns_waitcnt lgkmcnt(4)\nQmaaa 144,72,0\n"
"buffer_load_dwordx4 v[84:87], v60, s[48:51], s31 offen offset:2048\nQmaaa 144,76,28\nQmaaa 148,72,4\nQmaaa 148,76,32\nQmaaa 152,72,8\nQta 64,69,41728\ns_mov_b32 s31, 358400\nQmaaa 152,76,36\n"
"buffer_load_dwordx4 v[88:91], v60, s[48:51], s31 offen offset:0\nQmaaa 156,72,12\nQmaaa 156,76,40\nQta 66,69,42752\nQmaaa 160,72,16\nQmaaa 160,76,44\nbuffer_load_dwordx4 v[120:123], v60, s[48:51], s31 offen offset:1024\nQmaaa 164,72,20\n"
"Qta 68,69,49920\nQmaaa 164,76,48\nQmaaa 168,72,24\nQmaaa 168,76,52\nQta 70,69,50944\ns_waitcnt lgkmcnt(4)\nQmvaa 0,56,0\nbuffer_load_dwordx4 v[152:155], v60, s[48:51], s31 offen offset:2048\n"
"Qmvaa 0,60,28\nQmvaa 4,56,4\nQmvaa 4,60,32\nQmvaa 8,56,8\nQta 72,69,57344\nQmvaa 8,60,36\nbuffer_load_dwordx4 v[184:187], v60, s[48:51], s31 offen offset:3072\nQmvaa 12,56,12\n"
"Qmvaa 12,60,40\nQta 74,69,57856\nQmvaa 16,56,16\ns_mov_b32 s31, 362496\nQmvaa 16,60,44\nbuffer_load_dwordx4 v[216:219], v60, s[48:51], s31 offen offset:0\nQmvaa 20,56,20\nQta 76,69,58368\n"
"v_add_u32 v75, 1, v75\nQmvaa 20,60,48\ns_waitcnt vmcnt(20)\nds_write_b32 v73, v75\nQmvaa 24,56,24\nQmvaa 24,60,52\nQta 78,69,58880\ns_waitcnt lgkmcnt(5)\n"
"Qmvaa 28,64,0\nbuffer_load_dwordx4 v[248:251], v60, s[48:51], s31 offen offset:1024\nQmvaa 28,68,28\nQmvaa 32,64,4\nQmvaa 32,68,32\nQmvaa 36,64,8\nQta 56,69,57600\nQmvaa 36,68,36\n"
"ds_read_b32 v66, v74\nQmvaa 40,64,12\nbuffer_load_dwordx4 v[116:119], v60, s[48:51], s31 offen offset:2048\nQmvaa 40,68,40\nQta 58,69,58112\nQmvaa 44,64,16\nQmvaa 44,68,44\ns_waitcnt lgkmcnt(1)\n"
"v_cmp_gt_u32 vcc, v75, v66\nQmvaa 48,64,20\ns_cbranch_vccnz .Lso1020\n.Lro1020:\nQmvaa 48,68,48\nQta 60,69,58624\ns_add_u32 m0, s96, 132096\ns_mov_b32 s95, 1110016\n"
"Qmvaa 52,64,24\nbuffer_load_dwordx4 v61, s[52:55], s95 offen lds\nQmvaa 52,68,52\ns_waitcnt lgkmcnt(4)\nQmaaa 172,72,0\nQta 62,69,59136\nQmaaa 172,76,28\ns_mov_b32 s95, 1129472\n"
"Qmaaa 176,72,4\nbuffer_load_dwordx4 v61, s[52:55], s95 offen offset:1024 lds\nQmaaa 176,76,32\nQmaaa 180,72,8\nQta 64,68,16384\nQmaaa 180,76,36\ns_add_u32 m0, s96, 140288\ns_mov_b32 s95, 1069056\n"
"Qmaaa 184,72,12\nbuffer_load_dwordx4 v62, s[52:55], s95 offen lds\nQmaaa 184,76,40\nQta 66,68,17408\nQmaaa 188,72,16\nQmaaa 188,76,44\nQmaaa 192,72,20\nQta 68,68,24576\n"
"s_mov_b32 s95, 1108992\nQmaaa 192,76,48\nbuffer_load_dwordx4 v62, s[52:55], s95 offen offset:1024 lds\nQmaaa 196,72,24\nQmaaa 196,76,52\nQta 70,68,25600\ns_waitcnt lgkmcnt(4)\nQmvaa 92,56,0\n"
"s_add_u32 m0, s96, 148480\ns_mov_b32 s95, 1069056\nQmvaa 92,60,28\nbuffer_load_dwordx4 v63, s[52:55], s95 offen lds\nQmvaa 124,56,4\nQmvaa 124,60,32\nQmvaa 156,56,8\nQta 72,68,16640\n"
"s_mov_b32 s95, 1088512\nQmvaa 156,60,36\nbuffer_load_dwordx4 v63, s[52:55], s95 offen offset:1024 lds\nQmvaa 188,56,12\nQmvaa 188,60,40\nQta 74,68,17664\nQmvaa 220,56,16\ns_add_u32 m0, s96, 65536\n"
"s_mov_b32 s95, 1110016\nQmvaa 220,60,44\nbuffer_load_dwordx4 v63, s[52:55], s95 offen lds\nQmvaa 252,56,20\nQta 76,68,24832\nQmvaa 252,60,48\nQmvaa 100,56,24\nQmvaa 100,60,52\n"
"Qta 78,68,25856\ns_mov_b32 s95, 1129472\ns_waitcnt lgkmcnt(4)\nQmaaa 200,64,0\nbuffer_load_dwordx4 v63, s[52:55], s95 offen offset:1024 lds\nQmaaa 200,68,28\nQmaaa 204,64,4\nQmaaa 204,68,32\n"
"Qmaaa 208,64,8\nQta 56,68,16896\ns_add_u32 m0, s96, 73728\ns_add_u32 s95, s67, 4096\nQmaaa 208,68,36\nbuffer_load_dwordx4 v61, s[52:55], s95 offen lds\nQmaaa 212,64,12\nQmaaa 212,68,40\n"
"Qta 58,68,17920\nQmaaa 216,64,16\ns_add_u32 s95, s67, 23552\nQmaaa 216,68,44\nbuffer_load_dwordx4 v61, s[52:55], s95 offen offset:1024 lds\nQmaaa 220,64,20\nQta 60,68,25088\nQmaaa 220,68,48\n"
"Qmaaa 224,64,24\nQmaaa 224,68,52\nQta 62,68,26112\ns_waitcnt lgkmcnt(4)\nQmaaa 228,72,0\nQmaaa 228,76,28\nQmaaa 232,72,4\nbuffer_store_dwordx4 v[244:247], v60, s[56:59], s88 offen offset:2048 sc1\n"
"Qmaaa 232,76,32\nQmaaa 236,72,8\nQta 64,68,17152\nQmaaa 236,76,36\nQmaaa 240,72,12\nQmaaa 240,76,40\nQta 66,68,18176\nQmaaa 244,72,16\n"
"Qmaaa 244,76,44\nQmaaa 248,72,20\nQta 68,68,25344\nQmaaa 248,76,48\ns_add_u32 m0, s96, 81920\ns_add_u32 s95, s67, 45056\nQmaaa 252,72,24\nbuffer_load_dwordx4 v61, s[52:55], s95 offen lds\n"
"Qmaaa 252,76,52\nQta 70,68,26368\ns_waitcnt lgkmcnt(4)\nQmvaa 132,56,0\nQmvaa 132,60,28\ns_add_u32 s95, s67, 64512\nQmvaa 164,56,4\nbuffer_load_dwordx4 v61, s[52:55], s95 offen offset:1024 lds\n"
"Qmvaa 164,60,32\nQmvaa 196,56,8\nQta 72,68,32768\nQmvaa 196,60,36\ns_add_u32 m0, s96, 90112\ns_add_u32 s95, s67, 4096\nQmvaa 228,56,12\nbuffer_load_dwordx4 v62, s[52:55], s95 offen lds\n"
"Qmvaa 228,60,40\nQta 74,68,33792\nQmvaa 108,56,16\nQmvaa 108,60,44\nQmvaa 140,56,20\nQta 76,68,0\ns_add_u32 s95, s67, 44032\nQmvaa 140,60,48\n"
"buffer_load_dwordx4 v62, s[52:55], s95 offen offset:1024 lds\nQmvaa 172,56,24\nQmvaa 172,60,52\nQta 78,68,1024\ns_waitcnt lgkmcnt(4)\nQmvaa 204,64,0\nbuffer_load_dword v78, v76, s[24:27], 0 offen sc1\ns_mov_b32 s31, 372736\n"
"Qmvaa 204,68,28\nbuffer_load_dwordx4 a[0:3], v60, s[48:51], s31 offen offset:0\nQmvaa 236,64,4\nQmvaa 236,68,32\nQmvaa 96,64,8\nQta 56,68,33024\nQmvaa 96,68,36\nbuffer_load_dwordx4 a[4:7], v60, s[48:51], s31 offen offset:1024\n"
"Qmvaa 128,64,12\nQmvaa 128,68,40\nQta 58,68,34048\nQmvaa 160,64,16\nQmvaa 160,68,44\nbuffer_load_dwordx4 a[8:11], v60, s[48:51], s31 offen offset:2048\nQmvaa 192,64,20\nQta 60,68,256\n"
"Qmvaa 192,68,48\nQmvaa 224,64,24\nQmvaa 224,68,52\nQta 62,68,1280\ns_waitcnt vmcnt(19)\ns_waitcnt lgkmcnt(4)\nQmaav 116,72,80\nbuffer_load_dwordx4 a[12:15], v60, s[48:51], s31 offen offset:3072\n"
"Qmaav 116,76,88\nQmaav 120,72,112\nQmaav 120,76,120\nQmaav 124,72,144\nQta 64,68,33280\ns_mov_b32 s31, 376832\nQmaav 124,76,152\nbuffer_load_dwordx4 a[16:19], v60, s[48:51], s31 offen offset:0\n"
"Qmaav 128,72,176\nQmaav 128,76,184\nQta 66,68,34304\nQmaav 132,72,208\nQmaav 132,76,216\nbuffer_load_dwordx4 a[20:23], v60, s[48:51], s31 offen offset:1024\nQmaav 136,72,240\nQta 68,68,512\n"
"Qmaav 136,76,248\nQmaav 140,72,84\nQmaav 140,76,116\nQta 70,68,1536\ns_waitcnt lgkmcnt(4)\nQmaav 144,56,80\nbuffer_load_dwordx4 a[24:27], v60, s[48:51], s31 offen offset:2048\nQmaav 144,60,88\n"
"Qmaav 148,56,112\nQmaav 148,60,120\nQmaav 152,56,144\nQta 72,68,33536\ns_mov_b32 s31, 387072\nQmaav 152,60,152\nbuffer_load_dwordx4 a[28:31], v60, s[48:51], s31 offen offset:0\nQmaav 156,56,176\n"
"Qmaav 156,60,184\nQta 74,68,34560\nQmaav 160,56,208\nQmaav 160,60,216\nbuffer_load_dwordx4 a[32:35], v60, s[48:51], s31 offen offset:1024\nQmaav 164,56,240\nQta 76,68,768\nQmaav 164,60,248\n"
"Qmaav 168,56,84\nQmaav 168,60,116\nQta 78,68,1792\ns_waitcnt lgkmcnt(4)\nQmvav 0,64,80\nbuffer_load_dwordx4 a[36:39], v60, s[48:51], s31 offen offset:2048\nQmvav 0,68,88\nQmvav 4,64,112\n"
"Qmvav 4,68,120\nQmvav 8,64,144\nQta 56,68,8192\nQmvav 8,68,152\nbuffer_load_dwordx4 a[40:43], v60, s[48:51], s31 offen offset:3072\nQmvav 12,64,176\nQmvav 12,68,184\nQta 58,68,8704\n"
"Qmvav 16,64,208\ns_mov_b32 s31, 391168\nQmvav 16,68,216\nbuffer_load_dwordx4 a[44:47], v60, s[48:51], s31 offen offset:0\nQmvav 20,64,240\nQta 60,68,9216\nv_add_u32 v75, 1, v75\nQmvav 20,68,248\n"
"s_waitcnt vmcnt(24)\nds_write_b32 v73, v75\nQmvav 24,64,84\nQmvav 24,68,116\nQta 62,68,9728\ns_waitcnt lgkmcnt(5)\nQmvav 28,72,80\nbuffer_load_dwordx4 a[48:51], v60, s[48:51], s31 offen offset:1024\n"
"Qmvav 28,76,88\nQmvav 32,72,112\nQmvav 32,76,120\ns_waitcnt vmcnt(13)\nv_sub_u32 v66, v78, v65\nv_cmp_gt_u32 vcc, 2, v66\ns_and_b64 vcc, vcc, s[28:29]\ns_cbranch_scc1 .Lf2_w\n"
".Lf2_r:\nQmvav 36,72,144\nQta 64,68,8448\nQmvav 36,76,152\nds_read_b32 v66, v74\nQmvav 40,72,176\nbuffer_load_dwordx4 a[52:55], v60, s[48:51], s31 offen offset:2048\nQmvav 40,76,184\n"
"Qta 66,68,8960\nQmvav 44,72,208\nQmvav 44,76,216\ns_waitcnt lgkmcnt(1)\nv_cmp_gt_u32 vcc, v75, v66\nQmvav 48,72,240\ns_cbranch_vccnz .Lso1160\n.Lro1160:\n"
"Qmvav 48,76,248\nQta 68,68,9472\ns_add_u32 s31, s74, 0\nQmvav 52,72,84\nbuffer_load_dwordx4 a[80:83], v60, s[48:51], s31 offen offset:0 sc1\nQmvav 52,76,116\ns_waitcnt lgkmcnt(4)\nQmaav 172,56,80\n"
"Qta 70,68,9984\nQmaav 172,60,88\nQmaav 176,56,112\nbuffer_load_dwordx4 a[84:87], v60, s[48:51], s31 offen offset:1024 sc1\nQmaav 176,60,120\nQmaav 180,56,144\nQta 72,68,40960\nQmaav 180,60,152\n"
"Qmaav 184,56,176\nbuffer_load_dwordx4 a[88:91], v60, s[48:51], s31 offen offset:2048 sc1\nQmaav 184,60,184\nQta 74,68,41984\nQmaav 188,56,208\nQmaav 188,60,216\nQmaav 192,56,240\nQta 76,68,49152\n"
"Qmaav 192,60,248\nbuffer_load_dwordx4 a[92:95], v60, s[48:51], s31 offen offset:3072 sc1\nQmaav 196,56,84\nQmaav 196,60,116\nQta 78,68,50176\ns_waitcnt lgkmcnt(4)\nQmvav 92,64,80\ns_add_u32 s31, s74, 4096\n"
"Qmvav 92,68,88\nbuffer_load_dwordx4 a[96:99], v60, s[48:51], s31 offen offset:0 sc1\nQmvav 124,64,112\nQmvav 124,68,120\nQmvav 156,64,144\nQta 56,68,41216\nQmvav 156,68,152\nbuffer_load_dwordx4 a[100:103], v60, s[48:51], s31 offen offset:1024 sc1\n"
"Qmvav 188,64,176\nQmvav 188,68,184\nQta 58,68,42240\nQmvav 220,64,208\nQmvav 220,68,216\nbuffer_load_dwordx4 a[104:107], v60, s[48:51], s31 offen offset:2048 sc1\nQmvav 252,64,240\nQta 60,68,49408\n"
"Qmvav 252,68,248\nQmvav 100,64,84\nQmvav 100,68,116\nQta 62,68,50432\ns_add_u32 s31, s74, 14336\ns_waitcnt lgkmcnt(4)\nQmaav 200,72,80\nbuffer_load_dwordx4 a[108:111], v60, s[48:51], s31 offen offset:0 sc1\n"
"Qmaav 200,76,88\nQmaav 204,72,112\nQmaav 204,76,120\nQmaav 208,72,144\nQta 64,68,41472\nQmaav 208,76,152\nbuffer_load_dwordx4 a[112:115], v60, s[48:51], s31 offen offset:1024 sc1\nQmaav 212,72,176\n"
"Qmaav 212,76,184\nQta 66,68,42496\nQmaav 216,72,208\nQmaav 216,76,216\nbuffer_load_dwordx4 v[168:171], v60, s[48:51], s31 offen offset:2048 sc1\nQmaav 220,72,240\nQta 68,68,49664\nQmaav 220,76,248\n"
"Qmaav 224,72,84\nQmaav 224,76,116\nQta 70,68,50688\ns_waitcnt lgkmcnt(4)\nQmaav 228,56,80\nbuffer_load_dwordx4 v[200:203], v60, s[48:51], s31 offen offset:3072 sc1\nQmaav 228,60,88\nQmaav 232,56,112\n"
"Qmaav 232,60,120\nQmaav 236,56,144\nQta 72,68,41728\ns_add_u32 s31, s74, 18432\nQmaav 236,60,152\nbuffer_load_dwordx4 v[232:235], v60, s[48:51], s31 offen offset:0 sc1\nQmaav 240,56,176\nQmaav 240,60,184\n"
"Qta 74,68,42752\nQmaav 244,56,208\nQmaav 244,60,216\nbuffer_load_dwordx4 v[212:215], v60, s[48:51], s31 offen offset:1024 sc1\nQmaav 248,56,240\nQta 76,68,49920\nQmaav 248,60,248\nQmaav 252,56,84\n"
"Qmaav 252,60,116\nQta 78,68,50944\ns_waitcnt lgkmcnt(4)\nQmvav 132,64,80\nbuffer_load_dwordx4 v[244:247], v60, s[48:51], s31 offen offset:2048 sc1\nQmvav 132,68,88\nQmvav 164,64,112\nQmvav 164,68,120\n"
"Qmvav 196,64,144\nQta 56,68,57344\ns_add_u32 m0, s96, 98304\ns_add_u32 s95, s67, 4096\nQmvav 196,68,152\nbuffer_load_dwordx4 v63, s[52:55], s95 offen lds\nQmvav 228,64,176\n.Lpg17:\n"
"s_cbranch_execz .Lpg21\nQmvav 228,68,184\nQta 58,68,58368\nQmvav 108,64,208\ns_add_u32 s95, s67, 23552\nQmvav 108,68,216\nbuffer_load_dwordx4 v63, s[52:55], s95 offen offset:1024 lds\nQmvav 140,64,240\n"
"Qta 60,70,1024\nQmvav 140,68,248\nQmvav 172,64,84\nQmvav 172,68,116\nQta 62,70,2048\ns_add_u32 m0, s96, 106496\ns_add_u32 s95, s67, 45056\ns_waitcnt lgkmcnt(4)\n"
"Qmvav 204,72,80\nbuffer_load_dwordx4 v63, s[52:55], s95 offen lds\nQmvav 204,76,88\nQmvav 236,72,112\nQmvav 236,76,120\nQmvav 96,72,144\nQta 64,68,57600\ns_add_u32 s95, s67, 64512\n"
"Qmvav 96,76,152\nbuffer_load_dwordx4 v63, s[52:55], s95 offen offset:1024 lds\nQmvav 128,72,176\nQmvav 128,76,184\nQta 66,68,58624\nQmvav 160,72,208\ns_add_u32 m0, s96, 114688\ns_add_u32 s95, s72, 4096\n"
"Qmvav 160,76,216\nbuffer_load_dwordx4 v61, s[52:55], s95 offen lds\nQmvav 192,72,240\nQta 68,70,1280\nQmvav 192,76,248\nQmvav 224,72,84\nQmvav 224,76,116\nQta 70,70,2304\n"
"s_add_u32 s95, s72, 23552\ns_waitcnt vmcnt(19)\ns_waitcnt lgkmcnt(4)\nQmaaa 116,56,0\nbuffer_load_dwordx4 v61, s[52:55], s95 offen offset:1024 lds\nQmaaa 116,60,28\nQmaaa 120,56,4\nQmaaa 120,60,32\n"
"Qmaaa 124,56,8\nQta 72,68,57856\ns_add_u32 m0, s96, 122880\ns_add_u32 s95, s72, 45056\nQmaaa 124,60,36\nbuffer_load_dwordx4 v61, s[52:55], s95 offen lds\nQmaaa 128,56,12\nQmaaa 128,60,40\n"
"Qta 74,68,58880\nQmaaa 132,56,16\ns_add_u32 s95, s72, 64512\nQmaaa 132,60,44\nbuffer_load_dwordx4 v61, s[52:55], s95 offen offset:1024 lds\nQmaaa 136,56,20\nQta 76,70,1536\nQmaaa 136,60,48\n"
"Qmaaa 140,56,24\nQmaaa 140,60,52\nQta 78,70,2560\ns_add_u32 m0, s96, 16384\ns_add_u32 s95, s72, 4096\ns_waitcnt lgkmcnt(4)\nQmaaa 144,64,0\nbuffer_load_dwordx4 v62, s[52:55], s95 offen lds\n"
"Qmaaa 144,68,28\nQmaaa 148,64,4\nQmaaa 148,68,32\nQmaaa 152,64,8\nQta 56,68,58112\ns_add_u32 s95, s72, 44032\nQmaaa 152,68,36\nbuffer_load_dwordx4 v62, s[52:55], s95 offen offset:1024 lds\n"
"Qmaaa 156,64,12\nQmaaa 156,68,40\nQta 58,68,59136\nQmaaa 160,64,16\ns_add_u32 m0, s96, 24576\ns_add_u32 s95, s72, 4096\nQmaaa 160,68,44\nbuffer_load_dwordx4 v63, s[52:55], s95 offen lds\n"
"Qmaaa 164,64,20\nQta 60,70,1792\nQmaaa 164,68,48\nQmaaa 168,64,24\nQmaaa 168,68,52\nQta 62,70,2816\ns_add_u32 s95, s72, 23552\ns_waitcnt lgkmcnt(4)\n"
"Qmvaa 0,72,0\nbuffer_load_dwordx4 v63, s[52:55], s95 offen offset:1024 lds\nQmvaa 0,76,28\nQmvaa 4,72,4\nQmvaa 4,76,32\nQmvaa 8,72,8\nQta 64,70,9216\ns_add_u32 s31, s75, 0\n"
"Qmvaa 8,76,36\nbuffer_load_dwordx4 v[80:83], v60, s[48:51], s31 offen offset:0 sc1\nQmvaa 12,72,12\nQmvaa 12,76,40\nQta 66,70,9728\nQmvaa 16,72,16\nQmvaa 16,76,44\nbuffer_load_dwordx4 v[112:115], v60, s[48:51], s31 offen offset:1024 sc1\n"
"Qmvaa 20,72,20\nQta 68,70,10240\nv_add_u32 v75, 1, v75\nQmvaa 20,76,48\ns_waitcnt vmcnt(43)\nds_write_b32 v73, v75\nQmvaa 24,72,24\nQmvaa 24,76,52\n"
"Qta 70,70,10752\ns_waitcnt lgkmcnt(5)\nQmvaa 28,56,0\nbuffer_load_dwordx4 v[144:147], v60, s[48:51], s31 offen offset:2048 sc1\nQmvaa 28,60,28\nQmvaa 32,56,4\nQmvaa 32,60,32\nQmvaa 36,56,8\n"
"Qta 72,70,9472\nQmvaa 36,60,36\nds_read_b32 v66, v74\nQmvaa 40,56,12\nbuffer_load_dwordx4 v[176:179], v60, s[48:51], s31 offen offset:3072 sc1\nQmvaa 40,60,40\nQta 74,70,9984\nQmvaa 44,56,16\n"
"v_add_u32 v67, 3, v65\nQmvaa 44,60,44\ns_waitcnt lgkmcnt(1)\nv_cmp_gt_u32 vcc, v75, v66\nQmvaa 48,56,20\ns_cbranch_vccnz .Lso1300\n.Lro1300:\nQmvaa 48,60,48\n"
"Qta 76,70,10496\ns_add_u32 s31, s75, 4096\nQmvaa 52,56,24\nbuffer_load_dwordx4 v[208:211], v60, s[48:51], s31 offen offset:0 sc1\nQmvaa 52,60,52\nbuffer_store_dword v67, v77, s[24:27], 0 offen sc1\ns_waitcnt lgkmcnt(4)\nQmaaa 172,64,0\n"
"Qta 78,70,11008\nQmaaa 172,68,28\nQmaaa 176,64,4\nbuffer_load_dwordx4 v[240:243], v60, s[48:51], s31 offen offset:1024 sc1\nQmaaa 176,68,32\nQmaaa 180,64,8\nQta 56,70,17408\nQmaaa 180,68,36\n"
"Qmaaa 184,64,12\nbuffer_load_dwordx4 v[84:87], v60, s[48:51], s31 offen offset:2048 sc1\nQmaaa 184,68,40\nQta 58,70,18432\nQmaaa 188,64,16\nQmaaa 188,68,44\nQmaaa 192,64,20\nQta 60,69,0\n"
"s_add_u32 s31, s75, 14336\nQmaaa 192,68,48\nbuffer_load_dwordx4 v[88:91], v60, s[48:51], s31 offen offset:0 sc1\nQmaaa 196,64,24\nQmaaa 196,68,52\nQta 62,69,1024\ns_waitcnt lgkmcnt(4)\nQmvaa 92,72,0\n"
"Qmvaa 92,76,28\nbuffer_load_dwordx4 v[120:123], v60, s[48:51], s31 offen offset:1024 sc1\nQmvaa 124,72,4\nQmvaa 124,76,32\nQmvaa 156,72,8\nQta 64,70,17664\nQmvaa 156,76,36\nbuffer_load_dwordx4 v[152:155], v60, s[48:51], s31 offen offset:2048 sc1\n"
"Qmvaa 188,72,12\nQmvaa 188,76,40\nQta 66,70,18688\nQmvaa 220,72,16\nQmvaa 220,76,44\nbuffer_load_dwordx4 v[184:187], v60, s[48:51], s31 offen offset:3072 sc1\nQmvaa 252,72,20\nQta 68,69,256\n"
"Qmvaa 252,76,48\nQmvaa 100,72,24\nQmvaa 100,76,52\nQta 70,69,1280\ns_add_u32 s31, s75, 18432\ns_waitcnt lgkmcnt(4)\nQmaaa 200,56,0\nbuffer_load_dwordx4 v[216:219], v60, s[48:51], s31 offen offset:0 sc1\n"
"Qmaaa 200,60,28\nQmaaa 204,56,4\nQmaaa 204,60,32\nQmaaa 208,56,8\nQta 72,70,17920\nQmaaa 208,60,36\nbuffer_load_dwordx4 v[248:251], v60, s[48:51], s31 offen offset:1024 sc1\nQmaaa 212,56,12\n"
"Qmaaa 212,60,40\nQta 74,70,18944\nQmaaa 216,56,16\nQmaaa 216,60,44\nbuffer_load_dwordx4 v[116:119], v60, s[48:51], s31 offen offset:2048 sc1\nQmaaa 220,56,20\nQta 76,69,512\nQmaaa 220,60,48\n"
"Qmaaa 224,56,24\nQmaaa 224,60,52\nQta 78,69,1536\ns_add_u32 m0, s96, 32768\ns_add_u32 s95, s72, 45056\ns_waitcnt lgkmcnt(4)\nQmaaa 228,64,0\nbuffer_load_dwordx4 v63, s[52:55], s95 offen lds\n"
"Qmaaa 228,68,28\nQmaaa 232,64,4\nQmaaa 232,68,32\nQmaaa 236,64,8\nQta 56,70,18176\ns_add_u32 s95, s72, 64512\nQmaaa 236,68,36\nbuffer_load_dwordx4 v63, s[52:55], s95 offen offset:1024 lds\n"
"Qmaaa 240,64,12\nQmaaa 240,68,40\nQta 58,70,19200\nQmaaa 244,64,16\ns_add_u32 m0, s96, 0\ns_add_u32 s95, s73, 4096\nQmaaa 244,68,44\nbuffer_load_dwordx4 v61, s[52:55], s95 offen lds\n"
"Qmaaa 248,64,20\nQta 60,69,768\nQmaaa 248,68,48\nQmaaa 252,64,24\nQmaaa 252,68,52\nQta 62,69,1792\ns_add_u32 s95, s73, 23552\ns_waitcnt lgkmcnt(4)\n"
"Qmvaa 132,72,0\nbuffer_load_dwordx4 v61, s[52:55], s95 offen offset:1024 lds\nQmvaa 132,76,28\nQmvaa 164,72,4\nQmvaa 164,76,32\nQmvaa 196,72,8\nQta 64,69,8192\ns_add_u32 m0, s96, 8192\n"
"s_add_u32 s95, s73, 45056\nQmvaa 196,76,36\nbuffer_load_dwordx4 v61, s[52:55], s95 offen lds\nQmvaa 228,72,12\nQmvaa 228,76,40\nQta 66,69,9216\nQmvaa 108,72,16\ns_add_u32 s95, s73, 64512\n"
"Qmvaa 108,76,44\nbuffer_load_dwordx4 v61, s[52:55], s95 offen offset:1024 lds\nQmvaa 140,72,20\nQta 68,69,16384\nQmvaa 140,76,48\nQmvaa 172,72,24\nQmvaa 172,76,52\nQta 70,69,17408\n"
"s_add_u32 m0, s96, 40960\ns_add_u32 s95, s73, 4096\ns_waitcnt lgkmcnt(4)\nQmvaa 204,56,0\nbuffer_load_dwordx4 v62, s[52:55], s95 offen lds\nQmvaa 204,60,28\nQmvaa 236,56,4\nQmvaa 236,60,32\n"
"Qmvaa 96,56,8\nQta 72,69,8448\ns_add_u32 s95, s73, 44032\nQmvaa 96,60,36\nbuffer_load_dwordx4 v62, s[52:55], s95 offen offset:1024 lds\nQmvaa 128,56,12\nQmvaa 128,60,40\nQta 74,69,9472\n"
"Qmvaa 160,56,16\ns_add_u32 m0, s96, 49152\ns_add_u32 s95, s73, 4096\nQmvaa 160,60,44\nbuffer_load_dwordx4 v63, s[52:55], s95 offen lds\nQmvaa 192,56,20\nQta 76,69,16640\nQmvaa 192,60,48\n"
"Qmvaa 224,56,24\nQmvaa 224,60,52\nQta 78,69,17664\ns_add_u32 s95, s73, 23552\ns_waitcnt vmcnt(36)\ns_waitcnt lgkmcnt(4)\nQmaaa 116,64,80\nbuffer_load_dwordx4 v63, s[52:55], s95 offen offset:1024 lds\n"
"Qmaaa 116,68,108\nQmaaa 120,64,84\nQmaaa 120,68,112\nQmaaa 124,64,88\nQta 56,69,8704\ns_add_u32 s31, s77, 0\nQmaav 124,68,168\nbuffer_load_dwordx4 a[0:3], v60, s[48:51], s31 offen offset:0 sc1\n"
"Qmaaa 128,64,92\nQmaav 128,68,200\nQta 58,69,9728\nQmaaa 132,64,96\nQmaav 132,68,232\nbuffer_load_dwordx4 a[4:7], v60, s[48:51], s31 offen offset:1024 sc1\nQmaaa 136,64,100\nQta 60,69,16896\n"
"Qmaav 136,68,212\nQmaaa 140,64,104\nQmaav 140,68,244\nQta 62,69,17920\ns_waitcnt lgkmcnt(4)\nQmaaa 144,72,80\nbuffer_load_dwordx4 a[8:11], v60, s[48:51], s31 offen offset:2048 sc1\nQmaaa 144,76,108\n"
"Qmaaa 148,72,84\nQmaaa 148,76,112\nQmaaa 152,72,88\nQta 64,69,8960\nQmaav 152,76,168\nbuffer_load_dwordx4 a[12:15], v60, s[48:51], s31 offen offset:3072 sc1\nQmaaa 156,72,92\nQmaav 156,76,200\n"
"Qta 66,69,9984\nQmaaa 160,72,96\ns_add_u32 s31, s77, 4096\nQmaav 160,76,232\nbuffer_load_dwordx4 a[16:19], v60, s[48:51], s31 offen offset:0 sc1\nQmaaa 164,72,100\nQta 68,69,17152\nQmaav 164,76,212\n"
"Qmaaa 168,72,104\nQmaav 168,76,244\nQta 70,69,18176\ns_waitcnt lgkmcnt(4)\nQmvaa 0,56,80\nbuffer_load_dwordx4 a[20:23], v60, s[48:51], s31 offen offset:1024 sc1\nQmvaa 0,60,108\nQmvaa 4,56,84\n"
"Qmvaa 4,60,112\nQmvaa 8,56,88\nQta 72,69,24576\nQmvav 8,60,168\nbuffer_load_dwordx4 a[24:27], v60, s[48:51], s31 offen offset:2048 sc1\nQmvaa 12,56,92\nQmvav 12,60,200\nQta 74,69,25088\n"
"Qmvaa 16,56,96\ns_add_u32 s31, s77, 14336\nQmvav 16,60,232\nbuffer_load_dwordx4 a[28:31], v60, s[48:51], s31 offen offset:0 sc1\nQmvaa 20,56,100\nQta 76,69,25600\nv_add_u32 v75, 1, v75\nQmvav 20,60,212\n"
"s_waitcnt vmcnt(43)\nds_write_b32 v73, v75\nQmvaa 24,56,104\nQmvav 24,60,244\nQta 78,69,26112\ns_waitcnt lgkmcnt(5)\nQmvaa 28,64,80\nbuffer_load_dwordx4 a[32:35], v60, s[48:51], s31 offen offset:1024 sc1\n"
"Qmvaa 28,68,108\nQmvaa 32,64,84\nQmvaa 32,68,112\nQmvaa 36,64,88\nQta 56,69,24832\nQmvav 36,68,168\nds_read_b32 v66, v74\nQmvaa 40,64,92\n"
"buffer_load_dwordx4 a[36:39], v60, s[48:51], s31 offen offset:2048 sc1\nQmvav 40,68,200\nQta 58,69,25344\nQmvaa 44,64,96\nQmvav 44,68,232\ns_waitcnt lgkmcnt(1)\nv_cmp_gt_u32 vcc, v75, v66\nQmvaa 48,64,100\n"
"s_cbranch_vccnz .Lso1440\n.Lro1440:\nQmvav 48,68,212\nQta 60,69,25856\nv_add_u32 v75, 1, v75\nQmvaa 52,64,104\ns_waitcnt vmcnt(39)\nds_write_b32 v73, v75\n"
"Qmvav 52,68,244\nbuffer_load_dwordx4 a[40:43], v60, s[48:51], s31 offen offset:3072 sc1\ns_waitcnt lgkmcnt(5)\nQmaaa 172,72,80\nQta 62,69,26368\nQmaaa 172,76,108\nQmaaa 176,72,84\ns_add_u32 s31, s77, 18432\n"
"Qmaaa 176,76,112\nbuffer_load_dwordx4 a[44:47], v60, s[48:51], s31 offen offset:0 sc1\nQmaaa 180,72,88\nQta 64,69,32768\nQmaav 180,76,168\nQmaaa 184,72,92\nds_read_b32 v66, v74\nQmaav 184,76,200\n"
"Qta 66,69,33792\nQmaaa 188,72,96\nbuffer_load_dwordx4 a[48:51], v60, s[48:51], s31 offen offset:1024 sc1\nQmaav 188,76,232\nbuffer_load_dword v78, v76, s[24:27], 0 offen sc1\nQmaaa 192,72,100\ns_waitcnt lgkmcnt(1)\nv_cmp_gt_u32 vcc, v75, v66\n"
"Qmaav 192,76,212\ns_cbranch_vccnz .Lso1455\n.Lro1455:\nQmaaa 196,72,104\nQta 68,69,40960\nQmaav 196,76,244\nbuffer_load_dwordx4 a[52:55], v60, s[48:51], s31 offen offset:2048 sc1\ns_waitcnt lgkmcnt(4)\n"
"Qmvaa 92,56,80\nQmvaa 92,60,108\nQta 70,69,41984\nQmvaa 124,56,84\ns_add_u32 m0, s96, 57344\ns_add_u32 s95, s73, 45056\nQmvaa 124,60,112\nbuffer_load_dwordx4 v63, s[52:55], s95 offen lds\n"
"Qmvaa 156,56,88\nQta 72,69,33024\nQmvav 156,60,168\nQmvaa 188,56,92\nQmvav 188,60,200\nQta 74,69,34048\ns_add_u32 s95, s73, 64512\nQmvaa 220,56,96\n"
"buffer_load_dwordx4 v63, s[52:55], s95 offen offset:1024 lds\nQmvav 220,60,232\nQmvaa 252,56,100\nQta 76,69,41216\nQmvav 252,60,212\nQmvaa 100,56,104\nQmvav 100,60,244\nQta 78,69,42240\n"
"s_waitcnt lgkmcnt(4)\nQmaaa 200,64,80\nQmaaa 200,68,108\nQmaaa 204,64,84\nQmaaa 204,68,112\nQmaaa 208,64,88\nQta 56,69,33280\nQmaav 208,68,168\n"
"Qmaaa 212,64,92\nQmaav 212,68,200\nQta 58,69,34304\nQmaaa 216,64,96\nQmaav 216,68,232\nQmaaa 220,64,100\nQta 60,69,41472\nQmaav 220,68,212\n"
"Qmaaa 224,64,104\nQmaav 224,68,244\nQta 62,69,42496\ns_waitcnt lgkmcnt(4)\nQmaaa 228,72,80\nQmaaa 228,76,108\nQmaaa 232,72,84\nQmaaa 232,76,112\n"
"Qmaaa 236,72,88\nQta 64,69,33536\nQmaav 236,76,168\nQmaaa 240,72,92\nQmaav 240,76,200\nQta 66,69,34560\nQmaaa 244,72,96\nQmaav 244,76,232\n"
"Qmaaa 248,72,100\nQta 68,69,41728\nQmaav 248,76,212\nQmaaa 252,72,104\nQmaav 252,76,244\nQta 70,69,42752\ns_waitcnt lgkmcnt(4)\nQmvaa 132,56,80\n"
"Qmvaa 132,60,108\nQmvaa 164,56,84\nQmvaa 164,60,112\nQmvaa 196,56,88\nQta 72,69,49152\nQmvav 196,60,168\nQmvaa 228,56,92\nQmvav 228,60,200\n"
"Qta 74,69,50176\nQmvaa 108,56,96\nQmvav 108,60,232\nQmvaa 140,56,100\nQta 76,69,57344\nQmvav 140,60,212\ns_waitcnt vmcnt(3)\nv_sub_u32 v66, v78, v65\n"
"v_cmp_gt_u32 vcc, 3, v66\ns_and_b64 vcc, vcc, s[28:29]\ns_cbranch_scc1 .Lf3_w\n.Lf3_r:\nv_add_u32 v75, 1, v75\nQmvaa 172,56,104\ns_waitcnt vmcnt(42)\nds_write_b32 v73, v75\n"
"Qmvav 172,60,244\nQta 78,69,58368\ns_waitcnt lgkmcnt(5)\nQmvaa 204,64,80\ns_add_u32 s31, s74, 28672\nQmvaa 204,68,108\nbuffer_load_dwordx4 a[80:83], v60, s[48:51], s31 offen offset:0 sc1\nQmvaa 236,64,84\n"
"Qmvaa 236,68,112\ns_bitcmp1_b32 %19, 2\ns_cbranch_scc1 .Lrole1\nQmvaa 96,64,88\nQta 56,69,49408\ns_lshl_b32 s46, %19, 4\nQmvav 96,68,168\nbuffer_load_dwordx4 a[84:87], v60, s[48:51], s31 offen offset:1024 sc1\n"
"Qmvaa 128,64,92\nds_read_b32 v66, v74\ns_add_u32 s46, s46, 128\nQmvav 128,68,200\nQta 58,69,50432\nv_mov_b32 v77, 0x7ffff000\n.Lpg18:\ns_cbranch_execz .Lpg22\n"
"Qmvaa 160,64,96\nQmvav 160,68,232\nbuffer_load_dwordx4 a[88:91], v60, s[48:51], s31 offen offset:2048 sc1\nQmvaa 192,64,100\ns_waitcnt lgkmcnt(1)\nv_cmp_gt_u32 vcc, v75, v66\nQmvav 192,68,212\ns_cbranch_vccnz .Lso1525r0\n"
".Lro1525r0:\nQmvaa 224,64,104\nQta 60,69,57600\nQmvav 224,68,244\nbuffer_load_dwordx4 a[92:95], v60, s[48:51], s31 offen offset:3072 sc1\ns_waitcnt vmcnt(31)\ns_waitcnt lgkmcnt(4)\nQmaav 116,72,80\n"
"Qmaav 116,76,88\nQta 62,69,58624\nQmaav 120,72,112\ns_add_u32 m0, s96, 65536\ns_add_u32 s95, s67, 86016\nQmaav 120,76,120\nbuffer_load_dwordx4 v63, s[52:55], s95 offen lds\nQmaav 124,72,144\n"
"Qta 64,69,49664\nQmaav 124,76,152\nQmaav 128,72,176\nQmaav 128,76,184\nQta 66,69,50688\ns_add_u32 s95, s67, 105472\nQmaav 132,72,208\nbuffer_load_dwordx4 v63, s[52:55], s95 offen offset:1024 lds\n"
"Qmaav 132,76,216\nQmaav 136,72,240\nQta 68,69,57856\nQmaav 136,76,248\ns_add_u32 m0, s96, 73728\ns_add_u32 s95, s67, 126976\nQmaav 140,72,84\nbuffer_load_dwordx4 v63, s[52:55], s95 offen lds\n"
"Qmaav 140,76,116\nQta 70,69,58880\ns_waitcnt lgkmcnt(4)\nQmaav 144,56,80\nQmaav 144,60,88\ns_add_u32 s95, s67, 146432\nQmaav 148,56,112\nbuffer_load_dwordx4 v63, s[52:55], s95 offen offset:1024 lds\n"
"Qmaav 148,60,120\nQmaav 152,56,144\nQta 72,69,49920\nQmaav 152,60,152\ns_add_u32 m0, s96, 81920\ns_add_u32 s95, s72, 86016\nQmaav 156,56,176\nbuffer_load_dwordx4 v63, s[52:55], s95 offen lds\n"
"Qmaav 156,60,184\nQta 74,69,50944\nQmaav 160,56,208\nQmaav 160,60,216\nQmaav 164,56,240\nQta 76,69,58112\ns_add_u32 s95, s72, 105472\nQmaav 164,60,248\n"
"buffer_load_dwordx4 v63, s[52:55], s95 offen offset:1024 lds\nQmaav 168,56,84\nQmaav 168,60,116\nQta 78,69,59136\ns_waitcnt lgkmcnt(4)\nQmvav 0,64,80\ns_add_u32 m0, s96, 90112\ns_add_u32 s95, s72, 126976\n"
"Qmvav 0,68,88\nbuffer_load_dwordx4 v63, s[52:55], s95 offen lds\nQmvav 4,64,112\nQmvav 4,68,120\nQmvav 8,64,144\nQta 56,68,16384\ns_add_u32 s95, s72, 146432\nQmvav 8,68,152\n"
"buffer_load_dwordx4 v63, s[52:55], s95 offen offset:1024 lds\nQmvav 12,64,176\nQmvav 12,68,184\nQta 58,68,16896\nQmvav 16,64,208\ns_add_u32 s31, s74, 32768\nQmvav 16,68,216\nbuffer_load_dwordx4 a[96:99], v60, s[48:51], s31 offen offset:0 sc1\n"
"Qmvav 20,64,240\nQta 60,68,17408\nQmvav 20,68,248\nQmvav 24,64,84\nQmvav 24,68,116\nQta 62,68,17920\ns_waitcnt lgkmcnt(4)\nQmvav 28,72,80\n"
"buffer_load_dwordx4 a[100:103], v60, s[48:51], s31 offen offset:1024 sc1\nQmvav 28,76,88\nQmvav 32,72,112\nQmvav 32,76,120\nQmvav 36,72,144\nQta 64,68,16640\nQmvav 36,76,152\nbuffer_load_dwordx4 a[104:107], v60, s[48:51], s31 offen offset:2048 sc1\n"
"Qmvav 40,72,176\nQmvav 40,76,184\nQta 66,68,17152\nQmvav 44,72,208\ns_add_u32 s31, s74, 43008\nv_cmp_eq_u32 s[44:45], s46, v60\ns_nop 1\nv_cndmask_b32 v77, v77, v76, s[44:45]\n"
"v_add_u32 v67, 1, v65\ns_xor_b32 s46, %19, 12\ns_lshl_b32 s28, 1, s46\nQmvav 44,76,216\nbuffer_load_dwordx4 a[108:111], v60, s[48:51], s31 offen offset:0 sc1\nQmvav 48,72,240\nQta 68,68,17664\nQmvav 48,76,248\n"
"v_add_u32 v75, 1, v75\nQmvav 52,72,84\ns_waitcnt vmcnt(37)\nds_write_b32 v73, v75\nQmvav 52,76,116\nQta 70,68,18176\ns_waitcnt lgkmcnt(5)\nQmaav 172,56,80\n"
"buffer_load_dwordx4 a[112:115], v60, s[48:51], s31 offen offset:1024 sc1\nQmaav 172,60,88\nQmaav 176,56,112\nQmaav 176,60,120\nQmaav 180,56,144\nQta 72,68,24576\nQmaav 180,60,152\nbuffer_load_dwordx4 v[168:171], v60, s[48:51], s31 offen offset:2048 sc1\n"
"Qmaav 184,56,176\nds_read_b32 v66, v74\nQmaav 184,60,184\nQta 74,68,25600\nQmaav 188,56,208\nQmaav 188,60,216\nbuffer_load_dwordx4 v[200:203], v60, s[48:51], s31 offen offset:3072 sc1\nQmaav 192,56,240\n"
"s_waitcnt lgkmcnt(1)\nv_cmp_gt_u32 vcc, v75, v66\nQmaav 192,60,248\ns_cbranch_vccnz .Lso1595r0\n.Lro1595r0:\nQmaav 196,56,84\nQta 76,68,32768\ns_add_u32 s31, s74, 47104\n"
"Qmaav 196,60,116\nbuffer_load_dwordx4 v[232:235], v60, s[48:51], s31 offen offset:0 sc1\ns_waitcnt lgkmcnt(4)\nQmvav 92,64,80\nQmvav 92,68,88\nQta 78,68,33792\nQmvav 124,64,112\nQmvav 124,68,120\n"
"buffer_load_dwordx4 v[212:215], v60, s[48:51], s31 offen offset:1024 sc1\nQmvav 156,64,144\nQta 56,68,24832\nQmvav 156,68,152\nQmvav 188,64,176\nQmvav 188,68,184\nQta 58,68,25856\nQmvav 220,64,208\n"
"buffer_load_dwordx4 v[244:247], v60, s[48:51], s31 offen offset:2048 sc1\nQmvav 220,68,216\nQmvav 252,64,240\nQta 60,68,33024\nQmvav 252,68,248\ns_add_u32 m0, s91, 0\ns_add_u32 s31, s77, 28672\nQmvav 100,64,84\n"
"buffer_load_dwordx4 v60, s[48:51], s31 offen sc1 lds\nQmvav 100,68,116\nQta 62,68,34048\ns_waitcnt lgkmcnt(4)\nQmaav 200,72,80\nQmaav 200,76,88\nQmaav 204,72,112\nbuffer_load_dwordx4 v60, s[48:51], s31 offen offset:1024 sc1 lds\n"
"Qmaav 204,76,120\nQmaav 208,72,144\nQta 64,68,25088\nQmaav 208,76,152\nQmaav 212,72,176\nbuffer_load_dwordx4 v60, s[48:51], s31 offen offset:2048 sc1 lds\nQmaav 212,76,184\nQta 66,68,26112\n"
"Qmaav 216,72,208\nQmaav 216,76,216\nQmaav 220,72,240\nQta 68,68,33280\nQmaav 220,76,248\nbuffer_load_dwordx4 v60, s[48:51], s31 offen offset:3072 sc1 lds\nQmaav 224,72,84\nQmaav 224,76,116\n"
"Qta 70,68,34304\ns_waitcnt lgkmcnt(4)\nQmaav 228,56,80\ns_add_u32 m0, s91, 4096\ns_add_u32 s31, s77, 32768\nQmaav 228,60,88\nbuffer_load_dwordx4 v60, s[48:51], s31 offen sc1 lds\nQmaav 232,56,112\n"
"Qmaav 232,60,120\nQmaav 236,56,144\nQta 72,68,25344\nQmaav 236,60,152\nbuffer_load_dwordx4 v60, s[48:51], s31 offen offset:1024 sc1 lds\nQmaav 240,56,176\nQmaav 240,60,184\nQta 74,68,26368\n"
"Qmaav 244,56,208\nQmaav 244,60,216\nbuffer_load_dwordx4 v60, s[48:51], s31 offen offset:2048 sc1 lds\nQmaav 248,56,240\nQta 76,68,33536\nQmaav 248,60,248\nQmaav 252,56,84\nQmaav 252,60,116\n"
"Qta 78,68,34560\ns_add_u32 m0, s91, 33792\ns_add_u32 s31, s77, 43008\ns_waitcnt lgkmcnt(4)\nQmvav 132,64,80\nbuffer_load_dwordx4 v60, s[48:51], s31 offen sc1 lds\nQmvav 132,68,88\nQmvav 164,64,112\n"
"Qmvav 164,68,120\nQmvav 196,64,144\nQta 56,68,0\nQmvav 196,68,152\nbuffer_load_dwordx4 v60, s[48:51], s31 offen offset:1024 sc1 lds\nQmvav 228,64,176\nQmvav 228,68,184\nQta 58,68,1024\n"
"Qmvav 108,64,208\nQmvav 108,68,216\nbuffer_load_dwordx4 v60, s[48:51], s31 offen offset:2048 sc1 lds\nQmvav 140,64,240\nQta 60,68,8192\nQmvav 140,68,248\nv_add_u32 v75, 1, v75\nQmvav 172,64,84\n"
"s_waitcnt vmcnt(49)\nds_write_b32 v73, v75\nQmvav 172,68,116\nQta 62,68,9216\ns_waitcnt lgkmcnt(5)\nQmvav 204,72,80\nbuffer_load_dwordx4 v60, s[48:51], s31 offen offset:3072 sc1 lds\nQmvav 204,76,88\n"
"Qmvav 236,72,112\nQmvav 236,76,120\nQmvav 96,72,144\nQta 64,68,256\ns_add_u32 m0, s91, 37888\ns_add_u32 s31, s77, 47104\nQmvav 96,76,152\nbuffer_load_dwordx4 v60, s[48:51], s31 offen sc1 lds\n"
"Qmvav 128,72,176\nds_read_b32 v66, v74\nQmvav 128,76,184\nQta 66,68,1280\nQmvav 160,72,208\nQmvav 160,76,216\nbuffer_load_dwordx4 v60, s[48:51], s31 offen offset:1024 sc1 lds\nQmvav 192,72,240\n"
"s_waitcnt lgkmcnt(1)\nv_cmp_gt_u32 vcc, v75, v66\nQmvav 192,76,248\ns_cbranch_vccnz .Lso1665r0\n.Lro1665r0:\nQmvav 224,72,84\nQta 68,68,8448\nQmvav 224,76,116\n"
"buffer_load_dwordx4 v60, s[48:51], s31 offen offset:2048 sc1 lds\ns_waitcnt vmcnt(38)\ns_waitcnt lgkmcnt(4)\nQmaaa 116,56,0\nQmaaa 116,60,28\nQta 70,68,9472\nQmaaa 120,56,4\ns_add_u32 m0, s96, 16384\n"
"s_add_u32 s95, s73, 86016\nQmaaa 120,60,32\nbuffer_load_dwordx4 v63, s[52:55], s95 offen lds\nQmaaa 124,56,8\nQta 72,68,512\nQmaaa 124,60,36\nQmaaa 128,56,12\nQmaaa 128,60,40\n"
"Qta 74,68,1536\ns_add_u32 s95, s73, 105472\nQmaaa 132,56,16\nbuffer_load_dwordx4 v63, s[52:55], s95 offen offset:1024 lds\nQmaaa 132,60,44\nQmaaa 136,56,20\nQta 76,68,8704\nQmaaa 136,60,48\n"
"s_add_u32 m0, s96, 24576\ns_add_u32 s95, s73, 126976\nQmaaa 140,56,24\nbuffer_load_dwordx4 v63, s[52:55], s95 offen lds\nQmaaa 140,60,52\nQta 78,68,9728\ns_waitcnt lgkmcnt(4)\nQmaaa 144,64,0\n"
"Qmaaa 144,68,28\ns_add_u32 s95, s73, 146432\nQmaaa 148,64,4\nbuffer_load_dwordx4 v63, s[52:55], s95 offen offset:1024 lds\nQmaaa 148,68,32\nQmaaa 152,64,8\nQta 56,68,768\nQmaaa 152,68,36\n"
"s_add_u32 s31, s75, 28672\nQmaaa 156,64,12\nbuffer_load_dwordx4 v[80:83], v60, s[48:51], s31 offen offset:0 sc1\nQmaaa 156,68,40\nQta 58,68,1792\nQmaaa 160,64,16\nQmaaa 160,68,44\nQmaaa 164,64,20\n"
"Qta 60,68,8960\nQmaaa 164,68,48\nbuffer_load_dwordx4 v[112:115], v60, s[48:51], s31 offen offset:1024 sc1\nQmaaa 168,64,24\nQmaaa 168,68,52\nQta 62,68,9984\ns_waitcnt lgkmcnt(4)\nQmvaa 0,72,0\n"
"Qmvaa 0,76,28\nbuffer_load_dwordx4 v[144:147], v60, s[48:51], s31 offen offset:2048 sc1\nQmvaa 4,72,4\nQmvaa 4,76,32\nQmvaa 8,72,8\nQta 64,68,40960\nQmvaa 8,76,36\nbuffer_load_dwordx4 v[176:179], v60, s[48:51], s31 offen offset:3072 sc1\n"
"Qmvaa 12,72,12\nQmvaa 12,76,40\nQta 66,68,41472\nQmvaa 16,72,16\ns_add_u32 s31, s75, 32768\nQmvaa 16,76,44\nbuffer_load_dwordx4 v[208:211], v60, s[48:51], s31 offen offset:0 sc1\nQmvaa 20,72,20\n"
"Qta 68,68,41984\nQmvaa 20,76,48\nQmvaa 24,72,24\nQmvaa 24,76,52\nQta 70,68,42496\ns_waitcnt lgkmcnt(4)\nQmvaa 28,56,0\nbuffer_load_dwordx4 v[240:243], v60, s[48:51], s31 offen offset:1024 sc1\n"
"Qmvaa 28,60,28\nQmvaa 32,56,4\nQmvaa 32,60,32\nQmvaa 36,56,8\nQta 72,68,41216\nQmvaa 36,60,36\nbuffer_load_dwordx4 v[84:87], v60, s[48:51], s31 offen offset:2048 sc1\nQmvaa 40,56,12\n"
"Qmvaa 40,60,40\nQta 74,68,41728\nQmvaa 44,56,16\ns_add_u32 s31, s75, 43008\nQmvaa 44,60,44\nbuffer_load_dwordx4 v[88:91], v60, s[48:51], s31 offen offset:0 sc1\nQmvaa 48,56,20\nQta 76,68,42240\n"
"Qmvaa 48,60,48\nv_add_u32 v75, 1, v75\nQmvaa 52,56,24\ns_waitcnt vmcnt(38)\nds_write_b32 v73, v75\nQmvaa 52,60,52\nQta 78,68,42752\ns_waitcnt lgkmcnt(5)\n"
"Qmaaa 172,64,0\nbuffer_load_dwordx4 v[120:123], v60, s[48:51], s31 offen offset:1024 sc1\nQmaaa 172,68,28\nQmaaa 176,64,4\nQmaaa 176,68,32\nQmaaa 180,64,8\nQta 56,68,49152\nQmaaa 180,68,36\n"
"buffer_load_dwordx4 v[152:155], v60, s[48:51], s31 offen offset:2048 sc1\nQmaaa 184,64,12\nds_read_b32 v66, v74\nQmaaa 184,68,40\nQta 58,68,50176\nQmaaa 188,64,16\nQmaaa 188,68,44\nbuffer_load_dwordx4 v[184:187], v60, s[48:51], s31 offen offset:3072 sc1\n"
"Qmaaa 192,64,20\ns_waitcnt lgkmcnt(1)\nv_cmp_gt_u32 vcc, v75, v66\nQmaaa 192,68,48\ns_cbranch_vccnz .Lso1735r0\n.Lro1735r0:\nQmaaa 196,64,24\nQta 60,68,57344\n"
"s_add_u32 s31, s75, 47104\nQmaaa 196,68,52\nbuffer_load_dwordx4 v[216:219], v60, s[48:51], s31 offen offset:0 sc1\ns_waitcnt lgkmcnt(4)\nQmvaa 92,72,0\nQmvaa 92,76,28\nQta 62,68,58368\nQmvaa 124,72,4\n"
"Qmvaa 124,76,32\nbuffer_load_dwordx4 v[248:251], v60, s[48:51], s31 offen offset:1024 sc1\nQmvaa 156,72,8\nQta 64,68,49408\nQmvaa 156,76,36\nQmvaa 188,72,12\nQmvaa 188,76,40\nQta 66,68,50432\n"
"Qmvaa 220,72,16\nbuffer_load_dwordx4 v[116:119], v60, s[48:51], s31 offen offset:2048 sc1\nQmvaa 220,76,44\nQmvaa 252,72,20\nQta 68,68,57600\nQmvaa 252,76,48\ns_add_u32 m0, s96, 32768\ns_add_u32 s95, s67, 86016\n"
"Qmvaa 100,72,24\nbuffer_load_dwordx4 v61, s[52:55], s95 offen lds\nQmvaa 100,76,52\nQta 70,68,58624\ns_waitcnt lgkmcnt(4)\nQmaaa 200,56,0\nQmaaa 200,60,28\ns_add_u32 s95, s67, 105472\n"
"Qmaaa 204,56,4\nbuffer_load_dwordx4 v61, s[52:55], s95 offen offset:1024 lds\nQmaaa 204,60,32\nQmaaa 208,56,8\nQta 72,68,49664\nQmaaa 208,60,36\ns_add_u32 m0, s96, 0\ns_add_u32 s95, s67, 126976\n"
"Qmaaa 212,56,12\nbuffer_load_dwordx4 v61, s[52:55], s95 offen lds\nQmaaa 212,60,40\nQta 74,68,50688\nQmaaa 216,56,16\nQmaaa 216,60,44\nQmaaa 220,56,20\nQta 76,68,57856\n"
"s_add_u32 s95, s67, 146432\nQmaaa 220,60,48\nbuffer_load_dwordx4 v61, s[52:55], s95 offen offset:1024 lds\nQmaaa 224,56,24\nQmaaa 224,60,52\nQta 78,68,58880\ns_waitcnt lgkmcnt(4)\nQmaaa 228,64,0\n"
"s_add_u32 m0, s96, 8192\ns_add_u32 s95, s72, 86016\nQmaaa 228,68,28\nbuffer_load_dwordx4 v61, s[52:55], s95 offen lds\nQmaaa 232,64,4\nQmaaa 232,68,32\nQmaaa 236,64,8\nQta 56,68,49920\n"
"s_add_u32 s95, s72, 105472\nQmaaa 236,68,36\nbuffer_load_dwordx4 v61, s[52:55], s95 offen offset:1024 lds\nQmaaa 240,64,12\nQmaaa 240,68,40\nQta 58,68,50944\nQmaaa 244,64,16\nQmaaa 244,68,44\n"
"Qmaaa 248,64,20\nQta 60,68,58112\nQmaaa 248,68,48\nQmaaa 252,64,24\nQmaaa 252,68,52\nQta 62,68,59136\ns_waitcnt lgkmcnt(4)\nQmvaa 132,72,0\n"
"Qmvaa 132,76,28\nQmvaa 164,72,4\nQmvaa 164,76,32\nQmvaa 196,72,8\nQta 64,69,0\nQmvaa 196,76,36\nQmvaa 228,72,12\nQmvaa 228,76,40\n"
"Qta 66,69,1024\nQmvaa 108,72,16\nQmvaa 108,76,44\nQmvaa 140,72,20\nQta 68,69,8192\nQmvaa 140,76,48\nv_add_u32 v75, 1, v75\nQmvaa 172,72,24\n"
"s_waitcnt vmcnt(20)\nds_write_b32 v73, v75\nQmvaa 172,76,52\nQta 70,69,9216\ns_waitcnt lgkmcnt(5)\nQmvaa 204,56,0\ns_waitcnt vmcnt(24)\nQda 0,58,0\n"
"Qmvaa 204,60,28\nQmvaa 236,56,4\nQda 28,58,33792\nQmvaa 236,60,32\nQmvaa 96,56,8\nQta 72,69,16384\nQmvaa 96,60,36\nQda 4,58,1024\n"
"Qmvaa 128,56,12\nds_read_b32 v66, v74\nQmvaa 128,60,40\nQta 74,69,17408\nQmvaa 160,56,16\nQda 32,58,34816\nQmvaa 160,60,44\nQmvaa 192,56,20\n"
"s_waitcnt lgkmcnt(2)\nv_cmp_gt_u32 vcc, v75, v66\nQmvaa 192,60,48\ns_cbranch_vccnz .Lso1805r0\n.Lro1805r0:\nQmvaa 224,56,24\nQta 76,69,24576\ns_add_u32 m0, s96, 40960\n"
"s_add_u32 s95, s72, 126976\nQmvaa 224,60,52\nbuffer_load_dwordx4 v61, s[52:55], s95 offen lds\ns_waitcnt vmcnt(39)\ns_waitcnt lgkmcnt(8)\nQmaaa 200,64,80\nQda 8,58,2048\nQmaaa 200,68,108\n"
".Lpg19:\ns_cbranch_execz .Lpg23\nQta 78,69,25600\nQmaaa 204,64,84\nQda 36,58,35840\ns_add_u32 s95, s72, 146432\nQmaaa 204,68,112\nbuffer_load_dwordx4 v61, s[52:55], s95 offen offset:1024 lds\n"
"Qmaaa 208,64,88\nQta 56,68,16384\nQmaav 208,68,168\nQda 12,58,3072\nQmaaa 212,64,92\nQmaav 212,68,200\nQta 58,68,17408\ns_add_u32 m0, s96, 49152\n"
"s_add_u32 s95, s73, 86016\nQmaaa 216,64,96\nbuffer_load_dwordx4 v61, s[52:55], s95 offen lds\nQmaav 216,68,232\nQda 40,58,36864\nQmaaa 220,64,100\nQta 60,68,24576\nQmaav 220,68,212\n"
"Qda 16,58,4096\ns_add_u32 s95, s73, 105472\nQmaaa 224,64,104\nbuffer_load_dwordx4 v61, s[52:55], s95 offen offset:1024 lds\nQmaav 224,68,244\nQta 62,68,25600\ns_waitcnt vmcnt(10)\ns_waitcnt lgkmcnt(8)\n"
"Qmaav 200,72,80\nQda 44,58,37888\nQmaav 200,76,88\ns_add_u32 m0, s96, 57344\ns_add_u32 s95, s73, 126976\nQmaav 204,72,112\nbuffer_load_dwordx4 v61, s[52:55], s95 offen lds\nQmaav 204,76,120\n"
"Qda 20,58,5120\nQmaav 208,72,144\nQta 64,69,256\nQmaav 208,76,152\nQda 48,58,38912\ns_add_u32 s95, s73, 146432\nQmaav 212,72,176\nbuffer_load_dwordx4 v61, s[52:55], s95 offen offset:1024 lds\n"
"Qmaav 212,76,184\nQta 66,69,1280\nQmaav 216,72,208\nQda 24,58,6144\nQmaav 216,76,216\nQmaav 220,72,240\nQta 68,69,8448\nQmaav 220,76,248\n"
"Qda 52,58,39936\nQmaav 224,72,84\nQmaav 224,76,116\nQta 70,69,9472\ns_waitcnt lgkmcnt(9)\nQmaaa 200,56,0\nQmaaa 200,60,28\nQmaaa 204,56,4\n"
"Qmaaa 204,60,32\ns_waitcnt lgkmcnt(9)\nQmaaa 208,56,8\nQta 72,69,16640\nQmaaa 208,60,36\ns_nop 1\nds_write_b128 v58, a[200:203] offset:0\nQmaaa 212,56,12\n"
"Qmaaa 212,60,40\nQta 74,69,17664\ns_waitcnt lgkmcnt(4)\nQmaaa 216,56,16\nds_write_b128 v58, a[204:207] offset:1024\nQmaaa 216,60,44\nQmaaa 220,56,20\nQta 76,69,24832\n"
"Qmaaa 220,60,48\nds_write_b128 v58, a[208:211] offset:2048\nQmaaa 224,56,24\nQmaaa 224,60,52\nQta 78,69,25856\ns_waitcnt lgkmcnt(7)\nQmaaa 228,64,80\nds_write_b128 v58, a[212:215] offset:3072\n"
"Qmaaa 228,68,108\nQmaaa 232,64,84\nQmaaa 232,68,112\nds_write_b128 v58, a[216:219] offset:4096\nQmaaa 236,64,88\nQta 56,68,16640\nQmaav 236,68,168\nQmaaa 240,64,92\n"
"ds_write_b128 v58, a[220:223] offset:5120\nQmaav 240,68,200\nQta 58,68,17664\nQmaaa 244,64,96\nQmaav 244,68,232\nds_write_b128 v58, a[224:227] offset:6144\nQmaaa 248,64,100\nQta 60,68,24832\n"
"Qmaav 248,68,212\nQmaaa 252,64,104\nQmaav 252,68,244\nQta 62,68,25856\ns_waitcnt lgkmcnt(8)\nQmaav 228,72,80\nv_add_u32 v75, 1, v75\nQmaav 228,76,88\n"
"ds_write_b32 v73, v75\nQmaav 232,72,112\nQmaav 232,76,120\nQmaav 236,72,144\nQta 64,69,512\nQmaav 236,76,152\nQmaav 240,72,176\nQmaav 240,76,184\n"
"Qta 66,69,1536\nQmaav 244,72,208\nQmaav 244,76,216\nds_read_b32 v66, v74\nQmaav 248,72,240\nQta 68,69,8704\nQmaav 248,76,248\nQmaav 252,72,84\n"
"Qmaav 252,76,116\ns_waitcnt lgkmcnt(1)\nv_cmp_gt_u32 vcc, v75, v66\ns_waitcnt lgkmcnt(5)\nQmaaa 228,56,0\ns_cbranch_vccnz .Lso1878r0\n.Lro1878r0:\nQmaaa 228,60,28\n"
"Qta 70,69,9728\nQmaaa 232,56,4\nQmaaa 232,60,32\nQdv 148,59,2048\nQmaaa 236,56,8\nQta 72,69,16896\nQmaaa 236,60,36\ns_nop 0\n"
"ds_write_b128 v58, a[228:231] offset:33792\nQmaaa 240,56,12\nQdv 180,59,3072\nQmaaa 240,60,40\nQta 74,69,17920\nQmaaa 244,56,16\nds_write_b128 v58, a[232:235] offset:34816\nQmaaa 244,60,44\n"
"Qdv 104,59,5120\nQmaaa 248,56,20\nQta 76,69,25088\nQmaaa 248,60,48\nds_write_b128 v58, a[236:239] offset:35840\nQmaaa 252,56,24\nQdv 136,59,0\ns_waitcnt lgkmcnt(9)\n"
"Qa 196,196,148\nQmaaa 252,60,52\nQta 78,69,26112\nQa 197,197,149\ns_waitcnt lgkmcnt(10)\nQmvaa 132,64,80\nds_write_b128 v58, a[240:243] offset:36864\nQmvaa 132,68,108\n"
"Qa 198,198,150\nQa 199,199,151\nQmvaa 164,64,84\ns_waitcnt lgkmcnt(8)\nQa 228,228,180\nQa 229,229,181\nQmvaa 164,68,112\nds_write_b128 v58, a[244:247] offset:37888\n"
"Qmvaa 196,64,88\nQta 56,68,16896\nQa 230,230,182\nQa 231,231,183\nQmvav 196,68,168\nQdv 148,59,4096\ns_waitcnt lgkmcnt(8)\nQa 140,140,104\n"
"Qmvaa 228,64,92\nds_write_b128 v58, a[248:251] offset:38912\nQmvav 228,68,200\nQta 58,68,17920\nQa 141,141,105\nQmvaa 108,64,96\nQa 142,142,106\nQa 143,143,107\n"
"Qmvav 108,68,232\nQdv 180,59,6144\ns_waitcnt lgkmcnt(8)\nQa 132,132,136\nQmvaa 140,64,100\nQta 60,68,25088\nQa 133,133,137\nQmvav 140,68,212\n"
"ds_write_b128 v58, a[252:255] offset:39936\nQmvaa 172,64,104\nQa 134,134,138\nQa 135,135,139\nQmvav 172,68,244\nQta 62,68,26112\ns_waitcnt lgkmcnt(6)\nQa 108,108,148\n"
"s_waitcnt lgkmcnt(6)\nQmvav 132,72,80\nQdv 104,59,1024\nv_add_u32 v75, 1, v75\nQmvav 132,76,88\ns_waitcnt vmcnt(10)\nds_write_b32 v73, v75\nQmvav 164,72,112\n"
"Qa 109,109,149\nQa 110,110,150\nQmvav 164,76,120\nQa 111,111,151\ns_waitcnt lgkmcnt(5)\nQa 172,172,180\nQmvav 196,72,144\nQta 64,69,768\n"
"Qa 173,173,181\nQmvav 196,76,152\nQa 174,174,182\nQa 175,175,183\nQmvav 228,72,176\nQmvav 228,76,184\nQta 66,69,1792\ns_waitcnt lgkmcnt(3)\n"
"Qa 164,164,104\nQmvav 108,72,208\nQa 165,165,105\nQa 166,166,106\nQmvav 108,76,216\nds_read_b32 v66, v74\nQa 167,167,107\nQmvav 140,72,240\n"
"Qta 68,69,8960\nQmvav 140,76,248\nQmvav 172,72,84\nQmvav 172,76,116\ns_waitcnt lgkmcnt(1)\nv_cmp_gt_u32 vcc, v75, v66\ns_waitcnt lgkmcnt(6)\nQmvaa 132,56,0\n"
"s_cbranch_vccnz .Lso1920r0\n.Lro1920r0:\ns_add_u32 s68, s64, 0\nQmvaa 132,60,28\nQta 70,69,9984\nQmvaa 164,56,4\nQmvaa 164,60,32\nQdv 136,59,35840\n"
"Qmvaa 196,56,8\nQta 72,69,17152\ns_nop 1\nQk 132,132,133\nQmvaa 196,60,36\nQk 133,134,135\nQmvaa 228,56,12\nQdv 148,59,36864\n"
"Qk 134,164,165\nQmvaa 228,60,40\nQta 74,69,18176\nQk 135,166,167\nQmvaa 108,56,16\nbuffer_store_dwordx4 v[132:135], v60, s[24:27], s68 offen offset:0 sc1\nQk 196,196,197\nQmvaa 108,60,44\n"
"Qdv 180,59,38912\nQk 197,198,199\nQmvaa 140,56,20\nQta 76,69,25344\nQk 198,228,229\nQmvaa 140,60,48\ns_waitcnt lgkmcnt(5)\nQa 96,96,136\n"
"Qa 97,97,137\nQmvaa 172,56,24\nQdv 104,59,33792\nQa 98,98,138\nQmvaa 172,60,52\nQta 78,69,26368\nQa 99,99,139\ns_waitcnt lgkmcnt(11)\n"
"Qmvaa 204,64,80\ns_waitcnt lgkmcnt(5)\nQa 128,128,148\nQa 129,129,149\nQmvaa 204,68,108\nQa 130,130,150\nQa 131,131,151\nQmvaa 236,64,84\n"
"Qk 199,230,231\nQk 108,108,109\nQmvaa 236,68,112\ns_waitcnt lgkmcnt(3)\nQa 192,192,180\nQa 193,193,181\nQmvaa 96,64,88\nQta 56,68,17152\n"
"Qa 194,194,182\nQmvav 96,68,168\nQdv 136,59,37888\nQa 195,195,183\nQmvaa 128,64,92\ns_waitcnt lgkmcnt(3)\nQa 204,204,104\nQa 205,205,105\n"
"Qmvav 128,68,200\nQta 58,68,18176\nQa 206,206,106\nQmvaa 160,64,96\nbuffer_store_dwordx4 v[196:199], v60, s[24:27], s68 offen offset:1024 sc1\nQa 207,207,107\nQmvav 160,68,232\nQdv 148,59,39936\n"
"Qk 109,110,111\nQmvaa 192,64,100\nQta 60,68,25344\nQk 110,140,141\nQmvav 192,68,212\nQk 111,142,143\nQk 172,172,173\nQmvaa 224,64,104\n"
"buffer_store_dwordx4 v[108:111], v60, s[24:27], s68 offen offset:2048 sc1\nv_add_u32 v75, 1, v75\nQmvav 224,68,244\ns_waitcnt vmcnt(3)\nds_write_b32 v73, v75\ns_waitcnt lgkmcnt(10)\nQmvav 204,72,80\nQta 62,68,26368\n"
"s_waitcnt lgkmcnt(5)\nQa 160,160,136\nQmvav 204,76,88\nQdv 180,59,34816\nQa 161,161,137\nQmvav 236,72,112\nQa 162,162,138\nQa 163,163,139\n"
"Qmvav 236,76,120\ns_waitcnt lgkmcnt(4)\nQa 224,224,148\nQa 225,225,149\nQmvav 96,72,144\nQta 64,68,32768\nQa 226,226,150\nQmvav 96,76,152\n"
"Qa 227,227,151\nQk 173,174,175\nQmvav 128,72,176\nQmvav 128,76,184\nds_read_b32 v66, v74\ns_waitcnt lgkmcnt(2)\nQa 236,236,180\nQmvav 160,72,208\n"
"Qta 66,68,33792\nQa 237,237,181\nQmvav 160,76,216\nQa 238,238,182\nQa 239,239,183\nQmvav 192,72,240\nQmvav 192,76,248\ns_waitcnt lgkmcnt(1)\n"
"v_cmp_gt_u32 vcc, v75, v66\nQmvav 224,72,84\ns_cbranch_vccnz .Lso1960r0\n.Lro1960r0:\nQmvav 224,76,116\nQta 68,68,0\ns_add_u32 m0, s96, 65536\ns_add_u32 s95, s67, 86016\n"
"s_waitcnt lgkmcnt(5)\nQmvaa 204,56,0\nbuffer_load_dwordx4 v62, s[52:55], s95 offen lds\nQmvaa 204,60,28\nQmvaa 236,56,4\nQta 70,68,1024\nQmvaa 236,60,32\ns_add_u32 s95, s67, 125952\n"
"Qmvaa 96,56,8\nbuffer_load_dwordx4 v62, s[52:55], s95 offen offset:1024 lds\nQmvaa 96,60,36\nQta 72,68,8192\nQk 174,204,205\nQmvaa 128,56,12\nQk 175,206,207\nQk 236,236,237\n"
"Qmvaa 128,60,40\nbuffer_store_dwordx4 v[172:175], v60, s[24:27], s68 offen offset:3072 sc1\nQk 237,238,239\nQmvaa 160,56,16\nQta 74,68,9216\ns_add_u32 m0, s96, 73728\ns_add_u32 s95, s72, 86016\nQmvaa 160,60,44\n"
"buffer_load_dwordx4 v62, s[52:55], s95 offen lds\nQmvaa 192,56,20\nQk 238,96,97\nQk 239,98,99\nQmvaa 192,60,48\nQta 76,68,40960\ns_add_u32 s68, s64, 4096\nQmvaa 224,56,24\n"
"buffer_store_dwordx4 v[236:239], v60, s[24:27], s68 offen offset:0 sc1\ns_add_u32 s95, s72, 125952\nQmvaa 224,60,52\nbuffer_load_dwordx4 v62, s[52:55], s95 offen offset:1024 lds\ns_waitcnt lgkmcnt(3)\nQmaaa 116,64,80\nQta 78,68,41984\nQk 128,128,129\n"
"Qmaaa 116,68,108\nQk 129,130,131\nQk 130,160,161\nQmaaa 120,64,84\ns_add_u32 m0, s96, 81920\ns_add_u32 s95, s73, 86016\nQmaaa 120,68,112\nbuffer_load_dwordx4 v62, s[52:55], s95 offen lds\n"
"Qmaaa 124,64,88\nQta 56,68,49152\nQk 131,162,163\nQmaav 124,68,168\nbuffer_store_dwordx4 v[128:131], v60, s[24:27], s68 offen offset:1024 sc1\nQk 192,192,193\nQmaaa 128,64,92\nQk 193,194,195\n"
"Qk 194,224,225\nQmaav 128,68,200\nQta 58,68,50176\ns_add_u32 s95, s73, 125952\nQmaaa 132,64,96\nbuffer_load_dwordx4 v62, s[52:55], s95 offen offset:1024 lds\nQmaav 132,68,232\nQk 195,226,227\n"
"Qmaaa 136,64,100\nQta 60,68,57344\nv_bfe_u32 v61, v60, 6, 2\nQmaav 136,68,212\nv_lshrrev_b32 v62, 8, v60\nv_xor_b32 v61, v61, v62\nQmaaa 140,64,104\nv_bfe_u32 v62, v60, 5, 1\n"
"v_lshl_or_b32 v61, v61, 1, v62\nQmaav 140,68,244\nQta 62,68,58368\nv_and_b32 v62, 16, v60\ns_waitcnt lgkmcnt(4)\nQmaav 116,72,80\nv_mov_b32 v63, 0x3f80\nv_lshlrev_b32 v63, v62, v63\n"
"Qmaav 116,76,88\nv_min_u32 v62, 1, v61\nv_add_u32 v62, -1, v62\nQmaav 120,72,112\nv_and_b32 v62, v62, v63\nv_xor_b32 v61, 1, v61\nQmaav 120,76,120\nv_min_u32 v61, 1, v61\n"
"v_add_u32 v61, -1, v61\nQmaav 124,72,144\nQta 64,68,33024\nv_and_b32 v61, v61, v63\nQmaav 124,76,152\nv_accvgpr_write_b32 a202, v62\nv_accvgpr_write_b32 a203, v61\nQmaav 128,72,176\n"
"v_accvgpr_write_b32 a200, 0\nv_accvgpr_write_b32 a201, 0\nQmaav 128,76,184\nQta 66,68,34048\nv_accvgpr_write_b32 a204, 0\nQmaav 132,72,208\nbuffer_store_dwordx4 v[192:195], v60, s[24:27], s68 offen offset:2048 sc1\nv_accvgpr_write_b32 a205, 0\n"
"Qmaav 132,76,216\nQmaav 136,72,240\nQta 68,68,256\nQmaav 136,76,248\nQmaav 140,72,84\nQmaav 140,76,116\nQta 70,68,1280\ns_waitcnt lgkmcnt(4)\n"
"Qmaaa 116,56,0\nQmaaa 116,60,28\nQmaaa 120,56,4\nQmaaa 120,60,32\nQmaaa 124,56,8\nQta 72,68,8448\nQmaaa 124,60,36\ns_nop 2\n"
"ds_write_b128 v58, a[116:119] offset:0\nQmaaa 128,56,12\nQmaaa 128,60,40\nQta 74,68,9472\nQmaaa 132,56,16\nds_write_b128 v58, a[120:123] offset:1024\nQmaaa 132,60,44\nQmaaa 136,56,20\n"
"Qta 76,68,41216\nQmaaa 136,60,48\nds_write_b128 v58, a[124:127] offset:2048\nQmaaa 140,56,24\nQmaaa 140,60,52\nQta 78,68,42240\ns_waitcnt lgkmcnt(7)\nQmaaa 144,64,80\n"
"ds_write_b128 v58, a[128:131] offset:3072\nQmaaa 144,68,108\nQmaaa 148,64,84\nQmaaa 148,68,112\nds_write_b128 v58, a[132:135] offset:4096\nQmaaa 152,64,88\nQta 56,68,49408\nQmaav 152,68,168\n"
"Qmaaa 156,64,92\nds_write_b128 v58, a[136:139] offset:5120\nQmaav 156,68,200\nQta 58,68,50432\nQmaaa 160,64,96\nQmaav 160,68,232\nds_write_b128 v58, a[140:143] offset:6144\nQmaaa 164,64,100\n"
"Qta 60,68,57600\nQmaav 164,68,212\nQmaaa 168,64,104\nQmaav 168,68,244\nQta 62,68,58624\ns_waitcnt lgkmcnt(8)\nQmaav 144,72,80\nv_add_u32 v75, 1, v75\n"
"Qmaav 144,76,88\ns_waitcnt vmcnt(8)\nds_write_b32 v73, v75\nQmaav 148,72,112\nQmaav 148,76,120\nQmaav 152,72,144\nQta 64,68,33280\nQmaav 152,76,152\n"
"Qmaav 156,72,176\nQmaav 156,76,184\nQta 66,68,34304\nQmaav 160,72,208\nQmaav 160,76,216\nds_read_b32 v66, v74\nQmaav 164,72,240\nQta 68,68,512\n"
"Qmaav 164,76,248\nQmaav 168,72,84\nQmaav 168,76,116\ns_waitcnt lgkmcnt(1)\nv_cmp_gt_u32 vcc, v75, v66\ns_waitcnt lgkmcnt(5)\nQmaaa 144,56,0\ns_cbranch_vccnz .Lso2046r0\n"
".Lro2046r0:\nQmaaa 144,60,28\nQta 70,68,1536\nQmaaa 148,56,4\nQmaaa 148,60,32\nQdv 104,59,2048\nQmaaa 152,56,8\nQta 72,68,8704\n"
"Qmaaa 152,60,36\ns_nop 0\nds_write_b128 v58, a[144:147] offset:33792\nQmaaa 156,56,12\nQdv 136,59,3072\nQmaaa 156,60,40\nQta 74,68,9728\nQmaaa 160,56,16\n"
"ds_write_b128 v58, a[148:151] offset:34816\nQmaaa 160,60,44\nQdv 148,59,5120\nQmaaa 164,56,20\n.Lpg20:\ns_cbranch_execz .Lpg24\nQta 76,68,41472\nQmaaa 164,60,48\n"
"ds_write_b128 v58, a[152:155] offset:35840\nQmaaa 168,56,24\nQdv 180,59,0\ns_waitcnt lgkmcnt(9)\nQa 8,8,104\nQmaaa 168,60,52\nQta 78,68,42496\nQa 9,9,105\n"
"s_waitcnt lgkmcnt(10)\nQmvaa 0,64,80\nds_write_b128 v58, a[156:159] offset:36864\nQmvaa 0,68,108\nQa 10,10,106\nQa 11,11,107\nQmvaa 4,64,84\ns_waitcnt lgkmcnt(8)\n"
"Qa 12,12,136\nQa 13,13,137\nQmvaa 4,68,112\nds_write_b128 v58, a[160:163] offset:37888\nQmvaa 8,64,88\nQta 56,68,49664\nQa 14,14,138\nQa 15,15,139\n"
"Qmvav 8,68,168\nQdv 104,59,4096\ns_waitcnt lgkmcnt(8)\nQa 20,20,148\nQmvaa 12,64,92\nds_write_b128 v58, a[164:167] offset:38912\nQmvav 12,68,200\nQta 58,68,50688\n"
"Qa 21,21,149\nQmvaa 16,64,96\nQa 22,22,150\nQa 23,23,151\nQmvav 16,68,232\nQdv 136,59,6144\ns_waitcnt lgkmcnt(8)\nQa 0,0,180\n"
"Qmvaa 20,64,100\nQta 60,68,57856\nQa 1,1,181\nQmvav 20,68,212\nds_write_b128 v58, a[168:171] offset:39936\nQmvaa 24,64,104\nQa 2,2,182\nQa 3,3,183\n"
"Qmvav 24,68,244\nQta 62,68,58880\ns_waitcnt lgkmcnt(6)\nQa 16,16,104\ns_waitcnt lgkmcnt(6)\nQmvav 0,72,80\nQdv 148,59,1024\nv_add_u32 v75, 1, v75\n"
"Qmvav 0,76,88\ns_waitcnt vmcnt(1)\nds_write_b32 v73, v75\nQmvav 4,72,112\nQa 17,17,105\nQa 18,18,106\nQmvav 4,76,120\nQa 19,19,107\n"
"s_waitcnt lgkmcnt(5)\nQa 24,24,136\nQmvav 8,72,144\nQta 64,69,0\nQa 25,25,137\nQmvav 8,76,152\nQa 26,26,138\nQa 27,27,139\n"
"Qmvav 12,72,176\ns_waitcnt vmcnt(0)\nbuffer_store_dword v67, v77, s[24:27], 0 offen sc1\nQmvav 12,76,184\nQta 66,69,512\ns_waitcnt lgkmcnt(3)\nQa 4,4,148\nQmvav 16,72,208\n"
"Qa 5,5,149\nQa 6,6,150\nQmvav 16,76,216\nds_read_b32 v66, v74\nQa 7,7,151\nQmvav 20,72,240\nQta 68,69,1024\nQmvav 20,76,248\n"
"Qmvav 24,72,84\nQmvav 24,76,116\ns_waitcnt lgkmcnt(1)\nv_cmp_gt_u32 vcc, v75, v66\ns_waitcnt lgkmcnt(6)\nQmvaa 0,56,0\ns_cbranch_vccnz .Lso2088r0\n.Lro2088r0:\n"
"Qmvaa 0,60,28\nQta 70,69,1536\nQmvaa 4,56,4\nQmvaa 4,60,32\nQdv 180,59,33792\nQmvaa 8,56,8\nQta 72,69,8192\nQmvaa 8,60,36\n"
"Qmvaa 12,56,12\nQdv 104,59,34816\nQmvaa 12,60,40\nQta 74,69,8704\nQmvaa 16,56,16\nQmvaa 16,60,44\nQdv 136,59,35840\nQmvaa 20,56,20\n"
"Qta 76,69,9216\nQmvaa 20,60,48\ns_waitcnt lgkmcnt(5)\nQa 28,28,180\nQa 29,29,181\nQmvaa 24,56,24\nQdv 148,59,36864\nQa 30,30,182\n"
"Qmvaa 24,60,52\nQta 78,69,9728\nQa 31,31,183\ns_waitcnt lgkmcnt(11)\nQmaaa 172,64,80\ns_waitcnt lgkmcnt(5)\nQa 32,32,104\nQa 33,33,105\n"
"Qmaaa 172,68,108\nQa 34,34,106\nQa 35,35,107\nQmaaa 176,64,84\nQmaaa 176,68,112\ns_waitcnt lgkmcnt(3)\nQa 36,36,136\nQa 37,37,137\n"
"Qmaaa 180,64,88\nQta 56,69,16384\nQa 38,38,138\nQmaav 180,68,168\nQdv 180,59,37888\nQa 39,39,139\nQmaaa 184,64,92\ns_waitcnt lgkmcnt(3)\n"
"Qa 40,40,148\nQa 41,41,149\nQmaav 184,68,200\nQta 58,69,16896\nQa 42,42,150\nQmaaa 188,64,96\nQdv 104,59,38912\nQa 43,43,151\n"
"Qmaav 188,68,232\nQmaaa 192,64,100\nQta 60,69,17408\nQmaav 192,68,212\nQdv 136,59,39936\nQmaaa 196,64,104\ns_waitcnt lgkmcnt(4)\nQa 44,44,180\n"
"Qa 45,45,181\nQmaav 196,68,244\nQta 62,69,17920\nQa 46,46,182\ns_waitcnt lgkmcnt(5)\nQmaav 172,72,80\nQa 47,47,183\nQmaav 172,76,88\n"
"Qmaav 176,72,112\ns_waitcnt lgkmcnt(3)\nQa 48,48,104\nQa 49,49,105\nQmaav 176,76,120\nQa 50,50,106\nQa 51,51,107\nQmaav 180,72,144\n"
"Qta 64,68,33536\nQmaav 180,76,152\ns_waitcnt lgkmcnt(2)\nQa 52,52,136\nQa 53,53,137\nQmaav 184,72,176\nQa 54,54,138\nQa 55,55,139\n"
"Qmaav 184,76,184\nQta 66,68,34560\nQmaav 188,72,208\nQmaav 188,76,216\nQmaav 192,72,240\nQta 68,68,768\nQmaav 192,76,248\nQmaav 196,72,84\n"
"Qmaav 196,76,116\nQta 70,68,1792\ns_waitcnt lgkmcnt(4)\nQmaaa 172,56,0\nQmaaa 172,60,28\nQmaaa 176,56,4\nQmaaa 176,60,32\nQmaaa 180,56,8\n"
"Qta 72,68,8960\nQmaaa 180,60,36\ns_nop 2\nds_write_b128 v58, a[172:175] offset:0\nQmaaa 184,56,12\nQmaaa 184,60,40\nQta 74,68,9984\nQmaaa 188,56,16\n"
"ds_write_b128 v58, a[176:179] offset:1024\nQmaaa 188,60,44\nQmaaa 192,56,20\nQta 76,68,41728\nQmaaa 192,60,48\nds_write_b128 v58, a[180:183] offset:2048\nQmaaa 196,56,24\nQmaaa 196,60,52\n"
"Qta 78,68,42752\ns_waitcnt lgkmcnt(7)\nQmvaa 28,64,80\nds_write_b128 v58, a[184:187] offset:3072\nQmvaa 28,68,108\nQmvaa 32,64,84\nQmvaa 32,68,112\nds_write_b128 v58, a[188:191] offset:4096\n"
"Qmvaa 36,64,88\nQta 56,68,49920\nQmvav 36,68,168\nQmvaa 40,64,92\nds_write_b128 v58, a[192:195] offset:5120\nQmvav 40,68,200\nQta 58,68,50944\nQmvaa 44,64,96\n"
"Qmvav 44,68,232\nbuffer_load_dword v78, v76, s[24:27], 0 offen sc1\nQmvaa 48,64,100\nQta 60,68,58112\nQmvav 48,68,212\nds_write_b128 v58, a[196:199] offset:6144\nQmvaa 52,64,104\nQmvav 52,68,244\n"
"Qta 62,68,59136\ns_waitcnt lgkmcnt(8)\nQmvav 28,72,80\nv_add_u32 v75, 1, v75\nQmvav 28,76,88\nds_write_b32 v73, v75\nQmvav 32,72,112\nQmvav 32,76,120\n"
"Qmvav 36,72,144\nQta 64,69,256\nQmvav 36,76,152\nQmvav 40,72,176\nQmvav 40,76,184\nQta 66,69,768\nQmvav 44,72,208\nQmvav 44,76,216\n"
"ds_read_b32 v66, v74\nQmvav 48,72,240\nQta 68,69,1280\nQmvav 48,76,248\nQmvav 52,72,84\nQmvav 52,76,116\ns_waitcnt lgkmcnt(1)\nv_cmp_gt_u32 vcc, v75, v66\n"
"s_waitcnt lgkmcnt(5)\nQmvaa 28,56,0\ns_cbranch_vccnz .Lso2172r0\n.Lro2172r0:\nQmvaa 28,60,28\nQta 70,69,1792\nQmvaa 32,56,4\nQmvaa 32,60,32\n"
"Qdv 148,59,2048\nQmvaa 36,56,8\nQta 72,69,8448\nQmvaa 36,60,36\nQmvaa 40,56,12\nQdv 180,59,3072\nQmvaa 40,60,40\nQta 74,69,8960\n"
"Qmvaa 44,56,16\nQmvaa 44,60,44\nQdv 104,59,5120\nQmvaa 48,56,20\nQta 76,69,9472\nQmvaa 48,60,48\ns_waitcnt lgkmcnt(5)\nQa 156,156,148\n"
"Qa 157,157,149\nQmvaa 52,56,24\nQdv 136,59,0\nQa 158,158,150\nQmvaa 52,60,52\nQta 78,69,9984\nQa 159,159,151\ns_waitcnt lgkmcnt(11)\n"
"Qmvaa 92,64,80\ns_waitcnt lgkmcnt(5)\nQa 188,188,180\nQa 189,189,181\nQmvaa 92,68,108\nQa 190,190,182\nQa 191,191,183\nQmvaa 124,64,84\n"
"s_waitcnt vmcnt(0)\nv_sub_u32 v66, v78, v65\nv_cmp_gt_u32 vcc, 1, v66\ns_and_b64 vcc, vcc, s[28:29]\ns_cbranch_scc1 .Lx0_w\n.Lx0_r:\nQmvaa 124,68,112\ns_add_u32 s69, s98, 0\n"
"buffer_load_dwordx4 a[208:211], v60, s[24:27], s69 offen offset:0 sc1\nQmvaa 156,64,88\nQta 56,69,16640\ns_waitcnt lgkmcnt(4)\nQa 252,252,104\nQmvav 156,68,168\nbuffer_load_dwordx4 a[212:215], v60, s[24:27], s69 offen offset:1024 sc1\nQmvaa 188,64,92\n"
"Qdv 148,59,4096\nQa 253,253,105\nQmvav 188,68,200\nbuffer_load_dwordx4 a[216:219], v60, s[24:27], s69 offen offset:2048 sc1\nQmvaa 220,64,96\nQta 58,69,17152\nQa 254,254,106\nQa 255,255,107\n"
"Qmvav 220,68,232\nbuffer_load_dwordx4 a[220:223], v60, s[24:27], s69 offen offset:3072 sc1\nQmvaa 252,64,100\nQdv 180,59,6144\ns_waitcnt lgkmcnt(5)\nQa 92,92,136\nQmvav 252,68,212\ns_add_u32 s69, s98, 4096\n"
"buffer_load_dwordx4 a[224:227], v60, s[24:27], s69 offen offset:0 sc1\nQmvaa 100,64,104\nQta 60,69,17664\nQa 93,93,137\nQa 94,94,138\nQa 95,95,139\nQmvav 100,68,244\nbuffer_load_dwordx4 a[228:231], v60, s[24:27], s69 offen offset:1024 sc1\n"
"s_waitcnt lgkmcnt(9)\nQmvav 92,72,80\nQdv 104,59,1024\ns_waitcnt lgkmcnt(4)\nQa 220,220,148\nQmvav 92,76,88\nbuffer_load_dwordx4 a[232:235], v60, s[24:27], s69 offen offset:2048 sc1\nQmvav 124,72,112\n"
"Qta 62,69,18176\nQa 221,221,149\nQmvav 124,76,120\nbuffer_load_dwordx4 a[236:239], v60, s[24:27], s69 offen offset:3072 sc1\nQmvav 156,72,144\nQa 222,222,150\nQa 223,223,151\nQmvav 156,76,152\n"
"s_add_u32 s69, s98, 8192\nbuffer_load_dwordx4 a[240:243], v60, s[24:27], s69 offen offset:0 sc1\nQmvav 188,72,176\ns_waitcnt lgkmcnt(3)\nQa 100,100,180\nQa 101,101,181\nQmvav 188,76,184\nbuffer_load_dwordx4 a[244:247], v60, s[24:27], s69 offen offset:1024 sc1\n"
"Qmvav 220,72,208\nv_add_u32 v75, 1, v75\nQa 102,102,182\nQmvav 220,76,216\nds_write_b32 v73, v75\nQa 103,103,183\nQmvav 252,72,240\nbuffer_load_dwordx4 a[248:251], v60, s[24:27], s69 offen offset:2048 sc1\n"
"Qmvav 252,76,248\ns_waitcnt lgkmcnt(2)\nQa 124,124,104\nQa 125,125,105\nQmvav 100,72,84\nQa 126,126,106\nQa 127,127,107\nQmvav 100,76,116\n"
"s_waitcnt lgkmcnt(1)\nQmvaa 92,56,0\nQmvaa 92,60,28\nQmvaa 124,56,4\nQmvaa 124,60,32\nds_read_b32 v66, v74\nQmvaa 156,56,8\nQmvaa 156,60,36\n"
"Qmvaa 188,56,12\nQmvaa 188,60,40\ns_waitcnt lgkmcnt(0)\nv_cmp_gt_u32 vcc, v75, v66\nQmvaa 220,56,16\ns_cbranch_vccnz .Lso2222r0\n.Lro2222r0:\nQmvaa 220,60,44\n"
"Qmvaa 252,56,20\nQmvaa 252,60,48\nQmvaa 100,56,24\nQmvaa 100,60,52\ns_waitcnt vmcnt(10)\nQmvaa 92,202,208\nQmvaa 124,200,208\ns_waitcnt vmcnt(9)\n"
"Qmvaa 156,202,212\nQmvaa 188,200,212\ns_nop 3\nQk 92,92,93\nQk 93,94,95\nds_write_b64 v56, v[92:93] offset:64\ns_waitcnt vmcnt(8)\nQmvaa 220,202,216\n"
"Qk 124,124,125\nQk 125,126,127\nds_write_b64 v56, v[124:125] offset:3392\nQmvaa 252,200,216\nQk 156,156,157\nQk 157,158,159\nds_write_b64 v56, v[156:157] offset:6720\ns_waitcnt vmcnt(7)\n"
"Qmvaa 100,202,220\nQk 188,188,189\nQk 189,190,191\nds_write_b64 v56, v[188:189] offset:10048\nQmvaa 0,200,220\nQk 220,220,221\nQk 221,222,223\nds_write_b64 v56, v[220:221] offset:13376\n"
"s_waitcnt vmcnt(6)\nQmvaa 4,202,224\nQk 252,252,253\nQk 253,254,255\nds_write_b64 v56, v[252:253] offset:16704\nQmvaa 8,200,224\nQk 100,100,101\nQk 101,102,103\n"
"ds_write_b64 v56, v[100:101] offset:20032\ns_waitcnt vmcnt(5)\nQmvaa 12,202,228\nQk 0,0,1\nQk 1,2,3\nds_write_b64 v56, v[0:1] offset:0\nQmvaa 16,200,228\nQk 4,4,5\n"
"Qk 5,6,7\nds_write_b64 v56, v[4:5] offset:3328\ns_waitcnt vmcnt(4)\nQmvaa 20,202,232\nQk 8,8,9\nQk 9,10,11\nds_write_b64 v56, v[8:9] offset:6656\nQmvaa 24,200,232\n"
"Qk 12,12,13\nQk 13,14,15\nds_write_b64 v56, v[12:13] offset:9984\ns_waitcnt vmcnt(3)\nQmvaa 28,202,236\nQk 16,16,17\nQk 17,18,19\nds_write_b64 v56, v[16:17] offset:13312\n"
"Qmvaa 32,200,236\nQk 20,20,21\nQk 21,22,23\nds_write_b64 v56, v[20:21] offset:16640\ns_waitcnt vmcnt(2)\nQmvaa 36,202,240\nQk 24,24,25\nQk 25,26,27\n"
"ds_write_b64 v56, v[24:25] offset:19968\nQmvaa 40,200,240\nQk 28,28,29\nQk 29,30,31\nds_write_b64 v56, v[28:29] offset:32\ns_waitcnt vmcnt(1)\nQmvaa 44,202,244\nQk 32,32,33\n"
"Qk 33,34,35\nds_write_b64 v56, v[32:33] offset:3360\nQmvaa 48,200,244\nQk 36,36,37\nQk 37,38,39\nds_write_b64 v56, v[36:37] offset:6688\ns_waitcnt vmcnt(0)\nQmvaa 52,202,248\n"
"Qk 40,40,41\nQk 41,42,43\nds_write_b64 v56, v[40:41] offset:10016\nQk 44,44,45\nQk 45,46,47\nds_write_b64 v56, v[44:45] offset:13344\nQk 48,48,49\nQk 49,50,51\n"
"ds_write_b64 v56, v[48:49] offset:16672\nQk 52,52,53\nQk 53,54,55\nds_write_b64 v56, v[52:53] offset:20000\ns_waitcnt lgkmcnt(0)\ns_barrier\nQda 0,57,0\nQda 4,57,3328\n"
"Qda 8,57,6656\nQda 12,57,9984\nQda 16,57,13312\nQda 20,57,16640\nQda 24,57,19968\nQda 28,71,64\nQda 32,71,6720\nQda 36,71,13376\n"
"Qda 40,71,20032\ns_mov_b32 s68, 0\ns_waitcnt lgkmcnt(10)\nbuffer_store_dwordx4 a[0:3], v64, s[40:43], s68 offen offset:0 sc1\ns_mov_b32 s68, 40960\ns_waitcnt lgkmcnt(9)\nbuffer_store_dwordx4 a[4:7], v64, s[40:43], s68 offen offset:0 sc1\ns_mov_b32 s68, 81920\n"
"s_waitcnt lgkmcnt(8)\nbuffer_store_dwordx4 a[8:11], v64, s[40:43], s68 offen offset:0 sc1\ns_mov_b32 s68, 122880\ns_waitcnt lgkmcnt(7)\nbuffer_store_dwordx4 a[12:15], v64, s[40:43], s68 offen offset:0 sc1\ns_mov_b32 s68, 163840\ns_waitcnt lgkmcnt(6)\nbuffer_store_dwordx4 a[16:19], v64, s[40:43], s68 offen offset:0 sc1\n"
"s_mov_b32 s68, 204800\ns_waitcnt lgkmcnt(5)\nbuffer_store_dwordx4 a[20:23], v64, s[40:43], s68 offen offset:0 sc1\ns_mov_b32 s68, 245760\ns_waitcnt lgkmcnt(4)\nbuffer_store_dwordx4 a[24:27], v64, s[40:43], s68 offen offset:0 sc1\ns_mov_b32 s68, 0\ns_waitcnt lgkmcnt(3)\n"
"buffer_store_dwordx4 a[28:31], v72, s[40:43], s68 offen offset:0 sc1\ns_mov_b32 s68, 81920\ns_waitcnt lgkmcnt(2)\nbuffer_store_dwordx4 a[32:35], v72, s[40:43], s68 offen offset:0 sc1\ns_mov_b32 s68, 163840\ns_waitcnt lgkmcnt(1)\nbuffer_store_dwordx4 a[36:39], v72, s[40:43], s68 offen offset:0 sc1\ns_mov_b32 s68, 245760\n"
"s_cmp_eq_u32 s93, 1\ns_cselect_b32 s68, 0x7ffff000, s68\ns_waitcnt lgkmcnt(0)\nbuffer_store_dwordx4 a[40:43], v72, s[40:43], s68 offen offset:0 sc1\ns_branch .Lfin\n.Lrole1:\n.Lpg21:\ns_cbranch_execz .Lpg25\n"
"Qmvaa 96,64,88\nQta 56,69,49408\ns_lshl_b32 s46, %19, 4\nQmvav 96,68,168\nbuffer_load_dwordx4 a[84:87], v60, s[48:51], s31 offen offset:1024 sc1\nQmvaa 128,64,92\nds_read_b32 v66, v74\ns_add_u32 s46, s46, 128\n"
"Qmvav 128,68,200\nQta 58,69,50432\nv_mov_b32 v77, 0x7ffff000\nQmvaa 160,64,96\nQmvav 160,68,232\nbuffer_load_dwordx4 a[88:91], v60, s[48:51], s31 offen offset:2048 sc1\nQmvaa 192,64,100\ns_waitcnt lgkmcnt(1)\n"
"v_cmp_gt_u32 vcc, v75, v66\nQmvav 192,68,212\ns_cbranch_vccnz .Lso1525r1\n.Lro1525r1:\nQmvaa 224,64,104\nQta 60,69,57600\nQmvav 224,68,244\nbuffer_load_dwordx4 a[92:95], v60, s[48:51], s31 offen offset:3072 sc1\n"
"s_waitcnt vmcnt(31)\ns_waitcnt lgkmcnt(4)\nQmaav 116,72,80\nQmaav 116,76,88\nQta 62,69,58624\nQmaav 120,72,112\ns_add_u32 m0, s96, 65536\ns_add_u32 s95, s67, 86016\n"
"Qmaav 120,76,120\nbuffer_load_dwordx4 v62, s[52:55], s95 offen lds\nQmaav 124,72,144\nQta 64,69,49664\nQmaav 124,76,152\nQmaav 128,72,176\nQmaav 128,76,184\nQta 66,69,50688\n"
"s_add_u32 s95, s67, 125952\nQmaav 132,72,208\nbuffer_load_dwordx4 v62, s[52:55], s95 offen offset:1024 lds\nQmaav 132,76,216\nQmaav 136,72,240\nQta 68,69,57856\nQmaav 136,76,248\ns_add_u32 m0, s96, 73728\n"
"s_add_u32 s95, s72, 86016\nQmaav 140,72,84\nbuffer_load_dwordx4 v62, s[52:55], s95 offen lds\nQmaav 140,76,116\n.Lpg22:\ns_cbranch_execz .Ltr8\nQta 70,69,58880\ns_waitcnt lgkmcnt(4)\n"
"Qmaav 144,56,80\nQmaav 144,60,88\ns_add_u32 s95, s72, 125952\nQmaav 148,56,112\nbuffer_load_dwordx4 v62, s[52:55], s95 offen offset:1024 lds\nQmaav 148,60,120\nQmaav 152,56,144\nQta 72,69,49920\n"
"Qmaav 152,60,152\ns_add_u32 m0, s96, 81920\ns_add_u32 s95, s73, 86016\nQmaav 156,56,176\nbuffer_load_dwordx4 v62, s[52:55], s95 offen lds\nQmaav 156,60,184\nQta 74,69,50944\nQmaav 160,56,208\n"
"Qmaav 160,60,216\nQmaav 164,56,240\nQta 76,69,58112\ns_add_u32 s95, s73, 125952\nQmaav 164,60,248\nbuffer_load_dwordx4 v62, s[52:55], s95 offen offset:1024 lds\nQmaav 168,56,84\nQmaav 168,60,116\n"
"Qta 78,69,59136\ns_waitcnt lgkmcnt(4)\nQmvav 0,64,80\ns_add_u32 m0, s96, 90112\ns_add_u32 s95, s67, 86016\nQmvav 0,68,88\nbuffer_load_dwordx4 v61, s[52:55], s95 offen lds\nQmvav 4,64,112\n"
"Qmvav 4,68,120\nQmvav 8,64,144\nQta 56,68,16384\ns_add_u32 s95, s67, 105472\nQmvav 8,68,152\nbuffer_load_dwordx4 v61, s[52:55], s95 offen offset:1024 lds\nQmvav 12,64,176\nQmvav 12,68,184\n"
"Qta 58,68,16896\nQmvav 16,64,208\ns_add_u32 s31, s74, 32768\nQmvav 16,68,216\nbuffer_load_dwordx4 a[96:99], v60, s[48:51], s31 offen offset:0 sc1\nQmvav 20,64,240\nQta 60,68,17408\nQmvav 20,68,248\n"
"Qmvav 24,64,84\nQmvav 24,68,116\nQta 62,68,17920\ns_waitcnt lgkmcnt(4)\nQmvav 28,72,80\nbuffer_load_dwordx4 a[100:103], v60, s[48:51], s31 offen offset:1024 sc1\nQmvav 28,76,88\nQmvav 32,72,112\n"
"Qmvav 32,76,120\nQmvav 36,72,144\nQta 64,68,16640\nQmvav 36,76,152\nbuffer_load_dwordx4 a[104:107], v60, s[48:51], s31 offen offset:2048 sc1\nQmvav 40,72,176\nQmvav 40,76,184\nQta 66,68,17152\n"
"Qmvav 44,72,208\ns_add_u32 s31, s74, 43008\nv_cmp_eq_u32 s[44:45], s46, v60\ns_nop 1\nv_cndmask_b32 v77, v77, v76, s[44:45]\nv_add_u32 v67, 1, v65\ns_xor_b32 s46, %19, 12\ns_lshl_b32 s28, 1, s46\n"
"Qmvav 44,76,216\nbuffer_load_dwordx4 a[108:111], v60, s[48:51], s31 offen offset:0 sc1\nQmvav 48,72,240\nQta 68,68,17664\nQmvav 48,76,248\nv_add_u32 v75, 1, v75\nQmvav 52,72,84\ns_waitcnt vmcnt(37)\n"
"ds_write_b32 v73, v75\nQmvav 52,76,116\nQta 70,68,18176\ns_waitcnt lgkmcnt(5)\nQmaav 172,56,80\nbuffer_load_dwordx4 a[112:115], v60, s[48:51], s31 offen offset:1024 sc1\nQmaav 172,60,88\nQmaav 176,56,112\n"
"Qmaav 176,60,120\nQmaav 180,56,144\nQta 72,68,24576\nQmaav 180,60,152\nbuffer_load_dwordx4 v[168:171], v60, s[48:51], s31 offen offset:2048 sc1\nQmaav 184,56,176\nds_read_b32 v66, v74\nQmaav 184,60,184\n"
"Qta 74,68,25600\nQmaav 188,56,208\nQmaav 188,60,216\nbuffer_load_dwordx4 v[200:203], v60, s[48:51], s31 offen offset:3072 sc1\nQmaav 192,56,240\ns_waitcnt lgkmcnt(1)\nv_cmp_gt_u32 vcc, v75, v66\nQmaav 192,60,248\n"
"s_cbranch_vccnz .Lso1595r1\n.Lro1595r1:\nQmaav 196,56,84\nQta 76,68,32768\ns_add_u32 s31, s74, 47104\nQmaav 196,60,116\nbuffer_load_dwordx4 v[232:235], v60, s[48:51], s31 offen offset:0 sc1\ns_waitcnt lgkmcnt(4)\n"
"Qmvav 92,64,80\nQmvav 92,68,88\nQta 78,68,33792\nQmvav 124,64,112\nQmvav 124,68,120\nbuffer_load_dwordx4 v[212:215], v60, s[48:51], s31 offen offset:1024 sc1\nQmvav 156,64,144\nQta 56,68,24832\n"
"Qmvav 156,68,152\nQmvav 188,64,176\nQmvav 188,68,184\nQta 58,68,25856\nQmvav 220,64,208\nbuffer_load_dwordx4 v[244:247], v60, s[48:51], s31 offen offset:2048 sc1\nQmvav 220,68,216\nQmvav 252,64,240\n"
"Qta 60,68,33024\nQmvav 252,68,248\ns_add_u32 m0, s91, 0\ns_add_u32 s31, s77, 28672\nQmvav 100,64,84\nbuffer_load_dwordx4 v60, s[48:51], s31 offen sc1 lds\nQmvav 100,68,116\nQta 62,68,34048\n"
"s_waitcnt lgkmcnt(4)\nQmaav 200,72,80\nQmaav 200,76,88\nQmaav 204,72,112\nbuffer_load_dwordx4 v60, s[48:51], s31 offen offset:1024 sc1 lds\nQmaav 204,76,120\nQmaav 208,72,144\nQta 64,68,25088\n"
"Qmaav 208,76,152\nQmaav 212,72,176\nbuffer_load_dwordx4 v60, s[48:51], s31 offen offset:2048 sc1 lds\nQmaav 212,76,184\nQta 66,68,26112\nQmaav 216,72,208\nQmaav 216,76,216\nQmaav 220,72,240\n"
"Qta 68,68,33280\nQmaav 220,76,248\nbuffer_load_dwordx4 v60, s[48:51], s31 offen offset:3072 sc1 lds\nQmaav 224,72,84\nQmaav 224,76,116\nQta 70,68,34304\ns_waitcnt lgkmcnt(4)\nQmaav 228,56,80\n"
"s_add_u32 m0, s91, 4096\ns_add_u32 s31, s77, 32768\nQmaav 228,60,88\nbuffer_load_dwordx4 v60, s[48:51], s31 offen sc1 lds\nQmaav 232,56,112\nQmaav 232,60,120\nQmaav 236,56,144\nQta 72,68,25344\n"
"Qmaav 236,60,152\nbuffer_load_dwordx4 v60, s[48:51], s31 offen offset:1024 sc1 lds\nQmaav 240,56,176\nQmaav 240,60,184\nQta 74,68,26368\nQmaav 244,56,208\nQmaav 244,60,216\nbuffer_load_dwordx4 v60, s[48:51], s31 offen offset:2048 sc1 lds\n"
"Qmaav 248,56,240\nQta 76,68,33536\nQmaav 248,60,248\nQmaav 252,56,84\nQmaav 252,60,116\nQta 78,68,34560\ns_add_u32 m0, s91, 33792\ns_add_u32 s31, s77, 43008\n"
"s_waitcnt lgkmcnt(4)\nQmvav 132,64,80\nbuffer_load_dwordx4 v60, s[48:51], s31 offen sc1 lds\nQmvav 132,68,88\nQmvav 164,64,112\nQmvav 164,68,120\nQmvav 196,64,144\nQta 56,68,0\n"
"Qmvav 196,68,152\nbuffer_load_dwordx4 v60, s[48:51], s31 offen offset:1024 sc1 lds\nQmvav 228,64,176\nQmvav 228,68,184\nQta 58,68,1024\nQmvav 108,64,208\nQmvav 108,68,216\nbuffer_load_dwordx4 v60, s[48:51], s31 offen offset:2048 sc1 lds\n"
"Qmvav 140,64,240\nQta 60,68,8192\nQmvav 140,68,248\nv_add_u32 v75, 1, v75\nQmvav 172,64,84\ns_waitcnt vmcnt(49)\nds_write_b32 v73, v75\nQmvav 172,68,116\n"
"Qta 62,68,9216\ns_waitcnt lgkmcnt(5)\nQmvav 204,72,80\nbuffer_load_dwordx4 v60, s[48:51], s31 offen offset:3072 sc1 lds\nQmvav 204,76,88\nQmvav 236,72,112\nQmvav 236,76,120\nQmvav 96,72,144\n"
"Qta 64,68,256\ns_add_u32 m0, s91, 37888\ns_add_u32 s31, s77, 47104\nQmvav 96,76,152\nbuffer_load_dwordx4 v60, s[48:51], s31 offen sc1 lds\nQmvav 128,72,176\nds_read_b32 v66, v74\nQmvav 128,76,184\n"
"Qta 66,68,1280\nQmvav 160,72,208\nQmvav 160,76,216\nbuffer_load_dwordx4 v60, s[48:51], s31 offen offset:1024 sc1 lds\nQmvav 192,72,240\ns_waitcnt lgkmcnt(1)\nv_cmp_gt_u32 vcc, v75, v66\nQmvav 192,76,248\n"
"s_cbranch_vccnz .Lso1665r1\n.Lro1665r1:\nQmvav 224,72,84\nQta 68,68,8448\nQmvav 224,76,116\nbuffer_load_dwordx4 v60, s[48:51], s31 offen offset:2048 sc1 lds\ns_waitcnt vmcnt(38)\ns_waitcnt lgkmcnt(4)\n"
"Qmaaa 116,56,0\nQmaaa 116,60,28\nQta 70,68,9472\nQmaaa 120,56,4\ns_add_u32 m0, s96, 16384\ns_add_u32 s95, s67, 126976\nQmaaa 120,60,32\nbuffer_load_dwordx4 v61, s[52:55], s95 offen lds\n"
"Qmaaa 124,56,8\nQta 72,68,512\nQmaaa 124,60,36\nQmaaa 128,56,12\nQmaaa 128,60,40\nQta 74,68,1536\ns_add_u32 s95, s67, 146432\nQmaaa 132,56,16\n"
"buffer_load_dwordx4 v61, s[52:55], s95 offen offset:1024 lds\nQmaaa 132,60,44\nQmaaa 136,56,20\nQta 76,68,8704\nQmaaa 136,60,48\ns_add_u32 s31, s75, 28672\nQmaaa 140,56,24\nbuffer_load_dwordx4 v[80:83], v60, s[48:51], s31 offen offset:0 sc1\n"
"Qmaaa 140,60,52\nQta 78,68,9728\ns_waitcnt lgkmcnt(4)\nQmaaa 144,64,0\nQmaaa 144,68,28\nQmaaa 148,64,4\nbuffer_load_dwordx4 v[112:115], v60, s[48:51], s31 offen offset:1024 sc1\nQmaaa 148,68,32\n"
"Qmaaa 152,64,8\nQta 56,68,768\nQmaaa 152,68,36\nQmaaa 156,64,12\nbuffer_load_dwordx4 v[144:147], v60, s[48:51], s31 offen offset:2048 sc1\nQmaaa 156,68,40\nQta 58,68,1792\nQmaaa 160,64,16\n"
"Qmaaa 160,68,44\nQmaaa 164,64,20\nQta 60,68,8960\nQmaaa 164,68,48\nbuffer_load_dwordx4 v[176:179], v60, s[48:51], s31 offen offset:3072 sc1\nQmaaa 168,64,24\nQmaaa 168,68,52\nQta 62,68,9984\n"
"s_waitcnt lgkmcnt(4)\nQmvaa 0,72,0\ns_add_u32 s31, s75, 32768\nQmvaa 0,76,28\nbuffer_load_dwordx4 v[208:211], v60, s[48:51], s31 offen offset:0 sc1\nQmvaa 4,72,4\nQmvaa 4,76,32\nQmvaa 8,72,8\n"
"Qta 64,68,40960\nQmvaa 8,76,36\nbuffer_load_dwordx4 v[240:243], v60, s[48:51], s31 offen offset:1024 sc1\nQmvaa 12,72,12\nQmvaa 12,76,40\nQta 66,68,41472\nQmvaa 16,72,16\nQmvaa 16,76,44\n"
"buffer_load_dwordx4 v[84:87], v60, s[48:51], s31 offen offset:2048 sc1\nQmvaa 20,72,20\nQta 68,68,41984\nQmvaa 20,76,48\nQmvaa 24,72,24\nQmvaa 24,76,52\nQta 70,68,42496\ns_add_u32 s31, s75, 43008\n"
"s_waitcnt lgkmcnt(4)\nQmvaa 28,56,0\nbuffer_load_dwordx4 v[88:91], v60, s[48:51], s31 offen offset:0 sc1\nQmvaa 28,60,28\nQmvaa 32,56,4\nQmvaa 32,60,32\nQmvaa 36,56,8\nQta 72,68,41216\n"
"Qmvaa 36,60,36\nbuffer_load_dwordx4 v[120:123], v60, s[48:51], s31 offen offset:1024 sc1\nQmvaa 40,56,12\nQmvaa 40,60,40\nQta 74,68,41728\nQmvaa 44,56,16\nQmvaa 44,60,44\nbuffer_load_dwordx4 v[152:155], v60, s[48:51], s31 offen offset:2048 sc1\n"
"Qmvaa 48,56,20\nQta 76,68,42240\nQmvaa 48,60,48\nv_add_u32 v75, 1, v75\nQmvaa 52,56,24\ns_waitcnt vmcnt(40)\nds_write_b32 v73, v75\nQmvaa 52,60,52\n"
"Qta 78,68,42752\ns_waitcnt lgkmcnt(5)\nQmaaa 172,64,0\nbuffer_load_dwordx4 v[184:187], v60, s[48:51], s31 offen offset:3072 sc1\nQmaaa 172,68,28\nQmaaa 176,64,4\nQmaaa 176,68,32\nQmaaa 180,64,8\n"
"Qta 56,68,49152\ns_add_u32 s31, s75, 47104\nQmaaa 180,68,36\nbuffer_load_dwordx4 v[216:219], v60, s[48:51], s31 offen offset:0 sc1\nQmaaa 184,64,12\nds_read_b32 v66, v74\nQmaaa 184,68,40\nQta 58,68,50176\n"
"Qmaaa 188,64,16\nQmaaa 188,68,44\nbuffer_load_dwordx4 v[248:251], v60, s[48:51], s31 offen offset:1024 sc1\nQmaaa 192,64,20\ns_waitcnt lgkmcnt(1)\nv_cmp_gt_u32 vcc, v75, v66\nQmaaa 192,68,48\ns_cbranch_vccnz .Lso1735r1\n"
".Lro1735r1:\nQmaaa 196,64,24\nQta 60,68,57344\nQmaaa 196,68,52\nbuffer_load_dwordx4 v[116:119], v60, s[48:51], s31 offen offset:2048 sc1\ns_waitcnt lgkmcnt(4)\nQmvaa 92,72,0\nQmvaa 92,76,28\n"
"Qta 62,68,58368\nQmvaa 124,72,4\ns_add_u32 m0, s96, 24576\ns_add_u32 s95, s72, 86016\nQmvaa 124,76,32\nbuffer_load_dwordx4 v61, s[52:55], s95 offen lds\nQmvaa 156,72,8\nQta 64,68,49408\n"
"Qmvaa 156,76,36\nQmvaa 188,72,12\nQmvaa 188,76,40\nQta 66,68,50432\ns_add_u32 s95, s72, 105472\nQmvaa 220,72,16\nbuffer_load_dwordx4 v61, s[52:55], s95 offen offset:1024 lds\nQmvaa 220,76,44\n"
"Qmvaa 252,72,20\nQta 68,68,57600\nQmvaa 252,76,48\ns_add_u32 m0, s96, 32768\ns_add_u32 s95, s72, 126976\nQmvaa 100,72,24\nbuffer_load_dwordx4 v61, s[52:55], s95 offen lds\nQmvaa 100,76,52\n"
"Qta 70,68,58624\ns_waitcnt lgkmcnt(4)\nQmaaa 200,56,0\nQmaaa 200,60,28\ns_add_u32 s95, s72, 146432\nQmaaa 204,56,4\nbuffer_load_dwordx4 v61, s[52:55], s95 offen offset:1024 lds\nQmaaa 204,60,32\n"
"Qmaaa 208,56,8\nQta 72,68,49664\nQmaaa 208,60,36\ns_add_u32 m0, s96, 0\ns_add_u32 s95, s73, 86016\nQmaaa 212,56,12\nbuffer_load_dwordx4 v61, s[52:55], s95 offen lds\nQmaaa 212,60,40\n"
"Qta 74,68,50688\nQmaaa 216,56,16\nQmaaa 216,60,44\nQmaaa 220,56,20\nQta 76,68,57856\ns_add_u32 s95, s73, 105472\nQmaaa 220,60,48\nbuffer_load_dwordx4 v61, s[52:55], s95 offen offset:1024 lds\n"
"Qmaaa 224,56,24\nQmaaa 224,60,52\nQta 78,68,58880\ns_waitcnt lgkmcnt(4)\nQmaaa 228,64,0\ns_add_u32 m0, s96, 8192\ns_add_u32 s95, s73, 126976\nQmaaa 228,68,28\n"
"buffer_load_dwordx4 v61, s[52:55], s95 offen lds\nQmaaa 232,64,4\nQmaaa 232,68,32\nQmaaa 236,64,8\nQta 56,68,49920\ns_add_u32 s95, s73, 146432\nQmaaa 236,68,36\nbuffer_load_dwordx4 v61, s[52:55], s95 offen offset:1024 lds\n"
"Qmaaa 240,64,12\nQmaaa 240,68,40\nQta 58,68,50944\nQmaaa 244,64,16\nQmaaa 244,68,44\nQmaaa 248,64,20\nQta 60,68,58112\nQmaaa 248,68,48\n"
"Qmaaa 252,64,24\nQmaaa 252,68,52\nQta 62,68,59136\ns_waitcnt lgkmcnt(4)\nQmvaa 132,72,0\nQmvaa 132,76,28\nQmvaa 164,72,4\nQmvaa 164,76,32\n"
"Qmvaa 196,72,8\nQta 64,69,0\nQmvaa 196,76,36\nQmvaa 228,72,12\nQmvaa 228,76,40\nQta 66,69,512\nQmvaa 108,72,16\nQmvaa 108,76,44\n"
"Qmvaa 140,72,20\nQta 68,69,1024\nQmvaa 140,76,48\nv_add_u32 v75, 1, v75\nQmvaa 172,72,24\ns_waitcnt vmcnt(22)\nds_write_b32 v73, v75\nQmvaa 172,76,52\n"
"Qta 70,69,1536\ns_waitcnt lgkmcnt(5)\nQmvaa 204,56,0\ns_waitcnt vmcnt(24)\nQda 0,58,0\nQmvaa 204,60,28\nQmvaa 236,56,4\nQda 28,58,33792\n"
"Qmvaa 236,60,32\nQmvaa 96,56,8\nQta 72,69,8192\nQmvaa 96,60,36\nQda 4,58,1024\nQmvaa 128,56,12\nds_read_b32 v66, v74\nQmvaa 128,60,40\n"
"Qta 74,69,8704\nQmvaa 160,56,16\nQda 32,58,34816\nQmvaa 160,60,44\nQmvaa 192,56,20\ns_waitcnt lgkmcnt(2)\nv_cmp_gt_u32 vcc, v75, v66\nQmvaa 192,60,48\n"
"s_cbranch_vccnz .Lso1805r1\n.Lro1805r1:\nQmvaa 224,56,24\nQta 76,69,9216\ns_add_u32 m0, s96, 40960\ns_add_u32 s95, s67, 86016\nQmvaa 224,60,52\nbuffer_load_dwordx4 v63, s[52:55], s95 offen lds\n"
"s_waitcnt vmcnt(39)\ns_waitcnt lgkmcnt(8)\nQmaaa 172,64,80\nQda 8,58,2048\nQmaaa 172,68,108\nQta 78,69,9728\nQmaaa 176,64,84\nQda 36,58,35840\n"
"s_add_u32 s95, s67, 105472\nQmaaa 176,68,112\nbuffer_load_dwordx4 v63, s[52:55], s95 offen offset:1024 lds\nQmaaa 180,64,88\nQta 56,69,16384\nQmaav 180,68,168\nQda 12,58,3072\nQmaaa 184,64,92\n"
"Qmaav 184,68,200\nQta 58,69,16896\ns_add_u32 m0, s96, 49152\ns_add_u32 s95, s67, 126976\nQmaaa 188,64,96\nbuffer_load_dwordx4 v63, s[52:55], s95 offen lds\nQmaav 188,68,232\nQda 40,58,36864\n"
"Qmaaa 192,64,100\nQta 60,69,17408\nQmaav 192,68,212\nQda 16,58,4096\ns_add_u32 s95, s67, 146432\nQmaaa 196,64,104\nbuffer_load_dwordx4 v63, s[52:55], s95 offen offset:1024 lds\nQmaav 196,68,244\n"
"Qta 62,69,17920\ns_waitcnt vmcnt(12)\ns_waitcnt lgkmcnt(8)\nQmaav 172,72,80\nQda 44,58,37888\nQmaav 172,76,88\ns_add_u32 m0, s96, 57344\ns_add_u32 s95, s72, 86016\n"
"Qmaav 176,72,112\nbuffer_load_dwordx4 v63, s[52:55], s95 offen lds\nQmaav 176,76,120\n.Lpg23:\ns_cbranch_execz .Ltr8\nQda 20,58,5120\nQmaav 180,72,144\nQta 64,69,24576\n"
"v_add_u32 v75, 1, v75\nQmaav 180,76,152\ns_waitcnt vmcnt(5)\nds_write_b32 v73, v75\ns_add_u32 s95, s72, 105472\nQmaav 184,72,176\nbuffer_load_dwordx4 v63, s[52:55], s95 offen offset:1024 lds\nQmaav 184,76,184\n"
"Qta 66,69,25600\nQmaav 188,72,208\nQda 48,58,38912\nQmaav 188,76,216\nQmaav 192,72,240\nQta 68,68,16384\nQmaav 192,76,248\nQda 24,58,6144\n"
"Qmaav 196,72,84\nQmaav 196,76,116\nds_read_b32 v66, v74\ns_waitcnt lgkmcnt(9)\nQmaaa 172,56,0\nQta 70,68,17408\nQmaaa 172,60,28\nQda 52,58,39936\n"
"Qmaaa 176,56,4\nQmaaa 176,60,32\ns_waitcnt lgkmcnt(2)\nv_cmp_gt_u32 vcc, v75, v66\ns_waitcnt lgkmcnt(11)\nQmaaa 180,56,8\ns_cbranch_vccnz .Lso1840r1\n.Lro1840r1:\n"
"Qmaaa 180,60,36\nQta 72,68,24576\nQmaaa 184,56,12\nds_write_b128 v58, a[172:175] offset:0\nQmaaa 184,60,40\ns_waitcnt lgkmcnt(2)\nQmaaa 188,56,16\nQta 74,68,25600\n"
"Qmaaa 188,60,44\nds_write_b128 v58, a[176:179] offset:1024\nQmaaa 192,56,20\nQmaaa 192,60,48\nQta 76,68,32768\nQmaaa 196,56,24\nds_write_b128 v58, a[180:183] offset:2048\nQmaaa 196,60,52\n"
"s_waitcnt lgkmcnt(7)\nQmaaa 116,64,80\nQta 78,68,33792\nQmaaa 116,68,108\nds_write_b128 v58, a[184:187] offset:3072\nQmaaa 120,64,84\nQmaaa 120,68,112\nQmaaa 124,64,88\n"
"Qta 56,68,0\nQmaav 124,68,168\nds_write_b128 v58, a[188:191] offset:4096\nQmaaa 128,64,92\nQmaav 128,68,200\nQta 58,68,1024\nQmaaa 132,64,96\nds_write_b128 v58, a[192:195] offset:5120\n"
"Qmaav 132,68,232\nQmaaa 136,64,100\nQta 60,68,8192\nQmaav 136,68,212\nds_write_b128 v58, a[196:199] offset:6144\nQmaaa 140,64,104\nQmaav 140,68,244\nQta 62,68,9216\n"
"s_waitcnt lgkmcnt(8)\nQmaav 116,72,80\nv_add_u32 v75, 1, v75\nQmaav 116,76,88\nds_write_b32 v73, v75\nQmaav 120,72,112\nQmaav 120,76,120\nQmaav 124,72,144\n"
"Qta 64,69,256\nQmaav 124,76,152\nQmaav 128,72,176\nQmaav 128,76,184\nQta 66,69,768\nQmaav 132,72,208\nQmaav 132,76,216\nds_read_b32 v66, v74\n"
"Qmaav 136,72,240\nQta 68,69,1280\nQmaav 136,76,248\nQmaav 140,72,84\nQmaav 140,76,116\ns_waitcnt lgkmcnt(1)\nv_cmp_gt_u32 vcc, v75, v66\ns_waitcnt lgkmcnt(5)\n"
"Qmaaa 116,56,0\ns_cbranch_vccnz .Lso1878r1\n.Lro1878r1:\nQmaaa 116,60,28\nQta 70,69,1792\nQmaaa 120,56,4\nQmaaa 120,60,32\nQdv 148,59,2048\n"
"Qmaaa 124,56,8\nQta 72,69,8448\nQmaaa 124,60,36\ns_nop 0\nds_write_b128 v58, a[116:119] offset:33792\nQmaaa 128,56,12\nQdv 180,59,3072\nQmaaa 128,60,40\n"
"Qta 74,69,8960\nQmaaa 132,56,16\nds_write_b128 v58, a[120:123] offset:34816\nQmaaa 132,60,44\nQdv 104,59,5120\nQmaaa 136,56,20\nQta 76,69,9472\nQmaaa 136,60,48\n"
"ds_write_b128 v58, a[124:127] offset:35840\nQmaaa 140,56,24\nQdv 136,59,0\ns_waitcnt lgkmcnt(9)\nQa 156,156,148\nQmaaa 140,60,52\nQta 78,69,9984\nQa 157,157,149\n"
"s_waitcnt lgkmcnt(10)\nQmvaa 92,64,80\nds_write_b128 v58, a[128:131] offset:36864\nQmvaa 92,68,108\nQa 158,158,150\nQa 159,159,151\nQmvaa 124,64,84\ns_waitcnt lgkmcnt(8)\n"
"Qa 188,188,180\nQa 189,189,181\nQmvaa 124,68,112\nds_write_b128 v58, a[132:135] offset:37888\nQmvaa 156,64,88\nQta 56,69,16640\nQa 190,190,182\nQa 191,191,183\n"
"Qmvav 156,68,168\nQdv 148,59,4096\ns_waitcnt lgkmcnt(8)\nQa 252,252,104\nQmvaa 188,64,92\nds_write_b128 v58, a[136:139] offset:38912\nQmvav 188,68,200\nQta 58,69,17152\n"
"Qa 253,253,105\nQmvaa 220,64,96\nQa 254,254,106\nQa 255,255,107\nQmvav 220,68,232\nQdv 180,59,6144\ns_waitcnt lgkmcnt(8)\nQa 92,92,136\n"
"Qmvaa 252,64,100\nQta 60,69,17664\nQa 93,93,137\nQmvav 252,68,212\nds_write_b128 v58, a[140:143] offset:39936\nQmvaa 100,64,104\nQa 94,94,138\nQa 95,95,139\n"
"Qmvav 100,68,244\nQta 62,69,18176\ns_waitcnt lgkmcnt(6)\nQa 220,220,148\ns_waitcnt lgkmcnt(6)\nQmvav 92,72,80\nQdv 104,59,1024\nv_add_u32 v75, 1, v75\n"
"Qmvav 92,76,88\nds_write_b32 v73, v75\nQa 221,221,149\nQmvav 124,72,112\nQa 222,222,150\nQa 223,223,151\nQmvav 124,76,120\ns_waitcnt lgkmcnt(5)\n"
"Qa 100,100,180\nQa 101,101,181\nQmvav 156,72,144\nQta 64,69,24832\nQa 102,102,182\nQmvav 156,76,152\nQa 103,103,183\nQmvav 188,72,176\n"
"Qmvav 188,76,184\nQta 66,69,25856\ns_waitcnt lgkmcnt(3)\nQa 124,124,104\nQmvav 220,72,208\nQa 125,125,105\nQa 126,126,106\nQmvav 220,76,216\n"
"ds_read_b32 v66, v74\nQa 127,127,107\nQmvav 252,72,240\nQta 68,68,16640\nQmvav 252,76,248\nQmvav 100,72,84\nQmvav 100,76,116\ns_waitcnt lgkmcnt(1)\n"
"v_cmp_gt_u32 vcc, v75, v66\ns_waitcnt lgkmcnt(6)\nQmvaa 92,56,0\ns_cbranch_vccnz .Lso1920r1\n.Lro1920r1:\ns_add_u32 s68, s64, 0\nQmvaa 92,60,28\nQta 70,68,17664\n"
"s_add_u32 m0, s96, 65536\ns_add_u32 s95, s72, 126976\nQmvaa 124,56,4\nbuffer_load_dwordx4 v63, s[52:55], s95 offen lds\nQmvaa 124,60,32\nQdv 136,59,33792\nQmvaa 156,56,8\nQta 72,68,24832\n"
"Qk 92,92,93\nQmvaa 156,60,36\ns_add_u32 s95, s72, 146432\nQk 93,94,95\nQmvaa 188,56,12\nbuffer_load_dwordx4 v63, s[52:55], s95 offen offset:1024 lds\nQmvaa 188,60,40\nQta 74,68,25856\n"
"Qk 94,124,125\nQmvaa 220,56,16\nQdv 148,59,34816\nQk 95,126,127\nQmvaa 220,60,44\nbuffer_store_dwordx4 v[92:95], v60, s[24:27], s68 offen offset:0 sc1\nQk 156,156,157\nQmvaa 252,56,20\n"
"Qta 76,68,33024\ns_add_u32 m0, s96, 73728\ns_add_u32 s95, s73, 86016\nQmvaa 252,60,48\nbuffer_load_dwordx4 v63, s[52:55], s95 offen lds\nv_add_u32 v75, 1, v75\nQmvaa 100,56,24\nds_write_b32 v73, v75\n"
"s_waitcnt lgkmcnt(5)\nQa 0,0,136\nQmvaa 100,60,52\nQta 78,68,34048\nQa 1,1,137\ns_waitcnt lgkmcnt(6)\nQmaaa 144,64,80\nQdv 180,59,35840\n"
"s_add_u32 s95, s73, 105472\nQmaaa 144,68,108\nbuffer_load_dwordx4 v63, s[52:55], s95 offen offset:1024 lds\nQmaaa 148,64,84\nQa 2,2,138\nQa 3,3,139\nQmaaa 148,68,112\nQdv 104,59,36864\n"
"s_waitcnt lgkmcnt(5)\nQa 4,4,148\nQmaaa 152,64,88\nQta 56,68,256\ns_add_u32 m0, s96, 81920\ns_add_u32 s95, s73, 126976\nQmaav 152,68,168\nbuffer_load_dwordx4 v63, s[52:55], s95 offen lds\n"
"Qmaaa 156,64,92\nds_read_b32 v66, v74\nQa 5,5,149\nQa 6,6,150\nQa 7,7,151\nQmaav 156,68,200\nQta 58,68,1280\ns_waitcnt lgkmcnt(4)\n"
"Qa 8,8,180\nQmaaa 160,64,96\nQdv 136,59,37888\ns_add_u32 s95, s73, 146432\nQmaav 160,68,232\nbuffer_load_dwordx4 v63, s[52:55], s95 offen offset:1024 lds\nQmaaa 164,64,100\ns_waitcnt lgkmcnt(2)\n"
"v_cmp_gt_u32 vcc, v75, v66\nQmaav 164,68,212\ns_cbranch_vccnz .Lso1945r1\n.Lro1945r1:\nQa 9,9,181\nQa 10,10,182\nQa 11,11,183\nQmaaa 168,64,104\n"
"Qta 60,68,8448\ns_waitcnt lgkmcnt(5)\nQa 12,12,104\nQmaav 168,68,244\nQdv 148,59,38912\nQa 13,13,105\ns_waitcnt lgkmcnt(6)\nQmaav 144,72,80\n"
"Qa 14,14,106\nQa 15,15,107\nQmaav 144,76,88\nQta 62,68,9472\ns_waitcnt lgkmcnt(3)\nQa 16,16,136\nQmaav 148,72,112\nQdv 180,59,39936\n"
"Qa 17,17,137\nQmaav 148,76,120\nQa 18,18,138\nQa 19,19,139\nQmaav 152,72,144\nQta 64,69,25088\nv_bfe_u32 v61, v60, 6, 2\nQmaav 152,76,152\n"
"s_waitcnt lgkmcnt(3)\nQa 20,20,148\nQa 21,21,149\nQmaav 156,72,176\nQa 22,22,150\nQa 23,23,151\nQmaav 156,76,184\nQta 66,69,26112\n"
"v_lshrrev_b32 v62, 8, v60\nQmaav 160,72,208\nv_xor_b32 v61, v61, v62\nv_bfe_u32 v62, v60, 5, 1\nQmaav 160,76,216\nv_lshl_or_b32 v61, v61, 1, v62\nv_and_b32 v62, 16, v60\nQmaav 164,72,240\n"
"Qta 68,68,16896\ns_waitcnt lgkmcnt(3)\nQa 24,24,180\nQmaav 164,76,248\nQa 25,25,181\nQa 26,26,182\nQmaav 168,72,84\nQa 27,27,183\n"
"v_mov_b32 v63, 0x3f80\nQmaav 168,76,116\nQta 70,68,17920\nv_lshlrev_b32 v63, v62, v63\ns_waitcnt lgkmcnt(5)\nQmaaa 144,56,0\nv_min_u32 v62, 1, v61\nv_add_u32 v62, -1, v62\n"
"Qmaaa 144,60,28\nv_and_b32 v62, v62, v63\nv_xor_b32 v61, 1, v61\nQmaaa 148,56,4\nv_min_u32 v61, 1, v61\nv_add_u32 v61, -1, v61\nQmaaa 148,60,32\nv_and_b32 v61, v61, v63\n"
"v_accvgpr_write_b32 a174, v62\nQmaaa 152,56,8\nQta 72,68,25088\nv_accvgpr_write_b32 a175, v61\nQmaaa 152,60,36\nds_write_b128 v58, a[144:147] offset:0\nQmaaa 156,56,12\nv_accvgpr_write_b32 a172, 0\n"
"v_accvgpr_write_b32 a173, 0\nQmaaa 156,60,40\nQta 74,68,26112\nv_accvgpr_write_b32 a176, 0\nQmaaa 160,56,16\nds_write_b128 v58, a[148:151] offset:1024\nQmaaa 160,60,44\nv_accvgpr_write_b32 a177, 0\n"
"Qk 157,158,159\nQmaaa 164,56,20\nQta 76,68,33280\nQk 158,188,189\nQmaaa 164,60,48\nds_write_b128 v58, a[152:155] offset:2048\nQmaaa 168,56,24\nQk 159,190,191\n"
"Qk 220,220,221\nQmaaa 168,60,52\nQta 78,68,34304\nbuffer_store_dwordx4 v[156:159], v60, s[24:27], s68 offen offset:1024 sc1\ns_waitcnt lgkmcnt(7)\nQmvaa 0,64,80\nds_write_b128 v58, a[156:159] offset:3072\nQmvaa 0,68,108\n"
"Qk 221,222,223\nQk 222,252,253\nQmvaa 4,64,84\nQk 223,254,255\nQk 100,100,101\nQmvaa 4,68,112\nds_write_b128 v58, a[160:163] offset:4096\nQmvaa 8,64,88\n"
"Qta 56,68,512\nbuffer_store_dwordx4 v[220:223], v60, s[24:27], s68 offen offset:2048 sc1\nQmvav 8,68,168\nQk 101,102,103\nQmvaa 12,64,92\nds_write_b128 v58, a[164:167] offset:5120\nQmvav 12,68,200\nQta 58,68,1536\n"
"Qmvaa 16,64,96\nQmvav 16,68,232\nds_write_b128 v58, a[168:171] offset:6144\nQmvaa 20,64,100\nQta 60,68,8704\nQmvav 20,68,212\nQmvaa 24,64,104\nQmvav 24,68,244\n"
"Qta 62,68,9728\ns_waitcnt lgkmcnt(8)\nQmvav 0,72,80\nv_add_u32 v75, 1, v75\nQmvav 0,76,88\ns_waitcnt vmcnt(11)\nds_write_b32 v73, v75\nQmvav 4,72,112\n"
"Qmvav 4,76,120\nQmvav 8,72,144\nQta 64,69,25344\nQmvav 8,76,152\nQmvav 12,72,176\nQmvav 12,76,184\nQta 66,69,26368\nQmvav 16,72,208\n"
"Qmvav 16,76,216\nds_read_b32 v66, v74\nQmvav 20,72,240\nQta 68,68,17152\nQmvav 20,76,248\nQmvav 24,72,84\nQmvav 24,76,116\ns_waitcnt lgkmcnt(1)\n"
"v_cmp_gt_u32 vcc, v75, v66\ns_waitcnt lgkmcnt(5)\nQmvaa 0,56,0\ns_cbranch_vccnz .Lso2004r1\n.Lro2004r1:\nQmvaa 0,60,28\nQta 70,68,18176\nQmvaa 4,56,4\n"
"Qmvaa 4,60,32\nQdv 104,59,2048\nQmvaa 8,56,8\nQta 72,68,25344\ns_nop 1\nQk 102,0,1\nQmvaa 8,60,36\nQk 103,2,3\n"
"Qmvaa 12,56,12\nQdv 136,59,3072\nbuffer_store_dwordx4 v[100:103], v60, s[24:27], s68 offen offset:3072 sc1\nQmvaa 12,60,40\nQta 74,68,26368\nQk 4,4,5\nQmvaa 16,56,16\nQk 5,6,7\n"
"Qk 6,8,9\nQmvaa 16,60,44\nQdv 148,59,5120\nQk 7,10,11\nQmvaa 20,56,20\nQta 76,68,33536\ns_add_u32 s68, s64, 4096\nQmvaa 20,60,48\n"
"s_waitcnt lgkmcnt(5)\nQa 36,36,104\nQa 37,37,105\nQmvaa 24,56,24\nQdv 180,59,0\nQa 38,38,106\nQmvaa 24,60,52\nQta 78,68,34560\n"
"Qa 39,39,107\ns_waitcnt lgkmcnt(11)\nQmvaa 28,64,80\ns_waitcnt lgkmcnt(5)\nQa 40,40,136\nQa 41,41,137\nQmvaa 28,68,108\nQa 42,42,138\n"
"Qa 43,43,139\nQmvaa 32,64,84\nbuffer_store_dwordx4 v[4:7], v60, s[24:27], s68 offen offset:0 sc1\nQk 12,12,13\nQmvaa 32,68,112\ns_waitcnt lgkmcnt(3)\nQa 48,48,148\nQa 49,49,149\n"
"Qmvaa 36,64,88\nQta 56,68,768\nQa 50,50,150\nQmvav 36,68,168\nQdv 104,59,4096\nQa 51,51,151\nQmvaa 40,64,92\ns_waitcnt lgkmcnt(3)\n"
"Qa 28,28,180\nQa 29,29,181\nQmvav 40,68,200\nQta 58,68,1792\nQa 30,30,182\nQmvaa 44,64,96\nQa 31,31,183\nQk 13,14,15\n"
"Qmvav 44,68,232\nQdv 136,59,6144\nQk 14,16,17\nQmvaa 48,64,100\nQta 60,68,8960\nQk 15,18,19\nQmvav 48,68,212\nbuffer_store_dwordx4 v[12:15], v60, s[24:27], s68 offen offset:1024 sc1\n"
"Qk 20,20,21\nQmvaa 52,64,104\nQk 21,22,23\nQk 22,24,25\nQmvav 52,68,244\nQta 62,68,9984\nv_add_u32 v75, 1, v75\ns_waitcnt lgkmcnt(6)\n"
"Qmvav 28,72,80\ns_waitcnt vmcnt(5)\nds_write_b32 v73, v75\nQmvav 28,76,88\nQdv 148,59,1024\ns_waitcnt lgkmcnt(6)\nQa 44,44,104\nQmvav 32,72,112\n"
"Qa 45,45,105\nQa 46,46,106\nQmvav 32,76,120\nQa 47,47,107\ns_waitcnt lgkmcnt(4)\nQa 52,52,136\nQmvav 36,72,144\nQta 64,68,40960\n"
"Qa 53,53,137\nQmvav 36,76,152\nQa 54,54,138\nQa 55,55,139\nQmvav 40,72,176\nQk 23,26,27\nQmvav 40,76,184\nQta 66,68,41984\n"
"s_waitcnt lgkmcnt(2)\nQa 32,32,148\nQmvav 44,72,208\nds_read_b32 v66, v74\nQa 33,33,149\nQmvav 44,76,216\nQa 34,34,150\nQa 35,35,151\n"
"Qmvav 48,72,240\nQta 68,68,49152\nbuffer_store_dwordx4 v[20:23], v60, s[24:27], s68 offen offset:2048 sc1\nQmvav 48,76,248\nQmvav 52,72,84\ns_waitcnt lgkmcnt(1)\nv_cmp_gt_u32 vcc, v75, v66\nQmvav 52,76,116\n"
"s_cbranch_vccnz .Lso2045r1\n.Lro2045r1:\ns_waitcnt lgkmcnt(6)\nQmvaa 28,56,0\nQta 70,68,50176\nQmvaa 28,60,28\nQmvaa 32,56,4\nQmvaa 32,60,32\n"
"Qmvaa 36,56,8\nQta 72,68,57344\ns_nop 3\nQk 28,28,29\nQmvaa 36,60,36\nQk 29,30,31\nQmvaa 40,56,12\nQk 30,32,33\n"
"Qk 31,34,35\nQmvaa 40,60,40\nQta 74,68,58368\nbuffer_store_dwordx4 v[28:31], v60, s[24:27], s68 offen offset:3072 sc1\nQmvaa 44,56,16\nQk 36,36,37\nQk 37,38,39\nQmvaa 44,60,44\n"
"s_add_u32 s68, s64, 8192\nQmvaa 48,56,20\nQta 76,69,0\nQk 38,40,41\nQmvaa 48,60,48\n.Lpg24:\ns_cbranch_execz .Ltr8\nQk 39,42,43\n"
"Qmvaa 52,56,24\nbuffer_store_dwordx4 v[36:39], v60, s[24:27], s68 offen offset:0 sc1\nQk 44,44,45\nQmvaa 52,60,52\nQta 78,69,1024\nQk 45,46,47\ns_waitcnt lgkmcnt(4)\nQmaaa 200,64,80\n"
"Qk 46,48,49\nQk 47,50,51\nQmaaa 200,68,108\nbuffer_store_dwordx4 v[44:47], v60, s[24:27], s68 offen offset:1024 sc1\nQmaaa 204,64,84\nQk 52,52,53\nQk 53,54,55\nQmaaa 204,68,112\n"
"v_mov_b32 v54, 0\nv_mov_b32 v55, 0\nQmaaa 208,64,88\nQta 56,69,8192\nbuffer_store_dwordx4 v[52:55], v60, s[24:27], s68 offen offset:2048 sc1\nQmaav 208,68,168\nQmaaa 212,64,92\nQmaav 212,68,200\n"
"Qta 58,69,9216\nQmaaa 216,64,96\nQmaav 216,68,232\nQmaaa 220,64,100\nQta 60,69,16384\nQmaav 220,68,212\nQmaaa 224,64,104\nQmaav 224,68,244\n"
"Qta 62,69,17408\ns_waitcnt lgkmcnt(4)\nQmaav 200,72,80\nQmaav 200,76,88\nQmaav 204,72,112\nQmaav 204,76,120\nQmaav 208,72,144\nQta 64,68,41216\n"
"Qmaav 208,76,152\nQmaav 212,72,176\nQmaav 212,76,184\nQta 66,68,42240\nQmaav 216,72,208\nQmaav 216,76,216\nQmaav 220,72,240\nQta 68,68,49408\n"
"Qmaav 220,76,248\nQmaav 224,72,84\nQmaav 224,76,116\nQta 70,68,50432\ns_waitcnt lgkmcnt(4)\nQmaaa 200,56,0\nQmaaa 200,60,28\nQmaaa 204,56,4\n"
"Qmaaa 204,60,32\nQmaaa 208,56,8\nQta 72,68,57600\nQmaaa 208,60,36\ns_nop 2\nds_write_b128 v58, a[200:203] offset:33792\nQmaaa 212,56,12\nQmaaa 212,60,40\n"
"Qta 74,68,58624\nQmaaa 216,56,16\nds_write_b128 v58, a[204:207] offset:34816\nQmaaa 216,60,44\nQmaaa 220,56,20\nQta 76,69,256\nQmaaa 220,60,48\nds_write_b128 v58, a[208:211] offset:35840\n"
"Qmaaa 224,56,24\nQmaaa 224,60,52\nQta 78,69,1280\ns_waitcnt lgkmcnt(7)\nQmaaa 228,64,80\nds_write_b128 v58, a[212:215] offset:36864\nQmaaa 228,68,108\nQmaaa 232,64,84\n"
"s_waitcnt vmcnt(0)\nbuffer_store_dword v67, v77, s[24:27], 0 offen sc1\nQmaaa 232,68,112\nds_write_b128 v58, a[216:219] offset:37888\nQmaaa 236,64,88\nQta 56,69,8448\nQmaav 236,68,168\nQmaaa 240,64,92\n"
"ds_write_b128 v58, a[220:223] offset:38912\nQmaav 240,68,200\nQta 58,69,9472\nQmaaa 244,64,96\nQmaav 244,68,232\nds_write_b128 v58, a[224:227] offset:39936\nQmaaa 248,64,100\nQta 60,69,16640\n"
"Qmaav 248,68,212\nQmaaa 252,64,104\nQmaav 252,68,244\nQta 62,69,17664\ns_waitcnt lgkmcnt(8)\nQmaav 228,72,80\nv_add_u32 v75, 1, v75\nQmaav 228,76,88\n"
"ds_write_b32 v73, v75\nQmaav 232,72,112\nQmaav 232,76,120\nQmaav 236,72,144\nQta 64,68,41472\nQmaav 236,76,152\nQmaav 240,72,176\nQmaav 240,76,184\n"
"Qta 66,68,42496\nQmaav 244,72,208\nQmaav 244,76,216\nds_read_b32 v66, v74\nQmaav 248,72,240\nQta 68,68,49664\nQmaav 248,76,248\nQmaav 252,72,84\n"
"buffer_load_dword v78, v76, s[24:27], 0 offen sc1\nQmaav 252,76,116\ns_waitcnt lgkmcnt(1)\nv_cmp_gt_u32 vcc, v75, v66\ns_waitcnt lgkmcnt(5)\nQmaaa 228,56,0\ns_cbranch_vccnz .Lso2130r1\n.Lro2130r1:\n"
"Qmaaa 228,60,28\nQta 70,68,50688\nQmaaa 232,56,4\nQmaaa 232,60,32\nQdv 180,59,35840\nQmaaa 236,56,8\nQta 72,68,57856\nQmaaa 236,60,36\n"
"s_nop 0\nds_write_b128 v58, a[228:231] offset:0\nQmaaa 240,56,12\nQdv 104,59,36864\nQmaaa 240,60,40\nQta 74,68,58880\nQmaaa 244,56,16\nds_write_b128 v58, a[232:235] offset:1024\n"
"Qmaaa 244,60,44\nQdv 136,59,38912\nQmaaa 248,56,20\nQta 76,69,512\nQmaaa 248,60,48\nds_write_b128 v58, a[236:239] offset:2048\nQmaaa 252,56,24\nQdv 148,59,33792\n"
"s_waitcnt lgkmcnt(9)\nQa 196,196,180\nQmaaa 252,60,52\nQta 78,69,1536\nQa 197,197,181\ns_waitcnt lgkmcnt(10)\nQmvaa 132,64,80\nds_write_b128 v58, a[240:243] offset:3072\n"
"Qmvaa 132,68,108\nQa 198,198,182\nQa 199,199,183\nQmvaa 164,64,84\ns_waitcnt lgkmcnt(8)\nQa 228,228,104\nQa 229,229,105\nQmvaa 164,68,112\n"
"ds_write_b128 v58, a[244:247] offset:4096\nQmvaa 196,64,88\nQta 56,69,8704\nQa 230,230,106\nQa 231,231,107\nQmvav 196,68,168\nQdv 180,59,37888\ns_waitcnt lgkmcnt(8)\n"
"Qa 140,140,136\nQmvaa 228,64,92\nds_write_b128 v58, a[248:251] offset:5120\nQmvav 228,68,200\nQta 58,69,9728\nQa 141,141,137\nQmvaa 108,64,96\nQa 142,142,138\n"
"Qa 143,143,139\nQmvav 108,68,232\nQdv 104,59,39936\ns_waitcnt lgkmcnt(8)\nQa 132,132,148\nQmvaa 140,64,100\nQta 60,69,16896\nQa 133,133,149\n"
"Qmvav 140,68,212\nds_write_b128 v58, a[252:255] offset:6144\nQmvaa 172,64,104\nQa 134,134,150\nQa 135,135,151\nQmvav 172,68,244\nQta 62,69,17920\ns_waitcnt lgkmcnt(6)\n"
"Qa 108,108,180\ns_waitcnt lgkmcnt(6)\nQmvav 132,72,80\nQdv 136,59,34816\nv_add_u32 v75, 1, v75\nQmvav 132,76,88\nds_write_b32 v73, v75\nQa 109,109,181\n"
"Qmvav 164,72,112\nQa 110,110,182\nQa 111,111,183\nQmvav 164,76,120\ns_waitcnt lgkmcnt(5)\nQa 172,172,104\nQa 173,173,105\nQmvav 196,72,144\n"
"Qta 64,68,41728\nQa 174,174,106\nQmvav 196,76,152\nQa 175,175,107\nQmvav 228,72,176\nQmvav 228,76,184\nQta 66,68,42752\ns_waitcnt lgkmcnt(3)\n"
"Qa 164,164,136\nQmvav 108,72,208\nQa 165,165,137\nQa 166,166,138\nQmvav 108,76,216\nds_read_b32 v66, v74\nQa 167,167,139\nQmvav 140,72,240\n"
"Qta 68,68,49920\nQmvav 140,76,248\nQmvav 172,72,84\nQmvav 172,76,116\ns_waitcnt lgkmcnt(1)\nv_cmp_gt_u32 vcc, v75, v66\ns_waitcnt lgkmcnt(6)\nQmvaa 132,56,0\n"
"s_cbranch_vccnz .Lso2172r1\n.Lro2172r1:\nQmvaa 132,60,28\ns_waitcnt vmcnt(0)\nv_sub_u32 v66, v78, v65\nv_cmp_gt_u32 vcc, 1, v66\ns_and_b64 vcc, vcc, s[28:29]\ns_cbranch_scc1 .Lx1_w\n"
".Lx1_r:\nQmvaa 164,56,4\ns_add_u32 s69, s98, 0\nbuffer_load_dwordx4 a[180:183], v60, s[24:27], s69 offen offset:0 sc1\nQmvaa 164,60,32\nQta 70,68,50944\nQmvaa 196,56,8\nbuffer_load_dwordx4 a[184:187], v60, s[24:27], s69 offen offset:1024 sc1\n"
"Qmvaa 196,60,36\nQdv 148,59,2048\nQmvaa 228,56,12\nbuffer_load_dwordx4 a[188:191], v60, s[24:27], s69 offen offset:2048 sc1\nQmvaa 228,60,40\nQta 72,68,58112\nQmvaa 108,56,16\nbuffer_load_dwordx4 a[192:195], v60, s[24:27], s69 offen offset:3072 sc1\n"
"Qmvaa 108,60,44\nQdv 180,59,4096\nQmvaa 140,56,20\ns_add_u32 s69, s98, 4096\nbuffer_load_dwordx4 a[196:199], v60, s[24:27], s69 offen offset:0 sc1\nQmvaa 140,60,48\nQta 74,68,59136\ns_waitcnt lgkmcnt(3)\n"
"Qa 96,96,148\nQmvaa 172,56,24\nbuffer_load_dwordx4 a[116:119], v60, s[24:27], s69 offen offset:1024 sc1\nQmvaa 172,60,52\nQdv 104,59,0\nQa 97,97,149\ns_waitcnt lgkmcnt(4)\nQmvaa 204,64,80\n"
"buffer_load_dwordx4 a[120:123], v60, s[24:27], s69 offen offset:2048 sc1\nQmvaa 204,68,108\nQta 76,69,768\nQa 98,98,150\nQmvaa 236,64,84\nQdv 136,59,1024\nQa 99,99,151\nQmvaa 236,68,112\n"
"s_waitcnt lgkmcnt(4)\nQa 160,160,180\nQa 161,161,181\nQmvaa 96,64,88\nQta 78,69,1792\nQa 162,162,182\nQmvav 96,68,168\nQdv 148,59,3072\n"
"Qa 163,163,183\nQmvaa 128,64,92\ns_waitcnt lgkmcnt(4)\nQa 204,204,104\nQa 205,205,105\nQmvav 128,68,200\nQta 56,69,8960\nQa 206,206,106\n"
"Qmvaa 160,64,96\nQdv 180,59,5120\nQa 207,207,107\nQmvav 160,68,232\nQmvaa 192,64,100\nQta 58,69,9984\ns_waitcnt lgkmcnt(5)\nQa 236,236,136\n"
"Qmvav 192,68,212\nQa 237,237,137\nQa 238,238,138\nQmvaa 224,64,104\nQa 239,239,139\nQmvav 224,68,244\nQta 60,69,17152\ns_waitcnt lgkmcnt(4)\n"
"Qa 128,128,148\ns_waitcnt lgkmcnt(4)\nQmvav 204,72,80\nQa 129,129,149\nQa 130,130,150\nQmvav 204,76,88\nQa 131,131,151\nQmvav 236,72,112\n"
"Qta 62,69,18176\ns_waitcnt lgkmcnt(3)\nQa 192,192,180\nQmvav 236,76,120\nQa 193,193,181\nQa 194,194,182\nQmvav 96,72,144\nQa 195,195,183\n"
"Qmvav 96,76,152\nQmvav 128,72,176\nQmvav 128,76,184\nQmvav 160,72,208\nQmvav 160,76,216\nQdv 104,59,6144\nQmvav 192,72,240\nQmvav 192,76,248\n"
"v_add_u32 v75, 1, v75\nQmvav 224,72,84\nds_write_b32 v73, v75\nQmvav 224,76,116\ns_waitcnt lgkmcnt(2)\nQmvaa 204,56,0\nQmvaa 204,60,28\nQmvaa 236,56,4\n"
"Qmvaa 236,60,32\ns_waitcnt lgkmcnt(1)\ns_nop 1\nQa 224,224,104\nQa 225,225,105\nQmvaa 96,56,8\nQa 226,226,106\nQa 227,227,107\n"
"Qmvaa 96,60,36\nQmvaa 128,56,12\nds_read_b32 v66, v74\nQmvaa 128,60,40\nQmvaa 160,56,16\nQmvaa 160,60,44\nQmvaa 192,56,20\ns_waitcnt lgkmcnt(0)\n"
"v_cmp_gt_u32 vcc, v75, v66\nQmvaa 192,60,48\ns_cbranch_vccnz .Lso2225r1\n.Lro2225r1:\nQmvaa 224,56,24\nQmvaa 224,60,52\ns_waitcnt vmcnt(6)\nQmvaa 132,174,180\n"
"Qmvaa 164,172,180\ns_waitcnt vmcnt(5)\nQmvaa 196,174,184\nQmvaa 228,172,184\ns_nop 3\nQk 132,132,133\nQk 133,134,135\nds_write_b64 v56, v[132:133] offset:0\n"
"s_waitcnt vmcnt(4)\nQmvaa 108,174,188\nQk 164,164,165\nQk 165,166,167\nds_write_b64 v56, v[164:165] offset:3328\nQmvaa 140,172,188\nQk 196,196,197\nQk 197,198,199\n"
"ds_write_b64 v56, v[196:197] offset:6656\ns_waitcnt vmcnt(3)\nQmvaa 172,174,192\nQk 228,228,229\nQk 229,230,231\nds_write_b64 v56, v[228:229] offset:9984\nQmvaa 204,172,192\nQk 108,108,109\n"
"Qk 109,110,111\nds_write_b64 v56, v[108:109] offset:13312\ns_waitcnt vmcnt(2)\nQmvaa 236,174,196\nQk 140,140,141\nQk 141,142,143\nds_write_b64 v56, v[140:141] offset:16640\nQmvaa 96,172,196\n"
"Qk 172,172,173\nQk 173,174,175\nds_write_b64 v56, v[172:173] offset:19968\ns_waitcnt vmcnt(1)\nQmvaa 128,174,116\nQk 204,204,205\nQk 205,206,207\nds_write_b64 v56, v[204:205] offset:32\n"
"Qmvaa 160,172,116\nQk 236,236,237\nQk 237,238,239\nds_write_b64 v56, v[236:237] offset:3360\ns_waitcnt vmcnt(0)\nQmvaa 192,174,120\nQk 96,96,97\nQk 97,98,99\n"
"ds_write_b64 v56, v[96:97] offset:6688\nQmvaa 224,172,120\nQk 128,128,129\nQk 129,130,131\nds_write_b64 v56, v[128:129] offset:10016\nQk 160,160,161\nQk 161,162,163\nds_write_b64 v56, v[160:161] offset:13344\n"
"Qk 192,192,193\nQk 193,194,195\nds_write_b64 v56, v[192:193] offset:16672\nQk 224,224,225\nQk 225,226,227\nds_write_b64 v56, v[224:225] offset:20000\ns_waitcnt lgkmcnt(0)\ns_barrier\n"
"Qda 0,57,0\nQda 4,57,3328\nQda 8,57,6656\nQda 12,57,9984\nQda 16,57,13312\nQda 20,57,16640\nQda 24,57,19968\ns_mov_b32 s68, 0\n"
"s_waitcnt lgkmcnt(6)\nbuffer_store_dwordx4 a[0:3], v64, s[40:43], s68 offen offset:0 sc1\ns_mov_b32 s68, 40960\ns_waitcnt lgkmcnt(5)\nbuffer_store_dwordx4 a[4:7], v64, s[40:43], s68 offen offset:0 sc1\ns_mov_b32 s68, 81920\ns_waitcnt lgkmcnt(4)\nbuffer_store_dwordx4 a[8:11], v64, s[40:43], s68 offen offset:0 sc1\n"
"s_mov_b32 s68, 122880\ns_waitcnt lgkmcnt(3)\nbuffer_store_dwordx4 a[12:15], v64, s[40:43], s68 offen offset:0 sc1\ns_mov_b32 s68, 163840\ns_waitcnt lgkmcnt(2)\nbuffer_store_dwordx4 a[16:19], v64, s[40:43], s68 offen offset:0 sc1\ns_mov_b32 s68, 204800\ns_waitcnt lgkmcnt(1)\n"
"buffer_store_dwordx4 a[20:23], v64, s[40:43], s68 offen offset:0 sc1\ns_mov_b32 s68, 245760\ns_waitcnt lgkmcnt(0)\nbuffer_store_dwordx4 a[24:27], v64, s[40:43], s68 offen offset:0 sc1\n.Lfin:\n.Lpg25:\ns_cbranch_execz .Ltr8\ns_waitcnt vmcnt(0) lgkmcnt(0)\n"
"s_branch .Lexit\n.Lend:\n.Lfb:\ns_waitcnt vmcnt(0) lgkmcnt(0)\nv_lshrrev_b32 v52, 4, v60\nv_and_b32 v53, 15, v52\nv_lshrrev_b32 v52, 4, v52\nv_lshlrev_b32 v66, 8, v53\n"
"v_lshl_add_u32 v66, v52, 4, v66\nv_add_u32 v66, 1024, v66\nv_lshlrev_b32 v67, 10, v52\nv_lshl_add_u32 v67, v53, 1, v67\nv_add_u32 v67, 1024, v67\nv_mov_b32 v52, 0x3f803f80\nv_accvgpr_write_b32 a204, v52\nv_accvgpr_write_b32 a205, v52\n"
"v_accvgpr_write_b32 a206, v52\nv_accvgpr_write_b32 a207, v52\nv_mov_b32 v24, 0xff800000\nv_mov_b32 v28, 0xff800000\nv_mov_b32 v32, 0xff800000\nv_mov_b32 v36, 0xff800000\nv_mov_b32 v40, 0xff800000\nv_mov_b32 v44, 0xff800000\n"
"v_mov_b32 v48, 0xff800000\ns_lshl_b32 s89, s67, 21\ns_mov_b32 s66, 0\n.Lfb1:\ns_add_u32 s68, s89, 4096\nbuffer_load_dwordx4 a[112:115], v66, s[40:43], s89 offen offset:0\nbuffer_load_dwordx4 a[116:119], v66, s[40:43], s89 offen offset:64\nbuffer_load_dwordx4 a[120:123], v66, s[40:43], s89 offen offset:128\n"
"buffer_load_dwordx4 a[124:127], v66, s[40:43], s89 offen offset:192\nbuffer_load_dwordx4 a[128:131], v66, s[40:43], s68 offen offset:0\nbuffer_load_dwordx4 a[132:135], v66, s[40:43], s68 offen offset:64\nbuffer_load_dwordx4 a[136:139], v66, s[40:43], s68 offen offset:128\nbuffer_load_dwordx4 a[140:143], v66, s[40:43], s68 offen offset:192\ns_waitcnt vmcnt(0)\nQzvaa 0,112,0\nQmvaa 0,116,4\n"
"Qmvaa 0,120,8\nQmvaa 0,124,12\nQzvaa 4,128,0\nQmvaa 4,132,4\nQmvaa 4,136,8\nQmvaa 4,140,12\ns_nop 7\nv_max3_f32 v24, v24, v0, v1\n"
"v_max3_f32 v24, v24, v2, v3\nv_max3_f32 v24, v24, v4, v5\nv_max3_f32 v24, v24, v6, v7\nQzvaa 0,112,16\nQmvaa 0,116,20\nQmvaa 0,120,24\nQmvaa 0,124,28\nQzvaa 4,128,16\n"
"Qmvaa 4,132,20\nQmvaa 4,136,24\nQmvaa 4,140,28\ns_nop 7\nv_max3_f32 v28, v28, v0, v1\nv_max3_f32 v28, v28, v2, v3\nv_max3_f32 v28, v28, v4, v5\nv_max3_f32 v28, v28, v6, v7\n"
"Qzvaa 0,112,32\nQmvaa 0,116,36\nQmvaa 0,120,40\nQmvaa 0,124,44\nQzvaa 4,128,32\nQmvaa 4,132,36\nQmvaa 4,136,40\nQmvaa 4,140,44\n"
"s_nop 7\nv_max3_f32 v32, v32, v0, v1\nv_max3_f32 v32, v32, v2, v3\nv_max3_f32 v32, v32, v4, v5\nv_max3_f32 v32, v32, v6, v7\nQzvaa 0,112,48\nQmvaa 0,116,52\nQmvaa 0,120,56\n"
"Qmvaa 0,124,60\nQzvaa 4,128,48\nQmvaa 4,132,52\nQmvaa 4,136,56\nQmvaa 4,140,60\ns_nop 7\nv_max3_f32 v36, v36, v0, v1\nv_max3_f32 v36, v36, v2, v3\n"
"v_max3_f32 v36, v36, v4, v5\nv_max3_f32 v36, v36, v6, v7\nQzvaa 0,112,64\nQmvaa 0,116,68\nQmvaa 0,120,72\nQmvaa 0,124,76\nQzvaa 4,128,64\nQmvaa 4,132,68\n"
"Qmvaa 4,136,72\nQmvaa 4,140,76\ns_nop 7\nv_max3_f32 v40, v40, v0, v1\nv_max3_f32 v40, v40, v2, v3\nv_max3_f32 v40, v40, v4, v5\nv_max3_f32 v40, v40, v6, v7\nQzvaa 0,112,80\n"
"Qmvaa 0,116,84\nQmvaa 0,120,88\nQmvaa 0,124,92\nQzvaa 4,128,80\nQmvaa 4,132,84\nQmvaa 4,136,88\nQmvaa 4,140,92\ns_nop 7\n"
"v_max3_f32 v44, v44, v0, v1\nv_max3_f32 v44, v44, v2, v3\nv_max3_f32 v44, v44, v4, v5\nv_max3_f32 v44, v44, v6, v7\nQzvaa 0,112,96\nQmvaa 0,116,100\nQmvaa 0,120,104\nQmvaa 0,124,108\n"
"Qzvaa 4,128,96\nQmvaa 4,132,100\nQmvaa 4,136,104\nQmvaa 4,140,108\ns_nop 7\nv_max3_f32 v48, v48, v0, v1\nv_max3_f32 v48, v48, v2, v3\nv_max3_f32 v48, v48, v4, v5\n"
"v_max3_f32 v48, v48, v6, v7\ns_add_u32 s89, s89, 8192\ns_add_u32 s66, s66, 1\ns_cmp_lt_u32 s66, 32\ns_cbranch_scc1 .Lfb1\nv_mov_b32 v25, v24\ns_nop 1\nv_permlane32_swap_b32 v24, v25\n"
"v_max_f32 v24, v24, v25\nv_mov_b32 v25, v24\ns_nop 1\nv_permlane16_swap_b32 v24, v25\nv_max_f32 v24, v24, v25\nv_sub_f32 v24, 0, v24\nv_mov_b32 v25, v24\nv_mov_b32 v26, v24\n"
"v_mov_b32 v27, v24\nv_mov_b32 v29, v28\ns_nop 1\nv_permlane32_swap_b32 v28, v29\nv_max_f32 v28, v28, v29\nv_mov_b32 v29, v28\ns_nop 1\nv_permlane16_swap_b32 v28, v29\n"
"v_max_f32 v28, v28, v29\nv_sub_f32 v28, 0, v28\nv_mov_b32 v29, v28\nv_mov_b32 v30, v28\nv_mov_b32 v31, v28\nv_mov_b32 v33, v32\ns_nop 1\nv_permlane32_swap_b32 v32, v33\n"
"v_max_f32 v32, v32, v33\nv_mov_b32 v33, v32\ns_nop 1\nv_permlane16_swap_b32 v32, v33\nv_max_f32 v32, v32, v33\nv_sub_f32 v32, 0, v32\nv_mov_b32 v33, v32\nv_mov_b32 v34, v32\n"
"v_mov_b32 v35, v32\nv_mov_b32 v37, v36\ns_nop 1\nv_permlane32_swap_b32 v36, v37\nv_max_f32 v36, v36, v37\nv_mov_b32 v37, v36\ns_nop 1\nv_permlane16_swap_b32 v36, v37\n"
"v_max_f32 v36, v36, v37\nv_sub_f32 v36, 0, v36\nv_mov_b32 v37, v36\nv_mov_b32 v38, v36\nv_mov_b32 v39, v36\nv_mov_b32 v41, v40\ns_nop 1\nv_permlane32_swap_b32 v40, v41\n"
"v_max_f32 v40, v40, v41\nv_mov_b32 v41, v40\ns_nop 1\nv_permlane16_swap_b32 v40, v41\nv_max_f32 v40, v40, v41\nv_sub_f32 v40, 0, v40\nv_mov_b32 v41, v40\nv_mov_b32 v42, v40\n"
"v_mov_b32 v43, v40\nv_mov_b32 v45, v44\ns_nop 1\nv_permlane32_swap_b32 v44, v45\nv_max_f32 v44, v44, v45\nv_mov_b32 v45, v44\ns_nop 1\nv_permlane16_swap_b32 v44, v45\n"
"v_max_f32 v44, v44, v45\nv_sub_f32 v44, 0, v44\nv_mov_b32 v45, v44\nv_mov_b32 v46, v44\nv_mov_b32 v47, v44\nv_mov_b32 v49, v48\ns_nop 1\nv_permlane32_swap_b32 v48, v49\n"
"v_max_f32 v48, v48, v49\nv_mov_b32 v49, v48\ns_nop 1\nv_permlane16_swap_b32 v48, v49\nv_max_f32 v48, v48, v49\nv_sub_f32 v48, 0, v48\nv_mov_b32 v49, v48\nv_mov_b32 v50, v48\n"
"v_mov_b32 v51, v48\nv_mov_b32 v52, 0\nv_mov_b32 v80, 0\nv_mov_b32 v81, 0\nv_mov_b32 v82, 0\nv_mov_b32 v83, 0\nv_mov_b32 v84, 0\nv_mov_b32 v85, 0\n"
"v_mov_b32 v86, 0\nv_mov_b32 v87, 0\nv_mov_b32 v88, 0\nv_mov_b32 v89, 0\nv_mov_b32 v90, 0\nv_mov_b32 v91, 0\nv_mov_b32 v92, 0\nv_mov_b32 v93, 0\n"
"v_mov_b32 v94, 0\nv_mov_b32 v95, 0\nv_mov_b32 v96, 0\nv_mov_b32 v97, 0\nv_mov_b32 v98, 0\nv_mov_b32 v99, 0\nv_mov_b32 v100, 0\nv_mov_b32 v101, 0\n"
"v_mov_b32 v102, 0\nv_mov_b32 v103, 0\nv_mov_b32 v104, 0\nv_mov_b32 v105, 0\nv_mov_b32 v106, 0\nv_mov_b32 v107, 0\nv_mov_b32 v108, 0\nv_mov_b32 v109, 0\n"
"v_mov_b32 v110, 0\nv_mov_b32 v111, 0\nv_accvgpr_write_b32 a176, v52\nv_accvgpr_write_b32 a177, v52\nv_accvgpr_write_b32 a178, v52\nv_accvgpr_write_b32 a179, v52\nv_mov_b32 v112, 0\nv_mov_b32 v113, 0\n"
"v_mov_b32 v114, 0\nv_mov_b32 v115, 0\nv_mov_b32 v116, 0\nv_mov_b32 v117, 0\nv_mov_b32 v118, 0\nv_mov_b32 v119, 0\nv_mov_b32 v120, 0\nv_mov_b32 v121, 0\n"
"v_mov_b32 v122, 0\nv_mov_b32 v123, 0\nv_mov_b32 v124, 0\nv_mov_b32 v125, 0\nv_mov_b32 v126, 0\nv_mov_b32 v127, 0\nv_mov_b32 v128, 0\nv_mov_b32 v129, 0\n"
"v_mov_b32 v130, 0\nv_mov_b32 v131, 0\nv_mov_b32 v132, 0\nv_mov_b32 v133, 0\nv_mov_b32 v134, 0\nv_mov_b32 v135, 0\nv_mov_b32 v136, 0\nv_mov_b32 v137, 0\n"
"v_mov_b32 v138, 0\nv_mov_b32 v139, 0\nv_mov_b32 v140, 0\nv_mov_b32 v141, 0\nv_mov_b32 v142, 0\nv_mov_b32 v143, 0\nv_accvgpr_write_b32 a180, v52\nv_accvgpr_write_b32 a181, v52\n"
"v_accvgpr_write_b32 a182, v52\nv_accvgpr_write_b32 a183, v52\nv_mov_b32 v144, 0\nv_mov_b32 v145, 0\nv_mov_b32 v146, 0\nv_mov_b32 v147, 0\nv_mov_b32 v148, 0\nv_mov_b32 v149, 0\n"
"v_mov_b32 v150, 0\nv_mov_b32 v151, 0\nv_mov_b32 v152, 0\nv_mov_b32 v153, 0\nv_mov_b32 v154, 0\nv_mov_b32 v155, 0\nv_mov_b32 v156, 0\nv_mov_b32 v157, 0\n"
"v_mov_b32 v158, 0\nv_mov_b32 v159, 0\nv_mov_b32 v160, 0\nv_mov_b32 v161, 0\nv_mov_b32 v162, 0\nv_mov_b32 v163, 0\nv_mov_b32 v164, 0\nv_mov_b32 v165, 0\n"
"v_mov_b32 v166, 0\nv_mov_b32 v167, 0\nv_mov_b32 v168, 0\nv_mov_b32 v169, 0\nv_mov_b32 v170, 0\nv_mov_b32 v171, 0\nv_mov_b32 v172, 0\nv_mov_b32 v173, 0\n"
"v_mov_b32 v174, 0\nv_mov_b32 v175, 0\nv_accvgpr_write_b32 a184, v52\nv_accvgpr_write_b32 a185, v52\nv_accvgpr_write_b32 a186, v52\nv_accvgpr_write_b32 a187, v52\nv_mov_b32 v176, 0\nv_mov_b32 v177, 0\n"
"v_mov_b32 v178, 0\nv_mov_b32 v179, 0\nv_mov_b32 v180, 0\nv_mov_b32 v181, 0\nv_mov_b32 v182, 0\nv_mov_b32 v183, 0\nv_mov_b32 v184, 0\nv_mov_b32 v185, 0\n"
"v_mov_b32 v186, 0\nv_mov_b32 v187, 0\nv_mov_b32 v188, 0\nv_mov_b32 v189, 0\nv_mov_b32 v190, 0\nv_mov_b32 v191, 0\nv_mov_b32 v192, 0\nv_mov_b32 v193, 0\n"
"v_mov_b32 v194, 0\nv_mov_b32 v195, 0\nv_mov_b32 v196, 0\nv_mov_b32 v197, 0\nv_mov_b32 v198, 0\nv_mov_b32 v199, 0\nv_mov_b32 v200, 0\nv_mov_b32 v201, 0\n"
"v_mov_b32 v202, 0\nv_mov_b32 v203, 0\nv_mov_b32 v204, 0\nv_mov_b32 v205, 0\nv_mov_b32 v206, 0\nv_mov_b32 v207, 0\nv_accvgpr_write_b32 a188, v52\nv_accvgpr_write_b32 a189, v52\n"
"v_accvgpr_write_b32 a190, v52\nv_accvgpr_write_b32 a191, v52\nv_mov_b32 v208, 0\nv_mov_b32 v209, 0\nv_mov_b32 v210, 0\nv_mov_b32 v211, 0\nv_mov_b32 v212, 0\nv_mov_b32 v213, 0\n"
"v_mov_b32 v214, 0\nv_mov_b32 v215, 0\nv_mov_b32 v216, 0\nv_mov_b32 v217, 0\nv_mov_b32 v218, 0\nv_mov_b32 v219, 0\nv_mov_b32 v220, 0\nv_mov_b32 v221, 0\n"
"v_mov_b32 v222, 0\nv_mov_b32 v223, 0\nv_mov_b32 v224, 0\nv_mov_b32 v225, 0\nv_mov_b32 v226, 0\nv_mov_b32 v227, 0\nv_mov_b32 v228, 0\nv_mov_b32 v229, 0\n"
"v_mov_b32 v230, 0\nv_mov_b32 v231, 0\nv_mov_b32 v232, 0\nv_mov_b32 v233, 0\nv_mov_b32 v234, 0\nv_mov_b32 v235, 0\nv_mov_b32 v236, 0\nv_mov_b32 v237, 0\n"
"v_mov_b32 v238, 0\nv_mov_b32 v239, 0\nv_accvgpr_write_b32 a192, v52\nv_accvgpr_write_b32 a193, v52\nv_accvgpr_write_b32 a194, v52\nv_accvgpr_write_b32 a195, v52\nv_mov_b32 v240, 0\nv_mov_b32 v241, 0\n"
"v_mov_b32 v242, 0\nv_mov_b32 v243, 0\nv_mov_b32 v244, 0\nv_mov_b32 v245, 0\nv_mov_b32 v246, 0\nv_mov_b32 v247, 0\nv_mov_b32 v248, 0\nv_mov_b32 v249, 0\n"
"v_mov_b32 v250, 0\nv_mov_b32 v251, 0\nv_mov_b32 v252, 0\nv_mov_b32 v253, 0\nv_mov_b32 v254, 0\nv_mov_b32 v255, 0\nv_accvgpr_write_b32 a208, v52\nv_accvgpr_write_b32 a209, v52\n"
"v_accvgpr_write_b32 a210, v52\nv_accvgpr_write_b32 a211, v52\nv_accvgpr_write_b32 a212, v52\nv_accvgpr_write_b32 a213, v52\nv_accvgpr_write_b32 a214, v52\nv_accvgpr_write_b32 a215, v52\nv_accvgpr_write_b32 a216, v52\nv_accvgpr_write_b32 a217, v52\n"
"v_accvgpr_write_b32 a218, v52\nv_accvgpr_write_b32 a219, v52\nv_accvgpr_write_b32 a220, v52\nv_accvgpr_write_b32 a221, v52\nv_accvgpr_write_b32 a222, v52\nv_accvgpr_write_b32 a223, v52\nv_accvgpr_write_b32 a196, v52\nv_accvgpr_write_b32 a197, v52\n"
"v_accvgpr_write_b32 a198, v52\nv_accvgpr_write_b32 a199, v52\nv_accvgpr_write_b32 a224, v52\nv_accvgpr_write_b32 a225, v52\nv_accvgpr_write_b32 a226, v52\nv_accvgpr_write_b32 a227, v52\nv_accvgpr_write_b32 a228, v52\nv_accvgpr_write_b32 a229, v52\n"
"v_accvgpr_write_b32 a230, v52\nv_accvgpr_write_b32 a231, v52\nv_accvgpr_write_b32 a232, v52\nv_accvgpr_write_b32 a233, v52\nv_accvgpr_write_b32 a234, v52\nv_accvgpr_write_b32 a235, v52\nv_accvgpr_write_b32 a236, v52\nv_accvgpr_write_b32 a237, v52\n"
"v_accvgpr_write_b32 a238, v52\nv_accvgpr_write_b32 a239, v52\nv_accvgpr_write_b32 a240, v52\nv_accvgpr_write_b32 a241, v52\nv_accvgpr_write_b32 a242, v52\nv_accvgpr_write_b32 a243, v52\nv_accvgpr_write_b32 a244, v52\nv_accvgpr_write_b32 a245, v52\n"
"v_accvgpr_write_b32 a246, v52\nv_accvgpr_write_b32 a247, v52\nv_accvgpr_write_b32 a248, v52\nv_accvgpr_write_b32 a249, v52\nv_accvgpr_write_b32 a250, v52\nv_accvgpr_write_b32 a251, v52\nv_accvgpr_write_b32 a252, v52\nv_accvgpr_write_b32 a253, v52\n"
"v_accvgpr_write_b32 a254, v52\nv_accvgpr_write_b32 a255, v52\nv_accvgpr_write_b32 a200, v52\nv_accvgpr_write_b32 a201, v52\nv_accvgpr_write_b32 a202, v52\nv_accvgpr_write_b32 a203, v52\ns_lshl_b32 s89, s67, 21\ns_mov_b32 s66, 0\n"
".Lfb2:\ns_add_u32 s68, s89, 4096\nbuffer_load_dwordx4 a[112:115], v66, s[40:43], s89 offen offset:0\nbuffer_load_dwordx4 a[116:119], v66, s[40:43], s89 offen offset:64\nbuffer_load_dwordx4 a[120:123], v66, s[40:43], s89 offen offset:128\nbuffer_load_dwordx4 a[124:127], v66, s[40:43], s89 offen offset:192\nbuffer_load_dwordx4 a[128:131], v66, s[40:43], s68 offen offset:0\nbuffer_load_dwordx4 a[132:135], v66, s[40:43], s68 offen offset:64\n"
"buffer_load_dwordx4 a[136:139], v66, s[40:43], s68 offen offset:128\nbuffer_load_dwordx4 a[140:143], v66, s[40:43], s68 offen offset:192\nbuffer_load_ushort v8, v67, s[44:47], s89 offen offset:0\nbuffer_load_ushort v9, v67, s[44:47], s89 offen offset:256\nbuffer_load_ushort v10, v67, s[44:47], s89 offen offset:512\nbuffer_load_ushort v11, v67, s[44:47], s89 offen offset:768\nbuffer_load_ushort v12, v67, s[44:47], s68 offen offset:0\nbuffer_load_ushort v13, v67, s[44:47], s68 offen offset:256\n"
"buffer_load_ushort v14, v67, s[44:47], s68 offen offset:512\nbuffer_load_ushort v15, v67, s[44:47], s68 offen offset:768\ns_waitcnt vmcnt(0)\nv_lshl_or_b32 v8, v9, 16, v8\nv_accvgpr_write_b32 a144, v8\nv_lshl_or_b32 v10, v11, 16, v10\nv_accvgpr_write_b32 a145, v10\nv_lshl_or_b32 v12, v13, 16, v12\n"
"v_accvgpr_write_b32 a146, v12\nv_lshl_or_b32 v14, v15, 16, v14\nv_accvgpr_write_b32 a147, v14\nbuffer_load_ushort v8, v67, s[44:47], s89 offen offset:32\nbuffer_load_ushort v9, v67, s[44:47], s89 offen offset:288\nbuffer_load_ushort v10, v67, s[44:47], s89 offen offset:544\nbuffer_load_ushort v11, v67, s[44:47], s89 offen offset:800\nbuffer_load_ushort v12, v67, s[44:47], s68 offen offset:32\n"
"buffer_load_ushort v13, v67, s[44:47], s68 offen offset:288\nbuffer_load_ushort v14, v67, s[44:47], s68 offen offset:544\nbuffer_load_ushort v15, v67, s[44:47], s68 offen offset:800\ns_waitcnt vmcnt(0)\nv_lshl_or_b32 v8, v9, 16, v8\nv_accvgpr_write_b32 a148, v8\nv_lshl_or_b32 v10, v11, 16, v10\nv_accvgpr_write_b32 a149, v10\n"
"v_lshl_or_b32 v12, v13, 16, v12\nv_accvgpr_write_b32 a150, v12\nv_lshl_or_b32 v14, v15, 16, v14\nv_accvgpr_write_b32 a151, v14\nbuffer_load_ushort v8, v67, s[44:47], s89 offen offset:64\nbuffer_load_ushort v9, v67, s[44:47], s89 offen offset:320\nbuffer_load_ushort v10, v67, s[44:47], s89 offen offset:576\nbuffer_load_ushort v11, v67, s[44:47], s89 offen offset:832\n"
"buffer_load_ushort v12, v67, s[44:47], s68 offen offset:64\nbuffer_load_ushort v13, v67, s[44:47], s68 offen offset:320\nbuffer_load_ushort v14, v67, s[44:47], s68 offen offset:576\nbuffer_load_ushort v15, v67, s[44:47], s68 offen offset:832\ns_waitcnt vmcnt(0)\nv_lshl_or_b32 v8, v9, 16, v8\nv_accvgpr_write_b32 a152, v8\nv_lshl_or_b32 v10, v11, 16, v10\n"
"v_accvgpr_write_b32 a153, v10\nv_lshl_or_b32 v12, v13, 16, v12\nv_accvgpr_write_b32 a154, v12\nv_lshl_or_b32 v14, v15, 16, v14\nv_accvgpr_write_b32 a155, v14\nbuffer_load_ushort v8, v67, s[44:47], s89 offen offset:96\nbuffer_load_ushort v9, v67, s[44:47], s89 offen offset:352\nbuffer_load_ushort v10, v67, s[44:47], s89 offen offset:608\n"
"buffer_load_ushort v11, v67, s[44:47], s89 offen offset:864\nbuffer_load_ushort v12, v67, s[44:47], s68 offen offset:96\nbuffer_load_ushort v13, v67, s[44:47], s68 offen offset:352\nbuffer_load_ushort v14, v67, s[44:47], s68 offen offset:608\nbuffer_load_ushort v15, v67, s[44:47], s68 offen offset:864\ns_waitcnt vmcnt(0)\nv_lshl_or_b32 v8, v9, 16, v8\nv_accvgpr_write_b32 a156, v8\n"
"v_lshl_or_b32 v10, v11, 16, v10\nv_accvgpr_write_b32 a157, v10\nv_lshl_or_b32 v12, v13, 16, v12\nv_accvgpr_write_b32 a158, v12\nv_lshl_or_b32 v14, v15, 16, v14\nv_accvgpr_write_b32 a159, v14\nbuffer_load_ushort v8, v67, s[44:47], s89 offen offset:128\nbuffer_load_ushort v9, v67, s[44:47], s89 offen offset:384\n"
"buffer_load_ushort v10, v67, s[44:47], s89 offen offset:640\nbuffer_load_ushort v11, v67, s[44:47], s89 offen offset:896\nbuffer_load_ushort v12, v67, s[44:47], s68 offen offset:128\nbuffer_load_ushort v13, v67, s[44:47], s68 offen offset:384\nbuffer_load_ushort v14, v67, s[44:47], s68 offen offset:640\nbuffer_load_ushort v15, v67, s[44:47], s68 offen offset:896\ns_waitcnt vmcnt(0)\nv_lshl_or_b32 v8, v9, 16, v8\n"
"v_accvgpr_write_b32 a160, v8\nv_lshl_or_b32 v10, v11, 16, v10\nv_accvgpr_write_b32 a161, v10\nv_lshl_or_b32 v12, v13, 16, v12\nv_accvgpr_write_b32 a162, v12\nv_lshl_or_b32 v14, v15, 16, v14\nv_accvgpr_write_b32 a163, v14\nbuffer_load_ushort v8, v67, s[44:47], s89 offen offset:160\n"
"buffer_load_ushort v9, v67, s[44:47], s89 offen offset:416\nbuffer_load_ushort v10, v67, s[44:47], s89 offen offset:672\nbuffer_load_ushort v11, v67, s[44:47], s89 offen offset:928\nbuffer_load_ushort v12, v67, s[44:47], s68 offen offset:160\nbuffer_load_ushort v13, v67, s[44:47], s68 offen offset:416\nbuffer_load_ushort v14, v67, s[44:47], s68 offen offset:672\nbuffer_load_ushort v15, v67, s[44:47], s68 offen offset:928\ns_waitcnt vmcnt(0)\n"
"v_lshl_or_b32 v8, v9, 16, v8\nv_accvgpr_write_b32 a164, v8\nv_lshl_or_b32 v10, v11, 16, v10\nv_accvgpr_write_b32 a165, v10\nv_lshl_or_b32 v12, v13, 16, v12\nv_accvgpr_write_b32 a166, v12\nv_lshl_or_b32 v14, v15, 16, v14\nv_accvgpr_write_b32 a167, v14\n"
"buffer_load_ushort v8, v67, s[44:47], s89 offen offset:192\nbuffer_load_ushort v9, v67, s[44:47], s89 offen offset:448\nbuffer_load_ushort v10, v67, s[44:47], s89 offen offset:704\nbuffer_load_ushort v11, v67, s[44:47], s89 offen offset:960\nbuffer_load_ushort v12, v67, s[44:47], s68 offen offset:192\nbuffer_load_ushort v13, v67, s[44:47], s68 offen offset:448\nbuffer_load_ushort v14, v67, s[44:47], s68 offen offset:704\nbuffer_load_ushort v15, v67, s[44:47], s68 offen offset:960\n"
"s_waitcnt vmcnt(0)\nv_lshl_or_b32 v8, v9, 16, v8\nv_accvgpr_write_b32 a168, v8\nv_lshl_or_b32 v10, v11, 16, v10\nv_accvgpr_write_b32 a169, v10\nv_lshl_or_b32 v12, v13, 16, v12\nv_accvgpr_write_b32 a170, v12\nv_lshl_or_b32 v14, v15, 16, v14\n"
"v_accvgpr_write_b32 a171, v14\nbuffer_load_ushort v8, v67, s[44:47], s89 offen offset:224\nbuffer_load_ushort v9, v67, s[44:47], s89 offen offset:480\nbuffer_load_ushort v10, v67, s[44:47], s89 offen offset:736\nbuffer_load_ushort v11, v67, s[44:47], s89 offen offset:992\nbuffer_load_ushort v12, v67, s[44:47], s68 offen offset:224\nbuffer_load_ushort v13, v67, s[44:47], s68 offen offset:480\nbuffer_load_ushort v14, v67, s[44:47], s68 offen offset:736\n"
"buffer_load_ushort v15, v67, s[44:47], s68 offen offset:992\ns_waitcnt vmcnt(0)\nv_lshl_or_b32 v8, v9, 16, v8\nv_accvgpr_write_b32 a172, v8\nv_lshl_or_b32 v10, v11, 16, v10\nv_accvgpr_write_b32 a173, v10\nv_lshl_or_b32 v12, v13, 16, v12\nv_accvgpr_write_b32 a174, v12\n"
"v_lshl_or_b32 v14, v15, 16, v14\nv_accvgpr_write_b32 a175, v14\nQcvaa 0,112,0,24\nQmvaa 0,116,4\nQmvaa 0,120,8\nQmvaa 0,124,12\nQcvaa 4,128,0,24\nQmvaa 4,132,4\n"
"Qmvaa 4,136,8\nQmvaa 4,140,12\ns_nop 7\nQe 0\nQe 1\nQe 2\nQe 3\nQe 4\n"
"Qe 5\nQe 6\nQe 7\ns_nop 0\nQk 16,0,1\nQk 17,2,3\nQk 18,4,5\nQk 19,6,7\n"
"s_nop 1\nQmvav 80,144,16\nQmvav 84,148,16\nQmvav 88,152,16\nQmvav 92,156,16\nQmvav 96,160,16\nQmvav 100,164,16\nQmvav 104,168,16\n"
"Qmvav 108,172,16\nQmaav 176,204,16\nQcvaa 0,112,16,28\nQmvaa 0,116,20\nQmvaa 0,120,24\nQmvaa 0,124,28\nQcvaa 4,128,16,28\nQmvaa 4,132,20\n"
"Qmvaa 4,136,24\nQmvaa 4,140,28\ns_nop 7\nQe 0\nQe 1\nQe 2\nQe 3\nQe 4\n"
"Qe 5\nQe 6\nQe 7\ns_nop 0\nQk 16,0,1\nQk 17,2,3\nQk 18,4,5\nQk 19,6,7\n"
"s_nop 1\nQmvav 112,144,16\nQmvav 116,148,16\nQmvav 120,152,16\nQmvav 124,156,16\nQmvav 128,160,16\nQmvav 132,164,16\nQmvav 136,168,16\n"
"Qmvav 140,172,16\nQmaav 180,204,16\nQcvaa 0,112,32,32\nQmvaa 0,116,36\nQmvaa 0,120,40\nQmvaa 0,124,44\nQcvaa 4,128,32,32\nQmvaa 4,132,36\n"
"Qmvaa 4,136,40\nQmvaa 4,140,44\ns_nop 7\nQe 0\nQe 1\nQe 2\nQe 3\nQe 4\n"
"Qe 5\nQe 6\nQe 7\ns_nop 0\nQk 16,0,1\nQk 17,2,3\nQk 18,4,5\nQk 19,6,7\n"
"s_nop 1\nQmvav 144,144,16\nQmvav 148,148,16\nQmvav 152,152,16\nQmvav 156,156,16\nQmvav 160,160,16\nQmvav 164,164,16\nQmvav 168,168,16\n"
"Qmvav 172,172,16\nQmaav 184,204,16\nQcvaa 0,112,48,36\nQmvaa 0,116,52\nQmvaa 0,120,56\nQmvaa 0,124,60\nQcvaa 4,128,48,36\nQmvaa 4,132,52\n"
"Qmvaa 4,136,56\nQmvaa 4,140,60\ns_nop 7\nQe 0\nQe 1\nQe 2\nQe 3\nQe 4\n"
"Qe 5\nQe 6\nQe 7\ns_nop 0\nQk 16,0,1\nQk 17,2,3\nQk 18,4,5\nQk 19,6,7\n"
"s_nop 1\nQmvav 176,144,16\nQmvav 180,148,16\nQmvav 184,152,16\nQmvav 188,156,16\nQmvav 192,160,16\nQmvav 196,164,16\nQmvav 200,168,16\n"
"Qmvav 204,172,16\nQmaav 188,204,16\nQcvaa 0,112,64,40\nQmvaa 0,116,68\nQmvaa 0,120,72\nQmvaa 0,124,76\nQcvaa 4,128,64,40\nQmvaa 4,132,68\n"
"Qmvaa 4,136,72\nQmvaa 4,140,76\ns_nop 7\nQe 0\nQe 1\nQe 2\nQe 3\nQe 4\n"
"Qe 5\nQe 6\nQe 7\ns_nop 0\nQk 16,0,1\nQk 17,2,3\nQk 18,4,5\nQk 19,6,7\n"
"s_nop 1\nQmvav 208,144,16\nQmvav 212,148,16\nQmvav 216,152,16\nQmvav 220,156,16\nQmvav 224,160,16\nQmvav 228,164,16\nQmvav 232,168,16\n"
"Qmvav 236,172,16\nQmaav 192,204,16\nQcvaa 0,112,80,44\nQmvaa 0,116,84\nQmvaa 0,120,88\nQmvaa 0,124,92\nQcvaa 4,128,80,44\nQmvaa 4,132,84\n"
"Qmvaa 4,136,88\nQmvaa 4,140,92\ns_nop 7\nQe 0\nQe 1\nQe 2\nQe 3\nQe 4\n"
"Qe 5\nQe 6\nQe 7\ns_nop 0\nQk 16,0,1\nQk 17,2,3\nQk 18,4,5\nQk 19,6,7\n"
"s_nop 1\nQmvav 240,144,16\nQmvav 244,148,16\nQmvav 248,152,16\nQmvav 252,156,16\nQmaav 208,160,16\nQmaav 212,164,16\nQmaav 216,168,16\n"
"Qmaav 220,172,16\nQmaav 196,204,16\nQcvaa 0,112,96,48\nQmvaa 0,116,100\nQmvaa 0,120,104\nQmvaa 0,124,108\nQcvaa 4,128,96,48\nQmvaa 4,132,100\n"
"Qmvaa 4,136,104\nQmvaa 4,140,108\ns_nop 7\nQe 0\nQe 1\nQe 2\nQe 3\nQe 4\n"
"Qe 5\nQe 6\nQe 7\ns_nop 0\nQk 16,0,1\nQk 17,2,3\nQk 18,4,5\nQk 19,6,7\n"
"s_nop 1\nQmaav 224,144,16\nQmaav 228,148,16\nQmaav 232,152,16\nQmaav 236,156,16\nQmaav 240,160,16\nQmaav 244,164,16\nQmaav 248,168,16\n"
"Qmaav 252,172,16\nQmaav 200,204,16\ns_add_u32 s89, s89, 8192\ns_add_u32 s66, s66, 1\ns_cmp_lt_u32 s66, 32\ns_cbranch_scc1 .Lfb2\ns_nop 7\nv_accvgpr_read_b32 v0, a176\n"
"v_accvgpr_read_b32 v4, a180\nv_accvgpr_read_b32 v8, a184\nv_accvgpr_read_b32 v12, a188\nv_accvgpr_read_b32 v16, a192\nv_accvgpr_read_b32 v20, a196\nv_accvgpr_read_b32 v24, a200\ns_cmp_eq_u32 s67, 0\ns_cbranch_scc1 .Lfbret0\n"
"s_branch .Lfbret1\n.Lf2_w:\ns_mov_b32 s97, 0\n.Lf2_l:\ns_sleep 1\nbuffer_load_dword v78, v76, s[24:27], 0 offen sc1\ns_waitcnt vmcnt(0)\ns_add_u32 s97, s97, 1\n"
"s_cmp_gt_u32 s97, 1048576\ns_cbranch_scc1 .Lf2_r\nv_sub_u32 v66, v78, v65\nv_cmp_gt_u32 vcc, 2, v66\ns_and_b64 vcc, vcc, s[28:29]\ns_cbranch_scc1 .Lf2_l\ns_branch .Lf2_r\n.Lf3_w:\n"
"s_mov_b32 s97, 0\n.Lf3_l:\ns_sleep 1\nbuffer_load_dword v78, v76, s[24:27], 0 offen sc1\ns_waitcnt vmcnt(0)\ns_add_u32 s97, s97, 1\ns_cmp_gt_u32 s97, 1048576\ns_cbranch_scc1 .Lf3_r\n"
"v_sub_u32 v66, v78, v65\nv_cmp_gt_u32 vcc, 3, v66\ns_and_b64 vcc, vcc, s[28:29]\ns_cbranch_scc1 .Lf3_l\ns_branch .Lf3_r\n.Lx0_w:\ns_mov_b32 s97, 0\n.Lx0_l:\n"
"s_sleep 1\nbuffer_load_dword v78, v76, s[24:27], 0 offen sc1\ns_waitcnt vmcnt(0)\ns_add_u32 s97, s97, 1\ns_cmp_gt_u32 s97, 1048576\ns_cbranch_scc1 .Lx0_r\nv_sub_u32 v66, v78, v65\nv_cmp_gt_u32 vcc, 1, v66\n"
"s_and_b64 vcc, vcc, s[28:29]\ns_cbranch_scc1 .Lx0_l\ns_branch .Lx0_r\n.Lx1_w:\ns_mov_b32 s97, 0\n.Lx1_l:\ns_sleep 1\nbuffer_load_dword v78, v76, s[24:27], 0 offen sc1\n"
"s_waitcnt vmcnt(0)\ns_add_u32 s97, s97, 1\ns_cmp_gt_u32 s97, 1048576\ns_cbranch_scc1 .Lx1_r\nv_sub_u32 v66, v78, v65\nv_cmp_gt_u32 vcc, 1, v66\ns_and_b64 vcc, vcc, s[28:29]\ns_cbranch_scc1 .Lx1_l\n"
"s_branch .Lx1_r\n.Lexit:\n"
  :: "s"((unsigned)kp), "s"((unsigned)(kp >> 32)), "s"((unsigned)vp), "s"((unsigned)(vp >> 32)), "s"((unsigned)xp), "s"((unsigned)(xp >> 32)),
     "s"((unsigned)wp), "s"((unsigned)(wp >> 32)), "s"((unsigned)op), "s"((unsigned)(op >> 32)), "s"(ldsb), "s"(wid), "s"(head0),
     "s"((unsigned)dp), "s"((unsigned)(dp >> 32)), "s"((unsigned)wop), "s"((unsigned)(wop >> 32)), "s"((unsigned)yp), "s"((unsigned)(yp >> 32)),
     "s"(hg), "s"(b * 8u + hg)
  : "v0", "v1", "v2", "v3", "v4", "v5", "v6", "v7", "v8", "v9", "v10", "v11", "v12", "v13", "v14", "v15", "v16", "v17", "v18", "v19", "v20", "v21", "v22", "v23", "v24", "v25", "v26", "v27", "v28", "v29", "v30", "v31", "v32", "v33", "v34", "v35", "v36", "v37", "v38", "v39", "v40", "v41", "v42", "v43", "v44", "v45", "v46", "v47", "v48", "v49", "v50", "v51", "v52", "v53", "v54", "v55", "v56", "v57", "v58", "v59", "v60", "v61", "v62", "v63", "v64", "v65", "v66", "v67", "v68", "v69", "v70", "v71", "v72", "v73", "v74", "v75", "v76", "v77", "v78", "v79", "v80", "v81", "v82", "v83", "v84", "v85", "v86", "v87", "v88", "v89", "v90", "v91", "v92", "v93", "v94", "v95", "v96", "v97", "v98", "v99", "v100", "v101", "v102", "v103", "v104", "v105", "v106", "v107", "v108", "v109", "v110", "v111", "v112", "v113", "v114", "v115", "v116", "v117", "v118", "v119", "v120", "v121", "v122", "v123", "v124", "v125", "v126", "v127", "v128", "v129", "v130", "v131", "v132", "v133", "v134", "v135", "v136", "v137", "v138", "v139", "v140", "v141", "v142", "v143", "v144", "v145", "v146", "v147", "v148", "v149", "v150", "v151", "v152", "v153", "v154", "v155", "v156", "v157", "v158", "v159", "v160", "v161", "v162", "v163", "v164", "v165", "v166", "v167", "v168", "v169", "v170", "v171", "v172", "v173", "v174", "v175", "v176", "v177", "v178", "v179", "v180", "v181", "v182", "v183", "v184", "v185", "v186", "v187", "v188", "v189", "v190", "v191", "v192", "v193", "v194", "v195", "v196", "v197", "v198", "v199", "v200", "v201", "v202", "v203", "v204", "v205", "v206", "v207", "v208", "v209", "v210", "v211", "v212", "v213", "v214", "v215", "v216", "v217", "v218", "v219", "v220", "v221", "v222", "v223", "v224", "v225", "v226", "v227", "v228", "v229", "v230", "v231", "v232", "v233", "v234", "v235", "v236", "v237", "v238", "v239", "v240", "v241", "v242", "v243", "v244", "v245", "v246", "v247", "v248", "v249", "v250", "v251", "v252", "v253", "v254", "v255", "a0", "a1", "a2", "a3", "a4", "a5", "a6", "a7", "a8", "a9", "a10", "a11", "a12", "a13", "a14", "a15", "a16", "a17", "a18", "a19", "a20", "a21", "a22", "a23", "a24", "a25", "a26", "a27", "a28", "a29", "a30", "a31", "a32", "a33", "a34", "a35", "a36", "a37", "a38", "a39", "a40", "a41", "a42", "a43", "a44", "a45", "a46", "a47", "a48", "a49", "a50", "a51", "a52", "a53", "a54", "a55", "a56", "a57", "a58", "a59", "a60", "a61", "a62", "a63", "a64", "a65", "a66", "a67", "a68", "a69", "a70", "a71", "a72", "a73", "a74", "a75", "a76", "a77", "a78", "a79", "a80", "a81", "a82", "a83", "a84", "a85", "a86", "a87", "a88", "a89", "a90", "a91", "a92", "a93", "a94", "a95", "a96", "a97", "a98", "a99", "a100", "a101", "a102", "a103", "a104", "a105", "a106", "a107", "a108", "a109", "a110", "a111", "a112", "a113", "a114", "a115", "a116", "a117", "a118", "a119", "a120", "a121", "a122", "a123", "a124", "a125", "a126", "a127", "a128", "a129", "a130", "a131", "a132", "a133", "a134", "a135", "a136", "a137", "a138", "a139", "a140", "a141", "a142", "a143", "a144", "a145", "a146", "a147", "a148", "a149", "a150", "a151", "a152", "a153", "a154", "a155", "a156", "a157", "a158", "a159", "a160", "a161", "a162", "a163", "a164", "a165", "a166", "a167", "a168", "a169", "a170", "a171", "a172", "a173", "a174", "a175", "a176", "a177", "a178", "a179", "a180", "a181", "a182", "a183", "a184", "a185", "a186", "a187", "a188", "a189", "a190", "a191", "a192", "a193", "a194", "a195", "a196", "a197", "a198", "a199", "a200", "a201", "a202", "a203", "a204", "a205", "a206", "a207", "a208", "a209", "a210", "a211", "a212", "a213", "a214", "a215", "a216", "a217", "a218", "a219", "a220", "a221", "a222", "a223", "a224", "a225", "a226", "a227", "a228", "a229", "a230", "a231", "a232", "a233", "a234", "a235", "a236", "a237", "a238", "a239", "a240", "a241", "a242", "a243", "a244", "a245", "a246", "a247", "a248", "a249", "a250", "a251", "a252", "a253", "a254", "a255", "s24", "s25", "s26", "s27", "s28", "s29", "s30", "s31", "s40", "s41", "s42", "s43", "s44", "s45", "s46", "s47", "s48", "s49", "s50", "s51", "s52", "s53", "s54", "s55", "s56", "s57", "s58", "s59", "s60", "s61", "s62", "s63", "s64", "s65", "s66", "s67", "s68", "s69", "s70", "s71", "s72", "s73", "s74", "s75", "s76", "s77", "s78", "s79", "s80", "s81", "s82", "s83", "s84", "s85", "s86", "s87", "s88", "s89", "s90", "s91", "s92", "s93", "s94", "s95", "s96", "s97", "s98", "s99", "vcc", "scc", "memory");
}

extern "C" void kernel_launch(void* const* d_in, const int* in_sizes, int n_in, void* d_out, int out_size, void* d_ws, size_t ws_size, hipStream_t stream) {
  typedef const unsigned short* P;
  (void)hipFuncSetAttribute((const void*)mha2_asm, hipFuncAttributeMaxDynamicSharedMemorySize, 163840);
  hipLaunchKernelGGL(mha2_asm, dim3(256), dim3(256), 163840, stream, (P)d_in[0], (P)d_in[5], (P)d_in[3], (P)d_in[8], (P)d_in[1], (P)d_in[6], (P)d_in[2], (P)d_in[7],
                     (P)d_in[4], (P)d_in[9], (char*)d_ws, (unsigned short*)d_out);
}
```

```cpp
#include <hip/hip_runtime.h>
#include <stdint.h>

__global__ __launch_bounds__(256, 1) __attribute__((aligned(4096)))
void mha2_asm(const unsigned short* __restrict__ X0, const unsigned short* __restrict__ X1, const unsigned short* __restrict__ W0, const unsigned short* __restrict__ W1,
              const unsigned short* __restrict__ K0, const unsigned short* __restrict__ K1,
              const unsigned short* __restrict__ V0, const unsigned short* __restrict__ V1,
              const unsigned short* __restrict__ WO0, const unsigned short* __restrict__ WO1, char* ws, unsigned short* out) {
  extern __shared__ __attribute__((aligned(16))) char lds[];
  const unsigned c = blockIdx.x;
  const unsigned xcd = c & 7u, idx = c >> 3;
  const unsigned hg = idx & 7u, bb = xcd * 4u + (idx >> 3);
  const unsigned bk = bb >> 4, b = bb & 15u;
  const unsigned wid = __builtin_amdgcn_readfirstlane(threadIdx.x >> 6);
  const unsigned head0 = (hg >> 2) * 16u + (hg & 3u) * 2u + (wid >> 1);
  const unsigned long long kp = (unsigned long long)(bk ? K1 : K0) + ((unsigned long long)b * 32u + head0) * 262144ull - 1024ull;
  const unsigned long long vp = (unsigned long long)(bk ? V1 : V0) + ((unsigned long long)b * 32u + head0) * 262144ull - 1024ull;
  const unsigned long long xp = (unsigned long long)(bk ? X1 : X0) + (unsigned long long)b * (224u * 1280u);
  const unsigned long long wp = (unsigned long long)(bk ? W1 : W0);
  const unsigned long long op = (unsigned long long)ws + 0ull + (unsigned long long)bb * 2295808ull;
  const unsigned ldsb = (unsigned)(size_t)lds;
  const unsigned long long wop = (unsigned long long)(bk ? WO1 : WO0) - 4096ull;
  const unsigned long long yp = (unsigned long long)out + (unsigned long long)b * (224ull * 2560ull) + bk * 1280u + (hg & 3u) * 320u;
  const unsigned long long dp = (unsigned long long)ws + (100ull << 20) + (c * 4u + wid) * 256u;
  asm volatile(
".macro Qta d,v,o\nds_read_b64_tr_b16 a[\\d:\\d+1], v\\v offset:\\o\n.endm\n.macro Qdv d,v,o\nds_read_b128 v[\\d:\\d+3], v\\v offset:\\o\n.endm\n.macro Qda d,v,o\nds_read_b128 a[\\d:\\d+3], v\\v offset:\\o\n"
".endm\n.macro Qzvav d,a,b\nv_mfma_f32_16x16x32_bf16 v[\\d:\\d+3], a[\\a:\\a+3], v[\\b:\\b+3], 0\n.endm\n.macro Qmvaa d,a,b\nv_mfma_f32_16x16x32_bf16 v[\\d:\\d+3], a[\\a:\\a+3], a[\\b:\\b+3], v[\\d:\\d+3]\n.endm\n.macro Qzaav d,a,b\n"
"v_mfma_f32_16x16x32_bf16 a[\\d:\\d+3], a[\\a:\\a+3], v[\\b:\\b+3], 0\n.endm\n.macro Qmaaa d,a,b\nv_mfma_f32_16x16x32_bf16 a[\\d:\\d+3], a[\\a:\\a+3], a[\\b:\\b+3], a[\\d:\\d+3]\n.endm\n.macro Qmvav d,a,b\nv_mfma_f32_16x16x32_bf16 v[\\d:\\d+3], a[\\a:\\a+3], v[\\b:\\b+3], v[\\d:\\d+3]\n.endm\n"
".macro Qmaav d,a,b\nv_mfma_f32_16x16x32_bf16 a[\\d:\\d+3], a[\\a:\\a+3], v[\\b:\\b+3], a[\\d:\\d+3]\n.endm\n.macro Qk d,a,b\nv_cvt_pk_bf16_f32 v\\d, v\\a, v\\b\n.endm\n.macro Qzvaa d,a,b\nv_mfma_f32_16x16x32_bf16 v[\\d:\\d+3], a[\\a:\\a+3], a[\\b:\\b+3], 0\n"
".endm\n.macro Qa d,a,b\nv_add_f32 v\\d, v\\a, v\\b\n.endm\n.macro Qe d\nv_exp_f32 v\\d, v\\d\n.endm\n.macro Qcvaa d,a,b,c\n"
"v_mfma_f32_16x16x32_bf16 v[\\d:\\d+3], a[\\a:\\a+3], a[\\b:\\b+3], v[\\c:\\c+3]\n.endm\n.macro Qzvva d,a,b\nv_mfma_f32_16x16x32_bf16 v[\\d:\\d+3], v[\\a:\\a+3], a[\\b:\\b+3], 0\n.endm\n.macro Qzava d,a,b\nv_mfma_f32_16x16x32_bf16 a[\\d:\\d+3], v[\\a:\\a+3], a[\\b:\\b+3], 0\n.endm\n"
".macro Qmvva d,a,b\nv_mfma_f32_16x16x32_bf16 v[\\d:\\d+3], v[\\a:\\a+3], a[\\b:\\b+3], v[\\d:\\d+3]\n.endm\n.macro Qmava d,a,b\nv_mfma_f32_16x16x32_bf16 a[\\d:\\d+3], v[\\a:\\a+3], a[\\b:\\b+3], a[\\d:\\d+3]\n.endm\n.macro Qzvvv d,a,b\nv_mfma_f32_16x16x32_bf16 v[\\d:\\d+3], v[\\a:\\a+3], v[\\b:\\b+3], 0\n"
".endm\n.p2align 7\nv_mbcnt_lo_u32_b32 v0, -1, 0\nv_mbcnt_hi_u32_b32 v0, -1, v0\nv_lshlrev_b32 v60, 4, v0\ns_add_u32 m0, %10, 160768\ns_getpc_b64 s[56:57]\n.Lpc0:\n"
"s_and_b32 s56, s56, 0xffffff80\ns_and_b32 s57, s57, 0xffff\ns_mov_b32 s58, .Lend-.Lpc0\ns_mov_b32 s59, 0x20000\ns_and_b32 s94, %20, 31\ns_lshl_b32 s94, s94, 2\ns_add_u32 s94, s94, %11\ns_lshl_b32 s94, s94, 10\n"
"buffer_load_dwordx4 v60, s[56:59], s94 offen lds\ns_mov_b32 s52, %6\ns_and_b32 s53, %7, 0xffff\ns_mov_b32 s54, 5242880\ns_mov_b32 s55, 0x20000\ns_mov_b32 s48, %4\ns_and_b32 s49, %5, 0xffff\ns_mov_b32 s50, 286720\n"
"s_mov_b32 s51, 0x20000\ns_mov_b32 s70, %11\ns_and_b32 s92, s70, 1\ns_lshr_b32 s93, s70, 1\nv_lshrrev_b32 v2, 4, v0\ns_lshl_b32 s68, s92, 18\nv_and_b32 v4, 3, v0\nv_lshlrev_b32 v4, 4, v4\n"
"v_and_b32 v1, 1, v2\nv_lshl_add_u32 v4, v1, 6, v4\nv_lshrrev_b32 v1, 5, v0\nv_lshl_add_u32 v4, v1, 15, v4\nv_bfe_u32 v1, v0, 2, 2\nv_lshl_add_u32 v4, v1, 13, v4\nv_add_u32 v62, s68, v4\ns_lshl_b32 s87, %12, 8\n"
"s_mul_i32 s62, s70, 4128\ns_add_u32 s62, s62, %10\ns_add_u32 s62, s62, 33024\ns_add_u32 m0, s62, 0\ns_add_u32 s66, s87, 0\ns_nop 0\nbuffer_load_dwordx4 v62, s[52:55], s66 offen lds\ns_add_u32 s66, s87, 64504\n"
"s_nop 0\nbuffer_load_dwordx4 v62, s[52:55], s66 offen offset:1032 lds\ns_add_u32 s66, s87, 129008\ns_nop 0\nbuffer_load_dwordx4 v62, s[52:55], s66 offen offset:2064 lds\ns_add_u32 s66, s87, 193512\ns_nop 0\nbuffer_load_dwordx4 v62, s[52:55], s66 offen offset:3096 lds\n"
"s_lshl_b32 s69, s92, 1\nv_lshrrev_b32 v3, 5, v0\nv_and_b32 v4, 3, v0\nv_xor_b32 v4, s69, v4\nv_lshlrev_b32 v4, 4, v4\nv_lshl_add_u32 v4, v3, 6, v4\nv_bfe_u32 v1, v0, 2, 3\nv_mul_u32_u24 v64, 1280, v1\n"
"v_add_u32 v64, v64, v4\ns_mul_i32 s86, s93, 143360\ns_mul_i32 s68, s92, 10240\ns_add_u32 s86, s86, s68\ns_mul_i32 s63, s92, 14336\ns_mul_i32 s68, s93, 7168\ns_add_u32 s63, s63, s68\ns_add_u32 s63, s63, %10\n"
"s_add_u32 s63, s63, 99072\ns_add_u32 m0, s63, 33024\ns_add_u32 s97, s86, 0\ns_nop 0\nbuffer_load_dwordx4 v64, s[48:51], s97 offen lds\ns_add_u32 s97, s86, 19456\ns_nop 0\nbuffer_load_dwordx4 v64, s[48:51], s97 offen offset:1024 lds\n"
"s_add_u32 s97, s86, 38912\ns_nop 0\nbuffer_load_dwordx4 v64, s[48:51], s97 offen offset:2048 lds\ns_add_u32 s97, s86, 58368\ns_nop 0\nbuffer_load_dwordx4 v64, s[48:51], s97 offen offset:3072 lds\ns_add_u32 m0, s63, 37120\ns_add_u32 s97, s86, 81920\n"
"s_nop 0\nbuffer_load_dwordx4 v64, s[48:51], s97 offen lds\ns_add_u32 s97, s86, 101376\ns_nop 0\nbuffer_load_dwordx4 v64, s[48:51], s97 offen offset:1024 lds\ns_add_u32 s97, s86, 120832\ns_nop 0\nbuffer_load_dwordx4 v64, s[48:51], s97 offen offset:2048 lds\n"
"s_add_u32 m0, s62, 16512\ns_add_u32 s66, s87, 128\ns_nop 0\nbuffer_load_dwordx4 v62, s[52:55], s66 offen lds\ns_mov_b32 s24, %8\ns_and_b32 s25, %9, 0xffff\ns_mov_b32 s26, 2295808\ns_mov_b32 s27, 0x20000\n"
"v_and_b32 v5, 7, v0\nv_lshlrev_b32 v5, 7, v5\ns_add_u32 s66, s87, 64632\ns_nop 0\nbuffer_load_dwordx4 v62, s[52:55], s66 offen offset:1032 lds\nv_add_u32 v76, 2293760, v5\ns_lshl_b32 s68, s70, 2\ns_add_u32 s68, s68, 4\n"
"v_bfe_u32 v5, v0, 3, 1\nv_mad_u32_u24 v76, v5, s68, v76\nbuffer_load_dword v65, v76, s[24:27], 0 offen sc1\ns_add_u32 s66, s87, 129136\ns_nop 0\nbuffer_load_dwordx4 v62, s[52:55], s66 offen offset:2064 lds\ns_mov_b32 s40, %0\ns_and_b32 s41, %1, 0xffff\n"
"s_mov_b32 s42, 2360320\ns_mov_b32 s43, 0x20000\ns_mov_b32 s44, %2\ns_and_b32 s45, %3, 0xffff\ns_add_u32 s66, s87, 193640\ns_nop 0\nbuffer_load_dwordx4 v62, s[52:55], s66 offen offset:3096 lds\ns_mov_b32 s46, 2360320\n"
"s_mov_b32 s47, 0x20000\ns_mov_b32 s56, %8\ns_and_b32 s57, %9, 0xffff\ns_mov_b32 s58, 0\ns_mov_b32 s59, 0x20000\ns_mov_b64 exec, 0\ns_cmp_eq_u32 s70, 0\ns_cbranch_scc1 .Lpg0\n"
"s_cmp_eq_u32 s70, 1\ns_cbranch_scc1 .Lpg1\ns_cmp_eq_u32 s70, 2\ns_cbranch_scc1 .Lpg2\ns_branch .Lpg3\n.Ltr0:\ns_mov_b64 exec, -1\ns_mov_b32 s60, 0\n"
"s_mov_b32 s90, 896\ns_mov_b32 s67, 0\ns_mov_b32 s72, 0x3e0293ee\ns_lshl_b32 s68, s92, 12\ns_add_u32 s88, s68, 128\ns_add_u32 m0, s63, 0\ns_add_u32 s97, s86, 128\ns_nop 0\n"
"buffer_load_dwordx4 v64, s[48:51], s97 offen lds\nv_lshl_add_u32 v4, v1, 8, v4\nv_add_u32 v69, s88, v4\nv_and_b32 v4, 1, v0\nv_lshlrev_b32 v4, 4, v4\nv_lshl_add_u32 v4, v2, 5, v4\nv_bfe_u32 v1, v0, 1, 3\ns_add_u32 s97, s86, 19584\n"
"s_nop 0\nbuffer_load_dwordx4 v64, s[48:51], s97 offen offset:1024 lds\nv_lshl_add_u32 v4, v1, 8, v4\nv_add_u32 v72, s88, v4\ns_lshl_b32 s69, s93, 14\ns_add_u32 s69, s69, %10\ns_add_u32 s61, s69, s68\nv_bfe_u32 v3, v0, 3, 1\n"
"s_add_u32 s97, s86, 39040\ns_nop 0\nbuffer_load_dwordx4 v64, s[48:51], s97 offen offset:2048 lds\nv_lshlrev_b32 v4, 1, v3\nv_xor_b32 v4, v4, v2\nv_and_b32 v1, 7, v0\nv_lshl_add_u32 v4, v1, 2, v4\nv_lshlrev_b32 v4, 4, v4\n"
"v_lshl_add_u32 v1, v3, 12, v4\ns_add_u32 s97, s86, 58496\ns_nop 0\nbuffer_load_dwordx4 v64, s[48:51], s97 offen offset:3072 lds\nv_add_u32 v68, s69, v1\ns_mul_i32 s68, s92, 7168\ns_add_u32 s68, s68, %10\ns_add_u32 s68, s68, 99072\n"
"v_mul_u32_u24 v1, 14336, v3\nv_add3_u32 v63, v1, v4, s68\ns_add_u32 m0, s63, 4096\ns_add_u32 s97, s86, 82048\ns_nop 0\nbuffer_load_dwordx4 v64, s[48:51], s97 offen lds\nv_lshrrev_b32 v3, 5, v0\nv_and_b32 v4, 31, v0\n"
"v_lshlrev_b32 v4, 3, v4\nv_lshl_add_u32 v4, v3, 12, v4\nv_add_u32 v4, 8192, v4\nv_add_u32 v70, s69, v4\ns_add_u32 s97, s86, 101504\ns_nop 0\nbuffer_load_dwordx4 v64, s[48:51], s97 offen offset:1024 lds\nv_add_u32 v71, 66048, v70\n"
"s_mul_i32 s68, s93, 8256\ns_add_u32 s68, s68, %10\ns_add_u32 s68, s68, 33024\nv_mul_u32_u24 v3, 1032, v2\nv_and_b32 v4, 15, v0\ns_add_u32 s97, s86, 120960\ns_nop 0\nbuffer_load_dwordx4 v64, s[48:51], s97 offen offset:2048 lds\n"
"v_lshlrev_b32 v4, 4, v4\nv_add3_u32 v61, v3, v4, s68\ns_lshl_b32 s68, s70, 2\ns_add_u32 s69, %10, 131840\ns_add_u32 s68, s68, s69\nv_mov_b32 v73, s68\ns_add_u32 m0, s62, 33024\ns_add_u32 s66, s87, 524288\n"
"s_nop 0\nbuffer_load_dwordx4 v62, s[52:55], s66 offen lds\nv_and_b32 v3, 3, v0\nv_lshl_add_u32 v74, v3, 2, s69\nv_mov_b32 v75, 0\nds_write_b32 v73, v75\ns_waitcnt lgkmcnt(0)\ns_mul_i32 s91, s70, 7168\n"
"s_add_u32 s66, s87, 588792\ns_nop 0\nbuffer_load_dwordx4 v62, s[52:55], s66 offen offset:1032 lds\ns_add_u32 s91, s91, %10\ns_add_u32 s91, s91, 98304\ns_lshl_b32 s96, s70, 11\ns_add_u32 s96, s96, %10\nv_and_b32 v3, 1, v0\n"
"v_mul_u32_u24 v3, 0xaaaa, v3\ns_add_u32 s66, s87, 653296\ns_nop 0\nbuffer_load_dwordx4 v62, s[52:55], s66 offen offset:2064 lds\nv_or_b32 v77, 0x4444, v3\ns_and_b32 s68, %19, 4\ns_lshl_b32 s28, 15, s68\ns_mov_b32 s29, 0\n"
"s_mov_b32 s71, 0\ns_and_b32 s68, %19, 3\ns_add_u32 s66, s87, 717800\ns_nop 0\nbuffer_load_dwordx4 v62, s[52:55], s66 offen offset:3096 lds\ns_lshl_b32 s99, 3072, s68\ns_or_b32 s99, s99, 0x302\ns_mov_b32 s94, 0x7ffff000\n"
"s_mul_i32 s68, %20, 40960\ns_mul_i32 s69, s93, 20480\ns_add_u32 s68, s68, s69\ns_mov_b64 exec, 0\ns_cmp_eq_u32 s70, 0\ns_cbranch_scc1 .Lpg4\ns_cmp_eq_u32 s70, 1\ns_cbranch_scc1 .Lpg5\n"
"s_cmp_eq_u32 s70, 2\ns_cbranch_scc1 .Lpg6\ns_branch .Lpg7\n.Ltr9:\ns_mov_b64 exec, -1\ns_lshl_b32 s69, s92, 10\ns_add_u32 s68, s68, s69\ns_add_u32 s68, s68, 2048\n"
"s_add_u32 s94, %6, s68\ns_addc_u32 s95, %7, 0\ns_and_b32 s95, s95, 0xffff\ns_add_u32 m0, s62, 49536\ns_add_u32 s66, s87, 524416\ns_nop 0\nbuffer_load_dwordx4 v62, s[52:55], s66 offen lds\ns_mov_b32 s75, 17408\n"
"s_add_u32 s66, s87, 588920\ns_nop 0\nbuffer_load_dwordx4 v62, s[52:55], s66 offen offset:1032 lds\ns_add_u32 s66, s87, 653424\ns_nop 0\nbuffer_load_dwordx4 v62, s[52:55], s66 offen offset:2064 lds\ns_add_u32 s66, s87, 717928\ns_nop 0\n"
"buffer_load_dwordx4 v62, s[52:55], s66 offen offset:3096 lds\ns_mov_b64 exec, 0\ns_cmp_eq_u32 s70, 0\ns_cbranch_scc1 .Lpg8\ns_cmp_eq_u32 s70, 1\ns_cbranch_scc1 .Lpg9\ns_cmp_eq_u32 s70, 2\ns_cbranch_scc1 .Lpg10\n"
"s_branch .Lpg11\n.Ltr10:\ns_mov_b64 exec, -1\n.Lround:\ns_cmp_lg_u32 s67, 0\ns_cselect_b64 vcc, -1, 0\ns_waitcnt vmcnt(15)\ns_barrier\n"
"Qta 112,61,0\nQta 114,61,512\nQdv 24,63,33024\nQta 116,61,4128\nQta 118,61,4640\nQdv 28,63,34048\nQta 120,61,8\nQta 122,61,520\n"
"Qdv 32,63,35072\nQta 124,61,4136\nQta 126,61,4648\nQdv 36,63,36096\nQta 128,61,256\nQta 130,61,768\nQdv 40,63,37120\nQta 132,61,4384\n"
"Qta 134,61,4896\nQdv 44,63,38144\nQta 136,61,264\nQta 138,61,776\nQdv 48,63,39168\nQta 140,61,4392\nQta 142,61,4904\nQda 176,63,33536\n"
"Qda 180,63,34560\nQda 184,63,35584\nQda 188,63,36608\nQda 192,63,37632\nQda 196,63,38656\nQda 200,63,39680\ns_waitcnt lgkmcnt(6)\nQzvav 80,112,24\n"
"Qmvaa 80,116,176\ns_add_u32 s68, s71, 0\nQzvav 84,120,24\ns_cbranch_vccz .Lst27\nbuffer_store_dwordx4 a[0:3], v60, s[56:59], s68 offen offset:0 sc1\n.Lst27:\nQmvaa 84,124,176\nQzvav 88,128,24\n"
"s_add_u32 s68, s71, 14336\nQmvaa 88,132,176\ns_cbranch_vccz .Lst26\nbuffer_store_dwordx4 a[4:7], v60, s[56:59], s68 offen offset:0 sc1\n.Lst26:\nQzvav 92,136,24\nQmvaa 92,140,176\ns_waitcnt lgkmcnt(0)\n"
"Qzvav 112,112,28\ns_waitcnt vmcnt(15)\ns_barrier\nQmvaa 112,116,180\nQta 144,61,16512\ns_add_u32 m0, s62, 0\ns_add_u32 s69, s87, 1048576\nQzvav 116,120,28\n"
"buffer_load_dwordx4 v62, s[52:55], s69 offen lds\nQmvaa 116,124,180\nQta 146,61,17024\nQzvav 120,128,28\nQmvaa 120,132,180\nQta 148,61,20640\ns_add_u32 s69, s87, 1113080\nQzvav 124,136,28\n"
"buffer_load_dwordx4 v62, s[52:55], s69 offen offset:1032 lds\nQmvaa 124,140,180\nQta 150,61,21152\nQzvav 144,112,32\nQmvaa 144,116,184\nQta 152,61,16520\ns_add_u32 s69, s87, 1177584\nQzvav 148,120,32\n"
"buffer_load_dwordx4 v62, s[52:55], s69 offen offset:2064 lds\nQmvaa 148,124,184\nQta 154,61,17032\nQzvav 152,128,32\nQmvaa 152,132,184\nQta 156,61,20648\ns_add_u32 s69, s87, 1242088\nQzvav 156,136,32\n"
"buffer_load_dwordx4 v62, s[52:55], s69 offen offset:3096 lds\nQmvaa 156,140,184\nQta 158,61,21160\nQzvav 176,112,36\nQmvaa 176,116,188\nQta 160,61,16768\ns_add_u32 m0, s63, 33024\ns_add_u32 s88, s86, 256\n"
"Qzvav 180,120,36\nbuffer_load_dwordx4 v64, s[48:51], s88 offen lds\nQmvaa 180,124,188\nQta 162,61,17280\nQzvav 184,128,36\nQmvaa 184,132,188\nQta 164,61,20896\ns_add_u32 s88, s86, 19712\n"
"Qzvav 188,136,36\nbuffer_load_dwordx4 v64, s[48:51], s88 offen offset:1024 lds\nQmvaa 188,140,188\nQta 166,61,21408\nQzvav 208,112,40\nQmvaa 208,116,192\nQta 168,61,16776\ns_add_u32 s88, s86, 39168\n"
"Qzvav 212,120,40\nbuffer_load_dwordx4 v64, s[48:51], s88 offen offset:2048 lds\nQmvaa 212,124,192\nQta 170,61,17288\nQzvav 216,128,40\nQmvaa 216,132,192\nQta 172,61,20904\ns_add_u32 s88, s86, 58624\n"
"Qzvav 220,136,40\nbuffer_load_dwordx4 v64, s[48:51], s88 offen offset:3072 lds\nQmvaa 220,140,192\nQta 174,61,21416\nQzvav 240,112,44\nQmvaa 240,116,196\ns_add_u32 m0, s63, 37120\ns_add_u32 s88, s86, 82176\n"
"Qzvav 244,120,44\nbuffer_load_dwordx4 v64, s[48:51], s88 offen lds\nQmvaa 244,124,196\nQzvav 248,128,44\nQmvaa 248,132,196\ns_add_u32 s88, s86, 101632\nQzvav 252,136,44\nbuffer_load_dwordx4 v64, s[48:51], s88 offen offset:1024 lds\n"
"Qmvaa 252,140,196\nQzaav 224,112,48\nQmaaa 224,116,200\ns_add_u32 s88, s86, 121088\nQzaav 228,120,48\nbuffer_load_dwordx4 v64, s[48:51], s88 offen offset:2048 lds\nQmaaa 228,124,200\nQzaav 232,128,48\n"
"Qmaaa 232,132,200\nQzaav 236,136,48\nQmaaa 236,140,200\ns_waitcnt lgkmcnt(0)\nQzvav 96,144,24\nQmvaa 96,148,176\ns_add_u32 s68, s71, 28672\nQzvav 100,152,24\n"
"s_cbranch_vccz .Lst25\nbuffer_store_dwordx4 a[8:11], v60, s[56:59], s68 offen offset:0 sc1\n.Lst25:\nQmvaa 100,156,176\nQzvav 104,160,24\ns_add_u32 s68, s71, 43008\nQmvaa 104,164,176\ns_cbranch_vccz .Lst24\n"
"buffer_store_dwordx4 a[12:15], v60, s[56:59], s68 offen offset:0 sc1\n.Lst24:\nQzvav 108,168,24\nQmvaa 108,172,176\ns_waitcnt lgkmcnt(0)\nQzvav 128,144,28\ns_waitcnt vmcnt(15)\ns_barrier\n"
"Qmvaa 128,148,180\nQdv 24,63,0\ns_add_u32 m0, s62, 16512\ns_add_u32 s69, s87, 1048704\nQzvav 132,152,28\nbuffer_load_dwordx4 v62, s[52:55], s69 offen lds\nQmvaa 132,156,180\nQda 176,63,512\n"
"Qzvav 136,160,28\nQta 112,61,33024\nQmvaa 136,164,180\ns_add_u32 s69, s87, 1113208\nQzvav 140,168,28\nbuffer_load_dwordx4 v62, s[52:55], s69 offen offset:1032 lds\nQmvaa 140,172,180\nQdv 28,63,1024\n"
"Qzvav 160,144,32\nQda 180,63,1536\nQmvaa 160,148,184\nQta 114,61,33536\ns_add_u32 s69, s87, 1177712\nQzvav 164,152,32\nbuffer_load_dwordx4 v62, s[52:55], s69 offen offset:2064 lds\nQmvaa 164,156,184\n"
"Qta 116,61,37152\nQzvav 168,160,32\nQmvaa 168,164,184\nQta 118,61,37664\ns_add_u32 s69, s87, 1242216\nQzvav 172,168,32\nbuffer_load_dwordx4 v62, s[52:55], s69 offen offset:3096 lds\nQmvaa 172,172,184\n"
"Qdv 32,63,2048\nQzvav 192,144,36\nQda 184,63,2560\nQmvaa 192,148,188\nQta 120,61,33032\ns_add_u32 m0, s61, 0\nQzvav 196,152,36\ns_cbranch_vccnz .Ld0q0\n"
"buffer_load_dwordx4 v69, s[40:43], s90 offen nt lds\n.Ld0q0:\nQmvaa 196,156,188\nQta 122,61,33544\nQzvav 200,160,36\nQmvaa 200,164,188\nQta 124,61,37160\nQzvav 204,168,36\n"
"Qdv 36,63,3072\nQmvaa 204,172,188\nQda 188,63,3584\nQzvav 224,144,40\nQta 126,61,37672\nQmvaa 224,148,192\nQzvav 228,152,40\nQta 128,61,33280\n"
"Qmvaa 228,156,192\nQzvav 232,160,40\nQta 130,61,33792\nQmvaa 232,164,192\nQzvav 236,168,40\nQdv 40,63,4096\nQmvaa 236,172,192\nQda 192,63,4608\n"
"Qzaav 208,144,44\nQta 132,61,37408\nQmaaa 208,148,196\nQzaav 212,152,44\nQta 134,61,37920\nQmaaa 212,156,196\nQzaav 216,160,44\nQta 136,61,33288\n"
"Qmaaa 216,164,196\nQzaav 220,168,44\nQdv 44,63,5120\nQmaaa 220,172,196\nQda 196,63,5632\nQzaav 240,144,48\nQta 138,61,33800\nQmaaa 240,148,200\n"
"Qzaav 244,152,48\nQta 140,61,37416\nQmaaa 244,156,200\nQzaav 248,160,48\nQta 142,61,37928\nQmaaa 248,164,200\nQzaav 252,168,48\nQdv 48,63,6144\n"
"Qmaaa 252,172,200\nQda 200,63,6656\ns_waitcnt lgkmcnt(2)\nQmvav 80,112,24\nQmvaa 80,116,176\ns_add_u32 s68, s71, 0\nQmvav 84,120,24\ns_cbranch_vccz .Lst23\n"
"buffer_store_dwordx4 a[16:19], v60, s[56:59], s68 offen offset:1024 sc1\n.Lst23:\nQmvaa 84,124,176\nQmvav 88,128,24\ns_add_u32 s68, s71, 14336\nQmvaa 88,132,176\ns_cbranch_vccz .Lst22\nbuffer_store_dwordx4 a[20:23], v60, s[56:59], s68 offen offset:1024 sc1\n"
".Lpg0:\ns_cbranch_execz .Ltr0\n.Lst22:\nQmvav 92,136,24\nQmvaa 92,140,176\ns_waitcnt lgkmcnt(0)\nQmvav 112,112,28\ns_waitcnt vmcnt(16)\n"
"s_barrier\nQmvaa 112,116,180\nQta 144,61,49536\ns_add_u32 m0, s62, 33024\ns_add_u32 s69, s87, 1572864\nQmvav 116,120,28\nbuffer_load_dwordx4 v62, s[52:55], s69 offen lds\nQmvaa 116,124,180\n"
"Qta 146,61,50048\nQmvav 120,128,28\nQmvaa 120,132,180\nQta 148,61,53664\ns_add_u32 s69, s87, 1637368\nQmvav 124,136,28\nbuffer_load_dwordx4 v62, s[52:55], s69 offen offset:1032 lds\nQmvaa 124,140,180\n"
"Qta 150,61,54176\nQmvav 144,112,32\nQmvaa 144,116,184\nQta 152,61,49544\ns_add_u32 s69, s87, 1701872\nQmvav 148,120,32\nbuffer_load_dwordx4 v62, s[52:55], s69 offen offset:2064 lds\nQmvaa 148,124,184\n"
"Qta 154,61,50056\nQmvav 152,128,32\nQmvaa 152,132,184\nQta 156,61,53672\ns_add_u32 s69, s87, 1766376\nQmvav 156,136,32\nbuffer_load_dwordx4 v62, s[52:55], s69 offen offset:3096 lds\nQmvaa 156,140,184\n"
"Qta 158,61,54184\nQmvav 176,112,36\nQmvaa 176,116,188\nQta 160,61,49792\ns_add_u32 m0, s63, 0\ns_add_u32 s88, s86, 384\nQmvav 180,120,36\nbuffer_load_dwordx4 v64, s[48:51], s88 offen lds\n"
"Qmvaa 180,124,188\nQta 162,61,50304\nQmvav 184,128,36\nQmvaa 184,132,188\nQta 164,61,53920\ns_add_u32 s88, s86, 19840\nQmvav 188,136,36\nbuffer_load_dwordx4 v64, s[48:51], s88 offen offset:1024 lds\n"
"Qmvaa 188,140,188\nQta 166,61,54432\nQmvav 208,112,40\nQmvaa 208,116,192\nQta 168,61,49800\ns_add_u32 s88, s86, 39296\nQmvav 212,120,40\nbuffer_load_dwordx4 v64, s[48:51], s88 offen offset:2048 lds\n"
"Qmvaa 212,124,192\nQta 170,61,50312\nQmvav 216,128,40\nQmvaa 216,132,192\nQta 172,61,53928\ns_add_u32 s88, s86, 58752\nQmvav 220,136,40\nbuffer_load_dwordx4 v64, s[48:51], s88 offen offset:3072 lds\n"
"Qmvaa 220,140,192\nQta 174,61,54440\nQmvav 240,112,44\nQmvaa 240,116,196\ns_add_u32 m0, s63, 4096\ns_add_u32 s88, s86, 82304\nQmvav 244,120,44\nbuffer_load_dwordx4 v64, s[48:51], s88 offen lds\n"
"Qmvaa 244,124,196\nQmvav 248,128,44\nQmvaa 248,132,196\ns_add_u32 s88, s86, 101760\nQmvav 252,136,44\nbuffer_load_dwordx4 v64, s[48:51], s88 offen offset:1024 lds\nQmvaa 252,140,196\nQmaav 224,112,48\n"
"Qmaaa 224,116,200\ns_add_u32 s88, s86, 121216\nQmaav 228,120,48\nbuffer_load_dwordx4 v64, s[48:51], s88 offen offset:2048 lds\nQmaaa 228,124,200\nQmaav 232,128,48\nQmaaa 232,132,200\nQmaav 236,136,48\n"
"Qmaaa 236,140,200\ns_waitcnt lgkmcnt(0)\nQmvav 96,144,24\nQmvaa 96,148,176\ns_add_u32 s68, s71, 28672\nQmvav 100,152,24\ns_cbranch_vccz .Lst21\nbuffer_store_dwordx4 a[24:27], v60, s[56:59], s68 offen offset:1024 sc1\n"
".Lst21:\nQmvaa 100,156,176\nQmvav 104,160,24\ns_add_u32 s68, s71, 43008\nQmvaa 104,164,176\ns_cbranch_vccz .Lst20\nbuffer_store_dwordx4 a[28:31], v60, s[56:59], s68 offen offset:1024 sc1\n.Lst20:\n"
"Qmvav 108,168,24\nQmvaa 108,172,176\ns_waitcnt lgkmcnt(0)\nQmvav 128,144,28\ns_waitcnt vmcnt(16)\ns_barrier\nQmvaa 128,148,180\nQdv 24,63,33024\n"
"s_add_u32 m0, s62, 49536\ns_add_u32 s69, s87, 1572992\nQmvav 132,152,28\nbuffer_load_dwordx4 v62, s[52:55], s69 offen lds\nQmvaa 132,156,180\nQda 176,63,33536\nQmvav 136,160,28\nQta 112,61,0\n"
"Qmvaa 136,164,180\ns_add_u32 s69, s87, 1637496\nQmvav 140,168,28\nbuffer_load_dwordx4 v62, s[52:55], s69 offen offset:1032 lds\nQmvaa 140,172,180\nQdv 28,63,34048\nQmvav 160,144,32\nQda 180,63,34560\n"
"Qmvaa 160,148,184\nQta 114,61,512\ns_add_u32 s69, s87, 1702000\nQmvav 164,152,32\nbuffer_load_dwordx4 v62, s[52:55], s69 offen offset:2064 lds\nQmvaa 164,156,184\nQta 116,61,4128\nQmvav 168,160,32\n"
"Qmvaa 168,164,184\nQta 118,61,4640\ns_add_u32 s69, s87, 1766504\nQmvav 172,168,32\nbuffer_load_dwordx4 v62, s[52:55], s69 offen offset:3096 lds\nQmvaa 172,172,184\nQdv 32,63,35072\nQmvav 192,144,36\n"
"Qda 184,63,35584\nQmvaa 192,148,188\nQta 120,61,8\ns_add_u32 m0, s61, 0\nQmvav 196,152,36\ns_cbranch_vccnz .Ld0q1\nbuffer_load_dwordx4 v69, s[40:43], s60 offen offset:1024 nt lds\n.Ld0q1:\n"
"Qmvaa 196,156,188\nQta 122,61,520\nQmvav 200,160,36\nQmvaa 200,164,188\nQta 124,61,4136\nQmvav 204,168,36\nQdv 36,63,36096\nQmvaa 204,172,188\n"
"Qda 188,63,36608\nQmvav 224,144,40\nQta 126,61,4648\nQmvaa 224,148,192\nQmvav 228,152,40\nQta 128,61,256\nQmvaa 228,156,192\nQmvav 232,160,40\n"
"Qta 130,61,768\nQmvaa 232,164,192\nQmvav 236,168,40\nQdv 40,63,37120\nQmvaa 236,172,192\nQda 192,63,37632\nQmaav 208,144,44\nQta 132,61,4384\n"
"Qmaaa 208,148,196\nQmaav 212,152,44\nQta 134,61,4896\nQmaaa 212,156,196\nQmaav 216,160,44\nQta 136,61,264\nQmaaa 216,164,196\nQmaav 220,168,44\n"
"Qdv 44,63,38144\nQmaaa 220,172,196\nQda 196,63,38656\nQmaav 240,144,48\nQta 138,61,776\nQmaaa 240,148,200\nQmaav 244,152,48\nQta 140,61,4392\n"
"Qmaaa 244,156,200\nQmaav 248,160,48\nQta 142,61,4904\nQmaaa 248,164,200\nQmaav 252,168,48\nQdv 48,63,39168\nQmaaa 252,172,200\nQda 200,63,39680\n"
"s_waitcnt lgkmcnt(2)\nQmvav 80,112,24\nQmvaa 80,116,176\ns_add_u32 s68, s71, 0\nQmvav 84,120,24\ns_cbranch_vccz .Lst19\nbuffer_store_dwordx4 a[32:35], v60, s[56:59], s68 offen offset:2048 sc1\n.Lst19:\n"
"Qmvaa 84,124,176\nQmvav 88,128,24\ns_add_u32 s68, s71, 14336\nQmvaa 88,132,176\ns_cbranch_vccz .Lst18\nbuffer_store_dwordx4 a[36:39], v60, s[56:59], s68 offen offset:2048 sc1\n.Lst18:\nQmvav 92,136,24\n"
"Qmvaa 92,140,176\ns_waitcnt lgkmcnt(0)\nQmvav 112,112,28\ns_waitcnt vmcnt(17)\ns_barrier\nQmvaa 112,116,180\nQta 144,61,16512\ns_add_u32 m0, s62, 0\n"
"s_add_u32 s69, s87, 2097152\nQmvav 116,120,28\nbuffer_load_dwordx4 v62, s[52:55], s69 offen lds\nQmvaa 116,124,180\nQta 146,61,17024\nQmvav 120,128,28\nQmvaa 120,132,180\nQta 148,61,20640\n"
"s_add_u32 s69, s87, 2161656\nQmvav 124,136,28\nbuffer_load_dwordx4 v62, s[52:55], s69 offen offset:1032 lds\nQmvaa 124,140,180\nQta 150,61,21152\nQmvav 144,112,32\nQmvaa 144,116,184\nQta 152,61,16520\n"
"s_add_u32 s69, s87, 2226160\nQmvav 148,120,32\nbuffer_load_dwordx4 v62, s[52:55], s69 offen offset:2064 lds\nQmvaa 148,124,184\nQta 154,61,17032\nQmvav 152,128,32\nQmvaa 152,132,184\nQta 156,61,20648\n"
"s_add_u32 s69, s87, 2290664\nQmvav 156,136,32\nbuffer_load_dwordx4 v62, s[52:55], s69 offen offset:3096 lds\nQmvaa 156,140,184\nQta 158,61,21160\nQmvav 176,112,36\nQmvaa 176,116,188\nQta 160,61,16768\n"
"s_add_u32 m0, s63, 33024\ns_add_u32 s88, s86, 512\nQmvav 180,120,36\nbuffer_load_dwordx4 v64, s[48:51], s88 offen lds\nQmvaa 180,124,188\nQta 162,61,17280\nQmvav 184,128,36\nQmvaa 184,132,188\n"
"Qta 164,61,20896\ns_add_u32 s88, s86, 19968\nQmvav 188,136,36\nbuffer_load_dwordx4 v64, s[48:51], s88 offen offset:1024 lds\nQmvaa 188,140,188\nQta 166,61,21408\nQmvav 208,112,40\nQmvaa 208,116,192\n"
"Qta 168,61,16776\ns_add_u32 s88, s86, 39424\nQmvav 212,120,40\nbuffer_load_dwordx4 v64, s[48:51], s88 offen offset:2048 lds\nQmvaa 212,124,192\nQta 170,61,17288\nQmvav 216,128,40\nQmvaa 216,132,192\n"
"Qta 172,61,20904\ns_add_u32 s88, s86, 58880\nQmvav 220,136,40\nbuffer_load_dwordx4 v64, s[48:51], s88 offen offset:3072 lds\nQmvaa 220,140,192\nQta 174,61,21416\nQmvav 240,112,44\nQmvaa 240,116,196\n"
"s_add_u32 m0, s63, 37120\ns_add_u32 s88, s86, 82432\nQmvav 244,120,44\nbuffer_load_dwordx4 v64, s[48:51], s88 offen lds\nQmvaa 244,124,196\nQmvav 248,128,44\nQmvaa 248,132,196\ns_add_u32 s88, s86, 101888\n"
"Qmvav 252,136,44\nbuffer_load_dwordx4 v64, s[48:51], s88 offen offset:1024 lds\nQmvaa 252,140,196\nQmaav 224,112,48\nQmaaa 224,116,200\ns_add_u32 s88, s86, 121344\nQmaav 228,120,48\nbuffer_load_dwordx4 v64, s[48:51], s88 offen offset:2048 lds\n"
"Qmaaa 228,124,200\nQmaav 232,128,48\nQmaaa 232,132,200\nQmaav 236,136,48\nQmaaa 236,140,200\ns_waitcnt lgkmcnt(0)\nQmvav 96,144,24\nQmvaa 96,148,176\n"
"s_add_u32 s68, s71, 28672\nQmvav 100,152,24\ns_cbranch_vccz .Lst17\nbuffer_store_dwordx4 a[40:43], v60, s[56:59], s68 offen offset:2048 sc1\n.Lst17:\nQmvaa 100,156,176\nQmvav 104,160,24\ns_add_u32 s68, s71, 43008\n"
"Qmvaa 104,164,176\ns_cbranch_vccz .Lst16\nbuffer_store_dwordx4 a[44:47], v60, s[56:59], s68 offen offset:2048 sc1\n.Lst16:\nQmvav 108,168,24\nQmvaa 108,172,176\ns_waitcnt lgkmcnt(0)\nQmvav 128,144,28\n"
"s_waitcnt vmcnt(16)\ns_barrier\nQmvaa 128,148,180\nQdv 24,63,0\ns_add_u32 m0, s62, 16512\ns_add_u32 s69, s87, 2097280\nQmvav 132,152,28\nbuffer_load_dwordx4 v62, s[52:55], s69 offen lds\n"
"Qmvaa 132,156,180\nQda 176,63,512\nQmvav 136,160,28\nQta 112,61,33024\nQmvaa 136,164,180\ns_add_u32 s69, s87, 2161784\nQmvav 140,168,28\nbuffer_load_dwordx4 v62, s[52:55], s69 offen offset:1032 lds\n"
"Qmvaa 140,172,180\nQdv 28,63,1024\nQmvav 160,144,32\nQda 180,63,1536\nQmvaa 160,148,184\nQta 114,61,33536\ns_add_u32 s69, s87, 2226288\nQmvav 164,152,32\n"
"buffer_load_dwordx4 v62, s[52:55], s69 offen offset:2064 lds\nQmvaa 164,156,184\nQta 116,61,37152\nQmvav 168,160,32\nQmvaa 168,164,184\nQta 118,61,37664\ns_add_u32 s69, s87, 2290792\nQmvav 172,168,32\n"
"buffer_load_dwordx4 v62, s[52:55], s69 offen offset:3096 lds\nQmvaa 172,172,184\nQdv 32,63,2048\nQmvav 192,144,36\nQda 184,63,2560\nQmvaa 192,148,188\nQta 120,61,33032\ns_add_u32 m0, s61, 0\n"
"Qmvav 196,152,36\ns_cbranch_vccnz .Ld0q2\nbuffer_load_dwordx4 v69, s[40:43], s90 offen offset:2048 nt lds\n.Ld0q2:\nQmvaa 196,156,188\nQta 122,61,33544\nQmvav 200,160,36\nQmvaa 200,164,188\n"
"Qta 124,61,37160\nQmvav 204,168,36\nQdv 36,63,3072\nQmvaa 204,172,188\nQda 188,63,3584\nQmvav 224,144,40\nQta 126,61,37672\nQmvaa 224,148,192\n"
"Qmvav 228,152,40\nQta 128,61,33280\nQmvaa 228,156,192\nQmvav 232,160,40\nQta 130,61,33792\nQmvaa 232,164,192\nQmvav 236,168,40\nQdv 40,63,4096\n"
"Qmvaa 236,172,192\nQda 192,63,4608\nQmaav 208,144,44\nQta 132,61,37408\nQmaaa 208,148,196\nQmaav 212,152,44\nQta 134,61,37920\nQmaaa 212,156,196\n"
"Qmaav 216,160,44\nQta 136,61,33288\nQmaaa 216,164,196\nQmaav 220,168,44\nQdv 44,63,5120\nQmaaa 220,172,196\nQda 196,63,5632\nQmaav 240,144,48\n"
"Qta 138,61,33800\nQmaaa 240,148,200\nQmaav 244,152,48\nQta 140,61,37416\nQmaaa 244,156,200\nQmaav 248,160,48\nQta 142,61,37928\nQmaaa 248,164,200\n"
"Qmaav 252,168,48\nQdv 48,63,6144\nQmaaa 252,172,200\nQda 200,63,6656\ns_waitcnt lgkmcnt(2)\nQmvav 80,112,24\nQmvaa 80,116,176\ns_add_u32 s68, s71, 0\n"
"Qmvav 84,120,24\ns_cbranch_vccz .Lst15\nbuffer_store_dwordx4 a[48:51], v60, s[56:59], s68 offen offset:3072 sc1\n.Lst15:\nQmvaa 84,124,176\nQmvav 88,128,24\ns_add_u32 s68, s71, 14336\nQmvaa 88,132,176\n"
"s_cbranch_vccz .Lst14\nbuffer_store_dwordx4 a[52:55], v60, s[56:59], s68 offen offset:3072 sc1\n.Lst14:\nQmvav 92,136,24\nQmvaa 92,140,176\ns_waitcnt lgkmcnt(0)\nQmvav 112,112,28\ns_waitcnt vmcnt(17)\n"
"s_barrier\nQmvaa 112,116,180\nQta 144,61,49536\ns_add_u32 m0, s62, 33024\ns_add_u32 s69, s87, 2621440\nQmvav 116,120,28\nbuffer_load_dwordx4 v62, s[52:55], s69 offen lds\nQmvaa 116,124,180\n"
"Qta 146,61,50048\nQmvav 120,128,28\nQmvaa 120,132,180\nQta 148,61,53664\ns_add_u32 s69, s87, 2685944\nQmvav 124,136,28\nbuffer_load_dwordx4 v62, s[52:55], s69 offen offset:1032 lds\nQmvaa 124,140,180\n"
"Qta 150,61,54176\nQmvav 144,112,32\nQmvaa 144,116,184\nQta 152,61,49544\ns_add_u32 s69, s87, 2750448\nQmvav 148,120,32\nbuffer_load_dwordx4 v62, s[52:55], s69 offen offset:2064 lds\nQmvaa 148,124,184\n"
"Qta 154,61,50056\nQmvav 152,128,32\nQmvaa 152,132,184\nQta 156,61,53672\ns_add_u32 s69, s87, 2814952\nQmvav 156,136,32\nbuffer_load_dwordx4 v62, s[52:55], s69 offen offset:3096 lds\nQmvaa 156,140,184\n"
"Qta 158,61,54184\nQmvav 176,112,36\nQmvaa 176,116,188\nQta 160,61,49792\ns_add_u32 m0, s63, 0\ns_add_u32 s88, s86, 640\nQmvav 180,120,36\nbuffer_load_dwordx4 v64, s[48:51], s88 offen lds\n"
"Qmvaa 180,124,188\nQta 162,61,50304\nQmvav 184,128,36\nQmvaa 184,132,188\nQta 164,61,53920\ns_add_u32 s88, s86, 20096\nQmvav 188,136,36\nbuffer_load_dwordx4 v64, s[48:51], s88 offen offset:1024 lds\n"
"Qmvaa 188,140,188\nQta 166,61,54432\nQmvav 208,112,40\nQmvaa 208,116,192\nQta 168,61,49800\ns_add_u32 s88, s86, 39552\nQmvav 212,120,40\nbuffer_load_dwordx4 v64, s[48:51], s88 offen offset:2048 lds\n"
"Qmvaa 212,124,192\nQta 170,61,50312\nQmvav 216,128,40\nQmvaa 216,132,192\nQta 172,61,53928\ns_add_u32 s88, s86, 59008\nQmvav 220,136,40\nbuffer_load_dwordx4 v64, s[48:51], s88 offen offset:3072 lds\n"
"Qmvaa 220,140,192\nQta 174,61,54440\nQmvav 240,112,44\nQmvaa 240,116,196\ns_add_u32 m0, s63, 4096\ns_add_u32 s88, s86, 82560\nQmvav 244,120,44\nbuffer_load_dwordx4 v64, s[48:51], s88 offen lds\n"
"Qmvaa 244,124,196\nQmvav 248,128,44\nQmvaa 248,132,196\ns_add_u32 s88, s86, 102016\nQmvav 252,136,44\nbuffer_load_dwordx4 v64, s[48:51], s88 offen offset:1024 lds\nQmvaa 252,140,196\nQmaav 224,112,48\n"
"Qmaaa 224,116,200\ns_add_u32 s88, s86, 121472\nQmaav 228,120,48\nbuffer_load_dwordx4 v64, s[48:51], s88 offen offset:2048 lds\nQmaaa 228,124,200\nQmaav 232,128,48\nQmaaa 232,132,200\nQmaav 236,136,48\n"
"Qmaaa 236,140,200\ns_waitcnt lgkmcnt(0)\nQmvav 96,144,24\nQmvaa 96,148,176\ns_add_u32 s68, s71, 28672\nQmvav 100,152,24\ns_cbranch_vccz .Lst13\nbuffer_store_dwordx4 a[56:59], v60, s[56:59], s68 offen offset:3072 sc1\n"
".Lpg1:\ns_cbranch_execz .Ltr0\n.Lst13:\nQmvaa 100,156,176\nQmvav 104,160,24\ns_add_u32 s68, s71, 43008\nQmvaa 104,164,176\ns_cbranch_vccz .Lst12\n"
"buffer_store_dwordx4 a[60:63], v60, s[56:59], s68 offen offset:3072 sc1\n.Lst12:\nQmvav 108,168,24\nQmvaa 108,172,176\ns_waitcnt lgkmcnt(0)\nQmvav 128,144,28\ns_waitcnt vmcnt(16)\ns_barrier\n"
"Qmvaa 128,148,180\nQdv 24,63,33024\ns_add_u32 m0, s62, 49536\ns_add_u32 s69, s87, 2621568\nQmvav 132,152,28\nbuffer_load_dwordx4 v62, s[52:55], s69 offen lds\nQmvaa 132,156,180\nQda 176,63,33536\n"
"Qmvav 136,160,28\nQta 112,61,0\nQmvaa 136,164,180\ns_add_u32 s69, s87, 2686072\nQmvav 140,168,28\nbuffer_load_dwordx4 v62, s[52:55], s69 offen offset:1032 lds\nQmvaa 140,172,180\nQdv 28,63,34048\n"
"Qmvav 160,144,32\nQda 180,63,34560\nQmvaa 160,148,184\nQta 114,61,512\ns_add_u32 s69, s87, 2750576\nQmvav 164,152,32\nbuffer_load_dwordx4 v62, s[52:55], s69 offen offset:2064 lds\nQmvaa 164,156,184\n"
"Qta 116,61,4128\nQmvav 168,160,32\nQmvaa 168,164,184\nQta 118,61,4640\ns_add_u32 s69, s87, 2815080\nQmvav 172,168,32\nbuffer_load_dwordx4 v62, s[52:55], s69 offen offset:3096 lds\nQmvaa 172,172,184\n"
"Qdv 32,63,35072\nQmvav 192,144,36\nQda 184,63,35584\nQmvaa 192,148,188\nQta 120,61,8\ns_add_u32 m0, s61, 0\nQmvav 196,152,36\ns_cbranch_vccnz .Ld0q3\n"
"buffer_load_dwordx4 v69, s[40:43], s60 offen offset:3072 nt lds\n.Ld0q3:\nQmvaa 196,156,188\nQta 122,61,520\nQmvav 200,160,36\nQmvaa 200,164,188\nQta 124,61,4136\nQmvav 204,168,36\n"
"Qdv 36,63,36096\nQmvaa 204,172,188\nQda 188,63,36608\nQmvav 224,144,40\nQta 126,61,4648\nQmvaa 224,148,192\nQmvav 228,152,40\nQta 128,61,256\n"
"Qmvaa 228,156,192\nQmvav 232,160,40\nQta 130,61,768\nQmvaa 232,164,192\nQmvav 236,168,40\nQdv 40,63,37120\nQmvaa 236,172,192\nQda 192,63,37632\n"
"Qmaav 208,144,44\nQta 132,61,4384\nQmaaa 208,148,196\nQmaav 212,152,44\nQta 134,61,4896\nQmaaa 212,156,196\nQmaav 216,160,44\nQta 136,61,264\n"
"Qmaaa 216,164,196\nQmaav 220,168,44\nQdv 44,63,38144\nQmaaa 220,172,196\nQda 196,63,38656\nQmaav 240,144,48\nQta 138,61,776\nQmaaa 240,148,200\n"
"Qmaav 244,152,48\nQta 140,61,4392\nQmaaa 244,156,200\nQmaav 248,160,48\nQta 142,61,4904\nQmaaa 248,164,200\nQmaav 252,168,48\nQdv 48,63,39168\n"
"Qmaaa 252,172,200\nQda 200,63,39680\ns_waitcnt lgkmcnt(2)\nQmvav 80,112,24\nQmvaa 80,116,176\ns_add_u32 s68, s71, 4096\nQmvav 84,120,24\ns_cbranch_vccz .Lst11\n"
"buffer_store_dwordx4 a[64:67], v60, s[56:59], s68 offen offset:0 sc1\n.Lst11:\nQmvaa 84,124,176\nQmvav 88,128,24\ns_add_u32 s68, s71, 18432\nQmvaa 88,132,176\ns_cbranch_vccz .Lst10\nbuffer_store_dwordx4 a[68:71], v60, s[56:59], s68 offen offset:0 sc1\n"
".Lst10:\nQmvav 92,136,24\nQmvaa 92,140,176\ns_waitcnt lgkmcnt(0)\nQmvav 112,112,28\ns_waitcnt vmcnt(17)\ns_barrier\nQmvaa 112,116,180\n"
"Qta 144,61,16512\ns_add_u32 m0, s62, 0\ns_add_u32 s69, s87, 3145728\nQmvav 116,120,28\nbuffer_load_dwordx4 v62, s[52:55], s69 offen lds\nQmvaa 116,124,180\nQta 146,61,17024\nQmvav 120,128,28\n"
"Qmvaa 120,132,180\nQta 148,61,20640\ns_add_u32 s69, s87, 3210232\nQmvav 124,136,28\nbuffer_load_dwordx4 v62, s[52:55], s69 offen offset:1032 lds\nQmvaa 124,140,180\nQta 150,61,21152\nQmvav 144,112,32\n"
"Qmvaa 144,116,184\nQta 152,61,16520\ns_add_u32 s69, s87, 3274736\nQmvav 148,120,32\nbuffer_load_dwordx4 v62, s[52:55], s69 offen offset:2064 lds\nQmvaa 148,124,184\nQta 154,61,17032\nQmvav 152,128,32\n"
"Qmvaa 152,132,184\nQta 156,61,20648\ns_add_u32 s69, s87, 3339240\nQmvav 156,136,32\nbuffer_load_dwordx4 v62, s[52:55], s69 offen offset:3096 lds\nQmvaa 156,140,184\nQta 158,61,21160\nQmvav 176,112,36\n"
"Qmvaa 176,116,188\nQta 160,61,16768\ns_add_u32 m0, s63, 33024\ns_add_u32 s88, s86, 768\nQmvav 180,120,36\nbuffer_load_dwordx4 v64, s[48:51], s88 offen lds\nQmvaa 180,124,188\nQta 162,61,17280\n"
"Qmvav 184,128,36\nQmvaa 184,132,188\nQta 164,61,20896\ns_add_u32 s88, s86, 20224\nQmvav 188,136,36\nbuffer_load_dwordx4 v64, s[48:51], s88 offen offset:1024 lds\nQmvaa 188,140,188\nQta 166,61,21408\n"
"Qmvav 208,112,40\nQmvaa 208,116,192\nQta 168,61,16776\ns_add_u32 s88, s86, 39680\nQmvav 212,120,40\nbuffer_load_dwordx4 v64, s[48:51], s88 offen offset:2048 lds\nQmvaa 212,124,192\nQta 170,61,17288\n"
"Qmvav 216,128,40\nQmvaa 216,132,192\nQta 172,61,20904\ns_add_u32 s88, s86, 59136\nQmvav 220,136,40\nbuffer_load_dwordx4 v64, s[48:51], s88 offen offset:3072 lds\nQmvaa 220,140,192\nQta 174,61,21416\n"
"Qmvav 240,112,44\nQmvaa 240,116,196\ns_add_u32 m0, s63, 37120\ns_add_u32 s88, s86, 82688\nQmvav 244,120,44\nbuffer_load_dwordx4 v64, s[48:51], s88 offen lds\nQmvaa 244,124,196\nQmvav 248,128,44\n"
"Qmvaa 248,132,196\ns_add_u32 s88, s86, 102144\nQmvav 252,136,44\nbuffer_load_dwordx4 v64, s[48:51], s88 offen offset:1024 lds\nQmvaa 252,140,196\nQmaav 224,112,48\nQmaaa 224,116,200\ns_add_u32 s88, s86, 121600\n"
"Qmaav 228,120,48\nbuffer_load_dwordx4 v64, s[48:51], s88 offen offset:2048 lds\nQmaaa 228,124,200\nQmaav 232,128,48\nQmaaa 232,132,200\nQmaav 236,136,48\nQmaaa 236,140,200\ns_waitcnt lgkmcnt(0)\n"
"Qmvav 96,144,24\nQmvaa 96,148,176\ns_add_u32 s68, s71, 32768\nQmvav 100,152,24\ns_cbranch_vccz .Lst9\nbuffer_store_dwordx4 a[72:75], v60, s[56:59], s68 offen offset:0 sc1\n.Lst9:\nQmvaa 100,156,176\n"
"Qmvav 104,160,24\ns_add_u32 s68, s71, 47104\nQmvaa 104,164,176\ns_cbranch_vccz .Lst8\nbuffer_store_dwordx4 a[76:79], v60, s[56:59], s68 offen offset:0 sc1\n.Lst8:\nQmvav 108,168,24\nQmvaa 108,172,176\n"
"s_waitcnt lgkmcnt(0)\nQmvav 128,144,28\ns_waitcnt vmcnt(16)\ns_barrier\nQmvaa 128,148,180\nQdv 24,63,0\ns_add_u32 m0, s62, 16512\ns_add_u32 s69, s87, 3145856\n"
"Qmvav 132,152,28\nbuffer_load_dwordx4 v62, s[52:55], s69 offen lds\nQmvaa 132,156,180\nQda 176,63,512\nQmvav 136,160,28\nQta 112,61,33024\nQmvaa 136,164,180\ns_add_u32 s69, s87, 3210360\n"
"Qmvav 140,168,28\nbuffer_load_dwordx4 v62, s[52:55], s69 offen offset:1032 lds\nQmvaa 140,172,180\nQdv 28,63,1024\nQmvav 160,144,32\nQda 180,63,1536\nQmvaa 160,148,184\nQta 114,61,33536\n"
"s_add_u32 s69, s87, 3274864\nQmvav 164,152,32\nbuffer_load_dwordx4 v62, s[52:55], s69 offen offset:2064 lds\nQmvaa 164,156,184\nQta 116,61,37152\nQmvav 168,160,32\nQmvaa 168,164,184\nQta 118,61,37664\n"
"s_add_u32 s69, s87, 3339368\nQmvav 172,168,32\nbuffer_load_dwordx4 v62, s[52:55], s69 offen offset:3096 lds\nQmvaa 172,172,184\nQdv 32,63,2048\nQmvav 192,144,36\nQda 184,63,2560\nQmvaa 192,148,188\n"
"Qta 120,61,33032\ns_add_u32 m0, s61, 8192\nQmvav 196,152,36\ns_cbranch_vccnz .Ld0q4\nbuffer_load_dwordx4 v72, s[44:47], s90 offen nt lds\n.Ld0q4:\nQmvaa 196,156,188\nQta 122,61,33544\n"
"Qmvav 200,160,36\nQmvaa 200,164,188\nQta 124,61,37160\nQmvav 204,168,36\nQdv 36,63,3072\nQmvaa 204,172,188\nQda 188,63,3584\nQmvav 224,144,40\n"
"Qta 126,61,37672\nQmvaa 224,148,192\nQmvav 228,152,40\nQta 128,61,33280\nQmvaa 228,156,192\nQmvav 232,160,40\nQta 130,61,33792\nQmvaa 232,164,192\n"
"Qmvav 236,168,40\nQdv 40,63,4096\nQmvaa 236,172,192\nQda 192,63,4608\nQmaav 208,144,44\nQta 132,61,37408\nQmaaa 208,148,196\nQmaav 212,152,44\n"
"Qta 134,61,37920\nQmaaa 212,156,196\nQmaav 216,160,44\nQta 136,61,33288\nQmaaa 216,164,196\nQmaav 220,168,44\nQdv 44,63,5120\nQmaaa 220,172,196\n"
"Qda 196,63,5632\nQmaav 240,144,48\nQta 138,61,33800\nQmaaa 240,148,200\nQmaav 244,152,48\nQta 140,61,37416\nQmaaa 244,156,200\nQmaav 248,160,48\n"
"Qta 142,61,37928\nQmaaa 248,164,200\nQmaav 252,168,48\nQdv 48,63,6144\nQmaaa 252,172,200\nQda 200,63,6656\ns_waitcnt lgkmcnt(2)\nQmvav 80,112,24\n"
"Qmvaa 80,116,176\ns_add_u32 s68, s71, 4096\nQmvav 84,120,24\ns_cbranch_vccz .Lst7\nbuffer_store_dwordx4 a[80:83], v60, s[56:59], s68 offen offset:1024 sc1\n.Lst7:\nQmvaa 84,124,176\nQmvav 88,128,24\n"
"s_add_u32 s68, s71, 18432\nQmvaa 88,132,176\ns_cbranch_vccz .Lst6\nbuffer_store_dwordx4 a[84:87], v60, s[56:59], s68 offen offset:1024 sc1\n.Lst6:\nQmvav 92,136,24\nQmvaa 92,140,176\ns_waitcnt lgkmcnt(0)\n"
"Qmvav 112,112,28\ns_waitcnt vmcnt(17)\ns_barrier\nQmvaa 112,116,180\nQta 144,61,49536\ns_add_u32 m0, s62, 33024\ns_add_u32 s69, s87, 3670016\nQmvav 116,120,28\n"
"buffer_load_dwordx4 v62, s[52:55], s69 offen lds\nQmvaa 116,124,180\nQta 146,61,50048\nQmvav 120,128,28\nQmvaa 120,132,180\nQta 148,61,53664\ns_add_u32 s69, s87, 3734520\nQmvav 124,136,28\n"
"buffer_load_dwordx4 v62, s[52:55], s69 offen offset:1032 lds\nQmvaa 124,140,180\nQta 150,61,54176\nQmvav 144,112,32\nQmvaa 144,116,184\nQta 152,61,49544\ns_add_u32 s69, s87, 3799024\nQmvav 148,120,32\n"
"buffer_load_dwordx4 v62, s[52:55], s69 offen offset:2064 lds\nQmvaa 148,124,184\nQta 154,61,50056\nQmvav 152,128,32\nQmvaa 152,132,184\nQta 156,61,53672\ns_add_u32 s69, s87, 3863528\nQmvav 156,136,32\n"
"buffer_load_dwordx4 v62, s[52:55], s69 offen offset:3096 lds\nQmvaa 156,140,184\nQta 158,61,54184\nQmvav 176,112,36\nQmvaa 176,116,188\nQta 160,61,49792\ns_add_u32 m0, s63, 0\ns_add_u32 s88, s86, 896\n"
"Qmvav 180,120,36\nbuffer_load_dwordx4 v64, s[48:51], s88 offen lds\nQmvaa 180,124,188\nQta 162,61,50304\nQmvav 184,128,36\nQmvaa 184,132,188\nQta 164,61,53920\ns_add_u32 s88, s86, 20352\n"
"Qmvav 188,136,36\nbuffer_load_dwordx4 v64, s[48:51], s88 offen offset:1024 lds\nQmvaa 188,140,188\nQta 166,61,54432\nQmvav 208,112,40\nQmvaa 208,116,192\nQta 168,61,49800\ns_add_u32 s88, s86, 39808\n"
"Qmvav 212,120,40\nbuffer_load_dwordx4 v64, s[48:51], s88 offen offset:2048 lds\nQmvaa 212,124,192\nQta 170,61,50312\nQmvav 216,128,40\nQmvaa 216,132,192\nQta 172,61,53928\ns_add_u32 s88, s86, 59264\n"
"Qmvav 220,136,40\nbuffer_load_dwordx4 v64, s[48:51], s88 offen offset:3072 lds\nQmvaa 220,140,192\nQta 174,61,54440\nQmvav 240,112,44\nQmvaa 240,116,196\ns_add_u32 m0, s63, 4096\ns_add_u32 s88, s86, 82816\n"
"Qmvav 244,120,44\nbuffer_load_dwordx4 v64, s[48:51], s88 offen lds\nQmvaa 244,124,196\nQmvav 248,128,44\nQmvaa 248,132,196\ns_add_u32 s88, s86, 102272\nQmvav 252,136,44\nbuffer_load_dwordx4 v64, s[48:51], s88 offen offset:1024 lds\n"
"Qmvaa 252,140,196\nQmaav 224,112,48\nQmaaa 224,116,200\ns_add_u32 s88, s86, 121728\nQmaav 228,120,48\nbuffer_load_dwordx4 v64, s[48:51], s88 offen offset:2048 lds\nQmaaa 228,124,200\nQmaav 232,128,48\n"
"Qmaaa 232,132,200\nQmaav 236,136,48\nQmaaa 236,140,200\ns_waitcnt lgkmcnt(0)\nQmvav 96,144,24\nQmvaa 96,148,176\ns_add_u32 s68, s71, 32768\nQmvav 100,152,24\n"
"s_cbranch_vccz .Lst5\nbuffer_store_dwordx4 a[88:91], v60, s[56:59], s68 offen offset:1024 sc1\n.Lst5:\nQmvaa 100,156,176\nQmvav 104,160,24\ns_add_u32 s68, s71, 47104\nQmvaa 104,164,176\ns_cbranch_vccz .Lst4\n"
"buffer_store_dwordx4 a[92:95], v60, s[56:59], s68 offen offset:1024 sc1\n.Lst4:\nQmvav 108,168,24\nQmvaa 108,172,176\ns_waitcnt lgkmcnt(0)\nQmvav 128,144,28\ns_waitcnt vmcnt(16)\ns_barrier\n"
"Qmvaa 128,148,180\nQdv 24,63,33024\ns_add_u32 m0, s62, 49536\ns_add_u32 s69, s87, 3670144\nQmvav 132,152,28\nbuffer_load_dwordx4 v62, s[52:55], s69 offen lds\nQmvaa 132,156,180\nQda 176,63,33536\n"
"Qmvav 136,160,28\nQta 112,61,0\nQmvaa 136,164,180\ns_add_u32 s69, s87, 3734648\nQmvav 140,168,28\nbuffer_load_dwordx4 v62, s[52:55], s69 offen offset:1032 lds\nQmvaa 140,172,180\nQdv 28,63,34048\n"
"Qmvav 160,144,32\nQda 180,63,34560\nQmvaa 160,148,184\nQta 114,61,512\ns_add_u32 s69, s87, 3799152\nQmvav 164,152,32\nbuffer_load_dwordx4 v62, s[52:55], s69 offen offset:2064 lds\nQmvaa 164,156,184\n"
"Qta 116,61,4128\nQmvav 168,160,32\nQmvaa 168,164,184\nQta 118,61,4640\ns_add_u32 s69, s87, 3863656\nQmvav 172,168,32\nbuffer_load_dwordx4 v62, s[52:55], s69 offen offset:3096 lds\nQmvaa 172,172,184\n"
"Qdv 32,63,35072\nQmvav 192,144,36\nQda 184,63,35584\nQmvaa 192,148,188\nQta 120,61,8\ns_add_u32 m0, s61, 8192\nQmvav 196,152,36\ns_cbranch_vccnz .Ld0q5\n"
"buffer_load_dwordx4 v72, s[44:47], s60 offen offset:1024 nt lds\n.Ld0q5:\nQmvaa 196,156,188\nQta 122,61,520\nQmvav 200,160,36\nQmvaa 200,164,188\nQta 124,61,4136\nQmvav 204,168,36\n"
"Qdv 36,63,36096\nQmvaa 204,172,188\nQda 188,63,36608\nQmvav 224,144,40\nQta 126,61,4648\nQmvaa 224,148,192\nQmvav 228,152,40\nQta 128,61,256\n"
"Qmvaa 228,156,192\nQmvav 232,160,40\nQta 130,61,768\nQmvaa 232,164,192\nQmvav 236,168,40\nQdv 40,63,37120\nQmvaa 236,172,192\nQda 192,63,37632\n"
"Qmaav 208,144,44\nQta 132,61,4384\nQmaaa 208,148,196\nQmaav 212,152,44\nQta 134,61,4896\nQmaaa 212,156,196\nQmaav 216,160,44\nQta 136,61,264\n"
"Qmaaa 216,164,196\nQmaav 220,168,44\nQdv 44,63,38144\nQmaaa 220,172,196\nQda 196,63,38656\nQmaav 240,144,48\nQta 138,61,776\nQmaaa 240,148,200\n"
"Qmaav 244,152,48\nQta 140,61,4392\nQmaaa 244,156,200\nQmaav 248,160,48\nQta 142,61,4904\nQmaaa 248,164,200\nQmaav 252,168,48\n.Lpg2:\n"
"s_cbranch_execz .Ltr0\nQdv 48,63,39168\nQmaaa 252,172,200\nQda 200,63,39680\ns_waitcnt lgkmcnt(2)\nQmvav 80,112,24\nQmvaa 80,116,176\ns_add_u32 s68, s71, 4096\n"
"Qmvav 84,120,24\ns_cbranch_vccz .Lst3\nbuffer_store_dwordx4 a[96:99], v60, s[56:59], s68 offen offset:2048 sc1\n.Lst3:\nQmvaa 84,124,176\nQmvav 88,128,24\ns_add_u32 s68, s71, 18432\nQmvaa 88,132,176\n"
"s_cbranch_vccz .Lst2\nbuffer_store_dwordx4 a[100:103], v60, s[56:59], s68 offen offset:2048 sc1\n.Lst2:\nQmvav 92,136,24\nQmvaa 92,140,176\ns_waitcnt lgkmcnt(0)\nQmvav 112,112,28\ns_waitcnt vmcnt(17)\n"
"s_barrier\nQmvaa 112,116,180\nQta 144,61,16512\ns_add_u32 m0, s62, 0\ns_add_u32 s69, s87, 4194304\nQmvav 116,120,28\nbuffer_load_dwordx4 v62, s[52:55], s69 offen lds\nQmvaa 116,124,180\n"
"Qta 146,61,17024\nQmvav 120,128,28\nQmvaa 120,132,180\nQta 148,61,20640\ns_add_u32 s69, s87, 4258808\nQmvav 124,136,28\nbuffer_load_dwordx4 v62, s[52:55], s69 offen offset:1032 lds\nQmvaa 124,140,180\n"
"Qta 150,61,21152\nQmvav 144,112,32\nQmvaa 144,116,184\nQta 152,61,16520\ns_add_u32 s69, s87, 4323312\nQmvav 148,120,32\nbuffer_load_dwordx4 v62, s[52:55], s69 offen offset:2064 lds\nQmvaa 148,124,184\n"
"Qta 154,61,17032\nQmvav 152,128,32\nQmvaa 152,132,184\nQta 156,61,20648\ns_add_u32 s69, s87, 4387816\nQmvav 156,136,32\nbuffer_load_dwordx4 v62, s[52:55], s69 offen offset:3096 lds\nQmvaa 156,140,184\n"
"Qta 158,61,21160\nQmvav 176,112,36\nQmvaa 176,116,188\nQta 160,61,16768\ns_add_u32 m0, s63, 33024\ns_add_u32 s88, s86, 1024\nQmvav 180,120,36\nbuffer_load_dwordx4 v64, s[48:51], s88 offen lds\n"
"Qmvaa 180,124,188\nQta 162,61,17280\nQmvav 184,128,36\nQmvaa 184,132,188\nQta 164,61,20896\ns_add_u32 s88, s86, 20480\nQmvav 188,136,36\nbuffer_load_dwordx4 v64, s[48:51], s88 offen offset:1024 lds\n"
"Qmvaa 188,140,188\nQta 166,61,21408\nQmvav 208,112,40\nQmvaa 208,116,192\nQta 168,61,16776\ns_add_u32 s88, s86, 39936\nQmvav 212,120,40\nbuffer_load_dwordx4 v64, s[48:51], s88 offen offset:2048 lds\n"
"Qmvaa 212,124,192\nQta 170,61,17288\nQmvav 216,128,40\nQmvaa 216,132,192\nQta 172,61,20904\ns_add_u32 s88, s86, 59392\nQmvav 220,136,40\nbuffer_load_dwordx4 v64, s[48:51], s88 offen offset:3072 lds\n"
"Qmvaa 220,140,192\nQta 174,61,21416\nQmvav 240,112,44\nQmvaa 240,116,196\ns_add_u32 m0, s63, 37120\ns_add_u32 s88, s86, 82944\nQmvav 244,120,44\nbuffer_load_dwordx4 v64, s[48:51], s88 offen lds\n"
"Qmvaa 244,124,196\nQmvav 248,128,44\nQmvaa 248,132,196\ns_add_u32 s88, s86, 102400\nQmvav 252,136,44\nbuffer_load_dwordx4 v64, s[48:51], s88 offen offset:1024 lds\nQmvaa 252,140,196\nQmaav 224,112,48\n"
"Qmaaa 224,116,200\ns_add_u32 s88, s86, 121856\nQmaav 228,120,48\nbuffer_load_dwordx4 v64, s[48:51], s88 offen offset:2048 lds\nQmaaa 228,124,200\nQmaav 232,128,48\nQmaaa 232,132,200\nQmaav 236,136,48\n"
"Qmaaa 236,140,200\ns_waitcnt lgkmcnt(0)\nQmvav 96,144,24\nQmvaa 96,148,176\ns_add_u32 s68, s71, 32768\nQmvav 100,152,24\ns_cbranch_vccz .Lst1\nbuffer_store_dwordx4 a[104:107], v60, s[56:59], s68 offen offset:2048 sc1\n"
".Lst1:\nQmvaa 100,156,176\nQmvav 104,160,24\ns_add_u32 s68, s71, 47104\nQmvaa 104,164,176\ns_cbranch_vccz .Lst0\nbuffer_store_dwordx4 a[108:111], v60, s[56:59], s68 offen offset:2048 sc1\n.Lst0:\n"
"Qmvav 108,168,24\nQmvaa 108,172,176\ns_waitcnt lgkmcnt(0)\nQmvav 128,144,28\ns_waitcnt vmcnt(16)\ns_barrier\nQmvaa 128,148,180\nQdv 24,63,0\n"
"s_add_u32 m0, s62, 16512\ns_add_u32 s69, s87, 4194432\nQmvav 132,152,28\nbuffer_load_dwordx4 v62, s[52:55], s69 offen lds\nQmvaa 132,156,180\nQda 176,63,512\nQmvav 136,160,28\nQta 112,61,33024\n"
"Qmvaa 136,164,180\ns_add_u32 s69, s87, 4258936\nQmvav 140,168,28\nbuffer_load_dwordx4 v62, s[52:55], s69 offen offset:1032 lds\nQmvaa 140,172,180\nQdv 28,63,1024\nQmvav 160,144,32\nQda 180,63,1536\n"
"Qmvaa 160,148,184\nQta 114,61,33536\ns_add_u32 s69, s87, 4323440\nQmvav 164,152,32\nbuffer_load_dwordx4 v62, s[52:55], s69 offen offset:2064 lds\nQmvaa 164,156,184\nQta 116,61,37152\nQmvav 168,160,32\n"
"Qmvaa 168,164,184\nQta 118,61,37664\ns_add_u32 s69, s87, 4387944\nQmvav 172,168,32\nbuffer_load_dwordx4 v62, s[52:55], s69 offen offset:3096 lds\nQmvaa 172,172,184\nQdv 32,63,2048\nQmvav 192,144,36\n"
"Qda 184,63,2560\nQmvaa 192,148,188\nQta 120,61,33032\ns_add_u32 m0, s61, 8192\nQmvav 196,152,36\ns_cbranch_vccnz .Ld0q6\nbuffer_load_dwordx4 v72, s[44:47], s90 offen offset:2048 nt lds\n.Ld0q6:\n"
"Qmvaa 196,156,188\nQta 122,61,33544\nQmvav 200,160,36\nQmvaa 200,164,188\nQta 124,61,37160\nQmvav 204,168,36\nQdv 36,63,3072\nQmvaa 204,172,188\n"
"Qda 188,63,3584\nQmvav 224,144,40\nQta 126,61,37672\nQmvaa 224,148,192\nQmvav 228,152,40\nQta 128,61,33280\nQmvaa 228,156,192\nQmvav 232,160,40\n"
"Qta 130,61,33792\nQmvaa 232,164,192\nQmvav 236,168,40\nQdv 40,63,4096\nQmvaa 236,172,192\nQda 192,63,4608\nQmaav 208,144,44\nQta 132,61,37408\n"
"Qmaaa 208,148,196\nQmaav 212,152,44\nQta 134,61,37920\nQmaaa 212,156,196\nQmaav 216,160,44\nQta 136,61,33288\nQmaaa 216,164,196\nQmaav 220,168,44\n"
"Qdv 44,63,5120\nQmaaa 220,172,196\nQda 196,63,5632\nQmaav 240,144,48\nQta 138,61,33800\nQmaaa 240,148,200\nQmaav 244,152,48\nQta 140,61,37416\n"
"Qmaaa 244,156,200\nQmaav 248,160,48\nQta 142,61,37928\nQmaaa 248,164,200\nQmaav 252,168,48\nQdv 48,63,6144\nQmaaa 252,172,200\nQda 200,63,6656\n"
"s_waitcnt lgkmcnt(2)\nQmvav 80,112,24\nQmvaa 80,116,176\nQmvav 84,120,24\nQmvaa 84,124,176\nQmvav 88,128,24\nQmvaa 88,132,176\nQmvav 92,136,24\n"
"Qmvaa 92,140,176\ns_waitcnt lgkmcnt(0)\nQmvav 112,112,28\ns_waitcnt vmcnt(17)\ns_barrier\nQmvaa 112,116,180\nQta 144,61,49536\ns_add_u32 m0, s62, 33024\n"
"s_add_u32 s69, s87, 4718592\nQmvav 116,120,28\nbuffer_load_dwordx4 v62, s[52:55], s69 offen lds\nQmvaa 116,124,180\nQta 146,61,50048\nQmvav 120,128,28\nQmvaa 120,132,180\nQta 148,61,53664\n"
"s_add_u32 s69, s87, 4783096\nQmvav 124,136,28\nbuffer_load_dwordx4 v62, s[52:55], s69 offen offset:1032 lds\nQmvaa 124,140,180\nQta 150,61,54176\nQmvav 144,112,32\nQmvaa 144,116,184\nQta 152,61,49544\n"
"s_add_u32 s69, s87, 4847600\nQmvav 148,120,32\nbuffer_load_dwordx4 v62, s[52:55], s69 offen offset:2064 lds\nQmvaa 148,124,184\nQta 154,61,50056\nQmvav 152,128,32\nQmvaa 152,132,184\nQta 156,61,53672\n"
"s_add_u32 s69, s87, 4912104\nQmvav 156,136,32\nbuffer_load_dwordx4 v62, s[52:55], s69 offen offset:3096 lds\nQmvaa 156,140,184\nQta 158,61,54184\nQmvav 176,112,36\nQmvaa 176,116,188\nQta 160,61,49792\n"
"s_add_u32 m0, s63, 0\ns_add_u32 s88, s86, 1152\nQmvav 180,120,36\nbuffer_load_dwordx4 v64, s[48:51], s88 offen lds\nQmvaa 180,124,188\nQta 162,61,50304\nQmvav 184,128,36\nQmvaa 184,132,188\n"
"Qta 164,61,53920\ns_add_u32 s88, s86, 20608\nQmvav 188,136,36\nbuffer_load_dwordx4 v64, s[48:51], s88 offen offset:1024 lds\nQmvaa 188,140,188\nQta 166,61,54432\nQmvav 208,112,40\nQmvaa 208,116,192\n"
"Qta 168,61,49800\ns_add_u32 s88, s86, 40064\nQmvav 212,120,40\nbuffer_load_dwordx4 v64, s[48:51], s88 offen offset:2048 lds\nQmvaa 212,124,192\nQta 170,61,50312\nQmvav 216,128,40\nQmvaa 216,132,192\n"
"Qta 172,61,53928\ns_add_u32 s88, s86, 59520\nQmvav 220,136,40\nbuffer_load_dwordx4 v64, s[48:51], s88 offen offset:3072 lds\nQmvaa 220,140,192\nQta 174,61,54440\nQmvav 240,112,44\nQmvaa 240,116,196\n"
"s_add_u32 m0, s63, 4096\ns_add_u32 s88, s86, 83072\nQmvav 244,120,44\nbuffer_load_dwordx4 v64, s[48:51], s88 offen lds\nQmvaa 244,124,196\nQmvav 248,128,44\nQmvaa 248,132,196\ns_add_u32 s88, s86, 102528\n"
"Qmvav 252,136,44\nbuffer_load_dwordx4 v64, s[48:51], s88 offen offset:1024 lds\nQmvaa 252,140,196\nQmaav 224,112,48\nQmaaa 224,116,200\ns_add_u32 s88, s86, 121984\nQmaav 228,120,48\nbuffer_load_dwordx4 v64, s[48:51], s88 offen offset:2048 lds\n"
"Qmaaa 228,124,200\nQmaav 232,128,48\nQmaaa 232,132,200\nQmaav 236,136,48\nQmaaa 236,140,200\ns_waitcnt lgkmcnt(0)\nQmvav 96,144,24\nQmvaa 96,148,176\n"
"Qmvav 100,152,24\nQmvaa 100,156,176\nQmvav 104,160,24\nQmvaa 104,164,176\nQmvav 108,168,24\nQmvaa 108,172,176\ns_waitcnt lgkmcnt(0)\nQmvav 128,144,28\n"
"s_waitcnt vmcnt(16)\ns_barrier\nQmvaa 128,148,180\nQdv 24,63,33024\ns_add_u32 m0, s62, 49536\ns_add_u32 s69, s87, 4718720\nQmvav 132,152,28\nbuffer_load_dwordx4 v62, s[52:55], s69 offen lds\n"
"Qmvaa 132,156,180\nQda 176,63,33536\nQmvav 136,160,28\nQta 112,61,0\nQmvaa 136,164,180\ns_add_u32 s69, s87, 4783224\nQmvav 140,168,28\nbuffer_load_dwordx4 v62, s[52:55], s69 offen offset:1032 lds\n"
"Qmvaa 140,172,180\nQdv 28,63,34048\nQmvav 160,144,32\nQda 180,63,34560\nQmvaa 160,148,184\nQta 114,61,512\ns_add_u32 s69, s87, 4847728\nQmvav 164,152,32\n"
"buffer_load_dwordx4 v62, s[52:55], s69 offen offset:2064 lds\nQmvaa 164,156,184\nQta 116,61,4128\nQmvav 168,160,32\nQmvaa 168,164,184\nQta 118,61,4640\ns_add_u32 s69, s87, 4912232\nQmvav 172,168,32\n"
"buffer_load_dwordx4 v62, s[52:55], s69 offen offset:3096 lds\nQmvaa 172,172,184\nQdv 32,63,35072\nQmvav 192,144,36\nQda 184,63,35584\nQmvaa 192,148,188\nQta 120,61,8\ns_add_u32 m0, s61, 8192\n"
"Qmvav 196,152,36\ns_cbranch_vccnz .Ld0q7\nbuffer_load_dwordx4 v72, s[44:47], s60 offen offset:3072 nt lds\ns_add_u32 s60, s60, 8192\ns_add_u32 s90, s60, 896\n.Ld0q7:\nQmvaa 196,156,188\nQta 122,61,520\n"
"Qmvav 200,160,36\nQmvaa 200,164,188\nQta 124,61,4136\nQmvav 204,168,36\nQdv 36,63,36096\nQmvaa 204,172,188\nQda 188,63,36608\nQmvav 224,144,40\n"
"Qta 126,61,4648\nQmvaa 224,148,192\nQmvav 228,152,40\nQta 128,61,256\nQmvaa 228,156,192\nQmvav 232,160,40\nQta 130,61,768\nQmvaa 232,164,192\n"
"Qmvav 236,168,40\nQdv 40,63,37120\nQmvaa 236,172,192\nQda 192,63,37632\nQmaav 208,144,44\nQta 132,61,4384\nQmaaa 208,148,196\nQmaav 212,152,44\n"
"Qta 134,61,4896\nQmaaa 212,156,196\nQmaav 216,160,44\nQta 136,61,264\nQmaaa 216,164,196\nQmaav 220,168,44\nQdv 44,63,38144\nQmaaa 220,172,196\n"
"Qda 196,63,38656\nQmaav 240,144,48\nQta 138,61,776\nQmaaa 240,148,200\nQmaav 244,152,48\nQta 140,61,4392\nQmaaa 244,156,200\nQmaav 248,160,48\n"
"Qta 142,61,4904\nQmaaa 248,164,200\nQmaav 252,168,48\nQdv 48,63,39168\nQmaaa 252,172,200\nQda 200,63,39680\ns_waitcnt lgkmcnt(2)\nQmvav 80,112,24\n"
"Qmvaa 80,116,176\nQmvav 84,120,24\nQmvaa 84,124,176\nQmvav 88,128,24\nQmvaa 88,132,176\nQmvav 92,136,24\nQmvaa 92,140,176\ns_waitcnt lgkmcnt(0)\n"
"Qmvav 112,112,28\ns_waitcnt vmcnt(15)\ns_barrier\nQmvaa 112,116,180\nQta 144,61,16512\ns_mov_b32 s56, s94\ns_mov_b32 s57, s95\ns_mov_b32 s58, s75\n"
"s_mov_b32 s94, 0\ns_add_u32 m0, %10, 160768\nQmvav 116,120,28\nbuffer_load_dwordx4 v60, s[56:59], s94 offen lds\nQmvaa 116,124,180\nQta 146,61,17024\nQmvav 120,128,28\nQmvaa 120,132,180\n"
"Qta 148,61,20640\ns_add_u32 s94, s94, 4096\nQmvav 124,136,28\nbuffer_load_dwordx4 v60, s[56:59], s94 offen lds\nQmvaa 124,140,180\nQta 150,61,21152\nQmvav 144,112,32\nQmvaa 144,116,184\n"
"Qta 152,61,16520\ns_add_u32 s94, s94, 4096\nQmvav 148,120,32\nbuffer_load_dwordx4 v60, s[56:59], s94 offen lds\nQmvaa 148,124,184\nQta 154,61,17032\nQmvav 152,128,32\nQmvaa 152,132,184\n"
"Qta 156,61,20648\ns_add_u32 s94, s94, 4096\nQmvav 156,136,32\nbuffer_load_dwordx4 v60, s[56:59], s94 offen lds\nQmvaa 156,140,184\nQta 158,61,21160\nQmvav 176,112,36\nQmvaa 176,116,188\n"
"Qta 160,61,16768\ns_add_u32 s94, s94, 4096\nQmvav 180,120,36\nbuffer_load_dwordx4 v60, s[56:59], s94 offen lds\nQmvaa 180,124,188\nQta 162,61,17280\nQmvav 184,128,36\nQmvaa 184,132,188\n"
"Qta 164,61,20896\ns_add_u32 s94, s94, 4096\nQmvav 188,136,36\nbuffer_load_dwordx4 v60, s[56:59], s94 offen lds\nQmvaa 188,140,188\nQta 166,61,21408\nQmvav 208,112,40\nQmvaa 208,116,192\n"
"Qta 168,61,16776\ns_add_u32 s94, s94, 4096\nQmvav 212,120,40\nbuffer_load_dwordx4 v60, s[56:59], s94 offen lds\nQmvaa 212,124,192\nQta 170,61,17288\nQmvav 216,128,40\nQmvaa 216,132,192\n"
"Qta 172,61,20904\ns_add_u32 s94, s94, 4096\nQmvav 220,136,40\nbuffer_load_dwordx4 v60, s[56:59], s94 offen lds\nQmvaa 220,140,192\nQta 174,61,21416\nQmvav 240,112,44\nQmvaa 240,116,196\n"
"s_add_u32 s94, s94, 4096\nQmvav 244,120,44\nbuffer_load_dwordx4 v60, s[56:59], s94 offen lds\nQmvaa 244,124,196\nQmvav 248,128,44\nQmvaa 248,132,196\ns_add_u32 s94, s94, 4096\nQmvav 252,136,44\n"
"buffer_load_dwordx4 v60, s[56:59], s94 offen lds\nQmvaa 252,140,196\nQmaav 224,112,48\nQmaaa 224,116,200\nQmaav 228,120,48\nQmaaa 228,124,200\nQmaav 232,128,48\nQmaaa 232,132,200\n"
"Qmaav 236,136,48\nQmaaa 236,140,200\ns_waitcnt lgkmcnt(0)\nQmvav 96,144,24\nQmvaa 96,148,176\nQmvav 100,152,24\nQmvaa 100,156,176\n.Lpg3:\n"
"s_cbranch_execz .Ltr0\nQmvav 104,160,24\nQmvaa 104,164,176\nQmvav 108,168,24\nQmvaa 108,172,176\ns_waitcnt lgkmcnt(0)\nQmvav 128,144,28\ns_waitcnt vmcnt(14)\n"
"s_barrier\nQmvaa 128,148,180\nQdv 24,63,0\ns_add_u32 m0, s61, 33024\nQmvav 132,152,28\nbuffer_load_dwordx4 v69, s[40:43], s90 offen nt lds\nQmvaa 132,156,180\nQda 176,63,512\n"
"Qmvav 136,160,28\nQta 112,61,33024\nQmvaa 136,164,180\nQmvav 140,168,28\nbuffer_load_dwordx4 v69, s[40:43], s60 offen offset:1024 nt lds\nQmvaa 140,172,180\nQdv 28,63,1024\nQmvav 160,144,32\n"
"Qda 180,63,1536\nQmvaa 160,148,184\nQta 114,61,33536\nQmvav 164,152,32\nbuffer_load_dwordx4 v69, s[40:43], s90 offen offset:2048 nt lds\nQmvaa 164,156,184\nQta 116,61,37152\nQmvav 168,160,32\n"
"Qmvaa 168,164,184\nQta 118,61,37664\nQmvav 172,168,32\nbuffer_load_dwordx4 v69, s[40:43], s60 offen offset:3072 nt lds\nQmvaa 172,172,184\nQdv 32,63,2048\nQmvav 192,144,36\nQda 184,63,2560\n"
"Qmvaa 192,148,188\nQta 120,61,33032\ns_add_u32 m0, s61, 41216\nQmvav 196,152,36\nbuffer_load_dwordx4 v72, s[44:47], s90 offen nt lds\nQmvaa 196,156,188\nQta 122,61,33544\nQmvav 200,160,36\n"
"Qmvaa 200,164,188\nQta 124,61,37160\nQmvav 204,168,36\nbuffer_load_dwordx4 v72, s[44:47], s60 offen offset:1024 nt lds\nQmvaa 204,172,188\nQdv 36,63,3072\nQmvav 224,144,40\nQda 188,63,3584\n"
"Qmvaa 224,148,192\nQta 126,61,37672\nQmvav 228,152,40\nbuffer_load_dwordx4 v72, s[44:47], s90 offen offset:2048 nt lds\nQmvaa 228,156,192\nQta 128,61,33280\nQmvav 232,160,40\nQmvaa 232,164,192\n"
"Qta 130,61,33792\nQmvav 236,168,40\nbuffer_load_dwordx4 v72, s[44:47], s60 offen offset:3072 nt lds\ns_add_u32 s60, s60, 8192\ns_add_u32 s90, s60, 896\nQmvaa 236,172,192\nQdv 40,63,4096\nQmaav 208,144,44\n"
"Qda 192,63,4608\nQmaaa 208,148,196\nQta 132,61,37408\nQmaav 212,152,44\nQmaaa 212,156,196\nQta 134,61,37920\nQmaav 216,160,44\nQmaaa 216,164,196\n"
"Qta 136,61,33288\nQmaav 220,168,44\nQdv 44,63,5120\nQmaaa 220,172,196\nQda 196,63,5632\nQmaav 240,144,48\nQta 138,61,33800\nQmaaa 240,148,200\n"
"Qmaav 244,152,48\nQta 140,61,37416\nQmaaa 244,156,200\nQmaav 248,160,48\nQta 142,61,37928\nQmaaa 248,164,200\nQmaav 252,168,48\nQdv 48,63,6144\n"
"Qmaaa 252,172,200\nQda 200,63,6656\ns_waitcnt lgkmcnt(2)\nQmvav 80,112,24\nQmvaa 80,116,176\nQmvav 84,120,24\nQmvaa 84,124,176\nQmvav 88,128,24\n"
"Qmvaa 88,132,176\nQmvav 92,136,24\nQmvaa 92,140,176\ns_waitcnt lgkmcnt(0)\nQmvav 112,112,28\ns_waitcnt vmcnt(18)\ns_barrier\nQmvaa 112,116,180\n"
"Qta 144,61,49536\nQmvav 116,120,28\nQmvaa 116,124,180\nQta 146,61,50048\nQmvav 120,128,28\nQmvaa 120,132,180\nQta 148,61,53664\nQmvav 124,136,28\n"
"Qmvaa 124,140,180\nQta 150,61,54176\nQmvav 144,112,32\nQmvaa 144,116,184\nQta 152,61,49544\nQmvav 148,120,32\nQmvaa 148,124,184\nQta 154,61,50056\n"
"Qmvav 152,128,32\nQmvaa 152,132,184\nQta 156,61,53672\nQmvav 156,136,32\nQmvaa 156,140,184\nQta 158,61,54184\nQmvav 176,112,36\nQmvaa 176,116,188\n"
"Qta 160,61,49792\nQmvav 180,120,36\nQmvaa 180,124,188\nQta 162,61,50304\nQmvav 184,128,36\nQmvaa 184,132,188\nQta 164,61,53920\nQmvav 188,136,36\n"
"Qmvaa 188,140,188\nQta 166,61,54432\nQmvav 208,112,40\nQmvaa 208,116,192\nQta 168,61,49800\nQmvav 212,120,40\nQmvaa 212,124,192\nQta 170,61,50312\n"
"Qmvav 216,128,40\nQmvaa 216,132,192\nQta 172,61,53928\nQmvav 220,136,40\nQmvaa 220,140,192\nQta 174,61,54440\nQmvav 240,112,44\nQmvaa 240,116,196\n"
"Qmvav 244,120,44\nQmvaa 244,124,196\nQmvav 248,128,44\nQmvaa 248,132,196\nQmvav 252,136,44\nQmvaa 252,140,196\nQmaav 224,112,48\nQmaaa 224,116,200\n"
"Qmaav 228,120,48\nQmaaa 228,124,200\nQmaav 232,128,48\nQmaaa 232,132,200\nQmaav 236,136,48\nQmaaa 236,140,200\ns_waitcnt lgkmcnt(0)\nQmvav 96,144,24\n"
"Qmvaa 96,148,176\nQmvav 100,152,24\nQmvaa 100,156,176\nQmvav 104,160,24\nQmvaa 104,164,176\nQmvav 108,168,24\nQmvaa 108,172,176\ns_waitcnt lgkmcnt(0)\n"
"Qmvav 128,144,28\ns_waitcnt vmcnt(18)\ns_barrier\nQmvaa 128,148,180\nQda 112,68,0\ns_add_u32 m0, s61, 66048\nv_accvgpr_write_b32 a176, 0\nQmvav 132,152,28\n"
"buffer_load_dwordx4 v69, s[40:43], s90 offen nt lds\nQmvaa 132,156,180\nQda 116,68,512\nv_accvgpr_write_b32 a177, 0\nQmvav 136,160,28\nQmvaa 136,164,180\nQda 120,68,1024\nv_accvgpr_write_b32 a178, 0\n"
"Qmvav 140,168,28\nbuffer_load_dwordx4 v69, s[40:43], s60 offen offset:1024 nt lds\nQmvaa 140,172,180\nQda 124,68,1536\nv_accvgpr_write_b32 a179, 0\nQmvav 160,144,32\nQmvaa 160,148,184\nQda 128,68,2048\n"
"v_accvgpr_write_b32 a180, 0\nQmvav 164,152,32\nbuffer_load_dwordx4 v69, s[40:43], s90 offen offset:2048 nt lds\nQmvaa 164,156,184\nQda 132,68,2560\nv_accvgpr_write_b32 a181, 0\nQmvav 168,160,32\nQmvaa 168,164,184\n"
"Qda 136,68,3072\nv_accvgpr_write_b32 a182, 0\nQmvav 172,168,32\nbuffer_load_dwordx4 v69, s[40:43], s60 offen offset:3072 nt lds\nQmvaa 172,172,184\nQda 140,68,3584\nv_accvgpr_write_b32 a183, 0\nQmvav 192,144,36\n"
"Qmvaa 192,148,188\ns_add_u32 m0, s61, 74240\nv_accvgpr_write_b32 a184, 0\nQmvav 196,152,36\nbuffer_load_dwordx4 v72, s[44:47], s90 offen nt lds\nQmvaa 196,156,188\nv_accvgpr_write_b32 a185, 0\nQmvav 200,160,36\n"
"Qmvaa 200,164,188\nv_accvgpr_write_b32 a186, 0\nQmvav 204,168,36\nbuffer_load_dwordx4 v72, s[44:47], s60 offen offset:1024 nt lds\nQmvaa 204,172,188\nv_accvgpr_write_b32 a187, 0\nQmvav 224,144,40\nQmvaa 224,148,192\n"
"v_accvgpr_write_b32 a188, 0\nQmvav 228,152,40\nbuffer_load_dwordx4 v72, s[44:47], s90 offen offset:2048 nt lds\nQmvaa 228,156,192\nv_accvgpr_write_b32 a189, 0\nQmvav 232,160,40\nQmvaa 232,164,192\nv_accvgpr_write_b32 a190, 0\n"
"Qmvav 236,168,40\nbuffer_load_dwordx4 v72, s[44:47], s60 offen offset:3072 nt lds\ns_add_u32 s60, s60, 8192\ns_add_u32 s90, s60, 896\nQmvaa 236,172,192\nv_accvgpr_write_b32 a191, 0\nQmaav 208,144,44\nQmaaa 208,148,196\n"
"v_accvgpr_write_b32 a192, 0\nQmaav 212,152,44\nQmaaa 212,156,196\nv_accvgpr_write_b32 a193, 0\nQmaav 216,160,44\nQmaaa 216,164,196\nv_accvgpr_write_b32 a194, 0\nQmaav 220,168,44\n"
"Qmaaa 220,172,196\nv_accvgpr_write_b32 a195, 0\nQmaav 240,144,48\nQmaaa 240,148,200\nv_accvgpr_write_b32 a196, 0\nQmaav 244,152,48\nQmaaa 244,156,200\nv_accvgpr_write_b32 a197, 0\n"
"Qmaav 248,160,48\nQmaaa 248,164,200\nv_accvgpr_write_b32 a198, 0\nQmaav 252,168,48\nQmaaa 252,172,200\nv_accvgpr_write_b32 a199, 0\nQta 148,70,1024\nQta 150,70,3072\n"
"Qta 156,70,1280\nQta 158,70,3328\nQta 164,70,1536\nQta 166,70,3584\nQta 172,70,1792\nQta 174,70,3840\nv_pk_mul_f32 v[80:81], v[80:81], s[72:73] op_sel_hi:[1,0]\nv_pk_mul_f32 v[82:83], v[82:83], s[72:73] op_sel_hi:[1,0]\n"
"v_pk_mul_f32 v[84:85], v[84:85], s[72:73] op_sel_hi:[1,0]\nv_pk_mul_f32 v[86:87], v[86:87], s[72:73] op_sel_hi:[1,0]\nQk 80,80,81\nQk 81,82,83\nQk 82,84,85\nQk 83,86,87\nv_accvgpr_write_b32 a0, v80\nv_accvgpr_write_b32 a1, v81\n"
"v_accvgpr_write_b32 a2, v82\nv_accvgpr_write_b32 a3, v83\nv_pk_mul_f32 v[88:89], v[88:89], s[72:73] op_sel_hi:[1,0]\nv_pk_mul_f32 v[90:91], v[90:91], s[72:73] op_sel_hi:[1,0]\nv_pk_mul_f32 v[92:93], v[92:93], s[72:73] op_sel_hi:[1,0]\nv_pk_mul_f32 v[94:95], v[94:95], s[72:73] op_sel_hi:[1,0]\nQk 88,88,89\nQk 89,90,91\n"
"Qk 90,92,93\nQk 91,94,95\nv_accvgpr_write_b32 a4, v88\nv_accvgpr_write_b32 a5, v89\nv_accvgpr_write_b32 a6, v90\nv_accvgpr_write_b32 a7, v91\nv_pk_mul_f32 v[96:97], v[96:97], s[72:73] op_sel_hi:[1,0]\nv_pk_mul_f32 v[98:99], v[98:99], s[72:73] op_sel_hi:[1,0]\n"
"v_pk_mul_f32 v[100:101], v[100:101], s[72:73] op_sel_hi:[1,0]\nv_pk_mul_f32 v[102:103], v[102:103], s[72:73] op_sel_hi:[1,0]\nQk 96,96,97\nQk 97,98,99\nQk 98,100,101\nQk 99,102,103\nv_accvgpr_write_b32 a8, v96\nv_accvgpr_write_b32 a9, v97\n"
"v_accvgpr_write_b32 a10, v98\nv_accvgpr_write_b32 a11, v99\nv_pk_mul_f32 v[104:105], v[104:105], s[72:73] op_sel_hi:[1,0]\nv_pk_mul_f32 v[106:107], v[106:107], s[72:73] op_sel_hi:[1,0]\nv_pk_mul_f32 v[108:109], v[108:109], s[72:73] op_sel_hi:[1,0]\nv_pk_mul_f32 v[110:111], v[110:111], s[72:73] op_sel_hi:[1,0]\nQk 104,104,105\nQk 105,106,107\n"
"Qk 106,108,109\nQk 107,110,111\nv_accvgpr_write_b32 a12, v104\nv_accvgpr_write_b32 a13, v105\nv_accvgpr_write_b32 a14, v106\nv_accvgpr_write_b32 a15, v107\nv_pk_mul_f32 v[112:113], v[112:113], s[72:73] op_sel_hi:[1,0]\nv_pk_mul_f32 v[114:115], v[114:115], s[72:73] op_sel_hi:[1,0]\n"
"v_pk_mul_f32 v[116:117], v[116:117], s[72:73] op_sel_hi:[1,0]\nv_pk_mul_f32 v[118:119], v[118:119], s[72:73] op_sel_hi:[1,0]\nQk 112,112,113\nQk 113,114,115\nQk 114,116,117\nQk 115,118,119\nv_accvgpr_write_b32 a16, v112\nv_accvgpr_write_b32 a17, v113\n"
"v_accvgpr_write_b32 a18, v114\nv_accvgpr_write_b32 a19, v115\nv_pk_mul_f32 v[120:121], v[120:121], s[72:73] op_sel_hi:[1,0]\nv_pk_mul_f32 v[122:123], v[122:123], s[72:73] op_sel_hi:[1,0]\nv_pk_mul_f32 v[124:125], v[124:125], s[72:73] op_sel_hi:[1,0]\nv_pk_mul_f32 v[126:127], v[126:127], s[72:73] op_sel_hi:[1,0]\nQk 120,120,121\nQk 121,122,123\n"
"Qk 122,124,125\nQk 123,126,127\nv_accvgpr_write_b32 a20, v120\nv_accvgpr_write_b32 a21, v121\nv_accvgpr_write_b32 a22, v122\nv_accvgpr_write_b32 a23, v123\nv_pk_mul_f32 v[128:129], v[128:129], s[72:73] op_sel_hi:[1,0]\nv_pk_mul_f32 v[130:131], v[130:131], s[72:73] op_sel_hi:[1,0]\n"
"v_pk_mul_f32 v[132:133], v[132:133], s[72:73] op_sel_hi:[1,0]\nv_pk_mul_f32 v[134:135], v[134:135], s[72:73] op_sel_hi:[1,0]\nQk 128,128,129\nQk 129,130,131\nQk 130,132,133\nQk 131,134,135\nv_accvgpr_write_b32 a24, v128\nv_accvgpr_write_b32 a25, v129\n"
"v_accvgpr_write_b32 a26, v130\nv_accvgpr_write_b32 a27, v131\nv_pk_mul_f32 v[136:137], v[136:137], s[72:73] op_sel_hi:[1,0]\nv_pk_mul_f32 v[138:139], v[138:139], s[72:73] op_sel_hi:[1,0]\nv_pk_mul_f32 v[140:141], v[140:141], s[72:73] op_sel_hi:[1,0]\nv_pk_mul_f32 v[142:143], v[142:143], s[72:73] op_sel_hi:[1,0]\nQk 136,136,137\nQk 137,138,139\n"
"Qk 138,140,141\nQk 139,142,143\nv_accvgpr_write_b32 a28, v136\nv_accvgpr_write_b32 a29, v137\nv_accvgpr_write_b32 a30, v138\nv_accvgpr_write_b32 a31, v139\nv_pk_mul_f32 v[144:145], v[144:145], s[72:73] op_sel_hi:[1,0]\nv_pk_mul_f32 v[146:147], v[146:147], s[72:73] op_sel_hi:[1,0]\n"
"v_pk_mul_f32 v[148:149], v[148:149], s[72:73] op_sel_hi:[1,0]\nv_pk_mul_f32 v[150:151], v[150:151], s[72:73] op_sel_hi:[1,0]\nQk 144,144,145\nQk 145,146,147\nQk 146,148,149\nQk 147,150,151\nv_accvgpr_write_b32 a32, v144\nv_accvgpr_write_b32 a33, v145\n"
"v_accvgpr_write_b32 a34, v146\nv_accvgpr_write_b32 a35, v147\nv_pk_mul_f32 v[152:153], v[152:153], s[72:73] op_sel_hi:[1,0]\nv_pk_mul_f32 v[154:155], v[154:155], s[72:73] op_sel_hi:[1,0]\nv_pk_mul_f32 v[156:157], v[156:157], s[72:73] op_sel_hi:[1,0]\nv_pk_mul_f32 v[158:159], v[158:159], s[72:73] op_sel_hi:[1,0]\nQk 152,152,153\nQk 153,154,155\n"
"Qk 154,156,157\nQk 155,158,159\nv_accvgpr_write_b32 a36, v152\nv_accvgpr_write_b32 a37, v153\nv_accvgpr_write_b32 a38, v154\nv_accvgpr_write_b32 a39, v155\nv_pk_mul_f32 v[160:161], v[160:161], s[72:73] op_sel_hi:[1,0]\nv_pk_mul_f32 v[162:163], v[162:163], s[72:73] op_sel_hi:[1,0]\n"
"v_pk_mul_f32 v[164:165], v[164:165], s[72:73] op_sel_hi:[1,0]\nv_pk_mul_f32 v[166:167], v[166:167], s[72:73] op_sel_hi:[1,0]\nQk 160,160,161\nQk 161,162,163\nQk 162,164,165\nQk 163,166,167\nv_accvgpr_write_b32 a40, v160\nv_accvgpr_write_b32 a41, v161\n"
"v_accvgpr_write_b32 a42, v162\nv_accvgpr_write_b32 a43, v163\nv_pk_mul_f32 v[168:169], v[168:169], s[72:73] op_sel_hi:[1,0]\nv_pk_mul_f32 v[170:171], v[170:171], s[72:73] op_sel_hi:[1,0]\nv_pk_mul_f32 v[172:173], v[172:173], s[72:73] op_sel_hi:[1,0]\nv_pk_mul_f32 v[174:175], v[174:175], s[72:73] op_sel_hi:[1,0]\nQk 168,168,169\nQk 169,170,171\n"
"Qk 170,172,173\nQk 171,174,175\nv_accvgpr_write_b32 a44, v168\nv_accvgpr_write_b32 a45, v169\nv_accvgpr_write_b32 a46, v170\nv_accvgpr_write_b32 a47, v171\nv_pk_mul_f32 v[176:177], v[176:177], s[72:73] op_sel_hi:[1,0]\nv_pk_mul_f32 v[178:179], v[178:179], s[72:73] op_sel_hi:[1,0]\n"
"v_pk_mul_f32 v[180:181], v[180:181], s[72:73] op_sel_hi:[1,0]\nv_pk_mul_f32 v[182:183], v[182:183], s[72:73] op_sel_hi:[1,0]\nQk 176,176,177\nQk 177,178,179\nQk 178,180,181\nQk 179,182,183\nv_accvgpr_write_b32 a48, v176\nv_accvgpr_write_b32 a49, v177\n"
"v_accvgpr_write_b32 a50, v178\nv_accvgpr_write_b32 a51, v179\nv_pk_mul_f32 v[184:185], v[184:185], s[72:73] op_sel_hi:[1,0]\nv_pk_mul_f32 v[186:187], v[186:187], s[72:73] op_sel_hi:[1,0]\nv_pk_mul_f32 v[188:189], v[188:189], s[72:73] op_sel_hi:[1,0]\nv_pk_mul_f32 v[190:191], v[190:191], s[72:73] op_sel_hi:[1,0]\nQk 184,184,185\nQk 185,186,187\n"
"Qk 186,188,189\nQk 187,190,191\nv_accvgpr_write_b32 a52, v184\nv_accvgpr_write_b32 a53, v185\nv_accvgpr_write_b32 a54, v186\nv_accvgpr_write_b32 a55, v187\nv_pk_mul_f32 v[192:193], v[192:193], s[72:73] op_sel_hi:[1,0]\nv_pk_mul_f32 v[194:195], v[194:195], s[72:73] op_sel_hi:[1,0]\n"
"v_pk_mul_f32 v[196:197], v[196:197], s[72:73] op_sel_hi:[1,0]\nv_pk_mul_f32 v[198:199], v[198:199], s[72:73] op_sel_hi:[1,0]\nQk 192,192,193\nQk 193,194,195\nQk 194,196,197\nQk 195,198,199\nv_accvgpr_write_b32 a56, v192\nv_accvgpr_write_b32 a57, v193\n"
"v_accvgpr_write_b32 a58, v194\nv_accvgpr_write_b32 a59, v195\nv_pk_mul_f32 v[200:201], v[200:201], s[72:73] op_sel_hi:[1,0]\nv_pk_mul_f32 v[202:203], v[202:203], s[72:73] op_sel_hi:[1,0]\nv_pk_mul_f32 v[204:205], v[204:205], s[72:73] op_sel_hi:[1,0]\nv_pk_mul_f32 v[206:207], v[206:207], s[72:73] op_sel_hi:[1,0]\nQk 200,200,201\nQk 201,202,203\n"
"Qk 202,204,205\nQk 203,206,207\nv_accvgpr_write_b32 a60, v200\nv_accvgpr_write_b32 a61, v201\nv_accvgpr_write_b32 a62, v202\nv_accvgpr_write_b32 a63, v203\nv_pk_mul_f32 v[208:209], v[208:209], s[72:73] op_sel_hi:[1,0]\nv_pk_mul_f32 v[210:211], v[210:211], s[72:73] op_sel_hi:[1,0]\n"
"v_pk_mul_f32 v[212:213], v[212:213], s[72:73] op_sel_hi:[1,0]\nv_pk_mul_f32 v[214:215], v[214:215], s[72:73] op_sel_hi:[1,0]\nQk 208,208,209\nQk 209,210,211\nQk 210,212,213\nQk 211,214,215\nv_accvgpr_write_b32 a64, v208\nv_accvgpr_write_b32 a65, v209\n"
"v_accvgpr_write_b32 a66, v210\nv_accvgpr_write_b32 a67, v211\nv_pk_mul_f32 v[216:217], v[216:217], s[72:73] op_sel_hi:[1,0]\nv_pk_mul_f32 v[218:219], v[218:219], s[72:73] op_sel_hi:[1,0]\nv_pk_mul_f32 v[220:221], v[220:221], s[72:73] op_sel_hi:[1,0]\nv_pk_mul_f32 v[222:223], v[222:223], s[72:73] op_sel_hi:[1,0]\nQk 216,216,217\nQk 217,218,219\n"
"Qk 218,220,221\nQk 219,222,223\nv_accvgpr_write_b32 a68, v216\nv_accvgpr_write_b32 a69, v217\nv_accvgpr_write_b32 a70, v218\nv_accvgpr_write_b32 a71, v219\nv_pk_mul_f32 v[224:225], v[224:225], s[72:73] op_sel_hi:[1,0]\nv_pk_mul_f32 v[226:227], v[226:227], s[72:73] op_sel_hi:[1,0]\n"
"v_pk_mul_f32 v[228:229], v[228:229], s[72:73] op_sel_hi:[1,0]\nv_pk_mul_f32 v[230:231], v[230:231], s[72:73] op_sel_hi:[1,0]\nQk 224,224,225\nQk 225,226,227\nQk 226,228,229\nQk 227,230,231\nv_accvgpr_write_b32 a72, v224\nv_accvgpr_write_b32 a73, v225\n"
"v_accvgpr_write_b32 a74, v226\nv_accvgpr_write_b32 a75, v227\nv_pk_mul_f32 v[232:233], v[232:233], s[72:73] op_sel_hi:[1,0]\nv_pk_mul_f32 v[234:235], v[234:235], s[72:73] op_sel_hi:[1,0]\nv_pk_mul_f32 v[236:237], v[236:237], s[72:73] op_sel_hi:[1,0]\nv_pk_mul_f32 v[238:239], v[238:239], s[72:73] op_sel_hi:[1,0]\n.Lpg4:\ns_cbranch_execz .Ltr9\n"
"Qk 232,232,233\nQk 233,234,235\nQk 234,236,237\nQk 235,238,239\nv_accvgpr_write_b32 a76, v232\nv_accvgpr_write_b32 a77, v233\nv_accvgpr_write_b32 a78, v234\nv_accvgpr_write_b32 a79, v235\n"
"v_pk_mul_f32 v[240:241], v[240:241], s[72:73] op_sel_hi:[1,0]\nv_pk_mul_f32 v[242:243], v[242:243], s[72:73] op_sel_hi:[1,0]\nv_pk_mul_f32 v[244:245], v[244:245], s[72:73] op_sel_hi:[1,0]\nv_pk_mul_f32 v[246:247], v[246:247], s[72:73] op_sel_hi:[1,0]\nQk 240,240,241\nQk 241,242,243\nQk 242,244,245\nQk 243,246,247\n"
"v_accvgpr_write_b32 a80, v240\nv_accvgpr_write_b32 a81, v241\nv_accvgpr_write_b32 a82, v242\nv_accvgpr_write_b32 a83, v243\nv_pk_mul_f32 v[248:249], v[248:249], s[72:73] op_sel_hi:[1,0]\nv_pk_mul_f32 v[250:251], v[250:251], s[72:73] op_sel_hi:[1,0]\nv_pk_mul_f32 v[252:253], v[252:253], s[72:73] op_sel_hi:[1,0]\nv_pk_mul_f32 v[254:255], v[254:255], s[72:73] op_sel_hi:[1,0]\n"
"Qk 248,248,249\nQk 249,250,251\nQk 250,252,253\nQk 251,254,255\nv_accvgpr_write_b32 a84, v248\nv_accvgpr_write_b32 a85, v249\nv_accvgpr_write_b32 a86, v250\nv_accvgpr_write_b32 a87, v251\n"
"v_accvgpr_read_b32 v0, a208\nv_accvgpr_read_b32 v1, a209\nv_accvgpr_read_b32 v2, a210\nv_accvgpr_read_b32 v3, a211\nv_accvgpr_read_b32 v4, a212\nv_accvgpr_read_b32 v5, a213\nv_accvgpr_read_b32 v6, a214\nv_accvgpr_read_b32 v7, a215\n"
"v_pk_mul_f32 v[0:1], v[0:1], s[72:73] op_sel_hi:[1,0]\nv_pk_mul_f32 v[2:3], v[2:3], s[72:73] op_sel_hi:[1,0]\nv_pk_mul_f32 v[4:5], v[4:5], s[72:73] op_sel_hi:[1,0]\nv_pk_mul_f32 v[6:7], v[6:7], s[72:73] op_sel_hi:[1,0]\nQk 0,0,1\nQk 1,2,3\nQk 2,4,5\nQk 3,6,7\n"
"v_accvgpr_write_b32 a88, v0\nv_accvgpr_write_b32 a89, v1\nv_accvgpr_write_b32 a90, v2\nv_accvgpr_write_b32 a91, v3\nv_accvgpr_read_b32 v8, a216\nv_accvgpr_read_b32 v9, a217\nv_accvgpr_read_b32 v10, a218\nv_accvgpr_read_b32 v11, a219\n"
"v_accvgpr_read_b32 v12, a220\nv_accvgpr_read_b32 v13, a221\nv_accvgpr_read_b32 v14, a222\nv_accvgpr_read_b32 v15, a223\nv_pk_mul_f32 v[8:9], v[8:9], s[72:73] op_sel_hi:[1,0]\nv_pk_mul_f32 v[10:11], v[10:11], s[72:73] op_sel_hi:[1,0]\nv_pk_mul_f32 v[12:13], v[12:13], s[72:73] op_sel_hi:[1,0]\nv_pk_mul_f32 v[14:15], v[14:15], s[72:73] op_sel_hi:[1,0]\n"
"Qk 8,8,9\nQk 9,10,11\nQk 10,12,13\nQk 11,14,15\nv_accvgpr_write_b32 a92, v8\nv_accvgpr_write_b32 a93, v9\nv_accvgpr_write_b32 a94, v10\nv_accvgpr_write_b32 a95, v11\n"
"v_accvgpr_read_b32 v0, a224\nv_accvgpr_read_b32 v1, a225\nv_accvgpr_read_b32 v2, a226\nv_accvgpr_read_b32 v3, a227\nv_accvgpr_read_b32 v4, a228\nv_accvgpr_read_b32 v5, a229\nv_accvgpr_read_b32 v6, a230\nv_accvgpr_read_b32 v7, a231\n"
"v_pk_mul_f32 v[0:1], v[0:1], s[72:73] op_sel_hi:[1,0]\nv_pk_mul_f32 v[2:3], v[2:3], s[72:73] op_sel_hi:[1,0]\nv_pk_mul_f32 v[4:5], v[4:5], s[72:73] op_sel_hi:[1,0]\nv_pk_mul_f32 v[6:7], v[6:7], s[72:73] op_sel_hi:[1,0]\nQk 0,0,1\nQk 1,2,3\nQk 2,4,5\nQk 3,6,7\n"
"v_accvgpr_write_b32 a96, v0\nv_accvgpr_write_b32 a97, v1\nv_accvgpr_write_b32 a98, v2\nv_accvgpr_write_b32 a99, v3\nv_accvgpr_read_b32 v8, a232\nv_accvgpr_read_b32 v9, a233\nv_accvgpr_read_b32 v10, a234\nv_accvgpr_read_b32 v11, a235\n"
"v_accvgpr_read_b32 v12, a236\nv_accvgpr_read_b32 v13, a237\nv_accvgpr_read_b32 v14, a238\nv_accvgpr_read_b32 v15, a239\nv_pk_mul_f32 v[8:9], v[8:9], s[72:73] op_sel_hi:[1,0]\nv_pk_mul_f32 v[10:11], v[10:11], s[72:73] op_sel_hi:[1,0]\nv_pk_mul_f32 v[12:13], v[12:13], s[72:73] op_sel_hi:[1,0]\nv_pk_mul_f32 v[14:15], v[14:15], s[72:73] op_sel_hi:[1,0]\n"
"Qk 8,8,9\nQk 9,10,11\nQk 10,12,13\nQk 11,14,15\nv_accvgpr_write_b32 a100, v8\nv_accvgpr_write_b32 a101, v9\nv_accvgpr_write_b32 a102, v10\nv_accvgpr_write_b32 a103, v11\n"
"v_accvgpr_read_b32 v0, a240\nv_accvgpr_read_b32 v1, a241\nv_accvgpr_read_b32 v2, a242\nv_accvgpr_read_b32 v3, a243\nv_accvgpr_read_b32 v4, a244\nv_accvgpr_read_b32 v5, a245\nv_accvgpr_read_b32 v6, a246\nv_accvgpr_read_b32 v7, a247\n"
"v_pk_mul_f32 v[0:1], v[0:1], s[72:73] op_sel_hi:[1,0]\nv_pk_mul_f32 v[2:3], v[2:3], s[72:73] op_sel_hi:[1,0]\nv_pk_mul_f32 v[4:5], v[4:5], s[72:73] op_sel_hi:[1,0]\nv_pk_mul_f32 v[6:7], v[6:7], s[72:73] op_sel_hi:[1,0]\nQk 0,0,1\nQk 1,2,3\nQk 2,4,5\nQk 3,6,7\n"
"v_accvgpr_write_b32 a104, v0\nv_accvgpr_write_b32 a105, v1\nv_accvgpr_write_b32 a106, v2\nv_accvgpr_write_b32 a107, v3\nv_accvgpr_read_b32 v8, a248\nv_accvgpr_read_b32 v9, a249\nv_accvgpr_read_b32 v10, a250\nv_accvgpr_read_b32 v11, a251\n"
"v_accvgpr_read_b32 v12, a252\nv_accvgpr_read_b32 v13, a253\nv_accvgpr_read_b32 v14, a254\nv_accvgpr_read_b32 v15, a255\nv_pk_mul_f32 v[8:9], v[8:9], s[72:73] op_sel_hi:[1,0]\nv_pk_mul_f32 v[10:11], v[10:11], s[72:73] op_sel_hi:[1,0]\nv_pk_mul_f32 v[12:13], v[12:13], s[72:73] op_sel_hi:[1,0]\nv_pk_mul_f32 v[14:15], v[14:15], s[72:73] op_sel_hi:[1,0]\n"
"Qk 8,8,9\nQk 9,10,11\nQk 10,12,13\nQk 11,14,15\nv_accvgpr_write_b32 a108, v8\nv_accvgpr_write_b32 a109, v9\nv_accvgpr_write_b32 a110, v10\nv_accvgpr_write_b32 a111, v11\n"
"v_mov_b32 v66, 0x3f803f80\nv_accvgpr_write_b32 a200, 0\nv_accvgpr_write_b32 a201, 0\nv_accvgpr_write_b32 a202, 0\nv_accvgpr_write_b32 a203, 0\ns_mov_b32 vcc_lo, 0x33333333\ns_mov_b32 vcc_hi, 0xcccccccc\nv_cndmask_b32 v66, 0, v66, vcc\n"
"v_accvgpr_write_b32 a204, v66\nv_accvgpr_write_b32 a205, v66\nv_accvgpr_write_b32 a206, v66\nv_accvgpr_write_b32 a207, v66\ns_add_u32 s87, s87, 2048\ns_waitcnt lgkmcnt(8)\nQzvaa 0,112,0\nQmvaa 0,116,4\n"
"Qmvaa 0,120,8\nQmvaa 0,124,12\nQzvaa 4,128,0\nQmvaa 4,132,4\nQmvaa 4,136,8\nQmvaa 4,140,12\ns_nop 3\nv_max3_f32 v66, v0, v1, v2\n"
"s_nop 2\nv_max3_f32 v66, v66, v3, v4\nv_max3_f32 v66, v66, v5, v6\nv_max_f32 v66, v66, v7\nQzvaa 8,112,16\nv_mov_b32 v67, v66\nQmvaa 8,116,20\ns_nop 0\n"
"v_permlane32_swap_b32 v66, v67\nv_max_f32 v66, v66, v67\nQmvaa 8,120,24\nv_mov_b32 v67, v66\nQmvaa 8,124,28\ns_nop 0\nv_permlane16_swap_b32 v66, v67\nv_max_f32 v66, v66, v67\n"
"Qzvaa 12,128,16\ns_nop 1\nv_max_f32_dpp v66, v66, v66 row_ror:8 row_mask:0xf bank_mask:0xf\ns_nop 1\nv_max_f32_dpp v66, v66, v66 row_ror:4 row_mask:0xf bank_mask:0xf\nQmvaa 12,132,20\ns_nop 1\nv_max_f32_dpp v66, v66, v66 row_ror:2 row_mask:0xf bank_mask:0xf\n"
"s_nop 1\nv_max_f32_dpp v66, v66, v66 row_ror:1 row_mask:0xf bank_mask:0xf\nQmvaa 12,136,24\nv_sub_f32 v56, 0xc1980000, v66\nv_mov_b32 v57, v56\nQmvaa 12,140,28\nv_mov_b32 v58, v56\nv_mov_b32 v59, v56\n"
"Qa 0,0,56\nQa 1,1,56\nQa 2,2,56\nQa 3,3,56\nQe 0\nQe 1\nQe 2\nQe 3\n"
"Qk 16,0,1\nQk 17,2,3\nQa 4,4,56\nQa 5,5,56\nQa 6,6,56\nQa 7,7,56\nQcvaa 0,112,32,56\nQe 4\n"
"Qmvaa 0,116,36\nQe 5\nQmvaa 0,120,40\nQe 6\nQe 7\nQmvaa 0,124,44\nQk 18,4,5\nQk 19,6,7\n"
"Qcvaa 4,128,32,56\nQa 8,8,56\nQa 9,9,56\nQmvaa 4,132,36\nQa 10,10,56\nQa 11,11,56\nQmvaa 4,136,40\nQe 8\n"
"Qmvaa 4,140,44\ns_waitcnt lgkmcnt(0)\nQa 12,12,56\nQzvva 96,16,148\nQe 9\nQzvva 100,16,156\nQe 10\nQzvva 104,16,164\n"
"Qe 11\nQzvva 108,16,172\nQk 20,8,9\nQk 21,10,11\nQcvaa 8,112,48,56\nQa 13,13,56\nQa 14,14,56\nQmvaa 8,116,52\n"
"Qa 15,15,56\nQmvaa 8,120,56\nQe 12\nQe 13\nQe 14\nQe 15\nQmvaa 8,124,60\nQk 22,12,13\n"
"Qk 23,14,15\nQcvaa 12,128,48,56\nQe 0\nQmvaa 12,132,52\nQe 1\nv_smfmac_f32_16x16x64_bf16 a[176:179], a[204:207], v[16:23], v77\nQk 24,0,1\nQmvaa 12,136,56\n"
"Qe 2\nQmvaa 12,140,60\nQe 3\ns_add_u32 m0, s61, 99072\nQzvva 128,20,148\nbuffer_load_dwordx4 v69, s[40:43], s90 offen nt lds\nQzvva 132,20,156\nQk 25,2,3\n"
"Qzvva 136,20,164\nQe 4\nbuffer_load_dwordx4 v69, s[40:43], s60 offen offset:1024 nt lds\nQzvva 140,20,172\nQe 5\nQcvaa 0,112,64,56\nQe 6\nQmvaa 0,116,68\n"
"Qe 7\nQmvaa 0,120,72\nQk 26,4,5\nQk 27,6,7\nQmvaa 0,124,76\nbuffer_load_dwordx4 v69, s[40:43], s90 offen offset:2048 nt lds\nQcvaa 4,128,64,56\nQe 8\n"
"Qmvaa 4,132,68\nQe 9\nQmvaa 4,136,72\nQe 10\nQmvaa 4,140,76\nQe 11\nbuffer_load_dwordx4 v69, s[40:43], s60 offen offset:3072 nt lds\nQzvva 160,24,148\n"
"Qk 28,8,9\nQk 29,10,11\nQzvva 164,24,156\nQe 12\ns_add_u32 m0, s61, 107264\nQzvva 168,24,164\nbuffer_load_dwordx4 v72, s[44:47], s90 offen nt lds\nQzvva 172,24,172\n"
"Qe 13\nQcvaa 8,112,80,56\nQe 14\nQmvaa 8,116,84\nQe 15\nQmvaa 8,120,88\nbuffer_load_dwordx4 v72, s[44:47], s60 offen offset:1024 nt lds\nQmvaa 8,124,92\n"
"Qk 30,12,13\nQk 31,14,15\nQcvaa 12,128,80,56\nQe 0\nQmvaa 12,132,84\nQe 1\nv_smfmac_f32_16x16x64_bf16 a[180:183], a[204:207], v[24:31], v77\nQk 32,0,1\n"
"Qmvaa 12,136,88\nbuffer_load_dwordx4 v72, s[44:47], s90 offen offset:2048 nt lds\nQmvaa 12,140,92\nQe 2\nQzvva 192,28,148\nQe 3\ns_waitcnt vmcnt(15)\ns_barrier\n"
"Qzvva 196,28,156\nbuffer_load_dwordx4 v72, s[44:47], s60 offen offset:3072 nt lds\nQzvva 200,28,164\nQk 33,2,3\ns_add_u32 s60, s60, 8192\ns_and_b32 s68, s60, 0x40000\ns_mul_i32 s68, s68, 7\ns_add_u32 s60, s60, s68\n"
"s_add_u32 s90, s60, 896\nQzvva 204,28,172\nQe 4\nQcvaa 0,112,96,56\nQda 112,68,33024\nQmvaa 0,116,100\nQda 116,68,33536\nQmvaa 0,120,104\n"
"Qda 120,68,34048\nQe 5\nQe 6\nQe 7\nQmvaa 0,124,108\nQda 124,68,34560\nQk 34,4,5\nQk 35,6,7\n"
"Qcvaa 4,128,96,56\nQda 128,68,35072\nQmvaa 4,132,100\nQda 132,68,35584\nQmvaa 4,136,104\nQda 136,68,36096\nQmvaa 4,140,108\nQda 140,68,36608\n"
"Qzvva 224,32,148\ns_memrealtime s[74:75]\nQzvva 228,32,156\nQe 8\nQzvva 232,32,164\nQe 9\nQe 10\nQe 11\n"
"Qzvva 236,32,172\ns_waitcnt lgkmcnt(0)\nQk 36,8,9\nQk 37,10,11\nQcvaa 8,112,0,56\nQe 12\nQmvaa 8,116,4\nv_add_u32 v68, 66048, v68\n"
"Qmvaa 8,120,8\nQe 13\nQe 14\nQe 15\nQmvaa 8,124,12\nQk 38,12,13\nQk 39,14,15\nQcvaa 12,128,0,56\n"
"Qe 0\nQmvaa 12,132,4\nQe 1\nv_smfmac_f32_16x16x64_bf16 a[184:187], a[204:207], v[32:39], v77\nQk 40,0,1\nQmvaa 12,136,8\nQe 2\nQmvaa 12,140,12\n"
"Qe 3\ns_waitcnt lgkmcnt(0)\nQzava 208,36,148\ns_mov_b32 s73, 140\nQk 41,2,3\ns_add_u32 s77, s74, s73\nQzava 212,36,156\ns_sub_u32 s73, s73, 4\n"
"s_max_u32 s73, s73, 120\nQzava 216,36,164\nQe 4\nQzava 220,36,172\nQe 8\nQcvaa 0,112,16,56\nQe 9\nQmvaa 0,116,20\n"
"Qe 10\nQmvaa 0,120,24\nQe 11\nQe 5\nQe 6\nQe 7\nQmvaa 0,124,28\nQk 44,8,9\n"
"Qk 45,10,11\nQk 42,4,5\nQk 43,6,7\nQcvaa 4,128,16,56\nQe 12\nQmvaa 4,132,20\nQe 13\nQmvaa 4,136,24\n"
"Qe 14\nQmvaa 4,140,28\nQe 15\nQta 144,70,0\nQta 146,70,2048\nQzava 240,40,148\nQta 148,70,33024\nQta 150,70,35072\n"
"Qta 152,70,256\nQta 154,70,2304\nQzava 244,40,156\nQta 156,70,33280\nQta 158,70,35328\nQta 160,70,512\nQta 162,70,2560\nQzava 248,40,164\n"
"Qta 164,70,33536\nQta 166,70,35584\nQta 168,70,768\nQta 170,70,2816\nQzava 252,40,172\nQta 172,70,33792\nQta 174,70,35840\nQcvaa 8,112,32,56\n"
"v_add_u32 v75, 1, v75\nQk 46,12,13\nQmvaa 8,116,36\ns_waitcnt vmcnt(8)\nds_write_b32 v73, v75\nQmvaa 8,120,40\nQk 47,14,15\nQmvaa 8,124,44\n"
"Qe 0\nQcvaa 12,128,32,56\nQe 1\nQmvaa 12,132,36\nQe 2\nv_smfmac_f32_16x16x64_bf16 a[188:191], a[204:207], v[40:47], v77\nQk 48,0,1\nQmvaa 12,136,40\n"
"s_waitcnt lgkmcnt(1)\nQmvaa 12,140,44\nQe 3\nQzvva 80,16,144\nQk 49,2,3\nQmvva 80,44,148\nQe 4\nQzvva 84,16,152\n"
"Qe 5\nQmvva 84,44,156\nQe 6\nQzvva 88,16,160\nQe 7\nQmvva 88,44,164\nQk 50,4,5\nQk 51,6,7\n"
"Qzvva 92,16,168\nQe 8\nQmvva 92,44,172\nQe 9\nQcvaa 0,112,48,56\nQe 10\nQmvaa 0,116,52\nds_read_b32 v66, v74\n"
"Qk 52,8,9\nQmvaa 0,120,56\nQe 11\nQmvaa 0,124,60\nQk 53,10,11\nQcvaa 4,128,48,56\nQe 12\nQmvaa 4,132,52\n"
"Qe 13\nQmvaa 4,136,56\ns_waitcnt lgkmcnt(0)\nv_cmp_gt_u32 vcc, v75, v66\nQmvaa 4,140,60\ns_cbranch_vccnz .Lsa1\n.Lra1:\nQk 54,12,13\n"
"Qzvva 112,20,144\ns_add_u32 m0, s61, 0\nQmvva 112,48,148\nbuffer_load_dwordx4 v69, s[40:43], s90 offen nt lds\nQzvva 116,20,152\nQe 14\nQmvva 116,48,156\nQe 15\n"
"Qzvva 120,20,160\nQk 55,14,15\nQmvva 120,48,164\nQe 0\nQzvva 124,20,168\nbuffer_load_dwordx4 v69, s[40:43], s60 offen offset:1024 nt lds\nQmvva 124,48,172\nQe 1\n"
"Qcvaa 8,112,64,56\nQe 2\nQmvaa 8,116,68\nQe 3\nQmvaa 8,120,72\nQk 20,0,1\nQk 21,2,3\nQmvaa 8,124,76\n"
"buffer_load_dwordx4 v69, s[40:43], s90 offen offset:2048 nt lds\nQcvaa 12,128,64,56\nQe 4\nQmvaa 12,132,68\nQe 5\nv_smfmac_f32_16x16x64_bf16 a[192:195], a[204:207], v[48:55], v77\nQk 22,4,5\nQmvaa 12,136,72\n"
"Qe 6\nQmvaa 12,140,76\nbuffer_load_dwordx4 v69, s[40:43], s60 offen offset:3072 nt lds\nQzvva 144,24,144\nQe 7\nQmvva 144,52,148\nQk 23,6,7\nQzvva 148,24,152\n"
"Qe 8\nQmvva 148,52,156\ns_add_u32 m0, s61, 8192\nQzvva 152,24,160\nbuffer_load_dwordx4 v72, s[44:47], s90 offen nt lds\nQmvva 152,52,164\nQe 9\nQzvva 156,24,168\n"
"Qe 10\nQmvva 156,52,172\nQe 11\nQcvaa 0,112,80,56\nQk 24,8,9\nQk 25,10,11\nQmvaa 0,116,84\nbuffer_load_dwordx4 v72, s[44:47], s60 offen offset:1024 nt lds\n"
"Qmvaa 0,120,88\nQe 12\nQmvaa 0,124,92\nQe 13\nQcvaa 4,128,80,56\nQe 14\nQmvaa 4,132,84\nQe 15\n"
"Qmvaa 4,136,88\nbuffer_load_dwordx4 v72, s[44:47], s90 offen offset:2048 nt lds\nQmvaa 4,140,92\nQk 26,12,13\nQk 27,14,15\nQzvva 176,28,144\nQmvva 176,20,148\nQzvva 180,28,152\n"
"Qmvva 180,20,156\nbuffer_load_dwordx4 v72, s[44:47], s60 offen offset:3072 nt lds\nQzvva 184,28,160\n.Lpg5:\ns_cbranch_execz .Ltr9\nQe 0\nQmvva 184,20,164\nQe 1\n"
"Qzvva 188,28,168\ns_add_u32 s60, s60, 8192\ns_add_u32 s90, s60, 896\nQmvva 188,20,172\nQe 2\nQcvaa 8,112,96,56\nQda 112,68,0\nQk 28,0,1\n"
"Qmvaa 8,116,100\nQda 116,68,512\nQmvaa 8,120,104\nQda 120,68,1024\nQmvaa 8,124,108\nQda 124,68,1536\nQcvaa 12,128,96,56\nQda 128,68,2048\n"
"Qmvaa 12,132,100\nQda 132,68,2560\nv_smfmac_f32_16x16x64_bf16 a[196:199], a[204:207], v[20:27], v77\nQmvaa 12,136,104\nQda 136,68,3072\nQmvaa 12,140,108\nQda 140,68,3584\nQzvva 208,32,144\n"
"s_memrealtime s[74:75]\nQmvva 208,24,148\nQe 3\nQzvva 212,32,152\nQk 29,2,3\nQmvva 212,24,156\nQe 4\nQzvva 216,32,160\n"
"Qe 5\nQmvva 216,24,164\nQe 6\nQzvva 220,32,168\nQe 7\nQmvva 220,24,172\ns_waitcnt lgkmcnt(0)\nQk 30,4,5\n"
"Qcvaa 0,112,0,56\nQk 31,6,7\nQmvaa 0,116,4\nQe 8\nQmvaa 0,120,8\nQe 9\nQmvaa 0,124,12\nQe 10\n"
"Qcvaa 4,128,0,56\nQe 11\nQmvaa 4,132,4\nQk 32,8,9\nQk 33,10,11\nQmvaa 4,136,8\nQe 12\nQmvaa 4,140,12\n"
"Qe 13\nQzvva 240,36,144\nQe 14\nQmvva 240,28,148\ns_waitcnt lgkmcnt(0)\ns_sub_u32 s75, s77, s74\nQzvva 244,36,152\ns_cmp_lt_u32 s75, 256\n"
"s_cbranch_scc1 .Lsp1\n.Lrp1:\nQmvva 244,28,156\ns_add_u32 s77, s74, s73\nQk 34,12,13\nQzvva 248,36,160\ns_sub_u32 s73, s73, 4\nQmvva 248,28,164\n"
"s_max_u32 s73, s73, 120\nQzvva 252,36,168\nQe 15\nQmvva 252,28,172\nQk 35,14,15\nQcvaa 8,112,16,56\nQe 0\nQmvaa 8,116,20\n"
"Qe 1\nQmvaa 8,120,24\nQe 2\nQmvaa 8,124,28\nQe 3\nQcvaa 12,128,16,56\nQk 36,0,1\nQk 37,2,3\n"
"Qmvaa 12,132,20\nQe 4\nv_smfmac_f32_16x16x64_bf16 a[200:203], a[204:207], v[28:35], v77\nQmvaa 12,136,24\nQe 5\nQmvaa 12,140,28\nQe 6\nQzava 224,40,144\n"
"Qta 144,70,34048\nQta 146,70,36096\nQmava 224,32,148\nQta 148,71,1024\nQta 150,71,3072\nQzava 228,40,152\nQta 152,70,34304\nQta 154,70,36352\n"
"Qmava 228,32,156\nQta 156,71,1280\nQta 158,71,3328\nQzava 232,40,160\nQta 160,70,34560\nQta 162,70,36608\nQmava 232,32,164\nQta 164,71,1536\n"
"Qta 166,71,3584\nQzava 236,40,168\nQta 168,70,34816\nQta 170,70,36864\nQmava 236,32,172\nQta 172,71,1792\nQta 174,71,3840\ns_lshl_b32 s66, s67, 3\n"
".Lloop:\ns_bitcmp1_b32 s99, s66\ns_cbranch_scc1 .Lhook\n.Lhookret:\nQcvaa 0,112,32,56\nv_add_u32 v75, 1, v75\nQk 38,4,5\nQmvaa 0,116,36\n"
"s_waitcnt vmcnt(8)\nds_write_b32 v73, v75\nQmvaa 0,120,40\nQe 7\nQmvaa 0,124,44\nQk 39,6,7\nQcvaa 4,128,32,56\nQe 8\n"
"Qmvaa 4,132,36\nQe 9\nQmvaa 4,136,40\nQe 10\nQmvaa 4,140,44\ns_waitcnt lgkmcnt(1)\nQk 40,8,9\nQmvva 96,44,144\n"
"Qe 11\nQmvva 96,36,148\nQk 41,10,11\nQmvva 100,44,152\nQe 12\nQmvva 100,36,156\nQe 13\nQmvva 104,44,160\n"
"Qe 14\nQmvva 104,36,164\nQe 15\nQmvva 108,44,168\nQk 42,12,13\nQk 43,14,15\nQmvva 108,36,172\nQe 0\n"
"Qcvaa 8,112,48,56\nQe 1\nQmvaa 8,116,52\nQe 2\nQmvaa 8,120,56\nds_read_b32 v66, v74\nQk 44,0,1\nQmvaa 8,124,60\n"
"Qe 3\nQcvaa 12,128,48,56\nQk 45,2,3\nQmvaa 12,132,52\nQe 4\nv_smfmac_f32_16x16x64_bf16 a[176:179], a[204:207], v[36:43], v77\nQmvaa 12,136,56\ns_waitcnt lgkmcnt(0)\n"
"v_cmp_gt_u32 vcc, v75, v66\nQmvaa 12,140,60\ns_cbranch_vccnz .Lsa2\n.Lra2:\nQmvva 128,48,144\ns_add_u32 m0, s61, 33024\nQmvva 128,40,148\nbuffer_load_dwordx4 v69, s[40:43], s90 offen nt lds\n"
"Qmvva 132,48,152\nQe 5\nQmvva 132,40,156\nQe 6\nQmvva 136,48,160\nQe 7\nQmvva 136,40,164\nQk 46,4,5\n"
"Qk 47,6,7\nQmvva 140,48,168\nbuffer_load_dwordx4 v69, s[40:43], s60 offen offset:1024 nt lds\nQmvva 140,40,172\nQe 8\nQcvaa 0,112,64,56\nQe 9\nQmvaa 0,116,68\n"
"Qe 10\nQmvaa 0,120,72\nQe 11\nQmvaa 0,124,76\nbuffer_load_dwordx4 v69, s[40:43], s90 offen offset:2048 nt lds\nQcvaa 4,128,64,56\nQk 48,8,9\nQk 49,10,11\n"
"Qmvaa 4,132,68\nQe 12\nQmvaa 4,136,72\nQe 13\nQmvaa 4,140,76\nQe 14\nQmvva 160,52,144\nbuffer_load_dwordx4 v69, s[40:43], s60 offen offset:3072 nt lds\n"
"Qmvva 160,44,148\nQe 15\nQmvva 164,52,152\nQk 50,12,13\nQk 51,14,15\nQmvva 164,44,156\nQe 0\nQmvva 168,52,160\n"
"s_add_u32 m0, s61, 41216\nQmvva 168,44,164\nbuffer_load_dwordx4 v72, s[44:47], s90 offen nt lds\nQmvva 172,52,168\nQe 1\nQmvva 172,44,172\nQe 2\nQcvaa 8,112,80,56\n"
"Qe 3\nQmvaa 8,116,84\nQk 16,0,1\nQk 17,2,3\nQmvaa 8,120,88\nbuffer_load_dwordx4 v72, s[44:47], s60 offen offset:1024 nt lds\nQmvaa 8,124,92\nQe 4\n"
"Qcvaa 12,128,80,56\nQe 5\nQmvaa 12,132,84\nQe 6\nv_smfmac_f32_16x16x64_bf16 a[180:183], a[204:207], v[44:51], v77\nQk 18,4,5\nQmvaa 12,136,88\nbuffer_load_dwordx4 v72, s[44:47], s90 offen offset:2048 nt lds\n"
"Qmvaa 12,140,92\nQe 7\nQmvva 192,20,144\nQk 19,6,7\nQmvva 192,48,148\nQmvva 196,20,152\nQe 8\nQmvva 196,48,156\n"
"buffer_load_dwordx4 v72, s[44:47], s60 offen offset:3072 nt lds\nQmvva 200,20,160\nQe 9\nQmvva 200,48,164\nQe 10\nQmvva 204,20,168\ns_add_u32 s60, s60, 8192\ns_add_u32 s90, s60, 896\n"
"Qmvva 204,48,172\nQe 11\nQcvaa 0,112,96,56\nQda 112,68,33024\nQk 20,8,9\nQmvaa 0,116,100\nQda 116,68,33536\nQk 21,10,11\n"
"Qmvaa 0,120,104\nQda 120,68,34048\nQmvaa 0,124,108\nQda 124,68,34560\nQcvaa 4,128,96,56\nQda 128,68,35072\nQmvaa 4,132,100\nQda 132,68,35584\n"
"Qmvaa 4,136,104\nQda 136,68,36096\nQmvaa 4,140,108\nQda 140,68,36608\nQmvva 224,24,144\nQe 12\nQmvva 224,16,148\ns_memrealtime s[74:75]\n"
"Qmvva 228,24,152\nQe 13\nQmvva 228,16,156\nQe 14\nQmvva 232,24,160\nQe 15\nQmvva 232,16,164\nQk 22,12,13\n"
"Qk 23,14,15\nQmvva 236,24,168\nQe 0\nQmvva 236,16,172\ns_waitcnt lgkmcnt(0)\nQcvaa 8,112,0,56\nQe 1\nQmvaa 8,116,4\n"
"v_subrev_u32 v68, 66048, v68\nQk 24,0,1\nQmvaa 8,120,8\nQe 2\nQmvaa 8,124,12\nQe 3\nQcvaa 12,128,0,56\nQk 25,2,3\n"
"Qmvaa 12,132,4\nQe 4\nv_smfmac_f32_16x16x64_bf16 a[184:187], a[204:207], v[16:23], v77\nQmvaa 12,136,8\nQe 5\nQmvaa 12,140,12\nQe 6\nQmava 208,28,144\n"
"s_waitcnt lgkmcnt(0)\ns_sub_u32 s75, s77, s74\nQmava 208,20,148\ns_cmp_lt_u32 s75, 256\ns_cbranch_scc1 .Lsp2\n.Lrp2:\nQmava 212,28,152\ns_add_u32 s77, s74, s73\n"
"Qk 26,4,5\nQmava 212,20,156\ns_sub_u32 s73, s73, 4\nQmava 216,28,160\ns_max_u32 s73, s73, 120\nQmava 216,20,164\nQe 7\nQmava 220,28,168\n"
"Qk 27,6,7\nQmava 220,20,172\nQe 8\nQcvaa 0,112,16,56\nQe 9\nQmvaa 0,116,20\nQe 10\nQmvaa 0,120,24\n"
"Qe 11\nQmvaa 0,124,28\nQk 28,8,9\nQk 29,10,11\nQcvaa 4,128,16,56\nQe 12\nQmvaa 4,132,20\nQe 13\n"
"Qmvaa 4,136,24\nQe 14\nQmvaa 4,140,28\nQe 15\nQmava 240,32,144\nQta 144,71,0\nQta 146,71,2048\nQmava 240,24,148\n"
"Qta 148,71,33024\nQta 150,71,35072\nQmava 244,32,152\nQta 152,71,256\nQta 154,71,2304\nQmava 244,24,156\nQta 156,71,33280\nQta 158,71,35328\n"
"Qmava 248,32,160\nQta 160,71,512\nQta 162,71,2560\nQmava 248,24,164\nQta 164,71,33536\nQta 166,71,35584\nQmava 252,32,168\nQta 168,71,768\n"
"Qta 170,71,2816\nQmava 252,24,172\nQta 172,71,33792\nQta 174,71,35840\nQcvaa 8,112,32,56\nv_add_u32 v75, 1, v75\nQk 30,12,13\nQmvaa 8,116,36\n"
"s_waitcnt vmcnt(8)\nds_write_b32 v73, v75\nQmvaa 8,120,40\nQk 31,14,15\nQmvaa 8,124,44\nQe 0\nQcvaa 12,128,32,56\nQe 1\n"
"Qmvaa 12,132,36\nQe 2\nv_smfmac_f32_16x16x64_bf16 a[188:191], a[204:207], v[24:31], v77\nQk 32,0,1\nQmvaa 12,136,40\ns_waitcnt lgkmcnt(1)\nQmvaa 12,140,44\nQe 3\n"
"Qmvva 80,36,144\nQk 33,2,3\nQmvva 80,28,148\nQe 4\nQmvva 84,36,152\nQe 5\nQmvva 84,28,156\nQe 6\n"
"Qmvva 88,36,160\nQe 7\nQmvva 88,28,164\nQk 34,4,5\nQk 35,6,7\nQmvva 92,36,168\nQe 8\nQmvva 92,28,172\n"
"Qe 9\nQcvaa 0,112,48,56\nQe 10\nQmvaa 0,116,52\nds_read_b32 v66, v74\nQk 36,8,9\nQmvaa 0,120,56\nQe 11\n"
"Qmvaa 0,124,60\nQk 37,10,11\nQcvaa 4,128,48,56\nQe 12\nQmvaa 4,132,52\nQe 13\nQmvaa 4,136,56\ns_waitcnt lgkmcnt(0)\n"
"v_cmp_gt_u32 vcc, v75, v66\nQmvaa 4,140,60\ns_cbranch_vccnz .Lsa3\n.Lra3:\nQk 38,12,13\nQmvva 112,40,144\ns_add_u32 m0, s61, 66048\nQmvva 112,32,148\n"
"buffer_load_dwordx4 v69, s[40:43], s90 offen nt lds\nQmvva 116,40,152\nQe 14\nQmvva 116,32,156\nQe 15\nQmvva 120,40,160\nQk 39,14,15\nQmvva 120,32,164\n"
"Qe 0\nQmvva 124,40,168\nbuffer_load_dwordx4 v69, s[40:43], s60 offen offset:1024 nt lds\nQmvva 124,32,172\nQe 1\nQcvaa 8,112,64,56\nQe 2\nQmvaa 8,116,68\n"
"Qe 3\nQmvaa 8,120,72\nQk 40,0,1\nQk 41,2,3\nQmvaa 8,124,76\nbuffer_load_dwordx4 v69, s[40:43], s90 offen offset:2048 nt lds\nQcvaa 12,128,64,56\nQe 4\n"
"Qmvaa 12,132,68\nQe 5\nv_smfmac_f32_16x16x64_bf16 a[192:195], a[204:207], v[32:39], v77\nQk 42,4,5\nQmvaa 12,136,72\nQe 6\nQmvaa 12,140,76\nbuffer_load_dwordx4 v69, s[40:43], s60 offen offset:3072 nt lds\n"
"Qmvva 144,44,144\nQe 7\nQmvva 144,36,148\nQk 43,6,7\nQmvva 148,44,152\nQe 8\nQmvva 148,36,156\ns_add_u32 m0, s61, 74240\n"
"Qmvva 152,44,160\nbuffer_load_dwordx4 v72, s[44:47], s90 offen nt lds\nQmvva 152,36,164\nQe 9\nQmvva 156,44,168\nQe 10\nQmvva 156,36,172\nQe 11\n"
"Qcvaa 0,112,80,56\nQk 44,8,9\nQk 45,10,11\nQmvaa 0,116,84\nbuffer_load_dwordx4 v72, s[44:47], s60 offen offset:1024 nt lds\nQmvaa 0,120,88\nQe 12\nQmvaa 0,124,92\n"
"Qe 13\nQcvaa 4,128,80,56\nQe 14\nQmvaa 4,132,84\nQe 15\nQmvaa 4,136,88\nbuffer_load_dwordx4 v72, s[44:47], s90 offen offset:2048 nt lds\nQmvaa 4,140,92\n"
"Qk 46,12,13\nQk 47,14,15\nQmvva 176,48,144\nQmvva 176,40,148\nQmvva 180,48,152\nQmvva 180,40,156\nbuffer_load_dwordx4 v72, s[44:47], s60 offen offset:3072 nt lds\nQmvva 184,48,160\n"
"Qe 0\nQmvva 184,40,164\nQe 1\nQmvva 188,48,168\ns_add_u32 s60, s60, 8192\ns_add_u32 s90, s60, 896\nQmvva 188,40,172\nQe 2\n"
"Qcvaa 8,112,96,56\nQda 112,68,0\nQk 48,0,1\nQmvaa 8,116,100\nQda 116,68,512\nQmvaa 8,120,104\nQda 120,68,1024\nQmvaa 8,124,108\n"
"Qda 124,68,1536\nQcvaa 12,128,96,56\nQda 128,68,2048\nQmvaa 12,132,100\nQda 132,68,2560\nv_smfmac_f32_16x16x64_bf16 a[196:199], a[204:207], v[40:47], v77\nQmvaa 12,136,104\nQda 136,68,3072\n"
"Qmvaa 12,140,108\nQda 140,68,3584\nQmvva 208,16,144\ns_memrealtime s[74:75]\nQmvva 208,44,148\nQe 3\nQmvva 212,16,152\nQk 49,2,3\n"
"Qmvva 212,44,156\nQe 4\nQmvva 216,16,160\nQe 5\nQmvva 216,44,164\nQe 6\nQmvva 220,16,168\nQe 7\n"
"Qmvva 220,44,172\ns_waitcnt lgkmcnt(0)\nQk 50,4,5\nQcvaa 0,112,0,56\nQk 51,6,7\nQmvaa 0,116,4\nQe 8\nQmvaa 0,120,8\n"
"Qe 9\nQmvaa 0,124,12\nQe 10\nQcvaa 4,128,0,56\nQe 11\nQmvaa 4,132,4\nQk 52,8,9\nQk 53,10,11\n"
"Qmvaa 4,136,8\nQe 12\nQmvaa 4,140,12\nQe 13\nQmvva 240,20,144\nQe 14\nQmvva 240,48,148\ns_waitcnt lgkmcnt(0)\n"
"s_sub_u32 s75, s77, s74\nQmvva 244,20,152\ns_cmp_lt_u32 s75, 256\ns_cbranch_scc1 .Lsp3\n.Lrp3:\nQmvva 244,48,156\ns_add_u32 s77, s74, s73\nQk 54,12,13\n"
"Qmvva 248,20,160\ns_sub_u32 s73, s73, 4\nQmvva 248,48,164\ns_max_u32 s73, s73, 120\nQmvva 252,20,168\nQe 15\nQmvva 252,48,172\nQk 55,14,15\n"
"Qcvaa 8,112,16,56\nQe 0\nQmvaa 8,116,20\nQe 1\nQmvaa 8,120,24\n.Lpg6:\ns_cbranch_execz .Ltr9\nQe 2\n"
"Qmvaa 8,124,28\nQe 3\nQcvaa 12,128,16,56\nQk 16,0,1\nQk 17,2,3\nQmvaa 12,132,20\nQe 4\nv_smfmac_f32_16x16x64_bf16 a[200:203], a[204:207], v[48:55], v77\n"
"Qmvaa 12,136,24\nQe 5\nQmvaa 12,140,28\nQe 6\nQmava 224,24,144\nQta 144,71,34048\nQta 146,71,36096\nQmava 224,52,148\n"
"Qta 148,70,1024\nQta 150,70,3072\nQmava 228,24,152\nQta 152,71,34304\nQta 154,71,36352\nQmava 228,52,156\nQta 156,70,1280\nQta 158,70,3328\n"
"Qmava 232,24,160\nQta 160,71,34560\nQta 162,71,36608\nQmava 232,52,164\nQta 164,70,1536\nQta 166,70,3584\nQmava 236,24,168\nQta 168,71,34816\n"
"Qta 170,71,36864\nQmava 236,52,172\nQta 172,70,1792\nQta 174,70,3840\nQcvaa 0,112,32,56\nv_add_u32 v75, 1, v75\nQk 18,4,5\nQmvaa 0,116,36\n"
"s_waitcnt vmcnt(8)\nds_write_b32 v73, v75\nQmvaa 0,120,40\nQe 7\nQmvaa 0,124,44\nQk 19,6,7\nQcvaa 4,128,32,56\nQe 8\n"
"Qmvaa 4,132,36\nQe 9\nQmvaa 4,136,40\nQe 10\nQmvaa 4,140,44\ns_waitcnt lgkmcnt(1)\nQk 20,8,9\nQmvva 96,28,144\n"
"Qe 11\nQmvva 96,16,148\nQk 21,10,11\nQmvva 100,28,152\nQe 12\nQmvva 100,16,156\nQe 13\nQmvva 104,28,160\n"
"Qe 14\nQmvva 104,16,164\nQe 15\nQmvva 108,28,168\nQk 22,12,13\nQk 23,14,15\nQmvva 108,16,172\nQe 0\n"
"Qcvaa 8,112,48,56\nQe 1\nQmvaa 8,116,52\nQe 2\nQmvaa 8,120,56\nds_read_b32 v66, v74\nQk 24,0,1\nQmvaa 8,124,60\n"
"Qe 3\nQcvaa 12,128,48,56\nQk 25,2,3\nQmvaa 12,132,52\nQe 4\nv_smfmac_f32_16x16x64_bf16 a[176:179], a[204:207], v[16:23], v77\nQmvaa 12,136,56\ns_waitcnt lgkmcnt(0)\n"
"v_cmp_gt_u32 vcc, v75, v66\nQmvaa 12,140,60\ns_cbranch_vccnz .Lsa4\n.Lra4:\nQmvva 128,32,144\ns_add_u32 m0, s61, 99072\nQmvva 128,20,148\nbuffer_load_dwordx4 v69, s[40:43], s90 offen nt lds\n"
"Qmvva 132,32,152\nQe 5\nQmvva 132,20,156\nQe 6\nQmvva 136,32,160\nQe 7\nQmvva 136,20,164\nQk 26,4,5\n"
"Qk 27,6,7\nQmvva 140,32,168\nbuffer_load_dwordx4 v69, s[40:43], s60 offen offset:1024 nt lds\nQmvva 140,20,172\nQe 8\nQcvaa 0,112,64,56\nQe 9\nQmvaa 0,116,68\n"
"Qe 10\nQmvaa 0,120,72\nQe 11\nQmvaa 0,124,76\nbuffer_load_dwordx4 v69, s[40:43], s90 offen offset:2048 nt lds\nQcvaa 4,128,64,56\nQk 28,8,9\nQk 29,10,11\n"
"Qmvaa 4,132,68\nQe 12\nQmvaa 4,136,72\nQe 13\nQmvaa 4,140,76\nQe 14\nQmvva 160,36,144\nbuffer_load_dwordx4 v69, s[40:43], s60 offen offset:3072 nt lds\n"
"Qmvva 160,24,148\nQe 15\nQmvva 164,36,152\nQk 30,12,13\nQk 31,14,15\nQmvva 164,24,156\nQe 0\nQmvva 168,36,160\n"
"s_add_u32 m0, s61, 107264\nQmvva 168,24,164\nbuffer_load_dwordx4 v72, s[44:47], s90 offen nt lds\nQmvva 172,36,168\nQe 1\nQmvva 172,24,172\nQe 2\nQcvaa 8,112,80,56\n"
"Qe 3\nQmvaa 8,116,84\nQk 32,0,1\nQk 33,2,3\nQmvaa 8,120,88\nbuffer_load_dwordx4 v72, s[44:47], s60 offen offset:1024 nt lds\nQmvaa 8,124,92\nQe 4\n"
"Qcvaa 12,128,80,56\nQe 5\nQmvaa 12,132,84\nQe 6\nv_smfmac_f32_16x16x64_bf16 a[180:183], a[204:207], v[24:31], v77\nQk 34,4,5\nQmvaa 12,136,88\nbuffer_load_dwordx4 v72, s[44:47], s90 offen offset:2048 nt lds\n"
"Qmvaa 12,140,92\nQe 7\nQmvva 192,40,144\nQk 35,6,7\nQmvva 192,28,148\nQmvva 196,40,152\nQe 8\nQmvva 196,28,156\n"
"buffer_load_dwordx4 v72, s[44:47], s60 offen offset:3072 nt lds\nQmvva 200,40,160\nQe 9\nQmvva 200,28,164\nQe 10\nQmvva 204,40,168\ns_add_u32 s60, s60, 8192\ns_and_b32 s68, s60, 0x40000\n"
"s_mul_i32 s68, s68, 7\ns_add_u32 s60, s60, s68\ns_add_u32 s90, s60, 896\nQmvva 204,28,172\nQe 11\nQcvaa 0,112,96,56\nQda 112,68,33024\nQk 36,8,9\n"
"Qmvaa 0,116,100\nQda 116,68,33536\nQk 37,10,11\nQmvaa 0,120,104\nQda 120,68,34048\nQmvaa 0,124,108\nQda 124,68,34560\nQcvaa 4,128,96,56\n"
"Qda 128,68,35072\nQmvaa 4,132,100\nQda 132,68,35584\nQmvaa 4,136,104\nQda 136,68,36096\nQmvaa 4,140,108\nQda 140,68,36608\nQmvva 224,44,144\n"
"Qe 12\nQmvva 224,32,148\ns_memrealtime s[74:75]\nQmvva 228,44,152\nQe 13\nQmvva 228,32,156\nQe 14\nQmvva 232,44,160\n"
"Qe 15\nQmvva 232,32,164\nQk 38,12,13\nQk 39,14,15\nQmvva 236,44,168\nQe 0\nQmvva 236,32,172\ns_waitcnt lgkmcnt(0)\n"
"Qcvaa 8,112,0,56\nQe 1\nQmvaa 8,116,4\nv_add_u32 v68, 66048, v68\nQk 40,0,1\nQmvaa 8,120,8\nQe 2\nQmvaa 8,124,12\n"
"Qe 3\nQcvaa 12,128,0,56\nQk 41,2,3\nQmvaa 12,132,4\nQe 4\nv_smfmac_f32_16x16x64_bf16 a[184:187], a[204:207], v[32:39], v77\nQmvaa 12,136,8\nQe 5\n"
"Qmvaa 12,140,12\nQe 6\nQmava 208,48,144\ns_waitcnt lgkmcnt(0)\ns_sub_u32 s75, s77, s74\nQmava 208,36,148\ns_cmp_lt_u32 s75, 256\ns_cbranch_scc1 .Lsp4\n"
".Lrp4:\nQmava 212,48,152\ns_add_u32 s77, s74, s73\nQk 42,4,5\nQmava 212,36,156\ns_sub_u32 s73, s73, 4\nQmava 216,48,160\ns_max_u32 s73, s73, 120\n"
"Qmava 216,36,164\nQe 7\nQmava 220,48,168\nQk 43,6,7\nQmava 220,36,172\nQe 8\nQcvaa 0,112,16,56\nQe 9\n"
"Qmvaa 0,116,20\nQe 10\nQmvaa 0,120,24\nQe 11\nQmvaa 0,124,28\nQk 44,8,9\nQk 45,10,11\nQcvaa 4,128,16,56\n"
"Qe 12\nQmvaa 4,132,20\nQe 13\nQmvaa 4,136,24\nQe 14\nQmvaa 4,140,28\nQe 15\nQmava 240,52,144\n"
"Qta 144,70,0\nQta 146,70,2048\nQmava 240,40,148\nQta 148,70,33024\nQta 150,70,35072\nQmava 244,52,152\nQta 152,70,256\nQta 154,70,2304\n"
"Qmava 244,40,156\nQta 156,70,33280\nQta 158,70,35328\nQmava 248,52,160\nQta 160,70,512\nQta 162,70,2560\nQmava 248,40,164\nQta 164,70,33536\n"
"Qta 166,70,35584\nQmava 252,52,168\nQta 168,70,768\nQta 170,70,2816\nQmava 252,40,172\nQta 172,70,33792\nQta 174,70,35840\nQcvaa 8,112,32,56\n"
"v_add_u32 v75, 1, v75\nQk 46,12,13\nQmvaa 8,116,36\ns_waitcnt vmcnt(8)\nds_write_b32 v73, v75\nQmvaa 8,120,40\nQk 47,14,15\nQmvaa 8,124,44\n"
"Qe 0\nQcvaa 12,128,32,56\nQe 1\nQmvaa 12,132,36\nQe 2\nv_smfmac_f32_16x16x64_bf16 a[188:191], a[204:207], v[40:47], v77\nQk 48,0,1\nQmvaa 12,136,40\n"
"s_waitcnt lgkmcnt(1)\nQmvaa 12,140,44\nQe 3\nQmvva 80,16,144\nQk 49,2,3\nQmvva 80,44,148\nQe 4\nQmvva 84,16,152\n"
"Qe 5\nQmvva 84,44,156\nQe 6\nQmvva 88,16,160\nQe 7\nQmvva 88,44,164\nQk 50,4,5\nQk 51,6,7\n"
"Qmvva 92,16,168\nQe 8\nQmvva 92,44,172\nQe 9\nQcvaa 0,112,48,56\nQe 10\nQmvaa 0,116,52\nds_read_b32 v66, v74\n"
"Qk 52,8,9\nQmvaa 0,120,56\nQe 11\nQmvaa 0,124,60\nQk 53,10,11\nQcvaa 4,128,48,56\nQe 12\nQmvaa 4,132,52\n"
"Qe 13\nQmvaa 4,136,56\ns_waitcnt lgkmcnt(0)\nv_cmp_gt_u32 vcc, v75, v66\nQmvaa 4,140,60\ns_cbranch_vccnz .Lsa5\n.Lra5:\nQk 54,12,13\n"
"Qmvva 112,20,144\ns_add_u32 m0, s61, 0\nQmvva 112,48,148\nbuffer_load_dwordx4 v69, s[40:43], s90 offen nt lds\nQmvva 116,20,152\nQe 14\nQmvva 116,48,156\nQe 15\n"
"Qmvva 120,20,160\nQk 55,14,15\nQmvva 120,48,164\nQe 0\nQmvva 124,20,168\nbuffer_load_dwordx4 v69, s[40:43], s60 offen offset:1024 nt lds\nQmvva 124,48,172\nQe 1\n"
"Qcvaa 8,112,64,56\nQe 2\nQmvaa 8,116,68\nQe 3\nQmvaa 8,120,72\nQk 20,0,1\nQk 21,2,3\nQmvaa 8,124,76\n"
"buffer_load_dwordx4 v69, s[40:43], s90 offen offset:2048 nt lds\nQcvaa 12,128,64,56\nQe 4\nQmvaa 12,132,68\nQe 5\nv_smfmac_f32_16x16x64_bf16 a[192:195], a[204:207], v[48:55], v77\nQk 22,4,5\nQmvaa 12,136,72\n"
"Qe 6\nQmvaa 12,140,76\nbuffer_load_dwordx4 v69, s[40:43], s60 offen offset:3072 nt lds\nQmvva 144,24,144\nQe 7\nQmvva 144,52,148\nQk 23,6,7\nQmvva 148,24,152\n"
"Qe 8\nQmvva 148,52,156\ns_add_u32 m0, s61, 8192\nQmvva 152,24,160\nbuffer_load_dwordx4 v72, s[44:47], s90 offen nt lds\nQmvva 152,52,164\nQe 9\nQmvva 156,24,168\n"
"Qe 10\nQmvva 156,52,172\nQe 11\nQcvaa 0,112,80,56\nQk 24,8,9\nQk 25,10,11\nQmvaa 0,116,84\nbuffer_load_dwordx4 v72, s[44:47], s60 offen offset:1024 nt lds\n"
"Qmvaa 0,120,88\nQe 12\nQmvaa 0,124,92\nQe 13\nQcvaa 4,128,80,56\nQe 14\nQmvaa 4,132,84\nQe 15\n"
"Qmvaa 4,136,88\nbuffer_load_dwordx4 v72, s[44:47], s90 offen offset:2048 nt lds\nQmvaa 4,140,92\nQk 26,12,13\nQk 27,14,15\nQmvva 176,28,144\nQmvva 176,20,148\nQmvva 180,28,152\n"
"Qmvva 180,20,156\nbuffer_load_dwordx4 v72, s[44:47], s60 offen offset:3072 nt lds\nQmvva 184,28,160\nQe 0\nQmvva 184,20,164\nQe 1\nQmvva 188,28,168\ns_add_u32 s60, s60, 8192\n"
"s_add_u32 s90, s60, 896\nQmvva 188,20,172\nQe 2\nQcvaa 8,112,96,56\nQda 112,68,0\nQk 28,0,1\nQmvaa 8,116,100\nQda 116,68,512\n"
"Qmvaa 8,120,104\nQda 120,68,1024\nQmvaa 8,124,108\nQda 124,68,1536\nQcvaa 12,128,96,56\nQda 128,68,2048\nQmvaa 12,132,100\nQda 132,68,2560\n"
"v_smfmac_f32_16x16x64_bf16 a[196:199], a[204:207], v[20:27], v77\nQmvaa 12,136,104\nQda 136,68,3072\nQmvaa 12,140,108\nQda 140,68,3584\nQmvva 208,32,144\ns_memrealtime s[74:75]\nQmvva 208,24,148\n"
"Qe 3\nQmvva 212,32,152\nQk 29,2,3\nQmvva 212,24,156\nQe 4\nQmvva 216,32,160\nQe 5\nQmvva 216,24,164\n"
"Qe 6\nQmvva 220,32,168\nQe 7\nQmvva 220,24,172\ns_waitcnt lgkmcnt(0)\nQk 30,4,5\nQcvaa 0,112,0,56\nQk 31,6,7\n"
"Qmvaa 0,116,4\nQe 8\nQmvaa 0,120,8\nQe 9\nQmvaa 0,124,12\nQe 10\nQcvaa 4,128,0,56\nQe 11\n"
"Qmvaa 4,132,4\nQk 32,8,9\nQk 33,10,11\nQmvaa 4,136,8\nQe 12\nQmvaa 4,140,12\nQe 13\nQmvva 240,36,144\n"
"Qe 14\nQmvva 240,28,148\ns_waitcnt lgkmcnt(0)\ns_sub_u32 s75, s77, s74\nQmvva 244,36,152\ns_cmp_lt_u32 s75, 256\ns_cbranch_scc1 .Lsp5\n.Lrp5:\n"
"Qmvva 244,28,156\ns_add_u32 s77, s74, s73\nQk 34,12,13\nQmvva 248,36,160\ns_sub_u32 s73, s73, 4\nQmvva 248,28,164\ns_max_u32 s73, s73, 120\nQmvva 252,36,168\n"
"Qe 15\nQmvva 252,28,172\nQk 35,14,15\nQcvaa 8,112,16,56\nQe 0\nQmvaa 8,116,20\nQe 1\nQmvaa 8,120,24\n"
"Qe 2\nQmvaa 8,124,28\nQe 3\nQcvaa 12,128,16,56\nQk 36,0,1\nQk 37,2,3\nQmvaa 12,132,20\nQe 4\n"
"v_smfmac_f32_16x16x64_bf16 a[200:203], a[204:207], v[28:35], v77\nQmvaa 12,136,24\nQe 5\nQmvaa 12,140,28\nQe 6\nQmava 224,40,144\nQta 144,70,34048\nQta 146,70,36096\n"
"Qmava 224,32,148\nQta 148,71,1024\nQta 150,71,3072\nQmava 228,40,152\nQta 152,70,34304\nQta 154,70,36352\nQmava 228,32,156\nQta 156,71,1280\n"
"Qta 158,71,3328\nQmava 232,40,160\nQta 160,70,34560\nQta 162,70,36608\nQmava 232,32,164\nQta 164,71,1536\nQta 166,71,3584\nQmava 236,40,168\n"
"Qta 168,70,34816\nQta 170,70,36864\nQmava 236,32,172\nQta 172,71,1792\nQta 174,71,3840\ns_add_u32 s66, s66, 1\ns_bitcmp1_b32 0x7f7f, s66\ns_cbranch_scc1 .Lloop\n"
"s_cmp_lg_u32 s67, 0\ns_cbranch_scc1 .Ltail1\nQcvaa 0,112,32,56\nv_add_u32 v75, 1, v75\nQk 38,4,5\nQmvaa 0,116,36\ns_waitcnt vmcnt(8)\nds_write_b32 v73, v75\n"
"Qmvaa 0,120,40\nQe 7\nQmvaa 0,124,44\nQk 39,6,7\nQcvaa 4,128,32,56\nQe 8\nQmvaa 4,132,36\nQe 9\n"
"Qmvaa 4,136,40\nQe 10\nQmvaa 4,140,44\ns_waitcnt lgkmcnt(1)\nQk 40,8,9\nQmvva 96,44,144\nQe 11\nQmvva 96,36,148\n"
"Qk 41,10,11\nQmvva 100,44,152\nQe 12\nQmvva 100,36,156\nQe 13\nQmvva 104,44,160\nQe 14\nQmvva 104,36,164\n"
"Qe 15\nQmvva 108,44,168\nQk 42,12,13\nQk 43,14,15\nQmvva 108,36,172\nQe 0\nQcvaa 8,112,48,56\nQe 1\n"
"Qmvaa 8,116,52\nQe 2\nQmvaa 8,120,56\nds_read_b32 v66, v74\n.Lpg7:\ns_cbranch_execz .Ltr9\nQk 44,0,1\nQmvaa 8,124,60\n"
"Qe 3\nQcvaa 12,128,48,56\nQk 45,2,3\nQmvaa 12,132,52\nQe 4\nv_smfmac_f32_16x16x64_bf16 a[176:179], a[204:207], v[36:43], v77\nQmvaa 12,136,56\ns_waitcnt lgkmcnt(0)\n"
"v_cmp_gt_u32 vcc, v75, v66\nQmvaa 12,140,60\ns_cbranch_vccnz .Lsa30\n.Lra30:\nQmvva 128,48,144\ns_add_u32 m0, s62, 0\ns_add_u32 s69, s87, 0\nQmvva 128,40,148\n"
"buffer_load_dwordx4 v62, s[52:55], s69 offen lds\nQmvva 132,48,152\nQe 5\nQmvva 132,40,156\nQe 6\nQmvva 136,48,160\nQe 7\nQmvva 136,40,164\n"
"s_add_u32 s69, s87, 64504\nQk 46,4,5\nQk 47,6,7\nQmvva 140,48,168\nbuffer_load_dwordx4 v62, s[52:55], s69 offen offset:1032 lds\nQmvva 140,40,172\nQe 8\nQcvaa 0,112,64,56\n"
"Qe 9\nQmvaa 0,116,68\nQe 10\nQmvaa 0,120,72\ns_add_u32 s69, s87, 129008\nQe 11\nQmvaa 0,124,76\nbuffer_load_dwordx4 v62, s[52:55], s69 offen offset:2064 lds\n"
"Qcvaa 4,128,64,56\nQk 48,8,9\nQk 49,10,11\nQmvaa 4,132,68\nQe 12\nQmvaa 4,136,72\nQe 13\nQmvaa 4,140,76\n"
"s_add_u32 s69, s87, 193512\nQe 14\nQmvva 160,52,144\nbuffer_load_dwordx4 v62, s[52:55], s69 offen offset:3096 lds\nQmvva 160,44,148\nQe 15\nQmvva 164,52,152\nQk 50,12,13\n"
"Qk 51,14,15\nQmvva 164,44,156\nQe 0\nQmvva 168,52,160\ns_add_u32 m0, s62, 16512\ns_add_u32 s69, s87, 128\nQmvva 168,44,164\nbuffer_load_dwordx4 v62, s[52:55], s69 offen lds\n"
"Qmvva 172,52,168\nQe 1\nQmvva 172,44,172\nQe 2\nQcvaa 8,112,80,56\nQe 3\nQmvaa 8,116,84\ns_add_u32 s69, s87, 64632\n"
"Qk 16,0,1\nQk 17,2,3\nQmvaa 8,120,88\nbuffer_load_dwordx4 v62, s[52:55], s69 offen offset:1032 lds\nQmvaa 8,124,92\nQe 4\nQcvaa 12,128,80,56\nQe 5\n"
"Qmvaa 12,132,84\nQe 6\nv_smfmac_f32_16x16x64_bf16 a[180:183], a[204:207], v[44:51], v77\ns_add_u32 s69, s87, 129136\nQk 18,4,5\nQmvaa 12,136,88\nbuffer_load_dwordx4 v62, s[52:55], s69 offen offset:2064 lds\nQmvaa 12,140,92\n"
"Qe 7\nQmvva 192,20,144\nQk 19,6,7\nQmvva 192,48,148\nQmvva 196,20,152\ns_add_u32 s69, s87, 193640\nQe 8\nQmvva 196,48,156\n"
"buffer_load_dwordx4 v62, s[52:55], s69 offen offset:3096 lds\nQmvva 200,20,160\nQe 9\nQmvva 200,48,164\nQe 10\nQmvva 204,20,168\nQmvva 204,48,172\nQe 11\n"
"Qcvaa 0,112,96,56\nQda 112,68,33024\nQk 20,8,9\nQmvaa 0,116,100\nQda 116,68,33536\nQk 21,10,11\nQmvaa 0,120,104\nQda 120,68,34048\n"
"Qmvaa 0,124,108\nQda 124,68,34560\nQcvaa 4,128,96,56\nQda 128,68,35072\nQmvaa 4,132,100\nQda 132,68,35584\nQmvaa 4,136,104\nQda 136,68,36096\n"
"Qmvaa 4,140,108\nQda 140,68,36608\nQmvva 224,24,144\nQe 12\nQmvva 224,16,148\ns_add_u32 m0, s63, 33024\ns_add_u32 s88, s86, 0\nQmvva 228,24,152\n"
"buffer_load_dwordx4 v64, s[48:51], s88 offen lds\nQe 13\nQmvva 228,16,156\nQe 14\nQmvva 232,24,160\nQe 15\nQmvva 232,16,164\ns_add_u32 s88, s86, 19456\n"
"Qk 22,12,13\nQk 23,14,15\nQmvva 236,24,168\nbuffer_load_dwordx4 v64, s[48:51], s88 offen offset:1024 lds\nQe 0\nQmvva 236,16,172\ns_waitcnt lgkmcnt(0)\nQcvaa 8,112,0,56\n"
"Qe 1\nQmvaa 8,116,4\nv_subrev_u32 v68, 66048, v68\ns_add_u32 s88, s86, 38912\nQk 24,0,1\nQmvaa 8,120,8\nbuffer_load_dwordx4 v64, s[48:51], s88 offen offset:2048 lds\nQe 2\n"
"Qmvaa 8,124,12\nQe 3\nQcvaa 12,128,0,56\nQk 25,2,3\nQmvaa 12,132,4\nQe 4\nv_smfmac_f32_16x16x64_bf16 a[184:187], a[204:207], v[16:23], v77\ns_add_u32 s88, s86, 58368\n"
"Qmvaa 12,136,8\nbuffer_load_dwordx4 v64, s[48:51], s88 offen offset:3072 lds\nQe 5\nQmvaa 12,140,12\nQe 6\nQmava 208,28,144\ns_add_u32 m0, s63, 37120\ns_add_u32 s88, s86, 81920\n"
"Qmava 208,20,148\nbuffer_load_dwordx4 v64, s[48:51], s88 offen lds\nQmava 212,28,152\nQk 26,4,5\nQmava 212,20,156\nQmava 216,28,160\ns_add_u32 s88, s86, 101376\nQmava 216,20,164\n"
"buffer_load_dwordx4 v64, s[48:51], s88 offen offset:1024 lds\nQe 7\nQmava 220,28,168\nQk 27,6,7\nQmava 220,20,172\nQe 8\nQcvaa 0,112,16,56\ns_add_u32 s88, s86, 120832\n"
"Qe 9\nQmvaa 0,116,20\nbuffer_load_dwordx4 v64, s[48:51], s88 offen offset:2048 lds\nQe 10\nQmvaa 0,120,24\nQe 11\nQmvaa 0,124,28\nQk 28,8,9\n"
"Qk 29,10,11\nQcvaa 4,128,16,56\nQe 12\nQmvaa 4,132,20\nQe 13\nQmvaa 4,136,24\nQe 14\nQmvaa 4,140,28\n"
"Qe 15\nQmava 240,32,144\nQta 144,71,0\nQta 146,71,2048\nQmava 240,24,148\nQta 148,71,33024\nQta 150,71,35072\nQmava 244,32,152\n"
"Qta 152,71,256\nQta 154,71,2304\nQmava 244,24,156\nQta 156,71,33280\nQta 158,71,35328\nQmava 248,32,160\nQta 160,71,512\nQta 162,71,2560\n"
"Qmava 248,24,164\nQta 164,71,33536\nQta 166,71,35584\nQmava 252,32,168\nQta 168,71,768\nQta 170,71,2816\nQmava 252,24,172\nQta 172,71,33792\n"
"Qta 174,71,35840\nQcvaa 8,112,32,56\nv_add_u32 v75, 1, v75\nQk 30,12,13\nQmvaa 8,116,36\nds_write_b32 v73, v75\nQmvaa 8,120,40\nQk 31,14,15\n"
"Qmvaa 8,124,44\nQe 0\nQcvaa 12,128,32,56\nQe 1\nQmvaa 12,132,36\nQe 2\nv_smfmac_f32_16x16x64_bf16 a[188:191], a[204:207], v[24:31], v77\nQk 32,0,1\n"
"Qmvaa 12,136,40\ns_waitcnt lgkmcnt(1)\nQmvaa 12,140,44\nQe 3\nQmvva 80,36,144\nQk 33,2,3\nQmvva 80,28,148\nQe 4\n"
"Qmvva 84,36,152\nQe 5\nQmvva 84,28,156\nQe 6\nQmvva 88,36,160\nQe 7\nQmvva 88,28,164\nQk 34,4,5\n"
"Qk 35,6,7\nQmvva 92,36,168\nQe 8\nQmvva 92,28,172\nQe 9\nQcvaa 0,112,48,56\nQe 10\nQmvaa 0,116,52\n"
"ds_read_b32 v66, v74\nQk 36,8,9\nQmvaa 0,120,56\nQe 11\nQmvaa 0,124,60\nQk 37,10,11\nQcvaa 4,128,48,56\nQe 12\n"
"Qmvaa 4,132,52\nQe 13\nQmvaa 4,136,56\ns_waitcnt lgkmcnt(0)\nv_cmp_gt_u32 vcc, v75, v66\nQmvaa 4,140,60\ns_cbranch_vccnz .Lsa31\n.Lra31:\n"
"Qk 38,12,13\nQmvva 112,40,144\ns_add_u32 m0, s62, 33024\ns_add_u32 s69, s87, 524288\nQmvva 112,32,148\nbuffer_load_dwordx4 v62, s[52:55], s69 offen lds\nQmvva 116,40,152\nQe 14\n"
"Qmvva 116,32,156\nQe 15\nQmvva 120,40,160\nQk 39,14,15\nQmvva 120,32,164\ns_add_u32 s69, s87, 588792\nQe 0\nQmvva 124,40,168\n"
"buffer_load_dwordx4 v62, s[52:55], s69 offen offset:1032 lds\nQmvva 124,32,172\nQe 1\nQcvaa 8,112,64,56\nQe 2\nQmvaa 8,116,68\nQe 3\nQmvaa 8,120,72\n"
"s_add_u32 s69, s87, 653296\nQk 40,0,1\nQk 41,2,3\nQmvaa 8,124,76\nbuffer_load_dwordx4 v62, s[52:55], s69 offen offset:2064 lds\nQcvaa 12,128,64,56\nQe 4\nQmvaa 12,132,68\n"
"Qe 5\nv_smfmac_f32_16x16x64_bf16 a[192:195], a[204:207], v[32:39], v77\nQk 42,4,5\nQmvaa 12,136,72\ns_add_u32 s69, s87, 717800\nQe 6\nQmvaa 12,140,76\nbuffer_load_dwordx4 v62, s[52:55], s69 offen offset:3096 lds\n"
"Qmvva 144,44,144\nQe 7\nQmvva 144,36,148\nQk 43,6,7\nQmvva 148,44,152\nQe 8\nQmvva 148,36,156\ns_add_u32 m0, s62, 49536\n"
"s_add_u32 s69, s87, 524416\nQmvva 152,44,160\nbuffer_load_dwordx4 v62, s[52:55], s69 offen lds\nQmvva 152,36,164\nQe 9\nQmvva 156,44,168\nQe 10\nQmvva 156,36,172\n"
"Qe 11\nQcvaa 0,112,80,56\ns_add_u32 s69, s87, 588920\nQk 44,8,9\nQk 45,10,11\nQmvaa 0,116,84\nbuffer_load_dwordx4 v62, s[52:55], s69 offen offset:1032 lds\nQmvaa 0,120,88\n"
"Qe 12\nQmvaa 0,124,92\nQe 13\nQcvaa 4,128,80,56\nQe 14\nQmvaa 4,132,84\ns_add_u32 s69, s87, 653424\nQe 15\n"
"Qmvaa 4,136,88\nbuffer_load_dwordx4 v62, s[52:55], s69 offen offset:2064 lds\nQmvaa 4,140,92\nQk 46,12,13\nQk 47,14,15\nQmvva 176,48,144\nQmvva 176,40,148\nQmvva 180,48,152\n"
"s_add_u32 s69, s87, 717928\nQmvva 180,40,156\nbuffer_load_dwordx4 v62, s[52:55], s69 offen offset:3096 lds\nQmvva 184,48,160\nQe 0\nQmvva 184,40,164\nQe 1\nQmvva 188,48,168\n"
"Qmvva 188,40,172\nQe 2\nQcvaa 8,112,96,56\nQk 48,0,1\nQmvaa 8,116,100\nQmvaa 8,120,104\nQmvaa 8,124,108\nQcvaa 12,128,96,56\n"
"Qmvaa 12,132,100\nv_smfmac_f32_16x16x64_bf16 a[196:199], a[204:207], v[40:47], v77\nQmvaa 12,136,104\nQmvaa 12,140,108\nQmvva 208,16,144\nQta 112,71,34048\nQmvva 208,44,148\nQta 114,71,36096\n"
"Qe 3\nQmvva 212,16,152\nQta 116,71,34304\nQk 49,2,3\nQmvva 212,44,156\nQta 118,71,36352\nQe 4\nQmvva 216,16,160\n"
"Qta 120,71,34560\nQe 5\nQmvva 216,44,164\nQta 122,71,36608\nQe 6\nQmvva 220,16,168\nQta 124,71,34816\nQe 7\n"
"Qmvva 220,44,172\nQta 126,71,36864\nQk 50,4,5\nQk 51,6,7\nQe 8\nQe 9\nQe 10\nQe 11\n"
"Qk 52,8,9\nQk 53,10,11\nQe 12\nQe 13\nQmvva 240,20,144\nv_add_u32 v75, 1, v75\nQe 14\nQmvva 240,48,148\n"
"ds_write_b32 v73, v75\nQmvva 244,20,152\nQmvva 244,48,156\nQk 54,12,13\nQmvva 248,20,160\nQmvva 248,48,164\nQmvva 252,20,168\nQe 15\n"
"Qmvva 252,48,172\nQk 55,14,15\ns_nop 1\nv_smfmac_f32_16x16x64_bf16 a[200:203], a[204:207], v[48:55], v77\nQmava 224,24,144\nds_read_b32 v66, v74\nv_accvgpr_read_b32 v0, a176\nQmava 224,52,148\n"
"v_accvgpr_read_b32 v1, a177\nv_accvgpr_read_b32 v6, a178\nQmava 228,24,152\nv_accvgpr_read_b32 v7, a179\nv_accvgpr_read_b32 v8, a180\nQmava 228,52,156\nv_accvgpr_read_b32 v9, a181\nv_accvgpr_read_b32 v14, a182\n"
"Qmava 232,24,160\nv_accvgpr_read_b32 v15, a183\nv_accvgpr_read_b32 v16, a184\nQmava 232,52,164\ns_waitcnt lgkmcnt(0)\nv_cmp_gt_u32 vcc, v75, v66\nQmava 236,24,168\ns_cbranch_vccnz .Lsa32\n"
".Lra32:\nv_accvgpr_read_b32 v17, a185\nQmava 236,52,172\ns_waitcnt lgkmcnt(0)\nv_accvgpr_read_b32 v22, a186\nQmvva 96,28,112\ns_add_u32 m0, s63, 0\ns_add_u32 s88, s86, 128\n"
"v_accvgpr_read_b32 v23, a187\nQmvva 100,28,116\nbuffer_load_dwordx4 v64, s[48:51], s88 offen lds\nQmvva 104,28,120\nv_accvgpr_read_b32 v24, a188\nv_accvgpr_read_b32 v25, a189\nQmvva 108,28,124\nv_accvgpr_read_b32 v2, a190\n"
"v_accvgpr_read_b32 v3, a191\nQmvva 128,32,112\ns_add_u32 s88, s86, 19584\nv_accvgpr_read_b32 v4, a192\nQmvva 132,32,116\nbuffer_load_dwordx4 v64, s[48:51], s88 offen offset:1024 lds\nQmvva 136,32,120\nv_accvgpr_read_b32 v5, a193\n"
"v_accvgpr_read_b32 v10, a194\nQmvva 140,32,124\nv_accvgpr_read_b32 v11, a195\nv_accvgpr_read_b32 v12, a196\nQmvva 160,36,112\ns_add_u32 s88, s86, 39040\nv_accvgpr_read_b32 v13, a197\nQmvva 164,36,116\n"
"buffer_load_dwordx4 v64, s[48:51], s88 offen offset:2048 lds\nQmvva 168,36,120\nv_accvgpr_read_b32 v18, a198\nv_accvgpr_read_b32 v19, a199\nQmvva 172,36,124\nv_accvgpr_read_b32 v20, a200\nv_accvgpr_read_b32 v21, a201\nQmvva 192,40,112\n"
"s_add_u32 s88, s86, 58496\nv_accvgpr_read_b32 v26, a202\nQmvva 196,40,116\nbuffer_load_dwordx4 v64, s[48:51], s88 offen offset:3072 lds\nQmvva 200,40,120\nv_accvgpr_read_b32 v27, a203\nQa 8,8,9\nQmvva 204,40,124\n"
"Qa 10,10,11\nQa 8,8,10\nQmvva 224,44,112\ns_add_u32 m0, s63, 4096\ns_add_u32 s88, s86, 82048\nQa 16,16,17\nQmvva 228,44,116\nbuffer_load_dwordx4 v64, s[48:51], s88 offen lds\n"
"Qmvva 232,44,120\nQa 18,18,19\nQa 16,16,18\nQmvva 236,44,124\nQa 4,4,5\nQa 6,6,7\nQmava 208,48,112\ns_add_u32 s88, s86, 101504\n"
"Qa 4,4,6\nQmava 212,48,116\nbuffer_load_dwordx4 v64, s[48:51], s88 offen offset:1024 lds\nQmava 216,48,120\nQa 12,12,13\nQa 14,14,15\nQmava 220,48,124\nQa 12,12,14\n"
"Qa 0,0,1\nQmava 240,52,112\ns_add_u32 s88, s86, 120960\nQa 2,2,3\nQmava 244,52,116\nbuffer_load_dwordx4 v64, s[48:51], s88 offen offset:2048 lds\nQmava 248,52,120\nQa 0,0,2\n"
"Qa 24,24,25\nQmava 252,52,124\nQa 26,26,27\nQa 24,24,26\nQa 20,20,21\nQa 22,22,23\nQa 20,20,22\nv_max3_u32 v9, v8, v16, v4\n"
"v_max3_u32 v9, v9, v12, v0\nv_max3_u32 v9, v9, v24, v20\nv_min3_u32 v11, v8, v16, v4\nv_min3_u32 v11, v11, v12, v0\nv_min3_u32 v11, v11, v24, v20\nv_cmp_lt_u32 vcc, 0x44800000, v9\ns_mov_b64 s[74:75], vcc\nv_cmp_gt_u32 vcc, 0x2b800000, v11\n"
"s_or_b64 s[74:75], s[74:75], vcc\ns_cmp_lg_u64 s[74:75], 0\ns_cbranch_scc1 .Lfb\n.Lfbret0:\nv_rcp_f32 v2, v0\nv_rcp_f32 v6, v4\nv_rcp_f32 v10, v8\nv_rcp_f32 v14, v12\n"
"v_rcp_f32 v18, v16\nv_rcp_f32 v22, v20\nv_rcp_f32 v26, v24\nv_and_b32 v66, 16, v60\nv_mov_b32 v67, 0xffff\nv_lshlrev_b32 v66, v66, v67\ns_mov_b32 vcc_lo, 0x00c0000c\ns_mov_b32 vcc_hi, 0xc0000c00\n"
"v_cndmask_b32 v67, 0, v66, vcc\ns_lshr_b64 vcc, vcc, 2\nv_cndmask_b32 v66, 0, v66, vcc\nv_mov_b64 v[0:1], 0\nQk 2,2,2\nv_and_b32 v3, v2, v67\nv_and_b32 v2, v2, v66\nv_mov_b64 v[4:5], 0\n"
"Qk 6,6,6\nv_and_b32 v7, v6, v67\nv_and_b32 v6, v6, v66\nv_mov_b64 v[8:9], 0\nQk 10,10,10\nv_and_b32 v11, v10, v67\nv_and_b32 v10, v10, v66\nv_mov_b64 v[12:13], 0\n"
"Qk 14,14,14\nv_and_b32 v15, v14, v67\nv_and_b32 v14, v14, v66\nv_mov_b64 v[16:17], 0\nQk 18,18,18\nv_and_b32 v19, v18, v67\nv_and_b32 v18, v18, v66\nv_mov_b64 v[20:21], 0\n"
"Qk 22,22,22\nv_and_b32 v23, v22, v67\nv_and_b32 v22, v22, v66\nv_mov_b64 v[24:25], 0\nQk 26,26,26\nv_and_b32 v27, v26, v67\nv_and_b32 v26, v26, v66\nv_mov_b64 v[28:29], 0\n"
"Qk 80,80,81\nQk 81,82,83\nQk 82,84,85\nQk 83,86,87\ns_nop 1\nQzvvv 84,80,0\nQzvvv 80,80,2\n.Lpg8:\n"
"s_cbranch_execz .Ltr10\nQk 88,88,89\nQk 89,90,91\nQk 90,92,93\nQk 91,94,95\ns_nop 1\nQzvvv 92,88,0\nQzvvv 88,88,2\n"
"Qk 96,96,97\nQk 97,98,99\nQk 98,100,101\nQk 99,102,103\nQk 80,80,81\nQk 81,82,83\nQzvvv 100,96,0\nQk 82,84,85\n"
"Qk 83,86,87\nQzvvv 96,96,2\nv_accvgpr_write_b32 a0, v80\nv_accvgpr_write_b32 a1, v81\nv_accvgpr_write_b32 a2, v82\nv_accvgpr_write_b32 a3, v83\nQk 104,104,105\nQk 105,106,107\n"
"Qk 106,108,109\nQk 107,110,111\nQk 88,88,89\nQk 89,90,91\nQzvvv 108,104,0\nQk 90,92,93\nQk 91,94,95\nQzvvv 104,104,2\n"
"v_accvgpr_write_b32 a4, v88\nv_accvgpr_write_b32 a5, v89\nv_accvgpr_write_b32 a6, v90\nv_accvgpr_write_b32 a7, v91\nQk 112,112,113\nQk 113,114,115\nQk 114,116,117\nQk 115,118,119\n"
"Qk 96,96,97\nQk 97,98,99\nQzvvv 116,112,4\nQk 98,100,101\nQk 99,102,103\nQzvvv 112,112,6\nv_accvgpr_write_b32 a8, v96\nv_accvgpr_write_b32 a9, v97\n"
"v_accvgpr_write_b32 a10, v98\nv_accvgpr_write_b32 a11, v99\nQk 120,120,121\nQk 121,122,123\nQk 122,124,125\nQk 123,126,127\nQk 104,104,105\nQk 105,106,107\n"
"Qzvvv 124,120,4\nQk 106,108,109\nQk 107,110,111\nQzvvv 120,120,6\nv_accvgpr_write_b32 a12, v104\nv_accvgpr_write_b32 a13, v105\nv_accvgpr_write_b32 a14, v106\nv_accvgpr_write_b32 a15, v107\n"
"Qk 128,128,129\nQk 129,130,131\nQk 130,132,133\nQk 131,134,135\nQk 112,112,113\nQk 113,114,115\nQzvvv 132,128,4\nQk 114,116,117\n"
"Qk 115,118,119\nQzvvv 128,128,6\nv_accvgpr_write_b32 a16, v112\nv_accvgpr_write_b32 a17, v113\nv_accvgpr_write_b32 a18, v114\nv_accvgpr_write_b32 a19, v115\nQk 136,136,137\nQk 137,138,139\n"
"Qk 138,140,141\nQk 139,142,143\nQk 120,120,121\nQk 121,122,123\nQzvvv 140,136,4\nQk 122,124,125\nQk 123,126,127\nQzvvv 136,136,6\n"
"v_accvgpr_write_b32 a20, v120\nv_accvgpr_write_b32 a21, v121\nv_accvgpr_write_b32 a22, v122\nv_accvgpr_write_b32 a23, v123\nQk 144,144,145\nQk 145,146,147\nQk 146,148,149\nQk 147,150,151\n"
"Qk 128,128,129\nQk 129,130,131\nQzvvv 148,144,8\nQk 130,132,133\nQk 131,134,135\nQzvvv 144,144,10\nv_accvgpr_write_b32 a24, v128\nv_accvgpr_write_b32 a25, v129\n"
"v_accvgpr_write_b32 a26, v130\nv_accvgpr_write_b32 a27, v131\nQk 152,152,153\nQk 153,154,155\nQk 154,156,157\nQk 155,158,159\nQk 136,136,137\nQk 137,138,139\n"
"Qzvvv 156,152,8\nQk 138,140,141\nQk 139,142,143\nQzvvv 152,152,10\nv_accvgpr_write_b32 a28, v136\nv_accvgpr_write_b32 a29, v137\nv_accvgpr_write_b32 a30, v138\nv_accvgpr_write_b32 a31, v139\n"
"Qk 160,160,161\nQk 161,162,163\nQk 162,164,165\nQk 163,166,167\nQk 144,144,145\nQk 145,146,147\nQzvvv 164,160,8\nQk 146,148,149\n"
"Qk 147,150,151\nQzvvv 160,160,10\nv_accvgpr_write_b32 a32, v144\nv_accvgpr_write_b32 a33, v145\nv_accvgpr_write_b32 a34, v146\nv_accvgpr_write_b32 a35, v147\nQk 168,168,169\nQk 169,170,171\n"
"Qk 170,172,173\nQk 171,174,175\nQk 152,152,153\nQk 153,154,155\nQzvvv 172,168,8\nQk 154,156,157\nQk 155,158,159\nQzvvv 168,168,10\n"
"v_accvgpr_write_b32 a36, v152\nv_accvgpr_write_b32 a37, v153\nv_accvgpr_write_b32 a38, v154\nv_accvgpr_write_b32 a39, v155\nQk 176,176,177\nQk 177,178,179\nQk 178,180,181\nQk 179,182,183\n"
"Qk 160,160,161\nQk 161,162,163\nQzvvv 180,176,12\nQk 162,164,165\nQk 163,166,167\nQzvvv 176,176,14\nv_accvgpr_write_b32 a40, v160\nv_accvgpr_write_b32 a41, v161\n"
"v_accvgpr_write_b32 a42, v162\nv_accvgpr_write_b32 a43, v163\nQk 184,184,185\nQk 185,186,187\nQk 186,188,189\nQk 187,190,191\nQk 168,168,169\nQk 169,170,171\n"
"Qzvvv 188,184,12\nQk 170,172,173\nQk 171,174,175\nQzvvv 184,184,14\nv_accvgpr_write_b32 a44, v168\nv_accvgpr_write_b32 a45, v169\nv_accvgpr_write_b32 a46, v170\nv_accvgpr_write_b32 a47, v171\n"
"Qk 192,192,193\nQk 193,194,195\nQk 194,196,197\nQk 195,198,199\nQk 176,176,177\nQk 177,178,179\nQzvvv 196,192,12\nQk 178,180,181\n"
"Qk 179,182,183\nQzvvv 192,192,14\nv_accvgpr_write_b32 a48, v176\nv_accvgpr_write_b32 a49, v177\nv_accvgpr_write_b32 a50, v178\nv_accvgpr_write_b32 a51, v179\nQk 200,200,201\nQk 201,202,203\n"
"Qk 202,204,205\nQk 203,206,207\nQk 184,184,185\nQk 185,186,187\nQzvvv 204,200,12\nQk 186,188,189\nQk 187,190,191\nQzvvv 200,200,14\n"
"v_accvgpr_write_b32 a52, v184\nv_accvgpr_write_b32 a53, v185\nv_accvgpr_write_b32 a54, v186\nv_accvgpr_write_b32 a55, v187\nQk 208,208,209\nQk 209,210,211\nQk 210,212,213\nQk 211,214,215\n"
"Qk 192,192,193\nQk 193,194,195\nQzvvv 212,208,16\nQk 194,196,197\nQk 195,198,199\nQzvvv 208,208,18\nv_accvgpr_write_b32 a56, v192\nv_accvgpr_write_b32 a57, v193\n"
"v_accvgpr_write_b32 a58, v194\nv_accvgpr_write_b32 a59, v195\nQk 216,216,217\nQk 217,218,219\nQk 218,220,221\nQk 219,222,223\nQk 200,200,201\nQk 201,202,203\n"
"Qzvvv 220,216,16\nQk 202,204,205\nQk 203,206,207\nQzvvv 216,216,18\nv_accvgpr_write_b32 a60, v200\nv_accvgpr_write_b32 a61, v201\nv_accvgpr_write_b32 a62, v202\nv_accvgpr_write_b32 a63, v203\n"
"Qk 224,224,225\nQk 225,226,227\nQk 226,228,229\nQk 227,230,231\nQk 208,208,209\nQk 209,210,211\nQzvvv 228,224,16\nQk 210,212,213\n"
"Qk 211,214,215\nQzvvv 224,224,18\nv_accvgpr_write_b32 a64, v208\nv_accvgpr_write_b32 a65, v209\nv_accvgpr_write_b32 a66, v210\nv_accvgpr_write_b32 a67, v211\nQk 232,232,233\nQk 233,234,235\n"
"Qk 234,236,237\nQk 235,238,239\nQk 216,216,217\nQk 217,218,219\nQzvvv 236,232,16\nQk 218,220,221\nQk 219,222,223\nQzvvv 232,232,18\n"
"v_accvgpr_write_b32 a68, v216\nv_accvgpr_write_b32 a69, v217\nv_accvgpr_write_b32 a70, v218\nv_accvgpr_write_b32 a71, v219\nQk 240,240,241\nQk 241,242,243\nQk 242,244,245\nQk 243,246,247\n"
"Qk 224,224,225\nQk 225,226,227\nQzvvv 244,240,20\nQk 226,228,229\nQk 227,230,231\nQzvvv 240,240,22\nv_accvgpr_write_b32 a72, v224\nv_accvgpr_write_b32 a73, v225\n"
"v_accvgpr_write_b32 a74, v226\nv_accvgpr_write_b32 a75, v227\nQk 248,248,249\nQk 249,250,251\nQk 250,252,253\nQk 251,254,255\nQk 232,232,233\nQk 233,234,235\n"
"Qzvvv 252,248,20\nQk 234,236,237\nQk 235,238,239\nQzvvv 248,248,22\nv_accvgpr_write_b32 a76, v232\nv_accvgpr_write_b32 a77, v233\nv_accvgpr_write_b32 a78, v234\nv_accvgpr_write_b32 a79, v235\n"
"v_accvgpr_read_b32 v48, a208\nv_accvgpr_read_b32 v49, a209\nv_accvgpr_read_b32 v50, a210\nv_accvgpr_read_b32 v51, a211\nv_accvgpr_read_b32 v36, a212\nv_accvgpr_read_b32 v37, a213\nv_accvgpr_read_b32 v38, a214\nv_accvgpr_read_b32 v39, a215\n"
"Qk 48,48,49\nQk 49,50,51\nQk 50,36,37\nQk 51,38,39\nQk 240,240,241\nQk 241,242,243\nQzvvv 36,48,20\nQk 242,244,245\n"
"Qk 243,246,247\nQzvvv 48,48,22\nv_accvgpr_write_b32 a80, v240\nv_accvgpr_write_b32 a81, v241\nv_accvgpr_write_b32 a82, v242\nv_accvgpr_write_b32 a83, v243\nv_accvgpr_read_b32 v52, a216\nv_accvgpr_read_b32 v53, a217\n"
"v_accvgpr_read_b32 v54, a218\nv_accvgpr_read_b32 v55, a219\nv_accvgpr_read_b32 v40, a220\nv_accvgpr_read_b32 v41, a221\nv_accvgpr_read_b32 v42, a222\nv_accvgpr_read_b32 v43, a223\nQk 52,52,53\nQk 53,54,55\n"
"Qk 54,40,41\nQk 55,42,43\nQk 248,248,249\nQk 249,250,251\nQzvvv 40,52,20\nQk 250,252,253\nQk 251,254,255\nQzvvv 52,52,22\n"
"v_accvgpr_write_b32 a84, v248\nv_accvgpr_write_b32 a85, v249\nv_accvgpr_write_b32 a86, v250\nv_accvgpr_write_b32 a87, v251\nv_accvgpr_read_b32 v44, a224\nv_accvgpr_read_b32 v45, a225\nv_accvgpr_read_b32 v46, a226\nv_accvgpr_read_b32 v47, a227\n"
"v_accvgpr_read_b32 v32, a228\nv_accvgpr_read_b32 v33, a229\nv_accvgpr_read_b32 v34, a230\nv_accvgpr_read_b32 v35, a231\nQk 44,44,45\nQk 45,46,47\nQk 46,32,33\nQk 47,34,35\n"
"Qk 48,48,49\nQk 49,50,51\nQzvvv 32,44,24\nQk 50,36,37\nQk 51,38,39\nQzvvv 44,44,26\nv_accvgpr_write_b32 a88, v48\nv_accvgpr_write_b32 a89, v49\n"
"v_accvgpr_write_b32 a90, v50\nv_accvgpr_write_b32 a91, v51\nv_accvgpr_read_b32 v48, a232\nv_accvgpr_read_b32 v49, a233\nv_accvgpr_read_b32 v50, a234\nv_accvgpr_read_b32 v51, a235\nv_accvgpr_read_b32 v36, a236\nv_accvgpr_read_b32 v37, a237\n"
"v_accvgpr_read_b32 v38, a238\nv_accvgpr_read_b32 v39, a239\nQk 48,48,49\nQk 49,50,51\nQk 50,36,37\nQk 51,38,39\nQk 52,52,53\nQk 53,54,55\n"
"Qzvvv 36,48,24\nQk 54,40,41\nQk 55,42,43\nQzvvv 48,48,26\nv_accvgpr_write_b32 a92, v52\nv_accvgpr_write_b32 a93, v53\nv_accvgpr_write_b32 a94, v54\nv_accvgpr_write_b32 a95, v55\n"
"v_accvgpr_read_b32 v52, a240\nv_accvgpr_read_b32 v53, a241\nv_accvgpr_read_b32 v54, a242\nv_accvgpr_read_b32 v55, a243\nv_accvgpr_read_b32 v40, a244\nv_accvgpr_read_b32 v41, a245\nv_accvgpr_read_b32 v42, a246\nv_accvgpr_read_b32 v43, a247\n"
"Qk 52,52,53\nQk 53,54,55\nQk 54,40,41\nQk 55,42,43\nQk 44,44,45\nQk 45,46,47\nQzvvv 40,52,24\nQk 46,32,33\n"
"Qk 47,34,35\nQzvvv 52,52,26\nv_accvgpr_write_b32 a96, v44\nv_accvgpr_write_b32 a97, v45\nv_accvgpr_write_b32 a98, v46\nv_accvgpr_write_b32 a99, v47\nv_accvgpr_read_b32 v44, a248\nv_accvgpr_read_b32 v45, a249\n"
"v_accvgpr_read_b32 v46, a250\nv_accvgpr_read_b32 v47, a251\nv_accvgpr_read_b32 v32, a252\nv_accvgpr_read_b32 v33, a253\nv_accvgpr_read_b32 v34, a254\nv_accvgpr_read_b32 v35, a255\nQk 44,44,45\nQk 45,46,47\n"
"Qk 46,32,33\nQk 47,34,35\nQk 48,48,49\nQk 49,50,51\nQzvvv 32,44,24\nQk 50,36,37\nQk 51,38,39\nQzvvv 44,44,26\n"
"v_accvgpr_write_b32 a100, v48\nv_accvgpr_write_b32 a101, v49\nv_accvgpr_write_b32 a102, v50\nv_accvgpr_write_b32 a103, v51\nQk 52,52,53\nQk 53,54,55\nQk 54,40,41\nQk 55,42,43\n"
"v_accvgpr_write_b32 a104, v52\nv_accvgpr_write_b32 a105, v53\nv_accvgpr_write_b32 a106, v54\nv_accvgpr_write_b32 a107, v55\nQk 44,44,45\nQk 45,46,47\nQk 46,32,33\nQk 47,34,35\n"
"v_accvgpr_write_b32 a108, v44\nv_accvgpr_write_b32 a109, v45\nv_accvgpr_write_b32 a110, v46\nv_accvgpr_write_b32 a111, v47\ns_mov_b32 s56, %8\ns_and_b32 s57, %9, 0xffff\ns_mov_b32 s94, 0x7ffff000\ns_mul_i32 s68, %20, 40960\n"
"s_lshl_b32 s69, s70, 10\ns_add_u32 s68, s68, s69\ns_add_u32 s68, s68, 4096\ns_add_u32 s94, %15, s68\ns_addc_u32 s95, %16, 0\ns_and_b32 s95, s95, 0xffff\ns_mov_b32 s75, 37888\ns_mov_b32 s58, 1835008\n"
"s_mov_b32 s67, 1\ns_mul_i32 s71, %12, 57344\ns_mul_i32 s68, s92, 7168\ns_add_u32 s71, s71, s68\ns_mov_b64 exec, 0\ns_cmp_eq_u32 s70, 0\ns_cbranch_scc1 .Lpg12\ns_cmp_eq_u32 s70, 1\n"
"s_cbranch_scc1 .Lpg13\ns_cmp_eq_u32 s70, 2\ns_cbranch_scc1 .Lpg14\ns_branch .Lpg15\n.Ltr6:\ns_mov_b64 exec, -1\ns_branch .Lround\n.Lhook:\n"
".Lpg9:\ns_cbranch_execz .Ltr10\ns_cmp_eq_u32 s66, 1\ns_cbranch_scc1 .Lh1\ns_cmp_eq_u32 s66, 8\ns_cbranch_scc1 .Lh8\ns_cmp_eq_u32 s66, 9\ns_cbranch_scc1 .Lh9\n"
"s_lshr_b32 s68, s99, s66\ns_cmp_eq_u32 s68, 1\ns_cbranch_scc1 .Lh12\ns_branch .Lh11\n.Lh1:\ns_cmp_eq_u32 s70, 0\ns_cselect_b32 s68, 128, 0\nv_mov_b32 v66, 0x7ffff000\n"
"v_cmp_gt_u32 vcc, s68, v60\nv_not_b32 v67, v65\ns_lshl_b32 s68, %19, 2\nv_cndmask_b32 v66, v66, v76, vcc\nv_readlane_b32 s30, v67, %19\nbuffer_store_dword v67, v66, s[24:27], s68 offen offset:1024 sc1\ns_branch .Lhookret\n.Lh8:\n"
"v_bfe_u32 v66, v60, 4, 3\ns_lshl_b32 s68, %19, 7\nv_lshlrev_b32 v66, 2, v66\ns_add_u32 s68, s68, 2293760\nbuffer_load_dword v78, v66, s[24:27], s68 offen offset:1024 sc1\ns_branch .Lhookret\n.Lh9:\ns_waitcnt vmcnt(32)\n"
"s_mov_b32 s97, 0\n.Lack_l:\nv_cmp_ne_u32 vcc, s30, v78\ns_cmp_eq_u64 vcc, 0\ns_cbranch_scc1 .Lack_ok\ns_sleep 1\nv_bfe_u32 v66, v60, 4, 3\ns_lshl_b32 s68, %19, 7\n"
"v_lshlrev_b32 v66, 2, v66\ns_add_u32 s68, s68, 2293760\nbuffer_load_dword v78, v66, s[24:27], s68 offen offset:1024 sc1\ns_waitcnt vmcnt(0)\ns_add_u32 s97, s97, 1\ns_cmp_lt_u32 s97, 1048576\ns_cbranch_scc1 .Lack_l\n.Lack_ok:\n"
"s_lshl_b32 s68, %19, 4\ns_cmp_eq_u32 s70, 0\ns_cselect_b32 s68, s68, -1\nv_mov_b32 v67, 0x7ffff000\nv_cmp_eq_u32 vcc, s68, v60\ns_nop 1\nv_cndmask_b32 v67, v67, v76, vcc\nv_add_u32 v66, 1, v65\n"
"buffer_store_dword v66, v67, s[24:27], 0 offen sc1\ns_branch .Lhookret\n.Lh11:\nbuffer_load_dword v78, v76, s[24:27], 0 offen sc1\ns_branch .Lhookret\n.Lh12:\ns_waitcnt vmcnt(32)\nv_sub_u32 v66, v78, v65\n"
"v_cmp_gt_u32 vcc, 1, v66\ns_and_b64 vcc, vcc, s[28:29]\ns_cbranch_scc1 .Lf1_w\n.Lf1_r:\ns_cmp_lg_u32 s70, 3\ns_cbranch_scc1 .Lf1_n\nbuffer_inv sc1\n.Lf1_n:\n"
"s_lshr_b32 s88, %19, 2\ns_lshl_b32 s88, s88, 4\ns_add_u32 s88, s88, s93\ns_mul_i32 s68, s88, 57344\ns_mul_i32 s69, s92, 7168\ns_add_u32 s68, s68, s69\ns_add_u32 s48, %8, s68\ns_addc_u32 s49, %9, 0\n"
"s_and_b32 s49, s49, 0xffff\ns_sub_u32 s50, 1835008, s68\ns_mul_i32 s68, s88, 163840\ns_and_b32 s69, %19, 3\ns_mul_i32 s69, s69, 320\ns_add_u32 s68, s68, s69\ns_add_u32 s52, %15, s68\ns_addc_u32 s53, %16, 0\n"
"s_and_b32 s53, s53, 0xffff\ns_sub_u32 s54, 5246976, s68\ns_add_u32 s71, s71, 458752\ns_and_b32 s87, %19, 3\ns_mul_i32 s87, s87, 327680\ns_add_u32 s87, s87, 1310720\ns_mul_i32 s69, s92, 10240\nv_bfe_u32 v66, v60, 5, 3\n"
"v_mul_u32_u24 v66, 1280, v66\nv_and_b32 v67, 16, v60\nv_add3_u32 v67, v67, v66, s69\n.Lpg10:\ns_cbranch_execz .Ltr10\ns_and_b32 s69, %19, 1\nv_lshrrev_b32 v66, 8, v60\ns_lshl_b32 s68, s93, 1\n"
"v_xor_b32 v66, s68, v66\nv_lshl_add_u32 v66, v66, 5, v67\ns_lshr_b32 s68, 128, s69\nv_add_u32 v61, s68, v66\ns_mul_i32 s68, s69, 192\nv_add_u32 v63, s68, v66\nv_bfe_u32 v66, v60, 8, 1\nv_xor_b32 v66, s93, v66\n"
"v_lshl_add_u32 v67, v66, 5, v67\nv_lshrrev_b32 v66, 9, v60\nv_mul_u32_u24 v66, 20480, v66\ns_xor_b32 s69, s69, 1\ns_lshl_b32 s69, s69, 8\nv_add3_u32 v62, v66, v67, s69\ns_branch .Lhookret\n.Lf1_w:\n"
"s_mov_b32 s97, 0\n.Lf1_l:\ns_sleep 1\nbuffer_load_dword v78, v76, s[24:27], 0 offen sc1\ns_waitcnt vmcnt(0)\ns_add_u32 s97, s97, 1\ns_cmp_gt_u32 s97, 1048576\ns_cbranch_scc1 .Lf1_r\n"
"v_sub_u32 v66, v78, v65\nv_cmp_gt_u32 vcc, 1, v66\ns_and_b64 vcc, vcc, s[28:29]\ns_cbranch_scc1 .Lf1_l\ns_branch .Lf1_r\n.Lsa1:\nds_read_b32 v66, v74\ns_waitcnt lgkmcnt(0)\n"
"v_cmp_gt_u32 vcc, v75, v66\ns_cbranch_vccnz .Lsa1\ns_branch .Lra1\n.Lsp1:\ns_memrealtime s[74:75]\ns_waitcnt lgkmcnt(0)\ns_sub_u32 s75, s77, s74\ns_cmp_lt_u32 s75, 256\n"
"s_cbranch_scc1 .Lsp1\ns_mov_b32 s74, s77\ns_branch .Lrp1\n.Lsa2:\nds_read_b32 v66, v74\ns_waitcnt lgkmcnt(0)\nv_cmp_gt_u32 vcc, v75, v66\ns_cbranch_vccnz .Lsa2\n"
"s_branch .Lra2\n.Lsp2:\ns_memrealtime s[74:75]\ns_waitcnt lgkmcnt(0)\ns_sub_u32 s75, s77, s74\ns_cmp_lt_u32 s75, 256\ns_cbranch_scc1 .Lsp2\ns_mov_b32 s74, s77\n"
"s_branch .Lrp2\n.Lsa3:\nds_read_b32 v66, v74\ns_waitcnt lgkmcnt(0)\nv_cmp_gt_u32 vcc, v75, v66\ns_cbranch_vccnz .Lsa3\ns_branch .Lra3\n.Lsp3:\n"
"s_memrealtime s[74:75]\ns_waitcnt lgkmcnt(0)\ns_sub_u32 s75, s77, s74\ns_cmp_lt_u32 s75, 256\ns_cbranch_scc1 .Lsp3\ns_mov_b32 s74, s77\ns_branch .Lrp3\n.Lsa4:\n"
"ds_read_b32 v66, v74\ns_waitcnt lgkmcnt(0)\nv_cmp_gt_u32 vcc, v75, v66\ns_cbranch_vccnz .Lsa4\ns_branch .Lra4\n.Lsp4:\ns_memrealtime s[74:75]\ns_waitcnt lgkmcnt(0)\n"
"s_sub_u32 s75, s77, s74\ns_cmp_lt_u32 s75, 256\ns_cbranch_scc1 .Lsp4\ns_mov_b32 s74, s77\ns_branch .Lrp4\n.Lsa5:\nds_read_b32 v66, v74\ns_waitcnt lgkmcnt(0)\n"
"v_cmp_gt_u32 vcc, v75, v66\ns_cbranch_vccnz .Lsa5\ns_branch .Lra5\n.Lsp5:\ns_memrealtime s[74:75]\ns_waitcnt lgkmcnt(0)\ns_sub_u32 s75, s77, s74\ns_cmp_lt_u32 s75, 256\n"
"s_cbranch_scc1 .Lsp5\ns_mov_b32 s74, s77\ns_branch .Lrp5\n.Lsa30:\nds_read_b32 v66, v74\ns_waitcnt lgkmcnt(0)\nv_cmp_gt_u32 vcc, v75, v66\ns_cbranch_vccnz .Lsa30\n"
"s_branch .Lra30\n.Lsa31:\nds_read_b32 v66, v74\ns_waitcnt lgkmcnt(0)\nv_cmp_gt_u32 vcc, v75, v66\ns_cbranch_vccnz .Lsa31\ns_branch .Lra31\n.Lsa32:\n"
"ds_read_b32 v66, v74\ns_waitcnt lgkmcnt(0)\nv_cmp_gt_u32 vcc, v75, v66\ns_cbranch_vccnz .Lsa32\ns_branch .Lra32\n.Lsa30x:\nds_read_b32 v66, v74\ns_waitcnt lgkmcnt(0)\n"
"v_cmp_gt_u32 vcc, v75, v66\ns_cbranch_vccnz .Lsa30x\ns_branch .Lra30x\n.Lsa31x:\nds_read_b32 v66, v74\ns_waitcnt lgkmcnt(0)\nv_cmp_gt_u32 vcc, v75, v66\ns_cbranch_vccnz .Lsa31x\n"
"s_branch .Lra31x\n.Lso40:\nds_read_b32 v66, v74\ns_waitcnt lgkmcnt(0)\nv_cmp_gt_u32 vcc, v75, v66\ns_cbranch_vccnz .Lso40\ns_branch .Lro40\n.Lso180:\n"
"ds_read_b32 v66, v74\ns_waitcnt lgkmcnt(0)\nv_cmp_gt_u32 vcc, v75, v66\ns_cbranch_vccnz .Lso180\ns_branch .Lro180\n.Lso320:\nds_read_b32 v66, v74\ns_waitcnt lgkmcnt(0)\n"
"v_cmp_gt_u32 vcc, v75, v66\ns_cbranch_vccnz .Lso320\ns_branch .Lro320\n.Lso460:\nds_read_b32 v66, v74\ns_waitcnt lgkmcnt(0)\nv_cmp_gt_u32 vcc, v75, v66\ns_cbranch_vccnz .Lso460\n"
"s_branch .Lro460\n.Lso600:\nds_read_b32 v66, v74\ns_waitcnt lgkmcnt(0)\nv_cmp_gt_u32 vcc, v75, v66\ns_cbranch_vccnz .Lso600\ns_branch .Lro600\n.Lso740:\n"
"ds_read_b32 v66, v74\ns_waitcnt lgkmcnt(0)\nv_cmp_gt_u32 vcc, v75, v66\ns_cbranch_vccnz .Lso740\ns_branch .Lro740\n.Lso880:\nds_read_b32 v66, v74\ns_waitcnt lgkmcnt(0)\n"
"v_cmp_gt_u32 vcc, v75, v66\ns_cbranch_vccnz .Lso880\ns_branch .Lro880\n.Lso1020:\nds_read_b32 v66, v74\ns_waitcnt lgkmcnt(0)\nv_cmp_gt_u32 vcc, v75, v66\ns_cbranch_vccnz .Lso1020\n"
"s_branch .Lro1020\n.Lso1160:\nds_read_b32 v66, v74\ns_waitcnt lgkmcnt(0)\nv_cmp_gt_u32 vcc, v75, v66\ns_cbranch_vccnz .Lso1160\ns_branch .Lro1160\n.Lso1300:\n"
"ds_read_b32 v66, v74\ns_waitcnt lgkmcnt(0)\nv_cmp_gt_u32 vcc, v75, v66\ns_cbranch_vccnz .Lso1300\ns_branch .Lro1300\n.Lso1440:\nds_read_b32 v66, v74\ns_waitcnt lgkmcnt(0)\n"
"v_cmp_gt_u32 vcc, v75, v66\ns_cbranch_vccnz .Lso1440\ns_branch .Lro1440\n.Lso1455:\nds_read_b32 v66, v74\ns_waitcnt lgkmcnt(0)\nv_cmp_gt_u32 vcc, v75, v66\ns_cbranch_vccnz .Lso1455\n"
"s_branch .Lro1455\n.Lso1525r0:\nds_read_b32 v66, v74\ns_waitcnt lgkmcnt(0)\nv_cmp_gt_u32 vcc, v75, v66\ns_cbranch_vccnz .Lso1525r0\ns_branch .Lro1525r0\n.Lso1595r0:\n"
"ds_read_b32 v66, v74\ns_waitcnt lgkmcnt(0)\nv_cmp_gt_u32 vcc, v75, v66\ns_cbranch_vccnz .Lso1595r0\ns_branch .Lro1595r0\n.Lso1665r0:\nds_read_b32 v66, v74\ns_waitcnt lgkmcnt(0)\n"
"v_cmp_gt_u32 vcc, v75, v66\ns_cbranch_vccnz .Lso1665r0\ns_branch .Lro1665r0\n.Lso1735r0:\nds_read_b32 v66, v74\ns_waitcnt lgkmcnt(0)\nv_cmp_gt_u32 vcc, v75, v66\ns_cbranch_vccnz .Lso1735r0\n"
"s_branch .Lro1735r0\n.Lso1805r0:\nds_read_b32 v66, v74\ns_waitcnt lgkmcnt(0)\nv_cmp_gt_u32 vcc, v75, v66\ns_cbranch_vccnz .Lso1805r0\ns_branch .Lro1805r0\n.Lso1878r0:\n"
"ds_read_b32 v66, v74\ns_waitcnt lgkmcnt(0)\nv_cmp_gt_u32 vcc, v75, v66\ns_cbranch_vccnz .Lso1878r0\ns_branch .Lro1878r0\n.Lso1920r0:\nds_read_b32 v66, v74\ns_waitcnt lgkmcnt(0)\n"
"v_cmp_gt_u32 vcc, v75, v66\ns_cbranch_vccnz .Lso1920r0\ns_branch .Lro1920r0\n.Lso1960r0:\nds_read_b32 v66, v74\ns_waitcnt lgkmcnt(0)\nv_cmp_gt_u32 vcc, v75, v66\ns_cbranch_vccnz .Lso1960r0\n"
"s_branch .Lro1960r0\n.Lso2046r0:\nds_read_b32 v66, v74\ns_waitcnt lgkmcnt(0)\nv_cmp_gt_u32 vcc, v75, v66\ns_cbranch_vccnz .Lso2046r0\ns_branch .Lro2046r0\n.Lso2088r0:\n"
"ds_read_b32 v66, v74\ns_waitcnt lgkmcnt(0)\nv_cmp_gt_u32 vcc, v75, v66\ns_cbranch_vccnz .Lso2088r0\ns_branch .Lro2088r0\n.Lso2172r0:\nds_read_b32 v66, v74\ns_waitcnt lgkmcnt(0)\n"
"v_cmp_gt_u32 vcc, v75, v66\ns_cbranch_vccnz .Lso2172r0\ns_branch .Lro2172r0\n.Lso2222r0:\nds_read_b32 v66, v74\ns_waitcnt lgkmcnt(0)\nv_cmp_gt_u32 vcc, v75, v66\ns_cbranch_vccnz .Lso2222r0\n"
"s_branch .Lro2222r0\n.Lso1525r1:\nds_read_b32 v66, v74\ns_waitcnt lgkmcnt(0)\nv_cmp_gt_u32 vcc, v75, v66\ns_cbranch_vccnz .Lso1525r1\ns_branch .Lro1525r1\n.Lso1595r1:\n"
"ds_read_b32 v66, v74\ns_waitcnt lgkmcnt(0)\nv_cmp_gt_u32 vcc, v75, v66\ns_cbranch_vccnz .Lso1595r1\ns_branch .Lro1595r1\n.Lso1665r1:\nds_read_b32 v66, v74\ns_waitcnt lgkmcnt(0)\n"
"v_cmp_gt_u32 vcc, v75, v66\ns_cbranch_vccnz .Lso1665r1\ns_branch .Lro1665r1\n.Lso1735r1:\nds_read_b32 v66, v74\ns_waitcnt lgkmcnt(0)\nv_cmp_gt_u32 vcc, v75, v66\ns_cbranch_vccnz .Lso1735r1\n"
"s_branch .Lro1735r1\n.Lso1805r1:\nds_read_b32 v66, v74\ns_waitcnt lgkmcnt(0)\nv_cmp_gt_u32 vcc, v75, v66\ns_cbranch_vccnz .Lso1805r1\ns_branch .Lro1805r1\n.Lso1840r1:\n"
"ds_read_b32 v66, v74\ns_waitcnt lgkmcnt(0)\nv_cmp_gt_u32 vcc, v75, v66\ns_cbranch_vccnz .Lso1840r1\ns_branch .Lro1840r1\n.Lso1878r1:\nds_read_b32 v66, v74\ns_waitcnt lgkmcnt(0)\n"
"v_cmp_gt_u32 vcc, v75, v66\ns_cbranch_vccnz .Lso1878r1\ns_branch .Lro1878r1\n.Lso1920r1:\nds_read_b32 v66, v74\ns_waitcnt lgkmcnt(0)\nv_cmp_gt_u32 vcc, v75, v66\ns_cbranch_vccnz .Lso1920r1\n"
"s_branch .Lro1920r1\n.Lso1945r1:\nds_read_b32 v66, v74\ns_waitcnt lgkmcnt(0)\nv_cmp_gt_u32 vcc, v75, v66\ns_cbranch_vccnz .Lso1945r1\ns_branch .Lro1945r1\n.Lso2004r1:\n"
"ds_read_b32 v66, v74\ns_waitcnt lgkmcnt(0)\nv_cmp_gt_u32 vcc, v75, v66\ns_cbranch_vccnz .Lso2004r1\ns_branch .Lro2004r1\n.Lso2045r1:\nds_read_b32 v66, v74\ns_waitcnt lgkmcnt(0)\n"
"v_cmp_gt_u32 vcc, v75, v66\ns_cbranch_vccnz .Lso2045r1\ns_branch .Lro2045r1\n.Lso2130r1:\nds_read_b32 v66, v74\ns_waitcnt lgkmcnt(0)\nv_cmp_gt_u32 vcc, v75, v66\ns_cbranch_vccnz .Lso2130r1\n"
"s_branch .Lro2130r1\n.Lso2172r1:\nds_read_b32 v66, v74\ns_waitcnt lgkmcnt(0)\nv_cmp_gt_u32 vcc, v75, v66\ns_cbranch_vccnz .Lso2172r1\ns_branch .Lro2172r1\n.Lso2225r1:\n"
"ds_read_b32 v66, v74\ns_waitcnt lgkmcnt(0)\nv_cmp_gt_u32 vcc, v75, v66\ns_cbranch_vccnz .Lso2225r1\ns_branch .Lro2225r1\n.Ltail1:\n.Lpg11:\ns_cbranch_execz .Ltr10\n"
"Qcvaa 0,112,32,56\nv_add_u32 v75, 1, v75\nQk 38,4,5\nQmvaa 0,116,36\ns_waitcnt vmcnt(0)\nds_write_b32 v73, v75\nQmvaa 0,120,40\nQe 7\n"
"Qmvaa 0,124,44\nQk 39,6,7\nQcvaa 4,128,32,56\nQe 8\nQmvaa 4,132,36\nQe 9\nQmvaa 4,136,40\nQe 10\n"
"Qmvaa 4,140,44\ns_waitcnt lgkmcnt(1)\nQk 40,8,9\nQmvva 96,44,144\nQe 11\nQmvva 96,36,148\nQk 41,10,11\nQmvva 100,44,152\n"
"Qe 12\nQmvva 100,36,156\nQe 13\nQmvva 104,44,160\nQe 14\nQmvva 104,36,164\nQe 15\nQmvva 108,44,168\n"
"Qk 42,12,13\nQk 43,14,15\nQmvva 108,36,172\nQe 0\nQcvaa 8,112,48,56\nQe 1\nQmvaa 8,116,52\nQe 2\n"
"Qmvaa 8,120,56\nds_read_b32 v66, v74\nQk 44,0,1\nQmvaa 8,124,60\nQe 3\nQcvaa 12,128,48,56\nQk 45,2,3\nQmvaa 12,132,52\n"
"Qe 4\nv_smfmac_f32_16x16x64_bf16 a[176:179], a[204:207], v[36:43], v77\nQmvaa 12,136,56\ns_waitcnt lgkmcnt(0)\nv_cmp_gt_u32 vcc, v75, v66\nQmvaa 12,140,60\ns_cbranch_vccnz .Lsa30x\n.Lra30x:\n"
"Qmvva 128,48,144\ns_add_u32 m0, s96, 0\ns_add_u32 s95, s87, 4096\nQmvva 128,40,148\nbuffer_load_dwordx4 v61, s[52:55], s95 offen lds\nQmvva 132,48,152\nQe 5\nQmvva 132,40,156\n"
"Qe 6\nQmvva 136,48,160\nQe 7\nQmvva 136,40,164\ns_add_u32 s95, s87, 23552\nQk 46,4,5\nQk 47,6,7\nQmvva 140,48,168\n"
"buffer_load_dwordx4 v61, s[52:55], s95 offen offset:1024 lds\nQmvva 140,40,172\nQe 8\nQcvaa 0,112,64,56\nQe 9\nQmvaa 0,116,68\nQe 10\nQmvaa 0,120,72\n"
"s_add_u32 m0, s96, 8192\ns_add_u32 s95, s87, 45056\nQe 11\nQmvaa 0,124,76\nbuffer_load_dwordx4 v61, s[52:55], s95 offen lds\nQcvaa 4,128,64,56\nQk 48,8,9\nQk 49,10,11\n"
"Qmvaa 4,132,68\nQe 12\nQmvaa 4,136,72\nQe 13\nQmvaa 4,140,76\ns_add_u32 s95, s87, 64512\nQe 14\nQmvva 160,52,144\n"
"buffer_load_dwordx4 v61, s[52:55], s95 offen offset:1024 lds\nQmvva 160,44,148\nQe 15\nQmvva 164,52,152\nQk 50,12,13\nQk 51,14,15\nQmvva 164,44,156\nQe 0\n"
"Qmvva 168,52,160\ns_add_u32 m0, s96, 16384\ns_add_u32 s95, s87, 4096\nQmvva 168,44,164\nbuffer_load_dwordx4 v62, s[52:55], s95 offen lds\nQmvva 172,52,168\nQe 1\nQmvva 172,44,172\n"
"Qe 2\nQcvaa 8,112,80,56\nQe 3\nQmvaa 8,116,84\ns_add_u32 s95, s87, 44032\nQk 16,0,1\nQk 17,2,3\nQmvaa 8,120,88\n"
"buffer_load_dwordx4 v62, s[52:55], s95 offen offset:1024 lds\nQmvaa 8,124,92\nQe 4\nQcvaa 12,128,80,56\nQe 5\nQmvaa 12,132,84\nQe 6\nv_smfmac_f32_16x16x64_bf16 a[180:183], a[204:207], v[44:51], v77\n"
"s_add_u32 m0, s96, 24576\ns_add_u32 s95, s87, 4096\nQk 18,4,5\nQmvaa 12,136,88\nbuffer_load_dwordx4 v63, s[52:55], s95 offen lds\nQmvaa 12,140,92\nQe 7\nQmvva 192,20,144\n"
"Qk 19,6,7\nQmvva 192,48,148\nQmvva 196,20,152\ns_add_u32 s95, s87, 23552\nQe 8\nQmvva 196,48,156\nbuffer_load_dwordx4 v63, s[52:55], s95 offen offset:1024 lds\nQmvva 200,20,160\n"
"Qe 9\nQmvva 200,48,164\nQe 10\nQmvva 204,20,168\nQmvva 204,48,172\ns_add_u32 m0, s96, 32768\ns_add_u32 s95, s87, 45056\nQe 11\n"
"Qcvaa 0,112,96,56\nQda 112,68,33024\nbuffer_load_dwordx4 v63, s[52:55], s95 offen lds\nQk 20,8,9\nQmvaa 0,116,100\nQda 116,68,33536\nQk 21,10,11\nQmvaa 0,120,104\n"
"Qda 120,68,34048\nQmvaa 0,124,108\nQda 124,68,34560\nQcvaa 4,128,96,56\nQda 128,68,35072\ns_add_u32 s95, s87, 64512\nQmvaa 4,132,100\nQda 132,68,35584\n"
"buffer_load_dwordx4 v63, s[52:55], s95 offen offset:1024 lds\nQmvaa 4,136,104\nQda 136,68,36096\nQmvaa 4,140,108\nQda 140,68,36608\nQmvva 224,24,144\nQe 12\nQmvva 224,16,148\n"
"s_add_u32 m0, s96, 40960\ns_mov_b32 s95, 4096\nQmvva 228,24,152\nbuffer_load_dwordx4 v61, s[52:55], s95 offen lds\nQe 13\nQmvva 228,16,156\nQe 14\nQmvva 232,24,160\n"
"Qe 15\nQmvva 232,16,164\nQk 22,12,13\nQk 23,14,15\nQmvva 236,24,168\ns_mov_b32 s95, 23552\nQe 0\nQmvva 236,16,172\n"
"buffer_load_dwordx4 v61, s[52:55], s95 offen offset:1024 lds\ns_waitcnt lgkmcnt(0)\nQcvaa 8,112,0,56\nQe 1\nQmvaa 8,116,4\nv_subrev_u32 v68, 66048, v68\nQk 24,0,1\nQmvaa 8,120,8\n"
"Qe 2\nQmvaa 8,124,12\ns_add_u32 m0, s96, 49152\ns_mov_b32 s95, 45056\nQe 3\nQcvaa 12,128,0,56\nbuffer_load_dwordx4 v61, s[52:55], s95 offen lds\nQk 25,2,3\n"
"Qmvaa 12,132,4\nQe 4\nv_smfmac_f32_16x16x64_bf16 a[184:187], a[204:207], v[16:23], v77\nQmvaa 12,136,8\nQe 5\nQmvaa 12,140,12\ns_mov_b32 s95, 64512\nQe 6\n"
"Qmava 208,28,144\nbuffer_load_dwordx4 v61, s[52:55], s95 offen offset:1024 lds\nQmava 208,20,148\nQmava 212,28,152\nQk 26,4,5\nQmava 212,20,156\nQmava 216,28,160\ns_add_u32 m0, s96, 57344\n"
"s_mov_b32 s95, 4096\nQmava 216,20,164\nbuffer_load_dwordx4 v62, s[52:55], s95 offen lds\nQe 7\nQmava 220,28,168\nQk 27,6,7\nQmava 220,20,172\nQe 8\n"
"Qcvaa 0,112,16,56\nQe 9\nQmvaa 0,116,20\ns_mov_b32 s95, 44032\nQe 10\nQmvaa 0,120,24\nbuffer_load_dwordx4 v62, s[52:55], s95 offen offset:1024 lds\nQe 11\n"
"Qmvaa 0,124,28\nQk 28,8,9\nQk 29,10,11\nQcvaa 4,128,16,56\nQe 12\nQmvaa 4,132,20\nQe 13\nQmvaa 4,136,24\n"
"s_add_u32 m0, s96, 132096\ns_mov_b32 s95, 4096\nQe 14\nQmvaa 4,140,28\nbuffer_load_dwordx4 v63, s[52:55], s95 offen lds\nQe 15\nQmava 240,32,144\nQta 144,71,0\n"
"Qta 146,71,2048\nQmava 240,24,148\nQta 148,71,33024\nQta 150,71,35072\nQmava 244,32,152\nQta 152,71,256\nQta 154,71,2304\nQmava 244,24,156\n"
"Qta 156,71,33280\nQta 158,71,35328\ns_mov_b32 s95, 23552\nQmava 248,32,160\nQta 160,71,512\nQta 162,71,2560\nbuffer_load_dwordx4 v63, s[52:55], s95 offen offset:1024 lds\nQmava 248,24,164\n"
"Qta 164,71,33536\nQta 166,71,35584\nQmava 252,32,168\nQta 168,71,768\nQta 170,71,2816\nQmava 252,24,172\nQta 172,71,33792\nQta 174,71,35840\n"
"Qcvaa 8,112,32,56\nv_add_u32 v75, 1, v75\nQk 30,12,13\nQmvaa 8,116,36\nds_write_b32 v73, v75\nQmvaa 8,120,40\nQk 31,14,15\nQmvaa 8,124,44\n"
"Qe 0\nQcvaa 12,128,32,56\nQe 1\nQmvaa 12,132,36\nQe 2\nv_smfmac_f32_16x16x64_bf16 a[188:191], a[204:207], v[24:31], v77\nQk 32,0,1\nQmvaa 12,136,40\n"
"s_waitcnt lgkmcnt(1)\nQmvaa 12,140,44\nQe 3\nQmvva 80,36,144\nQk 33,2,3\nQmvva 80,28,148\nQe 4\nQmvva 84,36,152\n"
"Qe 5\nQmvva 84,28,156\nQe 6\nQmvva 88,36,160\nQe 7\nQmvva 88,28,164\nQk 34,4,5\nQk 35,6,7\n"
"Qmvva 92,36,168\nQe 8\nQmvva 92,28,172\nQe 9\nQcvaa 0,112,48,56\nQe 10\nQmvaa 0,116,52\nds_read_b32 v66, v74\n"
"Qk 36,8,9\nQmvaa 0,120,56\nQe 11\nQmvaa 0,124,60\nQk 37,10,11\nQcvaa 4,128,48,56\nQe 12\nQmvaa 4,132,52\n"
"Qe 13\nQmvaa 4,136,56\ns_waitcnt lgkmcnt(0)\nv_cmp_gt_u32 vcc, v75, v66\nQmvaa 4,140,60\ns_cbranch_vccnz .Lsa31x\n.Lra31x:\nQk 38,12,13\n"
"Qmvva 112,40,144\ns_add_u32 m0, s96, 140288\ns_mov_b32 s95, 45056\nQmvva 112,32,148\nbuffer_load_dwordx4 v63, s[52:55], s95 offen lds\nQmvva 116,40,152\nQe 14\nQmvva 116,32,156\n"
"Qe 15\nQmvva 120,40,160\nQk 39,14,15\nQmvva 120,32,164\ns_mov_b32 s95, 64512\nQe 0\nQmvva 124,40,168\nbuffer_load_dwordx4 v63, s[52:55], s95 offen offset:1024 lds\n"
"Qmvva 124,32,172\nQe 1\nQcvaa 8,112,64,56\nQe 2\nQmvaa 8,116,68\nQe 3\nQmvaa 8,120,72\ns_add_u32 m0, s96, 148480\n"
"s_add_u32 s95, s87, 86016\nQk 40,0,1\nQk 41,2,3\nQmvaa 8,124,76\nbuffer_load_dwordx4 v61, s[52:55], s95 offen lds\nQcvaa 12,128,64,56\nQe 4\nQmvaa 12,132,68\n"
"Qe 5\nv_smfmac_f32_16x16x64_bf16 a[192:195], a[204:207], v[32:39], v77\nQk 42,4,5\nQmvaa 12,136,72\ns_add_u32 s95, s87, 105472\n.Lpg12:\ns_cbranch_execz .Ltr6\nQe 6\n"
"Qmvaa 12,140,76\nbuffer_load_dwordx4 v61, s[52:55], s95 offen offset:1024 lds\nQmvva 144,44,144\nQe 7\nQmvva 144,36,148\nQk 43,6,7\nQmvva 148,44,152\nQe 8\n"
"Qmvva 148,36,156\ns_add_u32 m0, s96, 65536\ns_add_u32 s95, s87, 126976\nQmvva 152,44,160\nbuffer_load_dwordx4 v61, s[52:55], s95 offen lds\nQmvva 152,36,164\nQe 9\nQmvva 156,44,168\n"
"Qe 10\nQmvva 156,36,172\nQe 11\nQcvaa 0,112,80,56\ns_add_u32 s95, s87, 146432\nQk 44,8,9\nQk 45,10,11\nQmvaa 0,116,84\n"
"buffer_load_dwordx4 v61, s[52:55], s95 offen offset:1024 lds\nQmvaa 0,120,88\nQe 12\nQmvaa 0,124,92\nQe 13\nQcvaa 4,128,80,56\nQe 14\nQmvaa 4,132,84\n"
"s_add_u32 m0, s96, 73728\ns_add_u32 s95, s87, 86016\nQe 15\nQmvaa 4,136,88\nbuffer_load_dwordx4 v62, s[52:55], s95 offen lds\nQmvaa 4,140,92\nQk 46,12,13\nQk 47,14,15\n"
"Qmvva 176,48,144\nQmvva 176,40,148\nQmvva 180,48,152\ns_add_u32 s95, s87, 125952\nQmvva 180,40,156\nbuffer_load_dwordx4 v62, s[52:55], s95 offen offset:1024 lds\nQmvva 184,48,160\nQe 0\n"
"Qmvva 184,40,164\nQe 1\nQmvva 188,48,168\nQmvva 188,40,172\ns_add_u32 m0, s96, 81920\ns_add_u32 s95, s87, 86016\nQe 2\nQcvaa 8,112,96,56\n"
"buffer_load_dwordx4 v63, s[52:55], s95 offen lds\nQk 48,0,1\nQmvaa 8,116,100\nQmvaa 8,120,104\nQmvaa 8,124,108\nQcvaa 12,128,96,56\ns_add_u32 s95, s87, 105472\nQmvaa 12,132,100\n"
"buffer_load_dwordx4 v63, s[52:55], s95 offen offset:1024 lds\nv_smfmac_f32_16x16x64_bf16 a[196:199], a[204:207], v[40:47], v77\nQmvaa 12,136,104\nQmvaa 12,140,108\nQmvva 208,16,144\nQta 112,71,34048\ns_add_u32 m0, s96, 90112\ns_add_u32 s95, s87, 126976\n"
"Qmvva 208,44,148\nQta 114,71,36096\nbuffer_load_dwordx4 v63, s[52:55], s95 offen lds\nQe 3\nQmvva 212,16,152\nQta 116,71,34304\nQk 49,2,3\nQmvva 212,44,156\n"
"Qta 118,71,36352\nQe 4\nQmvva 216,16,160\nQta 120,71,34560\nQe 5\nQmvva 216,44,164\nQta 122,71,36608\ns_add_u32 s95, s87, 146432\n"
"Qe 6\nQmvva 220,16,168\nQta 124,71,34816\nbuffer_load_dwordx4 v63, s[52:55], s95 offen offset:1024 lds\nQe 7\nQmvva 220,44,172\nQta 126,71,36864\nQk 50,4,5\n"
"Qk 51,6,7\nQe 8\nQe 9\nQe 10\nQe 11\nQk 52,8,9\nQk 53,10,11\nQe 12\n"
"Qe 13\nQmvva 240,20,144\nQe 14\nQmvva 240,48,148\nQmvva 244,20,152\nQmvva 244,48,156\nQk 54,12,13\nQmvva 248,20,160\n"
"Qmvva 248,48,164\nQmvva 252,20,168\nQe 15\nQmvva 252,48,172\nQk 55,14,15\ns_nop 1\nv_smfmac_f32_16x16x64_bf16 a[200:203], a[204:207], v[48:55], v77\nQmava 224,24,144\n"
"v_accvgpr_read_b32 v0, a176\nv_accvgpr_read_b32 v1, a177\nQmava 224,52,148\nv_accvgpr_read_b32 v6, a178\nv_accvgpr_read_b32 v7, a179\nQmava 228,24,152\nv_accvgpr_read_b32 v8, a180\nv_accvgpr_read_b32 v9, a181\n"
"Qmava 228,52,156\nv_accvgpr_read_b32 v14, a182\nv_accvgpr_read_b32 v15, a183\nQmava 232,24,160\nv_accvgpr_read_b32 v16, a184\nv_accvgpr_read_b32 v17, a185\nQmava 232,52,164\nv_accvgpr_read_b32 v22, a186\n"
"v_accvgpr_read_b32 v23, a187\nQmava 236,24,168\nv_accvgpr_read_b32 v2, a190\nv_accvgpr_read_b32 v3, a191\nQmava 236,52,172\ns_waitcnt lgkmcnt(0)\nv_accvgpr_read_b32 v24, a188\nQmvva 96,28,112\n"
"v_accvgpr_read_b32 v25, a189\nv_accvgpr_read_b32 v4, a192\nQmvva 100,28,116\nv_accvgpr_read_b32 v5, a193\nv_accvgpr_read_b32 v10, a194\nQmvva 104,28,120\nv_accvgpr_read_b32 v11, a195\nv_accvgpr_read_b32 v12, a196\n"
"Qmvva 108,28,124\nv_accvgpr_read_b32 v13, a197\nv_accvgpr_read_b32 v18, a198\nQmvva 128,32,112\nv_accvgpr_read_b32 v19, a199\nv_accvgpr_read_b32 v20, a200\nQmvva 132,32,116\nv_accvgpr_read_b32 v21, a201\n"
"v_accvgpr_read_b32 v26, a202\nQmvva 136,32,120\nv_accvgpr_read_b32 v27, a203\nQa 8,8,9\nQmvva 140,32,124\nQa 10,10,11\nQa 8,8,10\nQmvva 160,36,112\n"
"Qa 16,16,17\nQa 18,18,19\nQmvva 164,36,116\nQa 16,16,18\nQa 4,4,5\nQmvva 168,36,120\nQa 6,6,7\nQa 4,4,6\n"
"Qmvva 172,36,124\nQa 12,12,13\nQa 14,14,15\nQmvva 192,40,112\nQa 12,12,14\nQa 0,0,1\nQmvva 196,40,116\nQa 2,2,3\n"
"Qa 0,0,2\nQmvva 200,40,120\nQa 24,24,25\nQa 26,26,27\nQmvva 204,40,124\nQa 24,24,26\nQa 20,20,21\nQmvva 224,44,112\n"
"Qa 22,22,23\nQa 20,20,22\nQmvva 228,44,116\nv_max3_u32 v9, v8, v16, v4\nv_max3_u32 v9, v9, v12, v0\nQmvva 232,44,120\nv_max3_u32 v9, v9, v24, v20\nv_min3_u32 v11, v8, v16, v4\n"
"Qmvva 236,44,124\nv_min3_u32 v11, v11, v12, v0\nv_min3_u32 v11, v11, v24, v20\nQmava 208,48,112\nQmava 212,48,116\nQmava 216,48,120\nQmava 220,48,124\nQmava 240,52,112\n"
"Qmava 244,52,116\nQmava 248,52,120\nQmava 252,52,124\nv_cmp_lt_u32 vcc, 0x44800000, v9\ns_mov_b64 s[74:75], vcc\nv_cmp_gt_u32 vcc, 0x2b800000, v11\ns_or_b64 s[74:75], s[74:75], vcc\ns_cmp_lg_u64 s[74:75], 0\n"
"s_cbranch_scc1 .Lfb\n.Lfbret1:\ns_mov_b32 s56, %8\ns_and_b32 s57, %9, 0xffff\ns_mov_b32 s58, 1835008\ns_lshl_b32 s68, %19, 4\ns_cmp_eq_u32 s70, 0\ns_cselect_b32 s68, s68, -1\n"
"v_mov_b32 v77, 0x7ffff000\nv_cmp_eq_u32 vcc, s68, v60\ns_nop 1\nv_cndmask_b32 v77, v77, v76, vcc\ns_lshl_b32 s68, s93, 12\ns_add_u32 s68, s68, %10\nv_lshrrev_b32 v66, 9, v60\nv_lshlrev_b32 v66, 11, v66\n"
"v_bfe_u32 v67, v60, 1, 8\nv_add3_u32 v68, v66, v67, s68\nv_add_u32 v69, 65536, v68\nv_add_u32 v70, 131072, v68\ns_mov_b64 exec, 0\ns_cmp_eq_u32 s70, 0\ns_cbranch_scc1 .Lpg16\ns_cmp_eq_u32 s70, 1\n"
"s_cbranch_scc1 .Lpg17\ns_cmp_eq_u32 s70, 2\ns_cbranch_scc1 .Lpg18\ns_branch .Lpg19\n.Ltr8:\ns_mov_b64 exec, -1\nv_rcp_f32 v2, v0\nv_rcp_f32 v6, v4\n"
"v_rcp_f32 v10, v8\nv_rcp_f32 v14, v12\nv_rcp_f32 v18, v16\nv_rcp_f32 v22, v20\nv_rcp_f32 v26, v24\nv_and_b32 v66, 16, v60\nv_mov_b32 v67, 0xffff\nv_lshlrev_b32 v66, v66, v67\n"
"s_mov_b32 vcc_lo, 0x00c0000c\ns_mov_b32 vcc_hi, 0xc0000c00\nv_cndmask_b32 v67, 0, v66, vcc\ns_lshr_b64 vcc, vcc, 2\nv_cndmask_b32 v66, 0, v66, vcc\nv_mov_b64 v[0:1], 0\nQk 2,2,2\nv_and_b32 v3, v2, v67\n"
"v_and_b32 v2, v2, v66\nv_mov_b64 v[4:5], 0\nQk 6,6,6\nv_and_b32 v7, v6, v67\nv_and_b32 v6, v6, v66\nv_mov_b64 v[8:9], 0\nQk 10,10,10\nv_and_b32 v11, v10, v67\n"
"v_and_b32 v10, v10, v66\nv_mov_b64 v[12:13], 0\nQk 14,14,14\nv_and_b32 v15, v14, v67\nv_and_b32 v14, v14, v66\nv_mov_b64 v[16:17], 0\nQk 18,18,18\nv_and_b32 v19, v18, v67\n"
"v_and_b32 v18, v18, v66\nv_mov_b64 v[20:21], 0\nQk 22,22,22\nv_and_b32 v23, v22, v67\nv_and_b32 v22, v22, v66\nv_mov_b64 v[24:25], 0\nQk 26,26,26\nv_and_b32 v27, v26, v67\n"
"v_and_b32 v26, v26, v66\nv_mov_b64 v[28:29], 0\nQk 80,80,81\nQk 81,82,83\nQk 82,84,85\nQk 83,86,87\ns_nop 1\nQzvvv 84,80,0\n"
"Qzvvv 80,80,2\nQk 112,112,113\nQk 113,114,115\nQk 114,116,117\nQk 115,118,119\ns_nop 1\nQzvvv 116,112,4\nQzvvv 112,112,6\n"
"Qk 144,144,145\nQk 145,146,147\nQk 146,148,149\nQk 147,150,151\nQk 80,80,81\nQk 81,82,83\nQzvvv 148,144,8\nQk 82,84,85\n"
"Qk 83,86,87\nQzvvv 144,144,10\nQk 176,176,177\nQk 177,178,179\nQk 178,180,181\nQk 179,182,183\nQk 112,112,113\nQk 113,114,115\n"
"Qzvvv 180,176,12\nQk 114,116,117\nQk 115,118,119\nQzvvv 176,176,14\nQk 208,208,209\nQk 209,210,211\nQk 210,212,213\nQk 211,214,215\n"
"Qk 144,144,145\nQk 145,146,147\nQzvvv 212,208,16\nQk 146,148,149\nQk 147,150,151\nQzvvv 208,208,18\nQk 240,240,241\nQk 241,242,243\n"
"Qk 242,244,245\nQk 243,246,247\nQk 176,176,177\nQk 177,178,179\nQzvvv 244,240,20\nQk 178,180,181\nQk 179,182,183\nQzvvv 240,240,22\n"
"v_accvgpr_read_b32 v84, a224\nv_accvgpr_read_b32 v85, a225\nv_accvgpr_read_b32 v86, a226\nv_accvgpr_read_b32 v87, a227\nv_accvgpr_read_b32 v32, a228\nv_accvgpr_read_b32 v33, a229\nv_accvgpr_read_b32 v34, a230\nv_accvgpr_read_b32 v35, a231\n"
"Qk 84,84,85\nQk 85,86,87\nQk 86,32,33\nQk 87,34,35\nQk 208,208,209\nQk 209,210,211\nQzvvv 32,84,24\nQk 210,212,213\n"
"Qk 211,214,215\nQzvvv 84,84,26\nQk 88,88,89\nQk 89,90,91\nQk 90,92,93\nQk 91,94,95\nQk 240,240,241\nQk 241,242,243\n"
"Qzvvv 92,88,0\nQk 242,244,245\nQk 243,246,247\nQzvvv 88,88,2\nQk 120,120,121\nQk 121,122,123\nQk 122,124,125\nQk 123,126,127\n"
"Qk 84,84,85\nQk 85,86,87\nQzvvv 124,120,4\nQk 86,32,33\nQk 87,34,35\nQzvvv 120,120,6\nQk 152,152,153\nQk 153,154,155\n"
"Qk 154,156,157\nQk 155,158,159\nQk 88,88,89\nQk 89,90,91\nQzvvv 156,152,8\nQk 90,92,93\nQk 91,94,95\nQzvvv 152,152,10\n"
"s_waitcnt vmcnt(24)\ns_barrier\nQk 184,184,185\nQk 185,186,187\nQk 186,188,189\nQk 187,190,191\nQk 120,120,121\nQk 121,122,123\n"
"Qzvvv 188,184,12\nQk 122,124,125\nQk 123,126,127\nQzvvv 184,184,14\nQta 56,68,0\nQk 216,216,217\nQk 217,218,219\nQk 218,220,221\n"
"Qk 219,222,223\nQk 152,152,153\nQk 153,154,155\nQzvvv 220,216,16\nQk 154,156,157\nQk 155,158,159\nQzvvv 216,216,18\nQta 58,68,1024\n"
"Qk 248,248,249\nQk 249,250,251\nQk 250,252,253\nQk 251,254,255\nQk 184,184,185\nQk 185,186,187\nQzvvv 252,248,20\nQk 186,188,189\n"
"Qk 187,190,191\nQzvvv 248,248,22\nQta 60,68,8192\nv_accvgpr_read_b32 v116, a232\nv_accvgpr_read_b32 v117, a233\nv_accvgpr_read_b32 v118, a234\nv_accvgpr_read_b32 v119, a235\nv_accvgpr_read_b32 v36, a236\n"
"v_accvgpr_read_b32 v37, a237\nv_accvgpr_read_b32 v38, a238\nv_accvgpr_read_b32 v39, a239\nQk 116,116,117\nQk 117,118,119\nQk 118,36,37\nQk 119,38,39\nQk 216,216,217\n"
"Qk 217,218,219\nQzvvv 36,116,24\nQk 218,220,221\nQk 219,222,223\nQzvvv 116,116,26\nQta 62,68,9216\ns_mov_b32 s31, 0\nQk 96,96,97\n"
"Qk 97,98,99\nQk 98,100,101\nQk 99,102,103\nQk 248,248,249\nQk 249,250,251\nQzvvv 100,96,0\nQk 250,252,253\nQk 251,254,255\n"
"Qzvvv 96,96,2\nbuffer_load_dwordx4 a[0:3], v60, s[48:51], s31 offen offset:0\nQta 64,68,256\nQk 128,128,129\nQk 129,130,131\nQk 130,132,133\nQk 131,134,135\nQk 116,116,117\n"
"Qk 117,118,119\nQzvvv 132,128,4\nQk 118,36,37\nQk 119,38,39\nQzvvv 128,128,6\nbuffer_load_dwordx4 a[4:7], v60, s[48:51], s31 offen offset:1024\nQta 66,68,1280\nQk 160,160,161\n"
"Qk 161,162,163\nQk 162,164,165\nQk 163,166,167\nQk 96,96,97\nQk 97,98,99\nQzvvv 164,160,8\nQk 98,100,101\nQk 99,102,103\n"
"Qzvvv 160,160,10\nbuffer_load_dwordx4 a[8:11], v60, s[48:51], s31 offen offset:2048\nQta 68,68,8448\nQk 192,192,193\nQk 193,194,195\nQk 194,196,197\nQk 195,198,199\nQk 128,128,129\n"
"Qk 129,130,131\nQzvvv 196,192,12\nQk 130,132,133\nQk 131,134,135\nQzvvv 192,192,14\nbuffer_load_dwordx4 a[12:15], v60, s[48:51], s31 offen offset:3072\nQta 70,68,9472\ns_mov_b32 s31, 4096\n"
"Qk 224,224,225\nQk 225,226,227\nQk 226,228,229\nQk 227,230,231\nQk 160,160,161\nQk 161,162,163\nQzvvv 228,224,16\nQk 162,164,165\n"
"Qk 163,166,167\nQzvvv 224,224,18\nbuffer_load_dwordx4 a[16:19], v60, s[48:51], s31 offen offset:0\nv_accvgpr_read_b32 v148, a208\nv_accvgpr_read_b32 v149, a209\nv_accvgpr_read_b32 v150, a210\nv_accvgpr_read_b32 v151, a211\nv_accvgpr_read_b32 v36, a212\n"
"v_accvgpr_read_b32 v37, a213\nv_accvgpr_read_b32 v38, a214\nv_accvgpr_read_b32 v39, a215\nQk 148,148,149\nQk 149,150,151\nQk 150,36,37\nQk 151,38,39\nQk 192,192,193\n"
"Qk 193,194,195\nQzvvv 36,148,20\nQk 194,196,197\nQk 195,198,199\nQzvvv 148,148,22\nbuffer_load_dwordx4 a[20:23], v60, s[48:51], s31 offen offset:1024\nv_accvgpr_read_b32 v180, a240\nv_accvgpr_read_b32 v181, a241\n"
"v_accvgpr_read_b32 v182, a242\nv_accvgpr_read_b32 v183, a243\nv_accvgpr_read_b32 v40, a244\nv_accvgpr_read_b32 v41, a245\nv_accvgpr_read_b32 v42, a246\nv_accvgpr_read_b32 v43, a247\nQk 180,180,181\nQk 181,182,183\n"
"Qk 182,40,41\nQk 183,42,43\nQk 224,224,225\nQk 225,226,227\nQzvvv 40,180,24\nQk 226,228,229\nQk 227,230,231\nQzvvv 180,180,26\n"
"buffer_load_dwordx4 a[24:27], v60, s[48:51], s31 offen offset:2048\ns_mov_b32 s31, 14336\nQk 104,104,105\nQk 105,106,107\nQk 106,108,109\nQk 107,110,111\nQk 148,148,149\nQk 149,150,151\n"
"Qzvvv 108,104,0\nQk 150,36,37\nQk 151,38,39\nQzvvv 104,104,2\nbuffer_load_dwordx4 a[28:31], v60, s[48:51], s31 offen offset:0\nQk 136,136,137\nQk 137,138,139\nQk 138,140,141\n"
"Qk 139,142,143\nQk 180,180,181\nQk 181,182,183\nQzvvv 140,136,4\nQk 182,40,41\nQk 183,42,43\nQzvvv 136,136,6\nbuffer_load_dwordx4 a[32:35], v60, s[48:51], s31 offen offset:1024\n"
"Qk 168,168,169\n.Lpg13:\ns_cbranch_execz .Ltr6\nQk 169,170,171\nQk 170,172,173\nQk 171,174,175\nQk 104,104,105\nQk 105,106,107\n"
"Qzvvv 172,168,8\nQk 106,108,109\nQk 107,110,111\nQzvvv 168,168,10\nbuffer_load_dwordx4 a[36:39], v60, s[48:51], s31 offen offset:2048\nQk 200,200,201\nQk 201,202,203\nQk 202,204,205\n"
"Qk 203,206,207\nQk 136,136,137\nQk 137,138,139\nQzvvv 204,200,12\nQk 138,140,141\nQk 139,142,143\nQzvvv 200,200,14\nbuffer_load_dwordx4 a[40:43], v60, s[48:51], s31 offen offset:3072\n"
"s_mov_b32 s31, 18432\nQk 232,232,233\nQk 233,234,235\nQk 234,236,237\nQk 235,238,239\nQk 168,168,169\nQk 169,170,171\nQzvvv 236,232,16\n"
"Qk 170,172,173\nQk 171,174,175\nQzvvv 232,232,18\nbuffer_load_dwordx4 a[44:47], v60, s[48:51], s31 offen offset:0\nv_accvgpr_read_b32 v212, a216\nv_accvgpr_read_b32 v213, a217\nv_accvgpr_read_b32 v214, a218\nv_accvgpr_read_b32 v215, a219\n"
"v_accvgpr_read_b32 v40, a220\nv_accvgpr_read_b32 v41, a221\nv_accvgpr_read_b32 v42, a222\nv_accvgpr_read_b32 v43, a223\nQk 212,212,213\nQk 213,214,215\nQk 214,40,41\nQk 215,42,43\n"
"Qk 200,200,201\nQk 201,202,203\nQzvvv 40,212,20\nQk 202,204,205\nQk 203,206,207\nQzvvv 212,212,22\nbuffer_load_dwordx4 a[48:51], v60, s[48:51], s31 offen offset:1024\nv_accvgpr_read_b32 v244, a248\n"
"v_accvgpr_read_b32 v245, a249\nv_accvgpr_read_b32 v246, a250\nv_accvgpr_read_b32 v247, a251\nv_accvgpr_read_b32 v32, a252\nv_accvgpr_read_b32 v33, a253\nv_accvgpr_read_b32 v34, a254\nv_accvgpr_read_b32 v35, a255\nQk 244,244,245\n"
"Qk 245,246,247\nQk 246,32,33\nQk 247,34,35\nQk 232,232,233\nQk 233,234,235\nQzvvv 32,244,24\nQk 234,236,237\nQk 235,238,239\n"
"Qzvvv 244,244,26\nbuffer_load_dwordx4 a[52:55], v60, s[48:51], s31 offen offset:2048\nQk 212,212,213\nQk 213,214,215\nQk 214,40,41\nQk 215,42,43\ns_nop 2\nQk 244,244,245\n"
"Qk 245,246,247\nQk 246,32,33\nQk 247,34,35\ns_waitcnt lgkmcnt(4)\nQzaav 116,56,80\ns_add_u32 m0, s96, 98304\ns_mov_b32 s95, 86016\nQmaav 116,60,88\n"
"buffer_load_dwordx4 v61, s[52:55], s95 offen lds\nQzaav 120,56,112\nQmaav 120,60,120\nQzaav 124,56,144\nQta 72,68,512\ns_mov_b32 s95, 105472\nQmaav 124,60,152\nbuffer_load_dwordx4 v61, s[52:55], s95 offen offset:1024 lds\n"
"Qzaav 128,56,176\nQmaav 128,60,184\nQta 74,68,1536\nQzaav 132,56,208\nQmaav 132,60,216\nQzaav 136,56,240\nQta 76,68,8704\nQmaav 136,60,248\n"
"s_add_u32 s88, s71, 0\nQzaav 140,56,84\nbuffer_store_dwordx4 v[80:83], v60, s[56:59], s88 offen offset:0 sc1\nQmaav 140,60,116\nQta 78,68,9728\ns_waitcnt lgkmcnt(4)\nQzaav 144,64,80\nQmaav 144,68,88\n"
"Qzaav 148,64,112\nQmaav 148,68,120\nQzaav 152,64,144\nQta 56,68,768\nQmaav 152,68,152\nQzaav 156,64,176\nQmaav 156,68,184\nQta 58,68,1792\n"
"s_add_u32 m0, s96, 106496\ns_mov_b32 s95, 126976\nQzaav 160,64,208\nbuffer_load_dwordx4 v61, s[52:55], s95 offen lds\nQmaav 160,68,216\nQzaav 164,64,240\nQta 60,68,8960\nQmaav 164,68,248\n"
"s_mov_b32 s95, 146432\nQzaav 168,64,84\nbuffer_load_dwordx4 v61, s[52:55], s95 offen offset:1024 lds\nQmaav 168,68,116\nQta 62,68,9984\ns_waitcnt lgkmcnt(4)\nQzvav 0,72,80\nQmvav 0,76,88\n"
"Qzvav 4,72,112\nQmvav 4,76,120\nQzvav 8,72,144\nQta 64,68,16384\nQmvav 8,76,152\nbuffer_store_dwordx4 v[112:115], v60, s[56:59], s88 offen offset:1024 sc1\nQzvav 12,72,176\nQmvav 12,76,184\n"
"Qta 66,68,16896\nQzvav 16,72,208\nQmvav 16,76,216\nQzvav 20,72,240\nQta 68,68,17408\nv_add_u32 v75, 1, v75\nQmvav 20,76,248\ns_waitcnt vmcnt(34)\n"
"ds_write_b32 v73, v75\nQzvav 24,72,84\nQmvav 24,76,116\nQta 70,68,17920\ns_waitcnt lgkmcnt(5)\nQzvav 28,56,80\ns_add_u32 m0, s96, 114688\ns_mov_b32 s95, 86016\n"
"Qmvav 28,60,88\nbuffer_load_dwordx4 v62, s[52:55], s95 offen lds\nQzvav 32,56,112\nQmvav 32,60,120\nQzvav 36,56,144\nQta 72,68,16640\nQmvav 36,60,152\nds_read_b32 v66, v74\n"
"s_mov_b32 s95, 125952\nQzvav 40,56,176\nbuffer_load_dwordx4 v62, s[52:55], s95 offen offset:1024 lds\nQmvav 40,60,184\nQta 74,68,17152\nQzvav 44,56,208\nQmvav 44,60,216\ns_waitcnt lgkmcnt(1)\n"
"v_cmp_gt_u32 vcc, v75, v66\nQzvav 48,56,240\ns_cbranch_vccnz .Lso40\n.Lro40:\nQmvav 48,60,248\nQta 76,68,17664\nQzvav 52,56,84\nbuffer_store_dwordx4 v[144:147], v60, s[56:59], s88 offen offset:2048 sc1\n"
"Qmvav 52,60,116\ns_waitcnt lgkmcnt(4)\nQzaav 172,64,80\nQta 78,68,18176\nQmaav 172,68,88\nQzaav 176,64,112\nQmaav 176,68,120\nQzaav 180,64,144\n"
"Qta 56,68,24576\nQmaav 180,68,152\nQzaav 184,64,176\nQmaav 184,68,184\nQta 58,68,25600\ns_add_u32 m0, s96, 122880\ns_mov_b32 s95, 86016\nQzaav 188,64,208\n"
"buffer_load_dwordx4 v63, s[52:55], s95 offen lds\nQmaav 188,68,216\nQzaav 192,64,240\nQta 60,68,32768\nQmaav 192,68,248\ns_mov_b32 s95, 105472\nQzaav 196,64,84\nbuffer_load_dwordx4 v63, s[52:55], s95 offen offset:1024 lds\n"
"Qmaav 196,68,116\nQta 62,68,33792\ns_waitcnt lgkmcnt(4)\nQzvav 92,72,80\nQmvav 92,76,88\nQzvav 124,72,112\nbuffer_store_dwordx4 v[176:179], v60, s[56:59], s88 offen offset:3072 sc1\nQmvav 124,76,120\n"
"Qzvav 156,72,144\nQta 64,68,24832\nQmvav 156,76,152\nQzvav 188,72,176\nQmvav 188,76,184\nQta 66,68,25856\nQzvav 220,72,208\nQmvav 220,76,216\n"
"Qzvav 252,72,240\nQta 68,68,33024\nQmvav 252,76,248\ns_add_u32 m0, s96, 0\ns_mov_b32 s95, 126976\nQzvav 100,72,84\nbuffer_load_dwordx4 v63, s[52:55], s95 offen lds\nQmvav 100,76,116\n"
"Qta 70,68,34048\ns_waitcnt lgkmcnt(4)\nQzaav 200,56,80\nQmaav 200,60,88\ns_mov_b32 s95, 146432\nQzaav 204,56,112\nbuffer_load_dwordx4 v63, s[52:55], s95 offen offset:1024 lds\nQmaav 204,60,120\n"
"Qzaav 208,56,144\nQta 72,68,25088\nQmaav 208,60,152\nQzaav 212,56,176\nQmaav 212,60,184\nQta 74,68,26112\ns_add_u32 s88, s71, 4096\nQzaav 216,56,208\n"
"buffer_store_dwordx4 v[208:211], v60, s[56:59], s88 offen offset:0 sc1\nQmaav 216,60,216\nQzaav 220,56,240\nQta 76,68,33280\nQmaav 220,60,248\nQzaav 224,56,84\nQmaav 224,60,116\nQta 78,68,34304\n"
"s_waitcnt lgkmcnt(4)\nQzaav 228,64,80\nQmaav 228,68,88\nQzaav 232,64,112\nQmaav 232,68,120\nQzaav 236,64,144\nQta 56,68,25344\ns_add_u32 m0, s96, 8192\n"
"s_mov_b32 s95, 331776\nQmaav 236,68,152\nbuffer_load_dwordx4 v61, s[52:55], s95 offen lds\nQzaav 240,64,176\nQmaav 240,68,184\nQta 58,68,26368\nQzaav 244,64,208\ns_mov_b32 s95, 351232\n"
"Qmaav 244,68,216\nbuffer_load_dwordx4 v61, s[52:55], s95 offen offset:1024 lds\nQzaav 248,64,240\nQta 60,68,33536\nQmaav 248,68,248\nQzaav 252,64,84\nQmaav 252,68,116\nQta 62,68,34560\n"
"s_waitcnt lgkmcnt(4)\nQzvav 132,72,80\nbuffer_store_dwordx4 v[240:243], v60, s[56:59], s88 offen offset:1024 sc1\nQmvav 132,76,88\nQzvav 164,72,112\nQmvav 164,76,120\nQzvav 196,72,144\nQta 64,68,40960\n"
"Qmvav 196,76,152\nQzvav 228,72,176\nQmvav 228,76,184\nQta 66,68,41984\nQzvav 108,72,208\nQmvav 108,76,216\nQzvav 140,72,240\nQta 68,68,49152\n"
"Qmvav 140,76,248\nQzvav 172,72,84\nQmvav 172,76,116\nQta 70,68,50176\ns_waitcnt lgkmcnt(4)\nQzvav 204,56,80\nQmvav 204,60,88\nQzvav 236,56,112\n"
"Qmvav 236,60,120\nQzaav 96,56,144\nQta 72,68,41216\nQmaav 96,60,152\nQzaav 100,56,176\nbuffer_store_dwordx4 v[84:87], v60, s[56:59], s88 offen offset:2048 sc1\nQmaav 100,60,184\nQta 74,68,42240\n"
"Qzaav 104,56,208\nQmaav 104,60,216\nQzaav 108,56,240\nQta 76,68,49408\nQmaav 108,60,248\nQzaav 112,56,84\nQmaav 112,60,116\nQta 78,68,50432\n"
"s_waitcnt vmcnt(19)\ns_waitcnt lgkmcnt(4)\nQmaaa 116,64,0\nQmaaa 116,68,28\nQmaaa 120,64,4\nQmaaa 120,68,32\nQmaaa 124,64,8\nQta 56,68,41472\n"
"Qmaaa 124,68,36\nQmaaa 128,64,12\nQmaaa 128,68,40\nQta 58,68,42496\nQmaaa 132,64,16\nQmaaa 132,68,44\nQmaaa 136,64,20\nQta 60,68,49664\n"
"s_and_b32 s68, %19, 3\nQmaaa 136,68,48\ns_add_u32 s88, s71, 14336\nQmaaa 140,64,24\nbuffer_store_dwordx4 v[88:91], v60, s[56:59], s88 offen offset:0 sc1\nQmaaa 140,68,52\nQta 62,68,50688\ns_waitcnt lgkmcnt(4)\n"
"Qmaaa 144,72,0\ns_cmp_lt_u32 s68, 1\ns_cselect_b32 s68, 40, 32\nQmaaa 144,76,28\ns_mul_i32 s67, s68, 40960\ns_mul_i32 s74, s68, 14336\nQmaaa 148,72,4\ns_and_b32 s68, %19, 3\n"
"Qmaaa 148,76,32\ns_cmp_lt_u32 s68, 2\ns_cselect_b32 s68, 48, 40\nQmaaa 152,72,8\nQta 64,68,41728\ns_mul_i32 s72, s68, 40960\nQmaaa 152,76,36\ns_mul_i32 s75, s68, 14336\n"
"s_and_b32 s68, %19, 3\nQmaaa 156,72,12\ns_cmp_lt_u32 s68, 3\ns_cselect_b32 s68, 56, 48\nQmaaa 156,76,40\nQta 66,68,42752\ns_mul_i32 s73, s68, 40960\nQmaaa 160,72,16\n"
"s_mul_i32 s77, s68, 14336\nv_add_u32 v58, s91, v60\nQmaaa 160,76,44\ns_mul_i32 s68, s93, 28672\ns_sub_u32 s68, 14336, s68\nQmaaa 164,72,20\nQta 68,68,49920\nv_add_u32 v59, s68, v58\n"
"Qmaaa 164,76,48\nQmaaa 168,72,24\nQmaaa 168,76,52\nQta 70,68,50944\ns_waitcnt lgkmcnt(4)\nQmvaa 0,56,0\nQmvaa 0,60,28\nQmvaa 4,56,4\n"
"Qmvaa 4,60,32\nQmvaa 8,56,8\nQta 72,68,57344\nQmvaa 8,60,36\nbuffer_store_dwordx4 v[120:123], v60, s[56:59], s88 offen offset:1024 sc1\nQmvaa 12,56,12\nQmvaa 12,60,40\nQta 74,68,57856\n"
"Qmvaa 16,56,16\nQmvaa 16,60,44\nQmvaa 20,56,20\nQta 76,68,58368\nv_add_u32 v75, 1, v75\nQmvaa 20,60,48\ns_waitcnt vmcnt(39)\nds_write_b32 v73, v75\n"
"Qmvaa 24,56,24\nQmvaa 24,60,52\nQta 78,68,58880\ns_waitcnt lgkmcnt(5)\nQmvaa 28,64,0\nQmvaa 28,68,28\nQmvaa 32,64,4\nQmvaa 32,68,32\n"
"Qmvaa 36,64,8\nQta 56,68,57600\nQmvaa 36,68,36\nds_read_b32 v66, v74\nQmvaa 40,64,12\nQmvaa 40,68,40\nQta 58,68,58112\nQmvaa 44,64,16\n"
"Qmvaa 44,68,44\ns_waitcnt lgkmcnt(1)\nv_cmp_gt_u32 vcc, v75, v66\nQmvaa 48,64,20\ns_cbranch_vccnz .Lso180\n.Lro180:\nQmvaa 48,68,48\nQta 60,68,58624\n"
"Qmvaa 52,64,24\nbuffer_store_dwordx4 v[152:155], v60, s[56:59], s88 offen offset:2048 sc1\nQmvaa 52,68,52\ns_waitcnt lgkmcnt(4)\nQmaaa 172,72,0\nQta 62,68,59136\nQmaaa 172,76,28\nQmaaa 176,72,4\n"
"Qmaaa 176,76,32\nQmaaa 180,72,8\nQta 64,70,1024\nQmaaa 180,76,36\nQmaaa 184,72,12\nQmaaa 184,76,40\nQta 66,70,2048\ns_add_u32 m0, s96, 16384\n"
"s_mov_b32 s95, 372736\nQmaaa 188,72,16\nbuffer_load_dwordx4 v61, s[52:55], s95 offen lds\nQmaaa 188,76,44\nQmaaa 192,72,20\nQta 68,70,9216\nQmaaa 192,76,48\ns_mov_b32 s95, 392192\n"
"Qmaaa 196,72,24\nbuffer_load_dwordx4 v61, s[52:55], s95 offen offset:1024 lds\nQmaaa 196,76,52\nQta 70,70,10240\ns_waitcnt lgkmcnt(4)\nQmvaa 92,56,0\nQmvaa 92,60,28\nQmvaa 124,56,4\n"
"buffer_store_dwordx4 v[184:187], v60, s[56:59], s88 offen offset:3072 sc1\nQmvaa 124,60,32\nQmvaa 156,56,8\nQta 72,70,1280\nQmvaa 156,60,36\nQmvaa 188,56,12\nQmvaa 188,60,40\nQta 74,70,2304\n"
"Qmvaa 220,56,16\nQmvaa 220,60,44\nQmvaa 252,56,20\nQta 76,70,9472\nQmvaa 252,60,48\ns_add_u32 m0, s96, 24576\ns_mov_b32 s95, 331776\nQmvaa 100,56,24\n"
"buffer_load_dwordx4 v62, s[52:55], s95 offen lds\nQmvaa 100,60,52\nQta 78,70,10496\ns_waitcnt lgkmcnt(4)\nQmaaa 200,64,0\nQmaaa 200,68,28\ns_mov_b32 s95, 371712\nQmaaa 204,64,4\n"
"buffer_load_dwordx4 v62, s[52:55], s95 offen offset:1024 lds\nQmaaa 204,68,32\nQmaaa 208,64,8\nQta 56,70,1536\nQmaaa 208,68,36\nQmaaa 212,64,12\nQmaaa 212,68,40\nQta 58,70,2560\n"
"s_add_u32 s88, s71, 18432\nQmaaa 216,64,16\nbuffer_store_dwordx4 v[216:219], v60, s[56:59], s88 offen offset:0 sc1\nQmaaa 216,68,44\nQmaaa 220,64,20\nQta 60,70,9728\nQmaaa 220,68,48\nQmaaa 224,64,24\n"
"Qmaaa 224,68,52\nQta 62,70,10752\ns_waitcnt lgkmcnt(4)\nQmaaa 228,72,0\nQmaaa 228,76,28\nQmaaa 232,72,4\nQmaaa 232,76,32\nQmaaa 236,72,8\n"
"Qta 64,70,1792\ns_add_u32 m0, s96, 32768\ns_mov_b32 s95, 331776\nQmaaa 236,76,36\nbuffer_load_dwordx4 v63, s[52:55], s95 offen lds\nQmaaa 240,72,12\nQmaaa 240,76,40\nQta 66,70,2816\n"
"Qmaaa 244,72,16\ns_mov_b32 s95, 351232\nQmaaa 244,76,44\nbuffer_load_dwordx4 v63, s[52:55], s95 offen offset:1024 lds\nQmaaa 248,72,20\nQta 68,70,9984\nQmaaa 248,76,48\nQmaaa 252,72,24\n"
"Qmaaa 252,76,52\nQta 70,70,11008\ns_waitcnt lgkmcnt(4)\nQmvaa 132,56,0\nbuffer_store_dwordx4 v[248:251], v60, s[56:59], s88 offen offset:1024 sc1\nQmvaa 132,60,28\nQmvaa 164,56,4\nQmvaa 164,60,32\n"
"Qmvaa 196,56,8\nQta 72,70,17408\nQmvaa 196,60,36\nQmvaa 228,56,12\nQmvaa 228,60,40\nQta 74,70,18432\nQmvaa 108,56,16\nQmvaa 108,60,44\n"
"Qmvaa 140,56,20\nQta 76,69,0\n.Lpg14:\ns_cbranch_execz .Ltr6\ns_add_u32 m0, s96, 40960\ns_mov_b32 s95, 372736\nQmvaa 140,60,48\nbuffer_load_dwordx4 v63, s[52:55], s95 offen lds\n"
"Qmvaa 172,56,24\nQmvaa 172,60,52\nQta 78,69,1024\ns_waitcnt lgkmcnt(4)\nQmvaa 204,64,0\ns_mov_b32 s95, 392192\nQmvaa 204,68,28\nbuffer_load_dwordx4 v63, s[52:55], s95 offen offset:1024 lds\n"
"Qmvaa 236,64,4\nQmvaa 236,68,32\nQmaaa 96,64,8\nQta 56,70,17664\nQmaaa 96,68,36\nQmaaa 100,64,12\nbuffer_store_dwordx4 v[116:119], v60, s[56:59], s88 offen offset:2048 sc1\nQmaaa 100,68,40\n"
"Qta 58,70,18688\nQmaaa 104,64,16\nQmaaa 104,68,44\nQmaaa 108,64,20\nQta 60,69,256\nQmaaa 108,68,48\nQmaaa 112,64,24\nQmaaa 112,68,52\n"
"Qta 62,69,1280\ns_waitcnt lgkmcnt(4)\nQmaav 116,72,96\nQmaav 116,76,104\ns_mov_b32 s31, 28672\nQmaav 120,72,128\nbuffer_load_dwordx4 a[0:3], v60, s[48:51], s31 offen offset:0\nQmaav 120,76,136\n"
"Qmaav 124,72,160\nQta 64,70,17920\nQmaav 124,76,168\nQmaav 128,72,192\nbuffer_load_dwordx4 a[4:7], v60, s[48:51], s31 offen offset:1024\nQmaav 128,76,200\nQta 66,70,18944\nQmaav 132,72,224\n"
"Qmaav 132,76,232\nQmaav 136,72,148\nQta 68,69,512\nQmaav 136,76,212\nbuffer_load_dwordx4 a[8:11], v60, s[48:51], s31 offen offset:2048\nQmaav 140,72,180\nQmaav 140,76,244\nQta 70,69,1536\n"
"s_waitcnt lgkmcnt(4)\nQmaav 144,56,96\nQmaav 144,60,104\nbuffer_load_dwordx4 a[12:15], v60, s[48:51], s31 offen offset:3072\nQmaav 148,56,128\nQmaav 148,60,136\nQmaav 152,56,160\nQta 72,70,18176\n"
"s_mov_b32 s31, 32768\nQmaav 152,60,168\nbuffer_load_dwordx4 a[16:19], v60, s[48:51], s31 offen offset:0\nQmaav 156,56,192\nQmaav 156,60,200\nQta 74,70,19200\nQmaav 160,56,224\nQmaav 160,60,232\n"
"buffer_load_dwordx4 a[20:23], v60, s[48:51], s31 offen offset:1024\nQmaav 164,56,148\nQta 76,69,768\nQmaav 164,60,212\nQmaav 168,56,180\nQmaav 168,60,244\nQta 78,69,1792\ns_waitcnt lgkmcnt(4)\n"
"Qmvav 0,64,96\nbuffer_load_dwordx4 a[24:27], v60, s[48:51], s31 offen offset:2048\nQmvav 0,68,104\nQmvav 4,64,128\nQmvav 4,68,136\nQmvav 8,64,160\nQta 56,69,8192\ns_mov_b32 s31, 43008\n"
"Qmvav 8,68,168\nbuffer_load_dwordx4 a[28:31], v60, s[48:51], s31 offen offset:0\nQmvav 12,64,192\nQmvav 12,68,200\nQta 58,69,8704\nQmvav 16,64,224\nQmvav 16,68,232\nbuffer_load_dwordx4 a[32:35], v60, s[48:51], s31 offen offset:1024\n"
"Qmvav 20,64,148\nQta 60,69,9216\nv_add_u32 v75, 1, v75\nQmvav 20,68,212\ns_waitcnt vmcnt(35)\nds_write_b32 v73, v75\nQmvav 24,64,180\nQmvav 24,68,244\n"
"Qta 62,69,9728\ns_waitcnt lgkmcnt(5)\nQmvav 28,72,96\nbuffer_load_dwordx4 a[36:39], v60, s[48:51], s31 offen offset:2048\nQmvav 28,76,104\nQmvav 32,72,128\nQmvav 32,76,136\nQmvav 36,72,160\n"
"Qta 64,69,8448\nQmvav 36,76,168\nds_read_b32 v66, v74\nQmvav 40,72,192\nbuffer_load_dwordx4 a[40:43], v60, s[48:51], s31 offen offset:3072\nQmvav 40,76,200\nQta 66,69,8960\nQmvav 44,72,224\n"
"Qmvav 44,76,232\ns_waitcnt lgkmcnt(1)\nv_cmp_gt_u32 vcc, v75, v66\nQmvav 48,72,148\ns_cbranch_vccnz .Lso320\n.Lro320:\nQmvav 48,76,212\nQta 68,69,9472\n"
"s_mov_b32 s31, 47104\nQmvav 52,72,180\nbuffer_load_dwordx4 a[44:47], v60, s[48:51], s31 offen offset:0\nQmvav 52,76,244\ns_waitcnt lgkmcnt(4)\nQmaav 172,56,96\nQta 70,69,9984\nQmaav 172,60,104\n"
"Qmaav 176,56,128\nbuffer_load_dwordx4 a[48:51], v60, s[48:51], s31 offen offset:1024\nQmaav 176,60,136\nQmaav 180,56,160\nQta 72,69,16384\nQmaav 180,60,168\nQmaav 184,56,192\nbuffer_load_dwordx4 a[52:55], v60, s[48:51], s31 offen offset:2048\n"
"Qmaav 184,60,200\nQta 74,69,17408\nQmaav 188,56,224\nQmaav 188,60,232\nQmaav 192,56,148\nQta 76,69,24576\ns_mov_b32 s31, 114688\nQmaav 192,60,212\n"
"buffer_load_dwordx4 v[80:83], v60, s[48:51], s31 offen offset:0\nQmaav 196,56,180\nQmaav 196,60,244\nQta 78,69,25600\ns_waitcnt lgkmcnt(4)\nQmvav 92,64,96\nQmvav 92,68,104\nbuffer_load_dwordx4 v[112:115], v60, s[48:51], s31 offen offset:1024\n"
"Qmvav 124,64,128\nQmvav 124,68,136\nQmvav 156,64,160\nQta 56,69,16640\nQmvav 156,68,168\nbuffer_load_dwordx4 v[144:147], v60, s[48:51], s31 offen offset:2048\nQmvav 188,64,192\nQmvav 188,68,200\n"
"Qta 58,69,17664\nQmvav 220,64,224\nQmvav 220,68,232\nbuffer_load_dwordx4 v[176:179], v60, s[48:51], s31 offen offset:3072\nQmvav 252,64,148\nQta 60,69,24832\nQmvav 252,68,212\nQmvav 100,64,180\n"
"Qmvav 100,68,244\nQta 62,69,25856\ns_mov_b32 s31, 118784\ns_waitcnt lgkmcnt(4)\nQmaav 200,72,96\nbuffer_load_dwordx4 v[208:211], v60, s[48:51], s31 offen offset:0\nQmaav 200,76,104\nQmaav 204,72,128\n"
"Qmaav 204,76,136\nQmaav 208,72,160\nQta 64,69,16896\nQmaav 208,76,168\nbuffer_load_dwordx4 v[240:243], v60, s[48:51], s31 offen offset:1024\nQmaav 212,72,192\nQmaav 212,76,200\nQta 66,69,17920\n"
"Qmaav 216,72,224\nQmaav 216,76,232\nbuffer_load_dwordx4 v[84:87], v60, s[48:51], s31 offen offset:2048\nQmaav 220,72,148\nQta 68,69,25088\nQmaav 220,76,212\nQmaav 224,72,180\nQmaav 224,76,244\n"
"Qta 70,69,26112\ns_mov_b32 s31, 129024\ns_waitcnt lgkmcnt(4)\nQmaav 228,56,96\nbuffer_load_dwordx4 v[88:91], v60, s[48:51], s31 offen offset:0\nQmaav 228,60,104\nQmaav 232,56,128\nQmaav 232,60,136\n"
"Qmaav 236,56,160\nQta 72,69,17152\nQmaav 236,60,168\nbuffer_load_dwordx4 v[120:123], v60, s[48:51], s31 offen offset:1024\nQmaav 240,56,192\nQmaav 240,60,200\nQta 74,69,18176\nQmaav 244,56,224\n"
"Qmaav 244,60,232\nbuffer_load_dwordx4 v[152:155], v60, s[48:51], s31 offen offset:2048\nQmaav 248,56,148\nQta 76,69,25344\nQmaav 248,60,212\nQmaav 252,56,180\nQmaav 252,60,244\nQta 78,69,26368\n"
"s_waitcnt lgkmcnt(4)\nQmvav 132,64,96\nbuffer_load_dwordx4 v[184:187], v60, s[48:51], s31 offen offset:3072\nQmvav 132,68,104\nQmvav 164,64,128\nQmvav 164,68,136\nQmvav 196,64,160\nQta 56,69,32768\n"
"s_mov_b32 s31, 133120\nQmvav 196,68,168\nbuffer_load_dwordx4 v[216:219], v60, s[48:51], s31 offen offset:0\nQmvav 228,64,192\nQmvav 228,68,200\nQta 58,69,33792\nQmvav 108,64,224\nQmvav 108,68,232\n"
"buffer_load_dwordx4 v[248:251], v60, s[48:51], s31 offen offset:1024\nQmvav 140,64,148\nQta 60,69,40960\nQmvav 140,68,212\nQmvav 172,64,180\nQmvav 172,68,244\nQta 62,69,41984\ns_waitcnt lgkmcnt(4)\n"
"Qmvav 204,72,96\nbuffer_load_dwordx4 v[116:119], v60, s[48:51], s31 offen offset:2048\nQmvav 204,76,104\nQmvav 236,72,128\nQmvav 236,76,136\nQmaav 96,72,160\nQta 64,69,33024\ns_add_u32 m0, s96, 49152\n"
"s_mov_b32 s95, 413696\nQmaav 96,76,168\nbuffer_load_dwordx4 v61, s[52:55], s95 offen lds\nQmaav 100,72,192\nQmaav 100,76,200\nQta 66,69,34048\nQmaav 104,72,224\ns_mov_b32 s95, 433152\n"
"Qmaav 104,76,232\nbuffer_load_dwordx4 v61, s[52:55], s95 offen offset:1024 lds\nQmaav 108,72,148\nQta 68,69,41216\nQmaav 108,76,212\nQmaav 112,72,180\nQmaav 112,76,244\nQta 70,69,42240\n"
"s_add_u32 m0, s96, 57344\ns_mov_b32 s95, 454656\ns_waitcnt vmcnt(16)\ns_waitcnt lgkmcnt(4)\nQmaaa 116,56,0\nbuffer_load_dwordx4 v61, s[52:55], s95 offen lds\nQmaaa 116,60,28\nQmaaa 120,56,4\n"
"Qmaaa 120,60,32\nQmaaa 124,56,8\nQta 72,69,33280\ns_mov_b32 s95, 474112\nQmaaa 124,60,36\nbuffer_load_dwordx4 v61, s[52:55], s95 offen offset:1024 lds\nQmaaa 128,56,12\nQmaaa 128,60,40\n"
"Qta 74,69,34304\nQmaaa 132,56,16\nQmaaa 132,60,44\nQmaaa 136,56,20\nQta 76,69,41472\ns_add_u32 s88, s71, 28672\nQmaaa 136,60,48\nbuffer_store_dwordx4 v[96:99], v60, s[56:59], s88 offen offset:0 sc1\n"
"Qmaaa 140,56,24\nQmaaa 140,60,52\nQta 78,69,42496\ns_waitcnt lgkmcnt(4)\nQmaaa 144,64,0\nQmaaa 144,68,28\nQmaaa 148,64,4\nQmaaa 148,68,32\n"
"Qmaaa 152,64,8\nQta 56,69,33536\nQmaaa 152,68,36\nQmaaa 156,64,12\nQmaaa 156,68,40\nQta 58,69,34560\ns_add_u32 m0, s96, 132096\ns_mov_b32 s95, 413696\n"
"Qmaaa 160,64,16\nbuffer_load_dwordx4 v62, s[52:55], s95 offen lds\nQmaaa 160,68,44\nQmaaa 164,64,20\nQta 60,69,41728\nQmaaa 164,68,48\ns_mov_b32 s95, 453632\nQmaaa 168,64,24\n"
"buffer_load_dwordx4 v62, s[52:55], s95 offen offset:1024 lds\nQmaaa 168,68,52\nQta 62,69,42752\ns_waitcnt lgkmcnt(4)\nQmvaa 0,72,0\nQmvaa 0,76,28\nQmvaa 4,72,4\nbuffer_store_dwordx4 v[128:131], v60, s[56:59], s88 offen offset:1024 sc1\n"
"Qmvaa 4,76,32\nQmvaa 8,72,8\nQta 64,69,49152\nQmvaa 8,76,36\nQmvaa 12,72,12\nQmvaa 12,76,40\nQta 66,69,49664\nQmvaa 16,72,16\n"
"Qmvaa 16,76,44\nQmvaa 20,72,20\nQta 68,69,50176\nv_add_u32 v75, 1, v75\nQmvaa 20,76,48\ns_waitcnt vmcnt(36)\nds_write_b32 v73, v75\ns_add_u32 m0, s96, 140288\n"
"s_mov_b32 s95, 413696\nQmvaa 24,72,24\nbuffer_load_dwordx4 v63, s[52:55], s95 offen lds\nQmvaa 24,76,52\nQta 70,69,50688\ns_waitcnt lgkmcnt(5)\nQmvaa 28,56,0\nQmvaa 28,60,28\n"
"s_mov_b32 s95, 433152\nQmvaa 32,56,4\nbuffer_load_dwordx4 v63, s[52:55], s95 offen offset:1024 lds\nQmvaa 32,60,32\nQmvaa 36,56,8\nQta 72,69,49408\nQmvaa 36,60,36\nds_read_b32 v66, v74\n"
"Qmvaa 40,56,12\nQmvaa 40,60,40\nQta 74,69,49920\nQmvaa 44,56,16\nbuffer_store_dwordx4 v[160:163], v60, s[56:59], s88 offen offset:2048 sc1\nv_add_u32 v67, 2, v65\nQmvaa 44,60,44\ns_waitcnt lgkmcnt(1)\n"
"v_cmp_gt_u32 vcc, v75, v66\nQmvaa 48,56,20\ns_cbranch_vccnz .Lso460\n.Lro460:\nQmvaa 48,60,48\nQta 76,69,50432\nQmvaa 52,56,24\nbuffer_store_dword v67, v77, s[24:27], 0 offen sc1\n"
"Qmvaa 52,60,52\ns_waitcnt lgkmcnt(4)\nQmaaa 172,64,0\nQta 78,69,50944\nQmaaa 172,68,28\nQmaaa 176,64,4\nQmaaa 176,68,32\nQmaaa 180,64,8\n"
"Qta 56,69,57344\ns_add_u32 m0, s96, 148480\ns_mov_b32 s95, 454656\nQmaaa 180,68,36\nbuffer_load_dwordx4 v63, s[52:55], s95 offen lds\nQmaaa 184,64,12\nQmaaa 184,68,40\nQta 58,69,58368\n"
"Qmaaa 188,64,16\ns_mov_b32 s95, 474112\nQmaaa 188,68,44\nbuffer_load_dwordx4 v63, s[52:55], s95 offen offset:1024 lds\nQmaaa 192,64,20\nQta 60,68,0\nQmaaa 192,68,48\nQmaaa 196,64,24\n"
"Qmaaa 196,68,52\nQta 62,68,1024\ns_waitcnt lgkmcnt(4)\nQmvaa 92,72,0\nbuffer_store_dwordx4 v[192:195], v60, s[56:59], s88 offen offset:3072 sc1\nQmvaa 92,76,28\nQmvaa 124,72,4\nQmvaa 124,76,32\n"
"Qmvaa 156,72,8\nQta 64,69,57600\nQmvaa 156,76,36\nQmvaa 188,72,12\nQmvaa 188,76,40\nQta 66,69,58624\nQmvaa 220,72,16\nQmvaa 220,76,44\n"
"Qmvaa 252,72,20\nQta 68,68,256\ns_add_u32 m0, s96, 65536\ns_mov_b32 s95, 659456\nQmvaa 252,76,48\nbuffer_load_dwordx4 v61, s[52:55], s95 offen lds\nQmvaa 100,72,24\nQmvaa 100,76,52\n"
"Qta 70,68,1280\ns_waitcnt lgkmcnt(4)\nQmaaa 200,56,0\ns_mov_b32 s95, 678912\nQmaaa 200,60,28\nbuffer_load_dwordx4 v61, s[52:55], s95 offen offset:1024 lds\nQmaaa 204,56,4\nQmaaa 204,60,32\n"
"Qmaaa 208,56,8\nQta 72,69,57856\nQmaaa 208,60,36\ns_add_u32 s88, s71, 32768\nQmaaa 212,56,12\nbuffer_store_dwordx4 v[224:227], v60, s[56:59], s88 offen offset:0 sc1\nQmaaa 212,60,40\nQta 74,69,58880\n"
"Qmaaa 216,56,16\nQmaaa 216,60,44\nQmaaa 220,56,20\nQta 76,68,512\nQmaaa 220,60,48\nQmaaa 224,56,24\nQmaaa 224,60,52\nQta 78,68,1536\n"
"s_waitcnt lgkmcnt(4)\nQmaaa 228,64,0\nQmaaa 228,68,28\ns_add_u32 m0, s96, 73728\ns_mov_b32 s95, 700416\nQmaaa 232,64,4\nbuffer_load_dwordx4 v61, s[52:55], s95 offen lds\nQmaaa 232,68,32\n"
"Qmaaa 236,64,8\nQta 56,69,58112\nQmaaa 236,68,36\ns_mov_b32 s95, 719872\nQmaaa 240,64,12\nbuffer_load_dwordx4 v61, s[52:55], s95 offen offset:1024 lds\nQmaaa 240,68,40\nQta 58,69,59136\n"
"Qmaaa 244,64,16\nQmaaa 244,68,44\nQmaaa 248,64,20\nQta 60,68,768\nQmaaa 248,68,48\nQmaaa 252,64,24\nbuffer_store_dwordx4 v[148:151], v60, s[56:59], s88 offen offset:1024 sc1\nQmaaa 252,68,52\n"
"Qta 62,68,1792\ns_waitcnt lgkmcnt(4)\nQmvaa 132,72,0\nQmvaa 132,76,28\ns_mov_b32 s40, %17\ns_and_b32 s41, %18, 0xffff\nQmvaa 164,72,4\ns_mov_b32 s42, 571200\n"
"s_lshl_b32 s66, %19, 2\nQmvaa 164,76,32\ns_add_u32 s66, s66, s70\ns_mul_i32 s98, s66, 11264\nQmvaa 196,72,8\nQta 64,68,8192\ns_add_u32 s98, s98, 1835008\nQmvaa 196,76,36\n"
"s_xor_b32 s66, s66, 16\ns_mul_i32 s64, s66, 11264\nQmvaa 228,72,12\ns_add_u32 s64, s64, 1835008\ns_mul_i32 s66, s92, 33792\nQmvaa 228,76,40\nQta 66,68,9216\ns_add_u32 m0, s96, 81920\n"
"s_mov_b32 s95, 659456\nQmvaa 108,72,16\nbuffer_load_dwordx4 v62, s[52:55], s95 offen lds\nQmvaa 108,76,44\ns_add_u32 s66, s66, %10\ns_add_u32 s66, s66, 98304\nQmvaa 140,72,20\nQta 68,68,16384\n"
"s_mul_i32 s68, s93, 96\nQmvaa 140,76,48\ns_mov_b32 s95, 699392\ns_sub_u32 s68, s66, s68\nQmvaa 172,72,24\nbuffer_load_dwordx4 v62, s[52:55], s95 offen offset:1024 lds\nQmvaa 172,76,52\nQta 70,68,17408\n"
"s_add_u32 s68, s68, 96\ns_waitcnt lgkmcnt(4)\nQmvaa 204,56,0\nv_bfe_u32 v148, v60, 4, 4\nv_mul_u32_u24 v148, 208, v148\nQmvaa 204,60,28\nv_lshrrev_b32 v149, 8, v60\nv_lshl_add_u32 v148, v149, 3, v148\n"
"Qmvaa 236,56,4\nv_add_u32 v56, s68, v148\ns_lshl_b32 s68, s93, 3\nQmvaa 236,60,32\nv_lshrrev_b32 v148, 7, v60\nv_add_u32 v148, s68, v148\nQmaaa 96,56,8\nQta 72,68,8448\n"
"v_and_b32 v149, 127, v60\nQmaaa 96,60,36\nbuffer_store_dwordx4 v[180:183], v60, s[56:59], s88 offen offset:2048 sc1\nQmaaa 100,56,12\nv_and_b32 v150, 64, v60\nv_lshrrev_b32 v150, 1, v150\nQmaaa 100,60,40\nQta 74,68,9472\n"
"v_mul_u32_u24 v57, 208, v148\nQmaaa 104,56,16\nv_add3_u32 v57, v57, v149, s66\nv_add_u32 v57, v57, v150\nQmaaa 104,60,44\n.Lpg15:\ns_cbranch_execz .Ltr6\ns_and_b32 s65, %19, 1\n"
"s_and_b32 s68, %19, 4\nQmaaa 108,56,20\nQta 76,68,16640\ns_lshl_b32 s68, s68, 2\nQmaaa 108,60,48\ns_lshl_b32 s69, s65, 3\ns_add_u32 s68, s68, s69\nQmaaa 112,56,24\n"
"s_lshr_b32 s68, 0xc0004080, s68\ns_and_b32 s68, s68, 255\nQmaaa 112,60,52\nQta 78,68,17664\ns_mul_i32 s69, s92, 286720\ns_waitcnt vmcnt(24)\ns_waitcnt lgkmcnt(4)\nQmaav 116,64,80\n"
"s_mov_b32 s31, 143360\ns_add_u32 s68, s68, s69\nQmaav 116,68,88\nbuffer_load_dwordx4 a[0:3], v60, s[48:51], s31 offen offset:0\nQmaav 120,64,112\nv_mul_u32_u24 v64, 2560, v148\nv_add3_u32 v64, v64, v149, s68\nQmaav 120,68,120\n"
"s_lshl_b32 s68, s93, 4\nv_lshrrev_b32 v148, 6, v60\nQmaav 124,64,144\nQta 56,68,8704\nv_add_u32 v148, s68, v148\nQmaav 124,68,152\nbuffer_load_dwordx4 a[4:7], v60, s[48:51], s31 offen offset:1024\nQmaav 128,64,176\n"
"v_and_b32 v149, 63, v60\nv_and_b32 v150, 32, v60\nQmaav 128,68,184\nQta 58,68,9728\nv_lshlrev_b32 v150, 1, v150\nQmaav 132,64,208\nv_mul_u32_u24 v71, 208, v148\nv_add3_u32 v71, v71, v149, s66\n"
"Qmaav 132,68,216\nbuffer_load_dwordx4 a[8:11], v60, s[48:51], s31 offen offset:2048\nQmaav 136,64,240\nQta 60,68,16896\nv_add_u32 v71, v71, v150\nQmaav 136,68,248\ns_xor_b32 s65, s65, 1\ns_lshl_b32 s65, s65, 8\n"
"Qmaav 140,64,84\ns_add_u32 s68, s65, s69\nv_mul_u32_u24 v72, 2560, v148\nQmaav 140,68,116\nQta 62,68,17920\nv_add3_u32 v72, v72, v149, s68\ns_waitcnt lgkmcnt(4)\nQmaav 144,72,80\n"
"buffer_load_dwordx4 a[12:15], v60, s[48:51], s31 offen offset:3072\nQmaav 144,76,88\nQmaav 148,72,112\nQmaav 148,76,120\nQmaav 152,72,144\nQta 64,68,8960\ns_mov_b32 s31, 147456\nQmaav 152,76,152\n"
"buffer_load_dwordx4 a[16:19], v60, s[48:51], s31 offen offset:0\nQmaav 156,72,176\nQmaav 156,76,184\nQta 66,68,9984\nQmaav 160,72,208\nQmaav 160,76,216\nbuffer_load_dwordx4 a[20:23], v60, s[48:51], s31 offen offset:1024\nQmaav 164,72,240\n"
"Qta 68,68,17152\nQmaav 164,76,248\nQmaav 168,72,84\nQmaav 168,76,116\nQta 70,68,18176\ns_waitcnt lgkmcnt(4)\nQmvav 0,56,80\nbuffer_load_dwordx4 a[24:27], v60, s[48:51], s31 offen offset:2048\n"
"Qmvav 0,60,88\nQmvav 4,56,112\nQmvav 4,60,120\nQmvav 8,56,144\nQta 72,68,24576\ns_mov_b32 s31, 157696\nQmvav 8,60,152\nbuffer_load_dwordx4 a[28:31], v60, s[48:51], s31 offen offset:0\n"
"Qmvav 12,56,176\nQmvav 12,60,184\nQta 74,68,25088\nQmvav 16,56,208\nQmvav 16,60,216\nbuffer_load_dwordx4 a[32:35], v60, s[48:51], s31 offen offset:1024\nQmvav 20,56,240\nQta 76,68,25600\n"
"v_add_u32 v75, 1, v75\nQmvav 20,60,248\ns_waitcnt vmcnt(26)\nds_write_b32 v73, v75\nQmvav 24,56,84\nQmvav 24,60,116\nQta 78,68,26112\ns_waitcnt lgkmcnt(5)\n"
"Qmvav 28,64,80\nbuffer_load_dwordx4 a[36:39], v60, s[48:51], s31 offen offset:2048\nQmvav 28,68,88\nQmvav 32,64,112\nQmvav 32,68,120\nQmvav 36,64,144\nQta 56,68,24832\nQmvav 36,68,152\n"
"ds_read_b32 v66, v74\nQmvav 40,64,176\nbuffer_load_dwordx4 a[40:43], v60, s[48:51], s31 offen offset:3072\nQmvav 40,68,184\nQta 58,68,25344\nQmvav 44,64,208\nQmvav 44,68,216\ns_waitcnt lgkmcnt(1)\n"
"v_cmp_gt_u32 vcc, v75, v66\nQmvav 48,64,240\ns_cbranch_vccnz .Lso600\n.Lro600:\nQmvav 48,68,248\nQta 60,68,25856\ns_mov_b32 s31, 161792\nQmvav 52,64,84\n"
"buffer_load_dwordx4 a[44:47], v60, s[48:51], s31 offen offset:0\nQmvav 52,68,116\ns_waitcnt lgkmcnt(4)\nQmaav 172,72,80\nQta 62,68,26368\nQmaav 172,76,88\nQmaav 176,72,112\nbuffer_load_dwordx4 a[48:51], v60, s[48:51], s31 offen offset:1024\n"
"Qmaav 176,76,120\nQmaav 180,72,144\nQta 64,68,32768\nQmaav 180,76,152\nQmaav 184,72,176\nbuffer_load_dwordx4 a[52:55], v60, s[48:51], s31 offen offset:2048\nQmaav 184,76,184\nQta 66,68,33792\n"
"Qmaav 188,72,208\nQmaav 188,76,216\nQmaav 192,72,240\nQta 68,68,40960\ns_add_u32 m0, s96, 90112\ns_mov_b32 s95, 659456\nQmaav 192,76,248\nbuffer_load_dwordx4 v63, s[52:55], s95 offen lds\n"
"Qmaav 196,72,84\nQmaav 196,76,116\nQta 70,68,41984\ns_waitcnt lgkmcnt(4)\nQmvav 92,56,80\ns_mov_b32 s95, 678912\nQmvav 92,60,88\nbuffer_load_dwordx4 v63, s[52:55], s95 offen offset:1024 lds\n"
"Qmvav 124,56,112\nQmvav 124,60,120\nQmvav 156,56,144\nQta 72,68,33024\ns_add_u32 m0, s96, 98304\ns_mov_b32 s95, 700416\nQmvav 156,60,152\nbuffer_load_dwordx4 v63, s[52:55], s95 offen lds\n"
"Qmvav 188,56,176\nQmvav 188,60,184\nQta 74,68,34048\nQmvav 220,56,208\ns_mov_b32 s95, 719872\nQmvav 220,60,216\nbuffer_load_dwordx4 v63, s[52:55], s95 offen offset:1024 lds\nQmvav 252,56,240\n"
"Qta 76,68,41216\nQmvav 252,60,248\nQmvav 100,56,84\nQmvav 100,60,116\nQta 78,68,42240\ns_add_u32 m0, s96, 106496\ns_mov_b32 s95, 741376\ns_waitcnt lgkmcnt(4)\n"
"Qmaav 200,64,80\nbuffer_load_dwordx4 v61, s[52:55], s95 offen lds\nQmaav 200,68,88\nQmaav 204,64,112\nQmaav 204,68,120\nQmaav 208,64,144\nQta 56,68,33280\ns_mov_b32 s95, 760832\n"
"Qmaav 208,68,152\nbuffer_load_dwordx4 v61, s[52:55], s95 offen offset:1024 lds\nQmaav 212,64,176\nQmaav 212,68,184\nQta 58,68,34304\nQmaav 216,64,208\ns_add_u32 m0, s96, 114688\ns_mov_b32 s95, 782336\n"
"Qmaav 216,68,216\nbuffer_load_dwordx4 v61, s[52:55], s95 offen lds\nQmaav 220,64,240\nQta 60,68,41472\nQmaav 220,68,248\nQmaav 224,64,84\nQmaav 224,68,116\nQta 62,68,42496\n"
"s_waitcnt lgkmcnt(4)\nQmaav 228,72,80\nQmaav 228,76,88\ns_add_u32 s88, s71, 43008\nQmaav 232,72,112\nbuffer_store_dwordx4 v[104:107], v60, s[56:59], s88 offen offset:0 sc1\nQmaav 232,76,120\nQmaav 236,72,144\n"
"Qta 64,68,33536\nQmaav 236,76,152\nQmaav 240,72,176\nQmaav 240,76,184\nQta 66,68,34560\nQmaav 244,72,208\nQmaav 244,76,216\nQmaav 248,72,240\n"
"Qta 68,68,41728\nQmaav 248,76,248\nQmaav 252,72,84\nQmaav 252,76,116\nQta 70,68,42752\ns_waitcnt lgkmcnt(4)\nQmvav 132,56,80\nbuffer_store_dwordx4 v[136:139], v60, s[56:59], s88 offen offset:1024 sc1\n"
"Qmvav 132,60,88\nQmvav 164,56,112\nQmvav 164,60,120\nQmvav 196,56,144\nQta 72,68,49152\nQmvav 196,60,152\nQmvav 228,56,176\nQmvav 228,60,184\n"
"Qta 74,68,50176\nQmvav 108,56,208\nQmvav 108,60,216\nQmvav 140,56,240\nQta 76,68,57344\ns_mov_b32 s95, 801792\nQmvav 140,60,248\nbuffer_load_dwordx4 v61, s[52:55], s95 offen offset:1024 lds\n"
"Qmvav 172,56,84\nQmvav 172,60,116\nQta 78,68,58368\ns_waitcnt lgkmcnt(4)\nQmvav 204,64,80\ns_mov_b32 s31, 229376\nQmvav 204,68,88\nbuffer_load_dwordx4 v[80:83], v60, s[48:51], s31 offen offset:0\n"
"Qmvav 236,64,112\nQmvav 236,68,120\nQmaav 96,64,144\nQta 56,68,49408\nQmaav 96,68,152\nbuffer_load_dwordx4 v[112:115], v60, s[48:51], s31 offen offset:1024\nQmaav 100,64,176\nQmaav 100,68,184\n"
"Qta 58,68,50432\nQmaav 104,64,208\nQmaav 104,68,216\nbuffer_load_dwordx4 v[144:147], v60, s[48:51], s31 offen offset:2048\nQmaav 108,64,240\nQta 60,68,57600\nQmaav 108,68,248\nQmaav 112,64,84\n"
"Qmaav 112,68,116\nQta 62,68,58624\ns_waitcnt vmcnt(13)\ns_waitcnt lgkmcnt(4)\nQmaaa 116,72,0\nbuffer_load_dwordx4 v[176:179], v60, s[48:51], s31 offen offset:3072\nQmaaa 116,76,28\nQmaaa 120,72,4\n"
"Qmaaa 120,76,32\nQmaaa 124,72,8\nQta 64,68,49664\ns_mov_b32 s31, 233472\nQmaaa 124,76,36\nbuffer_load_dwordx4 v[208:211], v60, s[48:51], s31 offen offset:0\nQmaaa 128,72,12\nQmaaa 128,76,40\n"
"Qta 66,68,50688\nQmaaa 132,72,16\nQmaaa 132,76,44\nbuffer_load_dwordx4 v[240:243], v60, s[48:51], s31 offen offset:1024\nQmaaa 136,72,20\nQta 68,68,57856\nQmaaa 136,76,48\nQmaaa 140,72,24\n"
"Qmaaa 140,76,52\nQta 70,68,58880\ns_waitcnt lgkmcnt(4)\nQmaaa 144,56,0\nbuffer_load_dwordx4 v[84:87], v60, s[48:51], s31 offen offset:2048\nQmaaa 144,60,28\nQmaaa 148,56,4\nQmaaa 148,60,32\n"
"Qmaaa 152,56,8\nQta 72,68,49920\ns_mov_b32 s31, 243712\nQmaaa 152,60,36\nbuffer_load_dwordx4 v[88:91], v60, s[48:51], s31 offen offset:0\nQmaaa 156,56,12\nQmaaa 156,60,40\nQta 74,68,50944\n"
"Qmaaa 160,56,16\nQmaaa 160,60,44\nbuffer_load_dwordx4 v[120:123], v60, s[48:51], s31 offen offset:1024\nQmaaa 164,56,20\nQta 76,68,58112\nQmaaa 164,60,48\nQmaaa 168,56,24\nQmaaa 168,60,52\n"
"Qta 78,68,59136\ns_waitcnt lgkmcnt(4)\nQmvaa 0,64,0\nbuffer_load_dwordx4 v[152:155], v60, s[48:51], s31 offen offset:2048\nQmvaa 0,68,28\nQmvaa 4,64,4\nQmvaa 4,68,32\nQmvaa 8,64,8\n"
"Qta 56,70,1024\nQmvaa 8,68,36\nbuffer_load_dwordx4 v[184:187], v60, s[48:51], s31 offen offset:3072\nQmvaa 12,64,12\nQmvaa 12,68,40\nQta 58,70,1536\nQmvaa 16,64,16\ns_mov_b32 s31, 247808\n"
"Qmvaa 16,68,44\nbuffer_load_dwordx4 v[216:219], v60, s[48:51], s31 offen offset:0\nQmvaa 20,64,20\nQta 60,70,2048\nv_add_u32 v75, 1, v75\nQmvaa 20,68,48\ns_waitcnt vmcnt(37)\nds_write_b32 v73, v75\n"
"Qmvaa 24,64,24\nQmvaa 24,68,52\nQta 62,70,2560\ns_waitcnt lgkmcnt(5)\nQmvaa 28,72,0\nbuffer_load_dwordx4 v[248:251], v60, s[48:51], s31 offen offset:1024\nQmvaa 28,76,28\nQmvaa 32,72,4\n"
"Qmvaa 32,76,32\nQmvaa 36,72,8\nQta 64,70,1280\nQmvaa 36,76,36\nds_read_b32 v66, v74\nQmvaa 40,72,12\nbuffer_load_dwordx4 v[116:119], v60, s[48:51], s31 offen offset:2048\nQmvaa 40,76,40\n"
"Qta 66,70,1792\nQmvaa 44,72,16\nQmvaa 44,76,44\ns_waitcnt lgkmcnt(1)\nv_cmp_gt_u32 vcc, v75, v66\nQmvaa 48,72,20\ns_cbranch_vccnz .Lso740\n.Lro740:\n"
"Qmvaa 48,76,48\nQta 68,70,2304\ns_add_u32 m0, s96, 122880\ns_mov_b32 s95, 741376\nQmvaa 52,72,24\nbuffer_load_dwordx4 v62, s[52:55], s95 offen lds\nQmvaa 52,76,52\ns_waitcnt lgkmcnt(4)\n"
"Qmaaa 172,56,0\nQta 70,70,2816\nQmaaa 172,60,28\ns_mov_b32 s95, 781312\nQmaaa 176,56,4\nbuffer_load_dwordx4 v62, s[52:55], s95 offen offset:1024 lds\nQmaaa 176,60,32\nQmaaa 180,56,8\n"
"Qta 72,70,9216\nQmaaa 180,60,36\ns_add_u32 m0, s96, 0\ns_mov_b32 s95, 741376\nQmaaa 184,56,12\nbuffer_load_dwordx4 v63, s[52:55], s95 offen lds\nQmaaa 184,60,40\nQta 74,70,10240\n"
"Qmaaa 188,56,16\nQmaaa 188,60,44\nQmaaa 192,56,20\nQta 76,70,17408\ns_mov_b32 s95, 760832\nQmaaa 192,60,48\nbuffer_load_dwordx4 v63, s[52:55], s95 offen offset:1024 lds\nQmaaa 196,56,24\n"
"Qmaaa 196,60,52\nQta 78,70,18432\ns_waitcnt lgkmcnt(4)\nQmvaa 92,64,0\ns_add_u32 m0, s96, 8192\ns_mov_b32 s95, 782336\nQmvaa 92,68,28\nbuffer_load_dwordx4 v63, s[52:55], s95 offen lds\n"
"Qmvaa 124,64,4\nQmvaa 124,68,32\nQmvaa 156,64,8\nQta 56,70,9472\ns_mov_b32 s95, 801792\nQmvaa 156,68,36\nbuffer_load_dwordx4 v63, s[52:55], s95 offen offset:1024 lds\nQmvaa 188,64,12\n"
"Qmvaa 188,68,40\nQta 58,70,10496\nQmvaa 220,64,16\ns_add_u32 m0, s96, 16384\ns_mov_b32 s95, 987136\nQmvaa 220,68,44\nbuffer_load_dwordx4 v61, s[52:55], s95 offen lds\nQmvaa 252,64,20\n"
"Qta 60,70,17664\nQmvaa 252,68,48\nQmvaa 100,64,24\nQmvaa 100,68,52\nQta 62,70,18688\ns_mov_b32 s95, 1006592\ns_waitcnt lgkmcnt(4)\nQmaaa 200,72,0\n"
"buffer_load_dwordx4 v61, s[52:55], s95 offen offset:1024 lds\nQmaaa 200,76,28\nQmaaa 204,72,4\nQmaaa 204,76,32\nQmaaa 208,72,8\nQta 64,70,9728\ns_add_u32 m0, s96, 24576\ns_mov_b32 s95, 1028096\n"
"Qmaaa 208,76,36\nbuffer_load_dwordx4 v61, s[52:55], s95 offen lds\nQmaaa 212,72,12\nQmaaa 212,76,40\nQta 66,70,10752\nQmaaa 216,72,16\ns_mov_b32 s95, 1047552\nQmaaa 216,76,44\n"
"buffer_load_dwordx4 v61, s[52:55], s95 offen offset:1024 lds\nQmaaa 220,72,20\nQta 68,70,17920\nQmaaa 220,76,48\nQmaaa 224,72,24\nQmaaa 224,76,52\nQta 70,70,18944\ns_waitcnt lgkmcnt(4)\n"
"Qmaaa 228,56,0\nQmaaa 228,60,28\nQmaaa 232,56,4\nbuffer_store_dwordx4 v[168:171], v60, s[56:59], s88 offen offset:2048 sc1\nQmaaa 232,60,32\nQmaaa 236,56,8\nQta 72,70,9984\nQmaaa 236,60,36\n"
"Qmaaa 240,56,12\nQmaaa 240,60,40\nQta 74,70,11008\nQmaaa 244,56,16\nQmaaa 244,60,44\nQmaaa 248,56,20\nQta 76,70,18176\nQmaaa 248,60,48\n"
"Qmaaa 252,56,24\nQmaaa 252,60,52\nQta 78,70,19200\ns_waitcnt lgkmcnt(4)\nQmvaa 132,64,0\nbuffer_store_dwordx4 v[200:203], v60, s[56:59], s88 offen offset:3072 sc1\nQmvaa 132,68,28\nQmvaa 164,64,4\n"
"Qmvaa 164,68,32\nQmvaa 196,64,8\nQta 56,69,0\nQmvaa 196,68,36\nQmvaa 228,64,12\nQmvaa 228,68,40\nQta 58,69,1024\nQmvaa 108,64,16\n"
"Qmvaa 108,68,44\nQmvaa 140,64,20\nQta 60,69,8192\ns_add_u32 m0, s96, 32768\ns_mov_b32 s95, 987136\nQmvaa 140,68,48\nbuffer_load_dwordx4 v62, s[52:55], s95 offen lds\nQmvaa 172,64,24\n"
"Qmvaa 172,68,52\nQta 62,69,9216\ns_waitcnt lgkmcnt(4)\nQmvaa 204,72,0\ns_mov_b32 s95, 1027072\nQmvaa 204,76,28\nbuffer_load_dwordx4 v62, s[52:55], s95 offen offset:1024 lds\nQmvaa 236,72,4\n"
"Qmvaa 236,76,32\nQmaaa 96,72,8\nQta 64,69,256\ns_mov_b32 s31, 258048\nQmaaa 96,76,36\nbuffer_load_dwordx4 a[0:3], v60, s[48:51], s31 offen offset:0\nQmaaa 100,72,12\nQmaaa 100,76,40\n"
"Qta 66,69,1280\nQmaaa 104,72,16\nQmaaa 104,76,44\nbuffer_load_dwordx4 a[4:7], v60, s[48:51], s31 offen offset:1024\nQmaaa 108,72,20\nQta 68,69,8448\nQmaaa 108,76,48\nQmaaa 112,72,24\n"
"Qmaaa 112,76,52\nQta 70,69,9472\ns_waitcnt vmcnt(16)\ns_waitcnt lgkmcnt(4)\nQmaav 116,56,80\nbuffer_load_dwordx4 a[8:11], v60, s[48:51], s31 offen offset:2048\nQmaav 116,60,88\nQmaav 120,56,112\n"
"Qmaav 120,60,120\nQmaav 124,56,144\nQta 72,69,512\nQmaav 124,60,152\nbuffer_load_dwordx4 a[12:15], v60, s[48:51], s31 offen offset:3072\nQmaav 128,56,176\nQmaav 128,60,184\nQta 74,69,1536\n"
"Qmaav 132,56,208\ns_mov_b32 s31, 262144\n.Lpg16:\ns_cbranch_execz .Lpg20\nQmaav 132,60,216\nbuffer_load_dwordx4 a[16:19], v60, s[48:51], s31 offen offset:0\nQmaav 136,56,240\nQta 76,69,8704\n"
"Qmaav 136,60,248\nQmaav 140,56,84\nQmaav 140,60,116\nQta 78,69,9728\ns_waitcnt lgkmcnt(4)\nQmaav 144,64,80\nbuffer_load_dwordx4 a[20:23], v60, s[48:51], s31 offen offset:1024\nQmaav 144,68,88\n"
"Qmaav 148,64,112\nQmaav 148,68,120\nQmaav 152,64,144\nQta 56,69,768\nQmaav 152,68,152\nbuffer_load_dwordx4 a[24:27], v60, s[48:51], s31 offen offset:2048\nQmaav 156,64,176\nQmaav 156,68,184\n"
"Qta 58,69,1792\nQmaav 160,64,208\ns_mov_b32 s31, 272384\nQmaav 160,68,216\nbuffer_load_dwordx4 a[28:31], v60, s[48:51], s31 offen offset:0\nQmaav 164,64,240\nQta 60,69,8960\nQmaav 164,68,248\n"
"Qmaav 168,64,84\nQmaav 168,68,116\nQta 62,69,9984\ns_waitcnt lgkmcnt(4)\nQmvav 0,72,80\nbuffer_load_dwordx4 a[32:35], v60, s[48:51], s31 offen offset:1024\nQmvav 0,76,88\nQmvav 4,72,112\n"
"Qmvav 4,76,120\nQmvav 8,72,144\nQta 64,69,16384\nQmvav 8,76,152\nbuffer_load_dwordx4 a[36:39], v60, s[48:51], s31 offen offset:2048\nQmvav 12,72,176\nQmvav 12,76,184\nQta 66,69,16896\n"
"Qmvav 16,72,208\nQmvav 16,76,216\nbuffer_load_dwordx4 a[40:43], v60, s[48:51], s31 offen offset:3072\nQmvav 20,72,240\nQta 68,69,17408\nv_add_u32 v75, 1, v75\nQmvav 20,76,248\ns_waitcnt vmcnt(23)\n"
"ds_write_b32 v73, v75\nQmvav 24,72,84\nQmvav 24,76,116\nQta 70,69,17920\ns_mov_b32 s31, 276480\ns_waitcnt lgkmcnt(5)\nQmvav 28,56,80\nbuffer_load_dwordx4 a[44:47], v60, s[48:51], s31 offen offset:0\n"
"Qmvav 28,60,88\nQmvav 32,56,112\nQmvav 32,60,120\nQmvav 36,56,144\nQta 72,69,16640\nQmvav 36,60,152\nds_read_b32 v66, v74\nQmvav 40,56,176\n"
"buffer_load_dwordx4 a[48:51], v60, s[48:51], s31 offen offset:1024\nQmvav 40,60,184\nQta 74,69,17152\nQmvav 44,56,208\nQmvav 44,60,216\ns_waitcnt lgkmcnt(1)\nv_cmp_gt_u32 vcc, v75, v66\nQmvav 48,56,240\n"
"s_cbranch_vccnz .Lso880\n.Lro880:\nQmvav 48,60,248\nQta 76,69,17664\nQmvav 52,56,84\nbuffer_load_dwordx4 a[52:55], v60, s[48:51], s31 offen offset:2048\nQmvav 52,60,116\ns_waitcnt lgkmcnt(4)\n"
"Qmaav 172,64,80\nQta 78,69,18176\nQmaav 172,68,88\ns_add_u32 m0, s96, 40960\ns_mov_b32 s95, 987136\nQmaav 176,64,112\nbuffer_load_dwordx4 v63, s[52:55], s95 offen lds\nQmaav 176,68,120\n"
"Qmaav 180,64,144\nQta 56,69,24576\nQmaav 180,68,152\ns_mov_b32 s95, 1006592\nQmaav 184,64,176\nbuffer_load_dwordx4 v63, s[52:55], s95 offen offset:1024 lds\nQmaav 184,68,184\nQta 58,69,25600\n"
"Qmaav 188,64,208\nQmaav 188,68,216\nQmaav 192,64,240\nQta 60,69,32768\ns_add_u32 m0, s96, 49152\ns_mov_b32 s95, 1028096\nQmaav 192,68,248\nbuffer_load_dwordx4 v63, s[52:55], s95 offen lds\n"
"Qmaav 196,64,84\nQmaav 196,68,116\nQta 62,69,33792\ns_waitcnt lgkmcnt(4)\nQmvav 92,72,80\ns_mov_b32 s95, 1047552\nQmvav 92,76,88\nbuffer_load_dwordx4 v63, s[52:55], s95 offen offset:1024 lds\n"
"Qmvav 124,72,112\nQmvav 124,76,120\nQmvav 156,72,144\nQta 64,69,24832\ns_add_u32 m0, s96, 57344\ns_mov_b32 s95, 1069056\nQmvav 156,76,152\nbuffer_load_dwordx4 v61, s[52:55], s95 offen lds\n"
"Qmvav 188,72,176\nQmvav 188,76,184\nQta 66,69,25856\nQmvav 220,72,208\ns_mov_b32 s95, 1088512\nQmvav 220,76,216\nbuffer_load_dwordx4 v61, s[52:55], s95 offen offset:1024 lds\nQmvav 252,72,240\n"
"Qta 68,69,33024\nQmvav 252,76,248\nQmvav 100,72,84\nQmvav 100,76,116\nQta 70,69,34048\ns_add_u32 m0, s96, 132096\ns_mov_b32 s95, 1110016\ns_waitcnt lgkmcnt(4)\n"
"Qmaav 200,56,80\nbuffer_load_dwordx4 v61, s[52:55], s95 offen lds\nQmaav 200,60,88\nQmaav 204,56,112\nQmaav 204,60,120\nQmaav 208,56,144\nQta 72,69,25088\ns_mov_b32 s95, 1129472\n"
"Qmaav 208,60,152\nbuffer_load_dwordx4 v61, s[52:55], s95 offen offset:1024 lds\nQmaav 212,56,176\nQmaav 212,60,184\nQta 74,69,26112\nQmaav 216,56,208\ns_add_u32 m0, s96, 140288\ns_mov_b32 s95, 1069056\n"
"Qmaav 216,60,216\nbuffer_load_dwordx4 v62, s[52:55], s95 offen lds\nQmaav 220,56,240\nQta 76,69,33280\nQmaav 220,60,248\nQmaav 224,56,84\nQmaav 224,60,116\nQta 78,69,34304\n"
"s_waitcnt lgkmcnt(4)\nQmaav 228,64,80\nQmaav 228,68,88\ns_add_u32 s88, s71, 47104\nQmaav 232,64,112\nbuffer_store_dwordx4 v[232:235], v60, s[56:59], s88 offen offset:0 sc1\nQmaav 232,68,120\nQmaav 236,64,144\n"
"Qta 56,69,25344\nQmaav 236,68,152\nQmaav 240,64,176\nQmaav 240,68,184\nQta 58,69,26368\nQmaav 244,64,208\nQmaav 244,68,216\nQmaav 248,64,240\n"
"Qta 60,69,33536\nQmaav 248,68,248\nQmaav 252,64,84\nQmaav 252,68,116\nQta 62,69,34560\ns_waitcnt lgkmcnt(4)\nQmvav 132,72,80\nbuffer_store_dwordx4 v[212:215], v60, s[56:59], s88 offen offset:1024 sc1\n"
"Qmvav 132,76,88\nQmvav 164,72,112\nQmvav 164,76,120\nQmvav 196,72,144\nQta 64,69,40960\nQmvav 196,76,152\nQmvav 228,72,176\nQmvav 228,76,184\n"
"Qta 66,69,41984\nQmvav 108,72,208\nQmvav 108,76,216\nQmvav 140,72,240\nQta 68,69,49152\ns_mov_b32 s95, 1108992\nQmvav 140,76,248\nbuffer_load_dwordx4 v62, s[52:55], s95 offen offset:1024 lds\n"
"Qmvav 172,72,84\nQmvav 172,76,116\nQta 70,69,50176\ns_waitcnt lgkmcnt(4)\nQmvav 204,56,80\ns_mov_b32 s31, 344064\nQmvav 204,60,88\nbuffer_load_dwordx4 v[80:83], v60, s[48:51], s31 offen offset:0\n"
"Qmvav 236,56,112\nQmvav 236,60,120\nQmaav 96,56,144\nQta 72,69,41216\nQmaav 96,60,152\nbuffer_load_dwordx4 v[112:115], v60, s[48:51], s31 offen offset:1024\nQmaav 100,56,176\nQmaav 100,60,184\n"
"Qta 74,69,42240\nQmaav 104,56,208\nQmaav 104,60,216\nbuffer_load_dwordx4 v[144:147], v60, s[48:51], s31 offen offset:2048\nQmaav 108,56,240\nQta 76,69,49408\nQmaav 108,60,248\nQmaav 112,56,84\n"
"v_accvgpr_read_b32 v96, a96\nv_accvgpr_read_b32 v97, a97\nQmaav 112,60,116\nQta 78,69,50432\nv_accvgpr_read_b32 v98, a98\ns_waitcnt vmcnt(15)\ns_waitcnt lgkmcnt(4)\nQmaaa 116,64,0\n"
"buffer_load_dwordx4 v[176:179], v60, s[48:51], s31 offen offset:3072\nQmaaa 116,68,28\nv_accvgpr_read_b32 v99, a99\nv_accvgpr_read_b32 v128, a100\nQmaaa 120,64,4\nv_accvgpr_read_b32 v129, a101\nv_accvgpr_read_b32 v130, a102\nQmaaa 120,68,32\n"
"v_accvgpr_read_b32 v131, a103\nv_accvgpr_read_b32 v160, a104\nQmaaa 124,64,8\nQta 56,69,41472\ns_mov_b32 s31, 348160\nQmaaa 124,68,36\nbuffer_load_dwordx4 v[208:211], v60, s[48:51], s31 offen offset:0\nQmaaa 128,64,12\n"
"v_accvgpr_read_b32 v161, a105\nv_accvgpr_read_b32 v162, a106\nQmaaa 128,68,40\nQta 58,69,42496\nv_accvgpr_read_b32 v163, a107\nQmaaa 132,64,16\nv_accvgpr_read_b32 v192, a108\nv_accvgpr_read_b32 v193, a109\n"
"Qmaaa 132,68,44\nbuffer_load_dwordx4 v[240:243], v60, s[48:51], s31 offen offset:1024\nQmaaa 136,64,20\nQta 60,69,49664\nv_accvgpr_read_b32 v194, a110\nQmaaa 136,68,48\nv_accvgpr_read_b32 v195, a111\nv_accvgpr_read_b32 v224, a112\n"
"Qmaaa 140,64,24\nv_accvgpr_read_b32 v225, a113\nv_accvgpr_read_b32 v226, a114\nQmaaa 140,68,52\nQta 62,69,50688\nv_accvgpr_read_b32 v227, a115\ns_waitcnt lgkmcnt(4)\nQmaaa 144,72,0\n"
"buffer_load_dwordx4 v[84:87], v60, s[48:51], s31 offen offset:2048\nQmaaa 144,76,28\nQmaaa 148,72,4\nQmaaa 148,76,32\nQmaaa 152,72,8\nQta 64,69,41728\ns_mov_b32 s31, 358400\nQmaaa 152,76,36\n"
"buffer_load_dwordx4 v[88:91], v60, s[48:51], s31 offen offset:0\nQmaaa 156,72,12\nQmaaa 156,76,40\nQta 66,69,42752\nQmaaa 160,72,16\nQmaaa 160,76,44\nbuffer_load_dwordx4 v[120:123], v60, s[48:51], s31 offen offset:1024\nQmaaa 164,72,20\n"
"Qta 68,69,49920\nQmaaa 164,76,48\nQmaaa 168,72,24\nQmaaa 168,76,52\nQta 70,69,50944\ns_waitcnt lgkmcnt(4)\nQmvaa 0,56,0\nbuffer_load_dwordx4 v[152:155], v60, s[48:51], s31 offen offset:2048\n"
"Qmvaa 0,60,28\nQmvaa 4,56,4\nQmvaa 4,60,32\nQmvaa 8,56,8\nQta 72,69,57344\nQmvaa 8,60,36\nbuffer_load_dwordx4 v[184:187], v60, s[48:51], s31 offen offset:3072\nQmvaa 12,56,12\n"
"Qmvaa 12,60,40\nQta 74,69,57856\nQmvaa 16,56,16\ns_mov_b32 s31, 362496\nQmvaa 16,60,44\nbuffer_load_dwordx4 v[216:219], v60, s[48:51], s31 offen offset:0\nQmvaa 20,56,20\nQta 76,69,58368\n"
"v_add_u32 v75, 1, v75\nQmvaa 20,60,48\ns_waitcnt vmcnt(38)\nds_write_b32 v73, v75\nQmvaa 24,56,24\nQmvaa 24,60,52\nQta 78,69,58880\ns_waitcnt lgkmcnt(5)\n"
"Qmvaa 28,64,0\nbuffer_load_dwordx4 v[248:251], v60, s[48:51], s31 offen offset:1024\nQmvaa 28,68,28\nQmvaa 32,64,4\nQmvaa 32,68,32\nQmvaa 36,64,8\nQta 56,69,57600\nQmvaa 36,68,36\n"
"ds_read_b32 v66, v74\nQmvaa 40,64,12\nbuffer_load_dwordx4 v[116:119], v60, s[48:51], s31 offen offset:2048\nQmvaa 40,68,40\nQta 58,69,58112\nQmvaa 44,64,16\nQmvaa 44,68,44\ns_waitcnt lgkmcnt(1)\n"
"v_cmp_gt_u32 vcc, v75, v66\nQmvaa 48,64,20\ns_cbranch_vccnz .Lso1020\n.Lro1020:\nQmvaa 48,68,48\nQta 60,69,58624\ns_add_u32 m0, s96, 148480\ns_mov_b32 s95, 1069056\n"
"Qmvaa 52,64,24\nbuffer_load_dwordx4 v63, s[52:55], s95 offen lds\nQmvaa 52,68,52\ns_waitcnt lgkmcnt(4)\nQmaaa 172,72,0\nQta 62,69,59136\nQmaaa 172,76,28\ns_mov_b32 s95, 1088512\n"
"Qmaaa 176,72,4\nbuffer_load_dwordx4 v63, s[52:55], s95 offen offset:1024 lds\nQmaaa 176,76,32\nQmaaa 180,72,8\nQta 64,68,0\nQmaaa 180,76,36\ns_add_u32 m0, s96, 65536\ns_mov_b32 s95, 1110016\n"
"Qmaaa 184,72,12\nbuffer_load_dwordx4 v63, s[52:55], s95 offen lds\nQmaaa 184,76,40\nQta 66,68,1024\nQmaaa 188,72,16\nQmaaa 188,76,44\nQmaaa 192,72,20\nQta 68,68,8192\n"
"s_mov_b32 s95, 1129472\nQmaaa 192,76,48\nbuffer_load_dwordx4 v63, s[52:55], s95 offen offset:1024 lds\nQmaaa 196,72,24\nQmaaa 196,76,52\nQta 70,68,9216\ns_waitcnt lgkmcnt(4)\nQmvaa 92,56,0\n"
"s_add_u32 m0, s96, 73728\ns_add_u32 s95, s67, 4096\nQmvaa 92,60,28\nbuffer_load_dwordx4 v61, s[52:55], s95 offen lds\nQmvaa 124,56,4\nQmvaa 124,60,32\nQmvaa 156,56,8\nQta 72,68,256\n"
"s_add_u32 s95, s67, 23552\nQmvaa 156,60,36\nbuffer_load_dwordx4 v61, s[52:55], s95 offen offset:1024 lds\nQmvaa 188,56,12\nQmvaa 188,60,40\nQta 74,68,1280\nQmvaa 220,56,16\ns_add_u32 m0, s96, 81920\n"
"s_add_u32 s95, s67, 45056\nQmvaa 220,60,44\nbuffer_load_dwordx4 v61, s[52:55], s95 offen lds\nQmvaa 252,56,20\nQta 76,68,8448\nQmvaa 252,60,48\nQmvaa 100,56,24\nQmvaa 100,60,52\n"
"Qta 78,68,9472\ns_add_u32 s95, s67, 64512\ns_waitcnt lgkmcnt(4)\nQmaaa 200,64,0\nbuffer_load_dwordx4 v61, s[52:55], s95 offen offset:1024 lds\nQmaaa 200,68,28\nQmaaa 204,64,4\nQmaaa 204,68,32\n"
"Qmaaa 208,64,8\nQta 56,68,512\ns_add_u32 m0, s96, 90112\ns_add_u32 s95, s67, 4096\nQmaaa 208,68,36\nbuffer_load_dwordx4 v62, s[52:55], s95 offen lds\nQmaaa 212,64,12\nQmaaa 212,68,40\n"
"Qta 58,68,1536\nQmaaa 216,64,16\ns_add_u32 s95, s67, 44032\nQmaaa 216,68,44\nbuffer_load_dwordx4 v62, s[52:55], s95 offen offset:1024 lds\nQmaaa 220,64,20\nQta 60,68,8704\nQmaaa 220,68,48\n"
"Qmaaa 224,64,24\nQmaaa 224,68,52\nQta 62,68,9728\ns_waitcnt lgkmcnt(4)\nQmaaa 228,72,0\nQmaaa 228,76,28\nQmaaa 232,72,4\nbuffer_store_dwordx4 v[244:247], v60, s[56:59], s88 offen offset:2048 sc1\n"
"Qmaaa 232,76,32\nQmaaa 236,72,8\nQta 64,68,768\nQmaaa 236,76,36\nQmaaa 240,72,12\nQmaaa 240,76,40\nQta 66,68,1792\nQmaaa 244,72,16\n"
"Qmaaa 244,76,44\nQmaaa 248,72,20\nQta 68,68,8960\nQmaaa 248,76,48\ns_add_u32 m0, s96, 98304\ns_add_u32 s95, s67, 4096\nQmaaa 252,72,24\nbuffer_load_dwordx4 v63, s[52:55], s95 offen lds\n"
"Qmaaa 252,76,52\nQta 70,68,9984\ns_waitcnt lgkmcnt(4)\nQmvaa 132,56,0\nQmvaa 132,60,28\ns_add_u32 s95, s67, 23552\nQmvaa 164,56,4\nbuffer_load_dwordx4 v63, s[52:55], s95 offen offset:1024 lds\n"
"Qmvaa 164,60,32\nQmvaa 196,56,8\nQta 72,68,16384\nQmvaa 196,60,36\ns_add_u32 m0, s96, 106496\ns_add_u32 s95, s67, 45056\nQmvaa 228,56,12\nbuffer_load_dwordx4 v63, s[52:55], s95 offen lds\n"
"Qmvaa 228,60,40\nQta 74,68,17408\nQmvaa 108,56,16\nQmvaa 108,60,44\nQmvaa 140,56,20\nQta 76,68,24576\ns_add_u32 s95, s67, 64512\nQmvaa 140,60,48\n"
"buffer_load_dwordx4 v63, s[52:55], s95 offen offset:1024 lds\nQmvaa 172,56,24\nQmvaa 172,60,52\nQta 78,68,25600\ns_waitcnt lgkmcnt(4)\nQmvaa 204,64,0\nbuffer_load_dword v78, v76, s[24:27], 0 offen sc1\ns_mov_b32 s31, 372736\n"
"Qmvaa 204,68,28\nbuffer_load_dwordx4 a[0:3], v60, s[48:51], s31 offen offset:0\nQmvaa 236,64,4\nQmvaa 236,68,32\nQmvaa 96,64,8\nQta 56,68,16640\nQmvaa 96,68,36\nbuffer_load_dwordx4 a[4:7], v60, s[48:51], s31 offen offset:1024\n"
"Qmvaa 128,64,12\nQmvaa 128,68,40\nQta 58,68,17664\nQmvaa 160,64,16\nQmvaa 160,68,44\nbuffer_load_dwordx4 a[8:11], v60, s[48:51], s31 offen offset:2048\nQmvaa 192,64,20\nQta 60,68,24832\n"
"Qmvaa 192,68,48\nQmvaa 224,64,24\nQmvaa 224,68,52\nQta 62,68,25856\ns_waitcnt vmcnt(19)\ns_waitcnt lgkmcnt(4)\nQmaav 116,72,80\nbuffer_load_dwordx4 a[12:15], v60, s[48:51], s31 offen offset:3072\n"
"Qmaav 116,76,88\nQmaav 120,72,112\nQmaav 120,76,120\nQmaav 124,72,144\nQta 64,68,16896\ns_mov_b32 s31, 376832\nQmaav 124,76,152\nbuffer_load_dwordx4 a[16:19], v60, s[48:51], s31 offen offset:0\n"
"Qmaav 128,72,176\nQmaav 128,76,184\nQta 66,68,17920\nQmaav 132,72,208\nQmaav 132,76,216\nbuffer_load_dwordx4 a[20:23], v60, s[48:51], s31 offen offset:1024\nQmaav 136,72,240\nQta 68,68,25088\n"
"Qmaav 136,76,248\nQmaav 140,72,84\nQmaav 140,76,116\nQta 70,68,26112\ns_waitcnt lgkmcnt(4)\nQmaav 144,56,80\n.Lpg17:\ns_cbranch_execz .Lpg21\n"
"buffer_load_dwordx4 a[24:27], v60, s[48:51], s31 offen offset:2048\nQmaav 144,60,88\nQmaav 148,56,112\nQmaav 148,60,120\nQmaav 152,56,144\nQta 72,68,17152\ns_mov_b32 s31, 387072\nQmaav 152,60,152\n"
"buffer_load_dwordx4 a[28:31], v60, s[48:51], s31 offen offset:0\nQmaav 156,56,176\nQmaav 156,60,184\nQta 74,68,18176\nQmaav 160,56,208\nQmaav 160,60,216\nbuffer_load_dwordx4 a[32:35], v60, s[48:51], s31 offen offset:1024\nQmaav 164,56,240\n"
"Qta 76,68,25344\nQmaav 164,60,248\nQmaav 168,56,84\nQmaav 168,60,116\nQta 78,68,26368\ns_waitcnt lgkmcnt(4)\nQmvav 0,64,80\nbuffer_load_dwordx4 a[36:39], v60, s[48:51], s31 offen offset:2048\n"
"Qmvav 0,68,88\nQmvav 4,64,112\nQmvav 4,68,120\nQmvav 8,64,144\nQta 56,68,32768\nQmvav 8,68,152\nbuffer_load_dwordx4 a[40:43], v60, s[48:51], s31 offen offset:3072\nQmvav 12,64,176\n"
"Qmvav 12,68,184\nQta 58,68,33280\nQmvav 16,64,208\ns_mov_b32 s31, 391168\nQmvav 16,68,216\nbuffer_load_dwordx4 a[44:47], v60, s[48:51], s31 offen offset:0\nQmvav 20,64,240\nQta 60,68,33792\n"
"v_add_u32 v75, 1, v75\nQmvav 20,68,248\ns_waitcnt vmcnt(42)\nds_write_b32 v73, v75\nQmvav 24,64,84\nQmvav 24,68,116\nQta 62,68,34304\ns_waitcnt lgkmcnt(5)\n"
"Qmvav 28,72,80\nbuffer_load_dwordx4 a[48:51], v60, s[48:51], s31 offen offset:1024\nQmvav 28,76,88\nQmvav 32,72,112\nQmvav 32,76,120\ns_waitcnt vmcnt(13)\nv_sub_u32 v66, v78, v65\nv_cmp_gt_u32 vcc, 2, v66\n"
"s_and_b64 vcc, vcc, s[28:29]\ns_cbranch_scc1 .Lf2_w\n.Lf2_r:\nQmvav 36,72,144\nQta 64,68,33024\nQmvav 36,76,152\nds_read_b32 v66, v74\nQmvav 40,72,176\n"
"buffer_load_dwordx4 a[52:55], v60, s[48:51], s31 offen offset:2048\nQmvav 40,76,184\nQta 66,68,33536\nQmvav 44,72,208\nQmvav 44,76,216\ns_waitcnt lgkmcnt(1)\nv_cmp_gt_u32 vcc, v75, v66\nQmvav 48,72,240\n"
"s_cbranch_vccnz .Lso1160\n.Lro1160:\nQmvav 48,76,248\nQta 68,68,34048\ns_add_u32 s31, s74, 0\nQmvav 52,72,84\nbuffer_load_dwordx4 a[80:83], v60, s[48:51], s31 offen offset:0 sc1\nQmvav 52,76,116\n"
"s_waitcnt lgkmcnt(4)\nQmaav 172,56,80\nQta 70,68,34560\nQmaav 172,60,88\nQmaav 176,56,112\nbuffer_load_dwordx4 a[84:87], v60, s[48:51], s31 offen offset:1024 sc1\nQmaav 176,60,120\nQmaav 180,56,144\n"
"Qta 72,68,40960\nQmaav 180,60,152\nQmaav 184,56,176\nbuffer_load_dwordx4 a[88:91], v60, s[48:51], s31 offen offset:2048 sc1\nQmaav 184,60,184\nQta 74,68,41984\nQmaav 188,56,208\nQmaav 188,60,216\n"
"Qmaav 192,56,240\nQta 76,68,49152\nQmaav 192,60,248\nbuffer_load_dwordx4 a[92:95], v60, s[48:51], s31 offen offset:3072 sc1\nQmaav 196,56,84\nQmaav 196,60,116\nQta 78,68,50176\ns_waitcnt lgkmcnt(4)\n"
"Qmvav 92,64,80\ns_add_u32 s31, s74, 4096\nQmvav 92,68,88\nbuffer_load_dwordx4 a[96:99], v60, s[48:51], s31 offen offset:0 sc1\nQmvav 124,64,112\nQmvav 124,68,120\nQmvav 156,64,144\nQta 56,68,41216\n"
"Qmvav 156,68,152\nbuffer_load_dwordx4 a[100:103], v60, s[48:51], s31 offen offset:1024 sc1\nQmvav 188,64,176\nQmvav 188,68,184\nQta 58,68,42240\nQmvav 220,64,208\nQmvav 220,68,216\nbuffer_load_dwordx4 a[104:107], v60, s[48:51], s31 offen offset:2048 sc1\n"
"Qmvav 252,64,240\nQta 60,68,49408\nQmvav 252,68,248\nQmvav 100,64,84\nQmvav 100,68,116\nQta 62,68,50432\ns_add_u32 s31, s74, 14336\ns_waitcnt lgkmcnt(4)\n"
"Qmaav 200,72,80\nbuffer_load_dwordx4 a[108:111], v60, s[48:51], s31 offen offset:0 sc1\nQmaav 200,76,88\nQmaav 204,72,112\nQmaav 204,76,120\nQmaav 208,72,144\nQta 64,68,41472\nQmaav 208,76,152\n"
"buffer_load_dwordx4 a[112:115], v60, s[48:51], s31 offen offset:1024 sc1\nQmaav 212,72,176\nQmaav 212,76,184\nQta 66,68,42496\nQmaav 216,72,208\nQmaav 216,76,216\nbuffer_load_dwordx4 v[168:171], v60, s[48:51], s31 offen offset:2048 sc1\nQmaav 220,72,240\n"
"Qta 68,68,49664\nQmaav 220,76,248\nQmaav 224,72,84\nQmaav 224,76,116\nQta 70,68,50688\ns_waitcnt lgkmcnt(4)\nQmaav 228,56,80\nbuffer_load_dwordx4 v[200:203], v60, s[48:51], s31 offen offset:3072 sc1\n"
"Qmaav 228,60,88\nQmaav 232,56,112\nQmaav 232,60,120\nQmaav 236,56,144\nQta 72,68,41728\ns_add_u32 s31, s74, 18432\nQmaav 236,60,152\nbuffer_load_dwordx4 v[232:235], v60, s[48:51], s31 offen offset:0 sc1\n"
"Qmaav 240,56,176\nQmaav 240,60,184\nQta 74,68,42752\nQmaav 244,56,208\nQmaav 244,60,216\nbuffer_load_dwordx4 v[212:215], v60, s[48:51], s31 offen offset:1024 sc1\nQmaav 248,56,240\nQta 76,68,49920\n"
"Qmaav 248,60,248\nQmaav 252,56,84\nQmaav 252,60,116\nQta 78,68,50944\ns_waitcnt lgkmcnt(4)\nQmvav 132,64,80\nbuffer_load_dwordx4 v[244:247], v60, s[48:51], s31 offen offset:2048 sc1\nQmvav 132,68,88\n"
"Qmvav 164,64,112\nQmvav 164,68,120\nQmvav 196,64,144\nQta 56,68,57344\ns_add_u32 m0, s96, 114688\ns_add_u32 s95, s72, 4096\nQmvav 196,68,152\nbuffer_load_dwordx4 v61, s[52:55], s95 offen lds\n"
"Qmvav 228,64,176\nQmvav 228,68,184\nQta 58,68,58368\nQmvav 108,64,208\ns_add_u32 s95, s72, 23552\nQmvav 108,68,216\nbuffer_load_dwordx4 v61, s[52:55], s95 offen offset:1024 lds\nQmvav 140,64,240\n"
"Qta 60,70,1024\nQmvav 140,68,248\nQmvav 172,64,84\nQmvav 172,68,116\nQta 62,70,2048\ns_add_u32 m0, s96, 122880\ns_add_u32 s95, s72, 45056\ns_waitcnt lgkmcnt(4)\n"
"Qmvav 204,72,80\nbuffer_load_dwordx4 v61, s[52:55], s95 offen lds\nQmvav 204,76,88\nQmvav 236,72,112\nQmvav 236,76,120\nQmvav 96,72,144\nQta 64,68,57600\ns_add_u32 s95, s72, 64512\n"
"Qmvav 96,76,152\nbuffer_load_dwordx4 v61, s[52:55], s95 offen offset:1024 lds\nQmvav 128,72,176\nQmvav 128,76,184\nQta 66,68,58624\nQmvav 160,72,208\ns_add_u32 m0, s96, 0\ns_add_u32 s95, s72, 4096\n"
"Qmvav 160,76,216\nbuffer_load_dwordx4 v62, s[52:55], s95 offen lds\nQmvav 192,72,240\nQta 68,70,1280\nQmvav 192,76,248\nQmvav 224,72,84\nQmvav 224,76,116\nQta 70,70,2304\n"
"s_add_u32 s95, s72, 44032\ns_waitcnt vmcnt(19)\ns_waitcnt lgkmcnt(4)\nQmaaa 116,56,0\nbuffer_load_dwordx4 v62, s[52:55], s95 offen offset:1024 lds\nQmaaa 116,60,28\nQmaaa 120,56,4\nQmaaa 120,60,32\n"
"Qmaaa 124,56,8\nQta 72,68,57856\ns_add_u32 m0, s96, 8192\ns_add_u32 s95, s72, 4096\nQmaaa 124,60,36\nbuffer_load_dwordx4 v63, s[52:55], s95 offen lds\nQmaaa 128,56,12\nQmaaa 128,60,40\n"
"Qta 74,68,58880\nQmaaa 132,56,16\ns_add_u32 s95, s72, 23552\nQmaaa 132,60,44\nbuffer_load_dwordx4 v63, s[52:55], s95 offen offset:1024 lds\nQmaaa 136,56,20\nQta 76,70,1536\nQmaaa 136,60,48\n"
"Qmaaa 140,56,24\nQmaaa 140,60,52\nQta 78,70,2560\ns_add_u32 s31, s75, 0\ns_waitcnt lgkmcnt(4)\nQmaaa 144,64,0\nbuffer_load_dwordx4 v[80:83], v60, s[48:51], s31 offen offset:0 sc1\nQmaaa 144,68,28\n"
"Qmaaa 148,64,4\nQmaaa 148,68,32\nQmaaa 152,64,8\nQta 56,68,58112\nQmaaa 152,68,36\nbuffer_load_dwordx4 v[112:115], v60, s[48:51], s31 offen offset:1024 sc1\nQmaaa 156,64,12\nQmaaa 156,68,40\n"
"Qta 58,68,59136\nQmaaa 160,64,16\nQmaaa 160,68,44\nbuffer_load_dwordx4 v[144:147], v60, s[48:51], s31 offen offset:2048 sc1\nQmaaa 164,64,20\nQta 60,70,1792\nQmaaa 164,68,48\nQmaaa 168,64,24\n"
"Qmaaa 168,68,52\nQta 62,70,2816\ns_waitcnt lgkmcnt(4)\nQmvaa 0,72,0\nbuffer_load_dwordx4 v[176:179], v60, s[48:51], s31 offen offset:3072 sc1\nQmvaa 0,76,28\nQmvaa 4,72,4\nQmvaa 4,76,32\n"
"Qmvaa 8,72,8\nQta 64,70,9216\ns_add_u32 s31, s75, 4096\nQmvaa 8,76,36\nbuffer_load_dwordx4 v[208:211], v60, s[48:51], s31 offen offset:0 sc1\nQmvaa 12,72,12\nQmvaa 12,76,40\nQta 66,70,9728\n"
"Qmvaa 16,72,16\nQmvaa 16,76,44\nbuffer_load_dwordx4 v[240:243], v60, s[48:51], s31 offen offset:1024 sc1\nQmvaa 20,72,20\nQta 68,70,10240\nv_add_u32 v75, 1, v75\nQmvaa 20,76,48\ns_waitcnt vmcnt(47)\n"
"ds_write_b32 v73, v75\nQmvaa 24,72,24\nQmvaa 24,76,52\nQta 70,70,10752\ns_waitcnt lgkmcnt(5)\nQmvaa 28,56,0\nbuffer_load_dwordx4 v[84:87], v60, s[48:51], s31 offen offset:2048 sc1\nQmvaa 28,60,28\n"
"Qmvaa 32,56,4\nQmvaa 32,60,32\nQmvaa 36,56,8\nQta 72,70,9472\nQmvaa 36,60,36\nds_read_b32 v66, v74\ns_add_u32 s31, s75, 14336\nQmvaa 40,56,12\n"
"buffer_load_dwordx4 v[88:91], v60, s[48:51], s31 offen offset:0 sc1\nQmvaa 40,60,40\nQta 74,70,9984\nQmvaa 44,56,16\nv_add_u32 v67, 3, v65\nQmvaa 44,60,44\ns_waitcnt lgkmcnt(1)\nv_cmp_gt_u32 vcc, v75, v66\n"
"Qmvaa 48,56,20\ns_cbranch_vccnz .Lso1300\n.Lro1300:\nQmvaa 48,60,48\nQta 76,70,10496\nQmvaa 52,56,24\nbuffer_load_dwordx4 v[120:123], v60, s[48:51], s31 offen offset:1024 sc1\nQmvaa 52,60,52\n"
"buffer_store_dword v67, v77, s[24:27], 0 offen sc1\ns_waitcnt lgkmcnt(4)\nQmaaa 172,64,0\nQta 78,70,11008\nQmaaa 172,68,28\nQmaaa 176,64,4\nbuffer_load_dwordx4 v[152:155], v60, s[48:51], s31 offen offset:2048 sc1\nQmaaa 176,68,32\n"
"Qmaaa 180,64,8\nQta 56,70,17408\nQmaaa 180,68,36\nQmaaa 184,64,12\nbuffer_load_dwordx4 v[184:187], v60, s[48:51], s31 offen offset:3072 sc1\nQmaaa 184,68,40\nQta 58,70,18432\nQmaaa 188,64,16\n"
"Qmaaa 188,68,44\nQmaaa 192,64,20\nQta 60,69,0\ns_add_u32 s31, s75, 18432\nQmaaa 192,68,48\nbuffer_load_dwordx4 v[216:219], v60, s[48:51], s31 offen offset:0 sc1\nQmaaa 196,64,24\nQmaaa 196,68,52\n"
"Qta 62,69,1024\ns_waitcnt lgkmcnt(4)\nQmvaa 92,72,0\nQmvaa 92,76,28\nbuffer_load_dwordx4 v[248:251], v60, s[48:51], s31 offen offset:1024 sc1\nQmvaa 124,72,4\nQmvaa 124,76,32\nQmvaa 156,72,8\n"
"Qta 64,70,17664\nQmvaa 156,76,36\nbuffer_load_dwordx4 v[116:119], v60, s[48:51], s31 offen offset:2048 sc1\nQmvaa 188,72,12\nQmvaa 188,76,40\nQta 66,70,18688\nQmvaa 220,72,16\ns_add_u32 m0, s96, 16384\n"
"s_add_u32 s95, s72, 45056\nQmvaa 220,76,44\nbuffer_load_dwordx4 v63, s[52:55], s95 offen lds\nQmvaa 252,72,20\nQta 68,69,256\nQmvaa 252,76,48\nQmvaa 100,72,24\nQmvaa 100,76,52\n"
"Qta 70,69,1280\ns_add_u32 s95, s72, 64512\ns_waitcnt lgkmcnt(4)\nQmaaa 200,56,0\nbuffer_load_dwordx4 v63, s[52:55], s95 offen offset:1024 lds\nQmaaa 200,60,28\nQmaaa 204,56,4\nQmaaa 204,60,32\n"
"Qmaaa 208,56,8\nQta 72,70,17920\ns_add_u32 m0, s96, 24576\ns_add_u32 s95, s73, 4096\nQmaaa 208,60,36\nbuffer_load_dwordx4 v61, s[52:55], s95 offen lds\nQmaaa 212,56,12\nQmaaa 212,60,40\n"
"Qta 74,70,18944\nQmaaa 216,56,16\ns_add_u32 s95, s73, 23552\nQmaaa 216,60,44\nbuffer_load_dwordx4 v61, s[52:55], s95 offen offset:1024 lds\nQmaaa 220,56,20\nQta 76,69,512\nQmaaa 220,60,48\n"
"Qmaaa 224,56,24\nQmaaa 224,60,52\nQta 78,69,1536\ns_add_u32 m0, s96, 32768\ns_add_u32 s95, s73, 45056\ns_waitcnt lgkmcnt(4)\nQmaaa 228,64,0\nbuffer_load_dwordx4 v61, s[52:55], s95 offen lds\n"
"Qmaaa 228,68,28\nQmaaa 232,64,4\nQmaaa 232,68,32\nQmaaa 236,64,8\nQta 56,70,18176\ns_add_u32 s95, s73, 64512\nQmaaa 236,68,36\nbuffer_load_dwordx4 v61, s[52:55], s95 offen offset:1024 lds\n"
"Qmaaa 240,64,12\nQmaaa 240,68,40\nQta 58,70,19200\nQmaaa 244,64,16\ns_add_u32 m0, s96, 40960\ns_add_u32 s95, s73, 4096\nQmaaa 244,68,44\nbuffer_load_dwordx4 v62, s[52:55], s95 offen lds\n"
"Qmaaa 248,64,20\nQta 60,69,768\nQmaaa 248,68,48\nQmaaa 252,64,24\nQmaaa 252,68,52\nQta 62,69,1792\ns_add_u32 s95, s73, 44032\ns_waitcnt lgkmcnt(4)\n"
"Qmvaa 132,72,0\nbuffer_load_dwordx4 v62, s[52:55], s95 offen offset:1024 lds\nQmvaa 132,76,28\nQmvaa 164,72,4\nQmvaa 164,76,32\nQmvaa 196,72,8\nQta 64,69,8192\ns_add_u32 m0, s96, 49152\n"
"s_add_u32 s95, s73, 4096\nQmvaa 196,76,36\nbuffer_load_dwordx4 v63, s[52:55], s95 offen lds\nQmvaa 228,72,12\nQmvaa 228,76,40\nQta 66,69,9216\nQmvaa 108,72,16\ns_add_u32 s95, s73, 23552\n"
"Qmvaa 108,76,44\nbuffer_load_dwordx4 v63, s[52:55], s95 offen offset:1024 lds\nQmvaa 140,72,20\nQta 68,69,16384\nQmvaa 140,76,48\nQmvaa 172,72,24\nQmvaa 172,76,52\nQta 70,69,17408\n"
"s_add_u32 m0, s96, 57344\ns_add_u32 s95, s73, 45056\ns_waitcnt lgkmcnt(4)\nQmvaa 204,56,0\nbuffer_load_dwordx4 v63, s[52:55], s95 offen lds\nQmvaa 204,60,28\nQmvaa 236,56,4\nQmvaa 236,60,32\n"
"Qmvaa 96,56,8\nQta 72,69,8448\ns_add_u32 s95, s73, 64512\nQmvaa 96,60,36\nbuffer_load_dwordx4 v63, s[52:55], s95 offen offset:1024 lds\nQmvaa 128,56,12\nQmvaa 128,60,40\nQta 74,69,9472\n"
"Qmvaa 160,56,16\ns_add_u32 s31, s77, 0\nQmvaa 160,60,44\nbuffer_load_dwordx4 a[0:3], v60, s[48:51], s31 offen offset:0 sc1\nQmvaa 192,56,20\nQta 76,69,16640\nQmvaa 192,60,48\nQmvaa 224,56,24\n"
"Qmvaa 224,60,52\nQta 78,69,17664\ns_waitcnt vmcnt(36)\ns_waitcnt lgkmcnt(4)\nQmaaa 116,64,80\nbuffer_load_dwordx4 a[4:7], v60, s[48:51], s31 offen offset:1024 sc1\nQmaaa 116,68,108\nQmaaa 120,64,84\n"
"Qmaaa 120,68,112\nQmaaa 124,64,88\nQta 56,69,8704\nQmaav 124,68,168\nbuffer_load_dwordx4 a[8:11], v60, s[48:51], s31 offen offset:2048 sc1\nQmaaa 128,64,92\nQmaav 128,68,200\nQta 58,69,9728\n"
"Qmaaa 132,64,96\nQmaav 132,68,232\nbuffer_load_dwordx4 a[12:15], v60, s[48:51], s31 offen offset:3072 sc1\nQmaaa 136,64,100\nQta 60,69,16896\nQmaav 136,68,212\nQmaaa 140,64,104\nQmaav 140,68,244\n"
"Qta 62,69,17920\ns_add_u32 s31, s77, 4096\ns_waitcnt lgkmcnt(4)\nQmaaa 144,72,80\nbuffer_load_dwordx4 a[16:19], v60, s[48:51], s31 offen offset:0 sc1\nQmaaa 144,76,108\nQmaaa 148,72,84\nQmaaa 148,76,112\n"
"Qmaaa 152,72,88\nQta 64,69,8960\nQmaav 152,76,168\nbuffer_load_dwordx4 a[20:23], v60, s[48:51], s31 offen offset:1024 sc1\nQmaaa 156,72,92\nQmaav 156,76,200\nQta 66,69,9984\nQmaaa 160,72,96\n"
"Qmaav 160,76,232\nbuffer_load_dwordx4 a[24:27], v60, s[48:51], s31 offen offset:2048 sc1\nQmaaa 164,72,100\nQta 68,69,17152\nQmaav 164,76,212\nQmaaa 168,72,104\nQmaav 168,76,244\nQta 70,69,18176\n"
"s_add_u32 s31, s77, 14336\ns_waitcnt lgkmcnt(4)\nQmvaa 0,56,80\nbuffer_load_dwordx4 a[28:31], v60, s[48:51], s31 offen offset:0 sc1\nQmvaa 0,60,108\nQmvaa 4,56,84\nQmvaa 4,60,112\nQmvaa 8,56,88\n"
".Lpg18:\ns_cbranch_execz .Lpg22\nQta 72,69,24576\nQmvav 8,60,168\nbuffer_load_dwordx4 a[32:35], v60, s[48:51], s31 offen offset:1024 sc1\nQmvaa 12,56,92\nQmvav 12,60,200\nQta 74,69,25088\n"
"Qmvaa 16,56,96\nQmvav 16,60,232\nbuffer_load_dwordx4 a[36:39], v60, s[48:51], s31 offen offset:2048 sc1\nQmvaa 20,56,100\nQta 76,69,25600\nv_add_u32 v75, 1, v75\nQmvav 20,60,212\ns_waitcnt vmcnt(63)\n"
"ds_write_b32 v73, v75\nQmvaa 24,56,104\nQmvav 24,60,244\nQta 78,69,26112\ns_waitcnt lgkmcnt(5)\nQmvaa 28,64,80\nbuffer_load_dwordx4 a[40:43], v60, s[48:51], s31 offen offset:3072 sc1\nQmvaa 28,68,108\n"
"Qmvaa 32,64,84\nQmvaa 32,68,112\nQmvaa 36,64,88\nQta 56,69,24832\nQmvav 36,68,168\nds_read_b32 v66, v74\ns_add_u32 s31, s77, 18432\nQmvaa 40,64,92\n"
"buffer_load_dwordx4 a[44:47], v60, s[48:51], s31 offen offset:0 sc1\nQmvav 40,68,200\nQta 58,69,25344\nQmvaa 44,64,96\nQmvav 44,68,232\ns_waitcnt lgkmcnt(1)\nv_cmp_gt_u32 vcc, v75, v66\nQmvaa 48,64,100\n"
"s_cbranch_vccnz .Lso1440\n.Lro1440:\nQmvav 48,68,212\nQta 60,69,25856\nv_add_u32 v75, 1, v75\nQmvaa 52,64,104\ns_waitcnt vmcnt(43)\nds_write_b32 v73, v75\n"
"Qmvav 52,68,244\nbuffer_load_dwordx4 a[48:51], v60, s[48:51], s31 offen offset:1024 sc1\ns_waitcnt lgkmcnt(5)\nQmaaa 172,72,80\nQta 62,69,26368\nQmaaa 172,76,108\nQmaaa 176,72,84\nQmaaa 176,76,112\n"
"buffer_load_dwordx4 a[52:55], v60, s[48:51], s31 offen offset:2048 sc1\nQmaaa 180,72,88\nQta 64,69,32768\nQmaav 180,76,168\nQmaaa 184,72,92\nds_read_b32 v66, v74\nQmaav 184,76,200\nQta 66,69,33792\n"
"Qmaaa 188,72,96\nbuffer_load_dword v78, v76, s[24:27], 0 offen sc1\nQmaav 188,76,232\nQmaaa 192,72,100\ns_waitcnt lgkmcnt(1)\nv_cmp_gt_u32 vcc, v75, v66\nQmaav 192,76,212\ns_cbranch_vccnz .Lso1455\n"
".Lro1455:\nQmaaa 196,72,104\nQta 68,69,40960\nQmaav 196,76,244\ns_waitcnt lgkmcnt(4)\nQmvaa 92,56,80\nQmvaa 92,60,108\nQta 70,69,41984\n"
"Qmvaa 124,56,84\nQmvaa 124,60,112\nQmvaa 156,56,88\nQta 72,69,33024\nQmvav 156,60,168\nQmvaa 188,56,92\nQmvav 188,60,200\nQta 74,69,34048\n"
"Qmvaa 220,56,96\nQmvav 220,60,232\nQmvaa 252,56,100\nQta 76,69,41216\nQmvav 252,60,212\nQmvaa 100,56,104\nQmvav 100,60,244\nQta 78,69,42240\n"
"s_waitcnt lgkmcnt(4)\nQmaaa 200,64,80\nQmaaa 200,68,108\nQmaaa 204,64,84\nQmaaa 204,68,112\nQmaaa 208,64,88\nQta 56,69,33280\nQmaav 208,68,168\n"
"Qmaaa 212,64,92\nQmaav 212,68,200\nQta 58,69,34304\nQmaaa 216,64,96\nQmaav 216,68,232\nQmaaa 220,64,100\nQta 60,69,41472\nQmaav 220,68,212\n"
"Qmaaa 224,64,104\nQmaav 224,68,244\nQta 62,69,42496\ns_waitcnt lgkmcnt(4)\nQmaaa 228,72,80\nQmaaa 228,76,108\nQmaaa 232,72,84\nQmaaa 232,76,112\n"
"Qmaaa 236,72,88\nQta 64,69,33536\nQmaav 236,76,168\nQmaaa 240,72,92\nQmaav 240,76,200\nQta 66,69,34560\nQmaaa 244,72,96\nQmaav 244,76,232\n"
"Qmaaa 248,72,100\nQta 68,69,41728\nQmaav 248,76,212\nQmaaa 252,72,104\nQmaav 252,76,244\nQta 70,69,42752\ns_waitcnt lgkmcnt(4)\nQmvaa 132,56,80\n"
"Qmvaa 132,60,108\nQmvaa 164,56,84\nQmvaa 164,60,112\nQmvaa 196,56,88\nQta 72,69,49152\nQmvav 196,60,168\nQmvaa 228,56,92\nQmvav 228,60,200\n"
"Qta 74,69,50176\nQmvaa 108,56,96\nQmvav 108,60,232\nQmvaa 140,56,100\nQta 76,69,57344\nQmvav 140,60,212\ns_waitcnt vmcnt(0)\nv_sub_u32 v66, v78, v65\n"
"v_cmp_gt_u32 vcc, 3, v66\ns_and_b64 vcc, vcc, s[28:29]\ns_cbranch_scc1 .Lf3_w\n.Lf3_r:\nv_add_u32 v75, 1, v75\nQmvaa 172,56,104\ns_waitcnt vmcnt(42)\nds_write_b32 v73, v75\n"
"Qmvav 172,60,244\nQta 78,69,58368\ns_waitcnt lgkmcnt(5)\nQmvaa 204,64,80\ns_add_u32 s31, s74, 28672\nQmvaa 204,68,108\nbuffer_load_dwordx4 a[80:83], v60, s[48:51], s31 offen offset:0 sc1\nQmvaa 236,64,84\n"
"Qmvaa 236,68,112\ns_bitcmp1_b32 %19, 2\ns_cbranch_scc1 .Lrole1\nQmvaa 96,64,88\nQta 56,69,49408\ns_lshl_b32 s46, %19, 4\nQmvav 96,68,168\nbuffer_load_dwordx4 a[84:87], v60, s[48:51], s31 offen offset:1024 sc1\n"
"Qmvaa 128,64,92\nds_read_b32 v66, v74\ns_add_u32 s46, s46, 128\nQmvav 128,68,200\nQta 58,69,50432\nv_mov_b32 v77, 0x7ffff000\nQmvaa 160,64,96\nQmvav 160,68,232\n"
"buffer_load_dwordx4 a[88:91], v60, s[48:51], s31 offen offset:2048 sc1\nQmvaa 192,64,100\ns_waitcnt lgkmcnt(1)\nv_cmp_gt_u32 vcc, v75, v66\nQmvav 192,68,212\ns_cbranch_vccnz .Lso1525r0\n.Lro1525r0:\nQmvaa 224,64,104\n"
"Qta 60,69,57600\nQmvav 224,68,244\nbuffer_load_dwordx4 a[92:95], v60, s[48:51], s31 offen offset:3072 sc1\ns_waitcnt vmcnt(31)\ns_waitcnt lgkmcnt(4)\nQmaav 116,72,80\nQmaav 116,76,88\nQta 62,69,58624\n"
"Qmaav 120,72,112\ns_add_u32 m0, s96, 65536\ns_add_u32 s95, s67, 86016\nQmaav 120,76,120\nbuffer_load_dwordx4 v63, s[52:55], s95 offen lds\nQmaav 124,72,144\nQta 64,69,49664\nQmaav 124,76,152\n"
"Qmaav 128,72,176\nQmaav 128,76,184\nQta 66,69,50688\ns_add_u32 s95, s67, 105472\nQmaav 132,72,208\nbuffer_load_dwordx4 v63, s[52:55], s95 offen offset:1024 lds\nQmaav 132,76,216\nQmaav 136,72,240\n"
"Qta 68,69,57856\nQmaav 136,76,248\ns_add_u32 m0, s96, 73728\ns_add_u32 s95, s67, 126976\nQmaav 140,72,84\nbuffer_load_dwordx4 v63, s[52:55], s95 offen lds\nQmaav 140,76,116\nQta 70,69,58880\n"
"s_waitcnt lgkmcnt(4)\nQmaav 144,56,80\nQmaav 144,60,88\ns_add_u32 s95, s67, 146432\nQmaav 148,56,112\nbuffer_load_dwordx4 v63, s[52:55], s95 offen offset:1024 lds\nQmaav 148,60,120\nQmaav 152,56,144\n"
"Qta 72,69,49920\nQmaav 152,60,152\ns_add_u32 m0, s96, 81920\ns_add_u32 s95, s72, 86016\nQmaav 156,56,176\nbuffer_load_dwordx4 v63, s[52:55], s95 offen lds\nQmaav 156,60,184\nQta 74,69,50944\n"
"Qmaav 160,56,208\nQmaav 160,60,216\nQmaav 164,56,240\nQta 76,69,58112\ns_add_u32 s95, s72, 105472\nQmaav 164,60,248\nbuffer_load_dwordx4 v63, s[52:55], s95 offen offset:1024 lds\nQmaav 168,56,84\n"
"Qmaav 168,60,116\nQta 78,69,59136\ns_waitcnt lgkmcnt(4)\nQmvav 0,64,80\ns_add_u32 m0, s96, 90112\ns_add_u32 s95, s72, 126976\nQmvav 0,68,88\nbuffer_load_dwordx4 v63, s[52:55], s95 offen lds\n"
"Qmvav 4,64,112\nQmvav 4,68,120\nQmvav 8,64,144\nQta 56,68,0\ns_add_u32 s95, s72, 146432\nQmvav 8,68,152\nbuffer_load_dwordx4 v63, s[52:55], s95 offen offset:1024 lds\nQmvav 12,64,176\n"
"Qmvav 12,68,184\nQta 58,68,512\nQmvav 16,64,208\ns_add_u32 s31, s74, 32768\nQmvav 16,68,216\nbuffer_load_dwordx4 a[96:99], v60, s[48:51], s31 offen offset:0 sc1\nQmvav 20,64,240\nQta 60,68,1024\n"
"Qmvav 20,68,248\nQmvav 24,64,84\nQmvav 24,68,116\nQta 62,68,1536\ns_waitcnt lgkmcnt(4)\nQmvav 28,72,80\nbuffer_load_dwordx4 a[100:103], v60, s[48:51], s31 offen offset:1024 sc1\nQmvav 28,76,88\n"
"Qmvav 32,72,112\nQmvav 32,76,120\nQmvav 36,72,144\nQta 64,68,256\nQmvav 36,76,152\nbuffer_load_dwordx4 a[104:107], v60, s[48:51], s31 offen offset:2048 sc1\nQmvav 40,72,176\nQmvav 40,76,184\n"
"Qta 66,68,768\nQmvav 44,72,208\ns_add_u32 s31, s74, 43008\nv_cmp_eq_u32 s[44:45], s46, v60\ns_nop 1\nv_cndmask_b32 v77, v77, v76, s[44:45]\nv_add_u32 v67, 1, v65\ns_xor_b32 s46, %19, 12\n"
"s_lshl_b32 s28, 1, s46\nQmvav 44,76,216\nbuffer_load_dwordx4 a[108:111], v60, s[48:51], s31 offen offset:0 sc1\nQmvav 48,72,240\nQta 68,68,1280\nQmvav 48,76,248\nv_add_u32 v75, 1, v75\nQmvav 52,72,84\n"
"s_waitcnt vmcnt(37)\nds_write_b32 v73, v75\nQmvav 52,76,116\nQta 70,68,1792\ns_waitcnt lgkmcnt(5)\nQmaav 172,56,80\nbuffer_load_dwordx4 a[112:115], v60, s[48:51], s31 offen offset:1024 sc1\nQmaav 172,60,88\n"
"Qmaav 176,56,112\nQmaav 176,60,120\nQmaav 180,56,144\nQta 72,68,8192\nQmaav 180,60,152\nbuffer_load_dwordx4 v[168:171], v60, s[48:51], s31 offen offset:2048 sc1\nQmaav 184,56,176\nds_read_b32 v66, v74\n"
"Qmaav 184,60,184\nQta 74,68,9216\nQmaav 188,56,208\nQmaav 188,60,216\nbuffer_load_dwordx4 v[200:203], v60, s[48:51], s31 offen offset:3072 sc1\nQmaav 192,56,240\ns_waitcnt lgkmcnt(1)\nv_cmp_gt_u32 vcc, v75, v66\n"
"Qmaav 192,60,248\ns_cbranch_vccnz .Lso1595r0\n.Lro1595r0:\nQmaav 196,56,84\nQta 76,68,16384\ns_add_u32 s31, s74, 47104\nQmaav 196,60,116\nbuffer_load_dwordx4 v[232:235], v60, s[48:51], s31 offen offset:0 sc1\n"
"s_waitcnt lgkmcnt(4)\nQmvav 92,64,80\nQmvav 92,68,88\nQta 78,68,17408\nQmvav 124,64,112\nQmvav 124,68,120\nbuffer_load_dwordx4 v[212:215], v60, s[48:51], s31 offen offset:1024 sc1\nQmvav 156,64,144\n"
"Qta 56,68,8448\nQmvav 156,68,152\nQmvav 188,64,176\nQmvav 188,68,184\nQta 58,68,9472\nQmvav 220,64,208\nbuffer_load_dwordx4 v[244:247], v60, s[48:51], s31 offen offset:2048 sc1\nQmvav 220,68,216\n"
"Qmvav 252,64,240\nQta 60,68,16640\nQmvav 252,68,248\ns_add_u32 m0, s91, 0\ns_add_u32 s31, s77, 28672\nQmvav 100,64,84\nbuffer_load_dwordx4 v60, s[48:51], s31 offen sc1 lds\nQmvav 100,68,116\n"
"Qta 62,68,17664\ns_waitcnt lgkmcnt(4)\nQmaav 200,72,80\nQmaav 200,76,88\nQmaav 204,72,112\nbuffer_load_dwordx4 v60, s[48:51], s31 offen offset:1024 sc1 lds\nQmaav 204,76,120\nQmaav 208,72,144\n"
"Qta 64,68,8704\nQmaav 208,76,152\nQmaav 212,72,176\nbuffer_load_dwordx4 v60, s[48:51], s31 offen offset:2048 sc1 lds\nQmaav 212,76,184\nQta 66,68,9728\nQmaav 216,72,208\nQmaav 216,76,216\n"
"Qmaav 220,72,240\nQta 68,68,16896\nQmaav 220,76,248\nbuffer_load_dwordx4 v60, s[48:51], s31 offen offset:3072 sc1 lds\nQmaav 224,72,84\nQmaav 224,76,116\nQta 70,68,17920\ns_waitcnt lgkmcnt(4)\n"
"Qmaav 228,56,80\ns_add_u32 m0, s91, 4096\ns_add_u32 s31, s77, 32768\nQmaav 228,60,88\nbuffer_load_dwordx4 v60, s[48:51], s31 offen sc1 lds\nQmaav 232,56,112\nQmaav 232,60,120\nQmaav 236,56,144\n"
"Qta 72,68,8960\nQmaav 236,60,152\nbuffer_load_dwordx4 v60, s[48:51], s31 offen offset:1024 sc1 lds\nQmaav 240,56,176\nQmaav 240,60,184\nQta 74,68,9984\nQmaav 244,56,208\nQmaav 244,60,216\n"
"buffer_load_dwordx4 v60, s[48:51], s31 offen offset:2048 sc1 lds\nQmaav 248,56,240\nQta 76,68,17152\nQmaav 248,60,248\nQmaav 252,56,84\nQmaav 252,60,116\nQta 78,68,18176\ns_add_u32 m0, s91, 33792\n"
"s_add_u32 s31, s77, 43008\ns_waitcnt lgkmcnt(4)\nQmvav 132,64,80\nbuffer_load_dwordx4 v60, s[48:51], s31 offen sc1 lds\nQmvav 132,68,88\nQmvav 164,64,112\nQmvav 164,68,120\nQmvav 196,64,144\n"
"Qta 56,68,24576\nQmvav 196,68,152\nbuffer_load_dwordx4 v60, s[48:51], s31 offen offset:1024 sc1 lds\nQmvav 228,64,176\nQmvav 228,68,184\nQta 58,68,25600\nQmvav 108,64,208\nQmvav 108,68,216\n"
"buffer_load_dwordx4 v60, s[48:51], s31 offen offset:2048 sc1 lds\nQmvav 140,64,240\nQta 60,68,32768\nQmvav 140,68,248\nv_add_u32 v75, 1, v75\nQmvav 172,64,84\ns_waitcnt vmcnt(49)\nds_write_b32 v73, v75\n"
"Qmvav 172,68,116\nQta 62,68,33792\ns_waitcnt lgkmcnt(5)\nQmvav 204,72,80\nbuffer_load_dwordx4 v60, s[48:51], s31 offen offset:3072 sc1 lds\nQmvav 204,76,88\nQmvav 236,72,112\nQmvav 236,76,120\n"
"Qmvav 96,72,144\nQta 64,68,24832\ns_add_u32 m0, s91, 37888\ns_add_u32 s31, s77, 47104\nQmvav 96,76,152\nbuffer_load_dwordx4 v60, s[48:51], s31 offen sc1 lds\nQmvav 128,72,176\nds_read_b32 v66, v74\n"
"Qmvav 128,76,184\nQta 66,68,25856\nQmvav 160,72,208\nQmvav 160,76,216\nbuffer_load_dwordx4 v60, s[48:51], s31 offen offset:1024 sc1 lds\nQmvav 192,72,240\ns_waitcnt lgkmcnt(1)\nv_cmp_gt_u32 vcc, v75, v66\n"
"Qmvav 192,76,248\ns_cbranch_vccnz .Lso1665r0\n.Lro1665r0:\nQmvav 224,72,84\nQta 68,68,33024\nQmvav 224,76,116\nbuffer_load_dwordx4 v60, s[48:51], s31 offen offset:2048 sc1 lds\ns_waitcnt vmcnt(37)\n"
"s_waitcnt lgkmcnt(4)\nQmaaa 116,56,0\nQmaaa 116,60,28\nQta 70,68,34048\nQmaaa 120,56,4\ns_add_u32 m0, s96, 0\ns_add_u32 s95, s73, 86016\nQmaaa 120,60,32\n"
"buffer_load_dwordx4 v63, s[52:55], s95 offen lds\nQmaaa 124,56,8\nQta 72,68,25088\nQmaaa 124,60,36\nQmaaa 128,56,12\nQmaaa 128,60,40\nQta 74,68,26112\ns_add_u32 s95, s73, 105472\n"
"Qmaaa 132,56,16\nbuffer_load_dwordx4 v63, s[52:55], s95 offen offset:1024 lds\nQmaaa 132,60,44\nQmaaa 136,56,20\nQta 76,68,33280\nQmaaa 136,60,48\ns_add_u32 m0, s96, 8192\ns_add_u32 s95, s73, 126976\n"
"Qmaaa 140,56,24\nbuffer_load_dwordx4 v63, s[52:55], s95 offen lds\nQmaaa 140,60,52\nQta 78,68,34304\ns_waitcnt lgkmcnt(4)\nQmaaa 144,64,0\nQmaaa 144,68,28\ns_add_u32 s95, s73, 146432\n"
"Qmaaa 148,64,4\nbuffer_load_dwordx4 v63, s[52:55], s95 offen offset:1024 lds\nQmaaa 148,68,32\nQmaaa 152,64,8\nQta 56,68,25344\nQmaaa 152,68,36\ns_add_u32 s31, s75, 28672\nQmaaa 156,64,12\n"
"buffer_load_dwordx4 v[80:83], v60, s[48:51], s31 offen offset:0 sc1\nQmaaa 156,68,40\nQta 58,68,26368\nQmaaa 160,64,16\nQmaaa 160,68,44\nQmaaa 164,64,20\nQta 60,68,33536\nQmaaa 164,68,48\n"
"buffer_load_dwordx4 v[112:115], v60, s[48:51], s31 offen offset:1024 sc1\nQmaaa 168,64,24\nQmaaa 168,68,52\nQta 62,68,34560\ns_waitcnt lgkmcnt(4)\nQmvaa 0,72,0\nQmvaa 0,76,28\nbuffer_load_dwordx4 v[144:147], v60, s[48:51], s31 offen offset:2048 sc1\n"
"Qmvaa 4,72,4\nQmvaa 4,76,32\nQmvaa 8,72,8\nQta 64,68,40960\nQmvaa 8,76,36\nbuffer_load_dwordx4 v[176:179], v60, s[48:51], s31 offen offset:3072 sc1\nQmvaa 12,72,12\nQmvaa 12,76,40\n"
"Qta 66,68,41472\nQmvaa 16,72,16\ns_add_u32 s31, s75, 32768\nQmvaa 16,76,44\nbuffer_load_dwordx4 v[208:211], v60, s[48:51], s31 offen offset:0 sc1\nQmvaa 20,72,20\nQta 68,68,41984\nQmvaa 20,76,48\n"
"Qmvaa 24,72,24\nQmvaa 24,76,52\nQta 70,68,42496\ns_waitcnt lgkmcnt(4)\nQmvaa 28,56,0\nbuffer_load_dwordx4 v[240:243], v60, s[48:51], s31 offen offset:1024 sc1\nQmvaa 28,60,28\nQmvaa 32,56,4\n"
"Qmvaa 32,60,32\nQmvaa 36,56,8\nQta 72,68,41216\nQmvaa 36,60,36\nbuffer_load_dwordx4 v[84:87], v60, s[48:51], s31 offen offset:2048 sc1\nQmvaa 40,56,12\nQmvaa 40,60,40\n.Lpg19:\n"
"s_cbranch_execz .Lpg23\nQta 74,68,41728\nQmvaa 44,56,16\ns_add_u32 s31, s75, 43008\nQmvaa 44,60,44\nbuffer_load_dwordx4 v[88:91], v60, s[48:51], s31 offen offset:0 sc1\nQmvaa 48,56,20\nQta 76,68,42240\n"
"Qmvaa 48,60,48\nv_add_u32 v75, 1, v75\nQmvaa 52,56,24\ns_waitcnt vmcnt(38)\nds_write_b32 v73, v75\nQmvaa 52,60,52\nQta 78,68,42752\ns_waitcnt lgkmcnt(5)\n"
"Qmaaa 172,64,0\nbuffer_load_dwordx4 v[120:123], v60, s[48:51], s31 offen offset:1024 sc1\nQmaaa 172,68,28\nQmaaa 176,64,4\nQmaaa 176,68,32\nQmaaa 180,64,8\nQta 56,68,49152\nQmaaa 180,68,36\n"
"buffer_load_dwordx4 v[152:155], v60, s[48:51], s31 offen offset:2048 sc1\nQmaaa 184,64,12\nds_read_b32 v66, v74\nQmaaa 184,68,40\nQta 58,68,50176\nQmaaa 188,64,16\nQmaaa 188,68,44\nbuffer_load_dwordx4 v[184:187], v60, s[48:51], s31 offen offset:3072 sc1\n"
"Qmaaa 192,64,20\ns_waitcnt lgkmcnt(1)\nv_cmp_gt_u32 vcc, v75, v66\nQmaaa 192,68,48\ns_cbranch_vccnz .Lso1735r0\n.Lro1735r0:\nQmaaa 196,64,24\nQta 60,68,57344\n"
"s_add_u32 s31, s75, 47104\nQmaaa 196,68,52\nbuffer_load_dwordx4 v[216:219], v60, s[48:51], s31 offen offset:0 sc1\ns_waitcnt lgkmcnt(4)\nQmvaa 92,72,0\nQmvaa 92,76,28\nQta 62,68,58368\nQmvaa 124,72,4\n"
"Qmvaa 124,76,32\nbuffer_load_dwordx4 v[248:251], v60, s[48:51], s31 offen offset:1024 sc1\nQmvaa 156,72,8\nQta 64,68,49408\nQmvaa 156,76,36\nQmvaa 188,72,12\nQmvaa 188,76,40\nQta 66,68,50432\n"
"Qmvaa 220,72,16\nbuffer_load_dwordx4 v[116:119], v60, s[48:51], s31 offen offset:2048 sc1\nQmvaa 220,76,44\nQmvaa 252,72,20\nQta 68,68,57600\nQmvaa 252,76,48\ns_add_u32 m0, s96, 16384\ns_add_u32 s95, s67, 86016\n"
"Qmvaa 100,72,24\nbuffer_load_dwordx4 v61, s[52:55], s95 offen lds\nQmvaa 100,76,52\nQta 70,68,58624\ns_waitcnt lgkmcnt(4)\nQmaaa 200,56,0\nQmaaa 200,60,28\ns_add_u32 s95, s67, 105472\n"
"Qmaaa 204,56,4\nbuffer_load_dwordx4 v61, s[52:55], s95 offen offset:1024 lds\nQmaaa 204,60,32\nQmaaa 208,56,8\nQta 72,68,49664\nQmaaa 208,60,36\ns_add_u32 m0, s96, 24576\ns_add_u32 s95, s67, 126976\n"
"Qmaaa 212,56,12\nbuffer_load_dwordx4 v61, s[52:55], s95 offen lds\nQmaaa 212,60,40\nQta 74,68,50688\nQmaaa 216,56,16\nQmaaa 216,60,44\nQmaaa 220,56,20\nQta 76,68,57856\n"
"s_add_u32 s95, s67, 146432\nQmaaa 220,60,48\nbuffer_load_dwordx4 v61, s[52:55], s95 offen offset:1024 lds\nQmaaa 224,56,24\nQmaaa 224,60,52\nQta 78,68,58880\ns_waitcnt lgkmcnt(4)\nQmaaa 228,64,0\n"
"s_add_u32 m0, s96, 32768\ns_add_u32 s95, s72, 86016\nQmaaa 228,68,28\nbuffer_load_dwordx4 v61, s[52:55], s95 offen lds\nQmaaa 232,64,4\nQmaaa 232,68,32\nQmaaa 236,64,8\nQta 56,68,49920\n"
"s_add_u32 s95, s72, 105472\nQmaaa 236,68,36\nbuffer_load_dwordx4 v61, s[52:55], s95 offen offset:1024 lds\nQmaaa 240,64,12\nQmaaa 240,68,40\nQta 58,68,50944\nQmaaa 244,64,16\nQmaaa 244,68,44\n"
"Qmaaa 248,64,20\nQta 60,68,58112\nQmaaa 248,68,48\nQmaaa 252,64,24\nQmaaa 252,68,52\nQta 62,68,59136\ns_waitcnt lgkmcnt(4)\nQmvaa 132,72,0\n"
"Qmvaa 132,76,28\nQmvaa 164,72,4\nQmvaa 164,76,32\nQmvaa 196,72,8\nQta 64,69,0\nQmvaa 196,76,36\nQmvaa 228,72,12\nQmvaa 228,76,40\n"
"Qta 66,69,1024\nQmvaa 108,72,16\nQmvaa 108,76,44\nQmvaa 140,72,20\nQta 68,69,8192\nQmvaa 140,76,48\nv_add_u32 v75, 1, v75\nQmvaa 172,72,24\n"
"s_waitcnt vmcnt(20)\nds_write_b32 v73, v75\nQmvaa 172,76,52\nQta 70,69,9216\ns_waitcnt lgkmcnt(5)\nQmvaa 204,56,0\ns_waitcnt vmcnt(24)\nQda 0,58,0\n"
"Qmvaa 204,60,28\nQmvaa 236,56,4\nQda 28,58,33792\nQmvaa 236,60,32\nQmvaa 96,56,8\nQta 72,69,16384\nQmvaa 96,60,36\nQda 4,58,1024\n"
"Qmvaa 128,56,12\nds_read_b32 v66, v74\nQmvaa 128,60,40\nQta 74,69,17408\nQmvaa 160,56,16\nQda 32,58,34816\nQmvaa 160,60,44\nQmvaa 192,56,20\n"
"s_waitcnt lgkmcnt(2)\nv_cmp_gt_u32 vcc, v75, v66\nQmvaa 192,60,48\ns_cbranch_vccnz .Lso1805r0\n.Lro1805r0:\nQmvaa 224,56,24\nQta 76,69,24576\ns_add_u32 m0, s96, 40960\n"
"s_add_u32 s95, s72, 126976\nQmvaa 224,60,52\nbuffer_load_dwordx4 v61, s[52:55], s95 offen lds\ns_waitcnt vmcnt(39)\ns_waitcnt lgkmcnt(8)\nQmaaa 200,64,80\nQda 8,58,2048\nQmaaa 200,68,108\n"
"Qta 78,69,25600\nQmaaa 204,64,84\nQda 36,58,35840\ns_add_u32 s95, s72, 146432\nQmaaa 204,68,112\nbuffer_load_dwordx4 v61, s[52:55], s95 offen offset:1024 lds\nQmaaa 208,64,88\nQta 56,68,0\n"
"Qmaav 208,68,168\nQda 12,58,3072\nQmaaa 212,64,92\nQmaav 212,68,200\nQta 58,68,1024\ns_add_u32 m0, s96, 49152\ns_add_u32 s95, s73, 86016\nQmaaa 216,64,96\n"
"buffer_load_dwordx4 v61, s[52:55], s95 offen lds\nQmaav 216,68,232\nQda 40,58,36864\nQmaaa 220,64,100\nQta 60,68,8192\nQmaav 220,68,212\nQda 16,58,4096\ns_add_u32 s95, s73, 105472\n"
"Qmaaa 224,64,104\nbuffer_load_dwordx4 v61, s[52:55], s95 offen offset:1024 lds\nQmaav 224,68,244\nQta 62,68,9216\ns_waitcnt vmcnt(10)\ns_waitcnt lgkmcnt(8)\nQmaav 200,72,80\nQda 44,58,37888\n"
"Qmaav 200,76,88\ns_add_u32 m0, s96, 57344\ns_add_u32 s95, s73, 126976\nQmaav 204,72,112\nbuffer_load_dwordx4 v61, s[52:55], s95 offen lds\nQmaav 204,76,120\nQda 20,58,5120\nQmaav 208,72,144\n"
"Qta 64,69,256\nQmaav 208,76,152\nQda 48,58,38912\ns_add_u32 s95, s73, 146432\nQmaav 212,72,176\nbuffer_load_dwordx4 v61, s[52:55], s95 offen offset:1024 lds\nQmaav 212,76,184\nQta 66,69,1280\n"
"Qmaav 216,72,208\nQda 24,58,6144\nQmaav 216,76,216\nQmaav 220,72,240\nQta 68,69,8448\nQmaav 220,76,248\nQda 52,58,39936\nQmaav 224,72,84\n"
"Qmaav 224,76,116\nQta 70,69,9472\ns_waitcnt lgkmcnt(9)\nQmaaa 200,56,0\nQmaaa 200,60,28\nQmaaa 204,56,4\nQmaaa 204,60,32\ns_waitcnt lgkmcnt(9)\n"
"Qmaaa 208,56,8\nQta 72,69,16640\nQmaaa 208,60,36\ns_nop 1\nds_write_b128 v58, a[200:203] offset:0\nQmaaa 212,56,12\nQmaaa 212,60,40\nQta 74,69,17664\n"
"s_waitcnt lgkmcnt(4)\nQmaaa 216,56,16\nds_write_b128 v58, a[204:207] offset:1024\nQmaaa 216,60,44\nQmaaa 220,56,20\nQta 76,69,24832\nQmaaa 220,60,48\nds_write_b128 v58, a[208:211] offset:2048\n"
"Qmaaa 224,56,24\nQmaaa 224,60,52\nQta 78,69,25856\ns_waitcnt lgkmcnt(7)\nQmaaa 228,64,80\nds_write_b128 v58, a[212:215] offset:3072\nQmaaa 228,68,108\nQmaaa 232,64,84\n"
"Qmaaa 232,68,112\nds_write_b128 v58, a[216:219] offset:4096\nQmaaa 236,64,88\nQta 56,68,256\nQmaav 236,68,168\nQmaaa 240,64,92\nds_write_b128 v58, a[220:223] offset:5120\nQmaav 240,68,200\n"
"Qta 58,68,1280\nQmaaa 244,64,96\nQmaav 244,68,232\nds_write_b128 v58, a[224:227] offset:6144\nQmaaa 248,64,100\nQta 60,68,8448\nQmaav 248,68,212\nQmaaa 252,64,104\n"
"Qmaav 252,68,244\nQta 62,68,9472\ns_waitcnt lgkmcnt(8)\nQmaav 228,72,80\nv_add_u32 v75, 1, v75\nQmaav 228,76,88\nds_write_b32 v73, v75\nQmaav 232,72,112\n"
"Qmaav 232,76,120\nQmaav 236,72,144\nQta 64,69,512\nQmaav 236,76,152\nQmaav 240,72,176\nQmaav 240,76,184\nQta 66,69,1536\nQmaav 244,72,208\n"
"Qmaav 244,76,216\nds_read_b32 v66, v74\nQmaav 248,72,240\nQta 68,69,8704\nQmaav 248,76,248\nQmaav 252,72,84\nQmaav 252,76,116\ns_waitcnt lgkmcnt(1)\n"
"v_cmp_gt_u32 vcc, v75, v66\ns_waitcnt lgkmcnt(5)\nQmaaa 228,56,0\ns_cbranch_vccnz .Lso1878r0\n.Lro1878r0:\nQmaaa 228,60,28\nQta 70,69,9728\nQmaaa 232,56,4\n"
"Qmaaa 232,60,32\nQdv 148,59,2048\nQmaaa 236,56,8\nQta 72,69,16896\nQmaaa 236,60,36\ns_nop 0\nds_write_b128 v58, a[228:231] offset:33792\nQmaaa 240,56,12\n"
"Qdv 180,59,3072\nQmaaa 240,60,40\nQta 74,69,17920\nQmaaa 244,56,16\nds_write_b128 v58, a[232:235] offset:34816\nQmaaa 244,60,44\nQdv 104,59,5120\nQmaaa 248,56,20\n"
"Qta 76,69,25088\nQmaaa 248,60,48\nds_write_b128 v58, a[236:239] offset:35840\nQmaaa 252,56,24\nQdv 136,59,0\ns_waitcnt lgkmcnt(9)\nQa 196,196,148\nQmaaa 252,60,52\n"
"Qta 78,69,26112\nQa 197,197,149\ns_waitcnt lgkmcnt(10)\nQmvaa 132,64,80\nds_write_b128 v58, a[240:243] offset:36864\nQmvaa 132,68,108\nQa 198,198,150\nQa 199,199,151\n"
"Qmvaa 164,64,84\ns_waitcnt lgkmcnt(8)\nQa 228,228,180\nQa 229,229,181\nQmvaa 164,68,112\nds_write_b128 v58, a[244:247] offset:37888\nQmvaa 196,64,88\nQta 56,68,512\n"
"Qa 230,230,182\nQa 231,231,183\nQmvav 196,68,168\nQdv 148,59,4096\ns_waitcnt lgkmcnt(8)\nQa 140,140,104\nQmvaa 228,64,92\nds_write_b128 v58, a[248:251] offset:38912\n"
"Qmvav 228,68,200\nQta 58,68,1536\nQa 141,141,105\nQmvaa 108,64,96\nQa 142,142,106\nQa 143,143,107\nQmvav 108,68,232\nQdv 180,59,6144\n"
"s_waitcnt lgkmcnt(8)\nQa 132,132,136\nQmvaa 140,64,100\nQta 60,68,8704\nQa 133,133,137\nQmvav 140,68,212\nds_write_b128 v58, a[252:255] offset:39936\nQmvaa 172,64,104\n"
"Qa 134,134,138\nQa 135,135,139\nQmvav 172,68,244\nQta 62,68,9728\ns_waitcnt lgkmcnt(6)\nQa 108,108,148\ns_waitcnt lgkmcnt(6)\nQmvav 132,72,80\n"
"Qdv 104,59,1024\nv_add_u32 v75, 1, v75\nQmvav 132,76,88\ns_waitcnt vmcnt(10)\nds_write_b32 v73, v75\nQmvav 164,72,112\nQa 109,109,149\nQa 110,110,150\n"
"Qmvav 164,76,120\nQa 111,111,151\ns_waitcnt lgkmcnt(5)\nQa 172,172,180\nQmvav 196,72,144\nQta 64,69,768\nQa 173,173,181\nQmvav 196,76,152\n"
"Qa 174,174,182\nQa 175,175,183\nQmvav 228,72,176\nQmvav 228,76,184\nQta 66,69,1792\ns_waitcnt lgkmcnt(3)\nQa 164,164,104\nQmvav 108,72,208\n"
"Qa 165,165,105\nQa 166,166,106\nQmvav 108,76,216\nds_read_b32 v66, v74\nQa 167,167,107\nQmvav 140,72,240\nQta 68,69,8960\nQmvav 140,76,248\n"
"Qmvav 172,72,84\nQmvav 172,76,116\ns_waitcnt lgkmcnt(1)\nv_cmp_gt_u32 vcc, v75, v66\ns_waitcnt lgkmcnt(6)\nQmvaa 132,56,0\ns_cbranch_vccnz .Lso1920r0\n.Lro1920r0:\n"
"s_add_u32 s68, s64, 0\nQmvaa 132,60,28\nQta 70,69,9984\nQmvaa 164,56,4\nQmvaa 164,60,32\nQdv 136,59,35840\nQmvaa 196,56,8\nQta 72,69,17152\n"
"s_nop 1\nQk 132,132,133\nQmvaa 196,60,36\nQk 133,134,135\nQmvaa 228,56,12\nQdv 148,59,36864\nQk 134,164,165\nQmvaa 228,60,40\n"
"Qta 74,69,18176\nQk 135,166,167\nQmvaa 108,56,16\nbuffer_store_dwordx4 v[132:135], v60, s[24:27], s68 offen offset:0 sc1\nQk 196,196,197\nQmvaa 108,60,44\nQdv 180,59,38912\nQk 197,198,199\n"
"Qmvaa 140,56,20\nQta 76,69,25344\nQk 198,228,229\nQmvaa 140,60,48\ns_waitcnt lgkmcnt(5)\nQa 96,96,136\nQa 97,97,137\nQmvaa 172,56,24\n"
"Qdv 104,59,33792\nQa 98,98,138\nQmvaa 172,60,52\nQta 78,69,26368\nQa 99,99,139\ns_waitcnt lgkmcnt(11)\nQmvaa 204,64,80\ns_waitcnt lgkmcnt(5)\n"
"Qa 128,128,148\nQa 129,129,149\nQmvaa 204,68,108\nQa 130,130,150\nQa 131,131,151\nQmvaa 236,64,84\nQk 199,230,231\nQk 108,108,109\n"
"Qmvaa 236,68,112\ns_waitcnt lgkmcnt(3)\nQa 192,192,180\nQa 193,193,181\nQmvaa 96,64,88\nQta 56,68,768\nQa 194,194,182\nQmvav 96,68,168\n"
"Qdv 136,59,37888\nQa 195,195,183\nQmvaa 128,64,92\ns_waitcnt lgkmcnt(3)\nQa 204,204,104\nQa 205,205,105\nQmvav 128,68,200\nQta 58,68,1792\n"
"Qa 206,206,106\nQmvaa 160,64,96\nbuffer_store_dwordx4 v[196:199], v60, s[24:27], s68 offen offset:1024 sc1\nQa 207,207,107\nQmvav 160,68,232\nQdv 148,59,39936\nQk 109,110,111\nQmvaa 192,64,100\n"
"Qta 60,68,8960\nQk 110,140,141\nQmvav 192,68,212\nQk 111,142,143\nQk 172,172,173\nQmvaa 224,64,104\nbuffer_store_dwordx4 v[108:111], v60, s[24:27], s68 offen offset:2048 sc1\nv_add_u32 v75, 1, v75\n"
"Qmvav 224,68,244\ns_waitcnt vmcnt(3)\nds_write_b32 v73, v75\ns_waitcnt lgkmcnt(10)\nQmvav 204,72,80\nQta 62,68,9984\ns_waitcnt lgkmcnt(5)\nQa 160,160,136\n"
"Qmvav 204,76,88\nQdv 180,59,34816\nQa 161,161,137\nQmvav 236,72,112\nQa 162,162,138\nQa 163,163,139\nQmvav 236,76,120\ns_waitcnt lgkmcnt(4)\n"
"Qa 224,224,148\nQa 225,225,149\nQmvav 96,72,144\nQta 64,68,16384\nQa 226,226,150\nQmvav 96,76,152\nQa 227,227,151\nQk 173,174,175\n"
"Qmvav 128,72,176\nQmvav 128,76,184\nds_read_b32 v66, v74\ns_waitcnt lgkmcnt(2)\nQa 236,236,180\nQmvav 160,72,208\nQta 66,68,17408\nQa 237,237,181\n"
"Qmvav 160,76,216\nQa 238,238,182\nQa 239,239,183\nQmvav 192,72,240\nQmvav 192,76,248\ns_waitcnt lgkmcnt(1)\nv_cmp_gt_u32 vcc, v75, v66\nQmvav 224,72,84\n"
"s_cbranch_vccnz .Lso1960r0\n.Lro1960r0:\nQmvav 224,76,116\nQta 68,68,24576\ns_add_u32 m0, s96, 65536\ns_add_u32 s95, s67, 86016\ns_waitcnt lgkmcnt(5)\nQmvaa 204,56,0\n"
"buffer_load_dwordx4 v62, s[52:55], s95 offen lds\nQmvaa 204,60,28\nQmvaa 236,56,4\nQta 70,68,25600\nQmvaa 236,60,32\ns_add_u32 s95, s67, 125952\nQmvaa 96,56,8\nbuffer_load_dwordx4 v62, s[52:55], s95 offen offset:1024 lds\n"
"Qmvaa 96,60,36\nQta 72,68,32768\nQk 174,204,205\nQmvaa 128,56,12\nQk 175,206,207\nQk 236,236,237\nQmvaa 128,60,40\nbuffer_store_dwordx4 v[172:175], v60, s[24:27], s68 offen offset:3072 sc1\n"
"Qk 237,238,239\nQmvaa 160,56,16\nQta 74,68,33792\ns_add_u32 m0, s96, 73728\ns_add_u32 s95, s72, 86016\nQmvaa 160,60,44\nbuffer_load_dwordx4 v62, s[52:55], s95 offen lds\nQmvaa 192,56,20\n"
"Qk 238,96,97\nQk 239,98,99\n.Lpg20:\ns_cbranch_execz .Lpg24\nQmvaa 192,60,48\nQta 76,68,40960\ns_add_u32 s68, s64, 4096\nQmvaa 224,56,24\n"
"buffer_store_dwordx4 v[236:239], v60, s[24:27], s68 offen offset:0 sc1\ns_add_u32 s95, s72, 125952\nQmvaa 224,60,52\nbuffer_load_dwordx4 v62, s[52:55], s95 offen offset:1024 lds\ns_waitcnt lgkmcnt(3)\nQmaaa 116,64,80\nQta 78,68,41984\nQk 128,128,129\n"
"Qmaaa 116,68,108\nQk 129,130,131\nQk 130,160,161\nQmaaa 120,64,84\ns_add_u32 m0, s96, 81920\ns_add_u32 s95, s73, 86016\nQmaaa 120,68,112\nbuffer_load_dwordx4 v62, s[52:55], s95 offen lds\n"
"Qmaaa 124,64,88\nQta 56,68,49152\nQk 131,162,163\nQmaav 124,68,168\nbuffer_store_dwordx4 v[128:131], v60, s[24:27], s68 offen offset:1024 sc1\nQk 192,192,193\nQmaaa 128,64,92\nQk 193,194,195\n"
"Qk 194,224,225\nQmaav 128,68,200\nQta 58,68,50176\ns_add_u32 s95, s73, 125952\nQmaaa 132,64,96\nbuffer_load_dwordx4 v62, s[52:55], s95 offen offset:1024 lds\nQmaav 132,68,232\nQk 195,226,227\n"
"Qmaaa 136,64,100\nQta 60,68,57344\nv_bfe_u32 v61, v60, 6, 2\nQmaav 136,68,212\nv_lshrrev_b32 v62, 8, v60\nv_xor_b32 v61, v61, v62\nQmaaa 140,64,104\nv_bfe_u32 v62, v60, 5, 1\n"
"v_lshl_or_b32 v61, v61, 1, v62\nQmaav 140,68,244\nQta 62,68,58368\nv_and_b32 v62, 16, v60\ns_waitcnt lgkmcnt(4)\nQmaav 116,72,80\nv_mov_b32 v63, 0x3f80\nv_lshlrev_b32 v63, v62, v63\n"
"Qmaav 116,76,88\nv_min_u32 v62, 1, v61\nv_add_u32 v62, -1, v62\nQmaav 120,72,112\nv_and_b32 v62, v62, v63\nv_xor_b32 v61, 1, v61\nQmaav 120,76,120\nv_min_u32 v61, 1, v61\n"
"v_add_u32 v61, -1, v61\nQmaav 124,72,144\nQta 64,68,16640\nv_and_b32 v61, v61, v63\nQmaav 124,76,152\nv_accvgpr_write_b32 a202, v62\nv_accvgpr_write_b32 a203, v61\nQmaav 128,72,176\n"
"v_accvgpr_write_b32 a200, 0\nv_accvgpr_write_b32 a201, 0\nQmaav 128,76,184\nQta 66,68,17664\nv_accvgpr_write_b32 a204, 0\nQmaav 132,72,208\nbuffer_store_dwordx4 v[192:195], v60, s[24:27], s68 offen offset:2048 sc1\nv_accvgpr_write_b32 a205, 0\n"
"Qmaav 132,76,216\nQmaav 136,72,240\nQta 68,68,24832\nQmaav 136,76,248\nQmaav 140,72,84\nQmaav 140,76,116\nQta 70,68,25856\ns_waitcnt lgkmcnt(4)\n"
"Qmaaa 116,56,0\nQmaaa 116,60,28\nQmaaa 120,56,4\nQmaaa 120,60,32\nQmaaa 124,56,8\nQta 72,68,33024\nQmaaa 124,60,36\ns_nop 2\n"
"ds_write_b128 v58, a[116:119] offset:0\nQmaaa 128,56,12\nQmaaa 128,60,40\nQta 74,68,34048\nQmaaa 132,56,16\nds_write_b128 v58, a[120:123] offset:1024\nQmaaa 132,60,44\nQmaaa 136,56,20\n"
"Qta 76,68,41216\nQmaaa 136,60,48\nds_write_b128 v58, a[124:127] offset:2048\nQmaaa 140,56,24\nQmaaa 140,60,52\nQta 78,68,42240\ns_waitcnt lgkmcnt(7)\nQmaaa 144,64,80\n"
"ds_write_b128 v58, a[128:131] offset:3072\nQmaaa 144,68,108\nQmaaa 148,64,84\nQmaaa 148,68,112\nds_write_b128 v58, a[132:135] offset:4096\nQmaaa 152,64,88\nQta 56,68,49408\nQmaav 152,68,168\n"
"Qmaaa 156,64,92\nds_write_b128 v58, a[136:139] offset:5120\nQmaav 156,68,200\nQta 58,68,50432\nQmaaa 160,64,96\nQmaav 160,68,232\nds_write_b128 v58, a[140:143] offset:6144\nQmaaa 164,64,100\n"
"Qta 60,68,57600\nQmaav 164,68,212\nQmaaa 168,64,104\nQmaav 168,68,244\nQta 62,68,58624\ns_waitcnt lgkmcnt(8)\nQmaav 144,72,80\nv_add_u32 v75, 1, v75\n"
"Qmaav 144,76,88\ns_waitcnt vmcnt(8)\nds_write_b32 v73, v75\nQmaav 148,72,112\nQmaav 148,76,120\nQmaav 152,72,144\nQta 64,68,16896\nQmaav 152,76,152\n"
"Qmaav 156,72,176\nQmaav 156,76,184\nQta 66,68,17920\nQmaav 160,72,208\nQmaav 160,76,216\nds_read_b32 v66, v74\nQmaav 164,72,240\nQta 68,68,25088\n"
"Qmaav 164,76,248\nQmaav 168,72,84\nQmaav 168,76,116\ns_waitcnt lgkmcnt(1)\nv_cmp_gt_u32 vcc, v75, v66\ns_waitcnt lgkmcnt(5)\nQmaaa 144,56,0\ns_cbranch_vccnz .Lso2046r0\n"
".Lro2046r0:\nQmaaa 144,60,28\nQta 70,68,26112\nQmaaa 148,56,4\nQmaaa 148,60,32\nQdv 104,59,2048\nQmaaa 152,56,8\nQta 72,68,33280\n"
"Qmaaa 152,60,36\ns_nop 0\nds_write_b128 v58, a[144:147] offset:33792\nQmaaa 156,56,12\nQdv 136,59,3072\nQmaaa 156,60,40\nQta 74,68,34304\nQmaaa 160,56,16\n"
"ds_write_b128 v58, a[148:151] offset:34816\nQmaaa 160,60,44\nQdv 148,59,5120\nQmaaa 164,56,20\nQta 76,68,41472\nQmaaa 164,60,48\nds_write_b128 v58, a[152:155] offset:35840\nQmaaa 168,56,24\n"
"Qdv 180,59,0\ns_waitcnt lgkmcnt(9)\nQa 8,8,104\nQmaaa 168,60,52\nQta 78,68,42496\nQa 9,9,105\ns_waitcnt lgkmcnt(10)\nQmvaa 0,64,80\n"
"ds_write_b128 v58, a[156:159] offset:36864\nQmvaa 0,68,108\nQa 10,10,106\nQa 11,11,107\nQmvaa 4,64,84\ns_waitcnt lgkmcnt(8)\nQa 12,12,136\nQa 13,13,137\n"
"Qmvaa 4,68,112\nds_write_b128 v58, a[160:163] offset:37888\nQmvaa 8,64,88\nQta 56,68,49664\nQa 14,14,138\nQa 15,15,139\nQmvav 8,68,168\nQdv 104,59,4096\n"
"s_waitcnt lgkmcnt(8)\nQa 20,20,148\nQmvaa 12,64,92\nds_write_b128 v58, a[164:167] offset:38912\nQmvav 12,68,200\nQta 58,68,50688\nQa 21,21,149\nQmvaa 16,64,96\n"
"Qa 22,22,150\nQa 23,23,151\nQmvav 16,68,232\nQdv 136,59,6144\ns_waitcnt lgkmcnt(8)\nQa 0,0,180\nQmvaa 20,64,100\nQta 60,68,57856\n"
"Qa 1,1,181\nQmvav 20,68,212\nds_write_b128 v58, a[168:171] offset:39936\nQmvaa 24,64,104\nQa 2,2,182\nQa 3,3,183\nQmvav 24,68,244\nQta 62,68,58880\n"
"s_waitcnt lgkmcnt(6)\nQa 16,16,104\ns_waitcnt lgkmcnt(6)\nQmvav 0,72,80\nQdv 148,59,1024\nv_add_u32 v75, 1, v75\nQmvav 0,76,88\ns_waitcnt vmcnt(1)\n"
"ds_write_b32 v73, v75\nQmvav 4,72,112\nQa 17,17,105\nQa 18,18,106\nQmvav 4,76,120\nQa 19,19,107\ns_waitcnt lgkmcnt(5)\nQa 24,24,136\n"
"Qmvav 8,72,144\nQta 64,69,0\nQa 25,25,137\nQmvav 8,76,152\nQa 26,26,138\nQa 27,27,139\nQmvav 12,72,176\ns_waitcnt vmcnt(0)\n"
"buffer_store_dword v67, v77, s[24:27], 0 offen sc1\nQmvav 12,76,184\nQta 66,69,512\ns_waitcnt lgkmcnt(3)\nQa 4,4,148\nQmvav 16,72,208\nQa 5,5,149\nQa 6,6,150\n"
"Qmvav 16,76,216\nds_read_b32 v66, v74\nQa 7,7,151\nQmvav 20,72,240\nQta 68,69,1024\nQmvav 20,76,248\nQmvav 24,72,84\nQmvav 24,76,116\n"
"s_waitcnt lgkmcnt(1)\nv_cmp_gt_u32 vcc, v75, v66\ns_waitcnt lgkmcnt(6)\nQmvaa 0,56,0\ns_cbranch_vccnz .Lso2088r0\n.Lro2088r0:\nQmvaa 0,60,28\nQta 70,69,1536\n"
"Qmvaa 4,56,4\nQmvaa 4,60,32\nQdv 180,59,33792\nQmvaa 8,56,8\nQta 72,69,8192\nQmvaa 8,60,36\nQmvaa 12,56,12\nQdv 104,59,34816\n"
"Qmvaa 12,60,40\nQta 74,69,8704\nQmvaa 16,56,16\nQmvaa 16,60,44\nQdv 136,59,35840\nQmvaa 20,56,20\nQta 76,69,9216\nQmvaa 20,60,48\n"
"s_waitcnt lgkmcnt(5)\nQa 28,28,180\nQa 29,29,181\nQmvaa 24,56,24\nQdv 148,59,36864\nQa 30,30,182\nQmvaa 24,60,52\nQta 78,69,9728\n"
"Qa 31,31,183\ns_waitcnt lgkmcnt(11)\nQmaaa 172,64,80\ns_waitcnt lgkmcnt(5)\nQa 32,32,104\nQa 33,33,105\nQmaaa 172,68,108\nQa 34,34,106\n"
"Qa 35,35,107\nQmaaa 176,64,84\nQmaaa 176,68,112\ns_waitcnt lgkmcnt(3)\nQa 36,36,136\nQa 37,37,137\nQmaaa 180,64,88\nQta 56,69,16384\n"
"Qa 38,38,138\nQmaav 180,68,168\nQdv 180,59,37888\nQa 39,39,139\nQmaaa 184,64,92\ns_waitcnt lgkmcnt(3)\nQa 40,40,148\nQa 41,41,149\n"
"Qmaav 184,68,200\nQta 58,69,16896\nQa 42,42,150\nQmaaa 188,64,96\nQdv 104,59,38912\nQa 43,43,151\nQmaav 188,68,232\nQmaaa 192,64,100\n"
"Qta 60,69,17408\nQmaav 192,68,212\nQdv 136,59,39936\nQmaaa 196,64,104\ns_waitcnt lgkmcnt(4)\nQa 44,44,180\nQa 45,45,181\nQmaav 196,68,244\n"
"Qta 62,69,17920\nQa 46,46,182\ns_waitcnt lgkmcnt(5)\nQmaav 172,72,80\nQa 47,47,183\nQmaav 172,76,88\nQmaav 176,72,112\ns_waitcnt lgkmcnt(3)\n"
"Qa 48,48,104\nQa 49,49,105\nQmaav 176,76,120\nQa 50,50,106\nQa 51,51,107\nQmaav 180,72,144\nQta 64,68,17152\nQmaav 180,76,152\n"
"s_waitcnt lgkmcnt(2)\nQa 52,52,136\nQa 53,53,137\nQmaav 184,72,176\nQa 54,54,138\nQa 55,55,139\nQmaav 184,76,184\nQta 66,68,18176\n"
"Qmaav 188,72,208\nQmaav 188,76,216\nQmaav 192,72,240\nQta 68,68,25344\nQmaav 192,76,248\nQmaav 196,72,84\nQmaav 196,76,116\nQta 70,68,26368\n"
"s_waitcnt lgkmcnt(4)\nQmaaa 172,56,0\nQmaaa 172,60,28\nQmaaa 176,56,4\nQmaaa 176,60,32\nQmaaa 180,56,8\nQta 72,68,33536\nQmaaa 180,60,36\n"
"s_nop 2\nds_write_b128 v58, a[172:175] offset:0\nQmaaa 184,56,12\nQmaaa 184,60,40\nQta 74,68,34560\nQmaaa 188,56,16\nds_write_b128 v58, a[176:179] offset:1024\nQmaaa 188,60,44\n"
"Qmaaa 192,56,20\nQta 76,68,41728\nQmaaa 192,60,48\nds_write_b128 v58, a[180:183] offset:2048\nQmaaa 196,56,24\nQmaaa 196,60,52\nQta 78,68,42752\ns_waitcnt lgkmcnt(7)\n"
"Qmvaa 28,64,80\nds_write_b128 v58, a[184:187] offset:3072\nQmvaa 28,68,108\nQmvaa 32,64,84\nQmvaa 32,68,112\nds_write_b128 v58, a[188:191] offset:4096\nQmvaa 36,64,88\nQta 56,68,49920\n"
"Qmvav 36,68,168\nQmvaa 40,64,92\nds_write_b128 v58, a[192:195] offset:5120\nQmvav 40,68,200\nQta 58,68,50944\nQmvaa 44,64,96\nQmvav 44,68,232\nbuffer_load_dword v78, v76, s[24:27], 0 offen sc1\n"
"Qmvaa 48,64,100\nQta 60,68,58112\nQmvav 48,68,212\nds_write_b128 v58, a[196:199] offset:6144\nQmvaa 52,64,104\nQmvav 52,68,244\nQta 62,68,59136\ns_waitcnt lgkmcnt(8)\n"
"Qmvav 28,72,80\nv_add_u32 v75, 1, v75\nQmvav 28,76,88\nds_write_b32 v73, v75\nQmvav 32,72,112\nQmvav 32,76,120\nQmvav 36,72,144\nQta 64,69,256\n"
"Qmvav 36,76,152\nQmvav 40,72,176\nQmvav 40,76,184\nQta 66,69,768\nQmvav 44,72,208\nQmvav 44,76,216\nds_read_b32 v66, v74\nQmvav 48,72,240\n"
"Qta 68,69,1280\nQmvav 48,76,248\nQmvav 52,72,84\nQmvav 52,76,116\ns_waitcnt lgkmcnt(1)\nv_cmp_gt_u32 vcc, v75, v66\ns_waitcnt lgkmcnt(5)\nQmvaa 28,56,0\n"
"s_cbranch_vccnz .Lso2172r0\n.Lro2172r0:\nQmvaa 28,60,28\nQta 70,69,1792\nQmvaa 32,56,4\nQmvaa 32,60,32\nQdv 148,59,2048\nQmvaa 36,56,8\n"
"Qta 72,69,8448\nQmvaa 36,60,36\nQmvaa 40,56,12\nQdv 180,59,3072\nQmvaa 40,60,40\nQta 74,69,8960\nQmvaa 44,56,16\nQmvaa 44,60,44\n"
"Qdv 104,59,5120\nQmvaa 48,56,20\nQta 76,69,9472\nQmvaa 48,60,48\ns_waitcnt lgkmcnt(5)\nQa 156,156,148\nQa 157,157,149\nQmvaa 52,56,24\n"
"Qdv 136,59,0\nQa 158,158,150\nQmvaa 52,60,52\nQta 78,69,9984\nQa 159,159,151\ns_waitcnt lgkmcnt(11)\nQmvaa 92,64,80\ns_waitcnt lgkmcnt(5)\n"
"Qa 188,188,180\nQa 189,189,181\nQmvaa 92,68,108\nQa 190,190,182\nQa 191,191,183\nQmvaa 124,64,84\ns_waitcnt vmcnt(0)\nv_sub_u32 v66, v78, v65\n"
"v_cmp_gt_u32 vcc, 1, v66\ns_and_b64 vcc, vcc, s[28:29]\ns_cbranch_scc1 .Lx0_w\n.Lx0_r:\nQmvaa 124,68,112\ns_add_u32 s69, s98, 0\nbuffer_load_dwordx4 a[208:211], v60, s[24:27], s69 offen offset:0 sc1\nQmvaa 156,64,88\n"
"Qta 56,69,16640\ns_waitcnt lgkmcnt(4)\nQa 252,252,104\nQmvav 156,68,168\nbuffer_load_dwordx4 a[212:215], v60, s[24:27], s69 offen offset:1024 sc1\nQmvaa 188,64,92\nQdv 148,59,4096\nQa 253,253,105\n"
"Qmvav 188,68,200\nbuffer_load_dwordx4 a[216:219], v60, s[24:27], s69 offen offset:2048 sc1\nQmvaa 220,64,96\nQta 58,69,17152\nQa 254,254,106\nQa 255,255,107\nQmvav 220,68,232\nbuffer_load_dwordx4 a[220:223], v60, s[24:27], s69 offen offset:3072 sc1\n"
"Qmvaa 252,64,100\nQdv 180,59,6144\ns_waitcnt lgkmcnt(5)\nQa 92,92,136\nQmvav 252,68,212\ns_add_u32 s69, s98, 4096\nbuffer_load_dwordx4 a[224:227], v60, s[24:27], s69 offen offset:0 sc1\nQmvaa 100,64,104\n"
"Qta 60,69,17664\nQa 93,93,137\nQa 94,94,138\nQa 95,95,139\nQmvav 100,68,244\nbuffer_load_dwordx4 a[228:231], v60, s[24:27], s69 offen offset:1024 sc1\ns_waitcnt lgkmcnt(9)\nQmvav 92,72,80\n"
"Qdv 104,59,1024\ns_waitcnt lgkmcnt(4)\nQa 220,220,148\nQmvav 92,76,88\nbuffer_load_dwordx4 a[232:235], v60, s[24:27], s69 offen offset:2048 sc1\nQmvav 124,72,112\nQta 62,69,18176\nQa 221,221,149\n"
"Qmvav 124,76,120\nbuffer_load_dwordx4 a[236:239], v60, s[24:27], s69 offen offset:3072 sc1\nQmvav 156,72,144\nQa 222,222,150\nQa 223,223,151\nQmvav 156,76,152\ns_add_u32 s69, s98, 8192\nbuffer_load_dwordx4 a[240:243], v60, s[24:27], s69 offen offset:0 sc1\n"
"Qmvav 188,72,176\ns_waitcnt lgkmcnt(3)\nQa 100,100,180\nQa 101,101,181\nQmvav 188,76,184\nbuffer_load_dwordx4 a[244:247], v60, s[24:27], s69 offen offset:1024 sc1\nQmvav 220,72,208\nv_add_u32 v75, 1, v75\n"
"Qa 102,102,182\nQmvav 220,76,216\nds_write_b32 v73, v75\nQa 103,103,183\nQmvav 252,72,240\nbuffer_load_dwordx4 a[248:251], v60, s[24:27], s69 offen offset:2048 sc1\nQmvav 252,76,248\ns_waitcnt lgkmcnt(2)\n"
"Qa 124,124,104\nQa 125,125,105\nQmvav 100,72,84\nQa 126,126,106\nQa 127,127,107\nQmvav 100,76,116\ns_waitcnt lgkmcnt(1)\nQmvaa 92,56,0\n"
"Qmvaa 92,60,28\nQmvaa 124,56,4\nQmvaa 124,60,32\nds_read_b32 v66, v74\nQmvaa 156,56,8\nQmvaa 156,60,36\nQmvaa 188,56,12\nQmvaa 188,60,40\n"
"s_waitcnt lgkmcnt(0)\nv_cmp_gt_u32 vcc, v75, v66\nQmvaa 220,56,16\ns_cbranch_vccnz .Lso2222r0\n.Lro2222r0:\nQmvaa 220,60,44\nQmvaa 252,56,20\nQmvaa 252,60,48\n"
"Qmvaa 100,56,24\nQmvaa 100,60,52\ns_waitcnt vmcnt(10)\nQmvaa 92,202,208\nQmvaa 124,200,208\ns_waitcnt vmcnt(9)\nQmvaa 156,202,212\nQmvaa 188,200,212\n"
"s_nop 3\nQk 92,92,93\nQk 93,94,95\nds_write_b64 v56, v[92:93] offset:64\ns_waitcnt vmcnt(8)\nQmvaa 220,202,216\nQk 124,124,125\nQk 125,126,127\n"
"ds_write_b64 v56, v[124:125] offset:3392\nQmvaa 252,200,216\nQk 156,156,157\n.Lpg21:\ns_cbranch_execz .Lpg25\nQk 157,158,159\nds_write_b64 v56, v[156:157] offset:6720\ns_waitcnt vmcnt(7)\n"
"Qmvaa 100,202,220\nQk 188,188,189\nQk 189,190,191\nds_write_b64 v56, v[188:189] offset:10048\nQmvaa 0,200,220\nQk 220,220,221\nQk 221,222,223\nds_write_b64 v56, v[220:221] offset:13376\n"
"s_waitcnt vmcnt(6)\nQmvaa 4,202,224\nQk 252,252,253\nQk 253,254,255\nds_write_b64 v56, v[252:253] offset:16704\nQmvaa 8,200,224\nQk 100,100,101\nQk 101,102,103\n"
"ds_write_b64 v56, v[100:101] offset:20032\ns_waitcnt vmcnt(5)\nQmvaa 12,202,228\nQk 0,0,1\nQk 1,2,3\nds_write_b64 v56, v[0:1] offset:0\nQmvaa 16,200,228\nQk 4,4,5\n"
"Qk 5,6,7\nds_write_b64 v56, v[4:5] offset:3328\ns_waitcnt vmcnt(4)\nQmvaa 20,202,232\nQk 8,8,9\nQk 9,10,11\nds_write_b64 v56, v[8:9] offset:6656\nQmvaa 24,200,232\n"
"Qk 12,12,13\nQk 13,14,15\nds_write_b64 v56, v[12:13] offset:9984\ns_waitcnt vmcnt(3)\nQmvaa 28,202,236\nQk 16,16,17\nQk 17,18,19\nds_write_b64 v56, v[16:17] offset:13312\n"
"Qmvaa 32,200,236\nQk 20,20,21\nQk 21,22,23\nds_write_b64 v56, v[20:21] offset:16640\ns_waitcnt vmcnt(2)\nQmvaa 36,202,240\nQk 24,24,25\nQk 25,26,27\n"
"ds_write_b64 v56, v[24:25] offset:19968\nQmvaa 40,200,240\nQk 28,28,29\nQk 29,30,31\nds_write_b64 v56, v[28:29] offset:32\ns_waitcnt vmcnt(1)\nQmvaa 44,202,244\nQk 32,32,33\n"
"Qk 33,34,35\nds_write_b64 v56, v[32:33] offset:3360\nQmvaa 48,200,244\nQk 36,36,37\nQk 37,38,39\nds_write_b64 v56, v[36:37] offset:6688\ns_waitcnt vmcnt(0)\nQmvaa 52,202,248\n"
"Qk 40,40,41\nQk 41,42,43\nds_write_b64 v56, v[40:41] offset:10016\nQk 44,44,45\nQk 45,46,47\nds_write_b64 v56, v[44:45] offset:13344\nQk 48,48,49\nQk 49,50,51\n"
"ds_write_b64 v56, v[48:49] offset:16672\nQk 52,52,53\nQk 53,54,55\nds_write_b64 v56, v[52:53] offset:20000\ns_waitcnt lgkmcnt(0)\ns_barrier\nQda 0,57,0\nQda 4,57,3328\n"
"Qda 8,57,6656\nQda 12,57,9984\nQda 16,57,13312\nQda 20,57,16640\nQda 24,57,19968\nQda 28,71,64\nQda 32,71,6720\nQda 36,71,13376\n"
"Qda 40,71,20032\ns_mov_b32 s68, 0\ns_waitcnt lgkmcnt(10)\nbuffer_store_dwordx4 a[0:3], v64, s[40:43], s68 offen offset:0 sc1\ns_mov_b32 s68, 40960\ns_waitcnt lgkmcnt(9)\nbuffer_store_dwordx4 a[4:7], v64, s[40:43], s68 offen offset:0 sc1\ns_mov_b32 s68, 81920\n"
"s_waitcnt lgkmcnt(8)\nbuffer_store_dwordx4 a[8:11], v64, s[40:43], s68 offen offset:0 sc1\ns_mov_b32 s68, 122880\ns_waitcnt lgkmcnt(7)\nbuffer_store_dwordx4 a[12:15], v64, s[40:43], s68 offen offset:0 sc1\ns_mov_b32 s68, 163840\ns_waitcnt lgkmcnt(6)\nbuffer_store_dwordx4 a[16:19], v64, s[40:43], s68 offen offset:0 sc1\n"
"s_mov_b32 s68, 204800\ns_waitcnt lgkmcnt(5)\nbuffer_store_dwordx4 a[20:23], v64, s[40:43], s68 offen offset:0 sc1\ns_mov_b32 s68, 245760\ns_waitcnt lgkmcnt(4)\nbuffer_store_dwordx4 a[24:27], v64, s[40:43], s68 offen offset:0 sc1\ns_mov_b32 s68, 0\ns_waitcnt lgkmcnt(3)\n"
"buffer_store_dwordx4 a[28:31], v72, s[40:43], s68 offen offset:0 sc1\ns_mov_b32 s68, 81920\ns_waitcnt lgkmcnt(2)\nbuffer_store_dwordx4 a[32:35], v72, s[40:43], s68 offen offset:0 sc1\ns_mov_b32 s68, 163840\ns_waitcnt lgkmcnt(1)\nbuffer_store_dwordx4 a[36:39], v72, s[40:43], s68 offen offset:0 sc1\ns_mov_b32 s68, 245760\n"
"s_cmp_eq_u32 s93, 1\ns_cselect_b32 s68, 0x7ffff000, s68\ns_waitcnt lgkmcnt(0)\nbuffer_store_dwordx4 a[40:43], v72, s[40:43], s68 offen offset:0 sc1\ns_branch .Lfin\n.Lrole1:\n.Lpg22:\ns_cbranch_execz .Lpg26\n"
"Qmvaa 96,64,88\nQta 56,69,49408\ns_lshl_b32 s46, %19, 4\nQmvav 96,68,168\nbuffer_load_dwordx4 a[84:87], v60, s[48:51], s31 offen offset:1024 sc1\nQmvaa 128,64,92\nds_read_b32 v66, v74\ns_add_u32 s46, s46, 128\n"
"Qmvav 128,68,200\nQta 58,69,50432\nv_mov_b32 v77, 0x7ffff000\nQmvaa 160,64,96\nQmvav 160,68,232\nbuffer_load_dwordx4 a[88:91], v60, s[48:51], s31 offen offset:2048 sc1\nQmvaa 192,64,100\ns_waitcnt lgkmcnt(1)\n"
"v_cmp_gt_u32 vcc, v75, v66\nQmvav 192,68,212\ns_cbranch_vccnz .Lso1525r1\n.Lro1525r1:\nQmvaa 224,64,104\nQta 60,69,57600\nQmvav 224,68,244\nbuffer_load_dwordx4 a[92:95], v60, s[48:51], s31 offen offset:3072 sc1\n"
"s_waitcnt vmcnt(31)\ns_waitcnt lgkmcnt(4)\nQmaav 116,72,80\nQmaav 116,76,88\nQta 62,69,58624\nQmaav 120,72,112\ns_add_u32 m0, s96, 65536\ns_add_u32 s95, s67, 86016\n"
"Qmaav 120,76,120\nbuffer_load_dwordx4 v62, s[52:55], s95 offen lds\nQmaav 124,72,144\nQta 64,69,49664\nQmaav 124,76,152\nQmaav 128,72,176\nQmaav 128,76,184\nQta 66,69,50688\n"
"s_add_u32 s95, s67, 125952\nQmaav 132,72,208\nbuffer_load_dwordx4 v62, s[52:55], s95 offen offset:1024 lds\nQmaav 132,76,216\nQmaav 136,72,240\nQta 68,69,57856\nQmaav 136,76,248\ns_add_u32 m0, s96, 73728\n"
"s_add_u32 s95, s72, 86016\nQmaav 140,72,84\nbuffer_load_dwordx4 v62, s[52:55], s95 offen lds\nQmaav 140,76,116\nQta 70,69,58880\ns_waitcnt lgkmcnt(4)\nQmaav 144,56,80\nQmaav 144,60,88\n"
"s_add_u32 s95, s72, 125952\nQmaav 148,56,112\nbuffer_load_dwordx4 v62, s[52:55], s95 offen offset:1024 lds\nQmaav 148,60,120\nQmaav 152,56,144\nQta 72,69,49920\nQmaav 152,60,152\ns_add_u32 m0, s96, 81920\n"
"s_add_u32 s95, s73, 86016\nQmaav 156,56,176\nbuffer_load_dwordx4 v62, s[52:55], s95 offen lds\nQmaav 156,60,184\nQta 74,69,50944\nQmaav 160,56,208\nQmaav 160,60,216\nQmaav 164,56,240\n"
"Qta 76,69,58112\ns_add_u32 s95, s73, 125952\nQmaav 164,60,248\nbuffer_load_dwordx4 v62, s[52:55], s95 offen offset:1024 lds\nQmaav 168,56,84\nQmaav 168,60,116\nQta 78,69,59136\ns_waitcnt lgkmcnt(4)\n"
"Qmvav 0,64,80\ns_add_u32 m0, s96, 90112\ns_add_u32 s95, s67, 86016\nQmvav 0,68,88\nbuffer_load_dwordx4 v61, s[52:55], s95 offen lds\nQmvav 4,64,112\nQmvav 4,68,120\nQmvav 8,64,144\n"
"Qta 56,68,0\ns_add_u32 s95, s67, 105472\nQmvav 8,68,152\nbuffer_load_dwordx4 v61, s[52:55], s95 offen offset:1024 lds\nQmvav 12,64,176\nQmvav 12,68,184\nQta 58,68,512\nQmvav 16,64,208\n"
"s_add_u32 s31, s74, 32768\nQmvav 16,68,216\nbuffer_load_dwordx4 a[96:99], v60, s[48:51], s31 offen offset:0 sc1\nQmvav 20,64,240\nQta 60,68,1024\nQmvav 20,68,248\nQmvav 24,64,84\nQmvav 24,68,116\n"
"Qta 62,68,1536\ns_waitcnt lgkmcnt(4)\nQmvav 28,72,80\nbuffer_load_dwordx4 a[100:103], v60, s[48:51], s31 offen offset:1024 sc1\nQmvav 28,76,88\nQmvav 32,72,112\nQmvav 32,76,120\nQmvav 36,72,144\n"
"Qta 64,68,256\nQmvav 36,76,152\nbuffer_load_dwordx4 a[104:107], v60, s[48:51], s31 offen offset:2048 sc1\nQmvav 40,72,176\nQmvav 40,76,184\nQta 66,68,768\nQmvav 44,72,208\ns_add_u32 s31, s74, 43008\n"
"v_cmp_eq_u32 s[44:45], s46, v60\ns_nop 1\nv_cndmask_b32 v77, v77, v76, s[44:45]\nv_add_u32 v67, 1, v65\ns_xor_b32 s46, %19, 12\ns_lshl_b32 s28, 1, s46\nQmvav 44,76,216\nbuffer_load_dwordx4 a[108:111], v60, s[48:51], s31 offen offset:0 sc1\n"
"Qmvav 48,72,240\nQta 68,68,1280\nQmvav 48,76,248\nv_add_u32 v75, 1, v75\nQmvav 52,72,84\ns_waitcnt vmcnt(37)\nds_write_b32 v73, v75\nQmvav 52,76,116\n"
"Qta 70,68,1792\ns_waitcnt lgkmcnt(5)\nQmaav 172,56,80\nbuffer_load_dwordx4 a[112:115], v60, s[48:51], s31 offen offset:1024 sc1\nQmaav 172,60,88\nQmaav 176,56,112\nQmaav 176,60,120\nQmaav 180,56,144\n"
"Qta 72,68,8192\nQmaav 180,60,152\nbuffer_load_dwordx4 v[168:171], v60, s[48:51], s31 offen offset:2048 sc1\nQmaav 184,56,176\nds_read_b32 v66, v74\nQmaav 184,60,184\nQta 74,68,9216\nQmaav 188,56,208\n"
"Qmaav 188,60,216\nbuffer_load_dwordx4 v[200:203], v60, s[48:51], s31 offen offset:3072 sc1\nQmaav 192,56,240\ns_waitcnt lgkmcnt(1)\nv_cmp_gt_u32 vcc, v75, v66\nQmaav 192,60,248\ns_cbranch_vccnz .Lso1595r1\n.Lro1595r1:\n"
"Qmaav 196,56,84\nQta 76,68,16384\ns_add_u32 s31, s74, 47104\nQmaav 196,60,116\nbuffer_load_dwordx4 v[232:235], v60, s[48:51], s31 offen offset:0 sc1\ns_waitcnt lgkmcnt(4)\nQmvav 92,64,80\nQmvav 92,68,88\n"
"Qta 78,68,17408\nQmvav 124,64,112\nQmvav 124,68,120\nbuffer_load_dwordx4 v[212:215], v60, s[48:51], s31 offen offset:1024 sc1\nQmvav 156,64,144\nQta 56,68,8448\nQmvav 156,68,152\nQmvav 188,64,176\n"
"Qmvav 188,68,184\nQta 58,68,9472\nQmvav 220,64,208\nbuffer_load_dwordx4 v[244:247], v60, s[48:51], s31 offen offset:2048 sc1\nQmvav 220,68,216\nQmvav 252,64,240\nQta 60,68,16640\nQmvav 252,68,248\n"
"s_add_u32 m0, s91, 0\ns_add_u32 s31, s77, 28672\nQmvav 100,64,84\nbuffer_load_dwordx4 v60, s[48:51], s31 offen sc1 lds\nQmvav 100,68,116\nQta 62,68,17664\ns_waitcnt lgkmcnt(4)\nQmaav 200,72,80\n"
"Qmaav 200,76,88\nQmaav 204,72,112\nbuffer_load_dwordx4 v60, s[48:51], s31 offen offset:1024 sc1 lds\nQmaav 204,76,120\nQmaav 208,72,144\nQta 64,68,8704\nQmaav 208,76,152\nQmaav 212,72,176\n"
"buffer_load_dwordx4 v60, s[48:51], s31 offen offset:2048 sc1 lds\nQmaav 212,76,184\nQta 66,68,9728\nQmaav 216,72,208\nQmaav 216,76,216\nQmaav 220,72,240\nQta 68,68,16896\nQmaav 220,76,248\n"
"buffer_load_dwordx4 v60, s[48:51], s31 offen offset:3072 sc1 lds\nQmaav 224,72,84\nQmaav 224,76,116\nQta 70,68,17920\ns_waitcnt lgkmcnt(4)\nQmaav 228,56,80\ns_add_u32 m0, s91, 4096\ns_add_u32 s31, s77, 32768\n"
"Qmaav 228,60,88\nbuffer_load_dwordx4 v60, s[48:51], s31 offen sc1 lds\nQmaav 232,56,112\nQmaav 232,60,120\nQmaav 236,56,144\nQta 72,68,8960\nQmaav 236,60,152\nbuffer_load_dwordx4 v60, s[48:51], s31 offen offset:1024 sc1 lds\n"
"Qmaav 240,56,176\nQmaav 240,60,184\nQta 74,68,9984\nQmaav 244,56,208\nQmaav 244,60,216\nbuffer_load_dwordx4 v60, s[48:51], s31 offen offset:2048 sc1 lds\nQmaav 248,56,240\nQta 76,68,17152\n"
"Qmaav 248,60,248\nQmaav 252,56,84\nQmaav 252,60,116\nQta 78,68,18176\ns_add_u32 m0, s91, 33792\ns_add_u32 s31, s77, 43008\ns_waitcnt lgkmcnt(4)\nQmvav 132,64,80\n"
"buffer_load_dwordx4 v60, s[48:51], s31 offen sc1 lds\nQmvav 132,68,88\nQmvav 164,64,112\nQmvav 164,68,120\nQmvav 196,64,144\nQta 56,68,24576\nQmvav 196,68,152\nbuffer_load_dwordx4 v60, s[48:51], s31 offen offset:1024 sc1 lds\n"
"Qmvav 228,64,176\nQmvav 228,68,184\nQta 58,68,25600\nQmvav 108,64,208\nQmvav 108,68,216\nbuffer_load_dwordx4 v60, s[48:51], s31 offen offset:2048 sc1 lds\nQmvav 140,64,240\nQta 60,68,32768\n"
"Qmvav 140,68,248\nv_add_u32 v75, 1, v75\nQmvav 172,64,84\ns_waitcnt vmcnt(49)\nds_write_b32 v73, v75\nQmvav 172,68,116\nQta 62,68,33792\ns_waitcnt lgkmcnt(5)\n"
"Qmvav 204,72,80\nbuffer_load_dwordx4 v60, s[48:51], s31 offen offset:3072 sc1 lds\nQmvav 204,76,88\nQmvav 236,72,112\nQmvav 236,76,120\nQmvav 96,72,144\nQta 64,68,24832\ns_add_u32 m0, s91, 37888\n"
"s_add_u32 s31, s77, 47104\nQmvav 96,76,152\nbuffer_load_dwordx4 v60, s[48:51], s31 offen sc1 lds\nQmvav 128,72,176\nds_read_b32 v66, v74\nQmvav 128,76,184\nQta 66,68,25856\nQmvav 160,72,208\n"
"Qmvav 160,76,216\nbuffer_load_dwordx4 v60, s[48:51], s31 offen offset:1024 sc1 lds\nQmvav 192,72,240\ns_waitcnt lgkmcnt(1)\nv_cmp_gt_u32 vcc, v75, v66\nQmvav 192,76,248\ns_cbranch_vccnz .Lso1665r1\n.Lro1665r1:\n"
"Qmvav 224,72,84\nQta 68,68,33024\nQmvav 224,76,116\nbuffer_load_dwordx4 v60, s[48:51], s31 offen offset:2048 sc1 lds\ns_waitcnt vmcnt(37)\ns_waitcnt lgkmcnt(4)\nQmaaa 116,56,0\nQmaaa 116,60,28\n"
"Qta 70,68,34048\nQmaaa 120,56,4\ns_add_u32 m0, s96, 0\ns_add_u32 s95, s67, 126976\nQmaaa 120,60,32\nbuffer_load_dwordx4 v61, s[52:55], s95 offen lds\nQmaaa 124,56,8\nQta 72,68,25088\n"
"Qmaaa 124,60,36\nQmaaa 128,56,12\nQmaaa 128,60,40\nQta 74,68,26112\ns_add_u32 s95, s67, 146432\nQmaaa 132,56,16\nbuffer_load_dwordx4 v61, s[52:55], s95 offen offset:1024 lds\nQmaaa 132,60,44\n"
"Qmaaa 136,56,20\nQta 76,68,33280\nQmaaa 136,60,48\ns_add_u32 s31, s75, 28672\nQmaaa 140,56,24\nbuffer_load_dwordx4 v[80:83], v60, s[48:51], s31 offen offset:0 sc1\nQmaaa 140,60,52\nQta 78,68,34304\n"
"s_waitcnt lgkmcnt(4)\nQmaaa 144,64,0\nQmaaa 144,68,28\nQmaaa 148,64,4\nbuffer_load_dwordx4 v[112:115], v60, s[48:51], s31 offen offset:1024 sc1\nQmaaa 148,68,32\nQmaaa 152,64,8\nQta 56,68,25344\n"
"Qmaaa 152,68,36\nQmaaa 156,64,12\nbuffer_load_dwordx4 v[144:147], v60, s[48:51], s31 offen offset:2048 sc1\nQmaaa 156,68,40\nQta 58,68,26368\nQmaaa 160,64,16\nQmaaa 160,68,44\nQmaaa 164,64,20\n"
"Qta 60,68,33536\nQmaaa 164,68,48\nbuffer_load_dwordx4 v[176:179], v60, s[48:51], s31 offen offset:3072 sc1\nQmaaa 168,64,24\nQmaaa 168,68,52\nQta 62,68,34560\ns_waitcnt lgkmcnt(4)\nQmvaa 0,72,0\n"
"s_add_u32 s31, s75, 32768\nQmvaa 0,76,28\nbuffer_load_dwordx4 v[208:211], v60, s[48:51], s31 offen offset:0 sc1\nQmvaa 4,72,4\nQmvaa 4,76,32\nQmvaa 8,72,8\nQta 64,68,40960\nQmvaa 8,76,36\n"
"buffer_load_dwordx4 v[240:243], v60, s[48:51], s31 offen offset:1024 sc1\nQmvaa 12,72,12\nQmvaa 12,76,40\nQta 66,68,41472\nQmvaa 16,72,16\nQmvaa 16,76,44\nbuffer_load_dwordx4 v[84:87], v60, s[48:51], s31 offen offset:2048 sc1\nQmvaa 20,72,20\n"
"Qta 68,68,41984\nQmvaa 20,76,48\nQmvaa 24,72,24\nQmvaa 24,76,52\nQta 70,68,42496\ns_add_u32 s31, s75, 43008\ns_waitcnt lgkmcnt(4)\nQmvaa 28,56,0\n"
"buffer_load_dwordx4 v[88:91], v60, s[48:51], s31 offen offset:0 sc1\nQmvaa 28,60,28\nQmvaa 32,56,4\nQmvaa 32,60,32\nQmvaa 36,56,8\nQta 72,68,41216\nQmvaa 36,60,36\nbuffer_load_dwordx4 v[120:123], v60, s[48:51], s31 offen offset:1024 sc1\n"
"Qmvaa 40,56,12\nQmvaa 40,60,40\nQta 74,68,41728\nQmvaa 44,56,16\nQmvaa 44,60,44\nbuffer_load_dwordx4 v[152:155], v60, s[48:51], s31 offen offset:2048 sc1\nQmvaa 48,56,20\nQta 76,68,42240\n"
"Qmvaa 48,60,48\nv_add_u32 v75, 1, v75\nQmvaa 52,56,24\ns_waitcnt vmcnt(40)\nds_write_b32 v73, v75\nQmvaa 52,60,52\nQta 78,68,42752\ns_waitcnt lgkmcnt(5)\n"
"Qmaaa 172,64,0\nbuffer_load_dwordx4 v[184:187], v60, s[48:51], s31 offen offset:3072 sc1\nQmaaa 172,68,28\nQmaaa 176,64,4\nQmaaa 176,68,32\nQmaaa 180,64,8\nQta 56,68,49152\ns_add_u32 s31, s75, 47104\n"
"Qmaaa 180,68,36\nbuffer_load_dwordx4 v[216:219], v60, s[48:51], s31 offen offset:0 sc1\nQmaaa 184,64,12\nds_read_b32 v66, v74\nQmaaa 184,68,40\nQta 58,68,50176\nQmaaa 188,64,16\nQmaaa 188,68,44\n"
"buffer_load_dwordx4 v[248:251], v60, s[48:51], s31 offen offset:1024 sc1\nQmaaa 192,64,20\ns_waitcnt lgkmcnt(1)\nv_cmp_gt_u32 vcc, v75, v66\nQmaaa 192,68,48\ns_cbranch_vccnz .Lso1735r1\n.Lro1735r1:\nQmaaa 196,64,24\n"
"Qta 60,68,57344\nQmaaa 196,68,52\nbuffer_load_dwordx4 v[116:119], v60, s[48:51], s31 offen offset:2048 sc1\ns_waitcnt lgkmcnt(4)\nQmvaa 92,72,0\n.Lpg23:\ns_cbranch_execz .Ltr8\nQmvaa 92,76,28\n"
"Qta 62,68,58368\nQmvaa 124,72,4\ns_add_u32 m0, s96, 8192\ns_add_u32 s95, s72, 86016\nQmvaa 124,76,32\nbuffer_load_dwordx4 v61, s[52:55], s95 offen lds\nQmvaa 156,72,8\nQta 64,68,49408\n"
"Qmvaa 156,76,36\nQmvaa 188,72,12\nQmvaa 188,76,40\nQta 66,68,50432\ns_add_u32 s95, s72, 105472\nQmvaa 220,72,16\nbuffer_load_dwordx4 v61, s[52:55], s95 offen offset:1024 lds\nQmvaa 220,76,44\n"
"Qmvaa 252,72,20\nQta 68,68,57600\nQmvaa 252,76,48\ns_add_u32 m0, s96, 16384\ns_add_u32 s95, s72, 126976\nQmvaa 100,72,24\nbuffer_load_dwordx4 v61, s[52:55], s95 offen lds\nQmvaa 100,76,52\n"
"Qta 70,68,58624\ns_waitcnt lgkmcnt(4)\nQmaaa 200,56,0\nQmaaa 200,60,28\ns_add_u32 s95, s72, 146432\nQmaaa 204,56,4\nbuffer_load_dwordx4 v61, s[52:55], s95 offen offset:1024 lds\nQmaaa 204,60,32\n"
"Qmaaa 208,56,8\nQta 72,68,49664\nQmaaa 208,60,36\ns_add_u32 m0, s96, 24576\ns_add_u32 s95, s73, 86016\nQmaaa 212,56,12\nbuffer_load_dwordx4 v61, s[52:55], s95 offen lds\nQmaaa 212,60,40\n"
"Qta 74,68,50688\nQmaaa 216,56,16\nQmaaa 216,60,44\nQmaaa 220,56,20\nQta 76,68,57856\ns_add_u32 s95, s73, 105472\nQmaaa 220,60,48\nbuffer_load_dwordx4 v61, s[52:55], s95 offen offset:1024 lds\n"
"Qmaaa 224,56,24\nQmaaa 224,60,52\nQta 78,68,58880\ns_waitcnt lgkmcnt(4)\nQmaaa 228,64,0\ns_add_u32 m0, s96, 32768\ns_add_u32 s95, s73, 126976\nQmaaa 228,68,28\n"
"buffer_load_dwordx4 v61, s[52:55], s95 offen lds\nQmaaa 232,64,4\nQmaaa 232,68,32\nQmaaa 236,64,8\nQta 56,68,49920\ns_add_u32 s95, s73, 146432\nQmaaa 236,68,36\nbuffer_load_dwordx4 v61, s[52:55], s95 offen offset:1024 lds\n"
"Qmaaa 240,64,12\nQmaaa 240,68,40\nQta 58,68,50944\nQmaaa 244,64,16\nQmaaa 244,68,44\nQmaaa 248,64,20\nQta 60,68,58112\nQmaaa 248,68,48\n"
"Qmaaa 252,64,24\nQmaaa 252,68,52\nQta 62,68,59136\ns_waitcnt lgkmcnt(4)\nQmvaa 132,72,0\nQmvaa 132,76,28\nQmvaa 164,72,4\nQmvaa 164,76,32\n"
"Qmvaa 196,72,8\nQta 64,69,0\nQmvaa 196,76,36\nQmvaa 228,72,12\nQmvaa 228,76,40\nQta 66,69,512\nQmvaa 108,72,16\nQmvaa 108,76,44\n"
"Qmvaa 140,72,20\nQta 68,69,1024\nQmvaa 140,76,48\nv_add_u32 v75, 1, v75\nQmvaa 172,72,24\ns_waitcnt vmcnt(22)\nds_write_b32 v73, v75\nQmvaa 172,76,52\n"
"Qta 70,69,1536\ns_waitcnt lgkmcnt(5)\nQmvaa 204,56,0\ns_waitcnt vmcnt(24)\nQda 0,58,0\nQmvaa 204,60,28\nQmvaa 236,56,4\nQda 28,58,33792\n"
"Qmvaa 236,60,32\nQmvaa 96,56,8\nQta 72,69,8192\nQmvaa 96,60,36\nQda 4,58,1024\nQmvaa 128,56,12\nds_read_b32 v66, v74\nQmvaa 128,60,40\n"
"Qta 74,69,8704\nQmvaa 160,56,16\nQda 32,58,34816\nQmvaa 160,60,44\nQmvaa 192,56,20\ns_waitcnt lgkmcnt(2)\nv_cmp_gt_u32 vcc, v75, v66\nQmvaa 192,60,48\n"
"s_cbranch_vccnz .Lso1805r1\n.Lro1805r1:\nQmvaa 224,56,24\nQta 76,69,9216\ns_add_u32 m0, s96, 40960\ns_add_u32 s95, s67, 86016\nQmvaa 224,60,52\nbuffer_load_dwordx4 v63, s[52:55], s95 offen lds\n"
"s_waitcnt vmcnt(39)\ns_waitcnt lgkmcnt(8)\nQmaaa 172,64,80\nQda 8,58,2048\nQmaaa 172,68,108\nQta 78,69,9728\nQmaaa 176,64,84\nQda 36,58,35840\n"
"s_add_u32 s95, s67, 105472\nQmaaa 176,68,112\nbuffer_load_dwordx4 v63, s[52:55], s95 offen offset:1024 lds\nQmaaa 180,64,88\nQta 56,69,16384\nQmaav 180,68,168\nQda 12,58,3072\nQmaaa 184,64,92\n"
"Qmaav 184,68,200\nQta 58,69,16896\ns_add_u32 m0, s96, 49152\ns_add_u32 s95, s67, 126976\nQmaaa 188,64,96\nbuffer_load_dwordx4 v63, s[52:55], s95 offen lds\nQmaav 188,68,232\nQda 40,58,36864\n"
"Qmaaa 192,64,100\nQta 60,69,17408\nQmaav 192,68,212\nQda 16,58,4096\ns_add_u32 s95, s67, 146432\nQmaaa 196,64,104\nbuffer_load_dwordx4 v63, s[52:55], s95 offen offset:1024 lds\nQmaav 196,68,244\n"
"Qta 62,69,17920\ns_waitcnt vmcnt(12)\ns_waitcnt lgkmcnt(8)\nQmaav 172,72,80\nQda 44,58,37888\nQmaav 172,76,88\ns_add_u32 m0, s96, 57344\ns_add_u32 s95, s72, 86016\n"
"Qmaav 176,72,112\nbuffer_load_dwordx4 v63, s[52:55], s95 offen lds\nQmaav 176,76,120\nQda 20,58,5120\nQmaav 180,72,144\nQta 64,69,24576\nv_add_u32 v75, 1, v75\nQmaav 180,76,152\n"
"s_waitcnt vmcnt(5)\nds_write_b32 v73, v75\ns_add_u32 s95, s72, 105472\nQmaav 184,72,176\nbuffer_load_dwordx4 v63, s[52:55], s95 offen offset:1024 lds\nQmaav 184,76,184\nQta 66,69,25600\nQmaav 188,72,208\n"
"Qda 48,58,38912\nQmaav 188,76,216\nQmaav 192,72,240\nQta 68,68,0\nQmaav 192,76,248\nQda 24,58,6144\nQmaav 196,72,84\nQmaav 196,76,116\n"
"ds_read_b32 v66, v74\ns_waitcnt lgkmcnt(9)\nQmaaa 172,56,0\nQta 70,68,1024\nQmaaa 172,60,28\nQda 52,58,39936\nQmaaa 176,56,4\nQmaaa 176,60,32\n"
"s_waitcnt lgkmcnt(2)\nv_cmp_gt_u32 vcc, v75, v66\ns_waitcnt lgkmcnt(11)\nQmaaa 180,56,8\ns_cbranch_vccnz .Lso1840r1\n.Lro1840r1:\nQmaaa 180,60,36\nQta 72,68,8192\n"
"Qmaaa 184,56,12\nds_write_b128 v58, a[172:175] offset:0\nQmaaa 184,60,40\ns_waitcnt lgkmcnt(2)\nQmaaa 188,56,16\nQta 74,68,9216\nQmaaa 188,60,44\nds_write_b128 v58, a[176:179] offset:1024\n"
"Qmaaa 192,56,20\nQmaaa 192,60,48\nQta 76,68,16384\nQmaaa 196,56,24\nds_write_b128 v58, a[180:183] offset:2048\nQmaaa 196,60,52\ns_waitcnt lgkmcnt(7)\nQmaaa 116,64,80\n"
"Qta 78,68,17408\nQmaaa 116,68,108\nds_write_b128 v58, a[184:187] offset:3072\nQmaaa 120,64,84\nQmaaa 120,68,112\nQmaaa 124,64,88\nQta 56,68,24576\nQmaav 124,68,168\n"
"ds_write_b128 v58, a[188:191] offset:4096\nQmaaa 128,64,92\nQmaav 128,68,200\nQta 58,68,25600\nQmaaa 132,64,96\nds_write_b128 v58, a[192:195] offset:5120\nQmaav 132,68,232\nQmaaa 136,64,100\n"
"Qta 60,68,32768\nQmaav 136,68,212\nds_write_b128 v58, a[196:199] offset:6144\nQmaaa 140,64,104\nQmaav 140,68,244\nQta 62,68,33792\ns_waitcnt lgkmcnt(8)\nQmaav 116,72,80\n"
"v_add_u32 v75, 1, v75\nQmaav 116,76,88\nds_write_b32 v73, v75\nQmaav 120,72,112\nQmaav 120,76,120\nQmaav 124,72,144\nQta 64,69,256\nQmaav 124,76,152\n"
"Qmaav 128,72,176\nQmaav 128,76,184\nQta 66,69,768\nQmaav 132,72,208\nQmaav 132,76,216\nds_read_b32 v66, v74\nQmaav 136,72,240\nQta 68,69,1280\n"
"Qmaav 136,76,248\nQmaav 140,72,84\nQmaav 140,76,116\ns_waitcnt lgkmcnt(1)\nv_cmp_gt_u32 vcc, v75, v66\ns_waitcnt lgkmcnt(5)\nQmaaa 116,56,0\ns_cbranch_vccnz .Lso1878r1\n"
".Lro1878r1:\nQmaaa 116,60,28\nQta 70,69,1792\nQmaaa 120,56,4\nQmaaa 120,60,32\nQdv 148,59,2048\nQmaaa 124,56,8\nQta 72,69,8448\n"
"Qmaaa 124,60,36\ns_nop 0\nds_write_b128 v58, a[116:119] offset:33792\nQmaaa 128,56,12\nQdv 180,59,3072\nQmaaa 128,60,40\nQta 74,69,8960\nQmaaa 132,56,16\n"
"ds_write_b128 v58, a[120:123] offset:34816\nQmaaa 132,60,44\nQdv 104,59,5120\nQmaaa 136,56,20\nQta 76,69,9472\nQmaaa 136,60,48\nds_write_b128 v58, a[124:127] offset:35840\nQmaaa 140,56,24\n"
"Qdv 136,59,0\ns_waitcnt lgkmcnt(9)\nQa 156,156,148\nQmaaa 140,60,52\nQta 78,69,9984\nQa 157,157,149\ns_waitcnt lgkmcnt(10)\nQmvaa 92,64,80\n"
"ds_write_b128 v58, a[128:131] offset:36864\nQmvaa 92,68,108\nQa 158,158,150\nQa 159,159,151\nQmvaa 124,64,84\ns_waitcnt lgkmcnt(8)\nQa 188,188,180\nQa 189,189,181\n"
"Qmvaa 124,68,112\nds_write_b128 v58, a[132:135] offset:37888\nQmvaa 156,64,88\nQta 56,69,16640\nQa 190,190,182\nQa 191,191,183\nQmvav 156,68,168\nQdv 148,59,4096\n"
"s_waitcnt lgkmcnt(8)\nQa 252,252,104\nQmvaa 188,64,92\nds_write_b128 v58, a[136:139] offset:38912\nQmvav 188,68,200\nQta 58,69,17152\nQa 253,253,105\nQmvaa 220,64,96\n"
"Qa 254,254,106\nQa 255,255,107\nQmvav 220,68,232\nQdv 180,59,6144\ns_waitcnt lgkmcnt(8)\nQa 92,92,136\nQmvaa 252,64,100\nQta 60,69,17664\n"
"Qa 93,93,137\nQmvav 252,68,212\nds_write_b128 v58, a[140:143] offset:39936\nQmvaa 100,64,104\nQa 94,94,138\nQa 95,95,139\nQmvav 100,68,244\nQta 62,69,18176\n"
"s_waitcnt lgkmcnt(6)\nQa 220,220,148\ns_waitcnt lgkmcnt(6)\nQmvav 92,72,80\nQdv 104,59,1024\nv_add_u32 v75, 1, v75\nQmvav 92,76,88\nds_write_b32 v73, v75\n"
"Qa 221,221,149\nQmvav 124,72,112\nQa 222,222,150\nQa 223,223,151\nQmvav 124,76,120\ns_waitcnt lgkmcnt(5)\nQa 100,100,180\nQa 101,101,181\n"
"Qmvav 156,72,144\nQta 64,69,24832\nQa 102,102,182\nQmvav 156,76,152\nQa 103,103,183\nQmvav 188,72,176\nQmvav 188,76,184\nQta 66,69,25856\n"
"s_waitcnt lgkmcnt(3)\nQa 124,124,104\nQmvav 220,72,208\nQa 125,125,105\nQa 126,126,106\nQmvav 220,76,216\nds_read_b32 v66, v74\nQa 127,127,107\n"
"Qmvav 252,72,240\nQta 68,68,256\nQmvav 252,76,248\nQmvav 100,72,84\nQmvav 100,76,116\ns_waitcnt lgkmcnt(1)\nv_cmp_gt_u32 vcc, v75, v66\ns_waitcnt lgkmcnt(6)\n"
"Qmvaa 92,56,0\ns_cbranch_vccnz .Lso1920r1\n.Lro1920r1:\ns_add_u32 s68, s64, 0\nQmvaa 92,60,28\nQta 70,68,1280\ns_add_u32 m0, s96, 65536\ns_add_u32 s95, s72, 126976\n"
"Qmvaa 124,56,4\nbuffer_load_dwordx4 v63, s[52:55], s95 offen lds\nQmvaa 124,60,32\nQdv 136,59,33792\nQmvaa 156,56,8\nQta 72,68,8448\nQk 92,92,93\nQmvaa 156,60,36\n"
"s_add_u32 s95, s72, 146432\nQk 93,94,95\nQmvaa 188,56,12\nbuffer_load_dwordx4 v63, s[52:55], s95 offen offset:1024 lds\nQmvaa 188,60,40\nQta 74,68,9472\nQk 94,124,125\nQmvaa 220,56,16\n"
"Qdv 148,59,34816\nQk 95,126,127\nQmvaa 220,60,44\nbuffer_store_dwordx4 v[92:95], v60, s[24:27], s68 offen offset:0 sc1\nQk 156,156,157\nQmvaa 252,56,20\nQta 76,68,16640\ns_add_u32 m0, s96, 73728\n"
"s_add_u32 s95, s73, 86016\nQmvaa 252,60,48\nbuffer_load_dwordx4 v63, s[52:55], s95 offen lds\nv_add_u32 v75, 1, v75\nQmvaa 100,56,24\nds_write_b32 v73, v75\ns_waitcnt lgkmcnt(5)\nQa 0,0,136\n"
"Qmvaa 100,60,52\nQta 78,68,17664\nQa 1,1,137\ns_waitcnt lgkmcnt(6)\nQmaaa 144,64,80\nQdv 180,59,35840\ns_add_u32 s95, s73, 105472\nQmaaa 144,68,108\n"
"buffer_load_dwordx4 v63, s[52:55], s95 offen offset:1024 lds\nQmaaa 148,64,84\nQa 2,2,138\nQa 3,3,139\nQmaaa 148,68,112\nQdv 104,59,36864\ns_waitcnt lgkmcnt(5)\nQa 4,4,148\n"
"Qmaaa 152,64,88\nQta 56,68,24832\ns_add_u32 m0, s96, 81920\ns_add_u32 s95, s73, 126976\nQmaav 152,68,168\nbuffer_load_dwordx4 v63, s[52:55], s95 offen lds\nQmaaa 156,64,92\nds_read_b32 v66, v74\n"
"Qa 5,5,149\nQa 6,6,150\nQa 7,7,151\nQmaav 156,68,200\nQta 58,68,25856\ns_waitcnt lgkmcnt(4)\nQa 8,8,180\nQmaaa 160,64,96\n"
"Qdv 136,59,37888\ns_add_u32 s95, s73, 146432\nQmaav 160,68,232\nbuffer_load_dwordx4 v63, s[52:55], s95 offen offset:1024 lds\nQmaaa 164,64,100\ns_waitcnt lgkmcnt(2)\nv_cmp_gt_u32 vcc, v75, v66\nQmaav 164,68,212\n"
"s_cbranch_vccnz .Lso1945r1\n.Lro1945r1:\nQa 9,9,181\nQa 10,10,182\nQa 11,11,183\nQmaaa 168,64,104\nQta 60,68,33024\ns_waitcnt lgkmcnt(5)\n"
"Qa 12,12,104\nQmaav 168,68,244\nQdv 148,59,38912\nQa 13,13,105\ns_waitcnt lgkmcnt(6)\nQmaav 144,72,80\nQa 14,14,106\nQa 15,15,107\n"
"Qmaav 144,76,88\nQta 62,68,34048\ns_waitcnt lgkmcnt(3)\nQa 16,16,136\nQmaav 148,72,112\nQdv 180,59,39936\nQa 17,17,137\nQmaav 148,76,120\n"
"Qa 18,18,138\nQa 19,19,139\nQmaav 152,72,144\nQta 64,69,25088\nv_bfe_u32 v61, v60, 6, 2\nQmaav 152,76,152\ns_waitcnt lgkmcnt(3)\nQa 20,20,148\n"
"Qa 21,21,149\nQmaav 156,72,176\nQa 22,22,150\nQa 23,23,151\nQmaav 156,76,184\nQta 66,69,26112\nv_lshrrev_b32 v62, 8, v60\nQmaav 160,72,208\n"
"v_xor_b32 v61, v61, v62\nv_bfe_u32 v62, v60, 5, 1\nQmaav 160,76,216\nv_lshl_or_b32 v61, v61, 1, v62\nv_and_b32 v62, 16, v60\nQmaav 164,72,240\nQta 68,68,512\ns_waitcnt lgkmcnt(3)\n"
"Qa 24,24,180\nQmaav 164,76,248\nQa 25,25,181\nQa 26,26,182\nQmaav 168,72,84\nQa 27,27,183\nv_mov_b32 v63, 0x3f80\nQmaav 168,76,116\n"
"Qta 70,68,1536\nv_lshlrev_b32 v63, v62, v63\ns_waitcnt lgkmcnt(5)\nQmaaa 144,56,0\nv_min_u32 v62, 1, v61\nv_add_u32 v62, -1, v62\nQmaaa 144,60,28\nv_and_b32 v62, v62, v63\n"
"v_xor_b32 v61, 1, v61\nQmaaa 148,56,4\nv_min_u32 v61, 1, v61\nv_add_u32 v61, -1, v61\nQmaaa 148,60,32\nv_and_b32 v61, v61, v63\nv_accvgpr_write_b32 a174, v62\nQmaaa 152,56,8\n"
"Qta 72,68,8704\nv_accvgpr_write_b32 a175, v61\nQmaaa 152,60,36\nds_write_b128 v58, a[144:147] offset:0\nQmaaa 156,56,12\nv_accvgpr_write_b32 a172, 0\nv_accvgpr_write_b32 a173, 0\nQmaaa 156,60,40\n"
"Qta 74,68,9728\nv_accvgpr_write_b32 a176, 0\nQmaaa 160,56,16\nds_write_b128 v58, a[148:151] offset:1024\nQmaaa 160,60,44\nv_accvgpr_write_b32 a177, 0\nQk 157,158,159\nQmaaa 164,56,20\n"
"Qta 76,68,16896\nQk 158,188,189\nQmaaa 164,60,48\nds_write_b128 v58, a[152:155] offset:2048\nQmaaa 168,56,24\nQk 159,190,191\nQk 220,220,221\nQmaaa 168,60,52\n"
"Qta 78,68,17920\nbuffer_store_dwordx4 v[156:159], v60, s[24:27], s68 offen offset:1024 sc1\ns_waitcnt lgkmcnt(7)\nQmvaa 0,64,80\nds_write_b128 v58, a[156:159] offset:3072\nQmvaa 0,68,108\nQk 221,222,223\nQk 222,252,253\n"
"Qmvaa 4,64,84\nQk 223,254,255\nQk 100,100,101\nQmvaa 4,68,112\nds_write_b128 v58, a[160:163] offset:4096\nQmvaa 8,64,88\nQta 56,68,25088\nbuffer_store_dwordx4 v[220:223], v60, s[24:27], s68 offen offset:2048 sc1\n"
"Qmvav 8,68,168\nQk 101,102,103\nQmvaa 12,64,92\n.Lpg24:\ns_cbranch_execz .Ltr8\nds_write_b128 v58, a[164:167] offset:5120\nQmvav 12,68,200\nQta 58,68,26112\n"
"Qmvaa 16,64,96\nQmvav 16,68,232\nds_write_b128 v58, a[168:171] offset:6144\nQmvaa 20,64,100\nQta 60,68,33280\nQmvav 20,68,212\nQmvaa 24,64,104\nQmvav 24,68,244\n"
"Qta 62,68,34304\ns_waitcnt lgkmcnt(8)\nQmvav 0,72,80\nv_add_u32 v75, 1, v75\nQmvav 0,76,88\ns_waitcnt vmcnt(11)\nds_write_b32 v73, v75\nQmvav 4,72,112\n"
"Qmvav 4,76,120\nQmvav 8,72,144\nQta 64,69,25344\nQmvav 8,76,152\nQmvav 12,72,176\nQmvav 12,76,184\nQta 66,69,26368\nQmvav 16,72,208\n"
"Qmvav 16,76,216\nds_read_b32 v66, v74\nQmvav 20,72,240\nQta 68,68,768\nQmvav 20,76,248\nQmvav 24,72,84\nQmvav 24,76,116\ns_waitcnt lgkmcnt(1)\n"
"v_cmp_gt_u32 vcc, v75, v66\ns_waitcnt lgkmcnt(5)\nQmvaa 0,56,0\ns_cbranch_vccnz .Lso2004r1\n.Lro2004r1:\nQmvaa 0,60,28\nQta 70,68,1792\nQmvaa 4,56,4\n"
"Qmvaa 4,60,32\nQdv 104,59,2048\nQmvaa 8,56,8\nQta 72,68,8960\ns_nop 1\nQk 102,0,1\nQmvaa 8,60,36\nQk 103,2,3\n"
"Qmvaa 12,56,12\nQdv 136,59,3072\nbuffer_store_dwordx4 v[100:103], v60, s[24:27], s68 offen offset:3072 sc1\nQmvaa 12,60,40\nQta 74,68,9984\nQk 4,4,5\nQmvaa 16,56,16\nQk 5,6,7\n"
"Qk 6,8,9\nQmvaa 16,60,44\nQdv 148,59,5120\nQk 7,10,11\nQmvaa 20,56,20\nQta 76,68,17152\ns_add_u32 s68, s64, 4096\nQmvaa 20,60,48\n"
"s_waitcnt lgkmcnt(5)\nQa 36,36,104\nQa 37,37,105\nQmvaa 24,56,24\nQdv 180,59,0\nQa 38,38,106\nQmvaa 24,60,52\nQta 78,68,18176\n"
"Qa 39,39,107\ns_waitcnt lgkmcnt(11)\nQmvaa 28,64,80\ns_waitcnt lgkmcnt(5)\nQa 40,40,136\nQa 41,41,137\nQmvaa 28,68,108\nQa 42,42,138\n"
"Qa 43,43,139\nQmvaa 32,64,84\nbuffer_store_dwordx4 v[4:7], v60, s[24:27], s68 offen offset:0 sc1\nQk 12,12,13\nQmvaa 32,68,112\ns_waitcnt lgkmcnt(3)\nQa 48,48,148\nQa 49,49,149\n"
"Qmvaa 36,64,88\nQta 56,68,25344\nQa 50,50,150\nQmvav 36,68,168\nQdv 104,59,4096\nQa 51,51,151\nQmvaa 40,64,92\ns_waitcnt lgkmcnt(3)\n"
"Qa 28,28,180\nQa 29,29,181\nQmvav 40,68,200\nQta 58,68,26368\nQa 30,30,182\nQmvaa 44,64,96\nQa 31,31,183\nQk 13,14,15\n"
"Qmvav 44,68,232\nQdv 136,59,6144\nQk 14,16,17\nQmvaa 48,64,100\nQta 60,68,33536\nQk 15,18,19\nQmvav 48,68,212\nbuffer_store_dwordx4 v[12:15], v60, s[24:27], s68 offen offset:1024 sc1\n"
"Qk 20,20,21\nQmvaa 52,64,104\nQk 21,22,23\nQk 22,24,25\nQmvav 52,68,244\nQta 62,68,34560\nv_add_u32 v75, 1, v75\ns_waitcnt lgkmcnt(6)\n"
"Qmvav 28,72,80\ns_waitcnt vmcnt(5)\nds_write_b32 v73, v75\nQmvav 28,76,88\nQdv 148,59,1024\ns_waitcnt lgkmcnt(6)\nQa 44,44,104\nQmvav 32,72,112\n"
"Qa 45,45,105\nQa 46,46,106\nQmvav 32,76,120\nQa 47,47,107\ns_waitcnt lgkmcnt(4)\nQa 52,52,136\nQmvav 36,72,144\nQta 64,68,40960\n"
"Qa 53,53,137\nQmvav 36,76,152\nQa 54,54,138\nQa 55,55,139\nQmvav 40,72,176\nQk 23,26,27\nQmvav 40,76,184\nQta 66,68,41984\n"
"s_waitcnt lgkmcnt(2)\nQa 32,32,148\nQmvav 44,72,208\nds_read_b32 v66, v74\nQa 33,33,149\nQmvav 44,76,216\nQa 34,34,150\nQa 35,35,151\n"
"Qmvav 48,72,240\nQta 68,68,49152\nbuffer_store_dwordx4 v[20:23], v60, s[24:27], s68 offen offset:2048 sc1\nQmvav 48,76,248\nQmvav 52,72,84\ns_waitcnt lgkmcnt(1)\nv_cmp_gt_u32 vcc, v75, v66\nQmvav 52,76,116\n"
"s_cbranch_vccnz .Lso2045r1\n.Lro2045r1:\ns_waitcnt lgkmcnt(6)\nQmvaa 28,56,0\nQta 70,68,50176\nQmvaa 28,60,28\nQmvaa 32,56,4\nQmvaa 32,60,32\n"
"Qmvaa 36,56,8\nQta 72,68,57344\ns_nop 3\nQk 28,28,29\nQmvaa 36,60,36\nQk 29,30,31\nQmvaa 40,56,12\nQk 30,32,33\n"
"Qk 31,34,35\nQmvaa 40,60,40\nQta 74,68,58368\nbuffer_store_dwordx4 v[28:31], v60, s[24:27], s68 offen offset:3072 sc1\nQmvaa 44,56,16\nQk 36,36,37\nQk 37,38,39\nQmvaa 44,60,44\n"
"s_add_u32 s68, s64, 8192\nQmvaa 48,56,20\nQta 76,69,0\nQk 38,40,41\nQmvaa 48,60,48\nQk 39,42,43\nQmvaa 52,56,24\nbuffer_store_dwordx4 v[36:39], v60, s[24:27], s68 offen offset:0 sc1\n"
"Qk 44,44,45\nQmvaa 52,60,52\nQta 78,69,1024\nQk 45,46,47\ns_waitcnt lgkmcnt(4)\nQmaaa 200,64,80\nQk 46,48,49\nQk 47,50,51\n"
"Qmaaa 200,68,108\nbuffer_store_dwordx4 v[44:47], v60, s[24:27], s68 offen offset:1024 sc1\nQmaaa 204,64,84\nQk 52,52,53\nQk 53,54,55\nQmaaa 204,68,112\nv_mov_b32 v54, 0\nv_mov_b32 v55, 0\n"
"Qmaaa 208,64,88\nQta 56,69,8192\nbuffer_store_dwordx4 v[52:55], v60, s[24:27], s68 offen offset:2048 sc1\nQmaav 208,68,168\nQmaaa 212,64,92\nQmaav 212,68,200\nQta 58,69,9216\nQmaaa 216,64,96\n"
"Qmaav 216,68,232\nQmaaa 220,64,100\nQta 60,69,16384\nQmaav 220,68,212\nQmaaa 224,64,104\nQmaav 224,68,244\nQta 62,69,17408\ns_waitcnt lgkmcnt(4)\n"
"Qmaav 200,72,80\nQmaav 200,76,88\nQmaav 204,72,112\nQmaav 204,76,120\nQmaav 208,72,144\nQta 64,68,41216\nQmaav 208,76,152\nQmaav 212,72,176\n"
"Qmaav 212,76,184\nQta 66,68,42240\nQmaav 216,72,208\nQmaav 216,76,216\nQmaav 220,72,240\nQta 68,68,49408\nQmaav 220,76,248\nQmaav 224,72,84\n"
"Qmaav 224,76,116\nQta 70,68,50432\ns_waitcnt lgkmcnt(4)\nQmaaa 200,56,0\nQmaaa 200,60,28\nQmaaa 204,56,4\nQmaaa 204,60,32\nQmaaa 208,56,8\n"
"Qta 72,68,57600\nQmaaa 208,60,36\ns_nop 2\nds_write_b128 v58, a[200:203] offset:33792\nQmaaa 212,56,12\nQmaaa 212,60,40\nQta 74,68,58624\nQmaaa 216,56,16\n"
"ds_write_b128 v58, a[204:207] offset:34816\nQmaaa 216,60,44\nQmaaa 220,56,20\nQta 76,69,256\nQmaaa 220,60,48\nds_write_b128 v58, a[208:211] offset:35840\nQmaaa 224,56,24\nQmaaa 224,60,52\n"
"Qta 78,69,1280\ns_waitcnt lgkmcnt(7)\nQmaaa 228,64,80\nds_write_b128 v58, a[212:215] offset:36864\nQmaaa 228,68,108\nQmaaa 232,64,84\ns_waitcnt vmcnt(0)\nbuffer_store_dword v67, v77, s[24:27], 0 offen sc1\n"
"Qmaaa 232,68,112\nds_write_b128 v58, a[216:219] offset:37888\nQmaaa 236,64,88\nQta 56,69,8448\nQmaav 236,68,168\nQmaaa 240,64,92\nds_write_b128 v58, a[220:223] offset:38912\nQmaav 240,68,200\n"
"Qta 58,69,9472\nQmaaa 244,64,96\nQmaav 244,68,232\nds_write_b128 v58, a[224:227] offset:39936\nQmaaa 248,64,100\nQta 60,69,16640\nQmaav 248,68,212\nQmaaa 252,64,104\n"
"Qmaav 252,68,244\nQta 62,69,17664\ns_waitcnt lgkmcnt(8)\nQmaav 228,72,80\nv_add_u32 v75, 1, v75\nQmaav 228,76,88\nds_write_b32 v73, v75\nQmaav 232,72,112\n"
"Qmaav 232,76,120\nQmaav 236,72,144\nQta 64,68,41472\nQmaav 236,76,152\nQmaav 240,72,176\nQmaav 240,76,184\nQta 66,68,42496\nQmaav 244,72,208\n"
"Qmaav 244,76,216\nds_read_b32 v66, v74\nQmaav 248,72,240\nQta 68,68,49664\nQmaav 248,76,248\nQmaav 252,72,84\nbuffer_load_dword v78, v76, s[24:27], 0 offen sc1\nQmaav 252,76,116\n"
"s_waitcnt lgkmcnt(1)\nv_cmp_gt_u32 vcc, v75, v66\ns_waitcnt lgkmcnt(5)\nQmaaa 228,56,0\ns_cbranch_vccnz .Lso2130r1\n.Lro2130r1:\nQmaaa 228,60,28\nQta 70,68,50688\n"
"Qmaaa 232,56,4\nQmaaa 232,60,32\nQdv 180,59,35840\nQmaaa 236,56,8\nQta 72,68,57856\nQmaaa 236,60,36\ns_nop 0\nds_write_b128 v58, a[228:231] offset:0\n"
"Qmaaa 240,56,12\nQdv 104,59,36864\nQmaaa 240,60,40\nQta 74,68,58880\nQmaaa 244,56,16\nds_write_b128 v58, a[232:235] offset:1024\nQmaaa 244,60,44\nQdv 136,59,38912\n"
"Qmaaa 248,56,20\nQta 76,69,512\nQmaaa 248,60,48\nds_write_b128 v58, a[236:239] offset:2048\nQmaaa 252,56,24\nQdv 148,59,33792\ns_waitcnt lgkmcnt(9)\nQa 196,196,180\n"
"Qmaaa 252,60,52\nQta 78,69,1536\nQa 197,197,181\ns_waitcnt lgkmcnt(10)\nQmvaa 132,64,80\nds_write_b128 v58, a[240:243] offset:3072\nQmvaa 132,68,108\nQa 198,198,182\n"
"Qa 199,199,183\nQmvaa 164,64,84\ns_waitcnt lgkmcnt(8)\nQa 228,228,104\nQa 229,229,105\nQmvaa 164,68,112\nds_write_b128 v58, a[244:247] offset:4096\nQmvaa 196,64,88\n"
"Qta 56,69,8704\nQa 230,230,106\nQa 231,231,107\nQmvav 196,68,168\nQdv 180,59,37888\ns_waitcnt lgkmcnt(8)\nQa 140,140,136\nQmvaa 228,64,92\n"
"ds_write_b128 v58, a[248:251] offset:5120\nQmvav 228,68,200\nQta 58,69,9728\nQa 141,141,137\nQmvaa 108,64,96\nQa 142,142,138\nQa 143,143,139\nQmvav 108,68,232\n"
"Qdv 104,59,39936\ns_waitcnt lgkmcnt(8)\nQa 132,132,148\nQmvaa 140,64,100\nQta 60,69,16896\nQa 133,133,149\nQmvav 140,68,212\nds_write_b128 v58, a[252:255] offset:6144\n"
"Qmvaa 172,64,104\nQa 134,134,150\nQa 135,135,151\nQmvav 172,68,244\nQta 62,69,17920\ns_waitcnt lgkmcnt(6)\nQa 108,108,180\ns_waitcnt lgkmcnt(6)\n"
"Qmvav 132,72,80\nQdv 136,59,34816\nv_add_u32 v75, 1, v75\nQmvav 132,76,88\nds_write_b32 v73, v75\nQa 109,109,181\nQmvav 164,72,112\nQa 110,110,182\n"
"Qa 111,111,183\nQmvav 164,76,120\ns_waitcnt lgkmcnt(5)\nQa 172,172,104\nQa 173,173,105\nQmvav 196,72,144\nQta 64,68,41728\nQa 174,174,106\n"
"Qmvav 196,76,152\nQa 175,175,107\nQmvav 228,72,176\nQmvav 228,76,184\nQta 66,68,42752\ns_waitcnt lgkmcnt(3)\nQa 164,164,136\nQmvav 108,72,208\n"
"Qa 165,165,137\nQa 166,166,138\nQmvav 108,76,216\nds_read_b32 v66, v74\nQa 167,167,139\nQmvav 140,72,240\nQta 68,68,49920\nQmvav 140,76,248\n"
"Qmvav 172,72,84\nQmvav 172,76,116\ns_waitcnt lgkmcnt(1)\nv_cmp_gt_u32 vcc, v75, v66\ns_waitcnt lgkmcnt(6)\nQmvaa 132,56,0\ns_cbranch_vccnz .Lso2172r1\n.Lro2172r1:\n"
"Qmvaa 132,60,28\ns_waitcnt vmcnt(0)\nv_sub_u32 v66, v78, v65\nv_cmp_gt_u32 vcc, 1, v66\ns_and_b64 vcc, vcc, s[28:29]\ns_cbranch_scc1 .Lx1_w\n.Lx1_r:\nQmvaa 164,56,4\n"
"s_add_u32 s69, s98, 0\nbuffer_load_dwordx4 a[180:183], v60, s[24:27], s69 offen offset:0 sc1\nQmvaa 164,60,32\nQta 70,68,50944\nQmvaa 196,56,8\nbuffer_load_dwordx4 a[184:187], v60, s[24:27], s69 offen offset:1024 sc1\nQmvaa 196,60,36\nQdv 148,59,2048\n"
"Qmvaa 228,56,12\nbuffer_load_dwordx4 a[188:191], v60, s[24:27], s69 offen offset:2048 sc1\nQmvaa 228,60,40\nQta 72,68,58112\nQmvaa 108,56,16\nbuffer_load_dwordx4 a[192:195], v60, s[24:27], s69 offen offset:3072 sc1\nQmvaa 108,60,44\nQdv 180,59,4096\n"
"Qmvaa 140,56,20\ns_add_u32 s69, s98, 4096\nbuffer_load_dwordx4 a[196:199], v60, s[24:27], s69 offen offset:0 sc1\nQmvaa 140,60,48\nQta 74,68,59136\ns_waitcnt lgkmcnt(3)\nQa 96,96,148\nQmvaa 172,56,24\n"
"buffer_load_dwordx4 a[116:119], v60, s[24:27], s69 offen offset:1024 sc1\nQmvaa 172,60,52\nQdv 104,59,0\nQa 97,97,149\ns_waitcnt lgkmcnt(4)\nQmvaa 204,64,80\nbuffer_load_dwordx4 a[120:123], v60, s[24:27], s69 offen offset:2048 sc1\nQmvaa 204,68,108\n"
"Qta 76,69,768\nQa 98,98,150\nQmvaa 236,64,84\nQdv 136,59,1024\nQa 99,99,151\nQmvaa 236,68,112\ns_waitcnt lgkmcnt(4)\nQa 160,160,180\n"
"Qa 161,161,181\nQmvaa 96,64,88\nQta 78,69,1792\nQa 162,162,182\nQmvav 96,68,168\nQdv 148,59,3072\nQa 163,163,183\nQmvaa 128,64,92\n"
"s_waitcnt lgkmcnt(4)\nQa 204,204,104\nQa 205,205,105\nQmvav 128,68,200\nQta 56,69,8960\nQa 206,206,106\nQmvaa 160,64,96\nQdv 180,59,5120\n"
"Qa 207,207,107\nQmvav 160,68,232\nQmvaa 192,64,100\nQta 58,69,9984\ns_waitcnt lgkmcnt(5)\nQa 236,236,136\nQmvav 192,68,212\nQa 237,237,137\n"
"Qa 238,238,138\nQmvaa 224,64,104\nQa 239,239,139\nQmvav 224,68,244\nQta 60,69,17152\ns_waitcnt lgkmcnt(4)\nQa 128,128,148\ns_waitcnt lgkmcnt(4)\n"
"Qmvav 204,72,80\nQa 129,129,149\nQa 130,130,150\nQmvav 204,76,88\nQa 131,131,151\nQmvav 236,72,112\nQta 62,69,18176\ns_waitcnt lgkmcnt(3)\n"
"Qa 192,192,180\nQmvav 236,76,120\nQa 193,193,181\nQa 194,194,182\nQmvav 96,72,144\nQa 195,195,183\nQmvav 96,76,152\nQmvav 128,72,176\n"
"Qmvav 128,76,184\nQmvav 160,72,208\nQmvav 160,76,216\nQdv 104,59,6144\nQmvav 192,72,240\nQmvav 192,76,248\nv_add_u32 v75, 1, v75\nQmvav 224,72,84\n"
"ds_write_b32 v73, v75\nQmvav 224,76,116\ns_waitcnt lgkmcnt(2)\nQmvaa 204,56,0\nQmvaa 204,60,28\nQmvaa 236,56,4\nQmvaa 236,60,32\ns_waitcnt lgkmcnt(1)\n"
"s_nop 1\nQa 224,224,104\nQa 225,225,105\nQmvaa 96,56,8\nQa 226,226,106\nQa 227,227,107\nQmvaa 96,60,36\nQmvaa 128,56,12\n"
"ds_read_b32 v66, v74\nQmvaa 128,60,40\nQmvaa 160,56,16\nQmvaa 160,60,44\nQmvaa 192,56,20\ns_waitcnt lgkmcnt(0)\nv_cmp_gt_u32 vcc, v75, v66\nQmvaa 192,60,48\n"
"s_cbranch_vccnz .Lso2225r1\n.Lro2225r1:\nQmvaa 224,56,24\nQmvaa 224,60,52\ns_waitcnt vmcnt(6)\nQmvaa 132,174,180\nQmvaa 164,172,180\ns_waitcnt vmcnt(5)\n"
"Qmvaa 196,174,184\nQmvaa 228,172,184\ns_nop 3\nQk 132,132,133\nQk 133,134,135\nds_write_b64 v56, v[132:133] offset:0\ns_waitcnt vmcnt(4)\nQmvaa 108,174,188\n"
"Qk 164,164,165\nQk 165,166,167\nds_write_b64 v56, v[164:165] offset:3328\nQmvaa 140,172,188\nQk 196,196,197\nQk 197,198,199\nds_write_b64 v56, v[196:197] offset:6656\ns_waitcnt vmcnt(3)\n"
"Qmvaa 172,174,192\nQk 228,228,229\nQk 229,230,231\nds_write_b64 v56, v[228:229] offset:9984\nQmvaa 204,172,192\nQk 108,108,109\nQk 109,110,111\nds_write_b64 v56, v[108:109] offset:13312\n"
"s_waitcnt vmcnt(2)\nQmvaa 236,174,196\nQk 140,140,141\nQk 141,142,143\nds_write_b64 v56, v[140:141] offset:16640\n.Lpg25:\ns_cbranch_execz .Ltr8\nQmvaa 96,172,196\n"
"Qk 172,172,173\nQk 173,174,175\nds_write_b64 v56, v[172:173] offset:19968\ns_waitcnt vmcnt(1)\nQmvaa 128,174,116\nQk 204,204,205\nQk 205,206,207\nds_write_b64 v56, v[204:205] offset:32\n"
"Qmvaa 160,172,116\nQk 236,236,237\nQk 237,238,239\nds_write_b64 v56, v[236:237] offset:3360\ns_waitcnt vmcnt(0)\nQmvaa 192,174,120\nQk 96,96,97\nQk 97,98,99\n"
"ds_write_b64 v56, v[96:97] offset:6688\nQmvaa 224,172,120\nQk 128,128,129\nQk 129,130,131\nds_write_b64 v56, v[128:129] offset:10016\nQk 160,160,161\nQk 161,162,163\nds_write_b64 v56, v[160:161] offset:13344\n"
"Qk 192,192,193\nQk 193,194,195\nds_write_b64 v56, v[192:193] offset:16672\nQk 224,224,225\nQk 225,226,227\nds_write_b64 v56, v[224:225] offset:20000\ns_waitcnt lgkmcnt(0)\ns_barrier\n"
"Qda 0,57,0\nQda 4,57,3328\nQda 8,57,6656\nQda 12,57,9984\nQda 16,57,13312\nQda 20,57,16640\nQda 24,57,19968\ns_mov_b32 s68, 0\n"
"s_waitcnt lgkmcnt(6)\nbuffer_store_dwordx4 a[0:3], v64, s[40:43], s68 offen offset:0 sc1\ns_mov_b32 s68, 40960\ns_waitcnt lgkmcnt(5)\nbuffer_store_dwordx4 a[4:7], v64, s[40:43], s68 offen offset:0 sc1\ns_mov_b32 s68, 81920\ns_waitcnt lgkmcnt(4)\nbuffer_store_dwordx4 a[8:11], v64, s[40:43], s68 offen offset:0 sc1\n"
"s_mov_b32 s68, 122880\ns_waitcnt lgkmcnt(3)\nbuffer_store_dwordx4 a[12:15], v64, s[40:43], s68 offen offset:0 sc1\ns_mov_b32 s68, 163840\ns_waitcnt lgkmcnt(2)\nbuffer_store_dwordx4 a[16:19], v64, s[40:43], s68 offen offset:0 sc1\ns_mov_b32 s68, 204800\ns_waitcnt lgkmcnt(1)\n"
"buffer_store_dwordx4 a[20:23], v64, s[40:43], s68 offen offset:0 sc1\ns_mov_b32 s68, 245760\ns_waitcnt lgkmcnt(0)\nbuffer_store_dwordx4 a[24:27], v64, s[40:43], s68 offen offset:0 sc1\n.Lfin:\n.Lpg26:\ns_cbranch_execz .Ltr8\ns_waitcnt vmcnt(0) lgkmcnt(0)\n"
"s_branch .Lexit\n.Lend:\n.Lfb:\ns_waitcnt vmcnt(0) lgkmcnt(0)\nv_lshrrev_b32 v52, 4, v60\nv_and_b32 v53, 15, v52\nv_lshrrev_b32 v52, 4, v52\nv_lshlrev_b32 v66, 8, v53\n"
"v_lshl_add_u32 v66, v52, 4, v66\nv_add_u32 v66, 1024, v66\nv_lshlrev_b32 v67, 10, v52\nv_lshl_add_u32 v67, v53, 1, v67\nv_add_u32 v67, 1024, v67\nv_mov_b32 v52, 0x3f803f80\nv_accvgpr_write_b32 a204, v52\nv_accvgpr_write_b32 a205, v52\n"
"v_accvgpr_write_b32 a206, v52\nv_accvgpr_write_b32 a207, v52\nv_mov_b32 v24, 0xff800000\nv_mov_b32 v28, 0xff800000\nv_mov_b32 v32, 0xff800000\nv_mov_b32 v36, 0xff800000\nv_mov_b32 v40, 0xff800000\nv_mov_b32 v44, 0xff800000\n"
"v_mov_b32 v48, 0xff800000\ns_lshl_b32 s89, s67, 21\ns_mov_b32 s66, 0\n.Lfb1:\ns_add_u32 s68, s89, 4096\nbuffer_load_dwordx4 a[112:115], v66, s[40:43], s89 offen offset:0\nbuffer_load_dwordx4 a[116:119], v66, s[40:43], s89 offen offset:64\nbuffer_load_dwordx4 a[120:123], v66, s[40:43], s89 offen offset:128\n"
"buffer_load_dwordx4 a[124:127], v66, s[40:43], s89 offen offset:192\nbuffer_load_dwordx4 a[128:131], v66, s[40:43], s68 offen offset:0\nbuffer_load_dwordx4 a[132:135], v66, s[40:43], s68 offen offset:64\nbuffer_load_dwordx4 a[136:139], v66, s[40:43], s68 offen offset:128\nbuffer_load_dwordx4 a[140:143], v66, s[40:43], s68 offen offset:192\ns_waitcnt vmcnt(0)\nQzvaa 0,112,0\nQmvaa 0,116,4\n"
"Qmvaa 0,120,8\nQmvaa 0,124,12\nQzvaa 4,128,0\nQmvaa 4,132,4\nQmvaa 4,136,8\nQmvaa 4,140,12\ns_nop 7\nv_max3_f32 v24, v24, v0, v1\n"
"v_max3_f32 v24, v24, v2, v3\nv_max3_f32 v24, v24, v4, v5\nv_max3_f32 v24, v24, v6, v7\nQzvaa 0,112,16\nQmvaa 0,116,20\nQmvaa 0,120,24\nQmvaa 0,124,28\nQzvaa 4,128,16\n"
"Qmvaa 4,132,20\nQmvaa 4,136,24\nQmvaa 4,140,28\ns_nop 7\nv_max3_f32 v28, v28, v0, v1\nv_max3_f32 v28, v28, v2, v3\nv_max3_f32 v28, v28, v4, v5\nv_max3_f32 v28, v28, v6, v7\n"
"Qzvaa 0,112,32\nQmvaa 0,116,36\nQmvaa 0,120,40\nQmvaa 0,124,44\nQzvaa 4,128,32\nQmvaa 4,132,36\nQmvaa 4,136,40\nQmvaa 4,140,44\n"
"s_nop 7\nv_max3_f32 v32, v32, v0, v1\nv_max3_f32 v32, v32, v2, v3\nv_max3_f32 v32, v32, v4, v5\nv_max3_f32 v32, v32, v6, v7\nQzvaa 0,112,48\nQmvaa 0,116,52\nQmvaa 0,120,56\n"
"Qmvaa 0,124,60\nQzvaa 4,128,48\nQmvaa 4,132,52\nQmvaa 4,136,56\nQmvaa 4,140,60\ns_nop 7\nv_max3_f32 v36, v36, v0, v1\nv_max3_f32 v36, v36, v2, v3\n"
"v_max3_f32 v36, v36, v4, v5\nv_max3_f32 v36, v36, v6, v7\nQzvaa 0,112,64\nQmvaa 0,116,68\nQmvaa 0,120,72\nQmvaa 0,124,76\nQzvaa 4,128,64\nQmvaa 4,132,68\n"
"Qmvaa 4,136,72\nQmvaa 4,140,76\ns_nop 7\nv_max3_f32 v40, v40, v0, v1\nv_max3_f32 v40, v40, v2, v3\nv_max3_f32 v40, v40, v4, v5\nv_max3_f32 v40, v40, v6, v7\nQzvaa 0,112,80\n"
"Qmvaa 0,116,84\nQmvaa 0,120,88\nQmvaa 0,124,92\nQzvaa 4,128,80\nQmvaa 4,132,84\nQmvaa 4,136,88\nQmvaa 4,140,92\ns_nop 7\n"
"v_max3_f32 v44, v44, v0, v1\nv_max3_f32 v44, v44, v2, v3\nv_max3_f32 v44, v44, v4, v5\nv_max3_f32 v44, v44, v6, v7\nQzvaa 0,112,96\nQmvaa 0,116,100\nQmvaa 0,120,104\nQmvaa 0,124,108\n"
"Qzvaa 4,128,96\nQmvaa 4,132,100\nQmvaa 4,136,104\nQmvaa 4,140,108\ns_nop 7\nv_max3_f32 v48, v48, v0, v1\nv_max3_f32 v48, v48, v2, v3\nv_max3_f32 v48, v48, v4, v5\n"
"v_max3_f32 v48, v48, v6, v7\ns_add_u32 s89, s89, 8192\ns_add_u32 s66, s66, 1\ns_cmp_lt_u32 s66, 32\ns_cbranch_scc1 .Lfb1\nv_mov_b32 v25, v24\ns_nop 1\nv_permlane32_swap_b32 v24, v25\n"
"v_max_f32 v24, v24, v25\nv_mov_b32 v25, v24\ns_nop 1\nv_permlane16_swap_b32 v24, v25\nv_max_f32 v24, v24, v25\nv_sub_f32 v24, 0, v24\nv_mov_b32 v25, v24\nv_mov_b32 v26, v24\n"
"v_mov_b32 v27, v24\nv_mov_b32 v29, v28\ns_nop 1\nv_permlane32_swap_b32 v28, v29\nv_max_f32 v28, v28, v29\nv_mov_b32 v29, v28\ns_nop 1\nv_permlane16_swap_b32 v28, v29\n"
"v_max_f32 v28, v28, v29\nv_sub_f32 v28, 0, v28\nv_mov_b32 v29, v28\nv_mov_b32 v30, v28\nv_mov_b32 v31, v28\nv_mov_b32 v33, v32\ns_nop 1\nv_permlane32_swap_b32 v32, v33\n"
"v_max_f32 v32, v32, v33\nv_mov_b32 v33, v32\ns_nop 1\nv_permlane16_swap_b32 v32, v33\nv_max_f32 v32, v32, v33\nv_sub_f32 v32, 0, v32\nv_mov_b32 v33, v32\nv_mov_b32 v34, v32\n"
"v_mov_b32 v35, v32\nv_mov_b32 v37, v36\ns_nop 1\nv_permlane32_swap_b32 v36, v37\nv_max_f32 v36, v36, v37\nv_mov_b32 v37, v36\ns_nop 1\nv_permlane16_swap_b32 v36, v37\n"
"v_max_f32 v36, v36, v37\nv_sub_f32 v36, 0, v36\nv_mov_b32 v37, v36\nv_mov_b32 v38, v36\nv_mov_b32 v39, v36\nv_mov_b32 v41, v40\ns_nop 1\nv_permlane32_swap_b32 v40, v41\n"
"v_max_f32 v40, v40, v41\nv_mov_b32 v41, v40\ns_nop 1\nv_permlane16_swap_b32 v40, v41\nv_max_f32 v40, v40, v41\nv_sub_f32 v40, 0, v40\nv_mov_b32 v41, v40\nv_mov_b32 v42, v40\n"
"v_mov_b32 v43, v40\nv_mov_b32 v45, v44\ns_nop 1\nv_permlane32_swap_b32 v44, v45\nv_max_f32 v44, v44, v45\nv_mov_b32 v45, v44\ns_nop 1\nv_permlane16_swap_b32 v44, v45\n"
"v_max_f32 v44, v44, v45\nv_sub_f32 v44, 0, v44\nv_mov_b32 v45, v44\nv_mov_b32 v46, v44\nv_mov_b32 v47, v44\nv_mov_b32 v49, v48\ns_nop 1\nv_permlane32_swap_b32 v48, v49\n"
"v_max_f32 v48, v48, v49\nv_mov_b32 v49, v48\ns_nop 1\nv_permlane16_swap_b32 v48, v49\nv_max_f32 v48, v48, v49\nv_sub_f32 v48, 0, v48\nv_mov_b32 v49, v48\nv_mov_b32 v50, v48\n"
"v_mov_b32 v51, v48\nv_mov_b32 v52, 0\nv_mov_b32 v80, 0\nv_mov_b32 v81, 0\nv_mov_b32 v82, 0\nv_mov_b32 v83, 0\nv_mov_b32 v84, 0\nv_mov_b32 v85, 0\n"
"v_mov_b32 v86, 0\nv_mov_b32 v87, 0\nv_mov_b32 v88, 0\nv_mov_b32 v89, 0\nv_mov_b32 v90, 0\nv_mov_b32 v91, 0\nv_mov_b32 v92, 0\nv_mov_b32 v93, 0\n"
"v_mov_b32 v94, 0\nv_mov_b32 v95, 0\nv_mov_b32 v96, 0\nv_mov_b32 v97, 0\nv_mov_b32 v98, 0\nv_mov_b32 v99, 0\nv_mov_b32 v100, 0\nv_mov_b32 v101, 0\n"
"v_mov_b32 v102, 0\nv_mov_b32 v103, 0\nv_mov_b32 v104, 0\nv_mov_b32 v105, 0\nv_mov_b32 v106, 0\nv_mov_b32 v107, 0\nv_mov_b32 v108, 0\nv_mov_b32 v109, 0\n"
"v_mov_b32 v110, 0\nv_mov_b32 v111, 0\nv_accvgpr_write_b32 a176, v52\nv_accvgpr_write_b32 a177, v52\nv_accvgpr_write_b32 a178, v52\nv_accvgpr_write_b32 a179, v52\nv_mov_b32 v112, 0\nv_mov_b32 v113, 0\n"
"v_mov_b32 v114, 0\nv_mov_b32 v115, 0\nv_mov_b32 v116, 0\nv_mov_b32 v117, 0\nv_mov_b32 v118, 0\nv_mov_b32 v119, 0\nv_mov_b32 v120, 0\nv_mov_b32 v121, 0\n"
"v_mov_b32 v122, 0\nv_mov_b32 v123, 0\nv_mov_b32 v124, 0\nv_mov_b32 v125, 0\nv_mov_b32 v126, 0\nv_mov_b32 v127, 0\nv_mov_b32 v128, 0\nv_mov_b32 v129, 0\n"
"v_mov_b32 v130, 0\nv_mov_b32 v131, 0\nv_mov_b32 v132, 0\nv_mov_b32 v133, 0\nv_mov_b32 v134, 0\nv_mov_b32 v135, 0\nv_mov_b32 v136, 0\nv_mov_b32 v137, 0\n"
"v_mov_b32 v138, 0\nv_mov_b32 v139, 0\nv_mov_b32 v140, 0\nv_mov_b32 v141, 0\nv_mov_b32 v142, 0\nv_mov_b32 v143, 0\nv_accvgpr_write_b32 a180, v52\nv_accvgpr_write_b32 a181, v52\n"
"v_accvgpr_write_b32 a182, v52\nv_accvgpr_write_b32 a183, v52\nv_mov_b32 v144, 0\nv_mov_b32 v145, 0\nv_mov_b32 v146, 0\nv_mov_b32 v147, 0\nv_mov_b32 v148, 0\nv_mov_b32 v149, 0\n"
"v_mov_b32 v150, 0\nv_mov_b32 v151, 0\nv_mov_b32 v152, 0\nv_mov_b32 v153, 0\nv_mov_b32 v154, 0\nv_mov_b32 v155, 0\nv_mov_b32 v156, 0\nv_mov_b32 v157, 0\n"
"v_mov_b32 v158, 0\nv_mov_b32 v159, 0\nv_mov_b32 v160, 0\nv_mov_b32 v161, 0\nv_mov_b32 v162, 0\nv_mov_b32 v163, 0\nv_mov_b32 v164, 0\nv_mov_b32 v165, 0\n"
"v_mov_b32 v166, 0\nv_mov_b32 v167, 0\nv_mov_b32 v168, 0\nv_mov_b32 v169, 0\nv_mov_b32 v170, 0\nv_mov_b32 v171, 0\nv_mov_b32 v172, 0\nv_mov_b32 v173, 0\n"
"v_mov_b32 v174, 0\nv_mov_b32 v175, 0\nv_accvgpr_write_b32 a184, v52\nv_accvgpr_write_b32 a185, v52\nv_accvgpr_write_b32 a186, v52\nv_accvgpr_write_b32 a187, v52\nv_mov_b32 v176, 0\nv_mov_b32 v177, 0\n"
"v_mov_b32 v178, 0\nv_mov_b32 v179, 0\nv_mov_b32 v180, 0\nv_mov_b32 v181, 0\nv_mov_b32 v182, 0\nv_mov_b32 v183, 0\nv_mov_b32 v184, 0\nv_mov_b32 v185, 0\n"
"v_mov_b32 v186, 0\nv_mov_b32 v187, 0\nv_mov_b32 v188, 0\nv_mov_b32 v189, 0\nv_mov_b32 v190, 0\nv_mov_b32 v191, 0\nv_mov_b32 v192, 0\nv_mov_b32 v193, 0\n"
"v_mov_b32 v194, 0\nv_mov_b32 v195, 0\nv_mov_b32 v196, 0\nv_mov_b32 v197, 0\nv_mov_b32 v198, 0\nv_mov_b32 v199, 0\nv_mov_b32 v200, 0\nv_mov_b32 v201, 0\n"
"v_mov_b32 v202, 0\nv_mov_b32 v203, 0\nv_mov_b32 v204, 0\nv_mov_b32 v205, 0\nv_mov_b32 v206, 0\nv_mov_b32 v207, 0\nv_accvgpr_write_b32 a188, v52\nv_accvgpr_write_b32 a189, v52\n"
"v_accvgpr_write_b32 a190, v52\nv_accvgpr_write_b32 a191, v52\nv_mov_b32 v208, 0\nv_mov_b32 v209, 0\nv_mov_b32 v210, 0\nv_mov_b32 v211, 0\nv_mov_b32 v212, 0\nv_mov_b32 v213, 0\n"
"v_mov_b32 v214, 0\nv_mov_b32 v215, 0\nv_mov_b32 v216, 0\nv_mov_b32 v217, 0\nv_mov_b32 v218, 0\nv_mov_b32 v219, 0\nv_mov_b32 v220, 0\nv_mov_b32 v221, 0\n"
"v_mov_b32 v222, 0\nv_mov_b32 v223, 0\nv_mov_b32 v224, 0\nv_mov_b32 v225, 0\nv_mov_b32 v226, 0\nv_mov_b32 v227, 0\nv_mov_b32 v228, 0\nv_mov_b32 v229, 0\n"
"v_mov_b32 v230, 0\nv_mov_b32 v231, 0\nv_mov_b32 v232, 0\nv_mov_b32 v233, 0\nv_mov_b32 v234, 0\nv_mov_b32 v235, 0\nv_mov_b32 v236, 0\nv_mov_b32 v237, 0\n"
"v_mov_b32 v238, 0\nv_mov_b32 v239, 0\nv_accvgpr_write_b32 a192, v52\nv_accvgpr_write_b32 a193, v52\nv_accvgpr_write_b32 a194, v52\nv_accvgpr_write_b32 a195, v52\nv_mov_b32 v240, 0\nv_mov_b32 v241, 0\n"
"v_mov_b32 v242, 0\nv_mov_b32 v243, 0\nv_mov_b32 v244, 0\nv_mov_b32 v245, 0\nv_mov_b32 v246, 0\nv_mov_b32 v247, 0\nv_mov_b32 v248, 0\nv_mov_b32 v249, 0\n"
"v_mov_b32 v250, 0\nv_mov_b32 v251, 0\nv_mov_b32 v252, 0\nv_mov_b32 v253, 0\nv_mov_b32 v254, 0\nv_mov_b32 v255, 0\nv_accvgpr_write_b32 a208, v52\nv_accvgpr_write_b32 a209, v52\n"
"v_accvgpr_write_b32 a210, v52\nv_accvgpr_write_b32 a211, v52\nv_accvgpr_write_b32 a212, v52\nv_accvgpr_write_b32 a213, v52\nv_accvgpr_write_b32 a214, v52\nv_accvgpr_write_b32 a215, v52\nv_accvgpr_write_b32 a216, v52\nv_accvgpr_write_b32 a217, v52\n"
"v_accvgpr_write_b32 a218, v52\nv_accvgpr_write_b32 a219, v52\nv_accvgpr_write_b32 a220, v52\nv_accvgpr_write_b32 a221, v52\nv_accvgpr_write_b32 a222, v52\nv_accvgpr_write_b32 a223, v52\nv_accvgpr_write_b32 a196, v52\nv_accvgpr_write_b32 a197, v52\n"
"v_accvgpr_write_b32 a198, v52\nv_accvgpr_write_b32 a199, v52\nv_accvgpr_write_b32 a224, v52\nv_accvgpr_write_b32 a225, v52\nv_accvgpr_write_b32 a226, v52\nv_accvgpr_write_b32 a227, v52\nv_accvgpr_write_b32 a228, v52\nv_accvgpr_write_b32 a229, v52\n"
"v_accvgpr_write_b32 a230, v52\nv_accvgpr_write_b32 a231, v52\nv_accvgpr_write_b32 a232, v52\nv_accvgpr_write_b32 a233, v52\nv_accvgpr_write_b32 a234, v52\nv_accvgpr_write_b32 a235, v52\nv_accvgpr_write_b32 a236, v52\nv_accvgpr_write_b32 a237, v52\n"
"v_accvgpr_write_b32 a238, v52\nv_accvgpr_write_b32 a239, v52\nv_accvgpr_write_b32 a240, v52\nv_accvgpr_write_b32 a241, v52\nv_accvgpr_write_b32 a242, v52\nv_accvgpr_write_b32 a243, v52\nv_accvgpr_write_b32 a244, v52\nv_accvgpr_write_b32 a245, v52\n"
"v_accvgpr_write_b32 a246, v52\nv_accvgpr_write_b32 a247, v52\nv_accvgpr_write_b32 a248, v52\nv_accvgpr_write_b32 a249, v52\nv_accvgpr_write_b32 a250, v52\nv_accvgpr_write_b32 a251, v52\nv_accvgpr_write_b32 a252, v52\nv_accvgpr_write_b32 a253, v52\n"
"v_accvgpr_write_b32 a254, v52\nv_accvgpr_write_b32 a255, v52\nv_accvgpr_write_b32 a200, v52\nv_accvgpr_write_b32 a201, v52\nv_accvgpr_write_b32 a202, v52\nv_accvgpr_write_b32 a203, v52\ns_lshl_b32 s89, s67, 21\ns_mov_b32 s66, 0\n"
".Lfb2:\ns_add_u32 s68, s89, 4096\nbuffer_load_dwordx4 a[112:115], v66, s[40:43], s89 offen offset:0\nbuffer_load_dwordx4 a[116:119], v66, s[40:43], s89 offen offset:64\nbuffer_load_dwordx4 a[120:123], v66, s[40:43], s89 offen offset:128\nbuffer_load_dwordx4 a[124:127], v66, s[40:43], s89 offen offset:192\nbuffer_load_dwordx4 a[128:131], v66, s[40:43], s68 offen offset:0\nbuffer_load_dwordx4 a[132:135], v66, s[40:43], s68 offen offset:64\n"
"buffer_load_dwordx4 a[136:139], v66, s[40:43], s68 offen offset:128\nbuffer_load_dwordx4 a[140:143], v66, s[40:43], s68 offen offset:192\nbuffer_load_ushort v8, v67, s[44:47], s89 offen offset:0\nbuffer_load_ushort v9, v67, s[44:47], s89 offen offset:256\nbuffer_load_ushort v10, v67, s[44:47], s89 offen offset:512\nbuffer_load_ushort v11, v67, s[44:47], s89 offen offset:768\nbuffer_load_ushort v12, v67, s[44:47], s68 offen offset:0\nbuffer_load_ushort v13, v67, s[44:47], s68 offen offset:256\n"
"buffer_load_ushort v14, v67, s[44:47], s68 offen offset:512\nbuffer_load_ushort v15, v67, s[44:47], s68 offen offset:768\ns_waitcnt vmcnt(0)\nv_lshl_or_b32 v8, v9, 16, v8\nv_accvgpr_write_b32 a144, v8\nv_lshl_or_b32 v10, v11, 16, v10\nv_accvgpr_write_b32 a145, v10\nv_lshl_or_b32 v12, v13, 16, v12\n"
"v_accvgpr_write_b32 a146, v12\nv_lshl_or_b32 v14, v15, 16, v14\nv_accvgpr_write_b32 a147, v14\nbuffer_load_ushort v8, v67, s[44:47], s89 offen offset:32\nbuffer_load_ushort v9, v67, s[44:47], s89 offen offset:288\nbuffer_load_ushort v10, v67, s[44:47], s89 offen offset:544\nbuffer_load_ushort v11, v67, s[44:47], s89 offen offset:800\nbuffer_load_ushort v12, v67, s[44:47], s68 offen offset:32\n"
"buffer_load_ushort v13, v67, s[44:47], s68 offen offset:288\nbuffer_load_ushort v14, v67, s[44:47], s68 offen offset:544\nbuffer_load_ushort v15, v67, s[44:47], s68 offen offset:800\ns_waitcnt vmcnt(0)\nv_lshl_or_b32 v8, v9, 16, v8\nv_accvgpr_write_b32 a148, v8\nv_lshl_or_b32 v10, v11, 16, v10\nv_accvgpr_write_b32 a149, v10\n"
"v_lshl_or_b32 v12, v13, 16, v12\nv_accvgpr_write_b32 a150, v12\nv_lshl_or_b32 v14, v15, 16, v14\nv_accvgpr_write_b32 a151, v14\nbuffer_load_ushort v8, v67, s[44:47], s89 offen offset:64\nbuffer_load_ushort v9, v67, s[44:47], s89 offen offset:320\nbuffer_load_ushort v10, v67, s[44:47], s89 offen offset:576\nbuffer_load_ushort v11, v67, s[44:47], s89 offen offset:832\n"
"buffer_load_ushort v12, v67, s[44:47], s68 offen offset:64\nbuffer_load_ushort v13, v67, s[44:47], s68 offen offset:320\nbuffer_load_ushort v14, v67, s[44:47], s68 offen offset:576\nbuffer_load_ushort v15, v67, s[44:47], s68 offen offset:832\ns_waitcnt vmcnt(0)\nv_lshl_or_b32 v8, v9, 16, v8\nv_accvgpr_write_b32 a152, v8\nv_lshl_or_b32 v10, v11, 16, v10\n"
"v_accvgpr_write_b32 a153, v10\nv_lshl_or_b32 v12, v13, 16, v12\nv_accvgpr_write_b32 a154, v12\nv_lshl_or_b32 v14, v15, 16, v14\nv_accvgpr_write_b32 a155, v14\nbuffer_load_ushort v8, v67, s[44:47], s89 offen offset:96\nbuffer_load_ushort v9, v67, s[44:47], s89 offen offset:352\nbuffer_load_ushort v10, v67, s[44:47], s89 offen offset:608\n"
"buffer_load_ushort v11, v67, s[44:47], s89 offen offset:864\nbuffer_load_ushort v12, v67, s[44:47], s68 offen offset:96\nbuffer_load_ushort v13, v67, s[44:47], s68 offen offset:352\nbuffer_load_ushort v14, v67, s[44:47], s68 offen offset:608\nbuffer_load_ushort v15, v67, s[44:47], s68 offen offset:864\ns_waitcnt vmcnt(0)\nv_lshl_or_b32 v8, v9, 16, v8\nv_accvgpr_write_b32 a156, v8\n"
"v_lshl_or_b32 v10, v11, 16, v10\nv_accvgpr_write_b32 a157, v10\nv_lshl_or_b32 v12, v13, 16, v12\nv_accvgpr_write_b32 a158, v12\nv_lshl_or_b32 v14, v15, 16, v14\nv_accvgpr_write_b32 a159, v14\nbuffer_load_ushort v8, v67, s[44:47], s89 offen offset:128\nbuffer_load_ushort v9, v67, s[44:47], s89 offen offset:384\n"
"buffer_load_ushort v10, v67, s[44:47], s89 offen offset:640\nbuffer_load_ushort v11, v67, s[44:47], s89 offen offset:896\nbuffer_load_ushort v12, v67, s[44:47], s68 offen offset:128\nbuffer_load_ushort v13, v67, s[44:47], s68 offen offset:384\nbuffer_load_ushort v14, v67, s[44:47], s68 offen offset:640\nbuffer_load_ushort v15, v67, s[44:47], s68 offen offset:896\ns_waitcnt vmcnt(0)\nv_lshl_or_b32 v8, v9, 16, v8\n"
"v_accvgpr_write_b32 a160, v8\nv_lshl_or_b32 v10, v11, 16, v10\nv_accvgpr_write_b32 a161, v10\nv_lshl_or_b32 v12, v13, 16, v12\nv_accvgpr_write_b32 a162, v12\nv_lshl_or_b32 v14, v15, 16, v14\nv_accvgpr_write_b32 a163, v14\nbuffer_load_ushort v8, v67, s[44:47], s89 offen offset:160\n"
"buffer_load_ushort v9, v67, s[44:47], s89 offen offset:416\nbuffer_load_ushort v10, v67, s[44:47], s89 offen offset:672\nbuffer_load_ushort v11, v67, s[44:47], s89 offen offset:928\nbuffer_load_ushort v12, v67, s[44:47], s68 offen offset:160\nbuffer_load_ushort v13, v67, s[44:47], s68 offen offset:416\nbuffer_load_ushort v14, v67, s[44:47], s68 offen offset:672\nbuffer_load_ushort v15, v67, s[44:47], s68 offen offset:928\ns_waitcnt vmcnt(0)\n"
"v_lshl_or_b32 v8, v9, 16, v8\nv_accvgpr_write_b32 a164, v8\nv_lshl_or_b32 v10, v11, 16, v10\nv_accvgpr_write_b32 a165, v10\nv_lshl_or_b32 v12, v13, 16, v12\nv_accvgpr_write_b32 a166, v12\nv_lshl_or_b32 v14, v15, 16, v14\nv_accvgpr_write_b32 a167, v14\n"
"buffer_load_ushort v8, v67, s[44:47], s89 offen offset:192\nbuffer_load_ushort v9, v67, s[44:47], s89 offen offset:448\nbuffer_load_ushort v10, v67, s[44:47], s89 offen offset:704\nbuffer_load_ushort v11, v67, s[44:47], s89 offen offset:960\nbuffer_load_ushort v12, v67, s[44:47], s68 offen offset:192\nbuffer_load_ushort v13, v67, s[44:47], s68 offen offset:448\nbuffer_load_ushort v14, v67, s[44:47], s68 offen offset:704\nbuffer_load_ushort v15, v67, s[44:47], s68 offen offset:960\n"
"s_waitcnt vmcnt(0)\nv_lshl_or_b32 v8, v9, 16, v8\nv_accvgpr_write_b32 a168, v8\nv_lshl_or_b32 v10, v11, 16, v10\nv_accvgpr_write_b32 a169, v10\nv_lshl_or_b32 v12, v13, 16, v12\nv_accvgpr_write_b32 a170, v12\nv_lshl_or_b32 v14, v15, 16, v14\n"
"v_accvgpr_write_b32 a171, v14\nbuffer_load_ushort v8, v67, s[44:47], s89 offen offset:224\nbuffer_load_ushort v9, v67, s[44:47], s89 offen offset:480\nbuffer_load_ushort v10, v67, s[44:47], s89 offen offset:736\nbuffer_load_ushort v11, v67, s[44:47], s89 offen offset:992\nbuffer_load_ushort v12, v67, s[44:47], s68 offen offset:224\nbuffer_load_ushort v13, v67, s[44:47], s68 offen offset:480\nbuffer_load_ushort v14, v67, s[44:47], s68 offen offset:736\n"
"buffer_load_ushort v15, v67, s[44:47], s68 offen offset:992\ns_waitcnt vmcnt(0)\nv_lshl_or_b32 v8, v9, 16, v8\nv_accvgpr_write_b32 a172, v8\nv_lshl_or_b32 v10, v11, 16, v10\nv_accvgpr_write_b32 a173, v10\nv_lshl_or_b32 v12, v13, 16, v12\nv_accvgpr_write_b32 a174, v12\n"
"v_lshl_or_b32 v14, v15, 16, v14\nv_accvgpr_write_b32 a175, v14\nQcvaa 0,112,0,24\nQmvaa 0,116,4\nQmvaa 0,120,8\nQmvaa 0,124,12\nQcvaa 4,128,0,24\nQmvaa 4,132,4\n"
"Qmvaa 4,136,8\nQmvaa 4,140,12\ns_nop 7\nQe 0\nQe 1\nQe 2\nQe 3\nQe 4\n"
"Qe 5\nQe 6\nQe 7\ns_nop 0\nQk 16,0,1\nQk 17,2,3\nQk 18,4,5\nQk 19,6,7\n"
"s_nop 1\nQmvva 80,16,144\nQmvva 84,16,148\nQmvva 88,16,152\nQmvva 92,16,156\nQmvva 96,16,160\nQmvva 100,16,164\nQmvva 104,16,168\n"
"Qmvva 108,16,172\nQmaav 176,204,16\nQcvaa 0,112,16,28\nQmvaa 0,116,20\nQmvaa 0,120,24\nQmvaa 0,124,28\nQcvaa 4,128,16,28\nQmvaa 4,132,20\n"
"Qmvaa 4,136,24\nQmvaa 4,140,28\ns_nop 7\nQe 0\nQe 1\nQe 2\nQe 3\nQe 4\n"
"Qe 5\nQe 6\nQe 7\ns_nop 0\nQk 16,0,1\nQk 17,2,3\nQk 18,4,5\nQk 19,6,7\n"
"s_nop 1\nQmvva 112,16,144\nQmvva 116,16,148\nQmvva 120,16,152\nQmvva 124,16,156\nQmvva 128,16,160\nQmvva 132,16,164\nQmvva 136,16,168\n"
"Qmvva 140,16,172\nQmaav 180,204,16\nQcvaa 0,112,32,32\nQmvaa 0,116,36\nQmvaa 0,120,40\nQmvaa 0,124,44\nQcvaa 4,128,32,32\nQmvaa 4,132,36\n"
"Qmvaa 4,136,40\nQmvaa 4,140,44\ns_nop 7\nQe 0\nQe 1\nQe 2\nQe 3\nQe 4\n"
"Qe 5\nQe 6\nQe 7\ns_nop 0\nQk 16,0,1\nQk 17,2,3\nQk 18,4,5\nQk 19,6,7\n"
"s_nop 1\nQmvva 144,16,144\nQmvva 148,16,148\nQmvva 152,16,152\nQmvva 156,16,156\nQmvva 160,16,160\nQmvva 164,16,164\nQmvva 168,16,168\n"
"Qmvva 172,16,172\nQmaav 184,204,16\nQcvaa 0,112,48,36\nQmvaa 0,116,52\nQmvaa 0,120,56\nQmvaa 0,124,60\nQcvaa 4,128,48,36\nQmvaa 4,132,52\n"
"Qmvaa 4,136,56\nQmvaa 4,140,60\ns_nop 7\nQe 0\nQe 1\nQe 2\nQe 3\nQe 4\n"
"Qe 5\nQe 6\nQe 7\ns_nop 0\nQk 16,0,1\nQk 17,2,3\nQk 18,4,5\nQk 19,6,7\n"
"s_nop 1\nQmvva 176,16,144\nQmvva 180,16,148\nQmvva 184,16,152\nQmvva 188,16,156\nQmvva 192,16,160\nQmvva 196,16,164\nQmvva 200,16,168\n"
"Qmvva 204,16,172\nQmaav 188,204,16\nQcvaa 0,112,64,40\nQmvaa 0,116,68\nQmvaa 0,120,72\nQmvaa 0,124,76\nQcvaa 4,128,64,40\nQmvaa 4,132,68\n"
"Qmvaa 4,136,72\nQmvaa 4,140,76\ns_nop 7\nQe 0\nQe 1\nQe 2\nQe 3\nQe 4\n"
"Qe 5\nQe 6\nQe 7\ns_nop 0\nQk 16,0,1\nQk 17,2,3\nQk 18,4,5\nQk 19,6,7\n"
"s_nop 1\nQmvva 208,16,144\nQmvva 212,16,148\nQmvva 216,16,152\nQmvva 220,16,156\nQmvva 224,16,160\nQmvva 228,16,164\nQmvva 232,16,168\n"
"Qmvva 236,16,172\nQmaav 192,204,16\nQcvaa 0,112,80,44\nQmvaa 0,116,84\nQmvaa 0,120,88\nQmvaa 0,124,92\nQcvaa 4,128,80,44\nQmvaa 4,132,84\n"
"Qmvaa 4,136,88\nQmvaa 4,140,92\ns_nop 7\nQe 0\nQe 1\nQe 2\nQe 3\nQe 4\n"
"Qe 5\nQe 6\nQe 7\ns_nop 0\nQk 16,0,1\nQk 17,2,3\nQk 18,4,5\nQk 19,6,7\n"
"s_nop 1\nQmvva 240,16,144\nQmvva 244,16,148\nQmvva 248,16,152\nQmvva 252,16,156\nQmava 208,16,160\nQmava 212,16,164\nQmava 216,16,168\n"
"Qmava 220,16,172\nQmaav 196,204,16\nQcvaa 0,112,96,48\nQmvaa 0,116,100\nQmvaa 0,120,104\nQmvaa 0,124,108\nQcvaa 4,128,96,48\nQmvaa 4,132,100\n"
"Qmvaa 4,136,104\nQmvaa 4,140,108\ns_nop 7\nQe 0\nQe 1\nQe 2\nQe 3\nQe 4\n"
"Qe 5\nQe 6\nQe 7\ns_nop 0\nQk 16,0,1\nQk 17,2,3\nQk 18,4,5\nQk 19,6,7\n"
"s_nop 1\nQmava 224,16,144\nQmava 228,16,148\nQmava 232,16,152\nQmava 236,16,156\nQmava 240,16,160\nQmava 244,16,164\nQmava 248,16,168\n"
"Qmava 252,16,172\nQmaav 200,204,16\ns_add_u32 s89, s89, 8192\ns_add_u32 s66, s66, 1\ns_cmp_lt_u32 s66, 32\ns_cbranch_scc1 .Lfb2\ns_nop 7\nv_accvgpr_read_b32 v0, a176\n"
"v_accvgpr_read_b32 v4, a180\nv_accvgpr_read_b32 v8, a184\nv_accvgpr_read_b32 v12, a188\nv_accvgpr_read_b32 v16, a192\nv_accvgpr_read_b32 v20, a196\nv_accvgpr_read_b32 v24, a200\ns_cmp_eq_u32 s67, 0\ns_cbranch_scc1 .Lfbret0\n"
"s_branch .Lfbret1\n.Lf2_w:\ns_mov_b32 s97, 0\n.Lf2_l:\ns_sleep 1\nbuffer_load_dword v78, v76, s[24:27], 0 offen sc1\ns_waitcnt vmcnt(0)\ns_add_u32 s97, s97, 1\n"
"s_cmp_gt_u32 s97, 1048576\ns_cbranch_scc1 .Lf2_r\nv_sub_u32 v66, v78, v65\nv_cmp_gt_u32 vcc, 2, v66\ns_and_b64 vcc, vcc, s[28:29]\ns_cbranch_scc1 .Lf2_l\ns_branch .Lf2_r\n.Lf3_w:\n"
"s_mov_b32 s97, 0\n.Lf3_l:\ns_sleep 1\nbuffer_load_dword v78, v76, s[24:27], 0 offen sc1\ns_waitcnt vmcnt(0)\ns_add_u32 s97, s97, 1\ns_cmp_gt_u32 s97, 1048576\ns_cbranch_scc1 .Lf3_r\n"
"v_sub_u32 v66, v78, v65\nv_cmp_gt_u32 vcc, 3, v66\ns_and_b64 vcc, vcc, s[28:29]\ns_cbranch_scc1 .Lf3_l\ns_branch .Lf3_r\n.Lx0_w:\ns_mov_b32 s97, 0\n.Lx0_l:\n"
"s_sleep 1\nbuffer_load_dword v78, v76, s[24:27], 0 offen sc1\ns_waitcnt vmcnt(0)\ns_add_u32 s97, s97, 1\ns_cmp_gt_u32 s97, 1048576\ns_cbranch_scc1 .Lx0_r\nv_sub_u32 v66, v78, v65\nv_cmp_gt_u32 vcc, 1, v66\n"
"s_and_b64 vcc, vcc, s[28:29]\ns_cbranch_scc1 .Lx0_l\ns_branch .Lx0_r\n.Lx1_w:\ns_mov_b32 s97, 0\n.Lx1_l:\ns_sleep 1\nbuffer_load_dword v78, v76, s[24:27], 0 offen sc1\n"
"s_waitcnt vmcnt(0)\ns_add_u32 s97, s97, 1\ns_cmp_gt_u32 s97, 1048576\ns_cbranch_scc1 .Lx1_r\nv_sub_u32 v66, v78, v65\nv_cmp_gt_u32 vcc, 1, v66\ns_and_b64 vcc, vcc, s[28:29]\ns_cbranch_scc1 .Lx1_l\n"
"s_branch .Lx1_r\n.Lexit:\n"
  :: "s"((unsigned)kp), "s"((unsigned)(kp >> 32)), "s"((unsigned)vp), "s"((unsigned)(vp >> 32)), "s"((unsigned)xp), "s"((unsigned)(xp >> 32)),
     "s"((unsigned)wp), "s"((unsigned)(wp >> 32)), "s"((unsigned)op), "s"((unsigned)(op >> 32)), "s"(ldsb), "s"(wid), "s"(head0),
     "s"((unsigned)dp), "s"((unsigned)(dp >> 32)), "s"((unsigned)wop), "s"((unsigned)(wop >> 32)), "s"((unsigned)yp), "s"((unsigned)(yp >> 32)),
     "s"(hg), "s"(b * 8u + hg)
  : "v0", "v1", "v2", "v3", "v4", "v5", "v6", "v7", "v8", "v9", "v10", "v11", "v12", "v13", "v14", "v15", "v16", "v17", "v18", "v19", "v20", "v21", "v22", "v23", "v24", "v25", "v26", "v27", "v28", "v29", "v30", "v31", "v32", "v33", "v34", "v35", "v36", "v37", "v38", "v39", "v40", "v41", "v42", "v43", "v44", "v45", "v46", "v47", "v48", "v49", "v50", "v51", "v52", "v53", "v54", "v55", "v56", "v57", "v58", "v59", "v60", "v61", "v62", "v63", "v64", "v65", "v66", "v67", "v68", "v69", "v70", "v71", "v72", "v73", "v74", "v75", "v76", "v77", "v78", "v79", "v80", "v81", "v82", "v83", "v84", "v85", "v86", "v87", "v88", "v89", "v90", "v91", "v92", "v93", "v94", "v95", "v96", "v97", "v98", "v99", "v100", "v101", "v102", "v103", "v104", "v105", "v106", "v107", "v108", "v109", "v110", "v111", "v112", "v113", "v114", "v115", "v116", "v117", "v118", "v119", "v120", "v121", "v122", "v123", "v124", "v125", "v126", "v127", "v128", "v129", "v130", "v131", "v132", "v133", "v134", "v135", "v136", "v137", "v138", "v139", "v140", "v141", "v142", "v143", "v144", "v145", "v146", "v147", "v148", "v149", "v150", "v151", "v152", "v153", "v154", "v155", "v156", "v157", "v158", "v159", "v160", "v161", "v162", "v163", "v164", "v165", "v166", "v167", "v168", "v169", "v170", "v171", "v172", "v173", "v174", "v175", "v176", "v177", "v178", "v179", "v180", "v181", "v182", "v183", "v184", "v185", "v186", "v187", "v188", "v189", "v190", "v191", "v192", "v193", "v194", "v195", "v196", "v197", "v198", "v199", "v200", "v201", "v202", "v203", "v204", "v205", "v206", "v207", "v208", "v209", "v210", "v211", "v212", "v213", "v214", "v215", "v216", "v217", "v218", "v219", "v220", "v221", "v222", "v223", "v224", "v225", "v226", "v227", "v228", "v229", "v230", "v231", "v232", "v233", "v234", "v235", "v236", "v237", "v238", "v239", "v240", "v241", "v242", "v243", "v244", "v245", "v246", "v247", "v248", "v249", "v250", "v251", "v252", "v253", "v254", "v255", "a0", "a1", "a2", "a3", "a4", "a5", "a6", "a7", "a8", "a9", "a10", "a11", "a12", "a13", "a14", "a15", "a16", "a17", "a18", "a19", "a20", "a21", "a22", "a23", "a24", "a25", "a26", "a27", "a28", "a29", "a30", "a31", "a32", "a33", "a34", "a35", "a36", "a37", "a38", "a39", "a40", "a41", "a42", "a43", "a44", "a45", "a46", "a47", "a48", "a49", "a50", "a51", "a52", "a53", "a54", "a55", "a56", "a57", "a58", "a59", "a60", "a61", "a62", "a63", "a64", "a65", "a66", "a67", "a68", "a69", "a70", "a71", "a72", "a73", "a74", "a75", "a76", "a77", "a78", "a79", "a80", "a81", "a82", "a83", "a84", "a85", "a86", "a87", "a88", "a89", "a90", "a91", "a92", "a93", "a94", "a95", "a96", "a97", "a98", "a99", "a100", "a101", "a102", "a103", "a104", "a105", "a106", "a107", "a108", "a109", "a110", "a111", "a112", "a113", "a114", "a115", "a116", "a117", "a118", "a119", "a120", "a121", "a122", "a123", "a124", "a125", "a126", "a127", "a128", "a129", "a130", "a131", "a132", "a133", "a134", "a135", "a136", "a137", "a138", "a139", "a140", "a141", "a142", "a143", "a144", "a145", "a146", "a147", "a148", "a149", "a150", "a151", "a152", "a153", "a154", "a155", "a156", "a157", "a158", "a159", "a160", "a161", "a162", "a163", "a164", "a165", "a166", "a167", "a168", "a169", "a170", "a171", "a172", "a173", "a174", "a175", "a176", "a177", "a178", "a179", "a180", "a181", "a182", "a183", "a184", "a185", "a186", "a187", "a188", "a189", "a190", "a191", "a192", "a193", "a194", "a195", "a196", "a197", "a198", "a199", "a200", "a201", "a202", "a203", "a204", "a205", "a206", "a207", "a208", "a209", "a210", "a211", "a212", "a213", "a214", "a215", "a216", "a217", "a218", "a219", "a220", "a221", "a222", "a223", "a224", "a225", "a226", "a227", "a228", "a229", "a230", "a231", "a232", "a233", "a234", "a235", "a236", "a237", "a238", "a239", "a240", "a241", "a242", "a243", "a244", "a245", "a246", "a247", "a248", "a249", "a250", "a251", "a252", "a253", "a254", "a255", "s24", "s25", "s26", "s27", "s28", "s29", "s30", "s31", "s40", "s41", "s42", "s43", "s44", "s45", "s46", "s47", "s48", "s49", "s50", "s51", "s52", "s53", "s54", "s55", "s56", "s57", "s58", "s59", "s60", "s61", "s62", "s63", "s64", "s65", "s66", "s67", "s68", "s69", "s70", "s71", "s72", "s73", "s74", "s75", "s76", "s77", "s78", "s79", "s80", "s81", "s82", "s83", "s84", "s85", "s86", "s87", "s88", "s89", "s90", "s91", "s92", "s93", "s94", "s95", "s96", "s97", "s98", "s99", "vcc", "scc", "memory");
}

extern "C" void kernel_launch(void* const* d_in, const int* in_sizes, int n_in, void* d_out, int out_size, void* d_ws, size_t ws_size, hipStream_t stream) {
  typedef const unsigned short* P;
  (void)hipFuncSetAttribute((const void*)mha2_asm, hipFuncAttributeMaxDynamicSharedMemorySize, 163840);
  hipLaunchKernelGGL(mha2_asm, dim3(256), dim3(256), 163840, stream, (P)d_in[0], (P)d_in[5], (P)d_in[3], (P)d_in[8], (P)d_in[1], (P)d_in[6], (P)d_in[2], (P)d_in[7],
                     (P)d_in[4], (P)d_in[9], (char*)d_ws, (unsigned short*)d_out);
}
```

```cpp
#include <hip/hip_runtime.h>
#include <stdint.h>

__global__ __launch_bounds__(256, 1) __attribute__((aligned(4096)))
void mha2_asm(const unsigned short* __restrict__ X0, const unsigned short* __restrict__ X1, const unsigned short* __restrict__ W0, const unsigned short* __restrict__ W1,
              const unsigned short* __restrict__ K0, const unsigned short* __restrict__ K1,
              const unsigned short* __restrict__ V0, const unsigned short* __restrict__ V1,
              const unsigned short* __restrict__ WO0, const unsigned short* __restrict__ WO1, char* ws, unsigned short* out) {
  extern __shared__ __attribute__((aligned(16))) char lds[];
  const unsigned c = blockIdx.x;
  const unsigned xcd = c & 7u, idx = c >> 3;
  const unsigned hg = idx & 7u, bb = xcd * 4u + (idx >> 3);
  const unsigned bk = bb >> 4, b = bb & 15u;
  const unsigned wid = __builtin_amdgcn_readfirstlane(threadIdx.x >> 6);
  const unsigned head0 = (hg >> 2) * 16u + (hg & 3u) * 2u + (wid >> 1);
  const unsigned long long kp = (unsigned long long)(bk ? K1 : K0) + ((unsigned long long)b * 32u + head0) * 262144ull - 1024ull;
  const unsigned long long vp = (unsigned long long)(bk ? V1 : V0) + ((unsigned long long)b * 32u + head0) * 262144ull - 1024ull;
  const unsigned long long xp = (unsigned long long)(bk ? X1 : X0) + (unsigned long long)b * (224u * 1280u);
  const unsigned long long wp = (unsigned long long)(bk ? W1 : W0);
  const unsigned long long op = (unsigned long long)ws + 0ull + (unsigned long long)bb * 2295808ull;
  const unsigned ldsb = (unsigned)(size_t)lds;
  const unsigned long long wop = (unsigned long long)(bk ? WO1 : WO0) - 4096ull;
  const unsigned long long yp = (unsigned long long)out + (unsigned long long)b * (224ull * 2560ull) + bk * 1280u + (hg & 3u) * 320u;
  const unsigned long long dp = (unsigned long long)ws + (100ull << 20) + (c * 4u + wid) * 256u;
  asm volatile(
".macro Qta d,v,o\nds_read_b64_tr_b16 a[\\d:\\d+1], v\\v offset:\\o\n.endm\n.macro Qdv d,v,o\nds_read_b128 v[\\d:\\d+3], v\\v offset:\\o\n.endm\n.macro Qda d,v,o\nds_read_b128 a[\\d:\\d+3], v\\v offset:\\o\n"
".endm\n.macro Qzvav d,a,b\nv_mfma_f32_16x16x32_bf16 v[\\d:\\d+3], a[\\a:\\a+3], v[\\b:\\b+3], 0\n.endm\n.macro Qmvaa d,a,b\nv_mfma_f32_16x16x32_bf16 v[\\d:\\d+3], a[\\a:\\a+3], a[\\b:\\b+3], v[\\d:\\d+3]\n.endm\n.macro Qzaav d,a,b\n"
"v_mfma_f32_16x16x32_bf16 a[\\d:\\d+3], a[\\a:\\a+3], v[\\b:\\b+3], 0\n.endm\n.macro Qmaaa d,a,b\nv_mfma_f32_16x16x32_bf16 a[\\d:\\d+3], a[\\a:\\a+3], a[\\b:\\b+3], a[\\d:\\d+3]\n.endm\n.macro Qmvav d,a,b\nv_mfma_f32_16x16x32_bf16 v[\\d:\\d+3], a[\\a:\\a+3], v[\\b:\\b+3], v[\\d:\\d+3]\n.endm\n"
".macro Qmaav d,a,b\nv_mfma_f32_16x16x32_bf16 a[\\d:\\d+3], a[\\a:\\a+3], v[\\b:\\b+3], a[\\d:\\d+3]\n.endm\n.macro Qk d,a,b\nv_cvt_pk_bf16_f32 v\\d, v\\a, v\\b\n.endm\n.macro Qzvaa d,a,b\nv_mfma_f32_16x16x32_bf16 v[\\d:\\d+3], a[\\a:\\a+3], a[\\b:\\b+3], 0\n"
".endm\n.macro Qa d,a,b\nv_add_f32 v\\d, v\\a, v\\b\n.endm\n.macro Qe d\nv_exp_f32 v\\d, v\\d\n.endm\n.macro Qcvaa d,a,b,c\n"
"v_mfma_f32_16x16x32_bf16 v[\\d:\\d+3], a[\\a:\\a+3], a[\\b:\\b+3], v[\\c:\\c+3]\n.endm\n.macro Qzvva d,a,b\nv_mfma_f32_16x16x32_bf16 v[\\d:\\d+3], v[\\a:\\a+3], a[\\b:\\b+3], 0\n.endm\n.macro Qzava d,a,b\nv_mfma_f32_16x16x32_bf16 a[\\d:\\d+3], v[\\a:\\a+3], a[\\b:\\b+3], 0\n.endm\n"
".macro Qmvva d,a,b\nv_mfma_f32_16x16x32_bf16 v[\\d:\\d+3], v[\\a:\\a+3], a[\\b:\\b+3], v[\\d:\\d+3]\n.endm\n.macro Qmava d,a,b\nv_mfma_f32_16x16x32_bf16 a[\\d:\\d+3], v[\\a:\\a+3], a[\\b:\\b+3], a[\\d:\\d+3]\n.endm\n.macro Qzvvv d,a,b\nv_mfma_f32_16x16x32_bf16 v[\\d:\\d+3], v[\\a:\\a+3], v[\\b:\\b+3], 0\n"
".endm\n.macro Qcaav d,a,b,c\nv_mfma_f32_16x16x32_bf16 a[\\d:\\d+3], a[\\a:\\a+3], v[\\b:\\b+3], a[\\c:\\c+3]\n.endm\n.macro Qcvav d,a,b,c\nv_mfma_f32_16x16x32_bf16 v[\\d:\\d+3], a[\\a:\\a+3], v[\\b:\\b+3], v[\\c:\\c+3]\n.endm\n.p2align 7\n"
"v_mbcnt_lo_u32_b32 v0, -1, 0\nv_mbcnt_hi_u32_b32 v0, -1, v0\nv_lshlrev_b32 v60, 4, v0\ns_add_u32 m0, %10, 160768\ns_getpc_b64 s[56:57]\n.Lpc0:\ns_and_b32 s56, s56, 0xffffff80\ns_and_b32 s57, s57, 0xffff\n"
"s_mov_b32 s58, .Lend-.Lpc0\ns_mov_b32 s59, 0x20000\ns_and_b32 s94, %20, 31\ns_lshl_b32 s94, s94, 2\ns_add_u32 s94, s94, %11\ns_lshl_b32 s94, s94, 10\nbuffer_load_dwordx4 v60, s[56:59], s94 offen lds\ns_mov_b32 s52, %6\n"
"s_and_b32 s53, %7, 0xffff\ns_mov_b32 s54, 5242880\ns_mov_b32 s55, 0x20000\ns_mov_b32 s48, %4\ns_and_b32 s49, %5, 0xffff\ns_mov_b32 s50, 286720\ns_mov_b32 s51, 0x20000\ns_mov_b32 s70, %11\n"
"s_and_b32 s92, s70, 1\ns_lshr_b32 s93, s70, 1\nv_lshrrev_b32 v2, 4, v0\ns_lshl_b32 s68, s92, 18\nv_and_b32 v4, 3, v0\nv_lshlrev_b32 v4, 4, v4\nv_and_b32 v1, 1, v2\nv_lshl_add_u32 v4, v1, 6, v4\n"
"v_lshrrev_b32 v1, 5, v0\nv_lshl_add_u32 v4, v1, 15, v4\nv_bfe_u32 v1, v0, 2, 2\nv_lshl_add_u32 v4, v1, 13, v4\nv_add_u32 v62, s68, v4\ns_lshl_b32 s87, %12, 8\ns_mul_i32 s62, s70, 4128\ns_add_u32 s62, s62, %10\n"
"s_add_u32 s62, s62, 33024\ns_add_u32 m0, s62, 0\ns_add_u32 s66, s87, 0\ns_nop 0\nbuffer_load_dwordx4 v62, s[52:55], s66 offen lds\ns_add_u32 s66, s87, 64504\ns_nop 0\nbuffer_load_dwordx4 v62, s[52:55], s66 offen offset:1032 lds\n"
"s_add_u32 s66, s87, 129008\ns_nop 0\nbuffer_load_dwordx4 v62, s[52:55], s66 offen offset:2064 lds\ns_add_u32 s66, s87, 193512\ns_nop 0\nbuffer_load_dwordx4 v62, s[52:55], s66 offen offset:3096 lds\ns_lshl_b32 s69, s92, 1\nv_lshrrev_b32 v3, 5, v0\n"
"v_and_b32 v4, 3, v0\nv_xor_b32 v4, s69, v4\nv_lshlrev_b32 v4, 4, v4\nv_lshl_add_u32 v4, v3, 6, v4\nv_bfe_u32 v1, v0, 2, 3\nv_mul_u32_u24 v64, 1280, v1\nv_add_u32 v64, v64, v4\ns_mul_i32 s86, s93, 143360\n"
"s_mul_i32 s68, s92, 10240\ns_add_u32 s86, s86, s68\ns_mul_i32 s63, s92, 14336\ns_mul_i32 s68, s93, 7168\ns_add_u32 s63, s63, s68\ns_add_u32 s63, s63, %10\ns_add_u32 s63, s63, 99072\ns_add_u32 m0, s63, 33024\n"
"s_add_u32 s97, s86, 0\ns_nop 0\nbuffer_load_dwordx4 v64, s[48:51], s97 offen lds\ns_add_u32 s97, s86, 19456\ns_nop 0\nbuffer_load_dwordx4 v64, s[48:51], s97 offen offset:1024 lds\ns_add_u32 s97, s86, 38912\ns_nop 0\n"
"buffer_load_dwordx4 v64, s[48:51], s97 offen offset:2048 lds\ns_add_u32 s97, s86, 58368\ns_nop 0\nbuffer_load_dwordx4 v64, s[48:51], s97 offen offset:3072 lds\ns_add_u32 m0, s63, 37120\ns_add_u32 s97, s86, 81920\ns_nop 0\nbuffer_load_dwordx4 v64, s[48:51], s97 offen lds\n"
"s_add_u32 s97, s86, 101376\ns_nop 0\nbuffer_load_dwordx4 v64, s[48:51], s97 offen offset:1024 lds\ns_add_u32 s97, s86, 120832\ns_nop 0\nbuffer_load_dwordx4 v64, s[48:51], s97 offen offset:2048 lds\ns_add_u32 m0, s62, 16512\ns_add_u32 s66, s87, 128\n"
"s_nop 0\nbuffer_load_dwordx4 v62, s[52:55], s66 offen lds\ns_mov_b32 s24, %8\ns_and_b32 s25, %9, 0xffff\ns_mov_b32 s26, 2295808\ns_mov_b32 s27, 0x20000\nv_and_b32 v5, 7, v0\nv_lshlrev_b32 v5, 7, v5\n"
"s_add_u32 s66, s87, 64632\ns_nop 0\nbuffer_load_dwordx4 v62, s[52:55], s66 offen offset:1032 lds\nv_add_u32 v76, 2293760, v5\ns_lshl_b32 s68, s70, 2\ns_add_u32 s68, s68, 4\nv_bfe_u32 v5, v0, 3, 1\nv_mad_u32_u24 v76, v5, s68, v76\n"
"buffer_load_dword v65, v76, s[24:27], 0 offen sc1\ns_add_u32 s66, s87, 129136\ns_nop 0\nbuffer_load_dwordx4 v62, s[52:55], s66 offen offset:2064 lds\ns_mov_b32 s40, %0\ns_and_b32 s41, %1, 0xffff\ns_mov_b32 s42, 2360320\ns_mov_b32 s43, 0x20000\n"
"s_mov_b32 s44, %2\ns_and_b32 s45, %3, 0xffff\ns_add_u32 s66, s87, 193640\ns_nop 0\nbuffer_load_dwordx4 v62, s[52:55], s66 offen offset:3096 lds\ns_mov_b32 s46, 2360320\ns_mov_b32 s47, 0x20000\ns_mov_b32 s56, %8\n"
"s_and_b32 s57, %9, 0xffff\ns_mov_b32 s58, 0\ns_mov_b32 s59, 0x20000\ns_mov_b64 exec, 0\ns_cmp_eq_u32 s70, 0\ns_cbranch_scc1 .Lpg0\ns_cmp_eq_u32 s70, 1\ns_cbranch_scc1 .Lpg1\n"
"s_cmp_eq_u32 s70, 2\ns_cbranch_scc1 .Lpg2\ns_branch .Lpg3\n.Ltr0:\ns_mov_b64 exec, -1\ns_mov_b32 s60, 0\ns_mov_b32 s90, 896\ns_mov_b32 s67, 0\n"
"s_mov_b32 s72, 0x3e0293ee\ns_lshl_b32 s68, s92, 12\ns_add_u32 s88, s68, 128\ns_add_u32 m0, s63, 0\ns_add_u32 s97, s86, 128\ns_nop 0\nbuffer_load_dwordx4 v64, s[48:51], s97 offen lds\nv_lshl_add_u32 v4, v1, 8, v4\n"
"v_add_u32 v69, s88, v4\nv_and_b32 v4, 1, v0\nv_lshlrev_b32 v4, 4, v4\nv_lshl_add_u32 v4, v2, 5, v4\nv_bfe_u32 v1, v0, 1, 3\ns_add_u32 s97, s86, 19584\ns_nop 0\nbuffer_load_dwordx4 v64, s[48:51], s97 offen offset:1024 lds\n"
"v_lshl_add_u32 v4, v1, 8, v4\nv_add_u32 v72, s88, v4\ns_lshl_b32 s69, s93, 14\ns_add_u32 s69, s69, %10\ns_add_u32 s61, s69, s68\nv_bfe_u32 v3, v0, 3, 1\ns_add_u32 s97, s86, 39040\ns_nop 0\n"
"buffer_load_dwordx4 v64, s[48:51], s97 offen offset:2048 lds\nv_lshlrev_b32 v4, 1, v3\nv_xor_b32 v4, v4, v2\nv_and_b32 v1, 7, v0\nv_lshl_add_u32 v4, v1, 2, v4\nv_lshlrev_b32 v4, 4, v4\nv_lshl_add_u32 v1, v3, 12, v4\ns_add_u32 s97, s86, 58496\n"
"s_nop 0\nbuffer_load_dwordx4 v64, s[48:51], s97 offen offset:3072 lds\nv_add_u32 v68, s69, v1\ns_mul_i32 s68, s92, 7168\ns_add_u32 s68, s68, %10\ns_add_u32 s68, s68, 99072\nv_mul_u32_u24 v1, 14336, v3\nv_add3_u32 v63, v1, v4, s68\n"
"s_add_u32 m0, s63, 4096\ns_add_u32 s97, s86, 82048\ns_nop 0\nbuffer_load_dwordx4 v64, s[48:51], s97 offen lds\nv_lshrrev_b32 v3, 5, v0\nv_and_b32 v4, 31, v0\nv_lshlrev_b32 v4, 3, v4\nv_lshl_add_u32 v4, v3, 12, v4\n"
"v_add_u32 v4, 8192, v4\nv_add_u32 v70, s69, v4\ns_add_u32 s97, s86, 101504\ns_nop 0\nbuffer_load_dwordx4 v64, s[48:51], s97 offen offset:1024 lds\nv_add_u32 v71, 66048, v70\ns_mul_i32 s68, s93, 8256\ns_add_u32 s68, s68, %10\n"
"s_add_u32 s68, s68, 33024\nv_mul_u32_u24 v3, 1032, v2\nv_and_b32 v4, 15, v0\ns_add_u32 s97, s86, 120960\ns_nop 0\nbuffer_load_dwordx4 v64, s[48:51], s97 offen offset:2048 lds\nv_lshlrev_b32 v4, 4, v4\nv_add3_u32 v61, v3, v4, s68\n"
"s_lshl_b32 s68, s70, 2\ns_add_u32 s69, %10, 131840\ns_add_u32 s68, s68, s69\nv_mov_b32 v73, s68\ns_add_u32 m0, s62, 33024\ns_add_u32 s66, s87, 524288\ns_nop 0\nbuffer_load_dwordx4 v62, s[52:55], s66 offen lds\n"
"v_and_b32 v3, 3, v0\nv_lshl_add_u32 v74, v3, 2, s69\nv_mov_b32 v75, 0\nds_write_b32 v73, v75\ns_waitcnt lgkmcnt(0)\ns_mul_i32 s91, s70, 7168\ns_add_u32 s66, s87, 588792\ns_nop 0\n"
"buffer_load_dwordx4 v62, s[52:55], s66 offen offset:1032 lds\ns_add_u32 s91, s91, %10\ns_add_u32 s91, s91, 98304\ns_lshl_b32 s96, s70, 11\ns_add_u32 s96, s96, %10\nv_and_b32 v3, 1, v0\nv_mul_u32_u24 v3, 0xaaaa, v3\ns_add_u32 s66, s87, 653296\n"
"s_nop 0\nbuffer_load_dwordx4 v62, s[52:55], s66 offen offset:2064 lds\nv_or_b32 v77, 0x4444, v3\ns_and_b32 s68, %19, 4\ns_lshl_b32 s28, 15, s68\ns_mov_b32 s29, 0\ns_mov_b32 s71, 0\ns_and_b32 s68, %19, 3\n"
"s_add_u32 s66, s87, 717800\ns_nop 0\nbuffer_load_dwordx4 v62, s[52:55], s66 offen offset:3096 lds\ns_lshl_b32 s99, 3072, s68\ns_or_b32 s99, s99, 0x302\ns_mov_b32 s94, 0x7ffff000\ns_mul_i32 s68, %20, 40960\ns_mul_i32 s69, s93, 20480\n"
"s_add_u32 s68, s68, s69\ns_mov_b64 exec, 0\ns_cmp_eq_u32 s70, 0\ns_cbranch_scc1 .Lpg4\ns_cmp_eq_u32 s70, 1\ns_cbranch_scc1 .Lpg5\ns_cmp_eq_u32 s70, 2\ns_cbranch_scc1 .Lpg6\n"
"s_branch .Lpg7\n.Ltr9:\ns_mov_b64 exec, -1\ns_lshl_b32 s69, s92, 10\ns_add_u32 s68, s68, s69\ns_add_u32 s68, s68, 2048\ns_add_u32 s94, %6, s68\ns_addc_u32 s95, %7, 0\n"
"s_and_b32 s95, s95, 0xffff\ns_add_u32 m0, s62, 49536\ns_add_u32 s66, s87, 524416\ns_nop 0\nbuffer_load_dwordx4 v62, s[52:55], s66 offen lds\ns_mov_b32 s75, 17408\ns_add_u32 s66, s87, 588920\ns_nop 0\n"
"buffer_load_dwordx4 v62, s[52:55], s66 offen offset:1032 lds\ns_add_u32 s66, s87, 653424\ns_nop 0\nbuffer_load_dwordx4 v62, s[52:55], s66 offen offset:2064 lds\ns_add_u32 s66, s87, 717928\ns_nop 0\nbuffer_load_dwordx4 v62, s[52:55], s66 offen offset:3096 lds\ns_mov_b64 exec, 0\n"
"s_cmp_eq_u32 s70, 0\ns_cbranch_scc1 .Lpg8\ns_cmp_eq_u32 s70, 1\ns_cbranch_scc1 .Lpg9\ns_cmp_eq_u32 s70, 2\ns_cbranch_scc1 .Lpg10\ns_branch .Lpg11\n.Ltr10:\n"
"s_mov_b64 exec, -1\n.Lround:\ns_cmp_lg_u32 s67, 0\ns_cselect_b64 vcc, -1, 0\ns_waitcnt vmcnt(15)\ns_barrier\nQta 112,61,0\nQta 114,61,512\n"
"Qdv 24,63,33024\nQta 116,61,4128\nQta 118,61,4640\nQdv 28,63,34048\nQta 120,61,8\nQta 122,61,520\nQdv 32,63,35072\nQta 124,61,4136\n"
"Qta 126,61,4648\nQdv 36,63,36096\nQta 128,61,256\nQta 130,61,768\nQdv 40,63,37120\nQta 132,61,4384\nQta 134,61,4896\nQdv 44,63,38144\n"
"Qta 136,61,264\nQta 138,61,776\nQdv 48,63,39168\nQta 140,61,4392\nQta 142,61,4904\nQda 176,63,33536\nQda 180,63,34560\nQda 184,63,35584\n"
"Qda 188,63,36608\nQda 192,63,37632\nQda 196,63,38656\nQda 200,63,39680\ns_waitcnt lgkmcnt(6)\nQzvav 80,112,24\nQmvaa 80,116,176\ns_add_u32 s68, s71, 0\n"
"Qzvav 84,120,24\ns_cbranch_vccz .Lst27\nbuffer_store_dwordx4 a[0:3], v60, s[56:59], s68 offen offset:0 sc1\n.Lst27:\nQmvaa 84,124,176\nQzvav 88,128,24\ns_add_u32 s68, s71, 14336\nQmvaa 88,132,176\n"
"s_cbranch_vccz .Lst26\nbuffer_store_dwordx4 a[4:7], v60, s[56:59], s68 offen offset:0 sc1\n.Lst26:\nQzvav 92,136,24\nQmvaa 92,140,176\ns_waitcnt lgkmcnt(0)\nQzvav 124,136,28\ns_waitcnt vmcnt(15)\n"
"s_barrier\nQmvaa 124,140,180\nQta 144,61,16512\ns_add_u32 m0, s62, 0\ns_add_u32 s69, s87, 1048576\nQzvav 120,128,28\nbuffer_load_dwordx4 v62, s[52:55], s69 offen lds\nQmvaa 120,132,180\n"
"Qta 146,61,17024\nQzvav 116,120,28\nQmvaa 116,124,180\nQta 148,61,20640\ns_add_u32 s69, s87, 1113080\nQzvav 112,112,28\nbuffer_load_dwordx4 v62, s[52:55], s69 offen offset:1032 lds\nQmvaa 112,116,180\n"
"Qta 150,61,21152\nQzvav 144,112,32\nQmvaa 144,116,184\nQta 152,61,16520\ns_add_u32 s69, s87, 1177584\nQzvav 148,120,32\nbuffer_load_dwordx4 v62, s[52:55], s69 offen offset:2064 lds\nQmvaa 148,124,184\n"
"Qta 154,61,17032\nQzvav 152,128,32\nQmvaa 152,132,184\nQta 156,61,20648\ns_add_u32 s69, s87, 1242088\nQzvav 156,136,32\nbuffer_load_dwordx4 v62, s[52:55], s69 offen offset:3096 lds\nQmvaa 156,140,184\n"
"Qta 158,61,21160\nQzvav 188,136,36\nQmvaa 188,140,188\nQta 160,61,16768\ns_add_u32 m0, s63, 33024\ns_add_u32 s88, s86, 256\nQzvav 184,128,36\nbuffer_load_dwordx4 v64, s[48:51], s88 offen lds\n"
"Qmvaa 184,132,188\nQta 162,61,17280\nQzvav 180,120,36\nQmvaa 180,124,188\nQta 164,61,20896\ns_add_u32 s88, s86, 19712\nQzvav 176,112,36\nbuffer_load_dwordx4 v64, s[48:51], s88 offen offset:1024 lds\n"
"Qmvaa 176,116,188\nQta 166,61,21408\nQzvav 208,112,40\nQmvaa 208,116,192\nQta 168,61,16776\ns_add_u32 s88, s86, 39168\nQzvav 212,120,40\nbuffer_load_dwordx4 v64, s[48:51], s88 offen offset:2048 lds\n"
"Qmvaa 212,124,192\nQta 170,61,17288\nQzvav 216,128,40\nQmvaa 216,132,192\nQta 172,61,20904\ns_add_u32 s88, s86, 58624\nQzvav 220,136,40\nbuffer_load_dwordx4 v64, s[48:51], s88 offen offset:3072 lds\n"
"Qmvaa 220,140,192\nQta 174,61,21416\nQzvav 252,136,44\nQmvaa 252,140,196\ns_add_u32 m0, s63, 37120\ns_add_u32 s88, s86, 82176\nQzvav 248,128,44\nbuffer_load_dwordx4 v64, s[48:51], s88 offen lds\n"
"Qmvaa 248,132,196\nQzvav 244,120,44\nQmvaa 244,124,196\ns_add_u32 s88, s86, 101632\nQzvav 240,112,44\nbuffer_load_dwordx4 v64, s[48:51], s88 offen offset:1024 lds\nQmvaa 240,116,196\nQzaav 224,112,48\n"
"Qmaaa 224,116,200\ns_add_u32 s88, s86, 121088\nQzaav 228,120,48\nbuffer_load_dwordx4 v64, s[48:51], s88 offen offset:2048 lds\nQmaaa 228,124,200\nQzaav 232,128,48\nQmaaa 232,132,200\nQzaav 236,136,48\n"
"Qmaaa 236,140,200\ns_waitcnt lgkmcnt(0)\nQzvav 96,144,24\nQmvaa 96,148,176\ns_add_u32 s68, s71, 28672\nQzvav 100,152,24\ns_cbranch_vccz .Lst25\nbuffer_store_dwordx4 a[8:11], v60, s[56:59], s68 offen offset:0 sc1\n"
".Lst25:\nQmvaa 100,156,176\nQzvav 104,160,24\ns_add_u32 s68, s71, 43008\nQmvaa 104,164,176\ns_cbranch_vccz .Lst24\nbuffer_store_dwordx4 a[12:15], v60, s[56:59], s68 offen offset:0 sc1\n.Lst24:\n"
"Qzvav 108,168,24\nQmvaa 108,172,176\ns_waitcnt lgkmcnt(0)\nQzvav 140,168,28\ns_waitcnt vmcnt(15)\ns_barrier\nQmvaa 140,172,180\nQdv 24,63,0\n"
"s_add_u32 m0, s62, 16512\ns_add_u32 s69, s87, 1048704\nQzvav 136,160,28\nbuffer_load_dwordx4 v62, s[52:55], s69 offen lds\nQmvaa 136,164,180\nQda 176,63,512\nQzvav 132,152,28\nQta 112,61,33024\n"
"Qmvaa 132,156,180\ns_add_u32 s69, s87, 1113208\nQzvav 128,144,28\nbuffer_load_dwordx4 v62, s[52:55], s69 offen offset:1032 lds\nQmvaa 128,148,180\nQdv 28,63,1024\nQzvav 160,144,32\nQda 180,63,1536\n"
"Qmvaa 160,148,184\nQta 114,61,33536\ns_add_u32 s69, s87, 1177712\nQzvav 164,152,32\nbuffer_load_dwordx4 v62, s[52:55], s69 offen offset:2064 lds\nQmvaa 164,156,184\nQta 116,61,37152\nQzvav 168,160,32\n"
"Qmvaa 168,164,184\nQta 118,61,37664\ns_add_u32 s69, s87, 1242216\nQzvav 172,168,32\nbuffer_load_dwordx4 v62, s[52:55], s69 offen offset:3096 lds\nQmvaa 172,172,184\nQdv 32,63,2048\nQzvav 204,168,36\n"
"Qda 184,63,2560\nQmvaa 204,172,188\nQta 120,61,33032\ns_add_u32 m0, s61, 0\nQzvav 200,160,36\ns_cbranch_vccnz .Ld0q0\nbuffer_load_dwordx4 v69, s[40:43], s90 offen nt lds\n.Ld0q0:\n"
"Qmvaa 200,164,188\nQta 122,61,33544\nQzvav 196,152,36\nQmvaa 196,156,188\nQta 124,61,37160\nQzvav 192,144,36\nQdv 36,63,3072\nQmvaa 192,148,188\n"
"Qda 188,63,3584\nQzvav 224,144,40\nQta 126,61,37672\nQmvaa 224,148,192\nQzvav 228,152,40\nQta 128,61,33280\nQmvaa 228,156,192\nQzvav 232,160,40\n"
"Qta 130,61,33792\nQmvaa 232,164,192\nQzvav 236,168,40\nQdv 40,63,4096\nQmvaa 236,172,192\nQda 192,63,4608\nQzaav 220,168,44\nQta 132,61,37408\n"
"Qmaaa 220,172,196\nQzaav 216,160,44\nQta 134,61,37920\nQmaaa 216,164,196\nQzaav 212,152,44\nQta 136,61,33288\nQmaaa 212,156,196\nQzaav 208,144,44\n"
"Qdv 44,63,5120\nQmaaa 208,148,196\nQda 196,63,5632\nQzaav 240,144,48\nQta 138,61,33800\nQmaaa 240,148,200\nQzaav 244,152,48\nQta 140,61,37416\n"
"Qmaaa 244,156,200\nQzaav 248,160,48\nQta 142,61,37928\nQmaaa 248,164,200\nQzaav 252,168,48\nQdv 48,63,6144\nQmaaa 252,172,200\nQda 200,63,6656\n"
"s_waitcnt lgkmcnt(2)\nQmvav 80,112,24\nQmvaa 80,116,176\ns_add_u32 s68, s71, 0\nQmvav 84,120,24\ns_cbranch_vccz .Lst23\nbuffer_store_dwordx4 a[16:19], v60, s[56:59], s68 offen offset:1024 sc1\n.Lst23:\n"
"Qmvaa 84,124,176\nQmvav 88,128,24\ns_add_u32 s68, s71, 14336\nQmvaa 88,132,176\ns_cbranch_vccz .Lst22\nbuffer_store_dwordx4 a[20:23], v60, s[56:59], s68 offen offset:1024 sc1\n.Lpg0:\ns_cbranch_execz .Ltr0\n"
".Lst22:\nQmvav 92,136,24\nQmvaa 92,140,176\ns_waitcnt lgkmcnt(0)\nQmvav 124,136,28\ns_waitcnt vmcnt(16)\ns_barrier\nQmvaa 124,140,180\n"
"Qta 144,61,49536\ns_add_u32 m0, s62, 33024\ns_add_u32 s69, s87, 1572864\nQmvav 120,128,28\nbuffer_load_dwordx4 v62, s[52:55], s69 offen lds\nQmvaa 120,132,180\nQta 146,61,50048\nQmvav 116,120,28\n"
"Qmvaa 116,124,180\nQta 148,61,53664\ns_add_u32 s69, s87, 1637368\nQmvav 112,112,28\nbuffer_load_dwordx4 v62, s[52:55], s69 offen offset:1032 lds\nQmvaa 112,116,180\nQta 150,61,54176\nQmvav 144,112,32\n"
"Qmvaa 144,116,184\nQta 152,61,49544\ns_add_u32 s69, s87, 1701872\nQmvav 148,120,32\nbuffer_load_dwordx4 v62, s[52:55], s69 offen offset:2064 lds\nQmvaa 148,124,184\nQta 154,61,50056\nQmvav 152,128,32\n"
"Qmvaa 152,132,184\nQta 156,61,53672\ns_add_u32 s69, s87, 1766376\nQmvav 156,136,32\nbuffer_load_dwordx4 v62, s[52:55], s69 offen offset:3096 lds\nQmvaa 156,140,184\nQta 158,61,54184\nQmvav 188,136,36\n"
"Qmvaa 188,140,188\nQta 160,61,49792\ns_add_u32 m0, s63, 0\ns_add_u32 s88, s86, 384\nQmvav 184,128,36\nbuffer_load_dwordx4 v64, s[48:51], s88 offen lds\nQmvaa 184,132,188\nQta 162,61,50304\n"
"Qmvav 180,120,36\nQmvaa 180,124,188\nQta 164,61,53920\ns_add_u32 s88, s86, 19840\nQmvav 176,112,36\nbuffer_load_dwordx4 v64, s[48:51], s88 offen offset:1024 lds\nQmvaa 176,116,188\nQta 166,61,54432\n"
"Qmvav 208,112,40\nQmvaa 208,116,192\nQta 168,61,49800\ns_add_u32 s88, s86, 39296\nQmvav 212,120,40\nbuffer_load_dwordx4 v64, s[48:51], s88 offen offset:2048 lds\nQmvaa 212,124,192\nQta 170,61,50312\n"
"Qmvav 216,128,40\nQmvaa 216,132,192\nQta 172,61,53928\ns_add_u32 s88, s86, 58752\nQmvav 220,136,40\nbuffer_load_dwordx4 v64, s[48:51], s88 offen offset:3072 lds\nQmvaa 220,140,192\nQta 174,61,54440\n"
"Qmvav 252,136,44\nQmvaa 252,140,196\ns_add_u32 m0, s63, 4096\ns_add_u32 s88, s86, 82304\nQmvav 248,128,44\nbuffer_load_dwordx4 v64, s[48:51], s88 offen lds\nQmvaa 248,132,196\nQmvav 244,120,44\n"
"Qmvaa 244,124,196\ns_add_u32 s88, s86, 101760\nQmvav 240,112,44\nbuffer_load_dwordx4 v64, s[48:51], s88 offen offset:1024 lds\nQmvaa 240,116,196\nQmaav 224,112,48\nQmaaa 224,116,200\ns_add_u32 s88, s86, 121216\n"
"Qmaav 228,120,48\nbuffer_load_dwordx4 v64, s[48:51], s88 offen offset:2048 lds\nQmaaa 228,124,200\nQmaav 232,128,48\nQmaaa 232,132,200\nQmaav 236,136,48\nQmaaa 236,140,200\ns_waitcnt lgkmcnt(0)\n"
"Qmvav 96,144,24\nQmvaa 96,148,176\ns_add_u32 s68, s71, 28672\nQmvav 100,152,24\ns_cbranch_vccz .Lst21\nbuffer_store_dwordx4 a[24:27], v60, s[56:59], s68 offen offset:1024 sc1\n.Lst21:\nQmvaa 100,156,176\n"
"Qmvav 104,160,24\ns_add_u32 s68, s71, 43008\nQmvaa 104,164,176\ns_cbranch_vccz .Lst20\nbuffer_store_dwordx4 a[28:31], v60, s[56:59], s68 offen offset:1024 sc1\n.Lst20:\nQmvav 108,168,24\nQmvaa 108,172,176\n"
"s_waitcnt lgkmcnt(0)\nQmvav 140,168,28\ns_waitcnt vmcnt(16)\ns_barrier\nQmvaa 140,172,180\nQdv 24,63,33024\ns_add_u32 m0, s62, 49536\ns_add_u32 s69, s87, 1572992\n"
"Qmvav 136,160,28\nbuffer_load_dwordx4 v62, s[52:55], s69 offen lds\nQmvaa 136,164,180\nQda 176,63,33536\nQmvav 132,152,28\nQta 112,61,0\nQmvaa 132,156,180\ns_add_u32 s69, s87, 1637496\n"
"Qmvav 128,144,28\nbuffer_load_dwordx4 v62, s[52:55], s69 offen offset:1032 lds\nQmvaa 128,148,180\nQdv 28,63,34048\nQmvav 160,144,32\nQda 180,63,34560\nQmvaa 160,148,184\nQta 114,61,512\n"
"s_add_u32 s69, s87, 1702000\nQmvav 164,152,32\nbuffer_load_dwordx4 v62, s[52:55], s69 offen offset:2064 lds\nQmvaa 164,156,184\nQta 116,61,4128\nQmvav 168,160,32\nQmvaa 168,164,184\nQta 118,61,4640\n"
"s_add_u32 s69, s87, 1766504\nQmvav 172,168,32\nbuffer_load_dwordx4 v62, s[52:55], s69 offen offset:3096 lds\nQmvaa 172,172,184\nQdv 32,63,35072\nQmvav 204,168,36\nQda 184,63,35584\nQmvaa 204,172,188\n"
"Qta 120,61,8\ns_add_u32 m0, s61, 0\nQmvav 200,160,36\ns_cbranch_vccnz .Ld0q1\nbuffer_load_dwordx4 v69, s[40:43], s60 offen offset:1024 nt lds\n.Ld0q1:\nQmvaa 200,164,188\nQta 122,61,520\n"
"Qmvav 196,152,36\nQmvaa 196,156,188\nQta 124,61,4136\nQmvav 192,144,36\nQdv 36,63,36096\nQmvaa 192,148,188\nQda 188,63,36608\nQmvav 224,144,40\n"
"Qta 126,61,4648\nQmvaa 224,148,192\nQmvav 228,152,40\nQta 128,61,256\nQmvaa 228,156,192\nQmvav 232,160,40\nQta 130,61,768\nQmvaa 232,164,192\n"
"Qmvav 236,168,40\nQdv 40,63,37120\nQmvaa 236,172,192\nQda 192,63,37632\nQmaav 220,168,44\nQta 132,61,4384\nQmaaa 220,172,196\nQmaav 216,160,44\n"
"Qta 134,61,4896\nQmaaa 216,164,196\nQmaav 212,152,44\nQta 136,61,264\nQmaaa 212,156,196\nQmaav 208,144,44\nQdv 44,63,38144\nQmaaa 208,148,196\n"
"Qda 196,63,38656\nQmaav 240,144,48\nQta 138,61,776\nQmaaa 240,148,200\nQmaav 244,152,48\nQta 140,61,4392\nQmaaa 244,156,200\nQmaav 248,160,48\n"
"Qta 142,61,4904\nQmaaa 248,164,200\nQmaav 252,168,48\nQdv 48,63,39168\nQmaaa 252,172,200\nQda 200,63,39680\ns_waitcnt lgkmcnt(2)\nQmvav 80,112,24\n"
"Qmvaa 80,116,176\ns_add_u32 s68, s71, 0\nQmvav 84,120,24\ns_cbranch_vccz .Lst19\nbuffer_store_dwordx4 a[32:35], v60, s[56:59], s68 offen offset:2048 sc1\n.Lst19:\nQmvaa 84,124,176\nQmvav 88,128,24\n"
"s_add_u32 s68, s71, 14336\nQmvaa 88,132,176\ns_cbranch_vccz .Lst18\nbuffer_store_dwordx4 a[36:39], v60, s[56:59], s68 offen offset:2048 sc1\n.Lst18:\nQmvav 92,136,24\nQmvaa 92,140,176\ns_waitcnt lgkmcnt(0)\n"
"Qmvav 124,136,28\ns_waitcnt vmcnt(17)\ns_barrier\nQmvaa 124,140,180\nQta 144,61,16512\ns_add_u32 m0, s62, 0\ns_add_u32 s69, s87, 2097152\nQmvav 120,128,28\n"
"buffer_load_dwordx4 v62, s[52:55], s69 offen lds\nQmvaa 120,132,180\nQta 146,61,17024\nQmvav 116,120,28\nQmvaa 116,124,180\nQta 148,61,20640\ns_add_u32 s69, s87, 2161656\nQmvav 112,112,28\n"
"buffer_load_dwordx4 v62, s[52:55], s69 offen offset:1032 lds\nQmvaa 112,116,180\nQta 150,61,21152\nQmvav 144,112,32\nQmvaa 144,116,184\nQta 152,61,16520\ns_add_u32 s69, s87, 2226160\nQmvav 148,120,32\n"
"buffer_load_dwordx4 v62, s[52:55], s69 offen offset:2064 lds\nQmvaa 148,124,184\nQta 154,61,17032\nQmvav 152,128,32\nQmvaa 152,132,184\nQta 156,61,20648\ns_add_u32 s69, s87, 2290664\nQmvav 156,136,32\n"
"buffer_load_dwordx4 v62, s[52:55], s69 offen offset:3096 lds\nQmvaa 156,140,184\nQta 158,61,21160\nQmvav 188,136,36\nQmvaa 188,140,188\nQta 160,61,16768\ns_add_u32 m0, s63, 33024\ns_add_u32 s88, s86, 512\n"
"Qmvav 184,128,36\nbuffer_load_dwordx4 v64, s[48:51], s88 offen lds\nQmvaa 184,132,188\nQta 162,61,17280\nQmvav 180,120,36\nQmvaa 180,124,188\nQta 164,61,20896\ns_add_u32 s88, s86, 19968\n"
"Qmvav 176,112,36\nbuffer_load_dwordx4 v64, s[48:51], s88 offen offset:1024 lds\nQmvaa 176,116,188\nQta 166,61,21408\nQmvav 208,112,40\nQmvaa 208,116,192\nQta 168,61,16776\ns_add_u32 s88, s86, 39424\n"
"Qmvav 212,120,40\nbuffer_load_dwordx4 v64, s[48:51], s88 offen offset:2048 lds\nQmvaa 212,124,192\nQta 170,61,17288\nQmvav 216,128,40\nQmvaa 216,132,192\nQta 172,61,20904\ns_add_u32 s88, s86, 58880\n"
"Qmvav 220,136,40\nbuffer_load_dwordx4 v64, s[48:51], s88 offen offset:3072 lds\nQmvaa 220,140,192\nQta 174,61,21416\nQmvav 252,136,44\nQmvaa 252,140,196\ns_add_u32 m0, s63, 37120\ns_add_u32 s88, s86, 82432\n"
"Qmvav 248,128,44\nbuffer_load_dwordx4 v64, s[48:51], s88 offen lds\nQmvaa 248,132,196\nQmvav 244,120,44\nQmvaa 244,124,196\ns_add_u32 s88, s86, 101888\nQmvav 240,112,44\nbuffer_load_dwordx4 v64, s[48:51], s88 offen offset:1024 lds\n"
"Qmvaa 240,116,196\nQmaav 224,112,48\nQmaaa 224,116,200\ns_add_u32 s88, s86, 121344\nQmaav 228,120,48\nbuffer_load_dwordx4 v64, s[48:51], s88 offen offset:2048 lds\nQmaaa 228,124,200\nQmaav 232,128,48\n"
"Qmaaa 232,132,200\nQmaav 236,136,48\nQmaaa 236,140,200\ns_waitcnt lgkmcnt(0)\nQmvav 96,144,24\nQmvaa 96,148,176\ns_add_u32 s68, s71, 28672\nQmvav 100,152,24\n"
"s_cbranch_vccz .Lst17\nbuffer_store_dwordx4 a[40:43], v60, s[56:59], s68 offen offset:2048 sc1\n.Lst17:\nQmvaa 100,156,176\nQmvav 104,160,24\ns_add_u32 s68, s71, 43008\nQmvaa 104,164,176\ns_cbranch_vccz .Lst16\n"
"buffer_store_dwordx4 a[44:47], v60, s[56:59], s68 offen offset:2048 sc1\n.Lst16:\nQmvav 108,168,24\nQmvaa 108,172,176\ns_waitcnt lgkmcnt(0)\nQmvav 140,168,28\ns_waitcnt vmcnt(16)\ns_barrier\n"
"Qmvaa 140,172,180\nQdv 24,63,0\ns_add_u32 m0, s62, 16512\ns_add_u32 s69, s87, 2097280\nQmvav 136,160,28\nbuffer_load_dwordx4 v62, s[52:55], s69 offen lds\nQmvaa 136,164,180\nQda 176,63,512\n"
"Qmvav 132,152,28\nQta 112,61,33024\nQmvaa 132,156,180\ns_add_u32 s69, s87, 2161784\nQmvav 128,144,28\nbuffer_load_dwordx4 v62, s[52:55], s69 offen offset:1032 lds\nQmvaa 128,148,180\nQdv 28,63,1024\n"
"Qmvav 160,144,32\nQda 180,63,1536\nQmvaa 160,148,184\nQta 114,61,33536\ns_add_u32 s69, s87, 2226288\nQmvav 164,152,32\nbuffer_load_dwordx4 v62, s[52:55], s69 offen offset:2064 lds\nQmvaa 164,156,184\n"
"Qta 116,61,37152\nQmvav 168,160,32\nQmvaa 168,164,184\nQta 118,61,37664\ns_add_u32 s69, s87, 2290792\nQmvav 172,168,32\nbuffer_load_dwordx4 v62, s[52:55], s69 offen offset:3096 lds\nQmvaa 172,172,184\n"
"Qdv 32,63,2048\nQmvav 204,168,36\nQda 184,63,2560\nQmvaa 204,172,188\nQta 120,61,33032\ns_add_u32 m0, s61, 0\nQmvav 200,160,36\ns_cbranch_vccnz .Ld0q2\n"
"buffer_load_dwordx4 v69, s[40:43], s90 offen offset:2048 nt lds\n.Ld0q2:\nQmvaa 200,164,188\nQta 122,61,33544\nQmvav 196,152,36\nQmvaa 196,156,188\nQta 124,61,37160\nQmvav 192,144,36\n"
"Qdv 36,63,3072\nQmvaa 192,148,188\nQda 188,63,3584\nQmvav 224,144,40\nQta 126,61,37672\nQmvaa 224,148,192\nQmvav 228,152,40\nQta 128,61,33280\n"
"Qmvaa 228,156,192\nQmvav 232,160,40\nQta 130,61,33792\nQmvaa 232,164,192\nQmvav 236,168,40\nQdv 40,63,4096\nQmvaa 236,172,192\nQda 192,63,4608\n"
"Qmaav 220,168,44\nQta 132,61,37408\nQmaaa 220,172,196\nQmaav 216,160,44\nQta 134,61,37920\nQmaaa 216,164,196\nQmaav 212,152,44\nQta 136,61,33288\n"
"Qmaaa 212,156,196\nQmaav 208,144,44\nQdv 44,63,5120\nQmaaa 208,148,196\nQda 196,63,5632\nQmaav 240,144,48\nQta 138,61,33800\nQmaaa 240,148,200\n"
"Qmaav 244,152,48\nQta 140,61,37416\nQmaaa 244,156,200\nQmaav 248,160,48\nQta 142,61,37928\nQmaaa 248,164,200\nQmaav 252,168,48\nQdv 48,63,6144\n"
"Qmaaa 252,172,200\nQda 200,63,6656\ns_waitcnt lgkmcnt(2)\nQmvav 80,112,24\nQmvaa 80,116,176\ns_add_u32 s68, s71, 0\nQmvav 84,120,24\ns_cbranch_vccz .Lst15\n"
"buffer_store_dwordx4 a[48:51], v60, s[56:59], s68 offen offset:3072 sc1\n.Lst15:\nQmvaa 84,124,176\nQmvav 88,128,24\ns_add_u32 s68, s71, 14336\nQmvaa 88,132,176\ns_cbranch_vccz .Lst14\nbuffer_store_dwordx4 a[52:55], v60, s[56:59], s68 offen offset:3072 sc1\n"
".Lst14:\nQmvav 92,136,24\nQmvaa 92,140,176\ns_waitcnt lgkmcnt(0)\nQmvav 124,136,28\ns_waitcnt vmcnt(17)\ns_barrier\nQmvaa 124,140,180\n"
"Qta 144,61,49536\ns_add_u32 m0, s62, 33024\ns_add_u32 s69, s87, 2621440\nQmvav 120,128,28\nbuffer_load_dwordx4 v62, s[52:55], s69 offen lds\nQmvaa 120,132,180\nQta 146,61,50048\nQmvav 116,120,28\n"
"Qmvaa 116,124,180\nQta 148,61,53664\ns_add_u32 s69, s87, 2685944\nQmvav 112,112,28\nbuffer_load_dwordx4 v62, s[52:55], s69 offen offset:1032 lds\nQmvaa 112,116,180\nQta 150,61,54176\nQmvav 144,112,32\n"
"Qmvaa 144,116,184\nQta 152,61,49544\ns_add_u32 s69, s87, 2750448\nQmvav 148,120,32\nbuffer_load_dwordx4 v62, s[52:55], s69 offen offset:2064 lds\nQmvaa 148,124,184\nQta 154,61,50056\nQmvav 152,128,32\n"
"Qmvaa 152,132,184\nQta 156,61,53672\ns_add_u32 s69, s87, 2814952\nQmvav 156,136,32\nbuffer_load_dwordx4 v62, s[52:55], s69 offen offset:3096 lds\nQmvaa 156,140,184\nQta 158,61,54184\nQmvav 188,136,36\n"
"Qmvaa 188,140,188\nQta 160,61,49792\ns_add_u32 m0, s63, 0\ns_add_u32 s88, s86, 640\nQmvav 184,128,36\nbuffer_load_dwordx4 v64, s[48:51], s88 offen lds\nQmvaa 184,132,188\nQta 162,61,50304\n"
"Qmvav 180,120,36\nQmvaa 180,124,188\nQta 164,61,53920\ns_add_u32 s88, s86, 20096\nQmvav 176,112,36\nbuffer_load_dwordx4 v64, s[48:51], s88 offen offset:1024 lds\nQmvaa 176,116,188\nQta 166,61,54432\n"
"Qmvav 208,112,40\nQmvaa 208,116,192\nQta 168,61,49800\ns_add_u32 s88, s86, 39552\nQmvav 212,120,40\nbuffer_load_dwordx4 v64, s[48:51], s88 offen offset:2048 lds\nQmvaa 212,124,192\nQta 170,61,50312\n"
"Qmvav 216,128,40\nQmvaa 216,132,192\nQta 172,61,53928\ns_add_u32 s88, s86, 59008\nQmvav 220,136,40\nbuffer_load_dwordx4 v64, s[48:51], s88 offen offset:3072 lds\nQmvaa 220,140,192\nQta 174,61,54440\n"
"Qmvav 252,136,44\nQmvaa 252,140,196\ns_add_u32 m0, s63, 4096\ns_add_u32 s88, s86, 82560\nQmvav 248,128,44\nbuffer_load_dwordx4 v64, s[48:51], s88 offen lds\nQmvaa 248,132,196\nQmvav 244,120,44\n"
"Qmvaa 244,124,196\ns_add_u32 s88, s86, 102016\nQmvav 240,112,44\nbuffer_load_dwordx4 v64, s[48:51], s88 offen offset:1024 lds\nQmvaa 240,116,196\nQmaav 224,112,48\nQmaaa 224,116,200\ns_add_u32 s88, s86, 121472\n"
"Qmaav 228,120,48\nbuffer_load_dwordx4 v64, s[48:51], s88 offen offset:2048 lds\nQmaaa 228,124,200\nQmaav 232,128,48\nQmaaa 232,132,200\nQmaav 236,136,48\nQmaaa 236,140,200\ns_waitcnt lgkmcnt(0)\n"
"Qmvav 96,144,24\nQmvaa 96,148,176\ns_add_u32 s68, s71, 28672\nQmvav 100,152,24\ns_cbranch_vccz .Lst13\nbuffer_store_dwordx4 a[56:59], v60, s[56:59], s68 offen offset:3072 sc1\n.Lpg1:\ns_cbranch_execz .Ltr0\n"
".Lst13:\nQmvaa 100,156,176\nQmvav 104,160,24\ns_add_u32 s68, s71, 43008\nQmvaa 104,164,176\ns_cbranch_vccz .Lst12\nbuffer_store_dwordx4 a[60:63], v60, s[56:59], s68 offen offset:3072 sc1\n.Lst12:\n"
"Qmvav 108,168,24\nQmvaa 108,172,176\ns_waitcnt lgkmcnt(0)\nQmvav 140,168,28\ns_waitcnt vmcnt(16)\ns_barrier\nQmvaa 140,172,180\nQdv 24,63,33024\n"
"s_add_u32 m0, s62, 49536\ns_add_u32 s69, s87, 2621568\nQmvav 136,160,28\nbuffer_load_dwordx4 v62, s[52:55], s69 offen lds\nQmvaa 136,164,180\nQda 176,63,33536\nQmvav 132,152,28\nQta 112,61,0\n"
"Qmvaa 132,156,180\ns_add_u32 s69, s87, 2686072\nQmvav 128,144,28\nbuffer_load_dwordx4 v62, s[52:55], s69 offen offset:1032 lds\nQmvaa 128,148,180\nQdv 28,63,34048\nQmvav 160,144,32\nQda 180,63,34560\n"
"Qmvaa 160,148,184\nQta 114,61,512\ns_add_u32 s69, s87, 2750576\nQmvav 164,152,32\nbuffer_load_dwordx4 v62, s[52:55], s69 offen offset:2064 lds\nQmvaa 164,156,184\nQta 116,61,4128\nQmvav 168,160,32\n"
"Qmvaa 168,164,184\nQta 118,61,4640\ns_add_u32 s69, s87, 2815080\nQmvav 172,168,32\nbuffer_load_dwordx4 v62, s[52:55], s69 offen offset:3096 lds\nQmvaa 172,172,184\nQdv 32,63,35072\nQmvav 204,168,36\n"
"Qda 184,63,35584\nQmvaa 204,172,188\nQta 120,61,8\ns_add_u32 m0, s61, 0\nQmvav 200,160,36\ns_cbranch_vccnz .Ld0q3\nbuffer_load_dwordx4 v69, s[40:43], s60 offen offset:3072 nt lds\n.Ld0q3:\n"
"Qmvaa 200,164,188\nQta 122,61,520\nQmvav 196,152,36\nQmvaa 196,156,188\nQta 124,61,4136\nQmvav 192,144,36\nQdv 36,63,36096\nQmvaa 192,148,188\n"
"Qda 188,63,36608\nQmvav 224,144,40\nQta 126,61,4648\nQmvaa 224,148,192\nQmvav 228,152,40\nQta 128,61,256\nQmvaa 228,156,192\nQmvav 232,160,40\n"
"Qta 130,61,768\nQmvaa 232,164,192\nQmvav 236,168,40\nQdv 40,63,37120\nQmvaa 236,172,192\nQda 192,63,37632\nQmaav 220,168,44\nQta 132,61,4384\n"
"Qmaaa 220,172,196\nQmaav 216,160,44\nQta 134,61,4896\nQmaaa 216,164,196\nQmaav 212,152,44\nQta 136,61,264\nQmaaa 212,156,196\nQmaav 208,144,44\n"
"Qdv 44,63,38144\nQmaaa 208,148,196\nQda 196,63,38656\nQmaav 240,144,48\nQta 138,61,776\nQmaaa 240,148,200\nQmaav 244,152,48\nQta 140,61,4392\n"
"Qmaaa 244,156,200\nQmaav 248,160,48\nQta 142,61,4904\nQmaaa 248,164,200\nQmaav 252,168,48\nQdv 48,63,39168\nQmaaa 252,172,200\nQda 200,63,39680\n"
"s_waitcnt lgkmcnt(2)\nQmvav 80,112,24\nQmvaa 80,116,176\ns_add_u32 s68, s71, 4096\nQmvav 84,120,24\ns_cbranch_vccz .Lst11\nbuffer_store_dwordx4 a[64:67], v60, s[56:59], s68 offen offset:0 sc1\n.Lst11:\n"
"Qmvaa 84,124,176\nQmvav 88,128,24\ns_add_u32 s68, s71, 18432\nQmvaa 88,132,176\ns_cbranch_vccz .Lst10\nbuffer_store_dwordx4 a[68:71], v60, s[56:59], s68 offen offset:0 sc1\n.Lst10:\nQmvav 92,136,24\n"
"Qmvaa 92,140,176\ns_waitcnt lgkmcnt(0)\nQmvav 124,136,28\ns_waitcnt vmcnt(17)\ns_barrier\nQmvaa 124,140,180\nQta 144,61,16512\ns_add_u32 m0, s62, 0\n"
"s_add_u32 s69, s87, 3145728\nQmvav 120,128,28\nbuffer_load_dwordx4 v62, s[52:55], s69 offen lds\nQmvaa 120,132,180\nQta 146,61,17024\nQmvav 116,120,28\nQmvaa 116,124,180\nQta 148,61,20640\n"
"s_add_u32 s69, s87, 3210232\nQmvav 112,112,28\nbuffer_load_dwordx4 v62, s[52:55], s69 offen offset:1032 lds\nQmvaa 112,116,180\nQta 150,61,21152\nQmvav 144,112,32\nQmvaa 144,116,184\nQta 152,61,16520\n"
"s_add_u32 s69, s87, 3274736\nQmvav 148,120,32\nbuffer_load_dwordx4 v62, s[52:55], s69 offen offset:2064 lds\nQmvaa 148,124,184\nQta 154,61,17032\nQmvav 152,128,32\nQmvaa 152,132,184\nQta 156,61,20648\n"
"s_add_u32 s69, s87, 3339240\nQmvav 156,136,32\nbuffer_load_dwordx4 v62, s[52:55], s69 offen offset:3096 lds\nQmvaa 156,140,184\nQta 158,61,21160\nQmvav 188,136,36\nQmvaa 188,140,188\nQta 160,61,16768\n"
"s_add_u32 m0, s63, 33024\ns_add_u32 s88, s86, 768\nQmvav 184,128,36\nbuffer_load_dwordx4 v64, s[48:51], s88 offen lds\nQmvaa 184,132,188\nQta 162,61,17280\nQmvav 180,120,36\nQmvaa 180,124,188\n"
"Qta 164,61,20896\ns_add_u32 s88, s86, 20224\nQmvav 176,112,36\nbuffer_load_dwordx4 v64, s[48:51], s88 offen offset:1024 lds\nQmvaa 176,116,188\nQta 166,61,21408\nQmvav 208,112,40\nQmvaa 208,116,192\n"
"Qta 168,61,16776\ns_add_u32 s88, s86, 39680\nQmvav 212,120,40\nbuffer_load_dwordx4 v64, s[48:51], s88 offen offset:2048 lds\nQmvaa 212,124,192\nQta 170,61,17288\nQmvav 216,128,40\nQmvaa 216,132,192\n"
"Qta 172,61,20904\ns_add_u32 s88, s86, 59136\nQmvav 220,136,40\nbuffer_load_dwordx4 v64, s[48:51], s88 offen offset:3072 lds\nQmvaa 220,140,192\nQta 174,61,21416\nQmvav 252,136,44\nQmvaa 252,140,196\n"
"s_add_u32 m0, s63, 37120\ns_add_u32 s88, s86, 82688\nQmvav 248,128,44\nbuffer_load_dwordx4 v64, s[48:51], s88 offen lds\nQmvaa 248,132,196\nQmvav 244,120,44\nQmvaa 244,124,196\ns_add_u32 s88, s86, 102144\n"
"Qmvav 240,112,44\nbuffer_load_dwordx4 v64, s[48:51], s88 offen offset:1024 lds\nQmvaa 240,116,196\nQmaav 224,112,48\nQmaaa 224,116,200\ns_add_u32 s88, s86, 121600\nQmaav 228,120,48\nbuffer_load_dwordx4 v64, s[48:51], s88 offen offset:2048 lds\n"
"Qmaaa 228,124,200\nQmaav 232,128,48\nQmaaa 232,132,200\nQmaav 236,136,48\nQmaaa 236,140,200\ns_waitcnt lgkmcnt(0)\nQmvav 96,144,24\nQmvaa 96,148,176\n"
"s_add_u32 s68, s71, 32768\nQmvav 100,152,24\ns_cbranch_vccz .Lst9\nbuffer_store_dwordx4 a[72:75], v60, s[56:59], s68 offen offset:0 sc1\n.Lst9:\nQmvaa 100,156,176\nQmvav 104,160,24\ns_add_u32 s68, s71, 47104\n"
"Qmvaa 104,164,176\ns_cbranch_vccz .Lst8\nbuffer_store_dwordx4 a[76:79], v60, s[56:59], s68 offen offset:0 sc1\n.Lst8:\nQmvav 108,168,24\nQmvaa 108,172,176\ns_waitcnt lgkmcnt(0)\nQmvav 140,168,28\n"
"s_waitcnt vmcnt(16)\ns_barrier\nQmvaa 140,172,180\nQdv 24,63,0\ns_add_u32 m0, s62, 16512\ns_add_u32 s69, s87, 3145856\nQmvav 136,160,28\nbuffer_load_dwordx4 v62, s[52:55], s69 offen lds\n"
"Qmvaa 136,164,180\nQda 176,63,512\nQmvav 132,152,28\nQta 112,61,33024\nQmvaa 132,156,180\ns_add_u32 s69, s87, 3210360\nQmvav 128,144,28\nbuffer_load_dwordx4 v62, s[52:55], s69 offen offset:1032 lds\n"
"Qmvaa 128,148,180\nQdv 28,63,1024\nQmvav 160,144,32\nQda 180,63,1536\nQmvaa 160,148,184\nQta 114,61,33536\ns_add_u32 s69, s87, 3274864\nQmvav 164,152,32\n"
"buffer_load_dwordx4 v62, s[52:55], s69 offen offset:2064 lds\nQmvaa 164,156,184\nQta 116,61,37152\nQmvav 168,160,32\nQmvaa 168,164,184\nQta 118,61,37664\ns_add_u32 s69, s87, 3339368\nQmvav 172,168,32\n"
"buffer_load_dwordx4 v62, s[52:55], s69 offen offset:3096 lds\nQmvaa 172,172,184\nQdv 32,63,2048\nQmvav 204,168,36\nQda 184,63,2560\nQmvaa 204,172,188\nQta 120,61,33032\ns_add_u32 m0, s61, 8192\n"
"Qmvav 200,160,36\ns_cbranch_vccnz .Ld0q4\nbuffer_load_dwordx4 v72, s[44:47], s90 offen nt lds\n.Ld0q4:\nQmvaa 200,164,188\nQta 122,61,33544\nQmvav 196,152,36\nQmvaa 196,156,188\n"
"Qta 124,61,37160\nQmvav 192,144,36\nQdv 36,63,3072\nQmvaa 192,148,188\nQda 188,63,3584\nQmvav 224,144,40\nQta 126,61,37672\nQmvaa 224,148,192\n"
"Qmvav 228,152,40\nQta 128,61,33280\nQmvaa 228,156,192\nQmvav 232,160,40\nQta 130,61,33792\nQmvaa 232,164,192\nQmvav 236,168,40\nQdv 40,63,4096\n"
"Qmvaa 236,172,192\nQda 192,63,4608\nQmaav 220,168,44\nQta 132,61,37408\nQmaaa 220,172,196\nQmaav 216,160,44\nQta 134,61,37920\nQmaaa 216,164,196\n"
"Qmaav 212,152,44\nQta 136,61,33288\nQmaaa 212,156,196\nQmaav 208,144,44\nQdv 44,63,5120\nQmaaa 208,148,196\nQda 196,63,5632\nQmaav 240,144,48\n"
"Qta 138,61,33800\nQmaaa 240,148,200\nQmaav 244,152,48\nQta 140,61,37416\nQmaaa 244,156,200\nQmaav 248,160,48\nQta 142,61,37928\nQmaaa 248,164,200\n"
"Qmaav 252,168,48\nQdv 48,63,6144\nQmaaa 252,172,200\nQda 200,63,6656\ns_waitcnt lgkmcnt(2)\nQmvav 80,112,24\nQmvaa 80,116,176\ns_add_u32 s68, s71, 4096\n"
"Qmvav 84,120,24\ns_cbranch_vccz .Lst7\nbuffer_store_dwordx4 a[80:83], v60, s[56:59], s68 offen offset:1024 sc1\n.Lst7:\nQmvaa 84,124,176\nQmvav 88,128,24\ns_add_u32 s68, s71, 18432\nQmvaa 88,132,176\n"
"s_cbranch_vccz .Lst6\nbuffer_store_dwordx4 a[84:87], v60, s[56:59], s68 offen offset:1024 sc1\n.Lst6:\nQmvav 92,136,24\nQmvaa 92,140,176\ns_waitcnt lgkmcnt(0)\nQmvav 124,136,28\ns_waitcnt vmcnt(17)\n"
"s_barrier\nQmvaa 124,140,180\nQta 144,61,49536\ns_add_u32 m0, s62, 33024\ns_add_u32 s69, s87, 3670016\nQmvav 120,128,28\nbuffer_load_dwordx4 v62, s[52:55], s69 offen lds\nQmvaa 120,132,180\n"
"Qta 146,61,50048\nQmvav 116,120,28\nQmvaa 116,124,180\nQta 148,61,53664\ns_add_u32 s69, s87, 3734520\nQmvav 112,112,28\nbuffer_load_dwordx4 v62, s[52:55], s69 offen offset:1032 lds\nQmvaa 112,116,180\n"
"Qta 150,61,54176\nQmvav 144,112,32\nQmvaa 144,116,184\nQta 152,61,49544\ns_add_u32 s69, s87, 3799024\nQmvav 148,120,32\nbuffer_load_dwordx4 v62, s[52:55], s69 offen offset:2064 lds\nQmvaa 148,124,184\n"
"Qta 154,61,50056\nQmvav 152,128,32\nQmvaa 152,132,184\nQta 156,61,53672\ns_add_u32 s69, s87, 3863528\nQmvav 156,136,32\nbuffer_load_dwordx4 v62, s[52:55], s69 offen offset:3096 lds\nQmvaa 156,140,184\n"
"Qta 158,61,54184\nQmvav 188,136,36\nQmvaa 188,140,188\nQta 160,61,49792\ns_add_u32 m0, s63, 0\ns_add_u32 s88, s86, 896\nQmvav 184,128,36\nbuffer_load_dwordx4 v64, s[48:51], s88 offen lds\n"
"Qmvaa 184,132,188\nQta 162,61,50304\nQmvav 180,120,36\nQmvaa 180,124,188\nQta 164,61,53920\ns_add_u32 s88, s86, 20352\nQmvav 176,112,36\nbuffer_load_dwordx4 v64, s[48:51], s88 offen offset:1024 lds\n"
"Qmvaa 176,116,188\nQta 166,61,54432\nQmvav 208,112,40\nQmvaa 208,116,192\nQta 168,61,49800\ns_add_u32 s88, s86, 39808\nQmvav 212,120,40\nbuffer_load_dwordx4 v64, s[48:51], s88 offen offset:2048 lds\n"
"Qmvaa 212,124,192\nQta 170,61,50312\nQmvav 216,128,40\nQmvaa 216,132,192\nQta 172,61,53928\ns_add_u32 s88, s86, 59264\nQmvav 220,136,40\nbuffer_load_dwordx4 v64, s[48:51], s88 offen offset:3072 lds\n"
"Qmvaa 220,140,192\nQta 174,61,54440\nQmvav 252,136,44\nQmvaa 252,140,196\ns_add_u32 m0, s63, 4096\ns_add_u32 s88, s86, 82816\nQmvav 248,128,44\nbuffer_load_dwordx4 v64, s[48:51], s88 offen lds\n"
"Qmvaa 248,132,196\nQmvav 244,120,44\nQmvaa 244,124,196\ns_add_u32 s88, s86, 102272\nQmvav 240,112,44\nbuffer_load_dwordx4 v64, s[48:51], s88 offen offset:1024 lds\nQmvaa 240,116,196\nQmaav 224,112,48\n"
"Qmaaa 224,116,200\ns_add_u32 s88, s86, 121728\nQmaav 228,120,48\nbuffer_load_dwordx4 v64, s[48:51], s88 offen offset:2048 lds\nQmaaa 228,124,200\nQmaav 232,128,48\nQmaaa 232,132,200\nQmaav 236,136,48\n"
"Qmaaa 236,140,200\ns_waitcnt lgkmcnt(0)\nQmvav 96,144,24\nQmvaa 96,148,176\ns_add_u32 s68, s71, 32768\nQmvav 100,152,24\ns_cbranch_vccz .Lst5\nbuffer_store_dwordx4 a[88:91], v60, s[56:59], s68 offen offset:1024 sc1\n"
".Lst5:\nQmvaa 100,156,176\nQmvav 104,160,24\ns_add_u32 s68, s71, 47104\nQmvaa 104,164,176\ns_cbranch_vccz .Lst4\nbuffer_store_dwordx4 a[92:95], v60, s[56:59], s68 offen offset:1024 sc1\n.Lst4:\n"
"Qmvav 108,168,24\nQmvaa 108,172,176\ns_waitcnt lgkmcnt(0)\nQmvav 140,168,28\ns_waitcnt vmcnt(16)\ns_barrier\nQmvaa 140,172,180\nQdv 24,63,33024\n"
"s_add_u32 m0, s62, 49536\ns_add_u32 s69, s87, 3670144\nQmvav 136,160,28\nbuffer_load_dwordx4 v62, s[52:55], s69 offen lds\nQmvaa 136,164,180\nQda 176,63,33536\nQmvav 132,152,28\nQta 112,61,0\n"
"Qmvaa 132,156,180\ns_add_u32 s69, s87, 3734648\nQmvav 128,144,28\nbuffer_load_dwordx4 v62, s[52:55], s69 offen offset:1032 lds\nQmvaa 128,148,180\nQdv 28,63,34048\nQmvav 160,144,32\nQda 180,63,34560\n"
"Qmvaa 160,148,184\nQta 114,61,512\ns_add_u32 s69, s87, 3799152\nQmvav 164,152,32\nbuffer_load_dwordx4 v62, s[52:55], s69 offen offset:2064 lds\nQmvaa 164,156,184\nQta 116,61,4128\nQmvav 168,160,32\n"
"Qmvaa 168,164,184\nQta 118,61,4640\ns_add_u32 s69, s87, 3863656\nQmvav 172,168,32\nbuffer_load_dwordx4 v62, s[52:55], s69 offen offset:3096 lds\nQmvaa 172,172,184\nQdv 32,63,35072\nQmvav 204,168,36\n"
"Qda 184,63,35584\nQmvaa 204,172,188\nQta 120,61,8\ns_add_u32 m0, s61, 8192\nQmvav 200,160,36\ns_cbranch_vccnz .Ld0q5\nbuffer_load_dwordx4 v72, s[44:47], s60 offen offset:1024 nt lds\n.Ld0q5:\n"
"Qmvaa 200,164,188\nQta 122,61,520\nQmvav 196,152,36\nQmvaa 196,156,188\nQta 124,61,4136\nQmvav 192,144,36\nQdv 36,63,36096\nQmvaa 192,148,188\n"
"Qda 188,63,36608\nQmvav 224,144,40\nQta 126,61,4648\nQmvaa 224,148,192\nQmvav 228,152,40\nQta 128,61,256\nQmvaa 228,156,192\nQmvav 232,160,40\n"
"Qta 130,61,768\nQmvaa 232,164,192\nQmvav 236,168,40\nQdv 40,63,37120\nQmvaa 236,172,192\nQda 192,63,37632\nQmaav 220,168,44\nQta 132,61,4384\n"
"Qmaaa 220,172,196\nQmaav 216,160,44\nQta 134,61,4896\nQmaaa 216,164,196\nQmaav 212,152,44\nQta 136,61,264\nQmaaa 212,156,196\nQmaav 208,144,44\n"
"Qdv 44,63,38144\nQmaaa 208,148,196\nQda 196,63,38656\nQmaav 240,144,48\nQta 138,61,776\nQmaaa 240,148,200\nQmaav 244,152,48\nQta 140,61,4392\n"
"Qmaaa 244,156,200\nQmaav 248,160,48\nQta 142,61,4904\nQmaaa 248,164,200\nQmaav 252,168,48\n.Lpg2:\ns_cbranch_execz .Ltr0\nQdv 48,63,39168\n"
"Qmaaa 252,172,200\nQda 200,63,39680\ns_waitcnt lgkmcnt(2)\nQmvav 80,112,24\nQmvaa 80,116,176\ns_add_u32 s68, s71, 4096\nQmvav 84,120,24\ns_cbranch_vccz .Lst3\n"
"buffer_store_dwordx4 a[96:99], v60, s[56:59], s68 offen offset:2048 sc1\n.Lst3:\nQmvaa 84,124,176\nQmvav 88,128,24\ns_add_u32 s68, s71, 18432\nQmvaa 88,132,176\ns_cbranch_vccz .Lst2\nbuffer_store_dwordx4 a[100:103], v60, s[56:59], s68 offen offset:2048 sc1\n"
".Lst2:\nQmvav 92,136,24\nQmvaa 92,140,176\ns_waitcnt lgkmcnt(0)\nQmvav 124,136,28\ns_waitcnt vmcnt(17)\ns_barrier\nQmvaa 124,140,180\n"
"Qta 144,61,16512\ns_add_u32 m0, s62, 0\ns_add_u32 s69, s87, 4194304\nQmvav 120,128,28\nbuffer_load_dwordx4 v62, s[52:55], s69 offen lds\nQmvaa 120,132,180\nQta 146,61,17024\nQmvav 116,120,28\n"
"Qmvaa 116,124,180\nQta 148,61,20640\ns_add_u32 s69, s87, 4258808\nQmvav 112,112,28\nbuffer_load_dwordx4 v62, s[52:55], s69 offen offset:1032 lds\nQmvaa 112,116,180\nQta 150,61,21152\nQmvav 144,112,32\n"
"Qmvaa 144,116,184\nQta 152,61,16520\ns_add_u32 s69, s87, 4323312\nQmvav 148,120,32\nbuffer_load_dwordx4 v62, s[52:55], s69 offen offset:2064 lds\nQmvaa 148,124,184\nQta 154,61,17032\nQmvav 152,128,32\n"
"Qmvaa 152,132,184\nQta 156,61,20648\ns_add_u32 s69, s87, 4387816\nQmvav 156,136,32\nbuffer_load_dwordx4 v62, s[52:55], s69 offen offset:3096 lds\nQmvaa 156,140,184\nQta 158,61,21160\nQmvav 188,136,36\n"
"Qmvaa 188,140,188\nQta 160,61,16768\ns_add_u32 m0, s63, 33024\ns_add_u32 s88, s86, 1024\nQmvav 184,128,36\nbuffer_load_dwordx4 v64, s[48:51], s88 offen lds\nQmvaa 184,132,188\nQta 162,61,17280\n"
"Qmvav 180,120,36\nQmvaa 180,124,188\nQta 164,61,20896\ns_add_u32 s88, s86, 20480\nQmvav 176,112,36\nbuffer_load_dwordx4 v64, s[48:51], s88 offen offset:1024 lds\nQmvaa 176,116,188\nQta 166,61,21408\n"
"Qmvav 208,112,40\nQmvaa 208,116,192\nQta 168,61,16776\ns_add_u32 s88, s86, 39936\nQmvav 212,120,40\nbuffer_load_dwordx4 v64, s[48:51], s88 offen offset:2048 lds\nQmvaa 212,124,192\nQta 170,61,17288\n"
"Qmvav 216,128,40\nQmvaa 216,132,192\nQta 172,61,20904\ns_add_u32 s88, s86, 59392\nQmvav 220,136,40\nbuffer_load_dwordx4 v64, s[48:51], s88 offen offset:3072 lds\nQmvaa 220,140,192\nQta 174,61,21416\n"
"Qmvav 252,136,44\nQmvaa 252,140,196\ns_add_u32 m0, s63, 37120\ns_add_u32 s88, s86, 82944\nQmvav 248,128,44\nbuffer_load_dwordx4 v64, s[48:51], s88 offen lds\nQmvaa 248,132,196\nQmvav 244,120,44\n"
"Qmvaa 244,124,196\ns_add_u32 s88, s86, 102400\nQmvav 240,112,44\nbuffer_load_dwordx4 v64, s[48:51], s88 offen offset:1024 lds\nQmvaa 240,116,196\nQmaav 224,112,48\nQmaaa 224,116,200\ns_add_u32 s88, s86, 121856\n"
"Qmaav 228,120,48\nbuffer_load_dwordx4 v64, s[48:51], s88 offen offset:2048 lds\nQmaaa 228,124,200\nQmaav 232,128,48\nQmaaa 232,132,200\nQmaav 236,136,48\nQmaaa 236,140,200\ns_waitcnt lgkmcnt(0)\n"
"Qmvav 96,144,24\nQmvaa 96,148,176\ns_add_u32 s68, s71, 32768\nQmvav 100,152,24\ns_cbranch_vccz .Lst1\nbuffer_store_dwordx4 a[104:107], v60, s[56:59], s68 offen offset:2048 sc1\n.Lst1:\nQmvaa 100,156,176\n"
"Qmvav 104,160,24\ns_add_u32 s68, s71, 47104\nQmvaa 104,164,176\ns_cbranch_vccz .Lst0\nbuffer_store_dwordx4 a[108:111], v60, s[56:59], s68 offen offset:2048 sc1\n.Lst0:\nQmvav 108,168,24\nQmvaa 108,172,176\n"
"s_waitcnt lgkmcnt(0)\nQmvav 140,168,28\ns_waitcnt vmcnt(16)\ns_barrier\nQmvaa 140,172,180\nQdv 24,63,0\ns_add_u32 m0, s62, 16512\ns_add_u32 s69, s87, 4194432\n"
"Qmvav 136,160,28\nbuffer_load_dwordx4 v62, s[52:55], s69 offen lds\nQmvaa 136,164,180\nQda 176,63,512\nQmvav 132,152,28\nQta 112,61,33024\nQmvaa 132,156,180\ns_add_u32 s69, s87, 4258936\n"
"Qmvav 128,144,28\nbuffer_load_dwordx4 v62, s[52:55], s69 offen offset:1032 lds\nQmvaa 128,148,180\nQdv 28,63,1024\nQmvav 160,144,32\nQda 180,63,1536\nQmvaa 160,148,184\nQta 114,61,33536\n"
"s_add_u32 s69, s87, 4323440\nQmvav 164,152,32\nbuffer_load_dwordx4 v62, s[52:55], s69 offen offset:2064 lds\nQmvaa 164,156,184\nQta 116,61,37152\nQmvav 168,160,32\nQmvaa 168,164,184\nQta 118,61,37664\n"
"s_add_u32 s69, s87, 4387944\nQmvav 172,168,32\nbuffer_load_dwordx4 v62, s[52:55], s69 offen offset:3096 lds\nQmvaa 172,172,184\nQdv 32,63,2048\nQmvav 204,168,36\nQda 184,63,2560\nQmvaa 204,172,188\n"
"Qta 120,61,33032\ns_add_u32 m0, s61, 8192\nQmvav 200,160,36\ns_cbranch_vccnz .Ld0q6\nbuffer_load_dwordx4 v72, s[44:47], s90 offen offset:2048 nt lds\n.Ld0q6:\nQmvaa 200,164,188\nQta 122,61,33544\n"
"Qmvav 196,152,36\nQmvaa 196,156,188\nQta 124,61,37160\nQmvav 192,144,36\nQdv 36,63,3072\nQmvaa 192,148,188\nQda 188,63,3584\nQmvav 224,144,40\n"
"Qta 126,61,37672\nQmvaa 224,148,192\nQmvav 228,152,40\nQta 128,61,33280\nQmvaa 228,156,192\nQmvav 232,160,40\nQta 130,61,33792\nQmvaa 232,164,192\n"
"Qmvav 236,168,40\nQdv 40,63,4096\nQmvaa 236,172,192\nQda 192,63,4608\nQmaav 220,168,44\nQta 132,61,37408\nQmaaa 220,172,196\nQmaav 216,160,44\n"
"Qta 134,61,37920\nQmaaa 216,164,196\nQmaav 212,152,44\nQta 136,61,33288\nQmaaa 212,156,196\nQmaav 208,144,44\nQdv 44,63,5120\nQmaaa 208,148,196\n"
"Qda 196,63,5632\nQmaav 240,144,48\nQta 138,61,33800\nQmaaa 240,148,200\nQmaav 244,152,48\nQta 140,61,37416\nQmaaa 244,156,200\nQmaav 248,160,48\n"
"Qta 142,61,37928\nQmaaa 248,164,200\nQmaav 252,168,48\nQdv 48,63,6144\nQmaaa 252,172,200\nQda 200,63,6656\ns_waitcnt lgkmcnt(2)\nQmvav 80,112,24\n"
"Qmvaa 80,116,176\nQmvav 84,120,24\nQmvaa 84,124,176\nQmvav 88,128,24\nQmvaa 88,132,176\nQmvav 92,136,24\nQmvaa 92,140,176\ns_waitcnt lgkmcnt(0)\n"
"Qmvav 124,136,28\ns_waitcnt vmcnt(17)\ns_barrier\nQmvaa 124,140,180\nQta 144,61,49536\ns_add_u32 m0, s62, 33024\ns_add_u32 s69, s87, 4718592\nQmvav 120,128,28\n"
"buffer_load_dwordx4 v62, s[52:55], s69 offen lds\nQmvaa 120,132,180\nQta 146,61,50048\nQmvav 116,120,28\nQmvaa 116,124,180\nQta 148,61,53664\ns_add_u32 s69, s87, 4783096\nQmvav 112,112,28\n"
"buffer_load_dwordx4 v62, s[52:55], s69 offen offset:1032 lds\nQmvaa 112,116,180\nQta 150,61,54176\nQmvav 144,112,32\nQmvaa 144,116,184\nQta 152,61,49544\ns_add_u32 s69, s87, 4847600\nQmvav 148,120,32\n"
"buffer_load_dwordx4 v62, s[52:55], s69 offen offset:2064 lds\nQmvaa 148,124,184\nQta 154,61,50056\nQmvav 152,128,32\nQmvaa 152,132,184\nQta 156,61,53672\ns_add_u32 s69, s87, 4912104\nQmvav 156,136,32\n"
"buffer_load_dwordx4 v62, s[52:55], s69 offen offset:3096 lds\nQmvaa 156,140,184\nQta 158,61,54184\nQmvav 188,136,36\nQmvaa 188,140,188\nQta 160,61,49792\ns_add_u32 m0, s63, 0\ns_add_u32 s88, s86, 1152\n"
"Qmvav 184,128,36\nbuffer_load_dwordx4 v64, s[48:51], s88 offen lds\nQmvaa 184,132,188\nQta 162,61,50304\nQmvav 180,120,36\nQmvaa 180,124,188\nQta 164,61,53920\ns_add_u32 s88, s86, 20608\n"
"Qmvav 176,112,36\nbuffer_load_dwordx4 v64, s[48:51], s88 offen offset:1024 lds\nQmvaa 176,116,188\nQta 166,61,54432\nQmvav 208,112,40\nQmvaa 208,116,192\nQta 168,61,49800\ns_add_u32 s88, s86, 40064\n"
"Qmvav 212,120,40\nbuffer_load_dwordx4 v64, s[48:51], s88 offen offset:2048 lds\nQmvaa 212,124,192\nQta 170,61,50312\nQmvav 216,128,40\nQmvaa 216,132,192\nQta 172,61,53928\ns_add_u32 s88, s86, 59520\n"
"Qmvav 220,136,40\nbuffer_load_dwordx4 v64, s[48:51], s88 offen offset:3072 lds\nQmvaa 220,140,192\nQta 174,61,54440\nQmvav 252,136,44\nQmvaa 252,140,196\ns_add_u32 m0, s63, 4096\ns_add_u32 s88, s86, 83072\n"
"Qmvav 248,128,44\nbuffer_load_dwordx4 v64, s[48:51], s88 offen lds\nQmvaa 248,132,196\nQmvav 244,120,44\nQmvaa 244,124,196\ns_add_u32 s88, s86, 102528\nQmvav 240,112,44\nbuffer_load_dwordx4 v64, s[48:51], s88 offen offset:1024 lds\n"
"Qmvaa 240,116,196\nQmaav 224,112,48\nQmaaa 224,116,200\ns_add_u32 s88, s86, 121984\nQmaav 228,120,48\nbuffer_load_dwordx4 v64, s[48:51], s88 offen offset:2048 lds\nQmaaa 228,124,200\nQmaav 232,128,48\n"
"Qmaaa 232,132,200\nQmaav 236,136,48\nQmaaa 236,140,200\ns_waitcnt lgkmcnt(0)\nQmvav 96,144,24\nQmvaa 96,148,176\nQmvav 100,152,24\nQmvaa 100,156,176\n"
"Qmvav 104,160,24\nQmvaa 104,164,176\nQmvav 108,168,24\nQmvaa 108,172,176\ns_waitcnt lgkmcnt(0)\nQmvav 140,168,28\ns_waitcnt vmcnt(16)\ns_barrier\n"
"Qmvaa 140,172,180\nQdv 24,63,33024\ns_add_u32 m0, s62, 49536\ns_add_u32 s69, s87, 4718720\nQmvav 136,160,28\nbuffer_load_dwordx4 v62, s[52:55], s69 offen lds\nQmvaa 136,164,180\nQda 176,63,33536\n"
"Qmvav 132,152,28\nQta 112,61,0\nQmvaa 132,156,180\ns_add_u32 s69, s87, 4783224\nQmvav 128,144,28\nbuffer_load_dwordx4 v62, s[52:55], s69 offen offset:1032 lds\nQmvaa 128,148,180\nQdv 28,63,34048\n"
"Qmvav 160,144,32\nQda 180,63,34560\nQmvaa 160,148,184\nQta 114,61,512\ns_add_u32 s69, s87, 4847728\nQmvav 164,152,32\nbuffer_load_dwordx4 v62, s[52:55], s69 offen offset:2064 lds\nQmvaa 164,156,184\n"
"Qta 116,61,4128\nQmvav 168,160,32\nQmvaa 168,164,184\nQta 118,61,4640\ns_add_u32 s69, s87, 4912232\nQmvav 172,168,32\nbuffer_load_dwordx4 v62, s[52:55], s69 offen offset:3096 lds\nQmvaa 172,172,184\n"
"Qdv 32,63,35072\nQmvav 204,168,36\nQda 184,63,35584\nQmvaa 204,172,188\nQta 120,61,8\ns_add_u32 m0, s61, 8192\nQmvav 200,160,36\ns_cbranch_vccnz .Ld0q7\n"
"buffer_load_dwordx4 v72, s[44:47], s60 offen offset:3072 nt lds\ns_add_u32 s60, s60, 8192\ns_add_u32 s90, s60, 896\n.Ld0q7:\nQmvaa 200,164,188\nQta 122,61,520\nQmvav 196,152,36\nQmvaa 196,156,188\n"
"Qta 124,61,4136\nQmvav 192,144,36\nQdv 36,63,36096\nQmvaa 192,148,188\nQda 188,63,36608\nQmvav 224,144,40\nQta 126,61,4648\nQmvaa 224,148,192\n"
"Qmvav 228,152,40\nQta 128,61,256\nQmvaa 228,156,192\nQmvav 232,160,40\nQta 130,61,768\nQmvaa 232,164,192\nQmvav 236,168,40\nQdv 40,63,37120\n"
"Qmvaa 236,172,192\nQda 192,63,37632\nQmaav 220,168,44\nQta 132,61,4384\nQmaaa 220,172,196\nQmaav 216,160,44\nQta 134,61,4896\nQmaaa 216,164,196\n"
"Qmaav 212,152,44\nQta 136,61,264\nQmaaa 212,156,196\nQmaav 208,144,44\nQdv 44,63,38144\nQmaaa 208,148,196\nQda 196,63,38656\nQmaav 240,144,48\n"
"Qta 138,61,776\nQmaaa 240,148,200\nQmaav 244,152,48\nQta 140,61,4392\nQmaaa 244,156,200\nQmaav 248,160,48\nQta 142,61,4904\nQmaaa 248,164,200\n"
"Qmaav 252,168,48\nQdv 48,63,39168\nQmaaa 252,172,200\nQda 200,63,39680\ns_waitcnt lgkmcnt(2)\nQmvav 80,112,24\nQmvaa 80,116,176\nQmvav 84,120,24\n"
"Qmvaa 84,124,176\nQmvav 88,128,24\nQmvaa 88,132,176\nQmvav 92,136,24\nQmvaa 92,140,176\ns_waitcnt lgkmcnt(0)\nQmvav 124,136,28\ns_waitcnt vmcnt(15)\n"
"s_barrier\nQmvaa 124,140,180\nQta 144,61,16512\ns_mov_b32 s56, s94\ns_mov_b32 s57, s95\ns_mov_b32 s58, s75\ns_mov_b32 s94, 0\ns_add_u32 m0, %10, 160768\n"
"Qmvav 120,128,28\nbuffer_load_dwordx4 v60, s[56:59], s94 offen lds\nQmvaa 120,132,180\nQta 146,61,17024\nQmvav 116,120,28\nQmvaa 116,124,180\nQta 148,61,20640\ns_add_u32 s94, s94, 4096\n"
"Qmvav 112,112,28\nbuffer_load_dwordx4 v60, s[56:59], s94 offen lds\nQmvaa 112,116,180\nQta 150,61,21152\nQmvav 144,112,32\nQmvaa 144,116,184\nQta 152,61,16520\ns_add_u32 s94, s94, 4096\n"
"Qmvav 148,120,32\nbuffer_load_dwordx4 v60, s[56:59], s94 offen lds\nQmvaa 148,124,184\nQta 154,61,17032\nQmvav 152,128,32\nQmvaa 152,132,184\nQta 156,61,20648\ns_add_u32 s94, s94, 4096\n"
"Qmvav 156,136,32\nbuffer_load_dwordx4 v60, s[56:59], s94 offen lds\nQmvaa 156,140,184\nQta 158,61,21160\nQmvav 188,136,36\nQmvaa 188,140,188\nQta 160,61,16768\ns_add_u32 s94, s94, 4096\n"
"Qmvav 184,128,36\nbuffer_load_dwordx4 v60, s[56:59], s94 offen lds\nQmvaa 184,132,188\nQta 162,61,17280\nQmvav 180,120,36\nQmvaa 180,124,188\nQta 164,61,20896\ns_add_u32 s94, s94, 4096\n"
"Qmvav 176,112,36\nbuffer_load_dwordx4 v60, s[56:59], s94 offen lds\nQmvaa 176,116,188\nQta 166,61,21408\nQmvav 208,112,40\nQmvaa 208,116,192\nQta 168,61,16776\ns_add_u32 s94, s94, 4096\n"
"Qmvav 212,120,40\nbuffer_load_dwordx4 v60, s[56:59], s94 offen lds\nQmvaa 212,124,192\nQta 170,61,17288\nQmvav 216,128,40\nQmvaa 216,132,192\nQta 172,61,20904\ns_add_u32 s94, s94, 4096\n"
"Qmvav 220,136,40\nbuffer_load_dwordx4 v60, s[56:59], s94 offen lds\nQmvaa 220,140,192\nQta 174,61,21416\nQmvav 252,136,44\nQmvaa 252,140,196\ns_add_u32 s94, s94, 4096\nQmvav 248,128,44\n"
"buffer_load_dwordx4 v60, s[56:59], s94 offen lds\nQmvaa 248,132,196\nQmvav 244,120,44\nQmvaa 244,124,196\ns_add_u32 s94, s94, 4096\nQmvav 240,112,44\nbuffer_load_dwordx4 v60, s[56:59], s94 offen lds\nQmvaa 240,116,196\n"
"Qmaav 224,112,48\nQmaaa 224,116,200\nQmaav 228,120,48\nQmaaa 228,124,200\nQmaav 232,128,48\nQmaaa 232,132,200\nQmaav 236,136,48\nQmaaa 236,140,200\n"
"s_waitcnt lgkmcnt(0)\nQmvav 96,144,24\nQmvaa 96,148,176\nQmvav 100,152,24\nQmvaa 100,156,176\n.Lpg3:\ns_cbranch_execz .Ltr0\nQmvav 104,160,24\n"
"Qmvaa 104,164,176\nQmvav 108,168,24\nQmvaa 108,172,176\ns_waitcnt lgkmcnt(0)\nQmvav 140,168,28\ns_waitcnt vmcnt(14)\ns_barrier\nQmvaa 140,172,180\n"
"Qdv 24,63,0\ns_add_u32 m0, s61, 33024\nQmvav 136,160,28\nbuffer_load_dwordx4 v69, s[40:43], s90 offen nt lds\nQmvaa 136,164,180\nQda 176,63,512\nQmvav 132,152,28\nQta 112,61,33024\n"
"Qmvaa 132,156,180\nQmvav 128,144,28\nbuffer_load_dwordx4 v69, s[40:43], s60 offen offset:1024 nt lds\nQmvaa 128,148,180\nQdv 28,63,1024\nQmvav 160,144,32\nQda 180,63,1536\nQmvaa 160,148,184\n"
"Qta 114,61,33536\nQmvav 164,152,32\nbuffer_load_dwordx4 v69, s[40:43], s90 offen offset:2048 nt lds\nQmvaa 164,156,184\nQta 116,61,37152\nQmvav 168,160,32\nQmvaa 168,164,184\nQta 118,61,37664\n"
"Qmvav 172,168,32\nbuffer_load_dwordx4 v69, s[40:43], s60 offen offset:3072 nt lds\nQmvaa 172,172,184\nQdv 32,63,2048\nQmvav 204,168,36\nQda 184,63,2560\nQmvaa 204,172,188\nQta 120,61,33032\n"
"s_add_u32 m0, s61, 41216\nQmvav 200,160,36\nbuffer_load_dwordx4 v72, s[44:47], s90 offen nt lds\nQmvaa 200,164,188\nQta 122,61,33544\nQmvav 196,152,36\nQmvaa 196,156,188\nQta 124,61,37160\n"
"Qmvav 192,144,36\nbuffer_load_dwordx4 v72, s[44:47], s60 offen offset:1024 nt lds\nQmvaa 192,148,188\nQdv 36,63,3072\nQmvav 224,144,40\nQda 188,63,3584\nQmvaa 224,148,192\nQta 126,61,37672\n"
"Qmvav 228,152,40\nbuffer_load_dwordx4 v72, s[44:47], s90 offen offset:2048 nt lds\nQmvaa 228,156,192\nQta 128,61,33280\nQmvav 232,160,40\nQmvaa 232,164,192\nQta 130,61,33792\nQmvav 236,168,40\n"
"buffer_load_dwordx4 v72, s[44:47], s60 offen offset:3072 nt lds\ns_add_u32 s60, s60, 8192\ns_add_u32 s90, s60, 896\nQmvaa 236,172,192\nQdv 40,63,4096\nQmaav 220,168,44\nQda 192,63,4608\nQmaaa 220,172,196\n"
"Qta 132,61,37408\nQmaav 216,160,44\nQmaaa 216,164,196\nQta 134,61,37920\nQmaav 212,152,44\nQmaaa 212,156,196\nQta 136,61,33288\nQmaav 208,144,44\n"
"Qdv 44,63,5120\nQmaaa 208,148,196\nQda 196,63,5632\nQmaav 240,144,48\nQta 138,61,33800\nQmaaa 240,148,200\nQmaav 244,152,48\nQta 140,61,37416\n"
"Qmaaa 244,156,200\nQmaav 248,160,48\nQta 142,61,37928\nQmaaa 248,164,200\nQmaav 252,168,48\nQdv 48,63,6144\nQmaaa 252,172,200\nQda 200,63,6656\n"
"s_waitcnt lgkmcnt(2)\nQmvav 80,112,24\nQmvaa 80,116,176\nQmvav 84,120,24\nQmvaa 84,124,176\nQmvav 88,128,24\nQmvaa 88,132,176\nQmvav 92,136,24\n"
"Qmvaa 92,140,176\ns_waitcnt lgkmcnt(0)\nQmvav 124,136,28\ns_waitcnt vmcnt(18)\ns_barrier\nQmvaa 124,140,180\nQta 144,61,49536\nQmvav 120,128,28\n"
"Qmvaa 120,132,180\nQta 146,61,50048\nQmvav 116,120,28\nQmvaa 116,124,180\nQta 148,61,53664\nQmvav 112,112,28\nQmvaa 112,116,180\nQta 150,61,54176\n"
"Qmvav 144,112,32\nQmvaa 144,116,184\nQta 152,61,49544\nQmvav 148,120,32\nQmvaa 148,124,184\nQta 154,61,50056\nQmvav 152,128,32\nQmvaa 152,132,184\n"
"Qta 156,61,53672\nQmvav 156,136,32\nQmvaa 156,140,184\nQta 158,61,54184\nQmvav 188,136,36\nQmvaa 188,140,188\nQta 160,61,49792\nQmvav 184,128,36\n"
"Qmvaa 184,132,188\nQta 162,61,50304\nQmvav 180,120,36\nQmvaa 180,124,188\nQta 164,61,53920\nQmvav 176,112,36\nQmvaa 176,116,188\nQta 166,61,54432\n"
"Qmvav 208,112,40\nQmvaa 208,116,192\nQta 168,61,49800\nQmvav 212,120,40\nQmvaa 212,124,192\nQta 170,61,50312\nQmvav 216,128,40\nQmvaa 216,132,192\n"
"Qta 172,61,53928\nQmvav 220,136,40\nQmvaa 220,140,192\nQta 174,61,54440\nQmvav 252,136,44\nQmvaa 252,140,196\nQmvav 248,128,44\nQmvaa 248,132,196\n"
"Qmvav 244,120,44\nQmvaa 244,124,196\nQmvav 240,112,44\nQmvaa 240,116,196\nQmaav 224,112,48\nQmaaa 224,116,200\nQmaav 228,120,48\nQmaaa 228,124,200\n"
"Qmaav 232,128,48\nQmaaa 232,132,200\nQmaav 236,136,48\nQmaaa 236,140,200\ns_waitcnt lgkmcnt(0)\nQmvav 96,144,24\nQmvaa 96,148,176\nQmvav 100,152,24\n"
"Qmvaa 100,156,176\nQmvav 104,160,24\nQmvaa 104,164,176\nQmvav 108,168,24\nQmvaa 108,172,176\ns_waitcnt lgkmcnt(0)\nQmvav 140,168,28\ns_waitcnt vmcnt(18)\n"
"s_barrier\nQmvaa 140,172,180\nQda 112,68,0\ns_add_u32 m0, s61, 66048\nv_accvgpr_write_b32 a176, 0\nQmvav 136,160,28\nbuffer_load_dwordx4 v69, s[40:43], s90 offen nt lds\nQmvaa 136,164,180\n"
"Qda 116,68,512\nv_accvgpr_write_b32 a177, 0\nQmvav 132,152,28\nQmvaa 132,156,180\nQda 120,68,1024\nv_accvgpr_write_b32 a178, 0\nQmvav 128,144,28\nbuffer_load_dwordx4 v69, s[40:43], s60 offen offset:1024 nt lds\n"
"Qmvaa 128,148,180\nQda 124,68,1536\nv_accvgpr_write_b32 a179, 0\nQmvav 160,144,32\nQmvaa 160,148,184\nQda 128,68,2048\nv_accvgpr_write_b32 a180, 0\nQmvav 164,152,32\n"
"buffer_load_dwordx4 v69, s[40:43], s90 offen offset:2048 nt lds\nQmvaa 164,156,184\nQda 132,68,2560\nv_accvgpr_write_b32 a181, 0\nQmvav 168,160,32\nQmvaa 168,164,184\nQda 136,68,3072\nv_accvgpr_write_b32 a182, 0\n"
"Qmvav 172,168,32\nbuffer_load_dwordx4 v69, s[40:43], s60 offen offset:3072 nt lds\nQmvaa 172,172,184\nQda 140,68,3584\nv_accvgpr_write_b32 a183, 0\nQmvav 204,168,36\nQmvaa 204,172,188\ns_add_u32 m0, s61, 74240\n"
"v_accvgpr_write_b32 a184, 0\nQmvav 200,160,36\nbuffer_load_dwordx4 v72, s[44:47], s90 offen nt lds\nQmvaa 200,164,188\nv_accvgpr_write_b32 a185, 0\nQmvav 196,152,36\nQmvaa 196,156,188\nv_accvgpr_write_b32 a186, 0\n"
"Qmvav 192,144,36\nbuffer_load_dwordx4 v72, s[44:47], s60 offen offset:1024 nt lds\nQmvaa 192,148,188\nv_accvgpr_write_b32 a187, 0\nQmvav 224,144,40\nQmvaa 224,148,192\nv_accvgpr_write_b32 a188, 0\nQmvav 228,152,40\n"
"buffer_load_dwordx4 v72, s[44:47], s90 offen offset:2048 nt lds\nQmvaa 228,156,192\nv_accvgpr_write_b32 a189, 0\nQmvav 232,160,40\nQmvaa 232,164,192\nv_accvgpr_write_b32 a190, 0\nQmvav 236,168,40\nbuffer_load_dwordx4 v72, s[44:47], s60 offen offset:3072 nt lds\n"
"s_add_u32 s60, s60, 8192\ns_add_u32 s90, s60, 896\nQmvaa 236,172,192\nv_accvgpr_write_b32 a191, 0\nQmaav 220,168,44\nQmaaa 220,172,196\nv_accvgpr_write_b32 a192, 0\nQmaav 216,160,44\n"
"Qmaaa 216,164,196\nv_accvgpr_write_b32 a193, 0\nQmaav 212,152,44\nQmaaa 212,156,196\nv_accvgpr_write_b32 a194, 0\nQmaav 208,144,44\nQmaaa 208,148,196\nv_accvgpr_write_b32 a195, 0\n"
"Qmaav 240,144,48\nQmaaa 240,148,200\nv_accvgpr_write_b32 a196, 0\nQmaav 244,152,48\nQmaaa 244,156,200\nv_accvgpr_write_b32 a197, 0\nQmaav 248,160,48\nQmaaa 248,164,200\n"
"v_accvgpr_write_b32 a198, 0\nQmaav 252,168,48\nQmaaa 252,172,200\nv_accvgpr_write_b32 a199, 0\nQta 148,70,1024\nQta 150,70,3072\nQta 156,70,1280\nQta 158,70,3328\n"
"Qta 164,70,1536\nQta 166,70,3584\nQta 172,70,1792\nQta 174,70,3840\nv_pk_mul_f32 v[80:81], v[80:81], s[72:73] op_sel_hi:[1,0]\nv_pk_mul_f32 v[82:83], v[82:83], s[72:73] op_sel_hi:[1,0]\nv_pk_mul_f32 v[84:85], v[84:85], s[72:73] op_sel_hi:[1,0]\nv_pk_mul_f32 v[86:87], v[86:87], s[72:73] op_sel_hi:[1,0]\n"
"Qk 80,80,81\nQk 81,82,83\nQk 82,84,85\nQk 83,86,87\nv_accvgpr_write_b32 a0, v80\nv_accvgpr_write_b32 a1, v81\nv_accvgpr_write_b32 a2, v82\nv_accvgpr_write_b32 a3, v83\n"
"v_pk_mul_f32 v[88:89], v[88:89], s[72:73] op_sel_hi:[1,0]\nv_pk_mul_f32 v[90:91], v[90:91], s[72:73] op_sel_hi:[1,0]\nv_pk_mul_f32 v[92:93], v[92:93], s[72:73] op_sel_hi:[1,0]\nv_pk_mul_f32 v[94:95], v[94:95], s[72:73] op_sel_hi:[1,0]\nQk 88,88,89\nQk 89,90,91\nQk 90,92,93\nQk 91,94,95\n"
"v_accvgpr_write_b32 a4, v88\nv_accvgpr_write_b32 a5, v89\nv_accvgpr_write_b32 a6, v90\nv_accvgpr_write_b32 a7, v91\nv_pk_mul_f32 v[96:97], v[96:97], s[72:73] op_sel_hi:[1,0]\nv_pk_mul_f32 v[98:99], v[98:99], s[72:73] op_sel_hi:[1,0]\nv_pk_mul_f32 v[100:101], v[100:101], s[72:73] op_sel_hi:[1,0]\nv_pk_mul_f32 v[102:103], v[102:103], s[72:73] op_sel_hi:[1,0]\n"
"Qk 96,96,97\nQk 97,98,99\nQk 98,100,101\nQk 99,102,103\nv_accvgpr_write_b32 a8, v96\nv_accvgpr_write_b32 a9, v97\nv_accvgpr_write_b32 a10, v98\nv_accvgpr_write_b32 a11, v99\n"
"v_pk_mul_f32 v[104:105], v[104:105], s[72:73] op_sel_hi:[1,0]\nv_pk_mul_f32 v[106:107], v[106:107], s[72:73] op_sel_hi:[1,0]\nv_pk_mul_f32 v[108:109], v[108:109], s[72:73] op_sel_hi:[1,0]\nv_pk_mul_f32 v[110:111], v[110:111], s[72:73] op_sel_hi:[1,0]\nQk 104,104,105\nQk 105,106,107\nQk 106,108,109\nQk 107,110,111\n"
"v_accvgpr_write_b32 a12, v104\nv_accvgpr_write_b32 a13, v105\nv_accvgpr_write_b32 a14, v106\nv_accvgpr_write_b32 a15, v107\nv_pk_mul_f32 v[112:113], v[112:113], s[72:73] op_sel_hi:[1,0]\nv_pk_mul_f32 v[114:115], v[114:115], s[72:73] op_sel_hi:[1,0]\nv_pk_mul_f32 v[116:117], v[116:117], s[72:73] op_sel_hi:[1,0]\nv_pk_mul_f32 v[118:119], v[118:119], s[72:73] op_sel_hi:[1,0]\n"
"Qk 112,112,113\nQk 113,114,115\nQk 114,116,117\nQk 115,118,119\nv_accvgpr_write_b32 a16, v112\nv_accvgpr_write_b32 a17, v113\nv_accvgpr_write_b32 a18, v114\nv_accvgpr_write_b32 a19, v115\n"
"v_pk_mul_f32 v[120:121], v[120:121], s[72:73] op_sel_hi:[1,0]\nv_pk_mul_f32 v[122:123], v[122:123], s[72:73] op_sel_hi:[1,0]\nv_pk_mul_f32 v[124:125], v[124:125], s[72:73] op_sel_hi:[1,0]\nv_pk_mul_f32 v[126:127], v[126:127], s[72:73] op_sel_hi:[1,0]\nQk 120,120,121\nQk 121,122,123\nQk 122,124,125\nQk 123,126,127\n"
"v_accvgpr_write_b32 a20, v120\nv_accvgpr_write_b32 a21, v121\nv_accvgpr_write_b32 a22, v122\nv_accvgpr_write_b32 a23, v123\nv_pk_mul_f32 v[128:129], v[128:129], s[72:73] op_sel_hi:[1,0]\nv_pk_mul_f32 v[130:131], v[130:131], s[72:73] op_sel_hi:[1,0]\nv_pk_mul_f32 v[132:133], v[132:133], s[72:73] op_sel_hi:[1,0]\nv_pk_mul_f32 v[134:135], v[134:135], s[72:73] op_sel_hi:[1,0]\n"
"Qk 128,128,129\nQk 129,130,131\nQk 130,132,133\nQk 131,134,135\nv_accvgpr_write_b32 a24, v128\nv_accvgpr_write_b32 a25, v129\nv_accvgpr_write_b32 a26, v130\nv_accvgpr_write_b32 a27, v131\n"
"v_pk_mul_f32 v[136:137], v[136:137], s[72:73] op_sel_hi:[1,0]\nv_pk_mul_f32 v[138:139], v[138:139], s[72:73] op_sel_hi:[1,0]\nv_pk_mul_f32 v[140:141], v[140:141], s[72:73] op_sel_hi:[1,0]\nv_pk_mul_f32 v[142:143], v[142:143], s[72:73] op_sel_hi:[1,0]\nQk 136,136,137\nQk 137,138,139\nQk 138,140,141\nQk 139,142,143\n"
"v_accvgpr_write_b32 a28, v136\nv_accvgpr_write_b32 a29, v137\nv_accvgpr_write_b32 a30, v138\nv_accvgpr_write_b32 a31, v139\nv_pk_mul_f32 v[144:145], v[144:145], s[72:73] op_sel_hi:[1,0]\nv_pk_mul_f32 v[146:147], v[146:147], s[72:73] op_sel_hi:[1,0]\nv_pk_mul_f32 v[148:149], v[148:149], s[72:73] op_sel_hi:[1,0]\nv_pk_mul_f32 v[150:151], v[150:151], s[72:73] op_sel_hi:[1,0]\n"
"Qk 144,144,145\nQk 145,146,147\nQk 146,148,149\nQk 147,150,151\nv_accvgpr_write_b32 a32, v144\nv_accvgpr_write_b32 a33, v145\nv_accvgpr_write_b32 a34, v146\nv_accvgpr_write_b32 a35, v147\n"
"v_pk_mul_f32 v[152:153], v[152:153], s[72:73] op_sel_hi:[1,0]\nv_pk_mul_f32 v[154:155], v[154:155], s[72:73] op_sel_hi:[1,0]\nv_pk_mul_f32 v[156:157], v[156:157], s[72:73] op_sel_hi:[1,0]\nv_pk_mul_f32 v[158:159], v[158:159], s[72:73] op_sel_hi:[1,0]\nQk 152,152,153\nQk 153,154,155\nQk 154,156,157\nQk 155,158,159\n"
"v_accvgpr_write_b32 a36, v152\nv_accvgpr_write_b32 a37, v153\nv_accvgpr_write_b32 a38, v154\nv_accvgpr_write_b32 a39, v155\nv_pk_mul_f32 v[160:161], v[160:161], s[72:73] op_sel_hi:[1,0]\nv_pk_mul_f32 v[162:163], v[162:163], s[72:73] op_sel_hi:[1,0]\nv_pk_mul_f32 v[164:165], v[164:165], s[72:73] op_sel_hi:[1,0]\nv_pk_mul_f32 v[166:167], v[166:167], s[72:73] op_sel_hi:[1,0]\n"
"Qk 160,160,161\nQk 161,162,163\nQk 162,164,165\nQk 163,166,167\nv_accvgpr_write_b32 a40, v160\nv_accvgpr_write_b32 a41, v161\nv_accvgpr_write_b32 a42, v162\nv_accvgpr_write_b32 a43, v163\n"
"v_pk_mul_f32 v[168:169], v[168:169], s[72:73] op_sel_hi:[1,0]\nv_pk_mul_f32 v[170:171], v[170:171], s[72:73] op_sel_hi:[1,0]\nv_pk_mul_f32 v[172:173], v[172:173], s[72:73] op_sel_hi:[1,0]\nv_pk_mul_f32 v[174:175], v[174:175], s[72:73] op_sel_hi:[1,0]\nQk 168,168,169\nQk 169,170,171\nQk 170,172,173\nQk 171,174,175\n"
"v_accvgpr_write_b32 a44, v168\nv_accvgpr_write_b32 a45, v169\nv_accvgpr_write_b32 a46, v170\nv_accvgpr_write_b32 a47, v171\nv_pk_mul_f32 v[176:177], v[176:177], s[72:73] op_sel_hi:[1,0]\nv_pk_mul_f32 v[178:179], v[178:179], s[72:73] op_sel_hi:[1,0]\nv_pk_mul_f32 v[180:181], v[180:181], s[72:73] op_sel_hi:[1,0]\nv_pk_mul_f32 v[182:183], v[182:183], s[72:73] op_sel_hi:[1,0]\n"
"Qk 176,176,177\nQk 177,178,179\nQk 178,180,181\nQk 179,182,183\nv_accvgpr_write_b32 a48, v176\nv_accvgpr_write_b32 a49, v177\nv_accvgpr_write_b32 a50, v178\nv_accvgpr_write_b32 a51, v179\n"
"v_pk_mul_f32 v[184:185], v[184:185], s[72:73] op_sel_hi:[1,0]\nv_pk_mul_f32 v[186:187], v[186:187], s[72:73] op_sel_hi:[1,0]\nv_pk_mul_f32 v[188:189], v[188:189], s[72:73] op_sel_hi:[1,0]\nv_pk_mul_f32 v[190:191], v[190:191], s[72:73] op_sel_hi:[1,0]\nQk 184,184,185\nQk 185,186,187\nQk 186,188,189\nQk 187,190,191\n"
"v_accvgpr_write_b32 a52, v184\nv_accvgpr_write_b32 a53, v185\nv_accvgpr_write_b32 a54, v186\nv_accvgpr_write_b32 a55, v187\nv_pk_mul_f32 v[192:193], v[192:193], s[72:73] op_sel_hi:[1,0]\nv_pk_mul_f32 v[194:195], v[194:195], s[72:73] op_sel_hi:[1,0]\nv_pk_mul_f32 v[196:197], v[196:197], s[72:73] op_sel_hi:[1,0]\nv_pk_mul_f32 v[198:199], v[198:199], s[72:73] op_sel_hi:[1,0]\n"
"Qk 192,192,193\nQk 193,194,195\nQk 194,196,197\nQk 195,198,199\nv_accvgpr_write_b32 a56, v192\nv_accvgpr_write_b32 a57, v193\nv_accvgpr_write_b32 a58, v194\nv_accvgpr_write_b32 a59, v195\n"
"v_pk_mul_f32 v[200:201], v[200:201], s[72:73] op_sel_hi:[1,0]\nv_pk_mul_f32 v[202:203], v[202:203], s[72:73] op_sel_hi:[1,0]\nv_pk_mul_f32 v[204:205], v[204:205], s[72:73] op_sel_hi:[1,0]\nv_pk_mul_f32 v[206:207], v[206:207], s[72:73] op_sel_hi:[1,0]\nQk 200,200,201\nQk 201,202,203\nQk 202,204,205\nQk 203,206,207\n"
"v_accvgpr_write_b32 a60, v200\nv_accvgpr_write_b32 a61, v201\nv_accvgpr_write_b32 a62, v202\nv_accvgpr_write_b32 a63, v203\nv_pk_mul_f32 v[208:209], v[208:209], s[72:73] op_sel_hi:[1,0]\nv_pk_mul_f32 v[210:211], v[210:211], s[72:73] op_sel_hi:[1,0]\nv_pk_mul_f32 v[212:213], v[212:213], s[72:73] op_sel_hi:[1,0]\nv_pk_mul_f32 v[214:215], v[214:215], s[72:73] op_sel_hi:[1,0]\n"
"Qk 208,208,209\nQk 209,210,211\nQk 210,212,213\nQk 211,214,215\nv_accvgpr_write_b32 a64, v208\nv_accvgpr_write_b32 a65, v209\nv_accvgpr_write_b32 a66, v210\nv_accvgpr_write_b32 a67, v211\n"
"v_pk_mul_f32 v[216:217], v[216:217], s[72:73] op_sel_hi:[1,0]\nv_pk_mul_f32 v[218:219], v[218:219], s[72:73] op_sel_hi:[1,0]\nv_pk_mul_f32 v[220:221], v[220:221], s[72:73] op_sel_hi:[1,0]\nv_pk_mul_f32 v[222:223], v[222:223], s[72:73] op_sel_hi:[1,0]\nQk 216,216,217\nQk 217,218,219\nQk 218,220,221\nQk 219,222,223\n"
"v_accvgpr_write_b32 a68, v216\nv_accvgpr_write_b32 a69, v217\nv_accvgpr_write_b32 a70, v218\nv_accvgpr_write_b32 a71, v219\nv_pk_mul_f32 v[224:225], v[224:225], s[72:73] op_sel_hi:[1,0]\nv_pk_mul_f32 v[226:227], v[226:227], s[72:73] op_sel_hi:[1,0]\nv_pk_mul_f32 v[228:229], v[228:229], s[72:73] op_sel_hi:[1,0]\nv_pk_mul_f32 v[230:231], v[230:231], s[72:73] op_sel_hi:[1,0]\n"
"Qk 224,224,225\nQk 225,226,227\nQk 226,228,229\nQk 227,230,231\nv_accvgpr_write_b32 a72, v224\nv_accvgpr_write_b32 a73, v225\nv_accvgpr_write_b32 a74, v226\nv_accvgpr_write_b32 a75, v227\n"
"v_pk_mul_f32 v[232:233], v[232:233], s[72:73] op_sel_hi:[1,0]\nv_pk_mul_f32 v[234:235], v[234:235], s[72:73] op_sel_hi:[1,0]\nv_pk_mul_f32 v[236:237], v[236:237], s[72:73] op_sel_hi:[1,0]\nv_pk_mul_f32 v[238:239], v[238:239], s[72:73] op_sel_hi:[1,0]\n.Lpg4:\ns_cbranch_execz .Ltr9\nQk 232,232,233\nQk 233,234,235\n"
"Qk 234,236,237\nQk 235,238,239\nv_accvgpr_write_b32 a76, v232\nv_accvgpr_write_b32 a77, v233\nv_accvgpr_write_b32 a78, v234\nv_accvgpr_write_b32 a79, v235\nv_pk_mul_f32 v[240:241], v[240:241], s[72:73] op_sel_hi:[1,0]\nv_pk_mul_f32 v[242:243], v[242:243], s[72:73] op_sel_hi:[1,0]\n"
"v_pk_mul_f32 v[244:245], v[244:245], s[72:73] op_sel_hi:[1,0]\nv_pk_mul_f32 v[246:247], v[246:247], s[72:73] op_sel_hi:[1,0]\nQk 240,240,241\nQk 241,242,243\nQk 242,244,245\nQk 243,246,247\nv_accvgpr_write_b32 a80, v240\nv_accvgpr_write_b32 a81, v241\n"
"v_accvgpr_write_b32 a82, v242\nv_accvgpr_write_b32 a83, v243\nv_pk_mul_f32 v[248:249], v[248:249], s[72:73] op_sel_hi:[1,0]\nv_pk_mul_f32 v[250:251], v[250:251], s[72:73] op_sel_hi:[1,0]\nv_pk_mul_f32 v[252:253], v[252:253], s[72:73] op_sel_hi:[1,0]\nv_pk_mul_f32 v[254:255], v[254:255], s[72:73] op_sel_hi:[1,0]\nQk 248,248,249\nQk 249,250,251\n"
"Qk 250,252,253\nQk 251,254,255\nv_accvgpr_write_b32 a84, v248\nv_accvgpr_write_b32 a85, v249\nv_accvgpr_write_b32 a86, v250\nv_accvgpr_write_b32 a87, v251\nv_accvgpr_read_b32 v0, a208\nv_accvgpr_read_b32 v1, a209\n"
"v_accvgpr_read_b32 v2, a210\nv_accvgpr_read_b32 v3, a211\nv_accvgpr_read_b32 v4, a212\nv_accvgpr_read_b32 v5, a213\nv_accvgpr_read_b32 v6, a214\nv_accvgpr_read_b32 v7, a215\nv_pk_mul_f32 v[0:1], v[0:1], s[72:73] op_sel_hi:[1,0]\nv_pk_mul_f32 v[2:3], v[2:3], s[72:73] op_sel_hi:[1,0]\n"
"v_pk_mul_f32 v[4:5], v[4:5], s[72:73] op_sel_hi:[1,0]\nv_pk_mul_f32 v[6:7], v[6:7], s[72:73] op_sel_hi:[1,0]\nQk 0,0,1\nQk 1,2,3\nQk 2,4,5\nQk 3,6,7\nv_accvgpr_write_b32 a88, v0\nv_accvgpr_write_b32 a89, v1\n"
"v_accvgpr_write_b32 a90, v2\nv_accvgpr_write_b32 a91, v3\nv_accvgpr_read_b32 v8, a216\nv_accvgpr_read_b32 v9, a217\nv_accvgpr_read_b32 v10, a218\nv_accvgpr_read_b32 v11, a219\nv_accvgpr_read_b32 v12, a220\nv_accvgpr_read_b32 v13, a221\n"
"v_accvgpr_read_b32 v14, a222\nv_accvgpr_read_b32 v15, a223\nv_pk_mul_f32 v[8:9], v[8:9], s[72:73] op_sel_hi:[1,0]\nv_pk_mul_f32 v[10:11], v[10:11], s[72:73] op_sel_hi:[1,0]\nv_pk_mul_f32 v[12:13], v[12:13], s[72:73] op_sel_hi:[1,0]\nv_pk_mul_f32 v[14:15], v[14:15], s[72:73] op_sel_hi:[1,0]\nQk 8,8,9\nQk 9,10,11\n"
"Qk 10,12,13\nQk 11,14,15\nv_accvgpr_write_b32 a92, v8\nv_accvgpr_write_b32 a93, v9\nv_accvgpr_write_b32 a94, v10\nv_accvgpr_write_b32 a95, v11\nv_accvgpr_read_b32 v0, a224\nv_accvgpr_read_b32 v1, a225\n"
"v_accvgpr_read_b32 v2, a226\nv_accvgpr_read_b32 v3, a227\nv_accvgpr_read_b32 v4, a228\nv_accvgpr_read_b32 v5, a229\nv_accvgpr_read_b32 v6, a230\nv_accvgpr_read_b32 v7, a231\nv_pk_mul_f32 v[0:1], v[0:1], s[72:73] op_sel_hi:[1,0]\nv_pk_mul_f32 v[2:3], v[2:3], s[72:73] op_sel_hi:[1,0]\n"
"v_pk_mul_f32 v[4:5], v[4:5], s[72:73] op_sel_hi:[1,0]\nv_pk_mul_f32 v[6:7], v[6:7], s[72:73] op_sel_hi:[1,0]\nQk 0,0,1\nQk 1,2,3\nQk 2,4,5\nQk 3,6,7\nv_accvgpr_write_b32 a96, v0\nv_accvgpr_write_b32 a97, v1\n"
"v_accvgpr_write_b32 a98, v2\nv_accvgpr_write_b32 a99, v3\nv_accvgpr_read_b32 v8, a232\nv_accvgpr_read_b32 v9, a233\nv_accvgpr_read_b32 v10, a234\nv_accvgpr_read_b32 v11, a235\nv_accvgpr_read_b32 v12, a236\nv_accvgpr_read_b32 v13, a237\n"
"v_accvgpr_read_b32 v14, a238\nv_accvgpr_read_b32 v15, a239\nv_pk_mul_f32 v[8:9], v[8:9], s[72:73] op_sel_hi:[1,0]\nv_pk_mul_f32 v[10:11], v[10:11], s[72:73] op_sel_hi:[1,0]\nv_pk_mul_f32 v[12:13], v[12:13], s[72:73] op_sel_hi:[1,0]\nv_pk_mul_f32 v[14:15], v[14:15], s[72:73] op_sel_hi:[1,0]\nQk 8,8,9\nQk 9,10,11\n"
"Qk 10,12,13\nQk 11,14,15\nv_accvgpr_write_b32 a100, v8\nv_accvgpr_write_b32 a101, v9\nv_accvgpr_write_b32 a102, v10\nv_accvgpr_write_b32 a103, v11\nv_accvgpr_read_b32 v0, a240\nv_accvgpr_read_b32 v1, a241\n"
"v_accvgpr_read_b32 v2, a242\nv_accvgpr_read_b32 v3, a243\nv_accvgpr_read_b32 v4, a244\nv_accvgpr_read_b32 v5, a245\nv_accvgpr_read_b32 v6, a246\nv_accvgpr_read_b32 v7, a247\nv_pk_mul_f32 v[0:1], v[0:1], s[72:73] op_sel_hi:[1,0]\nv_pk_mul_f32 v[2:3], v[2:3], s[72:73] op_sel_hi:[1,0]\n"
"v_pk_mul_f32 v[4:5], v[4:5], s[72:73] op_sel_hi:[1,0]\nv_pk_mul_f32 v[6:7], v[6:7], s[72:73] op_sel_hi:[1,0]\nQk 0,0,1\nQk 1,2,3\nQk 2,4,5\nQk 3,6,7\nv_accvgpr_write_b32 a104, v0\nv_accvgpr_write_b32 a105, v1\n"
"v_accvgpr_write_b32 a106, v2\nv_accvgpr_write_b32 a107, v3\nv_accvgpr_read_b32 v8, a248\nv_accvgpr_read_b32 v9, a249\nv_accvgpr_read_b32 v10, a250\nv_accvgpr_read_b32 v11, a251\nv_accvgpr_read_b32 v12, a252\nv_accvgpr_read_b32 v13, a253\n"
"v_accvgpr_read_b32 v14, a254\nv_accvgpr_read_b32 v15, a255\nv_pk_mul_f32 v[8:9], v[8:9], s[72:73] op_sel_hi:[1,0]\nv_pk_mul_f32 v[10:11], v[10:11], s[72:73] op_sel_hi:[1,0]\nv_pk_mul_f32 v[12:13], v[12:13], s[72:73] op_sel_hi:[1,0]\nv_pk_mul_f32 v[14:15], v[14:15], s[72:73] op_sel_hi:[1,0]\nQk 8,8,9\nQk 9,10,11\n"
"Qk 10,12,13\nQk 11,14,15\nv_accvgpr_write_b32 a108, v8\nv_accvgpr_write_b32 a109, v9\nv_accvgpr_write_b32 a110, v10\nv_accvgpr_write_b32 a111, v11\nv_mov_b32 v66, 0x3f803f80\nv_accvgpr_write_b32 a200, 0\n"
"v_accvgpr_write_b32 a201, 0\nv_accvgpr_write_b32 a202, 0\nv_accvgpr_write_b32 a203, 0\ns_mov_b32 vcc_lo, 0x33333333\ns_mov_b32 vcc_hi, 0xcccccccc\nv_cndmask_b32 v66, 0, v66, vcc\nv_accvgpr_write_b32 a204, v66\nv_accvgpr_write_b32 a205, v66\n"
"v_accvgpr_write_b32 a206, v66\nv_accvgpr_write_b32 a207, v66\ns_add_u32 s87, s87, 2048\ns_waitcnt lgkmcnt(8)\nQzvaa 0,112,0\nQmvaa 0,116,4\nQmvaa 0,120,8\nQmvaa 0,124,12\n"
"Qzvaa 4,140,12\nQmvaa 4,136,8\nQmvaa 4,132,4\nQmvaa 4,128,0\ns_nop 3\nv_max3_f32 v66, v0, v1, v2\ns_nop 2\nv_max3_f32 v66, v66, v3, v4\n"
"v_max3_f32 v66, v66, v5, v6\nv_max_f32 v66, v66, v7\nQzvaa 8,112,16\nv_mov_b32 v67, v66\nQmvaa 8,116,20\ns_nop 0\nv_permlane32_swap_b32 v66, v67\nv_max_f32 v66, v66, v67\n"
"Qmvaa 8,120,24\nv_mov_b32 v67, v66\nQmvaa 8,124,28\ns_nop 0\nv_permlane16_swap_b32 v66, v67\nv_max_f32 v66, v66, v67\nQzvaa 12,140,28\ns_nop 0\n"
"v_max_f32_dpp v66, v66, v66 row_ror:8 row_mask:0xf bank_mask:0xf\ns_nop 1\nv_max_f32_dpp v66, v66, v66 row_ror:4 row_mask:0xf bank_mask:0xf\nQmvaa 12,136,24\ns_nop 0\nv_max_f32_dpp v66, v66, v66 row_ror:2 row_mask:0xf bank_mask:0xf\ns_nop 1\nv_max_f32_dpp v66, v66, v66 row_ror:1 row_mask:0xf bank_mask:0xf\n"
"Qmvaa 12,132,20\nv_sub_f32 v56, 0xc1980000, v66\nv_mov_b32 v57, v56\nQmvaa 12,128,16\nv_mov_b32 v58, v56\nv_mov_b32 v59, v56\nQa 0,0,56\nQa 1,1,56\n"
"Qa 2,2,56\nQa 3,3,56\nQe 0\nQe 1\nQe 2\nQe 3\nQk 16,0,1\nQk 17,2,3\n"
"Qa 4,4,56\nQa 5,5,56\nQa 6,6,56\nQa 7,7,56\nQcvaa 0,112,32,56\nQe 4\nQmvaa 0,116,36\nQe 5\n"
"Qmvaa 0,120,40\nQe 6\nQe 7\nQmvaa 0,124,44\nQk 18,4,5\nQk 19,6,7\nQcvaa 4,140,44,56\nQa 8,8,56\n"
"Qa 9,9,56\nQmvaa 4,136,40\nQa 10,10,56\nQa 11,11,56\nQmvaa 4,132,36\nQe 8\nQmvaa 4,128,32\ns_waitcnt lgkmcnt(0)\n"
"Qa 12,12,56\nQzvva 96,16,148\nQe 9\nQzvva 100,16,156\nQe 10\nQzvva 104,16,164\nQe 11\nQzvva 108,16,172\n"
"Qk 20,8,9\nQk 21,10,11\nQcvaa 8,112,48,56\nQa 13,13,56\nQa 14,14,56\nQmvaa 8,116,52\nQa 15,15,56\nQmvaa 8,120,56\n"
"Qe 12\nQe 13\nQe 14\nQe 15\nQmvaa 8,124,60\nQk 22,12,13\nQk 23,14,15\nQcvaa 12,140,60,56\n"
"Qe 0\nQmvaa 12,136,56\nQe 1\nQmvaa 12,132,52\nQe 2\nQmvaa 12,128,48\nQe 3\nv_smfmac_f32_16x16x64_bf16 a[176:179], a[204:207], v[16:23], v77\n"
"Qk 24,0,1\ns_add_u32 m0, s61, 99072\nQzvva 128,20,148\nbuffer_load_dwordx4 v69, s[40:43], s90 offen nt lds\nQzvva 132,20,156\nQk 25,2,3\nQzvva 136,20,164\nQe 4\n"
"buffer_load_dwordx4 v69, s[40:43], s60 offen offset:1024 nt lds\nQzvva 140,20,172\nQe 5\nQcvaa 0,112,64,56\nQe 6\nQmvaa 0,116,68\nQe 7\nQmvaa 0,120,72\n"
"Qk 26,4,5\nQk 27,6,7\nQmvaa 0,124,76\nbuffer_load_dwordx4 v69, s[40:43], s90 offen offset:2048 nt lds\nQcvaa 4,140,76,56\nQe 8\nQmvaa 4,136,72\nQe 9\n"
"Qmvaa 4,132,68\nQe 10\nQmvaa 4,128,64\nQe 11\nbuffer_load_dwordx4 v69, s[40:43], s60 offen offset:3072 nt lds\nQzvva 160,24,148\nQk 28,8,9\nQk 29,10,11\n"
"Qzvva 164,24,156\nQe 12\ns_add_u32 m0, s61, 107264\nQzvva 168,24,164\nbuffer_load_dwordx4 v72, s[44:47], s90 offen nt lds\nQzvva 172,24,172\nQe 13\nQcvaa 8,112,80,56\n"
"Qe 14\nQmvaa 8,116,84\nQe 15\nQmvaa 8,120,88\nbuffer_load_dwordx4 v72, s[44:47], s60 offen offset:1024 nt lds\nQmvaa 8,124,92\nQk 30,12,13\nQk 31,14,15\n"
"Qcvaa 12,140,92,56\nQe 0\nQmvaa 12,136,88\nQe 1\nQmvaa 12,132,84\nQe 2\nQmvaa 12,128,80\nbuffer_load_dwordx4 v72, s[44:47], s90 offen offset:2048 nt lds\n"
"v_smfmac_f32_16x16x64_bf16 a[180:183], a[204:207], v[24:31], v77\nQk 32,0,1\nQzvva 192,28,148\nQe 3\ns_waitcnt vmcnt(15)\ns_barrier\nQzvva 196,28,156\nbuffer_load_dwordx4 v72, s[44:47], s60 offen offset:3072 nt lds\n"
"Qzvva 200,28,164\nQk 33,2,3\ns_add_u32 s60, s60, 8192\ns_and_b32 s68, s60, 0x40000\ns_mul_i32 s68, s68, 7\ns_add_u32 s60, s60, s68\ns_add_u32 s90, s60, 896\nQzvva 204,28,172\n"
"Qe 4\nQcvaa 0,112,96,56\nQda 112,68,33024\nQmvaa 0,116,100\nQda 116,68,33536\nQmvaa 0,120,104\nQda 120,68,34048\nQe 5\n"
"Qe 6\nQe 7\nQmvaa 0,124,108\nQda 124,68,34560\nQk 34,4,5\nQk 35,6,7\nQcvaa 4,140,108,56\nQda 140,68,36608\n"
"Qmvaa 4,136,104\nQda 136,68,36096\nQmvaa 4,132,100\nQda 132,68,35584\nQmvaa 4,128,96\nQda 128,68,35072\nQzvva 224,32,148\ns_memrealtime s[74:75]\n"
"Qzvva 228,32,156\nQe 8\nQzvva 232,32,164\nQe 9\nQe 10\nQe 11\nQzvva 236,32,172\ns_waitcnt lgkmcnt(0)\n"
"Qk 36,8,9\nQk 37,10,11\nQcvaa 8,112,0,56\nQe 12\nQmvaa 8,116,4\nv_add_u32 v68, 66048, v68\nQmvaa 8,120,8\nQe 13\n"
"Qe 14\nQe 15\nQmvaa 8,124,12\nQk 38,12,13\nQk 39,14,15\nQcvaa 12,140,12,56\nQe 0\nQmvaa 12,136,8\n"
"Qe 1\nQmvaa 12,132,4\nQe 2\nQmvaa 12,128,0\nQe 3\nv_smfmac_f32_16x16x64_bf16 a[184:187], a[204:207], v[32:39], v77\nQk 40,0,1\ns_waitcnt lgkmcnt(0)\n"
"Qzava 208,36,148\ns_mov_b32 s73, 140\nQk 41,2,3\ns_add_u32 s77, s74, s73\nQzava 212,36,156\ns_sub_u32 s73, s73, 4\ns_max_u32 s73, s73, 120\nQzava 216,36,164\n"
"Qe 4\nQzava 220,36,172\nQe 8\nQcvaa 0,112,16,56\nQe 9\nQmvaa 0,116,20\nQe 10\nQmvaa 0,120,24\n"
"Qe 11\nQe 5\nQe 6\nQe 7\nQmvaa 0,124,28\nQk 44,8,9\nQk 45,10,11\nQk 42,4,5\n"
"Qk 43,6,7\nQcvaa 4,140,28,56\nQe 12\nQmvaa 4,136,24\nQe 13\nQmvaa 4,132,20\nQe 14\nQmvaa 4,128,16\n"
"Qe 15\nQta 144,70,0\nQta 146,70,2048\nQzava 240,40,148\nQta 148,70,33024\nQta 150,70,35072\nQta 152,70,256\nQta 154,70,2304\n"
"Qzava 244,40,156\nQta 156,70,33280\nQta 158,70,35328\nQta 160,70,512\nQta 162,70,2560\nQzava 248,40,164\nQta 164,70,33536\nQta 166,70,35584\n"
"Qta 168,70,768\nQta 170,70,2816\nQzava 252,40,172\nQta 172,70,33792\nQta 174,70,35840\nQcvaa 8,112,32,56\nv_add_u32 v75, 1, v75\nQk 46,12,13\n"
"Qmvaa 8,116,36\ns_waitcnt vmcnt(8)\nds_write_b32 v73, v75\nQmvaa 8,120,40\nQk 47,14,15\nQmvaa 8,124,44\nQe 0\nQcvaa 12,140,44,56\n"
"Qe 1\nQmvaa 12,136,40\nQe 2\nQmvaa 12,132,36\nQe 3\nQmvaa 12,128,32\ns_waitcnt lgkmcnt(1)\nQk 48,0,1\n"
"v_smfmac_f32_16x16x64_bf16 a[188:191], a[204:207], v[40:47], v77\nQk 49,2,3\nQzvva 80,16,144\nQe 4\nQmvva 80,44,148\nQe 5\nQzvva 84,16,152\nQe 6\n"
"Qmvva 84,44,156\nQe 7\nQzvva 88,16,160\nQk 50,4,5\nQk 51,6,7\nQmvva 88,44,164\nQe 8\nQzvva 92,16,168\n"
"Qe 9\nQmvva 92,44,172\nQe 10\nQcvaa 0,112,48,56\nQe 11\nQmvaa 0,116,52\nds_read_b32 v66, v74\nQk 52,8,9\n"
"Qmvaa 0,120,56\nQk 53,10,11\nQmvaa 0,124,60\nQe 12\nQcvaa 4,140,60,56\nQe 13\nQmvaa 4,136,56\nQe 14\n"
"Qmvaa 4,132,52\ns_waitcnt lgkmcnt(0)\nv_cmp_gt_u32 vcc, v75, v66\nQmvaa 4,128,48\ns_cbranch_vccnz .Lsa1\n.Lra1:\nQk 54,12,13\nQzvva 112,20,144\n"
"s_add_u32 m0, s61, 0\nQmvva 112,48,148\nbuffer_load_dwordx4 v69, s[40:43], s90 offen nt lds\nQzvva 116,20,152\nQe 15\nQmvva 116,48,156\nQk 55,14,15\nQzvva 120,20,160\n"
"Qe 0\nQmvva 120,48,164\nQe 1\nQzvva 124,20,168\nbuffer_load_dwordx4 v69, s[40:43], s60 offen offset:1024 nt lds\nQmvva 124,48,172\nQe 2\nQcvaa 8,112,64,56\n"
"Qe 3\nQmvaa 8,116,68\nQk 20,0,1\nQk 21,2,3\nQmvaa 8,120,72\nQe 4\nQmvaa 8,124,76\nbuffer_load_dwordx4 v69, s[40:43], s90 offen offset:2048 nt lds\n"
"Qcvaa 12,140,76,56\nQe 5\nQmvaa 12,136,72\nQe 6\nQmvaa 12,132,68\nQe 7\nQmvaa 12,128,64\nQk 22,4,5\n"
"Qk 23,6,7\nv_smfmac_f32_16x16x64_bf16 a[192:195], a[204:207], v[48:55], v77\nQzvva 144,24,144\nbuffer_load_dwordx4 v69, s[40:43], s60 offen offset:3072 nt lds\nQmvva 144,52,148\nQzvva 148,24,152\nQe 8\nQmvva 148,52,156\n"
"s_add_u32 m0, s61, 8192\nQzvva 152,24,160\nbuffer_load_dwordx4 v72, s[44:47], s90 offen nt lds\nQmvva 152,52,164\nQe 9\nQzvva 156,24,168\nQe 10\nQmvva 156,52,172\n"
"Qe 11\nQcvaa 0,112,80,56\nQk 24,8,9\nQk 25,10,11\nQmvaa 0,116,84\nbuffer_load_dwordx4 v72, s[44:47], s60 offen offset:1024 nt lds\nQmvaa 0,120,88\nQe 12\n"
"Qmvaa 0,124,92\nQe 13\nQcvaa 4,140,92,56\nQe 14\nQmvaa 4,136,88\nQe 15\nQmvaa 4,132,84\nbuffer_load_dwordx4 v72, s[44:47], s90 offen offset:2048 nt lds\n"
"Qmvaa 4,128,80\nQk 26,12,13\nQk 27,14,15\nQzvva 176,28,144\nQmvva 176,20,148\nQzvva 180,28,152\nQmvva 180,20,156\nbuffer_load_dwordx4 v72, s[44:47], s60 offen offset:3072 nt lds\n"
"Qzvva 184,28,160\n.Lpg5:\ns_cbranch_execz .Ltr9\nQe 0\nQmvva 184,20,164\nQe 1\nQzvva 188,28,168\ns_add_u32 s60, s60, 8192\n"
"s_add_u32 s90, s60, 896\nQmvva 188,20,172\nQe 2\nQcvaa 8,112,96,56\nQda 112,68,0\nQk 28,0,1\nQmvaa 8,116,100\nQda 116,68,512\n"
"Qmvaa 8,120,104\nQda 120,68,1024\nQmvaa 8,124,108\nQda 124,68,1536\nQcvaa 12,140,108,56\nQda 140,68,3584\nQmvaa 12,136,104\nQda 136,68,3072\n"
"Qmvaa 12,132,100\nQda 132,68,2560\nQmvaa 12,128,96\nQda 128,68,2048\nv_smfmac_f32_16x16x64_bf16 a[196:199], a[204:207], v[20:27], v77\nQzvva 208,32,144\ns_memrealtime s[74:75]\nQmvva 208,24,148\n"
"Qe 3\nQzvva 212,32,152\nQk 29,2,3\nQmvva 212,24,156\nQe 4\nQzvva 216,32,160\nQe 5\nQmvva 216,24,164\n"
"Qe 6\nQzvva 220,32,168\nQe 7\nQmvva 220,24,172\ns_waitcnt lgkmcnt(0)\nQk 30,4,5\nQcvaa 0,112,0,56\nQk 31,6,7\n"
"Qmvaa 0,116,4\nQe 8\nQmvaa 0,120,8\nQe 9\nQmvaa 0,124,12\nQe 10\nQcvaa 4,140,12,56\nQe 11\n"
"Qmvaa 4,136,8\nQk 32,8,9\nQk 33,10,11\nQmvaa 4,132,4\nQe 12\nQmvaa 4,128,0\nQe 13\nQzvva 240,36,144\n"
"Qe 14\nQmvva 240,28,148\ns_waitcnt lgkmcnt(0)\ns_sub_u32 s75, s77, s74\nQzvva 244,36,152\ns_cmp_lt_u32 s75, 256\ns_cbranch_scc1 .Lsp1\n.Lrp1:\n"
"Qmvva 244,28,156\ns_add_u32 s77, s74, s73\nQk 34,12,13\nQzvva 248,36,160\ns_sub_u32 s73, s73, 4\nQmvva 248,28,164\ns_max_u32 s73, s73, 120\nQzvva 252,36,168\n"
"Qe 15\nQmvva 252,28,172\nQk 35,14,15\nQcvaa 8,112,16,56\nQe 0\nQmvaa 8,116,20\nQe 1\nQmvaa 8,120,24\n"
"Qe 2\nQmvaa 8,124,28\nQe 3\nQcvaa 12,140,28,56\nQk 36,0,1\nQk 37,2,3\nQmvaa 12,136,24\nQe 4\n"
"Qmvaa 12,132,20\nQe 5\nQmvaa 12,128,16\nQe 6\nv_smfmac_f32_16x16x64_bf16 a[200:203], a[204:207], v[28:35], v77\nQk 38,4,5\nQzava 224,40,144\nQta 144,70,34048\n"
"Qta 146,70,36096\nQmava 224,32,148\nQta 148,71,1024\nQta 150,71,3072\nQzava 228,40,152\nQta 152,70,34304\nQta 154,70,36352\nQmava 228,32,156\n"
"Qta 156,71,1280\nQta 158,71,3328\nQzava 232,40,160\nQta 160,70,34560\nQta 162,70,36608\nQmava 232,32,164\nQta 164,71,1536\nQta 166,71,3584\n"
"Qzava 236,40,168\nQta 168,70,34816\nQta 170,70,36864\nQmava 236,32,172\nQta 172,71,1792\nQta 174,71,3840\ns_lshl_b32 s66, s67, 3\n.Lloop:\n"
"s_bitcmp1_b32 s99, s66\ns_cbranch_scc1 .Lhook\n.Lhookret:\nQcvaa 0,112,32,56\nv_add_u32 v75, 1, v75\nQmvaa 0,116,36\ns_waitcnt vmcnt(8)\nds_write_b32 v73, v75\n"
"Qmvaa 0,120,40\nQe 7\nQmvaa 0,124,44\nQk 39,6,7\nQcvaa 4,140,44,56\nQe 8\nQmvaa 4,136,40\nQe 9\n"
"Qmvaa 4,132,36\nQe 10\nQmvaa 4,128,32\ns_waitcnt lgkmcnt(1)\nQk 40,8,9\nQmvva 96,44,144\nQe 11\nQmvva 96,36,148\n"
"Qk 41,10,11\nQmvva 100,44,152\nQe 12\nQmvva 100,36,156\nQe 13\nQmvva 104,44,160\nQe 14\nQmvva 104,36,164\n"
"Qe 15\nQmvva 108,44,168\nQk 42,12,13\nQk 43,14,15\nQmvva 108,36,172\nQe 0\nQcvaa 8,112,48,56\nQe 1\n"
"Qmvaa 8,116,52\nQe 2\nQmvaa 8,120,56\nds_read_b32 v66, v74\nQk 44,0,1\nQmvaa 8,124,60\nQe 3\nQcvaa 12,140,60,56\n"
"Qk 45,2,3\nQmvaa 12,136,56\nQe 4\nQmvaa 12,132,52\nQe 5\nQmvaa 12,128,48\ns_waitcnt lgkmcnt(0)\nv_cmp_gt_u32 vcc, v75, v66\n"
"v_smfmac_f32_16x16x64_bf16 a[176:179], a[204:207], v[36:43], v77\ns_cbranch_vccnz .Lsa2\n.Lra2:\nQmvva 128,48,144\ns_add_u32 m0, s61, 33024\nQk 46,4,5\nQmvva 128,40,148\nbuffer_load_dwordx4 v69, s[40:43], s90 offen nt lds\n"
"Qmvva 132,48,152\nQe 6\nQmvva 132,40,156\nQe 7\nQmvva 136,48,160\nQk 47,6,7\nQmvva 136,40,164\nQe 8\n"
"Qmvva 140,48,168\nbuffer_load_dwordx4 v69, s[40:43], s60 offen offset:1024 nt lds\nQmvva 140,40,172\nQe 9\nQcvaa 0,112,64,56\nQe 10\nQmvaa 0,116,68\nQe 11\n"
"Qmvaa 0,120,72\nQk 48,8,9\nQk 49,10,11\nQmvaa 0,124,76\nbuffer_load_dwordx4 v69, s[40:43], s90 offen offset:2048 nt lds\nQcvaa 4,140,76,56\nQe 12\nQmvaa 4,136,72\n"
"Qe 13\nQmvaa 4,132,68\nQe 14\nQmvaa 4,128,64\nQe 15\nQmvva 160,52,144\nbuffer_load_dwordx4 v69, s[40:43], s60 offen offset:3072 nt lds\nQmvva 160,44,148\n"
"Qk 50,12,13\nQk 51,14,15\nQmvva 164,52,152\nQmvva 164,44,156\nQe 0\nQmvva 168,52,160\ns_add_u32 m0, s61, 41216\nQmvva 168,44,164\n"
"buffer_load_dwordx4 v72, s[44:47], s90 offen nt lds\nQmvva 172,52,168\nQe 1\nQmvva 172,44,172\nQe 2\nQcvaa 8,112,80,56\nQe 3\nQmvaa 8,116,84\n"
"Qk 16,0,1\nQk 17,2,3\nQmvaa 8,120,88\nbuffer_load_dwordx4 v72, s[44:47], s60 offen offset:1024 nt lds\nQmvaa 8,124,92\nQe 4\nQcvaa 12,140,92,56\nQe 5\n"
"Qmvaa 12,136,88\nQe 6\nQmvaa 12,132,84\nQe 7\nQmvaa 12,128,80\nbuffer_load_dwordx4 v72, s[44:47], s90 offen offset:2048 nt lds\nv_smfmac_f32_16x16x64_bf16 a[180:183], a[204:207], v[44:51], v77\nQk 18,4,5\n"
"Qmvva 192,20,144\nQk 19,6,7\nQmvva 192,48,148\nQmvva 196,20,152\nQe 8\nQmvva 196,48,156\nbuffer_load_dwordx4 v72, s[44:47], s60 offen offset:3072 nt lds\nQmvva 200,20,160\n"
"Qe 9\nQmvva 200,48,164\nQe 10\nQmvva 204,20,168\ns_add_u32 s60, s60, 8192\ns_add_u32 s90, s60, 896\nQmvva 204,48,172\nQe 11\n"
"Qcvaa 0,112,96,56\nQda 112,68,33024\nQk 20,8,9\nQmvaa 0,116,100\nQda 116,68,33536\nQk 21,10,11\nQmvaa 0,120,104\nQda 120,68,34048\n"
"Qmvaa 0,124,108\nQda 124,68,34560\nQcvaa 4,140,108,56\nQda 140,68,36608\nQmvaa 4,136,104\nQda 136,68,36096\nQmvaa 4,132,100\nQda 132,68,35584\n"
"Qmvaa 4,128,96\nQda 128,68,35072\nQmvva 224,24,144\nQe 12\nQmvva 224,16,148\ns_memrealtime s[74:75]\nQmvva 228,24,152\nQe 13\n"
"Qmvva 228,16,156\nQe 14\nQmvva 232,24,160\nQe 15\nQmvva 232,16,164\nQk 22,12,13\nQk 23,14,15\nQmvva 236,24,168\n"
"Qe 0\nQmvva 236,16,172\ns_waitcnt lgkmcnt(0)\nQcvaa 8,112,0,56\nQe 1\nQmvaa 8,116,4\nv_subrev_u32 v68, 66048, v68\nQk 24,0,1\n"
"Qmvaa 8,120,8\nQe 2\nQmvaa 8,124,12\nQe 3\nQcvaa 12,140,12,56\nQk 25,2,3\nQmvaa 12,136,8\nQe 4\n"
"Qmvaa 12,132,4\nQe 5\nQmvaa 12,128,0\nQe 6\nv_smfmac_f32_16x16x64_bf16 a[184:187], a[204:207], v[16:23], v77\nQk 26,4,5\nQmava 208,28,144\ns_waitcnt lgkmcnt(0)\n"
"s_sub_u32 s75, s77, s74\nQmava 208,20,148\ns_cmp_lt_u32 s75, 256\ns_cbranch_scc1 .Lsp2\n.Lrp2:\nQmava 212,28,152\ns_add_u32 s77, s74, s73\nQmava 212,20,156\n"
"s_sub_u32 s73, s73, 4\nQmava 216,28,160\ns_max_u32 s73, s73, 120\nQmava 216,20,164\nQe 7\nQmava 220,28,168\nQk 27,6,7\nQmava 220,20,172\n"
"Qe 8\nQcvaa 0,112,16,56\nQe 9\nQmvaa 0,116,20\nQe 10\nQmvaa 0,120,24\nQe 11\nQmvaa 0,124,28\n"
"Qk 28,8,9\nQk 29,10,11\nQcvaa 4,140,28,56\nQe 12\nQmvaa 4,136,24\nQe 13\nQmvaa 4,132,20\nQe 14\n"
"Qmvaa 4,128,16\nQe 15\nQmava 240,32,144\nQta 144,71,0\nQta 146,71,2048\nQmava 240,24,148\nQta 148,71,33024\nQta 150,71,35072\n"
"Qmava 244,32,152\nQta 152,71,256\nQta 154,71,2304\nQmava 244,24,156\nQta 156,71,33280\nQta 158,71,35328\nQmava 248,32,160\nQta 160,71,512\n"
"Qta 162,71,2560\nQmava 248,24,164\nQta 164,71,33536\nQta 166,71,35584\nQmava 252,32,168\nQta 168,71,768\nQta 170,71,2816\nQmava 252,24,172\n"
"Qta 172,71,33792\nQta 174,71,35840\nQcvaa 8,112,32,56\nv_add_u32 v75, 1, v75\nQk 30,12,13\nQmvaa 8,116,36\ns_waitcnt vmcnt(8)\nds_write_b32 v73, v75\n"
"Qmvaa 8,120,40\nQk 31,14,15\nQmvaa 8,124,44\nQe 0\nQcvaa 12,140,44,56\nQe 1\nQmvaa 12,136,40\nQe 2\n"
"Qmvaa 12,132,36\nQe 3\nQmvaa 12,128,32\ns_waitcnt lgkmcnt(1)\nQk 32,0,1\nv_smfmac_f32_16x16x64_bf16 a[188:191], a[204:207], v[24:31], v77\nQk 33,2,3\nQmvva 80,36,144\n"
"Qe 4\nQmvva 80,28,148\nQe 5\nQmvva 84,36,152\nQe 6\nQmvva 84,28,156\nQe 7\nQmvva 88,36,160\n"
"Qk 34,4,5\nQk 35,6,7\nQmvva 88,28,164\nQe 8\nQmvva 92,36,168\nQe 9\nQmvva 92,28,172\nQe 10\n"
"Qcvaa 0,112,48,56\nQe 11\nQmvaa 0,116,52\nds_read_b32 v66, v74\nQk 36,8,9\nQmvaa 0,120,56\nQk 37,10,11\nQmvaa 0,124,60\n"
"Qe 12\nQcvaa 4,140,60,56\nQe 13\nQmvaa 4,136,56\nQe 14\nQmvaa 4,132,52\ns_waitcnt lgkmcnt(0)\nv_cmp_gt_u32 vcc, v75, v66\n"
"Qmvaa 4,128,48\ns_cbranch_vccnz .Lsa3\n.Lra3:\nQk 38,12,13\nQmvva 112,40,144\ns_add_u32 m0, s61, 66048\nQmvva 112,32,148\nbuffer_load_dwordx4 v69, s[40:43], s90 offen nt lds\n"
"Qmvva 116,40,152\nQe 15\nQmvva 116,32,156\nQk 39,14,15\nQmvva 120,40,160\nQe 0\nQmvva 120,32,164\nQe 1\n"
"Qmvva 124,40,168\nbuffer_load_dwordx4 v69, s[40:43], s60 offen offset:1024 nt lds\nQmvva 124,32,172\nQe 2\nQcvaa 8,112,64,56\nQe 3\nQmvaa 8,116,68\nQk 40,0,1\n"
"Qk 41,2,3\nQmvaa 8,120,72\nQe 4\nQmvaa 8,124,76\nbuffer_load_dwordx4 v69, s[40:43], s90 offen offset:2048 nt lds\nQcvaa 12,140,76,56\nQe 5\nQmvaa 12,136,72\n"
"Qe 6\nQmvaa 12,132,68\nQe 7\nQmvaa 12,128,64\nQk 42,4,5\nQk 43,6,7\nv_smfmac_f32_16x16x64_bf16 a[192:195], a[204:207], v[32:39], v77\nQmvva 144,44,144\n"
"buffer_load_dwordx4 v69, s[40:43], s60 offen offset:3072 nt lds\nQmvva 144,36,148\nQmvva 148,44,152\nQe 8\nQmvva 148,36,156\ns_add_u32 m0, s61, 74240\nQmvva 152,44,160\nbuffer_load_dwordx4 v72, s[44:47], s90 offen nt lds\n"
"Qmvva 152,36,164\nQe 9\nQmvva 156,44,168\nQe 10\nQmvva 156,36,172\nQe 11\nQcvaa 0,112,80,56\nQk 44,8,9\n"
"Qk 45,10,11\nQmvaa 0,116,84\nbuffer_load_dwordx4 v72, s[44:47], s60 offen offset:1024 nt lds\nQmvaa 0,120,88\nQe 12\nQmvaa 0,124,92\nQe 13\nQcvaa 4,140,92,56\n"
"Qe 14\nQmvaa 4,136,88\nQe 15\nQmvaa 4,132,84\nbuffer_load_dwordx4 v72, s[44:47], s90 offen offset:2048 nt lds\nQmvaa 4,128,80\nQk 46,12,13\nQk 47,14,15\n"
"Qmvva 176,48,144\nQmvva 176,40,148\nQmvva 180,48,152\nQmvva 180,40,156\nbuffer_load_dwordx4 v72, s[44:47], s60 offen offset:3072 nt lds\nQmvva 184,48,160\nQe 0\nQmvva 184,40,164\n"
"Qe 1\nQmvva 188,48,168\ns_add_u32 s60, s60, 8192\ns_add_u32 s90, s60, 896\nQmvva 188,40,172\nQe 2\nQcvaa 8,112,96,56\nQda 112,68,0\n"
"Qk 48,0,1\nQmvaa 8,116,100\nQda 116,68,512\nQmvaa 8,120,104\nQda 120,68,1024\nQmvaa 8,124,108\nQda 124,68,1536\nQcvaa 12,140,108,56\n"
"Qda 140,68,3584\nQmvaa 12,136,104\nQda 136,68,3072\nQmvaa 12,132,100\nQda 132,68,2560\nQmvaa 12,128,96\nQda 128,68,2048\nv_smfmac_f32_16x16x64_bf16 a[196:199], a[204:207], v[40:47], v77\n"
"Qmvva 208,16,144\ns_memrealtime s[74:75]\nQmvva 208,44,148\nQe 3\nQmvva 212,16,152\nQk 49,2,3\nQmvva 212,44,156\nQe 4\n"
"Qmvva 216,16,160\nQe 5\nQmvva 216,44,164\nQe 6\nQmvva 220,16,168\nQe 7\nQmvva 220,44,172\ns_waitcnt lgkmcnt(0)\n"
"Qk 50,4,5\nQcvaa 0,112,0,56\nQk 51,6,7\nQmvaa 0,116,4\nQe 8\nQmvaa 0,120,8\nQe 9\nQmvaa 0,124,12\n"
"Qe 10\nQcvaa 4,140,12,56\nQe 11\nQmvaa 4,136,8\nQk 52,8,9\nQk 53,10,11\nQmvaa 4,132,4\nQe 12\n"
"Qmvaa 4,128,0\nQe 13\nQmvva 240,20,144\nQe 14\nQmvva 240,48,148\ns_waitcnt lgkmcnt(0)\ns_sub_u32 s75, s77, s74\nQmvva 244,20,152\n"
"s_cmp_lt_u32 s75, 256\ns_cbranch_scc1 .Lsp3\n.Lrp3:\nQmvva 244,48,156\ns_add_u32 s77, s74, s73\nQk 54,12,13\nQmvva 248,20,160\ns_sub_u32 s73, s73, 4\n"
"Qmvva 248,48,164\ns_max_u32 s73, s73, 120\nQmvva 252,20,168\nQe 15\nQmvva 252,48,172\nQk 55,14,15\nQcvaa 8,112,16,56\nQe 0\n"
"Qmvaa 8,116,20\nQe 1\nQmvaa 8,120,24\n.Lpg6:\ns_cbranch_execz .Ltr9\nQe 2\nQmvaa 8,124,28\nQe 3\n"
"Qcvaa 12,140,28,56\nQk 16,0,1\nQk 17,2,3\nQmvaa 12,136,24\nQe 4\nQmvaa 12,132,20\nQe 5\nQmvaa 12,128,16\n"
"Qe 6\nv_smfmac_f32_16x16x64_bf16 a[200:203], a[204:207], v[48:55], v77\nQk 18,4,5\nQmava 224,24,144\nQta 144,71,34048\nQta 146,71,36096\nQmava 224,52,148\nQta 148,70,1024\n"
"Qta 150,70,3072\nQmava 228,24,152\nQta 152,71,34304\nQta 154,71,36352\nQmava 228,52,156\nQta 156,70,1280\nQta 158,70,3328\nQmava 232,24,160\n"
"Qta 160,71,34560\nQta 162,71,36608\nQmava 232,52,164\nQta 164,70,1536\nQta 166,70,3584\nQmava 236,24,168\nQta 168,71,34816\nQta 170,71,36864\n"
"Qmava 236,52,172\nQta 172,70,1792\nQta 174,70,3840\nQcvaa 0,112,32,56\nv_add_u32 v75, 1, v75\nQmvaa 0,116,36\ns_waitcnt vmcnt(8)\nds_write_b32 v73, v75\n"
"Qmvaa 0,120,40\nQe 7\nQmvaa 0,124,44\nQk 19,6,7\nQcvaa 4,140,44,56\nQe 8\nQmvaa 4,136,40\nQe 9\n"
"Qmvaa 4,132,36\nQe 10\nQmvaa 4,128,32\ns_waitcnt lgkmcnt(1)\nQk 20,8,9\nQmvva 96,28,144\nQe 11\nQmvva 96,16,148\n"
"Qk 21,10,11\nQmvva 100,28,152\nQe 12\nQmvva 100,16,156\nQe 13\nQmvva 104,28,160\nQe 14\nQmvva 104,16,164\n"
"Qe 15\nQmvva 108,28,168\nQk 22,12,13\nQk 23,14,15\nQmvva 108,16,172\nQe 0\nQcvaa 8,112,48,56\nQe 1\n"
"Qmvaa 8,116,52\nQe 2\nQmvaa 8,120,56\nds_read_b32 v66, v74\nQk 24,0,1\nQmvaa 8,124,60\nQe 3\nQcvaa 12,140,60,56\n"
"Qk 25,2,3\nQmvaa 12,136,56\nQe 4\nQmvaa 12,132,52\nQe 5\nQmvaa 12,128,48\ns_waitcnt lgkmcnt(0)\nv_cmp_gt_u32 vcc, v75, v66\n"
"v_smfmac_f32_16x16x64_bf16 a[176:179], a[204:207], v[16:23], v77\ns_cbranch_vccnz .Lsa4\n.Lra4:\nQmvva 128,32,144\ns_add_u32 m0, s61, 99072\nQk 26,4,5\nQmvva 128,20,148\nbuffer_load_dwordx4 v69, s[40:43], s90 offen nt lds\n"
"Qmvva 132,32,152\nQe 6\nQmvva 132,20,156\nQe 7\nQmvva 136,32,160\nQk 27,6,7\nQmvva 136,20,164\nQe 8\n"
"Qmvva 140,32,168\nbuffer_load_dwordx4 v69, s[40:43], s60 offen offset:1024 nt lds\nQmvva 140,20,172\nQe 9\nQcvaa 0,112,64,56\nQe 10\nQmvaa 0,116,68\nQe 11\n"
"Qmvaa 0,120,72\nQk 28,8,9\nQk 29,10,11\nQmvaa 0,124,76\nbuffer_load_dwordx4 v69, s[40:43], s90 offen offset:2048 nt lds\nQcvaa 4,140,76,56\nQe 12\nQmvaa 4,136,72\n"
"Qe 13\nQmvaa 4,132,68\nQe 14\nQmvaa 4,128,64\nQe 15\nQmvva 160,36,144\nbuffer_load_dwordx4 v69, s[40:43], s60 offen offset:3072 nt lds\nQmvva 160,24,148\n"
"Qk 30,12,13\nQk 31,14,15\nQmvva 164,36,152\nQmvva 164,24,156\nQe 0\nQmvva 168,36,160\ns_add_u32 m0, s61, 107264\nQmvva 168,24,164\n"
"buffer_load_dwordx4 v72, s[44:47], s90 offen nt lds\nQmvva 172,36,168\nQe 1\nQmvva 172,24,172\nQe 2\nQcvaa 8,112,80,56\nQe 3\nQmvaa 8,116,84\n"
"Qk 32,0,1\nQk 33,2,3\nQmvaa 8,120,88\nbuffer_load_dwordx4 v72, s[44:47], s60 offen offset:1024 nt lds\nQmvaa 8,124,92\nQe 4\nQcvaa 12,140,92,56\nQe 5\n"
"Qmvaa 12,136,88\nQe 6\nQmvaa 12,132,84\nQe 7\nQmvaa 12,128,80\nbuffer_load_dwordx4 v72, s[44:47], s90 offen offset:2048 nt lds\nv_smfmac_f32_16x16x64_bf16 a[180:183], a[204:207], v[24:31], v77\nQk 34,4,5\n"
"Qmvva 192,40,144\nQk 35,6,7\nQmvva 192,28,148\nQmvva 196,40,152\nQe 8\nQmvva 196,28,156\nbuffer_load_dwordx4 v72, s[44:47], s60 offen offset:3072 nt lds\nQmvva 200,40,160\n"
"Qe 9\nQmvva 200,28,164\nQe 10\nQmvva 204,40,168\ns_add_u32 s60, s60, 8192\ns_and_b32 s68, s60, 0x40000\ns_mul_i32 s68, s68, 7\ns_add_u32 s60, s60, s68\n"
"s_add_u32 s90, s60, 896\nQmvva 204,28,172\nQe 11\nQcvaa 0,112,96,56\nQda 112,68,33024\nQk 36,8,9\nQmvaa 0,116,100\nQda 116,68,33536\n"
"Qk 37,10,11\nQmvaa 0,120,104\nQda 120,68,34048\nQmvaa 0,124,108\nQda 124,68,34560\nQcvaa 4,140,108,56\nQda 140,68,36608\nQmvaa 4,136,104\n"
"Qda 136,68,36096\nQmvaa 4,132,100\nQda 132,68,35584\nQmvaa 4,128,96\nQda 128,68,35072\nQmvva 224,44,144\nQe 12\nQmvva 224,32,148\n"
"s_memrealtime s[74:75]\nQmvva 228,44,152\nQe 13\nQmvva 228,32,156\nQe 14\nQmvva 232,44,160\nQe 15\nQmvva 232,32,164\n"
"Qk 38,12,13\nQk 39,14,15\nQmvva 236,44,168\nQe 0\nQmvva 236,32,172\ns_waitcnt lgkmcnt(0)\nQcvaa 8,112,0,56\nQe 1\n"
"Qmvaa 8,116,4\nv_add_u32 v68, 66048, v68\nQk 40,0,1\nQmvaa 8,120,8\nQe 2\nQmvaa 8,124,12\nQe 3\nQcvaa 12,140,12,56\n"
"Qk 41,2,3\nQmvaa 12,136,8\nQe 4\nQmvaa 12,132,4\nQe 5\nQmvaa 12,128,0\nQe 6\nv_smfmac_f32_16x16x64_bf16 a[184:187], a[204:207], v[32:39], v77\n"
"Qk 42,4,5\nQmava 208,48,144\ns_waitcnt lgkmcnt(0)\ns_sub_u32 s75, s77, s74\nQmava 208,36,148\ns_cmp_lt_u32 s75, 256\ns_cbranch_scc1 .Lsp4\n.Lrp4:\n"
"Qmava 212,48,152\ns_add_u32 s77, s74, s73\nQmava 212,36,156\ns_sub_u32 s73, s73, 4\nQmava 216,48,160\ns_max_u32 s73, s73, 120\nQmava 216,36,164\nQe 7\n"
"Qmava 220,48,168\nQk 43,6,7\nQmava 220,36,172\nQe 8\nQcvaa 0,112,16,56\nQe 9\nQmvaa 0,116,20\nQe 10\n"
"Qmvaa 0,120,24\nQe 11\nQmvaa 0,124,28\nQk 44,8,9\nQk 45,10,11\nQcvaa 4,140,28,56\nQe 12\nQmvaa 4,136,24\n"
"Qe 13\nQmvaa 4,132,20\nQe 14\nQmvaa 4,128,16\nQe 15\nQmava 240,52,144\nQta 144,70,0\nQta 146,70,2048\n"
"Qmava 240,40,148\nQta 148,70,33024\nQta 150,70,35072\nQmava 244,52,152\nQta 152,70,256\nQta 154,70,2304\nQmava 244,40,156\nQta 156,70,33280\n"
"Qta 158,70,35328\nQmava 248,52,160\nQta 160,70,512\nQta 162,70,2560\nQmava 248,40,164\nQta 164,70,33536\nQta 166,70,35584\nQmava 252,52,168\n"
"Qta 168,70,768\nQta 170,70,2816\nQmava 252,40,172\nQta 172,70,33792\nQta 174,70,35840\nQcvaa 8,112,32,56\nv_add_u32 v75, 1, v75\nQk 46,12,13\n"
"Qmvaa 8,116,36\ns_waitcnt vmcnt(8)\nds_write_b32 v73, v75\nQmvaa 8,120,40\nQk 47,14,15\nQmvaa 8,124,44\nQe 0\nQcvaa 12,140,44,56\n"
"Qe 1\nQmvaa 12,136,40\nQe 2\nQmvaa 12,132,36\nQe 3\nQmvaa 12,128,32\ns_waitcnt lgkmcnt(1)\nQk 48,0,1\n"
"v_smfmac_f32_16x16x64_bf16 a[188:191], a[204:207], v[40:47], v77\nQk 49,2,3\nQmvva 80,16,144\nQe 4\nQmvva 80,44,148\nQe 5\nQmvva 84,16,152\nQe 6\n"
"Qmvva 84,44,156\nQe 7\nQmvva 88,16,160\nQk 50,4,5\nQk 51,6,7\nQmvva 88,44,164\nQe 8\nQmvva 92,16,168\n"
"Qe 9\nQmvva 92,44,172\nQe 10\nQcvaa 0,112,48,56\nQe 11\nQmvaa 0,116,52\nds_read_b32 v66, v74\nQk 52,8,9\n"
"Qmvaa 0,120,56\nQk 53,10,11\nQmvaa 0,124,60\nQe 12\nQcvaa 4,140,60,56\nQe 13\nQmvaa 4,136,56\nQe 14\n"
"Qmvaa 4,132,52\ns_waitcnt lgkmcnt(0)\nv_cmp_gt_u32 vcc, v75, v66\nQmvaa 4,128,48\ns_cbranch_vccnz .Lsa5\n.Lra5:\nQk 54,12,13\nQmvva 112,20,144\n"
"s_add_u32 m0, s61, 0\nQmvva 112,48,148\nbuffer_load_dwordx4 v69, s[40:43], s90 offen nt lds\nQmvva 116,20,152\nQe 15\nQmvva 116,48,156\nQk 55,14,15\nQmvva 120,20,160\n"
"Qe 0\nQmvva 120,48,164\nQe 1\nQmvva 124,20,168\nbuffer_load_dwordx4 v69, s[40:43], s60 offen offset:1024 nt lds\nQmvva 124,48,172\nQe 2\nQcvaa 8,112,64,56\n"
"Qe 3\nQmvaa 8,116,68\nQk 20,0,1\nQk 21,2,3\nQmvaa 8,120,72\nQe 4\nQmvaa 8,124,76\nbuffer_load_dwordx4 v69, s[40:43], s90 offen offset:2048 nt lds\n"
"Qcvaa 12,140,76,56\nQe 5\nQmvaa 12,136,72\nQe 6\nQmvaa 12,132,68\nQe 7\nQmvaa 12,128,64\nQk 22,4,5\n"
"Qk 23,6,7\nv_smfmac_f32_16x16x64_bf16 a[192:195], a[204:207], v[48:55], v77\nQmvva 144,24,144\nbuffer_load_dwordx4 v69, s[40:43], s60 offen offset:3072 nt lds\nQmvva 144,52,148\nQmvva 148,24,152\nQe 8\nQmvva 148,52,156\n"
"s_add_u32 m0, s61, 8192\nQmvva 152,24,160\nbuffer_load_dwordx4 v72, s[44:47], s90 offen nt lds\nQmvva 152,52,164\nQe 9\nQmvva 156,24,168\nQe 10\nQmvva 156,52,172\n"
"Qe 11\nQcvaa 0,112,80,56\nQk 24,8,9\nQk 25,10,11\nQmvaa 0,116,84\nbuffer_load_dwordx4 v72, s[44:47], s60 offen offset:1024 nt lds\nQmvaa 0,120,88\nQe 12\n"
"Qmvaa 0,124,92\nQe 13\nQcvaa 4,140,92,56\nQe 14\nQmvaa 4,136,88\nQe 15\nQmvaa 4,132,84\nbuffer_load_dwordx4 v72, s[44:47], s90 offen offset:2048 nt lds\n"
"Qmvaa 4,128,80\nQk 26,12,13\nQk 27,14,15\nQmvva 176,28,144\nQmvva 176,20,148\nQmvva 180,28,152\nQmvva 180,20,156\nbuffer_load_dwordx4 v72, s[44:47], s60 offen offset:3072 nt lds\n"
"Qmvva 184,28,160\nQe 0\nQmvva 184,20,164\nQe 1\nQmvva 188,28,168\ns_add_u32 s60, s60, 8192\ns_add_u32 s90, s60, 896\nQmvva 188,20,172\n"
"Qe 2\nQcvaa 8,112,96,56\nQda 112,68,0\nQk 28,0,1\nQmvaa 8,116,100\nQda 116,68,512\nQmvaa 8,120,104\nQda 120,68,1024\n"
"Qmvaa 8,124,108\nQda 124,68,1536\nQcvaa 12,140,108,56\nQda 140,68,3584\nQmvaa 12,136,104\nQda 136,68,3072\nQmvaa 12,132,100\nQda 132,68,2560\n"
"Qmvaa 12,128,96\nQda 128,68,2048\nv_smfmac_f32_16x16x64_bf16 a[196:199], a[204:207], v[20:27], v77\nQmvva 208,32,144\ns_memrealtime s[74:75]\nQmvva 208,24,148\nQe 3\nQmvva 212,32,152\n"
"Qk 29,2,3\nQmvva 212,24,156\nQe 4\nQmvva 216,32,160\nQe 5\nQmvva 216,24,164\nQe 6\nQmvva 220,32,168\n"
"Qe 7\nQmvva 220,24,172\ns_waitcnt lgkmcnt(0)\nQk 30,4,5\nQcvaa 0,112,0,56\nQk 31,6,7\nQmvaa 0,116,4\nQe 8\n"
"Qmvaa 0,120,8\nQe 9\nQmvaa 0,124,12\nQe 10\nQcvaa 4,140,12,56\nQe 11\nQmvaa 4,136,8\nQk 32,8,9\n"
"Qk 33,10,11\nQmvaa 4,132,4\nQe 12\nQmvaa 4,128,0\nQe 13\nQmvva 240,36,144\nQe 14\nQmvva 240,28,148\n"
"s_waitcnt lgkmcnt(0)\ns_sub_u32 s75, s77, s74\nQmvva 244,36,152\ns_cmp_lt_u32 s75, 256\ns_cbranch_scc1 .Lsp5\n.Lrp5:\nQmvva 244,28,156\ns_add_u32 s77, s74, s73\n"
"Qk 34,12,13\nQmvva 248,36,160\ns_sub_u32 s73, s73, 4\nQmvva 248,28,164\ns_max_u32 s73, s73, 120\nQmvva 252,36,168\nQe 15\nQmvva 252,28,172\n"
"Qk 35,14,15\nQcvaa 8,112,16,56\nQe 0\nQmvaa 8,116,20\nQe 1\nQmvaa 8,120,24\nQe 2\nQmvaa 8,124,28\n"
"Qe 3\nQcvaa 12,140,28,56\nQk 36,0,1\nQk 37,2,3\nQmvaa 12,136,24\nQe 4\nQmvaa 12,132,20\nQe 5\n"
"Qmvaa 12,128,16\nQe 6\nv_smfmac_f32_16x16x64_bf16 a[200:203], a[204:207], v[28:35], v77\nQk 38,4,5\nQmava 224,40,144\nQta 144,70,34048\nQta 146,70,36096\nQmava 224,32,148\n"
"Qta 148,71,1024\nQta 150,71,3072\nQmava 228,40,152\nQta 152,70,34304\nQta 154,70,36352\nQmava 228,32,156\nQta 156,71,1280\nQta 158,71,3328\n"
"Qmava 232,40,160\nQta 160,70,34560\nQta 162,70,36608\nQmava 232,32,164\nQta 164,71,1536\nQta 166,71,3584\nQmava 236,40,168\nQta 168,70,34816\n"
"Qta 170,70,36864\nQmava 236,32,172\nQta 172,71,1792\nQta 174,71,3840\ns_add_u32 s66, s66, 1\ns_bitcmp1_b32 0x7f7f, s66\ns_cbranch_scc1 .Lloop\ns_cmp_lg_u32 s67, 0\n"
"s_cbranch_scc1 .Ltail1\nQcvaa 0,112,32,56\nv_add_u32 v75, 1, v75\nQmvaa 0,116,36\ns_waitcnt vmcnt(8)\nds_write_b32 v73, v75\nQmvaa 0,120,40\nQe 7\n"
"Qmvaa 0,124,44\nQk 39,6,7\nQcvaa 4,140,44,56\nQe 8\nQmvaa 4,136,40\nQe 9\nQmvaa 4,132,36\nQe 10\n"
"Qmvaa 4,128,32\ns_waitcnt lgkmcnt(1)\nQk 40,8,9\nQmvva 96,44,144\nQe 11\nQmvva 96,36,148\nQk 41,10,11\nQmvva 100,44,152\n"
"Qe 12\nQmvva 100,36,156\nQe 13\nQmvva 104,44,160\nQe 14\nQmvva 104,36,164\nQe 15\nQmvva 108,44,168\n"
"Qk 42,12,13\nQk 43,14,15\nQmvva 108,36,172\nQe 0\nQcvaa 8,112,48,56\nQe 1\nQmvaa 8,116,52\nQe 2\n"
"Qmvaa 8,120,56\nds_read_b32 v66, v74\n.Lpg7:\ns_cbranch_execz .Ltr9\nQk 44,0,1\nQmvaa 8,124,60\nQe 3\nQcvaa 12,140,60,56\n"
"Qk 45,2,3\nQmvaa 12,136,56\nQe 4\nQmvaa 12,132,52\nQe 5\nQmvaa 12,128,48\ns_waitcnt lgkmcnt(0)\nv_cmp_gt_u32 vcc, v75, v66\n"
"v_smfmac_f32_16x16x64_bf16 a[176:179], a[204:207], v[36:43], v77\ns_cbranch_vccnz .Lsa30\n.Lra30:\nQmvva 128,48,144\ns_add_u32 m0, s62, 0\ns_add_u32 s69, s87, 0\nQk 46,4,5\nQmvva 128,40,148\n"
"buffer_load_dwordx4 v62, s[52:55], s69 offen lds\nQmvva 132,48,152\nQe 6\nQmvva 132,40,156\nQe 7\nQmvva 136,48,160\nQk 47,6,7\nQmvva 136,40,164\n"
"s_add_u32 s69, s87, 64504\nQe 8\nQmvva 140,48,168\nbuffer_load_dwordx4 v62, s[52:55], s69 offen offset:1032 lds\nQmvva 140,40,172\nQe 9\nQcvaa 0,112,64,56\nQe 10\n"
"Qmvaa 0,116,68\nQe 11\nQmvaa 0,120,72\ns_add_u32 s69, s87, 129008\nQk 48,8,9\nQk 49,10,11\nQmvaa 0,124,76\nbuffer_load_dwordx4 v62, s[52:55], s69 offen offset:2064 lds\n"
"Qcvaa 4,140,76,56\nQe 12\nQmvaa 4,136,72\nQe 13\nQmvaa 4,132,68\nQe 14\nQmvaa 4,128,64\ns_add_u32 s69, s87, 193512\n"
"Qe 15\nQmvva 160,52,144\nbuffer_load_dwordx4 v62, s[52:55], s69 offen offset:3096 lds\nQmvva 160,44,148\nQk 50,12,13\nQk 51,14,15\nQmvva 164,52,152\nQmvva 164,44,156\n"
"Qe 0\nQmvva 168,52,160\ns_add_u32 m0, s62, 16512\ns_add_u32 s69, s87, 128\nQmvva 168,44,164\nbuffer_load_dwordx4 v62, s[52:55], s69 offen lds\nQmvva 172,52,168\nQe 1\n"
"Qmvva 172,44,172\nQe 2\nQcvaa 8,112,80,56\nQe 3\nQmvaa 8,116,84\ns_add_u32 s69, s87, 64632\nQk 16,0,1\nQk 17,2,3\n"
"Qmvaa 8,120,88\nbuffer_load_dwordx4 v62, s[52:55], s69 offen offset:1032 lds\nQmvaa 8,124,92\nQe 4\nQcvaa 12,140,92,56\nQe 5\nQmvaa 12,136,88\nQe 6\n"
"Qmvaa 12,132,84\ns_add_u32 s69, s87, 129136\nQe 7\nQmvaa 12,128,80\nbuffer_load_dwordx4 v62, s[52:55], s69 offen offset:2064 lds\nv_smfmac_f32_16x16x64_bf16 a[180:183], a[204:207], v[44:51], v77\nQk 18,4,5\nQmvva 192,20,144\n"
"Qk 19,6,7\nQmvva 192,48,148\nQmvva 196,20,152\ns_add_u32 s69, s87, 193640\nQe 8\nQmvva 196,48,156\nbuffer_load_dwordx4 v62, s[52:55], s69 offen offset:3096 lds\nQmvva 200,20,160\n"
"Qe 9\nQmvva 200,48,164\nQe 10\nQmvva 204,20,168\nQmvva 204,48,172\nQe 11\nQcvaa 0,112,96,56\nQda 112,68,33024\n"
"Qk 20,8,9\nQmvaa 0,116,100\nQda 116,68,33536\nQk 21,10,11\nQmvaa 0,120,104\nQda 120,68,34048\nQmvaa 0,124,108\nQda 124,68,34560\n"
"Qcvaa 4,140,108,56\nQda 140,68,36608\nQmvaa 4,136,104\nQda 136,68,36096\nQmvaa 4,132,100\nQda 132,68,35584\nQmvaa 4,128,96\nQda 128,68,35072\n"
"Qmvva 224,24,144\nQe 12\nQmvva 224,16,148\ns_add_u32 m0, s63, 33024\ns_add_u32 s88, s86, 0\nQmvva 228,24,152\nbuffer_load_dwordx4 v64, s[48:51], s88 offen lds\nQe 13\n"
"Qmvva 228,16,156\nQe 14\nQmvva 232,24,160\nQe 15\nQmvva 232,16,164\ns_add_u32 s88, s86, 19456\nQk 22,12,13\nQk 23,14,15\n"
"Qmvva 236,24,168\nbuffer_load_dwordx4 v64, s[48:51], s88 offen offset:1024 lds\nQe 0\nQmvva 236,16,172\ns_waitcnt lgkmcnt(0)\nQcvaa 8,112,0,56\nQe 1\nQmvaa 8,116,4\n"
"v_subrev_u32 v68, 66048, v68\ns_add_u32 s88, s86, 38912\nQk 24,0,1\nQmvaa 8,120,8\nbuffer_load_dwordx4 v64, s[48:51], s88 offen offset:2048 lds\nQe 2\nQmvaa 8,124,12\nQe 3\n"
"Qcvaa 12,140,12,56\nQk 25,2,3\nQmvaa 12,136,8\ns_add_u32 s88, s86, 58368\nQe 4\nQmvaa 12,132,4\nbuffer_load_dwordx4 v64, s[48:51], s88 offen offset:3072 lds\nQe 5\n"
"Qmvaa 12,128,0\nQe 6\nv_smfmac_f32_16x16x64_bf16 a[184:187], a[204:207], v[16:23], v77\nQk 26,4,5\nQmava 208,28,144\ns_add_u32 m0, s63, 37120\ns_add_u32 s88, s86, 81920\nQmava 208,20,148\n"
"buffer_load_dwordx4 v64, s[48:51], s88 offen lds\nQmava 212,28,152\nQmava 212,20,156\nQmava 216,28,160\ns_add_u32 s88, s86, 101376\nQmava 216,20,164\nbuffer_load_dwordx4 v64, s[48:51], s88 offen offset:1024 lds\nQe 7\n"
"Qmava 220,28,168\nQk 27,6,7\nQmava 220,20,172\nQe 8\nQcvaa 0,112,16,56\ns_add_u32 s88, s86, 120832\nQe 9\nQmvaa 0,116,20\n"
"buffer_load_dwordx4 v64, s[48:51], s88 offen offset:2048 lds\nQe 10\nQmvaa 0,120,24\nQe 11\nQmvaa 0,124,28\nQk 28,8,9\nQk 29,10,11\nQcvaa 4,140,28,56\n"
"Qe 12\nQmvaa 4,136,24\nQe 13\nQmvaa 4,132,20\nQe 14\nQmvaa 4,128,16\nQe 15\nQmava 240,32,144\n"
"Qta 144,71,0\nQta 146,71,2048\nQmava 240,24,148\nQta 148,71,33024\nQta 150,71,35072\nQmava 244,32,152\nQta 152,71,256\nQta 154,71,2304\n"
"Qmava 244,24,156\nQta 156,71,33280\nQta 158,71,35328\nQmava 248,32,160\nQta 160,71,512\nQta 162,71,2560\nQmava 248,24,164\nQta 164,71,33536\n"
"Qta 166,71,35584\nQmava 252,32,168\nQta 168,71,768\nQta 170,71,2816\nQmava 252,24,172\nQta 172,71,33792\nQta 174,71,35840\nQcvaa 8,112,32,56\n"
"v_add_u32 v75, 1, v75\nQk 30,12,13\nQmvaa 8,116,36\nds_write_b32 v73, v75\nQmvaa 8,120,40\nQk 31,14,15\nQmvaa 8,124,44\nQe 0\n"
"Qcvaa 12,140,44,56\nQe 1\nQmvaa 12,136,40\nQe 2\nQmvaa 12,132,36\nQe 3\nQmvaa 12,128,32\ns_waitcnt lgkmcnt(1)\n"
"Qk 32,0,1\nv_smfmac_f32_16x16x64_bf16 a[188:191], a[204:207], v[24:31], v77\nQk 33,2,3\nQmvva 80,36,144\nQe 4\nQmvva 80,28,148\nQe 5\nQmvva 84,36,152\n"
"Qe 6\nQmvva 84,28,156\nQe 7\nQmvva 88,36,160\nQk 34,4,5\nQk 35,6,7\nQmvva 88,28,164\nQe 8\n"
"Qmvva 92,36,168\nQe 9\nQmvva 92,28,172\nQe 10\nQcvaa 0,112,48,56\nQe 11\nQmvaa 0,116,52\nds_read_b32 v66, v74\n"
"Qk 36,8,9\nQmvaa 0,120,56\nQk 37,10,11\nQmvaa 0,124,60\nQe 12\nQcvaa 4,140,60,56\nQe 13\nQmvaa 4,136,56\n"
"Qe 14\nQmvaa 4,132,52\ns_waitcnt lgkmcnt(0)\nv_cmp_gt_u32 vcc, v75, v66\nQmvaa 4,128,48\ns_cbranch_vccnz .Lsa31\n.Lra31:\nQk 38,12,13\n"
"Qmvva 112,40,144\ns_add_u32 m0, s62, 33024\ns_add_u32 s69, s87, 524288\nQmvva 112,32,148\nbuffer_load_dwordx4 v62, s[52:55], s69 offen lds\nQmvva 116,40,152\nQe 15\nQmvva 116,32,156\n"
"Qk 39,14,15\nQmvva 120,40,160\nQe 0\nQmvva 120,32,164\ns_add_u32 s69, s87, 588792\nQe 1\nQmvva 124,40,168\nbuffer_load_dwordx4 v62, s[52:55], s69 offen offset:1032 lds\n"
"Qmvva 124,32,172\nQe 2\nQcvaa 8,112,64,56\nQe 3\nQmvaa 8,116,68\nQk 40,0,1\nQk 41,2,3\nQmvaa 8,120,72\n"
"s_add_u32 s69, s87, 653296\nQe 4\nQmvaa 8,124,76\nbuffer_load_dwordx4 v62, s[52:55], s69 offen offset:2064 lds\nQcvaa 12,140,76,56\nQe 5\nQmvaa 12,136,72\nQe 6\n"
"Qmvaa 12,132,68\nQe 7\nQmvaa 12,128,64\nQk 42,4,5\nQk 43,6,7\nv_smfmac_f32_16x16x64_bf16 a[192:195], a[204:207], v[32:39], v77\ns_add_u32 s69, s87, 717800\nQmvva 144,44,144\n"
"buffer_load_dwordx4 v62, s[52:55], s69 offen offset:3096 lds\nQmvva 144,36,148\nQmvva 148,44,152\nQe 8\nQmvva 148,36,156\ns_add_u32 m0, s62, 49536\ns_add_u32 s69, s87, 524416\nQmvva 152,44,160\n"
"buffer_load_dwordx4 v62, s[52:55], s69 offen lds\nQmvva 152,36,164\nQe 9\nQmvva 156,44,168\nQe 10\nQmvva 156,36,172\nQe 11\nQcvaa 0,112,80,56\n"
"s_add_u32 s69, s87, 588920\nQk 44,8,9\nQk 45,10,11\nQmvaa 0,116,84\nbuffer_load_dwordx4 v62, s[52:55], s69 offen offset:1032 lds\nQmvaa 0,120,88\nQe 12\nQmvaa 0,124,92\n"
"Qe 13\nQcvaa 4,140,92,56\nQe 14\nQmvaa 4,136,88\ns_add_u32 s69, s87, 653424\nQe 15\nQmvaa 4,132,84\nbuffer_load_dwordx4 v62, s[52:55], s69 offen offset:2064 lds\n"
"Qmvaa 4,128,80\nQk 46,12,13\nQk 47,14,15\nQmvva 176,48,144\nQmvva 176,40,148\nQmvva 180,48,152\ns_add_u32 s69, s87, 717928\nQmvva 180,40,156\n"
"buffer_load_dwordx4 v62, s[52:55], s69 offen offset:3096 lds\nQmvva 184,48,160\nQe 0\nQmvva 184,40,164\nQe 1\nQmvva 188,48,168\nQmvva 188,40,172\nQe 2\n"
"Qcvaa 8,112,96,56\nQk 48,0,1\nQmvaa 8,116,100\nQmvaa 8,120,104\nQmvaa 8,124,108\nQcvaa 12,140,108,56\nQmvaa 12,136,104\nQmvaa 12,132,100\n"
"Qmvaa 12,128,96\nv_smfmac_f32_16x16x64_bf16 a[196:199], a[204:207], v[40:47], v77\nQmvva 208,16,144\nQta 112,71,34048\nQmvva 208,44,148\nQta 114,71,36096\nQe 3\nQmvva 212,16,152\n"
"Qta 116,71,34304\nQk 49,2,3\nQmvva 212,44,156\nQta 118,71,36352\nQe 4\nQmvva 216,16,160\nQta 120,71,34560\nQe 5\n"
"Qmvva 216,44,164\nQta 122,71,36608\nQe 6\nQmvva 220,16,168\nQta 124,71,34816\nQe 7\nQmvva 220,44,172\nQta 126,71,36864\n"
"Qk 50,4,5\nQk 51,6,7\nQe 8\nQe 9\nQe 10\nQe 11\nQk 52,8,9\nQk 53,10,11\n"
"Qe 12\nQe 13\nQmvva 240,20,144\nv_add_u32 v75, 1, v75\nQe 14\nQmvva 240,48,148\nds_write_b32 v73, v75\nQmvva 244,20,152\n"
"Qmvva 244,48,156\nQk 54,12,13\nQmvva 248,20,160\nQmvva 248,48,164\nQmvva 252,20,168\nQe 15\nQmvva 252,48,172\nQk 55,14,15\n"
"s_nop 1\nv_smfmac_f32_16x16x64_bf16 a[200:203], a[204:207], v[48:55], v77\nQmava 224,24,144\nds_read_b32 v66, v74\nv_accvgpr_read_b32 v0, a176\nQmava 224,52,148\nv_accvgpr_read_b32 v1, a177\nv_accvgpr_read_b32 v6, a178\n"
"Qmava 228,24,152\nv_accvgpr_read_b32 v7, a179\nv_accvgpr_read_b32 v8, a180\nQmava 228,52,156\nv_accvgpr_read_b32 v9, a181\nv_accvgpr_read_b32 v14, a182\nQmava 232,24,160\nv_accvgpr_read_b32 v15, a183\n"
"v_accvgpr_read_b32 v16, a184\nQmava 232,52,164\ns_waitcnt lgkmcnt(0)\nv_cmp_gt_u32 vcc, v75, v66\nQmava 236,24,168\ns_cbranch_vccnz .Lsa32\n.Lra32:\nv_accvgpr_read_b32 v17, a185\n"
"Qmava 236,52,172\ns_waitcnt lgkmcnt(0)\nv_accvgpr_read_b32 v22, a186\nQmvva 96,28,112\ns_add_u32 m0, s63, 0\ns_add_u32 s88, s86, 128\nv_accvgpr_read_b32 v23, a187\nQmvva 100,28,116\n"
"buffer_load_dwordx4 v64, s[48:51], s88 offen lds\nQmvva 104,28,120\nv_accvgpr_read_b32 v24, a188\nv_accvgpr_read_b32 v25, a189\nQmvva 108,28,124\nv_accvgpr_read_b32 v2, a190\nv_accvgpr_read_b32 v3, a191\nQmvva 128,32,112\n"
"s_add_u32 s88, s86, 19584\nv_accvgpr_read_b32 v4, a192\nQmvva 132,32,116\nbuffer_load_dwordx4 v64, s[48:51], s88 offen offset:1024 lds\nQmvva 136,32,120\nv_accvgpr_read_b32 v5, a193\nv_accvgpr_read_b32 v10, a194\nQmvva 140,32,124\n"
"v_accvgpr_read_b32 v11, a195\nv_accvgpr_read_b32 v12, a196\nQmvva 160,36,112\ns_add_u32 s88, s86, 39040\nv_accvgpr_read_b32 v13, a197\nQmvva 164,36,116\nbuffer_load_dwordx4 v64, s[48:51], s88 offen offset:2048 lds\nQmvva 168,36,120\n"
"v_accvgpr_read_b32 v18, a198\nv_accvgpr_read_b32 v19, a199\nQmvva 172,36,124\nv_accvgpr_read_b32 v20, a200\nv_accvgpr_read_b32 v21, a201\nQmvva 192,40,112\ns_add_u32 s88, s86, 58496\nv_accvgpr_read_b32 v26, a202\n"
"Qmvva 196,40,116\nbuffer_load_dwordx4 v64, s[48:51], s88 offen offset:3072 lds\nQmvva 200,40,120\nv_accvgpr_read_b32 v27, a203\nQa 8,8,9\nQmvva 204,40,124\nQa 10,10,11\nQa 8,8,10\n"
"Qmvva 224,44,112\ns_add_u32 m0, s63, 4096\ns_add_u32 s88, s86, 82048\nQa 16,16,17\nQmvva 228,44,116\nbuffer_load_dwordx4 v64, s[48:51], s88 offen lds\nQmvva 232,44,120\nQa 18,18,19\n"
"Qa 16,16,18\nQmvva 236,44,124\nQa 4,4,5\nQa 6,6,7\nQmava 208,48,112\ns_add_u32 s88, s86, 101504\nQa 4,4,6\nQmava 212,48,116\n"
"buffer_load_dwordx4 v64, s[48:51], s88 offen offset:1024 lds\nQmava 216,48,120\nQa 12,12,13\nQa 14,14,15\nQmava 220,48,124\nQa 12,12,14\nQa 0,0,1\nQmava 240,52,112\n"
"s_add_u32 s88, s86, 120960\nQa 2,2,3\nQmava 244,52,116\nbuffer_load_dwordx4 v64, s[48:51], s88 offen offset:2048 lds\nQmava 248,52,120\nQa 0,0,2\nQa 24,24,25\nQmava 252,52,124\n"
"Qa 26,26,27\nQa 24,24,26\nQa 20,20,21\nQa 22,22,23\nQa 20,20,22\nv_max3_u32 v9, v8, v16, v4\nv_max3_u32 v9, v9, v12, v0\nv_max3_u32 v9, v9, v24, v20\n"
"v_min3_u32 v11, v8, v16, v4\nv_min3_u32 v11, v11, v12, v0\nv_min3_u32 v11, v11, v24, v20\nv_cmp_lt_u32 vcc, 0x44800000, v9\ns_mov_b64 s[74:75], vcc\nv_cmp_gt_u32 vcc, 0x2b800000, v11\ns_or_b64 s[74:75], s[74:75], vcc\ns_cmp_lg_u64 s[74:75], 0\n"
"s_cbranch_scc1 .Lfb\n.Lfbret0:\nv_rcp_f32 v2, v0\nv_rcp_f32 v6, v4\nv_rcp_f32 v10, v8\nv_rcp_f32 v14, v12\nv_rcp_f32 v18, v16\nv_rcp_f32 v22, v20\n"
"v_rcp_f32 v26, v24\nv_and_b32 v66, 16, v60\nv_mov_b32 v67, 0xffff\nv_lshlrev_b32 v66, v66, v67\ns_mov_b32 vcc_lo, 0x00c0000c\ns_mov_b32 vcc_hi, 0xc0000c00\nv_cndmask_b32 v67, 0, v66, vcc\ns_lshr_b64 vcc, vcc, 2\n"
"v_cndmask_b32 v66, 0, v66, vcc\nv_mov_b64 v[0:1], 0\nQk 2,2,2\nv_and_b32 v3, v2, v67\nv_and_b32 v2, v2, v66\nv_mov_b64 v[4:5], 0\nQk 6,6,6\nv_and_b32 v7, v6, v67\n"
"v_and_b32 v6, v6, v66\nv_mov_b64 v[8:9], 0\nQk 10,10,10\nv_and_b32 v11, v10, v67\nv_and_b32 v10, v10, v66\nv_mov_b64 v[12:13], 0\nQk 14,14,14\nv_and_b32 v15, v14, v67\n"
"v_and_b32 v14, v14, v66\nv_mov_b64 v[16:17], 0\nQk 18,18,18\nv_and_b32 v19, v18, v67\nv_and_b32 v18, v18, v66\nv_mov_b64 v[20:21], 0\nQk 22,22,22\nv_and_b32 v23, v22, v67\n"
"v_and_b32 v22, v22, v66\nv_mov_b64 v[24:25], 0\nQk 26,26,26\nv_and_b32 v27, v26, v67\nv_and_b32 v26, v26, v66\nv_mov_b64 v[28:29], 0\nQk 80,80,81\nQk 81,82,83\n"
"Qk 82,84,85\nQk 83,86,87\ns_nop 1\nQzvvv 84,80,0\nQzvvv 80,80,2\n.Lpg8:\ns_cbranch_execz .Ltr10\nQk 88,88,89\n"
"Qk 89,90,91\nQk 90,92,93\nQk 91,94,95\ns_nop 1\nQzvvv 92,88,0\nQzvvv 88,88,2\nQk 96,96,97\nQk 97,98,99\n"
"Qk 98,100,101\nQk 99,102,103\nQk 80,80,81\nQk 81,82,83\nQzvvv 100,96,0\nQk 82,84,85\nQk 83,86,87\nQzvvv 96,96,2\n"
"v_accvgpr_write_b32 a0, v80\nv_accvgpr_write_b32 a1, v81\nv_accvgpr_write_b32 a2, v82\nv_accvgpr_write_b32 a3, v83\nQk 104,104,105\nQk 105,106,107\nQk 106,108,109\nQk 107,110,111\n"
"Qk 88,88,89\nQk 89,90,91\nQzvvv 108,104,0\nQk 90,92,93\nQk 91,94,95\nQzvvv 104,104,2\nv_accvgpr_write_b32 a4, v88\nv_accvgpr_write_b32 a5, v89\n"
"v_accvgpr_write_b32 a6, v90\nv_accvgpr_write_b32 a7, v91\nQk 112,112,113\nQk 113,114,115\nQk 114,116,117\nQk 115,118,119\nQk 96,96,97\nQk 97,98,99\n"
"Qzvvv 116,112,4\nQk 98,100,101\nQk 99,102,103\nQzvvv 112,112,6\nv_accvgpr_write_b32 a8, v96\nv_accvgpr_write_b32 a9, v97\nv_accvgpr_write_b32 a10, v98\nv_accvgpr_write_b32 a11, v99\n"
"Qk 120,120,121\nQk 121,122,123\nQk 122,124,125\nQk 123,126,127\nQk 104,104,105\nQk 105,106,107\nQzvvv 124,120,4\nQk 106,108,109\n"
"Qk 107,110,111\nQzvvv 120,120,6\nv_accvgpr_write_b32 a12, v104\nv_accvgpr_write_b32 a13, v105\nv_accvgpr_write_b32 a14, v106\nv_accvgpr_write_b32 a15, v107\nQk 128,128,129\nQk 129,130,131\n"
"Qk 130,132,133\nQk 131,134,135\nQk 112,112,113\nQk 113,114,115\nQzvvv 132,128,4\nQk 114,116,117\nQk 115,118,119\nQzvvv 128,128,6\n"
"v_accvgpr_write_b32 a16, v112\nv_accvgpr_write_b32 a17, v113\nv_accvgpr_write_b32 a18, v114\nv_accvgpr_write_b32 a19, v115\nQk 136,136,137\nQk 137,138,139\nQk 138,140,141\nQk 139,142,143\n"
"Qk 120,120,121\nQk 121,122,123\nQzvvv 140,136,4\nQk 122,124,125\nQk 123,126,127\nQzvvv 136,136,6\nv_accvgpr_write_b32 a20, v120\nv_accvgpr_write_b32 a21, v121\n"
"v_accvgpr_write_b32 a22, v122\nv_accvgpr_write_b32 a23, v123\nQk 144,144,145\nQk 145,146,147\nQk 146,148,149\nQk 147,150,151\nQk 128,128,129\nQk 129,130,131\n"
"Qzvvv 148,144,8\nQk 130,132,133\nQk 131,134,135\nQzvvv 144,144,10\nv_accvgpr_write_b32 a24, v128\nv_accvgpr_write_b32 a25, v129\nv_accvgpr_write_b32 a26, v130\nv_accvgpr_write_b32 a27, v131\n"
"Qk 152,152,153\nQk 153,154,155\nQk 154,156,157\nQk 155,158,159\nQk 136,136,137\nQk 137,138,139\nQzvvv 156,152,8\nQk 138,140,141\n"
"Qk 139,142,143\nQzvvv 152,152,10\nv_accvgpr_write_b32 a28, v136\nv_accvgpr_write_b32 a29, v137\nv_accvgpr_write_b32 a30, v138\nv_accvgpr_write_b32 a31, v139\nQk 160,160,161\nQk 161,162,163\n"
"Qk 162,164,165\nQk 163,166,167\nQk 144,144,145\nQk 145,146,147\nQzvvv 164,160,8\nQk 146,148,149\nQk 147,150,151\nQzvvv 160,160,10\n"
"v_accvgpr_write_b32 a32, v144\nv_accvgpr_write_b32 a33, v145\nv_accvgpr_write_b32 a34, v146\nv_accvgpr_write_b32 a35, v147\nQk 168,168,169\nQk 169,170,171\nQk 170,172,173\nQk 171,174,175\n"
"Qk 152,152,153\nQk 153,154,155\nQzvvv 172,168,8\nQk 154,156,157\nQk 155,158,159\nQzvvv 168,168,10\nv_accvgpr_write_b32 a36, v152\nv_accvgpr_write_b32 a37, v153\n"
"v_accvgpr_write_b32 a38, v154\nv_accvgpr_write_b32 a39, v155\nQk 176,176,177\nQk 177,178,179\nQk 178,180,181\nQk 179,182,183\nQk 160,160,161\nQk 161,162,163\n"
"Qzvvv 180,176,12\nQk 162,164,165\nQk 163,166,167\nQzvvv 176,176,14\nv_accvgpr_write_b32 a40, v160\nv_accvgpr_write_b32 a41, v161\nv_accvgpr_write_b32 a42, v162\nv_accvgpr_write_b32 a43, v163\n"
"Qk 184,184,185\nQk 185,186,187\nQk 186,188,189\nQk 187,190,191\nQk 168,168,169\nQk 169,170,171\nQzvvv 188,184,12\nQk 170,172,173\n"
"Qk 171,174,175\nQzvvv 184,184,14\nv_accvgpr_write_b32 a44, v168\nv_accvgpr_write_b32 a45, v169\nv_accvgpr_write_b32 a46, v170\nv_accvgpr_write_b32 a47, v171\nQk 192,192,193\nQk 193,194,195\n"
"Qk 194,196,197\nQk 195,198,199\nQk 176,176,177\nQk 177,178,179\nQzvvv 196,192,12\nQk 178,180,181\nQk 179,182,183\nQzvvv 192,192,14\n"
"v_accvgpr_write_b32 a48, v176\nv_accvgpr_write_b32 a49, v177\nv_accvgpr_write_b32 a50, v178\nv_accvgpr_write_b32 a51, v179\nQk 200,200,201\nQk 201,202,203\nQk 202,204,205\nQk 203,206,207\n"
"Qk 184,184,185\nQk 185,186,187\nQzvvv 204,200,12\nQk 186,188,189\nQk 187,190,191\nQzvvv 200,200,14\nv_accvgpr_write_b32 a52, v184\nv_accvgpr_write_b32 a53, v185\n"
"v_accvgpr_write_b32 a54, v186\nv_accvgpr_write_b32 a55, v187\nQk 208,208,209\nQk 209,210,211\nQk 210,212,213\nQk 211,214,215\nQk 192,192,193\nQk 193,194,195\n"
"Qzvvv 212,208,16\nQk 194,196,197\nQk 195,198,199\nQzvvv 208,208,18\nv_accvgpr_write_b32 a56, v192\nv_accvgpr_write_b32 a57, v193\nv_accvgpr_write_b32 a58, v194\nv_accvgpr_write_b32 a59, v195\n"
"Qk 216,216,217\nQk 217,218,219\nQk 218,220,221\nQk 219,222,223\nQk 200,200,201\nQk 201,202,203\nQzvvv 220,216,16\nQk 202,204,205\n"
"Qk 203,206,207\nQzvvv 216,216,18\nv_accvgpr_write_b32 a60, v200\nv_accvgpr_write_b32 a61, v201\nv_accvgpr_write_b32 a62, v202\nv_accvgpr_write_b32 a63, v203\nQk 224,224,225\nQk 225,226,227\n"
"Qk 226,228,229\nQk 227,230,231\nQk 208,208,209\nQk 209,210,211\nQzvvv 228,224,16\nQk 210,212,213\nQk 211,214,215\nQzvvv 224,224,18\n"
"v_accvgpr_write_b32 a64, v208\nv_accvgpr_write_b32 a65, v209\nv_accvgpr_write_b32 a66, v210\nv_accvgpr_write_b32 a67, v211\nQk 232,232,233\nQk 233,234,235\nQk 234,236,237\nQk 235,238,239\n"
"Qk 216,216,217\nQk 217,218,219\nQzvvv 236,232,16\nQk 218,220,221\nQk 219,222,223\nQzvvv 232,232,18\nv_accvgpr_write_b32 a68, v216\nv_accvgpr_write_b32 a69, v217\n"
"v_accvgpr_write_b32 a70, v218\nv_accvgpr_write_b32 a71, v219\nQk 240,240,241\nQk 241,242,243\nQk 242,244,245\nQk 243,246,247\nQk 224,224,225\nQk 225,226,227\n"
"Qzvvv 244,240,20\nQk 226,228,229\nQk 227,230,231\nQzvvv 240,240,22\nv_accvgpr_write_b32 a72, v224\nv_accvgpr_write_b32 a73, v225\nv_accvgpr_write_b32 a74, v226\nv_accvgpr_write_b32 a75, v227\n"
"Qk 248,248,249\nQk 249,250,251\nQk 250,252,253\nQk 251,254,255\nQk 232,232,233\nQk 233,234,235\nQzvvv 252,248,20\nQk 234,236,237\n"
"Qk 235,238,239\nQzvvv 248,248,22\nv_accvgpr_write_b32 a76, v232\nv_accvgpr_write_b32 a77, v233\nv_accvgpr_write_b32 a78, v234\nv_accvgpr_write_b32 a79, v235\nv_accvgpr_read_b32 v48, a208\nv_accvgpr_read_b32 v49, a209\n"
"v_accvgpr_read_b32 v50, a210\nv_accvgpr_read_b32 v51, a211\nv_accvgpr_read_b32 v36, a212\nv_accvgpr_read_b32 v37, a213\nv_accvgpr_read_b32 v38, a214\nv_accvgpr_read_b32 v39, a215\nQk 48,48,49\nQk 49,50,51\n"
"Qk 50,36,37\nQk 51,38,39\nQk 240,240,241\nQk 241,242,243\nQzvvv 36,48,20\nQk 242,244,245\nQk 243,246,247\nQzvvv 48,48,22\n"
"v_accvgpr_write_b32 a80, v240\nv_accvgpr_write_b32 a81, v241\nv_accvgpr_write_b32 a82, v242\nv_accvgpr_write_b32 a83, v243\nv_accvgpr_read_b32 v52, a216\nv_accvgpr_read_b32 v53, a217\nv_accvgpr_read_b32 v54, a218\nv_accvgpr_read_b32 v55, a219\n"
"v_accvgpr_read_b32 v40, a220\nv_accvgpr_read_b32 v41, a221\nv_accvgpr_read_b32 v42, a222\nv_accvgpr_read_b32 v43, a223\nQk 52,52,53\nQk 53,54,55\nQk 54,40,41\nQk 55,42,43\n"
"Qk 248,248,249\nQk 249,250,251\nQzvvv 40,52,20\nQk 250,252,253\nQk 251,254,255\nQzvvv 52,52,22\nv_accvgpr_write_b32 a84, v248\nv_accvgpr_write_b32 a85, v249\n"
"v_accvgpr_write_b32 a86, v250\nv_accvgpr_write_b32 a87, v251\nv_accvgpr_read_b32 v44, a224\nv_accvgpr_read_b32 v45, a225\nv_accvgpr_read_b32 v46, a226\nv_accvgpr_read_b32 v47, a227\nv_accvgpr_read_b32 v32, a228\nv_accvgpr_read_b32 v33, a229\n"
"v_accvgpr_read_b32 v34, a230\nv_accvgpr_read_b32 v35, a231\nQk 44,44,45\nQk 45,46,47\nQk 46,32,33\nQk 47,34,35\nQk 48,48,49\nQk 49,50,51\n"
"Qzvvv 32,44,24\nQk 50,36,37\nQk 51,38,39\nQzvvv 44,44,26\nv_accvgpr_write_b32 a88, v48\nv_accvgpr_write_b32 a89, v49\nv_accvgpr_write_b32 a90, v50\nv_accvgpr_write_b32 a91, v51\n"
"v_accvgpr_read_b32 v48, a232\nv_accvgpr_read_b32 v49, a233\nv_accvgpr_read_b32 v50, a234\nv_accvgpr_read_b32 v51, a235\nv_accvgpr_read_b32 v36, a236\nv_accvgpr_read_b32 v37, a237\nv_accvgpr_read_b32 v38, a238\nv_accvgpr_read_b32 v39, a239\n"
"Qk 48,48,49\nQk 49,50,51\nQk 50,36,37\nQk 51,38,39\nQk 52,52,53\nQk 53,54,55\nQzvvv 36,48,24\nQk 54,40,41\n"
"Qk 55,42,43\nQzvvv 48,48,26\nv_accvgpr_write_b32 a92, v52\nv_accvgpr_write_b32 a93, v53\nv_accvgpr_write_b32 a94, v54\nv_accvgpr_write_b32 a95, v55\nv_accvgpr_read_b32 v52, a240\nv_accvgpr_read_b32 v53, a241\n"
"v_accvgpr_read_b32 v54, a242\nv_accvgpr_read_b32 v55, a243\nv_accvgpr_read_b32 v40, a244\nv_accvgpr_read_b32 v41, a245\nv_accvgpr_read_b32 v42, a246\nv_accvgpr_read_b32 v43, a247\nQk 52,52,53\nQk 53,54,55\n"
"Qk 54,40,41\nQk 55,42,43\nQk 44,44,45\nQk 45,46,47\nQzvvv 40,52,24\nQk 46,32,33\nQk 47,34,35\nQzvvv 52,52,26\n"
"v_accvgpr_write_b32 a96, v44\nv_accvgpr_write_b32 a97, v45\nv_accvgpr_write_b32 a98, v46\nv_accvgpr_write_b32 a99, v47\nv_accvgpr_read_b32 v44, a248\nv_accvgpr_read_b32 v45, a249\nv_accvgpr_read_b32 v46, a250\nv_accvgpr_read_b32 v47, a251\n"
"v_accvgpr_read_b32 v32, a252\nv_accvgpr_read_b32 v33, a253\nv_accvgpr_read_b32 v34, a254\nv_accvgpr_read_b32 v35, a255\nQk 44,44,45\nQk 45,46,47\nQk 46,32,33\nQk 47,34,35\n"
"Qk 48,48,49\nQk 49,50,51\nQzvvv 32,44,24\nQk 50,36,37\nQk 51,38,39\nQzvvv 44,44,26\nv_accvgpr_write_b32 a100, v48\nv_accvgpr_write_b32 a101, v49\n"
"v_accvgpr_write_b32 a102, v50\nv_accvgpr_write_b32 a103, v51\nQk 52,52,53\nQk 53,54,55\nQk 54,40,41\nQk 55,42,43\nv_accvgpr_write_b32 a104, v52\nv_accvgpr_write_b32 a105, v53\n"
"v_accvgpr_write_b32 a106, v54\nv_accvgpr_write_b32 a107, v55\nQk 44,44,45\nQk 45,46,47\nQk 46,32,33\nQk 47,34,35\nv_accvgpr_write_b32 a108, v44\nv_accvgpr_write_b32 a109, v45\n"
"v_accvgpr_write_b32 a110, v46\nv_accvgpr_write_b32 a111, v47\ns_mov_b32 s56, %8\ns_and_b32 s57, %9, 0xffff\ns_mov_b32 s94, 0x7ffff000\ns_mul_i32 s68, %20, 40960\ns_lshl_b32 s69, s70, 10\ns_add_u32 s68, s68, s69\n"
"s_add_u32 s68, s68, 4096\ns_add_u32 s94, %15, s68\ns_addc_u32 s95, %16, 0\ns_and_b32 s95, s95, 0xffff\ns_mov_b32 s75, 37888\ns_mov_b32 s58, 1835008\ns_mov_b32 s67, 1\ns_mul_i32 s71, %12, 57344\n"
"s_mul_i32 s68, s92, 7168\ns_add_u32 s71, s71, s68\ns_mov_b64 exec, 0\ns_cmp_eq_u32 s70, 0\ns_cbranch_scc1 .Lpg12\ns_cmp_eq_u32 s70, 1\ns_cbranch_scc1 .Lpg13\ns_cmp_eq_u32 s70, 2\n"
"s_cbranch_scc1 .Lpg14\ns_branch .Lpg15\n.Ltr6:\ns_mov_b64 exec, -1\ns_branch .Lround\n.Lhook:\n.Lpg9:\ns_cbranch_execz .Ltr10\n"
"s_cmp_eq_u32 s66, 1\ns_cbranch_scc1 .Lh1\ns_cmp_eq_u32 s66, 8\ns_cbranch_scc1 .Lh8\ns_cmp_eq_u32 s66, 9\ns_cbranch_scc1 .Lh9\ns_lshr_b32 s68, s99, s66\ns_cmp_eq_u32 s68, 1\n"
"s_cbranch_scc1 .Lh12\ns_branch .Lh11\n.Lh1:\ns_cmp_eq_u32 s70, 0\ns_cselect_b32 s68, 128, 0\nv_mov_b32 v66, 0x7ffff000\nv_cmp_gt_u32 vcc, s68, v60\nv_not_b32 v67, v65\n"
"s_lshl_b32 s68, %19, 2\nv_cndmask_b32 v66, v66, v76, vcc\nv_readlane_b32 s30, v67, %19\nbuffer_store_dword v67, v66, s[24:27], s68 offen offset:1024 sc1\ns_branch .Lhookret\n.Lh8:\nv_bfe_u32 v66, v60, 4, 3\ns_lshl_b32 s68, %19, 7\n"
"v_lshlrev_b32 v66, 2, v66\ns_add_u32 s68, s68, 2293760\nbuffer_load_dword v78, v66, s[24:27], s68 offen offset:1024 sc1\ns_branch .Lhookret\n.Lh9:\ns_waitcnt vmcnt(32)\ns_mov_b32 s97, 0\n.Lack_l:\n"
"v_cmp_ne_u32 vcc, s30, v78\ns_cmp_eq_u64 vcc, 0\ns_cbranch_scc1 .Lack_ok\ns_sleep 1\nv_bfe_u32 v66, v60, 4, 3\ns_lshl_b32 s68, %19, 7\nv_lshlrev_b32 v66, 2, v66\ns_add_u32 s68, s68, 2293760\n"
"buffer_load_dword v78, v66, s[24:27], s68 offen offset:1024 sc1\ns_waitcnt vmcnt(0)\ns_add_u32 s97, s97, 1\ns_cmp_lt_u32 s97, 1048576\ns_cbranch_scc1 .Lack_l\n.Lack_ok:\ns_lshl_b32 s68, %19, 4\ns_cmp_eq_u32 s70, 0\n"
"s_cselect_b32 s68, s68, -1\nv_mov_b32 v67, 0x7ffff000\nv_cmp_eq_u32 vcc, s68, v60\ns_nop 1\nv_cndmask_b32 v67, v67, v76, vcc\nv_add_u32 v66, 1, v65\nbuffer_store_dword v66, v67, s[24:27], 0 offen sc1\ns_branch .Lhookret\n"
".Lh11:\nbuffer_load_dword v78, v76, s[24:27], 0 offen sc1\ns_branch .Lhookret\n.Lh12:\ns_waitcnt vmcnt(32)\nv_sub_u32 v66, v78, v65\nv_cmp_gt_u32 vcc, 1, v66\ns_and_b64 vcc, vcc, s[28:29]\n"
"s_cbranch_scc1 .Lf1_w\n.Lf1_r:\ns_cmp_lg_u32 s70, 3\ns_cbranch_scc1 .Lf1_n\nbuffer_inv sc1\n.Lf1_n:\ns_lshr_b32 s88, %19, 2\ns_lshl_b32 s88, s88, 4\n"
"s_add_u32 s88, s88, s93\ns_mul_i32 s68, s88, 57344\ns_mul_i32 s69, s92, 7168\ns_add_u32 s68, s68, s69\ns_add_u32 s48, %8, s68\ns_addc_u32 s49, %9, 0\ns_and_b32 s49, s49, 0xffff\ns_sub_u32 s50, 1835008, s68\n"
"s_mul_i32 s68, s88, 163840\ns_and_b32 s69, %19, 3\ns_mul_i32 s69, s69, 320\ns_add_u32 s68, s68, s69\ns_add_u32 s52, %15, s68\ns_addc_u32 s53, %16, 0\ns_and_b32 s53, s53, 0xffff\ns_sub_u32 s54, 5246976, s68\n"
"s_add_u32 s71, s71, 458752\ns_and_b32 s87, %19, 3\ns_mul_i32 s87, s87, 327680\ns_add_u32 s87, s87, 1310720\ns_mul_i32 s69, s92, 10240\nv_bfe_u32 v66, v60, 5, 3\nv_mul_u32_u24 v66, 1280, v66\nv_and_b32 v67, 16, v60\n"
"v_add3_u32 v67, v67, v66, s69\n.Lpg10:\ns_cbranch_execz .Ltr10\ns_and_b32 s69, %19, 1\nv_lshrrev_b32 v66, 8, v60\ns_lshl_b32 s68, s93, 1\nv_xor_b32 v66, s68, v66\nv_lshl_add_u32 v66, v66, 5, v67\n"
"s_lshr_b32 s68, 128, s69\nv_add_u32 v61, s68, v66\ns_mul_i32 s68, s69, 192\nv_add_u32 v63, s68, v66\nv_bfe_u32 v66, v60, 8, 1\nv_xor_b32 v66, s93, v66\nv_lshl_add_u32 v67, v66, 5, v67\nv_lshrrev_b32 v66, 9, v60\n"
"v_mul_u32_u24 v66, 20480, v66\ns_xor_b32 s69, s69, 1\ns_lshl_b32 s69, s69, 8\nv_add3_u32 v62, v66, v67, s69\ns_branch .Lhookret\n.Lf1_w:\ns_mov_b32 s97, 0\n.Lf1_l:\n"
"s_sleep 1\nbuffer_load_dword v78, v76, s[24:27], 0 offen sc1\ns_waitcnt vmcnt(0)\ns_add_u32 s97, s97, 1\ns_cmp_gt_u32 s97, 1048576\ns_cbranch_scc1 .Lf1_r\nv_sub_u32 v66, v78, v65\nv_cmp_gt_u32 vcc, 1, v66\n"
"s_and_b64 vcc, vcc, s[28:29]\ns_cbranch_scc1 .Lf1_l\ns_branch .Lf1_r\n.Lsa1:\nds_read_b32 v66, v74\ns_waitcnt lgkmcnt(0)\nv_cmp_gt_u32 vcc, v75, v66\ns_cbranch_vccnz .Lsa1\n"
"s_branch .Lra1\n.Lsp1:\ns_memrealtime s[74:75]\ns_waitcnt lgkmcnt(0)\ns_sub_u32 s75, s77, s74\ns_cmp_lt_u32 s75, 256\ns_cbranch_scc1 .Lsp1\ns_mov_b32 s74, s77\n"
"s_branch .Lrp1\n.Lsa2:\nds_read_b32 v66, v74\ns_waitcnt lgkmcnt(0)\nv_cmp_gt_u32 vcc, v75, v66\ns_cbranch_vccnz .Lsa2\ns_branch .Lra2\n.Lsp2:\n"
"s_memrealtime s[74:75]\ns_waitcnt lgkmcnt(0)\ns_sub_u32 s75, s77, s74\ns_cmp_lt_u32 s75, 256\ns_cbranch_scc1 .Lsp2\ns_mov_b32 s74, s77\ns_branch .Lrp2\n.Lsa3:\n"
"ds_read_b32 v66, v74\ns_waitcnt lgkmcnt(0)\nv_cmp_gt_u32 vcc, v75, v66\ns_cbranch_vccnz .Lsa3\ns_branch .Lra3\n.Lsp3:\ns_memrealtime s[74:75]\ns_waitcnt lgkmcnt(0)\n"
"s_sub_u32 s75, s77, s74\ns_cmp_lt_u32 s75, 256\ns_cbranch_scc1 .Lsp3\ns_mov_b32 s74, s77\ns_branch .Lrp3\n.Lsa4:\nds_read_b32 v66, v74\ns_waitcnt lgkmcnt(0)\n"
"v_cmp_gt_u32 vcc, v75, v66\ns_cbranch_vccnz .Lsa4\ns_branch .Lra4\n.Lsp4:\ns_memrealtime s[74:75]\ns_waitcnt lgkmcnt(0)\ns_sub_u32 s75, s77, s74\ns_cmp_lt_u32 s75, 256\n"
"s_cbranch_scc1 .Lsp4\ns_mov_b32 s74, s77\ns_branch .Lrp4\n.Lsa5:\nds_read_b32 v66, v74\ns_waitcnt lgkmcnt(0)\nv_cmp_gt_u32 vcc, v75, v66\ns_cbranch_vccnz .Lsa5\n"
"s_branch .Lra5\n.Lsp5:\ns_memrealtime s[74:75]\ns_waitcnt lgkmcnt(0)\ns_sub_u32 s75, s77, s74\ns_cmp_lt_u32 s75, 256\ns_cbranch_scc1 .Lsp5\ns_mov_b32 s74, s77\n"
"s_branch .Lrp5\n.Lsa30:\nds_read_b32 v66, v74\ns_waitcnt lgkmcnt(0)\nv_cmp_gt_u32 vcc, v75, v66\ns_cbranch_vccnz .Lsa30\ns_branch .Lra30\n.Lsa31:\n"
"ds_read_b32 v66, v74\ns_waitcnt lgkmcnt(0)\nv_cmp_gt_u32 vcc, v75, v66\ns_cbranch_vccnz .Lsa31\ns_branch .Lra31\n.Lsa32:\nds_read_b32 v66, v74\ns_waitcnt lgkmcnt(0)\n"
"v_cmp_gt_u32 vcc, v75, v66\ns_cbranch_vccnz .Lsa32\ns_branch .Lra32\n.Lsa30x:\nds_read_b32 v66, v74\ns_waitcnt lgkmcnt(0)\nv_cmp_gt_u32 vcc, v75, v66\ns_cbranch_vccnz .Lsa30x\n"
"s_branch .Lra30x\n.Lsa31x:\nds_read_b32 v66, v74\ns_waitcnt lgkmcnt(0)\nv_cmp_gt_u32 vcc, v75, v66\ns_cbranch_vccnz .Lsa31x\ns_branch .Lra31x\n.Lso40:\n"
"ds_read_b32 v66, v74\ns_waitcnt lgkmcnt(0)\nv_cmp_gt_u32 vcc, v75, v66\ns_cbranch_vccnz .Lso40\ns_branch .Lro40\n.Lso180:\nds_read_b32 v66, v74\ns_waitcnt lgkmcnt(0)\n"
"v_cmp_gt_u32 vcc, v75, v66\ns_cbranch_vccnz .Lso180\ns_branch .Lro180\n.Lso320:\nds_read_b32 v66, v74\ns_waitcnt lgkmcnt(0)\nv_cmp_gt_u32 vcc, v75, v66\ns_cbranch_vccnz .Lso320\n"
"s_branch .Lro320\n.Lso460:\nds_read_b32 v66, v74\ns_waitcnt lgkmcnt(0)\nv_cmp_gt_u32 vcc, v75, v66\ns_cbranch_vccnz .Lso460\ns_branch .Lro460\n.Lso600:\n"
"ds_read_b32 v66, v74\ns_waitcnt lgkmcnt(0)\nv_cmp_gt_u32 vcc, v75, v66\ns_cbranch_vccnz .Lso600\ns_branch .Lro600\n.Lso740:\nds_read_b32 v66, v74\ns_waitcnt lgkmcnt(0)\n"
"v_cmp_gt_u32 vcc, v75, v66\ns_cbranch_vccnz .Lso740\ns_branch .Lro740\n.Lso880:\nds_read_b32 v66, v74\ns_waitcnt lgkmcnt(0)\nv_cmp_gt_u32 vcc, v75, v66\ns_cbranch_vccnz .Lso880\n"
"s_branch .Lro880\n.Lso1020:\nds_read_b32 v66, v74\ns_waitcnt lgkmcnt(0)\nv_cmp_gt_u32 vcc, v75, v66\ns_cbranch_vccnz .Lso1020\ns_branch .Lro1020\n.Lso1160:\n"
"ds_read_b32 v66, v74\ns_waitcnt lgkmcnt(0)\nv_cmp_gt_u32 vcc, v75, v66\ns_cbranch_vccnz .Lso1160\ns_branch .Lro1160\n.Lso1300:\nds_read_b32 v66, v74\ns_waitcnt lgkmcnt(0)\n"
"v_cmp_gt_u32 vcc, v75, v66\ns_cbranch_vccnz .Lso1300\ns_branch .Lro1300\n.Lso1440:\nds_read_b32 v66, v74\ns_waitcnt lgkmcnt(0)\nv_cmp_gt_u32 vcc, v75, v66\ns_cbranch_vccnz .Lso1440\n"
"s_branch .Lro1440\n.Lso1455:\nds_read_b32 v66, v74\ns_waitcnt lgkmcnt(0)\nv_cmp_gt_u32 vcc, v75, v66\ns_cbranch_vccnz .Lso1455\ns_branch .Lro1455\n.Lso1525r0:\n"
"ds_read_b32 v66, v74\ns_waitcnt lgkmcnt(0)\nv_cmp_gt_u32 vcc, v75, v66\ns_cbranch_vccnz .Lso1525r0\ns_branch .Lro1525r0\n.Lso1595r0:\nds_read_b32 v66, v74\ns_waitcnt lgkmcnt(0)\n"
"v_cmp_gt_u32 vcc, v75, v66\ns_cbranch_vccnz .Lso1595r0\ns_branch .Lro1595r0\n.Lso1665r0:\nds_read_b32 v66, v74\ns_waitcnt lgkmcnt(0)\nv_cmp_gt_u32 vcc, v75, v66\ns_cbranch_vccnz .Lso1665r0\n"
"s_branch .Lro1665r0\n.Lso1735r0:\nds_read_b32 v66, v74\ns_waitcnt lgkmcnt(0)\nv_cmp_gt_u32 vcc, v75, v66\ns_cbranch_vccnz .Lso1735r0\ns_branch .Lro1735r0\n.Lso1805r0:\n"
"ds_read_b32 v66, v74\ns_waitcnt lgkmcnt(0)\nv_cmp_gt_u32 vcc, v75, v66\ns_cbranch_vccnz .Lso1805r0\ns_branch .Lro1805r0\n.Lso1878r0:\nds_read_b32 v66, v74\ns_waitcnt lgkmcnt(0)\n"
"v_cmp_gt_u32 vcc, v75, v66\ns_cbranch_vccnz .Lso1878r0\ns_branch .Lro1878r0\n.Lso1920r0:\nds_read_b32 v66, v74\ns_waitcnt lgkmcnt(0)\nv_cmp_gt_u32 vcc, v75, v66\ns_cbranch_vccnz .Lso1920r0\n"
"s_branch .Lro1920r0\n.Lso1960r0:\nds_read_b32 v66, v74\ns_waitcnt lgkmcnt(0)\nv_cmp_gt_u32 vcc, v75, v66\ns_cbranch_vccnz .Lso1960r0\ns_branch .Lro1960r0\n.Lso2046r0:\n"
"ds_read_b32 v66, v74\ns_waitcnt lgkmcnt(0)\nv_cmp_gt_u32 vcc, v75, v66\ns_cbranch_vccnz .Lso2046r0\ns_branch .Lro2046r0\n.Lso2088r0:\nds_read_b32 v66, v74\ns_waitcnt lgkmcnt(0)\n"
"v_cmp_gt_u32 vcc, v75, v66\ns_cbranch_vccnz .Lso2088r0\ns_branch .Lro2088r0\n.Lso2172r0:\nds_read_b32 v66, v74\ns_waitcnt lgkmcnt(0)\nv_cmp_gt_u32 vcc, v75, v66\ns_cbranch_vccnz .Lso2172r0\n"
"s_branch .Lro2172r0\n.Lso2222r0:\nds_read_b32 v66, v74\ns_waitcnt lgkmcnt(0)\nv_cmp_gt_u32 vcc, v75, v66\ns_cbranch_vccnz .Lso2222r0\ns_branch .Lro2222r0\n.Lso1525r1:\n"
"ds_read_b32 v66, v74\ns_waitcnt lgkmcnt(0)\nv_cmp_gt_u32 vcc, v75, v66\ns_cbranch_vccnz .Lso1525r1\ns_branch .Lro1525r1\n.Lso1595r1:\nds_read_b32 v66, v74\ns_waitcnt lgkmcnt(0)\n"
"v_cmp_gt_u32 vcc, v75, v66\ns_cbranch_vccnz .Lso1595r1\ns_branch .Lro1595r1\n.Lso1665r1:\nds_read_b32 v66, v74\ns_waitcnt lgkmcnt(0)\nv_cmp_gt_u32 vcc, v75, v66\ns_cbranch_vccnz .Lso1665r1\n"
"s_branch .Lro1665r1\n.Lso1735r1:\nds_read_b32 v66, v74\ns_waitcnt lgkmcnt(0)\nv_cmp_gt_u32 vcc, v75, v66\ns_cbranch_vccnz .Lso1735r1\ns_branch .Lro1735r1\n.Lso1805r1:\n"
"ds_read_b32 v66, v74\ns_waitcnt lgkmcnt(0)\nv_cmp_gt_u32 vcc, v75, v66\ns_cbranch_vccnz .Lso1805r1\ns_branch .Lro1805r1\n.Lso1840r1:\nds_read_b32 v66, v74\ns_waitcnt lgkmcnt(0)\n"
"v_cmp_gt_u32 vcc, v75, v66\ns_cbranch_vccnz .Lso1840r1\ns_branch .Lro1840r1\n.Lso1878r1:\nds_read_b32 v66, v74\ns_waitcnt lgkmcnt(0)\nv_cmp_gt_u32 vcc, v75, v66\ns_cbranch_vccnz .Lso1878r1\n"
"s_branch .Lro1878r1\n.Lso1920r1:\nds_read_b32 v66, v74\ns_waitcnt lgkmcnt(0)\nv_cmp_gt_u32 vcc, v75, v66\ns_cbranch_vccnz .Lso1920r1\ns_branch .Lro1920r1\n.Lso1945r1:\n"
"ds_read_b32 v66, v74\ns_waitcnt lgkmcnt(0)\nv_cmp_gt_u32 vcc, v75, v66\ns_cbranch_vccnz .Lso1945r1\ns_branch .Lro1945r1\n.Lso2004r1:\nds_read_b32 v66, v74\ns_waitcnt lgkmcnt(0)\n"
"v_cmp_gt_u32 vcc, v75, v66\ns_cbranch_vccnz .Lso2004r1\ns_branch .Lro2004r1\n.Lso2045r1:\nds_read_b32 v66, v74\ns_waitcnt lgkmcnt(0)\nv_cmp_gt_u32 vcc, v75, v66\ns_cbranch_vccnz .Lso2045r1\n"
"s_branch .Lro2045r1\n.Lso2130r1:\nds_read_b32 v66, v74\ns_waitcnt lgkmcnt(0)\nv_cmp_gt_u32 vcc, v75, v66\ns_cbranch_vccnz .Lso2130r1\ns_branch .Lro2130r1\n.Lso2172r1:\n"
"ds_read_b32 v66, v74\ns_waitcnt lgkmcnt(0)\nv_cmp_gt_u32 vcc, v75, v66\ns_cbranch_vccnz .Lso2172r1\ns_branch .Lro2172r1\n.Lso2225r1:\nds_read_b32 v66, v74\ns_waitcnt lgkmcnt(0)\n"
"v_cmp_gt_u32 vcc, v75, v66\ns_cbranch_vccnz .Lso2225r1\ns_branch .Lro2225r1\n.Ltail1:\n.Lpg11:\ns_cbranch_execz .Ltr10\nQcvaa 0,112,32,56\nv_add_u32 v75, 1, v75\n"
"Qmvaa 0,116,36\ns_waitcnt vmcnt(0)\nds_write_b32 v73, v75\nQmvaa 0,120,40\nQe 7\nQmvaa 0,124,44\nQk 39,6,7\nQcvaa 4,140,44,56\n"
"Qe 8\nQmvaa 4,136,40\nQe 9\nQmvaa 4,132,36\nQe 10\nQmvaa 4,128,32\ns_waitcnt lgkmcnt(1)\nQk 40,8,9\n"
"Qmvva 96,44,144\nQe 11\nQmvva 96,36,148\nQk 41,10,11\nQmvva 100,44,152\nQe 12\nQmvva 100,36,156\nQe 13\n"
"Qmvva 104,44,160\nQe 14\nQmvva 104,36,164\nQe 15\nQmvva 108,44,168\nQk 42,12,13\nQk 43,14,15\nQmvva 108,36,172\n"
"Qe 0\nQcvaa 8,112,48,56\nQe 1\nQmvaa 8,116,52\nQe 2\nQmvaa 8,120,56\nds_read_b32 v66, v74\nQk 44,0,1\n"
"Qmvaa 8,124,60\nQe 3\nQcvaa 12,140,60,56\nQk 45,2,3\nQmvaa 12,136,56\nQe 4\nQmvaa 12,132,52\nQe 5\n"
"Qmvaa 12,128,48\ns_waitcnt lgkmcnt(0)\nv_cmp_gt_u32 vcc, v75, v66\nv_smfmac_f32_16x16x64_bf16 a[176:179], a[204:207], v[36:43], v77\ns_cbranch_vccnz .Lsa30x\n.Lra30x:\nQmvva 128,48,144\ns_add_u32 m0, s96, 0\n"
"s_add_u32 s95, s87, 4096\nQk 46,4,5\nQmvva 128,40,148\nbuffer_load_dwordx4 v61, s[52:55], s95 offen lds\nQmvva 132,48,152\nQe 6\nQmvva 132,40,156\nQe 7\n"
"Qmvva 136,48,160\nQk 47,6,7\nQmvva 136,40,164\ns_add_u32 s95, s87, 23552\nQe 8\nQmvva 140,48,168\nbuffer_load_dwordx4 v61, s[52:55], s95 offen offset:1024 lds\nQmvva 140,40,172\n"
"Qe 9\nQcvaa 0,112,64,56\nQe 10\nQmvaa 0,116,68\nQe 11\nQmvaa 0,120,72\ns_add_u32 m0, s96, 8192\ns_add_u32 s95, s87, 45056\n"
"Qk 48,8,9\nQk 49,10,11\nQmvaa 0,124,76\nbuffer_load_dwordx4 v61, s[52:55], s95 offen lds\nQcvaa 4,140,76,56\nQe 12\nQmvaa 4,136,72\nQe 13\n"
"Qmvaa 4,132,68\nQe 14\nQmvaa 4,128,64\ns_add_u32 s95, s87, 64512\nQe 15\nQmvva 160,52,144\nbuffer_load_dwordx4 v61, s[52:55], s95 offen offset:1024 lds\nQmvva 160,44,148\n"
"Qk 50,12,13\nQk 51,14,15\nQmvva 164,52,152\nQmvva 164,44,156\nQe 0\nQmvva 168,52,160\ns_add_u32 m0, s96, 16384\ns_add_u32 s95, s87, 4096\n"
"Qmvva 168,44,164\nbuffer_load_dwordx4 v62, s[52:55], s95 offen lds\nQmvva 172,52,168\nQe 1\nQmvva 172,44,172\nQe 2\nQcvaa 8,112,80,56\nQe 3\n"
"Qmvaa 8,116,84\ns_add_u32 s95, s87, 44032\nQk 16,0,1\nQk 17,2,3\nQmvaa 8,120,88\nbuffer_load_dwordx4 v62, s[52:55], s95 offen offset:1024 lds\nQmvaa 8,124,92\nQe 4\n"
"Qcvaa 12,140,92,56\nQe 5\nQmvaa 12,136,88\nQe 6\nQmvaa 12,132,84\ns_add_u32 m0, s96, 24576\ns_add_u32 s95, s87, 4096\nQe 7\n"
"Qmvaa 12,128,80\nbuffer_load_dwordx4 v63, s[52:55], s95 offen lds\nv_smfmac_f32_16x16x64_bf16 a[180:183], a[204:207], v[44:51], v77\nQk 18,4,5\nQmvva 192,20,144\nQk 19,6,7\nQmvva 192,48,148\nQmvva 196,20,152\n"
"s_add_u32 s95, s87, 23552\nQe 8\nQmvva 196,48,156\nbuffer_load_dwordx4 v63, s[52:55], s95 offen offset:1024 lds\nQmvva 200,20,160\nQe 9\nQmvva 200,48,164\nQe 10\n"
"Qmvva 204,20,168\nQmvva 204,48,172\ns_add_u32 m0, s96, 32768\ns_add_u32 s95, s87, 45056\nQe 11\nQcvaa 0,112,96,56\nQda 112,68,33024\nbuffer_load_dwordx4 v63, s[52:55], s95 offen lds\n"
"Qk 20,8,9\nQmvaa 0,116,100\nQda 116,68,33536\nQk 21,10,11\nQmvaa 0,120,104\nQda 120,68,34048\nQmvaa 0,124,108\nQda 124,68,34560\n"
"Qcvaa 4,140,108,56\nQda 140,68,36608\ns_add_u32 s95, s87, 64512\nQmvaa 4,136,104\nQda 136,68,36096\nbuffer_load_dwordx4 v63, s[52:55], s95 offen offset:1024 lds\nQmvaa 4,132,100\nQda 132,68,35584\n"
"Qmvaa 4,128,96\nQda 128,68,35072\nQmvva 224,24,144\nQe 12\nQmvva 224,16,148\ns_add_u32 m0, s96, 40960\ns_mov_b32 s95, 4096\nQmvva 228,24,152\n"
"buffer_load_dwordx4 v61, s[52:55], s95 offen lds\nQe 13\nQmvva 228,16,156\nQe 14\nQmvva 232,24,160\nQe 15\nQmvva 232,16,164\nQk 22,12,13\n"
"Qk 23,14,15\nQmvva 236,24,168\ns_mov_b32 s95, 23552\nQe 0\nQmvva 236,16,172\nbuffer_load_dwordx4 v61, s[52:55], s95 offen offset:1024 lds\ns_waitcnt lgkmcnt(0)\nQcvaa 8,112,0,56\n"
"Qe 1\nQmvaa 8,116,4\nv_subrev_u32 v68, 66048, v68\nQk 24,0,1\nQmvaa 8,120,8\nQe 2\nQmvaa 8,124,12\ns_add_u32 m0, s96, 49152\n"
"s_mov_b32 s95, 45056\nQe 3\nQcvaa 12,140,12,56\nbuffer_load_dwordx4 v61, s[52:55], s95 offen lds\nQk 25,2,3\nQmvaa 12,136,8\nQe 4\nQmvaa 12,132,4\n"
"Qe 5\nQmvaa 12,128,0\nQe 6\nv_smfmac_f32_16x16x64_bf16 a[184:187], a[204:207], v[16:23], v77\ns_mov_b32 s95, 64512\nQk 26,4,5\nQmava 208,28,144\nbuffer_load_dwordx4 v61, s[52:55], s95 offen offset:1024 lds\n"
"Qmava 208,20,148\nQmava 212,28,152\nQmava 212,20,156\nQmava 216,28,160\ns_add_u32 m0, s96, 57344\ns_mov_b32 s95, 4096\nQmava 216,20,164\nbuffer_load_dwordx4 v62, s[52:55], s95 offen lds\n"
"Qe 7\nQmava 220,28,168\nQk 27,6,7\nQmava 220,20,172\nQe 8\nQcvaa 0,112,16,56\nQe 9\nQmvaa 0,116,20\n"
"s_mov_b32 s95, 44032\nQe 10\nQmvaa 0,120,24\nbuffer_load_dwordx4 v62, s[52:55], s95 offen offset:1024 lds\nQe 11\nQmvaa 0,124,28\nQk 28,8,9\nQk 29,10,11\n"
"Qcvaa 4,140,28,56\nQe 12\nQmvaa 4,136,24\nQe 13\nQmvaa 4,132,20\ns_add_u32 m0, s96, 132096\ns_mov_b32 s95, 4096\nQe 14\n"
"Qmvaa 4,128,16\nbuffer_load_dwordx4 v63, s[52:55], s95 offen lds\nQe 15\nQmava 240,32,144\nQta 144,71,0\nQta 146,71,2048\nQmava 240,24,148\nQta 148,71,33024\n"
"Qta 150,71,35072\nQmava 244,32,152\nQta 152,71,256\nQta 154,71,2304\nQmava 244,24,156\nQta 156,71,33280\nQta 158,71,35328\ns_mov_b32 s95, 23552\n"
"Qmava 248,32,160\nQta 160,71,512\nQta 162,71,2560\nbuffer_load_dwordx4 v63, s[52:55], s95 offen offset:1024 lds\nQmava 248,24,164\nQta 164,71,33536\nQta 166,71,35584\nQmava 252,32,168\n"
"Qta 168,71,768\nQta 170,71,2816\nQmava 252,24,172\nQta 172,71,33792\nQta 174,71,35840\nQcvaa 8,112,32,56\nv_add_u32 v75, 1, v75\nQk 30,12,13\n"
"Qmvaa 8,116,36\nds_write_b32 v73, v75\nQmvaa 8,120,40\nQk 31,14,15\nQmvaa 8,124,44\nQe 0\nQcvaa 12,140,44,56\nQe 1\n"
"Qmvaa 12,136,40\nQe 2\nQmvaa 12,132,36\nQe 3\nQmvaa 12,128,32\ns_waitcnt lgkmcnt(1)\nQk 32,0,1\nv_smfmac_f32_16x16x64_bf16 a[188:191], a[204:207], v[24:31], v77\n"
"Qk 33,2,3\nQmvva 80,36,144\nQe 4\nQmvva 80,28,148\nQe 5\nQmvva 84,36,152\nQe 6\nQmvva 84,28,156\n"
"Qe 7\nQmvva 88,36,160\nQk 34,4,5\nQk 35,6,7\nQmvva 88,28,164\nQe 8\nQmvva 92,36,168\nQe 9\n"
"Qmvva 92,28,172\nQe 10\nQcvaa 0,112,48,56\nQe 11\nQmvaa 0,116,52\nds_read_b32 v66, v74\nQk 36,8,9\nQmvaa 0,120,56\n"
"Qk 37,10,11\nQmvaa 0,124,60\nQe 12\nQcvaa 4,140,60,56\nQe 13\nQmvaa 4,136,56\nQe 14\nQmvaa 4,132,52\n"
"s_waitcnt lgkmcnt(0)\nv_cmp_gt_u32 vcc, v75, v66\nQmvaa 4,128,48\ns_cbranch_vccnz .Lsa31x\n.Lra31x:\nQk 38,12,13\nQmvva 112,40,144\ns_add_u32 m0, s96, 140288\n"
"s_mov_b32 s95, 45056\nQmvva 112,32,148\nbuffer_load_dwordx4 v63, s[52:55], s95 offen lds\nQmvva 116,40,152\nQe 15\nQmvva 116,32,156\nQk 39,14,15\nQmvva 120,40,160\n"
"Qe 0\nQmvva 120,32,164\ns_mov_b32 s95, 64512\nQe 1\nQmvva 124,40,168\nbuffer_load_dwordx4 v63, s[52:55], s95 offen offset:1024 lds\nQmvva 124,32,172\nQe 2\n"
"Qcvaa 8,112,64,56\nQe 3\nQmvaa 8,116,68\nQk 40,0,1\nQk 41,2,3\nQmvaa 8,120,72\ns_add_u32 m0, s96, 148480\ns_add_u32 s95, s87, 86016\n"
"Qe 4\nQmvaa 8,124,76\nbuffer_load_dwordx4 v61, s[52:55], s95 offen lds\nQcvaa 12,140,76,56\nQe 5\nQmvaa 12,136,72\nQe 6\nQmvaa 12,132,68\n"
"Qe 7\nQmvaa 12,128,64\nQk 42,4,5\nQk 43,6,7\n.Lpg12:\ns_cbranch_execz .Ltr6\nv_smfmac_f32_16x16x64_bf16 a[192:195], a[204:207], v[32:39], v77\ns_add_u32 s95, s87, 105472\n"
"Qmvva 144,44,144\nbuffer_load_dwordx4 v61, s[52:55], s95 offen offset:1024 lds\nQmvva 144,36,148\nQmvva 148,44,152\nQe 8\nQmvva 148,36,156\ns_add_u32 m0, s96, 65536\ns_add_u32 s95, s87, 126976\n"
"Qmvva 152,44,160\nbuffer_load_dwordx4 v61, s[52:55], s95 offen lds\nQmvva 152,36,164\nQe 9\nQmvva 156,44,168\nQe 10\nQmvva 156,36,172\nQe 11\n"
"Qcvaa 0,112,80,56\ns_add_u32 s95, s87, 146432\nQk 44,8,9\nQk 45,10,11\nQmvaa 0,116,84\nbuffer_load_dwordx4 v61, s[52:55], s95 offen offset:1024 lds\nQmvaa 0,120,88\nQe 12\n"
"Qmvaa 0,124,92\nQe 13\nQcvaa 4,140,92,56\nQe 14\nQmvaa 4,136,88\ns_add_u32 m0, s96, 73728\ns_add_u32 s95, s87, 86016\nQe 15\n"
"Qmvaa 4,132,84\nbuffer_load_dwordx4 v62, s[52:55], s95 offen lds\nQmvaa 4,128,80\nQk 46,12,13\nQk 47,14,15\nQmvva 176,48,144\nQmvva 176,40,148\nQmvva 180,48,152\n"
"s_add_u32 s95, s87, 125952\nQmvva 180,40,156\nbuffer_load_dwordx4 v62, s[52:55], s95 offen offset:1024 lds\nQmvva 184,48,160\nQe 0\nQmvva 184,40,164\nQe 1\nQmvva 188,48,168\n"
"Qmvva 188,40,172\ns_add_u32 m0, s96, 81920\ns_add_u32 s95, s87, 86016\nQe 2\nQcvaa 8,112,96,56\nbuffer_load_dwordx4 v63, s[52:55], s95 offen lds\nQk 48,0,1\nQmvaa 8,116,100\n"
"Qmvaa 8,120,104\nQmvaa 8,124,108\nQcvaa 12,140,108,56\ns_add_u32 s95, s87, 105472\nQmvaa 12,136,104\nbuffer_load_dwordx4 v63, s[52:55], s95 offen offset:1024 lds\nQmvaa 12,132,100\nQmvaa 12,128,96\n"
"v_smfmac_f32_16x16x64_bf16 a[196:199], a[204:207], v[40:47], v77\nQmvva 208,16,144\nQta 112,71,34048\ns_add_u32 m0, s96, 90112\ns_add_u32 s95, s87, 126976\nQmvva 208,44,148\nQta 114,71,36096\nbuffer_load_dwordx4 v63, s[52:55], s95 offen lds\n"
"Qe 3\nQmvva 212,16,152\nQta 116,71,34304\nQk 49,2,3\nQmvva 212,44,156\nQta 118,71,36352\nQe 4\nQmvva 216,16,160\n"
"Qta 120,71,34560\nQe 5\nQmvva 216,44,164\nQta 122,71,36608\ns_add_u32 s95, s87, 146432\nQe 6\nQmvva 220,16,168\nQta 124,71,34816\n"
"buffer_load_dwordx4 v63, s[52:55], s95 offen offset:1024 lds\nQe 7\nQmvva 220,44,172\nQta 126,71,36864\nQk 50,4,5\nQk 51,6,7\nQe 8\nQe 9\n"
"Qe 10\nQe 11\nQk 52,8,9\nQk 53,10,11\nQe 12\nQe 13\nQmvva 240,20,144\nQe 14\n"
"Qmvva 240,48,148\nQmvva 244,20,152\nQmvva 244,48,156\nQk 54,12,13\nQmvva 248,20,160\nQmvva 248,48,164\nQmvva 252,20,168\nQe 15\n"
"Qmvva 252,48,172\nQk 55,14,15\ns_nop 1\nv_smfmac_f32_16x16x64_bf16 a[200:203], a[204:207], v[48:55], v77\nQmava 224,24,144\nv_accvgpr_read_b32 v0, a176\nv_accvgpr_read_b32 v1, a177\nQmava 224,52,148\n"
"v_accvgpr_read_b32 v6, a178\nv_accvgpr_read_b32 v7, a179\nQmava 228,24,152\nv_accvgpr_read_b32 v8, a180\nv_accvgpr_read_b32 v9, a181\nQmava 228,52,156\nv_accvgpr_read_b32 v14, a182\nv_accvgpr_read_b32 v15, a183\n"
"Qmava 232,24,160\nv_accvgpr_read_b32 v16, a184\nv_accvgpr_read_b32 v17, a185\nQmava 232,52,164\nv_accvgpr_read_b32 v22, a186\nv_accvgpr_read_b32 v23, a187\nQmava 236,24,168\nv_accvgpr_read_b32 v2, a190\n"
"v_accvgpr_read_b32 v3, a191\nQmava 236,52,172\ns_waitcnt lgkmcnt(0)\nv_accvgpr_read_b32 v24, a188\nQmvva 96,28,112\nv_accvgpr_read_b32 v25, a189\nv_accvgpr_read_b32 v4, a192\nQmvva 100,28,116\n"
"v_accvgpr_read_b32 v5, a193\nv_accvgpr_read_b32 v10, a194\nQmvva 104,28,120\nv_accvgpr_read_b32 v11, a195\nv_accvgpr_read_b32 v12, a196\nQmvva 108,28,124\nv_accvgpr_read_b32 v13, a197\nv_accvgpr_read_b32 v18, a198\n"
"Qmvva 128,32,112\nv_accvgpr_read_b32 v19, a199\nv_accvgpr_read_b32 v20, a200\nQmvva 132,32,116\nv_accvgpr_read_b32 v21, a201\nv_accvgpr_read_b32 v26, a202\nQmvva 136,32,120\nv_accvgpr_read_b32 v27, a203\n"
"Qa 8,8,9\nQmvva 140,32,124\nQa 10,10,11\nQa 8,8,10\nQmvva 160,36,112\nQa 16,16,17\nQa 18,18,19\nQmvva 164,36,116\n"
"Qa 16,16,18\nQa 4,4,5\nQmvva 168,36,120\nQa 6,6,7\nQa 4,4,6\nQmvva 172,36,124\nQa 12,12,13\nQa 14,14,15\n"
"Qmvva 192,40,112\nQa 12,12,14\nQa 0,0,1\nQmvva 196,40,116\nQa 2,2,3\nQa 0,0,2\nQmvva 200,40,120\nQa 24,24,25\n"
"Qa 26,26,27\nQmvva 204,40,124\nQa 24,24,26\nQa 20,20,21\nQmvva 224,44,112\nQa 22,22,23\nQa 20,20,22\nQmvva 228,44,116\n"
"v_max3_u32 v9, v8, v16, v4\nv_max3_u32 v9, v9, v12, v0\nQmvva 232,44,120\nv_max3_u32 v9, v9, v24, v20\nv_min3_u32 v11, v8, v16, v4\nQmvva 236,44,124\nv_min3_u32 v11, v11, v12, v0\nv_min3_u32 v11, v11, v24, v20\n"
"Qmava 208,48,112\nQmava 212,48,116\nQmava 216,48,120\nQmava 220,48,124\nQmava 240,52,112\nQmava 244,52,116\nQmava 248,52,120\nQmava 252,52,124\n"
"v_cmp_lt_u32 vcc, 0x44800000, v9\ns_mov_b64 s[74:75], vcc\nv_cmp_gt_u32 vcc, 0x2b800000, v11\ns_or_b64 s[74:75], s[74:75], vcc\ns_cmp_lg_u64 s[74:75], 0\ns_cbranch_scc1 .Lfb\n.Lfbret1:\ns_mov_b32 s56, %8\n"
"s_and_b32 s57, %9, 0xffff\ns_mov_b32 s58, 1835008\ns_lshl_b32 s68, %19, 4\ns_cmp_eq_u32 s70, 0\ns_cselect_b32 s68, s68, -1\nv_mov_b32 v77, 0x7ffff000\nv_cmp_eq_u32 vcc, s68, v60\ns_nop 1\n"
"v_cndmask_b32 v77, v77, v76, vcc\ns_lshl_b32 s68, s93, 12\ns_add_u32 s68, s68, %10\nv_lshrrev_b32 v66, 9, v60\nv_lshlrev_b32 v66, 11, v66\nv_bfe_u32 v67, v60, 1, 8\nv_add3_u32 v68, v66, v67, s68\nv_add_u32 v69, 65536, v68\n"
"v_add_u32 v70, 131072, v68\ns_mov_b64 exec, 0\ns_cmp_eq_u32 s70, 0\ns_cbranch_scc1 .Lpg16\ns_cmp_eq_u32 s70, 1\ns_cbranch_scc1 .Lpg17\ns_cmp_eq_u32 s70, 2\ns_cbranch_scc1 .Lpg18\n"
"s_branch .Lpg19\n.Ltr8:\ns_mov_b64 exec, -1\nv_rcp_f32 v2, v0\nv_rcp_f32 v6, v4\nv_rcp_f32 v10, v8\nv_rcp_f32 v14, v12\nv_rcp_f32 v18, v16\n"
"v_rcp_f32 v22, v20\nv_rcp_f32 v26, v24\nv_and_b32 v66, 16, v60\nv_mov_b32 v67, 0xffff\nv_lshlrev_b32 v66, v66, v67\ns_mov_b32 vcc_lo, 0x00c0000c\ns_mov_b32 vcc_hi, 0xc0000c00\nv_cndmask_b32 v67, 0, v66, vcc\n"
"s_lshr_b64 vcc, vcc, 2\nv_cndmask_b32 v66, 0, v66, vcc\nv_mov_b64 v[0:1], 0\nQk 2,2,2\nv_and_b32 v3, v2, v67\nv_and_b32 v2, v2, v66\nv_mov_b64 v[4:5], 0\nQk 6,6,6\n"
"v_and_b32 v7, v6, v67\nv_and_b32 v6, v6, v66\nv_mov_b64 v[8:9], 0\nQk 10,10,10\nv_and_b32 v11, v10, v67\nv_and_b32 v10, v10, v66\nv_mov_b64 v[12:13], 0\nQk 14,14,14\n"
"v_and_b32 v15, v14, v67\nv_and_b32 v14, v14, v66\nv_mov_b64 v[16:17], 0\nQk 18,18,18\nv_and_b32 v19, v18, v67\nv_and_b32 v18, v18, v66\nv_mov_b64 v[20:21], 0\nQk 22,22,22\n"
"v_and_b32 v23, v22, v67\nv_and_b32 v22, v22, v66\nv_mov_b64 v[24:25], 0\nQk 26,26,26\nv_and_b32 v27, v26, v67\nv_and_b32 v26, v26, v66\nv_mov_b64 v[28:29], 0\nQk 80,80,81\n"
"Qk 81,82,83\nQk 82,84,85\nQk 83,86,87\ns_nop 1\nQzvvv 84,80,0\nQzvvv 80,80,2\nQk 112,112,113\nQk 113,114,115\n"
"Qk 114,116,117\nQk 115,118,119\ns_nop 1\nQzvvv 116,112,4\nQzvvv 112,112,6\nQk 144,144,145\nQk 145,146,147\nQk 146,148,149\n"
"Qk 147,150,151\nQk 80,80,81\nQk 81,82,83\nQzvvv 148,144,8\nQk 82,84,85\nQk 83,86,87\nQzvvv 144,144,10\nQk 176,176,177\n"
"Qk 177,178,179\nQk 178,180,181\nQk 179,182,183\nQk 112,112,113\nQk 113,114,115\nQzvvv 180,176,12\nQk 114,116,117\nQk 115,118,119\n"
"Qzvvv 176,176,14\nQk 208,208,209\nQk 209,210,211\nQk 210,212,213\nQk 211,214,215\nQk 144,144,145\nQk 145,146,147\nQzvvv 212,208,16\n"
"Qk 146,148,149\nQk 147,150,151\nQzvvv 208,208,18\nQk 240,240,241\nQk 241,242,243\nQk 242,244,245\nQk 243,246,247\nQk 176,176,177\n"
"Qk 177,178,179\nQzvvv 244,240,20\nQk 178,180,181\nQk 179,182,183\nQzvvv 240,240,22\nv_accvgpr_read_b32 v84, a224\nv_accvgpr_read_b32 v85, a225\nv_accvgpr_read_b32 v86, a226\n"
"v_accvgpr_read_b32 v87, a227\nv_accvgpr_read_b32 v32, a228\nv_accvgpr_read_b32 v33, a229\nv_accvgpr_read_b32 v34, a230\nv_accvgpr_read_b32 v35, a231\nQk 84,84,85\nQk 85,86,87\nQk 86,32,33\n"
"Qk 87,34,35\nQk 208,208,209\nQk 209,210,211\nQzvvv 32,84,24\nQk 210,212,213\nQk 211,214,215\nQzvvv 84,84,26\nQk 88,88,89\n"
"Qk 89,90,91\nQk 90,92,93\nQk 91,94,95\nQk 240,240,241\nQk 241,242,243\nQzvvv 92,88,0\nQk 242,244,245\nQk 243,246,247\n"
"Qzvvv 88,88,2\nQk 120,120,121\nQk 121,122,123\nQk 122,124,125\nQk 123,126,127\nQk 84,84,85\nQk 85,86,87\nQzvvv 124,120,4\n"
"Qk 86,32,33\nQk 87,34,35\nQzvvv 120,120,6\nQk 152,152,153\nQk 153,154,155\nQk 154,156,157\nQk 155,158,159\nQk 88,88,89\n"
"Qk 89,90,91\nQzvvv 156,152,8\nQk 90,92,93\nQk 91,94,95\nQzvvv 152,152,10\ns_waitcnt vmcnt(20)\ns_barrier\nQk 184,184,185\n"
"Qk 185,186,187\nQk 186,188,189\nQk 187,190,191\nQk 120,120,121\nQk 121,122,123\nQzvvv 188,184,12\nQk 122,124,125\nQk 123,126,127\n"
"Qzvvv 184,184,14\nQta 56,68,0\nQk 216,216,217\nQk 217,218,219\nQk 218,220,221\nQk 219,222,223\nQk 152,152,153\nQk 153,154,155\n"
"Qzvvv 220,216,16\nQk 154,156,157\nQk 155,158,159\nQzvvv 216,216,18\nQta 58,68,1024\nQk 248,248,249\nQk 249,250,251\nQk 250,252,253\n"
"Qk 251,254,255\nQk 184,184,185\nQk 185,186,187\nQzvvv 252,248,20\nQk 186,188,189\nQk 187,190,191\nQzvvv 248,248,22\nQta 60,68,8192\n"
"v_accvgpr_read_b32 v116, a232\nv_accvgpr_read_b32 v117, a233\nv_accvgpr_read_b32 v118, a234\nv_accvgpr_read_b32 v119, a235\nv_accvgpr_read_b32 v36, a236\nv_accvgpr_read_b32 v37, a237\nv_accvgpr_read_b32 v38, a238\nv_accvgpr_read_b32 v39, a239\n"
"Qk 116,116,117\nQk 117,118,119\nQk 118,36,37\nQk 119,38,39\nQk 216,216,217\nQk 217,218,219\nQzvvv 36,116,24\nQk 218,220,221\n"
"Qk 219,222,223\nQzvvv 116,116,26\nQta 62,68,9216\ns_mov_b32 s31, 0\nQk 96,96,97\nQk 97,98,99\nQk 98,100,101\nQk 99,102,103\n"
"Qk 248,248,249\nQk 249,250,251\nQzvvv 100,96,0\nQk 250,252,253\nQk 251,254,255\nQzvvv 96,96,2\nbuffer_load_dwordx4 a[0:3], v60, s[48:51], s31 offen offset:0\nQta 64,68,256\n"
"Qk 128,128,129\nQk 129,130,131\nQk 130,132,133\nQk 131,134,135\nQk 116,116,117\nQk 117,118,119\nQzvvv 132,128,4\nQk 118,36,37\n"
"Qk 119,38,39\nQzvvv 128,128,6\nbuffer_load_dwordx4 a[4:7], v60, s[48:51], s31 offen offset:1024\nQta 66,68,1280\ns_waitcnt lgkmcnt(2)\nQzvav 56,56,80\nQmvav 56,60,88\nQk 160,160,161\n"
"Qk 161,162,163\nQk 162,164,165\nQk 163,166,167\nQk 96,96,97\nQk 97,98,99\nQzvvv 164,160,8\nQk 98,100,101\nQk 99,102,103\n"
"Qzvvv 160,160,10\nbuffer_load_dwordx4 a[8:11], v60, s[48:51], s31 offen offset:2048\nQta 68,68,8448\nQk 192,192,193\nQk 193,194,195\nQk 194,196,197\nQk 195,198,199\nQk 128,128,129\n"
"Qk 129,130,131\nQzvvv 196,192,12\nQk 130,132,133\nQk 131,134,135\nQzvvv 192,192,14\nbuffer_load_dwordx4 a[12:15], v60, s[48:51], s31 offen offset:3072\nQta 70,68,9472\nv_max3_f32 v66, |v56|, |v57|, |v58|\n"
"v_max_f32 v66, v66, |v59|\nv_mov_b32 v67, v66\ns_mov_b32 s31, 4096\nQk 224,224,225\nQk 225,226,227\nQk 226,228,229\nQk 227,230,231\nQk 160,160,161\n"
"Qk 161,162,163\nQzvvv 228,224,16\nQk 162,164,165\nQk 163,166,167\nQzvvv 224,224,18\nbuffer_load_dwordx4 a[16:19], v60, s[48:51], s31 offen offset:0\nv_permlane32_swap_b32 v66, v67\nv_max_f32 v66, v66, v67\n"
"v_mov_b32 v67, v66\nv_accvgpr_read_b32 v148, a208\nv_accvgpr_read_b32 v149, a209\nv_accvgpr_read_b32 v150, a210\nv_accvgpr_read_b32 v151, a211\nv_accvgpr_read_b32 v36, a212\nv_accvgpr_read_b32 v37, a213\nv_accvgpr_read_b32 v38, a214\n"
"v_accvgpr_read_b32 v39, a215\nQk 148,148,149\nQk 149,150,151\nQk 150,36,37\nQk 151,38,39\nQk 192,192,193\nQk 193,194,195\nQzvvv 36,148,20\n"
"Qk 194,196,197\nQk 195,198,199\nQzvvv 148,148,22\nbuffer_load_dwordx4 a[20:23], v60, s[48:51], s31 offen offset:1024\nv_permlane16_swap_b32 v66, v67\nv_max_f32 v66, v66, v67\nv_accvgpr_read_b32 v180, a240\nv_accvgpr_read_b32 v181, a241\n"
"v_accvgpr_read_b32 v182, a242\nv_accvgpr_read_b32 v183, a243\nv_accvgpr_read_b32 v40, a244\nv_accvgpr_read_b32 v41, a245\nv_accvgpr_read_b32 v42, a246\nv_accvgpr_read_b32 v43, a247\nQk 180,180,181\nQk 181,182,183\n"
"Qk 182,40,41\nQk 183,42,43\nQk 224,224,225\nQk 225,226,227\nQzvvv 40,180,24\nQk 226,228,229\nQk 227,230,231\nQzvvv 180,180,26\n"
"buffer_load_dwordx4 a[24:27], v60, s[48:51], s31 offen offset:2048\nv_max_f32_dpp v66, v66, v66 row_ror:8 row_mask:0xf bank_mask:0xf\ns_mov_b32 s31, 14336\nQk 104,104,105\nQk 105,106,107\nQk 106,108,109\nQk 107,110,111\nQk 148,148,149\n"
"Qk 149,150,151\nQzvvv 108,104,0\nQk 150,36,37\nQk 151,38,39\nQzvvv 104,104,2\nbuffer_load_dwordx4 a[28:31], v60, s[48:51], s31 offen offset:0\nv_max_f32_dpp v66, v66, v66 row_ror:4 row_mask:0xf bank_mask:0xf\nQk 136,136,137\n"
"Qk 137,138,139\nQk 138,140,141\n.Lpg13:\ns_cbranch_execz .Ltr6\nQk 139,142,143\nQk 180,180,181\nQk 181,182,183\nQzvvv 140,136,4\n"
"Qk 182,40,41\nQk 183,42,43\nQzvvv 136,136,6\nbuffer_load_dwordx4 a[32:35], v60, s[48:51], s31 offen offset:1024\nv_max_f32_dpp v66, v66, v66 row_ror:2 row_mask:0xf bank_mask:0xf\nQk 168,168,169\nQk 169,170,171\nQk 170,172,173\n"
"Qk 171,174,175\nQk 104,104,105\nQk 105,106,107\nQzvvv 172,168,8\nQk 106,108,109\nQk 107,110,111\nQzvvv 168,168,10\nbuffer_load_dwordx4 a[36:39], v60, s[48:51], s31 offen offset:2048\n"
"v_max_f32_dpp v66, v66, v66 row_ror:1 row_mask:0xf bank_mask:0xf\nv_and_b32 v66, 0x7f800000, v66\nv_add_u32 v66, 0x2000000, v66\nQk 200,200,201\nQk 201,202,203\nQk 202,204,205\nQk 203,206,207\nQk 136,136,137\n"
"Qk 137,138,139\nQzvvv 204,200,12\nQk 138,140,141\nQk 139,142,143\nQzvvv 200,200,14\nbuffer_load_dwordx4 a[40:43], v60, s[48:51], s31 offen offset:3072\nv_min_u32 v66, 0x7e000000, v66\nv_or_b32 v66, 0x400000, v66\n"
"ds_write_b32 v73, v66 offset:16\nv_xor_b32 v67, 0x80000000, v66\ns_mov_b32 s31, 18432\nQk 232,232,233\nQk 233,234,235\nQk 234,236,237\nQk 235,238,239\nQk 168,168,169\n"
"Qk 169,170,171\nQzvvv 236,232,16\nQk 170,172,173\nQk 171,174,175\nQzvvv 232,232,18\nbuffer_load_dwordx4 a[44:47], v60, s[48:51], s31 offen offset:0\nv_accvgpr_write_b32 a112, v67\nv_accvgpr_write_b32 a113, v67\n"
"v_accvgpr_write_b32 a114, v67\nv_accvgpr_write_b32 a115, v67\nv_accvgpr_read_b32 v212, a216\nv_accvgpr_read_b32 v213, a217\nv_accvgpr_read_b32 v214, a218\nv_accvgpr_read_b32 v215, a219\nv_accvgpr_read_b32 v40, a220\nv_accvgpr_read_b32 v41, a221\n"
"v_accvgpr_read_b32 v42, a222\nv_accvgpr_read_b32 v43, a223\nQk 212,212,213\nQk 213,214,215\nQk 214,40,41\nQk 215,42,43\nQk 200,200,201\nQk 201,202,203\n"
"Qzvvv 40,212,20\nQk 202,204,205\nQk 203,206,207\nQzvvv 212,212,22\nbuffer_load_dwordx4 a[48:51], v60, s[48:51], s31 offen offset:1024\nv_accvgpr_read_b32 v244, a248\nv_accvgpr_read_b32 v245, a249\nv_accvgpr_read_b32 v246, a250\n"
"v_accvgpr_read_b32 v247, a251\nv_accvgpr_read_b32 v32, a252\nv_accvgpr_read_b32 v33, a253\nv_accvgpr_read_b32 v34, a254\nv_accvgpr_read_b32 v35, a255\nQk 244,244,245\nQk 245,246,247\nQk 246,32,33\n"
"Qk 247,34,35\nQk 232,232,233\nQk 233,234,235\nQzvvv 32,244,24\nQk 234,236,237\nQk 235,238,239\nQzvvv 244,244,26\nbuffer_load_dwordx4 a[52:55], v60, s[48:51], s31 offen offset:2048\n"
"Qk 212,212,213\nQk 213,214,215\nQk 214,40,41\nQk 215,42,43\ns_nop 2\nQk 244,244,245\nQk 245,246,247\nQk 246,32,33\n"
"Qk 247,34,35\ns_waitcnt lgkmcnt(5)\nQcaav 140,56,84,112\ns_add_u32 m0, s96, 98304\ns_mov_b32 s95, 86016\nQmaav 140,60,116\nbuffer_load_dwordx4 v61, s[52:55], s95 offen lds\nQcaav 136,56,240,112\n"
"Qmaav 136,60,248\nQcaav 132,56,208,112\nQta 72,68,16384\ns_mov_b32 s95, 105472\nQmaav 132,60,216\nbuffer_load_dwordx4 v61, s[52:55], s95 offen offset:1024 lds\nQcaav 128,56,176,112\nQmaav 128,60,184\n"
"Qta 74,68,16896\nQcaav 124,56,144,112\nQmaav 124,60,152\nQcaav 120,56,112,112\nQta 76,68,17408\nQmaav 120,60,120\ns_add_u32 s88, s71, 0\nQcaav 116,56,80,112\n"
"buffer_store_dwordx4 v[80:83], v60, s[56:59], s88 offen offset:0 sc1\nQmaav 116,60,88\nQta 78,68,17920\ns_waitcnt lgkmcnt(5)\nQcaav 144,64,80,112\nQmaav 144,68,88\nQcaav 148,64,112,112\nQmaav 148,68,120\n"
"Qcaav 152,64,144,112\nQta 56,68,24576\nQmaav 152,68,152\nQcaav 156,64,176,112\nQmaav 156,68,184\nQta 58,68,25600\ns_add_u32 m0, s96, 106496\ns_mov_b32 s95, 126976\n"
"Qcaav 160,64,208,112\nbuffer_load_dwordx4 v61, s[52:55], s95 offen lds\nQmaav 160,68,216\nQcaav 164,64,240,112\nQta 60,68,32768\nQmaav 164,68,248\ns_mov_b32 s95, 146432\nQcaav 168,64,84,112\n"
"buffer_load_dwordx4 v61, s[52:55], s95 offen offset:1024 lds\nQmaav 168,68,116\nQta 62,68,33792\ns_waitcnt lgkmcnt(4)\nQcaav 196,72,84,112\nQmaav 196,76,116\nQcaav 192,72,240,112\nQmaav 192,76,248\n"
"Qcaav 188,72,208,112\nQta 64,68,24832\nQmaav 188,76,216\nbuffer_store_dwordx4 v[112:115], v60, s[56:59], s88 offen offset:1024 sc1\nQcaav 184,72,176,112\nQmaav 184,76,184\nQta 66,68,25856\nQcaav 180,72,144,112\n"
"Qmaav 180,76,152\nQcaav 176,72,112,112\nQta 68,68,33024\nv_add_u32 v75, 1, v75\nQmaav 176,76,120\ns_waitcnt vmcnt(34)\nds_write_b32 v73, v75\nQcaav 172,72,80,112\n"
"Qmaav 172,76,88\nQta 70,68,34048\ns_waitcnt lgkmcnt(5)\nQcaav 200,56,80,112\ns_add_u32 m0, s96, 114688\ns_mov_b32 s95, 86016\nQmaav 200,60,88\nbuffer_load_dwordx4 v62, s[52:55], s95 offen lds\n"
"Qcaav 204,56,112,112\nQmaav 204,60,120\nQcaav 208,56,144,112\nQta 72,68,512\nQmaav 208,60,152\nds_read_b32 v66, v74\ns_mov_b32 s95, 125952\nQcaav 212,56,176,112\n"
"buffer_load_dwordx4 v62, s[52:55], s95 offen offset:1024 lds\nQmaav 212,60,184\nQta 74,68,1536\nQcaav 216,56,208,112\nQmaav 216,60,216\ns_waitcnt lgkmcnt(1)\nv_cmp_gt_u32 vcc, v75, v66\nQcaav 220,56,240,112\n"
"s_cbranch_vccnz .Lso40\n.Lro40:\nQmaav 220,60,248\nQta 76,68,8704\nQcaav 224,56,84,112\nbuffer_store_dwordx4 v[144:147], v60, s[56:59], s88 offen offset:2048 sc1\nv_xor_b32 v56, 8, v73\nQmaav 224,60,116\n"
"ds_read_b32 v56, v56 offset:16\ns_waitcnt lgkmcnt(5)\nQcaav 252,64,84,112\nQta 78,68,9728\nQmaav 252,68,116\nQcaav 248,64,240,112\nQmaav 248,68,248\nQcaav 244,64,208,112\n"
"Qta 56,68,768\nQmaav 244,68,216\nQcaav 240,64,176,112\ns_waitcnt lgkmcnt(2)\nv_mov_b32 v57, v56\nv_mov_b32 v58, v56\nQmaav 240,68,184\nQta 58,68,1792\n"
"s_add_u32 m0, s96, 122880\ns_mov_b32 s95, 86016\nQcaav 236,64,144,112\nbuffer_load_dwordx4 v63, s[52:55], s95 offen lds\nQmaav 236,68,152\nv_mov_b32 v59, v56\nv_accvgpr_write_b32 a80, v56\nQcaav 232,64,112,112\n"
"Qta 60,68,8960\nv_accvgpr_write_b32 a81, v56\nQmaav 232,68,120\ns_mov_b32 s95, 105472\nv_accvgpr_write_b32 a82, v56\nQcaav 228,64,80,112\nbuffer_load_dwordx4 v63, s[52:55], s95 offen offset:1024 lds\nQmaav 228,68,88\n"
"Qta 62,68,9984\nv_accvgpr_write_b32 a83, v56\ns_waitcnt lgkmcnt(4)\nQcvav 0,72,80,56\nQmvav 0,76,88\nQcvav 4,72,112,56\nbuffer_store_dwordx4 v[176:179], v60, s[56:59], s88 offen offset:3072 sc1\nQmvav 4,76,120\n"
"Qcvav 8,72,144,56\nQta 64,68,16640\nQmvav 8,76,152\nQcvav 12,72,176,56\nQmvav 12,76,184\nQta 66,68,17152\nQcvav 16,72,208,56\nQmvav 16,76,216\n"
"Qcvav 20,72,240,56\nQta 68,68,17664\nQmvav 20,76,248\nQcvav 24,72,84,56\nQmvav 24,76,116\nQta 70,68,18176\ns_waitcnt lgkmcnt(4)\nQcvav 52,56,84,56\n"
"Qmvav 52,60,116\nQcvav 48,56,240,56\nQmvav 48,60,248\nQcvav 44,56,208,56\nQta 72,68,25088\nQmvav 44,60,216\nQcvav 40,56,176,56\nQmvav 40,60,184\n"
"Qta 74,68,26112\ns_add_u32 s88, s71, 4096\nQcvav 36,56,144,56\nbuffer_store_dwordx4 v[208:211], v60, s[56:59], s88 offen offset:0 sc1\nQmvav 36,60,152\nQcvav 32,56,112,56\nQta 76,68,33280\nQmvav 32,60,120\n"
"Qcvav 28,56,80,56\nQmvav 28,60,88\nQta 78,68,34304\ns_waitcnt lgkmcnt(4)\nQcvav 92,64,80,56\nQmvav 92,68,88\nQcvav 124,64,112,56\nQmvav 124,68,120\n"
"Qcvav 156,64,144,56\nQta 56,68,25344\nQmvav 156,68,152\nQcvav 188,64,176,56\nQmvav 188,68,184\nQta 58,68,26368\nQcvav 220,64,208,56\nQmvav 220,68,216\n"
"Qcvav 252,64,240,56\nQta 60,68,33536\nQmvav 252,68,248\nQcvav 100,64,84,56\nQmvav 100,68,116\nQta 62,68,34560\ns_waitcnt lgkmcnt(4)\nQcvav 172,72,84,56\n"
"buffer_store_dwordx4 v[240:243], v60, s[56:59], s88 offen offset:1024 sc1\nQmvav 172,76,116\nQcvav 140,72,240,56\nQmvav 140,76,248\nQcvav 108,72,208,56\nQta 64,68,40960\nQmvav 108,76,216\nQcvav 228,72,176,56\n"
"Qmvav 228,76,184\nQta 66,68,41984\nQcvav 196,72,144,56\nQmvav 196,76,152\nQcvav 164,72,112,56\nQta 68,68,49152\nQmvav 164,76,120\nQcvav 132,72,80,56\n"
"Qmvav 132,76,88\nQta 70,68,50176\ns_waitcnt lgkmcnt(4)\nQcvav 204,56,80,56\nQmvav 204,60,88\nQcvav 236,56,112,56\nQmvav 236,60,120\nQcaav 96,56,144,80\n"
"Qta 72,68,41216\nQmaav 96,60,152\nQcaav 100,56,176,80\nbuffer_store_dwordx4 v[84:87], v60, s[56:59], s88 offen offset:2048 sc1\nQmaav 100,60,184\nQta 74,68,42240\nQcaav 104,56,208,80\nQmaav 104,60,216\n"
"Qcaav 108,56,240,80\nQta 76,68,49408\nQmaav 108,60,248\nQcaav 112,56,84,80\nQmaav 112,60,116\nQta 78,68,50432\ns_waitcnt vmcnt(15)\ns_waitcnt lgkmcnt(4)\n"
"Qmaaa 140,64,24\nQmaaa 140,68,52\nQmaaa 136,64,20\nQmaaa 136,68,48\nQmaaa 132,64,16\nQta 56,68,41472\nQmaaa 132,68,44\nQmaaa 128,64,12\n"
"Qmaaa 128,68,40\nQta 58,68,42496\nQmaaa 124,64,8\nQmaaa 124,68,36\nQmaaa 120,64,4\nQta 60,68,49664\ns_and_b32 s68, %19, 3\nQmaaa 120,68,32\n"
"s_add_u32 s88, s71, 14336\nQmaaa 116,64,0\nbuffer_store_dwordx4 v[88:91], v60, s[56:59], s88 offen offset:0 sc1\nQmaaa 116,68,28\nQta 62,68,50688\ns_waitcnt lgkmcnt(4)\nQmaaa 144,72,0\ns_cmp_lt_u32 s68, 1\n"
"s_cselect_b32 s68, 40, 32\nQmaaa 144,76,28\ns_mul_i32 s67, s68, 40960\ns_mul_i32 s74, s68, 14336\nQmaaa 148,72,4\ns_and_b32 s68, %19, 3\nQmaaa 148,76,32\ns_cmp_lt_u32 s68, 2\n"
"s_cselect_b32 s68, 48, 40\nQmaaa 152,72,8\nQta 64,68,41728\ns_mul_i32 s72, s68, 40960\nQmaaa 152,76,36\ns_mul_i32 s75, s68, 14336\ns_and_b32 s68, %19, 3\nQmaaa 156,72,12\n"
"s_cmp_lt_u32 s68, 3\ns_cselect_b32 s68, 56, 48\nQmaaa 156,76,40\nQta 66,68,42752\ns_mul_i32 s73, s68, 40960\nQmaaa 160,72,16\ns_mul_i32 s77, s68, 14336\nv_add_u32 v58, s91, v60\n"
"Qmaaa 160,76,44\ns_mul_i32 s68, s93, 28672\ns_sub_u32 s68, 14336, s68\nQmaaa 164,72,20\nQta 68,68,49920\nv_add_u32 v59, s68, v58\nQmaaa 164,76,48\nQmaaa 168,72,24\n"
"Qmaaa 168,76,52\nQta 70,68,50944\ns_waitcnt lgkmcnt(4)\nQmvaa 24,56,24\nQmvaa 24,60,52\nQmvaa 20,56,20\nQmvaa 20,60,48\nQmvaa 16,56,16\n"
"Qta 72,68,57344\nQmvaa 16,60,44\nbuffer_store_dwordx4 v[120:123], v60, s[56:59], s88 offen offset:1024 sc1\nQmvaa 12,56,12\nQmvaa 12,60,40\nQta 74,68,57856\nQmvaa 8,56,8\nQmvaa 8,60,36\n"
"Qmvaa 4,56,4\nQta 76,68,58368\nv_add_u32 v75, 1, v75\nQmvaa 4,60,32\ns_waitcnt vmcnt(35)\nds_write_b32 v73, v75\nQmvaa 0,56,0\nQmvaa 0,60,28\n"
"Qta 78,68,58880\ns_waitcnt lgkmcnt(5)\nQmvaa 28,64,0\nQmvaa 28,68,28\nQmvaa 32,64,4\nQmvaa 32,68,32\nQmvaa 36,64,8\nQta 56,68,57600\n"
"Qmvaa 36,68,36\nds_read_b32 v66, v74\nQmvaa 40,64,12\nQmvaa 40,68,40\nQta 58,68,58112\nQmvaa 44,64,16\nQmvaa 44,68,44\ns_waitcnt lgkmcnt(1)\n"
"v_cmp_gt_u32 vcc, v75, v66\nQmvaa 48,64,20\ns_cbranch_vccnz .Lso180\n.Lro180:\nQmvaa 48,68,48\nQta 60,68,58624\nQmvaa 52,64,24\nbuffer_store_dwordx4 v[152:155], v60, s[56:59], s88 offen offset:2048 sc1\n"
"Qmvaa 52,68,52\ns_waitcnt lgkmcnt(4)\nQmaaa 196,72,24\nQta 62,68,59136\nQmaaa 196,76,52\nQmaaa 192,72,20\nQmaaa 192,76,48\nQmaaa 188,72,16\n"
"Qta 64,70,1024\nQmaaa 188,76,44\nQmaaa 184,72,12\nQmaaa 184,76,40\nQta 66,70,2048\ns_add_u32 m0, s96, 0\ns_mov_b32 s95, 126976\nQmaaa 180,72,8\n"
"buffer_load_dwordx4 v63, s[52:55], s95 offen lds\nQmaaa 180,76,36\nQmaaa 176,72,4\nQta 68,70,9216\nQmaaa 176,76,32\ns_mov_b32 s95, 146432\nQmaaa 172,72,0\nbuffer_load_dwordx4 v63, s[52:55], s95 offen offset:1024 lds\n"
"Qmaaa 172,76,28\nQta 70,70,10240\ns_waitcnt lgkmcnt(4)\nQmvaa 92,56,0\nQmvaa 92,60,28\nQmvaa 124,56,4\nbuffer_store_dwordx4 v[184:187], v60, s[56:59], s88 offen offset:3072 sc1\nQmvaa 124,60,32\n"
"Qmvaa 156,56,8\nQta 72,70,1280\nQmvaa 156,60,36\nQmvaa 188,56,12\nQmvaa 188,60,40\nQta 74,70,2304\nQmvaa 220,56,16\nQmvaa 220,60,44\n"
"Qmvaa 252,56,20\nQta 76,70,9472\nQmvaa 252,60,48\ns_add_u32 m0, s96, 8192\ns_mov_b32 s95, 331776\nQmvaa 100,56,24\nbuffer_load_dwordx4 v61, s[52:55], s95 offen lds\nQmvaa 100,60,52\n"
"Qta 78,70,10496\ns_waitcnt lgkmcnt(4)\nQmaaa 224,64,24\nQmaaa 224,68,52\ns_mov_b32 s95, 351232\nQmaaa 220,64,20\nbuffer_load_dwordx4 v61, s[52:55], s95 offen offset:1024 lds\nQmaaa 220,68,48\n"
"Qmaaa 216,64,16\nQta 56,70,1536\nQmaaa 216,68,44\nQmaaa 212,64,12\nQmaaa 212,68,40\nQta 58,70,2560\ns_add_u32 s88, s71, 18432\nQmaaa 208,64,8\n"
"buffer_store_dwordx4 v[216:219], v60, s[56:59], s88 offen offset:0 sc1\nQmaaa 208,68,36\nQmaaa 204,64,4\nQta 60,70,9728\nQmaaa 204,68,32\nQmaaa 200,64,0\nQmaaa 200,68,28\nQta 62,70,10752\n"
"s_waitcnt lgkmcnt(4)\nQmaaa 228,72,0\nQmaaa 228,76,28\nQmaaa 232,72,4\nQmaaa 232,76,32\nQmaaa 236,72,8\nQta 64,70,1792\ns_add_u32 m0, s96, 16384\n"
"s_mov_b32 s95, 372736\nQmaaa 236,76,36\nbuffer_load_dwordx4 v61, s[52:55], s95 offen lds\nQmaaa 240,72,12\nQmaaa 240,76,40\nQta 66,70,2816\nQmaaa 244,72,16\ns_mov_b32 s95, 392192\n"
"Qmaaa 244,76,44\nbuffer_load_dwordx4 v61, s[52:55], s95 offen offset:1024 lds\nQmaaa 248,72,20\nQta 68,70,9984\nQmaaa 248,76,48\nQmaaa 252,72,24\n.Lpg14:\ns_cbranch_execz .Ltr6\n"
"Qmaaa 252,76,52\nQta 70,70,11008\ns_waitcnt lgkmcnt(4)\nQmvaa 172,56,24\nbuffer_store_dwordx4 v[248:251], v60, s[56:59], s88 offen offset:1024 sc1\nQmvaa 172,60,52\nQmvaa 140,56,20\nQmvaa 140,60,48\n"
"Qmvaa 108,56,16\nQta 72,70,17408\nQmvaa 108,60,44\nQmvaa 228,56,12\nQmvaa 228,60,40\nQta 74,70,18432\nQmvaa 196,56,8\nQmvaa 196,60,36\n"
"Qmvaa 164,56,4\nQta 76,69,0\ns_add_u32 m0, s96, 24576\ns_mov_b32 s95, 331776\nQmvaa 164,60,32\nbuffer_load_dwordx4 v62, s[52:55], s95 offen lds\nQmvaa 132,56,0\nQmvaa 132,60,28\n"
"Qta 78,69,1024\ns_waitcnt lgkmcnt(4)\nQmvaa 204,64,0\ns_mov_b32 s95, 371712\nQmvaa 204,68,28\nbuffer_load_dwordx4 v62, s[52:55], s95 offen offset:1024 lds\nQmvaa 236,64,4\nQmvaa 236,68,32\n"
"Qmaaa 96,64,8\nQta 56,70,17664\nQmaaa 96,68,36\nQmaaa 100,64,12\nbuffer_store_dwordx4 v[116:119], v60, s[56:59], s88 offen offset:2048 sc1\nQmaaa 100,68,40\nQta 58,70,18688\nQmaaa 104,64,16\n"
"Qmaaa 104,68,44\nQmaaa 108,64,20\nQta 60,69,256\nQmaaa 108,68,48\nQmaaa 112,64,24\nQmaaa 112,68,52\nQta 62,69,1280\ns_waitcnt lgkmcnt(4)\n"
"Qmaav 140,72,180\nQmaav 140,76,244\ns_mov_b32 s31, 28672\nQmaav 136,72,148\nbuffer_load_dwordx4 a[0:3], v60, s[48:51], s31 offen offset:0\nQmaav 136,76,212\nQmaav 132,72,224\nQta 64,70,17920\n"
"Qmaav 132,76,232\nQmaav 128,72,192\nbuffer_load_dwordx4 a[4:7], v60, s[48:51], s31 offen offset:1024\nQmaav 128,76,200\nQta 66,70,18944\nQmaav 124,72,160\nQmaav 124,76,168\nQmaav 120,72,128\n"
"Qta 68,69,512\nQmaav 120,76,136\nbuffer_load_dwordx4 a[8:11], v60, s[48:51], s31 offen offset:2048\nQmaav 116,72,96\nQmaav 116,76,104\nQta 70,69,1536\ns_waitcnt lgkmcnt(4)\nQmaav 144,56,96\n"
"Qmaav 144,60,104\nbuffer_load_dwordx4 a[12:15], v60, s[48:51], s31 offen offset:3072\nQmaav 148,56,128\nQmaav 148,60,136\nQmaav 152,56,160\nQta 72,70,18176\ns_mov_b32 s31, 32768\nQmaav 152,60,168\n"
"buffer_load_dwordx4 a[16:19], v60, s[48:51], s31 offen offset:0\nQmaav 156,56,192\nQmaav 156,60,200\nQta 74,70,19200\nQmaav 160,56,224\nQmaav 160,60,232\nbuffer_load_dwordx4 a[20:23], v60, s[48:51], s31 offen offset:1024\nQmaav 164,56,148\n"
"Qta 76,69,768\nQmaav 164,60,212\nQmaav 168,56,180\nQmaav 168,60,244\nQta 78,69,1792\ns_waitcnt lgkmcnt(4)\nQmvav 24,64,180\nbuffer_load_dwordx4 a[24:27], v60, s[48:51], s31 offen offset:2048\n"
"Qmvav 24,68,244\nQmvav 20,64,148\nQmvav 20,68,212\nQmvav 16,64,224\nQta 56,69,8192\ns_mov_b32 s31, 43008\nQmvav 16,68,232\nbuffer_load_dwordx4 a[28:31], v60, s[48:51], s31 offen offset:0\n"
"Qmvav 12,64,192\nQmvav 12,68,200\nQta 58,69,8704\nQmvav 8,64,160\nQmvav 8,68,168\nbuffer_load_dwordx4 a[32:35], v60, s[48:51], s31 offen offset:1024\nQmvav 4,64,128\nQta 60,69,9216\n"
"v_add_u32 v75, 1, v75\nQmvav 4,68,136\ns_waitcnt vmcnt(31)\nds_write_b32 v73, v75\nQmvav 0,64,96\nQmvav 0,68,104\nQta 62,69,9728\ns_waitcnt lgkmcnt(5)\n"
"Qmvav 28,72,96\nbuffer_load_dwordx4 a[36:39], v60, s[48:51], s31 offen offset:2048\nQmvav 28,76,104\nQmvav 32,72,128\nQmvav 32,76,136\nQmvav 36,72,160\nQta 64,69,8448\nQmvav 36,76,168\n"
"ds_read_b32 v66, v74\nQmvav 40,72,192\nbuffer_load_dwordx4 a[40:43], v60, s[48:51], s31 offen offset:3072\nQmvav 40,76,200\nQta 66,69,8960\nQmvav 44,72,224\nQmvav 44,76,232\ns_waitcnt lgkmcnt(1)\n"
"v_cmp_gt_u32 vcc, v75, v66\nQmvav 48,72,148\ns_cbranch_vccnz .Lso320\n.Lro320:\nQmvav 48,76,212\nQta 68,69,9472\ns_mov_b32 s31, 47104\nQmvav 52,72,180\n"
"buffer_load_dwordx4 a[44:47], v60, s[48:51], s31 offen offset:0\nQmvav 52,76,244\ns_waitcnt lgkmcnt(4)\nQmaav 196,56,180\nQta 70,69,9984\nQmaav 196,60,244\nQmaav 192,56,148\nbuffer_load_dwordx4 a[48:51], v60, s[48:51], s31 offen offset:1024\n"
"Qmaav 192,60,212\nQmaav 188,56,224\nQta 72,69,16384\nQmaav 188,60,232\nQmaav 184,56,192\nbuffer_load_dwordx4 a[52:55], v60, s[48:51], s31 offen offset:2048\nQmaav 184,60,200\nQta 74,69,17408\n"
"Qmaav 180,56,160\nQmaav 180,60,168\nQmaav 176,56,128\nQta 76,69,24576\ns_mov_b32 s31, 114688\nQmaav 176,60,136\nbuffer_load_dwordx4 v[80:83], v60, s[48:51], s31 offen offset:0\nQmaav 172,56,96\n"
"Qmaav 172,60,104\nQta 78,69,25600\ns_waitcnt lgkmcnt(4)\nQmvav 92,64,96\nQmvav 92,68,104\nbuffer_load_dwordx4 v[112:115], v60, s[48:51], s31 offen offset:1024\nQmvav 124,64,128\nQmvav 124,68,136\n"
"Qmvav 156,64,160\nQta 56,69,16640\nQmvav 156,68,168\nbuffer_load_dwordx4 v[144:147], v60, s[48:51], s31 offen offset:2048\nQmvav 188,64,192\nQmvav 188,68,200\nQta 58,69,17664\nQmvav 220,64,224\n"
"Qmvav 220,68,232\nbuffer_load_dwordx4 v[176:179], v60, s[48:51], s31 offen offset:3072\nQmvav 252,64,148\nQta 60,69,24832\nQmvav 252,68,212\nQmvav 100,64,180\nQmvav 100,68,244\nQta 62,69,25856\n"
"s_mov_b32 s31, 118784\ns_waitcnt lgkmcnt(4)\nQmaav 224,72,180\nbuffer_load_dwordx4 v[208:211], v60, s[48:51], s31 offen offset:0\nQmaav 224,76,244\nQmaav 220,72,148\nQmaav 220,76,212\nQmaav 216,72,224\n"
"Qta 64,69,16896\nQmaav 216,76,232\nbuffer_load_dwordx4 v[240:243], v60, s[48:51], s31 offen offset:1024\nQmaav 212,72,192\nQmaav 212,76,200\nQta 66,69,17920\nQmaav 208,72,160\nQmaav 208,76,168\n"
"buffer_load_dwordx4 v[84:87], v60, s[48:51], s31 offen offset:2048\nQmaav 204,72,128\nQta 68,69,25088\nQmaav 204,76,136\nQmaav 200,72,96\nQmaav 200,76,104\nQta 70,69,26112\ns_mov_b32 s31, 129024\n"
"s_waitcnt lgkmcnt(4)\nQmaav 228,56,96\nbuffer_load_dwordx4 v[88:91], v60, s[48:51], s31 offen offset:0\nQmaav 228,60,104\nQmaav 232,56,128\nQmaav 232,60,136\nQmaav 236,56,160\nQta 72,69,17152\n"
"Qmaav 236,60,168\nbuffer_load_dwordx4 v[120:123], v60, s[48:51], s31 offen offset:1024\nQmaav 240,56,192\nQmaav 240,60,200\nQta 74,69,18176\nQmaav 244,56,224\nQmaav 244,60,232\nbuffer_load_dwordx4 v[152:155], v60, s[48:51], s31 offen offset:2048\n"
"Qmaav 248,56,148\nQta 76,69,25344\nQmaav 248,60,212\nQmaav 252,56,180\nQmaav 252,60,244\nQta 78,69,26368\ns_waitcnt lgkmcnt(4)\nQmvav 172,64,180\n"
"buffer_load_dwordx4 v[184:187], v60, s[48:51], s31 offen offset:3072\nQmvav 172,68,244\nQmvav 140,64,148\nQmvav 140,68,212\nQmvav 108,64,224\nQta 56,69,32768\ns_mov_b32 s31, 133120\nQmvav 108,68,232\n"
"buffer_load_dwordx4 v[216:219], v60, s[48:51], s31 offen offset:0\nQmvav 228,64,192\nQmvav 228,68,200\nQta 58,69,33792\nQmvav 196,64,160\nQmvav 196,68,168\nbuffer_load_dwordx4 v[248:251], v60, s[48:51], s31 offen offset:1024\nQmvav 164,64,128\n"
"Qta 60,69,40960\nQmvav 164,68,136\nQmvav 132,64,96\nQmvav 132,68,104\nQta 62,69,41984\ns_waitcnt lgkmcnt(4)\nQmvav 204,72,96\nbuffer_load_dwordx4 v[116:119], v60, s[48:51], s31 offen offset:2048\n"
"Qmvav 204,76,104\nQmvav 236,72,128\nQmvav 236,76,136\nQmaav 96,72,160\nQta 64,69,33024\ns_add_u32 m0, s96, 32768\ns_mov_b32 s95, 331776\nQmaav 96,76,168\n"
"buffer_load_dwordx4 v63, s[52:55], s95 offen lds\nQmaav 100,72,192\nQmaav 100,76,200\nQta 66,69,34048\nQmaav 104,72,224\ns_mov_b32 s95, 351232\nQmaav 104,76,232\nbuffer_load_dwordx4 v63, s[52:55], s95 offen offset:1024 lds\n"
"Qmaav 108,72,148\nQta 68,69,41216\nQmaav 108,76,212\nQmaav 112,72,180\nQmaav 112,76,244\nQta 70,69,42240\ns_add_u32 m0, s96, 40960\ns_mov_b32 s95, 372736\n"
"s_waitcnt vmcnt(16)\ns_waitcnt lgkmcnt(4)\nQmaaa 140,56,24\nbuffer_load_dwordx4 v63, s[52:55], s95 offen lds\nQmaaa 140,60,52\nQmaaa 136,56,20\nQmaaa 136,60,48\nQmaaa 132,56,16\n"
"Qta 72,69,33280\ns_mov_b32 s95, 392192\nQmaaa 132,60,44\nbuffer_load_dwordx4 v63, s[52:55], s95 offen offset:1024 lds\nQmaaa 128,56,12\nQmaaa 128,60,40\nQta 74,69,34304\nQmaaa 124,56,8\n"
"Qmaaa 124,60,36\nQmaaa 120,56,4\nQta 76,69,41472\ns_add_u32 s88, s71, 28672\nQmaaa 120,60,32\nbuffer_store_dwordx4 v[96:99], v60, s[56:59], s88 offen offset:0 sc1\nQmaaa 116,56,0\nQmaaa 116,60,28\n"
"Qta 78,69,42496\ns_waitcnt lgkmcnt(4)\nQmaaa 144,64,0\nQmaaa 144,68,28\nQmaaa 148,64,4\nQmaaa 148,68,32\nQmaaa 152,64,8\nQta 56,69,33536\n"
"Qmaaa 152,68,36\nQmaaa 156,64,12\nQmaaa 156,68,40\nQta 58,69,34560\ns_add_u32 m0, s96, 49152\ns_mov_b32 s95, 413696\nQmaaa 160,64,16\nbuffer_load_dwordx4 v61, s[52:55], s95 offen lds\n"
"Qmaaa 160,68,44\nQmaaa 164,64,20\nQta 60,69,41728\nQmaaa 164,68,48\ns_mov_b32 s95, 433152\nQmaaa 168,64,24\nbuffer_load_dwordx4 v61, s[52:55], s95 offen offset:1024 lds\nQmaaa 168,68,52\n"
"Qta 62,69,42752\ns_waitcnt lgkmcnt(4)\nQmvaa 24,72,24\nQmvaa 24,76,52\nQmvaa 20,72,20\nbuffer_store_dwordx4 v[128:131], v60, s[56:59], s88 offen offset:1024 sc1\nQmvaa 20,76,48\nQmvaa 16,72,16\n"
"Qta 64,69,49152\nQmvaa 16,76,44\nQmvaa 12,72,12\nQmvaa 12,76,40\nQta 66,69,49664\nQmvaa 8,72,8\nQmvaa 8,76,36\nQmvaa 4,72,4\n"
"Qta 68,69,50176\nv_add_u32 v75, 1, v75\nQmvaa 4,76,32\ns_waitcnt vmcnt(36)\nds_write_b32 v73, v75\ns_add_u32 m0, s96, 57344\ns_mov_b32 s95, 454656\nQmvaa 0,72,0\n"
"buffer_load_dwordx4 v61, s[52:55], s95 offen lds\nQmvaa 0,76,28\nQta 70,69,50688\ns_waitcnt lgkmcnt(5)\nQmvaa 28,56,0\nQmvaa 28,60,28\ns_mov_b32 s95, 474112\nQmvaa 32,56,4\n"
"buffer_load_dwordx4 v61, s[52:55], s95 offen offset:1024 lds\nQmvaa 32,60,32\nQmvaa 36,56,8\nQta 72,69,49408\nQmvaa 36,60,36\nds_read_b32 v66, v74\nQmvaa 40,56,12\nQmvaa 40,60,40\n"
"Qta 74,69,49920\nQmvaa 44,56,16\nbuffer_store_dwordx4 v[160:163], v60, s[56:59], s88 offen offset:2048 sc1\nv_add_u32 v67, 2, v65\nQmvaa 44,60,44\ns_waitcnt lgkmcnt(1)\nv_cmp_gt_u32 vcc, v75, v66\nQmvaa 48,56,20\n"
"s_cbranch_vccnz .Lso460\n.Lro460:\nQmvaa 48,60,48\nQta 76,69,50432\nQmvaa 52,56,24\nbuffer_store_dword v67, v77, s[24:27], 0 offen sc1\nQmvaa 52,60,52\ns_waitcnt lgkmcnt(4)\n"
"Qmaaa 196,64,24\nQta 78,69,50944\nQmaaa 196,68,52\nQmaaa 192,64,20\nQmaaa 192,68,48\nQmaaa 188,64,16\nQta 56,69,57344\ns_add_u32 m0, s96, 132096\n"
"s_mov_b32 s95, 413696\nQmaaa 188,68,44\nbuffer_load_dwordx4 v62, s[52:55], s95 offen lds\nQmaaa 184,64,12\nQmaaa 184,68,40\nQta 58,69,58368\nQmaaa 180,64,8\ns_mov_b32 s95, 453632\n"
"Qmaaa 180,68,36\nbuffer_load_dwordx4 v62, s[52:55], s95 offen offset:1024 lds\nQmaaa 176,64,4\nQta 60,68,0\nQmaaa 176,68,32\nQmaaa 172,64,0\nQmaaa 172,68,28\nQta 62,68,1024\n"
"s_waitcnt lgkmcnt(4)\nQmvaa 92,72,0\nbuffer_store_dwordx4 v[192:195], v60, s[56:59], s88 offen offset:3072 sc1\nQmvaa 92,76,28\nQmvaa 124,72,4\nQmvaa 124,76,32\nQmvaa 156,72,8\nQta 64,69,57600\n"
"Qmvaa 156,76,36\nQmvaa 188,72,12\nQmvaa 188,76,40\nQta 66,69,58624\nQmvaa 220,72,16\nQmvaa 220,76,44\nQmvaa 252,72,20\nQta 68,68,256\n"
"s_add_u32 m0, s96, 140288\ns_mov_b32 s95, 413696\nQmvaa 252,76,48\nbuffer_load_dwordx4 v63, s[52:55], s95 offen lds\nQmvaa 100,72,24\nQmvaa 100,76,52\nQta 70,68,1280\ns_waitcnt lgkmcnt(4)\n"
"Qmaaa 224,56,24\ns_mov_b32 s95, 433152\nQmaaa 224,60,52\nbuffer_load_dwordx4 v63, s[52:55], s95 offen offset:1024 lds\nQmaaa 220,56,20\nQmaaa 220,60,48\nQmaaa 216,56,16\nQta 72,69,57856\n"
"Qmaaa 216,60,44\ns_add_u32 s88, s71, 32768\nQmaaa 212,56,12\nbuffer_store_dwordx4 v[224:227], v60, s[56:59], s88 offen offset:0 sc1\nQmaaa 212,60,40\nQta 74,69,58880\nQmaaa 208,56,8\nQmaaa 208,60,36\n"
"Qmaaa 204,56,4\nQta 76,68,512\nQmaaa 204,60,32\nQmaaa 200,56,0\nQmaaa 200,60,28\nQta 78,68,1536\ns_waitcnt lgkmcnt(4)\nQmaaa 228,64,0\n"
"Qmaaa 228,68,28\ns_add_u32 m0, s96, 148480\ns_mov_b32 s95, 454656\nQmaaa 232,64,4\nbuffer_load_dwordx4 v63, s[52:55], s95 offen lds\nQmaaa 232,68,32\nQmaaa 236,64,8\nQta 56,69,58112\n"
"Qmaaa 236,68,36\ns_mov_b32 s95, 474112\nQmaaa 240,64,12\nbuffer_load_dwordx4 v63, s[52:55], s95 offen offset:1024 lds\nQmaaa 240,68,40\nQta 58,69,59136\nQmaaa 244,64,16\nQmaaa 244,68,44\n"
"Qmaaa 248,64,20\nQta 60,68,768\nQmaaa 248,68,48\nQmaaa 252,64,24\nbuffer_store_dwordx4 v[148:151], v60, s[56:59], s88 offen offset:1024 sc1\nQmaaa 252,68,52\nQta 62,68,1792\ns_waitcnt lgkmcnt(4)\n"
"Qmvaa 172,72,24\nQmvaa 172,76,52\ns_mov_b32 s40, %17\ns_and_b32 s41, %18, 0xffff\nQmvaa 140,72,20\ns_mov_b32 s42, 571200\ns_lshl_b32 s66, %19, 2\nQmvaa 140,76,48\n"
"s_add_u32 s66, s66, s70\ns_mul_i32 s98, s66, 11264\nQmvaa 108,72,16\nQta 64,68,8192\ns_add_u32 s98, s98, 1835008\nQmvaa 108,76,44\ns_xor_b32 s66, s66, 16\ns_mul_i32 s64, s66, 11264\n"
"Qmvaa 228,72,12\ns_add_u32 s64, s64, 1835008\ns_mul_i32 s66, s92, 33792\nQmvaa 228,76,40\nQta 66,68,9216\ns_add_u32 m0, s96, 65536\ns_mov_b32 s95, 659456\nQmvaa 196,72,8\n"
"buffer_load_dwordx4 v61, s[52:55], s95 offen lds\nQmvaa 196,76,36\ns_add_u32 s66, s66, %10\ns_add_u32 s66, s66, 98304\nQmvaa 164,72,4\nQta 68,68,16384\ns_mul_i32 s68, s93, 96\nQmvaa 164,76,32\n"
"s_mov_b32 s95, 678912\ns_sub_u32 s68, s66, s68\nQmvaa 132,72,0\nbuffer_load_dwordx4 v61, s[52:55], s95 offen offset:1024 lds\nQmvaa 132,76,28\nQta 70,68,17408\ns_add_u32 s68, s68, 96\ns_waitcnt lgkmcnt(4)\n"
"Qmvaa 204,56,0\nv_bfe_u32 v148, v60, 4, 4\nv_mul_u32_u24 v148, 208, v148\nQmvaa 204,60,28\nv_lshrrev_b32 v149, 8, v60\nv_lshl_add_u32 v148, v149, 3, v148\n.Lpg15:\ns_cbranch_execz .Ltr6\n"
"Qmvaa 236,56,4\nv_add_u32 v56, s68, v148\ns_lshl_b32 s68, s93, 3\nQmvaa 236,60,32\nv_lshrrev_b32 v148, 7, v60\nv_add_u32 v148, s68, v148\nQmaaa 96,56,8\nQta 72,68,8448\n"
"v_and_b32 v149, 127, v60\nQmaaa 96,60,36\nbuffer_store_dwordx4 v[180:183], v60, s[56:59], s88 offen offset:2048 sc1\nQmaaa 100,56,12\nv_and_b32 v150, 64, v60\nv_lshrrev_b32 v150, 1, v150\nQmaaa 100,60,40\nQta 74,68,9472\n"
"v_mul_u32_u24 v57, 208, v148\nQmaaa 104,56,16\nv_add3_u32 v57, v57, v149, s66\nv_add_u32 v57, v57, v150\nQmaaa 104,60,44\ns_and_b32 s65, %19, 1\ns_and_b32 s68, %19, 4\nQmaaa 108,56,20\n"
"Qta 76,68,16640\ns_lshl_b32 s68, s68, 2\nQmaaa 108,60,48\ns_lshl_b32 s69, s65, 3\ns_add_u32 s68, s68, s69\nQmaaa 112,56,24\ns_lshr_b32 s68, 0xc0004080, s68\ns_and_b32 s68, s68, 255\n"
"Qmaaa 112,60,52\nQta 78,68,17664\ns_mul_i32 s69, s92, 286720\ns_waitcnt vmcnt(24)\ns_waitcnt lgkmcnt(4)\nQmaav 140,64,84\ns_mov_b32 s31, 143360\ns_add_u32 s68, s68, s69\n"
"Qmaav 140,68,116\nbuffer_load_dwordx4 a[0:3], v60, s[48:51], s31 offen offset:0\nQmaav 136,64,240\nv_mul_u32_u24 v64, 2560, v148\nv_add3_u32 v64, v64, v149, s68\nQmaav 136,68,248\ns_lshl_b32 s68, s93, 4\nv_lshrrev_b32 v148, 6, v60\n"
"Qmaav 132,64,208\nQta 56,68,8704\nv_add_u32 v148, s68, v148\nQmaav 132,68,216\nbuffer_load_dwordx4 a[4:7], v60, s[48:51], s31 offen offset:1024\nQmaav 128,64,176\nv_and_b32 v149, 63, v60\nv_and_b32 v150, 32, v60\n"
"Qmaav 128,68,184\nQta 58,68,9728\nv_lshlrev_b32 v150, 1, v150\nQmaav 124,64,144\nv_mul_u32_u24 v71, 208, v148\nv_add3_u32 v71, v71, v149, s66\nQmaav 124,68,152\nbuffer_load_dwordx4 a[8:11], v60, s[48:51], s31 offen offset:2048\n"
"Qmaav 120,64,112\nQta 60,68,16896\nv_add_u32 v71, v71, v150\nQmaav 120,68,120\ns_xor_b32 s65, s65, 1\ns_lshl_b32 s65, s65, 8\nQmaav 116,64,80\ns_add_u32 s68, s65, s69\n"
"v_mul_u32_u24 v72, 2560, v148\nQmaav 116,68,88\nQta 62,68,17920\nv_add3_u32 v72, v72, v149, s68\ns_waitcnt lgkmcnt(4)\nQmaav 144,72,80\nbuffer_load_dwordx4 a[12:15], v60, s[48:51], s31 offen offset:3072\nQmaav 144,76,88\n"
"Qmaav 148,72,112\nQmaav 148,76,120\nQmaav 152,72,144\nQta 64,68,8960\ns_mov_b32 s31, 147456\nQmaav 152,76,152\nbuffer_load_dwordx4 a[16:19], v60, s[48:51], s31 offen offset:0\nQmaav 156,72,176\n"
"Qmaav 156,76,184\nQta 66,68,9984\nQmaav 160,72,208\nQmaav 160,76,216\nbuffer_load_dwordx4 a[20:23], v60, s[48:51], s31 offen offset:1024\nQmaav 164,72,240\nQta 68,68,17152\nQmaav 164,76,248\n"
"Qmaav 168,72,84\nQmaav 168,76,116\nQta 70,68,18176\ns_waitcnt lgkmcnt(4)\nQmvav 24,56,84\nbuffer_load_dwordx4 a[24:27], v60, s[48:51], s31 offen offset:2048\nQmvav 24,60,116\nQmvav 20,56,240\n"
"Qmvav 20,60,248\nQmvav 16,56,208\nQta 72,68,24576\ns_mov_b32 s31, 157696\nQmvav 16,60,216\nbuffer_load_dwordx4 a[28:31], v60, s[48:51], s31 offen offset:0\nQmvav 12,56,176\nQmvav 12,60,184\n"
"Qta 74,68,25088\nQmvav 8,56,144\nQmvav 8,60,152\nbuffer_load_dwordx4 a[32:35], v60, s[48:51], s31 offen offset:1024\nQmvav 4,56,112\nQta 76,68,25600\nv_add_u32 v75, 1, v75\nQmvav 4,60,120\n"
"s_waitcnt vmcnt(19)\nds_write_b32 v73, v75\nQmvav 0,56,80\nQmvav 0,60,88\nQta 78,68,26112\ns_waitcnt lgkmcnt(5)\nQmvav 28,64,80\nbuffer_load_dwordx4 a[36:39], v60, s[48:51], s31 offen offset:2048\n"
"Qmvav 28,68,88\nQmvav 32,64,112\nQmvav 32,68,120\nQmvav 36,64,144\nQta 56,68,24832\nQmvav 36,68,152\nds_read_b32 v66, v74\nQmvav 40,64,176\n"
"buffer_load_dwordx4 a[40:43], v60, s[48:51], s31 offen offset:3072\nQmvav 40,68,184\nQta 58,68,25344\nQmvav 44,64,208\nQmvav 44,68,216\ns_waitcnt lgkmcnt(1)\nv_cmp_gt_u32 vcc, v75, v66\nQmvav 48,64,240\n"
"s_cbranch_vccnz .Lso600\n.Lro600:\nQmvav 48,68,248\nQta 60,68,25856\ns_mov_b32 s31, 161792\nQmvav 52,64,84\nbuffer_load_dwordx4 a[44:47], v60, s[48:51], s31 offen offset:0\nQmvav 52,68,116\n"
"s_waitcnt lgkmcnt(4)\nQmaav 196,72,84\nQta 62,68,26368\nQmaav 196,76,116\nQmaav 192,72,240\nbuffer_load_dwordx4 a[48:51], v60, s[48:51], s31 offen offset:1024\nQmaav 192,76,248\nQmaav 188,72,208\n"
"Qta 64,68,32768\nQmaav 188,76,216\nQmaav 184,72,176\nbuffer_load_dwordx4 a[52:55], v60, s[48:51], s31 offen offset:2048\nQmaav 184,76,184\nQta 66,68,33792\nQmaav 180,72,144\nQmaav 180,76,152\n"
"Qmaav 176,72,112\nQta 68,68,40960\ns_add_u32 m0, s96, 73728\ns_mov_b32 s95, 700416\nQmaav 176,76,120\nbuffer_load_dwordx4 v61, s[52:55], s95 offen lds\nQmaav 172,72,80\nQmaav 172,76,88\n"
"Qta 70,68,41984\ns_waitcnt lgkmcnt(4)\nQmvav 92,56,80\ns_mov_b32 s95, 719872\nQmvav 92,60,88\nbuffer_load_dwordx4 v61, s[52:55], s95 offen offset:1024 lds\nQmvav 124,56,112\nQmvav 124,60,120\n"
"Qmvav 156,56,144\nQta 72,68,33024\ns_add_u32 m0, s96, 81920\ns_mov_b32 s95, 659456\nQmvav 156,60,152\nbuffer_load_dwordx4 v62, s[52:55], s95 offen lds\nQmvav 188,56,176\nQmvav 188,60,184\n"
"Qta 74,68,34048\nQmvav 220,56,208\ns_mov_b32 s95, 699392\nQmvav 220,60,216\nbuffer_load_dwordx4 v62, s[52:55], s95 offen offset:1024 lds\nQmvav 252,56,240\nQta 76,68,41216\nQmvav 252,60,248\n"
"Qmvav 100,56,84\nQmvav 100,60,116\nQta 78,68,42240\ns_add_u32 m0, s96, 90112\ns_mov_b32 s95, 659456\ns_waitcnt lgkmcnt(4)\nQmaav 224,64,84\nbuffer_load_dwordx4 v63, s[52:55], s95 offen lds\n"
"Qmaav 224,68,116\nQmaav 220,64,240\nQmaav 220,68,248\nQmaav 216,64,208\nQta 56,68,33280\ns_mov_b32 s95, 678912\nQmaav 216,68,216\nbuffer_load_dwordx4 v63, s[52:55], s95 offen offset:1024 lds\n"
"Qmaav 212,64,176\nQmaav 212,68,184\nQta 58,68,34304\nQmaav 208,64,144\ns_add_u32 m0, s96, 98304\ns_mov_b32 s95, 700416\nQmaav 208,68,152\nbuffer_load_dwordx4 v63, s[52:55], s95 offen lds\n"
"Qmaav 204,64,112\nQta 60,68,41472\nQmaav 204,68,120\nQmaav 200,64,80\nQmaav 200,68,88\nQta 62,68,42496\ns_waitcnt lgkmcnt(4)\nQmaav 228,72,80\n"
"Qmaav 228,76,88\ns_add_u32 s88, s71, 43008\nQmaav 232,72,112\nbuffer_store_dwordx4 v[104:107], v60, s[56:59], s88 offen offset:0 sc1\nQmaav 232,76,120\nQmaav 236,72,144\nQta 64,68,33536\nQmaav 236,76,152\n"
"Qmaav 240,72,176\nQmaav 240,76,184\nQta 66,68,34560\nQmaav 244,72,208\nQmaav 244,76,216\nQmaav 248,72,240\nQta 68,68,41728\nQmaav 248,76,248\n"
"Qmaav 252,72,84\nQmaav 252,76,116\nQta 70,68,42752\ns_waitcnt lgkmcnt(4)\nQmvav 172,56,84\nbuffer_store_dwordx4 v[136:139], v60, s[56:59], s88 offen offset:1024 sc1\nQmvav 172,60,116\nQmvav 140,56,240\n"
"Qmvav 140,60,248\nQmvav 108,56,208\nQta 72,68,49152\nQmvav 108,60,216\nQmvav 228,56,176\nQmvav 228,60,184\nQta 74,68,50176\nQmvav 196,56,144\n"
"Qmvav 196,60,152\nQmvav 164,56,112\nQta 76,68,57344\ns_mov_b32 s95, 719872\nQmvav 164,60,120\nbuffer_load_dwordx4 v63, s[52:55], s95 offen offset:1024 lds\nQmvav 132,56,80\nQmvav 132,60,88\n"
"Qta 78,68,58368\ns_waitcnt lgkmcnt(4)\nQmvav 204,64,80\ns_mov_b32 s31, 229376\nQmvav 204,68,88\nbuffer_load_dwordx4 v[80:83], v60, s[48:51], s31 offen offset:0\nQmvav 236,64,112\nQmvav 236,68,120\n"
"Qmaav 96,64,144\nQta 56,68,49408\nQmaav 96,68,152\nbuffer_load_dwordx4 v[112:115], v60, s[48:51], s31 offen offset:1024\nQmaav 100,64,176\nQmaav 100,68,184\nQta 58,68,50432\nQmaav 104,64,208\n"
"Qmaav 104,68,216\nbuffer_load_dwordx4 v[144:147], v60, s[48:51], s31 offen offset:2048\nQmaav 108,64,240\nQta 60,68,57600\nQmaav 108,68,248\nQmaav 112,64,84\nQmaav 112,68,116\nQta 62,68,58624\n"
"s_waitcnt vmcnt(13)\ns_waitcnt lgkmcnt(4)\nQmaaa 140,72,24\nbuffer_load_dwordx4 v[176:179], v60, s[48:51], s31 offen offset:3072\nQmaaa 140,76,52\nQmaaa 136,72,20\nQmaaa 136,76,48\nQmaaa 132,72,16\n"
"Qta 64,68,49664\ns_mov_b32 s31, 233472\nQmaaa 132,76,44\nbuffer_load_dwordx4 v[208:211], v60, s[48:51], s31 offen offset:0\nQmaaa 128,72,12\nQmaaa 128,76,40\nQta 66,68,50688\nQmaaa 124,72,8\n"
"Qmaaa 124,76,36\nbuffer_load_dwordx4 v[240:243], v60, s[48:51], s31 offen offset:1024\nQmaaa 120,72,4\nQta 68,68,57856\nQmaaa 120,76,32\nQmaaa 116,72,0\nQmaaa 116,76,28\nQta 70,68,58880\n"
"s_waitcnt lgkmcnt(4)\nQmaaa 144,56,0\nbuffer_load_dwordx4 v[84:87], v60, s[48:51], s31 offen offset:2048\nQmaaa 144,60,28\nQmaaa 148,56,4\nQmaaa 148,60,32\nQmaaa 152,56,8\nQta 72,68,49920\n"
"s_mov_b32 s31, 243712\nQmaaa 152,60,36\nbuffer_load_dwordx4 v[88:91], v60, s[48:51], s31 offen offset:0\nQmaaa 156,56,12\nQmaaa 156,60,40\nQta 74,68,50944\nQmaaa 160,56,16\nQmaaa 160,60,44\n"
"buffer_load_dwordx4 v[120:123], v60, s[48:51], s31 offen offset:1024\nQmaaa 164,56,20\nQta 76,68,58112\nQmaaa 164,60,48\nQmaaa 168,56,24\nQmaaa 168,60,52\nQta 78,68,59136\ns_waitcnt lgkmcnt(4)\n"
"Qmvaa 24,64,24\nbuffer_load_dwordx4 v[152:155], v60, s[48:51], s31 offen offset:2048\nQmvaa 24,68,52\nQmvaa 20,64,20\nQmvaa 20,68,48\nQmvaa 16,64,16\nQta 56,70,1024\nQmvaa 16,68,44\n"
"buffer_load_dwordx4 v[184:187], v60, s[48:51], s31 offen offset:3072\nQmvaa 12,64,12\nQmvaa 12,68,40\nQta 58,70,1536\nQmvaa 8,64,8\ns_mov_b32 s31, 247808\nQmvaa 8,68,36\nbuffer_load_dwordx4 v[216:219], v60, s[48:51], s31 offen offset:0\n"
"Qmvaa 4,64,4\nQta 60,70,2048\nv_add_u32 v75, 1, v75\nQmvaa 4,68,32\ns_waitcnt vmcnt(18)\nds_write_b32 v73, v75\nQmvaa 0,64,0\nQmvaa 0,68,28\n"
"Qta 62,70,2560\ns_waitcnt lgkmcnt(5)\nQmvaa 28,72,0\nbuffer_load_dwordx4 v[248:251], v60, s[48:51], s31 offen offset:1024\nQmvaa 28,76,28\nQmvaa 32,72,4\nQmvaa 32,76,32\nQmvaa 36,72,8\n"
"Qta 64,70,1280\nQmvaa 36,76,36\nds_read_b32 v66, v74\nQmvaa 40,72,12\nbuffer_load_dwordx4 v[116:119], v60, s[48:51], s31 offen offset:2048\nQmvaa 40,76,40\nQta 66,70,1792\nQmvaa 44,72,16\n"
"Qmvaa 44,76,44\ns_waitcnt lgkmcnt(1)\nv_cmp_gt_u32 vcc, v75, v66\nQmvaa 48,72,20\ns_cbranch_vccnz .Lso740\n.Lro740:\nQmvaa 48,76,48\nQta 68,70,2304\n"
"s_add_u32 m0, s96, 106496\ns_mov_b32 s95, 741376\nQmvaa 52,72,24\nbuffer_load_dwordx4 v61, s[52:55], s95 offen lds\nQmvaa 52,76,52\ns_waitcnt lgkmcnt(4)\nQmaaa 196,56,24\nQta 70,70,2816\n"
"Qmaaa 196,60,52\ns_mov_b32 s95, 760832\nQmaaa 192,56,20\nbuffer_load_dwordx4 v61, s[52:55], s95 offen offset:1024 lds\nQmaaa 192,60,48\nQmaaa 188,56,16\nQta 72,70,9216\nQmaaa 188,60,44\n"
"s_add_u32 m0, s96, 114688\ns_mov_b32 s95, 782336\nQmaaa 184,56,12\nbuffer_load_dwordx4 v61, s[52:55], s95 offen lds\nQmaaa 184,60,40\nQta 74,70,10240\nQmaaa 180,56,8\nQmaaa 180,60,36\n"
"Qmaaa 176,56,4\nQta 76,70,17408\ns_mov_b32 s95, 801792\nQmaaa 176,60,32\nbuffer_load_dwordx4 v61, s[52:55], s95 offen offset:1024 lds\nQmaaa 172,56,0\nQmaaa 172,60,28\nQta 78,70,18432\n"
"s_waitcnt lgkmcnt(4)\nQmvaa 92,64,0\ns_add_u32 m0, s96, 122880\ns_mov_b32 s95, 741376\nQmvaa 92,68,28\nbuffer_load_dwordx4 v62, s[52:55], s95 offen lds\nQmvaa 124,64,4\nQmvaa 124,68,32\n"
"Qmvaa 156,64,8\nQta 56,70,9472\ns_mov_b32 s95, 781312\nQmvaa 156,68,36\nbuffer_load_dwordx4 v62, s[52:55], s95 offen offset:1024 lds\nQmvaa 188,64,12\nQmvaa 188,68,40\nQta 58,70,10496\n"
"Qmvaa 220,64,16\ns_add_u32 m0, s96, 0\ns_mov_b32 s95, 741376\nQmvaa 220,68,44\nbuffer_load_dwordx4 v63, s[52:55], s95 offen lds\nQmvaa 252,64,20\nQta 60,70,17664\nQmvaa 252,68,48\n"
"Qmvaa 100,64,24\nQmvaa 100,68,52\nQta 62,70,18688\ns_mov_b32 s95, 760832\ns_waitcnt lgkmcnt(4)\nQmaaa 224,72,24\nbuffer_load_dwordx4 v63, s[52:55], s95 offen offset:1024 lds\nQmaaa 224,76,52\n"
"Qmaaa 220,72,20\nQmaaa 220,76,48\nQmaaa 216,72,16\nQta 64,70,9728\ns_add_u32 m0, s96, 8192\ns_mov_b32 s95, 782336\nQmaaa 216,76,44\nbuffer_load_dwordx4 v63, s[52:55], s95 offen lds\n"
"Qmaaa 212,72,12\nQmaaa 212,76,40\nQta 66,70,10752\nQmaaa 208,72,8\ns_mov_b32 s95, 801792\nQmaaa 208,76,36\nbuffer_load_dwordx4 v63, s[52:55], s95 offen offset:1024 lds\nQmaaa 204,72,4\n"
"Qta 68,70,17920\nQmaaa 204,76,32\nQmaaa 200,72,0\nQmaaa 200,76,28\nQta 70,70,18944\ns_waitcnt lgkmcnt(4)\nQmaaa 228,56,0\nQmaaa 228,60,28\n"
"Qmaaa 232,56,4\nbuffer_store_dwordx4 v[168:171], v60, s[56:59], s88 offen offset:2048 sc1\nQmaaa 232,60,32\nQmaaa 236,56,8\nQta 72,70,9984\nQmaaa 236,60,36\nQmaaa 240,56,12\nQmaaa 240,60,40\n"
"Qta 74,70,11008\nQmaaa 244,56,16\nQmaaa 244,60,44\nQmaaa 248,56,20\nQta 76,70,18176\nQmaaa 248,60,48\nQmaaa 252,56,24\nQmaaa 252,60,52\n"
"Qta 78,70,19200\ns_waitcnt lgkmcnt(4)\nQmvaa 172,64,24\nbuffer_store_dwordx4 v[200:203], v60, s[56:59], s88 offen offset:3072 sc1\nQmvaa 172,68,52\nQmvaa 140,64,20\nQmvaa 140,68,48\nQmvaa 108,64,16\n"
"Qta 56,69,0\nQmvaa 108,68,44\nQmvaa 228,64,12\nQmvaa 228,68,40\nQta 58,69,1024\nQmvaa 196,64,8\nQmvaa 196,68,36\nQmvaa 164,64,4\n"
"Qta 60,69,8192\ns_add_u32 m0, s96, 16384\ns_mov_b32 s95, 987136\nQmvaa 164,68,32\nbuffer_load_dwordx4 v61, s[52:55], s95 offen lds\nQmvaa 132,64,0\nQmvaa 132,68,28\nQta 62,69,9216\n"
"s_waitcnt lgkmcnt(4)\nQmvaa 204,72,0\ns_mov_b32 s95, 1006592\nQmvaa 204,76,28\nbuffer_load_dwordx4 v61, s[52:55], s95 offen offset:1024 lds\nQmvaa 236,72,4\nQmvaa 236,76,32\nQmaaa 96,72,8\n"
"Qta 64,69,256\ns_mov_b32 s31, 258048\nQmaaa 96,76,36\nbuffer_load_dwordx4 a[0:3], v60, s[48:51], s31 offen offset:0\nQmaaa 100,72,12\nQmaaa 100,76,40\nQta 66,69,1280\nQmaaa 104,72,16\n"
"Qmaaa 104,76,44\nbuffer_load_dwordx4 a[4:7], v60, s[48:51], s31 offen offset:1024\nQmaaa 108,72,20\nQta 68,69,8448\nQmaaa 108,76,48\n.Lpg16:\ns_cbranch_execz .Lpg20\nQmaaa 112,72,24\n"
"Qmaaa 112,76,52\nQta 70,69,9472\ns_waitcnt vmcnt(16)\ns_waitcnt lgkmcnt(4)\nQmaav 140,56,84\nbuffer_load_dwordx4 a[8:11], v60, s[48:51], s31 offen offset:2048\nQmaav 140,60,116\nQmaav 136,56,240\n"
"Qmaav 136,60,248\nQmaav 132,56,208\nQta 72,69,512\nQmaav 132,60,216\nbuffer_load_dwordx4 a[12:15], v60, s[48:51], s31 offen offset:3072\nQmaav 128,56,176\nQmaav 128,60,184\nQta 74,69,1536\n"
"Qmaav 124,56,144\ns_mov_b32 s31, 262144\nQmaav 124,60,152\nbuffer_load_dwordx4 a[16:19], v60, s[48:51], s31 offen offset:0\nQmaav 120,56,112\nQta 76,69,8704\nQmaav 120,60,120\nQmaav 116,56,80\n"
"Qmaav 116,60,88\nQta 78,69,9728\ns_waitcnt lgkmcnt(4)\nQmaav 144,64,80\nbuffer_load_dwordx4 a[20:23], v60, s[48:51], s31 offen offset:1024\nQmaav 144,68,88\nQmaav 148,64,112\nQmaav 148,68,120\n"
"Qmaav 152,64,144\nQta 56,69,768\nQmaav 152,68,152\nbuffer_load_dwordx4 a[24:27], v60, s[48:51], s31 offen offset:2048\nQmaav 156,64,176\nQmaav 156,68,184\nQta 58,69,1792\nQmaav 160,64,208\n"
"s_mov_b32 s31, 272384\nQmaav 160,68,216\nbuffer_load_dwordx4 a[28:31], v60, s[48:51], s31 offen offset:0\nQmaav 164,64,240\nQta 60,69,8960\nQmaav 164,68,248\nQmaav 168,64,84\nQmaav 168,68,116\n"
"Qta 62,69,9984\ns_waitcnt lgkmcnt(4)\nQmvav 24,72,84\nbuffer_load_dwordx4 a[32:35], v60, s[48:51], s31 offen offset:1024\nQmvav 24,76,116\nQmvav 20,72,240\nQmvav 20,76,248\nQmvav 16,72,208\n"
"Qta 64,69,16384\nQmvav 16,76,216\nbuffer_load_dwordx4 a[36:39], v60, s[48:51], s31 offen offset:2048\nQmvav 12,72,176\nQmvav 12,76,184\nQta 66,69,16896\nQmvav 8,72,144\nQmvav 8,76,152\n"
"buffer_load_dwordx4 a[40:43], v60, s[48:51], s31 offen offset:3072\nQmvav 4,72,112\nQta 68,69,17408\nv_add_u32 v75, 1, v75\nQmvav 4,76,120\ns_waitcnt vmcnt(19)\nds_write_b32 v73, v75\nQmvav 0,72,80\n"
"Qmvav 0,76,88\nQta 70,69,17920\ns_mov_b32 s31, 276480\ns_waitcnt lgkmcnt(5)\nQmvav 28,56,80\nbuffer_load_dwordx4 a[44:47], v60, s[48:51], s31 offen offset:0\nQmvav 28,60,88\nQmvav 32,56,112\n"
"Qmvav 32,60,120\nQmvav 36,56,144\nQta 72,69,16640\nQmvav 36,60,152\nds_read_b32 v66, v74\nQmvav 40,56,176\nbuffer_load_dwordx4 a[48:51], v60, s[48:51], s31 offen offset:1024\nQmvav 40,60,184\n"
"Qta 74,69,17152\nQmvav 44,56,208\nQmvav 44,60,216\ns_waitcnt lgkmcnt(1)\nv_cmp_gt_u32 vcc, v75, v66\nQmvav 48,56,240\ns_cbranch_vccnz .Lso880\n.Lro880:\n"
"Qmvav 48,60,248\nQta 76,69,17664\nQmvav 52,56,84\nbuffer_load_dwordx4 a[52:55], v60, s[48:51], s31 offen offset:2048\nQmvav 52,60,116\ns_waitcnt lgkmcnt(4)\nQmaav 196,64,84\nQta 78,69,18176\n"
"Qmaav 196,68,116\ns_add_u32 m0, s96, 24576\ns_mov_b32 s95, 1028096\nQmaav 192,64,240\nbuffer_load_dwordx4 v61, s[52:55], s95 offen lds\nQmaav 192,68,248\nQmaav 188,64,208\nQta 56,69,24576\n"
"Qmaav 188,68,216\ns_mov_b32 s95, 1047552\nQmaav 184,64,176\nbuffer_load_dwordx4 v61, s[52:55], s95 offen offset:1024 lds\nQmaav 184,68,184\nQta 58,69,25600\nQmaav 180,64,144\nQmaav 180,68,152\n"
"Qmaav 176,64,112\nQta 60,69,32768\ns_add_u32 m0, s96, 32768\ns_mov_b32 s95, 987136\nQmaav 176,68,120\nbuffer_load_dwordx4 v62, s[52:55], s95 offen lds\nQmaav 172,64,80\nQmaav 172,68,88\n"
"Qta 62,69,33792\ns_waitcnt lgkmcnt(4)\nQmvav 92,72,80\ns_mov_b32 s95, 1027072\nQmvav 92,76,88\nbuffer_load_dwordx4 v62, s[52:55], s95 offen offset:1024 lds\nQmvav 124,72,112\nQmvav 124,76,120\n"
"Qmvav 156,72,144\nQta 64,69,24832\ns_add_u32 m0, s96, 40960\ns_mov_b32 s95, 987136\nQmvav 156,76,152\nbuffer_load_dwordx4 v63, s[52:55], s95 offen lds\nQmvav 188,72,176\nQmvav 188,76,184\n"
"Qta 66,69,25856\nQmvav 220,72,208\ns_mov_b32 s95, 1006592\nQmvav 220,76,216\nbuffer_load_dwordx4 v63, s[52:55], s95 offen offset:1024 lds\nQmvav 252,72,240\nQta 68,69,33024\nQmvav 252,76,248\n"
"Qmvav 100,72,84\nQmvav 100,76,116\nQta 70,69,34048\ns_add_u32 m0, s96, 49152\ns_mov_b32 s95, 1028096\ns_waitcnt lgkmcnt(4)\nQmaav 224,56,84\nbuffer_load_dwordx4 v63, s[52:55], s95 offen lds\n"
"Qmaav 224,60,116\nQmaav 220,56,240\nQmaav 220,60,248\nQmaav 216,56,208\nQta 72,69,25088\ns_mov_b32 s95, 1047552\nQmaav 216,60,216\nbuffer_load_dwordx4 v63, s[52:55], s95 offen offset:1024 lds\n"
"Qmaav 212,56,176\nQmaav 212,60,184\nQta 74,69,26112\nQmaav 208,56,144\ns_add_u32 m0, s96, 57344\ns_mov_b32 s95, 1069056\nQmaav 208,60,152\nbuffer_load_dwordx4 v61, s[52:55], s95 offen lds\n"
"Qmaav 204,56,112\nQta 76,69,33280\nQmaav 204,60,120\nQmaav 200,56,80\nQmaav 200,60,88\nQta 78,69,34304\ns_waitcnt lgkmcnt(4)\nQmaav 228,64,80\n"
"Qmaav 228,68,88\ns_add_u32 s88, s71, 47104\nQmaav 232,64,112\nbuffer_store_dwordx4 v[232:235], v60, s[56:59], s88 offen offset:0 sc1\nQmaav 232,68,120\nQmaav 236,64,144\nQta 56,69,25344\nQmaav 236,68,152\n"
"Qmaav 240,64,176\nQmaav 240,68,184\nQta 58,69,26368\nQmaav 244,64,208\nQmaav 244,68,216\nQmaav 248,64,240\nQta 60,69,33536\nQmaav 248,68,248\n"
"Qmaav 252,64,84\nQmaav 252,68,116\nQta 62,69,34560\ns_waitcnt lgkmcnt(4)\nQmvav 172,72,84\nbuffer_store_dwordx4 v[212:215], v60, s[56:59], s88 offen offset:1024 sc1\nQmvav 172,76,116\nQmvav 140,72,240\n"
"Qmvav 140,76,248\nQmvav 108,72,208\nQta 64,69,40960\nQmvav 108,76,216\nQmvav 228,72,176\nQmvav 228,76,184\nQta 66,69,41984\nQmvav 196,72,144\n"
"Qmvav 196,76,152\nQmvav 164,72,112\nQta 68,69,49152\ns_mov_b32 s95, 1088512\nQmvav 164,76,120\nbuffer_load_dwordx4 v61, s[52:55], s95 offen offset:1024 lds\nQmvav 132,72,80\nQmvav 132,76,88\n"
"Qta 70,69,50176\ns_waitcnt lgkmcnt(4)\nQmvav 204,56,80\ns_mov_b32 s31, 344064\nQmvav 204,60,88\nbuffer_load_dwordx4 v[80:83], v60, s[48:51], s31 offen offset:0\nQmvav 236,56,112\nQmvav 236,60,120\n"
"Qmaav 96,56,144\nQta 72,69,41216\nQmaav 96,60,152\nbuffer_load_dwordx4 v[112:115], v60, s[48:51], s31 offen offset:1024\nQmaav 100,56,176\nQmaav 100,60,184\nQta 74,69,42240\nQmaav 104,56,208\n"
"Qmaav 104,60,216\nbuffer_load_dwordx4 v[144:147], v60, s[48:51], s31 offen offset:2048\nQmaav 108,56,240\nQta 76,69,49408\nQmaav 108,60,248\nQmaav 112,56,84\nv_accvgpr_read_b32 v96, a96\nv_accvgpr_read_b32 v97, a97\n"
"Qmaav 112,60,116\nQta 78,69,50432\nv_accvgpr_read_b32 v98, a98\ns_waitcnt vmcnt(15)\ns_waitcnt lgkmcnt(4)\nQmaaa 140,64,24\nbuffer_load_dwordx4 v[176:179], v60, s[48:51], s31 offen offset:3072\nQmaaa 140,68,52\n"
"v_accvgpr_read_b32 v99, a99\nv_accvgpr_read_b32 v128, a100\nQmaaa 136,64,20\nv_accvgpr_read_b32 v129, a101\nv_accvgpr_read_b32 v130, a102\nQmaaa 136,68,48\nv_accvgpr_read_b32 v131, a103\nv_accvgpr_read_b32 v160, a104\n"
"Qmaaa 132,64,16\nQta 56,69,41472\ns_mov_b32 s31, 348160\nQmaaa 132,68,44\nbuffer_load_dwordx4 v[208:211], v60, s[48:51], s31 offen offset:0\nQmaaa 128,64,12\nv_accvgpr_read_b32 v161, a105\nv_accvgpr_read_b32 v162, a106\n"
"Qmaaa 128,68,40\nQta 58,69,42496\nv_accvgpr_read_b32 v163, a107\nQmaaa 124,64,8\nv_accvgpr_read_b32 v192, a108\nv_accvgpr_read_b32 v193, a109\nQmaaa 124,68,36\nbuffer_load_dwordx4 v[240:243], v60, s[48:51], s31 offen offset:1024\n"
"Qmaaa 120,64,4\nQta 60,69,49664\nv_accvgpr_read_b32 v194, a110\nQmaaa 120,68,32\nv_accvgpr_read_b32 v195, a111\nv_accvgpr_read_b32 v224, a112\nQmaaa 116,64,0\nv_accvgpr_read_b32 v225, a113\n"
"v_accvgpr_read_b32 v226, a114\nQmaaa 116,68,28\nQta 62,69,50688\nv_accvgpr_read_b32 v227, a115\ns_waitcnt lgkmcnt(4)\nQmaaa 144,72,0\nbuffer_load_dwordx4 v[84:87], v60, s[48:51], s31 offen offset:2048\nQmaaa 144,76,28\n"
"Qmaaa 148,72,4\nQmaaa 148,76,32\nQmaaa 152,72,8\nQta 64,69,41728\ns_mov_b32 s31, 358400\nQmaaa 152,76,36\nbuffer_load_dwordx4 v[88:91], v60, s[48:51], s31 offen offset:0\nQmaaa 156,72,12\n"
"Qmaaa 156,76,40\nQta 66,69,42752\nQmaaa 160,72,16\nQmaaa 160,76,44\nbuffer_load_dwordx4 v[120:123], v60, s[48:51], s31 offen offset:1024\nQmaaa 164,72,20\nQta 68,69,49920\nQmaaa 164,76,48\n"
"Qmaaa 168,72,24\nQmaaa 168,76,52\nQta 70,69,50944\ns_waitcnt lgkmcnt(4)\nQmvaa 24,56,24\nbuffer_load_dwordx4 v[152:155], v60, s[48:51], s31 offen offset:2048\nQmvaa 24,60,52\nQmvaa 20,56,20\n"
"Qmvaa 20,60,48\nQmvaa 16,56,16\nQta 72,69,57344\nQmvaa 16,60,44\nbuffer_load_dwordx4 v[184:187], v60, s[48:51], s31 offen offset:3072\nQmvaa 12,56,12\nQmvaa 12,60,40\nQta 74,69,57856\n"
"Qmvaa 8,56,8\ns_mov_b32 s31, 362496\nQmvaa 8,60,36\nbuffer_load_dwordx4 v[216:219], v60, s[48:51], s31 offen offset:0\nQmvaa 4,56,4\nQta 76,69,58368\nv_add_u32 v75, 1, v75\nQmvaa 4,60,32\n"
"s_waitcnt vmcnt(20)\nds_write_b32 v73, v75\nQmvaa 0,56,0\nQmvaa 0,60,28\nQta 78,69,58880\ns_waitcnt lgkmcnt(5)\nQmvaa 28,64,0\nbuffer_load_dwordx4 v[248:251], v60, s[48:51], s31 offen offset:1024\n"
"Qmvaa 28,68,28\nQmvaa 32,64,4\nQmvaa 32,68,32\nQmvaa 36,64,8\nQta 56,69,57600\nQmvaa 36,68,36\nds_read_b32 v66, v74\nQmvaa 40,64,12\n"
"buffer_load_dwordx4 v[116:119], v60, s[48:51], s31 offen offset:2048\nQmvaa 40,68,40\nQta 58,69,58112\nQmvaa 44,64,16\nQmvaa 44,68,44\ns_waitcnt lgkmcnt(1)\nv_cmp_gt_u32 vcc, v75, v66\nQmvaa 48,64,20\n"
"s_cbranch_vccnz .Lso1020\n.Lro1020:\nQmvaa 48,68,48\nQta 60,69,58624\ns_add_u32 m0, s96, 132096\ns_mov_b32 s95, 1110016\nQmvaa 52,64,24\nbuffer_load_dwordx4 v61, s[52:55], s95 offen lds\n"
"Qmvaa 52,68,52\ns_waitcnt lgkmcnt(4)\nQmaaa 196,72,24\nQta 62,69,59136\nQmaaa 196,76,52\ns_mov_b32 s95, 1129472\nQmaaa 192,72,20\nbuffer_load_dwordx4 v61, s[52:55], s95 offen offset:1024 lds\n"
"Qmaaa 192,76,48\nQmaaa 188,72,16\nQta 64,68,0\nQmaaa 188,76,44\ns_add_u32 m0, s96, 140288\ns_mov_b32 s95, 1069056\nQmaaa 184,72,12\nbuffer_load_dwordx4 v62, s[52:55], s95 offen lds\n"
"Qmaaa 184,76,40\nQta 66,68,1024\nQmaaa 180,72,8\nQmaaa 180,76,36\nQmaaa 176,72,4\nQta 68,68,8192\ns_mov_b32 s95, 1108992\nQmaaa 176,76,32\n"
"buffer_load_dwordx4 v62, s[52:55], s95 offen offset:1024 lds\nQmaaa 172,72,0\nQmaaa 172,76,28\nQta 70,68,9216\ns_waitcnt lgkmcnt(4)\nQmvaa 92,56,0\ns_add_u32 m0, s96, 148480\ns_mov_b32 s95, 1069056\n"
"Qmvaa 92,60,28\nbuffer_load_dwordx4 v63, s[52:55], s95 offen lds\nQmvaa 124,56,4\nQmvaa 124,60,32\nQmvaa 156,56,8\nQta 72,68,256\ns_mov_b32 s95, 1088512\nQmvaa 156,60,36\n"
"buffer_load_dwordx4 v63, s[52:55], s95 offen offset:1024 lds\nQmvaa 188,56,12\nQmvaa 188,60,40\nQta 74,68,1280\nQmvaa 220,56,16\ns_add_u32 m0, s96, 65536\ns_mov_b32 s95, 1110016\nQmvaa 220,60,44\n"
"buffer_load_dwordx4 v63, s[52:55], s95 offen lds\nQmvaa 252,56,20\nQta 76,68,8448\nQmvaa 252,60,48\nQmvaa 100,56,24\nQmvaa 100,60,52\nQta 78,68,9472\ns_mov_b32 s95, 1129472\n"
"s_waitcnt lgkmcnt(4)\nQmaaa 224,64,24\nbuffer_load_dwordx4 v63, s[52:55], s95 offen offset:1024 lds\nQmaaa 224,68,52\nQmaaa 220,64,20\nQmaaa 220,68,48\nQmaaa 216,64,16\nQta 56,68,512\n"
"s_add_u32 m0, s96, 73728\ns_add_u32 s95, s67, 4096\nQmaaa 216,68,44\nbuffer_load_dwordx4 v61, s[52:55], s95 offen lds\nQmaaa 212,64,12\nQmaaa 212,68,40\nQta 58,68,1536\nQmaaa 208,64,8\n"
"s_add_u32 s95, s67, 23552\nQmaaa 208,68,36\nbuffer_load_dwordx4 v61, s[52:55], s95 offen offset:1024 lds\nQmaaa 204,64,4\nQta 60,68,8704\nQmaaa 204,68,32\nQmaaa 200,64,0\nQmaaa 200,68,28\n"
"Qta 62,68,9728\ns_waitcnt lgkmcnt(4)\nQmaaa 228,72,0\nQmaaa 228,76,28\nQmaaa 232,72,4\nbuffer_store_dwordx4 v[244:247], v60, s[56:59], s88 offen offset:2048 sc1\nQmaaa 232,76,32\nQmaaa 236,72,8\n"
"Qta 64,68,768\nQmaaa 236,76,36\nQmaaa 240,72,12\nQmaaa 240,76,40\nQta 66,68,1792\nQmaaa 244,72,16\nQmaaa 244,76,44\nQmaaa 248,72,20\n"
"Qta 68,68,8960\nQmaaa 248,76,48\ns_add_u32 m0, s96, 81920\ns_add_u32 s95, s67, 45056\nQmaaa 252,72,24\nbuffer_load_dwordx4 v61, s[52:55], s95 offen lds\nQmaaa 252,76,52\nQta 70,68,9984\n"
"s_waitcnt lgkmcnt(4)\nQmvaa 172,56,24\nQmvaa 172,60,52\ns_add_u32 s95, s67, 64512\nQmvaa 140,56,20\nbuffer_load_dwordx4 v61, s[52:55], s95 offen offset:1024 lds\nQmvaa 140,60,48\nQmvaa 108,56,16\n"
"Qta 72,68,16384\nQmvaa 108,60,44\ns_add_u32 m0, s96, 90112\ns_add_u32 s95, s67, 4096\nQmvaa 228,56,12\nbuffer_load_dwordx4 v62, s[52:55], s95 offen lds\nQmvaa 228,60,40\nQta 74,68,17408\n"
"Qmvaa 196,56,8\nQmvaa 196,60,36\nQmvaa 164,56,4\nQta 76,68,24576\ns_add_u32 s95, s67, 44032\nQmvaa 164,60,32\nbuffer_load_dwordx4 v62, s[52:55], s95 offen offset:1024 lds\nQmvaa 132,56,0\n"
"Qmvaa 132,60,28\nQta 78,68,25600\ns_waitcnt lgkmcnt(4)\nQmvaa 204,64,0\nbuffer_load_dword v78, v76, s[24:27], 0 offen sc1\ns_mov_b32 s31, 372736\nQmvaa 204,68,28\nbuffer_load_dwordx4 a[0:3], v60, s[48:51], s31 offen offset:0\n"
"Qmvaa 236,64,4\nQmvaa 236,68,32\nQmvaa 96,64,8\nQta 56,68,16640\nQmvaa 96,68,36\nbuffer_load_dwordx4 a[4:7], v60, s[48:51], s31 offen offset:1024\nQmvaa 128,64,12\nQmvaa 128,68,40\n"
"Qta 58,68,17664\nQmvaa 160,64,16\nQmvaa 160,68,44\nbuffer_load_dwordx4 a[8:11], v60, s[48:51], s31 offen offset:2048\nQmvaa 192,64,20\nQta 60,68,24832\nQmvaa 192,68,48\nQmvaa 224,64,24\n"
"Qmvaa 224,68,52\nQta 62,68,25856\ns_waitcnt vmcnt(19)\ns_waitcnt lgkmcnt(4)\nQmaav 140,72,84\nbuffer_load_dwordx4 a[12:15], v60, s[48:51], s31 offen offset:3072\nQmaav 140,76,116\nQmaav 136,72,240\n"
"Qmaav 136,76,248\nQmaav 132,72,208\n.Lpg17:\ns_cbranch_execz .Lpg21\nQta 64,68,16896\ns_mov_b32 s31, 376832\nQmaav 132,76,216\nbuffer_load_dwordx4 a[16:19], v60, s[48:51], s31 offen offset:0\n"
"Qmaav 128,72,176\nQmaav 128,76,184\nQta 66,68,17920\nQmaav 124,72,144\nQmaav 124,76,152\nbuffer_load_dwordx4 a[20:23], v60, s[48:51], s31 offen offset:1024\nQmaav 120,72,112\nQta 68,68,25088\n"
"Qmaav 120,76,120\nQmaav 116,72,80\nQmaav 116,76,88\nQta 70,68,26112\ns_waitcnt lgkmcnt(4)\nQmaav 144,56,80\nbuffer_load_dwordx4 a[24:27], v60, s[48:51], s31 offen offset:2048\nQmaav 144,60,88\n"
"Qmaav 148,56,112\nQmaav 148,60,120\nQmaav 152,56,144\nQta 72,68,17152\ns_mov_b32 s31, 387072\nQmaav 152,60,152\nbuffer_load_dwordx4 a[28:31], v60, s[48:51], s31 offen offset:0\nQmaav 156,56,176\n"
"Qmaav 156,60,184\nQta 74,68,18176\nQmaav 160,56,208\nQmaav 160,60,216\nbuffer_load_dwordx4 a[32:35], v60, s[48:51], s31 offen offset:1024\nQmaav 164,56,240\nQta 76,68,25344\nQmaav 164,60,248\n"
"Qmaav 168,56,84\nQmaav 168,60,116\nQta 78,68,26368\ns_waitcnt lgkmcnt(4)\nQmvav 24,64,84\nbuffer_load_dwordx4 a[36:39], v60, s[48:51], s31 offen offset:2048\nQmvav 24,68,116\nQmvav 20,64,240\n"
"Qmvav 20,68,248\nQmvav 16,64,208\nQta 56,68,32768\nQmvav 16,68,216\nbuffer_load_dwordx4 a[40:43], v60, s[48:51], s31 offen offset:3072\nQmvav 12,64,176\nQmvav 12,68,184\nQta 58,68,33280\n"
"Qmvav 8,64,144\ns_mov_b32 s31, 391168\nQmvav 8,68,152\nbuffer_load_dwordx4 a[44:47], v60, s[48:51], s31 offen offset:0\nQmvav 4,64,112\nQta 60,68,33792\nv_add_u32 v75, 1, v75\nQmvav 4,68,120\n"
"s_waitcnt vmcnt(24)\nds_write_b32 v73, v75\nQmvav 0,64,80\nQmvav 0,68,88\nQta 62,68,34304\ns_waitcnt lgkmcnt(5)\nQmvav 28,72,80\nbuffer_load_dwordx4 a[48:51], v60, s[48:51], s31 offen offset:1024\n"
"Qmvav 28,76,88\nQmvav 32,72,112\nQmvav 32,76,120\ns_waitcnt vmcnt(13)\nv_sub_u32 v66, v78, v65\nv_cmp_gt_u32 vcc, 2, v66\ns_and_b64 vcc, vcc, s[28:29]\ns_cbranch_scc1 .Lf2_w\n"
".Lf2_r:\nQmvav 36,72,144\nQta 64,68,33024\nQmvav 36,76,152\nds_read_b32 v66, v74\nQmvav 40,72,176\nbuffer_load_dwordx4 a[52:55], v60, s[48:51], s31 offen offset:2048\nQmvav 40,76,184\n"
"Qta 66,68,33536\nQmvav 44,72,208\nQmvav 44,76,216\ns_waitcnt lgkmcnt(1)\nv_cmp_gt_u32 vcc, v75, v66\nQmvav 48,72,240\ns_cbranch_vccnz .Lso1160\n.Lro1160:\n"
"Qmvav 48,76,248\nQta 68,68,34048\ns_add_u32 s31, s74, 0\nQmvav 52,72,84\nbuffer_load_dwordx4 a[80:83], v60, s[48:51], s31 offen offset:0 sc1\nQmvav 52,76,116\ns_waitcnt lgkmcnt(4)\nQmaav 196,56,84\n"
"Qta 70,68,34560\nQmaav 196,60,116\nQmaav 192,56,240\nbuffer_load_dwordx4 a[84:87], v60, s[48:51], s31 offen offset:1024 sc1\nQmaav 192,60,248\nQmaav 188,56,208\nQta 72,68,40960\nQmaav 188,60,216\n"
"Qmaav 184,56,176\nbuffer_load_dwordx4 a[88:91], v60, s[48:51], s31 offen offset:2048 sc1\nQmaav 184,60,184\nQta 74,68,41984\nQmaav 180,56,144\nQmaav 180,60,152\nQmaav 176,56,112\nQta 76,68,49152\n"
"Qmaav 176,60,120\nbuffer_load_dwordx4 a[92:95], v60, s[48:51], s31 offen offset:3072 sc1\nQmaav 172,56,80\nQmaav 172,60,88\nQta 78,68,50176\ns_waitcnt lgkmcnt(4)\nQmvav 92,64,80\ns_add_u32 s31, s74, 4096\n"
"Qmvav 92,68,88\nbuffer_load_dwordx4 a[96:99], v60, s[48:51], s31 offen offset:0 sc1\nQmvav 124,64,112\nQmvav 124,68,120\nQmvav 156,64,144\nQta 56,68,41216\nQmvav 156,68,152\nbuffer_load_dwordx4 a[100:103], v60, s[48:51], s31 offen offset:1024 sc1\n"
"Qmvav 188,64,176\nQmvav 188,68,184\nQta 58,68,42240\nQmvav 220,64,208\nQmvav 220,68,216\nbuffer_load_dwordx4 a[104:107], v60, s[48:51], s31 offen offset:2048 sc1\nQmvav 252,64,240\nQta 60,68,49408\n"
"Qmvav 252,68,248\nQmvav 100,64,84\nQmvav 100,68,116\nQta 62,68,50432\ns_add_u32 s31, s74, 14336\ns_waitcnt lgkmcnt(4)\nQmaav 224,72,84\nbuffer_load_dwordx4 a[108:111], v60, s[48:51], s31 offen offset:0 sc1\n"
"Qmaav 224,76,116\nQmaav 220,72,240\nQmaav 220,76,248\nQmaav 216,72,208\nQta 64,68,41472\nQmaav 216,76,216\nbuffer_load_dwordx4 a[112:115], v60, s[48:51], s31 offen offset:1024 sc1\nQmaav 212,72,176\n"
"Qmaav 212,76,184\nQta 66,68,42496\nQmaav 208,72,144\nQmaav 208,76,152\nbuffer_load_dwordx4 v[168:171], v60, s[48:51], s31 offen offset:2048 sc1\nQmaav 204,72,112\nQta 68,68,49664\nQmaav 204,76,120\n"
"Qmaav 200,72,80\nQmaav 200,76,88\nQta 70,68,50688\ns_waitcnt lgkmcnt(4)\nQmaav 228,56,80\nbuffer_load_dwordx4 v[200:203], v60, s[48:51], s31 offen offset:3072 sc1\nQmaav 228,60,88\nQmaav 232,56,112\n"
"Qmaav 232,60,120\nQmaav 236,56,144\nQta 72,68,41728\ns_add_u32 s31, s74, 18432\nQmaav 236,60,152\nbuffer_load_dwordx4 v[232:235], v60, s[48:51], s31 offen offset:0 sc1\nQmaav 240,56,176\nQmaav 240,60,184\n"
"Qta 74,68,42752\nQmaav 244,56,208\nQmaav 244,60,216\nbuffer_load_dwordx4 v[212:215], v60, s[48:51], s31 offen offset:1024 sc1\nQmaav 248,56,240\nQta 76,68,49920\nQmaav 248,60,248\nQmaav 252,56,84\n"
"Qmaav 252,60,116\nQta 78,68,50944\ns_waitcnt lgkmcnt(4)\nQmvav 172,64,84\nbuffer_load_dwordx4 v[244:247], v60, s[48:51], s31 offen offset:2048 sc1\nQmvav 172,68,116\nQmvav 140,64,240\nQmvav 140,68,248\n"
"Qmvav 108,64,208\nQta 56,68,57344\ns_add_u32 m0, s96, 98304\ns_add_u32 s95, s67, 4096\nQmvav 108,68,216\nbuffer_load_dwordx4 v63, s[52:55], s95 offen lds\nQmvav 228,64,176\nQmvav 228,68,184\n"
"Qta 58,68,58368\nQmvav 196,64,144\ns_add_u32 s95, s67, 23552\nQmvav 196,68,152\nbuffer_load_dwordx4 v63, s[52:55], s95 offen offset:1024 lds\nQmvav 164,64,112\nQta 60,70,1024\nQmvav 164,68,120\n"
"Qmvav 132,64,80\nQmvav 132,68,88\nQta 62,70,2048\ns_add_u32 m0, s96, 106496\ns_add_u32 s95, s67, 45056\ns_waitcnt lgkmcnt(4)\nQmvav 204,72,80\nbuffer_load_dwordx4 v63, s[52:55], s95 offen lds\n"
"Qmvav 204,76,88\nQmvav 236,72,112\nQmvav 236,76,120\nQmvav 96,72,144\nQta 64,68,57600\ns_add_u32 s95, s67, 64512\nQmvav 96,76,152\nbuffer_load_dwordx4 v63, s[52:55], s95 offen offset:1024 lds\n"
"Qmvav 128,72,176\nQmvav 128,76,184\nQta 66,68,58624\nQmvav 160,72,208\ns_add_u32 m0, s96, 114688\ns_add_u32 s95, s72, 4096\nQmvav 160,76,216\nbuffer_load_dwordx4 v61, s[52:55], s95 offen lds\n"
"Qmvav 192,72,240\nQta 68,70,1280\nQmvav 192,76,248\nQmvav 224,72,84\nQmvav 224,76,116\nQta 70,70,2304\ns_add_u32 s95, s72, 23552\ns_waitcnt vmcnt(19)\n"
"s_waitcnt lgkmcnt(4)\nQmaaa 140,56,24\nbuffer_load_dwordx4 v61, s[52:55], s95 offen offset:1024 lds\nQmaaa 140,60,52\nQmaaa 136,56,20\nQmaaa 136,60,48\nQmaaa 132,56,16\nQta 72,68,57856\n"
"s_add_u32 m0, s96, 122880\ns_add_u32 s95, s72, 45056\nQmaaa 132,60,44\nbuffer_load_dwordx4 v61, s[52:55], s95 offen lds\nQmaaa 128,56,12\nQmaaa 128,60,40\nQta 74,68,58880\nQmaaa 124,56,8\n"
"s_add_u32 s95, s72, 64512\nQmaaa 124,60,36\nbuffer_load_dwordx4 v61, s[52:55], s95 offen offset:1024 lds\nQmaaa 120,56,4\nQta 76,70,1536\nQmaaa 120,60,32\nQmaaa 116,56,0\nQmaaa 116,60,28\n"
"Qta 78,70,2560\ns_add_u32 m0, s96, 0\ns_add_u32 s95, s72, 4096\ns_waitcnt lgkmcnt(4)\nQmaaa 144,64,0\nbuffer_load_dwordx4 v62, s[52:55], s95 offen lds\nQmaaa 144,68,28\nQmaaa 148,64,4\n"
"Qmaaa 148,68,32\nQmaaa 152,64,8\nQta 56,68,58112\ns_add_u32 s95, s72, 44032\nQmaaa 152,68,36\nbuffer_load_dwordx4 v62, s[52:55], s95 offen offset:1024 lds\nQmaaa 156,64,12\nQmaaa 156,68,40\n"
"Qta 58,68,59136\nQmaaa 160,64,16\ns_add_u32 m0, s96, 8192\ns_add_u32 s95, s72, 4096\nQmaaa 160,68,44\nbuffer_load_dwordx4 v63, s[52:55], s95 offen lds\nQmaaa 164,64,20\nQta 60,70,1792\n"
"Qmaaa 164,68,48\nQmaaa 168,64,24\nQmaaa 168,68,52\nQta 62,70,2816\ns_add_u32 s95, s72, 23552\ns_waitcnt lgkmcnt(4)\nQmvaa 24,72,24\nbuffer_load_dwordx4 v63, s[52:55], s95 offen offset:1024 lds\n"
"Qmvaa 24,76,52\nQmvaa 20,72,20\nQmvaa 20,76,48\nQmvaa 16,72,16\nQta 64,70,9216\ns_add_u32 s31, s75, 0\nQmvaa 16,76,44\nbuffer_load_dwordx4 v[80:83], v60, s[48:51], s31 offen offset:0 sc1\n"
"Qmvaa 12,72,12\nQmvaa 12,76,40\nQta 66,70,9728\nQmvaa 8,72,8\nQmvaa 8,76,36\nbuffer_load_dwordx4 v[112:115], v60, s[48:51], s31 offen offset:1024 sc1\nQmvaa 4,72,4\nQta 68,70,10240\n"
"v_add_u32 v75, 1, v75\nQmvaa 4,76,32\ns_waitcnt vmcnt(43)\nds_write_b32 v73, v75\nQmvaa 0,72,0\nQmvaa 0,76,28\nQta 70,70,10752\ns_waitcnt lgkmcnt(5)\n"
"Qmvaa 28,56,0\nbuffer_load_dwordx4 v[144:147], v60, s[48:51], s31 offen offset:2048 sc1\nQmvaa 28,60,28\nQmvaa 32,56,4\nQmvaa 32,60,32\nQmvaa 36,56,8\nQta 72,70,9472\nQmvaa 36,60,36\n"
"ds_read_b32 v66, v74\nQmvaa 40,56,12\nbuffer_load_dwordx4 v[176:179], v60, s[48:51], s31 offen offset:3072 sc1\nQmvaa 40,60,40\nQta 74,70,9984\nQmvaa 44,56,16\nv_add_u32 v67, 3, v65\nQmvaa 44,60,44\n"
"s_waitcnt lgkmcnt(1)\nv_cmp_gt_u32 vcc, v75, v66\nQmvaa 48,56,20\ns_cbranch_vccnz .Lso1300\n.Lro1300:\nQmvaa 48,60,48\nQta 76,70,10496\ns_add_u32 s31, s75, 4096\n"
"Qmvaa 52,56,24\nbuffer_load_dwordx4 v[208:211], v60, s[48:51], s31 offen offset:0 sc1\nQmvaa 52,60,52\nbuffer_store_dword v67, v77, s[24:27], 0 offen sc1\ns_waitcnt lgkmcnt(4)\nQmaaa 196,64,24\nQta 78,70,11008\nQmaaa 196,68,52\n"
"Qmaaa 192,64,20\nbuffer_load_dwordx4 v[240:243], v60, s[48:51], s31 offen offset:1024 sc1\nQmaaa 192,68,48\nQmaaa 188,64,16\nQta 56,70,17408\nQmaaa 188,68,44\nQmaaa 184,64,12\nbuffer_load_dwordx4 v[84:87], v60, s[48:51], s31 offen offset:2048 sc1\n"
"Qmaaa 184,68,40\nQta 58,70,18432\nQmaaa 180,64,8\nQmaaa 180,68,36\nQmaaa 176,64,4\nQta 60,69,0\ns_add_u32 s31, s75, 14336\nQmaaa 176,68,32\n"
"buffer_load_dwordx4 v[88:91], v60, s[48:51], s31 offen offset:0 sc1\nQmaaa 172,64,0\nQmaaa 172,68,28\nQta 62,69,1024\ns_waitcnt lgkmcnt(4)\nQmvaa 92,72,0\nQmvaa 92,76,28\nbuffer_load_dwordx4 v[120:123], v60, s[48:51], s31 offen offset:1024 sc1\n"
"Qmvaa 124,72,4\nQmvaa 124,76,32\nQmvaa 156,72,8\nQta 64,70,17664\nQmvaa 156,76,36\nbuffer_load_dwordx4 v[152:155], v60, s[48:51], s31 offen offset:2048 sc1\nQmvaa 188,72,12\nQmvaa 188,76,40\n"
"Qta 66,70,18688\nQmvaa 220,72,16\nQmvaa 220,76,44\nbuffer_load_dwordx4 v[184:187], v60, s[48:51], s31 offen offset:3072 sc1\nQmvaa 252,72,20\nQta 68,69,256\nQmvaa 252,76,48\nQmvaa 100,72,24\n"
"Qmvaa 100,76,52\nQta 70,69,1280\ns_add_u32 s31, s75, 18432\ns_waitcnt lgkmcnt(4)\nQmaaa 224,56,24\nbuffer_load_dwordx4 v[216:219], v60, s[48:51], s31 offen offset:0 sc1\nQmaaa 224,60,52\nQmaaa 220,56,20\n"
"Qmaaa 220,60,48\nQmaaa 216,56,16\nQta 72,70,17920\nQmaaa 216,60,44\nbuffer_load_dwordx4 v[248:251], v60, s[48:51], s31 offen offset:1024 sc1\nQmaaa 212,56,12\nQmaaa 212,60,40\nQta 74,70,18944\n"
"Qmaaa 208,56,8\nQmaaa 208,60,36\nbuffer_load_dwordx4 v[116:119], v60, s[48:51], s31 offen offset:2048 sc1\nQmaaa 204,56,4\nQta 76,69,512\nQmaaa 204,60,32\nQmaaa 200,56,0\nQmaaa 200,60,28\n"
"Qta 78,69,1536\ns_add_u32 m0, s96, 16384\ns_add_u32 s95, s72, 45056\ns_waitcnt lgkmcnt(4)\nQmaaa 228,64,0\nbuffer_load_dwordx4 v63, s[52:55], s95 offen lds\nQmaaa 228,68,28\nQmaaa 232,64,4\n"
"Qmaaa 232,68,32\nQmaaa 236,64,8\nQta 56,70,18176\ns_add_u32 s95, s72, 64512\nQmaaa 236,68,36\nbuffer_load_dwordx4 v63, s[52:55], s95 offen offset:1024 lds\nQmaaa 240,64,12\nQmaaa 240,68,40\n"
"Qta 58,70,19200\nQmaaa 244,64,16\ns_add_u32 m0, s96, 24576\ns_add_u32 s95, s73, 4096\nQmaaa 244,68,44\nbuffer_load_dwordx4 v61, s[52:55], s95 offen lds\nQmaaa 248,64,20\nQta 60,69,768\n"
"Qmaaa 248,68,48\nQmaaa 252,64,24\nQmaaa 252,68,52\nQta 62,69,1792\ns_add_u32 s95, s73, 23552\ns_waitcnt lgkmcnt(4)\nQmvaa 172,72,24\nbuffer_load_dwordx4 v61, s[52:55], s95 offen offset:1024 lds\n"
"Qmvaa 172,76,52\nQmvaa 140,72,20\nQmvaa 140,76,48\nQmvaa 108,72,16\nQta 64,69,8192\ns_add_u32 m0, s96, 32768\ns_add_u32 s95, s73, 45056\nQmvaa 108,76,44\n"
"buffer_load_dwordx4 v61, s[52:55], s95 offen lds\nQmvaa 228,72,12\nQmvaa 228,76,40\nQta 66,69,9216\nQmvaa 196,72,8\ns_add_u32 s95, s73, 64512\nQmvaa 196,76,36\nbuffer_load_dwordx4 v61, s[52:55], s95 offen offset:1024 lds\n"
"Qmvaa 164,72,4\nQta 68,69,16384\nQmvaa 164,76,32\nQmvaa 132,72,0\nQmvaa 132,76,28\nQta 70,69,17408\ns_add_u32 m0, s96, 40960\ns_add_u32 s95, s73, 4096\n"
"s_waitcnt lgkmcnt(4)\nQmvaa 204,56,0\nbuffer_load_dwordx4 v62, s[52:55], s95 offen lds\nQmvaa 204,60,28\nQmvaa 236,56,4\nQmvaa 236,60,32\nQmvaa 96,56,8\nQta 72,69,8448\n"
"s_add_u32 s95, s73, 44032\nQmvaa 96,60,36\nbuffer_load_dwordx4 v62, s[52:55], s95 offen offset:1024 lds\nQmvaa 128,56,12\nQmvaa 128,60,40\nQta 74,69,9472\nQmvaa 160,56,16\ns_add_u32 m0, s96, 49152\n"
"s_add_u32 s95, s73, 4096\nQmvaa 160,60,44\nbuffer_load_dwordx4 v63, s[52:55], s95 offen lds\nQmvaa 192,56,20\nQta 76,69,16640\nQmvaa 192,60,48\nQmvaa 224,56,24\nQmvaa 224,60,52\n"
"Qta 78,69,17664\ns_add_u32 s95, s73, 23552\ns_waitcnt vmcnt(36)\ns_waitcnt lgkmcnt(4)\nQmaaa 140,64,104\nbuffer_load_dwordx4 v63, s[52:55], s95 offen offset:1024 lds\nQmaav 140,68,244\nQmaaa 136,64,100\n"
"Qmaav 136,68,212\nQmaaa 132,64,96\nQta 56,69,8704\ns_add_u32 s31, s77, 0\nQmaav 132,68,232\nbuffer_load_dwordx4 a[0:3], v60, s[48:51], s31 offen offset:0 sc1\nQmaaa 128,64,92\nQmaav 128,68,200\n"
"Qta 58,69,9728\nQmaaa 124,64,88\nQmaav 124,68,168\nbuffer_load_dwordx4 a[4:7], v60, s[48:51], s31 offen offset:1024 sc1\nQmaaa 120,64,84\nQta 60,69,16896\nQmaaa 120,68,112\nQmaaa 116,64,80\n"
"Qmaaa 116,68,108\nQta 62,69,17920\ns_waitcnt lgkmcnt(4)\nQmaaa 144,72,80\nbuffer_load_dwordx4 a[8:11], v60, s[48:51], s31 offen offset:2048 sc1\nQmaaa 144,76,108\nQmaaa 148,72,84\nQmaaa 148,76,112\n"
"Qmaaa 152,72,88\nQta 64,69,8960\nQmaav 152,76,168\nbuffer_load_dwordx4 a[12:15], v60, s[48:51], s31 offen offset:3072 sc1\n.Lpg18:\ns_cbranch_execz .Lpg22\nQmaaa 156,72,92\nQmaav 156,76,200\n"
"Qta 66,69,9984\nQmaaa 160,72,96\ns_add_u32 s31, s77, 4096\nQmaav 160,76,232\nbuffer_load_dwordx4 a[16:19], v60, s[48:51], s31 offen offset:0 sc1\nQmaaa 164,72,100\nQta 68,69,17152\nQmaav 164,76,212\n"
"Qmaaa 168,72,104\nQmaav 168,76,244\nQta 70,69,18176\ns_waitcnt lgkmcnt(4)\nQmvaa 24,56,104\nbuffer_load_dwordx4 a[20:23], v60, s[48:51], s31 offen offset:1024 sc1\nQmvav 24,60,244\nQmvaa 20,56,100\n"
"Qmvav 20,60,212\nQmvaa 16,56,96\nQta 72,69,24576\nQmvav 16,60,232\nbuffer_load_dwordx4 a[24:27], v60, s[48:51], s31 offen offset:2048 sc1\nQmvaa 12,56,92\nQmvav 12,60,200\nQta 74,69,25088\n"
"Qmvaa 8,56,88\ns_add_u32 s31, s77, 14336\nQmvav 8,60,168\nbuffer_load_dwordx4 a[28:31], v60, s[48:51], s31 offen offset:0 sc1\nQmvaa 4,56,84\nQta 76,69,25600\nv_add_u32 v75, 1, v75\nQmvaa 4,60,112\n"
"s_waitcnt vmcnt(43)\nds_write_b32 v73, v75\nQmvaa 0,56,80\nQmvaa 0,60,108\nQta 78,69,26112\ns_waitcnt lgkmcnt(5)\nQmvaa 28,64,80\nbuffer_load_dwordx4 a[32:35], v60, s[48:51], s31 offen offset:1024 sc1\n"
"Qmvaa 28,68,108\nQmvaa 32,64,84\nQmvaa 32,68,112\nQmvaa 36,64,88\nQta 56,69,24832\nQmvav 36,68,168\nds_read_b32 v66, v74\nQmvaa 40,64,92\n"
"buffer_load_dwordx4 a[36:39], v60, s[48:51], s31 offen offset:2048 sc1\nQmvav 40,68,200\nQta 58,69,25344\nQmvaa 44,64,96\nQmvav 44,68,232\ns_waitcnt lgkmcnt(1)\nv_cmp_gt_u32 vcc, v75, v66\nQmvaa 48,64,100\n"
"s_cbranch_vccnz .Lso1440\n.Lro1440:\nQmvav 48,68,212\nQta 60,69,25856\nv_add_u32 v75, 1, v75\nQmvaa 52,64,104\ns_waitcnt vmcnt(39)\nds_write_b32 v73, v75\n"
"Qmvav 52,68,244\nbuffer_load_dwordx4 a[40:43], v60, s[48:51], s31 offen offset:3072 sc1\ns_waitcnt lgkmcnt(5)\nQmaaa 196,72,104\nQta 62,69,26368\nQmaav 196,76,244\nQmaaa 192,72,100\ns_add_u32 s31, s77, 18432\n"
"Qmaav 192,76,212\nbuffer_load_dwordx4 a[44:47], v60, s[48:51], s31 offen offset:0 sc1\nQmaaa 188,72,96\nQta 64,69,32768\nQmaav 188,76,232\nQmaaa 184,72,92\nds_read_b32 v66, v74\nQmaav 184,76,200\n"
"Qta 66,69,33792\nQmaaa 180,72,88\nbuffer_load_dwordx4 a[48:51], v60, s[48:51], s31 offen offset:1024 sc1\nQmaav 180,76,168\nbuffer_load_dword v78, v76, s[24:27], 0 offen sc1\nQmaaa 176,72,84\ns_waitcnt lgkmcnt(1)\nv_cmp_gt_u32 vcc, v75, v66\n"
"Qmaaa 176,76,112\ns_cbranch_vccnz .Lso1455\n.Lro1455:\nQmaaa 172,72,80\nQta 68,69,40960\nQmaaa 172,76,108\nbuffer_load_dwordx4 a[52:55], v60, s[48:51], s31 offen offset:2048 sc1\ns_waitcnt lgkmcnt(4)\n"
"Qmvaa 92,56,80\nQmvaa 92,60,108\nQta 70,69,41984\nQmvaa 124,56,84\ns_add_u32 m0, s96, 57344\ns_add_u32 s95, s73, 45056\nQmvaa 124,60,112\nbuffer_load_dwordx4 v63, s[52:55], s95 offen lds\n"
"Qmvaa 156,56,88\nQta 72,69,33024\nQmvav 156,60,168\nQmvaa 188,56,92\nQmvav 188,60,200\nQta 74,69,34048\ns_add_u32 s95, s73, 64512\nQmvaa 220,56,96\n"
"buffer_load_dwordx4 v63, s[52:55], s95 offen offset:1024 lds\nQmvav 220,60,232\nQmvaa 252,56,100\nQta 76,69,41216\nQmvav 252,60,212\nQmvaa 100,56,104\nQmvav 100,60,244\nQta 78,69,42240\n"
"s_waitcnt lgkmcnt(4)\nQmaaa 224,64,104\nQmaav 224,68,244\nQmaaa 220,64,100\nQmaav 220,68,212\nQmaaa 216,64,96\nQta 56,69,33280\nQmaav 216,68,232\n"
"Qmaaa 212,64,92\nQmaav 212,68,200\nQta 58,69,34304\nQmaaa 208,64,88\nQmaav 208,68,168\nQmaaa 204,64,84\nQta 60,69,41472\nQmaaa 204,68,112\n"
"Qmaaa 200,64,80\nQmaaa 200,68,108\nQta 62,69,42496\ns_waitcnt lgkmcnt(4)\nQmaaa 228,72,80\nQmaaa 228,76,108\nQmaaa 232,72,84\nQmaaa 232,76,112\n"
"Qmaaa 236,72,88\nQta 64,69,33536\nQmaav 236,76,168\nQmaaa 240,72,92\nQmaav 240,76,200\nQta 66,69,34560\nQmaaa 244,72,96\nQmaav 244,76,232\n"
"Qmaaa 248,72,100\nQta 68,69,41728\nQmaav 248,76,212\nQmaaa 252,72,104\nQmaav 252,76,244\nQta 70,69,42752\ns_waitcnt lgkmcnt(4)\nQmvaa 172,56,104\n"
"Qmvav 172,60,244\nQmvaa 140,56,100\nQmvav 140,60,212\nQmvaa 108,56,96\nQta 72,69,49152\nQmvav 108,60,232\nQmvaa 228,56,92\nQmvav 228,60,200\n"
"Qta 74,69,50176\nQmvaa 196,56,88\nQmvav 196,60,168\nQmvaa 164,56,84\nQta 76,69,57344\nQmvaa 164,60,112\ns_waitcnt vmcnt(3)\nv_sub_u32 v66, v78, v65\n"
"v_cmp_gt_u32 vcc, 3, v66\ns_and_b64 vcc, vcc, s[28:29]\ns_cbranch_scc1 .Lf3_w\n.Lf3_r:\nv_add_u32 v75, 1, v75\nQmvaa 132,56,80\ns_waitcnt vmcnt(42)\nds_write_b32 v73, v75\n"
"Qmvaa 132,60,108\nQta 78,69,58368\ns_waitcnt lgkmcnt(5)\nQmvaa 204,64,80\ns_add_u32 s31, s74, 28672\nQmvaa 204,68,108\nbuffer_load_dwordx4 a[80:83], v60, s[48:51], s31 offen offset:0 sc1\nQmvaa 236,64,84\n"
"Qmvaa 236,68,112\ns_bitcmp1_b32 %19, 2\ns_cbranch_scc1 .Lrole1\nQmvaa 96,64,88\nQta 56,69,49408\ns_lshl_b32 s46, %19, 4\nQmvav 96,68,168\nbuffer_load_dwordx4 a[84:87], v60, s[48:51], s31 offen offset:1024 sc1\n"
"Qmvaa 128,64,92\nds_read_b32 v66, v74\ns_add_u32 s46, s46, 128\nQmvav 128,68,200\nQta 58,69,50432\nv_mov_b32 v77, 0x7ffff000\nQmvaa 160,64,96\nQmvav 160,68,232\n"
"buffer_load_dwordx4 a[88:91], v60, s[48:51], s31 offen offset:2048 sc1\nQmvaa 192,64,100\ns_waitcnt lgkmcnt(1)\nv_cmp_gt_u32 vcc, v75, v66\nQmvav 192,68,212\ns_cbranch_vccnz .Lso1525r0\n.Lro1525r0:\nQmvaa 224,64,104\n"
"Qta 60,69,57600\nQmvav 224,68,244\nbuffer_load_dwordx4 a[92:95], v60, s[48:51], s31 offen offset:3072 sc1\ns_waitcnt vmcnt(31)\ns_waitcnt lgkmcnt(4)\nQmaav 140,72,84\nQmaav 140,76,116\nQta 62,69,58624\n"
"Qmaav 136,72,240\ns_add_u32 m0, s96, 65536\ns_add_u32 s95, s67, 86016\nQmaav 136,76,248\nbuffer_load_dwordx4 v63, s[52:55], s95 offen lds\nQmaav 132,72,208\nQta 64,69,49664\nQmaav 132,76,216\n"
"Qmaav 128,72,176\nQmaav 128,76,184\nQta 66,69,50688\ns_add_u32 s95, s67, 105472\nQmaav 124,72,144\nbuffer_load_dwordx4 v63, s[52:55], s95 offen offset:1024 lds\nQmaav 124,76,152\nQmaav 120,72,112\n"
"Qta 68,69,57856\nQmaav 120,76,120\ns_add_u32 m0, s96, 73728\ns_add_u32 s95, s67, 126976\nQmaav 116,72,80\nbuffer_load_dwordx4 v63, s[52:55], s95 offen lds\nQmaav 116,76,88\nQta 70,69,58880\n"
"s_waitcnt lgkmcnt(4)\nQmaav 144,56,80\nQmaav 144,60,88\ns_add_u32 s95, s67, 146432\nQmaav 148,56,112\nbuffer_load_dwordx4 v63, s[52:55], s95 offen offset:1024 lds\nQmaav 148,60,120\nQmaav 152,56,144\n"
"Qta 72,69,49920\nQmaav 152,60,152\ns_add_u32 m0, s96, 81920\ns_add_u32 s95, s72, 86016\nQmaav 156,56,176\nbuffer_load_dwordx4 v63, s[52:55], s95 offen lds\nQmaav 156,60,184\nQta 74,69,50944\n"
"Qmaav 160,56,208\nQmaav 160,60,216\nQmaav 164,56,240\nQta 76,69,58112\ns_add_u32 s95, s72, 105472\nQmaav 164,60,248\nbuffer_load_dwordx4 v63, s[52:55], s95 offen offset:1024 lds\nQmaav 168,56,84\n"
"Qmaav 168,60,116\nQta 78,69,59136\ns_waitcnt lgkmcnt(4)\nQmvav 24,64,84\ns_add_u32 m0, s96, 90112\ns_add_u32 s95, s72, 126976\nQmvav 24,68,116\nbuffer_load_dwordx4 v63, s[52:55], s95 offen lds\n"
"Qmvav 20,64,240\nQmvav 20,68,248\nQmvav 16,64,208\nQta 56,68,0\ns_add_u32 s95, s72, 146432\nQmvav 16,68,216\nbuffer_load_dwordx4 v63, s[52:55], s95 offen offset:1024 lds\nQmvav 12,64,176\n"
"Qmvav 12,68,184\nQta 58,68,512\nQmvav 8,64,144\ns_add_u32 s31, s74, 32768\nQmvav 8,68,152\nbuffer_load_dwordx4 a[96:99], v60, s[48:51], s31 offen offset:0 sc1\nQmvav 4,64,112\nQta 60,68,1024\n"
"Qmvav 4,68,120\nQmvav 0,64,80\nQmvav 0,68,88\nQta 62,68,1536\ns_waitcnt lgkmcnt(4)\nQmvav 28,72,80\nbuffer_load_dwordx4 a[100:103], v60, s[48:51], s31 offen offset:1024 sc1\nQmvav 28,76,88\n"
"Qmvav 32,72,112\nQmvav 32,76,120\nQmvav 36,72,144\nQta 64,68,256\nQmvav 36,76,152\nbuffer_load_dwordx4 a[104:107], v60, s[48:51], s31 offen offset:2048 sc1\nQmvav 40,72,176\nQmvav 40,76,184\n"
"Qta 66,68,768\nQmvav 44,72,208\ns_add_u32 s31, s74, 43008\nv_cmp_eq_u32 s[44:45], s46, v60\ns_nop 1\nv_cndmask_b32 v77, v77, v76, s[44:45]\nv_add_u32 v67, 1, v65\ns_xor_b32 s46, %19, 12\n"
"s_lshl_b32 s28, 1, s46\nQmvav 44,76,216\nbuffer_load_dwordx4 a[108:111], v60, s[48:51], s31 offen offset:0 sc1\nQmvav 48,72,240\nQta 68,68,1280\nQmvav 48,76,248\nv_add_u32 v75, 1, v75\nQmvav 52,72,84\n"
"s_waitcnt vmcnt(37)\nds_write_b32 v73, v75\nQmvav 52,76,116\nQta 70,68,1792\ns_waitcnt lgkmcnt(5)\nQmaav 196,56,84\nbuffer_load_dwordx4 a[112:115], v60, s[48:51], s31 offen offset:1024 sc1\nQmaav 196,60,116\n"
"Qmaav 192,56,240\nQmaav 192,60,248\nQmaav 188,56,208\nQta 72,68,8192\nQmaav 188,60,216\nbuffer_load_dwordx4 v[168:171], v60, s[48:51], s31 offen offset:2048 sc1\nQmaav 184,56,176\nds_read_b32 v66, v74\n"
"Qmaav 184,60,184\nQta 74,68,9216\nQmaav 180,56,144\nQmaav 180,60,152\nbuffer_load_dwordx4 v[200:203], v60, s[48:51], s31 offen offset:3072 sc1\nQmaav 176,56,112\ns_waitcnt lgkmcnt(1)\nv_cmp_gt_u32 vcc, v75, v66\n"
"Qmaav 176,60,120\ns_cbranch_vccnz .Lso1595r0\n.Lro1595r0:\nQmaav 172,56,80\nQta 76,68,16384\ns_add_u32 s31, s74, 47104\nQmaav 172,60,88\nbuffer_load_dwordx4 v[232:235], v60, s[48:51], s31 offen offset:0 sc1\n"
"s_waitcnt lgkmcnt(4)\nQmvav 92,64,80\nQmvav 92,68,88\nQta 78,68,17408\nQmvav 124,64,112\nQmvav 124,68,120\nbuffer_load_dwordx4 v[212:215], v60, s[48:51], s31 offen offset:1024 sc1\nQmvav 156,64,144\n"
"Qta 56,68,8448\nQmvav 156,68,152\nQmvav 188,64,176\nQmvav 188,68,184\nQta 58,68,9472\nQmvav 220,64,208\nbuffer_load_dwordx4 v[244:247], v60, s[48:51], s31 offen offset:2048 sc1\nQmvav 220,68,216\n"
"Qmvav 252,64,240\nQta 60,68,16640\nQmvav 252,68,248\ns_add_u32 m0, s91, 0\ns_add_u32 s31, s77, 28672\nQmvav 100,64,84\nbuffer_load_dwordx4 v60, s[48:51], s31 offen sc1 lds\nQmvav 100,68,116\n"
"Qta 62,68,17664\ns_waitcnt lgkmcnt(4)\nQmaav 224,72,84\nQmaav 224,76,116\nQmaav 220,72,240\nbuffer_load_dwordx4 v60, s[48:51], s31 offen offset:1024 sc1 lds\nQmaav 220,76,248\nQmaav 216,72,208\n"
"Qta 64,68,8704\nQmaav 216,76,216\nQmaav 212,72,176\nbuffer_load_dwordx4 v60, s[48:51], s31 offen offset:2048 sc1 lds\nQmaav 212,76,184\nQta 66,68,9728\nQmaav 208,72,144\nQmaav 208,76,152\n"
"Qmaav 204,72,112\nQta 68,68,16896\nQmaav 204,76,120\nbuffer_load_dwordx4 v60, s[48:51], s31 offen offset:3072 sc1 lds\nQmaav 200,72,80\nQmaav 200,76,88\nQta 70,68,17920\ns_waitcnt lgkmcnt(4)\n"
"Qmaav 228,56,80\ns_add_u32 m0, s91, 4096\ns_add_u32 s31, s77, 32768\nQmaav 228,60,88\nbuffer_load_dwordx4 v60, s[48:51], s31 offen sc1 lds\nQmaav 232,56,112\nQmaav 232,60,120\nQmaav 236,56,144\n"
"Qta 72,68,8960\nQmaav 236,60,152\nbuffer_load_dwordx4 v60, s[48:51], s31 offen offset:1024 sc1 lds\nQmaav 240,56,176\nQmaav 240,60,184\nQta 74,68,9984\nQmaav 244,56,208\nQmaav 244,60,216\n"
"buffer_load_dwordx4 v60, s[48:51], s31 offen offset:2048 sc1 lds\nQmaav 248,56,240\nQta 76,68,17152\nQmaav 248,60,248\nQmaav 252,56,84\nQmaav 252,60,116\nQta 78,68,18176\ns_add_u32 m0, s91, 33792\n"
"s_add_u32 s31, s77, 43008\ns_waitcnt lgkmcnt(4)\nQmvav 172,64,84\nbuffer_load_dwordx4 v60, s[48:51], s31 offen sc1 lds\nQmvav 172,68,116\nQmvav 140,64,240\nQmvav 140,68,248\nQmvav 108,64,208\n"
"Qta 56,68,24576\nQmvav 108,68,216\nbuffer_load_dwordx4 v60, s[48:51], s31 offen offset:1024 sc1 lds\nQmvav 228,64,176\nQmvav 228,68,184\nQta 58,68,25600\nQmvav 196,64,144\nQmvav 196,68,152\n"
"buffer_load_dwordx4 v60, s[48:51], s31 offen offset:2048 sc1 lds\nQmvav 164,64,112\nQta 60,68,32768\nQmvav 164,68,120\nv_add_u32 v75, 1, v75\nQmvav 132,64,80\ns_waitcnt vmcnt(49)\nds_write_b32 v73, v75\n"
"Qmvav 132,68,88\nQta 62,68,33792\ns_waitcnt lgkmcnt(5)\nQmvav 204,72,80\nbuffer_load_dwordx4 v60, s[48:51], s31 offen offset:3072 sc1 lds\nQmvav 204,76,88\nQmvav 236,72,112\nQmvav 236,76,120\n"
"Qmvav 96,72,144\nQta 64,68,24832\ns_add_u32 m0, s91, 37888\ns_add_u32 s31, s77, 47104\nQmvav 96,76,152\nbuffer_load_dwordx4 v60, s[48:51], s31 offen sc1 lds\nQmvav 128,72,176\nds_read_b32 v66, v74\n"
"Qmvav 128,76,184\nQta 66,68,25856\nQmvav 160,72,208\nQmvav 160,76,216\nbuffer_load_dwordx4 v60, s[48:51], s31 offen offset:1024 sc1 lds\nQmvav 192,72,240\ns_waitcnt lgkmcnt(1)\nv_cmp_gt_u32 vcc, v75, v66\n"
"Qmvav 192,76,248\ns_cbranch_vccnz .Lso1665r0\n.Lro1665r0:\nQmvav 224,72,84\nQta 68,68,33024\nQmvav 224,76,116\nbuffer_load_dwordx4 v60, s[48:51], s31 offen offset:2048 sc1 lds\ns_waitcnt vmcnt(38)\n"
"s_waitcnt lgkmcnt(4)\nQmaaa 140,56,24\nQmaaa 140,60,52\nQta 70,68,34048\nQmaaa 136,56,20\ns_add_u32 m0, s96, 0\ns_add_u32 s95, s73, 86016\nQmaaa 136,60,48\n"
"buffer_load_dwordx4 v63, s[52:55], s95 offen lds\nQmaaa 132,56,16\nQta 72,68,25088\nQmaaa 132,60,44\nQmaaa 128,56,12\nQmaaa 128,60,40\nQta 74,68,26112\ns_add_u32 s95, s73, 105472\n"
"Qmaaa 124,56,8\nbuffer_load_dwordx4 v63, s[52:55], s95 offen offset:1024 lds\nQmaaa 124,60,36\nQmaaa 120,56,4\nQta 76,68,33280\nQmaaa 120,60,32\ns_add_u32 m0, s96, 8192\ns_add_u32 s95, s73, 126976\n"
"Qmaaa 116,56,0\nbuffer_load_dwordx4 v63, s[52:55], s95 offen lds\nQmaaa 116,60,28\nQta 78,68,34304\ns_waitcnt lgkmcnt(4)\nQmaaa 144,64,0\nQmaaa 144,68,28\ns_add_u32 s95, s73, 146432\n"
"Qmaaa 148,64,4\nbuffer_load_dwordx4 v63, s[52:55], s95 offen offset:1024 lds\nQmaaa 148,68,32\nQmaaa 152,64,8\nQta 56,68,25344\nQmaaa 152,68,36\ns_add_u32 s31, s75, 28672\nQmaaa 156,64,12\n"
"buffer_load_dwordx4 v[80:83], v60, s[48:51], s31 offen offset:0 sc1\nQmaaa 156,68,40\nQta 58,68,26368\nQmaaa 160,64,16\nQmaaa 160,68,44\nQmaaa 164,64,20\nQta 60,68,33536\nQmaaa 164,68,48\n"
"buffer_load_dwordx4 v[112:115], v60, s[48:51], s31 offen offset:1024 sc1\nQmaaa 168,64,24\nQmaaa 168,68,52\nQta 62,68,34560\ns_waitcnt lgkmcnt(4)\nQmvaa 24,72,24\nQmvaa 24,76,52\nbuffer_load_dwordx4 v[144:147], v60, s[48:51], s31 offen offset:2048 sc1\n"
"Qmvaa 20,72,20\nQmvaa 20,76,48\n.Lpg19:\ns_cbranch_execz .Lpg23\nQmvaa 16,72,16\nQta 64,68,40960\nQmvaa 16,76,44\nbuffer_load_dwordx4 v[176:179], v60, s[48:51], s31 offen offset:3072 sc1\n"
"Qmvaa 12,72,12\nQmvaa 12,76,40\nQta 66,68,41472\nQmvaa 8,72,8\ns_add_u32 s31, s75, 32768\nQmvaa 8,76,36\nbuffer_load_dwordx4 v[208:211], v60, s[48:51], s31 offen offset:0 sc1\nQmvaa 4,72,4\n"
"Qta 68,68,41984\nQmvaa 4,76,32\nQmvaa 0,72,0\nQmvaa 0,76,28\nQta 70,68,42496\ns_waitcnt lgkmcnt(4)\nQmvaa 28,56,0\nbuffer_load_dwordx4 v[240:243], v60, s[48:51], s31 offen offset:1024 sc1\n"
"Qmvaa 28,60,28\nQmvaa 32,56,4\nQmvaa 32,60,32\nQmvaa 36,56,8\nQta 72,68,41216\nQmvaa 36,60,36\nbuffer_load_dwordx4 v[84:87], v60, s[48:51], s31 offen offset:2048 sc1\nQmvaa 40,56,12\n"
"Qmvaa 40,60,40\nQta 74,68,41728\nQmvaa 44,56,16\ns_add_u32 s31, s75, 43008\nQmvaa 44,60,44\nbuffer_load_dwordx4 v[88:91], v60, s[48:51], s31 offen offset:0 sc1\nQmvaa 48,56,20\nQta 76,68,42240\n"
"Qmvaa 48,60,48\nv_add_u32 v75, 1, v75\nQmvaa 52,56,24\ns_waitcnt vmcnt(38)\nds_write_b32 v73, v75\nQmvaa 52,60,52\nQta 78,68,42752\ns_waitcnt lgkmcnt(5)\n"
"Qmaaa 196,64,24\nbuffer_load_dwordx4 v[120:123], v60, s[48:51], s31 offen offset:1024 sc1\nQmaaa 196,68,52\nQmaaa 192,64,20\nQmaaa 192,68,48\nQmaaa 188,64,16\nQta 56,68,49152\nQmaaa 188,68,44\n"
"buffer_load_dwordx4 v[152:155], v60, s[48:51], s31 offen offset:2048 sc1\nQmaaa 184,64,12\nds_read_b32 v66, v74\nQmaaa 184,68,40\nQta 58,68,50176\nQmaaa 180,64,8\nQmaaa 180,68,36\nbuffer_load_dwordx4 v[184:187], v60, s[48:51], s31 offen offset:3072 sc1\n"
"Qmaaa 176,64,4\ns_waitcnt lgkmcnt(1)\nv_cmp_gt_u32 vcc, v75, v66\nQmaaa 176,68,32\ns_cbranch_vccnz .Lso1735r0\n.Lro1735r0:\nQmaaa 172,64,0\nQta 60,68,57344\n"
"s_add_u32 s31, s75, 47104\nQmaaa 172,68,28\nbuffer_load_dwordx4 v[216:219], v60, s[48:51], s31 offen offset:0 sc1\ns_waitcnt lgkmcnt(4)\nQmvaa 92,72,0\nQmvaa 92,76,28\nQta 62,68,58368\nQmvaa 124,72,4\n"
"Qmvaa 124,76,32\nbuffer_load_dwordx4 v[248:251], v60, s[48:51], s31 offen offset:1024 sc1\nQmvaa 156,72,8\nQta 64,68,49408\nQmvaa 156,76,36\nQmvaa 188,72,12\nQmvaa 188,76,40\nQta 66,68,50432\n"
"Qmvaa 220,72,16\nbuffer_load_dwordx4 v[116:119], v60, s[48:51], s31 offen offset:2048 sc1\nQmvaa 220,76,44\nQmvaa 252,72,20\nQta 68,68,57600\nQmvaa 252,76,48\ns_add_u32 m0, s96, 16384\ns_add_u32 s95, s67, 86016\n"
"Qmvaa 100,72,24\nbuffer_load_dwordx4 v61, s[52:55], s95 offen lds\nQmvaa 100,76,52\nQta 70,68,58624\ns_waitcnt lgkmcnt(4)\nQmaaa 224,56,24\nQmaaa 224,60,52\ns_add_u32 s95, s67, 105472\n"
"Qmaaa 220,56,20\nbuffer_load_dwordx4 v61, s[52:55], s95 offen offset:1024 lds\nQmaaa 220,60,48\nQmaaa 216,56,16\nQta 72,68,49664\nQmaaa 216,60,44\ns_add_u32 m0, s96, 24576\ns_add_u32 s95, s67, 126976\n"
"Qmaaa 212,56,12\nbuffer_load_dwordx4 v61, s[52:55], s95 offen lds\nQmaaa 212,60,40\nQta 74,68,50688\nQmaaa 208,56,8\nQmaaa 208,60,36\nQmaaa 204,56,4\nQta 76,68,57856\n"
"s_add_u32 s95, s67, 146432\nQmaaa 204,60,32\nbuffer_load_dwordx4 v61, s[52:55], s95 offen offset:1024 lds\nQmaaa 200,56,0\nQmaaa 200,60,28\nQta 78,68,58880\ns_waitcnt lgkmcnt(4)\nQmaaa 228,64,0\n"
"s_add_u32 m0, s96, 32768\ns_add_u32 s95, s72, 86016\nQmaaa 228,68,28\nbuffer_load_dwordx4 v61, s[52:55], s95 offen lds\nQmaaa 232,64,4\nQmaaa 232,68,32\nQmaaa 236,64,8\nQta 56,68,49920\n"
"s_add_u32 s95, s72, 105472\nQmaaa 236,68,36\nbuffer_load_dwordx4 v61, s[52:55], s95 offen offset:1024 lds\nQmaaa 240,64,12\nQmaaa 240,68,40\nQta 58,68,50944\nQmaaa 244,64,16\nQmaaa 244,68,44\n"
"Qmaaa 248,64,20\nQta 60,68,58112\nQmaaa 248,68,48\nQmaaa 252,64,24\nQmaaa 252,68,52\nQta 62,68,59136\ns_waitcnt lgkmcnt(4)\nQmvaa 172,72,24\n"
"Qmvaa 172,76,52\nQmvaa 140,72,20\nQmvaa 140,76,48\nQmvaa 108,72,16\nQta 64,69,0\nQmvaa 108,76,44\nQmvaa 228,72,12\nQmvaa 228,76,40\n"
"Qta 66,69,1024\nQmvaa 196,72,8\nQmvaa 196,76,36\nQmvaa 164,72,4\nQta 68,69,8192\nQmvaa 164,76,32\nv_add_u32 v75, 1, v75\nQmvaa 132,72,0\n"
"s_waitcnt vmcnt(20)\nds_write_b32 v73, v75\nQmvaa 132,76,28\nQta 70,69,9216\ns_waitcnt lgkmcnt(5)\nQmvaa 204,56,0\ns_waitcnt vmcnt(24)\nQda 0,58,0\n"
"Qmvaa 204,60,28\nQmvaa 236,56,4\nQda 28,58,33792\nQmvaa 236,60,32\nQmvaa 96,56,8\nQta 72,69,16384\nQmvaa 96,60,36\nQda 4,58,1024\n"
"Qmvaa 128,56,12\nds_read_b32 v66, v74\nQmvaa 128,60,40\nQta 74,69,17408\nQmvaa 160,56,16\nQda 32,58,34816\nQmvaa 160,60,44\nQmvaa 192,56,20\n"
"s_waitcnt lgkmcnt(2)\nv_cmp_gt_u32 vcc, v75, v66\nQmvaa 192,60,48\ns_cbranch_vccnz .Lso1805r0\n.Lro1805r0:\nQmvaa 224,56,24\nQta 76,69,24576\ns_add_u32 m0, s96, 40960\n"
"s_add_u32 s95, s72, 126976\nQmvaa 224,60,52\nbuffer_load_dwordx4 v61, s[52:55], s95 offen lds\ns_waitcnt vmcnt(39)\ns_waitcnt lgkmcnt(8)\nQmaaa 200,64,80\nQda 8,58,2048\nQmaaa 200,68,108\n"
"Qta 78,69,25600\nQmaaa 204,64,84\nQda 36,58,35840\ns_add_u32 s95, s72, 146432\nQmaaa 204,68,112\nbuffer_load_dwordx4 v61, s[52:55], s95 offen offset:1024 lds\nQmaaa 208,64,88\nQta 56,68,0\n"
"Qmaav 208,68,168\nQda 12,58,3072\nQmaaa 212,64,92\nQmaav 212,68,200\nQta 58,68,1024\ns_add_u32 m0, s96, 49152\ns_add_u32 s95, s73, 86016\nQmaaa 216,64,96\n"
"buffer_load_dwordx4 v61, s[52:55], s95 offen lds\nQmaav 216,68,232\nQda 40,58,36864\nQmaaa 220,64,100\nQta 60,68,8192\nQmaav 220,68,212\nQda 16,58,4096\ns_add_u32 s95, s73, 105472\n"
"Qmaaa 224,64,104\nbuffer_load_dwordx4 v61, s[52:55], s95 offen offset:1024 lds\nQmaav 224,68,244\nQta 62,68,9216\ns_waitcnt vmcnt(10)\ns_waitcnt lgkmcnt(8)\nQmaav 200,72,80\nQda 44,58,37888\n"
"Qmaav 200,76,88\ns_add_u32 m0, s96, 57344\ns_add_u32 s95, s73, 126976\nQmaav 204,72,112\nbuffer_load_dwordx4 v61, s[52:55], s95 offen lds\nQmaav 204,76,120\nQda 20,58,5120\nQmaav 208,72,144\n"
"Qta 64,69,256\nQmaav 208,76,152\nQda 48,58,38912\ns_add_u32 s95, s73, 146432\nQmaav 212,72,176\nbuffer_load_dwordx4 v61, s[52:55], s95 offen offset:1024 lds\nQmaav 212,76,184\nQta 66,69,1280\n"
"Qmaav 216,72,208\nQda 24,58,6144\nQmaav 216,76,216\nQmaav 220,72,240\nQta 68,69,8448\nQmaav 220,76,248\nQda 52,58,39936\nQmaav 224,72,84\n"
"Qmaav 224,76,116\nQta 70,69,9472\ns_waitcnt lgkmcnt(9)\nQmaaa 200,56,0\nQmaaa 200,60,28\nQmaaa 204,56,4\nQmaaa 204,60,32\ns_waitcnt lgkmcnt(9)\n"
"Qmaaa 208,56,8\nQta 72,69,16640\nQmaaa 208,60,36\ns_nop 1\nds_write_b128 v58, a[200:203] offset:0\nQmaaa 212,56,12\nQmaaa 212,60,40\nQta 74,69,17664\n"
"s_waitcnt lgkmcnt(4)\nQmaaa 216,56,16\nds_write_b128 v58, a[204:207] offset:1024\nQmaaa 216,60,44\nQmaaa 220,56,20\nQta 76,69,24832\nQmaaa 220,60,48\nds_write_b128 v58, a[208:211] offset:2048\n"
"Qmaaa 224,56,24\nQmaaa 224,60,52\nQta 78,69,25856\ns_waitcnt lgkmcnt(7)\nQmaaa 228,64,80\nds_write_b128 v58, a[212:215] offset:3072\nQmaaa 228,68,108\nQmaaa 232,64,84\n"
"Qmaaa 232,68,112\nds_write_b128 v58, a[216:219] offset:4096\nQmaaa 236,64,88\nQta 56,68,256\nQmaav 236,68,168\nQmaaa 240,64,92\nds_write_b128 v58, a[220:223] offset:5120\nQmaav 240,68,200\n"
"Qta 58,68,1280\nQmaaa 244,64,96\nQmaav 244,68,232\nds_write_b128 v58, a[224:227] offset:6144\nQmaaa 248,64,100\nQta 60,68,8448\nQmaav 248,68,212\nQmaaa 252,64,104\n"
"Qmaav 252,68,244\nQta 62,68,9472\ns_waitcnt lgkmcnt(8)\nQmaav 228,72,80\nv_add_u32 v75, 1, v75\nQmaav 228,76,88\nds_write_b32 v73, v75\nQmaav 232,72,112\n"
"Qmaav 232,76,120\nQmaav 236,72,144\nQta 64,69,512\nQmaav 236,76,152\nQmaav 240,72,176\nQmaav 240,76,184\nQta 66,69,1536\nQmaav 244,72,208\n"
"Qmaav 244,76,216\nds_read_b32 v66, v74\nQmaav 248,72,240\nQta 68,69,8704\nQmaav 248,76,248\nQmaav 252,72,84\nQmaav 252,76,116\ns_waitcnt lgkmcnt(1)\n"
"v_cmp_gt_u32 vcc, v75, v66\ns_waitcnt lgkmcnt(5)\nQmaaa 228,56,0\ns_cbranch_vccnz .Lso1878r0\n.Lro1878r0:\nQmaaa 228,60,28\nQta 70,69,9728\nQmaaa 232,56,4\n"
"Qmaaa 232,60,32\nQdv 148,59,2048\nQmaaa 236,56,8\nQta 72,69,16896\nQmaaa 236,60,36\ns_nop 0\nds_write_b128 v58, a[228:231] offset:33792\nQmaaa 240,56,12\n"
"Qdv 180,59,3072\nQmaaa 240,60,40\nQta 74,69,17920\nQmaaa 244,56,16\nds_write_b128 v58, a[232:235] offset:34816\nQmaaa 244,60,44\nQdv 104,59,5120\nQmaaa 248,56,20\n"
"Qta 76,69,25088\nQmaaa 248,60,48\nds_write_b128 v58, a[236:239] offset:35840\nQmaaa 252,56,24\nQdv 136,59,0\ns_waitcnt lgkmcnt(9)\nQa 196,196,148\nQmaaa 252,60,52\n"
"Qta 78,69,26112\nQa 197,197,149\ns_waitcnt lgkmcnt(10)\nQmvaa 132,64,80\nds_write_b128 v58, a[240:243] offset:36864\nQmvaa 132,68,108\nQa 198,198,150\nQa 199,199,151\n"
"Qmvaa 164,64,84\ns_waitcnt lgkmcnt(8)\nQa 228,228,180\nQa 229,229,181\nQmvaa 164,68,112\nds_write_b128 v58, a[244:247] offset:37888\nQmvaa 196,64,88\nQta 56,68,512\n"
"Qa 230,230,182\nQa 231,231,183\nQmvav 196,68,168\nQdv 148,59,4096\ns_waitcnt lgkmcnt(8)\nQa 140,140,104\nQmvaa 228,64,92\nds_write_b128 v58, a[248:251] offset:38912\n"
"Qmvav 228,68,200\nQta 58,68,1536\nQa 141,141,105\nQmvaa 108,64,96\nQa 142,142,106\nQa 143,143,107\nQmvav 108,68,232\nQdv 180,59,6144\n"
"s_waitcnt lgkmcnt(8)\nQa 132,132,136\nQmvaa 140,64,100\nQta 60,68,8704\nQa 133,133,137\nQmvav 140,68,212\nds_write_b128 v58, a[252:255] offset:39936\nQmvaa 172,64,104\n"
"Qa 134,134,138\nQa 135,135,139\nQmvav 172,68,244\nQta 62,68,9728\ns_waitcnt lgkmcnt(6)\nQa 108,108,148\ns_waitcnt lgkmcnt(6)\nQmvav 132,72,80\n"
"Qdv 104,59,1024\nv_add_u32 v75, 1, v75\nQmvav 132,76,88\ns_waitcnt vmcnt(10)\nds_write_b32 v73, v75\nQmvav 164,72,112\nQa 109,109,149\nQa 110,110,150\n"
"Qmvav 164,76,120\nQa 111,111,151\ns_waitcnt lgkmcnt(5)\nQa 172,172,180\nQmvav 196,72,144\nQta 64,69,768\nQa 173,173,181\nQmvav 196,76,152\n"
"Qa 174,174,182\nQa 175,175,183\nQmvav 228,72,176\nQmvav 228,76,184\nQta 66,69,1792\ns_waitcnt lgkmcnt(3)\nQa 164,164,104\nQmvav 108,72,208\n"
"Qa 165,165,105\nQa 166,166,106\nQmvav 108,76,216\nds_read_b32 v66, v74\nQa 167,167,107\nQmvav 140,72,240\nQta 68,69,8960\nQmvav 140,76,248\n"
"Qmvav 172,72,84\nQmvav 172,76,116\ns_waitcnt lgkmcnt(1)\nv_cmp_gt_u32 vcc, v75, v66\ns_waitcnt lgkmcnt(6)\nQmvaa 132,56,0\ns_cbranch_vccnz .Lso1920r0\n.Lro1920r0:\n"
"s_add_u32 s68, s64, 0\nQmvaa 132,60,28\nQta 70,69,9984\nQmvaa 164,56,4\nQmvaa 164,60,32\nQdv 136,59,35840\nQmvaa 196,56,8\nQta 72,69,17152\n"
"s_nop 1\nQk 132,132,133\nQmvaa 196,60,36\nQk 133,134,135\nQmvaa 228,56,12\nQdv 148,59,36864\nQk 134,164,165\nQmvaa 228,60,40\n"
"Qta 74,69,18176\nQk 135,166,167\nQmvaa 108,56,16\nbuffer_store_dwordx4 v[132:135], v60, s[24:27], s68 offen offset:0 sc1\nQk 196,196,197\nQmvaa 108,60,44\nQdv 180,59,38912\nQk 197,198,199\n"
"Qmvaa 140,56,20\nQta 76,69,25344\nQk 198,228,229\nQmvaa 140,60,48\ns_waitcnt lgkmcnt(5)\nQa 96,96,136\nQa 97,97,137\nQmvaa 172,56,24\n"
"Qdv 104,59,33792\nQa 98,98,138\nQmvaa 172,60,52\nQta 78,69,26368\nQa 99,99,139\ns_waitcnt lgkmcnt(11)\nQmvaa 204,64,80\ns_waitcnt lgkmcnt(5)\n"
"Qa 128,128,148\nQa 129,129,149\nQmvaa 204,68,108\nQa 130,130,150\nQa 131,131,151\nQmvaa 236,64,84\nQk 199,230,231\nQk 108,108,109\n"
"Qmvaa 236,68,112\ns_waitcnt lgkmcnt(3)\nQa 192,192,180\nQa 193,193,181\nQmvaa 96,64,88\nQta 56,68,768\nQa 194,194,182\nQmvav 96,68,168\n"
"Qdv 136,59,37888\nQa 195,195,183\nQmvaa 128,64,92\ns_waitcnt lgkmcnt(3)\nQa 204,204,104\nQa 205,205,105\nQmvav 128,68,200\nQta 58,68,1792\n"
"Qa 206,206,106\nQmvaa 160,64,96\nbuffer_store_dwordx4 v[196:199], v60, s[24:27], s68 offen offset:1024 sc1\nQa 207,207,107\nQmvav 160,68,232\nQdv 148,59,39936\nQk 109,110,111\nQmvaa 192,64,100\n"
"Qta 60,68,8960\nQk 110,140,141\nQmvav 192,68,212\nQk 111,142,143\nQk 172,172,173\nQmvaa 224,64,104\nbuffer_store_dwordx4 v[108:111], v60, s[24:27], s68 offen offset:2048 sc1\nv_add_u32 v75, 1, v75\n"
"Qmvav 224,68,244\ns_waitcnt vmcnt(3)\nds_write_b32 v73, v75\ns_waitcnt lgkmcnt(10)\nQmvav 204,72,80\nQta 62,68,9984\ns_waitcnt lgkmcnt(5)\nQa 160,160,136\n"
"Qmvav 204,76,88\nQdv 180,59,34816\nQa 161,161,137\nQmvav 236,72,112\nQa 162,162,138\nQa 163,163,139\nQmvav 236,76,120\ns_waitcnt lgkmcnt(4)\n"
"Qa 224,224,148\nQa 225,225,149\nQmvav 96,72,144\nQta 64,68,16384\nQa 226,226,150\nQmvav 96,76,152\nQa 227,227,151\nQk 173,174,175\n"
"Qmvav 128,72,176\nQmvav 128,76,184\nds_read_b32 v66, v74\ns_waitcnt lgkmcnt(2)\nQa 236,236,180\nQmvav 160,72,208\nQta 66,68,17408\nQa 237,237,181\n"
"Qmvav 160,76,216\nQa 238,238,182\nQa 239,239,183\nQmvav 192,72,240\nQmvav 192,76,248\ns_waitcnt lgkmcnt(1)\nv_cmp_gt_u32 vcc, v75, v66\nQmvav 224,72,84\n"
"s_cbranch_vccnz .Lso1960r0\n.Lro1960r0:\nQmvav 224,76,116\nQta 68,68,24576\ns_add_u32 m0, s96, 65536\n.Lpg20:\ns_cbranch_execz .Lpg24\ns_add_u32 s95, s67, 86016\n"
"s_waitcnt lgkmcnt(5)\nQmvaa 204,56,0\nbuffer_load_dwordx4 v62, s[52:55], s95 offen lds\nQmvaa 204,60,28\nQmvaa 236,56,4\nQta 70,68,25600\nQmvaa 236,60,32\ns_add_u32 s95, s67, 125952\n"
"Qmvaa 96,56,8\nbuffer_load_dwordx4 v62, s[52:55], s95 offen offset:1024 lds\nQmvaa 96,60,36\nQta 72,68,32768\nQk 174,204,205\nQmvaa 128,56,12\nQk 175,206,207\nQk 236,236,237\n"
"Qmvaa 128,60,40\nbuffer_store_dwordx4 v[172:175], v60, s[24:27], s68 offen offset:3072 sc1\nQk 237,238,239\nQmvaa 160,56,16\nQta 74,68,33792\ns_add_u32 m0, s96, 73728\ns_add_u32 s95, s72, 86016\nQmvaa 160,60,44\n"
"buffer_load_dwordx4 v62, s[52:55], s95 offen lds\nQmvaa 192,56,20\nQk 238,96,97\nQk 239,98,99\nQmvaa 192,60,48\nQta 76,68,40960\ns_add_u32 s68, s64, 4096\nQmvaa 224,56,24\n"
"buffer_store_dwordx4 v[236:239], v60, s[24:27], s68 offen offset:0 sc1\ns_add_u32 s95, s72, 125952\nQmvaa 224,60,52\nbuffer_load_dwordx4 v62, s[52:55], s95 offen offset:1024 lds\ns_waitcnt lgkmcnt(3)\nQmaaa 116,64,80\nQta 78,68,41984\nQk 128,128,129\n"
"Qmaaa 116,68,108\nQk 129,130,131\nQk 130,160,161\nQmaaa 120,64,84\ns_add_u32 m0, s96, 81920\ns_add_u32 s95, s73, 86016\nQmaaa 120,68,112\nbuffer_load_dwordx4 v62, s[52:55], s95 offen lds\n"
"Qmaaa 124,64,88\nQta 56,68,49152\nQk 131,162,163\nQmaav 124,68,168\nbuffer_store_dwordx4 v[128:131], v60, s[24:27], s68 offen offset:1024 sc1\nQk 192,192,193\nQmaaa 128,64,92\nQk 193,194,195\n"
"Qk 194,224,225\nQmaav 128,68,200\nQta 58,68,50176\ns_add_u32 s95, s73, 125952\nQmaaa 132,64,96\nbuffer_load_dwordx4 v62, s[52:55], s95 offen offset:1024 lds\nQmaav 132,68,232\nQk 195,226,227\n"
"Qmaaa 136,64,100\nQta 60,68,57344\nv_bfe_u32 v61, v60, 6, 2\nQmaav 136,68,212\nv_lshrrev_b32 v62, 8, v60\nv_xor_b32 v61, v61, v62\nQmaaa 140,64,104\nv_bfe_u32 v62, v60, 5, 1\n"
"v_lshl_or_b32 v61, v61, 1, v62\nQmaav 140,68,244\nQta 62,68,58368\nv_and_b32 v62, 16, v60\ns_waitcnt lgkmcnt(4)\nQmaav 116,72,80\nv_mov_b32 v63, 0x3f80\nv_lshlrev_b32 v63, v62, v63\n"
"Qmaav 116,76,88\nv_min_u32 v62, 1, v61\nv_add_u32 v62, -1, v62\nQmaav 120,72,112\nv_and_b32 v62, v62, v63\nv_xor_b32 v61, 1, v61\nQmaav 120,76,120\nv_min_u32 v61, 1, v61\n"
"v_add_u32 v61, -1, v61\nQmaav 124,72,144\nQta 64,68,16640\nv_and_b32 v61, v61, v63\nQmaav 124,76,152\nv_accvgpr_write_b32 a202, v62\nv_accvgpr_write_b32 a203, v61\nQmaav 128,72,176\n"
"v_accvgpr_write_b32 a200, 0\nv_accvgpr_write_b32 a201, 0\nQmaav 128,76,184\nQta 66,68,17664\nv_accvgpr_write_b32 a204, 0\nQmaav 132,72,208\nbuffer_store_dwordx4 v[192:195], v60, s[24:27], s68 offen offset:2048 sc1\nv_accvgpr_write_b32 a205, 0\n"
"Qmaav 132,76,216\nQmaav 136,72,240\nQta 68,68,24832\nQmaav 136,76,248\nQmaav 140,72,84\nQmaav 140,76,116\nQta 70,68,25856\ns_waitcnt lgkmcnt(4)\n"
"Qmaaa 116,56,0\nQmaaa 116,60,28\nQmaaa 120,56,4\nQmaaa 120,60,32\nQmaaa 124,56,8\nQta 72,68,33024\nQmaaa 124,60,36\ns_nop 2\n"
"ds_write_b128 v58, a[116:119] offset:0\nQmaaa 128,56,12\nQmaaa 128,60,40\nQta 74,68,34048\nQmaaa 132,56,16\nds_write_b128 v58, a[120:123] offset:1024\nQmaaa 132,60,44\nQmaaa 136,56,20\n"
"Qta 76,68,41216\nQmaaa 136,60,48\nds_write_b128 v58, a[124:127] offset:2048\nQmaaa 140,56,24\nQmaaa 140,60,52\nQta 78,68,42240\ns_waitcnt lgkmcnt(7)\nQmaaa 144,64,80\n"
"ds_write_b128 v58, a[128:131] offset:3072\nQmaaa 144,68,108\nQmaaa 148,64,84\nQmaaa 148,68,112\nds_write_b128 v58, a[132:135] offset:4096\nQmaaa 152,64,88\nQta 56,68,49408\nQmaav 152,68,168\n"
"Qmaaa 156,64,92\nds_write_b128 v58, a[136:139] offset:5120\nQmaav 156,68,200\nQta 58,68,50432\nQmaaa 160,64,96\nQmaav 160,68,232\nds_write_b128 v58, a[140:143] offset:6144\nQmaaa 164,64,100\n"
"Qta 60,68,57600\nQmaav 164,68,212\nQmaaa 168,64,104\nQmaav 168,68,244\nQta 62,68,58624\ns_waitcnt lgkmcnt(8)\nQmaav 144,72,80\nv_add_u32 v75, 1, v75\n"
"Qmaav 144,76,88\ns_waitcnt vmcnt(8)\nds_write_b32 v73, v75\nQmaav 148,72,112\nQmaav 148,76,120\nQmaav 152,72,144\nQta 64,68,16896\nQmaav 152,76,152\n"
"Qmaav 156,72,176\nQmaav 156,76,184\nQta 66,68,17920\nQmaav 160,72,208\nQmaav 160,76,216\nds_read_b32 v66, v74\nQmaav 164,72,240\nQta 68,68,25088\n"
"Qmaav 164,76,248\nQmaav 168,72,84\nQmaav 168,76,116\ns_waitcnt lgkmcnt(1)\nv_cmp_gt_u32 vcc, v75, v66\ns_waitcnt lgkmcnt(5)\nQmaaa 144,56,0\ns_cbranch_vccnz .Lso2046r0\n"
".Lro2046r0:\nQmaaa 144,60,28\nQta 70,68,26112\nQmaaa 148,56,4\nQmaaa 148,60,32\nQdv 104,59,2048\nQmaaa 152,56,8\nQta 72,68,33280\n"
"Qmaaa 152,60,36\ns_nop 0\nds_write_b128 v58, a[144:147] offset:33792\nQmaaa 156,56,12\nQdv 136,59,3072\nQmaaa 156,60,40\nQta 74,68,34304\nQmaaa 160,56,16\n"
"ds_write_b128 v58, a[148:151] offset:34816\nQmaaa 160,60,44\nQdv 148,59,5120\nQmaaa 164,56,20\nQta 76,68,41472\nQmaaa 164,60,48\nds_write_b128 v58, a[152:155] offset:35840\nQmaaa 168,56,24\n"
"Qdv 180,59,0\ns_waitcnt lgkmcnt(9)\nQa 8,8,104\nQmaaa 168,60,52\nQta 78,68,42496\nQa 9,9,105\ns_waitcnt lgkmcnt(10)\nQmvaa 0,64,80\n"
"ds_write_b128 v58, a[156:159] offset:36864\nQmvaa 0,68,108\nQa 10,10,106\nQa 11,11,107\nQmvaa 4,64,84\ns_waitcnt lgkmcnt(8)\nQa 12,12,136\nQa 13,13,137\n"
"Qmvaa 4,68,112\nds_write_b128 v58, a[160:163] offset:37888\nQmvaa 8,64,88\nQta 56,68,49664\nQa 14,14,138\nQa 15,15,139\nQmvav 8,68,168\nQdv 104,59,4096\n"
"s_waitcnt lgkmcnt(8)\nQa 20,20,148\nQmvaa 12,64,92\nds_write_b128 v58, a[164:167] offset:38912\nQmvav 12,68,200\nQta 58,68,50688\nQa 21,21,149\nQmvaa 16,64,96\n"
"Qa 22,22,150\nQa 23,23,151\nQmvav 16,68,232\nQdv 136,59,6144\ns_waitcnt lgkmcnt(8)\nQa 0,0,180\nQmvaa 20,64,100\nQta 60,68,57856\n"
"Qa 1,1,181\nQmvav 20,68,212\nds_write_b128 v58, a[168:171] offset:39936\nQmvaa 24,64,104\nQa 2,2,182\nQa 3,3,183\nQmvav 24,68,244\nQta 62,68,58880\n"
"s_waitcnt lgkmcnt(6)\nQa 16,16,104\ns_waitcnt lgkmcnt(6)\nQmvav 0,72,80\nQdv 148,59,1024\nv_add_u32 v75, 1, v75\nQmvav 0,76,88\ns_waitcnt vmcnt(1)\n"
"ds_write_b32 v73, v75\nQmvav 4,72,112\nQa 17,17,105\nQa 18,18,106\nQmvav 4,76,120\nQa 19,19,107\ns_waitcnt lgkmcnt(5)\nQa 24,24,136\n"
"Qmvav 8,72,144\nQta 64,69,0\nQa 25,25,137\nQmvav 8,76,152\nQa 26,26,138\nQa 27,27,139\nQmvav 12,72,176\ns_waitcnt vmcnt(0)\n"
"buffer_store_dword v67, v77, s[24:27], 0 offen sc1\nQmvav 12,76,184\nQta 66,69,512\ns_waitcnt lgkmcnt(3)\nQa 4,4,148\nQmvav 16,72,208\nQa 5,5,149\nQa 6,6,150\n"
"Qmvav 16,76,216\nds_read_b32 v66, v74\nQa 7,7,151\nQmvav 20,72,240\nQta 68,69,1024\nQmvav 20,76,248\nQmvav 24,72,84\nQmvav 24,76,116\n"
"s_waitcnt lgkmcnt(1)\nv_cmp_gt_u32 vcc, v75, v66\ns_waitcnt lgkmcnt(6)\nQmvaa 0,56,0\ns_cbranch_vccnz .Lso2088r0\n.Lro2088r0:\nQmvaa 0,60,28\nQta 70,69,1536\n"
"Qmvaa 4,56,4\nQmvaa 4,60,32\nQdv 180,59,33792\nQmvaa 8,56,8\nQta 72,69,8192\nQmvaa 8,60,36\nQmvaa 12,56,12\nQdv 104,59,34816\n"
"Qmvaa 12,60,40\nQta 74,69,8704\nQmvaa 16,56,16\nQmvaa 16,60,44\nQdv 136,59,35840\nQmvaa 20,56,20\nQta 76,69,9216\nQmvaa 20,60,48\n"
"s_waitcnt lgkmcnt(5)\nQa 28,28,180\nQa 29,29,181\nQmvaa 24,56,24\nQdv 148,59,36864\nQa 30,30,182\nQmvaa 24,60,52\nQta 78,69,9728\n"
"Qa 31,31,183\ns_waitcnt lgkmcnt(11)\nQmaaa 172,64,80\ns_waitcnt lgkmcnt(5)\nQa 32,32,104\nQa 33,33,105\nQmaaa 172,68,108\nQa 34,34,106\n"
"Qa 35,35,107\nQmaaa 176,64,84\nQmaaa 176,68,112\ns_waitcnt lgkmcnt(3)\nQa 36,36,136\nQa 37,37,137\nQmaaa 180,64,88\nQta 56,69,16384\n"
"Qa 38,38,138\nQmaav 180,68,168\nQdv 180,59,37888\nQa 39,39,139\nQmaaa 184,64,92\ns_waitcnt lgkmcnt(3)\nQa 40,40,148\nQa 41,41,149\n"
"Qmaav 184,68,200\nQta 58,69,16896\nQa 42,42,150\nQmaaa 188,64,96\nQdv 104,59,38912\nQa 43,43,151\nQmaav 188,68,232\nQmaaa 192,64,100\n"
"Qta 60,69,17408\nQmaav 192,68,212\nQdv 136,59,39936\nQmaaa 196,64,104\ns_waitcnt lgkmcnt(4)\nQa 44,44,180\nQa 45,45,181\nQmaav 196,68,244\n"
"Qta 62,69,17920\nQa 46,46,182\ns_waitcnt lgkmcnt(5)\nQmaav 172,72,80\nQa 47,47,183\nQmaav 172,76,88\nQmaav 176,72,112\ns_waitcnt lgkmcnt(3)\n"
"Qa 48,48,104\nQa 49,49,105\nQmaav 176,76,120\nQa 50,50,106\nQa 51,51,107\nQmaav 180,72,144\nQta 64,68,17152\nQmaav 180,76,152\n"
"s_waitcnt lgkmcnt(2)\nQa 52,52,136\nQa 53,53,137\nQmaav 184,72,176\nQa 54,54,138\nQa 55,55,139\nQmaav 184,76,184\nQta 66,68,18176\n"
"Qmaav 188,72,208\nQmaav 188,76,216\nQmaav 192,72,240\nQta 68,68,25344\nQmaav 192,76,248\nQmaav 196,72,84\nQmaav 196,76,116\nQta 70,68,26368\n"
"s_waitcnt lgkmcnt(4)\nQmaaa 172,56,0\nQmaaa 172,60,28\nQmaaa 176,56,4\nQmaaa 176,60,32\nQmaaa 180,56,8\nQta 72,68,33536\nQmaaa 180,60,36\n"
"s_nop 2\nds_write_b128 v58, a[172:175] offset:0\nQmaaa 184,56,12\nQmaaa 184,60,40\nQta 74,68,34560\nQmaaa 188,56,16\nds_write_b128 v58, a[176:179] offset:1024\nQmaaa 188,60,44\n"
"Qmaaa 192,56,20\nQta 76,68,41728\nQmaaa 192,60,48\nds_write_b128 v58, a[180:183] offset:2048\nQmaaa 196,56,24\nQmaaa 196,60,52\nQta 78,68,42752\ns_waitcnt lgkmcnt(7)\n"
"Qmvaa 28,64,80\nds_write_b128 v58, a[184:187] offset:3072\nQmvaa 28,68,108\nQmvaa 32,64,84\nQmvaa 32,68,112\nds_write_b128 v58, a[188:191] offset:4096\nQmvaa 36,64,88\nQta 56,68,49920\n"
"Qmvav 36,68,168\nQmvaa 40,64,92\nds_write_b128 v58, a[192:195] offset:5120\nQmvav 40,68,200\nQta 58,68,50944\nQmvaa 44,64,96\nQmvav 44,68,232\nbuffer_load_dword v78, v76, s[24:27], 0 offen sc1\n"
"Qmvaa 48,64,100\nQta 60,68,58112\nQmvav 48,68,212\nds_write_b128 v58, a[196:199] offset:6144\nQmvaa 52,64,104\nQmvav 52,68,244\nQta 62,68,59136\ns_waitcnt lgkmcnt(8)\n"
"Qmvav 28,72,80\nv_add_u32 v75, 1, v75\nQmvav 28,76,88\nds_write_b32 v73, v75\nQmvav 32,72,112\nQmvav 32,76,120\nQmvav 36,72,144\nQta 64,69,256\n"
"Qmvav 36,76,152\nQmvav 40,72,176\nQmvav 40,76,184\nQta 66,69,768\nQmvav 44,72,208\nQmvav 44,76,216\nds_read_b32 v66, v74\nQmvav 48,72,240\n"
"Qta 68,69,1280\nQmvav 48,76,248\nQmvav 52,72,84\nQmvav 52,76,116\ns_waitcnt lgkmcnt(1)\nv_cmp_gt_u32 vcc, v75, v66\ns_waitcnt lgkmcnt(5)\nQmvaa 28,56,0\n"
"s_cbranch_vccnz .Lso2172r0\n.Lro2172r0:\nQmvaa 28,60,28\nQta 70,69,1792\nQmvaa 32,56,4\nQmvaa 32,60,32\nQdv 148,59,2048\nQmvaa 36,56,8\n"
"Qta 72,69,8448\nQmvaa 36,60,36\nQmvaa 40,56,12\nQdv 180,59,3072\nQmvaa 40,60,40\nQta 74,69,8960\nQmvaa 44,56,16\nQmvaa 44,60,44\n"
"Qdv 104,59,5120\nQmvaa 48,56,20\nQta 76,69,9472\nQmvaa 48,60,48\ns_waitcnt lgkmcnt(5)\nQa 156,156,148\nQa 157,157,149\nQmvaa 52,56,24\n"
"Qdv 136,59,0\nQa 158,158,150\nQmvaa 52,60,52\nQta 78,69,9984\nQa 159,159,151\ns_waitcnt lgkmcnt(11)\nQmvaa 92,64,80\ns_waitcnt lgkmcnt(5)\n"
"Qa 188,188,180\nQa 189,189,181\nQmvaa 92,68,108\nQa 190,190,182\nQa 191,191,183\nQmvaa 124,64,84\ns_waitcnt vmcnt(0)\nv_sub_u32 v66, v78, v65\n"
"v_cmp_gt_u32 vcc, 1, v66\ns_and_b64 vcc, vcc, s[28:29]\ns_cbranch_scc1 .Lx0_w\n.Lx0_r:\nQmvaa 124,68,112\ns_add_u32 s69, s98, 0\nbuffer_load_dwordx4 a[208:211], v60, s[24:27], s69 offen offset:0 sc1\nQmvaa 156,64,88\n"
"Qta 56,69,16640\ns_waitcnt lgkmcnt(4)\nQa 252,252,104\nQmvav 156,68,168\nbuffer_load_dwordx4 a[212:215], v60, s[24:27], s69 offen offset:1024 sc1\nQmvaa 188,64,92\nQdv 148,59,4096\nQa 253,253,105\n"
"Qmvav 188,68,200\nbuffer_load_dwordx4 a[216:219], v60, s[24:27], s69 offen offset:2048 sc1\nQmvaa 220,64,96\nQta 58,69,17152\nQa 254,254,106\nQa 255,255,107\nQmvav 220,68,232\nbuffer_load_dwordx4 a[220:223], v60, s[24:27], s69 offen offset:3072 sc1\n"
"Qmvaa 252,64,100\nQdv 180,59,6144\ns_waitcnt lgkmcnt(5)\nQa 92,92,136\nQmvav 252,68,212\ns_add_u32 s69, s98, 4096\nbuffer_load_dwordx4 a[224:227], v60, s[24:27], s69 offen offset:0 sc1\nQmvaa 100,64,104\n"
"Qta 60,69,17664\nQa 93,93,137\nQa 94,94,138\nQa 95,95,139\nQmvav 100,68,244\nbuffer_load_dwordx4 a[228:231], v60, s[24:27], s69 offen offset:1024 sc1\ns_waitcnt lgkmcnt(9)\nQmvav 92,72,80\n"
"Qdv 104,59,1024\ns_waitcnt lgkmcnt(4)\nQa 220,220,148\nQmvav 92,76,88\nbuffer_load_dwordx4 a[232:235], v60, s[24:27], s69 offen offset:2048 sc1\nQmvav 124,72,112\nQta 62,69,18176\nQa 221,221,149\n"
"Qmvav 124,76,120\nbuffer_load_dwordx4 a[236:239], v60, s[24:27], s69 offen offset:3072 sc1\nQmvav 156,72,144\nQa 222,222,150\nQa 223,223,151\nQmvav 156,76,152\ns_add_u32 s69, s98, 8192\nbuffer_load_dwordx4 a[240:243], v60, s[24:27], s69 offen offset:0 sc1\n"
"Qmvav 188,72,176\ns_waitcnt lgkmcnt(3)\nQa 100,100,180\nQa 101,101,181\nQmvav 188,76,184\nbuffer_load_dwordx4 a[244:247], v60, s[24:27], s69 offen offset:1024 sc1\nQmvav 220,72,208\nv_add_u32 v75, 1, v75\n"
"Qa 102,102,182\nQmvav 220,76,216\nds_write_b32 v73, v75\nQa 103,103,183\nQmvav 252,72,240\nbuffer_load_dwordx4 a[248:251], v60, s[24:27], s69 offen offset:2048 sc1\nQmvav 252,76,248\ns_waitcnt lgkmcnt(2)\n"
"Qa 124,124,104\nQa 125,125,105\nQmvav 100,72,84\nQa 126,126,106\nQa 127,127,107\nQmvav 100,76,116\ns_waitcnt lgkmcnt(1)\nQmvaa 92,56,0\n"
"Qmvaa 92,60,28\nQmvaa 124,56,4\nQmvaa 124,60,32\n.Lpg21:\ns_cbranch_execz .Lpg25\nds_read_b32 v66, v74\nQmvaa 156,56,8\nQmvaa 156,60,36\n"
"Qmvaa 188,56,12\nQmvaa 188,60,40\ns_waitcnt lgkmcnt(0)\nv_cmp_gt_u32 vcc, v75, v66\nQmvaa 220,56,16\ns_cbranch_vccnz .Lso2222r0\n.Lro2222r0:\nQmvaa 220,60,44\n"
"Qmvaa 252,56,20\nQmvaa 252,60,48\nQmvaa 100,56,24\nQmvaa 100,60,52\ns_waitcnt vmcnt(10)\nQmvaa 92,202,208\nQmvaa 124,200,208\ns_waitcnt vmcnt(9)\n"
"Qmvaa 156,202,212\nQmvaa 188,200,212\ns_nop 3\nQk 92,92,93\nQk 93,94,95\nds_write_b64 v56, v[92:93] offset:64\ns_waitcnt vmcnt(8)\nQmvaa 220,202,216\n"
"Qk 124,124,125\nQk 125,126,127\nds_write_b64 v56, v[124:125] offset:3392\nQmvaa 252,200,216\nQk 156,156,157\nQk 157,158,159\nds_write_b64 v56, v[156:157] offset:6720\ns_waitcnt vmcnt(7)\n"
"Qmvaa 100,202,220\nQk 188,188,189\nQk 189,190,191\nds_write_b64 v56, v[188:189] offset:10048\nQmvaa 0,200,220\nQk 220,220,221\nQk 221,222,223\nds_write_b64 v56, v[220:221] offset:13376\n"
"s_waitcnt vmcnt(6)\nQmvaa 4,202,224\nQk 252,252,253\nQk 253,254,255\nds_write_b64 v56, v[252:253] offset:16704\nQmvaa 8,200,224\nQk 100,100,101\nQk 101,102,103\n"
"ds_write_b64 v56, v[100:101] offset:20032\ns_waitcnt vmcnt(5)\nQmvaa 12,202,228\nQk 0,0,1\nQk 1,2,3\nds_write_b64 v56, v[0:1] offset:0\nQmvaa 16,200,228\nQk 4,4,5\n"
"Qk 5,6,7\nds_write_b64 v56, v[4:5] offset:3328\ns_waitcnt vmcnt(4)\nQmvaa 20,202,232\nQk 8,8,9\nQk 9,10,11\nds_write_b64 v56, v[8:9] offset:6656\nQmvaa 24,200,232\n"
"Qk 12,12,13\nQk 13,14,15\nds_write_b64 v56, v[12:13] offset:9984\ns_waitcnt vmcnt(3)\nQmvaa 28,202,236\nQk 16,16,17\nQk 17,18,19\nds_write_b64 v56, v[16:17] offset:13312\n"
"Qmvaa 32,200,236\nQk 20,20,21\nQk 21,22,23\nds_write_b64 v56, v[20:21] offset:16640\ns_waitcnt vmcnt(2)\nQmvaa 36,202,240\nQk 24,24,25\nQk 25,26,27\n"
"ds_write_b64 v56, v[24:25] offset:19968\nQmvaa 40,200,240\nQk 28,28,29\nQk 29,30,31\nds_write_b64 v56, v[28:29] offset:32\ns_waitcnt vmcnt(1)\nQmvaa 44,202,244\nQk 32,32,33\n"
"Qk 33,34,35\nds_write_b64 v56, v[32:33] offset:3360\nQmvaa 48,200,244\nQk 36,36,37\nQk 37,38,39\nds_write_b64 v56, v[36:37] offset:6688\ns_waitcnt vmcnt(0)\nQmvaa 52,202,248\n"
"Qk 40,40,41\nQk 41,42,43\nds_write_b64 v56, v[40:41] offset:10016\nQk 44,44,45\nQk 45,46,47\nds_write_b64 v56, v[44:45] offset:13344\nQk 48,48,49\nQk 49,50,51\n"
"ds_write_b64 v56, v[48:49] offset:16672\nQk 52,52,53\nQk 53,54,55\nds_write_b64 v56, v[52:53] offset:20000\ns_waitcnt lgkmcnt(0)\ns_barrier\nQda 0,57,0\nQda 4,57,3328\n"
"Qda 8,57,6656\nQda 12,57,9984\nQda 16,57,13312\nQda 20,57,16640\nQda 24,57,19968\nQda 28,71,64\nQda 32,71,6720\nQda 36,71,13376\n"
"Qda 40,71,20032\ns_mov_b32 s68, 0\ns_waitcnt lgkmcnt(10)\nbuffer_store_dwordx4 a[0:3], v64, s[40:43], s68 offen offset:0 sc1\ns_mov_b32 s68, 40960\ns_waitcnt lgkmcnt(9)\nbuffer_store_dwordx4 a[4:7], v64, s[40:43], s68 offen offset:0 sc1\ns_mov_b32 s68, 81920\n"
"s_waitcnt lgkmcnt(8)\nbuffer_store_dwordx4 a[8:11], v64, s[40:43], s68 offen offset:0 sc1\ns_mov_b32 s68, 122880\ns_waitcnt lgkmcnt(7)\nbuffer_store_dwordx4 a[12:15], v64, s[40:43], s68 offen offset:0 sc1\ns_mov_b32 s68, 163840\ns_waitcnt lgkmcnt(6)\nbuffer_store_dwordx4 a[16:19], v64, s[40:43], s68 offen offset:0 sc1\n"
"s_mov_b32 s68, 204800\ns_waitcnt lgkmcnt(5)\nbuffer_store_dwordx4 a[20:23], v64, s[40:43], s68 offen offset:0 sc1\ns_mov_b32 s68, 245760\ns_waitcnt lgkmcnt(4)\nbuffer_store_dwordx4 a[24:27], v64, s[40:43], s68 offen offset:0 sc1\ns_mov_b32 s68, 0\ns_waitcnt lgkmcnt(3)\n"
"buffer_store_dwordx4 a[28:31], v72, s[40:43], s68 offen offset:0 sc1\ns_mov_b32 s68, 81920\ns_waitcnt lgkmcnt(2)\nbuffer_store_dwordx4 a[32:35], v72, s[40:43], s68 offen offset:0 sc1\ns_mov_b32 s68, 163840\ns_waitcnt lgkmcnt(1)\nbuffer_store_dwordx4 a[36:39], v72, s[40:43], s68 offen offset:0 sc1\ns_mov_b32 s68, 245760\n"
"s_cmp_eq_u32 s93, 1\ns_cselect_b32 s68, 0x7ffff000, s68\ns_waitcnt lgkmcnt(0)\nbuffer_store_dwordx4 a[40:43], v72, s[40:43], s68 offen offset:0 sc1\ns_branch .Lfin\n.Lrole1:\n.Lpg22:\ns_cbranch_execz .Lpg26\n"
"Qmvaa 96,64,88\nQta 56,69,49408\ns_lshl_b32 s46, %19, 4\nQmvav 96,68,168\nbuffer_load_dwordx4 a[84:87], v60, s[48:51], s31 offen offset:1024 sc1\nQmvaa 128,64,92\nds_read_b32 v66, v74\ns_add_u32 s46, s46, 128\n"
"Qmvav 128,68,200\nQta 58,69,50432\nv_mov_b32 v77, 0x7ffff000\nQmvaa 160,64,96\nQmvav 160,68,232\nbuffer_load_dwordx4 a[88:91], v60, s[48:51], s31 offen offset:2048 sc1\nQmvaa 192,64,100\ns_waitcnt lgkmcnt(1)\n"
"v_cmp_gt_u32 vcc, v75, v66\nQmvav 192,68,212\ns_cbranch_vccnz .Lso1525r1\n.Lro1525r1:\nQmvaa 224,64,104\nQta 60,69,57600\nQmvav 224,68,244\nbuffer_load_dwordx4 a[92:95], v60, s[48:51], s31 offen offset:3072 sc1\n"
"s_waitcnt vmcnt(31)\ns_waitcnt lgkmcnt(4)\nQmaav 140,72,84\nQmaav 140,76,116\nQta 62,69,58624\nQmaav 136,72,240\ns_add_u32 m0, s96, 65536\ns_add_u32 s95, s67, 86016\n"
"Qmaav 136,76,248\nbuffer_load_dwordx4 v62, s[52:55], s95 offen lds\nQmaav 132,72,208\nQta 64,69,49664\nQmaav 132,76,216\nQmaav 128,72,176\nQmaav 128,76,184\nQta 66,69,50688\n"
"s_add_u32 s95, s67, 125952\nQmaav 124,72,144\nbuffer_load_dwordx4 v62, s[52:55], s95 offen offset:1024 lds\nQmaav 124,76,152\nQmaav 120,72,112\nQta 68,69,57856\nQmaav 120,76,120\ns_add_u32 m0, s96, 73728\n"
"s_add_u32 s95, s72, 86016\nQmaav 116,72,80\nbuffer_load_dwordx4 v62, s[52:55], s95 offen lds\nQmaav 116,76,88\nQta 70,69,58880\ns_waitcnt lgkmcnt(4)\nQmaav 144,56,80\nQmaav 144,60,88\n"
"s_add_u32 s95, s72, 125952\nQmaav 148,56,112\nbuffer_load_dwordx4 v62, s[52:55], s95 offen offset:1024 lds\nQmaav 148,60,120\nQmaav 152,56,144\nQta 72,69,49920\nQmaav 152,60,152\ns_add_u32 m0, s96, 81920\n"
"s_add_u32 s95, s73, 86016\nQmaav 156,56,176\nbuffer_load_dwordx4 v62, s[52:55], s95 offen lds\nQmaav 156,60,184\nQta 74,69,50944\nQmaav 160,56,208\nQmaav 160,60,216\nQmaav 164,56,240\n"
"Qta 76,69,58112\ns_add_u32 s95, s73, 125952\nQmaav 164,60,248\nbuffer_load_dwordx4 v62, s[52:55], s95 offen offset:1024 lds\nQmaav 168,56,84\nQmaav 168,60,116\nQta 78,69,59136\ns_waitcnt lgkmcnt(4)\n"
"Qmvav 24,64,84\ns_add_u32 m0, s96, 90112\ns_add_u32 s95, s67, 86016\nQmvav 24,68,116\nbuffer_load_dwordx4 v61, s[52:55], s95 offen lds\nQmvav 20,64,240\nQmvav 20,68,248\nQmvav 16,64,208\n"
"Qta 56,68,0\ns_add_u32 s95, s67, 105472\nQmvav 16,68,216\nbuffer_load_dwordx4 v61, s[52:55], s95 offen offset:1024 lds\nQmvav 12,64,176\nQmvav 12,68,184\nQta 58,68,512\nQmvav 8,64,144\n"
"s_add_u32 s31, s74, 32768\nQmvav 8,68,152\nbuffer_load_dwordx4 a[96:99], v60, s[48:51], s31 offen offset:0 sc1\nQmvav 4,64,112\nQta 60,68,1024\nQmvav 4,68,120\nQmvav 0,64,80\nQmvav 0,68,88\n"
"Qta 62,68,1536\ns_waitcnt lgkmcnt(4)\nQmvav 28,72,80\nbuffer_load_dwordx4 a[100:103], v60, s[48:51], s31 offen offset:1024 sc1\nQmvav 28,76,88\nQmvav 32,72,112\nQmvav 32,76,120\nQmvav 36,72,144\n"
"Qta 64,68,256\nQmvav 36,76,152\nbuffer_load_dwordx4 a[104:107], v60, s[48:51], s31 offen offset:2048 sc1\nQmvav 40,72,176\nQmvav 40,76,184\nQta 66,68,768\nQmvav 44,72,208\ns_add_u32 s31, s74, 43008\n"
"v_cmp_eq_u32 s[44:45], s46, v60\ns_nop 1\nv_cndmask_b32 v77, v77, v76, s[44:45]\nv_add_u32 v67, 1, v65\ns_xor_b32 s46, %19, 12\ns_lshl_b32 s28, 1, s46\nQmvav 44,76,216\nbuffer_load_dwordx4 a[108:111], v60, s[48:51], s31 offen offset:0 sc1\n"
"Qmvav 48,72,240\nQta 68,68,1280\nQmvav 48,76,248\nv_add_u32 v75, 1, v75\nQmvav 52,72,84\ns_waitcnt vmcnt(37)\nds_write_b32 v73, v75\nQmvav 52,76,116\n"
"Qta 70,68,1792\ns_waitcnt lgkmcnt(5)\nQmaav 196,56,84\nbuffer_load_dwordx4 a[112:115], v60, s[48:51], s31 offen offset:1024 sc1\nQmaav 196,60,116\nQmaav 192,56,240\nQmaav 192,60,248\nQmaav 188,56,208\n"
"Qta 72,68,8192\nQmaav 188,60,216\nbuffer_load_dwordx4 v[168:171], v60, s[48:51], s31 offen offset:2048 sc1\nQmaav 184,56,176\nds_read_b32 v66, v74\nQmaav 184,60,184\nQta 74,68,9216\nQmaav 180,56,144\n"
"Qmaav 180,60,152\nbuffer_load_dwordx4 v[200:203], v60, s[48:51], s31 offen offset:3072 sc1\nQmaav 176,56,112\ns_waitcnt lgkmcnt(1)\nv_cmp_gt_u32 vcc, v75, v66\nQmaav 176,60,120\ns_cbranch_vccnz .Lso1595r1\n.Lro1595r1:\n"
"Qmaav 172,56,80\nQta 76,68,16384\ns_add_u32 s31, s74, 47104\nQmaav 172,60,88\nbuffer_load_dwordx4 v[232:235], v60, s[48:51], s31 offen offset:0 sc1\ns_waitcnt lgkmcnt(4)\nQmvav 92,64,80\nQmvav 92,68,88\n"
"Qta 78,68,17408\nQmvav 124,64,112\nQmvav 124,68,120\nbuffer_load_dwordx4 v[212:215], v60, s[48:51], s31 offen offset:1024 sc1\nQmvav 156,64,144\nQta 56,68,8448\nQmvav 156,68,152\nQmvav 188,64,176\n"
"Qmvav 188,68,184\nQta 58,68,9472\nQmvav 220,64,208\nbuffer_load_dwordx4 v[244:247], v60, s[48:51], s31 offen offset:2048 sc1\nQmvav 220,68,216\nQmvav 252,64,240\nQta 60,68,16640\nQmvav 252,68,248\n"
"s_add_u32 m0, s91, 0\ns_add_u32 s31, s77, 28672\nQmvav 100,64,84\nbuffer_load_dwordx4 v60, s[48:51], s31 offen sc1 lds\nQmvav 100,68,116\nQta 62,68,17664\ns_waitcnt lgkmcnt(4)\nQmaav 224,72,84\n"
"Qmaav 224,76,116\nQmaav 220,72,240\nbuffer_load_dwordx4 v60, s[48:51], s31 offen offset:1024 sc1 lds\nQmaav 220,76,248\nQmaav 216,72,208\nQta 64,68,8704\nQmaav 216,76,216\nQmaav 212,72,176\n"
"buffer_load_dwordx4 v60, s[48:51], s31 offen offset:2048 sc1 lds\nQmaav 212,76,184\nQta 66,68,9728\nQmaav 208,72,144\nQmaav 208,76,152\nQmaav 204,72,112\nQta 68,68,16896\nQmaav 204,76,120\n"
"buffer_load_dwordx4 v60, s[48:51], s31 offen offset:3072 sc1 lds\nQmaav 200,72,80\nQmaav 200,76,88\nQta 70,68,17920\ns_waitcnt lgkmcnt(4)\nQmaav 228,56,80\ns_add_u32 m0, s91, 4096\ns_add_u32 s31, s77, 32768\n"
"Qmaav 228,60,88\nbuffer_load_dwordx4 v60, s[48:51], s31 offen sc1 lds\nQmaav 232,56,112\nQmaav 232,60,120\nQmaav 236,56,144\nQta 72,68,8960\nQmaav 236,60,152\nbuffer_load_dwordx4 v60, s[48:51], s31 offen offset:1024 sc1 lds\n"
"Qmaav 240,56,176\nQmaav 240,60,184\nQta 74,68,9984\nQmaav 244,56,208\nQmaav 244,60,216\nbuffer_load_dwordx4 v60, s[48:51], s31 offen offset:2048 sc1 lds\nQmaav 248,56,240\nQta 76,68,17152\n"
"Qmaav 248,60,248\nQmaav 252,56,84\nQmaav 252,60,116\nQta 78,68,18176\ns_add_u32 m0, s91, 33792\ns_add_u32 s31, s77, 43008\ns_waitcnt lgkmcnt(4)\nQmvav 172,64,84\n"
"buffer_load_dwordx4 v60, s[48:51], s31 offen sc1 lds\nQmvav 172,68,116\nQmvav 140,64,240\nQmvav 140,68,248\nQmvav 108,64,208\nQta 56,68,24576\nQmvav 108,68,216\nbuffer_load_dwordx4 v60, s[48:51], s31 offen offset:1024 sc1 lds\n"
"Qmvav 228,64,176\nQmvav 228,68,184\nQta 58,68,25600\nQmvav 196,64,144\nQmvav 196,68,152\nbuffer_load_dwordx4 v60, s[48:51], s31 offen offset:2048 sc1 lds\nQmvav 164,64,112\nQta 60,68,32768\n"
"Qmvav 164,68,120\nv_add_u32 v75, 1, v75\nQmvav 132,64,80\ns_waitcnt vmcnt(49)\nds_write_b32 v73, v75\nQmvav 132,68,88\nQta 62,68,33792\ns_waitcnt lgkmcnt(5)\n"
"Qmvav 204,72,80\nbuffer_load_dwordx4 v60, s[48:51], s31 offen offset:3072 sc1 lds\nQmvav 204,76,88\nQmvav 236,72,112\nQmvav 236,76,120\nQmvav 96,72,144\nQta 64,68,24832\ns_add_u32 m0, s91, 37888\n"
"s_add_u32 s31, s77, 47104\nQmvav 96,76,152\nbuffer_load_dwordx4 v60, s[48:51], s31 offen sc1 lds\nQmvav 128,72,176\nds_read_b32 v66, v74\nQmvav 128,76,184\nQta 66,68,25856\nQmvav 160,72,208\n"
"Qmvav 160,76,216\nbuffer_load_dwordx4 v60, s[48:51], s31 offen offset:1024 sc1 lds\nQmvav 192,72,240\ns_waitcnt lgkmcnt(1)\nv_cmp_gt_u32 vcc, v75, v66\nQmvav 192,76,248\ns_cbranch_vccnz .Lso1665r1\n.Lro1665r1:\n"
"Qmvav 224,72,84\nQta 68,68,33024\nQmvav 224,76,116\nbuffer_load_dwordx4 v60, s[48:51], s31 offen offset:2048 sc1 lds\ns_waitcnt vmcnt(38)\ns_waitcnt lgkmcnt(4)\nQmaaa 140,56,24\nQmaaa 140,60,52\n"
"Qta 70,68,34048\nQmaaa 136,56,20\ns_add_u32 m0, s96, 0\ns_add_u32 s95, s67, 126976\nQmaaa 136,60,48\nbuffer_load_dwordx4 v61, s[52:55], s95 offen lds\nQmaaa 132,56,16\nQta 72,68,25088\n"
"Qmaaa 132,60,44\nQmaaa 128,56,12\nQmaaa 128,60,40\nQta 74,68,26112\ns_add_u32 s95, s67, 146432\nQmaaa 124,56,8\nbuffer_load_dwordx4 v61, s[52:55], s95 offen offset:1024 lds\nQmaaa 124,60,36\n"
"Qmaaa 120,56,4\nQta 76,68,33280\nQmaaa 120,60,32\ns_add_u32 s31, s75, 28672\nQmaaa 116,56,0\nbuffer_load_dwordx4 v[80:83], v60, s[48:51], s31 offen offset:0 sc1\nQmaaa 116,60,28\nQta 78,68,34304\n"
"s_waitcnt lgkmcnt(4)\nQmaaa 144,64,0\nQmaaa 144,68,28\nQmaaa 148,64,4\nbuffer_load_dwordx4 v[112:115], v60, s[48:51], s31 offen offset:1024 sc1\nQmaaa 148,68,32\nQmaaa 152,64,8\nQta 56,68,25344\n"
"Qmaaa 152,68,36\nQmaaa 156,64,12\nbuffer_load_dwordx4 v[144:147], v60, s[48:51], s31 offen offset:2048 sc1\nQmaaa 156,68,40\nQta 58,68,26368\nQmaaa 160,64,16\nQmaaa 160,68,44\nQmaaa 164,64,20\n"
"Qta 60,68,33536\nQmaaa 164,68,48\nbuffer_load_dwordx4 v[176:179], v60, s[48:51], s31 offen offset:3072 sc1\nQmaaa 168,64,24\nQmaaa 168,68,52\nQta 62,68,34560\ns_waitcnt lgkmcnt(4)\nQmvaa 24,72,24\n"
"s_add_u32 s31, s75, 32768\nQmvaa 24,76,52\nbuffer_load_dwordx4 v[208:211], v60, s[48:51], s31 offen offset:0 sc1\nQmvaa 20,72,20\nQmvaa 20,76,48\nQmvaa 16,72,16\nQta 64,68,40960\nQmvaa 16,76,44\n"
"buffer_load_dwordx4 v[240:243], v60, s[48:51], s31 offen offset:1024 sc1\nQmvaa 12,72,12\nQmvaa 12,76,40\nQta 66,68,41472\nQmvaa 8,72,8\nQmvaa 8,76,36\nbuffer_load_dwordx4 v[84:87], v60, s[48:51], s31 offen offset:2048 sc1\nQmvaa 4,72,4\n"
"Qta 68,68,41984\nQmvaa 4,76,32\nQmvaa 0,72,0\nQmvaa 0,76,28\nQta 70,68,42496\ns_add_u32 s31, s75, 43008\ns_waitcnt lgkmcnt(4)\nQmvaa 28,56,0\n"
"buffer_load_dwordx4 v[88:91], v60, s[48:51], s31 offen offset:0 sc1\nQmvaa 28,60,28\nQmvaa 32,56,4\nQmvaa 32,60,32\nQmvaa 36,56,8\nQta 72,68,41216\nQmvaa 36,60,36\nbuffer_load_dwordx4 v[120:123], v60, s[48:51], s31 offen offset:1024 sc1\n"
"Qmvaa 40,56,12\nQmvaa 40,60,40\nQta 74,68,41728\nQmvaa 44,56,16\nQmvaa 44,60,44\nbuffer_load_dwordx4 v[152:155], v60, s[48:51], s31 offen offset:2048 sc1\nQmvaa 48,56,20\nQta 76,68,42240\n"
"Qmvaa 48,60,48\nv_add_u32 v75, 1, v75\nQmvaa 52,56,24\ns_waitcnt vmcnt(40)\nds_write_b32 v73, v75\nQmvaa 52,60,52\n.Lpg23:\ns_cbranch_execz .Ltr8\n"
"Qta 78,68,42752\ns_waitcnt lgkmcnt(5)\nQmaaa 196,64,24\nbuffer_load_dwordx4 v[184:187], v60, s[48:51], s31 offen offset:3072 sc1\nQmaaa 196,68,52\nQmaaa 192,64,20\nQmaaa 192,68,48\nQmaaa 188,64,16\n"
"Qta 56,68,49152\ns_add_u32 s31, s75, 47104\nQmaaa 188,68,44\nbuffer_load_dwordx4 v[216:219], v60, s[48:51], s31 offen offset:0 sc1\nQmaaa 184,64,12\nds_read_b32 v66, v74\nQmaaa 184,68,40\nQta 58,68,50176\n"
"Qmaaa 180,64,8\nQmaaa 180,68,36\nbuffer_load_dwordx4 v[248:251], v60, s[48:51], s31 offen offset:1024 sc1\nQmaaa 176,64,4\ns_waitcnt lgkmcnt(1)\nv_cmp_gt_u32 vcc, v75, v66\nQmaaa 176,68,32\ns_cbranch_vccnz .Lso1735r1\n"
".Lro1735r1:\nQmaaa 172,64,0\nQta 60,68,57344\nQmaaa 172,68,28\nbuffer_load_dwordx4 v[116:119], v60, s[48:51], s31 offen offset:2048 sc1\ns_waitcnt lgkmcnt(4)\nQmvaa 92,72,0\nQmvaa 92,76,28\n"
"Qta 62,68,58368\nQmvaa 124,72,4\ns_add_u32 m0, s96, 8192\ns_add_u32 s95, s72, 86016\nQmvaa 124,76,32\nbuffer_load_dwordx4 v61, s[52:55], s95 offen lds\nQmvaa 156,72,8\nQta 64,68,49408\n"
"Qmvaa 156,76,36\nQmvaa 188,72,12\nQmvaa 188,76,40\nQta 66,68,50432\ns_add_u32 s95, s72, 105472\nQmvaa 220,72,16\nbuffer_load_dwordx4 v61, s[52:55], s95 offen offset:1024 lds\nQmvaa 220,76,44\n"
"Qmvaa 252,72,20\nQta 68,68,57600\nQmvaa 252,76,48\ns_add_u32 m0, s96, 16384\ns_add_u32 s95, s72, 126976\nQmvaa 100,72,24\nbuffer_load_dwordx4 v61, s[52:55], s95 offen lds\nQmvaa 100,76,52\n"
"Qta 70,68,58624\ns_waitcnt lgkmcnt(4)\nQmaaa 224,56,24\nQmaaa 224,60,52\ns_add_u32 s95, s72, 146432\nQmaaa 220,56,20\nbuffer_load_dwordx4 v61, s[52:55], s95 offen offset:1024 lds\nQmaaa 220,60,48\n"
"Qmaaa 216,56,16\nQta 72,68,49664\nQmaaa 216,60,44\ns_add_u32 m0, s96, 24576\ns_add_u32 s95, s73, 86016\nQmaaa 212,56,12\nbuffer_load_dwordx4 v61, s[52:55], s95 offen lds\nQmaaa 212,60,40\n"
"Qta 74,68,50688\nQmaaa 208,56,8\nQmaaa 208,60,36\nQmaaa 204,56,4\nQta 76,68,57856\ns_add_u32 s95, s73, 105472\nQmaaa 204,60,32\nbuffer_load_dwordx4 v61, s[52:55], s95 offen offset:1024 lds\n"
"Qmaaa 200,56,0\nQmaaa 200,60,28\nQta 78,68,58880\ns_waitcnt lgkmcnt(4)\nQmaaa 228,64,0\ns_add_u32 m0, s96, 32768\ns_add_u32 s95, s73, 126976\nQmaaa 228,68,28\n"
"buffer_load_dwordx4 v61, s[52:55], s95 offen lds\nQmaaa 232,64,4\nQmaaa 232,68,32\nQmaaa 236,64,8\nQta 56,68,49920\ns_add_u32 s95, s73, 146432\nQmaaa 236,68,36\nbuffer_load_dwordx4 v61, s[52:55], s95 offen offset:1024 lds\n"
"Qmaaa 240,64,12\nQmaaa 240,68,40\nQta 58,68,50944\nQmaaa 244,64,16\nQmaaa 244,68,44\nQmaaa 248,64,20\nQta 60,68,58112\nQmaaa 248,68,48\n"
"Qmaaa 252,64,24\nQmaaa 252,68,52\nQta 62,68,59136\ns_waitcnt lgkmcnt(4)\nQmvaa 172,72,24\nQmvaa 172,76,52\nQmvaa 140,72,20\nQmvaa 140,76,48\n"
"Qmvaa 108,72,16\nQta 64,69,0\nQmvaa 108,76,44\nQmvaa 228,72,12\nQmvaa 228,76,40\nQta 66,69,512\nQmvaa 196,72,8\nQmvaa 196,76,36\n"
"Qmvaa 164,72,4\nQta 68,69,1024\nQmvaa 164,76,32\nv_add_u32 v75, 1, v75\nQmvaa 132,72,0\ns_waitcnt vmcnt(22)\nds_write_b32 v73, v75\nQmvaa 132,76,28\n"
"Qta 70,69,1536\ns_waitcnt lgkmcnt(5)\nQmvaa 204,56,0\ns_waitcnt vmcnt(24)\nQda 0,58,0\nQmvaa 204,60,28\nQmvaa 236,56,4\nQda 28,58,33792\n"
"Qmvaa 236,60,32\nQmvaa 96,56,8\nQta 72,69,8192\nQmvaa 96,60,36\nQda 4,58,1024\nQmvaa 128,56,12\nds_read_b32 v66, v74\nQmvaa 128,60,40\n"
"Qta 74,69,8704\nQmvaa 160,56,16\nQda 32,58,34816\nQmvaa 160,60,44\nQmvaa 192,56,20\ns_waitcnt lgkmcnt(2)\nv_cmp_gt_u32 vcc, v75, v66\nQmvaa 192,60,48\n"
"s_cbranch_vccnz .Lso1805r1\n.Lro1805r1:\nQmvaa 224,56,24\nQta 76,69,9216\ns_add_u32 m0, s96, 40960\ns_add_u32 s95, s67, 86016\nQmvaa 224,60,52\nbuffer_load_dwordx4 v63, s[52:55], s95 offen lds\n"
"s_waitcnt vmcnt(39)\ns_waitcnt lgkmcnt(8)\nQmaaa 172,64,80\nQda 8,58,2048\nQmaaa 172,68,108\nQta 78,69,9728\nQmaaa 176,64,84\nQda 36,58,35840\n"
"s_add_u32 s95, s67, 105472\nQmaaa 176,68,112\nbuffer_load_dwordx4 v63, s[52:55], s95 offen offset:1024 lds\nQmaaa 180,64,88\nQta 56,69,16384\nQmaav 180,68,168\nQda 12,58,3072\nQmaaa 184,64,92\n"
"Qmaav 184,68,200\nQta 58,69,16896\ns_add_u32 m0, s96, 49152\ns_add_u32 s95, s67, 126976\nQmaaa 188,64,96\nbuffer_load_dwordx4 v63, s[52:55], s95 offen lds\nQmaav 188,68,232\nQda 40,58,36864\n"
"Qmaaa 192,64,100\nQta 60,69,17408\nQmaav 192,68,212\nQda 16,58,4096\ns_add_u32 s95, s67, 146432\nQmaaa 196,64,104\nbuffer_load_dwordx4 v63, s[52:55], s95 offen offset:1024 lds\nQmaav 196,68,244\n"
"Qta 62,69,17920\ns_waitcnt vmcnt(12)\ns_waitcnt lgkmcnt(8)\nQmaav 172,72,80\nQda 44,58,37888\nQmaav 172,76,88\ns_add_u32 m0, s96, 57344\ns_add_u32 s95, s72, 86016\n"
"Qmaav 176,72,112\nbuffer_load_dwordx4 v63, s[52:55], s95 offen lds\nQmaav 176,76,120\nQda 20,58,5120\nQmaav 180,72,144\nQta 64,69,24576\nv_add_u32 v75, 1, v75\nQmaav 180,76,152\n"
"s_waitcnt vmcnt(5)\nds_write_b32 v73, v75\ns_add_u32 s95, s72, 105472\nQmaav 184,72,176\nbuffer_load_dwordx4 v63, s[52:55], s95 offen offset:1024 lds\nQmaav 184,76,184\nQta 66,69,25600\nQmaav 188,72,208\n"
"Qda 48,58,38912\nQmaav 188,76,216\nQmaav 192,72,240\nQta 68,68,0\nQmaav 192,76,248\nQda 24,58,6144\nQmaav 196,72,84\nQmaav 196,76,116\n"
"ds_read_b32 v66, v74\ns_waitcnt lgkmcnt(9)\nQmaaa 172,56,0\nQta 70,68,1024\nQmaaa 172,60,28\nQda 52,58,39936\nQmaaa 176,56,4\nQmaaa 176,60,32\n"
"s_waitcnt lgkmcnt(2)\nv_cmp_gt_u32 vcc, v75, v66\ns_waitcnt lgkmcnt(11)\nQmaaa 180,56,8\ns_cbranch_vccnz .Lso1840r1\n.Lro1840r1:\nQmaaa 180,60,36\nQta 72,68,8192\n"
"Qmaaa 184,56,12\nds_write_b128 v58, a[172:175] offset:0\nQmaaa 184,60,40\ns_waitcnt lgkmcnt(2)\nQmaaa 188,56,16\nQta 74,68,9216\nQmaaa 188,60,44\nds_write_b128 v58, a[176:179] offset:1024\n"
"Qmaaa 192,56,20\nQmaaa 192,60,48\nQta 76,68,16384\nQmaaa 196,56,24\nds_write_b128 v58, a[180:183] offset:2048\nQmaaa 196,60,52\ns_waitcnt lgkmcnt(7)\nQmaaa 116,64,80\n"
"Qta 78,68,17408\nQmaaa 116,68,108\nds_write_b128 v58, a[184:187] offset:3072\nQmaaa 120,64,84\nQmaaa 120,68,112\nQmaaa 124,64,88\nQta 56,68,24576\nQmaav 124,68,168\n"
"ds_write_b128 v58, a[188:191] offset:4096\nQmaaa 128,64,92\nQmaav 128,68,200\nQta 58,68,25600\nQmaaa 132,64,96\nds_write_b128 v58, a[192:195] offset:5120\nQmaav 132,68,232\nQmaaa 136,64,100\n"
"Qta 60,68,32768\nQmaav 136,68,212\nds_write_b128 v58, a[196:199] offset:6144\nQmaaa 140,64,104\nQmaav 140,68,244\nQta 62,68,33792\ns_waitcnt lgkmcnt(8)\nQmaav 116,72,80\n"
"v_add_u32 v75, 1, v75\nQmaav 116,76,88\nds_write_b32 v73, v75\nQmaav 120,72,112\nQmaav 120,76,120\nQmaav 124,72,144\nQta 64,69,256\nQmaav 124,76,152\n"
"Qmaav 128,72,176\nQmaav 128,76,184\nQta 66,69,768\nQmaav 132,72,208\nQmaav 132,76,216\nds_read_b32 v66, v74\nQmaav 136,72,240\nQta 68,69,1280\n"
"Qmaav 136,76,248\nQmaav 140,72,84\nQmaav 140,76,116\ns_waitcnt lgkmcnt(1)\nv_cmp_gt_u32 vcc, v75, v66\ns_waitcnt lgkmcnt(5)\nQmaaa 116,56,0\ns_cbranch_vccnz .Lso1878r1\n"
".Lro1878r1:\nQmaaa 116,60,28\nQta 70,69,1792\nQmaaa 120,56,4\nQmaaa 120,60,32\nQdv 148,59,2048\nQmaaa 124,56,8\nQta 72,69,8448\n"
"Qmaaa 124,60,36\ns_nop 0\nds_write_b128 v58, a[116:119] offset:33792\nQmaaa 128,56,12\nQdv 180,59,3072\nQmaaa 128,60,40\nQta 74,69,8960\nQmaaa 132,56,16\n"
"ds_write_b128 v58, a[120:123] offset:34816\nQmaaa 132,60,44\nQdv 104,59,5120\nQmaaa 136,56,20\nQta 76,69,9472\nQmaaa 136,60,48\nds_write_b128 v58, a[124:127] offset:35840\nQmaaa 140,56,24\n"
"Qdv 136,59,0\ns_waitcnt lgkmcnt(9)\nQa 156,156,148\nQmaaa 140,60,52\nQta 78,69,9984\nQa 157,157,149\ns_waitcnt lgkmcnt(10)\nQmvaa 92,64,80\n"
"ds_write_b128 v58, a[128:131] offset:36864\nQmvaa 92,68,108\nQa 158,158,150\nQa 159,159,151\nQmvaa 124,64,84\ns_waitcnt lgkmcnt(8)\nQa 188,188,180\nQa 189,189,181\n"
"Qmvaa 124,68,112\nds_write_b128 v58, a[132:135] offset:37888\nQmvaa 156,64,88\nQta 56,69,16640\nQa 190,190,182\nQa 191,191,183\nQmvav 156,68,168\nQdv 148,59,4096\n"
"s_waitcnt lgkmcnt(8)\nQa 252,252,104\nQmvaa 188,64,92\nds_write_b128 v58, a[136:139] offset:38912\nQmvav 188,68,200\nQta 58,69,17152\nQa 253,253,105\nQmvaa 220,64,96\n"
"Qa 254,254,106\nQa 255,255,107\nQmvav 220,68,232\nQdv 180,59,6144\ns_waitcnt lgkmcnt(8)\nQa 92,92,136\nQmvaa 252,64,100\nQta 60,69,17664\n"
"Qa 93,93,137\nQmvav 252,68,212\nds_write_b128 v58, a[140:143] offset:39936\nQmvaa 100,64,104\nQa 94,94,138\nQa 95,95,139\nQmvav 100,68,244\nQta 62,69,18176\n"
"s_waitcnt lgkmcnt(6)\nQa 220,220,148\ns_waitcnt lgkmcnt(6)\nQmvav 92,72,80\nQdv 104,59,1024\nv_add_u32 v75, 1, v75\nQmvav 92,76,88\nds_write_b32 v73, v75\n"
"Qa 221,221,149\nQmvav 124,72,112\nQa 222,222,150\nQa 223,223,151\nQmvav 124,76,120\ns_waitcnt lgkmcnt(5)\nQa 100,100,180\nQa 101,101,181\n"
"Qmvav 156,72,144\nQta 64,69,24832\nQa 102,102,182\nQmvav 156,76,152\nQa 103,103,183\nQmvav 188,72,176\nQmvav 188,76,184\nQta 66,69,25856\n"
"s_waitcnt lgkmcnt(3)\nQa 124,124,104\nQmvav 220,72,208\nQa 125,125,105\nQa 126,126,106\nQmvav 220,76,216\nds_read_b32 v66, v74\nQa 127,127,107\n"
"Qmvav 252,72,240\nQta 68,68,256\nQmvav 252,76,248\nQmvav 100,72,84\nQmvav 100,76,116\ns_waitcnt lgkmcnt(1)\nv_cmp_gt_u32 vcc, v75, v66\ns_waitcnt lgkmcnt(6)\n"
"Qmvaa 92,56,0\ns_cbranch_vccnz .Lso1920r1\n.Lro1920r1:\ns_add_u32 s68, s64, 0\nQmvaa 92,60,28\nQta 70,68,1280\ns_add_u32 m0, s96, 65536\ns_add_u32 s95, s72, 126976\n"
"Qmvaa 124,56,4\nbuffer_load_dwordx4 v63, s[52:55], s95 offen lds\nQmvaa 124,60,32\nQdv 136,59,33792\nQmvaa 156,56,8\nQta 72,68,8448\nQk 92,92,93\nQmvaa 156,60,36\n"
"s_add_u32 s95, s72, 146432\nQk 93,94,95\nQmvaa 188,56,12\nbuffer_load_dwordx4 v63, s[52:55], s95 offen offset:1024 lds\nQmvaa 188,60,40\nQta 74,68,9472\nQk 94,124,125\nQmvaa 220,56,16\n"
"Qdv 148,59,34816\nQk 95,126,127\nQmvaa 220,60,44\nbuffer_store_dwordx4 v[92:95], v60, s[24:27], s68 offen offset:0 sc1\nQk 156,156,157\nQmvaa 252,56,20\nQta 76,68,16640\ns_add_u32 m0, s96, 73728\n"
"s_add_u32 s95, s73, 86016\nQmvaa 252,60,48\nbuffer_load_dwordx4 v63, s[52:55], s95 offen lds\nv_add_u32 v75, 1, v75\nQmvaa 100,56,24\nds_write_b32 v73, v75\ns_waitcnt lgkmcnt(5)\nQa 0,0,136\n"
"Qmvaa 100,60,52\nQta 78,68,17664\nQa 1,1,137\ns_waitcnt lgkmcnt(6)\nQmaaa 144,64,80\nQdv 180,59,35840\ns_add_u32 s95, s73, 105472\nQmaaa 144,68,108\n"
"buffer_load_dwordx4 v63, s[52:55], s95 offen offset:1024 lds\nQmaaa 148,64,84\nQa 2,2,138\nQa 3,3,139\nQmaaa 148,68,112\nQdv 104,59,36864\ns_waitcnt lgkmcnt(5)\nQa 4,4,148\n"
"Qmaaa 152,64,88\nQta 56,68,24832\ns_add_u32 m0, s96, 81920\ns_add_u32 s95, s73, 126976\nQmaav 152,68,168\nbuffer_load_dwordx4 v63, s[52:55], s95 offen lds\nQmaaa 156,64,92\nds_read_b32 v66, v74\n"
"Qa 5,5,149\nQa 6,6,150\nQa 7,7,151\nQmaav 156,68,200\nQta 58,68,25856\ns_waitcnt lgkmcnt(4)\nQa 8,8,180\nQmaaa 160,64,96\n"
"Qdv 136,59,37888\ns_add_u32 s95, s73, 146432\nQmaav 160,68,232\nbuffer_load_dwordx4 v63, s[52:55], s95 offen offset:1024 lds\nQmaaa 164,64,100\ns_waitcnt lgkmcnt(2)\nv_cmp_gt_u32 vcc, v75, v66\nQmaav 164,68,212\n"
"s_cbranch_vccnz .Lso1945r1\n.Lro1945r1:\nQa 9,9,181\nQa 10,10,182\nQa 11,11,183\nQmaaa 168,64,104\nQta 60,68,33024\ns_waitcnt lgkmcnt(5)\n"
"Qa 12,12,104\nQmaav 168,68,244\nQdv 148,59,38912\nQa 13,13,105\ns_waitcnt lgkmcnt(6)\nQmaav 144,72,80\nQa 14,14,106\nQa 15,15,107\n"
"Qmaav 144,76,88\nQta 62,68,34048\ns_waitcnt lgkmcnt(3)\nQa 16,16,136\nQmaav 148,72,112\nQdv 180,59,39936\nQa 17,17,137\nQmaav 148,76,120\n"
"Qa 18,18,138\nQa 19,19,139\nQmaav 152,72,144\nQta 64,69,25088\nv_bfe_u32 v61, v60, 6, 2\nQmaav 152,76,152\ns_waitcnt lgkmcnt(3)\nQa 20,20,148\n"
"Qa 21,21,149\nQmaav 156,72,176\nQa 22,22,150\nQa 23,23,151\nQmaav 156,76,184\nQta 66,69,26112\nv_lshrrev_b32 v62, 8, v60\nQmaav 160,72,208\n"
"v_xor_b32 v61, v61, v62\nv_bfe_u32 v62, v60, 5, 1\nQmaav 160,76,216\nv_lshl_or_b32 v61, v61, 1, v62\nv_and_b32 v62, 16, v60\nQmaav 164,72,240\nQta 68,68,512\ns_waitcnt lgkmcnt(3)\n"
"Qa 24,24,180\nQmaav 164,76,248\nQa 25,25,181\nQa 26,26,182\nQmaav 168,72,84\nQa 27,27,183\nv_mov_b32 v63, 0x3f80\nQmaav 168,76,116\n"
"Qta 70,68,1536\nv_lshlrev_b32 v63, v62, v63\ns_waitcnt lgkmcnt(5)\nQmaaa 144,56,0\nv_min_u32 v62, 1, v61\nv_add_u32 v62, -1, v62\nQmaaa 144,60,28\nv_and_b32 v62, v62, v63\n"
"v_xor_b32 v61, 1, v61\nQmaaa 148,56,4\nv_min_u32 v61, 1, v61\nv_add_u32 v61, -1, v61\nQmaaa 148,60,32\nv_and_b32 v61, v61, v63\nv_accvgpr_write_b32 a174, v62\nQmaaa 152,56,8\n"
"Qta 72,68,8704\nv_accvgpr_write_b32 a175, v61\nQmaaa 152,60,36\nds_write_b128 v58, a[144:147] offset:0\nQmaaa 156,56,12\nv_accvgpr_write_b32 a172, 0\nv_accvgpr_write_b32 a173, 0\nQmaaa 156,60,40\n"
"Qta 74,68,9728\nv_accvgpr_write_b32 a176, 0\nQmaaa 160,56,16\nds_write_b128 v58, a[148:151] offset:1024\nQmaaa 160,60,44\nv_accvgpr_write_b32 a177, 0\nQk 157,158,159\n.Lpg24:\n"
"s_cbranch_execz .Ltr8\nQmaaa 164,56,20\nQta 76,68,16896\nQk 158,188,189\nQmaaa 164,60,48\nds_write_b128 v58, a[152:155] offset:2048\nQmaaa 168,56,24\nQk 159,190,191\n"
"Qk 220,220,221\nQmaaa 168,60,52\nQta 78,68,17920\nbuffer_store_dwordx4 v[156:159], v60, s[24:27], s68 offen offset:1024 sc1\ns_waitcnt lgkmcnt(7)\nQmvaa 0,64,80\nds_write_b128 v58, a[156:159] offset:3072\nQmvaa 0,68,108\n"
"Qk 221,222,223\nQk 222,252,253\nQmvaa 4,64,84\nQk 223,254,255\nQk 100,100,101\nQmvaa 4,68,112\nds_write_b128 v58, a[160:163] offset:4096\nQmvaa 8,64,88\n"
"Qta 56,68,25088\nbuffer_store_dwordx4 v[220:223], v60, s[24:27], s68 offen offset:2048 sc1\nQmvav 8,68,168\nQk 101,102,103\nQmvaa 12,64,92\nds_write_b128 v58, a[164:167] offset:5120\nQmvav 12,68,200\nQta 58,68,26112\n"
"Qmvaa 16,64,96\nQmvav 16,68,232\nds_write_b128 v58, a[168:171] offset:6144\nQmvaa 20,64,100\nQta 60,68,33280\nQmvav 20,68,212\nQmvaa 24,64,104\nQmvav 24,68,244\n"
"Qta 62,68,34304\ns_waitcnt lgkmcnt(8)\nQmvav 0,72,80\nv_add_u32 v75, 1, v75\nQmvav 0,76,88\ns_waitcnt vmcnt(11)\nds_write_b32 v73, v75\nQmvav 4,72,112\n"
"Qmvav 4,76,120\nQmvav 8,72,144\nQta 64,69,25344\nQmvav 8,76,152\nQmvav 12,72,176\nQmvav 12,76,184\nQta 66,69,26368\nQmvav 16,72,208\n"
"Qmvav 16,76,216\nds_read_b32 v66, v74\nQmvav 20,72,240\nQta 68,68,768\nQmvav 20,76,248\nQmvav 24,72,84\nQmvav 24,76,116\ns_waitcnt lgkmcnt(1)\n"
"v_cmp_gt_u32 vcc, v75, v66\ns_waitcnt lgkmcnt(5)\nQmvaa 0,56,0\ns_cbranch_vccnz .Lso2004r1\n.Lro2004r1:\nQmvaa 0,60,28\nQta 70,68,1792\nQmvaa 4,56,4\n"
"Qmvaa 4,60,32\nQdv 104,59,2048\nQmvaa 8,56,8\nQta 72,68,8960\ns_nop 1\nQk 102,0,1\nQmvaa 8,60,36\nQk 103,2,3\n"
"Qmvaa 12,56,12\nQdv 136,59,3072\nbuffer_store_dwordx4 v[100:103], v60, s[24:27], s68 offen offset:3072 sc1\nQmvaa 12,60,40\nQta 74,68,9984\nQk 4,4,5\nQmvaa 16,56,16\nQk 5,6,7\n"
"Qk 6,8,9\nQmvaa 16,60,44\nQdv 148,59,5120\nQk 7,10,11\nQmvaa 20,56,20\nQta 76,68,17152\ns_add_u32 s68, s64, 4096\nQmvaa 20,60,48\n"
"s_waitcnt lgkmcnt(5)\nQa 36,36,104\nQa 37,37,105\nQmvaa 24,56,24\nQdv 180,59,0\nQa 38,38,106\nQmvaa 24,60,52\nQta 78,68,18176\n"
"Qa 39,39,107\ns_waitcnt lgkmcnt(11)\nQmvaa 28,64,80\ns_waitcnt lgkmcnt(5)\nQa 40,40,136\nQa 41,41,137\nQmvaa 28,68,108\nQa 42,42,138\n"
"Qa 43,43,139\nQmvaa 32,64,84\nbuffer_store_dwordx4 v[4:7], v60, s[24:27], s68 offen offset:0 sc1\nQk 12,12,13\nQmvaa 32,68,112\ns_waitcnt lgkmcnt(3)\nQa 48,48,148\nQa 49,49,149\n"
"Qmvaa 36,64,88\nQta 56,68,25344\nQa 50,50,150\nQmvav 36,68,168\nQdv 104,59,4096\nQa 51,51,151\nQmvaa 40,64,92\ns_waitcnt lgkmcnt(3)\n"
"Qa 28,28,180\nQa 29,29,181\nQmvav 40,68,200\nQta 58,68,26368\nQa 30,30,182\nQmvaa 44,64,96\nQa 31,31,183\nQk 13,14,15\n"
"Qmvav 44,68,232\nQdv 136,59,6144\nQk 14,16,17\nQmvaa 48,64,100\nQta 60,68,33536\nQk 15,18,19\nQmvav 48,68,212\nbuffer_store_dwordx4 v[12:15], v60, s[24:27], s68 offen offset:1024 sc1\n"
"Qk 20,20,21\nQmvaa 52,64,104\nQk 21,22,23\nQk 22,24,25\nQmvav 52,68,244\nQta 62,68,34560\nv_add_u32 v75, 1, v75\ns_waitcnt lgkmcnt(6)\n"
"Qmvav 28,72,80\ns_waitcnt vmcnt(5)\nds_write_b32 v73, v75\nQmvav 28,76,88\nQdv 148,59,1024\ns_waitcnt lgkmcnt(6)\nQa 44,44,104\nQmvav 32,72,112\n"
"Qa 45,45,105\nQa 46,46,106\nQmvav 32,76,120\nQa 47,47,107\ns_waitcnt lgkmcnt(4)\nQa 52,52,136\nQmvav 36,72,144\nQta 64,68,40960\n"
"Qa 53,53,137\nQmvav 36,76,152\nQa 54,54,138\nQa 55,55,139\nQmvav 40,72,176\nQk 23,26,27\nQmvav 40,76,184\nQta 66,68,41984\n"
"s_waitcnt lgkmcnt(2)\nQa 32,32,148\nQmvav 44,72,208\nds_read_b32 v66, v74\nQa 33,33,149\nQmvav 44,76,216\nQa 34,34,150\nQa 35,35,151\n"
"Qmvav 48,72,240\nQta 68,68,49152\nbuffer_store_dwordx4 v[20:23], v60, s[24:27], s68 offen offset:2048 sc1\nQmvav 48,76,248\nQmvav 52,72,84\ns_waitcnt lgkmcnt(1)\nv_cmp_gt_u32 vcc, v75, v66\nQmvav 52,76,116\n"
"s_cbranch_vccnz .Lso2045r1\n.Lro2045r1:\ns_waitcnt lgkmcnt(6)\nQmvaa 28,56,0\nQta 70,68,50176\nQmvaa 28,60,28\nQmvaa 32,56,4\nQmvaa 32,60,32\n"
"Qmvaa 36,56,8\nQta 72,68,57344\ns_nop 3\nQk 28,28,29\nQmvaa 36,60,36\nQk 29,30,31\nQmvaa 40,56,12\nQk 30,32,33\n"
"Qk 31,34,35\nQmvaa 40,60,40\nQta 74,68,58368\nbuffer_store_dwordx4 v[28:31], v60, s[24:27], s68 offen offset:3072 sc1\nQmvaa 44,56,16\nQk 36,36,37\nQk 37,38,39\nQmvaa 44,60,44\n"
"s_add_u32 s68, s64, 8192\nQmvaa 48,56,20\nQta 76,69,0\nQk 38,40,41\nQmvaa 48,60,48\nQk 39,42,43\nQmvaa 52,56,24\nbuffer_store_dwordx4 v[36:39], v60, s[24:27], s68 offen offset:0 sc1\n"
"Qk 44,44,45\nQmvaa 52,60,52\nQta 78,69,1024\nQk 45,46,47\ns_waitcnt lgkmcnt(4)\nQmaaa 200,64,80\nQk 46,48,49\nQk 47,50,51\n"
"Qmaaa 200,68,108\nbuffer_store_dwordx4 v[44:47], v60, s[24:27], s68 offen offset:1024 sc1\nQmaaa 204,64,84\nQk 52,52,53\nQk 53,54,55\nQmaaa 204,68,112\nv_mov_b32 v54, 0\nv_mov_b32 v55, 0\n"
"Qmaaa 208,64,88\nQta 56,69,8192\nbuffer_store_dwordx4 v[52:55], v60, s[24:27], s68 offen offset:2048 sc1\nQmaav 208,68,168\nQmaaa 212,64,92\nQmaav 212,68,200\nQta 58,69,9216\nQmaaa 216,64,96\n"
"Qmaav 216,68,232\nQmaaa 220,64,100\nQta 60,69,16384\nQmaav 220,68,212\nQmaaa 224,64,104\nQmaav 224,68,244\nQta 62,69,17408\ns_waitcnt lgkmcnt(4)\n"
"Qmaav 200,72,80\nQmaav 200,76,88\nQmaav 204,72,112\nQmaav 204,76,120\nQmaav 208,72,144\nQta 64,68,41216\nQmaav 208,76,152\nQmaav 212,72,176\n"
"Qmaav 212,76,184\nQta 66,68,42240\nQmaav 216,72,208\nQmaav 216,76,216\nQmaav 220,72,240\nQta 68,68,49408\nQmaav 220,76,248\nQmaav 224,72,84\n"
"Qmaav 224,76,116\nQta 70,68,50432\ns_waitcnt lgkmcnt(4)\nQmaaa 200,56,0\nQmaaa 200,60,28\nQmaaa 204,56,4\nQmaaa 204,60,32\nQmaaa 208,56,8\n"
"Qta 72,68,57600\nQmaaa 208,60,36\ns_nop 2\nds_write_b128 v58, a[200:203] offset:33792\nQmaaa 212,56,12\nQmaaa 212,60,40\nQta 74,68,58624\nQmaaa 216,56,16\n"
"ds_write_b128 v58, a[204:207] offset:34816\nQmaaa 216,60,44\nQmaaa 220,56,20\nQta 76,69,256\nQmaaa 220,60,48\nds_write_b128 v58, a[208:211] offset:35840\nQmaaa 224,56,24\nQmaaa 224,60,52\n"
"Qta 78,69,1280\ns_waitcnt lgkmcnt(7)\nQmaaa 228,64,80\nds_write_b128 v58, a[212:215] offset:36864\nQmaaa 228,68,108\nQmaaa 232,64,84\ns_waitcnt vmcnt(0)\nbuffer_store_dword v67, v77, s[24:27], 0 offen sc1\n"
"Qmaaa 232,68,112\nds_write_b128 v58, a[216:219] offset:37888\nQmaaa 236,64,88\nQta 56,69,8448\nQmaav 236,68,168\nQmaaa 240,64,92\nds_write_b128 v58, a[220:223] offset:38912\nQmaav 240,68,200\n"
"Qta 58,69,9472\nQmaaa 244,64,96\nQmaav 244,68,232\nds_write_b128 v58, a[224:227] offset:39936\nQmaaa 248,64,100\nQta 60,69,16640\nQmaav 248,68,212\nQmaaa 252,64,104\n"
"Qmaav 252,68,244\nQta 62,69,17664\ns_waitcnt lgkmcnt(8)\nQmaav 228,72,80\nv_add_u32 v75, 1, v75\nQmaav 228,76,88\nds_write_b32 v73, v75\nQmaav 232,72,112\n"
"Qmaav 232,76,120\nQmaav 236,72,144\nQta 64,68,41472\nQmaav 236,76,152\nQmaav 240,72,176\nQmaav 240,76,184\nQta 66,68,42496\nQmaav 244,72,208\n"
"Qmaav 244,76,216\nds_read_b32 v66, v74\nQmaav 248,72,240\nQta 68,68,49664\nQmaav 248,76,248\nQmaav 252,72,84\nbuffer_load_dword v78, v76, s[24:27], 0 offen sc1\nQmaav 252,76,116\n"
"s_waitcnt lgkmcnt(1)\nv_cmp_gt_u32 vcc, v75, v66\ns_waitcnt lgkmcnt(5)\nQmaaa 228,56,0\ns_cbranch_vccnz .Lso2130r1\n.Lro2130r1:\nQmaaa 228,60,28\nQta 70,68,50688\n"
"Qmaaa 232,56,4\nQmaaa 232,60,32\nQdv 180,59,35840\nQmaaa 236,56,8\nQta 72,68,57856\nQmaaa 236,60,36\ns_nop 0\nds_write_b128 v58, a[228:231] offset:0\n"
"Qmaaa 240,56,12\nQdv 104,59,36864\nQmaaa 240,60,40\nQta 74,68,58880\nQmaaa 244,56,16\nds_write_b128 v58, a[232:235] offset:1024\nQmaaa 244,60,44\nQdv 136,59,38912\n"
"Qmaaa 248,56,20\nQta 76,69,512\nQmaaa 248,60,48\nds_write_b128 v58, a[236:239] offset:2048\nQmaaa 252,56,24\nQdv 148,59,33792\ns_waitcnt lgkmcnt(9)\nQa 196,196,180\n"
"Qmaaa 252,60,52\nQta 78,69,1536\nQa 197,197,181\ns_waitcnt lgkmcnt(10)\nQmvaa 132,64,80\nds_write_b128 v58, a[240:243] offset:3072\nQmvaa 132,68,108\nQa 198,198,182\n"
"Qa 199,199,183\nQmvaa 164,64,84\ns_waitcnt lgkmcnt(8)\nQa 228,228,104\nQa 229,229,105\nQmvaa 164,68,112\nds_write_b128 v58, a[244:247] offset:4096\nQmvaa 196,64,88\n"
"Qta 56,69,8704\nQa 230,230,106\nQa 231,231,107\nQmvav 196,68,168\nQdv 180,59,37888\ns_waitcnt lgkmcnt(8)\nQa 140,140,136\nQmvaa 228,64,92\n"
"ds_write_b128 v58, a[248:251] offset:5120\nQmvav 228,68,200\nQta 58,69,9728\nQa 141,141,137\nQmvaa 108,64,96\nQa 142,142,138\nQa 143,143,139\nQmvav 108,68,232\n"
"Qdv 104,59,39936\ns_waitcnt lgkmcnt(8)\nQa 132,132,148\nQmvaa 140,64,100\nQta 60,69,16896\nQa 133,133,149\nQmvav 140,68,212\nds_write_b128 v58, a[252:255] offset:6144\n"
"Qmvaa 172,64,104\nQa 134,134,150\nQa 135,135,151\nQmvav 172,68,244\nQta 62,69,17920\ns_waitcnt lgkmcnt(6)\nQa 108,108,180\ns_waitcnt lgkmcnt(6)\n"
"Qmvav 132,72,80\nQdv 136,59,34816\nv_add_u32 v75, 1, v75\nQmvav 132,76,88\nds_write_b32 v73, v75\nQa 109,109,181\nQmvav 164,72,112\nQa 110,110,182\n"
"Qa 111,111,183\nQmvav 164,76,120\ns_waitcnt lgkmcnt(5)\nQa 172,172,104\nQa 173,173,105\nQmvav 196,72,144\nQta 64,68,41728\nQa 174,174,106\n"
"Qmvav 196,76,152\nQa 175,175,107\nQmvav 228,72,176\nQmvav 228,76,184\nQta 66,68,42752\ns_waitcnt lgkmcnt(3)\nQa 164,164,136\nQmvav 108,72,208\n"
"Qa 165,165,137\nQa 166,166,138\nQmvav 108,76,216\nds_read_b32 v66, v74\nQa 167,167,139\nQmvav 140,72,240\nQta 68,68,49920\nQmvav 140,76,248\n"
"Qmvav 172,72,84\nQmvav 172,76,116\ns_waitcnt lgkmcnt(1)\nv_cmp_gt_u32 vcc, v75, v66\ns_waitcnt lgkmcnt(6)\nQmvaa 132,56,0\ns_cbranch_vccnz .Lso2172r1\n.Lro2172r1:\n"
"Qmvaa 132,60,28\ns_waitcnt vmcnt(0)\nv_sub_u32 v66, v78, v65\nv_cmp_gt_u32 vcc, 1, v66\ns_and_b64 vcc, vcc, s[28:29]\ns_cbranch_scc1 .Lx1_w\n.Lx1_r:\nQmvaa 164,56,4\n"
"s_add_u32 s69, s98, 0\nbuffer_load_dwordx4 a[180:183], v60, s[24:27], s69 offen offset:0 sc1\nQmvaa 164,60,32\nQta 70,68,50944\nQmvaa 196,56,8\nbuffer_load_dwordx4 a[184:187], v60, s[24:27], s69 offen offset:1024 sc1\nQmvaa 196,60,36\nQdv 148,59,2048\n"
"Qmvaa 228,56,12\nbuffer_load_dwordx4 a[188:191], v60, s[24:27], s69 offen offset:2048 sc1\nQmvaa 228,60,40\nQta 72,68,58112\nQmvaa 108,56,16\nbuffer_load_dwordx4 a[192:195], v60, s[24:27], s69 offen offset:3072 sc1\nQmvaa 108,60,44\nQdv 180,59,4096\n"
"Qmvaa 140,56,20\ns_add_u32 s69, s98, 4096\nbuffer_load_dwordx4 a[196:199], v60, s[24:27], s69 offen offset:0 sc1\nQmvaa 140,60,48\nQta 74,68,59136\ns_waitcnt lgkmcnt(3)\nQa 96,96,148\nQmvaa 172,56,24\n"
"buffer_load_dwordx4 a[116:119], v60, s[24:27], s69 offen offset:1024 sc1\nQmvaa 172,60,52\nQdv 104,59,0\nQa 97,97,149\ns_waitcnt lgkmcnt(4)\nQmvaa 204,64,80\nbuffer_load_dwordx4 a[120:123], v60, s[24:27], s69 offen offset:2048 sc1\nQmvaa 204,68,108\n"
"Qta 76,69,768\nQa 98,98,150\nQmvaa 236,64,84\nQdv 136,59,1024\nQa 99,99,151\nQmvaa 236,68,112\ns_waitcnt lgkmcnt(4)\nQa 160,160,180\n"
"Qa 161,161,181\nQmvaa 96,64,88\nQta 78,69,1792\nQa 162,162,182\nQmvav 96,68,168\nQdv 148,59,3072\nQa 163,163,183\nQmvaa 128,64,92\n"
"s_waitcnt lgkmcnt(4)\nQa 204,204,104\nQa 205,205,105\nQmvav 128,68,200\nQta 56,69,8960\nQa 206,206,106\nQmvaa 160,64,96\nQdv 180,59,5120\n"
"Qa 207,207,107\nQmvav 160,68,232\nQmvaa 192,64,100\nQta 58,69,9984\ns_waitcnt lgkmcnt(5)\nQa 236,236,136\nQmvav 192,68,212\nQa 237,237,137\n"
"Qa 238,238,138\nQmvaa 224,64,104\nQa 239,239,139\nQmvav 224,68,244\nQta 60,69,17152\ns_waitcnt lgkmcnt(4)\nQa 128,128,148\ns_waitcnt lgkmcnt(4)\n"
"Qmvav 204,72,80\nQa 129,129,149\nQa 130,130,150\nQmvav 204,76,88\nQa 131,131,151\nQmvav 236,72,112\nQta 62,69,18176\ns_waitcnt lgkmcnt(3)\n"
"Qa 192,192,180\nQmvav 236,76,120\nQa 193,193,181\nQa 194,194,182\nQmvav 96,72,144\nQa 195,195,183\nQmvav 96,76,152\nQmvav 128,72,176\n"
"Qmvav 128,76,184\nQmvav 160,72,208\nQmvav 160,76,216\nQdv 104,59,6144\nQmvav 192,72,240\nQmvav 192,76,248\nv_add_u32 v75, 1, v75\nQmvav 224,72,84\n"
"ds_write_b32 v73, v75\nQmvav 224,76,116\ns_waitcnt lgkmcnt(2)\nQmvaa 204,56,0\nQmvaa 204,60,28\nQmvaa 236,56,4\nQmvaa 236,60,32\ns_waitcnt lgkmcnt(1)\n"
"s_nop 1\nQa 224,224,104\nQa 225,225,105\nQmvaa 96,56,8\nQa 226,226,106\nQa 227,227,107\nQmvaa 96,60,36\nQmvaa 128,56,12\n"
"ds_read_b32 v66, v74\nQmvaa 128,60,40\nQmvaa 160,56,16\nQmvaa 160,60,44\nQmvaa 192,56,20\ns_waitcnt lgkmcnt(0)\nv_cmp_gt_u32 vcc, v75, v66\nQmvaa 192,60,48\n"
"s_cbranch_vccnz .Lso2225r1\n.Lro2225r1:\nQmvaa 224,56,24\nQmvaa 224,60,52\ns_waitcnt vmcnt(6)\nQmvaa 132,174,180\n.Lpg25:\ns_cbranch_execz .Ltr8\n"
"Qmvaa 164,172,180\ns_waitcnt vmcnt(5)\nQmvaa 196,174,184\nQmvaa 228,172,184\ns_nop 3\nQk 132,132,133\nQk 133,134,135\nds_write_b64 v56, v[132:133] offset:0\n"
"s_waitcnt vmcnt(4)\nQmvaa 108,174,188\nQk 164,164,165\nQk 165,166,167\nds_write_b64 v56, v[164:165] offset:3328\nQmvaa 140,172,188\nQk 196,196,197\nQk 197,198,199\n"
"ds_write_b64 v56, v[196:197] offset:6656\ns_waitcnt vmcnt(3)\nQmvaa 172,174,192\nQk 228,228,229\nQk 229,230,231\nds_write_b64 v56, v[228:229] offset:9984\nQmvaa 204,172,192\nQk 108,108,109\n"
"Qk 109,110,111\nds_write_b64 v56, v[108:109] offset:13312\ns_waitcnt vmcnt(2)\nQmvaa 236,174,196\nQk 140,140,141\nQk 141,142,143\nds_write_b64 v56, v[140:141] offset:16640\nQmvaa 96,172,196\n"
"Qk 172,172,173\nQk 173,174,175\nds_write_b64 v56, v[172:173] offset:19968\ns_waitcnt vmcnt(1)\nQmvaa 128,174,116\nQk 204,204,205\nQk 205,206,207\nds_write_b64 v56, v[204:205] offset:32\n"
"Qmvaa 160,172,116\nQk 236,236,237\nQk 237,238,239\nds_write_b64 v56, v[236:237] offset:3360\ns_waitcnt vmcnt(0)\nQmvaa 192,174,120\nQk 96,96,97\nQk 97,98,99\n"
"ds_write_b64 v56, v[96:97] offset:6688\nQmvaa 224,172,120\nQk 128,128,129\nQk 129,130,131\nds_write_b64 v56, v[128:129] offset:10016\nQk 160,160,161\nQk 161,162,163\nds_write_b64 v56, v[160:161] offset:13344\n"
"Qk 192,192,193\nQk 193,194,195\nds_write_b64 v56, v[192:193] offset:16672\nQk 224,224,225\nQk 225,226,227\nds_write_b64 v56, v[224:225] offset:20000\ns_waitcnt lgkmcnt(0)\ns_barrier\n"
"Qda 0,57,0\nQda 4,57,3328\nQda 8,57,6656\nQda 12,57,9984\nQda 16,57,13312\nQda 20,57,16640\nQda 24,57,19968\ns_mov_b32 s68, 0\n"
"s_waitcnt lgkmcnt(6)\nbuffer_store_dwordx4 a[0:3], v64, s[40:43], s68 offen offset:0 sc1\ns_mov_b32 s68, 40960\ns_waitcnt lgkmcnt(5)\nbuffer_store_dwordx4 a[4:7], v64, s[40:43], s68 offen offset:0 sc1\ns_mov_b32 s68, 81920\ns_waitcnt lgkmcnt(4)\nbuffer_store_dwordx4 a[8:11], v64, s[40:43], s68 offen offset:0 sc1\n"
"s_mov_b32 s68, 122880\ns_waitcnt lgkmcnt(3)\nbuffer_store_dwordx4 a[12:15], v64, s[40:43], s68 offen offset:0 sc1\ns_mov_b32 s68, 163840\ns_waitcnt lgkmcnt(2)\nbuffer_store_dwordx4 a[16:19], v64, s[40:43], s68 offen offset:0 sc1\ns_mov_b32 s68, 204800\ns_waitcnt lgkmcnt(1)\n"
"buffer_store_dwordx4 a[20:23], v64, s[40:43], s68 offen offset:0 sc1\ns_mov_b32 s68, 245760\ns_waitcnt lgkmcnt(0)\nbuffer_store_dwordx4 a[24:27], v64, s[40:43], s68 offen offset:0 sc1\n.Lfin:\n.Lpg26:\ns_cbranch_execz .Ltr8\ns_waitcnt vmcnt(0) lgkmcnt(0)\n"
"s_branch .Lexit\n.Lend:\n.Lfb:\ns_waitcnt vmcnt(0) lgkmcnt(0)\nv_lshrrev_b32 v52, 4, v60\nv_and_b32 v53, 15, v52\nv_lshrrev_b32 v52, 4, v52\nv_lshlrev_b32 v66, 8, v53\n"
"v_lshl_add_u32 v66, v52, 4, v66\nv_add_u32 v66, 1024, v66\nv_lshlrev_b32 v67, 10, v52\nv_lshl_add_u32 v67, v53, 1, v67\nv_add_u32 v67, 1024, v67\nv_mov_b32 v52, 0x3f803f80\nv_accvgpr_write_b32 a204, v52\nv_accvgpr_write_b32 a205, v52\n"
"v_accvgpr_write_b32 a206, v52\nv_accvgpr_write_b32 a207, v52\nv_mov_b32 v24, 0xff800000\nv_mov_b32 v28, 0xff800000\nv_mov_b32 v32, 0xff800000\nv_mov_b32 v36, 0xff800000\nv_mov_b32 v40, 0xff800000\nv_mov_b32 v44, 0xff800000\n"
"v_mov_b32 v48, 0xff800000\ns_lshl_b32 s89, s67, 21\ns_mov_b32 s66, 0\n.Lfb1:\ns_add_u32 s68, s89, 4096\nbuffer_load_dwordx4 a[112:115], v66, s[40:43], s89 offen offset:0\nbuffer_load_dwordx4 a[116:119], v66, s[40:43], s89 offen offset:64\nbuffer_load_dwordx4 a[120:123], v66, s[40:43], s89 offen offset:128\n"
"buffer_load_dwordx4 a[124:127], v66, s[40:43], s89 offen offset:192\nbuffer_load_dwordx4 a[128:131], v66, s[40:43], s68 offen offset:0\nbuffer_load_dwordx4 a[132:135], v66, s[40:43], s68 offen offset:64\nbuffer_load_dwordx4 a[136:139], v66, s[40:43], s68 offen offset:128\nbuffer_load_dwordx4 a[140:143], v66, s[40:43], s68 offen offset:192\ns_waitcnt vmcnt(0)\nQzvaa 0,112,0\nQmvaa 0,116,4\n"
"Qmvaa 0,120,8\nQmvaa 0,124,12\nQzvaa 4,128,0\nQmvaa 4,132,4\nQmvaa 4,136,8\nQmvaa 4,140,12\ns_nop 7\nv_max3_f32 v24, v24, v0, v1\n"
"v_max3_f32 v24, v24, v2, v3\nv_max3_f32 v24, v24, v4, v5\nv_max3_f32 v24, v24, v6, v7\nQzvaa 0,112,16\nQmvaa 0,116,20\nQmvaa 0,120,24\nQmvaa 0,124,28\nQzvaa 4,128,16\n"
"Qmvaa 4,132,20\nQmvaa 4,136,24\nQmvaa 4,140,28\ns_nop 7\nv_max3_f32 v28, v28, v0, v1\nv_max3_f32 v28, v28, v2, v3\nv_max3_f32 v28, v28, v4, v5\nv_max3_f32 v28, v28, v6, v7\n"
"Qzvaa 0,112,32\nQmvaa 0,116,36\nQmvaa 0,120,40\nQmvaa 0,124,44\nQzvaa 4,128,32\nQmvaa 4,132,36\nQmvaa 4,136,40\nQmvaa 4,140,44\n"
"s_nop 7\nv_max3_f32 v32, v32, v0, v1\nv_max3_f32 v32, v32, v2, v3\nv_max3_f32 v32, v32, v4, v5\nv_max3_f32 v32, v32, v6, v7\nQzvaa 0,112,48\nQmvaa 0,116,52\nQmvaa 0,120,56\n"
"Qmvaa 0,124,60\nQzvaa 4,128,48\nQmvaa 4,132,52\nQmvaa 4,136,56\nQmvaa 4,140,60\ns_nop 7\nv_max3_f32 v36, v36, v0, v1\nv_max3_f32 v36, v36, v2, v3\n"
"v_max3_f32 v36, v36, v4, v5\nv_max3_f32 v36, v36, v6, v7\nQzvaa 0,112,64\nQmvaa 0,116,68\nQmvaa 0,120,72\nQmvaa 0,124,76\nQzvaa 4,128,64\nQmvaa 4,132,68\n"
"Qmvaa 4,136,72\nQmvaa 4,140,76\ns_nop 7\nv_max3_f32 v40, v40, v0, v1\nv_max3_f32 v40, v40, v2, v3\nv_max3_f32 v40, v40, v4, v5\nv_max3_f32 v40, v40, v6, v7\nQzvaa 0,112,80\n"
"Qmvaa 0,116,84\nQmvaa 0,120,88\nQmvaa 0,124,92\nQzvaa 4,128,80\nQmvaa 4,132,84\nQmvaa 4,136,88\nQmvaa 4,140,92\ns_nop 7\n"
"v_max3_f32 v44, v44, v0, v1\nv_max3_f32 v44, v44, v2, v3\nv_max3_f32 v44, v44, v4, v5\nv_max3_f32 v44, v44, v6, v7\nQzvaa 0,112,96\nQmvaa 0,116,100\nQmvaa 0,120,104\nQmvaa 0,124,108\n"
"Qzvaa 4,128,96\nQmvaa 4,132,100\nQmvaa 4,136,104\nQmvaa 4,140,108\ns_nop 7\nv_max3_f32 v48, v48, v0, v1\nv_max3_f32 v48, v48, v2, v3\nv_max3_f32 v48, v48, v4, v5\n"
"v_max3_f32 v48, v48, v6, v7\ns_add_u32 s89, s89, 8192\ns_add_u32 s66, s66, 1\ns_cmp_lt_u32 s66, 32\ns_cbranch_scc1 .Lfb1\nv_mov_b32 v25, v24\ns_nop 1\nv_permlane32_swap_b32 v24, v25\n"
"v_max_f32 v24, v24, v25\nv_mov_b32 v25, v24\ns_nop 1\nv_permlane16_swap_b32 v24, v25\nv_max_f32 v24, v24, v25\nv_sub_f32 v24, 0, v24\nv_mov_b32 v25, v24\nv_mov_b32 v26, v24\n"
"v_mov_b32 v27, v24\nv_mov_b32 v29, v28\ns_nop 1\nv_permlane32_swap_b32 v28, v29\nv_max_f32 v28, v28, v29\nv_mov_b32 v29, v28\ns_nop 1\nv_permlane16_swap_b32 v28, v29\n"
"v_max_f32 v28, v28, v29\nv_sub_f32 v28, 0, v28\nv_mov_b32 v29, v28\nv_mov_b32 v30, v28\nv_mov_b32 v31, v28\nv_mov_b32 v33, v32\ns_nop 1\nv_permlane32_swap_b32 v32, v33\n"
"v_max_f32 v32, v32, v33\nv_mov_b32 v33, v32\ns_nop 1\nv_permlane16_swap_b32 v32, v33\nv_max_f32 v32, v32, v33\nv_sub_f32 v32, 0, v32\nv_mov_b32 v33, v32\nv_mov_b32 v34, v32\n"
"v_mov_b32 v35, v32\nv_mov_b32 v37, v36\ns_nop 1\nv_permlane32_swap_b32 v36, v37\nv_max_f32 v36, v36, v37\nv_mov_b32 v37, v36\ns_nop 1\nv_permlane16_swap_b32 v36, v37\n"
"v_max_f32 v36, v36, v37\nv_sub_f32 v36, 0, v36\nv_mov_b32 v37, v36\nv_mov_b32 v38, v36\nv_mov_b32 v39, v36\nv_mov_b32 v41, v40\ns_nop 1\nv_permlane32_swap_b32 v40, v41\n"
"v_max_f32 v40, v40, v41\nv_mov_b32 v41, v40\ns_nop 1\nv_permlane16_swap_b32 v40, v41\nv_max_f32 v40, v40, v41\nv_sub_f32 v40, 0, v40\nv_mov_b32 v41, v40\nv_mov_b32 v42, v40\n"
"v_mov_b32 v43, v40\nv_mov_b32 v45, v44\ns_nop 1\nv_permlane32_swap_b32 v44, v45\nv_max_f32 v44, v44, v45\nv_mov_b32 v45, v44\ns_nop 1\nv_permlane16_swap_b32 v44, v45\n"
"v_max_f32 v44, v44, v45\nv_sub_f32 v44, 0, v44\nv_mov_b32 v45, v44\nv_mov_b32 v46, v44\nv_mov_b32 v47, v44\nv_mov_b32 v49, v48\ns_nop 1\nv_permlane32_swap_b32 v48, v49\n"
"v_max_f32 v48, v48, v49\nv_mov_b32 v49, v48\ns_nop 1\nv_permlane16_swap_b32 v48, v49\nv_max_f32 v48, v48, v49\nv_sub_f32 v48, 0, v48\nv_mov_b32 v49, v48\nv_mov_b32 v50, v48\n"
"v_mov_b32 v51, v48\nv_mov_b32 v52, 0\nv_mov_b32 v80, 0\nv_mov_b32 v81, 0\nv_mov_b32 v82, 0\nv_mov_b32 v83, 0\nv_mov_b32 v84, 0\nv_mov_b32 v85, 0\n"
"v_mov_b32 v86, 0\nv_mov_b32 v87, 0\nv_mov_b32 v88, 0\nv_mov_b32 v89, 0\nv_mov_b32 v90, 0\nv_mov_b32 v91, 0\nv_mov_b32 v92, 0\nv_mov_b32 v93, 0\n"
"v_mov_b32 v94, 0\nv_mov_b32 v95, 0\nv_mov_b32 v96, 0\nv_mov_b32 v97, 0\nv_mov_b32 v98, 0\nv_mov_b32 v99, 0\nv_mov_b32 v100, 0\nv_mov_b32 v101, 0\n"
"v_mov_b32 v102, 0\nv_mov_b32 v103, 0\nv_mov_b32 v104, 0\nv_mov_b32 v105, 0\nv_mov_b32 v106, 0\nv_mov_b32 v107, 0\nv_mov_b32 v108, 0\nv_mov_b32 v109, 0\n"
"v_mov_b32 v110, 0\nv_mov_b32 v111, 0\nv_accvgpr_write_b32 a176, v52\nv_accvgpr_write_b32 a177, v52\nv_accvgpr_write_b32 a178, v52\nv_accvgpr_write_b32 a179, v52\nv_mov_b32 v112, 0\nv_mov_b32 v113, 0\n"
"v_mov_b32 v114, 0\nv_mov_b32 v115, 0\nv_mov_b32 v116, 0\nv_mov_b32 v117, 0\nv_mov_b32 v118, 0\nv_mov_b32 v119, 0\nv_mov_b32 v120, 0\nv_mov_b32 v121, 0\n"
"v_mov_b32 v122, 0\nv_mov_b32 v123, 0\nv_mov_b32 v124, 0\nv_mov_b32 v125, 0\nv_mov_b32 v126, 0\nv_mov_b32 v127, 0\nv_mov_b32 v128, 0\nv_mov_b32 v129, 0\n"
"v_mov_b32 v130, 0\nv_mov_b32 v131, 0\nv_mov_b32 v132, 0\nv_mov_b32 v133, 0\nv_mov_b32 v134, 0\nv_mov_b32 v135, 0\nv_mov_b32 v136, 0\nv_mov_b32 v137, 0\n"
"v_mov_b32 v138, 0\nv_mov_b32 v139, 0\nv_mov_b32 v140, 0\nv_mov_b32 v141, 0\nv_mov_b32 v142, 0\nv_mov_b32 v143, 0\nv_accvgpr_write_b32 a180, v52\nv_accvgpr_write_b32 a181, v52\n"
"v_accvgpr_write_b32 a182, v52\nv_accvgpr_write_b32 a183, v52\nv_mov_b32 v144, 0\nv_mov_b32 v145, 0\nv_mov_b32 v146, 0\nv_mov_b32 v147, 0\nv_mov_b32 v148, 0\nv_mov_b32 v149, 0\n"
"v_mov_b32 v150, 0\nv_mov_b32 v151, 0\nv_mov_b32 v152, 0\nv_mov_b32 v153, 0\nv_mov_b32 v154, 0\nv_mov_b32 v155, 0\nv_mov_b32 v156, 0\nv_mov_b32 v157, 0\n"
"v_mov_b32 v158, 0\nv_mov_b32 v159, 0\nv_mov_b32 v160, 0\nv_mov_b32 v161, 0\nv_mov_b32 v162, 0\nv_mov_b32 v163, 0\nv_mov_b32 v164, 0\nv_mov_b32 v165, 0\n"
"v_mov_b32 v166, 0\nv_mov_b32 v167, 0\nv_mov_b32 v168, 0\nv_mov_b32 v169, 0\nv_mov_b32 v170, 0\nv_mov_b32 v171, 0\nv_mov_b32 v172, 0\nv_mov_b32 v173, 0\n"
"v_mov_b32 v174, 0\nv_mov_b32 v175, 0\nv_accvgpr_write_b32 a184, v52\nv_accvgpr_write_b32 a185, v52\nv_accvgpr_write_b32 a186, v52\nv_accvgpr_write_b32 a187, v52\nv_mov_b32 v176, 0\nv_mov_b32 v177, 0\n"
"v_mov_b32 v178, 0\nv_mov_b32 v179, 0\nv_mov_b32 v180, 0\nv_mov_b32 v181, 0\nv_mov_b32 v182, 0\nv_mov_b32 v183, 0\nv_mov_b32 v184, 0\nv_mov_b32 v185, 0\n"
"v_mov_b32 v186, 0\nv_mov_b32 v187, 0\nv_mov_b32 v188, 0\nv_mov_b32 v189, 0\nv_mov_b32 v190, 0\nv_mov_b32 v191, 0\nv_mov_b32 v192, 0\nv_mov_b32 v193, 0\n"
"v_mov_b32 v194, 0\nv_mov_b32 v195, 0\nv_mov_b32 v196, 0\nv_mov_b32 v197, 0\nv_mov_b32 v198, 0\nv_mov_b32 v199, 0\nv_mov_b32 v200, 0\nv_mov_b32 v201, 0\n"
"v_mov_b32 v202, 0\nv_mov_b32 v203, 0\nv_mov_b32 v204, 0\nv_mov_b32 v205, 0\nv_mov_b32 v206, 0\nv_mov_b32 v207, 0\nv_accvgpr_write_b32 a188, v52\nv_accvgpr_write_b32 a189, v52\n"
"v_accvgpr_write_b32 a190, v52\nv_accvgpr_write_b32 a191, v52\nv_mov_b32 v208, 0\nv_mov_b32 v209, 0\nv_mov_b32 v210, 0\nv_mov_b32 v211, 0\nv_mov_b32 v212, 0\nv_mov_b32 v213, 0\n"
"v_mov_b32 v214, 0\nv_mov_b32 v215, 0\nv_mov_b32 v216, 0\nv_mov_b32 v217, 0\nv_mov_b32 v218, 0\nv_mov_b32 v219, 0\nv_mov_b32 v220, 0\nv_mov_b32 v221, 0\n"
"v_mov_b32 v222, 0\nv_mov_b32 v223, 0\nv_mov_b32 v224, 0\nv_mov_b32 v225, 0\nv_mov_b32 v226, 0\nv_mov_b32 v227, 0\nv_mov_b32 v228, 0\nv_mov_b32 v229, 0\n"
"v_mov_b32 v230, 0\nv_mov_b32 v231, 0\nv_mov_b32 v232, 0\nv_mov_b32 v233, 0\nv_mov_b32 v234, 0\nv_mov_b32 v235, 0\nv_mov_b32 v236, 0\nv_mov_b32 v237, 0\n"
"v_mov_b32 v238, 0\nv_mov_b32 v239, 0\nv_accvgpr_write_b32 a192, v52\nv_accvgpr_write_b32 a193, v52\nv_accvgpr_write_b32 a194, v52\nv_accvgpr_write_b32 a195, v52\nv_mov_b32 v240, 0\nv_mov_b32 v241, 0\n"
"v_mov_b32 v242, 0\nv_mov_b32 v243, 0\nv_mov_b32 v244, 0\nv_mov_b32 v245, 0\nv_mov_b32 v246, 0\nv_mov_b32 v247, 0\nv_mov_b32 v248, 0\nv_mov_b32 v249, 0\n"
"v_mov_b32 v250, 0\nv_mov_b32 v251, 0\nv_mov_b32 v252, 0\nv_mov_b32 v253, 0\nv_mov_b32 v254, 0\nv_mov_b32 v255, 0\nv_accvgpr_write_b32 a208, v52\nv_accvgpr_write_b32 a209, v52\n"
"v_accvgpr_write_b32 a210, v52\nv_accvgpr_write_b32 a211, v52\nv_accvgpr_write_b32 a212, v52\nv_accvgpr_write_b32 a213, v52\nv_accvgpr_write_b32 a214, v52\nv_accvgpr_write_b32 a215, v52\nv_accvgpr_write_b32 a216, v52\nv_accvgpr_write_b32 a217, v52\n"
"v_accvgpr_write_b32 a218, v52\nv_accvgpr_write_b32 a219, v52\nv_accvgpr_write_b32 a220, v52\nv_accvgpr_write_b32 a221, v52\nv_accvgpr_write_b32 a222, v52\nv_accvgpr_write_b32 a223, v52\nv_accvgpr_write_b32 a196, v52\nv_accvgpr_write_b32 a197, v52\n"
"v_accvgpr_write_b32 a198, v52\nv_accvgpr_write_b32 a199, v52\nv_accvgpr_write_b32 a224, v52\nv_accvgpr_write_b32 a225, v52\nv_accvgpr_write_b32 a226, v52\nv_accvgpr_write_b32 a227, v52\nv_accvgpr_write_b32 a228, v52\nv_accvgpr_write_b32 a229, v52\n"
"v_accvgpr_write_b32 a230, v52\nv_accvgpr_write_b32 a231, v52\nv_accvgpr_write_b32 a232, v52\nv_accvgpr_write_b32 a233, v52\nv_accvgpr_write_b32 a234, v52\nv_accvgpr_write_b32 a235, v52\nv_accvgpr_write_b32 a236, v52\nv_accvgpr_write_b32 a237, v52\n"
"v_accvgpr_write_b32 a238, v52\nv_accvgpr_write_b32 a239, v52\nv_accvgpr_write_b32 a240, v52\nv_accvgpr_write_b32 a241, v52\nv_accvgpr_write_b32 a242, v52\nv_accvgpr_write_b32 a243, v52\nv_accvgpr_write_b32 a244, v52\nv_accvgpr_write_b32 a245, v52\n"
"v_accvgpr_write_b32 a246, v52\nv_accvgpr_write_b32 a247, v52\nv_accvgpr_write_b32 a248, v52\nv_accvgpr_write_b32 a249, v52\nv_accvgpr_write_b32 a250, v52\nv_accvgpr_write_b32 a251, v52\nv_accvgpr_write_b32 a252, v52\nv_accvgpr_write_b32 a253, v52\n"
"v_accvgpr_write_b32 a254, v52\nv_accvgpr_write_b32 a255, v52\nv_accvgpr_write_b32 a200, v52\nv_accvgpr_write_b32 a201, v52\nv_accvgpr_write_b32 a202, v52\nv_accvgpr_write_b32 a203, v52\ns_lshl_b32 s89, s67, 21\ns_mov_b32 s66, 0\n"
".Lfb2:\ns_add_u32 s68, s89, 4096\nbuffer_load_dwordx4 a[112:115], v66, s[40:43], s89 offen offset:0\nbuffer_load_dwordx4 a[116:119], v66, s[40:43], s89 offen offset:64\nbuffer_load_dwordx4 a[120:123], v66, s[40:43], s89 offen offset:128\nbuffer_load_dwordx4 a[124:127], v66, s[40:43], s89 offen offset:192\nbuffer_load_dwordx4 a[128:131], v66, s[40:43], s68 offen offset:0\nbuffer_load_dwordx4 a[132:135], v66, s[40:43], s68 offen offset:64\n"
"buffer_load_dwordx4 a[136:139], v66, s[40:43], s68 offen offset:128\nbuffer_load_dwordx4 a[140:143], v66, s[40:43], s68 offen offset:192\nbuffer_load_ushort v8, v67, s[44:47], s89 offen offset:0\nbuffer_load_ushort v9, v67, s[44:47], s89 offen offset:256\nbuffer_load_ushort v10, v67, s[44:47], s89 offen offset:512\nbuffer_load_ushort v11, v67, s[44:47], s89 offen offset:768\nbuffer_load_ushort v12, v67, s[44:47], s68 offen offset:0\nbuffer_load_ushort v13, v67, s[44:47], s68 offen offset:256\n"
"buffer_load_ushort v14, v67, s[44:47], s68 offen offset:512\nbuffer_load_ushort v15, v67, s[44:47], s68 offen offset:768\ns_waitcnt vmcnt(0)\nv_lshl_or_b32 v8, v9, 16, v8\nv_accvgpr_write_b32 a144, v8\nv_lshl_or_b32 v10, v11, 16, v10\nv_accvgpr_write_b32 a145, v10\nv_lshl_or_b32 v12, v13, 16, v12\n"
"v_accvgpr_write_b32 a146, v12\nv_lshl_or_b32 v14, v15, 16, v14\nv_accvgpr_write_b32 a147, v14\nbuffer_load_ushort v8, v67, s[44:47], s89 offen offset:32\nbuffer_load_ushort v9, v67, s[44:47], s89 offen offset:288\nbuffer_load_ushort v10, v67, s[44:47], s89 offen offset:544\nbuffer_load_ushort v11, v67, s[44:47], s89 offen offset:800\nbuffer_load_ushort v12, v67, s[44:47], s68 offen offset:32\n"
"buffer_load_ushort v13, v67, s[44:47], s68 offen offset:288\nbuffer_load_ushort v14, v67, s[44:47], s68 offen offset:544\nbuffer_load_ushort v15, v67, s[44:47], s68 offen offset:800\ns_waitcnt vmcnt(0)\nv_lshl_or_b32 v8, v9, 16, v8\nv_accvgpr_write_b32 a148, v8\nv_lshl_or_b32 v10, v11, 16, v10\nv_accvgpr_write_b32 a149, v10\n"
"v_lshl_or_b32 v12, v13, 16, v12\nv_accvgpr_write_b32 a150, v12\nv_lshl_or_b32 v14, v15, 16, v14\nv_accvgpr_write_b32 a151, v14\nbuffer_load_ushort v8, v67, s[44:47], s89 offen offset:64\nbuffer_load_ushort v9, v67, s[44:47], s89 offen offset:320\nbuffer_load_ushort v10, v67, s[44:47], s89 offen offset:576\nbuffer_load_ushort v11, v67, s[44:47], s89 offen offset:832\n"
"buffer_load_ushort v12, v67, s[44:47], s68 offen offset:64\nbuffer_load_ushort v13, v67, s[44:47], s68 offen offset:320\nbuffer_load_ushort v14, v67, s[44:47], s68 offen offset:576\nbuffer_load_ushort v15, v67, s[44:47], s68 offen offset:832\ns_waitcnt vmcnt(0)\nv_lshl_or_b32 v8, v9, 16, v8\nv_accvgpr_write_b32 a152, v8\nv_lshl_or_b32 v10, v11, 16, v10\n"
"v_accvgpr_write_b32 a153, v10\nv_lshl_or_b32 v12, v13, 16, v12\nv_accvgpr_write_b32 a154, v12\nv_lshl_or_b32 v14, v15, 16, v14\nv_accvgpr_write_b32 a155, v14\nbuffer_load_ushort v8, v67, s[44:47], s89 offen offset:96\nbuffer_load_ushort v9, v67, s[44:47], s89 offen offset:352\nbuffer_load_ushort v10, v67, s[44:47], s89 offen offset:608\n"
"buffer_load_ushort v11, v67, s[44:47], s89 offen offset:864\nbuffer_load_ushort v12, v67, s[44:47], s68 offen offset:96\nbuffer_load_ushort v13, v67, s[44:47], s68 offen offset:352\nbuffer_load_ushort v14, v67, s[44:47], s68 offen offset:608\nbuffer_load_ushort v15, v67, s[44:47], s68 offen offset:864\ns_waitcnt vmcnt(0)\nv_lshl_or_b32 v8, v9, 16, v8\nv_accvgpr_write_b32 a156, v8\n"
"v_lshl_or_b32 v10, v11, 16, v10\nv_accvgpr_write_b32 a157, v10\nv_lshl_or_b32 v12, v13, 16, v12\nv_accvgpr_write_b32 a158, v12\nv_lshl_or_b32 v14, v15, 16, v14\nv_accvgpr_write_b32 a159, v14\nbuffer_load_ushort v8, v67, s[44:47], s89 offen offset:128\nbuffer_load_ushort v9, v67, s[44:47], s89 offen offset:384\n"
"buffer_load_ushort v10, v67, s[44:47], s89 offen offset:640\nbuffer_load_ushort v11, v67, s[44:47], s89 offen offset:896\nbuffer_load_ushort v12, v67, s[44:47], s68 offen offset:128\nbuffer_load_ushort v13, v67, s[44:47], s68 offen offset:384\nbuffer_load_ushort v14, v67, s[44:47], s68 offen offset:640\nbuffer_load_ushort v15, v67, s[44:47], s68 offen offset:896\ns_waitcnt vmcnt(0)\nv_lshl_or_b32 v8, v9, 16, v8\n"
"v_accvgpr_write_b32 a160, v8\nv_lshl_or_b32 v10, v11, 16, v10\nv_accvgpr_write_b32 a161, v10\nv_lshl_or_b32 v12, v13, 16, v12\nv_accvgpr_write_b32 a162, v12\nv_lshl_or_b32 v14, v15, 16, v14\nv_accvgpr_write_b32 a163, v14\nbuffer_load_ushort v8, v67, s[44:47], s89 offen offset:160\n"
"buffer_load_ushort v9, v67, s[44:47], s89 offen offset:416\nbuffer_load_ushort v10, v67, s[44:47], s89 offen offset:672\nbuffer_load_ushort v11, v67, s[44:47], s89 offen offset:928\nbuffer_load_ushort v12, v67, s[44:47], s68 offen offset:160\nbuffer_load_ushort v13, v67, s[44:47], s68 offen offset:416\nbuffer_load_ushort v14, v67, s[44:47], s68 offen offset:672\nbuffer_load_ushort v15, v67, s[44:47], s68 offen offset:928\ns_waitcnt vmcnt(0)\n"
"v_lshl_or_b32 v8, v9, 16, v8\nv_accvgpr_write_b32 a164, v8\nv_lshl_or_b32 v10, v11, 16, v10\nv_accvgpr_write_b32 a165, v10\nv_lshl_or_b32 v12, v13, 16, v12\nv_accvgpr_write_b32 a166, v12\nv_lshl_or_b32 v14, v15, 16, v14\nv_accvgpr_write_b32 a167, v14\n"
"buffer_load_ushort v8, v67, s[44:47], s89 offen offset:192\nbuffer_load_ushort v9, v67, s[44:47], s89 offen offset:448\nbuffer_load_ushort v10, v67, s[44:47], s89 offen offset:704\nbuffer_load_ushort v11, v67, s[44:47], s89 offen offset:960\nbuffer_load_ushort v12, v67, s[44:47], s68 offen offset:192\nbuffer_load_ushort v13, v67, s[44:47], s68 offen offset:448\nbuffer_load_ushort v14, v67, s[44:47], s68 offen offset:704\nbuffer_load_ushort v15, v67, s[44:47], s68 offen offset:960\n"
"s_waitcnt vmcnt(0)\nv_lshl_or_b32 v8, v9, 16, v8\nv_accvgpr_write_b32 a168, v8\nv_lshl_or_b32 v10, v11, 16, v10\nv_accvgpr_write_b32 a169, v10\nv_lshl_or_b32 v12, v13, 16, v12\nv_accvgpr_write_b32 a170, v12\nv_lshl_or_b32 v14, v15, 16, v14\n"
"v_accvgpr_write_b32 a171, v14\nbuffer_load_ushort v8, v67, s[44:47], s89 offen offset:224\nbuffer_load_ushort v9, v67, s[44:47], s89 offen offset:480\nbuffer_load_ushort v10, v67, s[44:47], s89 offen offset:736\nbuffer_load_ushort v11, v67, s[44:47], s89 offen offset:992\nbuffer_load_ushort v12, v67, s[44:47], s68 offen offset:224\nbuffer_load_ushort v13, v67, s[44:47], s68 offen offset:480\nbuffer_load_ushort v14, v67, s[44:47], s68 offen offset:736\n"
"buffer_load_ushort v15, v67, s[44:47], s68 offen offset:992\ns_waitcnt vmcnt(0)\nv_lshl_or_b32 v8, v9, 16, v8\nv_accvgpr_write_b32 a172, v8\nv_lshl_or_b32 v10, v11, 16, v10\nv_accvgpr_write_b32 a173, v10\nv_lshl_or_b32 v12, v13, 16, v12\nv_accvgpr_write_b32 a174, v12\n"
"v_lshl_or_b32 v14, v15, 16, v14\nv_accvgpr_write_b32 a175, v14\nQcvaa 0,112,0,24\nQmvaa 0,116,4\nQmvaa 0,120,8\nQmvaa 0,124,12\nQcvaa 4,128,0,24\nQmvaa 4,132,4\n"
"Qmvaa 4,136,8\nQmvaa 4,140,12\ns_nop 7\nQe 0\nQe 1\nQe 2\nQe 3\nQe 4\n"
"Qe 5\nQe 6\nQe 7\ns_nop 0\nQk 16,0,1\nQk 17,2,3\nQk 18,4,5\nQk 19,6,7\n"
"s_nop 1\nQmvva 80,16,144\nQmvva 84,16,148\nQmvva 88,16,152\nQmvva 92,16,156\nQmvva 96,16,160\nQmvva 100,16,164\nQmvva 104,16,168\n"
"Qmvva 108,16,172\nQmaav 176,204,16\nQcvaa 0,112,16,28\nQmvaa 0,116,20\nQmvaa 0,120,24\nQmvaa 0,124,28\nQcvaa 4,128,16,28\nQmvaa 4,132,20\n"
"Qmvaa 4,136,24\nQmvaa 4,140,28\ns_nop 7\nQe 0\nQe 1\nQe 2\nQe 3\nQe 4\n"
"Qe 5\nQe 6\nQe 7\ns_nop 0\nQk 16,0,1\nQk 17,2,3\nQk 18,4,5\nQk 19,6,7\n"
"s_nop 1\nQmvva 112,16,144\nQmvva 116,16,148\nQmvva 120,16,152\nQmvva 124,16,156\nQmvva 128,16,160\nQmvva 132,16,164\nQmvva 136,16,168\n"
"Qmvva 140,16,172\nQmaav 180,204,16\nQcvaa 0,112,32,32\nQmvaa 0,116,36\nQmvaa 0,120,40\nQmvaa 0,124,44\nQcvaa 4,128,32,32\nQmvaa 4,132,36\n"
"Qmvaa 4,136,40\nQmvaa 4,140,44\ns_nop 7\nQe 0\nQe 1\nQe 2\nQe 3\nQe 4\n"
"Qe 5\nQe 6\nQe 7\ns_nop 0\nQk 16,0,1\nQk 17,2,3\nQk 18,4,5\nQk 19,6,7\n"
"s_nop 1\nQmvva 144,16,144\nQmvva 148,16,148\nQmvva 152,16,152\nQmvva 156,16,156\nQmvva 160,16,160\nQmvva 164,16,164\nQmvva 168,16,168\n"
"Qmvva 172,16,172\nQmaav 184,204,16\nQcvaa 0,112,48,36\nQmvaa 0,116,52\nQmvaa 0,120,56\nQmvaa 0,124,60\nQcvaa 4,128,48,36\nQmvaa 4,132,52\n"
"Qmvaa 4,136,56\nQmvaa 4,140,60\ns_nop 7\nQe 0\nQe 1\nQe 2\nQe 3\nQe 4\n"
"Qe 5\nQe 6\nQe 7\ns_nop 0\nQk 16,0,1\nQk 17,2,3\nQk 18,4,5\nQk 19,6,7\n"
"s_nop 1\nQmvva 176,16,144\nQmvva 180,16,148\nQmvva 184,16,152\nQmvva 188,16,156\nQmvva 192,16,160\nQmvva 196,16,164\nQmvva 200,16,168\n"
"Qmvva 204,16,172\nQmaav 188,204,16\nQcvaa 0,112,64,40\nQmvaa 0,116,68\nQmvaa 0,120,72\nQmvaa 0,124,76\nQcvaa 4,128,64,40\nQmvaa 4,132,68\n"
"Qmvaa 4,136,72\nQmvaa 4,140,76\ns_nop 7\nQe 0\nQe 1\nQe 2\nQe 3\nQe 4\n"
"Qe 5\nQe 6\nQe 7\ns_nop 0\nQk 16,0,1\nQk 17,2,3\nQk 18,4,5\nQk 19,6,7\n"
"s_nop 1\nQmvva 208,16,144\nQmvva 212,16,148\nQmvva 216,16,152\nQmvva 220,16,156\nQmvva 224,16,160\nQmvva 228,16,164\nQmvva 232,16,168\n"
"Qmvva 236,16,172\nQmaav 192,204,16\nQcvaa 0,112,80,44\nQmvaa 0,116,84\nQmvaa 0,120,88\nQmvaa 0,124,92\nQcvaa 4,128,80,44\nQmvaa 4,132,84\n"
"Qmvaa 4,136,88\nQmvaa 4,140,92\ns_nop 7\nQe 0\nQe 1\nQe 2\nQe 3\nQe 4\n"
"Qe 5\nQe 6\nQe 7\ns_nop 0\nQk 16,0,1\nQk 17,2,3\nQk 18,4,5\nQk 19,6,7\n"
"s_nop 1\nQmvva 240,16,144\nQmvva 244,16,148\nQmvva 248,16,152\nQmvva 252,16,156\nQmava 208,16,160\nQmava 212,16,164\nQmava 216,16,168\n"
"Qmava 220,16,172\nQmaav 196,204,16\nQcvaa 0,112,96,48\nQmvaa 0,116,100\nQmvaa 0,120,104\nQmvaa 0,124,108\nQcvaa 4,128,96,48\nQmvaa 4,132,100\n"
"Qmvaa 4,136,104\nQmvaa 4,140,108\ns_nop 7\nQe 0\nQe 1\nQe 2\nQe 3\nQe 4\n"
"Qe 5\nQe 6\nQe 7\ns_nop 0\nQk 16,0,1\nQk 17,2,3\nQk 18,4,5\nQk 19,6,7\n"
"s_nop 1\nQmava 224,16,144\nQmava 228,16,148\nQmava 232,16,152\nQmava 236,16,156\nQmava 240,16,160\nQmava 244,16,164\nQmava 248,16,168\n"
"Qmava 252,16,172\nQmaav 200,204,16\ns_add_u32 s89, s89, 8192\ns_add_u32 s66, s66, 1\ns_cmp_lt_u32 s66, 32\ns_cbranch_scc1 .Lfb2\ns_nop 7\nv_accvgpr_read_b32 v0, a176\n"
"v_accvgpr_read_b32 v4, a180\nv_accvgpr_read_b32 v8, a184\nv_accvgpr_read_b32 v12, a188\nv_accvgpr_read_b32 v16, a192\nv_accvgpr_read_b32 v20, a196\nv_accvgpr_read_b32 v24, a200\ns_cmp_eq_u32 s67, 0\ns_cbranch_scc1 .Lfbret0\n"
"s_branch .Lfbret1\n.Lf2_w:\ns_mov_b32 s97, 0\n.Lf2_l:\ns_sleep 1\nbuffer_load_dword v78, v76, s[24:27], 0 offen sc1\ns_waitcnt vmcnt(0)\ns_add_u32 s97, s97, 1\n"
"s_cmp_gt_u32 s97, 1048576\ns_cbranch_scc1 .Lf2_r\nv_sub_u32 v66, v78, v65\nv_cmp_gt_u32 vcc, 2, v66\ns_and_b64 vcc, vcc, s[28:29]\ns_cbranch_scc1 .Lf2_l\ns_branch .Lf2_r\n.Lf3_w:\n"
"s_mov_b32 s97, 0\n.Lf3_l:\ns_sleep 1\nbuffer_load_dword v78, v76, s[24:27], 0 offen sc1\ns_waitcnt vmcnt(0)\ns_add_u32 s97, s97, 1\ns_cmp_gt_u32 s97, 1048576\ns_cbranch_scc1 .Lf3_r\n"
"v_sub_u32 v66, v78, v65\nv_cmp_gt_u32 vcc, 3, v66\ns_and_b64 vcc, vcc, s[28:29]\ns_cbranch_scc1 .Lf3_l\ns_branch .Lf3_r\n.Lx0_w:\ns_mov_b32 s97, 0\n.Lx0_l:\n"
"s_sleep 1\nbuffer_load_dword v78, v76, s[24:27], 0 offen sc1\ns_waitcnt vmcnt(0)\ns_add_u32 s97, s97, 1\ns_cmp_gt_u32 s97, 1048576\ns_cbranch_scc1 .Lx0_r\nv_sub_u32 v66, v78, v65\nv_cmp_gt_u32 vcc, 1, v66\n"
"s_and_b64 vcc, vcc, s[28:29]\ns_cbranch_scc1 .Lx0_l\ns_branch .Lx0_r\n.Lx1_w:\ns_mov_b32 s97, 0\n.Lx1_l:\ns_sleep 1\nbuffer_load_dword v78, v76, s[24:27], 0 offen sc1\n"
"s_waitcnt vmcnt(0)\ns_add_u32 s97, s97, 1\ns_cmp_gt_u32 s97, 1048576\ns_cbranch_scc1 .Lx1_r\nv_sub_u32 v66, v78, v65\nv_cmp_gt_u32 vcc, 1, v66\ns_and_b64 vcc, vcc, s[28:29]\ns_cbranch_scc1 .Lx1_l\n"
"s_branch .Lx1_r\n.Lexit:\n"
  :: "s"((unsigned)kp), "s"((unsigned)(kp >> 32)), "s"((unsigned)vp), "s"((unsigned)(vp >> 32)), "s"((unsigned)xp), "s"((unsigned)(xp >> 32)),
     "s"((unsigned)wp), "s"((unsigned)(wp >> 32)), "s"((unsigned)op), "s"((unsigned)(op >> 32)), "s"(ldsb), "s"(wid), "s"(head0),
     "s"((unsigned)dp), "s"((unsigned)(dp >> 32)), "s"((unsigned)wop), "s"((unsigned)(wop >> 32)), "s"((unsigned)yp), "s"((unsigned)(yp >> 32)),
     "s"(hg), "s"(b * 8u + hg)
  : "v0", "v1", "v2", "v3", "v4", "v5", "v6", "v7", "v8", "v9", "v10", "v11", "v12", "v13", "v14", "v15", "v16", "v17", "v18", "v19", "v20", "v21", "v22", "v23", "v24", "v25", "v26", "v27", "v28", "v29", "v30", "v31", "v32", "v33", "v34", "v35", "v36", "v37", "v38", "v39", "v40", "v41", "v42", "v43", "v44", "v45", "v46", "v47", "v48", "v49", "v50", "v51", "v52", "v53", "v54", "v55", "v56", "v57", "v58", "v59", "v60", "v61", "v62", "v63", "v64", "v65", "v66", "v67", "v68", "v69", "v70", "v71", "v72", "v73", "v74", "v75", "v76", "v77", "v78", "v79", "v80", "v81", "v82", "v83", "v84", "v85", "v86", "v87", "v88", "v89", "v90", "v91", "v92", "v93", "v94", "v95", "v96", "v97", "v98", "v99", "v100", "v101", "v102", "v103", "v104", "v105", "v106", "v107", "v108", "v109", "v110", "v111", "v112", "v113", "v114", "v115", "v116", "v117", "v118", "v119", "v120", "v121", "v122", "v123", "v124", "v125", "v126", "v127", "v128", "v129", "v130", "v131", "v132", "v133", "v134", "v135", "v136", "v137", "v138", "v139", "v140", "v141", "v142", "v143", "v144", "v145", "v146", "v147", "v148", "v149", "v150", "v151", "v152", "v153", "v154", "v155", "v156", "v157", "v158", "v159", "v160", "v161", "v162", "v163", "v164", "v165", "v166", "v167", "v168", "v169", "v170", "v171", "v172", "v173", "v174", "v175", "v176", "v177", "v178", "v179", "v180", "v181", "v182", "v183", "v184", "v185", "v186", "v187", "v188", "v189", "v190", "v191", "v192", "v193", "v194", "v195", "v196", "v197", "v198", "v199", "v200", "v201", "v202", "v203", "v204", "v205", "v206", "v207", "v208", "v209", "v210", "v211", "v212", "v213", "v214", "v215", "v216", "v217", "v218", "v219", "v220", "v221", "v222", "v223", "v224", "v225", "v226", "v227", "v228", "v229", "v230", "v231", "v232", "v233", "v234", "v235", "v236", "v237", "v238", "v239", "v240", "v241", "v242", "v243", "v244", "v245", "v246", "v247", "v248", "v249", "v250", "v251", "v252", "v253", "v254", "v255", "a0", "a1", "a2", "a3", "a4", "a5", "a6", "a7", "a8", "a9", "a10", "a11", "a12", "a13", "a14", "a15", "a16", "a17", "a18", "a19", "a20", "a21", "a22", "a23", "a24", "a25", "a26", "a27", "a28", "a29", "a30", "a31", "a32", "a33", "a34", "a35", "a36", "a37", "a38", "a39", "a40", "a41", "a42", "a43", "a44", "a45", "a46", "a47", "a48", "a49", "a50", "a51", "a52", "a53", "a54", "a55", "a56", "a57", "a58", "a59", "a60", "a61", "a62", "a63", "a64", "a65", "a66", "a67", "a68", "a69", "a70", "a71", "a72", "a73", "a74", "a75", "a76", "a77", "a78", "a79", "a80", "a81", "a82", "a83", "a84", "a85", "a86", "a87", "a88", "a89", "a90", "a91", "a92", "a93", "a94", "a95", "a96", "a97", "a98", "a99", "a100", "a101", "a102", "a103", "a104", "a105", "a106", "a107", "a108", "a109", "a110", "a111", "a112", "a113", "a114", "a115", "a116", "a117", "a118", "a119", "a120", "a121", "a122", "a123", "a124", "a125", "a126", "a127", "a128", "a129", "a130", "a131", "a132", "a133", "a134", "a135", "a136", "a137", "a138", "a139", "a140", "a141", "a142", "a143", "a144", "a145", "a146", "a147", "a148", "a149", "a150", "a151", "a152", "a153", "a154", "a155", "a156", "a157", "a158", "a159", "a160", "a161", "a162", "a163", "a164", "a165", "a166", "a167", "a168", "a169", "a170", "a171", "a172", "a173", "a174", "a175", "a176", "a177", "a178", "a179", "a180", "a181", "a182", "a183", "a184", "a185", "a186", "a187", "a188", "a189", "a190", "a191", "a192", "a193", "a194", "a195", "a196", "a197", "a198", "a199", "a200", "a201", "a202", "a203", "a204", "a205", "a206", "a207", "a208", "a209", "a210", "a211", "a212", "a213", "a214", "a215", "a216", "a217", "a218", "a219", "a220", "a221", "a222", "a223", "a224", "a225", "a226", "a227", "a228", "a229", "a230", "a231", "a232", "a233", "a234", "a235", "a236", "a237", "a238", "a239", "a240", "a241", "a242", "a243", "a244", "a245", "a246", "a247", "a248", "a249", "a250", "a251", "a252", "a253", "a254", "a255", "s24", "s25", "s26", "s27", "s28", "s29", "s30", "s31", "s40", "s41", "s42", "s43", "s44", "s45", "s46", "s47", "s48", "s49", "s50", "s51", "s52", "s53", "s54", "s55", "s56", "s57", "s58", "s59", "s60", "s61", "s62", "s63", "s64", "s65", "s66", "s67", "s68", "s69", "s70", "s71", "s72", "s73", "s74", "s75", "s76", "s77", "s78", "s79", "s80", "s81", "s82", "s83", "s84", "s85", "s86", "s87", "s88", "s89", "s90", "s91", "s92", "s93", "s94", "s95", "s96", "s97", "s98", "s99", "vcc", "scc", "memory");
}

extern "C" void kernel_launch(void* const* d_in, const int* in_sizes, int n_in, void* d_out, int out_size, void* d_ws, size_t ws_size, hipStream_t stream) {
  typedef const unsigned short* P;
  (void)hipFuncSetAttribute((const void*)mha2_asm, hipFuncAttributeMaxDynamicSharedMemorySize, 163840);
  hipLaunchKernelGGL(mha2_asm, dim3(256), dim3(256), 163840, stream, (P)d_in[0], (P)d_in[5], (P)d_in[3], (P)d_in[8], (P)d_in[1], (P)d_in[6], (P)d_in[2], (P)d_in[7],
                     (P)d_in[4], (P)d_in[9], (char*)d_ws, (unsigned short*)d_out);
}
```

```cpp
#include <hip/hip_runtime.h>
#include <stdint.h>

__global__ __launch_bounds__(256, 1) __attribute__((aligned(4096)))
void mha2_asm(const unsigned short* __restrict__ X0, const unsigned short* __restrict__ X1, const unsigned short* __restrict__ W0, const unsigned short* __restrict__ W1,
              const unsigned short* __restrict__ K0, const unsigned short* __restrict__ K1,
              const unsigned short* __restrict__ V0, const unsigned short* __restrict__ V1,
              const unsigned short* __restrict__ WO0, const unsigned short* __restrict__ WO1, char* ws, unsigned short* out) {
  extern __shared__ __attribute__((aligned(16))) char lds[];
  const unsigned c = blockIdx.x;
  const unsigned xcd = c & 7u, idx = c >> 3;
  const unsigned hg = idx & 7u, bb = xcd * 4u + (idx >> 3);
  const unsigned bk = bb >> 4, b = bb & 15u;
  const unsigned wid = __builtin_amdgcn_readfirstlane(threadIdx.x >> 6);
  const unsigned head0 = (hg >> 2) * 16u + (hg & 3u) * 2u + (wid >> 1);
  const unsigned long long kp = (unsigned long long)(bk ? K1 : K0) + ((unsigned long long)b * 32u + (head0 & ~1u)) * 262144ull - 1024ull;
  const unsigned long long vp = (unsigned long long)(bk ? V1 : V0) + ((unsigned long long)b * 32u + (head0 & ~1u)) * 262144ull - 1024ull;
  const unsigned long long xp = (unsigned long long)(bk ? X1 : X0) + (unsigned long long)b * (224u * 1280u);
  const unsigned long long wp = (unsigned long long)(bk ? W1 : W0);
  const unsigned long long op = (unsigned long long)ws + 0ull + (unsigned long long)bb * 2295808ull;
  const unsigned ldsb = (unsigned)(size_t)lds;
  const unsigned long long wop = (unsigned long long)(bk ? WO1 : WO0) - 4096ull;
  const unsigned long long yp = (unsigned long long)out + (unsigned long long)b * (224ull * 2560ull) + bk * 1280u + (hg & 3u) * 320u;
  const unsigned long long dp = (unsigned long long)ws + (100ull << 20) + (c * 4u + wid) * 256u;
  const unsigned hd = wid >> 1, ky = hg & 4u;
  const unsigned kx = (ky >> 0 & 1u) << 11 | (ky >> 1 & 1u) << 12 | (ky >> 2 & 1u) << 13 | (ky >> 3 & 1u) << 14 | (ky >> 4 & 1u) << 17;
  asm volatile(
".macro Qta d,v,o\nds_read_b64_tr_b16 a[\\d:\\d+1], v\\v offset:\\o\n.endm\n.macro Qdv d,v,o\nds_read_b128 v[\\d:\\d+3], v\\v offset:\\o\n.endm\n.macro Qda d,v,o\nds_read_b128 a[\\d:\\d+3], v\\v offset:\\o\n"
".endm\n.macro Qzvav d,a,b\nv_mfma_f32_16x16x32_bf16 v[\\d:\\d+3], a[\\a:\\a+3], v[\\b:\\b+3], 0\n.endm\n.macro Qmvaa d,a,b\nv_mfma_f32_16x16x32_bf16 v[\\d:\\d+3], a[\\a:\\a+3], a[\\b:\\b+3], v[\\d:\\d+3]\n.endm\n.macro Qzaav d,a,b\n"
"v_mfma_f32_16x16x32_bf16 a[\\d:\\d+3], a[\\a:\\a+3], v[\\b:\\b+3], 0\n.endm\n.macro Qmaaa d,a,b\nv_mfma_f32_16x16x32_bf16 a[\\d:\\d+3], a[\\a:\\a+3], a[\\b:\\b+3], a[\\d:\\d+3]\n.endm\n.macro Qmvav d,a,b\nv_mfma_f32_16x16x32_bf16 v[\\d:\\d+3], a[\\a:\\a+3], v[\\b:\\b+3], v[\\d:\\d+3]\n.endm\n"
".macro Qmaav d,a,b\nv_mfma_f32_16x16x32_bf16 a[\\d:\\d+3], a[\\a:\\a+3], v[\\b:\\b+3], a[\\d:\\d+3]\n.endm\n.macro Qk d,a,b\nv_cvt_pk_bf16_f32 v\\d, v\\a, v\\b\n.endm\n.macro Qzvaa d,a,b\nv_mfma_f32_16x16x32_bf16 v[\\d:\\d+3], a[\\a:\\a+3], a[\\b:\\b+3], 0\n"
".endm\n.macro Qa d,a,b\nv_add_f32 v\\d, v\\a, v\\b\n.endm\n.macro Qe d\nv_exp_f32 v\\d, v\\d\n.endm\n.macro Qcvaa d,a,b,c\n"
"v_mfma_f32_16x16x32_bf16 v[\\d:\\d+3], a[\\a:\\a+3], a[\\b:\\b+3], v[\\c:\\c+3]\n.endm\n.macro Qzvva d,a,b\nv_mfma_f32_16x16x32_bf16 v[\\d:\\d+3], v[\\a:\\a+3], a[\\b:\\b+3], 0\n.endm\n.macro Qzava d,a,b\nv_mfma_f32_16x16x32_bf16 a[\\d:\\d+3], v[\\a:\\a+3], a[\\b:\\b+3], 0\n.endm\n"
".macro Qmvva d,a,b\nv_mfma_f32_16x16x32_bf16 v[\\d:\\d+3], v[\\a:\\a+3], a[\\b:\\b+3], v[\\d:\\d+3]\n.endm\n.macro Qmava d,a,b\nv_mfma_f32_16x16x32_bf16 a[\\d:\\d+3], v[\\a:\\a+3], a[\\b:\\b+3], a[\\d:\\d+3]\n.endm\n.macro Qzvvv d,a,b\nv_mfma_f32_16x16x32_bf16 v[\\d:\\d+3], v[\\a:\\a+3], v[\\b:\\b+3], 0\n"
".endm\n.macro Qcaav d,a,b,c\nv_mfma_f32_16x16x32_bf16 a[\\d:\\d+3], a[\\a:\\a+3], v[\\b:\\b+3], a[\\c:\\c+3]\n.endm\n.macro Qcvav d,a,b,c\nv_mfma_f32_16x16x32_bf16 v[\\d:\\d+3], a[\\a:\\a+3], v[\\b:\\b+3], v[\\c:\\c+3]\n.endm\n.p2align 7\n"
"v_mbcnt_lo_u32_b32 v0, -1, 0\nv_mbcnt_hi_u32_b32 v0, -1, v0\nv_lshlrev_b32 v60, 4, v0\ns_add_u32 m0, %10, 160768\ns_getpc_b64 s[56:57]\n.Lpc0:\ns_and_b32 s56, s56, 0xffffff80\ns_and_b32 s57, s57, 0xffff\n"
"s_mov_b32 s58, .Lend-.Lpc0\ns_mov_b32 s59, 0x20000\ns_and_b32 s94, %20, 31\ns_lshl_b32 s94, s94, 2\ns_add_u32 s94, s94, %11\ns_lshl_b32 s94, s94, 10\nbuffer_load_dwordx4 v60, s[56:59], s94 offen lds\ns_mov_b32 s52, %6\n"
"s_and_b32 s53, %7, 0xffff\ns_mov_b32 s54, 5242880\ns_mov_b32 s55, 0x20000\ns_mov_b32 s48, %4\ns_and_b32 s49, %5, 0xffff\ns_mov_b32 s50, 286720\ns_mov_b32 s51, 0x20000\ns_mov_b32 s70, %11\n"
"s_and_b32 s92, s70, 1\ns_lshr_b32 s93, s70, 1\nv_lshrrev_b32 v2, 4, v0\ns_lshl_b32 s68, s92, 18\nv_and_b32 v4, 3, v0\nv_lshlrev_b32 v4, 4, v4\nv_and_b32 v1, 1, v2\nv_lshl_add_u32 v4, v1, 6, v4\n"
"v_lshrrev_b32 v1, 5, v0\nv_lshl_add_u32 v4, v1, 15, v4\nv_bfe_u32 v1, v0, 2, 2\nv_lshl_add_u32 v4, v1, 13, v4\nv_add_u32 v62, s68, v4\ns_lshl_b32 s87, %12, 8\ns_mul_i32 s62, s70, 4128\ns_add_u32 s62, s62, %10\n"
"s_add_u32 s62, s62, 33024\ns_add_u32 m0, s62, 0\ns_add_u32 s66, s87, 0\ns_nop 0\nbuffer_load_dwordx4 v62, s[52:55], s66 offen lds\ns_add_u32 s66, s87, 64504\ns_nop 0\nbuffer_load_dwordx4 v62, s[52:55], s66 offen offset:1032 lds\n"
"s_add_u32 s66, s87, 129008\ns_nop 0\nbuffer_load_dwordx4 v62, s[52:55], s66 offen offset:2064 lds\ns_add_u32 s66, s87, 193512\ns_nop 0\nbuffer_load_dwordx4 v62, s[52:55], s66 offen offset:3096 lds\ns_lshl_b32 s69, s92, 1\nv_lshrrev_b32 v3, 5, v0\n"
"v_and_b32 v4, 3, v0\nv_xor_b32 v4, s69, v4\nv_lshlrev_b32 v4, 4, v4\nv_lshl_add_u32 v4, v3, 6, v4\nv_bfe_u32 v1, v0, 2, 3\nv_mul_u32_u24 v64, 1280, v1\nv_add_u32 v64, v64, v4\ns_mul_i32 s86, s93, 143360\n"
"s_mul_i32 s68, s92, 10240\ns_add_u32 s86, s86, s68\ns_mul_i32 s63, s92, 14336\ns_mul_i32 s68, s93, 7168\ns_add_u32 s63, s63, s68\ns_add_u32 s63, s63, %10\ns_add_u32 s63, s63, 99072\ns_add_u32 m0, s63, 33024\n"
"s_add_u32 s97, s86, 0\ns_nop 0\nbuffer_load_dwordx4 v64, s[48:51], s97 offen lds\ns_add_u32 s97, s86, 19456\ns_nop 0\nbuffer_load_dwordx4 v64, s[48:51], s97 offen offset:1024 lds\ns_add_u32 s97, s86, 38912\ns_nop 0\n"
"buffer_load_dwordx4 v64, s[48:51], s97 offen offset:2048 lds\ns_add_u32 s97, s86, 58368\ns_nop 0\nbuffer_load_dwordx4 v64, s[48:51], s97 offen offset:3072 lds\ns_add_u32 m0, s63, 37120\ns_add_u32 s97, s86, 81920\ns_nop 0\nbuffer_load_dwordx4 v64, s[48:51], s97 offen lds\n"
"s_add_u32 s97, s86, 101376\ns_nop 0\nbuffer_load_dwordx4 v64, s[48:51], s97 offen offset:1024 lds\ns_add_u32 s97, s86, 120832\ns_nop 0\nbuffer_load_dwordx4 v64, s[48:51], s97 offen offset:2048 lds\ns_add_u32 m0, s62, 16512\ns_add_u32 s66, s87, 128\n"
"s_nop 0\nbuffer_load_dwordx4 v62, s[52:55], s66 offen lds\ns_mov_b32 s24, %8\ns_and_b32 s25, %9, 0xffff\ns_mov_b32 s26, 2295808\ns_mov_b32 s27, 0x20000\nv_and_b32 v5, 7, v0\nv_lshlrev_b32 v5, 7, v5\n"
"s_add_u32 s66, s87, 64632\ns_nop 0\nbuffer_load_dwordx4 v62, s[52:55], s66 offen offset:1032 lds\nv_add_u32 v76, 2293760, v5\ns_lshl_b32 s68, s70, 2\ns_add_u32 s68, s68, 4\nv_bfe_u32 v5, v0, 3, 1\nv_mad_u32_u24 v76, v5, s68, v76\n"
"buffer_load_dword v65, v76, s[24:27], 0 offen sc1\ns_add_u32 s66, s87, 129136\ns_nop 0\nbuffer_load_dwordx4 v62, s[52:55], s66 offen offset:2064 lds\ns_mov_b32 s40, %0\ns_and_b32 s41, %1, 0xffff\ns_mov_b32 s42, 2622464\ns_mov_b32 s43, 0x20000\n"
"s_mov_b32 s44, %2\ns_and_b32 s45, %3, 0xffff\ns_add_u32 s66, s87, 193640\ns_nop 0\nbuffer_load_dwordx4 v62, s[52:55], s66 offen offset:3096 lds\ns_mov_b32 s46, 2622464\ns_mov_b32 s47, 0x20000\ns_mov_b32 s56, %8\n"
"s_and_b32 s57, %9, 0xffff\ns_mov_b32 s58, 0\ns_mov_b32 s59, 0x20000\ns_mov_b64 exec, 0\ns_cmp_eq_u32 s70, 0\ns_cbranch_scc1 .Lpg0\ns_cmp_eq_u32 s70, 1\ns_cbranch_scc1 .Lpg1\n"
"s_cmp_eq_u32 s70, 2\ns_cbranch_scc1 .Lpg2\ns_branch .Lpg3\n.Ltr0:\ns_mov_b64 exec, -1\ns_mov_b32 s65, 0\ns_mov_b32 s60, %21\ns_add_u32 s90, s60, 260096\n"
"s_mov_b32 s67, 0\ns_mov_b32 s72, 0x3e0293ee\ns_lshl_b32 s68, s70, 8\ns_add_u32 m0, s63, 0\ns_add_u32 s97, s86, 128\ns_nop 0\nbuffer_load_dwordx4 v64, s[48:51], s97 offen lds\ns_lshl_b32 s69, s93, 5\n"
"s_or_b32 s88, s68, s69\nv_bfe_u32 v1, v0, 2, 2\nv_mul_u32_u24 v4, 0x8100, v1\nv_and_b32 v1, 3, v0\nv_lshl_or_b32 v4, v1, 4, v4\ns_add_u32 s97, s86, 19584\ns_nop 0\nbuffer_load_dwordx4 v64, s[48:51], s97 offen offset:1024 lds\n"
"v_and_b32 v1, 1, v2\nv_lshl_or_b32 v4, v1, 7, v4\nv_lshl_or_b32 v4, v3, 6, v4\nv_xor_b32 v4, s88, v4\nv_add_u32 v69, 1024, v4\nv_bfe_u32 v1, v0, 1, 2\ns_add_u32 s97, s86, 39040\ns_nop 0\n"
"buffer_load_dwordx4 v64, s[48:51], s97 offen offset:2048 lds\nv_mul_u32_u24 v4, 0x8100, v1\nv_and_b32 v1, 9, v0\nv_lshl_or_b32 v4, v1, 4, v4\nv_lshl_or_b32 v4, v2, 5, v4\nv_xor_b32 v4, s68, v4\nv_add_u32 v72, 1024, v4\ns_add_u32 s97, s86, 58496\n"
"s_nop 0\nbuffer_load_dwordx4 v64, s[48:51], s97 offen offset:3072 lds\ns_lshl_b32 s68, s70, 12\ns_add_u32 s61, s68, %10\ns_mul_i32 s68, s92, 8320\ns_lshl_b32 s69, s93, 12\ns_add_u32 s68, s68, s69\ns_add_u32 s68, s68, %10\n"
"s_add_u32 m0, s63, 4096\ns_add_u32 s97, s86, 82048\ns_nop 0\nbuffer_load_dwordx4 v64, s[48:51], s97 offen lds\ns_add_u32 s64, s68, 16384\ns_lshl_b32 s69, s93, 11\ns_add_u32 s69, s69, %10\nv_bfe_u32 v3, v0, 3, 1\n"
"v_lshlrev_b32 v4, 1, v3\nv_xor_b32 v4, v4, v2\ns_add_u32 s97, s86, 101504\ns_nop 0\nbuffer_load_dwordx4 v64, s[48:51], s97 offen offset:1024 lds\nv_and_b32 v1, 7, v0\nv_lshl_add_u32 v4, v1, 2, v4\nv_lshlrev_b32 v4, 4, v4\n"
"v_bfe_u32 v1, v0, 2, 2\nv_and_b32 v5, 255, v4\nv_lshl_or_b32 v1, v1, 12, v5\ns_add_u32 s97, s86, 120960\ns_nop 0\nbuffer_load_dwordx4 v64, s[48:51], s97 offen offset:2048 lds\nv_add_u32 v68, s69, v1\ns_mul_i32 s68, s92, 7168\n"
"s_add_u32 s68, s68, %10\ns_add_u32 s68, s68, 99072\nv_mul_u32_u24 v1, 14336, v3\nv_add3_u32 v63, v1, v4, s68\ns_add_u32 m0, s62, 33024\ns_add_u32 s66, s87, 524288\ns_nop 0\nbuffer_load_dwordx4 v62, s[52:55], s66 offen lds\n"
"v_lshrrev_b32 v3, 5, v0\nv_and_b32 v4, 31, v0\nv_lshlrev_b32 v4, 3, v4\nv_and_b32 v1, 1, v2\nv_lshl_add_u32 v4, v1, 13, v4\nv_lshl_add_u32 v4, v3, 12, v4\ns_add_u32 s66, s87, 588792\ns_nop 0\n"
"buffer_load_dwordx4 v62, s[52:55], s66 offen offset:1032 lds\nv_add_u32 v4, 16384, v4\nv_add_u32 v70, s69, v4\nv_add_u32 v71, 66048, v70\ns_mul_i32 s68, s93, 8256\ns_add_u32 s68, s68, %10\ns_add_u32 s68, s68, 33024\ns_add_u32 s66, s87, 653296\n"
"s_nop 0\nbuffer_load_dwordx4 v62, s[52:55], s66 offen offset:2064 lds\nv_mul_u32_u24 v3, 1032, v2\nv_and_b32 v4, 15, v0\nv_lshlrev_b32 v4, 4, v4\nv_add3_u32 v61, v3, v4, s68\ns_lshl_b32 s68, s70, 2\ns_add_u32 s69, %10, 131968\n"
"s_add_u32 s66, s87, 717800\ns_nop 0\nbuffer_load_dwordx4 v62, s[52:55], s66 offen offset:3096 lds\ns_add_u32 s68, s68, s69\nv_mov_b32 v73, s68\nv_and_b32 v3, 3, v0\nv_lshl_add_u32 v74, v3, 2, s69\nv_mov_b32 v75, 0\n"
"ds_write_b32 v73, v75\ns_mov_b64 exec, 0\ns_cmp_eq_u32 s70, 0\ns_cbranch_scc1 .Lpg4\ns_cmp_eq_u32 s70, 1\ns_cbranch_scc1 .Lpg5\ns_cmp_eq_u32 s70, 2\ns_cbranch_scc1 .Lpg6\n"
"s_branch .Lpg7\n.Ltr9:\ns_mov_b64 exec, -1\ns_waitcnt lgkmcnt(0)\ns_mul_i32 s91, s70, 7168\ns_add_u32 s91, s91, %10\ns_add_u32 s91, s91, 98304\ns_lshl_b32 s96, s70, 11\n"
"s_add_u32 s96, s96, %10\ns_add_u32 m0, s62, 49536\ns_add_u32 s66, s87, 524416\ns_nop 0\nbuffer_load_dwordx4 v62, s[52:55], s66 offen lds\nv_and_b32 v3, 1, v0\nv_mul_u32_u24 v3, 0xaaaa, v3\nv_or_b32 v77, 0x4444, v3\n"
"s_and_b32 s68, %19, 4\ns_lshl_b32 s28, 15, s68\ns_mov_b32 s29, 0\ns_add_u32 s66, s87, 588920\ns_nop 0\nbuffer_load_dwordx4 v62, s[52:55], s66 offen offset:1032 lds\ns_mov_b32 s71, 0\ns_and_b32 s68, %19, 3\n"
"s_lshl_b32 s99, 3072, s68\ns_or_b32 s99, s99, 0x302\ns_mov_b32 s94, 0x7ffff000\ns_mul_i32 s68, %20, 40960\ns_add_u32 s66, s87, 653424\ns_nop 0\nbuffer_load_dwordx4 v62, s[52:55], s66 offen offset:2064 lds\ns_mul_i32 s69, s93, 20480\n"
"s_add_u32 s68, s68, s69\ns_lshl_b32 s69, s92, 10\ns_add_u32 s68, s68, s69\ns_add_u32 s68, s68, 2048\ns_add_u32 s94, %6, s68\ns_addc_u32 s95, %7, 0\ns_add_u32 s66, s87, 717928\ns_nop 0\n"
"buffer_load_dwordx4 v62, s[52:55], s66 offen offset:3096 lds\ns_and_b32 s95, s95, 0xffff\ns_mov_b32 s75, 17408\ns_mov_b64 exec, 0\ns_cmp_eq_u32 s70, 0\ns_cbranch_scc1 .Lpg8\ns_cmp_eq_u32 s70, 1\ns_cbranch_scc1 .Lpg9\n"
"s_cmp_eq_u32 s70, 2\ns_cbranch_scc1 .Lpg10\ns_branch .Lpg11\n.Ltr10:\ns_mov_b64 exec, -1\n.Lround:\ns_cmp_lg_u32 s67, 0\ns_cselect_b64 vcc, -1, 0\n"
"s_waitcnt vmcnt(15)\ns_barrier\nQta 112,61,0\nQta 114,61,512\nQdv 24,63,33024\nQta 116,61,4128\nQta 118,61,4640\nQdv 28,63,34048\n"
"Qta 120,61,8\nQta 122,61,520\nQdv 32,63,35072\nQta 124,61,4136\nQta 126,61,4648\nQdv 36,63,36096\nQta 128,61,256\nQta 130,61,768\n"
"Qdv 40,63,37120\nQta 132,61,4384\nQta 134,61,4896\nQdv 44,63,38144\nQta 136,61,264\nQta 138,61,776\nQdv 48,63,39168\nQta 140,61,4392\n"
"Qta 142,61,4904\nQda 176,63,33536\nQda 180,63,34560\nQda 184,63,35584\nQda 188,63,36608\nQda 192,63,37632\nQda 196,63,38656\nQda 200,63,39680\n"
"s_waitcnt lgkmcnt(6)\nQzvav 80,112,24\nQmvaa 80,116,176\ns_add_u32 s68, s71, 0\nQzvav 84,120,24\ns_cbranch_vccz .Lst27\nbuffer_store_dwordx4 a[0:3], v60, s[56:59], s68 offen offset:0 sc1\n.Lst27:\n"
"Qmvaa 84,124,176\nQzvav 88,128,24\ns_add_u32 s68, s71, 14336\nQmvaa 88,132,176\ns_cbranch_vccz .Lst26\nbuffer_store_dwordx4 a[4:7], v60, s[56:59], s68 offen offset:0 sc1\n.Lst26:\nQzvav 92,136,24\n"
"Qmvaa 92,140,176\ns_waitcnt lgkmcnt(0)\nQzvav 124,136,28\ns_waitcnt vmcnt(15)\ns_barrier\nQmvaa 124,140,180\nQta 144,61,16512\ns_add_u32 m0, s62, 0\n"
"s_add_u32 s69, s87, 1048576\nQzvav 120,128,28\nbuffer_load_dwordx4 v62, s[52:55], s69 offen lds\nQmvaa 120,132,180\nQta 146,61,17024\nQzvav 116,120,28\nQmvaa 116,124,180\nQta 148,61,20640\n"
"s_add_u32 s69, s87, 1113080\nQzvav 112,112,28\nbuffer_load_dwordx4 v62, s[52:55], s69 offen offset:1032 lds\nQmvaa 112,116,180\nQta 150,61,21152\nQzvav 144,112,32\nQmvaa 144,116,184\nQta 152,61,16520\n"
"s_add_u32 s69, s87, 1177584\nQzvav 148,120,32\nbuffer_load_dwordx4 v62, s[52:55], s69 offen offset:2064 lds\nQmvaa 148,124,184\nQta 154,61,17032\nQzvav 152,128,32\nQmvaa 152,132,184\nQta 156,61,20648\n"
"s_add_u32 s69, s87, 1242088\nQzvav 156,136,32\nbuffer_load_dwordx4 v62, s[52:55], s69 offen offset:3096 lds\nQmvaa 156,140,184\nQta 158,61,21160\nQzvav 188,136,36\nQmvaa 188,140,188\nQta 160,61,16768\n"
"s_add_u32 m0, s63, 33024\ns_add_u32 s88, s86, 256\nQzvav 184,128,36\nbuffer_load_dwordx4 v64, s[48:51], s88 offen lds\nQmvaa 184,132,188\nQta 162,61,17280\nQzvav 180,120,36\nQmvaa 180,124,188\n"
"Qta 164,61,20896\ns_add_u32 s88, s86, 19712\nQzvav 176,112,36\nbuffer_load_dwordx4 v64, s[48:51], s88 offen offset:1024 lds\nQmvaa 176,116,188\nQta 166,61,21408\nQzvav 208,112,40\nQmvaa 208,116,192\n"
"Qta 168,61,16776\ns_add_u32 s88, s86, 39168\nQzvav 212,120,40\nbuffer_load_dwordx4 v64, s[48:51], s88 offen offset:2048 lds\nQmvaa 212,124,192\nQta 170,61,17288\nQzvav 216,128,40\nQmvaa 216,132,192\n"
"Qta 172,61,20904\ns_add_u32 s88, s86, 58624\nQzvav 220,136,40\nbuffer_load_dwordx4 v64, s[48:51], s88 offen offset:3072 lds\nQmvaa 220,140,192\nQta 174,61,21416\nQzvav 252,136,44\nQmvaa 252,140,196\n"
"s_add_u32 m0, s63, 37120\ns_add_u32 s88, s86, 82176\nQzvav 248,128,44\nbuffer_load_dwordx4 v64, s[48:51], s88 offen lds\nQmvaa 248,132,196\nQzvav 244,120,44\nQmvaa 244,124,196\ns_add_u32 s88, s86, 101632\n"
"Qzvav 240,112,44\nbuffer_load_dwordx4 v64, s[48:51], s88 offen offset:1024 lds\nQmvaa 240,116,196\nQzaav 224,112,48\nQmaaa 224,116,200\ns_add_u32 s88, s86, 121088\nQzaav 228,120,48\nbuffer_load_dwordx4 v64, s[48:51], s88 offen offset:2048 lds\n"
"Qmaaa 228,124,200\nQzaav 232,128,48\nQmaaa 232,132,200\nQzaav 236,136,48\nQmaaa 236,140,200\ns_waitcnt lgkmcnt(0)\nQzvav 96,144,24\nQmvaa 96,148,176\n"
"s_add_u32 s68, s71, 28672\nQzvav 100,152,24\ns_cbranch_vccz .Lst25\nbuffer_store_dwordx4 a[8:11], v60, s[56:59], s68 offen offset:0 sc1\n.Lst25:\nQmvaa 100,156,176\nQzvav 104,160,24\ns_add_u32 s68, s71, 43008\n"
"Qmvaa 104,164,176\ns_cbranch_vccz .Lst24\nbuffer_store_dwordx4 a[12:15], v60, s[56:59], s68 offen offset:0 sc1\n.Lst24:\nQzvav 108,168,24\nQmvaa 108,172,176\ns_waitcnt lgkmcnt(0)\nQzvav 140,168,28\n"
"s_waitcnt vmcnt(15)\ns_barrier\nQmvaa 140,172,180\nQdv 24,63,0\ns_add_u32 m0, s62, 16512\ns_add_u32 s69, s87, 1048704\nQzvav 136,160,28\nbuffer_load_dwordx4 v62, s[52:55], s69 offen lds\n"
"Qmvaa 136,164,180\nQda 176,63,512\nQzvav 132,152,28\nQta 112,61,33024\nQmvaa 132,156,180\ns_add_u32 s69, s87, 1113208\nQzvav 128,144,28\nbuffer_load_dwordx4 v62, s[52:55], s69 offen offset:1032 lds\n"
"Qmvaa 128,148,180\nQdv 28,63,1024\nQzvav 160,144,32\nQda 180,63,1536\nQmvaa 160,148,184\nQta 114,61,33536\ns_add_u32 s69, s87, 1177712\nQzvav 164,152,32\n"
"buffer_load_dwordx4 v62, s[52:55], s69 offen offset:2064 lds\nQmvaa 164,156,184\nQta 116,61,37152\nQzvav 168,160,32\nQmvaa 168,164,184\nQta 118,61,37664\ns_add_u32 s69, s87, 1242216\nQzvav 172,168,32\n"
"buffer_load_dwordx4 v62, s[52:55], s69 offen offset:3096 lds\nQmvaa 172,172,184\nQdv 32,63,2048\nQzvav 204,168,36\nQda 184,63,2560\nQmvaa 204,172,188\nQta 120,61,33032\ns_add_u32 m0, s61, 0\n"
"Qzvav 200,160,36\ns_cbranch_vccnz .Ld0q0\nbuffer_load_dwordx4 v69, s[40:43], s60 offen nt lds\n.Ld0q0:\nQmvaa 200,164,188\nQta 122,61,33544\nQzvav 196,152,36\nQmvaa 196,156,188\n"
"Qta 124,61,37160\nQzvav 192,144,36\nQdv 36,63,3072\nQmvaa 192,148,188\nQda 188,63,3584\nQzvav 224,144,40\nQta 126,61,37672\nQmvaa 224,148,192\n"
"Qzvav 228,152,40\nQta 128,61,33280\nQmvaa 228,156,192\nQzvav 232,160,40\nQta 130,61,33792\nQmvaa 232,164,192\nQzvav 236,168,40\nQdv 40,63,4096\n"
"Qmvaa 236,172,192\nQda 192,63,4608\nQzaav 220,168,44\nQta 132,61,37408\nQmaaa 220,172,196\nQzaav 216,160,44\nQta 134,61,37920\nQmaaa 216,164,196\n"
"Qzaav 212,152,44\nQta 136,61,33288\nQmaaa 212,156,196\nQzaav 208,144,44\nQdv 44,63,5120\nQmaaa 208,148,196\nQda 196,63,5632\nQzaav 240,144,48\n"
"Qta 138,61,33800\nQmaaa 240,148,200\nQzaav 244,152,48\nQta 140,61,37416\nQmaaa 244,156,200\nQzaav 248,160,48\nQta 142,61,37928\nQmaaa 248,164,200\n"
".Lpg0:\ns_cbranch_execz .Ltr0\nQzaav 252,168,48\nQdv 48,63,6144\nQmaaa 252,172,200\nQda 200,63,6656\ns_waitcnt lgkmcnt(2)\nQmvav 80,112,24\n"
"Qmvaa 80,116,176\ns_add_u32 s68, s71, 0\nQmvav 84,120,24\ns_cbranch_vccz .Lst23\nbuffer_store_dwordx4 a[16:19], v60, s[56:59], s68 offen offset:1024 sc1\n.Lst23:\nQmvaa 84,124,176\nQmvav 88,128,24\n"
"s_add_u32 s68, s71, 14336\nQmvaa 88,132,176\ns_cbranch_vccz .Lst22\nbuffer_store_dwordx4 a[20:23], v60, s[56:59], s68 offen offset:1024 sc1\n.Lst22:\nQmvav 92,136,24\nQmvaa 92,140,176\ns_waitcnt lgkmcnt(0)\n"
"Qmvav 124,136,28\ns_waitcnt vmcnt(16)\ns_barrier\nQmvaa 124,140,180\nQta 144,61,49536\ns_add_u32 m0, s62, 33024\ns_add_u32 s69, s87, 1572864\nQmvav 120,128,28\n"
"buffer_load_dwordx4 v62, s[52:55], s69 offen lds\nQmvaa 120,132,180\nQta 146,61,50048\nQmvav 116,120,28\nQmvaa 116,124,180\nQta 148,61,53664\ns_add_u32 s69, s87, 1637368\nQmvav 112,112,28\n"
"buffer_load_dwordx4 v62, s[52:55], s69 offen offset:1032 lds\nQmvaa 112,116,180\nQta 150,61,54176\nQmvav 144,112,32\nQmvaa 144,116,184\nQta 152,61,49544\ns_add_u32 s69, s87, 1701872\nQmvav 148,120,32\n"
"buffer_load_dwordx4 v62, s[52:55], s69 offen offset:2064 lds\nQmvaa 148,124,184\nQta 154,61,50056\nQmvav 152,128,32\nQmvaa 152,132,184\nQta 156,61,53672\ns_add_u32 s69, s87, 1766376\nQmvav 156,136,32\n"
"buffer_load_dwordx4 v62, s[52:55], s69 offen offset:3096 lds\nQmvaa 156,140,184\nQta 158,61,54184\nQmvav 188,136,36\nQmvaa 188,140,188\nQta 160,61,49792\ns_add_u32 m0, s63, 0\ns_add_u32 s88, s86, 384\n"
"Qmvav 184,128,36\nbuffer_load_dwordx4 v64, s[48:51], s88 offen lds\nQmvaa 184,132,188\nQta 162,61,50304\nQmvav 180,120,36\nQmvaa 180,124,188\nQta 164,61,53920\ns_add_u32 s88, s86, 19840\n"
"Qmvav 176,112,36\nbuffer_load_dwordx4 v64, s[48:51], s88 offen offset:1024 lds\nQmvaa 176,116,188\nQta 166,61,54432\nQmvav 208,112,40\nQmvaa 208,116,192\nQta 168,61,49800\ns_add_u32 s88, s86, 39296\n"
"Qmvav 212,120,40\nbuffer_load_dwordx4 v64, s[48:51], s88 offen offset:2048 lds\nQmvaa 212,124,192\nQta 170,61,50312\nQmvav 216,128,40\nQmvaa 216,132,192\nQta 172,61,53928\ns_add_u32 s88, s86, 58752\n"
"Qmvav 220,136,40\nbuffer_load_dwordx4 v64, s[48:51], s88 offen offset:3072 lds\nQmvaa 220,140,192\nQta 174,61,54440\nQmvav 252,136,44\nQmvaa 252,140,196\ns_add_u32 m0, s63, 4096\ns_add_u32 s88, s86, 82304\n"
"Qmvav 248,128,44\nbuffer_load_dwordx4 v64, s[48:51], s88 offen lds\nQmvaa 248,132,196\nQmvav 244,120,44\nQmvaa 244,124,196\ns_add_u32 s88, s86, 101760\nQmvav 240,112,44\nbuffer_load_dwordx4 v64, s[48:51], s88 offen offset:1024 lds\n"
"Qmvaa 240,116,196\nQmaav 224,112,48\nQmaaa 224,116,200\ns_add_u32 s88, s86, 121216\nQmaav 228,120,48\nbuffer_load_dwordx4 v64, s[48:51], s88 offen offset:2048 lds\nQmaaa 228,124,200\nQmaav 232,128,48\n"
"Qmaaa 232,132,200\nQmaav 236,136,48\nQmaaa 236,140,200\ns_waitcnt lgkmcnt(0)\nQmvav 96,144,24\nQmvaa 96,148,176\ns_add_u32 s68, s71, 28672\nQmvav 100,152,24\n"
"s_cbranch_vccz .Lst21\nbuffer_store_dwordx4 a[24:27], v60, s[56:59], s68 offen offset:1024 sc1\n.Lst21:\nQmvaa 100,156,176\nQmvav 104,160,24\ns_add_u32 s68, s71, 43008\nQmvaa 104,164,176\ns_cbranch_vccz .Lst20\n"
"buffer_store_dwordx4 a[28:31], v60, s[56:59], s68 offen offset:1024 sc1\n.Lst20:\nQmvav 108,168,24\nQmvaa 108,172,176\ns_waitcnt lgkmcnt(0)\nQmvav 140,168,28\ns_waitcnt vmcnt(16)\ns_barrier\n"
"Qmvaa 140,172,180\nQdv 24,63,33024\ns_add_u32 m0, s62, 49536\ns_add_u32 s69, s87, 1572992\nQmvav 136,160,28\nbuffer_load_dwordx4 v62, s[52:55], s69 offen lds\nQmvaa 136,164,180\nQda 176,63,33536\n"
"Qmvav 132,152,28\nQta 112,61,0\nQmvaa 132,156,180\ns_add_u32 s69, s87, 1637496\nQmvav 128,144,28\nbuffer_load_dwordx4 v62, s[52:55], s69 offen offset:1032 lds\nQmvaa 128,148,180\nQdv 28,63,34048\n"
"Qmvav 160,144,32\nQda 180,63,34560\nQmvaa 160,148,184\nQta 114,61,512\ns_add_u32 s69, s87, 1702000\nQmvav 164,152,32\nbuffer_load_dwordx4 v62, s[52:55], s69 offen offset:2064 lds\nQmvaa 164,156,184\n"
"Qta 116,61,4128\nQmvav 168,160,32\nQmvaa 168,164,184\nQta 118,61,4640\ns_add_u32 s69, s87, 1766504\nQmvav 172,168,32\nbuffer_load_dwordx4 v62, s[52:55], s69 offen offset:3096 lds\nQmvaa 172,172,184\n"
"Qdv 32,63,35072\nQmvav 204,168,36\nQda 184,63,35584\nQmvaa 204,172,188\nQta 120,61,8\ns_add_u32 m0, s61, 0\nQmvav 200,160,36\ns_cbranch_vccnz .Ld0q1\n"
"buffer_load_dwordx4 v69, s[40:43], s60 offen offset:1024 nt lds\n.Ld0q1:\nQmvaa 200,164,188\nQta 122,61,520\nQmvav 196,152,36\nQmvaa 196,156,188\nQta 124,61,4136\nQmvav 192,144,36\n"
"Qdv 36,63,36096\nQmvaa 192,148,188\nQda 188,63,36608\nQmvav 224,144,40\nQta 126,61,4648\nQmvaa 224,148,192\nQmvav 228,152,40\nQta 128,61,256\n"
"Qmvaa 228,156,192\nQmvav 232,160,40\nQta 130,61,768\nQmvaa 232,164,192\nQmvav 236,168,40\nQdv 40,63,37120\nQmvaa 236,172,192\nQda 192,63,37632\n"
"Qmaav 220,168,44\nQta 132,61,4384\nQmaaa 220,172,196\nQmaav 216,160,44\nQta 134,61,4896\nQmaaa 216,164,196\nQmaav 212,152,44\nQta 136,61,264\n"
"Qmaaa 212,156,196\nQmaav 208,144,44\nQdv 44,63,38144\nQmaaa 208,148,196\nQda 196,63,38656\nQmaav 240,144,48\nQta 138,61,776\nQmaaa 240,148,200\n"
"Qmaav 244,152,48\nQta 140,61,4392\nQmaaa 244,156,200\nQmaav 248,160,48\nQta 142,61,4904\nQmaaa 248,164,200\nQmaav 252,168,48\nQdv 48,63,39168\n"
"Qmaaa 252,172,200\nQda 200,63,39680\ns_waitcnt lgkmcnt(2)\nQmvav 80,112,24\nQmvaa 80,116,176\ns_add_u32 s68, s71, 0\nQmvav 84,120,24\ns_cbranch_vccz .Lst19\n"
"buffer_store_dwordx4 a[32:35], v60, s[56:59], s68 offen offset:2048 sc1\n.Lst19:\nQmvaa 84,124,176\nQmvav 88,128,24\ns_add_u32 s68, s71, 14336\nQmvaa 88,132,176\ns_cbranch_vccz .Lst18\nbuffer_store_dwordx4 a[36:39], v60, s[56:59], s68 offen offset:2048 sc1\n"
".Lst18:\nQmvav 92,136,24\nQmvaa 92,140,176\ns_waitcnt lgkmcnt(0)\nQmvav 124,136,28\ns_waitcnt vmcnt(17)\ns_barrier\nQmvaa 124,140,180\n"
"Qta 144,61,16512\ns_add_u32 m0, s62, 0\ns_add_u32 s69, s87, 2097152\nQmvav 120,128,28\nbuffer_load_dwordx4 v62, s[52:55], s69 offen lds\nQmvaa 120,132,180\nQta 146,61,17024\nQmvav 116,120,28\n"
"Qmvaa 116,124,180\nQta 148,61,20640\ns_add_u32 s69, s87, 2161656\nQmvav 112,112,28\nbuffer_load_dwordx4 v62, s[52:55], s69 offen offset:1032 lds\nQmvaa 112,116,180\nQta 150,61,21152\nQmvav 144,112,32\n"
"Qmvaa 144,116,184\nQta 152,61,16520\ns_add_u32 s69, s87, 2226160\nQmvav 148,120,32\nbuffer_load_dwordx4 v62, s[52:55], s69 offen offset:2064 lds\nQmvaa 148,124,184\nQta 154,61,17032\nQmvav 152,128,32\n"
"Qmvaa 152,132,184\nQta 156,61,20648\ns_add_u32 s69, s87, 2290664\nQmvav 156,136,32\nbuffer_load_dwordx4 v62, s[52:55], s69 offen offset:3096 lds\nQmvaa 156,140,184\nQta 158,61,21160\nQmvav 188,136,36\n"
"Qmvaa 188,140,188\nQta 160,61,16768\ns_add_u32 m0, s63, 33024\ns_add_u32 s88, s86, 512\nQmvav 184,128,36\nbuffer_load_dwordx4 v64, s[48:51], s88 offen lds\nQmvaa 184,132,188\nQta 162,61,17280\n"
"Qmvav 180,120,36\nQmvaa 180,124,188\nQta 164,61,20896\ns_add_u32 s88, s86, 19968\nQmvav 176,112,36\nbuffer_load_dwordx4 v64, s[48:51], s88 offen offset:1024 lds\nQmvaa 176,116,188\nQta 166,61,21408\n"
"Qmvav 208,112,40\nQmvaa 208,116,192\nQta 168,61,16776\ns_add_u32 s88, s86, 39424\nQmvav 212,120,40\nbuffer_load_dwordx4 v64, s[48:51], s88 offen offset:2048 lds\nQmvaa 212,124,192\nQta 170,61,17288\n"
"Qmvav 216,128,40\nQmvaa 216,132,192\nQta 172,61,20904\ns_add_u32 s88, s86, 58880\nQmvav 220,136,40\nbuffer_load_dwordx4 v64, s[48:51], s88 offen offset:3072 lds\nQmvaa 220,140,192\nQta 174,61,21416\n"
"Qmvav 252,136,44\nQmvaa 252,140,196\ns_add_u32 m0, s63, 37120\ns_add_u32 s88, s86, 82432\nQmvav 248,128,44\nbuffer_load_dwordx4 v64, s[48:51], s88 offen lds\nQmvaa 248,132,196\nQmvav 244,120,44\n"
"Qmvaa 244,124,196\ns_add_u32 s88, s86, 101888\nQmvav 240,112,44\nbuffer_load_dwordx4 v64, s[48:51], s88 offen offset:1024 lds\nQmvaa 240,116,196\nQmaav 224,112,48\nQmaaa 224,116,200\ns_add_u32 s88, s86, 121344\n"
"Qmaav 228,120,48\nbuffer_load_dwordx4 v64, s[48:51], s88 offen offset:2048 lds\nQmaaa 228,124,200\nQmaav 232,128,48\nQmaaa 232,132,200\nQmaav 236,136,48\nQmaaa 236,140,200\ns_waitcnt lgkmcnt(0)\n"
"Qmvav 96,144,24\nQmvaa 96,148,176\ns_add_u32 s68, s71, 28672\nQmvav 100,152,24\ns_cbranch_vccz .Lst17\nbuffer_store_dwordx4 a[40:43], v60, s[56:59], s68 offen offset:2048 sc1\n.Lst17:\nQmvaa 100,156,176\n"
"Qmvav 104,160,24\ns_add_u32 s68, s71, 43008\nQmvaa 104,164,176\ns_cbranch_vccz .Lst16\nbuffer_store_dwordx4 a[44:47], v60, s[56:59], s68 offen offset:2048 sc1\n.Lst16:\nQmvav 108,168,24\nQmvaa 108,172,176\n"
"s_waitcnt lgkmcnt(0)\nQmvav 140,168,28\ns_waitcnt vmcnt(16)\ns_barrier\nQmvaa 140,172,180\nQdv 24,63,0\ns_add_u32 m0, s62, 16512\ns_add_u32 s69, s87, 2097280\n"
"Qmvav 136,160,28\nbuffer_load_dwordx4 v62, s[52:55], s69 offen lds\nQmvaa 136,164,180\nQda 176,63,512\nQmvav 132,152,28\nQta 112,61,33024\nQmvaa 132,156,180\ns_add_u32 s69, s87, 2161784\n"
"Qmvav 128,144,28\nbuffer_load_dwordx4 v62, s[52:55], s69 offen offset:1032 lds\nQmvaa 128,148,180\nQdv 28,63,1024\nQmvav 160,144,32\nQda 180,63,1536\nQmvaa 160,148,184\nQta 114,61,33536\n"
"s_add_u32 s69, s87, 2226288\nQmvav 164,152,32\nbuffer_load_dwordx4 v62, s[52:55], s69 offen offset:2064 lds\nQmvaa 164,156,184\nQta 116,61,37152\nQmvav 168,160,32\nQmvaa 168,164,184\nQta 118,61,37664\n"
"s_add_u32 s69, s87, 2290792\nQmvav 172,168,32\nbuffer_load_dwordx4 v62, s[52:55], s69 offen offset:3096 lds\nQmvaa 172,172,184\nQdv 32,63,2048\nQmvav 204,168,36\nQda 184,63,2560\nQmvaa 204,172,188\n"
"Qta 120,61,33032\ns_add_u32 m0, s61, 0\nQmvav 200,160,36\ns_cbranch_vccnz .Ld0q2\nbuffer_load_dwordx4 v69, s[40:43], s90 offen offset:2048 nt lds\n.Ld0q2:\nQmvaa 200,164,188\nQta 122,61,33544\n"
"Qmvav 196,152,36\nQmvaa 196,156,188\nQta 124,61,37160\nQmvav 192,144,36\nQdv 36,63,3072\nQmvaa 192,148,188\nQda 188,63,3584\nQmvav 224,144,40\n"
"Qta 126,61,37672\nQmvaa 224,148,192\nQmvav 228,152,40\nQta 128,61,33280\nQmvaa 228,156,192\nQmvav 232,160,40\nQta 130,61,33792\nQmvaa 232,164,192\n"
"Qmvav 236,168,40\nQdv 40,63,4096\nQmvaa 236,172,192\nQda 192,63,4608\nQmaav 220,168,44\nQta 132,61,37408\nQmaaa 220,172,196\nQmaav 216,160,44\n"
"Qta 134,61,37920\nQmaaa 216,164,196\nQmaav 212,152,44\nQta 136,61,33288\nQmaaa 212,156,196\nQmaav 208,144,44\nQdv 44,63,5120\nQmaaa 208,148,196\n"
"Qda 196,63,5632\nQmaav 240,144,48\nQta 138,61,33800\nQmaaa 240,148,200\nQmaav 244,152,48\nQta 140,61,37416\nQmaaa 244,156,200\nQmaav 248,160,48\n"
"Qta 142,61,37928\nQmaaa 248,164,200\nQmaav 252,168,48\nQdv 48,63,6144\nQmaaa 252,172,200\nQda 200,63,6656\ns_waitcnt lgkmcnt(2)\nQmvav 80,112,24\n"
"Qmvaa 80,116,176\ns_add_u32 s68, s71, 0\nQmvav 84,120,24\ns_cbranch_vccz .Lst15\nbuffer_store_dwordx4 a[48:51], v60, s[56:59], s68 offen offset:3072 sc1\n.Lst15:\nQmvaa 84,124,176\nQmvav 88,128,24\n"
"s_add_u32 s68, s71, 14336\nQmvaa 88,132,176\ns_cbranch_vccz .Lst14\nbuffer_store_dwordx4 a[52:55], v60, s[56:59], s68 offen offset:3072 sc1\n.Lst14:\nQmvav 92,136,24\nQmvaa 92,140,176\ns_waitcnt lgkmcnt(0)\n"
"Qmvav 124,136,28\ns_waitcnt vmcnt(17)\ns_barrier\nQmvaa 124,140,180\nQta 144,61,49536\ns_add_u32 m0, s62, 33024\ns_add_u32 s69, s87, 2621440\nQmvav 120,128,28\n"
"buffer_load_dwordx4 v62, s[52:55], s69 offen lds\nQmvaa 120,132,180\nQta 146,61,50048\nQmvav 116,120,28\nQmvaa 116,124,180\nQta 148,61,53664\ns_add_u32 s69, s87, 2685944\nQmvav 112,112,28\n"
"buffer_load_dwordx4 v62, s[52:55], s69 offen offset:1032 lds\nQmvaa 112,116,180\nQta 150,61,54176\nQmvav 144,112,32\nQmvaa 144,116,184\nQta 152,61,49544\ns_add_u32 s69, s87, 2750448\nQmvav 148,120,32\n"
"buffer_load_dwordx4 v62, s[52:55], s69 offen offset:2064 lds\nQmvaa 148,124,184\nQta 154,61,50056\nQmvav 152,128,32\nQmvaa 152,132,184\nQta 156,61,53672\ns_add_u32 s69, s87, 2814952\nQmvav 156,136,32\n"
"buffer_load_dwordx4 v62, s[52:55], s69 offen offset:3096 lds\nQmvaa 156,140,184\nQta 158,61,54184\nQmvav 188,136,36\nQmvaa 188,140,188\nQta 160,61,49792\ns_add_u32 m0, s63, 0\ns_add_u32 s88, s86, 640\n"
"Qmvav 184,128,36\nbuffer_load_dwordx4 v64, s[48:51], s88 offen lds\nQmvaa 184,132,188\nQta 162,61,50304\nQmvav 180,120,36\nQmvaa 180,124,188\nQta 164,61,53920\ns_add_u32 s88, s86, 20096\n"
"Qmvav 176,112,36\nbuffer_load_dwordx4 v64, s[48:51], s88 offen offset:1024 lds\nQmvaa 176,116,188\nQta 166,61,54432\nQmvav 208,112,40\nQmvaa 208,116,192\nQta 168,61,49800\ns_add_u32 s88, s86, 39552\n"
"Qmvav 212,120,40\nbuffer_load_dwordx4 v64, s[48:51], s88 offen offset:2048 lds\nQmvaa 212,124,192\nQta 170,61,50312\nQmvav 216,128,40\nQmvaa 216,132,192\nQta 172,61,53928\ns_add_u32 s88, s86, 59008\n"
"Qmvav 220,136,40\nbuffer_load_dwordx4 v64, s[48:51], s88 offen offset:3072 lds\nQmvaa 220,140,192\nQta 174,61,54440\nQmvav 252,136,44\nQmvaa 252,140,196\ns_add_u32 m0, s63, 4096\ns_add_u32 s88, s86, 82560\n"
"Qmvav 248,128,44\nbuffer_load_dwordx4 v64, s[48:51], s88 offen lds\nQmvaa 248,132,196\nQmvav 244,120,44\nQmvaa 244,124,196\ns_add_u32 s88, s86, 102016\nQmvav 240,112,44\nbuffer_load_dwordx4 v64, s[48:51], s88 offen offset:1024 lds\n"
"Qmvaa 240,116,196\n.Lpg1:\ns_cbranch_execz .Ltr0\nQmaav 224,112,48\nQmaaa 224,116,200\ns_add_u32 s88, s86, 121472\nQmaav 228,120,48\nbuffer_load_dwordx4 v64, s[48:51], s88 offen offset:2048 lds\n"
"Qmaaa 228,124,200\nQmaav 232,128,48\nQmaaa 232,132,200\nQmaav 236,136,48\nQmaaa 236,140,200\ns_waitcnt lgkmcnt(0)\nQmvav 96,144,24\nQmvaa 96,148,176\n"
"s_add_u32 s68, s71, 28672\nQmvav 100,152,24\ns_cbranch_vccz .Lst13\nbuffer_store_dwordx4 a[56:59], v60, s[56:59], s68 offen offset:3072 sc1\n.Lst13:\nQmvaa 100,156,176\nQmvav 104,160,24\ns_add_u32 s68, s71, 43008\n"
"Qmvaa 104,164,176\ns_cbranch_vccz .Lst12\nbuffer_store_dwordx4 a[60:63], v60, s[56:59], s68 offen offset:3072 sc1\n.Lst12:\nQmvav 108,168,24\nQmvaa 108,172,176\ns_waitcnt lgkmcnt(0)\nQmvav 140,168,28\n"
"s_waitcnt vmcnt(16)\ns_barrier\nQmvaa 140,172,180\nQdv 24,63,33024\ns_add_u32 m0, s62, 49536\ns_add_u32 s69, s87, 2621568\nQmvav 136,160,28\nbuffer_load_dwordx4 v62, s[52:55], s69 offen lds\n"
"Qmvaa 136,164,180\nQda 176,63,33536\nQmvav 132,152,28\nQta 112,61,0\nQmvaa 132,156,180\ns_add_u32 s69, s87, 2686072\nQmvav 128,144,28\nbuffer_load_dwordx4 v62, s[52:55], s69 offen offset:1032 lds\n"
"Qmvaa 128,148,180\nQdv 28,63,34048\nQmvav 160,144,32\nQda 180,63,34560\nQmvaa 160,148,184\nQta 114,61,512\ns_add_u32 s69, s87, 2750576\nQmvav 164,152,32\n"
"buffer_load_dwordx4 v62, s[52:55], s69 offen offset:2064 lds\nQmvaa 164,156,184\nQta 116,61,4128\nQmvav 168,160,32\nQmvaa 168,164,184\nQta 118,61,4640\ns_add_u32 s69, s87, 2815080\nQmvav 172,168,32\n"
"buffer_load_dwordx4 v62, s[52:55], s69 offen offset:3096 lds\nQmvaa 172,172,184\nQdv 32,63,35072\nQmvav 204,168,36\nQda 184,63,35584\nQmvaa 204,172,188\nQta 120,61,8\ns_add_u32 m0, s61, 0\n"
"Qmvav 200,160,36\ns_cbranch_vccnz .Ld0q3\nbuffer_load_dwordx4 v69, s[40:43], s90 offen offset:3072 nt lds\n.Ld0q3:\nQmvaa 200,164,188\nQta 122,61,520\nQmvav 196,152,36\nQmvaa 196,156,188\n"
"Qta 124,61,4136\nQmvav 192,144,36\nQdv 36,63,36096\nQmvaa 192,148,188\nQda 188,63,36608\nQmvav 224,144,40\nQta 126,61,4648\nQmvaa 224,148,192\n"
"Qmvav 228,152,40\nQta 128,61,256\nQmvaa 228,156,192\nQmvav 232,160,40\nQta 130,61,768\nQmvaa 232,164,192\nQmvav 236,168,40\nQdv 40,63,37120\n"
"Qmvaa 236,172,192\nQda 192,63,37632\nQmaav 220,168,44\nQta 132,61,4384\nQmaaa 220,172,196\nQmaav 216,160,44\nQta 134,61,4896\nQmaaa 216,164,196\n"
"Qmaav 212,152,44\nQta 136,61,264\nQmaaa 212,156,196\nQmaav 208,144,44\nQdv 44,63,38144\nQmaaa 208,148,196\nQda 196,63,38656\nQmaav 240,144,48\n"
"Qta 138,61,776\nQmaaa 240,148,200\nQmaav 244,152,48\nQta 140,61,4392\nQmaaa 244,156,200\nQmaav 248,160,48\nQta 142,61,4904\nQmaaa 248,164,200\n"
"Qmaav 252,168,48\nQdv 48,63,39168\nQmaaa 252,172,200\nQda 200,63,39680\ns_waitcnt lgkmcnt(2)\nQmvav 80,112,24\nQmvaa 80,116,176\ns_add_u32 s68, s71, 4096\n"
"Qmvav 84,120,24\ns_cbranch_vccz .Lst11\nbuffer_store_dwordx4 a[64:67], v60, s[56:59], s68 offen offset:0 sc1\n.Lst11:\nQmvaa 84,124,176\nQmvav 88,128,24\ns_add_u32 s68, s71, 18432\nQmvaa 88,132,176\n"
"s_cbranch_vccz .Lst10\nbuffer_store_dwordx4 a[68:71], v60, s[56:59], s68 offen offset:0 sc1\n.Lst10:\nQmvav 92,136,24\nQmvaa 92,140,176\ns_waitcnt lgkmcnt(0)\nQmvav 124,136,28\ns_waitcnt vmcnt(17)\n"
"s_barrier\nQmvaa 124,140,180\nQta 144,61,16512\ns_add_u32 m0, s62, 0\ns_add_u32 s69, s87, 3145728\nQmvav 120,128,28\nbuffer_load_dwordx4 v62, s[52:55], s69 offen lds\nQmvaa 120,132,180\n"
"Qta 146,61,17024\nQmvav 116,120,28\nQmvaa 116,124,180\nQta 148,61,20640\ns_add_u32 s69, s87, 3210232\nQmvav 112,112,28\nbuffer_load_dwordx4 v62, s[52:55], s69 offen offset:1032 lds\nQmvaa 112,116,180\n"
"Qta 150,61,21152\nQmvav 144,112,32\nQmvaa 144,116,184\nQta 152,61,16520\ns_add_u32 s69, s87, 3274736\nQmvav 148,120,32\nbuffer_load_dwordx4 v62, s[52:55], s69 offen offset:2064 lds\nQmvaa 148,124,184\n"
"Qta 154,61,17032\nQmvav 152,128,32\nQmvaa 152,132,184\nQta 156,61,20648\ns_add_u32 s69, s87, 3339240\nQmvav 156,136,32\nbuffer_load_dwordx4 v62, s[52:55], s69 offen offset:3096 lds\nQmvaa 156,140,184\n"
"Qta 158,61,21160\nQmvav 188,136,36\nQmvaa 188,140,188\nQta 160,61,16768\ns_add_u32 m0, s63, 33024\ns_add_u32 s88, s86, 768\nQmvav 184,128,36\nbuffer_load_dwordx4 v64, s[48:51], s88 offen lds\n"
"Qmvaa 184,132,188\nQta 162,61,17280\nQmvav 180,120,36\nQmvaa 180,124,188\nQta 164,61,20896\ns_add_u32 s88, s86, 20224\nQmvav 176,112,36\nbuffer_load_dwordx4 v64, s[48:51], s88 offen offset:1024 lds\n"
"Qmvaa 176,116,188\nQta 166,61,21408\nQmvav 208,112,40\nQmvaa 208,116,192\nQta 168,61,16776\ns_add_u32 s88, s86, 39680\nQmvav 212,120,40\nbuffer_load_dwordx4 v64, s[48:51], s88 offen offset:2048 lds\n"
"Qmvaa 212,124,192\nQta 170,61,17288\nQmvav 216,128,40\nQmvaa 216,132,192\nQta 172,61,20904\ns_add_u32 s88, s86, 59136\nQmvav 220,136,40\nbuffer_load_dwordx4 v64, s[48:51], s88 offen offset:3072 lds\n"
"Qmvaa 220,140,192\nQta 174,61,21416\nQmvav 252,136,44\nQmvaa 252,140,196\ns_add_u32 m0, s63, 37120\ns_add_u32 s88, s86, 82688\nQmvav 248,128,44\nbuffer_load_dwordx4 v64, s[48:51], s88 offen lds\n"
"Qmvaa 248,132,196\nQmvav 244,120,44\nQmvaa 244,124,196\ns_add_u32 s88, s86, 102144\nQmvav 240,112,44\nbuffer_load_dwordx4 v64, s[48:51], s88 offen offset:1024 lds\nQmvaa 240,116,196\nQmaav 224,112,48\n"
"Qmaaa 224,116,200\ns_add_u32 s88, s86, 121600\nQmaav 228,120,48\nbuffer_load_dwordx4 v64, s[48:51], s88 offen offset:2048 lds\nQmaaa 228,124,200\nQmaav 232,128,48\nQmaaa 232,132,200\nQmaav 236,136,48\n"
"Qmaaa 236,140,200\ns_waitcnt lgkmcnt(0)\nQmvav 96,144,24\nQmvaa 96,148,176\ns_add_u32 s68, s71, 32768\nQmvav 100,152,24\ns_cbranch_vccz .Lst9\nbuffer_store_dwordx4 a[72:75], v60, s[56:59], s68 offen offset:0 sc1\n"
".Lst9:\nQmvaa 100,156,176\nQmvav 104,160,24\ns_add_u32 s68, s71, 47104\nQmvaa 104,164,176\ns_cbranch_vccz .Lst8\nbuffer_store_dwordx4 a[76:79], v60, s[56:59], s68 offen offset:0 sc1\n.Lst8:\n"
"Qmvav 108,168,24\nQmvaa 108,172,176\ns_waitcnt lgkmcnt(0)\nQmvav 140,168,28\ns_waitcnt vmcnt(16)\ns_barrier\nQmvaa 140,172,180\nQdv 24,63,0\n"
"s_add_u32 m0, s62, 16512\ns_add_u32 s69, s87, 3145856\nQmvav 136,160,28\nbuffer_load_dwordx4 v62, s[52:55], s69 offen lds\nQmvaa 136,164,180\nQda 176,63,512\nQmvav 132,152,28\nQta 112,61,33024\n"
"Qmvaa 132,156,180\ns_add_u32 s69, s87, 3210360\nQmvav 128,144,28\nbuffer_load_dwordx4 v62, s[52:55], s69 offen offset:1032 lds\nQmvaa 128,148,180\nQdv 28,63,1024\nQmvav 160,144,32\nQda 180,63,1536\n"
"Qmvaa 160,148,184\nQta 114,61,33536\ns_add_u32 s69, s87, 3274864\nQmvav 164,152,32\nbuffer_load_dwordx4 v62, s[52:55], s69 offen offset:2064 lds\nQmvaa 164,156,184\nQta 116,61,37152\nQmvav 168,160,32\n"
"Qmvaa 168,164,184\nQta 118,61,37664\ns_add_u32 s69, s87, 3339368\nQmvav 172,168,32\nbuffer_load_dwordx4 v62, s[52:55], s69 offen offset:3096 lds\nQmvaa 172,172,184\nQdv 32,63,2048\nQmvav 204,168,36\n"
"Qda 184,63,2560\nQmvaa 204,172,188\nQta 120,61,33032\ns_add_u32 m0, s64, 0\nQmvav 200,160,36\ns_cbranch_vccnz .Ld0q4\nbuffer_load_dwordx4 v72, s[44:47], s60 offen nt lds\n.Ld0q4:\n"
"Qmvaa 200,164,188\nQta 122,61,33544\nQmvav 196,152,36\nQmvaa 196,156,188\nQta 124,61,37160\nQmvav 192,144,36\nQdv 36,63,3072\nQmvaa 192,148,188\n"
"Qda 188,63,3584\nQmvav 224,144,40\nQta 126,61,37672\nQmvaa 224,148,192\nQmvav 228,152,40\nQta 128,61,33280\nQmvaa 228,156,192\nQmvav 232,160,40\n"
"Qta 130,61,33792\nQmvaa 232,164,192\nQmvav 236,168,40\nQdv 40,63,4096\nQmvaa 236,172,192\nQda 192,63,4608\nQmaav 220,168,44\nQta 132,61,37408\n"
"Qmaaa 220,172,196\nQmaav 216,160,44\nQta 134,61,37920\nQmaaa 216,164,196\nQmaav 212,152,44\nQta 136,61,33288\nQmaaa 212,156,196\nQmaav 208,144,44\n"
"Qdv 44,63,5120\nQmaaa 208,148,196\nQda 196,63,5632\nQmaav 240,144,48\nQta 138,61,33800\nQmaaa 240,148,200\nQmaav 244,152,48\nQta 140,61,37416\n"
"Qmaaa 244,156,200\nQmaav 248,160,48\nQta 142,61,37928\nQmaaa 248,164,200\nQmaav 252,168,48\nQdv 48,63,6144\nQmaaa 252,172,200\nQda 200,63,6656\n"
"s_waitcnt lgkmcnt(2)\nQmvav 80,112,24\nQmvaa 80,116,176\ns_add_u32 s68, s71, 4096\nQmvav 84,120,24\ns_cbranch_vccz .Lst7\nbuffer_store_dwordx4 a[80:83], v60, s[56:59], s68 offen offset:1024 sc1\n.Lst7:\n"
"Qmvaa 84,124,176\nQmvav 88,128,24\ns_add_u32 s68, s71, 18432\nQmvaa 88,132,176\ns_cbranch_vccz .Lst6\nbuffer_store_dwordx4 a[84:87], v60, s[56:59], s68 offen offset:1024 sc1\n.Lst6:\nQmvav 92,136,24\n"
"Qmvaa 92,140,176\ns_waitcnt lgkmcnt(0)\nQmvav 124,136,28\ns_waitcnt vmcnt(17)\ns_barrier\nQmvaa 124,140,180\nQta 144,61,49536\ns_add_u32 m0, s62, 33024\n"
"s_add_u32 s69, s87, 3670016\nQmvav 120,128,28\nbuffer_load_dwordx4 v62, s[52:55], s69 offen lds\nQmvaa 120,132,180\nQta 146,61,50048\nQmvav 116,120,28\nQmvaa 116,124,180\nQta 148,61,53664\n"
"s_add_u32 s69, s87, 3734520\nQmvav 112,112,28\nbuffer_load_dwordx4 v62, s[52:55], s69 offen offset:1032 lds\nQmvaa 112,116,180\nQta 150,61,54176\nQmvav 144,112,32\nQmvaa 144,116,184\nQta 152,61,49544\n"
"s_add_u32 s69, s87, 3799024\nQmvav 148,120,32\nbuffer_load_dwordx4 v62, s[52:55], s69 offen offset:2064 lds\nQmvaa 148,124,184\nQta 154,61,50056\nQmvav 152,128,32\nQmvaa 152,132,184\nQta 156,61,53672\n"
"s_add_u32 s69, s87, 3863528\nQmvav 156,136,32\nbuffer_load_dwordx4 v62, s[52:55], s69 offen offset:3096 lds\nQmvaa 156,140,184\nQta 158,61,54184\nQmvav 188,136,36\nQmvaa 188,140,188\nQta 160,61,49792\n"
"s_add_u32 m0, s63, 0\ns_add_u32 s88, s86, 896\nQmvav 184,128,36\nbuffer_load_dwordx4 v64, s[48:51], s88 offen lds\nQmvaa 184,132,188\nQta 162,61,50304\nQmvav 180,120,36\nQmvaa 180,124,188\n"
"Qta 164,61,53920\ns_add_u32 s88, s86, 20352\nQmvav 176,112,36\nbuffer_load_dwordx4 v64, s[48:51], s88 offen offset:1024 lds\nQmvaa 176,116,188\nQta 166,61,54432\nQmvav 208,112,40\nQmvaa 208,116,192\n"
"Qta 168,61,49800\ns_add_u32 s88, s86, 39808\nQmvav 212,120,40\nbuffer_load_dwordx4 v64, s[48:51], s88 offen offset:2048 lds\nQmvaa 212,124,192\nQta 170,61,50312\nQmvav 216,128,40\nQmvaa 216,132,192\n"
"Qta 172,61,53928\ns_add_u32 s88, s86, 59264\nQmvav 220,136,40\nbuffer_load_dwordx4 v64, s[48:51], s88 offen offset:3072 lds\nQmvaa 220,140,192\nQta 174,61,54440\nQmvav 252,136,44\nQmvaa 252,140,196\n"
"s_add_u32 m0, s63, 4096\ns_add_u32 s88, s86, 82816\nQmvav 248,128,44\nbuffer_load_dwordx4 v64, s[48:51], s88 offen lds\nQmvaa 248,132,196\nQmvav 244,120,44\nQmvaa 244,124,196\ns_add_u32 s88, s86, 102272\n"
"Qmvav 240,112,44\nbuffer_load_dwordx4 v64, s[48:51], s88 offen offset:1024 lds\nQmvaa 240,116,196\nQmaav 224,112,48\nQmaaa 224,116,200\ns_add_u32 s88, s86, 121728\nQmaav 228,120,48\nbuffer_load_dwordx4 v64, s[48:51], s88 offen offset:2048 lds\n"
"Qmaaa 228,124,200\nQmaav 232,128,48\nQmaaa 232,132,200\nQmaav 236,136,48\nQmaaa 236,140,200\ns_waitcnt lgkmcnt(0)\nQmvav 96,144,24\nQmvaa 96,148,176\n"
"s_add_u32 s68, s71, 32768\nQmvav 100,152,24\ns_cbranch_vccz .Lst5\nbuffer_store_dwordx4 a[88:91], v60, s[56:59], s68 offen offset:1024 sc1\n.Lst5:\nQmvaa 100,156,176\nQmvav 104,160,24\ns_add_u32 s68, s71, 47104\n"
"Qmvaa 104,164,176\ns_cbranch_vccz .Lst4\nbuffer_store_dwordx4 a[92:95], v60, s[56:59], s68 offen offset:1024 sc1\n.Lst4:\nQmvav 108,168,24\nQmvaa 108,172,176\ns_waitcnt lgkmcnt(0)\nQmvav 140,168,28\n"
"s_waitcnt vmcnt(16)\ns_barrier\nQmvaa 140,172,180\nQdv 24,63,33024\ns_add_u32 m0, s62, 49536\ns_add_u32 s69, s87, 3670144\nQmvav 136,160,28\nbuffer_load_dwordx4 v62, s[52:55], s69 offen lds\n"
"Qmvaa 136,164,180\nQda 176,63,33536\nQmvav 132,152,28\nQta 112,61,0\nQmvaa 132,156,180\ns_add_u32 s69, s87, 3734648\nQmvav 128,144,28\nbuffer_load_dwordx4 v62, s[52:55], s69 offen offset:1032 lds\n"
"Qmvaa 128,148,180\nQdv 28,63,34048\nQmvav 160,144,32\nQda 180,63,34560\nQmvaa 160,148,184\nQta 114,61,512\ns_add_u32 s69, s87, 3799152\nQmvav 164,152,32\n"
"buffer_load_dwordx4 v62, s[52:55], s69 offen offset:2064 lds\nQmvaa 164,156,184\nQta 116,61,4128\nQmvav 168,160,32\nQmvaa 168,164,184\nQta 118,61,4640\ns_add_u32 s69, s87, 3863656\nQmvav 172,168,32\n"
"buffer_load_dwordx4 v62, s[52:55], s69 offen offset:3096 lds\nQmvaa 172,172,184\nQdv 32,63,35072\nQmvav 204,168,36\nQda 184,63,35584\nQmvaa 204,172,188\nQta 120,61,8\ns_add_u32 m0, s64, 0\n"
"Qmvav 200,160,36\ns_cbranch_vccnz .Ld0q5\nbuffer_load_dwordx4 v72, s[44:47], s60 offen offset:1024 nt lds\n.Ld0q5:\nQmvaa 200,164,188\nQta 122,61,520\nQmvav 196,152,36\nQmvaa 196,156,188\n"
"Qta 124,61,4136\nQmvav 192,144,36\nQdv 36,63,36096\nQmvaa 192,148,188\nQda 188,63,36608\nQmvav 224,144,40\nQta 126,61,4648\nQmvaa 224,148,192\n"
"Qmvav 228,152,40\nQta 128,61,256\nQmvaa 228,156,192\nQmvav 232,160,40\nQta 130,61,768\nQmvaa 232,164,192\nQmvav 236,168,40\nQdv 40,63,37120\n"
"Qmvaa 236,172,192\nQda 192,63,37632\nQmaav 220,168,44\nQta 132,61,4384\nQmaaa 220,172,196\nQmaav 216,160,44\nQta 134,61,4896\nQmaaa 216,164,196\n"
"Qmaav 212,152,44\nQta 136,61,264\nQmaaa 212,156,196\n.Lpg2:\ns_cbranch_execz .Ltr0\nQmaav 208,144,44\nQdv 44,63,38144\nQmaaa 208,148,196\n"
"Qda 196,63,38656\nQmaav 240,144,48\nQta 138,61,776\nQmaaa 240,148,200\nQmaav 244,152,48\nQta 140,61,4392\nQmaaa 244,156,200\nQmaav 248,160,48\n"
"Qta 142,61,4904\nQmaaa 248,164,200\nQmaav 252,168,48\nQdv 48,63,39168\nQmaaa 252,172,200\nQda 200,63,39680\ns_waitcnt lgkmcnt(2)\nQmvav 80,112,24\n"
"Qmvaa 80,116,176\ns_add_u32 s68, s71, 4096\nQmvav 84,120,24\ns_cbranch_vccz .Lst3\nbuffer_store_dwordx4 a[96:99], v60, s[56:59], s68 offen offset:2048 sc1\n.Lst3:\nQmvaa 84,124,176\nQmvav 88,128,24\n"
"s_add_u32 s68, s71, 18432\nQmvaa 88,132,176\ns_cbranch_vccz .Lst2\nbuffer_store_dwordx4 a[100:103], v60, s[56:59], s68 offen offset:2048 sc1\n.Lst2:\nQmvav 92,136,24\nQmvaa 92,140,176\ns_waitcnt lgkmcnt(0)\n"
"Qmvav 124,136,28\ns_waitcnt vmcnt(17)\ns_barrier\nQmvaa 124,140,180\nQta 144,61,16512\ns_add_u32 m0, s62, 0\ns_add_u32 s69, s87, 4194304\nQmvav 120,128,28\n"
"buffer_load_dwordx4 v62, s[52:55], s69 offen lds\nQmvaa 120,132,180\nQta 146,61,17024\nQmvav 116,120,28\nQmvaa 116,124,180\nQta 148,61,20640\ns_add_u32 s69, s87, 4258808\nQmvav 112,112,28\n"
"buffer_load_dwordx4 v62, s[52:55], s69 offen offset:1032 lds\nQmvaa 112,116,180\nQta 150,61,21152\nQmvav 144,112,32\nQmvaa 144,116,184\nQta 152,61,16520\ns_add_u32 s69, s87, 4323312\nQmvav 148,120,32\n"
"buffer_load_dwordx4 v62, s[52:55], s69 offen offset:2064 lds\nQmvaa 148,124,184\nQta 154,61,17032\nQmvav 152,128,32\nQmvaa 152,132,184\nQta 156,61,20648\ns_add_u32 s69, s87, 4387816\nQmvav 156,136,32\n"
"buffer_load_dwordx4 v62, s[52:55], s69 offen offset:3096 lds\nQmvaa 156,140,184\nQta 158,61,21160\nQmvav 188,136,36\nQmvaa 188,140,188\nQta 160,61,16768\ns_add_u32 m0, s63, 33024\ns_add_u32 s88, s86, 1024\n"
"Qmvav 184,128,36\nbuffer_load_dwordx4 v64, s[48:51], s88 offen lds\nQmvaa 184,132,188\nQta 162,61,17280\nQmvav 180,120,36\nQmvaa 180,124,188\nQta 164,61,20896\ns_add_u32 s88, s86, 20480\n"
"Qmvav 176,112,36\nbuffer_load_dwordx4 v64, s[48:51], s88 offen offset:1024 lds\nQmvaa 176,116,188\nQta 166,61,21408\nQmvav 208,112,40\nQmvaa 208,116,192\nQta 168,61,16776\ns_add_u32 s88, s86, 39936\n"
"Qmvav 212,120,40\nbuffer_load_dwordx4 v64, s[48:51], s88 offen offset:2048 lds\nQmvaa 212,124,192\nQta 170,61,17288\nQmvav 216,128,40\nQmvaa 216,132,192\nQta 172,61,20904\ns_add_u32 s88, s86, 59392\n"
"Qmvav 220,136,40\nbuffer_load_dwordx4 v64, s[48:51], s88 offen offset:3072 lds\nQmvaa 220,140,192\nQta 174,61,21416\nQmvav 252,136,44\nQmvaa 252,140,196\ns_add_u32 m0, s63, 37120\ns_add_u32 s88, s86, 82944\n"
"Qmvav 248,128,44\nbuffer_load_dwordx4 v64, s[48:51], s88 offen lds\nQmvaa 248,132,196\nQmvav 244,120,44\nQmvaa 244,124,196\ns_add_u32 s88, s86, 102400\nQmvav 240,112,44\nbuffer_load_dwordx4 v64, s[48:51], s88 offen offset:1024 lds\n"
"Qmvaa 240,116,196\nQmaav 224,112,48\nQmaaa 224,116,200\ns_add_u32 s88, s86, 121856\nQmaav 228,120,48\nbuffer_load_dwordx4 v64, s[48:51], s88 offen offset:2048 lds\nQmaaa 228,124,200\nQmaav 232,128,48\n"
"Qmaaa 232,132,200\nQmaav 236,136,48\nQmaaa 236,140,200\ns_waitcnt lgkmcnt(0)\nQmvav 96,144,24\nQmvaa 96,148,176\ns_add_u32 s68, s71, 32768\nQmvav 100,152,24\n"
"s_cbranch_vccz .Lst1\nbuffer_store_dwordx4 a[104:107], v60, s[56:59], s68 offen offset:2048 sc1\n.Lst1:\nQmvaa 100,156,176\nQmvav 104,160,24\ns_add_u32 s68, s71, 47104\nQmvaa 104,164,176\ns_cbranch_vccz .Lst0\n"
"buffer_store_dwordx4 a[108:111], v60, s[56:59], s68 offen offset:2048 sc1\n.Lst0:\nQmvav 108,168,24\nQmvaa 108,172,176\ns_waitcnt lgkmcnt(0)\nQmvav 140,168,28\ns_waitcnt vmcnt(16)\ns_barrier\n"
"Qmvaa 140,172,180\nQdv 24,63,0\ns_add_u32 m0, s62, 16512\ns_add_u32 s69, s87, 4194432\nQmvav 136,160,28\nbuffer_load_dwordx4 v62, s[52:55], s69 offen lds\nQmvaa 136,164,180\nQda 176,63,512\n"
"Qmvav 132,152,28\nQta 112,61,33024\nQmvaa 132,156,180\ns_add_u32 s69, s87, 4258936\nQmvav 128,144,28\nbuffer_load_dwordx4 v62, s[52:55], s69 offen offset:1032 lds\nQmvaa 128,148,180\nQdv 28,63,1024\n"
"Qmvav 160,144,32\nQda 180,63,1536\nQmvaa 160,148,184\nQta 114,61,33536\ns_add_u32 s69, s87, 4323440\nQmvav 164,152,32\nbuffer_load_dwordx4 v62, s[52:55], s69 offen offset:2064 lds\nQmvaa 164,156,184\n"
"Qta 116,61,37152\nQmvav 168,160,32\nQmvaa 168,164,184\nQta 118,61,37664\ns_add_u32 s69, s87, 4387944\nQmvav 172,168,32\nbuffer_load_dwordx4 v62, s[52:55], s69 offen offset:3096 lds\nQmvaa 172,172,184\n"
"Qdv 32,63,2048\nQmvav 204,168,36\nQda 184,63,2560\nQmvaa 204,172,188\nQta 120,61,33032\ns_add_u32 m0, s64, 0\nQmvav 200,160,36\ns_cbranch_vccnz .Ld0q6\n"
"buffer_load_dwordx4 v72, s[44:47], s90 offen offset:2048 nt lds\n.Ld0q6:\nQmvaa 200,164,188\nQta 122,61,33544\nQmvav 196,152,36\nQmvaa 196,156,188\nQta 124,61,37160\nQmvav 192,144,36\n"
"Qdv 36,63,3072\nQmvaa 192,148,188\nQda 188,63,3584\nQmvav 224,144,40\nQta 126,61,37672\nQmvaa 224,148,192\nQmvav 228,152,40\nQta 128,61,33280\n"
"Qmvaa 228,156,192\nQmvav 232,160,40\nQta 130,61,33792\nQmvaa 232,164,192\nQmvav 236,168,40\nQdv 40,63,4096\nQmvaa 236,172,192\nQda 192,63,4608\n"
"Qmaav 220,168,44\nQta 132,61,37408\nQmaaa 220,172,196\nQmaav 216,160,44\nQta 134,61,37920\nQmaaa 216,164,196\nQmaav 212,152,44\nQta 136,61,33288\n"
"Qmaaa 212,156,196\nQmaav 208,144,44\nQdv 44,63,5120\nQmaaa 208,148,196\nQda 196,63,5632\nQmaav 240,144,48\nQta 138,61,33800\nQmaaa 240,148,200\n"
"Qmaav 244,152,48\nQta 140,61,37416\nQmaaa 244,156,200\nQmaav 248,160,48\nQta 142,61,37928\nQmaaa 248,164,200\nQmaav 252,168,48\nQdv 48,63,6144\n"
"Qmaaa 252,172,200\nQda 200,63,6656\ns_waitcnt lgkmcnt(2)\nQmvav 80,112,24\nQmvaa 80,116,176\nQmvav 84,120,24\nQmvaa 84,124,176\nQmvav 88,128,24\n"
"Qmvaa 88,132,176\nQmvav 92,136,24\nQmvaa 92,140,176\ns_waitcnt lgkmcnt(0)\nQmvav 124,136,28\ns_waitcnt vmcnt(17)\ns_barrier\nQmvaa 124,140,180\n"
"Qta 144,61,49536\ns_add_u32 m0, s62, 33024\ns_add_u32 s69, s87, 4718592\nQmvav 120,128,28\nbuffer_load_dwordx4 v62, s[52:55], s69 offen lds\nQmvaa 120,132,180\nQta 146,61,50048\nQmvav 116,120,28\n"
"Qmvaa 116,124,180\nQta 148,61,53664\ns_add_u32 s69, s87, 4783096\nQmvav 112,112,28\nbuffer_load_dwordx4 v62, s[52:55], s69 offen offset:1032 lds\nQmvaa 112,116,180\nQta 150,61,54176\nQmvav 144,112,32\n"
"Qmvaa 144,116,184\nQta 152,61,49544\ns_add_u32 s69, s87, 4847600\nQmvav 148,120,32\nbuffer_load_dwordx4 v62, s[52:55], s69 offen offset:2064 lds\nQmvaa 148,124,184\nQta 154,61,50056\nQmvav 152,128,32\n"
"Qmvaa 152,132,184\nQta 156,61,53672\ns_add_u32 s69, s87, 4912104\nQmvav 156,136,32\nbuffer_load_dwordx4 v62, s[52:55], s69 offen offset:3096 lds\nQmvaa 156,140,184\nQta 158,61,54184\nQmvav 188,136,36\n"
"Qmvaa 188,140,188\nQta 160,61,49792\ns_add_u32 m0, s63, 0\ns_add_u32 s88, s86, 1152\nQmvav 184,128,36\nbuffer_load_dwordx4 v64, s[48:51], s88 offen lds\nQmvaa 184,132,188\nQta 162,61,50304\n"
"Qmvav 180,120,36\nQmvaa 180,124,188\nQta 164,61,53920\ns_add_u32 s88, s86, 20608\nQmvav 176,112,36\nbuffer_load_dwordx4 v64, s[48:51], s88 offen offset:1024 lds\nQmvaa 176,116,188\nQta 166,61,54432\n"
"Qmvav 208,112,40\nQmvaa 208,116,192\nQta 168,61,49800\ns_add_u32 s88, s86, 40064\nQmvav 212,120,40\nbuffer_load_dwordx4 v64, s[48:51], s88 offen offset:2048 lds\nQmvaa 212,124,192\nQta 170,61,50312\n"
"Qmvav 216,128,40\nQmvaa 216,132,192\nQta 172,61,53928\ns_add_u32 s88, s86, 59520\nQmvav 220,136,40\nbuffer_load_dwordx4 v64, s[48:51], s88 offen offset:3072 lds\nQmvaa 220,140,192\nQta 174,61,54440\n"
"Qmvav 252,136,44\nQmvaa 252,140,196\ns_add_u32 m0, s63, 4096\ns_add_u32 s88, s86, 83072\nQmvav 248,128,44\nbuffer_load_dwordx4 v64, s[48:51], s88 offen lds\nQmvaa 248,132,196\nQmvav 244,120,44\n"
"Qmvaa 244,124,196\ns_add_u32 s88, s86, 102528\nQmvav 240,112,44\nbuffer_load_dwordx4 v64, s[48:51], s88 offen offset:1024 lds\nQmvaa 240,116,196\nQmaav 224,112,48\nQmaaa 224,116,200\ns_add_u32 s88, s86, 121984\n"
"Qmaav 228,120,48\nbuffer_load_dwordx4 v64, s[48:51], s88 offen offset:2048 lds\nQmaaa 228,124,200\nQmaav 232,128,48\nQmaaa 232,132,200\nQmaav 236,136,48\nQmaaa 236,140,200\ns_waitcnt lgkmcnt(0)\n"
"Qmvav 96,144,24\nQmvaa 96,148,176\nQmvav 100,152,24\nQmvaa 100,156,176\nQmvav 104,160,24\nQmvaa 104,164,176\nQmvav 108,168,24\nQmvaa 108,172,176\n"
"s_waitcnt lgkmcnt(0)\nQmvav 140,168,28\ns_waitcnt vmcnt(16)\ns_barrier\nQmvaa 140,172,180\nQdv 24,63,33024\ns_add_u32 m0, s62, 49536\ns_add_u32 s69, s87, 4718720\n"
"Qmvav 136,160,28\nbuffer_load_dwordx4 v62, s[52:55], s69 offen lds\nQmvaa 136,164,180\nQda 176,63,33536\nQmvav 132,152,28\nQta 112,61,0\nQmvaa 132,156,180\ns_add_u32 s69, s87, 4783224\n"
"Qmvav 128,144,28\nbuffer_load_dwordx4 v62, s[52:55], s69 offen offset:1032 lds\nQmvaa 128,148,180\nQdv 28,63,34048\nQmvav 160,144,32\nQda 180,63,34560\nQmvaa 160,148,184\nQta 114,61,512\n"
"s_add_u32 s69, s87, 4847728\nQmvav 164,152,32\nbuffer_load_dwordx4 v62, s[52:55], s69 offen offset:2064 lds\nQmvaa 164,156,184\nQta 116,61,4128\nQmvav 168,160,32\nQmvaa 168,164,184\nQta 118,61,4640\n"
"s_add_u32 s69, s87, 4912232\nQmvav 172,168,32\nbuffer_load_dwordx4 v62, s[52:55], s69 offen offset:3096 lds\nQmvaa 172,172,184\nQdv 32,63,35072\nQmvav 204,168,36\nQda 184,63,35584\nQmvaa 204,172,188\n"
"Qta 120,61,8\ns_add_u32 m0, s64, 0\nQmvav 200,160,36\ns_cbranch_vccnz .Ld0q7\nbuffer_load_dwordx4 v72, s[44:47], s90 offen offset:3072 nt lds\ns_add_u32 s65, s65, 2048\ns_xor_b32 s60, s65, %21\ns_add_u32 s90, s60, 260096\n"
".Ld0q7:\nQmvaa 200,164,188\nQta 122,61,520\nQmvav 196,152,36\nQmvaa 196,156,188\nQta 124,61,4136\nQmvav 192,144,36\nQdv 36,63,36096\n"
"Qmvaa 192,148,188\nQda 188,63,36608\nQmvav 224,144,40\nQta 126,61,4648\nQmvaa 224,148,192\nQmvav 228,152,40\nQta 128,61,256\nQmvaa 228,156,192\n"
"Qmvav 232,160,40\nQta 130,61,768\nQmvaa 232,164,192\nQmvav 236,168,40\nQdv 40,63,37120\nQmvaa 236,172,192\nQda 192,63,37632\nQmaav 220,168,44\n"
"Qta 132,61,4384\nQmaaa 220,172,196\nQmaav 216,160,44\nQta 134,61,4896\nQmaaa 216,164,196\nQmaav 212,152,44\nQta 136,61,264\nQmaaa 212,156,196\n"
"Qmaav 208,144,44\nQdv 44,63,38144\nQmaaa 208,148,196\nQda 196,63,38656\nQmaav 240,144,48\nQta 138,61,776\nQmaaa 240,148,200\nQmaav 244,152,48\n"
"Qta 140,61,4392\nQmaaa 244,156,200\nQmaav 248,160,48\nQta 142,61,4904\nQmaaa 248,164,200\nQmaav 252,168,48\nQdv 48,63,39168\nQmaaa 252,172,200\n"
"Qda 200,63,39680\ns_waitcnt lgkmcnt(2)\nQmvav 80,112,24\nQmvaa 80,116,176\nQmvav 84,120,24\nQmvaa 84,124,176\nQmvav 88,128,24\nQmvaa 88,132,176\n"
"Qmvav 92,136,24\nQmvaa 92,140,176\ns_waitcnt lgkmcnt(0)\nQmvav 124,136,28\ns_waitcnt vmcnt(15)\ns_barrier\nQmvaa 124,140,180\nQta 144,61,16512\n"
"s_mov_b32 s56, s94\ns_mov_b32 s57, s95\ns_mov_b32 s58, s75\ns_mov_b32 s94, 0\ns_add_u32 m0, %10, 160768\nQmvav 120,128,28\nbuffer_load_dwordx4 v60, s[56:59], s94 offen lds\nQmvaa 120,132,180\n"
"Qta 146,61,17024\nQmvav 116,120,28\nQmvaa 116,124,180\nQta 148,61,20640\ns_add_u32 s94, s94, 4096\nQmvav 112,112,28\nbuffer_load_dwordx4 v60, s[56:59], s94 offen lds\nQmvaa 112,116,180\n"
"Qta 150,61,21152\nQmvav 144,112,32\nQmvaa 144,116,184\nQta 152,61,16520\ns_add_u32 s94, s94, 4096\nQmvav 148,120,32\nbuffer_load_dwordx4 v60, s[56:59], s94 offen lds\nQmvaa 148,124,184\n"
"Qta 154,61,17032\nQmvav 152,128,32\nQmvaa 152,132,184\nQta 156,61,20648\ns_add_u32 s94, s94, 4096\nQmvav 156,136,32\nbuffer_load_dwordx4 v60, s[56:59], s94 offen lds\nQmvaa 156,140,184\n"
"Qta 158,61,21160\nQmvav 188,136,36\nQmvaa 188,140,188\nQta 160,61,16768\ns_add_u32 s94, s94, 4096\nQmvav 184,128,36\nbuffer_load_dwordx4 v60, s[56:59], s94 offen lds\nQmvaa 184,132,188\n"
"Qta 162,61,17280\nQmvav 180,120,36\nQmvaa 180,124,188\nQta 164,61,20896\ns_add_u32 s94, s94, 4096\nQmvav 176,112,36\nbuffer_load_dwordx4 v60, s[56:59], s94 offen lds\nQmvaa 176,116,188\n"
"Qta 166,61,21408\nQmvav 208,112,40\nQmvaa 208,116,192\nQta 168,61,16776\ns_add_u32 s94, s94, 4096\nQmvav 212,120,40\nbuffer_load_dwordx4 v60, s[56:59], s94 offen lds\nQmvaa 212,124,192\n"
"Qta 170,61,17288\nQmvav 216,128,40\nQmvaa 216,132,192\nQta 172,61,20904\ns_add_u32 s94, s94, 4096\nQmvav 220,136,40\nbuffer_load_dwordx4 v60, s[56:59], s94 offen lds\nQmvaa 220,140,192\n"
"Qta 174,61,21416\nQmvav 252,136,44\nQmvaa 252,140,196\ns_add_u32 s94, s94, 4096\nQmvav 248,128,44\nbuffer_load_dwordx4 v60, s[56:59], s94 offen lds\nQmvaa 248,132,196\nQmvav 244,120,44\n"
"Qmvaa 244,124,196\ns_add_u32 s94, s94, 4096\nQmvav 240,112,44\nbuffer_load_dwordx4 v60, s[56:59], s94 offen lds\n.Lpg3:\ns_cbranch_execz .Ltr0\nQmvaa 240,116,196\nQmaav 224,112,48\n"
"Qmaaa 224,116,200\nQmaav 228,120,48\nQmaaa 228,124,200\nQmaav 232,128,48\nQmaaa 232,132,200\nQmaav 236,136,48\nQmaaa 236,140,200\ns_waitcnt lgkmcnt(0)\n"
"Qmvav 96,144,24\nQmvaa 96,148,176\nQmvav 100,152,24\nQmvaa 100,156,176\nQmvav 104,160,24\nQmvaa 104,164,176\nQmvav 108,168,24\nQmvaa 108,172,176\n"
"s_waitcnt lgkmcnt(0)\nQmvav 140,168,28\ns_waitcnt vmcnt(14)\ns_barrier\nQmvaa 140,172,180\nQdv 24,63,0\ns_add_u32 m0, s61, 33024\nQmvav 136,160,28\n"
"buffer_load_dwordx4 v69, s[40:43], s60 offen nt lds\nQmvaa 136,164,180\nQda 176,63,512\nQmvav 132,152,28\nQta 112,61,33024\nQmvaa 132,156,180\nQmvav 128,144,28\nbuffer_load_dwordx4 v69, s[40:43], s60 offen offset:1024 nt lds\n"
"Qmvaa 128,148,180\nQdv 28,63,1024\nQmvav 160,144,32\nQda 180,63,1536\nQmvaa 160,148,184\nQta 114,61,33536\nQmvav 164,152,32\nbuffer_load_dwordx4 v69, s[40:43], s90 offen offset:2048 nt lds\n"
"Qmvaa 164,156,184\nQta 116,61,37152\nQmvav 168,160,32\nQmvaa 168,164,184\nQta 118,61,37664\nQmvav 172,168,32\nbuffer_load_dwordx4 v69, s[40:43], s90 offen offset:3072 nt lds\nQmvaa 172,172,184\n"
"Qdv 32,63,2048\nQmvav 204,168,36\nQda 184,63,2560\nQmvaa 204,172,188\nQta 120,61,33032\ns_add_u32 m0, s64, 33024\nQmvav 200,160,36\nbuffer_load_dwordx4 v72, s[44:47], s60 offen nt lds\n"
"Qmvaa 200,164,188\nQta 122,61,33544\nQmvav 196,152,36\nQmvaa 196,156,188\nQta 124,61,37160\nQmvav 192,144,36\nbuffer_load_dwordx4 v72, s[44:47], s60 offen offset:1024 nt lds\nQmvaa 192,148,188\n"
"Qdv 36,63,3072\nQmvav 224,144,40\nQda 188,63,3584\nQmvaa 224,148,192\nQta 126,61,37672\nQmvav 228,152,40\nbuffer_load_dwordx4 v72, s[44:47], s90 offen offset:2048 nt lds\nQmvaa 228,156,192\n"
"Qta 128,61,33280\nQmvav 232,160,40\nQmvaa 232,164,192\nQta 130,61,33792\nQmvav 236,168,40\nbuffer_load_dwordx4 v72, s[44:47], s90 offen offset:3072 nt lds\ns_add_u32 s65, s65, 2048\ns_xor_b32 s60, s65, %21\n"
"s_add_u32 s90, s60, 260096\nQmvaa 236,172,192\nQdv 40,63,4096\nQmaav 220,168,44\nQda 192,63,4608\nQmaaa 220,172,196\nQta 132,61,37408\nQmaav 216,160,44\n"
"Qmaaa 216,164,196\nQta 134,61,37920\nQmaav 212,152,44\nQmaaa 212,156,196\nQta 136,61,33288\nQmaav 208,144,44\nQdv 44,63,5120\nQmaaa 208,148,196\n"
"Qda 196,63,5632\nQmaav 240,144,48\nQta 138,61,33800\nQmaaa 240,148,200\nQmaav 244,152,48\nQta 140,61,37416\nQmaaa 244,156,200\nQmaav 248,160,48\n"
"Qta 142,61,37928\nQmaaa 248,164,200\nQmaav 252,168,48\nQdv 48,63,6144\nQmaaa 252,172,200\nQda 200,63,6656\ns_waitcnt lgkmcnt(2)\nQmvav 80,112,24\n"
"Qmvaa 80,116,176\nQmvav 84,120,24\nQmvaa 84,124,176\nQmvav 88,128,24\nQmvaa 88,132,176\nQmvav 92,136,24\nQmvaa 92,140,176\ns_waitcnt lgkmcnt(0)\n"
"Qmvav 124,136,28\ns_waitcnt vmcnt(18)\ns_barrier\nQmvaa 124,140,180\nQta 144,61,49536\nQmvav 120,128,28\nQmvaa 120,132,180\nQta 146,61,50048\n"
"Qmvav 116,120,28\nQmvaa 116,124,180\nQta 148,61,53664\nQmvav 112,112,28\nQmvaa 112,116,180\nQta 150,61,54176\nQmvav 144,112,32\nQmvaa 144,116,184\n"
"Qta 152,61,49544\nQmvav 148,120,32\nQmvaa 148,124,184\nQta 154,61,50056\nQmvav 152,128,32\nQmvaa 152,132,184\nQta 156,61,53672\nQmvav 156,136,32\n"
"Qmvaa 156,140,184\nQta 158,61,54184\nQmvav 188,136,36\nQmvaa 188,140,188\nQta 160,61,49792\nQmvav 184,128,36\nQmvaa 184,132,188\nQta 162,61,50304\n"
"Qmvav 180,120,36\nQmvaa 180,124,188\nQta 164,61,53920\nQmvav 176,112,36\nQmvaa 176,116,188\nQta 166,61,54432\nQmvav 208,112,40\nQmvaa 208,116,192\n"
"Qta 168,61,49800\nQmvav 212,120,40\nQmvaa 212,124,192\nQta 170,61,50312\nQmvav 216,128,40\nQmvaa 216,132,192\nQta 172,61,53928\nQmvav 220,136,40\n"
"Qmvaa 220,140,192\nQta 174,61,54440\nQmvav 252,136,44\nQmvaa 252,140,196\nQmvav 248,128,44\nQmvaa 248,132,196\nQmvav 244,120,44\nQmvaa 244,124,196\n"
"Qmvav 240,112,44\nQmvaa 240,116,196\nQmaav 224,112,48\nQmaaa 224,116,200\nQmaav 228,120,48\nQmaaa 228,124,200\nQmaav 232,128,48\nQmaaa 232,132,200\n"
"Qmaav 236,136,48\nQmaaa 236,140,200\ns_waitcnt lgkmcnt(0)\nQmvav 96,144,24\nQmvaa 96,148,176\nQmvav 100,152,24\nQmvaa 100,156,176\nQmvav 104,160,24\n"
"Qmvaa 104,164,176\nQmvav 108,168,24\nQmvaa 108,172,176\ns_waitcnt lgkmcnt(0)\nQmvav 140,168,28\ns_waitcnt vmcnt(18)\ns_barrier\nQmvaa 140,172,180\n"
"Qda 112,68,0\ns_add_u32 m0, s61, 66048\nv_accvgpr_write_b32 a176, 0\nQmvav 136,160,28\nbuffer_load_dwordx4 v69, s[40:43], s60 offen nt lds\nQmvaa 136,164,180\nQda 116,68,512\nv_accvgpr_write_b32 a177, 0\n"
"Qmvav 132,152,28\nQmvaa 132,156,180\nQda 120,68,256\nv_accvgpr_write_b32 a178, 0\nQmvav 128,144,28\nbuffer_load_dwordx4 v69, s[40:43], s60 offen offset:1024 nt lds\nQmvaa 128,148,180\nQda 124,68,768\n"
"v_accvgpr_write_b32 a179, 0\nQmvav 160,144,32\nQmvaa 160,148,184\nQda 128,68,1024\nv_accvgpr_write_b32 a180, 0\nQmvav 164,152,32\nbuffer_load_dwordx4 v69, s[40:43], s90 offen offset:2048 nt lds\nQmvaa 164,156,184\n"
"Qda 132,68,1536\nv_accvgpr_write_b32 a181, 0\nQmvav 168,160,32\nQmvaa 168,164,184\nQda 136,68,1280\nv_accvgpr_write_b32 a182, 0\nQmvav 172,168,32\nbuffer_load_dwordx4 v69, s[40:43], s90 offen offset:3072 nt lds\n"
"Qmvaa 172,172,184\nQda 140,68,1792\nv_accvgpr_write_b32 a183, 0\nQmvav 204,168,36\nQmvaa 204,172,188\ns_add_u32 m0, s64, 66048\nv_accvgpr_write_b32 a184, 0\nQmvav 200,160,36\n"
"buffer_load_dwordx4 v72, s[44:47], s60 offen nt lds\nQmvaa 200,164,188\nv_accvgpr_write_b32 a185, 0\nQmvav 196,152,36\nQmvaa 196,156,188\nv_accvgpr_write_b32 a186, 0\nQmvav 192,144,36\nbuffer_load_dwordx4 v72, s[44:47], s60 offen offset:1024 nt lds\n"
"Qmvaa 192,148,188\nv_accvgpr_write_b32 a187, 0\nQmvav 224,144,40\nQmvaa 224,148,192\nv_accvgpr_write_b32 a188, 0\nQmvav 228,152,40\nbuffer_load_dwordx4 v72, s[44:47], s90 offen offset:2048 nt lds\nQmvaa 228,156,192\n"
"v_accvgpr_write_b32 a189, 0\nQmvav 232,160,40\nQmvaa 232,164,192\nv_accvgpr_write_b32 a190, 0\nQmvav 236,168,40\nbuffer_load_dwordx4 v72, s[44:47], s90 offen offset:3072 nt lds\ns_add_u32 s65, s65, 2048\ns_xor_b32 s60, s65, %21\n"
"s_add_u32 s90, s60, 260096\nQmvaa 236,172,192\nv_accvgpr_write_b32 a191, 0\nQmaav 220,168,44\nQmaaa 220,172,196\nv_accvgpr_write_b32 a192, 0\nQmaav 216,160,44\nQmaaa 216,164,196\n"
"v_accvgpr_write_b32 a193, 0\nQmaav 212,152,44\nQmaaa 212,156,196\nv_accvgpr_write_b32 a194, 0\nQmaav 208,144,44\nQmaaa 208,148,196\nv_accvgpr_write_b32 a195, 0\nQmaav 240,144,48\n"
"Qmaaa 240,148,200\nv_accvgpr_write_b32 a196, 0\nQmaav 244,152,48\nQmaaa 244,156,200\nv_accvgpr_write_b32 a197, 0\nQmaav 248,160,48\nQmaaa 248,164,200\nv_accvgpr_write_b32 a198, 0\n"
"Qmaav 252,168,48\nQmaaa 252,172,200\nv_accvgpr_write_b32 a199, 0\nQta 148,70,128\nQta 150,70,1152\nQta 156,70,384\nQta 158,70,1408\nQta 164,70,640\n"
"Qta 166,70,1664\nQta 172,70,896\nQta 174,70,1920\nv_pk_mul_f32 v[80:81], v[80:81], s[72:73] op_sel_hi:[1,0]\nv_pk_mul_f32 v[82:83], v[82:83], s[72:73] op_sel_hi:[1,0]\nv_pk_mul_f32 v[84:85], v[84:85], s[72:73] op_sel_hi:[1,0]\nv_pk_mul_f32 v[86:87], v[86:87], s[72:73] op_sel_hi:[1,0]\nQk 80,80,81\n"
"Qk 81,82,83\nQk 82,84,85\nQk 83,86,87\nv_accvgpr_write_b32 a0, v80\nv_accvgpr_write_b32 a1, v81\nv_accvgpr_write_b32 a2, v82\nv_accvgpr_write_b32 a3, v83\nv_pk_mul_f32 v[88:89], v[88:89], s[72:73] op_sel_hi:[1,0]\n"
"v_pk_mul_f32 v[90:91], v[90:91], s[72:73] op_sel_hi:[1,0]\nv_pk_mul_f32 v[92:93], v[92:93], s[72:73] op_sel_hi:[1,0]\nv_pk_mul_f32 v[94:95], v[94:95], s[72:73] op_sel_hi:[1,0]\nQk 88,88,89\nQk 89,90,91\nQk 90,92,93\nQk 91,94,95\nv_accvgpr_write_b32 a4, v88\n"
"v_accvgpr_write_b32 a5, v89\nv_accvgpr_write_b32 a6, v90\nv_accvgpr_write_b32 a7, v91\nv_pk_mul_f32 v[96:97], v[96:97], s[72:73] op_sel_hi:[1,0]\nv_pk_mul_f32 v[98:99], v[98:99], s[72:73] op_sel_hi:[1,0]\nv_pk_mul_f32 v[100:101], v[100:101], s[72:73] op_sel_hi:[1,0]\nv_pk_mul_f32 v[102:103], v[102:103], s[72:73] op_sel_hi:[1,0]\nQk 96,96,97\n"
"Qk 97,98,99\nQk 98,100,101\nQk 99,102,103\nv_accvgpr_write_b32 a8, v96\nv_accvgpr_write_b32 a9, v97\nv_accvgpr_write_b32 a10, v98\nv_accvgpr_write_b32 a11, v99\nv_pk_mul_f32 v[104:105], v[104:105], s[72:73] op_sel_hi:[1,0]\n"
"v_pk_mul_f32 v[106:107], v[106:107], s[72:73] op_sel_hi:[1,0]\nv_pk_mul_f32 v[108:109], v[108:109], s[72:73] op_sel_hi:[1,0]\nv_pk_mul_f32 v[110:111], v[110:111], s[72:73] op_sel_hi:[1,0]\nQk 104,104,105\nQk 105,106,107\nQk 106,108,109\nQk 107,110,111\nv_accvgpr_write_b32 a12, v104\n"
"v_accvgpr_write_b32 a13, v105\nv_accvgpr_write_b32 a14, v106\nv_accvgpr_write_b32 a15, v107\nv_pk_mul_f32 v[112:113], v[112:113], s[72:73] op_sel_hi:[1,0]\nv_pk_mul_f32 v[114:115], v[114:115], s[72:73] op_sel_hi:[1,0]\nv_pk_mul_f32 v[116:117], v[116:117], s[72:73] op_sel_hi:[1,0]\nv_pk_mul_f32 v[118:119], v[118:119], s[72:73] op_sel_hi:[1,0]\nQk 112,112,113\n"
"Qk 113,114,115\nQk 114,116,117\nQk 115,118,119\nv_accvgpr_write_b32 a16, v112\nv_accvgpr_write_b32 a17, v113\nv_accvgpr_write_b32 a18, v114\nv_accvgpr_write_b32 a19, v115\nv_pk_mul_f32 v[120:121], v[120:121], s[72:73] op_sel_hi:[1,0]\n"
"v_pk_mul_f32 v[122:123], v[122:123], s[72:73] op_sel_hi:[1,0]\nv_pk_mul_f32 v[124:125], v[124:125], s[72:73] op_sel_hi:[1,0]\nv_pk_mul_f32 v[126:127], v[126:127], s[72:73] op_sel_hi:[1,0]\nQk 120,120,121\nQk 121,122,123\nQk 122,124,125\nQk 123,126,127\nv_accvgpr_write_b32 a20, v120\n"
"v_accvgpr_write_b32 a21, v121\nv_accvgpr_write_b32 a22, v122\nv_accvgpr_write_b32 a23, v123\nv_pk_mul_f32 v[128:129], v[128:129], s[72:73] op_sel_hi:[1,0]\nv_pk_mul_f32 v[130:131], v[130:131], s[72:73] op_sel_hi:[1,0]\nv_pk_mul_f32 v[132:133], v[132:133], s[72:73] op_sel_hi:[1,0]\nv_pk_mul_f32 v[134:135], v[134:135], s[72:73] op_sel_hi:[1,0]\nQk 128,128,129\n"
"Qk 129,130,131\nQk 130,132,133\nQk 131,134,135\nv_accvgpr_write_b32 a24, v128\nv_accvgpr_write_b32 a25, v129\nv_accvgpr_write_b32 a26, v130\nv_accvgpr_write_b32 a27, v131\nv_pk_mul_f32 v[136:137], v[136:137], s[72:73] op_sel_hi:[1,0]\n"
"v_pk_mul_f32 v[138:139], v[138:139], s[72:73] op_sel_hi:[1,0]\nv_pk_mul_f32 v[140:141], v[140:141], s[72:73] op_sel_hi:[1,0]\nv_pk_mul_f32 v[142:143], v[142:143], s[72:73] op_sel_hi:[1,0]\nQk 136,136,137\nQk 137,138,139\nQk 138,140,141\nQk 139,142,143\nv_accvgpr_write_b32 a28, v136\n"
"v_accvgpr_write_b32 a29, v137\nv_accvgpr_write_b32 a30, v138\nv_accvgpr_write_b32 a31, v139\nv_pk_mul_f32 v[144:145], v[144:145], s[72:73] op_sel_hi:[1,0]\nv_pk_mul_f32 v[146:147], v[146:147], s[72:73] op_sel_hi:[1,0]\nv_pk_mul_f32 v[148:149], v[148:149], s[72:73] op_sel_hi:[1,0]\nv_pk_mul_f32 v[150:151], v[150:151], s[72:73] op_sel_hi:[1,0]\nQk 144,144,145\n"
"Qk 145,146,147\nQk 146,148,149\nQk 147,150,151\nv_accvgpr_write_b32 a32, v144\nv_accvgpr_write_b32 a33, v145\nv_accvgpr_write_b32 a34, v146\nv_accvgpr_write_b32 a35, v147\nv_pk_mul_f32 v[152:153], v[152:153], s[72:73] op_sel_hi:[1,0]\n"
"v_pk_mul_f32 v[154:155], v[154:155], s[72:73] op_sel_hi:[1,0]\nv_pk_mul_f32 v[156:157], v[156:157], s[72:73] op_sel_hi:[1,0]\nv_pk_mul_f32 v[158:159], v[158:159], s[72:73] op_sel_hi:[1,0]\nQk 152,152,153\nQk 153,154,155\nQk 154,156,157\nQk 155,158,159\nv_accvgpr_write_b32 a36, v152\n"
"v_accvgpr_write_b32 a37, v153\nv_accvgpr_write_b32 a38, v154\nv_accvgpr_write_b32 a39, v155\nv_pk_mul_f32 v[160:161], v[160:161], s[72:73] op_sel_hi:[1,0]\nv_pk_mul_f32 v[162:163], v[162:163], s[72:73] op_sel_hi:[1,0]\nv_pk_mul_f32 v[164:165], v[164:165], s[72:73] op_sel_hi:[1,0]\nv_pk_mul_f32 v[166:167], v[166:167], s[72:73] op_sel_hi:[1,0]\nQk 160,160,161\n"
"Qk 161,162,163\nQk 162,164,165\nQk 163,166,167\nv_accvgpr_write_b32 a40, v160\nv_accvgpr_write_b32 a41, v161\nv_accvgpr_write_b32 a42, v162\nv_accvgpr_write_b32 a43, v163\nv_pk_mul_f32 v[168:169], v[168:169], s[72:73] op_sel_hi:[1,0]\n"
"v_pk_mul_f32 v[170:171], v[170:171], s[72:73] op_sel_hi:[1,0]\nv_pk_mul_f32 v[172:173], v[172:173], s[72:73] op_sel_hi:[1,0]\nv_pk_mul_f32 v[174:175], v[174:175], s[72:73] op_sel_hi:[1,0]\nQk 168,168,169\nQk 169,170,171\nQk 170,172,173\nQk 171,174,175\nv_accvgpr_write_b32 a44, v168\n"
"v_accvgpr_write_b32 a45, v169\nv_accvgpr_write_b32 a46, v170\nv_accvgpr_write_b32 a47, v171\nv_pk_mul_f32 v[176:177], v[176:177], s[72:73] op_sel_hi:[1,0]\nv_pk_mul_f32 v[178:179], v[178:179], s[72:73] op_sel_hi:[1,0]\nv_pk_mul_f32 v[180:181], v[180:181], s[72:73] op_sel_hi:[1,0]\nv_pk_mul_f32 v[182:183], v[182:183], s[72:73] op_sel_hi:[1,0]\nQk 176,176,177\n"
"Qk 177,178,179\nQk 178,180,181\nQk 179,182,183\nv_accvgpr_write_b32 a48, v176\nv_accvgpr_write_b32 a49, v177\nv_accvgpr_write_b32 a50, v178\nv_accvgpr_write_b32 a51, v179\nv_pk_mul_f32 v[184:185], v[184:185], s[72:73] op_sel_hi:[1,0]\n"
"v_pk_mul_f32 v[186:187], v[186:187], s[72:73] op_sel_hi:[1,0]\nv_pk_mul_f32 v[188:189], v[188:189], s[72:73] op_sel_hi:[1,0]\nv_pk_mul_f32 v[190:191], v[190:191], s[72:73] op_sel_hi:[1,0]\nQk 184,184,185\nQk 185,186,187\nQk 186,188,189\nQk 187,190,191\nv_accvgpr_write_b32 a52, v184\n"
"v_accvgpr_write_b32 a53, v185\nv_accvgpr_write_b32 a54, v186\nv_accvgpr_write_b32 a55, v187\nv_pk_mul_f32 v[192:193], v[192:193], s[72:73] op_sel_hi:[1,0]\nv_pk_mul_f32 v[194:195], v[194:195], s[72:73] op_sel_hi:[1,0]\nv_pk_mul_f32 v[196:197], v[196:197], s[72:73] op_sel_hi:[1,0]\nv_pk_mul_f32 v[198:199], v[198:199], s[72:73] op_sel_hi:[1,0]\nQk 192,192,193\n"
"Qk 193,194,195\nQk 194,196,197\nQk 195,198,199\nv_accvgpr_write_b32 a56, v192\nv_accvgpr_write_b32 a57, v193\nv_accvgpr_write_b32 a58, v194\nv_accvgpr_write_b32 a59, v195\nv_pk_mul_f32 v[200:201], v[200:201], s[72:73] op_sel_hi:[1,0]\n"
"v_pk_mul_f32 v[202:203], v[202:203], s[72:73] op_sel_hi:[1,0]\nv_pk_mul_f32 v[204:205], v[204:205], s[72:73] op_sel_hi:[1,0]\nv_pk_mul_f32 v[206:207], v[206:207], s[72:73] op_sel_hi:[1,0]\nQk 200,200,201\nQk 201,202,203\nQk 202,204,205\nQk 203,206,207\nv_accvgpr_write_b32 a60, v200\n"
"v_accvgpr_write_b32 a61, v201\nv_accvgpr_write_b32 a62, v202\nv_accvgpr_write_b32 a63, v203\nv_pk_mul_f32 v[208:209], v[208:209], s[72:73] op_sel_hi:[1,0]\nv_pk_mul_f32 v[210:211], v[210:211], s[72:73] op_sel_hi:[1,0]\nv_pk_mul_f32 v[212:213], v[212:213], s[72:73] op_sel_hi:[1,0]\nv_pk_mul_f32 v[214:215], v[214:215], s[72:73] op_sel_hi:[1,0]\nQk 208,208,209\n"
"Qk 209,210,211\nQk 210,212,213\nQk 211,214,215\nv_accvgpr_write_b32 a64, v208\nv_accvgpr_write_b32 a65, v209\nv_accvgpr_write_b32 a66, v210\nv_accvgpr_write_b32 a67, v211\nv_pk_mul_f32 v[216:217], v[216:217], s[72:73] op_sel_hi:[1,0]\n"
"v_pk_mul_f32 v[218:219], v[218:219], s[72:73] op_sel_hi:[1,0]\nv_pk_mul_f32 v[220:221], v[220:221], s[72:73] op_sel_hi:[1,0]\nv_pk_mul_f32 v[222:223], v[222:223], s[72:73] op_sel_hi:[1,0]\nQk 216,216,217\nQk 217,218,219\nQk 218,220,221\nQk 219,222,223\nv_accvgpr_write_b32 a68, v216\n"
"v_accvgpr_write_b32 a69, v217\nv_accvgpr_write_b32 a70, v218\nv_accvgpr_write_b32 a71, v219\nv_pk_mul_f32 v[224:225], v[224:225], s[72:73] op_sel_hi:[1,0]\n.Lpg4:\ns_cbranch_execz .Ltr9\nv_pk_mul_f32 v[226:227], v[226:227], s[72:73] op_sel_hi:[1,0]\nv_pk_mul_f32 v[228:229], v[228:229], s[72:73] op_sel_hi:[1,0]\n"
"v_pk_mul_f32 v[230:231], v[230:231], s[72:73] op_sel_hi:[1,0]\nQk 224,224,225\nQk 225,226,227\nQk 226,228,229\nQk 227,230,231\nv_accvgpr_write_b32 a72, v224\nv_accvgpr_write_b32 a73, v225\nv_accvgpr_write_b32 a74, v226\n"
"v_accvgpr_write_b32 a75, v227\nv_pk_mul_f32 v[232:233], v[232:233], s[72:73] op_sel_hi:[1,0]\nv_pk_mul_f32 v[234:235], v[234:235], s[72:73] op_sel_hi:[1,0]\nv_pk_mul_f32 v[236:237], v[236:237], s[72:73] op_sel_hi:[1,0]\nv_pk_mul_f32 v[238:239], v[238:239], s[72:73] op_sel_hi:[1,0]\nQk 232,232,233\nQk 233,234,235\nQk 234,236,237\n"
"Qk 235,238,239\nv_accvgpr_write_b32 a76, v232\nv_accvgpr_write_b32 a77, v233\nv_accvgpr_write_b32 a78, v234\nv_accvgpr_write_b32 a79, v235\nv_pk_mul_f32 v[240:241], v[240:241], s[72:73] op_sel_hi:[1,0]\nv_pk_mul_f32 v[242:243], v[242:243], s[72:73] op_sel_hi:[1,0]\nv_pk_mul_f32 v[244:245], v[244:245], s[72:73] op_sel_hi:[1,0]\n"
"v_pk_mul_f32 v[246:247], v[246:247], s[72:73] op_sel_hi:[1,0]\nQk 240,240,241\nQk 241,242,243\nQk 242,244,245\nQk 243,246,247\nv_accvgpr_write_b32 a80, v240\nv_accvgpr_write_b32 a81, v241\nv_accvgpr_write_b32 a82, v242\n"
"v_accvgpr_write_b32 a83, v243\nv_pk_mul_f32 v[248:249], v[248:249], s[72:73] op_sel_hi:[1,0]\nv_pk_mul_f32 v[250:251], v[250:251], s[72:73] op_sel_hi:[1,0]\nv_pk_mul_f32 v[252:253], v[252:253], s[72:73] op_sel_hi:[1,0]\nv_pk_mul_f32 v[254:255], v[254:255], s[72:73] op_sel_hi:[1,0]\nQk 248,248,249\nQk 249,250,251\nQk 250,252,253\n"
"Qk 251,254,255\nv_accvgpr_write_b32 a84, v248\nv_accvgpr_write_b32 a85, v249\nv_accvgpr_write_b32 a86, v250\nv_accvgpr_write_b32 a87, v251\nv_accvgpr_read_b32 v0, a208\nv_accvgpr_read_b32 v1, a209\nv_accvgpr_read_b32 v2, a210\n"
"v_accvgpr_read_b32 v3, a211\nv_accvgpr_read_b32 v4, a212\nv_accvgpr_read_b32 v5, a213\nv_accvgpr_read_b32 v6, a214\nv_accvgpr_read_b32 v7, a215\nv_pk_mul_f32 v[0:1], v[0:1], s[72:73] op_sel_hi:[1,0]\nv_pk_mul_f32 v[2:3], v[2:3], s[72:73] op_sel_hi:[1,0]\nv_pk_mul_f32 v[4:5], v[4:5], s[72:73] op_sel_hi:[1,0]\n"
"v_pk_mul_f32 v[6:7], v[6:7], s[72:73] op_sel_hi:[1,0]\nQk 0,0,1\nQk 1,2,3\nQk 2,4,5\nQk 3,6,7\nv_accvgpr_write_b32 a88, v0\nv_accvgpr_write_b32 a89, v1\nv_accvgpr_write_b32 a90, v2\n"
"v_accvgpr_write_b32 a91, v3\nv_accvgpr_read_b32 v8, a216\nv_accvgpr_read_b32 v9, a217\nv_accvgpr_read_b32 v10, a218\nv_accvgpr_read_b32 v11, a219\nv_accvgpr_read_b32 v12, a220\nv_accvgpr_read_b32 v13, a221\nv_accvgpr_read_b32 v14, a222\n"
"v_accvgpr_read_b32 v15, a223\nv_pk_mul_f32 v[8:9], v[8:9], s[72:73] op_sel_hi:[1,0]\nv_pk_mul_f32 v[10:11], v[10:11], s[72:73] op_sel_hi:[1,0]\nv_pk_mul_f32 v[12:13], v[12:13], s[72:73] op_sel_hi:[1,0]\nv_pk_mul_f32 v[14:15], v[14:15], s[72:73] op_sel_hi:[1,0]\nQk 8,8,9\nQk 9,10,11\nQk 10,12,13\n"
"Qk 11,14,15\nv_accvgpr_write_b32 a92, v8\nv_accvgpr_write_b32 a93, v9\nv_accvgpr_write_b32 a94, v10\nv_accvgpr_write_b32 a95, v11\nv_accvgpr_read_b32 v0, a224\nv_accvgpr_read_b32 v1, a225\nv_accvgpr_read_b32 v2, a226\n"
"v_accvgpr_read_b32 v3, a227\nv_accvgpr_read_b32 v4, a228\nv_accvgpr_read_b32 v5, a229\nv_accvgpr_read_b32 v6, a230\nv_accvgpr_read_b32 v7, a231\nv_pk_mul_f32 v[0:1], v[0:1], s[72:73] op_sel_hi:[1,0]\nv_pk_mul_f32 v[2:3], v[2:3], s[72:73] op_sel_hi:[1,0]\nv_pk_mul_f32 v[4:5], v[4:5], s[72:73] op_sel_hi:[1,0]\n"
"v_pk_mul_f32 v[6:7], v[6:7], s[72:73] op_sel_hi:[1,0]\nQk 0,0,1\nQk 1,2,3\nQk 2,4,5\nQk 3,6,7\nv_accvgpr_write_b32 a96, v0\nv_accvgpr_write_b32 a97, v1\nv_accvgpr_write_b32 a98, v2\n"
"v_accvgpr_write_b32 a99, v3\nv_accvgpr_read_b32 v8, a232\nv_accvgpr_read_b32 v9, a233\nv_accvgpr_read_b32 v10, a234\nv_accvgpr_read_b32 v11, a235\nv_accvgpr_read_b32 v12, a236\nv_accvgpr_read_b32 v13, a237\nv_accvgpr_read_b32 v14, a238\n"
"v_accvgpr_read_b32 v15, a239\nv_pk_mul_f32 v[8:9], v[8:9], s[72:73] op_sel_hi:[1,0]\nv_pk_mul_f32 v[10:11], v[10:11], s[72:73] op_sel_hi:[1,0]\nv_pk_mul_f32 v[12:13], v[12:13], s[72:73] op_sel_hi:[1,0]\nv_pk_mul_f32 v[14:15], v[14:15], s[72:73] op_sel_hi:[1,0]\nQk 8,8,9\nQk 9,10,11\nQk 10,12,13\n"
"Qk 11,14,15\nv_accvgpr_write_b32 a100, v8\nv_accvgpr_write_b32 a101, v9\nv_accvgpr_write_b32 a102, v10\nv_accvgpr_write_b32 a103, v11\nv_accvgpr_read_b32 v0, a240\nv_accvgpr_read_b32 v1, a241\nv_accvgpr_read_b32 v2, a242\n"
"v_accvgpr_read_b32 v3, a243\nv_accvgpr_read_b32 v4, a244\nv_accvgpr_read_b32 v5, a245\nv_accvgpr_read_b32 v6, a246\nv_accvgpr_read_b32 v7, a247\nv_pk_mul_f32 v[0:1], v[0:1], s[72:73] op_sel_hi:[1,0]\nv_pk_mul_f32 v[2:3], v[2:3], s[72:73] op_sel_hi:[1,0]\nv_pk_mul_f32 v[4:5], v[4:5], s[72:73] op_sel_hi:[1,0]\n"
"v_pk_mul_f32 v[6:7], v[6:7], s[72:73] op_sel_hi:[1,0]\nQk 0,0,1\nQk 1,2,3\nQk 2,4,5\nQk 3,6,7\nv_accvgpr_write_b32 a104, v0\nv_accvgpr_write_b32 a105, v1\nv_accvgpr_write_b32 a106, v2\n"
"v_accvgpr_write_b32 a107, v3\nv_accvgpr_read_b32 v8, a248\nv_accvgpr_read_b32 v9, a249\nv_accvgpr_read_b32 v10, a250\nv_accvgpr_read_b32 v11, a251\nv_accvgpr_read_b32 v12, a252\nv_accvgpr_read_b32 v13, a253\nv_accvgpr_read_b32 v14, a254\n"
"v_accvgpr_read_b32 v15, a255\nv_pk_mul_f32 v[8:9], v[8:9], s[72:73] op_sel_hi:[1,0]\nv_pk_mul_f32 v[10:11], v[10:11], s[72:73] op_sel_hi:[1,0]\nv_pk_mul_f32 v[12:13], v[12:13], s[72:73] op_sel_hi:[1,0]\nv_pk_mul_f32 v[14:15], v[14:15], s[72:73] op_sel_hi:[1,0]\nQk 8,8,9\nQk 9,10,11\nQk 10,12,13\n"
"Qk 11,14,15\nv_accvgpr_write_b32 a108, v8\nv_accvgpr_write_b32 a109, v9\nv_accvgpr_write_b32 a110, v10\nv_accvgpr_write_b32 a111, v11\nv_mov_b32 v66, 0x3f803f80\nv_accvgpr_write_b32 a200, 0\nv_accvgpr_write_b32 a201, 0\n"
"v_accvgpr_write_b32 a202, 0\nv_accvgpr_write_b32 a203, 0\ns_mov_b32 vcc_lo, 0x33333333\ns_mov_b32 vcc_hi, 0xcccccccc\nv_cndmask_b32 v66, 0, v66, vcc\nv_accvgpr_write_b32 a204, v66\nv_accvgpr_write_b32 a205, v66\nv_accvgpr_write_b32 a206, v66\n"
"v_accvgpr_write_b32 a207, v66\ns_add_u32 s87, s87, 2048\ns_waitcnt lgkmcnt(8)\nQzvaa 0,112,0\nQmvaa 0,116,4\nQmvaa 0,120,8\nQmvaa 0,124,12\nQzvaa 4,140,12\n"
"Qmvaa 4,136,8\nQmvaa 4,132,4\nQmvaa 4,128,0\ns_nop 3\nv_max3_f32 v66, v0, v1, v2\ns_nop 2\nv_max3_f32 v66, v66, v3, v4\nv_max3_f32 v66, v66, v5, v6\n"
"v_max_f32 v66, v66, v7\nQzvaa 8,112,16\nv_mov_b32 v67, v66\nQmvaa 8,116,20\ns_nop 0\nv_permlane32_swap_b32 v66, v67\nv_max_f32 v66, v66, v67\nQmvaa 8,120,24\n"
"v_mov_b32 v67, v66\nQmvaa 8,124,28\ns_nop 0\nv_permlane16_swap_b32 v66, v67\nv_max_f32 v66, v66, v67\nQzvaa 12,140,28\ns_nop 0\nv_max_f32_dpp v66, v66, v66 row_ror:8 row_mask:0xf bank_mask:0xf\n"
"s_nop 1\nv_max_f32_dpp v66, v66, v66 row_ror:4 row_mask:0xf bank_mask:0xf\nQmvaa 12,136,24\ns_nop 0\nv_max_f32_dpp v66, v66, v66 row_ror:2 row_mask:0xf bank_mask:0xf\ns_nop 1\nv_max_f32_dpp v66, v66, v66 row_ror:1 row_mask:0xf bank_mask:0xf\nQmvaa 12,132,20\n"
"v_sub_f32 v56, 0xc1980000, v66\nv_mov_b32 v57, v56\nQmvaa 12,128,16\nv_mov_b32 v58, v56\nv_mov_b32 v59, v56\nQa 0,0,56\nQa 1,1,56\nQa 2,2,56\n"
"Qa 3,3,56\nQe 0\nQe 1\nQe 2\nQe 3\nQk 16,0,1\nQk 17,2,3\nQa 4,4,56\n"
"Qa 5,5,56\nQa 6,6,56\nQa 7,7,56\nQcvaa 0,112,32,56\nQe 4\nQmvaa 0,116,36\nQe 5\nQmvaa 0,120,40\n"
"Qe 6\nQe 7\nQmvaa 0,124,44\nQk 18,4,5\nQk 19,6,7\nQcvaa 4,140,44,56\nQa 8,8,56\nQa 9,9,56\n"
"Qmvaa 4,136,40\nQa 10,10,56\nQa 11,11,56\nQmvaa 4,132,36\nQa 12,12,56\nQa 13,13,56\nQmvaa 4,128,32\ns_waitcnt lgkmcnt(0)\n"
"Qa 14,14,56\nQzvva 96,16,148\nQa 15,15,56\nQzvva 100,16,156\nQe 8\nQzvva 104,16,164\nQe 9\nQe 10\n"
"Qe 11\nQe 12\nQe 13\nQe 14\nQe 15\nQzvva 108,16,172\nQk 20,8,9\nQk 21,10,11\n"
"Qk 22,12,13\nQk 23,14,15\ns_nop 1\nQzvva 140,20,172\nQe 0\nQzvva 136,20,164\nQe 1\nQzvva 132,20,156\n"
"Qe 2\nQzvva 128,20,148\nQe 3\nv_smfmac_f32_16x16x64_bf16 a[176:179], a[204:207], v[16:23], v77\nQk 24,0,1\nQcvaa 8,112,48,56\ns_add_u32 m0, s61, 99072\nQk 25,2,3\n"
"Qmvaa 8,116,52\nbuffer_load_dwordx4 v69, s[40:43], s60 offen nt lds\nQmvaa 8,120,56\nQe 4\nQmvaa 8,124,60\ns_or_b32 s65, s65, 0x1d8000\ns_add_u32 s65, s65, 2048\nQcvaa 12,140,60,56\n"
"s_andn2_b32 s65, s65, 0x1d8000\nQmvaa 12,136,56\nQe 5\nQmvaa 12,132,52\nbuffer_load_dwordx4 v69, s[40:43], s60 offen offset:1024 nt lds\nQmvaa 12,128,48\nQe 6\nQcvaa 0,128,64,56\n"
"s_memrealtime s[74:75]\nQk 26,4,5\nQmvaa 0,132,68\nQe 7\nQmvaa 0,136,72\nQk 27,6,7\nQmvaa 0,140,76\nbuffer_load_dwordx4 v69, s[40:43], s90 offen offset:2048 nt lds\n"
"Qcvaa 4,124,76,56\nQe 8\nQmvaa 4,120,72\nQe 9\nQmvaa 4,116,68\nQe 10\nQmvaa 4,112,64\nQe 11\n"
"buffer_load_dwordx4 v69, s[40:43], s90 offen offset:3072 nt lds\nQzvva 160,24,148\nQe 12\nQzvva 164,24,156\nQe 13\ns_add_u32 m0, s64, 99072\nQzvva 168,24,164\nbuffer_load_dwordx4 v72, s[44:47], s60 offen nt lds\n"
"Qe 14\nQe 15\nQzvva 172,24,172\nQk 28,8,9\nQk 29,10,11\nQk 30,12,13\nQk 31,14,15\ns_nop 1\n"
"Qzvva 204,28,172\nQe 0\nbuffer_load_dwordx4 v72, s[44:47], s60 offen offset:1024 nt lds\nQzvva 200,28,164\nQe 1\nQzvva 196,28,156\nQe 2\nQzvva 192,28,148\n"
"buffer_load_dwordx4 v72, s[44:47], s90 offen offset:2048 nt lds\nv_smfmac_f32_16x16x64_bf16 a[180:183], a[204:207], v[24:31], v77\nQk 34,0,1\nQcvaa 8,112,80,56\nQe 3\nQmvaa 8,116,84\nQk 35,2,3\nQmvaa 8,120,88\n"
"s_waitcnt vmcnt(15)\ns_barrier\nQmvaa 8,124,92\nbuffer_load_dwordx4 v72, s[44:47], s90 offen offset:3072 nt lds\nQcvaa 12,140,92,56\ns_waitcnt lgkmcnt(0)\nQmvaa 12,136,88\nQmvaa 12,132,84\n"
"s_add_u32 s77, s74, 270\nQmvaa 12,128,80\ns_xor_b32 s60, s65, %21\ns_add_u32 s90, s60, 260096\nQcvaa 0,128,96,56\nQda 128,68,34048\nQmvaa 0,132,100\nQda 132,68,34560\n"
"Qmvaa 0,136,104\nQda 136,68,34304\nQe 4\nQe 5\nQe 6\nQe 7\nQmvaa 0,140,108\nQda 140,68,34816\n"
"Qk 32,4,5\nQk 33,6,7\nQcvaa 4,124,108,56\nQda 124,68,33792\nQmvaa 4,120,104\nQda 120,68,33280\nQmvaa 4,116,100\nQda 116,68,33536\n"
"Qmvaa 4,112,96\nQda 112,68,33024\nQzvva 224,32,148\nQe 8\nQzvva 228,32,156\nQe 9\nQzvva 232,32,164\nQe 10\n"
"Qe 11\nQzvva 236,32,172\ns_waitcnt lgkmcnt(0)\nQk 36,8,9\nQk 37,10,11\nQcvaa 8,112,0,56\nQe 12\nQmvaa 8,116,4\n"
"v_add_u32 v68, 66048, v68\nQmvaa 8,120,8\nQe 13\nQe 14\nQe 15\nQmvaa 8,124,12\nQk 38,12,13\nQk 39,14,15\n"
"Qcvaa 12,140,12,56\nQe 0\nQmvaa 12,136,8\nQe 1\nQmvaa 12,132,4\nQe 2\nQmvaa 12,128,0\nQe 3\n"
"v_smfmac_f32_16x16x64_bf16 a[184:187], a[204:207], v[32:39], v77\nQk 42,0,1\nQzava 208,36,148\nQe 4\nQzava 212,36,156\nQe 5\nQzava 216,36,164\nQe 6\n"
"Qe 8\nQe 7\nQzava 220,36,172\nQe 9\nQk 43,2,3\nQk 40,4,5\nQk 41,6,7\nQta 168,70,768\n"
"Qta 170,70,1792\nQzava 252,40,172\nQe 10\nQta 172,70,33792\nQta 174,70,34816\nQzava 248,40,164\nQe 11\nQta 160,70,512\n"
"Qta 162,70,1536\nQzava 244,40,156\nQk 44,8,9\nQk 45,10,11\nQta 164,70,33536\nQta 166,70,34560\nQzava 240,40,148\nQe 12\n"
"Qcvaa 0,112,16,56\nQta 152,70,256\nQta 154,70,1280\nQmvaa 0,116,20\nQe 13\nQmvaa 0,120,24\nQta 156,70,33280\nQta 158,70,34304\n"
"Qmvaa 0,124,28\nQe 14\nQcvaa 4,140,28,56\nQta 144,70,0\nQta 146,70,1024\nQmvaa 4,136,24\nQe 15\nQmvaa 4,132,20\n"
"Qta 148,70,33024\nQta 150,70,34048\nQmvaa 4,128,16\nQk 46,12,13\nQk 47,14,15\nQcvaa 8,128,32,56\nv_add_u32 v75, 1, v75\nQmvaa 8,132,36\n"
"s_waitcnt vmcnt(8)\nds_write_b32 v73, v75\nQmvaa 8,136,40\nQe 0\nQmvaa 8,140,44\nQe 1\nQcvaa 12,124,44,56\nQe 2\n"
"Qmvaa 12,120,40\nQe 3\nQmvaa 12,116,36\nQe 4\nQmvaa 12,112,32\ns_waitcnt lgkmcnt(1)\nQk 48,0,1\nv_smfmac_f32_16x16x64_bf16 a[188:191], a[204:207], v[40:47], v77\n"
"Qk 49,2,3\nQzvva 80,16,144\nQe 5\nQmvva 80,44,148\nQe 6\nQzvva 84,16,152\nQe 7\nQmvva 84,44,156\n"
"Qk 50,4,5\nQk 51,6,7\nQzvva 88,16,160\nQe 8\nQmvva 88,44,164\nQe 9\nQzvva 92,16,168\nQe 10\n"
"Qmvva 92,44,172\nQe 11\nQzvva 124,20,168\nQk 54,8,9\nQk 55,10,11\nQmvva 124,48,172\nds_read_b32 v66, v74\nQzvva 120,20,160\n"
"Qe 12\nQmvva 120,48,164\nQe 13\nQzvva 116,20,152\nQe 14\nQmvva 116,48,156\nQe 15\nQzvva 112,20,144\n"
"s_waitcnt lgkmcnt(0)\nv_cmp_gt_u32 vcc, v75, v66\nQmvva 112,48,148\ns_cbranch_vccnz .Lsa1\n.Lra1:\nQk 52,12,13\nQcvaa 0,112,48,56\ns_add_u32 m0, s61, 0\n"
"Qk 53,14,15\nQmvaa 0,116,52\nbuffer_load_dwordx4 v69, s[40:43], s60 offen nt lds\nQmvaa 0,120,56\nQmvaa 0,124,60\ns_add_u32 s65, s65, 2048\nQcvaa 4,140,60,56\nQmvaa 4,136,56\n"
"Qmvaa 4,132,52\nbuffer_load_dwordx4 v69, s[40:43], s60 offen offset:1024 nt lds\nQmvaa 4,128,48\nQcvaa 8,128,64,56\ns_nop 0\nQmvaa 8,132,68\nQe 0\nQmvaa 8,136,72\n"
"Qe 1\nQmvaa 8,140,76\nbuffer_load_dwordx4 v69, s[40:43], s90 offen offset:2048 nt lds\nQcvaa 12,124,76,56\nQe 2\nQmvaa 12,120,72\nQe 3\nQmvaa 12,116,68\n"
"Qe 4\nQmvaa 12,112,64\nQe 5\nv_smfmac_f32_16x16x64_bf16 a[192:195], a[204:207], v[48:55], v77\nQk 20,0,1\nQzvva 144,24,144\nbuffer_load_dwordx4 v69, s[40:43], s90 offen offset:3072 nt lds\nQmvva 144,52,148\n"
"Qe 6\nQzvva 148,24,152\nQe 7\nQmvva 148,52,156\ns_add_u32 m0, s64, 0\nQk 21,2,3\nQzvva 152,24,160\nbuffer_load_dwordx4 v72, s[44:47], s60 offen nt lds\n"
"Qmvva 152,52,164\nQk 22,4,5\nQk 23,6,7\nQzvva 156,24,168\nQe 8\nQmvva 156,52,172\nQe 9\nQzvva 188,28,168\n"
"Qe 10\nQmvva 188,20,172\nbuffer_load_dwordx4 v72, s[44:47], s60 offen offset:1024 nt lds\nQzvva 184,28,160\nQe 11\nQmvva 184,20,164\nQk 26,8,9\n.Lpg5:\n"
"s_cbranch_execz .Ltr9\nQk 27,10,11\nQzvva 180,28,152\nQe 12\nQmvva 180,20,156\nQe 13\nQzvva 176,28,144\nbuffer_load_dwordx4 v72, s[44:47], s90 offen offset:2048 nt lds\n"
"Qmvva 176,20,148\nQe 14\nQcvaa 0,112,80,56\nQe 15\nQmvaa 0,116,84\nQk 24,12,13\nQk 25,14,15\nQmvaa 0,120,88\n"
"Qmvaa 0,124,92\nbuffer_load_dwordx4 v72, s[44:47], s90 offen offset:3072 nt lds\nQcvaa 4,140,92,56\nQmvaa 4,136,88\nQmvaa 4,132,84\nQmvaa 4,128,80\ns_xor_b32 s60, s65, %21\ns_add_u32 s90, s60, 260096\n"
"Qcvaa 8,128,96,56\nQda 128,68,1024\nQmvaa 8,132,100\nQda 132,68,1536\nQmvaa 8,136,104\nQda 136,68,1280\nQmvaa 8,140,108\nQda 140,68,1792\n"
"Qcvaa 12,124,108,56\nQda 124,68,768\nQmvaa 12,120,104\nQda 120,68,256\nQmvaa 12,116,100\nQda 116,68,512\nQmvaa 12,112,96\nQda 112,68,0\n"
"v_smfmac_f32_16x16x64_bf16 a[196:199], a[204:207], v[20:27], v77\nQzvva 208,32,144\nQe 0\nQmvva 208,24,148\nQe 1\nQzvva 212,32,152\nQe 2\nQmvva 212,24,156\n"
"Qe 3\nQzvva 216,32,160\nQk 28,0,1\nQk 29,2,3\nQmvva 216,24,164\nQe 4\nQzvva 220,32,168\nQe 5\n"
"Qmvva 220,24,172\ns_waitcnt lgkmcnt(0)\nQk 30,4,5\nQcvaa 0,112,0,56\nQe 6\nQmvaa 0,116,4\nQe 7\nQmvaa 0,120,8\n"
"Qk 31,6,7\nQmvaa 0,124,12\nQe 8\nQcvaa 4,140,12,56\nQe 9\nQmvaa 4,136,8\nQe 10\nQmvaa 4,132,4\n"
"Qe 11\nQmvaa 4,128,0\nQe 12\nQzvva 240,36,144\nQe 13\nQmvva 240,28,148\nQe 14\nQzvva 244,36,152\n"
"Qe 15\nQmvva 244,28,156\nQk 34,8,9\nQk 35,10,11\nQzvva 248,36,160\nQk 32,12,13\nQk 33,14,15\nQmvva 248,28,164\n"
"Qe 0\nQzvva 252,36,168\nQe 1\nQmvva 252,28,172\nQe 2\nQzava 236,40,168\nQta 168,70,33920\nQta 170,70,34944\n"
"Qmava 236,32,172\nQe 3\nQzava 232,40,160\nQta 172,71,896\nQta 174,71,1920\nQmava 232,32,164\nQk 36,0,1\nQk 37,2,3\n"
"Qzava 228,40,152\nQta 160,70,33664\nQta 162,70,34688\nQmava 228,32,156\nQe 4\nQzava 224,40,144\nQta 164,71,640\nQta 166,71,1664\n"
"Qmava 224,32,148\nQe 5\nv_smfmac_f32_16x16x64_bf16 a[200:203], a[204:207], v[28:35], v77\nQta 152,70,33408\nQta 154,70,34432\nQcvaa 8,112,16,56\nQe 6\nQmvaa 8,116,20\n"
"Qta 156,71,384\nQta 158,71,1408\nQmvaa 8,120,24\nQe 7\nQmvaa 8,124,28\nQta 144,70,33152\nQta 146,70,34176\nQcvaa 12,140,28,56\n"
"Qk 38,4,5\nQk 39,6,7\nQmvaa 12,136,24\nQta 148,71,128\nQta 150,71,1152\nQmvaa 12,132,20\nQmvaa 12,128,16\ns_lshl_b32 s66, s67, 3\n"
".Lloop:\ns_bitcmp1_b32 s99, s66\ns_cbranch_scc1 .Lhook\n.Lhookret:\nQcvaa 0,128,32,56\nv_add_u32 v75, 1, v75\nQmvaa 0,132,36\ns_waitcnt vmcnt(8)\n"
"ds_write_b32 v73, v75\nQmvaa 0,136,40\nQe 8\nQmvaa 0,140,44\nQe 9\nQcvaa 4,124,44,56\nQe 10\nQmvaa 4,120,40\n"
"Qe 11\nQmvaa 4,116,36\nQe 12\nQmvaa 4,112,32\ns_waitcnt lgkmcnt(1)\nQk 40,8,9\nQmvva 96,44,144\nQe 13\n"
"Qmvva 96,36,148\nQe 14\nQmvva 100,44,152\nQe 15\nQmvva 100,36,156\nQk 41,10,11\nQk 42,12,13\nQmvva 104,44,160\n"
"Qk 43,14,15\nQmvva 104,36,164\nQe 0\nQmvva 108,44,168\nQe 1\nQmvva 108,36,172\nQe 2\nQmvva 140,48,168\n"
"Qe 3\nQmvva 140,40,172\nQk 46,0,1\nQk 47,2,3\nQmvva 136,48,160\nds_read_b32 v66, v74\nQmvva 136,40,164\nQe 4\n"
"Qmvva 132,48,152\nQe 5\nQmvva 132,40,156\nQe 6\nQmvva 128,48,144\nQe 7\nQmvva 128,40,148\ns_waitcnt lgkmcnt(0)\n"
"v_cmp_gt_u32 vcc, v75, v66\nv_smfmac_f32_16x16x64_bf16 a[176:179], a[204:207], v[36:43], v77\ns_cbranch_vccnz .Lsa2\n.Lra2:\nQcvaa 8,112,48,56\ns_add_u32 m0, s61, 33024\nQk 44,4,5\nQmvaa 8,116,52\n"
"buffer_load_dwordx4 v69, s[40:43], s60 offen nt lds\nQmvaa 8,120,56\nQk 45,6,7\nQmvaa 8,124,60\ns_add_u32 s65, s65, 2048\nQcvaa 12,140,60,56\nQmvaa 12,136,56\nQmvaa 12,132,52\n"
"buffer_load_dwordx4 v69, s[40:43], s60 offen offset:1024 nt lds\nQmvaa 12,128,48\nQcvaa 0,128,64,56\ns_memrealtime s[74:75]\nQmvaa 0,132,68\nQe 8\nQmvaa 0,136,72\nQe 9\n"
"Qmvaa 0,140,76\nbuffer_load_dwordx4 v69, s[40:43], s90 offen offset:2048 nt lds\nQcvaa 4,124,76,56\nQe 10\nQmvaa 4,120,72\nQe 11\nQmvaa 4,116,68\nQe 12\n"
"Qmvaa 4,112,64\nQe 13\nQmvva 160,52,144\nbuffer_load_dwordx4 v69, s[40:43], s90 offen offset:3072 nt lds\nQmvva 160,44,148\nQe 14\nQmvva 164,52,152\nQe 15\n"
"Qmvva 164,44,156\nQk 48,8,9\nQk 49,10,11\nQmvva 168,52,160\ns_add_u32 m0, s64, 33024\nQk 50,12,13\nQmvva 168,44,164\nbuffer_load_dwordx4 v72, s[44:47], s60 offen nt lds\n"
"Qmvva 172,52,168\nQk 51,14,15\nQmvva 172,44,172\nQe 0\nQmvva 204,20,168\nQe 1\nQmvva 204,48,172\nQe 2\n"
"Qmvva 200,20,160\nbuffer_load_dwordx4 v72, s[44:47], s60 offen offset:1024 nt lds\nQmvva 200,48,164\nQe 3\nQmvva 196,20,152\nQk 18,0,1\nQk 19,2,3\nQmvva 196,48,156\n"
"Qe 4\nQmvva 192,20,144\nQe 5\nQmvva 192,48,148\nbuffer_load_dwordx4 v72, s[44:47], s90 offen offset:2048 nt lds\nv_smfmac_f32_16x16x64_bf16 a[180:183], a[204:207], v[44:51], v77\nQk 16,4,5\nQcvaa 8,112,80,56\n"
"Qe 6\nQmvaa 8,116,84\nQe 7\nQmvaa 8,120,88\nQk 17,6,7\nQmvaa 8,124,92\nbuffer_load_dwordx4 v72, s[44:47], s90 offen offset:3072 nt lds\nQcvaa 12,140,92,56\n"
"s_waitcnt lgkmcnt(0)\nQmvaa 12,136,88\ns_cmp_gt_u32 s77, s74\ns_cbranch_scc1 .Lsp2\n.Lrp2:\nQmvaa 12,132,84\ns_add_u32 s77, s74, 239\nQmvaa 12,128,80\n"
"s_xor_b32 s60, s65, %21\ns_add_u32 s90, s60, 260096\nQcvaa 0,128,96,56\nQda 128,68,34048\nQmvaa 0,132,100\nQda 132,68,34560\nQmvaa 0,136,104\nQda 136,68,34304\n"
"Qmvaa 0,140,108\nQda 140,68,34816\nQcvaa 4,124,108,56\nQda 124,68,33792\nQmvaa 4,120,104\nQda 120,68,33280\nQmvaa 4,116,100\nQda 116,68,33536\n"
"Qmvaa 4,112,96\nQda 112,68,33024\nQmvva 224,24,144\nQe 8\nQmvva 224,16,148\nQe 9\nQmvva 228,24,152\nQe 10\n"
"Qmvva 228,16,156\nQe 11\nQmvva 232,24,160\nQk 20,8,9\nQk 21,10,11\nQmvva 232,16,164\nQe 12\nQmvva 236,24,168\n"
"Qe 13\nQmvva 236,16,172\ns_waitcnt lgkmcnt(0)\nQk 22,12,13\nQcvaa 8,112,0,56\nQe 14\nQmvaa 8,116,4\nv_subrev_u32 v68, 66048, v68\n"
"Qmvaa 8,120,8\nQe 15\nQmvaa 8,124,12\nQk 23,14,15\nQcvaa 12,140,12,56\nQe 0\nQmvaa 12,136,8\nQe 1\n"
"Qmvaa 12,132,4\nQe 2\nQmvaa 12,128,0\nQe 3\nv_smfmac_f32_16x16x64_bf16 a[184:187], a[204:207], v[16:23], v77\nQk 26,0,1\nQmava 208,28,144\nQe 4\n"
"Qmava 208,20,148\nQe 5\nQmava 212,28,152\nQe 6\nQmava 212,20,156\nQe 7\nQmava 216,28,160\nQk 27,2,3\n"
"Qk 24,4,5\nQmava 216,20,164\nQk 25,6,7\nQmava 220,28,168\nQe 8\nQmava 220,20,172\nQe 9\nQmava 252,32,168\n"
"Qta 168,71,768\nQta 170,71,1792\nQmava 252,24,172\nQe 10\nQmava 248,32,160\nQta 172,71,33792\nQta 174,71,34816\nQmava 248,24,164\n"
"Qe 11\nQmava 244,32,152\nQta 160,71,512\nQta 162,71,1536\nQmava 244,24,156\nQk 28,8,9\nQk 29,10,11\nQmava 240,32,144\n"
"Qta 164,71,33536\nQta 166,71,34560\nQmava 240,24,148\nQe 12\nQcvaa 0,112,16,56\nQta 152,71,256\nQta 154,71,1280\nQmvaa 0,116,20\n"
"Qe 13\nQmvaa 0,120,24\nQta 156,71,33280\nQta 158,71,34304\nQmvaa 0,124,28\nQe 14\nQcvaa 4,140,28,56\nQta 144,71,0\n"
"Qta 146,71,1024\nQmvaa 4,136,24\nQe 15\nQmvaa 4,132,20\nQta 148,71,33024\nQta 150,71,34048\nQmvaa 4,128,16\nQk 30,12,13\n"
"Qk 31,14,15\nQcvaa 8,128,32,56\nv_add_u32 v75, 1, v75\nQmvaa 8,132,36\ns_waitcnt vmcnt(8)\nds_write_b32 v73, v75\nQmvaa 8,136,40\nQe 0\n"
"Qmvaa 8,140,44\nQe 1\nQcvaa 12,124,44,56\nQe 2\nQmvaa 12,120,40\nQe 3\nQmvaa 12,116,36\nQe 4\n"
"Qmvaa 12,112,32\ns_waitcnt lgkmcnt(1)\nQk 32,0,1\nv_smfmac_f32_16x16x64_bf16 a[188:191], a[204:207], v[24:31], v77\nQk 33,2,3\nQmvva 80,36,144\nQe 5\nQmvva 80,28,148\n"
"Qe 6\nQmvva 84,36,152\nQe 7\nQmvva 84,28,156\nQk 34,4,5\nQk 35,6,7\nQmvva 88,36,160\nQe 8\n"
"Qmvva 88,28,164\nQe 9\nQmvva 92,36,168\nQe 10\nQmvva 92,28,172\nQe 11\nQmvva 124,40,168\nQk 38,8,9\n"
"Qk 39,10,11\nQmvva 124,32,172\nds_read_b32 v66, v74\nQmvva 120,40,160\nQe 12\nQmvva 120,32,164\nQe 13\nQmvva 116,40,152\n"
"Qe 14\nQmvva 116,32,156\nQe 15\nQmvva 112,40,144\ns_waitcnt lgkmcnt(0)\nv_cmp_gt_u32 vcc, v75, v66\nQmvva 112,32,148\ns_cbranch_vccnz .Lsa3\n"
".Lra3:\nQk 36,12,13\nQcvaa 0,112,48,56\ns_add_u32 m0, s61, 66048\nQk 37,14,15\nQmvaa 0,116,52\nbuffer_load_dwordx4 v69, s[40:43], s60 offen nt lds\nQmvaa 0,120,56\n"
"Qmvaa 0,124,60\ns_add_u32 s65, s65, 2048\nQcvaa 4,140,60,56\nQmvaa 4,136,56\nQmvaa 4,132,52\nbuffer_load_dwordx4 v69, s[40:43], s60 offen offset:1024 nt lds\nQmvaa 4,128,48\nQcvaa 8,128,64,56\n"
"s_nop 0\nQmvaa 8,132,68\nQe 0\nQmvaa 8,136,72\nQe 1\nQmvaa 8,140,76\nbuffer_load_dwordx4 v69, s[40:43], s90 offen offset:2048 nt lds\nQcvaa 12,124,76,56\n"
"Qe 2\nQmvaa 12,120,72\nQe 3\nQmvaa 12,116,68\nQe 4\nQmvaa 12,112,64\nQe 5\nv_smfmac_f32_16x16x64_bf16 a[192:195], a[204:207], v[32:39], v77\n"
"Qk 40,0,1\nQmvva 144,44,144\nbuffer_load_dwordx4 v69, s[40:43], s90 offen offset:3072 nt lds\nQmvva 144,36,148\nQe 6\nQmvva 148,44,152\nQe 7\nQmvva 148,36,156\n"
"s_add_u32 m0, s64, 66048\nQk 41,2,3\nQmvva 152,44,160\nbuffer_load_dwordx4 v72, s[44:47], s60 offen nt lds\nQmvva 152,36,164\nQk 42,4,5\nQk 43,6,7\nQmvva 156,44,168\n"
"Qe 8\nQmvva 156,36,172\nQe 9\nQmvva 188,48,168\nQe 10\nQmvva 188,40,172\nbuffer_load_dwordx4 v72, s[44:47], s60 offen offset:1024 nt lds\nQmvva 184,48,160\n"
"Qe 11\nQmvva 184,40,164\nQk 46,8,9\nQk 47,10,11\nQmvva 180,48,152\nQe 12\nQmvva 180,40,156\nQe 13\n"
"Qmvva 176,48,144\nbuffer_load_dwordx4 v72, s[44:47], s90 offen offset:2048 nt lds\nQmvva 176,40,148\nQe 14\nQcvaa 0,112,80,56\nQe 15\nQmvaa 0,116,84\nQk 44,12,13\n"
"Qk 45,14,15\nQmvaa 0,120,88\nQmvaa 0,124,92\nbuffer_load_dwordx4 v72, s[44:47], s90 offen offset:3072 nt lds\nQcvaa 4,140,92,56\nQmvaa 4,136,88\nQmvaa 4,132,84\nQmvaa 4,128,80\n"
"s_xor_b32 s60, s65, %21\ns_add_u32 s90, s60, 260096\nQcvaa 8,128,96,56\nQda 128,68,1024\nQmvaa 8,132,100\nQda 132,68,1536\nQmvaa 8,136,104\nQda 136,68,1280\n"
"Qmvaa 8,140,108\nQda 140,68,1792\nQcvaa 12,124,108,56\nQda 124,68,768\nQmvaa 12,120,104\nQda 120,68,256\nQmvaa 12,116,100\nQda 116,68,512\n"
"Qmvaa 12,112,96\nQda 112,68,0\nv_smfmac_f32_16x16x64_bf16 a[196:199], a[204:207], v[40:47], v77\nQmvva 208,16,144\nQe 0\nQmvva 208,44,148\nQe 1\nQmvva 212,16,152\n"
"Qe 2\nQmvva 212,44,156\nQe 3\nQmvva 216,16,160\nQk 48,0,1\nQk 49,2,3\nQmvva 216,44,164\nQe 4\n"
"Qmvva 220,16,168\nQe 5\nQmvva 220,44,172\ns_waitcnt lgkmcnt(0)\nQk 50,4,5\nQcvaa 0,112,0,56\nQe 6\nQmvaa 0,116,4\n"
"Qe 7\nQmvaa 0,120,8\nQk 51,6,7\nQmvaa 0,124,12\nQe 8\nQcvaa 4,140,12,56\nQe 9\nQmvaa 4,136,8\n"
"Qe 10\nQmvaa 4,132,4\nQe 11\nQmvaa 4,128,0\nQe 12\nQmvva 240,20,144\nQe 13\nQmvva 240,48,148\n"
"Qe 14\nQmvva 244,20,152\nQe 15\nQmvva 244,48,156\nQk 54,8,9\nQk 55,10,11\nQmvva 248,20,160\nQk 52,12,13\n"
"Qk 53,14,15\nQmvva 248,48,164\nQe 0\nQmvva 252,20,168\nQe 1\n.Lpg6:\ns_cbranch_execz .Ltr9\nQmvva 252,48,172\n"
"Qe 2\nQmava 236,24,168\nQta 168,71,33920\nQta 170,71,34944\nQmava 236,52,172\nQe 3\nQmava 232,24,160\nQta 172,70,896\n"
"Qta 174,70,1920\nQmava 232,52,164\nQk 16,0,1\nQk 17,2,3\nQmava 228,24,152\nQta 160,71,33664\nQta 162,71,34688\nQmava 228,52,156\n"
"Qe 4\nQmava 224,24,144\nQta 164,70,640\nQta 166,70,1664\nQmava 224,52,148\nQe 5\nv_smfmac_f32_16x16x64_bf16 a[200:203], a[204:207], v[48:55], v77\nQta 152,71,33408\n"
"Qta 154,71,34432\nQcvaa 8,112,16,56\nQe 6\nQmvaa 8,116,20\nQta 156,70,384\nQta 158,70,1408\nQmvaa 8,120,24\nQe 7\n"
"Qmvaa 8,124,28\nQta 144,71,33152\nQta 146,71,34176\nQcvaa 12,140,28,56\nQk 18,4,5\nQk 19,6,7\nQmvaa 12,136,24\nQta 148,70,128\n"
"Qta 150,70,1152\nQmvaa 12,132,20\nQmvaa 12,128,16\nQcvaa 0,128,32,56\nv_add_u32 v75, 1, v75\nQmvaa 0,132,36\ns_waitcnt vmcnt(8)\nds_write_b32 v73, v75\n"
"Qmvaa 0,136,40\nQe 8\nQmvaa 0,140,44\nQe 9\nQcvaa 4,124,44,56\nQe 10\nQmvaa 4,120,40\nQe 11\n"
"Qmvaa 4,116,36\nQe 12\nQmvaa 4,112,32\ns_waitcnt lgkmcnt(1)\nQk 20,8,9\nQmvva 96,28,144\nQe 13\nQmvva 96,16,148\n"
"Qe 14\nQmvva 100,28,152\nQe 15\nQmvva 100,16,156\nQk 21,10,11\nQk 22,12,13\nQmvva 104,28,160\nQk 23,14,15\n"
"Qmvva 104,16,164\nQe 0\nQmvva 108,28,168\nQe 1\nQmvva 108,16,172\nQe 2\nQmvva 140,32,168\nQe 3\n"
"Qmvva 140,20,172\nQk 26,0,1\nQk 27,2,3\nQmvva 136,32,160\nds_read_b32 v66, v74\nQmvva 136,20,164\nQe 4\nQmvva 132,32,152\n"
"Qe 5\nQmvva 132,20,156\nQe 6\nQmvva 128,32,144\nQe 7\nQmvva 128,20,148\ns_waitcnt lgkmcnt(0)\nv_cmp_gt_u32 vcc, v75, v66\n"
"v_smfmac_f32_16x16x64_bf16 a[176:179], a[204:207], v[16:23], v77\ns_cbranch_vccnz .Lsa4\n.Lra4:\nQcvaa 8,112,48,56\ns_add_u32 m0, s61, 99072\nQk 24,4,5\nQmvaa 8,116,52\nbuffer_load_dwordx4 v69, s[40:43], s60 offen nt lds\n"
"Qmvaa 8,120,56\nQk 25,6,7\nQmvaa 8,124,60\ns_or_b32 s65, s65, 0x1d8000\ns_add_u32 s65, s65, 2048\nQcvaa 12,140,60,56\ns_andn2_b32 s65, s65, 0x1d8000\nQmvaa 12,136,56\n"
"Qmvaa 12,132,52\nbuffer_load_dwordx4 v69, s[40:43], s60 offen offset:1024 nt lds\nQmvaa 12,128,48\nQcvaa 0,128,64,56\ns_memrealtime s[74:75]\nQmvaa 0,132,68\nQe 8\nQmvaa 0,136,72\n"
"Qe 9\nQmvaa 0,140,76\nbuffer_load_dwordx4 v69, s[40:43], s90 offen offset:2048 nt lds\nQcvaa 4,124,76,56\nQe 10\nQmvaa 4,120,72\nQe 11\nQmvaa 4,116,68\n"
"Qe 12\nQmvaa 4,112,64\nQe 13\nQmvva 160,36,144\nbuffer_load_dwordx4 v69, s[40:43], s90 offen offset:3072 nt lds\nQmvva 160,24,148\nQe 14\nQmvva 164,36,152\n"
"Qe 15\nQmvva 164,24,156\nQk 28,8,9\nQk 29,10,11\nQmvva 168,36,160\ns_add_u32 m0, s64, 99072\nQk 30,12,13\nQmvva 168,24,164\n"
"buffer_load_dwordx4 v72, s[44:47], s60 offen nt lds\nQmvva 172,36,168\nQk 31,14,15\nQmvva 172,24,172\nQe 0\nQmvva 204,40,168\nQe 1\nQmvva 204,28,172\n"
"Qe 2\nQmvva 200,40,160\nbuffer_load_dwordx4 v72, s[44:47], s60 offen offset:1024 nt lds\nQmvva 200,28,164\nQe 3\nQmvva 196,40,152\nQk 34,0,1\nQk 35,2,3\n"
"Qmvva 196,28,156\nQe 4\nQmvva 192,40,144\nQe 5\nQmvva 192,28,148\nbuffer_load_dwordx4 v72, s[44:47], s90 offen offset:2048 nt lds\nv_smfmac_f32_16x16x64_bf16 a[180:183], a[204:207], v[24:31], v77\nQk 32,4,5\n"
"Qcvaa 8,112,80,56\nQe 6\nQmvaa 8,116,84\nQe 7\nQmvaa 8,120,88\nQk 33,6,7\nQmvaa 8,124,92\nbuffer_load_dwordx4 v72, s[44:47], s90 offen offset:3072 nt lds\n"
"Qcvaa 12,140,92,56\ns_waitcnt lgkmcnt(0)\nQmvaa 12,136,88\ns_cmp_gt_u32 s77, s74\ns_cbranch_scc1 .Lsp4\n.Lrp4:\nQmvaa 12,132,84\ns_add_u32 s77, s74, 239\n"
"Qmvaa 12,128,80\ns_xor_b32 s60, s65, %21\ns_add_u32 s90, s60, 260096\nQcvaa 0,128,96,56\nQda 128,68,34048\nQmvaa 0,132,100\nQda 132,68,34560\nQmvaa 0,136,104\n"
"Qda 136,68,34304\nQmvaa 0,140,108\nQda 140,68,34816\nQcvaa 4,124,108,56\nQda 124,68,33792\nQmvaa 4,120,104\nQda 120,68,33280\nQmvaa 4,116,100\n"
"Qda 116,68,33536\nQmvaa 4,112,96\nQda 112,68,33024\nQmvva 224,44,144\nQe 8\nQmvva 224,32,148\nQe 9\nQmvva 228,44,152\n"
"Qe 10\nQmvva 228,32,156\nQe 11\nQmvva 232,44,160\nQk 36,8,9\nQk 37,10,11\nQmvva 232,32,164\nQe 12\n"
"Qmvva 236,44,168\nQe 13\nQmvva 236,32,172\ns_waitcnt lgkmcnt(0)\nQk 38,12,13\nQcvaa 8,112,0,56\nQe 14\nQmvaa 8,116,4\n"
"v_add_u32 v68, 66048, v68\nQmvaa 8,120,8\nQe 15\nQmvaa 8,124,12\nQk 39,14,15\nQcvaa 12,140,12,56\nQe 0\nQmvaa 12,136,8\n"
"Qe 1\nQmvaa 12,132,4\nQe 2\nQmvaa 12,128,0\nQe 3\nv_smfmac_f32_16x16x64_bf16 a[184:187], a[204:207], v[32:39], v77\nQk 42,0,1\nQmava 208,48,144\n"
"Qe 4\nQmava 208,36,148\nQe 5\nQmava 212,48,152\nQe 6\nQmava 212,36,156\nQe 7\nQmava 216,48,160\n"
"Qk 43,2,3\nQk 40,4,5\nQmava 216,36,164\nQk 41,6,7\nQmava 220,48,168\nQe 8\nQmava 220,36,172\nQe 9\n"
"Qmava 252,52,168\nQta 168,70,768\nQta 170,70,1792\nQmava 252,40,172\nQe 10\nQmava 248,52,160\nQta 172,70,33792\nQta 174,70,34816\n"
"Qmava 248,40,164\nQe 11\nQmava 244,52,152\nQta 160,70,512\nQta 162,70,1536\nQmava 244,40,156\nQk 44,8,9\nQk 45,10,11\n"
"Qmava 240,52,144\nQta 164,70,33536\nQta 166,70,34560\nQmava 240,40,148\nQe 12\nQcvaa 0,112,16,56\nQta 152,70,256\nQta 154,70,1280\n"
"Qmvaa 0,116,20\nQe 13\nQmvaa 0,120,24\nQta 156,70,33280\nQta 158,70,34304\nQmvaa 0,124,28\nQe 14\nQcvaa 4,140,28,56\n"
"Qta 144,70,0\nQta 146,70,1024\nQmvaa 4,136,24\nQe 15\nQmvaa 4,132,20\nQta 148,70,33024\nQta 150,70,34048\nQmvaa 4,128,16\n"
"Qk 46,12,13\nQk 47,14,15\nQcvaa 8,128,32,56\nv_add_u32 v75, 1, v75\nQmvaa 8,132,36\ns_waitcnt vmcnt(8)\nds_write_b32 v73, v75\nQmvaa 8,136,40\n"
"Qe 0\nQmvaa 8,140,44\nQe 1\nQcvaa 12,124,44,56\nQe 2\nQmvaa 12,120,40\nQe 3\nQmvaa 12,116,36\n"
"Qe 4\nQmvaa 12,112,32\ns_waitcnt lgkmcnt(1)\nQk 48,0,1\nv_smfmac_f32_16x16x64_bf16 a[188:191], a[204:207], v[40:47], v77\nQk 49,2,3\nQmvva 80,16,144\nQe 5\n"
"Qmvva 80,44,148\nQe 6\nQmvva 84,16,152\nQe 7\nQmvva 84,44,156\nQk 50,4,5\nQk 51,6,7\nQmvva 88,16,160\n"
"Qe 8\nQmvva 88,44,164\nQe 9\nQmvva 92,16,168\nQe 10\nQmvva 92,44,172\nQe 11\nQmvva 124,20,168\n"
"Qk 54,8,9\nQk 55,10,11\nQmvva 124,48,172\nds_read_b32 v66, v74\nQmvva 120,20,160\nQe 12\nQmvva 120,48,164\nQe 13\n"
"Qmvva 116,20,152\nQe 14\nQmvva 116,48,156\nQe 15\nQmvva 112,20,144\ns_waitcnt lgkmcnt(0)\nv_cmp_gt_u32 vcc, v75, v66\nQmvva 112,48,148\n"
"s_cbranch_vccnz .Lsa5\n.Lra5:\nQk 52,12,13\nQcvaa 0,112,48,56\ns_add_u32 m0, s61, 0\nQk 53,14,15\nQmvaa 0,116,52\nbuffer_load_dwordx4 v69, s[40:43], s60 offen nt lds\n"
"Qmvaa 0,120,56\nQmvaa 0,124,60\ns_add_u32 s65, s65, 2048\nQcvaa 4,140,60,56\nQmvaa 4,136,56\nQmvaa 4,132,52\nbuffer_load_dwordx4 v69, s[40:43], s60 offen offset:1024 nt lds\nQmvaa 4,128,48\n"
"Qcvaa 8,128,64,56\ns_nop 0\nQmvaa 8,132,68\nQe 0\nQmvaa 8,136,72\nQe 1\nQmvaa 8,140,76\nbuffer_load_dwordx4 v69, s[40:43], s90 offen offset:2048 nt lds\n"
"Qcvaa 12,124,76,56\nQe 2\nQmvaa 12,120,72\nQe 3\nQmvaa 12,116,68\nQe 4\nQmvaa 12,112,64\nQe 5\n"
"v_smfmac_f32_16x16x64_bf16 a[192:195], a[204:207], v[48:55], v77\nQk 20,0,1\nQmvva 144,24,144\nbuffer_load_dwordx4 v69, s[40:43], s90 offen offset:3072 nt lds\nQmvva 144,52,148\nQe 6\nQmvva 148,24,152\nQe 7\n"
"Qmvva 148,52,156\ns_add_u32 m0, s64, 0\nQk 21,2,3\nQmvva 152,24,160\nbuffer_load_dwordx4 v72, s[44:47], s60 offen nt lds\nQmvva 152,52,164\nQk 22,4,5\nQk 23,6,7\n"
"Qmvva 156,24,168\nQe 8\nQmvva 156,52,172\nQe 9\nQmvva 188,28,168\nQe 10\nQmvva 188,20,172\nbuffer_load_dwordx4 v72, s[44:47], s60 offen offset:1024 nt lds\n"
"Qmvva 184,28,160\nQe 11\nQmvva 184,20,164\nQk 26,8,9\nQk 27,10,11\nQmvva 180,28,152\nQe 12\nQmvva 180,20,156\n"
"Qe 13\nQmvva 176,28,144\nbuffer_load_dwordx4 v72, s[44:47], s90 offen offset:2048 nt lds\nQmvva 176,20,148\nQe 14\nQcvaa 0,112,80,56\nQe 15\nQmvaa 0,116,84\n"
"Qk 24,12,13\nQk 25,14,15\nQmvaa 0,120,88\nQmvaa 0,124,92\nbuffer_load_dwordx4 v72, s[44:47], s90 offen offset:3072 nt lds\nQcvaa 4,140,92,56\nQmvaa 4,136,88\nQmvaa 4,132,84\n"
"Qmvaa 4,128,80\ns_xor_b32 s60, s65, %21\ns_add_u32 s90, s60, 260096\nQcvaa 8,128,96,56\nQda 128,68,1024\nQmvaa 8,132,100\nQda 132,68,1536\nQmvaa 8,136,104\n"
"Qda 136,68,1280\nQmvaa 8,140,108\nQda 140,68,1792\nQcvaa 12,124,108,56\nQda 124,68,768\nQmvaa 12,120,104\nQda 120,68,256\nQmvaa 12,116,100\n"
"Qda 116,68,512\nQmvaa 12,112,96\nQda 112,68,0\nv_smfmac_f32_16x16x64_bf16 a[196:199], a[204:207], v[20:27], v77\nQmvva 208,32,144\nQe 0\nQmvva 208,24,148\nQe 1\n"
"Qmvva 212,32,152\nQe 2\nQmvva 212,24,156\nQe 3\nQmvva 216,32,160\nQk 28,0,1\nQk 29,2,3\nQmvva 216,24,164\n"
"Qe 4\nQmvva 220,32,168\nQe 5\nQmvva 220,24,172\ns_waitcnt lgkmcnt(0)\nQk 30,4,5\nQcvaa 0,112,0,56\nQe 6\n"
"Qmvaa 0,116,4\nQe 7\nQmvaa 0,120,8\nQk 31,6,7\nQmvaa 0,124,12\nQe 8\nQcvaa 4,140,12,56\nQe 9\n"
"Qmvaa 4,136,8\nQe 10\nQmvaa 4,132,4\nQe 11\nQmvaa 4,128,0\nQe 12\nQmvva 240,36,144\nQe 13\n"
"Qmvva 240,28,148\nQe 14\nQmvva 244,36,152\nQe 15\nQmvva 244,28,156\nQk 34,8,9\nQk 35,10,11\nQmvva 248,36,160\n"
"Qk 32,12,13\nQk 33,14,15\nQmvva 248,28,164\nQe 0\nQmvva 252,36,168\nQe 1\nQmvva 252,28,172\nQe 2\n"
"Qmava 236,40,168\nQta 168,70,33920\nQta 170,70,34944\nQmava 236,32,172\nQe 3\nQmava 232,40,160\nQta 172,71,896\nQta 174,71,1920\n"
"Qmava 232,32,164\nQk 36,0,1\nQk 37,2,3\nQmava 228,40,152\nQta 160,70,33664\nQta 162,70,34688\nQmava 228,32,156\nQe 4\n"
"Qmava 224,40,144\nQta 164,71,640\nQta 166,71,1664\nQmava 224,32,148\nQe 5\nv_smfmac_f32_16x16x64_bf16 a[200:203], a[204:207], v[28:35], v77\nQta 152,70,33408\nQta 154,70,34432\n"
"Qcvaa 8,112,16,56\nQe 6\nQmvaa 8,116,20\nQta 156,71,384\nQta 158,71,1408\nQmvaa 8,120,24\nQe 7\nQmvaa 8,124,28\n"
"Qta 144,70,33152\nQta 146,70,34176\nQcvaa 12,140,28,56\nQk 38,4,5\nQk 39,6,7\nQmvaa 12,136,24\nQta 148,71,128\nQta 150,71,1152\n"
"Qmvaa 12,132,20\nQmvaa 12,128,16\ns_add_u32 s66, s66, 1\ns_bitcmp1_b32 0x7f7f, s66\ns_cbranch_scc1 .Lloop\ns_cmp_lg_u32 s67, 0\ns_cbranch_scc1 .Ltail1\nQcvaa 0,128,32,56\n"
"v_add_u32 v75, 1, v75\nQmvaa 0,132,36\ns_waitcnt vmcnt(8)\nds_write_b32 v73, v75\nQmvaa 0,136,40\nQe 8\nQmvaa 0,140,44\nQe 9\n"
"Qcvaa 4,124,44,56\nQe 10\nQmvaa 4,120,40\nQe 11\nQmvaa 4,116,36\nQe 12\nQmvaa 4,112,32\ns_waitcnt lgkmcnt(1)\n"
"Qk 40,8,9\nQmvva 96,44,144\nQe 13\nQmvva 96,36,148\nQe 14\nQmvva 100,44,152\nQe 15\nQmvva 100,36,156\n"
"Qk 41,10,11\nQk 42,12,13\nQmvva 104,44,160\nQk 43,14,15\nQmvva 104,36,164\nQe 0\nQmvva 108,44,168\nQe 1\n"
"Qmvva 108,36,172\nQe 2\nQmvva 140,48,168\nQe 3\nQmvva 140,40,172\nQk 46,0,1\nQk 47,2,3\nQmvva 136,48,160\n"
"ds_read_b32 v66, v74\n.Lpg7:\ns_cbranch_execz .Ltr9\nQmvva 136,40,164\nQe 4\nQmvva 132,48,152\nQe 5\nQmvva 132,40,156\n"
"Qe 6\nQmvva 128,48,144\nQe 7\nQmvva 128,40,148\ns_waitcnt lgkmcnt(0)\nv_cmp_gt_u32 vcc, v75, v66\nv_smfmac_f32_16x16x64_bf16 a[176:179], a[204:207], v[36:43], v77\ns_cbranch_vccnz .Lsa30\n"
".Lra30:\nQcvaa 8,112,48,56\ns_add_u32 m0, s62, 0\ns_add_u32 s69, s87, 0\nQk 44,4,5\nQmvaa 8,116,52\nbuffer_load_dwordx4 v62, s[52:55], s69 offen lds\nQmvaa 8,120,56\n"
"Qk 45,6,7\nQmvaa 8,124,60\nQcvaa 12,140,60,56\nQmvaa 12,136,56\ns_add_u32 s69, s87, 64504\nQmvaa 12,132,52\nbuffer_load_dwordx4 v62, s[52:55], s69 offen offset:1032 lds\nQmvaa 12,128,48\n"
"Qcvaa 0,128,64,56\nQmvaa 0,132,68\nQe 8\nQmvaa 0,136,72\ns_add_u32 s69, s87, 129008\nQe 9\nQmvaa 0,140,76\nbuffer_load_dwordx4 v62, s[52:55], s69 offen offset:2064 lds\n"
"Qcvaa 4,124,76,56\nQe 10\nQmvaa 4,120,72\nQe 11\nQmvaa 4,116,68\nQe 12\nQmvaa 4,112,64\ns_add_u32 s69, s87, 193512\n"
"Qe 13\nQmvva 160,52,144\nbuffer_load_dwordx4 v62, s[52:55], s69 offen offset:3096 lds\nQmvva 160,44,148\nQe 14\nQmvva 164,52,152\nQe 15\nQmvva 164,44,156\n"
"Qk 48,8,9\nQk 49,10,11\nQmvva 168,52,160\ns_add_u32 m0, s62, 16512\ns_add_u32 s69, s87, 128\nQk 50,12,13\nQmvva 168,44,164\nbuffer_load_dwordx4 v62, s[52:55], s69 offen lds\n"
"Qmvva 172,52,168\nQk 51,14,15\nQmvva 172,44,172\nQe 0\nQmvva 204,20,168\nQe 1\nQmvva 204,48,172\ns_add_u32 s69, s87, 64632\n"
"Qe 2\nQmvva 200,20,160\nbuffer_load_dwordx4 v62, s[52:55], s69 offen offset:1032 lds\nQmvva 200,48,164\nQe 3\nQmvva 196,20,152\nQk 18,0,1\nQk 19,2,3\n"
"Qmvva 196,48,156\nQe 4\nQmvva 192,20,144\ns_add_u32 s69, s87, 129136\nQe 5\nQmvva 192,48,148\nbuffer_load_dwordx4 v62, s[52:55], s69 offen offset:2064 lds\nv_smfmac_f32_16x16x64_bf16 a[180:183], a[204:207], v[44:51], v77\n"
"Qk 16,4,5\nQcvaa 8,112,80,56\nQe 6\nQmvaa 8,116,84\nQe 7\nQmvaa 8,120,88\ns_add_u32 s69, s87, 193640\nQk 17,6,7\n"
"Qmvaa 8,124,92\nbuffer_load_dwordx4 v62, s[52:55], s69 offen offset:3096 lds\nQcvaa 12,140,92,56\nQmvaa 12,136,88\nQmvaa 12,132,84\nQmvaa 12,128,80\nQcvaa 0,128,96,56\nQda 128,68,34048\n"
"Qmvaa 0,132,100\nQda 132,68,34560\nQmvaa 0,136,104\nQda 136,68,34304\nQmvaa 0,140,108\nQda 140,68,34816\nQcvaa 4,124,108,56\nQda 124,68,33792\n"
"Qmvaa 4,120,104\nQda 120,68,33280\nQmvaa 4,116,100\nQda 116,68,33536\nQmvaa 4,112,96\nQda 112,68,33024\nQmvva 224,24,144\nQe 8\n"
"Qmvva 224,16,148\ns_add_u32 m0, s63, 33024\ns_add_u32 s88, s86, 0\nQe 9\nQmvva 228,24,152\nbuffer_load_dwordx4 v64, s[48:51], s88 offen lds\nQe 10\nQmvva 228,16,156\n"
"Qe 11\nQmvva 232,24,160\nQk 20,8,9\nQk 21,10,11\nQmvva 232,16,164\ns_add_u32 s88, s86, 19456\nQe 12\nQmvva 236,24,168\n"
"buffer_load_dwordx4 v64, s[48:51], s88 offen offset:1024 lds\nQe 13\nQmvva 236,16,172\ns_waitcnt lgkmcnt(0)\nQk 22,12,13\nQcvaa 8,112,0,56\nQe 14\nQmvaa 8,116,4\n"
"v_subrev_u32 v68, 66048, v68\ns_add_u32 s88, s86, 38912\nQmvaa 8,120,8\nbuffer_load_dwordx4 v64, s[48:51], s88 offen offset:2048 lds\nQe 15\nQmvaa 8,124,12\nQk 23,14,15\nQcvaa 12,140,12,56\n"
"Qe 0\nQmvaa 12,136,8\ns_add_u32 s88, s86, 58368\nQe 1\nQmvaa 12,132,4\nbuffer_load_dwordx4 v64, s[48:51], s88 offen offset:3072 lds\nQe 2\nQmvaa 12,128,0\n"
"Qe 3\nv_smfmac_f32_16x16x64_bf16 a[184:187], a[204:207], v[16:23], v77\nQk 26,0,1\nQmava 208,28,144\ns_add_u32 m0, s63, 37120\ns_add_u32 s88, s86, 81920\nQe 4\nQmava 208,20,148\n"
"buffer_load_dwordx4 v64, s[48:51], s88 offen lds\nQe 5\nQmava 212,28,152\nQe 6\nQmava 212,20,156\nQe 7\nQmava 216,28,160\ns_add_u32 s88, s86, 101376\n"
"Qk 27,2,3\nQk 24,4,5\nQmava 216,20,164\nbuffer_load_dwordx4 v64, s[48:51], s88 offen offset:1024 lds\nQk 25,6,7\nQmava 220,28,168\nQe 8\nQmava 220,20,172\n"
"Qe 9\nQmava 252,32,168\nQta 168,71,768\nQta 170,71,1792\ns_add_u32 s88, s86, 120832\nQmava 252,24,172\nbuffer_load_dwordx4 v64, s[48:51], s88 offen offset:2048 lds\nQe 10\n"
"Qmava 248,32,160\nQta 172,71,33792\nQta 174,71,34816\nQmava 248,24,164\nQe 11\nQmava 244,32,152\nQta 160,71,512\nQta 162,71,1536\n"
"Qmava 244,24,156\nQk 28,8,9\nQk 29,10,11\nQmava 240,32,144\nQta 164,71,33536\nQta 166,71,34560\nQmava 240,24,148\nQe 12\n"
"Qcvaa 0,112,16,56\nQta 152,71,256\nQta 154,71,1280\nQmvaa 0,116,20\nQe 13\nQmvaa 0,120,24\nQta 156,71,33280\nQta 158,71,34304\n"
"Qmvaa 0,124,28\nQe 14\nQcvaa 4,140,28,56\nQta 144,71,0\nQta 146,71,1024\nQmvaa 4,136,24\nQe 15\nQmvaa 4,132,20\n"
"Qta 148,71,33024\nQta 150,71,34048\nQmvaa 4,128,16\nQk 30,12,13\nQk 31,14,15\nQcvaa 8,128,32,56\nv_add_u32 v75, 1, v75\nQmvaa 8,132,36\n"
"ds_write_b32 v73, v75\nQmvaa 8,136,40\nQe 0\nQmvaa 8,140,44\nQe 1\nQcvaa 12,124,44,56\nQe 2\nQmvaa 12,120,40\n"
"Qe 3\nQmvaa 12,116,36\nQe 4\nQmvaa 12,112,32\ns_waitcnt lgkmcnt(1)\nQk 32,0,1\nv_smfmac_f32_16x16x64_bf16 a[188:191], a[204:207], v[24:31], v77\nQk 33,2,3\n"
"Qmvva 80,36,144\nQe 5\nQmvva 80,28,148\nQe 6\nQmvva 84,36,152\nQe 7\nQmvva 84,28,156\nQk 34,4,5\n"
"Qk 35,6,7\nQmvva 88,36,160\nQe 8\nQmvva 88,28,164\nQe 9\nQmvva 92,36,168\nQe 10\nQmvva 92,28,172\n"
"Qe 11\nQmvva 124,40,168\nQk 38,8,9\nQk 39,10,11\nQmvva 124,32,172\nds_read_b32 v66, v74\nQmvva 120,40,160\nQe 12\n"
"Qmvva 120,32,164\nQe 13\nQmvva 116,40,152\nQe 14\nQmvva 116,32,156\nQe 15\nQmvva 112,40,144\ns_waitcnt lgkmcnt(0)\n"
"v_cmp_gt_u32 vcc, v75, v66\nQmvva 112,32,148\ns_cbranch_vccnz .Lsa31\n.Lra31:\nQk 36,12,13\nQcvaa 0,112,48,56\ns_add_u32 m0, s62, 33024\ns_add_u32 s69, s87, 524288\n"
"Qk 37,14,15\nQmvaa 0,116,52\nbuffer_load_dwordx4 v62, s[52:55], s69 offen lds\nQmvaa 0,120,56\nQmvaa 0,124,60\nQcvaa 4,140,60,56\nQmvaa 4,136,56\ns_add_u32 s69, s87, 588792\n"
"Qmvaa 4,132,52\nbuffer_load_dwordx4 v62, s[52:55], s69 offen offset:1032 lds\nQmvaa 4,128,48\nQcvaa 8,128,64,56\nQmvaa 8,132,68\nQe 0\nQmvaa 8,136,72\ns_add_u32 s69, s87, 653296\n"
"Qe 1\nQmvaa 8,140,76\nbuffer_load_dwordx4 v62, s[52:55], s69 offen offset:2064 lds\nQcvaa 12,124,76,56\nQe 2\nQmvaa 12,120,72\nQe 3\nQmvaa 12,116,68\n"
"Qe 4\nQmvaa 12,112,64\nQe 5\nv_smfmac_f32_16x16x64_bf16 a[192:195], a[204:207], v[32:39], v77\ns_add_u32 s69, s87, 717800\nQk 40,0,1\nQmvva 144,44,144\nbuffer_load_dwordx4 v62, s[52:55], s69 offen offset:3096 lds\n"
"Qmvva 144,36,148\nQe 6\nQmvva 148,44,152\nQe 7\nQmvva 148,36,156\ns_add_u32 m0, s62, 49536\ns_add_u32 s69, s87, 524416\nQk 41,2,3\n"
"Qmvva 152,44,160\nbuffer_load_dwordx4 v62, s[52:55], s69 offen lds\nQmvva 152,36,164\nQk 42,4,5\nQk 43,6,7\nQmvva 156,44,168\nQe 8\nQmvva 156,36,172\n"
"Qe 9\nQmvva 188,48,168\ns_add_u32 s69, s87, 588920\nQe 10\nQmvva 188,40,172\nbuffer_load_dwordx4 v62, s[52:55], s69 offen offset:1032 lds\nQmvva 184,48,160\nQe 11\n"
"Qmvva 184,40,164\nQk 46,8,9\nQk 47,10,11\nQmvva 180,48,152\nQe 12\nQmvva 180,40,156\ns_add_u32 s69, s87, 653424\nQe 13\n"
"Qmvva 176,48,144\nbuffer_load_dwordx4 v62, s[52:55], s69 offen offset:2064 lds\nQmvva 176,40,148\nQe 14\nQcvaa 0,112,80,56\nQe 15\nQmvaa 0,116,84\nQk 44,12,13\n"
"Qk 45,14,15\nQmvaa 0,120,88\ns_add_u32 s69, s87, 717928\nQmvaa 0,124,92\nbuffer_load_dwordx4 v62, s[52:55], s69 offen offset:3096 lds\nQcvaa 4,140,92,56\nQmvaa 4,136,88\nQmvaa 4,132,84\n"
"Qmvaa 4,128,80\nQcvaa 8,128,96,56\nQmvaa 8,132,100\nQmvaa 8,136,104\nQmvaa 8,140,108\nQcvaa 12,124,108,56\nQmvaa 12,120,104\nQmvaa 12,116,100\n"
"Qmvaa 12,112,96\nv_smfmac_f32_16x16x64_bf16 a[196:199], a[204:207], v[40:47], v77\nQmvva 208,16,144\nQta 112,71,33152\nQe 0\nQmvva 208,44,148\nQta 114,71,34176\nQe 1\n"
"Qmvva 212,16,152\nQta 116,71,33408\nQe 2\nQmvva 212,44,156\nQta 118,71,34432\nQe 3\nQmvva 216,16,160\nQta 120,71,33664\n"
"Qk 48,0,1\nQk 49,2,3\nQmvva 216,44,164\nQta 122,71,34688\nQe 4\nQmvva 220,16,168\nQta 124,71,33920\nQe 5\n"
"Qmvva 220,44,172\nQta 126,71,34944\nQk 50,4,5\nQe 6\nQe 7\ns_nop 0\nQk 51,6,7\nQe 8\n"
"Qe 9\nQe 10\nQe 11\nQe 12\nQmvva 240,20,144\nv_add_u32 v75, 1, v75\nQe 13\nQmvva 240,48,148\n"
"ds_write_b32 v73, v75\nQe 14\nQmvva 244,20,152\nQe 15\nQmvva 244,48,156\nQk 54,8,9\nQk 55,10,11\nQmvva 248,20,160\n"
"Qk 52,12,13\nQk 53,14,15\nQmvva 248,48,164\nQmvva 252,20,168\nQmvva 252,48,172\nv_smfmac_f32_16x16x64_bf16 a[200:203], a[204:207], v[48:55], v77\nQmava 224,24,144\nds_read_b32 v66, v74\n"
"v_accvgpr_read_b32 v0, a176\nQmava 224,52,148\nv_accvgpr_read_b32 v1, a177\nv_accvgpr_read_b32 v6, a178\nQmava 228,24,152\nv_accvgpr_read_b32 v7, a179\nv_accvgpr_read_b32 v8, a180\nQmava 228,52,156\n"
"v_accvgpr_read_b32 v9, a181\nv_accvgpr_read_b32 v14, a182\nQmava 232,24,160\nv_accvgpr_read_b32 v15, a183\nv_accvgpr_read_b32 v16, a184\nQmava 232,52,164\ns_waitcnt lgkmcnt(0)\nv_cmp_gt_u32 vcc, v75, v66\n"
"Qmava 236,24,168\ns_cbranch_vccnz .Lsa32\n.Lra32:\nv_accvgpr_read_b32 v17, a185\nQmava 236,52,172\ns_waitcnt lgkmcnt(0)\nv_accvgpr_read_b32 v22, a186\nQmvva 96,28,112\n"
"s_add_u32 m0, s63, 0\ns_add_u32 s88, s86, 128\nv_accvgpr_read_b32 v23, a187\nQmvva 100,28,116\nbuffer_load_dwordx4 v64, s[48:51], s88 offen lds\nQmvva 104,28,120\nv_accvgpr_read_b32 v24, a188\nv_accvgpr_read_b32 v25, a189\n"
"Qmvva 108,28,124\nv_accvgpr_read_b32 v2, a190\nv_accvgpr_read_b32 v3, a191\nQmvva 128,32,112\ns_add_u32 s88, s86, 19584\nv_accvgpr_read_b32 v4, a192\nQmvva 132,32,116\nbuffer_load_dwordx4 v64, s[48:51], s88 offen offset:1024 lds\n"
"Qmvva 136,32,120\nv_accvgpr_read_b32 v5, a193\nv_accvgpr_read_b32 v10, a194\nQmvva 140,32,124\nv_accvgpr_read_b32 v11, a195\nv_accvgpr_read_b32 v12, a196\nQmvva 160,36,112\ns_add_u32 s88, s86, 39040\n"
"v_accvgpr_read_b32 v13, a197\nQmvva 164,36,116\nbuffer_load_dwordx4 v64, s[48:51], s88 offen offset:2048 lds\nQmvva 168,36,120\nv_accvgpr_read_b32 v18, a198\nv_accvgpr_read_b32 v19, a199\nQmvva 172,36,124\nv_accvgpr_read_b32 v20, a200\n"
"v_accvgpr_read_b32 v21, a201\nQmvva 192,40,112\ns_add_u32 s88, s86, 58496\nv_accvgpr_read_b32 v26, a202\nQmvva 196,40,116\nbuffer_load_dwordx4 v64, s[48:51], s88 offen offset:3072 lds\nQmvva 200,40,120\nv_accvgpr_read_b32 v27, a203\n"
"Qa 8,8,9\nQmvva 204,40,124\nQa 10,10,11\nQa 8,8,10\nQmvva 224,44,112\ns_add_u32 m0, s63, 4096\ns_add_u32 s88, s86, 82048\nQa 16,16,17\n"
"Qmvva 228,44,116\nbuffer_load_dwordx4 v64, s[48:51], s88 offen lds\nQmvva 232,44,120\nQa 18,18,19\nQa 16,16,18\nQmvva 236,44,124\nQa 4,4,5\nQa 6,6,7\n"
"Qmava 208,48,112\ns_add_u32 s88, s86, 101504\nQa 4,4,6\nQmava 212,48,116\nbuffer_load_dwordx4 v64, s[48:51], s88 offen offset:1024 lds\nQmava 216,48,120\nQa 12,12,13\nQa 14,14,15\n"
"Qmava 220,48,124\nQa 12,12,14\nQa 0,0,1\nQmava 240,52,112\ns_add_u32 s88, s86, 120960\nQa 2,2,3\nQmava 244,52,116\nbuffer_load_dwordx4 v64, s[48:51], s88 offen offset:2048 lds\n"
"Qmava 248,52,120\nQa 0,0,2\nQa 24,24,25\nQmava 252,52,124\nQa 26,26,27\nQa 24,24,26\nQa 20,20,21\nQa 22,22,23\n"
"Qa 20,20,22\nv_max3_u32 v9, v8, v16, v4\nv_max3_u32 v9, v9, v12, v0\nv_max3_u32 v9, v9, v24, v20\nv_min3_u32 v11, v8, v16, v4\nv_min3_u32 v11, v11, v12, v0\nv_min3_u32 v11, v11, v24, v20\nv_cmp_lt_u32 vcc, 0x44800000, v9\n"
"s_mov_b64 s[74:75], vcc\nv_cmp_gt_u32 vcc, 0x2b800000, v11\ns_or_b64 s[74:75], s[74:75], vcc\ns_cmp_lg_u64 s[74:75], 0\ns_cbranch_scc1 .Lfb\n.Lfbret0:\nv_rcp_f32 v2, v0\nv_rcp_f32 v6, v4\n"
"v_rcp_f32 v10, v8\nv_rcp_f32 v14, v12\nv_rcp_f32 v18, v16\nv_rcp_f32 v22, v20\nv_rcp_f32 v26, v24\nv_and_b32 v66, 16, v60\nv_mov_b32 v67, 0xffff\nv_lshlrev_b32 v66, v66, v67\n"
"s_mov_b32 vcc_lo, 0x00c0000c\ns_mov_b32 vcc_hi, 0xc0000c00\nv_cndmask_b32 v67, 0, v66, vcc\ns_lshr_b64 vcc, vcc, 2\nv_cndmask_b32 v66, 0, v66, vcc\nv_mov_b64 v[0:1], 0\nQk 2,2,2\nv_and_b32 v3, v2, v67\n"
"v_and_b32 v2, v2, v66\nv_mov_b64 v[4:5], 0\nQk 6,6,6\nv_and_b32 v7, v6, v67\nv_and_b32 v6, v6, v66\nv_mov_b64 v[8:9], 0\nQk 10,10,10\nv_and_b32 v11, v10, v67\n"
"v_and_b32 v10, v10, v66\nv_mov_b64 v[12:13], 0\nQk 14,14,14\nv_and_b32 v15, v14, v67\nv_and_b32 v14, v14, v66\nv_mov_b64 v[16:17], 0\nQk 18,18,18\nv_and_b32 v19, v18, v67\n"
"v_and_b32 v18, v18, v66\nv_mov_b64 v[20:21], 0\nQk 22,22,22\nv_and_b32 v23, v22, v67\nv_and_b32 v22, v22, v66\nv_mov_b64 v[24:25], 0\nQk 26,26,26\nv_and_b32 v27, v26, v67\n"
"v_and_b32 v26, v26, v66\nv_mov_b64 v[28:29], 0\nQk 80,80,81\nQk 81,82,83\nQk 82,84,85\nQk 83,86,87\ns_nop 1\nQzvvv 84,80,0\n"
"Qzvvv 80,80,2\nQk 88,88,89\nQk 89,90,91\nQk 90,92,93\n.Lpg8:\ns_cbranch_execz .Ltr10\nQk 91,94,95\ns_nop 1\n"
"Qzvvv 92,88,0\nQzvvv 88,88,2\nQk 96,96,97\nQk 97,98,99\nQk 98,100,101\nQk 99,102,103\nQk 80,80,81\nQk 81,82,83\n"
"Qzvvv 100,96,0\nQk 82,84,85\nQk 83,86,87\nQzvvv 96,96,2\nv_accvgpr_write_b32 a0, v80\nv_accvgpr_write_b32 a1, v81\nv_accvgpr_write_b32 a2, v82\nv_accvgpr_write_b32 a3, v83\n"
"Qk 104,104,105\nQk 105,106,107\nQk 106,108,109\nQk 107,110,111\nQk 88,88,89\nQk 89,90,91\nQzvvv 108,104,0\nQk 90,92,93\n"
"Qk 91,94,95\nQzvvv 104,104,2\nv_accvgpr_write_b32 a4, v88\nv_accvgpr_write_b32 a5, v89\nv_accvgpr_write_b32 a6, v90\nv_accvgpr_write_b32 a7, v91\nQk 112,112,113\nQk 113,114,115\n"
"Qk 114,116,117\nQk 115,118,119\nQk 96,96,97\nQk 97,98,99\nQzvvv 116,112,4\nQk 98,100,101\nQk 99,102,103\nQzvvv 112,112,6\n"
"v_accvgpr_write_b32 a8, v96\nv_accvgpr_write_b32 a9, v97\nv_accvgpr_write_b32 a10, v98\nv_accvgpr_write_b32 a11, v99\nQk 120,120,121\nQk 121,122,123\nQk 122,124,125\nQk 123,126,127\n"
"Qk 104,104,105\nQk 105,106,107\nQzvvv 124,120,4\nQk 106,108,109\nQk 107,110,111\nQzvvv 120,120,6\nv_accvgpr_write_b32 a12, v104\nv_accvgpr_write_b32 a13, v105\n"
"v_accvgpr_write_b32 a14, v106\nv_accvgpr_write_b32 a15, v107\nQk 128,128,129\nQk 129,130,131\nQk 130,132,133\nQk 131,134,135\nQk 112,112,113\nQk 113,114,115\n"
"Qzvvv 132,128,4\nQk 114,116,117\nQk 115,118,119\nQzvvv 128,128,6\nv_accvgpr_write_b32 a16, v112\nv_accvgpr_write_b32 a17, v113\nv_accvgpr_write_b32 a18, v114\nv_accvgpr_write_b32 a19, v115\n"
"Qk 136,136,137\nQk 137,138,139\nQk 138,140,141\nQk 139,142,143\nQk 120,120,121\nQk 121,122,123\nQzvvv 140,136,4\nQk 122,124,125\n"
"Qk 123,126,127\nQzvvv 136,136,6\nv_accvgpr_write_b32 a20, v120\nv_accvgpr_write_b32 a21, v121\nv_accvgpr_write_b32 a22, v122\nv_accvgpr_write_b32 a23, v123\nQk 144,144,145\nQk 145,146,147\n"
"Qk 146,148,149\nQk 147,150,151\nQk 128,128,129\nQk 129,130,131\nQzvvv 148,144,8\nQk 130,132,133\nQk 131,134,135\nQzvvv 144,144,10\n"
"v_accvgpr_write_b32 a24, v128\nv_accvgpr_write_b32 a25, v129\nv_accvgpr_write_b32 a26, v130\nv_accvgpr_write_b32 a27, v131\nQk 152,152,153\nQk 153,154,155\nQk 154,156,157\nQk 155,158,159\n"
"Qk 136,136,137\nQk 137,138,139\nQzvvv 156,152,8\nQk 138,140,141\nQk 139,142,143\nQzvvv 152,152,10\nv_accvgpr_write_b32 a28, v136\nv_accvgpr_write_b32 a29, v137\n"
"v_accvgpr_write_b32 a30, v138\nv_accvgpr_write_b32 a31, v139\nQk 160,160,161\nQk 161,162,163\nQk 162,164,165\nQk 163,166,167\nQk 144,144,145\nQk 145,146,147\n"
"Qzvvv 164,160,8\nQk 146,148,149\nQk 147,150,151\nQzvvv 160,160,10\nv_accvgpr_write_b32 a32, v144\nv_accvgpr_write_b32 a33, v145\nv_accvgpr_write_b32 a34, v146\nv_accvgpr_write_b32 a35, v147\n"
"Qk 168,168,169\nQk 169,170,171\nQk 170,172,173\nQk 171,174,175\nQk 152,152,153\nQk 153,154,155\nQzvvv 172,168,8\nQk 154,156,157\n"
"Qk 155,158,159\nQzvvv 168,168,10\nv_accvgpr_write_b32 a36, v152\nv_accvgpr_write_b32 a37, v153\nv_accvgpr_write_b32 a38, v154\nv_accvgpr_write_b32 a39, v155\nQk 176,176,177\nQk 177,178,179\n"
"Qk 178,180,181\nQk 179,182,183\nQk 160,160,161\nQk 161,162,163\nQzvvv 180,176,12\nQk 162,164,165\nQk 163,166,167\nQzvvv 176,176,14\n"
"v_accvgpr_write_b32 a40, v160\nv_accvgpr_write_b32 a41, v161\nv_accvgpr_write_b32 a42, v162\nv_accvgpr_write_b32 a43, v163\nQk 184,184,185\nQk 185,186,187\nQk 186,188,189\nQk 187,190,191\n"
"Qk 168,168,169\nQk 169,170,171\nQzvvv 188,184,12\nQk 170,172,173\nQk 171,174,175\nQzvvv 184,184,14\nv_accvgpr_write_b32 a44, v168\nv_accvgpr_write_b32 a45, v169\n"
"v_accvgpr_write_b32 a46, v170\nv_accvgpr_write_b32 a47, v171\nQk 192,192,193\nQk 193,194,195\nQk 194,196,197\nQk 195,198,199\nQk 176,176,177\nQk 177,178,179\n"
"Qzvvv 196,192,12\nQk 178,180,181\nQk 179,182,183\nQzvvv 192,192,14\nv_accvgpr_write_b32 a48, v176\nv_accvgpr_write_b32 a49, v177\nv_accvgpr_write_b32 a50, v178\nv_accvgpr_write_b32 a51, v179\n"
"Qk 200,200,201\nQk 201,202,203\nQk 202,204,205\nQk 203,206,207\nQk 184,184,185\nQk 185,186,187\nQzvvv 204,200,12\nQk 186,188,189\n"
"Qk 187,190,191\nQzvvv 200,200,14\nv_accvgpr_write_b32 a52, v184\nv_accvgpr_write_b32 a53, v185\nv_accvgpr_write_b32 a54, v186\nv_accvgpr_write_b32 a55, v187\nQk 208,208,209\nQk 209,210,211\n"
"Qk 210,212,213\nQk 211,214,215\nQk 192,192,193\nQk 193,194,195\nQzvvv 212,208,16\nQk 194,196,197\nQk 195,198,199\nQzvvv 208,208,18\n"
"v_accvgpr_write_b32 a56, v192\nv_accvgpr_write_b32 a57, v193\nv_accvgpr_write_b32 a58, v194\nv_accvgpr_write_b32 a59, v195\nQk 216,216,217\nQk 217,218,219\nQk 218,220,221\nQk 219,222,223\n"
"Qk 200,200,201\nQk 201,202,203\nQzvvv 220,216,16\nQk 202,204,205\nQk 203,206,207\nQzvvv 216,216,18\nv_accvgpr_write_b32 a60, v200\nv_accvgpr_write_b32 a61, v201\n"
"v_accvgpr_write_b32 a62, v202\nv_accvgpr_write_b32 a63, v203\nQk 224,224,225\nQk 225,226,227\nQk 226,228,229\nQk 227,230,231\nQk 208,208,209\nQk 209,210,211\n"
"Qzvvv 228,224,16\nQk 210,212,213\nQk 211,214,215\nQzvvv 224,224,18\nv_accvgpr_write_b32 a64, v208\nv_accvgpr_write_b32 a65, v209\nv_accvgpr_write_b32 a66, v210\nv_accvgpr_write_b32 a67, v211\n"
"Qk 232,232,233\nQk 233,234,235\nQk 234,236,237\nQk 235,238,239\nQk 216,216,217\nQk 217,218,219\nQzvvv 236,232,16\nQk 218,220,221\n"
"Qk 219,222,223\nQzvvv 232,232,18\nv_accvgpr_write_b32 a68, v216\nv_accvgpr_write_b32 a69, v217\nv_accvgpr_write_b32 a70, v218\nv_accvgpr_write_b32 a71, v219\nQk 240,240,241\nQk 241,242,243\n"
"Qk 242,244,245\nQk 243,246,247\nQk 224,224,225\nQk 225,226,227\nQzvvv 244,240,20\nQk 226,228,229\nQk 227,230,231\nQzvvv 240,240,22\n"
"v_accvgpr_write_b32 a72, v224\nv_accvgpr_write_b32 a73, v225\nv_accvgpr_write_b32 a74, v226\nv_accvgpr_write_b32 a75, v227\nQk 248,248,249\nQk 249,250,251\nQk 250,252,253\nQk 251,254,255\n"
"Qk 232,232,233\nQk 233,234,235\nQzvvv 252,248,20\nQk 234,236,237\nQk 235,238,239\nQzvvv 248,248,22\nv_accvgpr_write_b32 a76, v232\nv_accvgpr_write_b32 a77, v233\n"
"v_accvgpr_write_b32 a78, v234\nv_accvgpr_write_b32 a79, v235\nv_accvgpr_read_b32 v48, a208\nv_accvgpr_read_b32 v49, a209\nv_accvgpr_read_b32 v50, a210\nv_accvgpr_read_b32 v51, a211\nv_accvgpr_read_b32 v36, a212\nv_accvgpr_read_b32 v37, a213\n"
"v_accvgpr_read_b32 v38, a214\nv_accvgpr_read_b32 v39, a215\nQk 48,48,49\nQk 49,50,51\nQk 50,36,37\nQk 51,38,39\nQk 240,240,241\nQk 241,242,243\n"
"Qzvvv 36,48,20\nQk 242,244,245\nQk 243,246,247\nQzvvv 48,48,22\nv_accvgpr_write_b32 a80, v240\nv_accvgpr_write_b32 a81, v241\nv_accvgpr_write_b32 a82, v242\nv_accvgpr_write_b32 a83, v243\n"
"v_accvgpr_read_b32 v52, a216\nv_accvgpr_read_b32 v53, a217\nv_accvgpr_read_b32 v54, a218\nv_accvgpr_read_b32 v55, a219\nv_accvgpr_read_b32 v40, a220\nv_accvgpr_read_b32 v41, a221\nv_accvgpr_read_b32 v42, a222\nv_accvgpr_read_b32 v43, a223\n"
"Qk 52,52,53\nQk 53,54,55\nQk 54,40,41\nQk 55,42,43\nQk 248,248,249\nQk 249,250,251\nQzvvv 40,52,20\nQk 250,252,253\n"
"Qk 251,254,255\nQzvvv 52,52,22\nv_accvgpr_write_b32 a84, v248\nv_accvgpr_write_b32 a85, v249\nv_accvgpr_write_b32 a86, v250\nv_accvgpr_write_b32 a87, v251\nv_accvgpr_read_b32 v44, a224\nv_accvgpr_read_b32 v45, a225\n"
"v_accvgpr_read_b32 v46, a226\nv_accvgpr_read_b32 v47, a227\nv_accvgpr_read_b32 v32, a228\nv_accvgpr_read_b32 v33, a229\nv_accvgpr_read_b32 v34, a230\nv_accvgpr_read_b32 v35, a231\nQk 44,44,45\nQk 45,46,47\n"
"Qk 46,32,33\nQk 47,34,35\nQk 48,48,49\nQk 49,50,51\nQzvvv 32,44,24\nQk 50,36,37\nQk 51,38,39\nQzvvv 44,44,26\n"
"v_accvgpr_write_b32 a88, v48\nv_accvgpr_write_b32 a89, v49\nv_accvgpr_write_b32 a90, v50\nv_accvgpr_write_b32 a91, v51\nv_accvgpr_read_b32 v48, a232\nv_accvgpr_read_b32 v49, a233\nv_accvgpr_read_b32 v50, a234\nv_accvgpr_read_b32 v51, a235\n"
"v_accvgpr_read_b32 v36, a236\nv_accvgpr_read_b32 v37, a237\nv_accvgpr_read_b32 v38, a238\nv_accvgpr_read_b32 v39, a239\nQk 48,48,49\nQk 49,50,51\nQk 50,36,37\nQk 51,38,39\n"
"Qk 52,52,53\nQk 53,54,55\nQzvvv 36,48,24\nQk 54,40,41\nQk 55,42,43\nQzvvv 48,48,26\nv_accvgpr_write_b32 a92, v52\nv_accvgpr_write_b32 a93, v53\n"
"v_accvgpr_write_b32 a94, v54\nv_accvgpr_write_b32 a95, v55\nv_accvgpr_read_b32 v52, a240\nv_accvgpr_read_b32 v53, a241\nv_accvgpr_read_b32 v54, a242\nv_accvgpr_read_b32 v55, a243\nv_accvgpr_read_b32 v40, a244\nv_accvgpr_read_b32 v41, a245\n"
"v_accvgpr_read_b32 v42, a246\nv_accvgpr_read_b32 v43, a247\nQk 52,52,53\nQk 53,54,55\nQk 54,40,41\nQk 55,42,43\nQk 44,44,45\nQk 45,46,47\n"
"Qzvvv 40,52,24\nQk 46,32,33\nQk 47,34,35\nQzvvv 52,52,26\nv_accvgpr_write_b32 a96, v44\nv_accvgpr_write_b32 a97, v45\nv_accvgpr_write_b32 a98, v46\nv_accvgpr_write_b32 a99, v47\n"
"v_accvgpr_read_b32 v44, a248\nv_accvgpr_read_b32 v45, a249\nv_accvgpr_read_b32 v46, a250\nv_accvgpr_read_b32 v47, a251\nv_accvgpr_read_b32 v32, a252\nv_accvgpr_read_b32 v33, a253\nv_accvgpr_read_b32 v34, a254\nv_accvgpr_read_b32 v35, a255\n"
"Qk 44,44,45\nQk 45,46,47\nQk 46,32,33\nQk 47,34,35\nQk 48,48,49\nQk 49,50,51\nQzvvv 32,44,24\nQk 50,36,37\n"
"Qk 51,38,39\nQzvvv 44,44,26\nv_accvgpr_write_b32 a100, v48\nv_accvgpr_write_b32 a101, v49\nv_accvgpr_write_b32 a102, v50\nv_accvgpr_write_b32 a103, v51\nQk 52,52,53\nQk 53,54,55\n"
"Qk 54,40,41\nQk 55,42,43\nv_accvgpr_write_b32 a104, v52\nv_accvgpr_write_b32 a105, v53\nv_accvgpr_write_b32 a106, v54\nv_accvgpr_write_b32 a107, v55\nQk 44,44,45\nQk 45,46,47\n"
"Qk 46,32,33\nQk 47,34,35\nv_accvgpr_write_b32 a108, v44\nv_accvgpr_write_b32 a109, v45\nv_accvgpr_write_b32 a110, v46\nv_accvgpr_write_b32 a111, v47\ns_mov_b32 s56, %8\ns_and_b32 s57, %9, 0xffff\n"
"s_mov_b32 s94, 0x7ffff000\ns_mul_i32 s68, %20, 40960\ns_lshl_b32 s69, s70, 10\ns_add_u32 s68, s68, s69\ns_add_u32 s68, s68, 4096\ns_add_u32 s94, %15, s68\ns_addc_u32 s95, %16, 0\ns_and_b32 s95, s95, 0xffff\n"
"s_mov_b32 s75, 37888\ns_mov_b32 s58, 1835008\ns_mov_b32 s67, 1\ns_mul_i32 s71, %12, 57344\ns_mul_i32 s68, s92, 7168\ns_add_u32 s71, s71, s68\ns_mov_b64 exec, 0\ns_cmp_eq_u32 s70, 0\n"
"s_cbranch_scc1 .Lpg12\ns_cmp_eq_u32 s70, 1\ns_cbranch_scc1 .Lpg13\ns_cmp_eq_u32 s70, 2\ns_cbranch_scc1 .Lpg14\ns_branch .Lpg15\n.Ltr6:\ns_mov_b64 exec, -1\n"
"s_branch .Lround\n.Lhook:\n.Lpg9:\ns_cbranch_execz .Ltr10\ns_cmp_eq_u32 s66, 1\ns_cbranch_scc1 .Lh1\ns_cmp_eq_u32 s66, 8\ns_cbranch_scc1 .Lh8\n"
"s_cmp_eq_u32 s66, 9\ns_cbranch_scc1 .Lh9\ns_lshr_b32 s68, s99, s66\ns_cmp_eq_u32 s68, 1\ns_cbranch_scc1 .Lh12\ns_branch .Lh11\n.Lh1:\ns_cmp_eq_u32 s70, 0\n"
"s_cselect_b32 s68, 128, 0\nv_mov_b32 v66, 0x7ffff000\nv_cmp_gt_u32 vcc, s68, v60\nv_not_b32 v67, v65\ns_lshl_b32 s68, %19, 2\nv_cndmask_b32 v66, v66, v76, vcc\nv_readlane_b32 s30, v67, %19\nbuffer_store_dword v67, v66, s[24:27], s68 offen offset:1024 sc1\n"
"s_branch .Lhookret\n.Lh8:\nv_bfe_u32 v66, v60, 4, 3\ns_lshl_b32 s68, %19, 7\nv_lshlrev_b32 v66, 2, v66\ns_add_u32 s68, s68, 2293760\nbuffer_load_dword v78, v66, s[24:27], s68 offen offset:1024 sc1\ns_branch .Lhookret\n"
".Lh9:\ns_waitcnt vmcnt(32)\ns_mov_b32 s97, 0\n.Lack_l:\nv_cmp_ne_u32 vcc, s30, v78\ns_cmp_eq_u64 vcc, 0\ns_cbranch_scc1 .Lack_ok\ns_sleep 1\n"
"v_bfe_u32 v66, v60, 4, 3\ns_lshl_b32 s68, %19, 7\nv_lshlrev_b32 v66, 2, v66\ns_add_u32 s68, s68, 2293760\nbuffer_load_dword v78, v66, s[24:27], s68 offen offset:1024 sc1\ns_waitcnt vmcnt(0)\ns_add_u32 s97, s97, 1\ns_cmp_lt_u32 s97, 1048576\n"
"s_cbranch_scc1 .Lack_l\n.Lack_ok:\ns_lshl_b32 s68, %19, 4\ns_cmp_eq_u32 s70, 0\ns_cselect_b32 s68, s68, -1\nv_mov_b32 v67, 0x7ffff000\nv_cmp_eq_u32 vcc, s68, v60\ns_nop 1\n"
"v_cndmask_b32 v67, v67, v76, vcc\nv_add_u32 v66, 1, v65\nbuffer_store_dword v66, v67, s[24:27], 0 offen sc1\ns_branch .Lhookret\n.Lh11:\nbuffer_load_dword v78, v76, s[24:27], 0 offen sc1\ns_branch .Lhookret\n.Lh12:\n"
"s_waitcnt vmcnt(32)\nv_sub_u32 v66, v78, v65\nv_cmp_gt_u32 vcc, 1, v66\ns_and_b64 vcc, vcc, s[28:29]\ns_cbranch_scc1 .Lf1_w\n.Lf1_r:\ns_cmp_lg_u32 s70, 3\ns_cbranch_scc1 .Lf1_n\n"
"buffer_inv sc1\n.Lf1_n:\ns_lshr_b32 s88, %19, 2\ns_lshl_b32 s88, s88, 4\ns_add_u32 s88, s88, s93\ns_mul_i32 s68, s88, 57344\ns_mul_i32 s69, s92, 7168\ns_add_u32 s68, s68, s69\n"
"s_add_u32 s48, %8, s68\ns_addc_u32 s49, %9, 0\ns_and_b32 s49, s49, 0xffff\ns_sub_u32 s50, 1835008, s68\ns_mul_i32 s68, s88, 163840\ns_and_b32 s69, %19, 3\ns_mul_i32 s69, s69, 320\ns_add_u32 s68, s68, s69\n"
"s_add_u32 s52, %15, s68\ns_addc_u32 s53, %16, 0\ns_and_b32 s53, s53, 0xffff\ns_sub_u32 s54, 5246976, s68\ns_add_u32 s71, s71, 458752\ns_and_b32 s87, %19, 3\ns_mul_i32 s87, s87, 327680\ns_add_u32 s87, s87, 1310720\n"
"s_mul_i32 s69, s92, 10240\nv_bfe_u32 v66, v60, 5, 3\nv_mul_u32_u24 v66, 1280, v66\nv_and_b32 v67, 16, v60\nv_add3_u32 v67, v67, v66, s69\ns_and_b32 s69, %19, 1\nv_lshrrev_b32 v66, 8, v60\ns_lshl_b32 s68, s93, 1\n"
"v_xor_b32 v66, s68, v66\nv_lshl_add_u32 v66, v66, 5, v67\n.Lpg10:\ns_cbranch_execz .Ltr10\ns_lshr_b32 s68, 128, s69\nv_add_u32 v61, s68, v66\ns_mul_i32 s68, s69, 192\nv_add_u32 v63, s68, v66\n"
"v_bfe_u32 v66, v60, 8, 1\nv_xor_b32 v66, s93, v66\nv_lshl_add_u32 v67, v66, 5, v67\nv_lshrrev_b32 v66, 9, v60\nv_mul_u32_u24 v66, 20480, v66\ns_xor_b32 s69, s69, 1\ns_lshl_b32 s69, s69, 8\nv_add3_u32 v62, v66, v67, s69\n"
"s_branch .Lhookret\n.Lf1_w:\ns_mov_b32 s97, 0\n.Lf1_l:\ns_sleep 1\nbuffer_load_dword v78, v76, s[24:27], 0 offen sc1\ns_waitcnt vmcnt(0)\ns_add_u32 s97, s97, 1\n"
"s_cmp_gt_u32 s97, 1048576\ns_cbranch_scc1 .Lf1_r\nv_sub_u32 v66, v78, v65\nv_cmp_gt_u32 vcc, 1, v66\ns_and_b64 vcc, vcc, s[28:29]\ns_cbranch_scc1 .Lf1_l\ns_branch .Lf1_r\n.Lsa1:\n"
"ds_read_b32 v66, v74\ns_waitcnt lgkmcnt(0)\nv_cmp_gt_u32 vcc, v75, v66\ns_cbranch_vccnz .Lsa1\ns_branch .Lra1\n.Lsa2:\nds_read_b32 v66, v74\ns_waitcnt lgkmcnt(0)\n"
"v_cmp_gt_u32 vcc, v75, v66\ns_cbranch_vccnz .Lsa2\ns_branch .Lra2\n.Lsp2:\ns_sub_u32 s75, s77, s74\ns_cmp_lt_u32 s75, 1024\ns_cbranch_scc0 .Lrp2\ns_memrealtime s[74:75]\n"
"s_waitcnt lgkmcnt(0)\ns_branch .Lsp2\n.Lsa3:\nds_read_b32 v66, v74\ns_waitcnt lgkmcnt(0)\nv_cmp_gt_u32 vcc, v75, v66\ns_cbranch_vccnz .Lsa3\ns_branch .Lra3\n"
".Lsa4:\nds_read_b32 v66, v74\ns_waitcnt lgkmcnt(0)\nv_cmp_gt_u32 vcc, v75, v66\ns_cbranch_vccnz .Lsa4\ns_branch .Lra4\n.Lsp4:\ns_sub_u32 s75, s77, s74\n"
"s_cmp_lt_u32 s75, 1024\ns_cbranch_scc0 .Lrp4\ns_memrealtime s[74:75]\ns_waitcnt lgkmcnt(0)\ns_branch .Lsp4\n.Lsa5:\nds_read_b32 v66, v74\ns_waitcnt lgkmcnt(0)\n"
"v_cmp_gt_u32 vcc, v75, v66\ns_cbranch_vccnz .Lsa5\ns_branch .Lra5\n.Lsa30:\nds_read_b32 v66, v74\ns_waitcnt lgkmcnt(0)\nv_cmp_gt_u32 vcc, v75, v66\ns_cbranch_vccnz .Lsa30\n"
"s_branch .Lra30\n.Lsa31:\nds_read_b32 v66, v74\ns_waitcnt lgkmcnt(0)\nv_cmp_gt_u32 vcc, v75, v66\ns_cbranch_vccnz .Lsa31\ns_branch .Lra31\n.Lsa32:\n"
"ds_read_b32 v66, v74\ns_waitcnt lgkmcnt(0)\nv_cmp_gt_u32 vcc, v75, v66\ns_cbranch_vccnz .Lsa32\ns_branch .Lra32\n.Lsa30x:\nds_read_b32 v66, v74\ns_waitcnt lgkmcnt(0)\n"
"v_cmp_gt_u32 vcc, v75, v66\ns_cbranch_vccnz .Lsa30x\ns_branch .Lra30x\n.Lsa31x:\nds_read_b32 v66, v74\ns_waitcnt lgkmcnt(0)\nv_cmp_gt_u32 vcc, v75, v66\ns_cbranch_vccnz .Lsa31x\n"
"s_branch .Lra31x\n.Lso40:\nds_read_b32 v66, v74\ns_waitcnt lgkmcnt(0)\nv_cmp_gt_u32 vcc, v75, v66\ns_cbranch_vccnz .Lso40\ns_branch .Lro40\n.Lso180:\n"
"ds_read_b32 v66, v74\ns_waitcnt lgkmcnt(0)\nv_cmp_gt_u32 vcc, v75, v66\ns_cbranch_vccnz .Lso180\ns_branch .Lro180\n.Lso320:\nds_read_b32 v66, v74\ns_waitcnt lgkmcnt(0)\n"
"v_cmp_gt_u32 vcc, v75, v66\ns_cbranch_vccnz .Lso320\ns_branch .Lro320\n.Lso460:\nds_read_b32 v66, v74\ns_waitcnt lgkmcnt(0)\nv_cmp_gt_u32 vcc, v75, v66\ns_cbranch_vccnz .Lso460\n"
"s_branch .Lro460\n.Lso600:\nds_read_b32 v66, v74\ns_waitcnt lgkmcnt(0)\nv_cmp_gt_u32 vcc, v75, v66\ns_cbranch_vccnz .Lso600\ns_branch .Lro600\n.Lso740:\n"
"ds_read_b32 v66, v74\ns_waitcnt lgkmcnt(0)\nv_cmp_gt_u32 vcc, v75, v66\ns_cbranch_vccnz .Lso740\ns_branch .Lro740\n.Lso880:\nds_read_b32 v66, v74\ns_waitcnt lgkmcnt(0)\n"
"v_cmp_gt_u32 vcc, v75, v66\ns_cbranch_vccnz .Lso880\ns_branch .Lro880\n.Lso1020:\nds_read_b32 v66, v74\ns_waitcnt lgkmcnt(0)\nv_cmp_gt_u32 vcc, v75, v66\ns_cbranch_vccnz .Lso1020\n"
"s_branch .Lro1020\n.Lso1160:\nds_read_b32 v66, v74\ns_waitcnt lgkmcnt(0)\nv_cmp_gt_u32 vcc, v75, v66\ns_cbranch_vccnz .Lso1160\ns_branch .Lro1160\n.Lso1300:\n"
"ds_read_b32 v66, v74\ns_waitcnt lgkmcnt(0)\nv_cmp_gt_u32 vcc, v75, v66\ns_cbranch_vccnz .Lso1300\ns_branch .Lro1300\n.Lso1440:\nds_read_b32 v66, v74\ns_waitcnt lgkmcnt(0)\n"
"v_cmp_gt_u32 vcc, v75, v66\ns_cbranch_vccnz .Lso1440\ns_branch .Lro1440\n.Lso1455:\nds_read_b32 v66, v74\ns_waitcnt lgkmcnt(0)\nv_cmp_gt_u32 vcc, v75, v66\ns_cbranch_vccnz .Lso1455\n"
"s_branch .Lro1455\n.Lso1525r0:\nds_read_b32 v66, v74\ns_waitcnt lgkmcnt(0)\nv_cmp_gt_u32 vcc, v75, v66\ns_cbranch_vccnz .Lso1525r0\ns_branch .Lro1525r0\n.Lso1595r0:\n"
"ds_read_b32 v66, v74\ns_waitcnt lgkmcnt(0)\nv_cmp_gt_u32 vcc, v75, v66\ns_cbranch_vccnz .Lso1595r0\ns_branch .Lro1595r0\n.Lso1665r0:\nds_read_b32 v66, v74\ns_waitcnt lgkmcnt(0)\n"
"v_cmp_gt_u32 vcc, v75, v66\ns_cbranch_vccnz .Lso1665r0\ns_branch .Lro1665r0\n.Lso1735r0:\nds_read_b32 v66, v74\ns_waitcnt lgkmcnt(0)\nv_cmp_gt_u32 vcc, v75, v66\ns_cbranch_vccnz .Lso1735r0\n"
"s_branch .Lro1735r0\n.Lso1805r0:\nds_read_b32 v66, v74\ns_waitcnt lgkmcnt(0)\nv_cmp_gt_u32 vcc, v75, v66\ns_cbranch_vccnz .Lso1805r0\ns_branch .Lro1805r0\n.Lso1878r0:\n"
"ds_read_b32 v66, v74\ns_waitcnt lgkmcnt(0)\nv_cmp_gt_u32 vcc, v75, v66\ns_cbranch_vccnz .Lso1878r0\ns_branch .Lro1878r0\n.Lso1920r0:\nds_read_b32 v66, v74\ns_waitcnt lgkmcnt(0)\n"
"v_cmp_gt_u32 vcc, v75, v66\ns_cbranch_vccnz .Lso1920r0\ns_branch .Lro1920r0\n.Lso1960r0:\nds_read_b32 v66, v74\ns_waitcnt lgkmcnt(0)\nv_cmp_gt_u32 vcc, v75, v66\ns_cbranch_vccnz .Lso1960r0\n"
"s_branch .Lro1960r0\n.Lso2046r0:\nds_read_b32 v66, v74\ns_waitcnt lgkmcnt(0)\nv_cmp_gt_u32 vcc, v75, v66\ns_cbranch_vccnz .Lso2046r0\ns_branch .Lro2046r0\n.Lso2088r0:\n"
"ds_read_b32 v66, v74\ns_waitcnt lgkmcnt(0)\nv_cmp_gt_u32 vcc, v75, v66\ns_cbranch_vccnz .Lso2088r0\ns_branch .Lro2088r0\n.Lso2172r0:\nds_read_b32 v66, v74\ns_waitcnt lgkmcnt(0)\n"
"v_cmp_gt_u32 vcc, v75, v66\ns_cbranch_vccnz .Lso2172r0\ns_branch .Lro2172r0\n.Lso2222r0:\nds_read_b32 v66, v74\ns_waitcnt lgkmcnt(0)\nv_cmp_gt_u32 vcc, v75, v66\ns_cbranch_vccnz .Lso2222r0\n"
"s_branch .Lro2222r0\n.Lso1525r1:\nds_read_b32 v66, v74\ns_waitcnt lgkmcnt(0)\nv_cmp_gt_u32 vcc, v75, v66\ns_cbranch_vccnz .Lso1525r1\ns_branch .Lro1525r1\n.Lso1595r1:\n"
"ds_read_b32 v66, v74\ns_waitcnt lgkmcnt(0)\nv_cmp_gt_u32 vcc, v75, v66\ns_cbranch_vccnz .Lso1595r1\ns_branch .Lro1595r1\n.Lso1665r1:\nds_read_b32 v66, v74\ns_waitcnt lgkmcnt(0)\n"
"v_cmp_gt_u32 vcc, v75, v66\ns_cbranch_vccnz .Lso1665r1\ns_branch .Lro1665r1\n.Lso1735r1:\nds_read_b32 v66, v74\ns_waitcnt lgkmcnt(0)\nv_cmp_gt_u32 vcc, v75, v66\ns_cbranch_vccnz .Lso1735r1\n"
"s_branch .Lro1735r1\n.Lso1805r1:\nds_read_b32 v66, v74\ns_waitcnt lgkmcnt(0)\nv_cmp_gt_u32 vcc, v75, v66\ns_cbranch_vccnz .Lso1805r1\ns_branch .Lro1805r1\n.Lso1840r1:\n"
"ds_read_b32 v66, v74\ns_waitcnt lgkmcnt(0)\nv_cmp_gt_u32 vcc, v75, v66\ns_cbranch_vccnz .Lso1840r1\ns_branch .Lro1840r1\n.Lso1878r1:\nds_read_b32 v66, v74\ns_waitcnt lgkmcnt(0)\n"
"v_cmp_gt_u32 vcc, v75, v66\ns_cbranch_vccnz .Lso1878r1\ns_branch .Lro1878r1\n.Lso1920r1:\nds_read_b32 v66, v74\ns_waitcnt lgkmcnt(0)\nv_cmp_gt_u32 vcc, v75, v66\ns_cbranch_vccnz .Lso1920r1\n"
"s_branch .Lro1920r1\n.Lso1945r1:\nds_read_b32 v66, v74\ns_waitcnt lgkmcnt(0)\nv_cmp_gt_u32 vcc, v75, v66\ns_cbranch_vccnz .Lso1945r1\ns_branch .Lro1945r1\n.Lso2004r1:\n"
"ds_read_b32 v66, v74\ns_waitcnt lgkmcnt(0)\nv_cmp_gt_u32 vcc, v75, v66\ns_cbranch_vccnz .Lso2004r1\ns_branch .Lro2004r1\n.Lso2045r1:\nds_read_b32 v66, v74\ns_waitcnt lgkmcnt(0)\n"
"v_cmp_gt_u32 vcc, v75, v66\ns_cbranch_vccnz .Lso2045r1\ns_branch .Lro2045r1\n.Lso2130r1:\nds_read_b32 v66, v74\ns_waitcnt lgkmcnt(0)\nv_cmp_gt_u32 vcc, v75, v66\ns_cbranch_vccnz .Lso2130r1\n"
"s_branch .Lro2130r1\n.Lso2172r1:\nds_read_b32 v66, v74\ns_waitcnt lgkmcnt(0)\nv_cmp_gt_u32 vcc, v75, v66\ns_cbranch_vccnz .Lso2172r1\ns_branch .Lro2172r1\n.Lso2225r1:\n"
"ds_read_b32 v66, v74\ns_waitcnt lgkmcnt(0)\nv_cmp_gt_u32 vcc, v75, v66\ns_cbranch_vccnz .Lso2225r1\ns_branch .Lro2225r1\n.Ltail1:\n.Lpg11:\ns_cbranch_execz .Ltr10\n"
"Qcvaa 0,128,32,56\nv_add_u32 v75, 1, v75\nQmvaa 0,132,36\ns_waitcnt vmcnt(0)\nds_write_b32 v73, v75\nQmvaa 0,136,40\nQe 8\nQmvaa 0,140,44\n"
"Qe 9\nQcvaa 4,124,44,56\nQe 10\nQmvaa 4,120,40\nQe 11\nQmvaa 4,116,36\nQe 12\nQmvaa 4,112,32\n"
"s_waitcnt lgkmcnt(1)\nQk 40,8,9\nQmvva 96,44,144\nQe 13\nQmvva 96,36,148\nQe 14\nQmvva 100,44,152\nQe 15\n"
"Qmvva 100,36,156\nQk 41,10,11\nQk 42,12,13\nQmvva 104,44,160\nQk 43,14,15\nQmvva 104,36,164\nQe 0\nQmvva 108,44,168\n"
"Qe 1\nQmvva 108,36,172\nQe 2\nQmvva 140,48,168\nQe 3\nQmvva 140,40,172\nQk 46,0,1\nQk 47,2,3\n"
"Qmvva 136,48,160\nds_read_b32 v66, v74\nQmvva 136,40,164\nQe 4\nQmvva 132,48,152\nQe 5\nQmvva 132,40,156\nQe 6\n"
"Qmvva 128,48,144\nQe 7\nQmvva 128,40,148\ns_waitcnt lgkmcnt(0)\nv_cmp_gt_u32 vcc, v75, v66\nv_smfmac_f32_16x16x64_bf16 a[176:179], a[204:207], v[36:43], v77\ns_cbranch_vccnz .Lsa30x\n.Lra30x:\n"
"Qcvaa 8,112,48,56\ns_add_u32 m0, s96, 0\ns_add_u32 s95, s87, 4096\nQk 44,4,5\nQmvaa 8,116,52\nbuffer_load_dwordx4 v61, s[52:55], s95 offen lds\nQmvaa 8,120,56\nQk 45,6,7\n"
"Qmvaa 8,124,60\nQcvaa 12,140,60,56\nQmvaa 12,136,56\ns_add_u32 s95, s87, 23552\nQmvaa 12,132,52\nbuffer_load_dwordx4 v61, s[52:55], s95 offen offset:1024 lds\nQmvaa 12,128,48\nQcvaa 0,128,64,56\n"
"Qmvaa 0,132,68\nQe 8\nQmvaa 0,136,72\ns_add_u32 m0, s96, 8192\ns_add_u32 s95, s87, 45056\nQe 9\nQmvaa 0,140,76\nbuffer_load_dwordx4 v61, s[52:55], s95 offen lds\n"
"Qcvaa 4,124,76,56\nQe 10\nQmvaa 4,120,72\nQe 11\nQmvaa 4,116,68\nQe 12\nQmvaa 4,112,64\ns_add_u32 s95, s87, 64512\n"
"Qe 13\nQmvva 160,52,144\nbuffer_load_dwordx4 v61, s[52:55], s95 offen offset:1024 lds\nQmvva 160,44,148\nQe 14\nQmvva 164,52,152\nQe 15\nQmvva 164,44,156\n"
"Qk 48,8,9\nQk 49,10,11\nQmvva 168,52,160\ns_add_u32 m0, s96, 16384\ns_add_u32 s95, s87, 4096\nQk 50,12,13\nQmvva 168,44,164\nbuffer_load_dwordx4 v62, s[52:55], s95 offen lds\n"
"Qmvva 172,52,168\nQk 51,14,15\nQmvva 172,44,172\nQe 0\nQmvva 204,20,168\nQe 1\nQmvva 204,48,172\ns_add_u32 s95, s87, 44032\n"
"Qe 2\nQmvva 200,20,160\nbuffer_load_dwordx4 v62, s[52:55], s95 offen offset:1024 lds\nQmvva 200,48,164\nQe 3\nQmvva 196,20,152\nQk 18,0,1\nQk 19,2,3\n"
"Qmvva 196,48,156\nQe 4\nQmvva 192,20,144\ns_add_u32 m0, s96, 24576\ns_add_u32 s95, s87, 4096\nQe 5\nQmvva 192,48,148\nbuffer_load_dwordx4 v63, s[52:55], s95 offen lds\n"
"v_smfmac_f32_16x16x64_bf16 a[180:183], a[204:207], v[44:51], v77\nQk 16,4,5\nQcvaa 8,112,80,56\nQe 6\nQmvaa 8,116,84\nQe 7\nQmvaa 8,120,88\ns_add_u32 s95, s87, 23552\n"
"Qk 17,6,7\nQmvaa 8,124,92\nbuffer_load_dwordx4 v63, s[52:55], s95 offen offset:1024 lds\nQcvaa 12,140,92,56\nQmvaa 12,136,88\nQmvaa 12,132,84\nQmvaa 12,128,80\ns_add_u32 m0, s96, 32768\n"
"s_add_u32 s95, s87, 45056\nQcvaa 0,128,96,56\nQda 128,68,34048\nbuffer_load_dwordx4 v63, s[52:55], s95 offen lds\nQmvaa 0,132,100\nQda 132,68,34560\nQmvaa 0,136,104\nQda 136,68,34304\n"
"Qmvaa 0,140,108\nQda 140,68,34816\nQcvaa 4,124,108,56\nQda 124,68,33792\ns_add_u32 s95, s87, 64512\nQmvaa 4,120,104\nQda 120,68,33280\nbuffer_load_dwordx4 v63, s[52:55], s95 offen offset:1024 lds\n"
"Qmvaa 4,116,100\nQda 116,68,33536\nQmvaa 4,112,96\nQda 112,68,33024\nQmvva 224,24,144\nQe 8\nQmvva 224,16,148\ns_add_u32 m0, s96, 40960\n"
"s_mov_b32 s95, 4096\nQe 9\nQmvva 228,24,152\nbuffer_load_dwordx4 v61, s[52:55], s95 offen lds\nQe 10\nQmvva 228,16,156\nQe 11\nQmvva 232,24,160\n"
"Qk 20,8,9\nQk 21,10,11\nQmvva 232,16,164\nQe 12\nQmvva 236,24,168\ns_mov_b32 s95, 23552\nQe 13\nQmvva 236,16,172\n"
"buffer_load_dwordx4 v61, s[52:55], s95 offen offset:1024 lds\ns_waitcnt lgkmcnt(0)\nQk 22,12,13\nQcvaa 8,112,0,56\nQe 14\nQmvaa 8,116,4\nv_subrev_u32 v68, 66048, v68\nQmvaa 8,120,8\n"
"Qe 15\nQmvaa 8,124,12\ns_add_u32 m0, s96, 49152\ns_mov_b32 s95, 45056\nQk 23,14,15\nQcvaa 12,140,12,56\nbuffer_load_dwordx4 v61, s[52:55], s95 offen lds\nQe 0\n"
"Qmvaa 12,136,8\nQe 1\nQmvaa 12,132,4\nQe 2\nQmvaa 12,128,0\nQe 3\nv_smfmac_f32_16x16x64_bf16 a[184:187], a[204:207], v[16:23], v77\ns_mov_b32 s95, 64512\n"
"Qk 26,0,1\nQmava 208,28,144\nbuffer_load_dwordx4 v61, s[52:55], s95 offen offset:1024 lds\nQe 4\nQmava 208,20,148\nQe 5\nQmava 212,28,152\nQe 6\n"
"Qmava 212,20,156\nQe 7\nQmava 216,28,160\ns_add_u32 m0, s96, 57344\ns_mov_b32 s95, 4096\nQk 27,2,3\nQk 24,4,5\nQmava 216,20,164\n"
"buffer_load_dwordx4 v62, s[52:55], s95 offen lds\nQk 25,6,7\nQmava 220,28,168\nQe 8\nQmava 220,20,172\nQe 9\nQmava 252,32,168\nQta 168,71,768\n"
"Qta 170,71,1792\nQmava 252,24,172\ns_mov_b32 s95, 44032\nQe 10\nQmava 248,32,160\nQta 172,71,33792\nQta 174,71,34816\nbuffer_load_dwordx4 v62, s[52:55], s95 offen offset:1024 lds\n"
"Qmava 248,24,164\nQe 11\nQmava 244,32,152\nQta 160,71,512\nQta 162,71,1536\nQmava 244,24,156\nQk 28,8,9\nQk 29,10,11\n"
"Qmava 240,32,144\nQta 164,71,33536\nQta 166,71,34560\ns_add_u32 m0, s96, 132096\ns_mov_b32 s95, 4096\nQmava 240,24,148\nbuffer_load_dwordx4 v63, s[52:55], s95 offen lds\nQe 12\n"
"Qcvaa 0,112,16,56\nQta 152,71,256\nQta 154,71,1280\nQmvaa 0,116,20\nQe 13\nQmvaa 0,120,24\nQta 156,71,33280\nQta 158,71,34304\n"
"Qmvaa 0,124,28\ns_mov_b32 s95, 23552\nQe 14\nQcvaa 4,140,28,56\nQta 144,71,0\nQta 146,71,1024\nbuffer_load_dwordx4 v63, s[52:55], s95 offen offset:1024 lds\nQmvaa 4,136,24\n"
"Qe 15\nQmvaa 4,132,20\nQta 148,71,33024\nQta 150,71,34048\nQmvaa 4,128,16\nQk 30,12,13\nQk 31,14,15\nQcvaa 8,128,32,56\n"
"v_add_u32 v75, 1, v75\nQmvaa 8,132,36\nds_write_b32 v73, v75\nQmvaa 8,136,40\nQe 0\nQmvaa 8,140,44\nQe 1\nQcvaa 12,124,44,56\n"
"Qe 2\nQmvaa 12,120,40\nQe 3\nQmvaa 12,116,36\nQe 4\nQmvaa 12,112,32\ns_waitcnt lgkmcnt(1)\nQk 32,0,1\n"
"v_smfmac_f32_16x16x64_bf16 a[188:191], a[204:207], v[24:31], v77\nQk 33,2,3\nQmvva 80,36,144\nQe 5\nQmvva 80,28,148\nQe 6\nQmvva 84,36,152\nQe 7\n"
"Qmvva 84,28,156\nQk 34,4,5\nQk 35,6,7\nQmvva 88,36,160\nQe 8\nQmvva 88,28,164\nQe 9\nQmvva 92,36,168\n"
"Qe 10\nQmvva 92,28,172\nQe 11\nQmvva 124,40,168\nQk 38,8,9\nQk 39,10,11\nQmvva 124,32,172\nds_read_b32 v66, v74\n"
"Qmvva 120,40,160\nQe 12\nQmvva 120,32,164\nQe 13\nQmvva 116,40,152\nQe 14\nQmvva 116,32,156\nQe 15\n"
"Qmvva 112,40,144\ns_waitcnt lgkmcnt(0)\nv_cmp_gt_u32 vcc, v75, v66\nQmvva 112,32,148\ns_cbranch_vccnz .Lsa31x\n.Lra31x:\nQk 36,12,13\nQcvaa 0,112,48,56\n"
"s_add_u32 m0, s96, 140288\ns_mov_b32 s95, 45056\nQk 37,14,15\nQmvaa 0,116,52\nbuffer_load_dwordx4 v63, s[52:55], s95 offen lds\nQmvaa 0,120,56\nQmvaa 0,124,60\nQcvaa 4,140,60,56\n"
"Qmvaa 4,136,56\ns_mov_b32 s95, 64512\nQmvaa 4,132,52\nbuffer_load_dwordx4 v63, s[52:55], s95 offen offset:1024 lds\nQmvaa 4,128,48\nQcvaa 8,128,64,56\nQmvaa 8,132,68\nQe 0\n"
"Qmvaa 8,136,72\ns_add_u32 m0, s96, 148480\ns_add_u32 s95, s87, 86016\nQe 1\nQmvaa 8,140,76\nbuffer_load_dwordx4 v61, s[52:55], s95 offen lds\nQcvaa 12,124,76,56\nQe 2\n"
"Qmvaa 12,120,72\nQe 3\nQmvaa 12,116,68\nQe 4\nQmvaa 12,112,64\nQe 5\nv_smfmac_f32_16x16x64_bf16 a[192:195], a[204:207], v[32:39], v77\ns_add_u32 s95, s87, 105472\n"
"Qk 40,0,1\nQmvva 144,44,144\nbuffer_load_dwordx4 v61, s[52:55], s95 offen offset:1024 lds\nQmvva 144,36,148\nQe 6\nQmvva 148,44,152\nQe 7\nQmvva 148,36,156\n"
"s_add_u32 m0, s96, 65536\ns_add_u32 s95, s87, 126976\nQk 41,2,3\nQmvva 152,44,160\nbuffer_load_dwordx4 v61, s[52:55], s95 offen lds\nQmvva 152,36,164\nQk 42,4,5\nQk 43,6,7\n"
"Qmvva 156,44,168\nQe 8\nQmvva 156,36,172\nQe 9\nQmvva 188,48,168\ns_add_u32 s95, s87, 146432\nQe 10\nQmvva 188,40,172\n"
"buffer_load_dwordx4 v61, s[52:55], s95 offen offset:1024 lds\n.Lpg12:\ns_cbranch_execz .Ltr6\nQmvva 184,48,160\nQe 11\nQmvva 184,40,164\nQk 46,8,9\nQk 47,10,11\n"
"Qmvva 180,48,152\nQe 12\nQmvva 180,40,156\ns_add_u32 m0, s96, 73728\ns_add_u32 s95, s87, 86016\nQe 13\nQmvva 176,48,144\nbuffer_load_dwordx4 v62, s[52:55], s95 offen lds\n"
"Qmvva 176,40,148\nQe 14\nQcvaa 0,112,80,56\nQe 15\nQmvaa 0,116,84\nQk 44,12,13\nQk 45,14,15\nQmvaa 0,120,88\n"
"s_add_u32 s95, s87, 125952\nQmvaa 0,124,92\nbuffer_load_dwordx4 v62, s[52:55], s95 offen offset:1024 lds\nQcvaa 4,140,92,56\nQmvaa 4,136,88\nQmvaa 4,132,84\nQmvaa 4,128,80\ns_add_u32 m0, s96, 81920\n"
"s_add_u32 s95, s87, 86016\nQcvaa 8,128,96,56\nbuffer_load_dwordx4 v63, s[52:55], s95 offen lds\nQmvaa 8,132,100\nQmvaa 8,136,104\nQmvaa 8,140,108\nQcvaa 12,124,108,56\ns_add_u32 s95, s87, 105472\n"
"Qmvaa 12,120,104\nbuffer_load_dwordx4 v63, s[52:55], s95 offen offset:1024 lds\nQmvaa 12,116,100\nQmvaa 12,112,96\nv_smfmac_f32_16x16x64_bf16 a[196:199], a[204:207], v[40:47], v77\nQmvva 208,16,144\nQta 112,71,33152\ns_add_u32 m0, s96, 90112\n"
"s_add_u32 s95, s87, 126976\nQe 0\nQmvva 208,44,148\nQta 114,71,34176\nbuffer_load_dwordx4 v63, s[52:55], s95 offen lds\nQe 1\nQmvva 212,16,152\nQta 116,71,33408\n"
"Qe 2\nQmvva 212,44,156\nQta 118,71,34432\nQe 3\nQmvva 216,16,160\nQta 120,71,33664\nQk 48,0,1\nQk 49,2,3\n"
"Qmvva 216,44,164\nQta 122,71,34688\ns_add_u32 s95, s87, 146432\nQe 4\nQmvva 220,16,168\nQta 124,71,33920\nbuffer_load_dwordx4 v63, s[52:55], s95 offen offset:1024 lds\nQe 5\n"
"Qmvva 220,44,172\nQta 126,71,34944\nQk 50,4,5\nQe 6\nQe 7\ns_nop 0\nQk 51,6,7\nQe 8\n"
"Qe 9\nQe 10\nQe 11\nQe 12\nQmvva 240,20,144\nQe 13\nQmvva 240,48,148\nQe 14\n"
"Qmvva 244,20,152\nQe 15\nQmvva 244,48,156\nQk 54,8,9\nQk 55,10,11\nQmvva 248,20,160\nQk 52,12,13\nQk 53,14,15\n"
"Qmvva 248,48,164\nQmvva 252,20,168\nQmvva 252,48,172\nv_smfmac_f32_16x16x64_bf16 a[200:203], a[204:207], v[48:55], v77\nQmava 224,24,144\nv_accvgpr_read_b32 v0, a176\nv_accvgpr_read_b32 v1, a177\nQmava 224,52,148\n"
"v_accvgpr_read_b32 v6, a178\nv_accvgpr_read_b32 v7, a179\nQmava 228,24,152\nv_accvgpr_read_b32 v8, a180\nv_accvgpr_read_b32 v9, a181\nQmava 228,52,156\nv_accvgpr_read_b32 v14, a182\nv_accvgpr_read_b32 v15, a183\n"
"Qmava 232,24,160\nv_accvgpr_read_b32 v16, a184\nv_accvgpr_read_b32 v17, a185\nQmava 232,52,164\nv_accvgpr_read_b32 v22, a186\nv_accvgpr_read_b32 v23, a187\nQmava 236,24,168\nv_accvgpr_read_b32 v2, a190\n"
"v_accvgpr_read_b32 v3, a191\nQmava 236,52,172\ns_waitcnt lgkmcnt(0)\nv_accvgpr_read_b32 v24, a188\nQmvva 96,28,112\nv_accvgpr_read_b32 v25, a189\nv_accvgpr_read_b32 v4, a192\nQmvva 100,28,116\n"
"v_accvgpr_read_b32 v5, a193\nv_accvgpr_read_b32 v10, a194\nQmvva 104,28,120\nv_accvgpr_read_b32 v11, a195\nv_accvgpr_read_b32 v12, a196\nQmvva 108,28,124\nv_accvgpr_read_b32 v13, a197\nv_accvgpr_read_b32 v18, a198\n"
"Qmvva 128,32,112\nv_accvgpr_read_b32 v19, a199\nv_accvgpr_read_b32 v20, a200\nQmvva 132,32,116\nv_accvgpr_read_b32 v21, a201\nv_accvgpr_read_b32 v26, a202\nQmvva 136,32,120\nv_accvgpr_read_b32 v27, a203\n"
"Qa 8,8,9\nQmvva 140,32,124\nQa 10,10,11\nQa 8,8,10\nQmvva 160,36,112\nQa 16,16,17\nQa 18,18,19\nQmvva 164,36,116\n"
"Qa 16,16,18\nQa 4,4,5\nQmvva 168,36,120\nQa 6,6,7\nQa 4,4,6\nQmvva 172,36,124\nQa 12,12,13\nQa 14,14,15\n"
"Qmvva 192,40,112\nQa 12,12,14\nQa 0,0,1\nQmvva 196,40,116\nQa 2,2,3\nQa 0,0,2\nQmvva 200,40,120\nQa 24,24,25\n"
"Qa 26,26,27\nQmvva 204,40,124\nQa 24,24,26\nQa 20,20,21\nQmvva 224,44,112\nQa 22,22,23\nQa 20,20,22\nQmvva 228,44,116\n"
"v_max3_u32 v9, v8, v16, v4\nv_max3_u32 v9, v9, v12, v0\nQmvva 232,44,120\nv_max3_u32 v9, v9, v24, v20\nv_min3_u32 v11, v8, v16, v4\nQmvva 236,44,124\nv_min3_u32 v11, v11, v12, v0\nv_min3_u32 v11, v11, v24, v20\n"
"Qmava 208,48,112\nQmava 212,48,116\nQmava 216,48,120\nQmava 220,48,124\nQmava 240,52,112\nQmava 244,52,116\nQmava 248,52,120\nQmava 252,52,124\n"
"v_cmp_lt_u32 vcc, 0x44800000, v9\ns_mov_b64 s[74:75], vcc\nv_cmp_gt_u32 vcc, 0x2b800000, v11\ns_or_b64 s[74:75], s[74:75], vcc\ns_cmp_lg_u64 s[74:75], 0\ns_cbranch_scc1 .Lfb\n.Lfbret1:\ns_mov_b32 s56, %8\n"
"s_and_b32 s57, %9, 0xffff\ns_mov_b32 s58, 1835008\ns_lshl_b32 s68, %19, 4\ns_cmp_eq_u32 s70, 0\ns_cselect_b32 s68, s68, -1\nv_mov_b32 v77, 0x7ffff000\nv_cmp_eq_u32 vcc, s68, v60\ns_nop 1\n"
"v_cndmask_b32 v77, v77, v76, vcc\ns_lshl_b32 s68, s93, 12\ns_add_u32 s68, s68, %10\nv_lshrrev_b32 v66, 9, v60\nv_lshlrev_b32 v66, 11, v66\nv_bfe_u32 v67, v60, 1, 8\nv_add3_u32 v68, v66, v67, s68\nv_add_u32 v69, 65536, v68\n"
"v_add_u32 v70, 131072, v68\ns_mov_b64 exec, 0\ns_cmp_eq_u32 s70, 0\ns_cbranch_scc1 .Lpg16\ns_cmp_eq_u32 s70, 1\ns_cbranch_scc1 .Lpg17\ns_cmp_eq_u32 s70, 2\ns_cbranch_scc1 .Lpg18\n"
"s_branch .Lpg19\n.Ltr8:\ns_mov_b64 exec, -1\nv_rcp_f32 v2, v0\nv_rcp_f32 v6, v4\nv_rcp_f32 v10, v8\nv_rcp_f32 v14, v12\nv_rcp_f32 v18, v16\n"
"v_rcp_f32 v22, v20\nv_rcp_f32 v26, v24\nv_and_b32 v66, 16, v60\nv_mov_b32 v67, 0xffff\nv_lshlrev_b32 v66, v66, v67\ns_mov_b32 vcc_lo, 0x00c0000c\ns_mov_b32 vcc_hi, 0xc0000c00\nv_cndmask_b32 v67, 0, v66, vcc\n"
"s_lshr_b64 vcc, vcc, 2\nv_cndmask_b32 v66, 0, v66, vcc\nv_mov_b64 v[0:1], 0\nQk 2,2,2\nv_and_b32 v3, v2, v67\nv_and_b32 v2, v2, v66\nv_mov_b64 v[4:5], 0\nQk 6,6,6\n"
"v_and_b32 v7, v6, v67\nv_and_b32 v6, v6, v66\nv_mov_b64 v[8:9], 0\nQk 10,10,10\nv_and_b32 v11, v10, v67\nv_and_b32 v10, v10, v66\nv_mov_b64 v[12:13], 0\nQk 14,14,14\n"
"v_and_b32 v15, v14, v67\nv_and_b32 v14, v14, v66\nv_mov_b64 v[16:17], 0\nQk 18,18,18\nv_and_b32 v19, v18, v67\nv_and_b32 v18, v18, v66\nv_mov_b64 v[20:21], 0\nQk 22,22,22\n"
"v_and_b32 v23, v22, v67\nv_and_b32 v22, v22, v66\nv_mov_b64 v[24:25], 0\nQk 26,26,26\nv_and_b32 v27, v26, v67\nv_and_b32 v26, v26, v66\nv_mov_b64 v[28:29], 0\nQk 80,80,81\n"
"Qk 81,82,83\nQk 82,84,85\nQk 83,86,87\ns_nop 1\nQzvvv 84,80,0\nQzvvv 80,80,2\nQk 112,112,113\nQk 113,114,115\n"
"Qk 114,116,117\nQk 115,118,119\ns_nop 1\nQzvvv 116,112,4\nQzvvv 112,112,6\nQk 144,144,145\nQk 145,146,147\nQk 146,148,149\n"
"Qk 147,150,151\nQk 80,80,81\nQk 81,82,83\nQzvvv 148,144,8\nQk 82,84,85\nQk 83,86,87\nQzvvv 144,144,10\nQk 176,176,177\n"
"Qk 177,178,179\nQk 178,180,181\nQk 179,182,183\nQk 112,112,113\nQk 113,114,115\nQzvvv 180,176,12\nQk 114,116,117\nQk 115,118,119\n"
"Qzvvv 176,176,14\nQk 208,208,209\nQk 209,210,211\nQk 210,212,213\nQk 211,214,215\nQk 144,144,145\nQk 145,146,147\nQzvvv 212,208,16\n"
"Qk 146,148,149\nQk 147,150,151\nQzvvv 208,208,18\nQk 240,240,241\nQk 241,242,243\nQk 242,244,245\nQk 243,246,247\nQk 176,176,177\n"
"Qk 177,178,179\nQzvvv 244,240,20\nQk 178,180,181\nQk 179,182,183\nQzvvv 240,240,22\nv_accvgpr_read_b32 v84, a224\nv_accvgpr_read_b32 v85, a225\nv_accvgpr_read_b32 v86, a226\n"
"v_accvgpr_read_b32 v87, a227\nv_accvgpr_read_b32 v32, a228\nv_accvgpr_read_b32 v33, a229\nv_accvgpr_read_b32 v34, a230\nv_accvgpr_read_b32 v35, a231\nQk 84,84,85\nQk 85,86,87\nQk 86,32,33\n"
"Qk 87,34,35\nQk 208,208,209\nQk 209,210,211\nQzvvv 32,84,24\nQk 210,212,213\nQk 211,214,215\nQzvvv 84,84,26\nQk 88,88,89\n"
"Qk 89,90,91\nQk 90,92,93\nQk 91,94,95\nQk 240,240,241\nQk 241,242,243\nQzvvv 92,88,0\nQk 242,244,245\nQk 243,246,247\n"
"Qzvvv 88,88,2\nQk 120,120,121\nQk 121,122,123\nQk 122,124,125\nQk 123,126,127\nQk 84,84,85\nQk 85,86,87\nQzvvv 124,120,4\n"
"Qk 86,32,33\nQk 87,34,35\nQzvvv 120,120,6\nQk 152,152,153\nQk 153,154,155\nQk 154,156,157\nQk 155,158,159\nQk 88,88,89\n"
"Qk 89,90,91\nQzvvv 156,152,8\nQk 90,92,93\nQk 91,94,95\nQzvvv 152,152,10\ns_waitcnt vmcnt(20)\ns_barrier\nQk 184,184,185\n"
"Qk 185,186,187\nQk 186,188,189\nQk 187,190,191\nQk 120,120,121\nQk 121,122,123\nQzvvv 188,184,12\nQk 122,124,125\nQk 123,126,127\n"
"Qzvvv 184,184,14\nQta 56,68,0\nQk 216,216,217\nQk 217,218,219\nQk 218,220,221\nQk 219,222,223\nQk 152,152,153\nQk 153,154,155\n"
"Qzvvv 220,216,16\nQk 154,156,157\nQk 155,158,159\nQzvvv 216,216,18\nQta 58,68,1024\nQk 248,248,249\nQk 249,250,251\nQk 250,252,253\n"
"Qk 251,254,255\nQk 184,184,185\nQk 185,186,187\nQzvvv 252,248,20\nQk 186,188,189\nQk 187,190,191\nQzvvv 248,248,22\nQta 60,68,8192\n"
"v_accvgpr_read_b32 v116, a232\nv_accvgpr_read_b32 v117, a233\nv_accvgpr_read_b32 v118, a234\nv_accvgpr_read_b32 v119, a235\nv_accvgpr_read_b32 v36, a236\nv_accvgpr_read_b32 v37, a237\nv_accvgpr_read_b32 v38, a238\nv_accvgpr_read_b32 v39, a239\n"
"Qk 116,116,117\nQk 117,118,119\nQk 118,36,37\nQk 119,38,39\nQk 216,216,217\nQk 217,218,219\nQzvvv 36,116,24\nQk 218,220,221\n"
"Qk 219,222,223\nQzvvv 116,116,26\nQta 62,68,9216\ns_mov_b32 s31, 0\nQk 96,96,97\nQk 97,98,99\nQk 98,100,101\nQk 99,102,103\n"
"Qk 248,248,249\nQk 249,250,251\nQzvvv 100,96,0\nQk 250,252,253\nQk 251,254,255\nQzvvv 96,96,2\nbuffer_load_dwordx4 a[0:3], v60, s[48:51], s31 offen offset:0\nQta 64,68,256\n"
"Qk 128,128,129\nQk 129,130,131\nQk 130,132,133\nQk 131,134,135\nQk 116,116,117\nQk 117,118,119\nQzvvv 132,128,4\nQk 118,36,37\n"
"Qk 119,38,39\nQzvvv 128,128,6\nbuffer_load_dwordx4 a[4:7], v60, s[48:51], s31 offen offset:1024\nQta 66,68,1280\ns_waitcnt lgkmcnt(2)\nQzvav 56,56,80\nQmvav 56,60,88\nQk 160,160,161\n"
"Qk 161,162,163\nQk 162,164,165\nQk 163,166,167\nQk 96,96,97\nQk 97,98,99\nQzvvv 164,160,8\nQk 98,100,101\nQk 99,102,103\n"
"Qzvvv 160,160,10\nbuffer_load_dwordx4 a[8:11], v60, s[48:51], s31 offen offset:2048\nQta 68,68,8448\nQk 192,192,193\nQk 193,194,195\nQk 194,196,197\nQk 195,198,199\nQk 128,128,129\n"
"Qk 129,130,131\nQzvvv 196,192,12\nQk 130,132,133\nQk 131,134,135\nQzvvv 192,192,14\nbuffer_load_dwordx4 a[12:15], v60, s[48:51], s31 offen offset:3072\nQta 70,68,9472\nv_max3_f32 v66, |v56|, |v57|, |v58|\n"
"v_max_f32 v66, v66, |v59|\nv_mov_b32 v67, v66\ns_mov_b32 s31, 4096\nQk 224,224,225\nQk 225,226,227\nQk 226,228,229\nQk 227,230,231\nQk 160,160,161\n"
"Qk 161,162,163\nQzvvv 228,224,16\nQk 162,164,165\nQk 163,166,167\nQzvvv 224,224,18\nbuffer_load_dwordx4 a[16:19], v60, s[48:51], s31 offen offset:0\nv_permlane32_swap_b32 v66, v67\nv_max_f32 v66, v66, v67\n"
"v_mov_b32 v67, v66\nv_accvgpr_read_b32 v148, a208\nv_accvgpr_read_b32 v149, a209\nv_accvgpr_read_b32 v150, a210\nv_accvgpr_read_b32 v151, a211\nv_accvgpr_read_b32 v36, a212\nv_accvgpr_read_b32 v37, a213\nv_accvgpr_read_b32 v38, a214\n"
"v_accvgpr_read_b32 v39, a215\nQk 148,148,149\nQk 149,150,151\nQk 150,36,37\nQk 151,38,39\nQk 192,192,193\nQk 193,194,195\nQzvvv 36,148,20\n"
"Qk 194,196,197\nQk 195,198,199\nQzvvv 148,148,22\nbuffer_load_dwordx4 a[20:23], v60, s[48:51], s31 offen offset:1024\nv_permlane16_swap_b32 v66, v67\nv_max_f32 v66, v66, v67\nv_accvgpr_read_b32 v180, a240\nv_accvgpr_read_b32 v181, a241\n"
"v_accvgpr_read_b32 v182, a242\nv_accvgpr_read_b32 v183, a243\nv_accvgpr_read_b32 v40, a244\nv_accvgpr_read_b32 v41, a245\nv_accvgpr_read_b32 v42, a246\nv_accvgpr_read_b32 v43, a247\nQk 180,180,181\nQk 181,182,183\n"
"Qk 182,40,41\nQk 183,42,43\nQk 224,224,225\nQk 225,226,227\nQzvvv 40,180,24\nQk 226,228,229\nQk 227,230,231\nQzvvv 180,180,26\n"
"buffer_load_dwordx4 a[24:27], v60, s[48:51], s31 offen offset:2048\nv_max_f32_dpp v66, v66, v66 row_ror:8 row_mask:0xf bank_mask:0xf\ns_mov_b32 s31, 14336\nQk 104,104,105\nQk 105,106,107\nQk 106,108,109\nQk 107,110,111\nQk 148,148,149\n"
"Qk 149,150,151\nQzvvv 108,104,0\nQk 150,36,37\nQk 151,38,39\nQzvvv 104,104,2\nbuffer_load_dwordx4 a[28:31], v60, s[48:51], s31 offen offset:0\nv_max_f32_dpp v66, v66, v66 row_ror:4 row_mask:0xf bank_mask:0xf\nQk 136,136,137\n"
"Qk 137,138,139\nQk 138,140,141\nQk 139,142,143\nQk 180,180,181\nQk 181,182,183\nQzvvv 140,136,4\nQk 182,40,41\nQk 183,42,43\n"
"Qzvvv 136,136,6\nbuffer_load_dwordx4 a[32:35], v60, s[48:51], s31 offen offset:1024\nv_max_f32_dpp v66, v66, v66 row_ror:2 row_mask:0xf bank_mask:0xf\nQk 168,168,169\nQk 169,170,171\nQk 170,172,173\nQk 171,174,175\nQk 104,104,105\n"
"Qk 105,106,107\nQzvvv 172,168,8\nQk 106,108,109\nQk 107,110,111\nQzvvv 168,168,10\n.Lpg13:\ns_cbranch_execz .Ltr6\nbuffer_load_dwordx4 a[36:39], v60, s[48:51], s31 offen offset:2048\n"
"v_max_f32_dpp v66, v66, v66 row_ror:1 row_mask:0xf bank_mask:0xf\nv_and_b32 v66, 0x7f800000, v66\nv_add_u32 v66, 0x2000000, v66\nQk 200,200,201\nQk 201,202,203\nQk 202,204,205\nQk 203,206,207\nQk 136,136,137\n"
"Qk 137,138,139\nQzvvv 204,200,12\nQk 138,140,141\nQk 139,142,143\nQzvvv 200,200,14\nbuffer_load_dwordx4 a[40:43], v60, s[48:51], s31 offen offset:3072\nv_min_u32 v66, 0x7e000000, v66\nv_or_b32 v66, 0x400000, v66\n"
"ds_write_b32 v73, v66 offset:16\nv_xor_b32 v67, 0x80000000, v66\ns_mov_b32 s31, 18432\nQk 232,232,233\nQk 233,234,235\nQk 234,236,237\nQk 235,238,239\nQk 168,168,169\n"
"Qk 169,170,171\nQzvvv 236,232,16\nQk 170,172,173\nQk 171,174,175\nQzvvv 232,232,18\nbuffer_load_dwordx4 a[44:47], v60, s[48:51], s31 offen offset:0\nv_accvgpr_write_b32 a112, v67\nv_accvgpr_write_b32 a113, v67\n"
"v_accvgpr_write_b32 a114, v67\nv_accvgpr_write_b32 a115, v67\nv_accvgpr_read_b32 v212, a216\nv_accvgpr_read_b32 v213, a217\nv_accvgpr_read_b32 v214, a218\nv_accvgpr_read_b32 v215, a219\nv_accvgpr_read_b32 v40, a220\nv_accvgpr_read_b32 v41, a221\n"
"v_accvgpr_read_b32 v42, a222\nv_accvgpr_read_b32 v43, a223\nQk 212,212,213\nQk 213,214,215\nQk 214,40,41\nQk 215,42,43\nQk 200,200,201\nQk 201,202,203\n"
"Qzvvv 40,212,20\nQk 202,204,205\nQk 203,206,207\nQzvvv 212,212,22\nbuffer_load_dwordx4 a[48:51], v60, s[48:51], s31 offen offset:1024\nv_accvgpr_read_b32 v244, a248\nv_accvgpr_read_b32 v245, a249\nv_accvgpr_read_b32 v246, a250\n"
"v_accvgpr_read_b32 v247, a251\nv_accvgpr_read_b32 v32, a252\nv_accvgpr_read_b32 v33, a253\nv_accvgpr_read_b32 v34, a254\nv_accvgpr_read_b32 v35, a255\nQk 244,244,245\nQk 245,246,247\nQk 246,32,33\n"
"Qk 247,34,35\nQk 232,232,233\nQk 233,234,235\nQzvvv 32,244,24\nQk 234,236,237\nQk 235,238,239\nQzvvv 244,244,26\nbuffer_load_dwordx4 a[52:55], v60, s[48:51], s31 offen offset:2048\n"
"Qk 212,212,213\nQk 213,214,215\nQk 214,40,41\nQk 215,42,43\ns_nop 2\nQk 244,244,245\nQk 245,246,247\nQk 246,32,33\n"
"Qk 247,34,35\ns_waitcnt lgkmcnt(5)\nQcaav 140,56,84,112\ns_add_u32 m0, s96, 98304\ns_mov_b32 s95, 86016\nQmaav 140,60,116\nbuffer_load_dwordx4 v61, s[52:55], s95 offen lds\nQcaav 136,56,240,112\n"
"Qmaav 136,60,248\nQcaav 132,56,208,112\nQta 72,68,16384\ns_mov_b32 s95, 105472\nQmaav 132,60,216\nbuffer_load_dwordx4 v61, s[52:55], s95 offen offset:1024 lds\nQcaav 128,56,176,112\nQmaav 128,60,184\n"
"Qta 74,68,16896\nQcaav 124,56,144,112\nQmaav 124,60,152\nQcaav 120,56,112,112\nQta 76,68,17408\nQmaav 120,60,120\ns_add_u32 s88, s71, 0\nQcaav 116,56,80,112\n"
"buffer_store_dwordx4 v[80:83], v60, s[56:59], s88 offen offset:0 sc1\nQmaav 116,60,88\nQta 78,68,17920\ns_waitcnt lgkmcnt(5)\nQcaav 144,64,80,112\nQmaav 144,68,88\nQcaav 148,64,112,112\nQmaav 148,68,120\n"
"Qcaav 152,64,144,112\nQta 56,68,24576\nQmaav 152,68,152\nQcaav 156,64,176,112\nQmaav 156,68,184\nQta 58,68,25600\ns_add_u32 m0, s96, 106496\ns_mov_b32 s95, 126976\n"
"Qcaav 160,64,208,112\nbuffer_load_dwordx4 v61, s[52:55], s95 offen lds\nQmaav 160,68,216\nQcaav 164,64,240,112\nQta 60,68,32768\nQmaav 164,68,248\ns_mov_b32 s95, 146432\nQcaav 168,64,84,112\n"
"buffer_load_dwordx4 v61, s[52:55], s95 offen offset:1024 lds\nQmaav 168,68,116\nQta 62,68,33792\ns_waitcnt lgkmcnt(4)\nQcaav 196,72,84,112\nQmaav 196,76,116\nQcaav 192,72,240,112\nQmaav 192,76,248\n"
"Qcaav 188,72,208,112\nQta 64,68,24832\nQmaav 188,76,216\nbuffer_store_dwordx4 v[112:115], v60, s[56:59], s88 offen offset:1024 sc1\nQcaav 184,72,176,112\nQmaav 184,76,184\nQta 66,68,25856\nQcaav 180,72,144,112\n"
"Qmaav 180,76,152\nQcaav 176,72,112,112\nQta 68,68,33024\nv_add_u32 v75, 1, v75\nQmaav 176,76,120\ns_waitcnt vmcnt(34)\nds_write_b32 v73, v75\nQcaav 172,72,80,112\n"
"Qmaav 172,76,88\nQta 70,68,34048\ns_waitcnt lgkmcnt(5)\nQcaav 200,56,80,112\ns_add_u32 m0, s96, 114688\ns_mov_b32 s95, 86016\nQmaav 200,60,88\nbuffer_load_dwordx4 v62, s[52:55], s95 offen lds\n"
"Qcaav 204,56,112,112\nQmaav 204,60,120\nQcaav 208,56,144,112\nQta 72,68,512\nQmaav 208,60,152\nds_read_b32 v66, v74\ns_mov_b32 s95, 125952\nQcaav 212,56,176,112\n"
"buffer_load_dwordx4 v62, s[52:55], s95 offen offset:1024 lds\nQmaav 212,60,184\nQta 74,68,1536\nQcaav 216,56,208,112\nQmaav 216,60,216\ns_waitcnt lgkmcnt(1)\nv_cmp_gt_u32 vcc, v75, v66\nQcaav 220,56,240,112\n"
"s_cbranch_vccnz .Lso40\n.Lro40:\nQmaav 220,60,248\nQta 76,68,8704\nQcaav 224,56,84,112\nbuffer_store_dwordx4 v[144:147], v60, s[56:59], s88 offen offset:2048 sc1\nv_xor_b32 v56, 8, v73\nQmaav 224,60,116\n"
"ds_read_b32 v56, v56 offset:16\ns_waitcnt lgkmcnt(5)\nQcaav 252,64,84,112\nQta 78,68,9728\nQmaav 252,68,116\nQcaav 248,64,240,112\nQmaav 248,68,248\nQcaav 244,64,208,112\n"
"Qta 56,68,768\nQmaav 244,68,216\nQcaav 240,64,176,112\ns_waitcnt lgkmcnt(2)\nv_mov_b32 v57, v56\nv_mov_b32 v58, v56\nQmaav 240,68,184\nQta 58,68,1792\n"
"s_add_u32 m0, s96, 122880\ns_mov_b32 s95, 86016\nQcaav 236,64,144,112\nbuffer_load_dwordx4 v63, s[52:55], s95 offen lds\nQmaav 236,68,152\nv_mov_b32 v59, v56\nv_accvgpr_write_b32 a80, v56\nQcaav 232,64,112,112\n"
"Qta 60,68,8960\nv_accvgpr_write_b32 a81, v56\nQmaav 232,68,120\ns_mov_b32 s95, 105472\nv_accvgpr_write_b32 a82, v56\nQcaav 228,64,80,112\nbuffer_load_dwordx4 v63, s[52:55], s95 offen offset:1024 lds\nQmaav 228,68,88\n"
"Qta 62,68,9984\nv_accvgpr_write_b32 a83, v56\ns_waitcnt lgkmcnt(4)\nQcvav 0,72,80,56\nQmvav 0,76,88\nQcvav 4,72,112,56\nbuffer_store_dwordx4 v[176:179], v60, s[56:59], s88 offen offset:3072 sc1\nQmvav 4,76,120\n"
"Qcvav 8,72,144,56\nQta 64,68,16640\nQmvav 8,76,152\nQcvav 12,72,176,56\nQmvav 12,76,184\nQta 66,68,17152\nQcvav 16,72,208,56\nQmvav 16,76,216\n"
"Qcvav 20,72,240,56\nQta 68,68,17664\nQmvav 20,76,248\nQcvav 24,72,84,56\nQmvav 24,76,116\nQta 70,68,18176\ns_waitcnt lgkmcnt(4)\nQcvav 52,56,84,56\n"
"Qmvav 52,60,116\nQcvav 48,56,240,56\nQmvav 48,60,248\nQcvav 44,56,208,56\nQta 72,68,25088\nQmvav 44,60,216\nQcvav 40,56,176,56\nQmvav 40,60,184\n"
"Qta 74,68,26112\ns_add_u32 s88, s71, 4096\nQcvav 36,56,144,56\nbuffer_store_dwordx4 v[208:211], v60, s[56:59], s88 offen offset:0 sc1\nQmvav 36,60,152\nQcvav 32,56,112,56\nQta 76,68,33280\nQmvav 32,60,120\n"
"Qcvav 28,56,80,56\nQmvav 28,60,88\nQta 78,68,34304\ns_waitcnt lgkmcnt(4)\nQcvav 92,64,80,56\nQmvav 92,68,88\nQcvav 124,64,112,56\nQmvav 124,68,120\n"
"Qcvav 156,64,144,56\nQta 56,68,25344\nQmvav 156,68,152\nQcvav 188,64,176,56\nQmvav 188,68,184\nQta 58,68,26368\nQcvav 220,64,208,56\nQmvav 220,68,216\n"
"Qcvav 252,64,240,56\nQta 60,68,33536\nQmvav 252,68,248\nQcvav 100,64,84,56\nQmvav 100,68,116\nQta 62,68,34560\ns_waitcnt lgkmcnt(4)\nQcvav 172,72,84,56\n"
"buffer_store_dwordx4 v[240:243], v60, s[56:59], s88 offen offset:1024 sc1\nQmvav 172,76,116\nQcvav 140,72,240,56\nQmvav 140,76,248\nQcvav 108,72,208,56\nQta 64,68,40960\nQmvav 108,76,216\nQcvav 228,72,176,56\n"
"Qmvav 228,76,184\nQta 66,68,41984\nQcvav 196,72,144,56\nQmvav 196,76,152\nQcvav 164,72,112,56\nQta 68,68,49152\nQmvav 164,76,120\nQcvav 132,72,80,56\n"
"Qmvav 132,76,88\nQta 70,68,50176\ns_waitcnt lgkmcnt(4)\nQcvav 204,56,80,56\nQmvav 204,60,88\nQcvav 236,56,112,56\nQmvav 236,60,120\nQcaav 96,56,144,80\n"
"Qta 72,68,41216\nQmaav 96,60,152\nQcaav 100,56,176,80\nbuffer_store_dwordx4 v[84:87], v60, s[56:59], s88 offen offset:2048 sc1\nQmaav 100,60,184\nQta 74,68,42240\nQcaav 104,56,208,80\nQmaav 104,60,216\n"
"Qcaav 108,56,240,80\nQta 76,68,49408\nQmaav 108,60,248\nQcaav 112,56,84,80\nQmaav 112,60,116\nQta 78,68,50432\ns_waitcnt vmcnt(15)\ns_waitcnt lgkmcnt(4)\n"
"Qmaaa 140,64,24\nQmaaa 140,68,52\nQmaaa 136,64,20\nQmaaa 136,68,48\nQmaaa 132,64,16\nQta 56,68,41472\nQmaaa 132,68,44\nQmaaa 128,64,12\n"
"Qmaaa 128,68,40\nQta 58,68,42496\nQmaaa 124,64,8\nQmaaa 124,68,36\nQmaaa 120,64,4\nQta 60,68,49664\ns_and_b32 s68, %19, 3\nQmaaa 120,68,32\n"
"s_add_u32 s88, s71, 14336\nQmaaa 116,64,0\nbuffer_store_dwordx4 v[88:91], v60, s[56:59], s88 offen offset:0 sc1\nQmaaa 116,68,28\nQta 62,68,50688\ns_waitcnt lgkmcnt(4)\nQmaaa 144,72,0\ns_cmp_lt_u32 s68, 1\n"
"s_cselect_b32 s68, 40, 32\nQmaaa 144,76,28\ns_mul_i32 s67, s68, 40960\ns_mul_i32 s74, s68, 14336\nQmaaa 148,72,4\ns_and_b32 s68, %19, 3\nQmaaa 148,76,32\ns_cmp_lt_u32 s68, 2\n"
"s_cselect_b32 s68, 48, 40\nQmaaa 152,72,8\nQta 64,68,41728\ns_mul_i32 s72, s68, 40960\nQmaaa 152,76,36\ns_mul_i32 s75, s68, 14336\ns_and_b32 s68, %19, 3\nQmaaa 156,72,12\n"
"s_cmp_lt_u32 s68, 3\ns_cselect_b32 s68, 56, 48\nQmaaa 156,76,40\nQta 66,68,42752\ns_mul_i32 s73, s68, 40960\nQmaaa 160,72,16\ns_mul_i32 s77, s68, 14336\nv_add_u32 v58, s91, v60\n"
"Qmaaa 160,76,44\ns_mul_i32 s68, s93, 28672\ns_sub_u32 s68, 14336, s68\nQmaaa 164,72,20\nQta 68,68,49920\nv_add_u32 v59, s68, v58\nQmaaa 164,76,48\nQmaaa 168,72,24\n"
"Qmaaa 168,76,52\nQta 70,68,50944\ns_waitcnt lgkmcnt(4)\nQmvaa 24,56,24\nQmvaa 24,60,52\nQmvaa 20,56,20\nQmvaa 20,60,48\nQmvaa 16,56,16\n"
"Qta 72,68,57344\nQmvaa 16,60,44\nbuffer_store_dwordx4 v[120:123], v60, s[56:59], s88 offen offset:1024 sc1\nQmvaa 12,56,12\nQmvaa 12,60,40\nQta 74,68,57856\nQmvaa 8,56,8\nQmvaa 8,60,36\n"
"Qmvaa 4,56,4\nQta 76,68,58368\nv_add_u32 v75, 1, v75\nQmvaa 4,60,32\ns_waitcnt vmcnt(35)\nds_write_b32 v73, v75\nQmvaa 0,56,0\nQmvaa 0,60,28\n"
"Qta 78,68,58880\ns_waitcnt lgkmcnt(5)\nQmvaa 28,64,0\nQmvaa 28,68,28\nQmvaa 32,64,4\nQmvaa 32,68,32\nQmvaa 36,64,8\nQta 56,68,57600\n"
"Qmvaa 36,68,36\nds_read_b32 v66, v74\nQmvaa 40,64,12\nQmvaa 40,68,40\nQta 58,68,58112\nQmvaa 44,64,16\nQmvaa 44,68,44\ns_waitcnt lgkmcnt(1)\n"
"v_cmp_gt_u32 vcc, v75, v66\nQmvaa 48,64,20\ns_cbranch_vccnz .Lso180\n.Lro180:\nQmvaa 48,68,48\nQta 60,68,58624\nQmvaa 52,64,24\nbuffer_store_dwordx4 v[152:155], v60, s[56:59], s88 offen offset:2048 sc1\n"
"Qmvaa 52,68,52\ns_waitcnt lgkmcnt(4)\nQmaaa 196,72,24\nQta 62,68,59136\nQmaaa 196,76,52\nQmaaa 192,72,20\nQmaaa 192,76,48\nQmaaa 188,72,16\n"
"Qta 64,70,1024\nQmaaa 188,76,44\nQmaaa 184,72,12\nQmaaa 184,76,40\nQta 66,70,2048\ns_add_u32 m0, s96, 0\ns_mov_b32 s95, 126976\nQmaaa 180,72,8\n"
"buffer_load_dwordx4 v63, s[52:55], s95 offen lds\nQmaaa 180,76,36\nQmaaa 176,72,4\nQta 68,70,9216\nQmaaa 176,76,32\ns_mov_b32 s95, 146432\nQmaaa 172,72,0\nbuffer_load_dwordx4 v63, s[52:55], s95 offen offset:1024 lds\n"
"Qmaaa 172,76,28\nQta 70,70,10240\ns_waitcnt lgkmcnt(4)\nQmvaa 92,56,0\nQmvaa 92,60,28\nQmvaa 124,56,4\nbuffer_store_dwordx4 v[184:187], v60, s[56:59], s88 offen offset:3072 sc1\nQmvaa 124,60,32\n"
"Qmvaa 156,56,8\nQta 72,70,1280\nQmvaa 156,60,36\nQmvaa 188,56,12\nQmvaa 188,60,40\nQta 74,70,2304\nQmvaa 220,56,16\nQmvaa 220,60,44\n"
"Qmvaa 252,56,20\nQta 76,70,9472\nQmvaa 252,60,48\ns_add_u32 m0, s96, 8192\ns_mov_b32 s95, 331776\nQmvaa 100,56,24\nbuffer_load_dwordx4 v61, s[52:55], s95 offen lds\nQmvaa 100,60,52\n"
"Qta 78,70,10496\ns_waitcnt lgkmcnt(4)\nQmaaa 224,64,24\nQmaaa 224,68,52\ns_mov_b32 s95, 351232\nQmaaa 220,64,20\nbuffer_load_dwordx4 v61, s[52:55], s95 offen offset:1024 lds\nQmaaa 220,68,48\n"
"Qmaaa 216,64,16\nQta 56,70,1536\nQmaaa 216,68,44\nQmaaa 212,64,12\nQmaaa 212,68,40\nQta 58,70,2560\ns_add_u32 s88, s71, 18432\nQmaaa 208,64,8\n"
"buffer_store_dwordx4 v[216:219], v60, s[56:59], s88 offen offset:0 sc1\nQmaaa 208,68,36\nQmaaa 204,64,4\nQta 60,70,9728\nQmaaa 204,68,32\nQmaaa 200,64,0\nQmaaa 200,68,28\nQta 62,70,10752\n"
"s_waitcnt lgkmcnt(4)\nQmaaa 228,72,0\nQmaaa 228,76,28\nQmaaa 232,72,4\nQmaaa 232,76,32\nQmaaa 236,72,8\nQta 64,70,1792\ns_add_u32 m0, s96, 16384\n"
"s_mov_b32 s95, 372736\nQmaaa 236,76,36\nbuffer_load_dwordx4 v61, s[52:55], s95 offen lds\nQmaaa 240,72,12\nQmaaa 240,76,40\nQta 66,70,2816\nQmaaa 244,72,16\ns_mov_b32 s95, 392192\n"
"Qmaaa 244,76,44\nbuffer_load_dwordx4 v61, s[52:55], s95 offen offset:1024 lds\nQmaaa 248,72,20\nQta 68,70,9984\nQmaaa 248,76,48\nQmaaa 252,72,24\nQmaaa 252,76,52\nQta 70,70,11008\n"
"s_waitcnt lgkmcnt(4)\nQmvaa 172,56,24\nbuffer_store_dwordx4 v[248:251], v60, s[56:59], s88 offen offset:1024 sc1\nQmvaa 172,60,52\nQmvaa 140,56,20\nQmvaa 140,60,48\nQmvaa 108,56,16\nQta 72,70,17408\n"
"Qmvaa 108,60,44\nQmvaa 228,56,12\nQmvaa 228,60,40\nQta 74,70,18432\nQmvaa 196,56,8\nQmvaa 196,60,36\nQmvaa 164,56,4\nQta 76,69,0\n"
"s_add_u32 m0, s96, 24576\n.Lpg14:\ns_cbranch_execz .Ltr6\ns_mov_b32 s95, 331776\nQmvaa 164,60,32\nbuffer_load_dwordx4 v62, s[52:55], s95 offen lds\nQmvaa 132,56,0\nQmvaa 132,60,28\n"
"Qta 78,69,1024\ns_waitcnt lgkmcnt(4)\nQmvaa 204,64,0\ns_mov_b32 s95, 371712\nQmvaa 204,68,28\nbuffer_load_dwordx4 v62, s[52:55], s95 offen offset:1024 lds\nQmvaa 236,64,4\nQmvaa 236,68,32\n"
"Qmaaa 96,64,8\nQta 56,70,17664\nQmaaa 96,68,36\nQmaaa 100,64,12\nbuffer_store_dwordx4 v[116:119], v60, s[56:59], s88 offen offset:2048 sc1\nQmaaa 100,68,40\nQta 58,70,18688\nQmaaa 104,64,16\n"
"Qmaaa 104,68,44\nQmaaa 108,64,20\nQta 60,69,256\nQmaaa 108,68,48\nQmaaa 112,64,24\nQmaaa 112,68,52\nQta 62,69,1280\ns_waitcnt lgkmcnt(4)\n"
"Qmaav 140,72,180\nQmaav 140,76,244\ns_mov_b32 s31, 28672\nQmaav 136,72,148\nbuffer_load_dwordx4 a[0:3], v60, s[48:51], s31 offen offset:0\nQmaav 136,76,212\nQmaav 132,72,224\nQta 64,70,17920\n"
"Qmaav 132,76,232\nQmaav 128,72,192\nbuffer_load_dwordx4 a[4:7], v60, s[48:51], s31 offen offset:1024\nQmaav 128,76,200\nQta 66,70,18944\nQmaav 124,72,160\nQmaav 124,76,168\nQmaav 120,72,128\n"
"Qta 68,69,512\nQmaav 120,76,136\nbuffer_load_dwordx4 a[8:11], v60, s[48:51], s31 offen offset:2048\nQmaav 116,72,96\nQmaav 116,76,104\nQta 70,69,1536\ns_waitcnt lgkmcnt(4)\nQmaav 144,56,96\n"
"Qmaav 144,60,104\nbuffer_load_dwordx4 a[12:15], v60, s[48:51], s31 offen offset:3072\nQmaav 148,56,128\nQmaav 148,60,136\nQmaav 152,56,160\nQta 72,70,18176\ns_mov_b32 s31, 32768\nQmaav 152,60,168\n"
"buffer_load_dwordx4 a[16:19], v60, s[48:51], s31 offen offset:0\nQmaav 156,56,192\nQmaav 156,60,200\nQta 74,70,19200\nQmaav 160,56,224\nQmaav 160,60,232\nbuffer_load_dwordx4 a[20:23], v60, s[48:51], s31 offen offset:1024\nQmaav 164,56,148\n"
"Qta 76,69,768\nQmaav 164,60,212\nQmaav 168,56,180\nQmaav 168,60,244\nQta 78,69,1792\ns_waitcnt lgkmcnt(4)\nQmvav 24,64,180\nbuffer_load_dwordx4 a[24:27], v60, s[48:51], s31 offen offset:2048\n"
"Qmvav 24,68,244\nQmvav 20,64,148\nQmvav 20,68,212\nQmvav 16,64,224\nQta 56,69,8192\ns_mov_b32 s31, 43008\nQmvav 16,68,232\nbuffer_load_dwordx4 a[28:31], v60, s[48:51], s31 offen offset:0\n"
"Qmvav 12,64,192\nQmvav 12,68,200\nQta 58,69,8704\nQmvav 8,64,160\nQmvav 8,68,168\nbuffer_load_dwordx4 a[32:35], v60, s[48:51], s31 offen offset:1024\nQmvav 4,64,128\nQta 60,69,9216\n"
"v_add_u32 v75, 1, v75\nQmvav 4,68,136\ns_waitcnt vmcnt(31)\nds_write_b32 v73, v75\nQmvav 0,64,96\nQmvav 0,68,104\nQta 62,69,9728\ns_waitcnt lgkmcnt(5)\n"
"Qmvav 28,72,96\nbuffer_load_dwordx4 a[36:39], v60, s[48:51], s31 offen offset:2048\nQmvav 28,76,104\nQmvav 32,72,128\nQmvav 32,76,136\nQmvav 36,72,160\nQta 64,69,8448\nQmvav 36,76,168\n"
"ds_read_b32 v66, v74\nQmvav 40,72,192\nbuffer_load_dwordx4 a[40:43], v60, s[48:51], s31 offen offset:3072\nQmvav 40,76,200\nQta 66,69,8960\nQmvav 44,72,224\nQmvav 44,76,232\ns_waitcnt lgkmcnt(1)\n"
"v_cmp_gt_u32 vcc, v75, v66\nQmvav 48,72,148\ns_cbranch_vccnz .Lso320\n.Lro320:\nQmvav 48,76,212\nQta 68,69,9472\ns_mov_b32 s31, 47104\nQmvav 52,72,180\n"
"buffer_load_dwordx4 a[44:47], v60, s[48:51], s31 offen offset:0\nQmvav 52,76,244\ns_waitcnt lgkmcnt(4)\nQmaav 196,56,180\nQta 70,69,9984\nQmaav 196,60,244\nQmaav 192,56,148\nbuffer_load_dwordx4 a[48:51], v60, s[48:51], s31 offen offset:1024\n"
"Qmaav 192,60,212\nQmaav 188,56,224\nQta 72,69,16384\nQmaav 188,60,232\nQmaav 184,56,192\nbuffer_load_dwordx4 a[52:55], v60, s[48:51], s31 offen offset:2048\nQmaav 184,60,200\nQta 74,69,17408\n"
"Qmaav 180,56,160\nQmaav 180,60,168\nQmaav 176,56,128\nQta 76,69,24576\ns_mov_b32 s31, 114688\nQmaav 176,60,136\nbuffer_load_dwordx4 v[80:83], v60, s[48:51], s31 offen offset:0\nQmaav 172,56,96\n"
"Qmaav 172,60,104\nQta 78,69,25600\ns_waitcnt lgkmcnt(4)\nQmvav 92,64,96\nQmvav 92,68,104\nbuffer_load_dwordx4 v[112:115], v60, s[48:51], s31 offen offset:1024\nQmvav 124,64,128\nQmvav 124,68,136\n"
"Qmvav 156,64,160\nQta 56,69,16640\nQmvav 156,68,168\nbuffer_load_dwordx4 v[144:147], v60, s[48:51], s31 offen offset:2048\nQmvav 188,64,192\nQmvav 188,68,200\nQta 58,69,17664\nQmvav 220,64,224\n"
"Qmvav 220,68,232\nbuffer_load_dwordx4 v[176:179], v60, s[48:51], s31 offen offset:3072\nQmvav 252,64,148\nQta 60,69,24832\nQmvav 252,68,212\nQmvav 100,64,180\nQmvav 100,68,244\nQta 62,69,25856\n"
"s_mov_b32 s31, 118784\ns_waitcnt lgkmcnt(4)\nQmaav 224,72,180\nbuffer_load_dwordx4 v[208:211], v60, s[48:51], s31 offen offset:0\nQmaav 224,76,244\nQmaav 220,72,148\nQmaav 220,76,212\nQmaav 216,72,224\n"
"Qta 64,69,16896\nQmaav 216,76,232\nbuffer_load_dwordx4 v[240:243], v60, s[48:51], s31 offen offset:1024\nQmaav 212,72,192\nQmaav 212,76,200\nQta 66,69,17920\nQmaav 208,72,160\nQmaav 208,76,168\n"
"buffer_load_dwordx4 v[84:87], v60, s[48:51], s31 offen offset:2048\nQmaav 204,72,128\nQta 68,69,25088\nQmaav 204,76,136\nQmaav 200,72,96\nQmaav 200,76,104\nQta 70,69,26112\ns_mov_b32 s31, 129024\n"
"s_waitcnt lgkmcnt(4)\nQmaav 228,56,96\nbuffer_load_dwordx4 v[88:91], v60, s[48:51], s31 offen offset:0\nQmaav 228,60,104\nQmaav 232,56,128\nQmaav 232,60,136\nQmaav 236,56,160\nQta 72,69,17152\n"
"Qmaav 236,60,168\nbuffer_load_dwordx4 v[120:123], v60, s[48:51], s31 offen offset:1024\nQmaav 240,56,192\nQmaav 240,60,200\nQta 74,69,18176\nQmaav 244,56,224\nQmaav 244,60,232\nbuffer_load_dwordx4 v[152:155], v60, s[48:51], s31 offen offset:2048\n"
"Qmaav 248,56,148\nQta 76,69,25344\nQmaav 248,60,212\nQmaav 252,56,180\nQmaav 252,60,244\nQta 78,69,26368\ns_waitcnt lgkmcnt(4)\nQmvav 172,64,180\n"
"buffer_load_dwordx4 v[184:187], v60, s[48:51], s31 offen offset:3072\nQmvav 172,68,244\nQmvav 140,64,148\nQmvav 140,68,212\nQmvav 108,64,224\nQta 56,69,32768\ns_mov_b32 s31, 133120\nQmvav 108,68,232\n"
"buffer_load_dwordx4 v[216:219], v60, s[48:51], s31 offen offset:0\nQmvav 228,64,192\nQmvav 228,68,200\nQta 58,69,33792\nQmvav 196,64,160\nQmvav 196,68,168\nbuffer_load_dwordx4 v[248:251], v60, s[48:51], s31 offen offset:1024\nQmvav 164,64,128\n"
"Qta 60,69,40960\nQmvav 164,68,136\nQmvav 132,64,96\nQmvav 132,68,104\nQta 62,69,41984\ns_waitcnt lgkmcnt(4)\nQmvav 204,72,96\nbuffer_load_dwordx4 v[116:119], v60, s[48:51], s31 offen offset:2048\n"
"Qmvav 204,76,104\nQmvav 236,72,128\nQmvav 236,76,136\nQmaav 96,72,160\nQta 64,69,33024\ns_add_u32 m0, s96, 32768\ns_mov_b32 s95, 331776\nQmaav 96,76,168\n"
"buffer_load_dwordx4 v63, s[52:55], s95 offen lds\nQmaav 100,72,192\nQmaav 100,76,200\nQta 66,69,34048\nQmaav 104,72,224\ns_mov_b32 s95, 351232\nQmaav 104,76,232\nbuffer_load_dwordx4 v63, s[52:55], s95 offen offset:1024 lds\n"
"Qmaav 108,72,148\nQta 68,69,41216\nQmaav 108,76,212\nQmaav 112,72,180\nQmaav 112,76,244\nQta 70,69,42240\ns_add_u32 m0, s96, 40960\ns_mov_b32 s95, 372736\n"
"s_waitcnt vmcnt(16)\ns_waitcnt lgkmcnt(4)\nQmaaa 140,56,24\nbuffer_load_dwordx4 v63, s[52:55], s95 offen lds\nQmaaa 140,60,52\nQmaaa 136,56,20\nQmaaa 136,60,48\nQmaaa 132,56,16\n"
"Qta 72,69,33280\ns_mov_b32 s95, 392192\nQmaaa 132,60,44\nbuffer_load_dwordx4 v63, s[52:55], s95 offen offset:1024 lds\nQmaaa 128,56,12\nQmaaa 128,60,40\nQta 74,69,34304\nQmaaa 124,56,8\n"
"Qmaaa 124,60,36\nQmaaa 120,56,4\nQta 76,69,41472\ns_add_u32 s88, s71, 28672\nQmaaa 120,60,32\nbuffer_store_dwordx4 v[96:99], v60, s[56:59], s88 offen offset:0 sc1\nQmaaa 116,56,0\nQmaaa 116,60,28\n"
"Qta 78,69,42496\ns_waitcnt lgkmcnt(4)\nQmaaa 144,64,0\nQmaaa 144,68,28\nQmaaa 148,64,4\nQmaaa 148,68,32\nQmaaa 152,64,8\nQta 56,69,33536\n"
"Qmaaa 152,68,36\nQmaaa 156,64,12\nQmaaa 156,68,40\nQta 58,69,34560\ns_add_u32 m0, s96, 49152\ns_mov_b32 s95, 413696\nQmaaa 160,64,16\nbuffer_load_dwordx4 v61, s[52:55], s95 offen lds\n"
"Qmaaa 160,68,44\nQmaaa 164,64,20\nQta 60,69,41728\nQmaaa 164,68,48\ns_mov_b32 s95, 433152\nQmaaa 168,64,24\nbuffer_load_dwordx4 v61, s[52:55], s95 offen offset:1024 lds\nQmaaa 168,68,52\n"
"Qta 62,69,42752\ns_waitcnt lgkmcnt(4)\nQmvaa 24,72,24\nQmvaa 24,76,52\nQmvaa 20,72,20\nbuffer_store_dwordx4 v[128:131], v60, s[56:59], s88 offen offset:1024 sc1\nQmvaa 20,76,48\nQmvaa 16,72,16\n"
"Qta 64,69,49152\nQmvaa 16,76,44\nQmvaa 12,72,12\nQmvaa 12,76,40\nQta 66,69,49664\nQmvaa 8,72,8\nQmvaa 8,76,36\nQmvaa 4,72,4\n"
"Qta 68,69,50176\nv_add_u32 v75, 1, v75\nQmvaa 4,76,32\ns_waitcnt vmcnt(36)\nds_write_b32 v73, v75\ns_add_u32 m0, s96, 57344\ns_mov_b32 s95, 454656\nQmvaa 0,72,0\n"
"buffer_load_dwordx4 v61, s[52:55], s95 offen lds\nQmvaa 0,76,28\nQta 70,69,50688\ns_waitcnt lgkmcnt(5)\nQmvaa 28,56,0\nQmvaa 28,60,28\ns_mov_b32 s95, 474112\nQmvaa 32,56,4\n"
"buffer_load_dwordx4 v61, s[52:55], s95 offen offset:1024 lds\nQmvaa 32,60,32\nQmvaa 36,56,8\nQta 72,69,49408\nQmvaa 36,60,36\nds_read_b32 v66, v74\nQmvaa 40,56,12\nQmvaa 40,60,40\n"
"Qta 74,69,49920\nQmvaa 44,56,16\nbuffer_store_dwordx4 v[160:163], v60, s[56:59], s88 offen offset:2048 sc1\nv_add_u32 v67, 2, v65\nQmvaa 44,60,44\ns_waitcnt lgkmcnt(1)\nv_cmp_gt_u32 vcc, v75, v66\nQmvaa 48,56,20\n"
"s_cbranch_vccnz .Lso460\n.Lro460:\nQmvaa 48,60,48\nQta 76,69,50432\nQmvaa 52,56,24\nbuffer_store_dword v67, v77, s[24:27], 0 offen sc1\nQmvaa 52,60,52\ns_waitcnt lgkmcnt(4)\n"
"Qmaaa 196,64,24\nQta 78,69,50944\nQmaaa 196,68,52\nQmaaa 192,64,20\nQmaaa 192,68,48\nQmaaa 188,64,16\nQta 56,69,57344\ns_add_u32 m0, s96, 132096\n"
"s_mov_b32 s95, 413696\nQmaaa 188,68,44\nbuffer_load_dwordx4 v62, s[52:55], s95 offen lds\nQmaaa 184,64,12\nQmaaa 184,68,40\nQta 58,69,58368\nQmaaa 180,64,8\ns_mov_b32 s95, 453632\n"
"Qmaaa 180,68,36\nbuffer_load_dwordx4 v62, s[52:55], s95 offen offset:1024 lds\nQmaaa 176,64,4\nQta 60,68,0\nQmaaa 176,68,32\nQmaaa 172,64,0\nQmaaa 172,68,28\nQta 62,68,1024\n"
"s_waitcnt lgkmcnt(4)\nQmvaa 92,72,0\nbuffer_store_dwordx4 v[192:195], v60, s[56:59], s88 offen offset:3072 sc1\nQmvaa 92,76,28\nQmvaa 124,72,4\nQmvaa 124,76,32\nQmvaa 156,72,8\nQta 64,69,57600\n"
"Qmvaa 156,76,36\nQmvaa 188,72,12\nQmvaa 188,76,40\nQta 66,69,58624\nQmvaa 220,72,16\nQmvaa 220,76,44\nQmvaa 252,72,20\nQta 68,68,256\n"
"s_add_u32 m0, s96, 140288\ns_mov_b32 s95, 413696\nQmvaa 252,76,48\nbuffer_load_dwordx4 v63, s[52:55], s95 offen lds\nQmvaa 100,72,24\nQmvaa 100,76,52\nQta 70,68,1280\ns_waitcnt lgkmcnt(4)\n"
"Qmaaa 224,56,24\ns_mov_b32 s95, 433152\nQmaaa 224,60,52\nbuffer_load_dwordx4 v63, s[52:55], s95 offen offset:1024 lds\nQmaaa 220,56,20\nQmaaa 220,60,48\nQmaaa 216,56,16\nQta 72,69,57856\n"
"Qmaaa 216,60,44\ns_add_u32 s88, s71, 32768\nQmaaa 212,56,12\nbuffer_store_dwordx4 v[224:227], v60, s[56:59], s88 offen offset:0 sc1\nQmaaa 212,60,40\nQta 74,69,58880\nQmaaa 208,56,8\nQmaaa 208,60,36\n"
"Qmaaa 204,56,4\nQta 76,68,512\nQmaaa 204,60,32\nQmaaa 200,56,0\nQmaaa 200,60,28\nQta 78,68,1536\ns_waitcnt lgkmcnt(4)\nQmaaa 228,64,0\n"
"Qmaaa 228,68,28\ns_add_u32 m0, s96, 148480\ns_mov_b32 s95, 454656\nQmaaa 232,64,4\nbuffer_load_dwordx4 v63, s[52:55], s95 offen lds\nQmaaa 232,68,32\nQmaaa 236,64,8\nQta 56,69,58112\n"
"Qmaaa 236,68,36\ns_mov_b32 s95, 474112\nQmaaa 240,64,12\nbuffer_load_dwordx4 v63, s[52:55], s95 offen offset:1024 lds\nQmaaa 240,68,40\nQta 58,69,59136\nQmaaa 244,64,16\nQmaaa 244,68,44\n"
"Qmaaa 248,64,20\nQta 60,68,768\nQmaaa 248,68,48\nQmaaa 252,64,24\nbuffer_store_dwordx4 v[148:151], v60, s[56:59], s88 offen offset:1024 sc1\nQmaaa 252,68,52\nQta 62,68,1792\ns_waitcnt lgkmcnt(4)\n"
"Qmvaa 172,72,24\nQmvaa 172,76,52\ns_mov_b32 s40, %17\ns_and_b32 s41, %18, 0xffff\nQmvaa 140,72,20\ns_mov_b32 s42, 571200\ns_lshl_b32 s66, %19, 2\nQmvaa 140,76,48\n"
"s_add_u32 s66, s66, s70\ns_mul_i32 s98, s66, 11264\nQmvaa 108,72,16\nQta 64,68,8192\ns_add_u32 s98, s98, 1835008\nQmvaa 108,76,44\ns_xor_b32 s66, s66, 16\ns_mul_i32 s64, s66, 11264\n"
"Qmvaa 228,72,12\ns_add_u32 s64, s64, 1835008\ns_mul_i32 s66, s92, 33792\nQmvaa 228,76,40\nQta 66,68,9216\ns_add_u32 m0, s96, 65536\ns_mov_b32 s95, 659456\nQmvaa 196,72,8\n"
"buffer_load_dwordx4 v61, s[52:55], s95 offen lds\nQmvaa 196,76,36\ns_add_u32 s66, s66, %10\ns_add_u32 s66, s66, 98304\nQmvaa 164,72,4\nQta 68,68,16384\ns_mul_i32 s68, s93, 96\nQmvaa 164,76,32\n"
"s_mov_b32 s95, 678912\ns_sub_u32 s68, s66, s68\nQmvaa 132,72,0\nbuffer_load_dwordx4 v61, s[52:55], s95 offen offset:1024 lds\nQmvaa 132,76,28\nQta 70,68,17408\ns_add_u32 s68, s68, 96\ns_waitcnt lgkmcnt(4)\n"
"Qmvaa 204,56,0\nv_bfe_u32 v148, v60, 4, 4\nv_mul_u32_u24 v148, 208, v148\nQmvaa 204,60,28\nv_lshrrev_b32 v149, 8, v60\nv_lshl_add_u32 v148, v149, 3, v148\nQmvaa 236,56,4\nv_add_u32 v56, s68, v148\n"
"s_lshl_b32 s68, s93, 3\nQmvaa 236,60,32\nv_lshrrev_b32 v148, 7, v60\nv_add_u32 v148, s68, v148\nQmaaa 96,56,8\nQta 72,68,8448\nv_and_b32 v149, 127, v60\nQmaaa 96,60,36\n"
"buffer_store_dwordx4 v[180:183], v60, s[56:59], s88 offen offset:2048 sc1\nQmaaa 100,56,12\nv_and_b32 v150, 64, v60\nv_lshrrev_b32 v150, 1, v150\nQmaaa 100,60,40\nQta 74,68,9472\nv_mul_u32_u24 v57, 208, v148\nQmaaa 104,56,16\n"
"v_add3_u32 v57, v57, v149, s66\nv_add_u32 v57, v57, v150\nQmaaa 104,60,44\ns_and_b32 s65, %19, 1\ns_and_b32 s68, %19, 4\n.Lpg15:\ns_cbranch_execz .Ltr6\nQmaaa 108,56,20\n"
"Qta 76,68,16640\ns_lshl_b32 s68, s68, 2\nQmaaa 108,60,48\ns_lshl_b32 s69, s65, 3\ns_add_u32 s68, s68, s69\nQmaaa 112,56,24\ns_lshr_b32 s68, 0xc0004080, s68\ns_and_b32 s68, s68, 255\n"
"Qmaaa 112,60,52\nQta 78,68,17664\ns_mul_i32 s69, s92, 286720\ns_waitcnt vmcnt(24)\ns_waitcnt lgkmcnt(4)\nQmaav 140,64,84\ns_mov_b32 s31, 143360\ns_add_u32 s68, s68, s69\n"
"Qmaav 140,68,116\nbuffer_load_dwordx4 a[0:3], v60, s[48:51], s31 offen offset:0\nQmaav 136,64,240\nv_mul_u32_u24 v64, 2560, v148\nv_add3_u32 v64, v64, v149, s68\nQmaav 136,68,248\ns_lshl_b32 s68, s93, 4\nv_lshrrev_b32 v148, 6, v60\n"
"Qmaav 132,64,208\nQta 56,68,8704\nv_add_u32 v148, s68, v148\nQmaav 132,68,216\nbuffer_load_dwordx4 a[4:7], v60, s[48:51], s31 offen offset:1024\nQmaav 128,64,176\nv_and_b32 v149, 63, v60\nv_and_b32 v150, 32, v60\n"
"Qmaav 128,68,184\nQta 58,68,9728\nv_lshlrev_b32 v150, 1, v150\nQmaav 124,64,144\nv_mul_u32_u24 v71, 208, v148\nv_add3_u32 v71, v71, v149, s66\nQmaav 124,68,152\nbuffer_load_dwordx4 a[8:11], v60, s[48:51], s31 offen offset:2048\n"
"Qmaav 120,64,112\nQta 60,68,16896\nv_add_u32 v71, v71, v150\nQmaav 120,68,120\ns_xor_b32 s65, s65, 1\ns_lshl_b32 s65, s65, 8\nQmaav 116,64,80\ns_add_u32 s68, s65, s69\n"
"v_mul_u32_u24 v72, 2560, v148\nQmaav 116,68,88\nQta 62,68,17920\nv_add3_u32 v72, v72, v149, s68\ns_waitcnt lgkmcnt(4)\nQmaav 144,72,80\nbuffer_load_dwordx4 a[12:15], v60, s[48:51], s31 offen offset:3072\nQmaav 144,76,88\n"
"Qmaav 148,72,112\nQmaav 148,76,120\nQmaav 152,72,144\nQta 64,68,8960\ns_mov_b32 s31, 147456\nQmaav 152,76,152\nbuffer_load_dwordx4 a[16:19], v60, s[48:51], s31 offen offset:0\nQmaav 156,72,176\n"
"Qmaav 156,76,184\nQta 66,68,9984\nQmaav 160,72,208\nQmaav 160,76,216\nbuffer_load_dwordx4 a[20:23], v60, s[48:51], s31 offen offset:1024\nQmaav 164,72,240\nQta 68,68,17152\nQmaav 164,76,248\n"
"Qmaav 168,72,84\nQmaav 168,76,116\nQta 70,68,18176\ns_waitcnt lgkmcnt(4)\nQmvav 24,56,84\nbuffer_load_dwordx4 a[24:27], v60, s[48:51], s31 offen offset:2048\nQmvav 24,60,116\nQmvav 20,56,240\n"
"Qmvav 20,60,248\nQmvav 16,56,208\nQta 72,68,24576\ns_mov_b32 s31, 157696\nQmvav 16,60,216\nbuffer_load_dwordx4 a[28:31], v60, s[48:51], s31 offen offset:0\nQmvav 12,56,176\nQmvav 12,60,184\n"
"Qta 74,68,25088\nQmvav 8,56,144\nQmvav 8,60,152\nbuffer_load_dwordx4 a[32:35], v60, s[48:51], s31 offen offset:1024\nQmvav 4,56,112\nQta 76,68,25600\nv_add_u32 v75, 1, v75\nQmvav 4,60,120\n"
"s_waitcnt vmcnt(19)\nds_write_b32 v73, v75\nQmvav 0,56,80\nQmvav 0,60,88\nQta 78,68,26112\ns_waitcnt lgkmcnt(5)\nQmvav 28,64,80\nbuffer_load_dwordx4 a[36:39], v60, s[48:51], s31 offen offset:2048\n"
"Qmvav 28,68,88\nQmvav 32,64,112\nQmvav 32,68,120\nQmvav 36,64,144\nQta 56,68,24832\nQmvav 36,68,152\nds_read_b32 v66, v74\nQmvav 40,64,176\n"
"buffer_load_dwordx4 a[40:43], v60, s[48:51], s31 offen offset:3072\nQmvav 40,68,184\nQta 58,68,25344\nQmvav 44,64,208\nQmvav 44,68,216\ns_waitcnt lgkmcnt(1)\nv_cmp_gt_u32 vcc, v75, v66\nQmvav 48,64,240\n"
"s_cbranch_vccnz .Lso600\n.Lro600:\nQmvav 48,68,248\nQta 60,68,25856\ns_mov_b32 s31, 161792\nQmvav 52,64,84\nbuffer_load_dwordx4 a[44:47], v60, s[48:51], s31 offen offset:0\nQmvav 52,68,116\n"
"s_waitcnt lgkmcnt(4)\nQmaav 196,72,84\nQta 62,68,26368\nQmaav 196,76,116\nQmaav 192,72,240\nbuffer_load_dwordx4 a[48:51], v60, s[48:51], s31 offen offset:1024\nQmaav 192,76,248\nQmaav 188,72,208\n"
"Qta 64,68,32768\nQmaav 188,76,216\nQmaav 184,72,176\nbuffer_load_dwordx4 a[52:55], v60, s[48:51], s31 offen offset:2048\nQmaav 184,76,184\nQta 66,68,33792\nQmaav 180,72,144\nQmaav 180,76,152\n"
"Qmaav 176,72,112\nQta 68,68,40960\ns_add_u32 m0, s96, 73728\ns_mov_b32 s95, 700416\nQmaav 176,76,120\nbuffer_load_dwordx4 v61, s[52:55], s95 offen lds\nQmaav 172,72,80\nQmaav 172,76,88\n"
"Qta 70,68,41984\ns_waitcnt lgkmcnt(4)\nQmvav 92,56,80\ns_mov_b32 s95, 719872\nQmvav 92,60,88\nbuffer_load_dwordx4 v61, s[52:55], s95 offen offset:1024 lds\nQmvav 124,56,112\nQmvav 124,60,120\n"
"Qmvav 156,56,144\nQta 72,68,33024\ns_add_u32 m0, s96, 81920\ns_mov_b32 s95, 659456\nQmvav 156,60,152\nbuffer_load_dwordx4 v62, s[52:55], s95 offen lds\nQmvav 188,56,176\nQmvav 188,60,184\n"
"Qta 74,68,34048\nQmvav 220,56,208\ns_mov_b32 s95, 699392\nQmvav 220,60,216\nbuffer_load_dwordx4 v62, s[52:55], s95 offen offset:1024 lds\nQmvav 252,56,240\nQta 76,68,41216\nQmvav 252,60,248\n"
"Qmvav 100,56,84\nQmvav 100,60,116\nQta 78,68,42240\ns_add_u32 m0, s96, 90112\ns_mov_b32 s95, 659456\ns_waitcnt lgkmcnt(4)\nQmaav 224,64,84\nbuffer_load_dwordx4 v63, s[52:55], s95 offen lds\n"
"Qmaav 224,68,116\nQmaav 220,64,240\nQmaav 220,68,248\nQmaav 216,64,208\nQta 56,68,33280\ns_mov_b32 s95, 678912\nQmaav 216,68,216\nbuffer_load_dwordx4 v63, s[52:55], s95 offen offset:1024 lds\n"
"Qmaav 212,64,176\nQmaav 212,68,184\nQta 58,68,34304\nQmaav 208,64,144\ns_add_u32 m0, s96, 98304\ns_mov_b32 s95, 700416\nQmaav 208,68,152\nbuffer_load_dwordx4 v63, s[52:55], s95 offen lds\n"
"Qmaav 204,64,112\nQta 60,68,41472\nQmaav 204,68,120\nQmaav 200,64,80\nQmaav 200,68,88\nQta 62,68,42496\ns_waitcnt lgkmcnt(4)\nQmaav 228,72,80\n"
"Qmaav 228,76,88\ns_add_u32 s88, s71, 43008\nQmaav 232,72,112\nbuffer_store_dwordx4 v[104:107], v60, s[56:59], s88 offen offset:0 sc1\nQmaav 232,76,120\nQmaav 236,72,144\nQta 64,68,33536\nQmaav 236,76,152\n"
"Qmaav 240,72,176\nQmaav 240,76,184\nQta 66,68,34560\nQmaav 244,72,208\nQmaav 244,76,216\nQmaav 248,72,240\nQta 68,68,41728\nQmaav 248,76,248\n"
"Qmaav 252,72,84\nQmaav 252,76,116\nQta 70,68,42752\ns_waitcnt lgkmcnt(4)\nQmvav 172,56,84\nbuffer_store_dwordx4 v[136:139], v60, s[56:59], s88 offen offset:1024 sc1\nQmvav 172,60,116\nQmvav 140,56,240\n"
"Qmvav 140,60,248\nQmvav 108,56,208\nQta 72,68,49152\nQmvav 108,60,216\nQmvav 228,56,176\nQmvav 228,60,184\nQta 74,68,50176\nQmvav 196,56,144\n"
"Qmvav 196,60,152\nQmvav 164,56,112\nQta 76,68,57344\ns_mov_b32 s95, 719872\nQmvav 164,60,120\nbuffer_load_dwordx4 v63, s[52:55], s95 offen offset:1024 lds\nQmvav 132,56,80\nQmvav 132,60,88\n"
"Qta 78,68,58368\ns_waitcnt lgkmcnt(4)\nQmvav 204,64,80\ns_mov_b32 s31, 229376\nQmvav 204,68,88\nbuffer_load_dwordx4 v[80:83], v60, s[48:51], s31 offen offset:0\nQmvav 236,64,112\nQmvav 236,68,120\n"
"Qmaav 96,64,144\nQta 56,68,49408\nQmaav 96,68,152\nbuffer_load_dwordx4 v[112:115], v60, s[48:51], s31 offen offset:1024\nQmaav 100,64,176\nQmaav 100,68,184\nQta 58,68,50432\nQmaav 104,64,208\n"
"Qmaav 104,68,216\nbuffer_load_dwordx4 v[144:147], v60, s[48:51], s31 offen offset:2048\nQmaav 108,64,240\nQta 60,68,57600\nQmaav 108,68,248\nQmaav 112,64,84\nQmaav 112,68,116\nQta 62,68,58624\n"
"s_waitcnt vmcnt(13)\ns_waitcnt lgkmcnt(4)\nQmaaa 140,72,24\nbuffer_load_dwordx4 v[176:179], v60, s[48:51], s31 offen offset:3072\nQmaaa 140,76,52\nQmaaa 136,72,20\nQmaaa 136,76,48\nQmaaa 132,72,16\n"
"Qta 64,68,49664\ns_mov_b32 s31, 233472\nQmaaa 132,76,44\nbuffer_load_dwordx4 v[208:211], v60, s[48:51], s31 offen offset:0\nQmaaa 128,72,12\nQmaaa 128,76,40\nQta 66,68,50688\nQmaaa 124,72,8\n"
"Qmaaa 124,76,36\nbuffer_load_dwordx4 v[240:243], v60, s[48:51], s31 offen offset:1024\nQmaaa 120,72,4\nQta 68,68,57856\nQmaaa 120,76,32\nQmaaa 116,72,0\nQmaaa 116,76,28\nQta 70,68,58880\n"
"s_waitcnt lgkmcnt(4)\nQmaaa 144,56,0\nbuffer_load_dwordx4 v[84:87], v60, s[48:51], s31 offen offset:2048\nQmaaa 144,60,28\nQmaaa 148,56,4\nQmaaa 148,60,32\nQmaaa 152,56,8\nQta 72,68,49920\n"
"s_mov_b32 s31, 243712\nQmaaa 152,60,36\nbuffer_load_dwordx4 v[88:91], v60, s[48:51], s31 offen offset:0\nQmaaa 156,56,12\nQmaaa 156,60,40\nQta 74,68,50944\nQmaaa 160,56,16\nQmaaa 160,60,44\n"
"buffer_load_dwordx4 v[120:123], v60, s[48:51], s31 offen offset:1024\nQmaaa 164,56,20\nQta 76,68,58112\nQmaaa 164,60,48\nQmaaa 168,56,24\nQmaaa 168,60,52\nQta 78,68,59136\ns_waitcnt lgkmcnt(4)\n"
"Qmvaa 24,64,24\nbuffer_load_dwordx4 v[152:155], v60, s[48:51], s31 offen offset:2048\nQmvaa 24,68,52\nQmvaa 20,64,20\nQmvaa 20,68,48\nQmvaa 16,64,16\nQta 56,70,1024\nQmvaa 16,68,44\n"
"buffer_load_dwordx4 v[184:187], v60, s[48:51], s31 offen offset:3072\nQmvaa 12,64,12\nQmvaa 12,68,40\nQta 58,70,1536\nQmvaa 8,64,8\ns_mov_b32 s31, 247808\nQmvaa 8,68,36\nbuffer_load_dwordx4 v[216:219], v60, s[48:51], s31 offen offset:0\n"
"Qmvaa 4,64,4\nQta 60,70,2048\nv_add_u32 v75, 1, v75\nQmvaa 4,68,32\ns_waitcnt vmcnt(18)\nds_write_b32 v73, v75\nQmvaa 0,64,0\nQmvaa 0,68,28\n"
"Qta 62,70,2560\ns_waitcnt lgkmcnt(5)\nQmvaa 28,72,0\nbuffer_load_dwordx4 v[248:251], v60, s[48:51], s31 offen offset:1024\nQmvaa 28,76,28\nQmvaa 32,72,4\nQmvaa 32,76,32\nQmvaa 36,72,8\n"
"Qta 64,70,1280\nQmvaa 36,76,36\nds_read_b32 v66, v74\nQmvaa 40,72,12\nbuffer_load_dwordx4 v[116:119], v60, s[48:51], s31 offen offset:2048\nQmvaa 40,76,40\nQta 66,70,1792\nQmvaa 44,72,16\n"
"Qmvaa 44,76,44\ns_waitcnt lgkmcnt(1)\nv_cmp_gt_u32 vcc, v75, v66\nQmvaa 48,72,20\ns_cbranch_vccnz .Lso740\n.Lro740:\nQmvaa 48,76,48\nQta 68,70,2304\n"
"s_add_u32 m0, s96, 106496\ns_mov_b32 s95, 741376\nQmvaa 52,72,24\nbuffer_load_dwordx4 v61, s[52:55], s95 offen lds\nQmvaa 52,76,52\ns_waitcnt lgkmcnt(4)\nQmaaa 196,56,24\nQta 70,70,2816\n"
"Qmaaa 196,60,52\ns_mov_b32 s95, 760832\nQmaaa 192,56,20\nbuffer_load_dwordx4 v61, s[52:55], s95 offen offset:1024 lds\nQmaaa 192,60,48\nQmaaa 188,56,16\nQta 72,70,9216\nQmaaa 188,60,44\n"
"s_add_u32 m0, s96, 114688\ns_mov_b32 s95, 782336\nQmaaa 184,56,12\nbuffer_load_dwordx4 v61, s[52:55], s95 offen lds\nQmaaa 184,60,40\nQta 74,70,10240\nQmaaa 180,56,8\nQmaaa 180,60,36\n"
"Qmaaa 176,56,4\nQta 76,70,17408\ns_mov_b32 s95, 801792\nQmaaa 176,60,32\nbuffer_load_dwordx4 v61, s[52:55], s95 offen offset:1024 lds\nQmaaa 172,56,0\nQmaaa 172,60,28\nQta 78,70,18432\n"
"s_waitcnt lgkmcnt(4)\nQmvaa 92,64,0\ns_add_u32 m0, s96, 122880\ns_mov_b32 s95, 741376\nQmvaa 92,68,28\nbuffer_load_dwordx4 v62, s[52:55], s95 offen lds\nQmvaa 124,64,4\nQmvaa 124,68,32\n"
"Qmvaa 156,64,8\nQta 56,70,9472\ns_mov_b32 s95, 781312\nQmvaa 156,68,36\nbuffer_load_dwordx4 v62, s[52:55], s95 offen offset:1024 lds\nQmvaa 188,64,12\nQmvaa 188,68,40\nQta 58,70,10496\n"
"Qmvaa 220,64,16\ns_add_u32 m0, s96, 0\ns_mov_b32 s95, 741376\nQmvaa 220,68,44\nbuffer_load_dwordx4 v63, s[52:55], s95 offen lds\nQmvaa 252,64,20\nQta 60,70,17664\nQmvaa 252,68,48\n"
"Qmvaa 100,64,24\nQmvaa 100,68,52\nQta 62,70,18688\ns_mov_b32 s95, 760832\ns_waitcnt lgkmcnt(4)\nQmaaa 224,72,24\nbuffer_load_dwordx4 v63, s[52:55], s95 offen offset:1024 lds\nQmaaa 224,76,52\n"
"Qmaaa 220,72,20\nQmaaa 220,76,48\nQmaaa 216,72,16\nQta 64,70,9728\ns_add_u32 m0, s96, 8192\ns_mov_b32 s95, 782336\nQmaaa 216,76,44\nbuffer_load_dwordx4 v63, s[52:55], s95 offen lds\n"
"Qmaaa 212,72,12\nQmaaa 212,76,40\nQta 66,70,10752\nQmaaa 208,72,8\ns_mov_b32 s95, 801792\nQmaaa 208,76,36\nbuffer_load_dwordx4 v63, s[52:55], s95 offen offset:1024 lds\nQmaaa 204,72,4\n"
"Qta 68,70,17920\nQmaaa 204,76,32\nQmaaa 200,72,0\nQmaaa 200,76,28\nQta 70,70,18944\ns_waitcnt lgkmcnt(4)\nQmaaa 228,56,0\nQmaaa 228,60,28\n"
"Qmaaa 232,56,4\nbuffer_store_dwordx4 v[168:171], v60, s[56:59], s88 offen offset:2048 sc1\nQmaaa 232,60,32\nQmaaa 236,56,8\nQta 72,70,9984\nQmaaa 236,60,36\nQmaaa 240,56,12\nQmaaa 240,60,40\n"
"Qta 74,70,11008\nQmaaa 244,56,16\nQmaaa 244,60,44\nQmaaa 248,56,20\nQta 76,70,18176\nQmaaa 248,60,48\nQmaaa 252,56,24\nQmaaa 252,60,52\n"
"Qta 78,70,19200\ns_waitcnt lgkmcnt(4)\nQmvaa 172,64,24\nbuffer_store_dwordx4 v[200:203], v60, s[56:59], s88 offen offset:3072 sc1\nQmvaa 172,68,52\nQmvaa 140,64,20\nQmvaa 140,68,48\nQmvaa 108,64,16\n"
"Qta 56,69,0\nQmvaa 108,68,44\nQmvaa 228,64,12\nQmvaa 228,68,40\nQta 58,69,1024\nQmvaa 196,64,8\nQmvaa 196,68,36\nQmvaa 164,64,4\n"
"Qta 60,69,8192\ns_add_u32 m0, s96, 16384\ns_mov_b32 s95, 987136\nQmvaa 164,68,32\nbuffer_load_dwordx4 v61, s[52:55], s95 offen lds\nQmvaa 132,64,0\nQmvaa 132,68,28\nQta 62,69,9216\n"
"s_waitcnt lgkmcnt(4)\nQmvaa 204,72,0\ns_mov_b32 s95, 1006592\nQmvaa 204,76,28\nbuffer_load_dwordx4 v61, s[52:55], s95 offen offset:1024 lds\nQmvaa 236,72,4\nQmvaa 236,76,32\nQmaaa 96,72,8\n"
"Qta 64,69,256\ns_mov_b32 s31, 258048\nQmaaa 96,76,36\nbuffer_load_dwordx4 a[0:3], v60, s[48:51], s31 offen offset:0\nQmaaa 100,72,12\nQmaaa 100,76,40\nQta 66,69,1280\nQmaaa 104,72,16\n"
"Qmaaa 104,76,44\nbuffer_load_dwordx4 a[4:7], v60, s[48:51], s31 offen offset:1024\nQmaaa 108,72,20\nQta 68,69,8448\nQmaaa 108,76,48\nQmaaa 112,72,24\nQmaaa 112,76,52\nQta 70,69,9472\n"
"s_waitcnt vmcnt(16)\ns_waitcnt lgkmcnt(4)\nQmaav 140,56,84\nbuffer_load_dwordx4 a[8:11], v60, s[48:51], s31 offen offset:2048\nQmaav 140,60,116\nQmaav 136,56,240\nQmaav 136,60,248\nQmaav 132,56,208\n"
"Qta 72,69,512\nQmaav 132,60,216\nbuffer_load_dwordx4 a[12:15], v60, s[48:51], s31 offen offset:3072\nQmaav 128,56,176\nQmaav 128,60,184\nQta 74,69,1536\nQmaav 124,56,144\ns_mov_b32 s31, 262144\n"
"Qmaav 124,60,152\n.Lpg16:\ns_cbranch_execz .Lpg20\nbuffer_load_dwordx4 a[16:19], v60, s[48:51], s31 offen offset:0\nQmaav 120,56,112\nQta 76,69,8704\nQmaav 120,60,120\nQmaav 116,56,80\n"
"Qmaav 116,60,88\nQta 78,69,9728\ns_waitcnt lgkmcnt(4)\nQmaav 144,64,80\nbuffer_load_dwordx4 a[20:23], v60, s[48:51], s31 offen offset:1024\nQmaav 144,68,88\nQmaav 148,64,112\nQmaav 148,68,120\n"
"Qmaav 152,64,144\nQta 56,69,768\nQmaav 152,68,152\nbuffer_load_dwordx4 a[24:27], v60, s[48:51], s31 offen offset:2048\nQmaav 156,64,176\nQmaav 156,68,184\nQta 58,69,1792\nQmaav 160,64,208\n"
"s_mov_b32 s31, 272384\nQmaav 160,68,216\nbuffer_load_dwordx4 a[28:31], v60, s[48:51], s31 offen offset:0\nQmaav 164,64,240\nQta 60,69,8960\nQmaav 164,68,248\nQmaav 168,64,84\nQmaav 168,68,116\n"
"Qta 62,69,9984\ns_waitcnt lgkmcnt(4)\nQmvav 24,72,84\nbuffer_load_dwordx4 a[32:35], v60, s[48:51], s31 offen offset:1024\nQmvav 24,76,116\nQmvav 20,72,240\nQmvav 20,76,248\nQmvav 16,72,208\n"
"Qta 64,69,16384\nQmvav 16,76,216\nbuffer_load_dwordx4 a[36:39], v60, s[48:51], s31 offen offset:2048\nQmvav 12,72,176\nQmvav 12,76,184\nQta 66,69,16896\nQmvav 8,72,144\nQmvav 8,76,152\n"
"buffer_load_dwordx4 a[40:43], v60, s[48:51], s31 offen offset:3072\nQmvav 4,72,112\nQta 68,69,17408\nv_add_u32 v75, 1, v75\nQmvav 4,76,120\ns_waitcnt vmcnt(19)\nds_write_b32 v73, v75\nQmvav 0,72,80\n"
"Qmvav 0,76,88\nQta 70,69,17920\ns_mov_b32 s31, 276480\ns_waitcnt lgkmcnt(5)\nQmvav 28,56,80\nbuffer_load_dwordx4 a[44:47], v60, s[48:51], s31 offen offset:0\nQmvav 28,60,88\nQmvav 32,56,112\n"
"Qmvav 32,60,120\nQmvav 36,56,144\nQta 72,69,16640\nQmvav 36,60,152\nds_read_b32 v66, v74\nQmvav 40,56,176\nbuffer_load_dwordx4 a[48:51], v60, s[48:51], s31 offen offset:1024\nQmvav 40,60,184\n"
"Qta 74,69,17152\nQmvav 44,56,208\nQmvav 44,60,216\ns_waitcnt lgkmcnt(1)\nv_cmp_gt_u32 vcc, v75, v66\nQmvav 48,56,240\ns_cbranch_vccnz .Lso880\n.Lro880:\n"
"Qmvav 48,60,248\nQta 76,69,17664\nQmvav 52,56,84\nbuffer_load_dwordx4 a[52:55], v60, s[48:51], s31 offen offset:2048\nQmvav 52,60,116\ns_waitcnt lgkmcnt(4)\nQmaav 196,64,84\nQta 78,69,18176\n"
"Qmaav 196,68,116\ns_add_u32 m0, s96, 24576\ns_mov_b32 s95, 1028096\nQmaav 192,64,240\nbuffer_load_dwordx4 v61, s[52:55], s95 offen lds\nQmaav 192,68,248\nQmaav 188,64,208\nQta 56,69,24576\n"
"Qmaav 188,68,216\ns_mov_b32 s95, 1047552\nQmaav 184,64,176\nbuffer_load_dwordx4 v61, s[52:55], s95 offen offset:1024 lds\nQmaav 184,68,184\nQta 58,69,25600\nQmaav 180,64,144\nQmaav 180,68,152\n"
"Qmaav 176,64,112\nQta 60,69,32768\ns_add_u32 m0, s96, 32768\ns_mov_b32 s95, 987136\nQmaav 176,68,120\nbuffer_load_dwordx4 v62, s[52:55], s95 offen lds\nQmaav 172,64,80\nQmaav 172,68,88\n"
"Qta 62,69,33792\ns_waitcnt lgkmcnt(4)\nQmvav 92,72,80\ns_mov_b32 s95, 1027072\nQmvav 92,76,88\nbuffer_load_dwordx4 v62, s[52:55], s95 offen offset:1024 lds\nQmvav 124,72,112\nQmvav 124,76,120\n"
"Qmvav 156,72,144\nQta 64,69,24832\ns_add_u32 m0, s96, 40960\ns_mov_b32 s95, 987136\nQmvav 156,76,152\nbuffer_load_dwordx4 v63, s[52:55], s95 offen lds\nQmvav 188,72,176\nQmvav 188,76,184\n"
"Qta 66,69,25856\nQmvav 220,72,208\ns_mov_b32 s95, 1006592\nQmvav 220,76,216\nbuffer_load_dwordx4 v63, s[52:55], s95 offen offset:1024 lds\nQmvav 252,72,240\nQta 68,69,33024\nQmvav 252,76,248\n"
"Qmvav 100,72,84\nQmvav 100,76,116\nQta 70,69,34048\ns_add_u32 m0, s96, 49152\ns_mov_b32 s95, 1028096\ns_waitcnt lgkmcnt(4)\nQmaav 224,56,84\nbuffer_load_dwordx4 v63, s[52:55], s95 offen lds\n"
"Qmaav 224,60,116\nQmaav 220,56,240\nQmaav 220,60,248\nQmaav 216,56,208\nQta 72,69,25088\ns_mov_b32 s95, 1047552\nQmaav 216,60,216\nbuffer_load_dwordx4 v63, s[52:55], s95 offen offset:1024 lds\n"
"Qmaav 212,56,176\nQmaav 212,60,184\nQta 74,69,26112\nQmaav 208,56,144\ns_add_u32 m0, s96, 57344\ns_mov_b32 s95, 1069056\nQmaav 208,60,152\nbuffer_load_dwordx4 v61, s[52:55], s95 offen lds\n"
"Qmaav 204,56,112\nQta 76,69,33280\nQmaav 204,60,120\nQmaav 200,56,80\nQmaav 200,60,88\nQta 78,69,34304\ns_waitcnt lgkmcnt(4)\nQmaav 228,64,80\n"
"Qmaav 228,68,88\ns_add_u32 s88, s71, 47104\nQmaav 232,64,112\nbuffer_store_dwordx4 v[232:235], v60, s[56:59], s88 offen offset:0 sc1\nQmaav 232,68,120\nQmaav 236,64,144\nQta 56,69,25344\nQmaav 236,68,152\n"
"Qmaav 240,64,176\nQmaav 240,68,184\nQta 58,69,26368\nQmaav 244,64,208\nQmaav 244,68,216\nQmaav 248,64,240\nQta 60,69,33536\nQmaav 248,68,248\n"
"Qmaav 252,64,84\nQmaav 252,68,116\nQta 62,69,34560\ns_waitcnt lgkmcnt(4)\nQmvav 172,72,84\nbuffer_store_dwordx4 v[212:215], v60, s[56:59], s88 offen offset:1024 sc1\nQmvav 172,76,116\nQmvav 140,72,240\n"
"Qmvav 140,76,248\nQmvav 108,72,208\nQta 64,69,40960\nQmvav 108,76,216\nQmvav 228,72,176\nQmvav 228,76,184\nQta 66,69,41984\nQmvav 196,72,144\n"
"Qmvav 196,76,152\nQmvav 164,72,112\nQta 68,69,49152\ns_mov_b32 s95, 1088512\nQmvav 164,76,120\nbuffer_load_dwordx4 v61, s[52:55], s95 offen offset:1024 lds\nQmvav 132,72,80\nQmvav 132,76,88\n"
"Qta 70,69,50176\ns_waitcnt lgkmcnt(4)\nQmvav 204,56,80\ns_mov_b32 s31, 344064\nQmvav 204,60,88\nbuffer_load_dwordx4 v[80:83], v60, s[48:51], s31 offen offset:0\nQmvav 236,56,112\nQmvav 236,60,120\n"
"Qmaav 96,56,144\nQta 72,69,41216\nQmaav 96,60,152\nbuffer_load_dwordx4 v[112:115], v60, s[48:51], s31 offen offset:1024\nQmaav 100,56,176\nQmaav 100,60,184\nQta 74,69,42240\nQmaav 104,56,208\n"
"Qmaav 104,60,216\nbuffer_load_dwordx4 v[144:147], v60, s[48:51], s31 offen offset:2048\nQmaav 108,56,240\nQta 76,69,49408\nQmaav 108,60,248\nQmaav 112,56,84\nv_accvgpr_read_b32 v96, a96\nv_accvgpr_read_b32 v97, a97\n"
"Qmaav 112,60,116\nQta 78,69,50432\nv_accvgpr_read_b32 v98, a98\ns_waitcnt vmcnt(15)\ns_waitcnt lgkmcnt(4)\nQmaaa 140,64,24\nbuffer_load_dwordx4 v[176:179], v60, s[48:51], s31 offen offset:3072\nQmaaa 140,68,52\n"
"v_accvgpr_read_b32 v99, a99\nv_accvgpr_read_b32 v128, a100\nQmaaa 136,64,20\nv_accvgpr_read_b32 v129, a101\nv_accvgpr_read_b32 v130, a102\nQmaaa 136,68,48\nv_accvgpr_read_b32 v131, a103\nv_accvgpr_read_b32 v160, a104\n"
"Qmaaa 132,64,16\nQta 56,69,41472\ns_mov_b32 s31, 348160\nQmaaa 132,68,44\nbuffer_load_dwordx4 v[208:211], v60, s[48:51], s31 offen offset:0\nQmaaa 128,64,12\nv_accvgpr_read_b32 v161, a105\nv_accvgpr_read_b32 v162, a106\n"
"Qmaaa 128,68,40\nQta 58,69,42496\nv_accvgpr_read_b32 v163, a107\nQmaaa 124,64,8\nv_accvgpr_read_b32 v192, a108\nv_accvgpr_read_b32 v193, a109\nQmaaa 124,68,36\nbuffer_load_dwordx4 v[240:243], v60, s[48:51], s31 offen offset:1024\n"
"Qmaaa 120,64,4\nQta 60,69,49664\nv_accvgpr_read_b32 v194, a110\nQmaaa 120,68,32\nv_accvgpr_read_b32 v195, a111\nv_accvgpr_read_b32 v224, a112\nQmaaa 116,64,0\nv_accvgpr_read_b32 v225, a113\n"
"v_accvgpr_read_b32 v226, a114\nQmaaa 116,68,28\nQta 62,69,50688\nv_accvgpr_read_b32 v227, a115\ns_waitcnt lgkmcnt(4)\nQmaaa 144,72,0\nbuffer_load_dwordx4 v[84:87], v60, s[48:51], s31 offen offset:2048\nQmaaa 144,76,28\n"
"Qmaaa 148,72,4\nQmaaa 148,76,32\nQmaaa 152,72,8\nQta 64,69,41728\ns_mov_b32 s31, 358400\nQmaaa 152,76,36\nbuffer_load_dwordx4 v[88:91], v60, s[48:51], s31 offen offset:0\nQmaaa 156,72,12\n"
"Qmaaa 156,76,40\nQta 66,69,42752\nQmaaa 160,72,16\nQmaaa 160,76,44\nbuffer_load_dwordx4 v[120:123], v60, s[48:51], s31 offen offset:1024\nQmaaa 164,72,20\nQta 68,69,49920\nQmaaa 164,76,48\n"
"Qmaaa 168,72,24\nQmaaa 168,76,52\nQta 70,69,50944\ns_waitcnt lgkmcnt(4)\nQmvaa 24,56,24\nbuffer_load_dwordx4 v[152:155], v60, s[48:51], s31 offen offset:2048\nQmvaa 24,60,52\nQmvaa 20,56,20\n"
"Qmvaa 20,60,48\nQmvaa 16,56,16\nQta 72,69,57344\nQmvaa 16,60,44\nbuffer_load_dwordx4 v[184:187], v60, s[48:51], s31 offen offset:3072\nQmvaa 12,56,12\nQmvaa 12,60,40\nQta 74,69,57856\n"
"Qmvaa 8,56,8\ns_mov_b32 s31, 362496\nQmvaa 8,60,36\nbuffer_load_dwordx4 v[216:219], v60, s[48:51], s31 offen offset:0\nQmvaa 4,56,4\nQta 76,69,58368\nv_add_u32 v75, 1, v75\nQmvaa 4,60,32\n"
"s_waitcnt vmcnt(20)\nds_write_b32 v73, v75\nQmvaa 0,56,0\nQmvaa 0,60,28\nQta 78,69,58880\ns_waitcnt lgkmcnt(5)\nQmvaa 28,64,0\nbuffer_load_dwordx4 v[248:251], v60, s[48:51], s31 offen offset:1024\n"
"Qmvaa 28,68,28\nQmvaa 32,64,4\nQmvaa 32,68,32\nQmvaa 36,64,8\nQta 56,69,57600\nQmvaa 36,68,36\nds_read_b32 v66, v74\nQmvaa 40,64,12\n"
"buffer_load_dwordx4 v[116:119], v60, s[48:51], s31 offen offset:2048\nQmvaa 40,68,40\nQta 58,69,58112\nQmvaa 44,64,16\nQmvaa 44,68,44\ns_waitcnt lgkmcnt(1)\nv_cmp_gt_u32 vcc, v75, v66\nQmvaa 48,64,20\n"
"s_cbranch_vccnz .Lso1020\n.Lro1020:\nQmvaa 48,68,48\nQta 60,69,58624\ns_add_u32 m0, s96, 132096\ns_mov_b32 s95, 1110016\nQmvaa 52,64,24\nbuffer_load_dwordx4 v61, s[52:55], s95 offen lds\n"
"Qmvaa 52,68,52\ns_waitcnt lgkmcnt(4)\nQmaaa 196,72,24\nQta 62,69,59136\nQmaaa 196,76,52\ns_mov_b32 s95, 1129472\nQmaaa 192,72,20\nbuffer_load_dwordx4 v61, s[52:55], s95 offen offset:1024 lds\n"
"Qmaaa 192,76,48\nQmaaa 188,72,16\nQta 64,68,0\nQmaaa 188,76,44\ns_add_u32 m0, s96, 140288\ns_mov_b32 s95, 1069056\nQmaaa 184,72,12\nbuffer_load_dwordx4 v62, s[52:55], s95 offen lds\n"
"Qmaaa 184,76,40\nQta 66,68,1024\nQmaaa 180,72,8\nQmaaa 180,76,36\nQmaaa 176,72,4\nQta 68,68,8192\ns_mov_b32 s95, 1108992\nQmaaa 176,76,32\n"
"buffer_load_dwordx4 v62, s[52:55], s95 offen offset:1024 lds\nQmaaa 172,72,0\nQmaaa 172,76,28\nQta 70,68,9216\ns_waitcnt lgkmcnt(4)\nQmvaa 92,56,0\ns_add_u32 m0, s96, 148480\ns_mov_b32 s95, 1069056\n"
"Qmvaa 92,60,28\nbuffer_load_dwordx4 v63, s[52:55], s95 offen lds\nQmvaa 124,56,4\nQmvaa 124,60,32\nQmvaa 156,56,8\nQta 72,68,256\ns_mov_b32 s95, 1088512\nQmvaa 156,60,36\n"
"buffer_load_dwordx4 v63, s[52:55], s95 offen offset:1024 lds\nQmvaa 188,56,12\nQmvaa 188,60,40\nQta 74,68,1280\nQmvaa 220,56,16\ns_add_u32 m0, s96, 65536\ns_mov_b32 s95, 1110016\nQmvaa 220,60,44\n"
"buffer_load_dwordx4 v63, s[52:55], s95 offen lds\nQmvaa 252,56,20\nQta 76,68,8448\nQmvaa 252,60,48\nQmvaa 100,56,24\nQmvaa 100,60,52\nQta 78,68,9472\ns_mov_b32 s95, 1129472\n"
"s_waitcnt lgkmcnt(4)\nQmaaa 224,64,24\nbuffer_load_dwordx4 v63, s[52:55], s95 offen offset:1024 lds\nQmaaa 224,68,52\nQmaaa 220,64,20\nQmaaa 220,68,48\nQmaaa 216,64,16\nQta 56,68,512\n"
"s_add_u32 m0, s96, 73728\ns_add_u32 s95, s67, 4096\nQmaaa 216,68,44\nbuffer_load_dwordx4 v61, s[52:55], s95 offen lds\nQmaaa 212,64,12\nQmaaa 212,68,40\nQta 58,68,1536\nQmaaa 208,64,8\n"
"s_add_u32 s95, s67, 23552\nQmaaa 208,68,36\nbuffer_load_dwordx4 v61, s[52:55], s95 offen offset:1024 lds\nQmaaa 204,64,4\nQta 60,68,8704\nQmaaa 204,68,32\nQmaaa 200,64,0\nQmaaa 200,68,28\n"
"Qta 62,68,9728\ns_waitcnt lgkmcnt(4)\nQmaaa 228,72,0\nQmaaa 228,76,28\nQmaaa 232,72,4\nbuffer_store_dwordx4 v[244:247], v60, s[56:59], s88 offen offset:2048 sc1\nQmaaa 232,76,32\nQmaaa 236,72,8\n"
"Qta 64,68,768\nQmaaa 236,76,36\nQmaaa 240,72,12\nQmaaa 240,76,40\nQta 66,68,1792\nQmaaa 244,72,16\nQmaaa 244,76,44\nQmaaa 248,72,20\n"
"Qta 68,68,8960\nQmaaa 248,76,48\ns_add_u32 m0, s96, 81920\ns_add_u32 s95, s67, 45056\nQmaaa 252,72,24\nbuffer_load_dwordx4 v61, s[52:55], s95 offen lds\nQmaaa 252,76,52\nQta 70,68,9984\n"
"s_waitcnt lgkmcnt(4)\nQmvaa 172,56,24\nQmvaa 172,60,52\ns_add_u32 s95, s67, 64512\nQmvaa 140,56,20\nbuffer_load_dwordx4 v61, s[52:55], s95 offen offset:1024 lds\nQmvaa 140,60,48\nQmvaa 108,56,16\n"
"Qta 72,68,16384\nQmvaa 108,60,44\ns_add_u32 m0, s96, 90112\ns_add_u32 s95, s67, 4096\nQmvaa 228,56,12\nbuffer_load_dwordx4 v62, s[52:55], s95 offen lds\nQmvaa 228,60,40\nQta 74,68,17408\n"
"Qmvaa 196,56,8\nQmvaa 196,60,36\nQmvaa 164,56,4\nQta 76,68,24576\ns_add_u32 s95, s67, 44032\nQmvaa 164,60,32\nbuffer_load_dwordx4 v62, s[52:55], s95 offen offset:1024 lds\nQmvaa 132,56,0\n"
"Qmvaa 132,60,28\nQta 78,68,25600\ns_waitcnt lgkmcnt(4)\nQmvaa 204,64,0\nbuffer_load_dword v78, v76, s[24:27], 0 offen sc1\ns_mov_b32 s31, 372736\nQmvaa 204,68,28\nbuffer_load_dwordx4 a[0:3], v60, s[48:51], s31 offen offset:0\n"
"Qmvaa 236,64,4\nQmvaa 236,68,32\nQmvaa 96,64,8\nQta 56,68,16640\nQmvaa 96,68,36\nbuffer_load_dwordx4 a[4:7], v60, s[48:51], s31 offen offset:1024\nQmvaa 128,64,12\nQmvaa 128,68,40\n"
"Qta 58,68,17664\nQmvaa 160,64,16\nQmvaa 160,68,44\nbuffer_load_dwordx4 a[8:11], v60, s[48:51], s31 offen offset:2048\nQmvaa 192,64,20\nQta 60,68,24832\nQmvaa 192,68,48\nQmvaa 224,64,24\n"
"Qmvaa 224,68,52\nQta 62,68,25856\ns_waitcnt vmcnt(19)\ns_waitcnt lgkmcnt(4)\nQmaav 140,72,84\nbuffer_load_dwordx4 a[12:15], v60, s[48:51], s31 offen offset:3072\nQmaav 140,76,116\nQmaav 136,72,240\n"
"Qmaav 136,76,248\nQmaav 132,72,208\nQta 64,68,16896\ns_mov_b32 s31, 376832\nQmaav 132,76,216\nbuffer_load_dwordx4 a[16:19], v60, s[48:51], s31 offen offset:0\nQmaav 128,72,176\nQmaav 128,76,184\n"
"Qta 66,68,17920\nQmaav 124,72,144\nQmaav 124,76,152\nbuffer_load_dwordx4 a[20:23], v60, s[48:51], s31 offen offset:1024\nQmaav 120,72,112\nQta 68,68,25088\nQmaav 120,76,120\nQmaav 116,72,80\n"
"Qmaav 116,76,88\nQta 70,68,26112\ns_waitcnt lgkmcnt(4)\nQmaav 144,56,80\nbuffer_load_dwordx4 a[24:27], v60, s[48:51], s31 offen offset:2048\n.Lpg17:\ns_cbranch_execz .Lpg21\nQmaav 144,60,88\n"
"Qmaav 148,56,112\nQmaav 148,60,120\nQmaav 152,56,144\nQta 72,68,17152\ns_mov_b32 s31, 387072\nQmaav 152,60,152\nbuffer_load_dwordx4 a[28:31], v60, s[48:51], s31 offen offset:0\nQmaav 156,56,176\n"
"Qmaav 156,60,184\nQta 74,68,18176\nQmaav 160,56,208\nQmaav 160,60,216\nbuffer_load_dwordx4 a[32:35], v60, s[48:51], s31 offen offset:1024\nQmaav 164,56,240\nQta 76,68,25344\nQmaav 164,60,248\n"
"Qmaav 168,56,84\nQmaav 168,60,116\nQta 78,68,26368\ns_waitcnt lgkmcnt(4)\nQmvav 24,64,84\nbuffer_load_dwordx4 a[36:39], v60, s[48:51], s31 offen offset:2048\nQmvav 24,68,116\nQmvav 20,64,240\n"
"Qmvav 20,68,248\nQmvav 16,64,208\nQta 56,68,32768\nQmvav 16,68,216\nbuffer_load_dwordx4 a[40:43], v60, s[48:51], s31 offen offset:3072\nQmvav 12,64,176\nQmvav 12,68,184\nQta 58,68,33280\n"
"Qmvav 8,64,144\ns_mov_b32 s31, 391168\nQmvav 8,68,152\nbuffer_load_dwordx4 a[44:47], v60, s[48:51], s31 offen offset:0\nQmvav 4,64,112\nQta 60,68,33792\nv_add_u32 v75, 1, v75\nQmvav 4,68,120\n"
"s_waitcnt vmcnt(24)\nds_write_b32 v73, v75\nQmvav 0,64,80\nQmvav 0,68,88\nQta 62,68,34304\ns_waitcnt lgkmcnt(5)\nQmvav 28,72,80\nbuffer_load_dwordx4 a[48:51], v60, s[48:51], s31 offen offset:1024\n"
"Qmvav 28,76,88\nQmvav 32,72,112\nQmvav 32,76,120\ns_waitcnt vmcnt(13)\nv_sub_u32 v66, v78, v65\nv_cmp_gt_u32 vcc, 2, v66\ns_and_b64 vcc, vcc, s[28:29]\ns_cbranch_scc1 .Lf2_w\n"
".Lf2_r:\nQmvav 36,72,144\nQta 64,68,33024\nQmvav 36,76,152\nds_read_b32 v66, v74\nQmvav 40,72,176\nbuffer_load_dwordx4 a[52:55], v60, s[48:51], s31 offen offset:2048\nQmvav 40,76,184\n"
"Qta 66,68,33536\nQmvav 44,72,208\nQmvav 44,76,216\ns_waitcnt lgkmcnt(1)\nv_cmp_gt_u32 vcc, v75, v66\nQmvav 48,72,240\ns_cbranch_vccnz .Lso1160\n.Lro1160:\n"
"Qmvav 48,76,248\nQta 68,68,34048\ns_add_u32 s31, s74, 0\nQmvav 52,72,84\nbuffer_load_dwordx4 a[80:83], v60, s[48:51], s31 offen offset:0 sc1\nQmvav 52,76,116\ns_waitcnt lgkmcnt(4)\nQmaav 196,56,84\n"
"Qta 70,68,34560\nQmaav 196,60,116\nQmaav 192,56,240\nbuffer_load_dwordx4 a[84:87], v60, s[48:51], s31 offen offset:1024 sc1\nQmaav 192,60,248\nQmaav 188,56,208\nQta 72,68,40960\nQmaav 188,60,216\n"
"Qmaav 184,56,176\nbuffer_load_dwordx4 a[88:91], v60, s[48:51], s31 offen offset:2048 sc1\nQmaav 184,60,184\nQta 74,68,41984\nQmaav 180,56,144\nQmaav 180,60,152\nQmaav 176,56,112\nQta 76,68,49152\n"
"Qmaav 176,60,120\nbuffer_load_dwordx4 a[92:95], v60, s[48:51], s31 offen offset:3072 sc1\nQmaav 172,56,80\nQmaav 172,60,88\nQta 78,68,50176\ns_waitcnt lgkmcnt(4)\nQmvav 92,64,80\ns_add_u32 s31, s74, 4096\n"
"Qmvav 92,68,88\nbuffer_load_dwordx4 a[96:99], v60, s[48:51], s31 offen offset:0 sc1\nQmvav 124,64,112\nQmvav 124,68,120\nQmvav 156,64,144\nQta 56,68,41216\nQmvav 156,68,152\nbuffer_load_dwordx4 a[100:103], v60, s[48:51], s31 offen offset:1024 sc1\n"
"Qmvav 188,64,176\nQmvav 188,68,184\nQta 58,68,42240\nQmvav 220,64,208\nQmvav 220,68,216\nbuffer_load_dwordx4 a[104:107], v60, s[48:51], s31 offen offset:2048 sc1\nQmvav 252,64,240\nQta 60,68,49408\n"
"Qmvav 252,68,248\nQmvav 100,64,84\nQmvav 100,68,116\nQta 62,68,50432\ns_add_u32 s31, s74, 14336\ns_waitcnt lgkmcnt(4)\nQmaav 224,72,84\nbuffer_load_dwordx4 a[108:111], v60, s[48:51], s31 offen offset:0 sc1\n"
"Qmaav 224,76,116\nQmaav 220,72,240\nQmaav 220,76,248\nQmaav 216,72,208\nQta 64,68,41472\nQmaav 216,76,216\nbuffer_load_dwordx4 a[112:115], v60, s[48:51], s31 offen offset:1024 sc1\nQmaav 212,72,176\n"
"Qmaav 212,76,184\nQta 66,68,42496\nQmaav 208,72,144\nQmaav 208,76,152\nbuffer_load_dwordx4 v[168:171], v60, s[48:51], s31 offen offset:2048 sc1\nQmaav 204,72,112\nQta 68,68,49664\nQmaav 204,76,120\n"
"Qmaav 200,72,80\nQmaav 200,76,88\nQta 70,68,50688\ns_waitcnt lgkmcnt(4)\nQmaav 228,56,80\nbuffer_load_dwordx4 v[200:203], v60, s[48:51], s31 offen offset:3072 sc1\nQmaav 228,60,88\nQmaav 232,56,112\n"
"Qmaav 232,60,120\nQmaav 236,56,144\nQta 72,68,41728\ns_add_u32 s31, s74, 18432\nQmaav 236,60,152\nbuffer_load_dwordx4 v[232:235], v60, s[48:51], s31 offen offset:0 sc1\nQmaav 240,56,176\nQmaav 240,60,184\n"
"Qta 74,68,42752\nQmaav 244,56,208\nQmaav 244,60,216\nbuffer_load_dwordx4 v[212:215], v60, s[48:51], s31 offen offset:1024 sc1\nQmaav 248,56,240\nQta 76,68,49920\nQmaav 248,60,248\nQmaav 252,56,84\n"
"Qmaav 252,60,116\nQta 78,68,50944\ns_waitcnt lgkmcnt(4)\nQmvav 172,64,84\nbuffer_load_dwordx4 v[244:247], v60, s[48:51], s31 offen offset:2048 sc1\nQmvav 172,68,116\nQmvav 140,64,240\nQmvav 140,68,248\n"
"Qmvav 108,64,208\nQta 56,68,57344\ns_add_u32 m0, s96, 98304\ns_add_u32 s95, s67, 4096\nQmvav 108,68,216\nbuffer_load_dwordx4 v63, s[52:55], s95 offen lds\nQmvav 228,64,176\nQmvav 228,68,184\n"
"Qta 58,68,58368\nQmvav 196,64,144\ns_add_u32 s95, s67, 23552\nQmvav 196,68,152\nbuffer_load_dwordx4 v63, s[52:55], s95 offen offset:1024 lds\nQmvav 164,64,112\nQta 60,70,1024\nQmvav 164,68,120\n"
"Qmvav 132,64,80\nQmvav 132,68,88\nQta 62,70,2048\ns_add_u32 m0, s96, 106496\ns_add_u32 s95, s67, 45056\ns_waitcnt lgkmcnt(4)\nQmvav 204,72,80\nbuffer_load_dwordx4 v63, s[52:55], s95 offen lds\n"
"Qmvav 204,76,88\nQmvav 236,72,112\nQmvav 236,76,120\nQmvav 96,72,144\nQta 64,68,57600\ns_add_u32 s95, s67, 64512\nQmvav 96,76,152\nbuffer_load_dwordx4 v63, s[52:55], s95 offen offset:1024 lds\n"
"Qmvav 128,72,176\nQmvav 128,76,184\nQta 66,68,58624\nQmvav 160,72,208\ns_add_u32 m0, s96, 114688\ns_add_u32 s95, s72, 4096\nQmvav 160,76,216\nbuffer_load_dwordx4 v61, s[52:55], s95 offen lds\n"
"Qmvav 192,72,240\nQta 68,70,1280\nQmvav 192,76,248\nQmvav 224,72,84\nQmvav 224,76,116\nQta 70,70,2304\ns_add_u32 s95, s72, 23552\ns_waitcnt vmcnt(19)\n"
"s_waitcnt lgkmcnt(4)\nQmaaa 140,56,24\nbuffer_load_dwordx4 v61, s[52:55], s95 offen offset:1024 lds\nQmaaa 140,60,52\nQmaaa 136,56,20\nQmaaa 136,60,48\nQmaaa 132,56,16\nQta 72,68,57856\n"
"s_add_u32 m0, s96, 122880\ns_add_u32 s95, s72, 45056\nQmaaa 132,60,44\nbuffer_load_dwordx4 v61, s[52:55], s95 offen lds\nQmaaa 128,56,12\nQmaaa 128,60,40\nQta 74,68,58880\nQmaaa 124,56,8\n"
"s_add_u32 s95, s72, 64512\nQmaaa 124,60,36\nbuffer_load_dwordx4 v61, s[52:55], s95 offen offset:1024 lds\nQmaaa 120,56,4\nQta 76,70,1536\nQmaaa 120,60,32\nQmaaa 116,56,0\nQmaaa 116,60,28\n"
"Qta 78,70,2560\ns_add_u32 m0, s96, 0\ns_add_u32 s95, s72, 4096\ns_waitcnt lgkmcnt(4)\nQmaaa 144,64,0\nbuffer_load_dwordx4 v62, s[52:55], s95 offen lds\nQmaaa 144,68,28\nQmaaa 148,64,4\n"
"Qmaaa 148,68,32\nQmaaa 152,64,8\nQta 56,68,58112\ns_add_u32 s95, s72, 44032\nQmaaa 152,68,36\nbuffer_load_dwordx4 v62, s[52:55], s95 offen offset:1024 lds\nQmaaa 156,64,12\nQmaaa 156,68,40\n"
"Qta 58,68,59136\nQmaaa 160,64,16\ns_add_u32 m0, s96, 8192\ns_add_u32 s95, s72, 4096\nQmaaa 160,68,44\nbuffer_load_dwordx4 v63, s[52:55], s95 offen lds\nQmaaa 164,64,20\nQta 60,70,1792\n"
"Qmaaa 164,68,48\nQmaaa 168,64,24\nQmaaa 168,68,52\nQta 62,70,2816\ns_add_u32 s95, s72, 23552\ns_waitcnt lgkmcnt(4)\nQmvaa 24,72,24\nbuffer_load_dwordx4 v63, s[52:55], s95 offen offset:1024 lds\n"
"Qmvaa 24,76,52\nQmvaa 20,72,20\nQmvaa 20,76,48\nQmvaa 16,72,16\nQta 64,70,9216\ns_add_u32 s31, s75, 0\nQmvaa 16,76,44\nbuffer_load_dwordx4 v[80:83], v60, s[48:51], s31 offen offset:0 sc1\n"
"Qmvaa 12,72,12\nQmvaa 12,76,40\nQta 66,70,9728\nQmvaa 8,72,8\nQmvaa 8,76,36\nbuffer_load_dwordx4 v[112:115], v60, s[48:51], s31 offen offset:1024 sc1\nQmvaa 4,72,4\nQta 68,70,10240\n"
"v_add_u32 v75, 1, v75\nQmvaa 4,76,32\ns_waitcnt vmcnt(43)\nds_write_b32 v73, v75\nQmvaa 0,72,0\nQmvaa 0,76,28\nQta 70,70,10752\ns_waitcnt lgkmcnt(5)\n"
"Qmvaa 28,56,0\nbuffer_load_dwordx4 v[144:147], v60, s[48:51], s31 offen offset:2048 sc1\nQmvaa 28,60,28\nQmvaa 32,56,4\nQmvaa 32,60,32\nQmvaa 36,56,8\nQta 72,70,9472\nQmvaa 36,60,36\n"
"ds_read_b32 v66, v74\nQmvaa 40,56,12\nbuffer_load_dwordx4 v[176:179], v60, s[48:51], s31 offen offset:3072 sc1\nQmvaa 40,60,40\nQta 74,70,9984\nQmvaa 44,56,16\nv_add_u32 v67, 3, v65\nQmvaa 44,60,44\n"
"s_waitcnt lgkmcnt(1)\nv_cmp_gt_u32 vcc, v75, v66\nQmvaa 48,56,20\ns_cbranch_vccnz .Lso1300\n.Lro1300:\nQmvaa 48,60,48\nQta 76,70,10496\ns_add_u32 s31, s75, 4096\n"
"Qmvaa 52,56,24\nbuffer_load_dwordx4 v[208:211], v60, s[48:51], s31 offen offset:0 sc1\nQmvaa 52,60,52\nbuffer_store_dword v67, v77, s[24:27], 0 offen sc1\ns_waitcnt lgkmcnt(4)\nQmaaa 196,64,24\nQta 78,70,11008\nQmaaa 196,68,52\n"
"Qmaaa 192,64,20\nbuffer_load_dwordx4 v[240:243], v60, s[48:51], s31 offen offset:1024 sc1\nQmaaa 192,68,48\nQmaaa 188,64,16\nQta 56,70,17408\nQmaaa 188,68,44\nQmaaa 184,64,12\nbuffer_load_dwordx4 v[84:87], v60, s[48:51], s31 offen offset:2048 sc1\n"
"Qmaaa 184,68,40\nQta 58,70,18432\nQmaaa 180,64,8\nQmaaa 180,68,36\nQmaaa 176,64,4\nQta 60,69,0\ns_add_u32 s31, s75, 14336\nQmaaa 176,68,32\n"
"buffer_load_dwordx4 v[88:91], v60, s[48:51], s31 offen offset:0 sc1\nQmaaa 172,64,0\nQmaaa 172,68,28\nQta 62,69,1024\ns_waitcnt lgkmcnt(4)\nQmvaa 92,72,0\nQmvaa 92,76,28\nbuffer_load_dwordx4 v[120:123], v60, s[48:51], s31 offen offset:1024 sc1\n"
"Qmvaa 124,72,4\nQmvaa 124,76,32\nQmvaa 156,72,8\nQta 64,70,17664\nQmvaa 156,76,36\nbuffer_load_dwordx4 v[152:155], v60, s[48:51], s31 offen offset:2048 sc1\nQmvaa 188,72,12\nQmvaa 188,76,40\n"
"Qta 66,70,18688\nQmvaa 220,72,16\nQmvaa 220,76,44\nbuffer_load_dwordx4 v[184:187], v60, s[48:51], s31 offen offset:3072 sc1\nQmvaa 252,72,20\nQta 68,69,256\nQmvaa 252,76,48\nQmvaa 100,72,24\n"
"Qmvaa 100,76,52\nQta 70,69,1280\ns_add_u32 s31, s75, 18432\ns_waitcnt lgkmcnt(4)\nQmaaa 224,56,24\nbuffer_load_dwordx4 v[216:219], v60, s[48:51], s31 offen offset:0 sc1\nQmaaa 224,60,52\nQmaaa 220,56,20\n"
"Qmaaa 220,60,48\nQmaaa 216,56,16\nQta 72,70,17920\nQmaaa 216,60,44\nbuffer_load_dwordx4 v[248:251], v60, s[48:51], s31 offen offset:1024 sc1\nQmaaa 212,56,12\nQmaaa 212,60,40\nQta 74,70,18944\n"
"Qmaaa 208,56,8\nQmaaa 208,60,36\nbuffer_load_dwordx4 v[116:119], v60, s[48:51], s31 offen offset:2048 sc1\nQmaaa 204,56,4\nQta 76,69,512\nQmaaa 204,60,32\nQmaaa 200,56,0\nQmaaa 200,60,28\n"
"Qta 78,69,1536\ns_add_u32 m0, s96, 16384\ns_add_u32 s95, s72, 45056\ns_waitcnt lgkmcnt(4)\nQmaaa 228,64,0\nbuffer_load_dwordx4 v63, s[52:55], s95 offen lds\nQmaaa 228,68,28\nQmaaa 232,64,4\n"
"Qmaaa 232,68,32\nQmaaa 236,64,8\nQta 56,70,18176\ns_add_u32 s95, s72, 64512\nQmaaa 236,68,36\nbuffer_load_dwordx4 v63, s[52:55], s95 offen offset:1024 lds\nQmaaa 240,64,12\nQmaaa 240,68,40\n"
"Qta 58,70,19200\nQmaaa 244,64,16\ns_add_u32 m0, s96, 24576\ns_add_u32 s95, s73, 4096\nQmaaa 244,68,44\nbuffer_load_dwordx4 v61, s[52:55], s95 offen lds\nQmaaa 248,64,20\nQta 60,69,768\n"
"Qmaaa 248,68,48\nQmaaa 252,64,24\nQmaaa 252,68,52\nQta 62,69,1792\ns_add_u32 s95, s73, 23552\ns_waitcnt lgkmcnt(4)\nQmvaa 172,72,24\nbuffer_load_dwordx4 v61, s[52:55], s95 offen offset:1024 lds\n"
"Qmvaa 172,76,52\nQmvaa 140,72,20\nQmvaa 140,76,48\nQmvaa 108,72,16\nQta 64,69,8192\ns_add_u32 m0, s96, 32768\ns_add_u32 s95, s73, 45056\nQmvaa 108,76,44\n"
"buffer_load_dwordx4 v61, s[52:55], s95 offen lds\nQmvaa 228,72,12\nQmvaa 228,76,40\nQta 66,69,9216\nQmvaa 196,72,8\ns_add_u32 s95, s73, 64512\nQmvaa 196,76,36\nbuffer_load_dwordx4 v61, s[52:55], s95 offen offset:1024 lds\n"
"Qmvaa 164,72,4\nQta 68,69,16384\nQmvaa 164,76,32\nQmvaa 132,72,0\nQmvaa 132,76,28\nQta 70,69,17408\ns_add_u32 m0, s96, 40960\ns_add_u32 s95, s73, 4096\n"
"s_waitcnt lgkmcnt(4)\nQmvaa 204,56,0\nbuffer_load_dwordx4 v62, s[52:55], s95 offen lds\nQmvaa 204,60,28\nQmvaa 236,56,4\nQmvaa 236,60,32\nQmvaa 96,56,8\nQta 72,69,8448\n"
"s_add_u32 s95, s73, 44032\nQmvaa 96,60,36\nbuffer_load_dwordx4 v62, s[52:55], s95 offen offset:1024 lds\nQmvaa 128,56,12\nQmvaa 128,60,40\nQta 74,69,9472\nQmvaa 160,56,16\ns_add_u32 m0, s96, 49152\n"
"s_add_u32 s95, s73, 4096\nQmvaa 160,60,44\nbuffer_load_dwordx4 v63, s[52:55], s95 offen lds\nQmvaa 192,56,20\nQta 76,69,16640\nQmvaa 192,60,48\nQmvaa 224,56,24\nQmvaa 224,60,52\n"
"Qta 78,69,17664\ns_add_u32 s95, s73, 23552\ns_waitcnt vmcnt(36)\ns_waitcnt lgkmcnt(4)\nQmaaa 140,64,104\nbuffer_load_dwordx4 v63, s[52:55], s95 offen offset:1024 lds\nQmaav 140,68,244\nQmaaa 136,64,100\n"
"Qmaav 136,68,212\nQmaaa 132,64,96\nQta 56,69,8704\ns_add_u32 s31, s77, 0\nQmaav 132,68,232\nbuffer_load_dwordx4 a[0:3], v60, s[48:51], s31 offen offset:0 sc1\nQmaaa 128,64,92\nQmaav 128,68,200\n"
"Qta 58,69,9728\nQmaaa 124,64,88\nQmaav 124,68,168\nbuffer_load_dwordx4 a[4:7], v60, s[48:51], s31 offen offset:1024 sc1\nQmaaa 120,64,84\nQta 60,69,16896\nQmaaa 120,68,112\nQmaaa 116,64,80\n"
"Qmaaa 116,68,108\nQta 62,69,17920\ns_waitcnt lgkmcnt(4)\nQmaaa 144,72,80\nbuffer_load_dwordx4 a[8:11], v60, s[48:51], s31 offen offset:2048 sc1\nQmaaa 144,76,108\nQmaaa 148,72,84\nQmaaa 148,76,112\n"
"Qmaaa 152,72,88\nQta 64,69,8960\nQmaav 152,76,168\nbuffer_load_dwordx4 a[12:15], v60, s[48:51], s31 offen offset:3072 sc1\nQmaaa 156,72,92\nQmaav 156,76,200\nQta 66,69,9984\nQmaaa 160,72,96\n"
"s_add_u32 s31, s77, 4096\nQmaav 160,76,232\nbuffer_load_dwordx4 a[16:19], v60, s[48:51], s31 offen offset:0 sc1\nQmaaa 164,72,100\nQta 68,69,17152\nQmaav 164,76,212\nQmaaa 168,72,104\nQmaav 168,76,244\n"
"Qta 70,69,18176\ns_waitcnt lgkmcnt(4)\nQmvaa 24,56,104\nbuffer_load_dwordx4 a[20:23], v60, s[48:51], s31 offen offset:1024 sc1\nQmvav 24,60,244\nQmvaa 20,56,100\nQmvav 20,60,212\n.Lpg18:\n"
"s_cbranch_execz .Lpg22\nQmvaa 16,56,96\nQta 72,69,24576\nQmvav 16,60,232\nbuffer_load_dwordx4 a[24:27], v60, s[48:51], s31 offen offset:2048 sc1\nQmvaa 12,56,92\nQmvav 12,60,200\nQta 74,69,25088\n"
"Qmvaa 8,56,88\ns_add_u32 s31, s77, 14336\nQmvav 8,60,168\nbuffer_load_dwordx4 a[28:31], v60, s[48:51], s31 offen offset:0 sc1\nQmvaa 4,56,84\nQta 76,69,25600\nv_add_u32 v75, 1, v75\nQmvaa 4,60,112\n"
"s_waitcnt vmcnt(43)\nds_write_b32 v73, v75\nQmvaa 0,56,80\nQmvaa 0,60,108\nQta 78,69,26112\ns_waitcnt lgkmcnt(5)\nQmvaa 28,64,80\nbuffer_load_dwordx4 a[32:35], v60, s[48:51], s31 offen offset:1024 sc1\n"
"Qmvaa 28,68,108\nQmvaa 32,64,84\nQmvaa 32,68,112\nQmvaa 36,64,88\nQta 56,69,24832\nQmvav 36,68,168\nds_read_b32 v66, v74\nQmvaa 40,64,92\n"
"buffer_load_dwordx4 a[36:39], v60, s[48:51], s31 offen offset:2048 sc1\nQmvav 40,68,200\nQta 58,69,25344\nQmvaa 44,64,96\nQmvav 44,68,232\ns_waitcnt lgkmcnt(1)\nv_cmp_gt_u32 vcc, v75, v66\nQmvaa 48,64,100\n"
"s_cbranch_vccnz .Lso1440\n.Lro1440:\nQmvav 48,68,212\nQta 60,69,25856\nv_add_u32 v75, 1, v75\nQmvaa 52,64,104\ns_waitcnt vmcnt(39)\nds_write_b32 v73, v75\n"
"Qmvav 52,68,244\nbuffer_load_dwordx4 a[40:43], v60, s[48:51], s31 offen offset:3072 sc1\ns_waitcnt lgkmcnt(5)\nQmaaa 196,72,104\nQta 62,69,26368\nQmaav 196,76,244\nQmaaa 192,72,100\ns_add_u32 s31, s77, 18432\n"
"Qmaav 192,76,212\nbuffer_load_dwordx4 a[44:47], v60, s[48:51], s31 offen offset:0 sc1\nQmaaa 188,72,96\nQta 64,69,32768\nQmaav 188,76,232\nQmaaa 184,72,92\nds_read_b32 v66, v74\nQmaav 184,76,200\n"
"Qta 66,69,33792\nQmaaa 180,72,88\nbuffer_load_dwordx4 a[48:51], v60, s[48:51], s31 offen offset:1024 sc1\nQmaav 180,76,168\nbuffer_load_dword v78, v76, s[24:27], 0 offen sc1\nQmaaa 176,72,84\ns_waitcnt lgkmcnt(1)\nv_cmp_gt_u32 vcc, v75, v66\n"
"Qmaaa 176,76,112\ns_cbranch_vccnz .Lso1455\n.Lro1455:\nQmaaa 172,72,80\nQta 68,69,40960\nQmaaa 172,76,108\nbuffer_load_dwordx4 a[52:55], v60, s[48:51], s31 offen offset:2048 sc1\ns_waitcnt lgkmcnt(4)\n"
"Qmvaa 92,56,80\nQmvaa 92,60,108\nQta 70,69,41984\nQmvaa 124,56,84\ns_add_u32 m0, s96, 57344\ns_add_u32 s95, s73, 45056\nQmvaa 124,60,112\nbuffer_load_dwordx4 v63, s[52:55], s95 offen lds\n"
"Qmvaa 156,56,88\nQta 72,69,33024\nQmvav 156,60,168\nQmvaa 188,56,92\nQmvav 188,60,200\nQta 74,69,34048\ns_add_u32 s95, s73, 64512\nQmvaa 220,56,96\n"
"buffer_load_dwordx4 v63, s[52:55], s95 offen offset:1024 lds\nQmvav 220,60,232\nQmvaa 252,56,100\nQta 76,69,41216\nQmvav 252,60,212\nQmvaa 100,56,104\nQmvav 100,60,244\nQta 78,69,42240\n"
"s_waitcnt lgkmcnt(4)\nQmaaa 224,64,104\nQmaav 224,68,244\nQmaaa 220,64,100\nQmaav 220,68,212\nQmaaa 216,64,96\nQta 56,69,33280\nQmaav 216,68,232\n"
"Qmaaa 212,64,92\nQmaav 212,68,200\nQta 58,69,34304\nQmaaa 208,64,88\nQmaav 208,68,168\nQmaaa 204,64,84\nQta 60,69,41472\nQmaaa 204,68,112\n"
"Qmaaa 200,64,80\nQmaaa 200,68,108\nQta 62,69,42496\ns_waitcnt lgkmcnt(4)\nQmaaa 228,72,80\nQmaaa 228,76,108\nQmaaa 232,72,84\nQmaaa 232,76,112\n"
"Qmaaa 236,72,88\nQta 64,69,33536\nQmaav 236,76,168\nQmaaa 240,72,92\nQmaav 240,76,200\nQta 66,69,34560\nQmaaa 244,72,96\nQmaav 244,76,232\n"
"Qmaaa 248,72,100\nQta 68,69,41728\nQmaav 248,76,212\nQmaaa 252,72,104\nQmaav 252,76,244\nQta 70,69,42752\ns_waitcnt lgkmcnt(4)\nQmvaa 172,56,104\n"
"Qmvav 172,60,244\nQmvaa 140,56,100\nQmvav 140,60,212\nQmvaa 108,56,96\nQta 72,69,49152\nQmvav 108,60,232\nQmvaa 228,56,92\nQmvav 228,60,200\n"
"Qta 74,69,50176\nQmvaa 196,56,88\nQmvav 196,60,168\nQmvaa 164,56,84\nQta 76,69,57344\nQmvaa 164,60,112\ns_waitcnt vmcnt(3)\nv_sub_u32 v66, v78, v65\n"
"v_cmp_gt_u32 vcc, 3, v66\ns_and_b64 vcc, vcc, s[28:29]\ns_cbranch_scc1 .Lf3_w\n.Lf3_r:\nv_add_u32 v75, 1, v75\nQmvaa 132,56,80\ns_waitcnt vmcnt(42)\nds_write_b32 v73, v75\n"
"Qmvaa 132,60,108\nQta 78,69,58368\ns_waitcnt lgkmcnt(5)\nQmvaa 204,64,80\ns_add_u32 s31, s74, 28672\nQmvaa 204,68,108\nbuffer_load_dwordx4 a[80:83], v60, s[48:51], s31 offen offset:0 sc1\nQmvaa 236,64,84\n"
"Qmvaa 236,68,112\ns_bitcmp1_b32 %19, 2\ns_cbranch_scc1 .Lrole1\nQmvaa 96,64,88\nQta 56,69,49408\ns_lshl_b32 s46, %19, 4\nQmvav 96,68,168\nbuffer_load_dwordx4 a[84:87], v60, s[48:51], s31 offen offset:1024 sc1\n"
"Qmvaa 128,64,92\nds_read_b32 v66, v74\ns_add_u32 s46, s46, 128\nQmvav 128,68,200\nQta 58,69,50432\nv_mov_b32 v77, 0x7ffff000\nQmvaa 160,64,96\nQmvav 160,68,232\n"
"buffer_load_dwordx4 a[88:91], v60, s[48:51], s31 offen offset:2048 sc1\nQmvaa 192,64,100\ns_waitcnt lgkmcnt(1)\nv_cmp_gt_u32 vcc, v75, v66\nQmvav 192,68,212\ns_cbranch_vccnz .Lso1525r0\n.Lro1525r0:\nQmvaa 224,64,104\n"
"Qta 60,69,57600\nQmvav 224,68,244\nbuffer_load_dwordx4 a[92:95], v60, s[48:51], s31 offen offset:3072 sc1\ns_waitcnt vmcnt(31)\ns_waitcnt lgkmcnt(4)\nQmaav 140,72,84\nQmaav 140,76,116\nQta 62,69,58624\n"
"Qmaav 136,72,240\ns_add_u32 m0, s96, 65536\ns_add_u32 s95, s67, 86016\nQmaav 136,76,248\nbuffer_load_dwordx4 v63, s[52:55], s95 offen lds\nQmaav 132,72,208\nQta 64,69,49664\nQmaav 132,76,216\n"
"Qmaav 128,72,176\nQmaav 128,76,184\nQta 66,69,50688\ns_add_u32 s95, s67, 105472\nQmaav 124,72,144\nbuffer_load_dwordx4 v63, s[52:55], s95 offen offset:1024 lds\nQmaav 124,76,152\nQmaav 120,72,112\n"
"Qta 68,69,57856\nQmaav 120,76,120\ns_add_u32 m0, s96, 73728\ns_add_u32 s95, s67, 126976\nQmaav 116,72,80\nbuffer_load_dwordx4 v63, s[52:55], s95 offen lds\nQmaav 116,76,88\nQta 70,69,58880\n"
"s_waitcnt lgkmcnt(4)\nQmaav 144,56,80\nQmaav 144,60,88\ns_add_u32 s95, s67, 146432\nQmaav 148,56,112\nbuffer_load_dwordx4 v63, s[52:55], s95 offen offset:1024 lds\nQmaav 148,60,120\nQmaav 152,56,144\n"
"Qta 72,69,49920\nQmaav 152,60,152\ns_add_u32 m0, s96, 81920\ns_add_u32 s95, s72, 86016\nQmaav 156,56,176\nbuffer_load_dwordx4 v63, s[52:55], s95 offen lds\nQmaav 156,60,184\nQta 74,69,50944\n"
"Qmaav 160,56,208\nQmaav 160,60,216\nQmaav 164,56,240\nQta 76,69,58112\ns_add_u32 s95, s72, 105472\nQmaav 164,60,248\nbuffer_load_dwordx4 v63, s[52:55], s95 offen offset:1024 lds\nQmaav 168,56,84\n"
"Qmaav 168,60,116\nQta 78,69,59136\ns_waitcnt lgkmcnt(4)\nQmvav 24,64,84\ns_add_u32 m0, s96, 90112\ns_add_u32 s95, s72, 126976\nQmvav 24,68,116\nbuffer_load_dwordx4 v63, s[52:55], s95 offen lds\n"
"Qmvav 20,64,240\nQmvav 20,68,248\nQmvav 16,64,208\nQta 56,68,0\ns_add_u32 s95, s72, 146432\nQmvav 16,68,216\nbuffer_load_dwordx4 v63, s[52:55], s95 offen offset:1024 lds\nQmvav 12,64,176\n"
"Qmvav 12,68,184\nQta 58,68,512\nQmvav 8,64,144\ns_add_u32 s31, s74, 32768\nQmvav 8,68,152\nbuffer_load_dwordx4 a[96:99], v60, s[48:51], s31 offen offset:0 sc1\nQmvav 4,64,112\nQta 60,68,1024\n"
"Qmvav 4,68,120\nQmvav 0,64,80\nQmvav 0,68,88\nQta 62,68,1536\ns_waitcnt lgkmcnt(4)\nQmvav 28,72,80\nbuffer_load_dwordx4 a[100:103], v60, s[48:51], s31 offen offset:1024 sc1\nQmvav 28,76,88\n"
"Qmvav 32,72,112\nQmvav 32,76,120\nQmvav 36,72,144\nQta 64,68,256\nQmvav 36,76,152\nbuffer_load_dwordx4 a[104:107], v60, s[48:51], s31 offen offset:2048 sc1\nQmvav 40,72,176\nQmvav 40,76,184\n"
"Qta 66,68,768\nQmvav 44,72,208\ns_add_u32 s31, s74, 43008\nv_cmp_eq_u32 s[44:45], s46, v60\ns_nop 1\nv_cndmask_b32 v77, v77, v76, s[44:45]\nv_add_u32 v67, 1, v65\ns_xor_b32 s46, %19, 12\n"
"s_lshl_b32 s28, 1, s46\nQmvav 44,76,216\nbuffer_load_dwordx4 a[108:111], v60, s[48:51], s31 offen offset:0 sc1\nQmvav 48,72,240\nQta 68,68,1280\nQmvav 48,76,248\nv_add_u32 v75, 1, v75\nQmvav 52,72,84\n"
"s_waitcnt vmcnt(37)\nds_write_b32 v73, v75\nQmvav 52,76,116\nQta 70,68,1792\ns_waitcnt lgkmcnt(5)\nQmaav 196,56,84\nbuffer_load_dwordx4 a[112:115], v60, s[48:51], s31 offen offset:1024 sc1\nQmaav 196,60,116\n"
"Qmaav 192,56,240\nQmaav 192,60,248\nQmaav 188,56,208\nQta 72,68,8192\nQmaav 188,60,216\nbuffer_load_dwordx4 v[168:171], v60, s[48:51], s31 offen offset:2048 sc1\nQmaav 184,56,176\nds_read_b32 v66, v74\n"
"Qmaav 184,60,184\nQta 74,68,9216\nQmaav 180,56,144\nQmaav 180,60,152\nbuffer_load_dwordx4 v[200:203], v60, s[48:51], s31 offen offset:3072 sc1\nQmaav 176,56,112\ns_waitcnt lgkmcnt(1)\nv_cmp_gt_u32 vcc, v75, v66\n"
"Qmaav 176,60,120\ns_cbranch_vccnz .Lso1595r0\n.Lro1595r0:\nQmaav 172,56,80\nQta 76,68,16384\ns_add_u32 s31, s74, 47104\nQmaav 172,60,88\nbuffer_load_dwordx4 v[232:235], v60, s[48:51], s31 offen offset:0 sc1\n"
"s_waitcnt lgkmcnt(4)\nQmvav 92,64,80\nQmvav 92,68,88\nQta 78,68,17408\nQmvav 124,64,112\nQmvav 124,68,120\nbuffer_load_dwordx4 v[212:215], v60, s[48:51], s31 offen offset:1024 sc1\nQmvav 156,64,144\n"
"Qta 56,68,8448\nQmvav 156,68,152\nQmvav 188,64,176\nQmvav 188,68,184\nQta 58,68,9472\nQmvav 220,64,208\nbuffer_load_dwordx4 v[244:247], v60, s[48:51], s31 offen offset:2048 sc1\nQmvav 220,68,216\n"
"Qmvav 252,64,240\nQta 60,68,16640\nQmvav 252,68,248\ns_add_u32 m0, s91, 0\ns_add_u32 s31, s77, 28672\nQmvav 100,64,84\nbuffer_load_dwordx4 v60, s[48:51], s31 offen sc1 lds\nQmvav 100,68,116\n"
"Qta 62,68,17664\ns_waitcnt lgkmcnt(4)\nQmaav 224,72,84\nQmaav 224,76,116\nQmaav 220,72,240\nbuffer_load_dwordx4 v60, s[48:51], s31 offen offset:1024 sc1 lds\nQmaav 220,76,248\nQmaav 216,72,208\n"
"Qta 64,68,8704\nQmaav 216,76,216\nQmaav 212,72,176\nbuffer_load_dwordx4 v60, s[48:51], s31 offen offset:2048 sc1 lds\nQmaav 212,76,184\nQta 66,68,9728\nQmaav 208,72,144\nQmaav 208,76,152\n"
"Qmaav 204,72,112\nQta 68,68,16896\nQmaav 204,76,120\nbuffer_load_dwordx4 v60, s[48:51], s31 offen offset:3072 sc1 lds\nQmaav 200,72,80\nQmaav 200,76,88\nQta 70,68,17920\ns_waitcnt lgkmcnt(4)\n"
"Qmaav 228,56,80\ns_add_u32 m0, s91, 4096\ns_add_u32 s31, s77, 32768\nQmaav 228,60,88\nbuffer_load_dwordx4 v60, s[48:51], s31 offen sc1 lds\nQmaav 232,56,112\nQmaav 232,60,120\nQmaav 236,56,144\n"
"Qta 72,68,8960\nQmaav 236,60,152\nbuffer_load_dwordx4 v60, s[48:51], s31 offen offset:1024 sc1 lds\nQmaav 240,56,176\nQmaav 240,60,184\nQta 74,68,9984\nQmaav 244,56,208\nQmaav 244,60,216\n"
"buffer_load_dwordx4 v60, s[48:51], s31 offen offset:2048 sc1 lds\nQmaav 248,56,240\nQta 76,68,17152\nQmaav 248,60,248\nQmaav 252,56,84\nQmaav 252,60,116\nQta 78,68,18176\ns_add_u32 m0, s91, 33792\n"
"s_add_u32 s31, s77, 43008\ns_waitcnt lgkmcnt(4)\nQmvav 172,64,84\nbuffer_load_dwordx4 v60, s[48:51], s31 offen sc1 lds\nQmvav 172,68,116\nQmvav 140,64,240\nQmvav 140,68,248\nQmvav 108,64,208\n"
"Qta 56,68,24576\nQmvav 108,68,216\nbuffer_load_dwordx4 v60, s[48:51], s31 offen offset:1024 sc1 lds\nQmvav 228,64,176\nQmvav 228,68,184\nQta 58,68,25600\nQmvav 196,64,144\nQmvav 196,68,152\n"
"buffer_load_dwordx4 v60, s[48:51], s31 offen offset:2048 sc1 lds\nQmvav 164,64,112\nQta 60,68,32768\nQmvav 164,68,120\nv_add_u32 v75, 1, v75\nQmvav 132,64,80\ns_waitcnt vmcnt(49)\nds_write_b32 v73, v75\n"
"Qmvav 132,68,88\nQta 62,68,33792\ns_waitcnt lgkmcnt(5)\nQmvav 204,72,80\nbuffer_load_dwordx4 v60, s[48:51], s31 offen offset:3072 sc1 lds\nQmvav 204,76,88\nQmvav 236,72,112\nQmvav 236,76,120\n"
"Qmvav 96,72,144\nQta 64,68,24832\ns_add_u32 m0, s91, 37888\ns_add_u32 s31, s77, 47104\nQmvav 96,76,152\nbuffer_load_dwordx4 v60, s[48:51], s31 offen sc1 lds\nQmvav 128,72,176\nds_read_b32 v66, v74\n"
"Qmvav 128,76,184\nQta 66,68,25856\nQmvav 160,72,208\nQmvav 160,76,216\nbuffer_load_dwordx4 v60, s[48:51], s31 offen offset:1024 sc1 lds\nQmvav 192,72,240\ns_waitcnt lgkmcnt(1)\nv_cmp_gt_u32 vcc, v75, v66\n"
"Qmvav 192,76,248\ns_cbranch_vccnz .Lso1665r0\n.Lro1665r0:\nQmvav 224,72,84\nQta 68,68,33024\nQmvav 224,76,116\nbuffer_load_dwordx4 v60, s[48:51], s31 offen offset:2048 sc1 lds\ns_waitcnt vmcnt(38)\n"
"s_waitcnt lgkmcnt(4)\nQmaaa 140,56,24\nQmaaa 140,60,52\nQta 70,68,34048\nQmaaa 136,56,20\ns_add_u32 m0, s96, 0\ns_add_u32 s95, s73, 86016\nQmaaa 136,60,48\n"
"buffer_load_dwordx4 v63, s[52:55], s95 offen lds\nQmaaa 132,56,16\nQta 72,68,25088\nQmaaa 132,60,44\nQmaaa 128,56,12\nQmaaa 128,60,40\nQta 74,68,26112\ns_add_u32 s95, s73, 105472\n"
"Qmaaa 124,56,8\nbuffer_load_dwordx4 v63, s[52:55], s95 offen offset:1024 lds\nQmaaa 124,60,36\nQmaaa 120,56,4\nQta 76,68,33280\nQmaaa 120,60,32\ns_add_u32 m0, s96, 8192\ns_add_u32 s95, s73, 126976\n"
"Qmaaa 116,56,0\nbuffer_load_dwordx4 v63, s[52:55], s95 offen lds\nQmaaa 116,60,28\nQta 78,68,34304\ns_waitcnt lgkmcnt(4)\nQmaaa 144,64,0\nQmaaa 144,68,28\ns_add_u32 s95, s73, 146432\n"
"Qmaaa 148,64,4\nbuffer_load_dwordx4 v63, s[52:55], s95 offen offset:1024 lds\nQmaaa 148,68,32\nQmaaa 152,64,8\nQta 56,68,25344\nQmaaa 152,68,36\ns_add_u32 s31, s75, 28672\nQmaaa 156,64,12\n"
"buffer_load_dwordx4 v[80:83], v60, s[48:51], s31 offen offset:0 sc1\nQmaaa 156,68,40\nQta 58,68,26368\nQmaaa 160,64,16\nQmaaa 160,68,44\nQmaaa 164,64,20\nQta 60,68,33536\nQmaaa 164,68,48\n"
"buffer_load_dwordx4 v[112:115], v60, s[48:51], s31 offen offset:1024 sc1\nQmaaa 168,64,24\nQmaaa 168,68,52\nQta 62,68,34560\ns_waitcnt lgkmcnt(4)\nQmvaa 24,72,24\nQmvaa 24,76,52\nbuffer_load_dwordx4 v[144:147], v60, s[48:51], s31 offen offset:2048 sc1\n"
"Qmvaa 20,72,20\nQmvaa 20,76,48\nQmvaa 16,72,16\nQta 64,68,40960\nQmvaa 16,76,44\nbuffer_load_dwordx4 v[176:179], v60, s[48:51], s31 offen offset:3072 sc1\nQmvaa 12,72,12\nQmvaa 12,76,40\n"
"Qta 66,68,41472\nQmvaa 8,72,8\ns_add_u32 s31, s75, 32768\nQmvaa 8,76,36\nbuffer_load_dwordx4 v[208:211], v60, s[48:51], s31 offen offset:0 sc1\nQmvaa 4,72,4\nQta 68,68,41984\nQmvaa 4,76,32\n"
"Qmvaa 0,72,0\nQmvaa 0,76,28\nQta 70,68,42496\ns_waitcnt lgkmcnt(4)\nQmvaa 28,56,0\nbuffer_load_dwordx4 v[240:243], v60, s[48:51], s31 offen offset:1024 sc1\n.Lpg19:\ns_cbranch_execz .Lpg23\n"
"Qmvaa 28,60,28\nQmvaa 32,56,4\nQmvaa 32,60,32\nQmvaa 36,56,8\nQta 72,68,41216\nQmvaa 36,60,36\nbuffer_load_dwordx4 v[84:87], v60, s[48:51], s31 offen offset:2048 sc1\nQmvaa 40,56,12\n"
"Qmvaa 40,60,40\nQta 74,68,41728\nQmvaa 44,56,16\ns_add_u32 s31, s75, 43008\nQmvaa 44,60,44\nbuffer_load_dwordx4 v[88:91], v60, s[48:51], s31 offen offset:0 sc1\nQmvaa 48,56,20\nQta 76,68,42240\n"
"Qmvaa 48,60,48\nv_add_u32 v75, 1, v75\nQmvaa 52,56,24\ns_waitcnt vmcnt(38)\nds_write_b32 v73, v75\nQmvaa 52,60,52\nQta 78,68,42752\ns_waitcnt lgkmcnt(5)\n"
"Qmaaa 196,64,24\nbuffer_load_dwordx4 v[120:123], v60, s[48:51], s31 offen offset:1024 sc1\nQmaaa 196,68,52\nQmaaa 192,64,20\nQmaaa 192,68,48\nQmaaa 188,64,16\nQta 56,68,49152\nQmaaa 188,68,44\n"
"buffer_load_dwordx4 v[152:155], v60, s[48:51], s31 offen offset:2048 sc1\nQmaaa 184,64,12\nds_read_b32 v66, v74\nQmaaa 184,68,40\nQta 58,68,50176\nQmaaa 180,64,8\nQmaaa 180,68,36\nbuffer_load_dwordx4 v[184:187], v60, s[48:51], s31 offen offset:3072 sc1\n"
"Qmaaa 176,64,4\ns_waitcnt lgkmcnt(1)\nv_cmp_gt_u32 vcc, v75, v66\nQmaaa 176,68,32\ns_cbranch_vccnz .Lso1735r0\n.Lro1735r0:\nQmaaa 172,64,0\nQta 60,68,57344\n"
"s_add_u32 s31, s75, 47104\nQmaaa 172,68,28\nbuffer_load_dwordx4 v[216:219], v60, s[48:51], s31 offen offset:0 sc1\ns_waitcnt lgkmcnt(4)\nQmvaa 92,72,0\nQmvaa 92,76,28\nQta 62,68,58368\nQmvaa 124,72,4\n"
"Qmvaa 124,76,32\nbuffer_load_dwordx4 v[248:251], v60, s[48:51], s31 offen offset:1024 sc1\nQmvaa 156,72,8\nQta 64,68,49408\nQmvaa 156,76,36\nQmvaa 188,72,12\nQmvaa 188,76,40\nQta 66,68,50432\n"
"Qmvaa 220,72,16\nbuffer_load_dwordx4 v[116:119], v60, s[48:51], s31 offen offset:2048 sc1\nQmvaa 220,76,44\nQmvaa 252,72,20\nQta 68,68,57600\nQmvaa 252,76,48\ns_add_u32 m0, s96, 16384\ns_add_u32 s95, s67, 86016\n"
"Qmvaa 100,72,24\nbuffer_load_dwordx4 v61, s[52:55], s95 offen lds\nQmvaa 100,76,52\nQta 70,68,58624\ns_waitcnt lgkmcnt(4)\nQmaaa 224,56,24\nQmaaa 224,60,52\ns_add_u32 s95, s67, 105472\n"
"Qmaaa 220,56,20\nbuffer_load_dwordx4 v61, s[52:55], s95 offen offset:1024 lds\nQmaaa 220,60,48\nQmaaa 216,56,16\nQta 72,68,49664\nQmaaa 216,60,44\ns_add_u32 m0, s96, 24576\ns_add_u32 s95, s67, 126976\n"
"Qmaaa 212,56,12\nbuffer_load_dwordx4 v61, s[52:55], s95 offen lds\nQmaaa 212,60,40\nQta 74,68,50688\nQmaaa 208,56,8\nQmaaa 208,60,36\nQmaaa 204,56,4\nQta 76,68,57856\n"
"s_add_u32 s95, s67, 146432\nQmaaa 204,60,32\nbuffer_load_dwordx4 v61, s[52:55], s95 offen offset:1024 lds\nQmaaa 200,56,0\nQmaaa 200,60,28\nQta 78,68,58880\ns_waitcnt lgkmcnt(4)\nQmaaa 228,64,0\n"
"s_add_u32 m0, s96, 32768\ns_add_u32 s95, s72, 86016\nQmaaa 228,68,28\nbuffer_load_dwordx4 v61, s[52:55], s95 offen lds\nQmaaa 232,64,4\nQmaaa 232,68,32\nQmaaa 236,64,8\nQta 56,68,49920\n"
"s_add_u32 s95, s72, 105472\nQmaaa 236,68,36\nbuffer_load_dwordx4 v61, s[52:55], s95 offen offset:1024 lds\nQmaaa 240,64,12\nQmaaa 240,68,40\nQta 58,68,50944\nQmaaa 244,64,16\nQmaaa 244,68,44\n"
"Qmaaa 248,64,20\nQta 60,68,58112\nQmaaa 248,68,48\nQmaaa 252,64,24\nQmaaa 252,68,52\nQta 62,68,59136\ns_waitcnt lgkmcnt(4)\nQmvaa 172,72,24\n"
"Qmvaa 172,76,52\nQmvaa 140,72,20\nQmvaa 140,76,48\nQmvaa 108,72,16\nQta 64,69,0\nQmvaa 108,76,44\nQmvaa 228,72,12\nQmvaa 228,76,40\n"
"Qta 66,69,1024\nQmvaa 196,72,8\nQmvaa 196,76,36\nQmvaa 164,72,4\nQta 68,69,8192\nQmvaa 164,76,32\nv_add_u32 v75, 1, v75\nQmvaa 132,72,0\n"
"s_waitcnt vmcnt(20)\nds_write_b32 v73, v75\nQmvaa 132,76,28\nQta 70,69,9216\ns_waitcnt lgkmcnt(5)\nQmvaa 204,56,0\ns_waitcnt vmcnt(24)\nQda 0,58,0\n"
"Qmvaa 204,60,28\nQmvaa 236,56,4\nQda 28,58,33792\nQmvaa 236,60,32\nQmvaa 96,56,8\nQta 72,69,16384\nQmvaa 96,60,36\nQda 4,58,1024\n"
"Qmvaa 128,56,12\nds_read_b32 v66, v74\nQmvaa 128,60,40\nQta 74,69,17408\nQmvaa 160,56,16\nQda 32,58,34816\nQmvaa 160,60,44\nQmvaa 192,56,20\n"
"s_waitcnt lgkmcnt(2)\nv_cmp_gt_u32 vcc, v75, v66\nQmvaa 192,60,48\ns_cbranch_vccnz .Lso1805r0\n.Lro1805r0:\nQmvaa 224,56,24\nQta 76,69,24576\ns_add_u32 m0, s96, 40960\n"
"s_add_u32 s95, s72, 126976\nQmvaa 224,60,52\nbuffer_load_dwordx4 v61, s[52:55], s95 offen lds\ns_waitcnt vmcnt(39)\ns_waitcnt lgkmcnt(8)\nQmaaa 200,64,80\nQda 8,58,2048\nQmaaa 200,68,108\n"
"Qta 78,69,25600\nQmaaa 204,64,84\nQda 36,58,35840\ns_add_u32 s95, s72, 146432\nQmaaa 204,68,112\nbuffer_load_dwordx4 v61, s[52:55], s95 offen offset:1024 lds\nQmaaa 208,64,88\nQta 56,68,0\n"
"Qmaav 208,68,168\nQda 12,58,3072\nQmaaa 212,64,92\nQmaav 212,68,200\nQta 58,68,1024\ns_add_u32 m0, s96, 49152\ns_add_u32 s95, s73, 86016\nQmaaa 216,64,96\n"
"buffer_load_dwordx4 v61, s[52:55], s95 offen lds\nQmaav 216,68,232\nQda 40,58,36864\nQmaaa 220,64,100\nQta 60,68,8192\nQmaav 220,68,212\nQda 16,58,4096\ns_add_u32 s95, s73, 105472\n"
"Qmaaa 224,64,104\nbuffer_load_dwordx4 v61, s[52:55], s95 offen offset:1024 lds\nQmaav 224,68,244\nQta 62,68,9216\ns_waitcnt vmcnt(10)\ns_waitcnt lgkmcnt(8)\nQmaav 200,72,80\nQda 44,58,37888\n"
"Qmaav 200,76,88\ns_add_u32 m0, s96, 57344\ns_add_u32 s95, s73, 126976\nQmaav 204,72,112\nbuffer_load_dwordx4 v61, s[52:55], s95 offen lds\nQmaav 204,76,120\nQda 20,58,5120\nQmaav 208,72,144\n"
"Qta 64,69,256\nQmaav 208,76,152\nQda 48,58,38912\ns_add_u32 s95, s73, 146432\nQmaav 212,72,176\nbuffer_load_dwordx4 v61, s[52:55], s95 offen offset:1024 lds\nQmaav 212,76,184\nQta 66,69,1280\n"
"Qmaav 216,72,208\nQda 24,58,6144\nQmaav 216,76,216\nQmaav 220,72,240\nQta 68,69,8448\nQmaav 220,76,248\nQda 52,58,39936\nQmaav 224,72,84\n"
"Qmaav 224,76,116\nQta 70,69,9472\ns_waitcnt lgkmcnt(9)\nQmaaa 200,56,0\nQmaaa 200,60,28\nQmaaa 204,56,4\nQmaaa 204,60,32\ns_waitcnt lgkmcnt(9)\n"
"Qmaaa 208,56,8\nQta 72,69,16640\nQmaaa 208,60,36\ns_nop 1\nds_write_b128 v58, a[200:203] offset:0\nQmaaa 212,56,12\nQmaaa 212,60,40\nQta 74,69,17664\n"
"s_waitcnt lgkmcnt(4)\nQmaaa 216,56,16\nds_write_b128 v58, a[204:207] offset:1024\nQmaaa 216,60,44\nQmaaa 220,56,20\nQta 76,69,24832\nQmaaa 220,60,48\nds_write_b128 v58, a[208:211] offset:2048\n"
"Qmaaa 224,56,24\nQmaaa 224,60,52\nQta 78,69,25856\ns_waitcnt lgkmcnt(7)\nQmaaa 228,64,80\nds_write_b128 v58, a[212:215] offset:3072\nQmaaa 228,68,108\nQmaaa 232,64,84\n"
"Qmaaa 232,68,112\nds_write_b128 v58, a[216:219] offset:4096\nQmaaa 236,64,88\nQta 56,68,256\nQmaav 236,68,168\nQmaaa 240,64,92\nds_write_b128 v58, a[220:223] offset:5120\nQmaav 240,68,200\n"
"Qta 58,68,1280\nQmaaa 244,64,96\nQmaav 244,68,232\nds_write_b128 v58, a[224:227] offset:6144\nQmaaa 248,64,100\nQta 60,68,8448\nQmaav 248,68,212\nQmaaa 252,64,104\n"
"Qmaav 252,68,244\nQta 62,68,9472\ns_waitcnt lgkmcnt(8)\nQmaav 228,72,80\nv_add_u32 v75, 1, v75\nQmaav 228,76,88\nds_write_b32 v73, v75\nQmaav 232,72,112\n"
"Qmaav 232,76,120\nQmaav 236,72,144\nQta 64,69,512\nQmaav 236,76,152\nQmaav 240,72,176\nQmaav 240,76,184\nQta 66,69,1536\nQmaav 244,72,208\n"
"Qmaav 244,76,216\nds_read_b32 v66, v74\nQmaav 248,72,240\nQta 68,69,8704\nQmaav 248,76,248\nQmaav 252,72,84\nQmaav 252,76,116\ns_waitcnt lgkmcnt(1)\n"
"v_cmp_gt_u32 vcc, v75, v66\ns_waitcnt lgkmcnt(5)\nQmaaa 228,56,0\ns_cbranch_vccnz .Lso1878r0\n.Lro1878r0:\nQmaaa 228,60,28\nQta 70,69,9728\nQmaaa 232,56,4\n"
"Qmaaa 232,60,32\nQdv 148,59,2048\nQmaaa 236,56,8\nQta 72,69,16896\nQmaaa 236,60,36\ns_nop 0\nds_write_b128 v58, a[228:231] offset:33792\nQmaaa 240,56,12\n"
"Qdv 180,59,3072\nQmaaa 240,60,40\nQta 74,69,17920\nQmaaa 244,56,16\nds_write_b128 v58, a[232:235] offset:34816\nQmaaa 244,60,44\nQdv 104,59,5120\nQmaaa 248,56,20\n"
"Qta 76,69,25088\nQmaaa 248,60,48\nds_write_b128 v58, a[236:239] offset:35840\nQmaaa 252,56,24\nQdv 136,59,0\ns_waitcnt lgkmcnt(9)\nQa 196,196,148\nQmaaa 252,60,52\n"
"Qta 78,69,26112\nQa 197,197,149\ns_waitcnt lgkmcnt(10)\nQmvaa 132,64,80\nds_write_b128 v58, a[240:243] offset:36864\nQmvaa 132,68,108\nQa 198,198,150\nQa 199,199,151\n"
"Qmvaa 164,64,84\ns_waitcnt lgkmcnt(8)\nQa 228,228,180\nQa 229,229,181\nQmvaa 164,68,112\nds_write_b128 v58, a[244:247] offset:37888\nQmvaa 196,64,88\nQta 56,68,512\n"
"Qa 230,230,182\nQa 231,231,183\nQmvav 196,68,168\nQdv 148,59,4096\ns_waitcnt lgkmcnt(8)\nQa 140,140,104\nQmvaa 228,64,92\nds_write_b128 v58, a[248:251] offset:38912\n"
"Qmvav 228,68,200\nQta 58,68,1536\nQa 141,141,105\nQmvaa 108,64,96\nQa 142,142,106\nQa 143,143,107\nQmvav 108,68,232\nQdv 180,59,6144\n"
"s_waitcnt lgkmcnt(8)\nQa 132,132,136\nQmvaa 140,64,100\nQta 60,68,8704\nQa 133,133,137\nQmvav 140,68,212\nds_write_b128 v58, a[252:255] offset:39936\nQmvaa 172,64,104\n"
"Qa 134,134,138\nQa 135,135,139\nQmvav 172,68,244\nQta 62,68,9728\ns_waitcnt lgkmcnt(6)\nQa 108,108,148\ns_waitcnt lgkmcnt(6)\nQmvav 132,72,80\n"
"Qdv 104,59,1024\nv_add_u32 v75, 1, v75\nQmvav 132,76,88\ns_waitcnt vmcnt(10)\nds_write_b32 v73, v75\nQmvav 164,72,112\nQa 109,109,149\nQa 110,110,150\n"
"Qmvav 164,76,120\nQa 111,111,151\ns_waitcnt lgkmcnt(5)\nQa 172,172,180\nQmvav 196,72,144\nQta 64,69,768\nQa 173,173,181\nQmvav 196,76,152\n"
"Qa 174,174,182\nQa 175,175,183\nQmvav 228,72,176\nQmvav 228,76,184\nQta 66,69,1792\ns_waitcnt lgkmcnt(3)\nQa 164,164,104\nQmvav 108,72,208\n"
"Qa 165,165,105\nQa 166,166,106\nQmvav 108,76,216\nds_read_b32 v66, v74\nQa 167,167,107\nQmvav 140,72,240\nQta 68,69,8960\nQmvav 140,76,248\n"
"Qmvav 172,72,84\nQmvav 172,76,116\ns_waitcnt lgkmcnt(1)\nv_cmp_gt_u32 vcc, v75, v66\ns_waitcnt lgkmcnt(6)\nQmvaa 132,56,0\ns_cbranch_vccnz .Lso1920r0\n.Lro1920r0:\n"
"s_add_u32 s68, s64, 0\nQmvaa 132,60,28\nQta 70,69,9984\nQmvaa 164,56,4\nQmvaa 164,60,32\nQdv 136,59,35840\nQmvaa 196,56,8\nQta 72,69,17152\n"
"s_nop 1\nQk 132,132,133\nQmvaa 196,60,36\nQk 133,134,135\nQmvaa 228,56,12\nQdv 148,59,36864\nQk 134,164,165\nQmvaa 228,60,40\n"
"Qta 74,69,18176\nQk 135,166,167\nQmvaa 108,56,16\nbuffer_store_dwordx4 v[132:135], v60, s[24:27], s68 offen offset:0 sc1\nQk 196,196,197\nQmvaa 108,60,44\nQdv 180,59,38912\nQk 197,198,199\n"
"Qmvaa 140,56,20\nQta 76,69,25344\nQk 198,228,229\nQmvaa 140,60,48\ns_waitcnt lgkmcnt(5)\nQa 96,96,136\nQa 97,97,137\nQmvaa 172,56,24\n"
"Qdv 104,59,33792\nQa 98,98,138\nQmvaa 172,60,52\nQta 78,69,26368\nQa 99,99,139\ns_waitcnt lgkmcnt(11)\nQmvaa 204,64,80\ns_waitcnt lgkmcnt(5)\n"
"Qa 128,128,148\nQa 129,129,149\nQmvaa 204,68,108\nQa 130,130,150\nQa 131,131,151\nQmvaa 236,64,84\nQk 199,230,231\nQk 108,108,109\n"
"Qmvaa 236,68,112\ns_waitcnt lgkmcnt(3)\nQa 192,192,180\nQa 193,193,181\nQmvaa 96,64,88\nQta 56,68,768\nQa 194,194,182\nQmvav 96,68,168\n"
"Qdv 136,59,37888\nQa 195,195,183\nQmvaa 128,64,92\ns_waitcnt lgkmcnt(3)\nQa 204,204,104\nQa 205,205,105\nQmvav 128,68,200\nQta 58,68,1792\n"
"Qa 206,206,106\nQmvaa 160,64,96\nbuffer_store_dwordx4 v[196:199], v60, s[24:27], s68 offen offset:1024 sc1\nQa 207,207,107\nQmvav 160,68,232\nQdv 148,59,39936\nQk 109,110,111\nQmvaa 192,64,100\n"
"Qta 60,68,8960\nQk 110,140,141\nQmvav 192,68,212\nQk 111,142,143\nQk 172,172,173\nQmvaa 224,64,104\nbuffer_store_dwordx4 v[108:111], v60, s[24:27], s68 offen offset:2048 sc1\nv_add_u32 v75, 1, v75\n"
"Qmvav 224,68,244\ns_waitcnt vmcnt(3)\nds_write_b32 v73, v75\ns_waitcnt lgkmcnt(10)\nQmvav 204,72,80\nQta 62,68,9984\ns_waitcnt lgkmcnt(5)\nQa 160,160,136\n"
"Qmvav 204,76,88\nQdv 180,59,34816\nQa 161,161,137\nQmvav 236,72,112\nQa 162,162,138\nQa 163,163,139\nQmvav 236,76,120\ns_waitcnt lgkmcnt(4)\n"
"Qa 224,224,148\nQa 225,225,149\nQmvav 96,72,144\nQta 64,68,16384\nQa 226,226,150\nQmvav 96,76,152\nQa 227,227,151\nQk 173,174,175\n"
"Qmvav 128,72,176\nQmvav 128,76,184\nds_read_b32 v66, v74\ns_waitcnt lgkmcnt(2)\nQa 236,236,180\nQmvav 160,72,208\nQta 66,68,17408\nQa 237,237,181\n"
"Qmvav 160,76,216\nQa 238,238,182\nQa 239,239,183\nQmvav 192,72,240\nQmvav 192,76,248\ns_waitcnt lgkmcnt(1)\nv_cmp_gt_u32 vcc, v75, v66\nQmvav 224,72,84\n"
"s_cbranch_vccnz .Lso1960r0\n.Lro1960r0:\nQmvav 224,76,116\nQta 68,68,24576\ns_add_u32 m0, s96, 65536\ns_add_u32 s95, s67, 86016\ns_waitcnt lgkmcnt(5)\nQmvaa 204,56,0\n"
"buffer_load_dwordx4 v62, s[52:55], s95 offen lds\nQmvaa 204,60,28\nQmvaa 236,56,4\nQta 70,68,25600\nQmvaa 236,60,32\ns_add_u32 s95, s67, 125952\nQmvaa 96,56,8\nbuffer_load_dwordx4 v62, s[52:55], s95 offen offset:1024 lds\n"
"Qmvaa 96,60,36\nQta 72,68,32768\nQk 174,204,205\nQmvaa 128,56,12\nQk 175,206,207\nQk 236,236,237\nQmvaa 128,60,40\nbuffer_store_dwordx4 v[172:175], v60, s[24:27], s68 offen offset:3072 sc1\n"
"Qk 237,238,239\n.Lpg20:\ns_cbranch_execz .Lpg24\nQmvaa 160,56,16\nQta 74,68,33792\ns_add_u32 m0, s96, 73728\ns_add_u32 s95, s72, 86016\nQmvaa 160,60,44\n"
"buffer_load_dwordx4 v62, s[52:55], s95 offen lds\nQmvaa 192,56,20\nQk 238,96,97\nQk 239,98,99\nQmvaa 192,60,48\nQta 76,68,40960\ns_add_u32 s68, s64, 4096\nQmvaa 224,56,24\n"
"buffer_store_dwordx4 v[236:239], v60, s[24:27], s68 offen offset:0 sc1\ns_add_u32 s95, s72, 125952\nQmvaa 224,60,52\nbuffer_load_dwordx4 v62, s[52:55], s95 offen offset:1024 lds\ns_waitcnt lgkmcnt(3)\nQmaaa 116,64,80\nQta 78,68,41984\nQk 128,128,129\n"
"Qmaaa 116,68,108\nQk 129,130,131\nQk 130,160,161\nQmaaa 120,64,84\ns_add_u32 m0, s96, 81920\ns_add_u32 s95, s73, 86016\nQmaaa 120,68,112\nbuffer_load_dwordx4 v62, s[52:55], s95 offen lds\n"
"Qmaaa 124,64,88\nQta 56,68,49152\nQk 131,162,163\nQmaav 124,68,168\nbuffer_store_dwordx4 v[128:131], v60, s[24:27], s68 offen offset:1024 sc1\nQk 192,192,193\nQmaaa 128,64,92\nQk 193,194,195\n"
"Qk 194,224,225\nQmaav 128,68,200\nQta 58,68,50176\ns_add_u32 s95, s73, 125952\nQmaaa 132,64,96\nbuffer_load_dwordx4 v62, s[52:55], s95 offen offset:1024 lds\nQmaav 132,68,232\nQk 195,226,227\n"
"Qmaaa 136,64,100\nQta 60,68,57344\nv_bfe_u32 v61, v60, 6, 2\nQmaav 136,68,212\nv_lshrrev_b32 v62, 8, v60\nv_xor_b32 v61, v61, v62\nQmaaa 140,64,104\nv_bfe_u32 v62, v60, 5, 1\n"
"v_lshl_or_b32 v61, v61, 1, v62\nQmaav 140,68,244\nQta 62,68,58368\nv_and_b32 v62, 16, v60\ns_waitcnt lgkmcnt(4)\nQmaav 116,72,80\nv_mov_b32 v63, 0x3f80\nv_lshlrev_b32 v63, v62, v63\n"
"Qmaav 116,76,88\nv_min_u32 v62, 1, v61\nv_add_u32 v62, -1, v62\nQmaav 120,72,112\nv_and_b32 v62, v62, v63\nv_xor_b32 v61, 1, v61\nQmaav 120,76,120\nv_min_u32 v61, 1, v61\n"
"v_add_u32 v61, -1, v61\nQmaav 124,72,144\nQta 64,68,16640\nv_and_b32 v61, v61, v63\nQmaav 124,76,152\nv_accvgpr_write_b32 a202, v62\nv_accvgpr_write_b32 a203, v61\nQmaav 128,72,176\n"
"v_accvgpr_write_b32 a200, 0\nv_accvgpr_write_b32 a201, 0\nQmaav 128,76,184\nQta 66,68,17664\nv_accvgpr_write_b32 a204, 0\nQmaav 132,72,208\nbuffer_store_dwordx4 v[192:195], v60, s[24:27], s68 offen offset:2048 sc1\nv_accvgpr_write_b32 a205, 0\n"
"Qmaav 132,76,216\nQmaav 136,72,240\nQta 68,68,24832\nQmaav 136,76,248\nQmaav 140,72,84\nQmaav 140,76,116\nQta 70,68,25856\ns_waitcnt lgkmcnt(4)\n"
"Qmaaa 116,56,0\nQmaaa 116,60,28\nQmaaa 120,56,4\nQmaaa 120,60,32\nQmaaa 124,56,8\nQta 72,68,33024\nQmaaa 124,60,36\ns_nop 2\n"
"ds_write_b128 v58, a[116:119] offset:0\nQmaaa 128,56,12\nQmaaa 128,60,40\nQta 74,68,34048\nQmaaa 132,56,16\nds_write_b128 v58, a[120:123] offset:1024\nQmaaa 132,60,44\nQmaaa 136,56,20\n"
"Qta 76,68,41216\nQmaaa 136,60,48\nds_write_b128 v58, a[124:127] offset:2048\nQmaaa 140,56,24\nQmaaa 140,60,52\nQta 78,68,42240\ns_waitcnt lgkmcnt(7)\nQmaaa 144,64,80\n"
"ds_write_b128 v58, a[128:131] offset:3072\nQmaaa 144,68,108\nQmaaa 148,64,84\nQmaaa 148,68,112\nds_write_b128 v58, a[132:135] offset:4096\nQmaaa 152,64,88\nQta 56,68,49408\nQmaav 152,68,168\n"
"Qmaaa 156,64,92\nds_write_b128 v58, a[136:139] offset:5120\nQmaav 156,68,200\nQta 58,68,50432\nQmaaa 160,64,96\nQmaav 160,68,232\nds_write_b128 v58, a[140:143] offset:6144\nQmaaa 164,64,100\n"
"Qta 60,68,57600\nQmaav 164,68,212\nQmaaa 168,64,104\nQmaav 168,68,244\nQta 62,68,58624\ns_waitcnt lgkmcnt(8)\nQmaav 144,72,80\nv_add_u32 v75, 1, v75\n"
"Qmaav 144,76,88\ns_waitcnt vmcnt(8)\nds_write_b32 v73, v75\nQmaav 148,72,112\nQmaav 148,76,120\nQmaav 152,72,144\nQta 64,68,16896\nQmaav 152,76,152\n"
"Qmaav 156,72,176\nQmaav 156,76,184\nQta 66,68,17920\nQmaav 160,72,208\nQmaav 160,76,216\nds_read_b32 v66, v74\nQmaav 164,72,240\nQta 68,68,25088\n"
"Qmaav 164,76,248\nQmaav 168,72,84\nQmaav 168,76,116\ns_waitcnt lgkmcnt(1)\nv_cmp_gt_u32 vcc, v75, v66\ns_waitcnt lgkmcnt(5)\nQmaaa 144,56,0\ns_cbranch_vccnz .Lso2046r0\n"
".Lro2046r0:\nQmaaa 144,60,28\nQta 70,68,26112\nQmaaa 148,56,4\nQmaaa 148,60,32\nQdv 104,59,2048\nQmaaa 152,56,8\nQta 72,68,33280\n"
"Qmaaa 152,60,36\ns_nop 0\nds_write_b128 v58, a[144:147] offset:33792\nQmaaa 156,56,12\nQdv 136,59,3072\nQmaaa 156,60,40\nQta 74,68,34304\nQmaaa 160,56,16\n"
"ds_write_b128 v58, a[148:151] offset:34816\nQmaaa 160,60,44\nQdv 148,59,5120\nQmaaa 164,56,20\nQta 76,68,41472\nQmaaa 164,60,48\nds_write_b128 v58, a[152:155] offset:35840\nQmaaa 168,56,24\n"
"Qdv 180,59,0\ns_waitcnt lgkmcnt(9)\nQa 8,8,104\nQmaaa 168,60,52\nQta 78,68,42496\nQa 9,9,105\ns_waitcnt lgkmcnt(10)\nQmvaa 0,64,80\n"
"ds_write_b128 v58, a[156:159] offset:36864\nQmvaa 0,68,108\nQa 10,10,106\nQa 11,11,107\nQmvaa 4,64,84\ns_waitcnt lgkmcnt(8)\nQa 12,12,136\nQa 13,13,137\n"
"Qmvaa 4,68,112\nds_write_b128 v58, a[160:163] offset:37888\nQmvaa 8,64,88\nQta 56,68,49664\nQa 14,14,138\nQa 15,15,139\nQmvav 8,68,168\nQdv 104,59,4096\n"
"s_waitcnt lgkmcnt(8)\nQa 20,20,148\nQmvaa 12,64,92\nds_write_b128 v58, a[164:167] offset:38912\nQmvav 12,68,200\nQta 58,68,50688\nQa 21,21,149\nQmvaa 16,64,96\n"
"Qa 22,22,150\nQa 23,23,151\nQmvav 16,68,232\nQdv 136,59,6144\ns_waitcnt lgkmcnt(8)\nQa 0,0,180\nQmvaa 20,64,100\nQta 60,68,57856\n"
"Qa 1,1,181\nQmvav 20,68,212\nds_write_b128 v58, a[168:171] offset:39936\nQmvaa 24,64,104\nQa 2,2,182\nQa 3,3,183\nQmvav 24,68,244\nQta 62,68,58880\n"
"s_waitcnt lgkmcnt(6)\nQa 16,16,104\ns_waitcnt lgkmcnt(6)\nQmvav 0,72,80\nQdv 148,59,1024\nv_add_u32 v75, 1, v75\nQmvav 0,76,88\ns_waitcnt vmcnt(1)\n"
"ds_write_b32 v73, v75\nQmvav 4,72,112\nQa 17,17,105\nQa 18,18,106\nQmvav 4,76,120\nQa 19,19,107\ns_waitcnt lgkmcnt(5)\nQa 24,24,136\n"
"Qmvav 8,72,144\nQta 64,69,0\nQa 25,25,137\nQmvav 8,76,152\nQa 26,26,138\nQa 27,27,139\nQmvav 12,72,176\ns_waitcnt vmcnt(0)\n"
"buffer_store_dword v67, v77, s[24:27], 0 offen sc1\nQmvav 12,76,184\nQta 66,69,512\ns_waitcnt lgkmcnt(3)\nQa 4,4,148\nQmvav 16,72,208\nQa 5,5,149\nQa 6,6,150\n"
"Qmvav 16,76,216\nds_read_b32 v66, v74\nQa 7,7,151\nQmvav 20,72,240\nQta 68,69,1024\nQmvav 20,76,248\nQmvav 24,72,84\nQmvav 24,76,116\n"
"s_waitcnt lgkmcnt(1)\nv_cmp_gt_u32 vcc, v75, v66\ns_waitcnt lgkmcnt(6)\nQmvaa 0,56,0\ns_cbranch_vccnz .Lso2088r0\n.Lro2088r0:\nQmvaa 0,60,28\nQta 70,69,1536\n"
"Qmvaa 4,56,4\nQmvaa 4,60,32\nQdv 180,59,33792\nQmvaa 8,56,8\nQta 72,69,8192\nQmvaa 8,60,36\nQmvaa 12,56,12\nQdv 104,59,34816\n"
"Qmvaa 12,60,40\nQta 74,69,8704\nQmvaa 16,56,16\nQmvaa 16,60,44\nQdv 136,59,35840\nQmvaa 20,56,20\nQta 76,69,9216\nQmvaa 20,60,48\n"
"s_waitcnt lgkmcnt(5)\nQa 28,28,180\nQa 29,29,181\nQmvaa 24,56,24\nQdv 148,59,36864\nQa 30,30,182\nQmvaa 24,60,52\nQta 78,69,9728\n"
"Qa 31,31,183\ns_waitcnt lgkmcnt(11)\nQmaaa 172,64,80\ns_waitcnt lgkmcnt(5)\nQa 32,32,104\nQa 33,33,105\nQmaaa 172,68,108\nQa 34,34,106\n"
"Qa 35,35,107\nQmaaa 176,64,84\nQmaaa 176,68,112\ns_waitcnt lgkmcnt(3)\nQa 36,36,136\nQa 37,37,137\nQmaaa 180,64,88\nQta 56,69,16384\n"
"Qa 38,38,138\nQmaav 180,68,168\nQdv 180,59,37888\nQa 39,39,139\nQmaaa 184,64,92\ns_waitcnt lgkmcnt(3)\nQa 40,40,148\nQa 41,41,149\n"
"Qmaav 184,68,200\nQta 58,69,16896\nQa 42,42,150\nQmaaa 188,64,96\nQdv 104,59,38912\nQa 43,43,151\nQmaav 188,68,232\nQmaaa 192,64,100\n"
"Qta 60,69,17408\nQmaav 192,68,212\nQdv 136,59,39936\nQmaaa 196,64,104\ns_waitcnt lgkmcnt(4)\nQa 44,44,180\nQa 45,45,181\nQmaav 196,68,244\n"
"Qta 62,69,17920\nQa 46,46,182\ns_waitcnt lgkmcnt(5)\nQmaav 172,72,80\nQa 47,47,183\nQmaav 172,76,88\nQmaav 176,72,112\ns_waitcnt lgkmcnt(3)\n"
"Qa 48,48,104\nQa 49,49,105\nQmaav 176,76,120\nQa 50,50,106\nQa 51,51,107\nQmaav 180,72,144\nQta 64,68,17152\nQmaav 180,76,152\n"
"s_waitcnt lgkmcnt(2)\nQa 52,52,136\nQa 53,53,137\nQmaav 184,72,176\nQa 54,54,138\nQa 55,55,139\nQmaav 184,76,184\nQta 66,68,18176\n"
"Qmaav 188,72,208\nQmaav 188,76,216\nQmaav 192,72,240\nQta 68,68,25344\nQmaav 192,76,248\nQmaav 196,72,84\nQmaav 196,76,116\nQta 70,68,26368\n"
"s_waitcnt lgkmcnt(4)\nQmaaa 172,56,0\nQmaaa 172,60,28\nQmaaa 176,56,4\nQmaaa 176,60,32\nQmaaa 180,56,8\nQta 72,68,33536\nQmaaa 180,60,36\n"
"s_nop 2\nds_write_b128 v58, a[172:175] offset:0\nQmaaa 184,56,12\nQmaaa 184,60,40\nQta 74,68,34560\nQmaaa 188,56,16\nds_write_b128 v58, a[176:179] offset:1024\nQmaaa 188,60,44\n"
"Qmaaa 192,56,20\nQta 76,68,41728\nQmaaa 192,60,48\nds_write_b128 v58, a[180:183] offset:2048\nQmaaa 196,56,24\nQmaaa 196,60,52\nQta 78,68,42752\ns_waitcnt lgkmcnt(7)\n"
"Qmvaa 28,64,80\nds_write_b128 v58, a[184:187] offset:3072\nQmvaa 28,68,108\nQmvaa 32,64,84\nQmvaa 32,68,112\nds_write_b128 v58, a[188:191] offset:4096\nQmvaa 36,64,88\nQta 56,68,49920\n"
"Qmvav 36,68,168\nQmvaa 40,64,92\nds_write_b128 v58, a[192:195] offset:5120\nQmvav 40,68,200\nQta 58,68,50944\nQmvaa 44,64,96\nQmvav 44,68,232\nbuffer_load_dword v78, v76, s[24:27], 0 offen sc1\n"
"Qmvaa 48,64,100\nQta 60,68,58112\nQmvav 48,68,212\nds_write_b128 v58, a[196:199] offset:6144\nQmvaa 52,64,104\nQmvav 52,68,244\nQta 62,68,59136\ns_waitcnt lgkmcnt(8)\n"
"Qmvav 28,72,80\nv_add_u32 v75, 1, v75\nQmvav 28,76,88\nds_write_b32 v73, v75\nQmvav 32,72,112\nQmvav 32,76,120\nQmvav 36,72,144\nQta 64,69,256\n"
"Qmvav 36,76,152\nQmvav 40,72,176\nQmvav 40,76,184\nQta 66,69,768\nQmvav 44,72,208\nQmvav 44,76,216\nds_read_b32 v66, v74\nQmvav 48,72,240\n"
"Qta 68,69,1280\nQmvav 48,76,248\nQmvav 52,72,84\nQmvav 52,76,116\ns_waitcnt lgkmcnt(1)\nv_cmp_gt_u32 vcc, v75, v66\ns_waitcnt lgkmcnt(5)\nQmvaa 28,56,0\n"
"s_cbranch_vccnz .Lso2172r0\n.Lro2172r0:\nQmvaa 28,60,28\nQta 70,69,1792\nQmvaa 32,56,4\nQmvaa 32,60,32\nQdv 148,59,2048\nQmvaa 36,56,8\n"
"Qta 72,69,8448\nQmvaa 36,60,36\nQmvaa 40,56,12\nQdv 180,59,3072\nQmvaa 40,60,40\nQta 74,69,8960\nQmvaa 44,56,16\nQmvaa 44,60,44\n"
"Qdv 104,59,5120\nQmvaa 48,56,20\nQta 76,69,9472\nQmvaa 48,60,48\ns_waitcnt lgkmcnt(5)\nQa 156,156,148\nQa 157,157,149\nQmvaa 52,56,24\n"
"Qdv 136,59,0\nQa 158,158,150\nQmvaa 52,60,52\nQta 78,69,9984\nQa 159,159,151\ns_waitcnt lgkmcnt(11)\nQmvaa 92,64,80\ns_waitcnt lgkmcnt(5)\n"
"Qa 188,188,180\nQa 189,189,181\nQmvaa 92,68,108\nQa 190,190,182\nQa 191,191,183\nQmvaa 124,64,84\ns_waitcnt vmcnt(0)\nv_sub_u32 v66, v78, v65\n"
"v_cmp_gt_u32 vcc, 1, v66\ns_and_b64 vcc, vcc, s[28:29]\ns_cbranch_scc1 .Lx0_w\n.Lx0_r:\nQmvaa 124,68,112\ns_add_u32 s69, s98, 0\nbuffer_load_dwordx4 a[208:211], v60, s[24:27], s69 offen offset:0 sc1\nQmvaa 156,64,88\n"
"Qta 56,69,16640\ns_waitcnt lgkmcnt(4)\nQa 252,252,104\nQmvav 156,68,168\nbuffer_load_dwordx4 a[212:215], v60, s[24:27], s69 offen offset:1024 sc1\nQmvaa 188,64,92\nQdv 148,59,4096\nQa 253,253,105\n"
"Qmvav 188,68,200\nbuffer_load_dwordx4 a[216:219], v60, s[24:27], s69 offen offset:2048 sc1\nQmvaa 220,64,96\nQta 58,69,17152\nQa 254,254,106\nQa 255,255,107\nQmvav 220,68,232\nbuffer_load_dwordx4 a[220:223], v60, s[24:27], s69 offen offset:3072 sc1\n"
"Qmvaa 252,64,100\nQdv 180,59,6144\ns_waitcnt lgkmcnt(5)\nQa 92,92,136\nQmvav 252,68,212\ns_add_u32 s69, s98, 4096\nbuffer_load_dwordx4 a[224:227], v60, s[24:27], s69 offen offset:0 sc1\nQmvaa 100,64,104\n"
"Qta 60,69,17664\nQa 93,93,137\nQa 94,94,138\nQa 95,95,139\nQmvav 100,68,244\nbuffer_load_dwordx4 a[228:231], v60, s[24:27], s69 offen offset:1024 sc1\ns_waitcnt lgkmcnt(9)\nQmvav 92,72,80\n"
"Qdv 104,59,1024\ns_waitcnt lgkmcnt(4)\nQa 220,220,148\nQmvav 92,76,88\nbuffer_load_dwordx4 a[232:235], v60, s[24:27], s69 offen offset:2048 sc1\nQmvav 124,72,112\nQta 62,69,18176\nQa 221,221,149\n"
"Qmvav 124,76,120\nbuffer_load_dwordx4 a[236:239], v60, s[24:27], s69 offen offset:3072 sc1\nQmvav 156,72,144\nQa 222,222,150\nQa 223,223,151\nQmvav 156,76,152\ns_add_u32 s69, s98, 8192\nbuffer_load_dwordx4 a[240:243], v60, s[24:27], s69 offen offset:0 sc1\n"
"Qmvav 188,72,176\ns_waitcnt lgkmcnt(3)\nQa 100,100,180\nQa 101,101,181\nQmvav 188,76,184\nbuffer_load_dwordx4 a[244:247], v60, s[24:27], s69 offen offset:1024 sc1\nQmvav 220,72,208\nv_add_u32 v75, 1, v75\n"
"Qa 102,102,182\nQmvav 220,76,216\nds_write_b32 v73, v75\nQa 103,103,183\nQmvav 252,72,240\nbuffer_load_dwordx4 a[248:251], v60, s[24:27], s69 offen offset:2048 sc1\nQmvav 252,76,248\ns_waitcnt lgkmcnt(2)\n"
"Qa 124,124,104\nQa 125,125,105\nQmvav 100,72,84\nQa 126,126,106\nQa 127,127,107\nQmvav 100,76,116\ns_waitcnt lgkmcnt(1)\nQmvaa 92,56,0\n"
"Qmvaa 92,60,28\nQmvaa 124,56,4\nQmvaa 124,60,32\nds_read_b32 v66, v74\nQmvaa 156,56,8\nQmvaa 156,60,36\nQmvaa 188,56,12\nQmvaa 188,60,40\n"
"s_waitcnt lgkmcnt(0)\nv_cmp_gt_u32 vcc, v75, v66\nQmvaa 220,56,16\ns_cbranch_vccnz .Lso2222r0\n.Lro2222r0:\nQmvaa 220,60,44\nQmvaa 252,56,20\nQmvaa 252,60,48\n"
"Qmvaa 100,56,24\nQmvaa 100,60,52\ns_waitcnt vmcnt(10)\nQmvaa 92,202,208\nQmvaa 124,200,208\ns_waitcnt vmcnt(9)\nQmvaa 156,202,212\nQmvaa 188,200,212\n"
"s_nop 3\nQk 92,92,93\n.Lpg21:\ns_cbranch_execz .Lpg25\nQk 93,94,95\nds_write_b64 v56, v[92:93] offset:64\ns_waitcnt vmcnt(8)\nQmvaa 220,202,216\n"
"Qk 124,124,125\nQk 125,126,127\nds_write_b64 v56, v[124:125] offset:3392\nQmvaa 252,200,216\nQk 156,156,157\nQk 157,158,159\nds_write_b64 v56, v[156:157] offset:6720\ns_waitcnt vmcnt(7)\n"
"Qmvaa 100,202,220\nQk 188,188,189\nQk 189,190,191\nds_write_b64 v56, v[188:189] offset:10048\nQmvaa 0,200,220\nQk 220,220,221\nQk 221,222,223\nds_write_b64 v56, v[220:221] offset:13376\n"
"s_waitcnt vmcnt(6)\nQmvaa 4,202,224\nQk 252,252,253\nQk 253,254,255\nds_write_b64 v56, v[252:253] offset:16704\nQmvaa 8,200,224\nQk 100,100,101\nQk 101,102,103\n"
"ds_write_b64 v56, v[100:101] offset:20032\ns_waitcnt vmcnt(5)\nQmvaa 12,202,228\nQk 0,0,1\nQk 1,2,3\nds_write_b64 v56, v[0:1] offset:0\nQmvaa 16,200,228\nQk 4,4,5\n"
"Qk 5,6,7\nds_write_b64 v56, v[4:5] offset:3328\ns_waitcnt vmcnt(4)\nQmvaa 20,202,232\nQk 8,8,9\nQk 9,10,11\nds_write_b64 v56, v[8:9] offset:6656\nQmvaa 24,200,232\n"
"Qk 12,12,13\nQk 13,14,15\nds_write_b64 v56, v[12:13] offset:9984\ns_waitcnt vmcnt(3)\nQmvaa 28,202,236\nQk 16,16,17\nQk 17,18,19\nds_write_b64 v56, v[16:17] offset:13312\n"
"Qmvaa 32,200,236\nQk 20,20,21\nQk 21,22,23\nds_write_b64 v56, v[20:21] offset:16640\ns_waitcnt vmcnt(2)\nQmvaa 36,202,240\nQk 24,24,25\nQk 25,26,27\n"
"ds_write_b64 v56, v[24:25] offset:19968\nQmvaa 40,200,240\nQk 28,28,29\nQk 29,30,31\nds_write_b64 v56, v[28:29] offset:32\ns_waitcnt vmcnt(1)\nQmvaa 44,202,244\nQk 32,32,33\n"
"Qk 33,34,35\nds_write_b64 v56, v[32:33] offset:3360\nQmvaa 48,200,244\nQk 36,36,37\nQk 37,38,39\nds_write_b64 v56, v[36:37] offset:6688\ns_waitcnt vmcnt(0)\nQmvaa 52,202,248\n"
"Qk 40,40,41\nQk 41,42,43\nds_write_b64 v56, v[40:41] offset:10016\nQk 44,44,45\nQk 45,46,47\nds_write_b64 v56, v[44:45] offset:13344\nQk 48,48,49\nQk 49,50,51\n"
"ds_write_b64 v56, v[48:49] offset:16672\nQk 52,52,53\nQk 53,54,55\nds_write_b64 v56, v[52:53] offset:20000\ns_waitcnt lgkmcnt(0)\ns_barrier\nQda 0,57,0\nQda 4,57,3328\n"
"Qda 8,57,6656\nQda 12,57,9984\nQda 16,57,13312\nQda 20,57,16640\nQda 24,57,19968\nQda 28,71,64\nQda 32,71,6720\nQda 36,71,13376\n"
"Qda 40,71,20032\ns_mov_b32 s68, 0\ns_waitcnt lgkmcnt(10)\nbuffer_store_dwordx4 a[0:3], v64, s[40:43], s68 offen offset:0 sc1\ns_mov_b32 s68, 40960\ns_waitcnt lgkmcnt(9)\nbuffer_store_dwordx4 a[4:7], v64, s[40:43], s68 offen offset:0 sc1\ns_mov_b32 s68, 81920\n"
"s_waitcnt lgkmcnt(8)\nbuffer_store_dwordx4 a[8:11], v64, s[40:43], s68 offen offset:0 sc1\ns_mov_b32 s68, 122880\ns_waitcnt lgkmcnt(7)\nbuffer_store_dwordx4 a[12:15], v64, s[40:43], s68 offen offset:0 sc1\ns_mov_b32 s68, 163840\ns_waitcnt lgkmcnt(6)\nbuffer_store_dwordx4 a[16:19], v64, s[40:43], s68 offen offset:0 sc1\n"
"s_mov_b32 s68, 204800\ns_waitcnt lgkmcnt(5)\nbuffer_store_dwordx4 a[20:23], v64, s[40:43], s68 offen offset:0 sc1\ns_mov_b32 s68, 245760\ns_waitcnt lgkmcnt(4)\nbuffer_store_dwordx4 a[24:27], v64, s[40:43], s68 offen offset:0 sc1\ns_mov_b32 s68, 0\ns_waitcnt lgkmcnt(3)\n"
"buffer_store_dwordx4 a[28:31], v72, s[40:43], s68 offen offset:0 sc1\ns_mov_b32 s68, 81920\ns_waitcnt lgkmcnt(2)\nbuffer_store_dwordx4 a[32:35], v72, s[40:43], s68 offen offset:0 sc1\ns_mov_b32 s68, 163840\ns_waitcnt lgkmcnt(1)\nbuffer_store_dwordx4 a[36:39], v72, s[40:43], s68 offen offset:0 sc1\ns_mov_b32 s68, 245760\n"
"s_cmp_eq_u32 s93, 1\ns_cselect_b32 s68, 0x7ffff000, s68\ns_waitcnt lgkmcnt(0)\nbuffer_store_dwordx4 a[40:43], v72, s[40:43], s68 offen offset:0 sc1\ns_branch .Lfin\n.Lrole1:\n.Lpg22:\ns_cbranch_execz .Lpg26\n"
"Qmvaa 96,64,88\nQta 56,69,49408\ns_lshl_b32 s46, %19, 4\nQmvav 96,68,168\nbuffer_load_dwordx4 a[84:87], v60, s[48:51], s31 offen offset:1024 sc1\nQmvaa 128,64,92\nds_read_b32 v66, v74\ns_add_u32 s46, s46, 128\n"
"Qmvav 128,68,200\nQta 58,69,50432\nv_mov_b32 v77, 0x7ffff000\nQmvaa 160,64,96\nQmvav 160,68,232\nbuffer_load_dwordx4 a[88:91], v60, s[48:51], s31 offen offset:2048 sc1\nQmvaa 192,64,100\ns_waitcnt lgkmcnt(1)\n"
"v_cmp_gt_u32 vcc, v75, v66\nQmvav 192,68,212\ns_cbranch_vccnz .Lso1525r1\n.Lro1525r1:\nQmvaa 224,64,104\nQta 60,69,57600\nQmvav 224,68,244\nbuffer_load_dwordx4 a[92:95], v60, s[48:51], s31 offen offset:3072 sc1\n"
"s_waitcnt vmcnt(31)\ns_waitcnt lgkmcnt(4)\nQmaav 140,72,84\nQmaav 140,76,116\nQta 62,69,58624\nQmaav 136,72,240\ns_add_u32 m0, s96, 65536\ns_add_u32 s95, s67, 86016\n"
"Qmaav 136,76,248\nbuffer_load_dwordx4 v62, s[52:55], s95 offen lds\nQmaav 132,72,208\nQta 64,69,49664\nQmaav 132,76,216\nQmaav 128,72,176\nQmaav 128,76,184\nQta 66,69,50688\n"
"s_add_u32 s95, s67, 125952\nQmaav 124,72,144\nbuffer_load_dwordx4 v62, s[52:55], s95 offen offset:1024 lds\nQmaav 124,76,152\nQmaav 120,72,112\nQta 68,69,57856\nQmaav 120,76,120\ns_add_u32 m0, s96, 73728\n"
"s_add_u32 s95, s72, 86016\nQmaav 116,72,80\nbuffer_load_dwordx4 v62, s[52:55], s95 offen lds\nQmaav 116,76,88\nQta 70,69,58880\ns_waitcnt lgkmcnt(4)\nQmaav 144,56,80\nQmaav 144,60,88\n"
"s_add_u32 s95, s72, 125952\nQmaav 148,56,112\nbuffer_load_dwordx4 v62, s[52:55], s95 offen offset:1024 lds\nQmaav 148,60,120\nQmaav 152,56,144\nQta 72,69,49920\nQmaav 152,60,152\ns_add_u32 m0, s96, 81920\n"
"s_add_u32 s95, s73, 86016\nQmaav 156,56,176\nbuffer_load_dwordx4 v62, s[52:55], s95 offen lds\nQmaav 156,60,184\nQta 74,69,50944\nQmaav 160,56,208\nQmaav 160,60,216\nQmaav 164,56,240\n"
"Qta 76,69,58112\ns_add_u32 s95, s73, 125952\nQmaav 164,60,248\nbuffer_load_dwordx4 v62, s[52:55], s95 offen offset:1024 lds\nQmaav 168,56,84\nQmaav 168,60,116\nQta 78,69,59136\ns_waitcnt lgkmcnt(4)\n"
"Qmvav 24,64,84\ns_add_u32 m0, s96, 90112\ns_add_u32 s95, s67, 86016\nQmvav 24,68,116\nbuffer_load_dwordx4 v61, s[52:55], s95 offen lds\nQmvav 20,64,240\nQmvav 20,68,248\nQmvav 16,64,208\n"
"Qta 56,68,0\ns_add_u32 s95, s67, 105472\nQmvav 16,68,216\nbuffer_load_dwordx4 v61, s[52:55], s95 offen offset:1024 lds\nQmvav 12,64,176\nQmvav 12,68,184\nQta 58,68,512\nQmvav 8,64,144\n"
"s_add_u32 s31, s74, 32768\nQmvav 8,68,152\nbuffer_load_dwordx4 a[96:99], v60, s[48:51], s31 offen offset:0 sc1\nQmvav 4,64,112\nQta 60,68,1024\nQmvav 4,68,120\nQmvav 0,64,80\nQmvav 0,68,88\n"
"Qta 62,68,1536\ns_waitcnt lgkmcnt(4)\nQmvav 28,72,80\nbuffer_load_dwordx4 a[100:103], v60, s[48:51], s31 offen offset:1024 sc1\nQmvav 28,76,88\nQmvav 32,72,112\nQmvav 32,76,120\nQmvav 36,72,144\n"
"Qta 64,68,256\nQmvav 36,76,152\nbuffer_load_dwordx4 a[104:107], v60, s[48:51], s31 offen offset:2048 sc1\nQmvav 40,72,176\nQmvav 40,76,184\nQta 66,68,768\nQmvav 44,72,208\ns_add_u32 s31, s74, 43008\n"
"v_cmp_eq_u32 s[44:45], s46, v60\ns_nop 1\nv_cndmask_b32 v77, v77, v76, s[44:45]\nv_add_u32 v67, 1, v65\ns_xor_b32 s46, %19, 12\ns_lshl_b32 s28, 1, s46\nQmvav 44,76,216\nbuffer_load_dwordx4 a[108:111], v60, s[48:51], s31 offen offset:0 sc1\n"
"Qmvav 48,72,240\nQta 68,68,1280\nQmvav 48,76,248\nv_add_u32 v75, 1, v75\nQmvav 52,72,84\ns_waitcnt vmcnt(37)\nds_write_b32 v73, v75\nQmvav 52,76,116\n"
"Qta 70,68,1792\ns_waitcnt lgkmcnt(5)\nQmaav 196,56,84\nbuffer_load_dwordx4 a[112:115], v60, s[48:51], s31 offen offset:1024 sc1\nQmaav 196,60,116\nQmaav 192,56,240\nQmaav 192,60,248\nQmaav 188,56,208\n"
"Qta 72,68,8192\nQmaav 188,60,216\nbuffer_load_dwordx4 v[168:171], v60, s[48:51], s31 offen offset:2048 sc1\nQmaav 184,56,176\nds_read_b32 v66, v74\nQmaav 184,60,184\nQta 74,68,9216\nQmaav 180,56,144\n"
"Qmaav 180,60,152\nbuffer_load_dwordx4 v[200:203], v60, s[48:51], s31 offen offset:3072 sc1\nQmaav 176,56,112\ns_waitcnt lgkmcnt(1)\nv_cmp_gt_u32 vcc, v75, v66\nQmaav 176,60,120\ns_cbranch_vccnz .Lso1595r1\n.Lro1595r1:\n"
"Qmaav 172,56,80\nQta 76,68,16384\ns_add_u32 s31, s74, 47104\nQmaav 172,60,88\nbuffer_load_dwordx4 v[232:235], v60, s[48:51], s31 offen offset:0 sc1\ns_waitcnt lgkmcnt(4)\nQmvav 92,64,80\nQmvav 92,68,88\n"
"Qta 78,68,17408\nQmvav 124,64,112\nQmvav 124,68,120\nbuffer_load_dwordx4 v[212:215], v60, s[48:51], s31 offen offset:1024 sc1\nQmvav 156,64,144\nQta 56,68,8448\nQmvav 156,68,152\nQmvav 188,64,176\n"
"Qmvav 188,68,184\nQta 58,68,9472\nQmvav 220,64,208\nbuffer_load_dwordx4 v[244:247], v60, s[48:51], s31 offen offset:2048 sc1\nQmvav 220,68,216\nQmvav 252,64,240\nQta 60,68,16640\nQmvav 252,68,248\n"
"s_add_u32 m0, s91, 0\ns_add_u32 s31, s77, 28672\nQmvav 100,64,84\nbuffer_load_dwordx4 v60, s[48:51], s31 offen sc1 lds\nQmvav 100,68,116\nQta 62,68,17664\ns_waitcnt lgkmcnt(4)\nQmaav 224,72,84\n"
"Qmaav 224,76,116\nQmaav 220,72,240\nbuffer_load_dwordx4 v60, s[48:51], s31 offen offset:1024 sc1 lds\nQmaav 220,76,248\nQmaav 216,72,208\nQta 64,68,8704\nQmaav 216,76,216\nQmaav 212,72,176\n"
"buffer_load_dwordx4 v60, s[48:51], s31 offen offset:2048 sc1 lds\nQmaav 212,76,184\nQta 66,68,9728\nQmaav 208,72,144\nQmaav 208,76,152\nQmaav 204,72,112\nQta 68,68,16896\nQmaav 204,76,120\n"
"buffer_load_dwordx4 v60, s[48:51], s31 offen offset:3072 sc1 lds\nQmaav 200,72,80\nQmaav 200,76,88\nQta 70,68,17920\ns_waitcnt lgkmcnt(4)\nQmaav 228,56,80\ns_add_u32 m0, s91, 4096\ns_add_u32 s31, s77, 32768\n"
"Qmaav 228,60,88\nbuffer_load_dwordx4 v60, s[48:51], s31 offen sc1 lds\nQmaav 232,56,112\nQmaav 232,60,120\nQmaav 236,56,144\nQta 72,68,8960\nQmaav 236,60,152\nbuffer_load_dwordx4 v60, s[48:51], s31 offen offset:1024 sc1 lds\n"
"Qmaav 240,56,176\nQmaav 240,60,184\nQta 74,68,9984\nQmaav 244,56,208\nQmaav 244,60,216\nbuffer_load_dwordx4 v60, s[48:51], s31 offen offset:2048 sc1 lds\nQmaav 248,56,240\nQta 76,68,17152\n"
"Qmaav 248,60,248\nQmaav 252,56,84\nQmaav 252,60,116\nQta 78,68,18176\ns_add_u32 m0, s91, 33792\ns_add_u32 s31, s77, 43008\ns_waitcnt lgkmcnt(4)\nQmvav 172,64,84\n"
"buffer_load_dwordx4 v60, s[48:51], s31 offen sc1 lds\nQmvav 172,68,116\nQmvav 140,64,240\nQmvav 140,68,248\nQmvav 108,64,208\nQta 56,68,24576\nQmvav 108,68,216\nbuffer_load_dwordx4 v60, s[48:51], s31 offen offset:1024 sc1 lds\n"
"Qmvav 228,64,176\nQmvav 228,68,184\nQta 58,68,25600\nQmvav 196,64,144\nQmvav 196,68,152\nbuffer_load_dwordx4 v60, s[48:51], s31 offen offset:2048 sc1 lds\nQmvav 164,64,112\nQta 60,68,32768\n"
"Qmvav 164,68,120\nv_add_u32 v75, 1, v75\nQmvav 132,64,80\ns_waitcnt vmcnt(49)\nds_write_b32 v73, v75\nQmvav 132,68,88\nQta 62,68,33792\ns_waitcnt lgkmcnt(5)\n"
"Qmvav 204,72,80\nbuffer_load_dwordx4 v60, s[48:51], s31 offen offset:3072 sc1 lds\nQmvav 204,76,88\nQmvav 236,72,112\nQmvav 236,76,120\nQmvav 96,72,144\nQta 64,68,24832\ns_add_u32 m0, s91, 37888\n"
"s_add_u32 s31, s77, 47104\nQmvav 96,76,152\nbuffer_load_dwordx4 v60, s[48:51], s31 offen sc1 lds\nQmvav 128,72,176\nds_read_b32 v66, v74\nQmvav 128,76,184\nQta 66,68,25856\nQmvav 160,72,208\n"
"Qmvav 160,76,216\nbuffer_load_dwordx4 v60, s[48:51], s31 offen offset:1024 sc1 lds\nQmvav 192,72,240\ns_waitcnt lgkmcnt(1)\nv_cmp_gt_u32 vcc, v75, v66\nQmvav 192,76,248\ns_cbranch_vccnz .Lso1665r1\n.Lro1665r1:\n"
"Qmvav 224,72,84\nQta 68,68,33024\nQmvav 224,76,116\nbuffer_load_dwordx4 v60, s[48:51], s31 offen offset:2048 sc1 lds\ns_waitcnt vmcnt(38)\ns_waitcnt lgkmcnt(4)\nQmaaa 140,56,24\nQmaaa 140,60,52\n"
"Qta 70,68,34048\nQmaaa 136,56,20\ns_add_u32 m0, s96, 0\ns_add_u32 s95, s67, 126976\nQmaaa 136,60,48\nbuffer_load_dwordx4 v61, s[52:55], s95 offen lds\nQmaaa 132,56,16\nQta 72,68,25088\n"
"Qmaaa 132,60,44\nQmaaa 128,56,12\nQmaaa 128,60,40\nQta 74,68,26112\ns_add_u32 s95, s67, 146432\nQmaaa 124,56,8\nbuffer_load_dwordx4 v61, s[52:55], s95 offen offset:1024 lds\nQmaaa 124,60,36\n"
"Qmaaa 120,56,4\nQta 76,68,33280\nQmaaa 120,60,32\ns_add_u32 s31, s75, 28672\nQmaaa 116,56,0\nbuffer_load_dwordx4 v[80:83], v60, s[48:51], s31 offen offset:0 sc1\nQmaaa 116,60,28\nQta 78,68,34304\n"
"s_waitcnt lgkmcnt(4)\nQmaaa 144,64,0\nQmaaa 144,68,28\nQmaaa 148,64,4\nbuffer_load_dwordx4 v[112:115], v60, s[48:51], s31 offen offset:1024 sc1\nQmaaa 148,68,32\nQmaaa 152,64,8\nQta 56,68,25344\n"
"Qmaaa 152,68,36\nQmaaa 156,64,12\nbuffer_load_dwordx4 v[144:147], v60, s[48:51], s31 offen offset:2048 sc1\nQmaaa 156,68,40\nQta 58,68,26368\nQmaaa 160,64,16\nQmaaa 160,68,44\nQmaaa 164,64,20\n"
"Qta 60,68,33536\nQmaaa 164,68,48\nbuffer_load_dwordx4 v[176:179], v60, s[48:51], s31 offen offset:3072 sc1\nQmaaa 168,64,24\nQmaaa 168,68,52\nQta 62,68,34560\ns_waitcnt lgkmcnt(4)\nQmvaa 24,72,24\n"
"s_add_u32 s31, s75, 32768\nQmvaa 24,76,52\nbuffer_load_dwordx4 v[208:211], v60, s[48:51], s31 offen offset:0 sc1\nQmvaa 20,72,20\nQmvaa 20,76,48\nQmvaa 16,72,16\nQta 64,68,40960\nQmvaa 16,76,44\n"
"buffer_load_dwordx4 v[240:243], v60, s[48:51], s31 offen offset:1024 sc1\nQmvaa 12,72,12\nQmvaa 12,76,40\nQta 66,68,41472\nQmvaa 8,72,8\nQmvaa 8,76,36\nbuffer_load_dwordx4 v[84:87], v60, s[48:51], s31 offen offset:2048 sc1\nQmvaa 4,72,4\n"
"Qta 68,68,41984\nQmvaa 4,76,32\nQmvaa 0,72,0\nQmvaa 0,76,28\nQta 70,68,42496\ns_add_u32 s31, s75, 43008\ns_waitcnt lgkmcnt(4)\nQmvaa 28,56,0\n"
"buffer_load_dwordx4 v[88:91], v60, s[48:51], s31 offen offset:0 sc1\nQmvaa 28,60,28\nQmvaa 32,56,4\nQmvaa 32,60,32\nQmvaa 36,56,8\nQta 72,68,41216\nQmvaa 36,60,36\nbuffer_load_dwordx4 v[120:123], v60, s[48:51], s31 offen offset:1024 sc1\n"
"Qmvaa 40,56,12\nQmvaa 40,60,40\nQta 74,68,41728\nQmvaa 44,56,16\nQmvaa 44,60,44\nbuffer_load_dwordx4 v[152:155], v60, s[48:51], s31 offen offset:2048 sc1\nQmvaa 48,56,20\nQta 76,68,42240\n"
"Qmvaa 48,60,48\nv_add_u32 v75, 1, v75\nQmvaa 52,56,24\ns_waitcnt vmcnt(40)\nds_write_b32 v73, v75\nQmvaa 52,60,52\nQta 78,68,42752\ns_waitcnt lgkmcnt(5)\n"
"Qmaaa 196,64,24\nbuffer_load_dwordx4 v[184:187], v60, s[48:51], s31 offen offset:3072 sc1\nQmaaa 196,68,52\nQmaaa 192,64,20\nQmaaa 192,68,48\nQmaaa 188,64,16\nQta 56,68,49152\ns_add_u32 s31, s75, 47104\n"
"Qmaaa 188,68,44\nbuffer_load_dwordx4 v[216:219], v60, s[48:51], s31 offen offset:0 sc1\nQmaaa 184,64,12\nds_read_b32 v66, v74\nQmaaa 184,68,40\nQta 58,68,50176\nQmaaa 180,64,8\nQmaaa 180,68,36\n"
"buffer_load_dwordx4 v[248:251], v60, s[48:51], s31 offen offset:1024 sc1\n.Lpg23:\ns_cbranch_execz .Ltr8\nQmaaa 176,64,4\ns_waitcnt lgkmcnt(1)\nv_cmp_gt_u32 vcc, v75, v66\nQmaaa 176,68,32\ns_cbranch_vccnz .Lso1735r1\n"
".Lro1735r1:\nQmaaa 172,64,0\nQta 60,68,57344\nQmaaa 172,68,28\nbuffer_load_dwordx4 v[116:119], v60, s[48:51], s31 offen offset:2048 sc1\ns_waitcnt lgkmcnt(4)\nQmvaa 92,72,0\nQmvaa 92,76,28\n"
"Qta 62,68,58368\nQmvaa 124,72,4\ns_add_u32 m0, s96, 8192\ns_add_u32 s95, s72, 86016\nQmvaa 124,76,32\nbuffer_load_dwordx4 v61, s[52:55], s95 offen lds\nQmvaa 156,72,8\nQta 64,68,49408\n"
"Qmvaa 156,76,36\nQmvaa 188,72,12\nQmvaa 188,76,40\nQta 66,68,50432\ns_add_u32 s95, s72, 105472\nQmvaa 220,72,16\nbuffer_load_dwordx4 v61, s[52:55], s95 offen offset:1024 lds\nQmvaa 220,76,44\n"
"Qmvaa 252,72,20\nQta 68,68,57600\nQmvaa 252,76,48\ns_add_u32 m0, s96, 16384\ns_add_u32 s95, s72, 126976\nQmvaa 100,72,24\nbuffer_load_dwordx4 v61, s[52:55], s95 offen lds\nQmvaa 100,76,52\n"
"Qta 70,68,58624\ns_waitcnt lgkmcnt(4)\nQmaaa 224,56,24\nQmaaa 224,60,52\ns_add_u32 s95, s72, 146432\nQmaaa 220,56,20\nbuffer_load_dwordx4 v61, s[52:55], s95 offen offset:1024 lds\nQmaaa 220,60,48\n"
"Qmaaa 216,56,16\nQta 72,68,49664\nQmaaa 216,60,44\ns_add_u32 m0, s96, 24576\ns_add_u32 s95, s73, 86016\nQmaaa 212,56,12\nbuffer_load_dwordx4 v61, s[52:55], s95 offen lds\nQmaaa 212,60,40\n"
"Qta 74,68,50688\nQmaaa 208,56,8\nQmaaa 208,60,36\nQmaaa 204,56,4\nQta 76,68,57856\ns_add_u32 s95, s73, 105472\nQmaaa 204,60,32\nbuffer_load_dwordx4 v61, s[52:55], s95 offen offset:1024 lds\n"
"Qmaaa 200,56,0\nQmaaa 200,60,28\nQta 78,68,58880\ns_waitcnt lgkmcnt(4)\nQmaaa 228,64,0\ns_add_u32 m0, s96, 32768\ns_add_u32 s95, s73, 126976\nQmaaa 228,68,28\n"
"buffer_load_dwordx4 v61, s[52:55], s95 offen lds\nQmaaa 232,64,4\nQmaaa 232,68,32\nQmaaa 236,64,8\nQta 56,68,49920\ns_add_u32 s95, s73, 146432\nQmaaa 236,68,36\nbuffer_load_dwordx4 v61, s[52:55], s95 offen offset:1024 lds\n"
"Qmaaa 240,64,12\nQmaaa 240,68,40\nQta 58,68,50944\nQmaaa 244,64,16\nQmaaa 244,68,44\nQmaaa 248,64,20\nQta 60,68,58112\nQmaaa 248,68,48\n"
"Qmaaa 252,64,24\nQmaaa 252,68,52\nQta 62,68,59136\ns_waitcnt lgkmcnt(4)\nQmvaa 172,72,24\nQmvaa 172,76,52\nQmvaa 140,72,20\nQmvaa 140,76,48\n"
"Qmvaa 108,72,16\nQta 64,69,0\nQmvaa 108,76,44\nQmvaa 228,72,12\nQmvaa 228,76,40\nQta 66,69,512\nQmvaa 196,72,8\nQmvaa 196,76,36\n"
"Qmvaa 164,72,4\nQta 68,69,1024\nQmvaa 164,76,32\nv_add_u32 v75, 1, v75\nQmvaa 132,72,0\ns_waitcnt vmcnt(22)\nds_write_b32 v73, v75\nQmvaa 132,76,28\n"
"Qta 70,69,1536\ns_waitcnt lgkmcnt(5)\nQmvaa 204,56,0\ns_waitcnt vmcnt(24)\nQda 0,58,0\nQmvaa 204,60,28\nQmvaa 236,56,4\nQda 28,58,33792\n"
"Qmvaa 236,60,32\nQmvaa 96,56,8\nQta 72,69,8192\nQmvaa 96,60,36\nQda 4,58,1024\nQmvaa 128,56,12\nds_read_b32 v66, v74\nQmvaa 128,60,40\n"
"Qta 74,69,8704\nQmvaa 160,56,16\nQda 32,58,34816\nQmvaa 160,60,44\nQmvaa 192,56,20\ns_waitcnt lgkmcnt(2)\nv_cmp_gt_u32 vcc, v75, v66\nQmvaa 192,60,48\n"
"s_cbranch_vccnz .Lso1805r1\n.Lro1805r1:\nQmvaa 224,56,24\nQta 76,69,9216\ns_add_u32 m0, s96, 40960\ns_add_u32 s95, s67, 86016\nQmvaa 224,60,52\nbuffer_load_dwordx4 v63, s[52:55], s95 offen lds\n"
"s_waitcnt vmcnt(39)\ns_waitcnt lgkmcnt(8)\nQmaaa 172,64,80\nQda 8,58,2048\nQmaaa 172,68,108\nQta 78,69,9728\nQmaaa 176,64,84\nQda 36,58,35840\n"
"s_add_u32 s95, s67, 105472\nQmaaa 176,68,112\nbuffer_load_dwordx4 v63, s[52:55], s95 offen offset:1024 lds\nQmaaa 180,64,88\nQta 56,69,16384\nQmaav 180,68,168\nQda 12,58,3072\nQmaaa 184,64,92\n"
"Qmaav 184,68,200\nQta 58,69,16896\ns_add_u32 m0, s96, 49152\ns_add_u32 s95, s67, 126976\nQmaaa 188,64,96\nbuffer_load_dwordx4 v63, s[52:55], s95 offen lds\nQmaav 188,68,232\nQda 40,58,36864\n"
"Qmaaa 192,64,100\nQta 60,69,17408\nQmaav 192,68,212\nQda 16,58,4096\ns_add_u32 s95, s67, 146432\nQmaaa 196,64,104\nbuffer_load_dwordx4 v63, s[52:55], s95 offen offset:1024 lds\nQmaav 196,68,244\n"
"Qta 62,69,17920\ns_waitcnt vmcnt(12)\ns_waitcnt lgkmcnt(8)\nQmaav 172,72,80\nQda 44,58,37888\nQmaav 172,76,88\ns_add_u32 m0, s96, 57344\ns_add_u32 s95, s72, 86016\n"
"Qmaav 176,72,112\nbuffer_load_dwordx4 v63, s[52:55], s95 offen lds\nQmaav 176,76,120\nQda 20,58,5120\nQmaav 180,72,144\nQta 64,69,24576\nv_add_u32 v75, 1, v75\nQmaav 180,76,152\n"
"s_waitcnt vmcnt(5)\nds_write_b32 v73, v75\ns_add_u32 s95, s72, 105472\nQmaav 184,72,176\nbuffer_load_dwordx4 v63, s[52:55], s95 offen offset:1024 lds\nQmaav 184,76,184\nQta 66,69,25600\nQmaav 188,72,208\n"
"Qda 48,58,38912\nQmaav 188,76,216\nQmaav 192,72,240\nQta 68,68,0\nQmaav 192,76,248\nQda 24,58,6144\nQmaav 196,72,84\nQmaav 196,76,116\n"
"ds_read_b32 v66, v74\ns_waitcnt lgkmcnt(9)\nQmaaa 172,56,0\nQta 70,68,1024\nQmaaa 172,60,28\nQda 52,58,39936\nQmaaa 176,56,4\nQmaaa 176,60,32\n"
"s_waitcnt lgkmcnt(2)\nv_cmp_gt_u32 vcc, v75, v66\ns_waitcnt lgkmcnt(11)\nQmaaa 180,56,8\ns_cbranch_vccnz .Lso1840r1\n.Lro1840r1:\nQmaaa 180,60,36\nQta 72,68,8192\n"
"Qmaaa 184,56,12\nds_write_b128 v58, a[172:175] offset:0\nQmaaa 184,60,40\ns_waitcnt lgkmcnt(2)\nQmaaa 188,56,16\nQta 74,68,9216\nQmaaa 188,60,44\nds_write_b128 v58, a[176:179] offset:1024\n"
"Qmaaa 192,56,20\nQmaaa 192,60,48\nQta 76,68,16384\nQmaaa 196,56,24\nds_write_b128 v58, a[180:183] offset:2048\nQmaaa 196,60,52\ns_waitcnt lgkmcnt(7)\nQmaaa 116,64,80\n"
"Qta 78,68,17408\nQmaaa 116,68,108\nds_write_b128 v58, a[184:187] offset:3072\nQmaaa 120,64,84\nQmaaa 120,68,112\nQmaaa 124,64,88\nQta 56,68,24576\nQmaav 124,68,168\n"
"ds_write_b128 v58, a[188:191] offset:4096\nQmaaa 128,64,92\nQmaav 128,68,200\nQta 58,68,25600\nQmaaa 132,64,96\nds_write_b128 v58, a[192:195] offset:5120\nQmaav 132,68,232\nQmaaa 136,64,100\n"
"Qta 60,68,32768\nQmaav 136,68,212\nds_write_b128 v58, a[196:199] offset:6144\nQmaaa 140,64,104\nQmaav 140,68,244\nQta 62,68,33792\ns_waitcnt lgkmcnt(8)\nQmaav 116,72,80\n"
"v_add_u32 v75, 1, v75\nQmaav 116,76,88\nds_write_b32 v73, v75\nQmaav 120,72,112\nQmaav 120,76,120\nQmaav 124,72,144\nQta 64,69,256\nQmaav 124,76,152\n"
"Qmaav 128,72,176\nQmaav 128,76,184\nQta 66,69,768\nQmaav 132,72,208\nQmaav 132,76,216\nds_read_b32 v66, v74\nQmaav 136,72,240\nQta 68,69,1280\n"
"Qmaav 136,76,248\nQmaav 140,72,84\nQmaav 140,76,116\ns_waitcnt lgkmcnt(1)\nv_cmp_gt_u32 vcc, v75, v66\ns_waitcnt lgkmcnt(5)\nQmaaa 116,56,0\ns_cbranch_vccnz .Lso1878r1\n"
".Lro1878r1:\nQmaaa 116,60,28\nQta 70,69,1792\nQmaaa 120,56,4\nQmaaa 120,60,32\nQdv 148,59,2048\nQmaaa 124,56,8\nQta 72,69,8448\n"
"Qmaaa 124,60,36\ns_nop 0\nds_write_b128 v58, a[116:119] offset:33792\nQmaaa 128,56,12\nQdv 180,59,3072\nQmaaa 128,60,40\nQta 74,69,8960\nQmaaa 132,56,16\n"
"ds_write_b128 v58, a[120:123] offset:34816\nQmaaa 132,60,44\nQdv 104,59,5120\nQmaaa 136,56,20\nQta 76,69,9472\nQmaaa 136,60,48\nds_write_b128 v58, a[124:127] offset:35840\nQmaaa 140,56,24\n"
"Qdv 136,59,0\ns_waitcnt lgkmcnt(9)\nQa 156,156,148\nQmaaa 140,60,52\nQta 78,69,9984\nQa 157,157,149\ns_waitcnt lgkmcnt(10)\nQmvaa 92,64,80\n"
"ds_write_b128 v58, a[128:131] offset:36864\nQmvaa 92,68,108\nQa 158,158,150\nQa 159,159,151\nQmvaa 124,64,84\ns_waitcnt lgkmcnt(8)\nQa 188,188,180\nQa 189,189,181\n"
"Qmvaa 124,68,112\nds_write_b128 v58, a[132:135] offset:37888\nQmvaa 156,64,88\nQta 56,69,16640\nQa 190,190,182\nQa 191,191,183\nQmvav 156,68,168\nQdv 148,59,4096\n"
"s_waitcnt lgkmcnt(8)\nQa 252,252,104\nQmvaa 188,64,92\nds_write_b128 v58, a[136:139] offset:38912\nQmvav 188,68,200\nQta 58,69,17152\nQa 253,253,105\nQmvaa 220,64,96\n"
"Qa 254,254,106\nQa 255,255,107\nQmvav 220,68,232\nQdv 180,59,6144\ns_waitcnt lgkmcnt(8)\nQa 92,92,136\nQmvaa 252,64,100\nQta 60,69,17664\n"
"Qa 93,93,137\nQmvav 252,68,212\nds_write_b128 v58, a[140:143] offset:39936\nQmvaa 100,64,104\nQa 94,94,138\nQa 95,95,139\nQmvav 100,68,244\nQta 62,69,18176\n"
"s_waitcnt lgkmcnt(6)\nQa 220,220,148\ns_waitcnt lgkmcnt(6)\nQmvav 92,72,80\nQdv 104,59,1024\nv_add_u32 v75, 1, v75\nQmvav 92,76,88\nds_write_b32 v73, v75\n"
"Qa 221,221,149\nQmvav 124,72,112\nQa 222,222,150\nQa 223,223,151\nQmvav 124,76,120\ns_waitcnt lgkmcnt(5)\nQa 100,100,180\nQa 101,101,181\n"
"Qmvav 156,72,144\nQta 64,69,24832\nQa 102,102,182\nQmvav 156,76,152\nQa 103,103,183\nQmvav 188,72,176\nQmvav 188,76,184\nQta 66,69,25856\n"
"s_waitcnt lgkmcnt(3)\nQa 124,124,104\nQmvav 220,72,208\nQa 125,125,105\nQa 126,126,106\nQmvav 220,76,216\nds_read_b32 v66, v74\nQa 127,127,107\n"
"Qmvav 252,72,240\nQta 68,68,256\nQmvav 252,76,248\nQmvav 100,72,84\nQmvav 100,76,116\ns_waitcnt lgkmcnt(1)\nv_cmp_gt_u32 vcc, v75, v66\ns_waitcnt lgkmcnt(6)\n"
"Qmvaa 92,56,0\ns_cbranch_vccnz .Lso1920r1\n.Lro1920r1:\ns_add_u32 s68, s64, 0\nQmvaa 92,60,28\nQta 70,68,1280\ns_add_u32 m0, s96, 65536\ns_add_u32 s95, s72, 126976\n"
"Qmvaa 124,56,4\nbuffer_load_dwordx4 v63, s[52:55], s95 offen lds\nQmvaa 124,60,32\nQdv 136,59,33792\nQmvaa 156,56,8\nQta 72,68,8448\nQk 92,92,93\nQmvaa 156,60,36\n"
"s_add_u32 s95, s72, 146432\nQk 93,94,95\nQmvaa 188,56,12\nbuffer_load_dwordx4 v63, s[52:55], s95 offen offset:1024 lds\nQmvaa 188,60,40\nQta 74,68,9472\nQk 94,124,125\nQmvaa 220,56,16\n"
"Qdv 148,59,34816\nQk 95,126,127\nQmvaa 220,60,44\nbuffer_store_dwordx4 v[92:95], v60, s[24:27], s68 offen offset:0 sc1\nQk 156,156,157\nQmvaa 252,56,20\nQta 76,68,16640\ns_add_u32 m0, s96, 73728\n"
"s_add_u32 s95, s73, 86016\nQmvaa 252,60,48\nbuffer_load_dwordx4 v63, s[52:55], s95 offen lds\nv_add_u32 v75, 1, v75\nQmvaa 100,56,24\nds_write_b32 v73, v75\ns_waitcnt lgkmcnt(5)\nQa 0,0,136\n"
"Qmvaa 100,60,52\nQta 78,68,17664\nQa 1,1,137\ns_waitcnt lgkmcnt(6)\nQmaaa 144,64,80\nQdv 180,59,35840\ns_add_u32 s95, s73, 105472\nQmaaa 144,68,108\n"
"buffer_load_dwordx4 v63, s[52:55], s95 offen offset:1024 lds\nQmaaa 148,64,84\nQa 2,2,138\nQa 3,3,139\nQmaaa 148,68,112\nQdv 104,59,36864\ns_waitcnt lgkmcnt(5)\nQa 4,4,148\n"
"Qmaaa 152,64,88\nQta 56,68,24832\ns_add_u32 m0, s96, 81920\ns_add_u32 s95, s73, 126976\nQmaav 152,68,168\nbuffer_load_dwordx4 v63, s[52:55], s95 offen lds\nQmaaa 156,64,92\nds_read_b32 v66, v74\n"
"Qa 5,5,149\nQa 6,6,150\nQa 7,7,151\nQmaav 156,68,200\nQta 58,68,25856\ns_waitcnt lgkmcnt(4)\nQa 8,8,180\nQmaaa 160,64,96\n"
"Qdv 136,59,37888\ns_add_u32 s95, s73, 146432\nQmaav 160,68,232\nbuffer_load_dwordx4 v63, s[52:55], s95 offen offset:1024 lds\nQmaaa 164,64,100\ns_waitcnt lgkmcnt(2)\nv_cmp_gt_u32 vcc, v75, v66\nQmaav 164,68,212\n"
"s_cbranch_vccnz .Lso1945r1\n.Lro1945r1:\nQa 9,9,181\nQa 10,10,182\nQa 11,11,183\nQmaaa 168,64,104\nQta 60,68,33024\ns_waitcnt lgkmcnt(5)\n"
"Qa 12,12,104\nQmaav 168,68,244\nQdv 148,59,38912\nQa 13,13,105\ns_waitcnt lgkmcnt(6)\nQmaav 144,72,80\nQa 14,14,106\nQa 15,15,107\n"
"Qmaav 144,76,88\nQta 62,68,34048\ns_waitcnt lgkmcnt(3)\nQa 16,16,136\nQmaav 148,72,112\nQdv 180,59,39936\nQa 17,17,137\nQmaav 148,76,120\n"
"Qa 18,18,138\nQa 19,19,139\nQmaav 152,72,144\nQta 64,69,25088\nv_bfe_u32 v61, v60, 6, 2\nQmaav 152,76,152\ns_waitcnt lgkmcnt(3)\nQa 20,20,148\n"
"Qa 21,21,149\nQmaav 156,72,176\nQa 22,22,150\nQa 23,23,151\nQmaav 156,76,184\nQta 66,69,26112\nv_lshrrev_b32 v62, 8, v60\nQmaav 160,72,208\n"
"v_xor_b32 v61, v61, v62\nv_bfe_u32 v62, v60, 5, 1\nQmaav 160,76,216\nv_lshl_or_b32 v61, v61, 1, v62\nv_and_b32 v62, 16, v60\nQmaav 164,72,240\nQta 68,68,512\ns_waitcnt lgkmcnt(3)\n"
"Qa 24,24,180\nQmaav 164,76,248\nQa 25,25,181\nQa 26,26,182\nQmaav 168,72,84\nQa 27,27,183\nv_mov_b32 v63, 0x3f80\nQmaav 168,76,116\n"
"Qta 70,68,1536\nv_lshlrev_b32 v63, v62, v63\ns_waitcnt lgkmcnt(5)\nQmaaa 144,56,0\nv_min_u32 v62, 1, v61\nv_add_u32 v62, -1, v62\nQmaaa 144,60,28\nv_and_b32 v62, v62, v63\n"
"v_xor_b32 v61, 1, v61\nQmaaa 148,56,4\nv_min_u32 v61, 1, v61\nv_add_u32 v61, -1, v61\nQmaaa 148,60,32\nv_and_b32 v61, v61, v63\nv_accvgpr_write_b32 a174, v62\nQmaaa 152,56,8\n"
"Qta 72,68,8704\nv_accvgpr_write_b32 a175, v61\nQmaaa 152,60,36\nds_write_b128 v58, a[144:147] offset:0\nQmaaa 156,56,12\nv_accvgpr_write_b32 a172, 0\nv_accvgpr_write_b32 a173, 0\nQmaaa 156,60,40\n"
"Qta 74,68,9728\nv_accvgpr_write_b32 a176, 0\nQmaaa 160,56,16\nds_write_b128 v58, a[148:151] offset:1024\nQmaaa 160,60,44\nv_accvgpr_write_b32 a177, 0\nQk 157,158,159\nQmaaa 164,56,20\n"
"Qta 76,68,16896\nQk 158,188,189\nQmaaa 164,60,48\nds_write_b128 v58, a[152:155] offset:2048\nQmaaa 168,56,24\nQk 159,190,191\nQk 220,220,221\nQmaaa 168,60,52\n"
"Qta 78,68,17920\nbuffer_store_dwordx4 v[156:159], v60, s[24:27], s68 offen offset:1024 sc1\ns_waitcnt lgkmcnt(7)\nQmvaa 0,64,80\nds_write_b128 v58, a[156:159] offset:3072\nQmvaa 0,68,108\nQk 221,222,223\nQk 222,252,253\n"
"Qmvaa 4,64,84\nQk 223,254,255\n.Lpg24:\ns_cbranch_execz .Ltr8\nQk 100,100,101\nQmvaa 4,68,112\nds_write_b128 v58, a[160:163] offset:4096\nQmvaa 8,64,88\n"
"Qta 56,68,25088\nbuffer_store_dwordx4 v[220:223], v60, s[24:27], s68 offen offset:2048 sc1\nQmvav 8,68,168\nQk 101,102,103\nQmvaa 12,64,92\nds_write_b128 v58, a[164:167] offset:5120\nQmvav 12,68,200\nQta 58,68,26112\n"
"Qmvaa 16,64,96\nQmvav 16,68,232\nds_write_b128 v58, a[168:171] offset:6144\nQmvaa 20,64,100\nQta 60,68,33280\nQmvav 20,68,212\nQmvaa 24,64,104\nQmvav 24,68,244\n"
"Qta 62,68,34304\ns_waitcnt lgkmcnt(8)\nQmvav 0,72,80\nv_add_u32 v75, 1, v75\nQmvav 0,76,88\ns_waitcnt vmcnt(11)\nds_write_b32 v73, v75\nQmvav 4,72,112\n"
"Qmvav 4,76,120\nQmvav 8,72,144\nQta 64,69,25344\nQmvav 8,76,152\nQmvav 12,72,176\nQmvav 12,76,184\nQta 66,69,26368\nQmvav 16,72,208\n"
"Qmvav 16,76,216\nds_read_b32 v66, v74\nQmvav 20,72,240\nQta 68,68,768\nQmvav 20,76,248\nQmvav 24,72,84\nQmvav 24,76,116\ns_waitcnt lgkmcnt(1)\n"
"v_cmp_gt_u32 vcc, v75, v66\ns_waitcnt lgkmcnt(5)\nQmvaa 0,56,0\ns_cbranch_vccnz .Lso2004r1\n.Lro2004r1:\nQmvaa 0,60,28\nQta 70,68,1792\nQmvaa 4,56,4\n"
"Qmvaa 4,60,32\nQdv 104,59,2048\nQmvaa 8,56,8\nQta 72,68,8960\ns_nop 1\nQk 102,0,1\nQmvaa 8,60,36\nQk 103,2,3\n"
"Qmvaa 12,56,12\nQdv 136,59,3072\nbuffer_store_dwordx4 v[100:103], v60, s[24:27], s68 offen offset:3072 sc1\nQmvaa 12,60,40\nQta 74,68,9984\nQk 4,4,5\nQmvaa 16,56,16\nQk 5,6,7\n"
"Qk 6,8,9\nQmvaa 16,60,44\nQdv 148,59,5120\nQk 7,10,11\nQmvaa 20,56,20\nQta 76,68,17152\ns_add_u32 s68, s64, 4096\nQmvaa 20,60,48\n"
"s_waitcnt lgkmcnt(5)\nQa 36,36,104\nQa 37,37,105\nQmvaa 24,56,24\nQdv 180,59,0\nQa 38,38,106\nQmvaa 24,60,52\nQta 78,68,18176\n"
"Qa 39,39,107\ns_waitcnt lgkmcnt(11)\nQmvaa 28,64,80\ns_waitcnt lgkmcnt(5)\nQa 40,40,136\nQa 41,41,137\nQmvaa 28,68,108\nQa 42,42,138\n"
"Qa 43,43,139\nQmvaa 32,64,84\nbuffer_store_dwordx4 v[4:7], v60, s[24:27], s68 offen offset:0 sc1\nQk 12,12,13\nQmvaa 32,68,112\ns_waitcnt lgkmcnt(3)\nQa 48,48,148\nQa 49,49,149\n"
"Qmvaa 36,64,88\nQta 56,68,25344\nQa 50,50,150\nQmvav 36,68,168\nQdv 104,59,4096\nQa 51,51,151\nQmvaa 40,64,92\ns_waitcnt lgkmcnt(3)\n"
"Qa 28,28,180\nQa 29,29,181\nQmvav 40,68,200\nQta 58,68,26368\nQa 30,30,182\nQmvaa 44,64,96\nQa 31,31,183\nQk 13,14,15\n"
"Qmvav 44,68,232\nQdv 136,59,6144\nQk 14,16,17\nQmvaa 48,64,100\nQta 60,68,33536\nQk 15,18,19\nQmvav 48,68,212\nbuffer_store_dwordx4 v[12:15], v60, s[24:27], s68 offen offset:1024 sc1\n"
"Qk 20,20,21\nQmvaa 52,64,104\nQk 21,22,23\nQk 22,24,25\nQmvav 52,68,244\nQta 62,68,34560\nv_add_u32 v75, 1, v75\ns_waitcnt lgkmcnt(6)\n"
"Qmvav 28,72,80\ns_waitcnt vmcnt(5)\nds_write_b32 v73, v75\nQmvav 28,76,88\nQdv 148,59,1024\ns_waitcnt lgkmcnt(6)\nQa 44,44,104\nQmvav 32,72,112\n"
"Qa 45,45,105\nQa 46,46,106\nQmvav 32,76,120\nQa 47,47,107\ns_waitcnt lgkmcnt(4)\nQa 52,52,136\nQmvav 36,72,144\nQta 64,68,40960\n"
"Qa 53,53,137\nQmvav 36,76,152\nQa 54,54,138\nQa 55,55,139\nQmvav 40,72,176\nQk 23,26,27\nQmvav 40,76,184\nQta 66,68,41984\n"
"s_waitcnt lgkmcnt(2)\nQa 32,32,148\nQmvav 44,72,208\nds_read_b32 v66, v74\nQa 33,33,149\nQmvav 44,76,216\nQa 34,34,150\nQa 35,35,151\n"
"Qmvav 48,72,240\nQta 68,68,49152\nbuffer_store_dwordx4 v[20:23], v60, s[24:27], s68 offen offset:2048 sc1\nQmvav 48,76,248\nQmvav 52,72,84\ns_waitcnt lgkmcnt(1)\nv_cmp_gt_u32 vcc, v75, v66\nQmvav 52,76,116\n"
"s_cbranch_vccnz .Lso2045r1\n.Lro2045r1:\ns_waitcnt lgkmcnt(6)\nQmvaa 28,56,0\nQta 70,68,50176\nQmvaa 28,60,28\nQmvaa 32,56,4\nQmvaa 32,60,32\n"
"Qmvaa 36,56,8\nQta 72,68,57344\ns_nop 3\nQk 28,28,29\nQmvaa 36,60,36\nQk 29,30,31\nQmvaa 40,56,12\nQk 30,32,33\n"
"Qk 31,34,35\nQmvaa 40,60,40\nQta 74,68,58368\nbuffer_store_dwordx4 v[28:31], v60, s[24:27], s68 offen offset:3072 sc1\nQmvaa 44,56,16\nQk 36,36,37\nQk 37,38,39\nQmvaa 44,60,44\n"
"s_add_u32 s68, s64, 8192\nQmvaa 48,56,20\nQta 76,69,0\nQk 38,40,41\nQmvaa 48,60,48\nQk 39,42,43\nQmvaa 52,56,24\nbuffer_store_dwordx4 v[36:39], v60, s[24:27], s68 offen offset:0 sc1\n"
"Qk 44,44,45\nQmvaa 52,60,52\nQta 78,69,1024\nQk 45,46,47\ns_waitcnt lgkmcnt(4)\nQmaaa 200,64,80\nQk 46,48,49\nQk 47,50,51\n"
"Qmaaa 200,68,108\nbuffer_store_dwordx4 v[44:47], v60, s[24:27], s68 offen offset:1024 sc1\nQmaaa 204,64,84\nQk 52,52,53\nQk 53,54,55\nQmaaa 204,68,112\nv_mov_b32 v54, 0\nv_mov_b32 v55, 0\n"
"Qmaaa 208,64,88\nQta 56,69,8192\nbuffer_store_dwordx4 v[52:55], v60, s[24:27], s68 offen offset:2048 sc1\nQmaav 208,68,168\nQmaaa 212,64,92\nQmaav 212,68,200\nQta 58,69,9216\nQmaaa 216,64,96\n"
"Qmaav 216,68,232\nQmaaa 220,64,100\nQta 60,69,16384\nQmaav 220,68,212\nQmaaa 224,64,104\nQmaav 224,68,244\nQta 62,69,17408\ns_waitcnt lgkmcnt(4)\n"
"Qmaav 200,72,80\nQmaav 200,76,88\nQmaav 204,72,112\nQmaav 204,76,120\nQmaav 208,72,144\nQta 64,68,41216\nQmaav 208,76,152\nQmaav 212,72,176\n"
"Qmaav 212,76,184\nQta 66,68,42240\nQmaav 216,72,208\nQmaav 216,76,216\nQmaav 220,72,240\nQta 68,68,49408\nQmaav 220,76,248\nQmaav 224,72,84\n"
"Qmaav 224,76,116\nQta 70,68,50432\ns_waitcnt lgkmcnt(4)\nQmaaa 200,56,0\nQmaaa 200,60,28\nQmaaa 204,56,4\nQmaaa 204,60,32\nQmaaa 208,56,8\n"
"Qta 72,68,57600\nQmaaa 208,60,36\ns_nop 2\nds_write_b128 v58, a[200:203] offset:33792\nQmaaa 212,56,12\nQmaaa 212,60,40\nQta 74,68,58624\nQmaaa 216,56,16\n"
"ds_write_b128 v58, a[204:207] offset:34816\nQmaaa 216,60,44\nQmaaa 220,56,20\nQta 76,69,256\nQmaaa 220,60,48\nds_write_b128 v58, a[208:211] offset:35840\nQmaaa 224,56,24\nQmaaa 224,60,52\n"
"Qta 78,69,1280\ns_waitcnt lgkmcnt(7)\nQmaaa 228,64,80\nds_write_b128 v58, a[212:215] offset:36864\nQmaaa 228,68,108\nQmaaa 232,64,84\ns_waitcnt vmcnt(0)\nbuffer_store_dword v67, v77, s[24:27], 0 offen sc1\n"
"Qmaaa 232,68,112\nds_write_b128 v58, a[216:219] offset:37888\nQmaaa 236,64,88\nQta 56,69,8448\nQmaav 236,68,168\nQmaaa 240,64,92\nds_write_b128 v58, a[220:223] offset:38912\nQmaav 240,68,200\n"
"Qta 58,69,9472\nQmaaa 244,64,96\nQmaav 244,68,232\nds_write_b128 v58, a[224:227] offset:39936\nQmaaa 248,64,100\nQta 60,69,16640\nQmaav 248,68,212\nQmaaa 252,64,104\n"
"Qmaav 252,68,244\nQta 62,69,17664\ns_waitcnt lgkmcnt(8)\nQmaav 228,72,80\nv_add_u32 v75, 1, v75\nQmaav 228,76,88\nds_write_b32 v73, v75\nQmaav 232,72,112\n"
"Qmaav 232,76,120\nQmaav 236,72,144\nQta 64,68,41472\nQmaav 236,76,152\nQmaav 240,72,176\nQmaav 240,76,184\nQta 66,68,42496\nQmaav 244,72,208\n"
"Qmaav 244,76,216\nds_read_b32 v66, v74\nQmaav 248,72,240\nQta 68,68,49664\nQmaav 248,76,248\nQmaav 252,72,84\nbuffer_load_dword v78, v76, s[24:27], 0 offen sc1\nQmaav 252,76,116\n"
"s_waitcnt lgkmcnt(1)\nv_cmp_gt_u32 vcc, v75, v66\ns_waitcnt lgkmcnt(5)\nQmaaa 228,56,0\ns_cbranch_vccnz .Lso2130r1\n.Lro2130r1:\nQmaaa 228,60,28\nQta 70,68,50688\n"
"Qmaaa 232,56,4\nQmaaa 232,60,32\nQdv 180,59,35840\nQmaaa 236,56,8\nQta 72,68,57856\nQmaaa 236,60,36\ns_nop 0\nds_write_b128 v58, a[228:231] offset:0\n"
"Qmaaa 240,56,12\nQdv 104,59,36864\nQmaaa 240,60,40\nQta 74,68,58880\nQmaaa 244,56,16\nds_write_b128 v58, a[232:235] offset:1024\nQmaaa 244,60,44\nQdv 136,59,38912\n"
"Qmaaa 248,56,20\nQta 76,69,512\nQmaaa 248,60,48\nds_write_b128 v58, a[236:239] offset:2048\nQmaaa 252,56,24\nQdv 148,59,33792\ns_waitcnt lgkmcnt(9)\nQa 196,196,180\n"
"Qmaaa 252,60,52\nQta 78,69,1536\nQa 197,197,181\ns_waitcnt lgkmcnt(10)\nQmvaa 132,64,80\nds_write_b128 v58, a[240:243] offset:3072\nQmvaa 132,68,108\nQa 198,198,182\n"
"Qa 199,199,183\nQmvaa 164,64,84\ns_waitcnt lgkmcnt(8)\nQa 228,228,104\nQa 229,229,105\nQmvaa 164,68,112\nds_write_b128 v58, a[244:247] offset:4096\nQmvaa 196,64,88\n"
"Qta 56,69,8704\nQa 230,230,106\nQa 231,231,107\nQmvav 196,68,168\nQdv 180,59,37888\ns_waitcnt lgkmcnt(8)\nQa 140,140,136\nQmvaa 228,64,92\n"
"ds_write_b128 v58, a[248:251] offset:5120\nQmvav 228,68,200\nQta 58,69,9728\nQa 141,141,137\nQmvaa 108,64,96\nQa 142,142,138\nQa 143,143,139\nQmvav 108,68,232\n"
"Qdv 104,59,39936\ns_waitcnt lgkmcnt(8)\nQa 132,132,148\nQmvaa 140,64,100\nQta 60,69,16896\nQa 133,133,149\nQmvav 140,68,212\nds_write_b128 v58, a[252:255] offset:6144\n"
"Qmvaa 172,64,104\nQa 134,134,150\nQa 135,135,151\nQmvav 172,68,244\nQta 62,69,17920\ns_waitcnt lgkmcnt(6)\nQa 108,108,180\ns_waitcnt lgkmcnt(6)\n"
"Qmvav 132,72,80\nQdv 136,59,34816\nv_add_u32 v75, 1, v75\nQmvav 132,76,88\nds_write_b32 v73, v75\nQa 109,109,181\nQmvav 164,72,112\nQa 110,110,182\n"
"Qa 111,111,183\nQmvav 164,76,120\ns_waitcnt lgkmcnt(5)\nQa 172,172,104\nQa 173,173,105\nQmvav 196,72,144\nQta 64,68,41728\nQa 174,174,106\n"
"Qmvav 196,76,152\nQa 175,175,107\nQmvav 228,72,176\nQmvav 228,76,184\nQta 66,68,42752\ns_waitcnt lgkmcnt(3)\nQa 164,164,136\nQmvav 108,72,208\n"
"Qa 165,165,137\nQa 166,166,138\nQmvav 108,76,216\nds_read_b32 v66, v74\nQa 167,167,139\nQmvav 140,72,240\nQta 68,68,49920\nQmvav 140,76,248\n"
"Qmvav 172,72,84\nQmvav 172,76,116\ns_waitcnt lgkmcnt(1)\nv_cmp_gt_u32 vcc, v75, v66\ns_waitcnt lgkmcnt(6)\nQmvaa 132,56,0\ns_cbranch_vccnz .Lso2172r1\n.Lro2172r1:\n"
"Qmvaa 132,60,28\ns_waitcnt vmcnt(0)\nv_sub_u32 v66, v78, v65\nv_cmp_gt_u32 vcc, 1, v66\ns_and_b64 vcc, vcc, s[28:29]\ns_cbranch_scc1 .Lx1_w\n.Lx1_r:\nQmvaa 164,56,4\n"
"s_add_u32 s69, s98, 0\nbuffer_load_dwordx4 a[180:183], v60, s[24:27], s69 offen offset:0 sc1\nQmvaa 164,60,32\nQta 70,68,50944\nQmvaa 196,56,8\nbuffer_load_dwordx4 a[184:187], v60, s[24:27], s69 offen offset:1024 sc1\nQmvaa 196,60,36\nQdv 148,59,2048\n"
"Qmvaa 228,56,12\nbuffer_load_dwordx4 a[188:191], v60, s[24:27], s69 offen offset:2048 sc1\nQmvaa 228,60,40\nQta 72,68,58112\nQmvaa 108,56,16\nbuffer_load_dwordx4 a[192:195], v60, s[24:27], s69 offen offset:3072 sc1\nQmvaa 108,60,44\nQdv 180,59,4096\n"
"Qmvaa 140,56,20\ns_add_u32 s69, s98, 4096\nbuffer_load_dwordx4 a[196:199], v60, s[24:27], s69 offen offset:0 sc1\nQmvaa 140,60,48\nQta 74,68,59136\ns_waitcnt lgkmcnt(3)\nQa 96,96,148\nQmvaa 172,56,24\n"
"buffer_load_dwordx4 a[116:119], v60, s[24:27], s69 offen offset:1024 sc1\nQmvaa 172,60,52\nQdv 104,59,0\nQa 97,97,149\ns_waitcnt lgkmcnt(4)\nQmvaa 204,64,80\nbuffer_load_dwordx4 a[120:123], v60, s[24:27], s69 offen offset:2048 sc1\nQmvaa 204,68,108\n"
"Qta 76,69,768\nQa 98,98,150\nQmvaa 236,64,84\nQdv 136,59,1024\nQa 99,99,151\nQmvaa 236,68,112\ns_waitcnt lgkmcnt(4)\nQa 160,160,180\n"
"Qa 161,161,181\nQmvaa 96,64,88\nQta 78,69,1792\nQa 162,162,182\nQmvav 96,68,168\nQdv 148,59,3072\nQa 163,163,183\nQmvaa 128,64,92\n"
"s_waitcnt lgkmcnt(4)\nQa 204,204,104\nQa 205,205,105\nQmvav 128,68,200\nQta 56,69,8960\nQa 206,206,106\nQmvaa 160,64,96\nQdv 180,59,5120\n"
"Qa 207,207,107\nQmvav 160,68,232\nQmvaa 192,64,100\nQta 58,69,9984\ns_waitcnt lgkmcnt(5)\nQa 236,236,136\nQmvav 192,68,212\nQa 237,237,137\n"
"Qa 238,238,138\nQmvaa 224,64,104\nQa 239,239,139\nQmvav 224,68,244\nQta 60,69,17152\ns_waitcnt lgkmcnt(4)\nQa 128,128,148\ns_waitcnt lgkmcnt(4)\n"
"Qmvav 204,72,80\nQa 129,129,149\nQa 130,130,150\nQmvav 204,76,88\nQa 131,131,151\nQmvav 236,72,112\nQta 62,69,18176\ns_waitcnt lgkmcnt(3)\n"
"Qa 192,192,180\nQmvav 236,76,120\nQa 193,193,181\nQa 194,194,182\nQmvav 96,72,144\nQa 195,195,183\nQmvav 96,76,152\nQmvav 128,72,176\n"
"Qmvav 128,76,184\nQmvav 160,72,208\nQmvav 160,76,216\nQdv 104,59,6144\nQmvav 192,72,240\nQmvav 192,76,248\nv_add_u32 v75, 1, v75\nQmvav 224,72,84\n"
"ds_write_b32 v73, v75\nQmvav 224,76,116\ns_waitcnt lgkmcnt(2)\nQmvaa 204,56,0\nQmvaa 204,60,28\nQmvaa 236,56,4\nQmvaa 236,60,32\ns_waitcnt lgkmcnt(1)\n"
"s_nop 1\nQa 224,224,104\nQa 225,225,105\nQmvaa 96,56,8\nQa 226,226,106\nQa 227,227,107\nQmvaa 96,60,36\nQmvaa 128,56,12\n"
"ds_read_b32 v66, v74\nQmvaa 128,60,40\nQmvaa 160,56,16\nQmvaa 160,60,44\nQmvaa 192,56,20\ns_waitcnt lgkmcnt(0)\nv_cmp_gt_u32 vcc, v75, v66\nQmvaa 192,60,48\n"
"s_cbranch_vccnz .Lso2225r1\n.Lro2225r1:\nQmvaa 224,56,24\nQmvaa 224,60,52\ns_waitcnt vmcnt(6)\nQmvaa 132,174,180\nQmvaa 164,172,180\ns_waitcnt vmcnt(5)\n"
"Qmvaa 196,174,184\nQmvaa 228,172,184\ns_nop 3\nQk 132,132,133\nQk 133,134,135\nds_write_b64 v56, v[132:133] offset:0\ns_waitcnt vmcnt(4)\nQmvaa 108,174,188\n"
"Qk 164,164,165\nQk 165,166,167\nds_write_b64 v56, v[164:165] offset:3328\nQmvaa 140,172,188\nQk 196,196,197\nQk 197,198,199\nds_write_b64 v56, v[196:197] offset:6656\ns_waitcnt vmcnt(3)\n"
"Qmvaa 172,174,192\nQk 228,228,229\nQk 229,230,231\n.Lpg25:\ns_cbranch_execz .Ltr8\nds_write_b64 v56, v[228:229] offset:9984\nQmvaa 204,172,192\nQk 108,108,109\n"
"Qk 109,110,111\nds_write_b64 v56, v[108:109] offset:13312\ns_waitcnt vmcnt(2)\nQmvaa 236,174,196\nQk 140,140,141\nQk 141,142,143\nds_write_b64 v56, v[140:141] offset:16640\nQmvaa 96,172,196\n"
"Qk 172,172,173\nQk 173,174,175\nds_write_b64 v56, v[172:173] offset:19968\ns_waitcnt vmcnt(1)\nQmvaa 128,174,116\nQk 204,204,205\nQk 205,206,207\nds_write_b64 v56, v[204:205] offset:32\n"
"Qmvaa 160,172,116\nQk 236,236,237\nQk 237,238,239\nds_write_b64 v56, v[236:237] offset:3360\ns_waitcnt vmcnt(0)\nQmvaa 192,174,120\nQk 96,96,97\nQk 97,98,99\n"
"ds_write_b64 v56, v[96:97] offset:6688\nQmvaa 224,172,120\nQk 128,128,129\nQk 129,130,131\nds_write_b64 v56, v[128:129] offset:10016\nQk 160,160,161\nQk 161,162,163\nds_write_b64 v56, v[160:161] offset:13344\n"
"Qk 192,192,193\nQk 193,194,195\nds_write_b64 v56, v[192:193] offset:16672\nQk 224,224,225\nQk 225,226,227\nds_write_b64 v56, v[224:225] offset:20000\ns_waitcnt lgkmcnt(0)\ns_barrier\n"
"Qda 0,57,0\nQda 4,57,3328\nQda 8,57,6656\nQda 12,57,9984\nQda 16,57,13312\nQda 20,57,16640\nQda 24,57,19968\ns_mov_b32 s68, 0\n"
"s_waitcnt lgkmcnt(6)\nbuffer_store_dwordx4 a[0:3], v64, s[40:43], s68 offen offset:0 sc1\ns_mov_b32 s68, 40960\ns_waitcnt lgkmcnt(5)\nbuffer_store_dwordx4 a[4:7], v64, s[40:43], s68 offen offset:0 sc1\ns_mov_b32 s68, 81920\ns_waitcnt lgkmcnt(4)\nbuffer_store_dwordx4 a[8:11], v64, s[40:43], s68 offen offset:0 sc1\n"
"s_mov_b32 s68, 122880\ns_waitcnt lgkmcnt(3)\nbuffer_store_dwordx4 a[12:15], v64, s[40:43], s68 offen offset:0 sc1\ns_mov_b32 s68, 163840\ns_waitcnt lgkmcnt(2)\nbuffer_store_dwordx4 a[16:19], v64, s[40:43], s68 offen offset:0 sc1\ns_mov_b32 s68, 204800\ns_waitcnt lgkmcnt(1)\n"
"buffer_store_dwordx4 a[20:23], v64, s[40:43], s68 offen offset:0 sc1\ns_mov_b32 s68, 245760\ns_waitcnt lgkmcnt(0)\nbuffer_store_dwordx4 a[24:27], v64, s[40:43], s68 offen offset:0 sc1\n.Lfin:\n.Lpg26:\ns_cbranch_execz .Ltr8\ns_waitcnt vmcnt(0) lgkmcnt(0)\n"
"s_branch .Lexit\n.Lend:\n.Lfb:\ns_waitcnt vmcnt(0) lgkmcnt(0)\nv_lshrrev_b32 v52, 4, v60\nv_and_b32 v53, 15, v52\nv_lshrrev_b32 v52, 4, v52\nv_lshlrev_b32 v66, 8, v53\n"
"v_lshl_add_u32 v66, v52, 4, v66\nv_add_u32 v66, 1024, v66\nv_lshlrev_b32 v67, 10, v52\nv_lshl_add_u32 v67, v53, 1, v67\nv_add_u32 v67, 1024, v67\nv_mov_b32 v52, 0x3f803f80\nv_accvgpr_write_b32 a204, v52\nv_accvgpr_write_b32 a205, v52\n"
"v_accvgpr_write_b32 a206, v52\nv_accvgpr_write_b32 a207, v52\nv_mov_b32 v24, 0xff800000\nv_mov_b32 v28, 0xff800000\nv_mov_b32 v32, 0xff800000\nv_mov_b32 v36, 0xff800000\nv_mov_b32 v40, 0xff800000\nv_mov_b32 v44, 0xff800000\n"
"v_mov_b32 v48, 0xff800000\ns_lshl_b32 s89, s67, 21\ns_lshl_b32 s68, s93, 18\ns_add_u32 s89, s89, s68\ns_mov_b32 s66, 0\n.Lfb1:\ns_add_u32 s68, s89, 4096\nbuffer_load_dwordx4 a[112:115], v66, s[40:43], s89 offen offset:0\n"
"buffer_load_dwordx4 a[116:119], v66, s[40:43], s89 offen offset:64\nbuffer_load_dwordx4 a[120:123], v66, s[40:43], s89 offen offset:128\nbuffer_load_dwordx4 a[124:127], v66, s[40:43], s89 offen offset:192\nbuffer_load_dwordx4 a[128:131], v66, s[40:43], s68 offen offset:0\nbuffer_load_dwordx4 a[132:135], v66, s[40:43], s68 offen offset:64\nbuffer_load_dwordx4 a[136:139], v66, s[40:43], s68 offen offset:128\nbuffer_load_dwordx4 a[140:143], v66, s[40:43], s68 offen offset:192\ns_waitcnt vmcnt(0)\n"
"Qzvaa 0,112,0\nQmvaa 0,116,4\nQmvaa 0,120,8\nQmvaa 0,124,12\nQzvaa 4,128,0\nQmvaa 4,132,4\nQmvaa 4,136,8\nQmvaa 4,140,12\n"
"s_nop 7\nv_max3_f32 v24, v24, v0, v1\nv_max3_f32 v24, v24, v2, v3\nv_max3_f32 v24, v24, v4, v5\nv_max3_f32 v24, v24, v6, v7\nQzvaa 0,112,16\nQmvaa 0,116,20\nQmvaa 0,120,24\n"
"Qmvaa 0,124,28\nQzvaa 4,128,16\nQmvaa 4,132,20\nQmvaa 4,136,24\nQmvaa 4,140,28\ns_nop 7\nv_max3_f32 v28, v28, v0, v1\nv_max3_f32 v28, v28, v2, v3\n"
"v_max3_f32 v28, v28, v4, v5\nv_max3_f32 v28, v28, v6, v7\nQzvaa 0,112,32\nQmvaa 0,116,36\nQmvaa 0,120,40\nQmvaa 0,124,44\nQzvaa 4,128,32\nQmvaa 4,132,36\n"
"Qmvaa 4,136,40\nQmvaa 4,140,44\ns_nop 7\nv_max3_f32 v32, v32, v0, v1\nv_max3_f32 v32, v32, v2, v3\nv_max3_f32 v32, v32, v4, v5\nv_max3_f32 v32, v32, v6, v7\nQzvaa 0,112,48\n"
"Qmvaa 0,116,52\nQmvaa 0,120,56\nQmvaa 0,124,60\nQzvaa 4,128,48\nQmvaa 4,132,52\nQmvaa 4,136,56\nQmvaa 4,140,60\ns_nop 7\n"
"v_max3_f32 v36, v36, v0, v1\nv_max3_f32 v36, v36, v2, v3\nv_max3_f32 v36, v36, v4, v5\nv_max3_f32 v36, v36, v6, v7\nQzvaa 0,112,64\nQmvaa 0,116,68\nQmvaa 0,120,72\nQmvaa 0,124,76\n"
"Qzvaa 4,128,64\nQmvaa 4,132,68\nQmvaa 4,136,72\nQmvaa 4,140,76\ns_nop 7\nv_max3_f32 v40, v40, v0, v1\nv_max3_f32 v40, v40, v2, v3\nv_max3_f32 v40, v40, v4, v5\n"
"v_max3_f32 v40, v40, v6, v7\nQzvaa 0,112,80\nQmvaa 0,116,84\nQmvaa 0,120,88\nQmvaa 0,124,92\nQzvaa 4,128,80\nQmvaa 4,132,84\nQmvaa 4,136,88\n"
"Qmvaa 4,140,92\ns_nop 7\nv_max3_f32 v44, v44, v0, v1\nv_max3_f32 v44, v44, v2, v3\nv_max3_f32 v44, v44, v4, v5\nv_max3_f32 v44, v44, v6, v7\nQzvaa 0,112,96\nQmvaa 0,116,100\n"
"Qmvaa 0,120,104\nQmvaa 0,124,108\nQzvaa 4,128,96\nQmvaa 4,132,100\nQmvaa 4,136,104\nQmvaa 4,140,108\ns_nop 7\nv_max3_f32 v48, v48, v0, v1\n"
"v_max3_f32 v48, v48, v2, v3\nv_max3_f32 v48, v48, v4, v5\nv_max3_f32 v48, v48, v6, v7\ns_add_u32 s89, s89, 8192\ns_add_u32 s66, s66, 1\ns_cmp_lt_u32 s66, 32\ns_cbranch_scc1 .Lfb1\nv_mov_b32 v25, v24\n"
"s_nop 1\nv_permlane32_swap_b32 v24, v25\nv_max_f32 v24, v24, v25\nv_mov_b32 v25, v24\ns_nop 1\nv_permlane16_swap_b32 v24, v25\nv_max_f32 v24, v24, v25\nv_sub_f32 v24, 0, v24\n"
"v_mov_b32 v25, v24\nv_mov_b32 v26, v24\nv_mov_b32 v27, v24\nv_mov_b32 v29, v28\ns_nop 1\nv_permlane32_swap_b32 v28, v29\nv_max_f32 v28, v28, v29\nv_mov_b32 v29, v28\n"
"s_nop 1\nv_permlane16_swap_b32 v28, v29\nv_max_f32 v28, v28, v29\nv_sub_f32 v28, 0, v28\nv_mov_b32 v29, v28\nv_mov_b32 v30, v28\nv_mov_b32 v31, v28\nv_mov_b32 v33, v32\n"
"s_nop 1\nv_permlane32_swap_b32 v32, v33\nv_max_f32 v32, v32, v33\nv_mov_b32 v33, v32\ns_nop 1\nv_permlane16_swap_b32 v32, v33\nv_max_f32 v32, v32, v33\nv_sub_f32 v32, 0, v32\n"
"v_mov_b32 v33, v32\nv_mov_b32 v34, v32\nv_mov_b32 v35, v32\nv_mov_b32 v37, v36\ns_nop 1\nv_permlane32_swap_b32 v36, v37\nv_max_f32 v36, v36, v37\nv_mov_b32 v37, v36\n"
"s_nop 1\nv_permlane16_swap_b32 v36, v37\nv_max_f32 v36, v36, v37\nv_sub_f32 v36, 0, v36\nv_mov_b32 v37, v36\nv_mov_b32 v38, v36\nv_mov_b32 v39, v36\nv_mov_b32 v41, v40\n"
"s_nop 1\nv_permlane32_swap_b32 v40, v41\nv_max_f32 v40, v40, v41\nv_mov_b32 v41, v40\ns_nop 1\nv_permlane16_swap_b32 v40, v41\nv_max_f32 v40, v40, v41\nv_sub_f32 v40, 0, v40\n"
"v_mov_b32 v41, v40\nv_mov_b32 v42, v40\nv_mov_b32 v43, v40\nv_mov_b32 v45, v44\ns_nop 1\nv_permlane32_swap_b32 v44, v45\nv_max_f32 v44, v44, v45\nv_mov_b32 v45, v44\n"
"s_nop 1\nv_permlane16_swap_b32 v44, v45\nv_max_f32 v44, v44, v45\nv_sub_f32 v44, 0, v44\nv_mov_b32 v45, v44\nv_mov_b32 v46, v44\nv_mov_b32 v47, v44\nv_mov_b32 v49, v48\n"
"s_nop 1\nv_permlane32_swap_b32 v48, v49\nv_max_f32 v48, v48, v49\nv_mov_b32 v49, v48\ns_nop 1\nv_permlane16_swap_b32 v48, v49\nv_max_f32 v48, v48, v49\nv_sub_f32 v48, 0, v48\n"
"v_mov_b32 v49, v48\nv_mov_b32 v50, v48\nv_mov_b32 v51, v48\nv_mov_b32 v52, 0\nv_mov_b32 v80, 0\nv_mov_b32 v81, 0\nv_mov_b32 v82, 0\nv_mov_b32 v83, 0\n"
"v_mov_b32 v84, 0\nv_mov_b32 v85, 0\nv_mov_b32 v86, 0\nv_mov_b32 v87, 0\nv_mov_b32 v88, 0\nv_mov_b32 v89, 0\nv_mov_b32 v90, 0\nv_mov_b32 v91, 0\n"
"v_mov_b32 v92, 0\nv_mov_b32 v93, 0\nv_mov_b32 v94, 0\nv_mov_b32 v95, 0\nv_mov_b32 v96, 0\nv_mov_b32 v97, 0\nv_mov_b32 v98, 0\nv_mov_b32 v99, 0\n"
"v_mov_b32 v100, 0\nv_mov_b32 v101, 0\nv_mov_b32 v102, 0\nv_mov_b32 v103, 0\nv_mov_b32 v104, 0\nv_mov_b32 v105, 0\nv_mov_b32 v106, 0\nv_mov_b32 v107, 0\n"
"v_mov_b32 v108, 0\nv_mov_b32 v109, 0\nv_mov_b32 v110, 0\nv_mov_b32 v111, 0\nv_accvgpr_write_b32 a176, v52\nv_accvgpr_write_b32 a177, v52\nv_accvgpr_write_b32 a178, v52\nv_accvgpr_write_b32 a179, v52\n"
"v_mov_b32 v112, 0\nv_mov_b32 v113, 0\nv_mov_b32 v114, 0\nv_mov_b32 v115, 0\nv_mov_b32 v116, 0\nv_mov_b32 v117, 0\nv_mov_b32 v118, 0\nv_mov_b32 v119, 0\n"
"v_mov_b32 v120, 0\nv_mov_b32 v121, 0\nv_mov_b32 v122, 0\nv_mov_b32 v123, 0\nv_mov_b32 v124, 0\nv_mov_b32 v125, 0\nv_mov_b32 v126, 0\nv_mov_b32 v127, 0\n"
"v_mov_b32 v128, 0\nv_mov_b32 v129, 0\nv_mov_b32 v130, 0\nv_mov_b32 v131, 0\nv_mov_b32 v132, 0\nv_mov_b32 v133, 0\nv_mov_b32 v134, 0\nv_mov_b32 v135, 0\n"
"v_mov_b32 v136, 0\nv_mov_b32 v137, 0\nv_mov_b32 v138, 0\nv_mov_b32 v139, 0\nv_mov_b32 v140, 0\nv_mov_b32 v141, 0\nv_mov_b32 v142, 0\nv_mov_b32 v143, 0\n"
"v_accvgpr_write_b32 a180, v52\nv_accvgpr_write_b32 a181, v52\nv_accvgpr_write_b32 a182, v52\nv_accvgpr_write_b32 a183, v52\nv_mov_b32 v144, 0\nv_mov_b32 v145, 0\nv_mov_b32 v146, 0\nv_mov_b32 v147, 0\n"
"v_mov_b32 v148, 0\nv_mov_b32 v149, 0\nv_mov_b32 v150, 0\nv_mov_b32 v151, 0\nv_mov_b32 v152, 0\nv_mov_b32 v153, 0\nv_mov_b32 v154, 0\nv_mov_b32 v155, 0\n"
"v_mov_b32 v156, 0\nv_mov_b32 v157, 0\nv_mov_b32 v158, 0\nv_mov_b32 v159, 0\nv_mov_b32 v160, 0\nv_mov_b32 v161, 0\nv_mov_b32 v162, 0\nv_mov_b32 v163, 0\n"
"v_mov_b32 v164, 0\nv_mov_b32 v165, 0\nv_mov_b32 v166, 0\nv_mov_b32 v167, 0\nv_mov_b32 v168, 0\nv_mov_b32 v169, 0\nv_mov_b32 v170, 0\nv_mov_b32 v171, 0\n"
"v_mov_b32 v172, 0\nv_mov_b32 v173, 0\nv_mov_b32 v174, 0\nv_mov_b32 v175, 0\nv_accvgpr_write_b32 a184, v52\nv_accvgpr_write_b32 a185, v52\nv_accvgpr_write_b32 a186, v52\nv_accvgpr_write_b32 a187, v52\n"
"v_mov_b32 v176, 0\nv_mov_b32 v177, 0\nv_mov_b32 v178, 0\nv_mov_b32 v179, 0\nv_mov_b32 v180, 0\nv_mov_b32 v181, 0\nv_mov_b32 v182, 0\nv_mov_b32 v183, 0\n"
"v_mov_b32 v184, 0\nv_mov_b32 v185, 0\nv_mov_b32 v186, 0\nv_mov_b32 v187, 0\nv_mov_b32 v188, 0\nv_mov_b32 v189, 0\nv_mov_b32 v190, 0\nv_mov_b32 v191, 0\n"
"v_mov_b32 v192, 0\nv_mov_b32 v193, 0\nv_mov_b32 v194, 0\nv_mov_b32 v195, 0\nv_mov_b32 v196, 0\nv_mov_b32 v197, 0\nv_mov_b32 v198, 0\nv_mov_b32 v199, 0\n"
"v_mov_b32 v200, 0\nv_mov_b32 v201, 0\nv_mov_b32 v202, 0\nv_mov_b32 v203, 0\nv_mov_b32 v204, 0\nv_mov_b32 v205, 0\nv_mov_b32 v206, 0\nv_mov_b32 v207, 0\n"
"v_accvgpr_write_b32 a188, v52\nv_accvgpr_write_b32 a189, v52\nv_accvgpr_write_b32 a190, v52\nv_accvgpr_write_b32 a191, v52\nv_mov_b32 v208, 0\nv_mov_b32 v209, 0\nv_mov_b32 v210, 0\nv_mov_b32 v211, 0\n"
"v_mov_b32 v212, 0\nv_mov_b32 v213, 0\nv_mov_b32 v214, 0\nv_mov_b32 v215, 0\nv_mov_b32 v216, 0\nv_mov_b32 v217, 0\nv_mov_b32 v218, 0\nv_mov_b32 v219, 0\n"
"v_mov_b32 v220, 0\nv_mov_b32 v221, 0\nv_mov_b32 v222, 0\nv_mov_b32 v223, 0\nv_mov_b32 v224, 0\nv_mov_b32 v225, 0\nv_mov_b32 v226, 0\nv_mov_b32 v227, 0\n"
"v_mov_b32 v228, 0\nv_mov_b32 v229, 0\nv_mov_b32 v230, 0\nv_mov_b32 v231, 0\nv_mov_b32 v232, 0\nv_mov_b32 v233, 0\nv_mov_b32 v234, 0\nv_mov_b32 v235, 0\n"
"v_mov_b32 v236, 0\nv_mov_b32 v237, 0\nv_mov_b32 v238, 0\nv_mov_b32 v239, 0\nv_accvgpr_write_b32 a192, v52\nv_accvgpr_write_b32 a193, v52\nv_accvgpr_write_b32 a194, v52\nv_accvgpr_write_b32 a195, v52\n"
"v_mov_b32 v240, 0\nv_mov_b32 v241, 0\nv_mov_b32 v242, 0\nv_mov_b32 v243, 0\nv_mov_b32 v244, 0\nv_mov_b32 v245, 0\nv_mov_b32 v246, 0\nv_mov_b32 v247, 0\n"
"v_mov_b32 v248, 0\nv_mov_b32 v249, 0\nv_mov_b32 v250, 0\nv_mov_b32 v251, 0\nv_mov_b32 v252, 0\nv_mov_b32 v253, 0\nv_mov_b32 v254, 0\nv_mov_b32 v255, 0\n"
"v_accvgpr_write_b32 a208, v52\nv_accvgpr_write_b32 a209, v52\nv_accvgpr_write_b32 a210, v52\nv_accvgpr_write_b32 a211, v52\nv_accvgpr_write_b32 a212, v52\nv_accvgpr_write_b32 a213, v52\nv_accvgpr_write_b32 a214, v52\nv_accvgpr_write_b32 a215, v52\n"
"v_accvgpr_write_b32 a216, v52\nv_accvgpr_write_b32 a217, v52\nv_accvgpr_write_b32 a218, v52\nv_accvgpr_write_b32 a219, v52\nv_accvgpr_write_b32 a220, v52\nv_accvgpr_write_b32 a221, v52\nv_accvgpr_write_b32 a222, v52\nv_accvgpr_write_b32 a223, v52\n"
"v_accvgpr_write_b32 a196, v52\nv_accvgpr_write_b32 a197, v52\nv_accvgpr_write_b32 a198, v52\nv_accvgpr_write_b32 a199, v52\nv_accvgpr_write_b32 a224, v52\nv_accvgpr_write_b32 a225, v52\nv_accvgpr_write_b32 a226, v52\nv_accvgpr_write_b32 a227, v52\n"
"v_accvgpr_write_b32 a228, v52\nv_accvgpr_write_b32 a229, v52\nv_accvgpr_write_b32 a230, v52\nv_accvgpr_write_b32 a231, v52\nv_accvgpr_write_b32 a232, v52\nv_accvgpr_write_b32 a233, v52\nv_accvgpr_write_b32 a234, v52\nv_accvgpr_write_b32 a235, v52\n"
"v_accvgpr_write_b32 a236, v52\nv_accvgpr_write_b32 a237, v52\nv_accvgpr_write_b32 a238, v52\nv_accvgpr_write_b32 a239, v52\nv_accvgpr_write_b32 a240, v52\nv_accvgpr_write_b32 a241, v52\nv_accvgpr_write_b32 a242, v52\nv_accvgpr_write_b32 a243, v52\n"
"v_accvgpr_write_b32 a244, v52\nv_accvgpr_write_b32 a245, v52\nv_accvgpr_write_b32 a246, v52\nv_accvgpr_write_b32 a247, v52\nv_accvgpr_write_b32 a248, v52\nv_accvgpr_write_b32 a249, v52\nv_accvgpr_write_b32 a250, v52\nv_accvgpr_write_b32 a251, v52\n"
"v_accvgpr_write_b32 a252, v52\nv_accvgpr_write_b32 a253, v52\nv_accvgpr_write_b32 a254, v52\nv_accvgpr_write_b32 a255, v52\nv_accvgpr_write_b32 a200, v52\nv_accvgpr_write_b32 a201, v52\nv_accvgpr_write_b32 a202, v52\nv_accvgpr_write_b32 a203, v52\n"
"s_lshl_b32 s89, s67, 21\ns_lshl_b32 s68, s93, 18\ns_add_u32 s89, s89, s68\ns_mov_b32 s66, 0\n.Lfb2:\ns_add_u32 s68, s89, 4096\nbuffer_load_dwordx4 a[112:115], v66, s[40:43], s89 offen offset:0\nbuffer_load_dwordx4 a[116:119], v66, s[40:43], s89 offen offset:64\n"
"buffer_load_dwordx4 a[120:123], v66, s[40:43], s89 offen offset:128\nbuffer_load_dwordx4 a[124:127], v66, s[40:43], s89 offen offset:192\nbuffer_load_dwordx4 a[128:131], v66, s[40:43], s68 offen offset:0\nbuffer_load_dwordx4 a[132:135], v66, s[40:43], s68 offen offset:64\nbuffer_load_dwordx4 a[136:139], v66, s[40:43], s68 offen offset:128\nbuffer_load_dwordx4 a[140:143], v66, s[40:43], s68 offen offset:192\nbuffer_load_ushort v8, v67, s[44:47], s89 offen offset:0\nbuffer_load_ushort v9, v67, s[44:47], s89 offen offset:256\n"
"buffer_load_ushort v10, v67, s[44:47], s89 offen offset:512\nbuffer_load_ushort v11, v67, s[44:47], s89 offen offset:768\nbuffer_load_ushort v12, v67, s[44:47], s68 offen offset:0\nbuffer_load_ushort v13, v67, s[44:47], s68 offen offset:256\nbuffer_load_ushort v14, v67, s[44:47], s68 offen offset:512\nbuffer_load_ushort v15, v67, s[44:47], s68 offen offset:768\ns_waitcnt vmcnt(0)\nv_lshl_or_b32 v8, v9, 16, v8\n"
"v_accvgpr_write_b32 a144, v8\nv_lshl_or_b32 v10, v11, 16, v10\nv_accvgpr_write_b32 a145, v10\nv_lshl_or_b32 v12, v13, 16, v12\nv_accvgpr_write_b32 a146, v12\nv_lshl_or_b32 v14, v15, 16, v14\nv_accvgpr_write_b32 a147, v14\nbuffer_load_ushort v8, v67, s[44:47], s89 offen offset:32\n"
"buffer_load_ushort v9, v67, s[44:47], s89 offen offset:288\nbuffer_load_ushort v10, v67, s[44:47], s89 offen offset:544\nbuffer_load_ushort v11, v67, s[44:47], s89 offen offset:800\nbuffer_load_ushort v12, v67, s[44:47], s68 offen offset:32\nbuffer_load_ushort v13, v67, s[44:47], s68 offen offset:288\nbuffer_load_ushort v14, v67, s[44:47], s68 offen offset:544\nbuffer_load_ushort v15, v67, s[44:47], s68 offen offset:800\ns_waitcnt vmcnt(0)\n"
"v_lshl_or_b32 v8, v9, 16, v8\nv_accvgpr_write_b32 a148, v8\nv_lshl_or_b32 v10, v11, 16, v10\nv_accvgpr_write_b32 a149, v10\nv_lshl_or_b32 v12, v13, 16, v12\nv_accvgpr_write_b32 a150, v12\nv_lshl_or_b32 v14, v15, 16, v14\nv_accvgpr_write_b32 a151, v14\n"
"buffer_load_ushort v8, v67, s[44:47], s89 offen offset:64\nbuffer_load_ushort v9, v67, s[44:47], s89 offen offset:320\nbuffer_load_ushort v10, v67, s[44:47], s89 offen offset:576\nbuffer_load_ushort v11, v67, s[44:47], s89 offen offset:832\nbuffer_load_ushort v12, v67, s[44:47], s68 offen offset:64\nbuffer_load_ushort v13, v67, s[44:47], s68 offen offset:320\nbuffer_load_ushort v14, v67, s[44:47], s68 offen offset:576\nbuffer_load_ushort v15, v67, s[44:47], s68 offen offset:832\n"
"s_waitcnt vmcnt(0)\nv_lshl_or_b32 v8, v9, 16, v8\nv_accvgpr_write_b32 a152, v8\nv_lshl_or_b32 v10, v11, 16, v10\nv_accvgpr_write_b32 a153, v10\nv_lshl_or_b32 v12, v13, 16, v12\nv_accvgpr_write_b32 a154, v12\nv_lshl_or_b32 v14, v15, 16, v14\n"
"v_accvgpr_write_b32 a155, v14\nbuffer_load_ushort v8, v67, s[44:47], s89 offen offset:96\nbuffer_load_ushort v9, v67, s[44:47], s89 offen offset:352\nbuffer_load_ushort v10, v67, s[44:47], s89 offen offset:608\nbuffer_load_ushort v11, v67, s[44:47], s89 offen offset:864\nbuffer_load_ushort v12, v67, s[44:47], s68 offen offset:96\nbuffer_load_ushort v13, v67, s[44:47], s68 offen offset:352\nbuffer_load_ushort v14, v67, s[44:47], s68 offen offset:608\n"
"buffer_load_ushort v15, v67, s[44:47], s68 offen offset:864\ns_waitcnt vmcnt(0)\nv_lshl_or_b32 v8, v9, 16, v8\nv_accvgpr_write_b32 a156, v8\nv_lshl_or_b32 v10, v11, 16, v10\nv_accvgpr_write_b32 a157, v10\nv_lshl_or_b32 v12, v13, 16, v12\nv_accvgpr_write_b32 a158, v12\n"
"v_lshl_or_b32 v14, v15, 16, v14\nv_accvgpr_write_b32 a159, v14\nbuffer_load_ushort v8, v67, s[44:47], s89 offen offset:128\nbuffer_load_ushort v9, v67, s[44:47], s89 offen offset:384\nbuffer_load_ushort v10, v67, s[44:47], s89 offen offset:640\nbuffer_load_ushort v11, v67, s[44:47], s89 offen offset:896\nbuffer_load_ushort v12, v67, s[44:47], s68 offen offset:128\nbuffer_load_ushort v13, v67, s[44:47], s68 offen offset:384\n"
"buffer_load_ushort v14, v67, s[44:47], s68 offen offset:640\nbuffer_load_ushort v15, v67, s[44:47], s68 offen offset:896\ns_waitcnt vmcnt(0)\nv_lshl_or_b32 v8, v9, 16, v8\nv_accvgpr_write_b32 a160, v8\nv_lshl_or_b32 v10, v11, 16, v10\nv_accvgpr_write_b32 a161, v10\nv_lshl_or_b32 v12, v13, 16, v12\n"
"v_accvgpr_write_b32 a162, v12\nv_lshl_or_b32 v14, v15, 16, v14\nv_accvgpr_write_b32 a163, v14\nbuffer_load_ushort v8, v67, s[44:47], s89 offen offset:160\nbuffer_load_ushort v9, v67, s[44:47], s89 offen offset:416\nbuffer_load_ushort v10, v67, s[44:47], s89 offen offset:672\nbuffer_load_ushort v11, v67, s[44:47], s89 offen offset:928\nbuffer_load_ushort v12, v67, s[44:47], s68 offen offset:160\n"
"buffer_load_ushort v13, v67, s[44:47], s68 offen offset:416\nbuffer_load_ushort v14, v67, s[44:47], s68 offen offset:672\nbuffer_load_ushort v15, v67, s[44:47], s68 offen offset:928\ns_waitcnt vmcnt(0)\nv_lshl_or_b32 v8, v9, 16, v8\nv_accvgpr_write_b32 a164, v8\nv_lshl_or_b32 v10, v11, 16, v10\nv_accvgpr_write_b32 a165, v10\n"
"v_lshl_or_b32 v12, v13, 16, v12\nv_accvgpr_write_b32 a166, v12\nv_lshl_or_b32 v14, v15, 16, v14\nv_accvgpr_write_b32 a167, v14\nbuffer_load_ushort v8, v67, s[44:47], s89 offen offset:192\nbuffer_load_ushort v9, v67, s[44:47], s89 offen offset:448\nbuffer_load_ushort v10, v67, s[44:47], s89 offen offset:704\nbuffer_load_ushort v11, v67, s[44:47], s89 offen offset:960\n"
"buffer_load_ushort v12, v67, s[44:47], s68 offen offset:192\nbuffer_load_ushort v13, v67, s[44:47], s68 offen offset:448\nbuffer_load_ushort v14, v67, s[44:47], s68 offen offset:704\nbuffer_load_ushort v15, v67, s[44:47], s68 offen offset:960\ns_waitcnt vmcnt(0)\nv_lshl_or_b32 v8, v9, 16, v8\nv_accvgpr_write_b32 a168, v8\nv_lshl_or_b32 v10, v11, 16, v10\n"
"v_accvgpr_write_b32 a169, v10\nv_lshl_or_b32 v12, v13, 16, v12\nv_accvgpr_write_b32 a170, v12\nv_lshl_or_b32 v14, v15, 16, v14\nv_accvgpr_write_b32 a171, v14\nbuffer_load_ushort v8, v67, s[44:47], s89 offen offset:224\nbuffer_load_ushort v9, v67, s[44:47], s89 offen offset:480\nbuffer_load_ushort v10, v67, s[44:47], s89 offen offset:736\n"
"buffer_load_ushort v11, v67, s[44:47], s89 offen offset:992\nbuffer_load_ushort v12, v67, s[44:47], s68 offen offset:224\nbuffer_load_ushort v13, v67, s[44:47], s68 offen offset:480\nbuffer_load_ushort v14, v67, s[44:47], s68 offen offset:736\nbuffer_load_ushort v15, v67, s[44:47], s68 offen offset:992\ns_waitcnt vmcnt(0)\nv_lshl_or_b32 v8, v9, 16, v8\nv_accvgpr_write_b32 a172, v8\n"
"v_lshl_or_b32 v10, v11, 16, v10\nv_accvgpr_write_b32 a173, v10\nv_lshl_or_b32 v12, v13, 16, v12\nv_accvgpr_write_b32 a174, v12\nv_lshl_or_b32 v14, v15, 16, v14\nv_accvgpr_write_b32 a175, v14\nQcvaa 0,112,0,24\nQmvaa 0,116,4\n"
"Qmvaa 0,120,8\nQmvaa 0,124,12\nQcvaa 4,128,0,24\nQmvaa 4,132,4\nQmvaa 4,136,8\nQmvaa 4,140,12\ns_nop 7\nQe 0\n"
"Qe 1\nQe 2\nQe 3\nQe 4\nQe 5\nQe 6\nQe 7\ns_nop 0\n"
"Qk 16,0,1\nQk 17,2,3\nQk 18,4,5\nQk 19,6,7\ns_nop 1\nQmvva 80,16,144\nQmvva 84,16,148\nQmvva 88,16,152\n"
"Qmvva 92,16,156\nQmvva 96,16,160\nQmvva 100,16,164\nQmvva 104,16,168\nQmvva 108,16,172\nQmaav 176,204,16\nQcvaa 0,112,16,28\nQmvaa 0,116,20\n"
"Qmvaa 0,120,24\nQmvaa 0,124,28\nQcvaa 4,128,16,28\nQmvaa 4,132,20\nQmvaa 4,136,24\nQmvaa 4,140,28\ns_nop 7\nQe 0\n"
"Qe 1\nQe 2\nQe 3\nQe 4\nQe 5\nQe 6\nQe 7\ns_nop 0\n"
"Qk 16,0,1\nQk 17,2,3\nQk 18,4,5\nQk 19,6,7\ns_nop 1\nQmvva 112,16,144\nQmvva 116,16,148\nQmvva 120,16,152\n"
"Qmvva 124,16,156\nQmvva 128,16,160\nQmvva 132,16,164\nQmvva 136,16,168\nQmvva 140,16,172\nQmaav 180,204,16\nQcvaa 0,112,32,32\nQmvaa 0,116,36\n"
"Qmvaa 0,120,40\nQmvaa 0,124,44\nQcvaa 4,128,32,32\nQmvaa 4,132,36\nQmvaa 4,136,40\nQmvaa 4,140,44\ns_nop 7\nQe 0\n"
"Qe 1\nQe 2\nQe 3\nQe 4\nQe 5\nQe 6\nQe 7\ns_nop 0\n"
"Qk 16,0,1\nQk 17,2,3\nQk 18,4,5\nQk 19,6,7\ns_nop 1\nQmvva 144,16,144\nQmvva 148,16,148\nQmvva 152,16,152\n"
"Qmvva 156,16,156\nQmvva 160,16,160\nQmvva 164,16,164\nQmvva 168,16,168\nQmvva 172,16,172\nQmaav 184,204,16\nQcvaa 0,112,48,36\nQmvaa 0,116,52\n"
"Qmvaa 0,120,56\nQmvaa 0,124,60\nQcvaa 4,128,48,36\nQmvaa 4,132,52\nQmvaa 4,136,56\nQmvaa 4,140,60\ns_nop 7\nQe 0\n"
"Qe 1\nQe 2\nQe 3\nQe 4\nQe 5\nQe 6\nQe 7\ns_nop 0\n"
"Qk 16,0,1\nQk 17,2,3\nQk 18,4,5\nQk 19,6,7\ns_nop 1\nQmvva 176,16,144\nQmvva 180,16,148\nQmvva 184,16,152\n"
"Qmvva 188,16,156\nQmvva 192,16,160\nQmvva 196,16,164\nQmvva 200,16,168\nQmvva 204,16,172\nQmaav 188,204,16\nQcvaa 0,112,64,40\nQmvaa 0,116,68\n"
"Qmvaa 0,120,72\nQmvaa 0,124,76\nQcvaa 4,128,64,40\nQmvaa 4,132,68\nQmvaa 4,136,72\nQmvaa 4,140,76\ns_nop 7\nQe 0\n"
"Qe 1\nQe 2\nQe 3\nQe 4\nQe 5\nQe 6\nQe 7\ns_nop 0\n"
"Qk 16,0,1\nQk 17,2,3\nQk 18,4,5\nQk 19,6,7\ns_nop 1\nQmvva 208,16,144\nQmvva 212,16,148\nQmvva 216,16,152\n"
"Qmvva 220,16,156\nQmvva 224,16,160\nQmvva 228,16,164\nQmvva 232,16,168\nQmvva 236,16,172\nQmaav 192,204,16\nQcvaa 0,112,80,44\nQmvaa 0,116,84\n"
"Qmvaa 0,120,88\nQmvaa 0,124,92\nQcvaa 4,128,80,44\nQmvaa 4,132,84\nQmvaa 4,136,88\nQmvaa 4,140,92\ns_nop 7\nQe 0\n"
"Qe 1\nQe 2\nQe 3\nQe 4\nQe 5\nQe 6\nQe 7\ns_nop 0\n"
"Qk 16,0,1\nQk 17,2,3\nQk 18,4,5\nQk 19,6,7\ns_nop 1\nQmvva 240,16,144\nQmvva 244,16,148\nQmvva 248,16,152\n"
"Qmvva 252,16,156\nQmava 208,16,160\nQmava 212,16,164\nQmava 216,16,168\nQmava 220,16,172\nQmaav 196,204,16\nQcvaa 0,112,96,48\nQmvaa 0,116,100\n"
"Qmvaa 0,120,104\nQmvaa 0,124,108\nQcvaa 4,128,96,48\nQmvaa 4,132,100\nQmvaa 4,136,104\nQmvaa 4,140,108\ns_nop 7\nQe 0\n"
"Qe 1\nQe 2\nQe 3\nQe 4\nQe 5\nQe 6\nQe 7\ns_nop 0\n"
"Qk 16,0,1\nQk 17,2,3\nQk 18,4,5\nQk 19,6,7\ns_nop 1\nQmava 224,16,144\nQmava 228,16,148\nQmava 232,16,152\n"
"Qmava 236,16,156\nQmava 240,16,160\nQmava 244,16,164\nQmava 248,16,168\nQmava 252,16,172\nQmaav 200,204,16\ns_add_u32 s89, s89, 8192\ns_add_u32 s66, s66, 1\n"
"s_cmp_lt_u32 s66, 32\ns_cbranch_scc1 .Lfb2\ns_nop 7\nv_accvgpr_read_b32 v0, a176\nv_accvgpr_read_b32 v4, a180\nv_accvgpr_read_b32 v8, a184\nv_accvgpr_read_b32 v12, a188\nv_accvgpr_read_b32 v16, a192\n"
"v_accvgpr_read_b32 v20, a196\nv_accvgpr_read_b32 v24, a200\ns_cmp_eq_u32 s67, 0\ns_cbranch_scc1 .Lfbret0\ns_branch .Lfbret1\n.Lf2_w:\ns_mov_b32 s97, 0\n.Lf2_l:\n"
"s_sleep 1\nbuffer_load_dword v78, v76, s[24:27], 0 offen sc1\ns_waitcnt vmcnt(0)\ns_add_u32 s97, s97, 1\ns_cmp_gt_u32 s97, 1048576\ns_cbranch_scc1 .Lf2_r\nv_sub_u32 v66, v78, v65\nv_cmp_gt_u32 vcc, 2, v66\n"
"s_and_b64 vcc, vcc, s[28:29]\ns_cbranch_scc1 .Lf2_l\ns_branch .Lf2_r\n.Lf3_w:\ns_mov_b32 s97, 0\n.Lf3_l:\ns_sleep 1\nbuffer_load_dword v78, v76, s[24:27], 0 offen sc1\n"
"s_waitcnt vmcnt(0)\ns_add_u32 s97, s97, 1\ns_cmp_gt_u32 s97, 1048576\ns_cbranch_scc1 .Lf3_r\nv_sub_u32 v66, v78, v65\nv_cmp_gt_u32 vcc, 3, v66\ns_and_b64 vcc, vcc, s[28:29]\ns_cbranch_scc1 .Lf3_l\n"
"s_branch .Lf3_r\n.Lx0_w:\ns_mov_b32 s97, 0\n.Lx0_l:\ns_sleep 1\nbuffer_load_dword v78, v76, s[24:27], 0 offen sc1\ns_waitcnt vmcnt(0)\ns_add_u32 s97, s97, 1\n"
"s_cmp_gt_u32 s97, 1048576\ns_cbranch_scc1 .Lx0_r\nv_sub_u32 v66, v78, v65\nv_cmp_gt_u32 vcc, 1, v66\ns_and_b64 vcc, vcc, s[28:29]\ns_cbranch_scc1 .Lx0_l\ns_branch .Lx0_r\n.Lx1_w:\n"
"s_mov_b32 s97, 0\n.Lx1_l:\ns_sleep 1\nbuffer_load_dword v78, v76, s[24:27], 0 offen sc1\ns_waitcnt vmcnt(0)\ns_add_u32 s97, s97, 1\ns_cmp_gt_u32 s97, 1048576\ns_cbranch_scc1 .Lx1_r\n"
"v_sub_u32 v66, v78, v65\nv_cmp_gt_u32 vcc, 1, v66\ns_and_b64 vcc, vcc, s[28:29]\ns_cbranch_scc1 .Lx1_l\ns_branch .Lx1_r\n.Lexit:\n"
  :: "s"((unsigned)kp), "s"((unsigned)(kp >> 32)), "s"((unsigned)vp), "s"((unsigned)(vp >> 32)), "s"((unsigned)xp), "s"((unsigned)(xp >> 32)),
     "s"((unsigned)wp), "s"((unsigned)(wp >> 32)), "s"((unsigned)op), "s"((unsigned)(op >> 32)), "s"(ldsb), "s"(wid), "s"(head0),
     "s"((unsigned)dp), "s"((unsigned)(dp >> 32)), "s"((unsigned)wop), "s"((unsigned)(wop >> 32)), "s"((unsigned)yp), "s"((unsigned)(yp >> 32)),
     "s"(hg), "s"(b * 8u + hg), "s"(kx)
  : "v0", "v1", "v2", "v3", "v4", "v5", "v6", "v7", "v8", "v9", "v10", "v11", "v12", "v13", "v14", "v15", "v16", "v17", "v18", "v19", "v20", "v21", "v22", "v23", "v24", "v25", "v26", "v27", "v28", "v29", "v30", "v31", "v32", "v33", "v34", "v35", "v36", "v37", "v38", "v39", "v40", "v41", "v42", "v43", "v44", "v45", "v46", "v47", "v48", "v49", "v50", "v51", "v52", "v53", "v54", "v55", "v56", "v57", "v58", "v59", "v60", "v61", "v62", "v63", "v64", "v65", "v66", "v67", "v68", "v69", "v70", "v71", "v72", "v73", "v74", "v75", "v76", "v77", "v78", "v79", "v80", "v81", "v82", "v83", "v84", "v85", "v86", "v87", "v88", "v89", "v90", "v91", "v92", "v93", "v94", "v95", "v96", "v97", "v98", "v99", "v100", "v101", "v102", "v103", "v104", "v105", "v106", "v107", "v108", "v109", "v110", "v111", "v112", "v113", "v114", "v115", "v116", "v117", "v118", "v119", "v120", "v121", "v122", "v123", "v124", "v125", "v126", "v127", "v128", "v129", "v130", "v131", "v132", "v133", "v134", "v135", "v136", "v137", "v138", "v139", "v140", "v141", "v142", "v143", "v144", "v145", "v146", "v147", "v148", "v149", "v150", "v151", "v152", "v153", "v154", "v155", "v156", "v157", "v158", "v159", "v160", "v161", "v162", "v163", "v164", "v165", "v166", "v167", "v168", "v169", "v170", "v171", "v172", "v173", "v174", "v175", "v176", "v177", "v178", "v179", "v180", "v181", "v182", "v183", "v184", "v185", "v186", "v187", "v188", "v189", "v190", "v191", "v192", "v193", "v194", "v195", "v196", "v197", "v198", "v199", "v200", "v201", "v202", "v203", "v204", "v205", "v206", "v207", "v208", "v209", "v210", "v211", "v212", "v213", "v214", "v215", "v216", "v217", "v218", "v219", "v220", "v221", "v222", "v223", "v224", "v225", "v226", "v227", "v228", "v229", "v230", "v231", "v232", "v233", "v234", "v235", "v236", "v237", "v238", "v239", "v240", "v241", "v242", "v243", "v244", "v245", "v246", "v247", "v248", "v249", "v250", "v251", "v252", "v253", "v254", "v255", "a0", "a1", "a2", "a3", "a4", "a5", "a6", "a7", "a8", "a9", "a10", "a11", "a12", "a13", "a14", "a15", "a16", "a17", "a18", "a19", "a20", "a21", "a22", "a23", "a24", "a25", "a26", "a27", "a28", "a29", "a30", "a31", "a32", "a33", "a34", "a35", "a36", "a37", "a38", "a39", "a40", "a41", "a42", "a43", "a44", "a45", "a46", "a47", "a48", "a49", "a50", "a51", "a52", "a53", "a54", "a55", "a56", "a57", "a58", "a59", "a60", "a61", "a62", "a63", "a64", "a65", "a66", "a67", "a68", "a69", "a70", "a71", "a72", "a73", "a74", "a75", "a76", "a77", "a78", "a79", "a80", "a81", "a82", "a83", "a84", "a85", "a86", "a87", "a88", "a89", "a90", "a91", "a92", "a93", "a94", "a95", "a96", "a97", "a98", "a99", "a100", "a101", "a102", "a103", "a104", "a105", "a106", "a107", "a108", "a109", "a110", "a111", "a112", "a113", "a114", "a115", "a116", "a117", "a118", "a119", "a120", "a121", "a122", "a123", "a124", "a125", "a126", "a127", "a128", "a129", "a130", "a131", "a132", "a133", "a134", "a135", "a136", "a137", "a138", "a139", "a140", "a141", "a142", "a143", "a144", "a145", "a146", "a147", "a148", "a149", "a150", "a151", "a152", "a153", "a154", "a155", "a156", "a157", "a158", "a159", "a160", "a161", "a162", "a163", "a164", "a165", "a166", "a167", "a168", "a169", "a170", "a171", "a172", "a173", "a174", "a175", "a176", "a177", "a178", "a179", "a180", "a181", "a182", "a183", "a184", "a185", "a186", "a187", "a188", "a189", "a190", "a191", "a192", "a193", "a194", "a195", "a196", "a197", "a198", "a199", "a200", "a201", "a202", "a203", "a204", "a205", "a206", "a207", "a208", "a209", "a210", "a211", "a212", "a213", "a214", "a215", "a216", "a217", "a218", "a219", "a220", "a221", "a222", "a223", "a224", "a225", "a226", "a227", "a228", "a229", "a230", "a231", "a232", "a233", "a234", "a235", "a236", "a237", "a238", "a239", "a240", "a241", "a242", "a243", "a244", "a245", "a246", "a247", "a248", "a249", "a250", "a251", "a252", "a253", "a254", "a255", "s24", "s25", "s26", "s27", "s28", "s29", "s30", "s31", "s40", "s41", "s42", "s43", "s44", "s45", "s46", "s47", "s48", "s49", "s50", "s51", "s52", "s53", "s54", "s55", "s56", "s57", "s58", "s59", "s60", "s61", "s62", "s63", "s64", "s65", "s66", "s67", "s68", "s69", "s70", "s71", "s72", "s73", "s74", "s75", "s76", "s77", "s78", "s79", "s80", "s81", "s82", "s83", "s84", "s85", "s86", "s87", "s88", "s89", "s90", "s91", "s92", "s93", "s94", "s95", "s96", "s97", "s98", "s99", "vcc", "scc", "memory");
}

extern "C" void kernel_launch(void* const* d_in, const int* in_sizes, int n_in, void* d_out, int out_size, void* d_ws, size_t ws_size, hipStream_t stream) {
  typedef const unsigned short* P;
  (void)hipFuncSetAttribute((const void*)mha2_asm, hipFuncAttributeMaxDynamicSharedMemorySize, 163840);
  hipLaunchKernelGGL(mha2_asm, dim3(256), dim3(256), 163840, stream, (P)d_in[0], (P)d_in[5], (P)d_in[3], (P)d_in[8], (P)d_in[1], (P)d_in[6], (P)d_in[2], (P)d_in[7],
                     (P)d_in[4], (P)d_in[9], (char*)d_ws, (unsigned short*)d_out);
}
```

```cpp
#include <hip/hip_runtime.h>
#include <stdint.h>

__global__ __launch_bounds__(256, 1) __attribute__((aligned(4096)))
void mha2_asm(const unsigned short* __restrict__ X0, const unsigned short* __restrict__ X1, const unsigned short* __restrict__ W0, const unsigned short* __restrict__ W1,
              const unsigned short* __restrict__ K0, const unsigned short* __restrict__ K1,
              const unsigned short* __restrict__ V0, const unsigned short* __restrict__ V1,
              const unsigned short* __restrict__ WO0, const unsigned short* __restrict__ WO1, char* ws, unsigned short* out) {
  extern __shared__ __attribute__((aligned(16))) char lds[];
  const unsigned c = blockIdx.x;
  const unsigned xcd = c & 7u, idx = c >> 3;
  const unsigned hg = idx & 7u, bb = xcd * 4u + (idx >> 3);
  const unsigned bk = bb >> 4, b = bb & 15u;
  const unsigned wid = __builtin_amdgcn_readfirstlane(threadIdx.x >> 6);
  const unsigned head0 = (hg >> 2) * 16u + (hg & 3u) * 2u + (wid >> 1);
  const unsigned long long kp = (unsigned long long)(bk ? K1 : K0) + ((unsigned long long)b * 32u + (head0 & ~1u)) * 262144ull - 1024ull;
  const unsigned long long vp = (unsigned long long)(bk ? V1 : V0) + ((unsigned long long)b * 32u + (head0 & ~1u)) * 262144ull - 1024ull;
  const unsigned long long xp = (unsigned long long)(bk ? X1 : X0) + (unsigned long long)b * (224u * 1280u);
  const unsigned long long wp = (unsigned long long)(bk ? W1 : W0);
  const unsigned long long op = (unsigned long long)ws + 0ull + (unsigned long long)bb * 6291456ull;
  const unsigned ldsb = (unsigned)(size_t)lds;
  const unsigned long long wop = (unsigned long long)(bk ? WO1 : WO0) - 4096ull;
  const unsigned long long yp = (unsigned long long)out + (unsigned long long)b * (224ull * 2560ull) + bk * 1280u + (hg & 3u) * 320u;
  const unsigned long long dp = (unsigned long long)ws + (300ull << 20) + (c * 4u + wid) * 256u;
  const unsigned hd = wid >> 1, ky = hg & 4u;
  const unsigned kx = (ky >> 0 & 1u) << 11 | (ky >> 1 & 1u) << 12 | (ky >> 2 & 1u) << 13 | (ky >> 3 & 1u) << 14 | (ky >> 4 & 1u) << 17;
  asm volatile(
".macro Qta d,v,o\nds_read_b64_tr_b16 a[\\d:\\d+1], v\\v offset:\\o\n.endm\n.macro Qdv d,v,o\nds_read_b128 v[\\d:\\d+3], v\\v offset:\\o\n.endm\n.macro Qda d,v,o\nds_read_b128 a[\\d:\\d+3], v\\v offset:\\o\n"
".endm\n.macro Qzvav d,a,b\nv_mfma_f32_16x16x32_bf16 v[\\d:\\d+3], a[\\a:\\a+3], v[\\b:\\b+3], 0\n.endm\n.macro Qmvaa d,a,b\nv_mfma_f32_16x16x32_bf16 v[\\d:\\d+3], a[\\a:\\a+3], a[\\b:\\b+3], v[\\d:\\d+3]\n.endm\n.macro Qzaav d,a,b\n"
"v_mfma_f32_16x16x32_bf16 a[\\d:\\d+3], a[\\a:\\a+3], v[\\b:\\b+3], 0\n.endm\n.macro Qmaaa d,a,b\nv_mfma_f32_16x16x32_bf16 a[\\d:\\d+3], a[\\a:\\a+3], a[\\b:\\b+3], a[\\d:\\d+3]\n.endm\n.macro Qmvav d,a,b\nv_mfma_f32_16x16x32_bf16 v[\\d:\\d+3], a[\\a:\\a+3], v[\\b:\\b+3], v[\\d:\\d+3]\n.endm\n"
".macro Qmaav d,a,b\nv_mfma_f32_16x16x32_bf16 a[\\d:\\d+3], a[\\a:\\a+3], v[\\b:\\b+3], a[\\d:\\d+3]\n.endm\n.macro Qk d,a,b\nv_cvt_pk_bf16_f32 v\\d, v\\a, v\\b\n.endm\n.macro Qzvaa d,a,b\nv_mfma_f32_16x16x32_bf16 v[\\d:\\d+3], a[\\a:\\a+3], a[\\b:\\b+3], 0\n"
".endm\n.macro Qa d,a,b\nv_add_f32 v\\d, v\\a, v\\b\n.endm\n.macro Qe d\nv_exp_f32 v\\d, v\\d\n.endm\n.macro Qcvaa d,a,b,c\n"
"v_mfma_f32_16x16x32_bf16 v[\\d:\\d+3], a[\\a:\\a+3], a[\\b:\\b+3], v[\\c:\\c+3]\n.endm\n.macro Qzvva d,a,b\nv_mfma_f32_16x16x32_bf16 v[\\d:\\d+3], v[\\a:\\a+3], a[\\b:\\b+3], 0\n.endm\n.macro Qzava d,a,b\nv_mfma_f32_16x16x32_bf16 a[\\d:\\d+3], v[\\a:\\a+3], a[\\b:\\b+3], 0\n.endm\n"
".macro Qmvva d,a,b\nv_mfma_f32_16x16x32_bf16 v[\\d:\\d+3], v[\\a:\\a+3], a[\\b:\\b+3], v[\\d:\\d+3]\n.endm\n.macro Qmava d,a,b\nv_mfma_f32_16x16x32_bf16 a[\\d:\\d+3], v[\\a:\\a+3], a[\\b:\\b+3], a[\\d:\\d+3]\n.endm\n.macro Qzvvv d,a,b\nv_mfma_f32_16x16x32_bf16 v[\\d:\\d+3], v[\\a:\\a+3], v[\\b:\\b+3], 0\n"
".endm\n.macro Qcaav d,a,b,c\nv_mfma_f32_16x16x32_bf16 a[\\d:\\d+3], a[\\a:\\a+3], v[\\b:\\b+3], a[\\c:\\c+3]\n.endm\n.macro Qcvav d,a,b,c\nv_mfma_f32_16x16x32_bf16 v[\\d:\\d+3], a[\\a:\\a+3], v[\\b:\\b+3], v[\\c:\\c+3]\n.endm\n.p2align 7\n"
"v_mbcnt_lo_u32_b32 v0, -1, 0\nv_mbcnt_hi_u32_b32 v0, -1, v0\nv_lshlrev_b32 v60, 4, v0\ns_add_u32 m0, %10, 160768\ns_getpc_b64 s[56:57]\n.Lpc0:\ns_and_b32 s56, s56, 0xffffff80\ns_and_b32 s57, s57, 0xffff\n"
"s_mov_b32 s58, .Lend-.Lpc0\ns_mov_b32 s59, 0x20000\ns_and_b32 s94, %20, 31\ns_lshl_b32 s94, s94, 2\ns_add_u32 s94, s94, %11\ns_lshl_b32 s94, s94, 10\nbuffer_load_dwordx4 v60, s[56:59], s94 offen lds\ns_mov_b32 s52, %6\n"
"s_and_b32 s53, %7, 0xffff\ns_mov_b32 s54, 5242880\ns_mov_b32 s55, 0x20000\ns_mov_b32 s48, %4\ns_and_b32 s49, %5, 0xffff\ns_mov_b32 s50, 286720\ns_mov_b32 s51, 0x20000\ns_mov_b32 s70, %11\n"
"s_and_b32 s92, s70, 1\ns_lshr_b32 s93, s70, 1\nv_lshrrev_b32 v2, 4, v0\ns_lshl_b32 s68, s92, 18\nv_and_b32 v4, 3, v0\nv_lshlrev_b32 v4, 4, v4\nv_and_b32 v1, 1, v2\nv_lshl_add_u32 v4, v1, 6, v4\n"
"v_lshrrev_b32 v1, 5, v0\nv_lshl_add_u32 v4, v1, 15, v4\nv_bfe_u32 v1, v0, 2, 2\nv_lshl_add_u32 v4, v1, 13, v4\nv_add_u32 v62, s68, v4\ns_lshl_b32 s87, %12, 8\ns_mul_i32 s62, s70, 4128\ns_add_u32 s62, s62, %10\n"
"s_add_u32 s62, s62, 33024\ns_add_u32 m0, s62, 0\ns_add_u32 s66, s87, 0\ns_nop 0\nbuffer_load_dwordx4 v62, s[52:55], s66 offen lds\ns_add_u32 s66, s87, 64504\ns_nop 0\nbuffer_load_dwordx4 v62, s[52:55], s66 offen offset:1032 lds\n"
"s_add_u32 s66, s87, 129008\ns_nop 0\nbuffer_load_dwordx4 v62, s[52:55], s66 offen offset:2064 lds\ns_add_u32 s66, s87, 193512\ns_nop 0\nbuffer_load_dwordx4 v62, s[52:55], s66 offen offset:3096 lds\ns_lshl_b32 s69, s92, 1\nv_lshrrev_b32 v3, 5, v0\n"
"v_and_b32 v4, 3, v0\nv_xor_b32 v4, s69, v4\nv_lshlrev_b32 v4, 4, v4\nv_lshl_add_u32 v4, v3, 6, v4\nv_bfe_u32 v1, v0, 2, 3\nv_mul_u32_u24 v64, 1280, v1\nv_add_u32 v64, v64, v4\ns_mul_i32 s86, s93, 143360\n"
"s_mul_i32 s68, s92, 10240\ns_add_u32 s86, s86, s68\ns_mul_i32 s63, s92, 14336\ns_mul_i32 s68, s93, 7168\ns_add_u32 s63, s63, s68\ns_add_u32 s63, s63, %10\ns_add_u32 s63, s63, 99072\ns_add_u32 m0, s63, 33024\n"
"s_add_u32 s97, s86, 0\ns_nop 0\nbuffer_load_dwordx4 v64, s[48:51], s97 offen lds\ns_add_u32 s97, s86, 19456\ns_nop 0\nbuffer_load_dwordx4 v64, s[48:51], s97 offen offset:1024 lds\ns_add_u32 s97, s86, 38912\ns_nop 0\n"
"buffer_load_dwordx4 v64, s[48:51], s97 offen offset:2048 lds\ns_add_u32 s97, s86, 58368\ns_nop 0\nbuffer_load_dwordx4 v64, s[48:51], s97 offen offset:3072 lds\ns_add_u32 m0, s63, 37120\ns_add_u32 s97, s86, 81920\ns_nop 0\nbuffer_load_dwordx4 v64, s[48:51], s97 offen lds\n"
"s_add_u32 s97, s86, 101376\ns_nop 0\nbuffer_load_dwordx4 v64, s[48:51], s97 offen offset:1024 lds\ns_add_u32 s97, s86, 120832\ns_nop 0\nbuffer_load_dwordx4 v64, s[48:51], s97 offen offset:2048 lds\ns_add_u32 m0, s62, 16512\ns_add_u32 s66, s87, 128\n"
"s_nop 0\nbuffer_load_dwordx4 v62, s[52:55], s66 offen lds\ns_mov_b32 s24, %8\ns_and_b32 s25, %9, 0xffff\ns_mov_b32 s26, 6291456\ns_mov_b32 s27, 0x20000\nv_and_b32 v5, 7, v0\nv_lshlrev_b32 v5, 7, v5\n"
"s_add_u32 s66, s87, 64632\ns_nop 0\nbuffer_load_dwordx4 v62, s[52:55], s66 offen offset:1032 lds\nv_add_u32 v76, 5242880, v5\ns_lshl_b32 s68, s70, 2\ns_add_u32 s68, s68, 4\nv_bfe_u32 v5, v0, 3, 1\nv_mad_u32_u24 v76, v5, s68, v76\n"
"buffer_load_dword v65, v76, s[24:27], 0 offen sc1\ns_add_u32 s66, s87, 129136\ns_nop 0\nbuffer_load_dwordx4 v62, s[52:55], s66 offen offset:2064 lds\ns_mov_b32 s40, %0\ns_and_b32 s41, %1, 0xffff\ns_mov_b32 s42, 2622464\ns_mov_b32 s43, 0x20000\n"
"s_mov_b32 s44, %2\ns_and_b32 s45, %3, 0xffff\ns_add_u32 s66, s87, 193640\ns_nop 0\nbuffer_load_dwordx4 v62, s[52:55], s66 offen offset:3096 lds\ns_mov_b32 s46, 2622464\ns_mov_b32 s47, 0x20000\ns_mov_b32 s56, %8\n"
"s_and_b32 s57, %9, 0xffff\ns_mov_b32 s58, 0\ns_mov_b32 s59, 0x20000\ns_mov_b64 exec, 0\ns_cmp_eq_u32 s70, 0\ns_cbranch_scc1 .Lpg0\ns_cmp_eq_u32 s70, 1\ns_cbranch_scc1 .Lpg1\n"
"s_cmp_eq_u32 s70, 2\ns_cbranch_scc1 .Lpg2\ns_branch .Lpg3\n.Ltr0:\ns_mov_b64 exec, -1\ns_mov_b32 s65, 0\ns_mov_b32 s60, %21\ns_add_u32 s90, s60, 260096\n"
"s_mov_b32 s67, 0\ns_mov_b32 s72, 0x3e0293ee\ns_lshl_b32 s68, s70, 8\ns_add_u32 m0, s63, 0\ns_add_u32 s97, s86, 128\ns_nop 0\nbuffer_load_dwordx4 v64, s[48:51], s97 offen lds\ns_lshl_b32 s69, s93, 5\n"
"s_or_b32 s88, s68, s69\nv_bfe_u32 v1, v0, 2, 2\nv_mul_u32_u24 v4, 0x8100, v1\nv_and_b32 v1, 3, v0\nv_lshl_or_b32 v4, v1, 4, v4\ns_add_u32 s97, s86, 19584\ns_nop 0\nbuffer_load_dwordx4 v64, s[48:51], s97 offen offset:1024 lds\n"
"v_and_b32 v1, 1, v2\nv_lshl_or_b32 v4, v1, 7, v4\nv_lshl_or_b32 v4, v3, 6, v4\nv_xor_b32 v4, s88, v4\nv_add_u32 v69, 1024, v4\nv_bfe_u32 v1, v0, 1, 2\ns_add_u32 s97, s86, 39040\ns_nop 0\n"
"buffer_load_dwordx4 v64, s[48:51], s97 offen offset:2048 lds\nv_mul_u32_u24 v4, 0x8100, v1\nv_and_b32 v1, 9, v0\nv_lshl_or_b32 v4, v1, 4, v4\nv_lshl_or_b32 v4, v2, 5, v4\nv_xor_b32 v4, s68, v4\nv_add_u32 v72, 1024, v4\ns_add_u32 s97, s86, 58496\n"
"s_nop 0\nbuffer_load_dwordx4 v64, s[48:51], s97 offen offset:3072 lds\ns_lshl_b32 s68, s70, 12\ns_add_u32 s61, s68, %10\ns_mul_i32 s68, s92, 8320\ns_lshl_b32 s69, s93, 12\ns_add_u32 s68, s68, s69\ns_add_u32 s68, s68, %10\n"
"s_add_u32 m0, s63, 4096\ns_add_u32 s97, s86, 82048\ns_nop 0\nbuffer_load_dwordx4 v64, s[48:51], s97 offen lds\ns_add_u32 s64, s68, 16384\ns_lshl_b32 s69, s93, 11\ns_add_u32 s69, s69, %10\nv_bfe_u32 v3, v0, 3, 1\n"
"v_lshlrev_b32 v4, 1, v3\nv_xor_b32 v4, v4, v2\ns_add_u32 s97, s86, 101504\ns_nop 0\nbuffer_load_dwordx4 v64, s[48:51], s97 offen offset:1024 lds\nv_and_b32 v1, 7, v0\nv_lshl_add_u32 v4, v1, 2, v4\nv_lshlrev_b32 v4, 4, v4\n"
"v_bfe_u32 v1, v0, 2, 2\nv_and_b32 v5, 255, v4\nv_lshl_or_b32 v1, v1, 12, v5\ns_add_u32 s97, s86, 120960\ns_nop 0\nbuffer_load_dwordx4 v64, s[48:51], s97 offen offset:2048 lds\nv_add_u32 v68, s69, v1\ns_mul_i32 s68, s92, 7168\n"
"s_add_u32 s68, s68, %10\ns_add_u32 s68, s68, 99072\nv_mul_u32_u24 v1, 14336, v3\nv_add3_u32 v63, v1, v4, s68\ns_add_u32 m0, s62, 33024\ns_add_u32 s66, s87, 524288\ns_nop 0\nbuffer_load_dwordx4 v62, s[52:55], s66 offen lds\n"
"v_lshrrev_b32 v3, 5, v0\nv_and_b32 v4, 31, v0\nv_lshlrev_b32 v4, 3, v4\nv_and_b32 v1, 1, v2\nv_lshl_add_u32 v4, v1, 13, v4\nv_lshl_add_u32 v4, v3, 12, v4\ns_add_u32 s66, s87, 588792\ns_nop 0\n"
"buffer_load_dwordx4 v62, s[52:55], s66 offen offset:1032 lds\nv_add_u32 v4, 16384, v4\nv_add_u32 v70, s69, v4\nv_add_u32 v71, 66048, v70\ns_mul_i32 s68, s93, 8256\ns_add_u32 s68, s68, %10\ns_add_u32 s68, s68, 33024\ns_add_u32 s66, s87, 653296\n"
"s_nop 0\nbuffer_load_dwordx4 v62, s[52:55], s66 offen offset:2064 lds\nv_mul_u32_u24 v3, 1032, v2\nv_and_b32 v4, 15, v0\nv_lshlrev_b32 v4, 4, v4\nv_add3_u32 v61, v3, v4, s68\ns_lshl_b32 s68, s70, 2\ns_add_u32 s69, %10, 131968\n"
"s_add_u32 s66, s87, 717800\ns_nop 0\nbuffer_load_dwordx4 v62, s[52:55], s66 offen offset:3096 lds\ns_add_u32 s68, s68, s69\nv_mov_b32 v73, s68\nv_and_b32 v3, 3, v0\nv_lshl_add_u32 v74, v3, 2, s69\nv_mov_b32 v75, 0\n"
"ds_write_b32 v73, v75\ns_mov_b64 exec, 0\ns_cmp_eq_u32 s70, 0\ns_cbranch_scc1 .Lpg4\ns_cmp_eq_u32 s70, 1\ns_cbranch_scc1 .Lpg5\ns_cmp_eq_u32 s70, 2\ns_cbranch_scc1 .Lpg6\n"
"s_branch .Lpg7\n.Ltr9:\ns_mov_b64 exec, -1\ns_waitcnt lgkmcnt(0)\ns_mul_i32 s91, s70, 7168\ns_add_u32 s91, s91, %10\ns_add_u32 s91, s91, 98304\ns_lshl_b32 s96, s70, 11\n"
"s_add_u32 s96, s96, %10\ns_add_u32 m0, s62, 49536\ns_add_u32 s66, s87, 524416\ns_nop 0\nbuffer_load_dwordx4 v62, s[52:55], s66 offen lds\nv_and_b32 v3, 1, v0\nv_mul_u32_u24 v3, 0xaaaa, v3\nv_or_b32 v77, 0x4444, v3\n"
"s_and_b32 s68, %19, 4\ns_lshl_b32 s28, 15, s68\ns_mov_b32 s29, 0\ns_add_u32 s66, s87, 588920\ns_nop 0\nbuffer_load_dwordx4 v62, s[52:55], s66 offen offset:1032 lds\ns_and_b32 s68, %19, 4\ns_add_u32 s68, s68, %19\n"
"s_lshl_b32 s71, s68, 18\ns_mul_i32 s69, s70, 0x80100\ns_and_b32 s69, s69, 0x100100\ns_add_u32 s68, s71, s69\ns_add_u32 s66, s87, 653424\ns_nop 0\nbuffer_load_dwordx4 v62, s[52:55], s66 offen offset:2064 lds\ns_lshr_b32 s98, %20, 1\n"
"s_xor_b32 s98, s98, %20\ns_lshl_b32 s98, s98, 11\ns_and_b32 s98, s98, 8192\ns_add_u32 s68, s68, s98\ns_add_u32 s68, s68, 7168\ns_add_u32 s66, s87, 717928\ns_nop 0\nbuffer_load_dwordx4 v62, s[52:55], s66 offen offset:3096 lds\n"
"s_add_u32 s30, %8, s68\ns_addc_u32 s31, %9, 0\ns_mov_b32 s77, 0\nv_bfe_i32 v8, v0, 3, 1\nv_lshlrev_b32 v4, 10, v0\nv_and_b32 v4, 0x6000, v4\ns_mov_b64 exec, 0\ns_cmp_eq_u32 s70, 0\n"
"s_cbranch_scc1 .Lpg8\ns_cmp_eq_u32 s70, 1\ns_cbranch_scc1 .Lpg9\ns_cmp_eq_u32 s70, 2\ns_cbranch_scc1 .Lpg10\ns_branch .Lpg11\n.Ltr10:\ns_mov_b64 exec, -1\n"
"v_lshrrev_b32 v3, 5, v0\nv_lshl_or_b32 v4, v3, 16, v4\nv_and_b32 v3, 0x70, v60\nv_or_b32 v4, v3, v4\nv_or_b32 v8, 1, v8\nv_mov_b32 v9, v8\nv_mov_b32 v10, v8\nv_mov_b32 v11, v8\n"
"buffer_store_dwordx4 v[8:11], v4, s[24:27], s68 offen offset:0 sc1\ns_nop 1\ns_lshl_b32 s68, s70, 8\nv_lshl_or_b32 v3, v2, 15, v60\nv_xor_b32 v79, s68, v3\ns_and_b32 s68, %19, 3\ns_lshl_b32 s99, 3072, s68\ns_or_b32 s99, s99, 0x262\n"
"s_mov_b32 s94, 0x7ffff000\ns_mul_i32 s68, %20, 40960\ns_mul_i32 s69, s93, 20480\ns_add_u32 s68, s68, s69\ns_lshl_b32 s69, s92, 10\ns_add_u32 s68, s68, s69\ns_add_u32 s68, s68, 2048\ns_add_u32 s94, %6, s68\n"
"s_addc_u32 s95, %7, 0\ns_and_b32 s95, s95, 0xffff\ns_mov_b32 s75, 17408\n.Lround:\ns_cmp_lg_u32 s67, 0\ns_cselect_b64 vcc, -1, 0\ns_waitcnt vmcnt(15)\ns_barrier\n"
"Qta 112,61,0\nQta 114,61,512\nQdv 24,63,33024\nQta 116,61,4128\nQta 118,61,4640\nQdv 28,63,34048\nQta 120,61,8\nQta 122,61,520\n"
"Qdv 32,63,35072\nQta 124,61,4136\nQta 126,61,4648\nQdv 36,63,36096\nQta 128,61,256\nQta 130,61,768\nQdv 40,63,37120\nQta 132,61,4384\n"
"Qta 134,61,4896\nQdv 44,63,38144\nQta 136,61,264\nQta 138,61,776\nQdv 48,63,39168\nQta 140,61,4392\nQta 142,61,4904\nQda 176,63,33536\n"
"Qda 180,63,34560\nQda 184,63,35584\nQda 188,63,36608\nQda 192,63,37632\nQda 196,63,38656\nQda 200,63,39680\ns_waitcnt lgkmcnt(6)\nQzvav 80,112,24\n"
"Qmvaa 80,116,176\ns_add_u32 s68, s71, 0\nQzvav 84,120,24\ns_cbranch_vccz .Lst27\nbuffer_store_dwordx4 a[0:3], v79, s[56:59], s68 offen offset:0 sc1\n.Lst27:\nQmvaa 84,124,176\nQzvav 88,128,24\n"
"s_add_u32 s68, s71, 16384\nQmvaa 88,132,176\ns_cbranch_vccz .Lst26\nbuffer_store_dwordx4 a[4:7], v79, s[56:59], s68 offen offset:0 sc1\n.Lst26:\nQzvav 92,136,24\nQmvaa 92,140,176\ns_waitcnt lgkmcnt(0)\n"
"Qzvav 124,136,28\ns_waitcnt vmcnt(16)\ns_barrier\nQmvaa 124,140,180\nQta 144,61,16512\ns_add_u32 m0, s62, 0\ns_add_u32 s69, s87, 1048576\nQzvav 120,128,28\n"
"buffer_load_dwordx4 v62, s[52:55], s69 offen lds\nQmvaa 120,132,180\nQta 146,61,17024\nQzvav 116,120,28\nQmvaa 116,124,180\nQta 148,61,20640\ns_add_u32 s69, s87, 1113080\nQzvav 112,112,28\n"
"buffer_load_dwordx4 v62, s[52:55], s69 offen offset:1032 lds\nQmvaa 112,116,180\nQta 150,61,21152\nQzvav 144,112,32\nQmvaa 144,116,184\nQta 152,61,16520\ns_add_u32 s69, s87, 1177584\nQzvav 148,120,32\n"
"buffer_load_dwordx4 v62, s[52:55], s69 offen offset:2064 lds\nQmvaa 148,124,184\nQta 154,61,17032\nQzvav 152,128,32\nQmvaa 152,132,184\nQta 156,61,20648\ns_add_u32 s69, s87, 1242088\nQzvav 156,136,32\n"
"buffer_load_dwordx4 v62, s[52:55], s69 offen offset:3096 lds\nQmvaa 156,140,184\nQta 158,61,21160\nQzvav 188,136,36\nQmvaa 188,140,188\nQta 160,61,16768\ns_add_u32 m0, s63, 33024\ns_add_u32 s88, s86, 256\n"
"Qzvav 184,128,36\nbuffer_load_dwordx4 v64, s[48:51], s88 offen lds\nQmvaa 184,132,188\nQta 162,61,17280\nQzvav 180,120,36\nQmvaa 180,124,188\nQta 164,61,20896\ns_add_u32 s88, s86, 19712\n"
"Qzvav 176,112,36\nbuffer_load_dwordx4 v64, s[48:51], s88 offen offset:1024 lds\nQmvaa 176,116,188\nQta 166,61,21408\nQzvav 208,112,40\nQmvaa 208,116,192\nQta 168,61,16776\ns_add_u32 s88, s86, 39168\n"
"Qzvav 212,120,40\nbuffer_load_dwordx4 v64, s[48:51], s88 offen offset:2048 lds\nQmvaa 212,124,192\nQta 170,61,17288\nQzvav 216,128,40\nQmvaa 216,132,192\nQta 172,61,20904\ns_add_u32 s88, s86, 58624\n"
"Qzvav 220,136,40\nbuffer_load_dwordx4 v64, s[48:51], s88 offen offset:3072 lds\nQmvaa 220,140,192\nQta 174,61,21416\nQzvav 252,136,44\nQmvaa 252,140,196\ns_add_u32 m0, s63, 37120\ns_add_u32 s88, s86, 82176\n"
"Qzvav 248,128,44\nbuffer_load_dwordx4 v64, s[48:51], s88 offen lds\nQmvaa 248,132,196\nQzvav 244,120,44\nQmvaa 244,124,196\ns_add_u32 s88, s86, 101632\nQzvav 240,112,44\nbuffer_load_dwordx4 v64, s[48:51], s88 offen offset:1024 lds\n"
"Qmvaa 240,116,196\nQzaav 224,112,48\nQmaaa 224,116,200\ns_add_u32 s88, s86, 121088\nQzaav 228,120,48\nbuffer_load_dwordx4 v64, s[48:51], s88 offen offset:2048 lds\nQmaaa 228,124,200\nQzaav 232,128,48\n"
"Qmaaa 232,132,200\nQzaav 236,136,48\nQmaaa 236,140,200\ns_waitcnt lgkmcnt(0)\nQzvav 96,144,24\nQmvaa 96,148,176\ns_add_u32 s68, s71, 131072\nQzvav 100,152,24\n"
"s_cbranch_vccz .Lst25\nbuffer_store_dwordx4 a[8:11], v79, s[56:59], s68 offen offset:0 sc1\n.Lst25:\nQmvaa 100,156,176\nQzvav 104,160,24\ns_add_u32 s68, s71, 147456\nQmvaa 104,164,176\ns_cbranch_vccz .Lst24\n"
"buffer_store_dwordx4 a[12:15], v79, s[56:59], s68 offen offset:0 sc1\n.Lst24:\nQzvav 108,168,24\nQmvaa 108,172,176\ns_waitcnt lgkmcnt(0)\nQzvav 140,168,28\ns_waitcnt vmcnt(15)\ns_barrier\n"
"Qmvaa 140,172,180\nQdv 24,63,0\ns_add_u32 m0, s62, 16512\ns_add_u32 s69, s87, 1048704\nQzvav 136,160,28\nbuffer_load_dwordx4 v62, s[52:55], s69 offen lds\nQmvaa 136,164,180\nQda 176,63,512\n"
"Qzvav 132,152,28\nQta 112,61,33024\nQmvaa 132,156,180\ns_add_u32 s69, s87, 1113208\nQzvav 128,144,28\nbuffer_load_dwordx4 v62, s[52:55], s69 offen offset:1032 lds\nQmvaa 128,148,180\nQdv 28,63,1024\n"
"Qzvav 160,144,32\nQda 180,63,1536\nQmvaa 160,148,184\nQta 114,61,33536\ns_add_u32 s69, s87, 1177712\nQzvav 164,152,32\nbuffer_load_dwordx4 v62, s[52:55], s69 offen offset:2064 lds\nQmvaa 164,156,184\n"
"Qta 116,61,37152\nQzvav 168,160,32\nQmvaa 168,164,184\nQta 118,61,37664\ns_add_u32 s69, s87, 1242216\nQzvav 172,168,32\nbuffer_load_dwordx4 v62, s[52:55], s69 offen offset:3096 lds\nQmvaa 172,172,184\n"
"Qdv 32,63,2048\nQzvav 204,168,36\nQda 184,63,2560\nQmvaa 204,172,188\nQta 120,61,33032\ns_add_u32 m0, s61, 0\nQzvav 200,160,36\ns_cbranch_vccnz .Ld0q0\n"
"buffer_load_dwordx4 v69, s[40:43], s60 offen nt lds\n.Ld0q0:\nQmvaa 200,164,188\nQta 122,61,33544\nQzvav 196,152,36\nQmvaa 196,156,188\nQta 124,61,37160\nQzvav 192,144,36\n"
"Qdv 36,63,3072\nQmvaa 192,148,188\nQda 188,63,3584\nQzvav 224,144,40\nQta 126,61,37672\nQmvaa 224,148,192\nQzvav 228,152,40\nQta 128,61,33280\n"
"Qmvaa 228,156,192\nQzvav 232,160,40\nQta 130,61,33792\nQmvaa 232,164,192\nQzvav 236,168,40\nQdv 40,63,4096\nQmvaa 236,172,192\nQda 192,63,4608\n"
"Qzaav 220,168,44\nQta 132,61,37408\n.Lpg0:\ns_cbranch_execz .Ltr0\nQmaaa 220,172,196\nQzaav 216,160,44\nQta 134,61,37920\nQmaaa 216,164,196\n"
"Qzaav 212,152,44\nQta 136,61,33288\nQmaaa 212,156,196\nQzaav 208,144,44\nQdv 44,63,5120\nQmaaa 208,148,196\nQda 196,63,5632\nQzaav 240,144,48\n"
"Qta 138,61,33800\nQmaaa 240,148,200\nQzaav 244,152,48\nQta 140,61,37416\nQmaaa 244,156,200\nQzaav 248,160,48\nQta 142,61,37928\nQmaaa 248,164,200\n"
"Qzaav 252,168,48\nQdv 48,63,6144\nQmaaa 252,172,200\nQda 200,63,6656\ns_waitcnt lgkmcnt(2)\nQmvav 80,112,24\nQmvaa 80,116,176\ns_add_u32 s68, s71, 0\n"
"Qmvav 84,120,24\ns_cbranch_vccz .Lst23\nbuffer_store_dwordx4 a[16:19], v79, s[56:59], s68 offen offset:1024 sc1\n.Lst23:\nQmvaa 84,124,176\nQmvav 88,128,24\ns_add_u32 s68, s71, 16384\nQmvaa 88,132,176\n"
"s_cbranch_vccz .Lst22\nbuffer_store_dwordx4 a[20:23], v79, s[56:59], s68 offen offset:1024 sc1\n.Lst22:\nQmvav 92,136,24\nQmvaa 92,140,176\ns_waitcnt lgkmcnt(0)\nQmvav 124,136,28\ns_waitcnt vmcnt(17)\n"
"s_barrier\nQmvaa 124,140,180\nQta 144,61,49536\ns_add_u32 m0, s62, 33024\ns_add_u32 s69, s87, 1572864\nQmvav 120,128,28\nbuffer_load_dwordx4 v62, s[52:55], s69 offen lds\nQmvaa 120,132,180\n"
"Qta 146,61,50048\nQmvav 116,120,28\nQmvaa 116,124,180\nQta 148,61,53664\ns_add_u32 s69, s87, 1637368\nQmvav 112,112,28\nbuffer_load_dwordx4 v62, s[52:55], s69 offen offset:1032 lds\nQmvaa 112,116,180\n"
"Qta 150,61,54176\nQmvav 144,112,32\nQmvaa 144,116,184\nQta 152,61,49544\ns_add_u32 s69, s87, 1701872\nQmvav 148,120,32\nbuffer_load_dwordx4 v62, s[52:55], s69 offen offset:2064 lds\nQmvaa 148,124,184\n"
"Qta 154,61,50056\nQmvav 152,128,32\nQmvaa 152,132,184\nQta 156,61,53672\ns_add_u32 s69, s87, 1766376\nQmvav 156,136,32\nbuffer_load_dwordx4 v62, s[52:55], s69 offen offset:3096 lds\nQmvaa 156,140,184\n"
"Qta 158,61,54184\nQmvav 188,136,36\nQmvaa 188,140,188\nQta 160,61,49792\ns_add_u32 m0, s63, 0\ns_add_u32 s88, s86, 384\nQmvav 184,128,36\nbuffer_load_dwordx4 v64, s[48:51], s88 offen lds\n"
"Qmvaa 184,132,188\nQta 162,61,50304\nQmvav 180,120,36\nQmvaa 180,124,188\nQta 164,61,53920\ns_add_u32 s88, s86, 19840\nQmvav 176,112,36\nbuffer_load_dwordx4 v64, s[48:51], s88 offen offset:1024 lds\n"
"Qmvaa 176,116,188\nQta 166,61,54432\nQmvav 208,112,40\nQmvaa 208,116,192\nQta 168,61,49800\ns_add_u32 s88, s86, 39296\nQmvav 212,120,40\nbuffer_load_dwordx4 v64, s[48:51], s88 offen offset:2048 lds\n"
"Qmvaa 212,124,192\nQta 170,61,50312\nQmvav 216,128,40\nQmvaa 216,132,192\nQta 172,61,53928\ns_add_u32 s88, s86, 58752\nQmvav 220,136,40\nbuffer_load_dwordx4 v64, s[48:51], s88 offen offset:3072 lds\n"
"Qmvaa 220,140,192\nQta 174,61,54440\nQmvav 252,136,44\nQmvaa 252,140,196\ns_add_u32 m0, s63, 4096\ns_add_u32 s88, s86, 82304\nQmvav 248,128,44\nbuffer_load_dwordx4 v64, s[48:51], s88 offen lds\n"
"Qmvaa 248,132,196\nQmvav 244,120,44\nQmvaa 244,124,196\ns_add_u32 s88, s86, 101760\nQmvav 240,112,44\nbuffer_load_dwordx4 v64, s[48:51], s88 offen offset:1024 lds\nQmvaa 240,116,196\nQmaav 224,112,48\n"
"Qmaaa 224,116,200\ns_add_u32 s88, s86, 121216\nQmaav 228,120,48\nbuffer_load_dwordx4 v64, s[48:51], s88 offen offset:2048 lds\nQmaaa 228,124,200\nQmaav 232,128,48\nQmaaa 232,132,200\nQmaav 236,136,48\n"
"Qmaaa 236,140,200\ns_waitcnt lgkmcnt(0)\nQmvav 96,144,24\nQmvaa 96,148,176\ns_add_u32 s68, s71, 131072\nQmvav 100,152,24\ns_cbranch_vccz .Lst21\nbuffer_store_dwordx4 a[24:27], v79, s[56:59], s68 offen offset:1024 sc1\n"
".Lst21:\nQmvaa 100,156,176\nQmvav 104,160,24\ns_add_u32 s68, s71, 147456\nQmvaa 104,164,176\ns_cbranch_vccz .Lst20\nbuffer_store_dwordx4 a[28:31], v79, s[56:59], s68 offen offset:1024 sc1\n.Lst20:\n"
"Qmvav 108,168,24\nQmvaa 108,172,176\ns_waitcnt lgkmcnt(0)\nQmvav 140,168,28\ns_waitcnt vmcnt(16)\ns_barrier\nQmvaa 140,172,180\nQdv 24,63,33024\n"
"s_add_u32 m0, s62, 49536\ns_add_u32 s69, s87, 1572992\nQmvav 136,160,28\nbuffer_load_dwordx4 v62, s[52:55], s69 offen lds\nQmvaa 136,164,180\nQda 176,63,33536\nQmvav 132,152,28\nQta 112,61,0\n"
"Qmvaa 132,156,180\ns_add_u32 s69, s87, 1637496\nQmvav 128,144,28\nbuffer_load_dwordx4 v62, s[52:55], s69 offen offset:1032 lds\nQmvaa 128,148,180\nQdv 28,63,34048\nQmvav 160,144,32\nQda 180,63,34560\n"
"Qmvaa 160,148,184\nQta 114,61,512\ns_add_u32 s69, s87, 1702000\nQmvav 164,152,32\nbuffer_load_dwordx4 v62, s[52:55], s69 offen offset:2064 lds\nQmvaa 164,156,184\nQta 116,61,4128\nQmvav 168,160,32\n"
"Qmvaa 168,164,184\nQta 118,61,4640\ns_add_u32 s69, s87, 1766504\nQmvav 172,168,32\nbuffer_load_dwordx4 v62, s[52:55], s69 offen offset:3096 lds\nQmvaa 172,172,184\nQdv 32,63,35072\nQmvav 204,168,36\n"
"Qda 184,63,35584\nQmvaa 204,172,188\nQta 120,61,8\ns_add_u32 m0, s61, 0\nQmvav 200,160,36\ns_cbranch_vccnz .Ld0q1\nbuffer_load_dwordx4 v69, s[40:43], s60 offen offset:1024 nt lds\n.Ld0q1:\n"
"Qmvaa 200,164,188\nQta 122,61,520\nQmvav 196,152,36\nQmvaa 196,156,188\nQta 124,61,4136\nQmvav 192,144,36\nQdv 36,63,36096\nQmvaa 192,148,188\n"
"Qda 188,63,36608\nQmvav 224,144,40\nQta 126,61,4648\nQmvaa 224,148,192\nQmvav 228,152,40\nQta 128,61,256\nQmvaa 228,156,192\nQmvav 232,160,40\n"
"Qta 130,61,768\nQmvaa 232,164,192\nQmvav 236,168,40\nQdv 40,63,37120\nQmvaa 236,172,192\nQda 192,63,37632\nQmaav 220,168,44\nQta 132,61,4384\n"
"Qmaaa 220,172,196\nQmaav 216,160,44\nQta 134,61,4896\nQmaaa 216,164,196\nQmaav 212,152,44\nQta 136,61,264\nQmaaa 212,156,196\nQmaav 208,144,44\n"
"Qdv 44,63,38144\nQmaaa 208,148,196\nQda 196,63,38656\nQmaav 240,144,48\nQta 138,61,776\nQmaaa 240,148,200\nQmaav 244,152,48\nQta 140,61,4392\n"
"Qmaaa 244,156,200\nQmaav 248,160,48\nQta 142,61,4904\nQmaaa 248,164,200\nQmaav 252,168,48\nQdv 48,63,39168\nQmaaa 252,172,200\nQda 200,63,39680\n"
"s_waitcnt lgkmcnt(2)\nQmvav 80,112,24\nQmvaa 80,116,176\ns_add_u32 s68, s71, 0\nQmvav 84,120,24\ns_cbranch_vccz .Lst19\nbuffer_store_dwordx4 a[32:35], v79, s[56:59], s68 offen offset:2048 sc1\n.Lst19:\n"
"Qmvaa 84,124,176\nQmvav 88,128,24\ns_add_u32 s68, s71, 16384\nQmvaa 88,132,176\ns_cbranch_vccz .Lst18\nbuffer_store_dwordx4 a[36:39], v79, s[56:59], s68 offen offset:2048 sc1\n.Lst18:\nQmvav 92,136,24\n"
"Qmvaa 92,140,176\ns_waitcnt lgkmcnt(0)\nQmvav 124,136,28\ns_waitcnt vmcnt(17)\ns_barrier\nQmvaa 124,140,180\nQta 144,61,16512\ns_add_u32 m0, s62, 0\n"
"s_add_u32 s69, s87, 2097152\nQmvav 120,128,28\nbuffer_load_dwordx4 v62, s[52:55], s69 offen lds\nQmvaa 120,132,180\nQta 146,61,17024\nQmvav 116,120,28\nQmvaa 116,124,180\nQta 148,61,20640\n"
"s_add_u32 s69, s87, 2161656\nQmvav 112,112,28\nbuffer_load_dwordx4 v62, s[52:55], s69 offen offset:1032 lds\nQmvaa 112,116,180\nQta 150,61,21152\nQmvav 144,112,32\nQmvaa 144,116,184\nQta 152,61,16520\n"
"s_add_u32 s69, s87, 2226160\nQmvav 148,120,32\nbuffer_load_dwordx4 v62, s[52:55], s69 offen offset:2064 lds\nQmvaa 148,124,184\nQta 154,61,17032\nQmvav 152,128,32\nQmvaa 152,132,184\nQta 156,61,20648\n"
"s_add_u32 s69, s87, 2290664\nQmvav 156,136,32\nbuffer_load_dwordx4 v62, s[52:55], s69 offen offset:3096 lds\nQmvaa 156,140,184\nQta 158,61,21160\nQmvav 188,136,36\nQmvaa 188,140,188\nQta 160,61,16768\n"
"s_add_u32 m0, s63, 33024\ns_add_u32 s88, s86, 512\nQmvav 184,128,36\nbuffer_load_dwordx4 v64, s[48:51], s88 offen lds\nQmvaa 184,132,188\nQta 162,61,17280\nQmvav 180,120,36\nQmvaa 180,124,188\n"
"Qta 164,61,20896\ns_add_u32 s88, s86, 19968\nQmvav 176,112,36\nbuffer_load_dwordx4 v64, s[48:51], s88 offen offset:1024 lds\nQmvaa 176,116,188\nQta 166,61,21408\nQmvav 208,112,40\nQmvaa 208,116,192\n"
"Qta 168,61,16776\ns_add_u32 s88, s86, 39424\nQmvav 212,120,40\nbuffer_load_dwordx4 v64, s[48:51], s88 offen offset:2048 lds\nQmvaa 212,124,192\nQta 170,61,17288\nQmvav 216,128,40\nQmvaa 216,132,192\n"
"Qta 172,61,20904\ns_add_u32 s88, s86, 58880\nQmvav 220,136,40\nbuffer_load_dwordx4 v64, s[48:51], s88 offen offset:3072 lds\nQmvaa 220,140,192\nQta 174,61,21416\nQmvav 252,136,44\nQmvaa 252,140,196\n"
"s_add_u32 m0, s63, 37120\ns_add_u32 s88, s86, 82432\nQmvav 248,128,44\nbuffer_load_dwordx4 v64, s[48:51], s88 offen lds\nQmvaa 248,132,196\nQmvav 244,120,44\nQmvaa 244,124,196\ns_add_u32 s88, s86, 101888\n"
"Qmvav 240,112,44\nbuffer_load_dwordx4 v64, s[48:51], s88 offen offset:1024 lds\nQmvaa 240,116,196\nQmaav 224,112,48\nQmaaa 224,116,200\ns_add_u32 s88, s86, 121344\nQmaav 228,120,48\nbuffer_load_dwordx4 v64, s[48:51], s88 offen offset:2048 lds\n"
"Qmaaa 228,124,200\nQmaav 232,128,48\nQmaaa 232,132,200\nQmaav 236,136,48\nQmaaa 236,140,200\ns_waitcnt lgkmcnt(0)\nQmvav 96,144,24\nQmvaa 96,148,176\n"
"s_add_u32 s68, s71, 131072\nQmvav 100,152,24\ns_cbranch_vccz .Lst17\nbuffer_store_dwordx4 a[40:43], v79, s[56:59], s68 offen offset:2048 sc1\n.Lst17:\nQmvaa 100,156,176\nQmvav 104,160,24\ns_add_u32 s68, s71, 147456\n"
"Qmvaa 104,164,176\ns_cbranch_vccz .Lst16\nbuffer_store_dwordx4 a[44:47], v79, s[56:59], s68 offen offset:2048 sc1\n.Lst16:\nQmvav 108,168,24\nQmvaa 108,172,176\ns_waitcnt lgkmcnt(0)\nQmvav 140,168,28\n"
"s_waitcnt vmcnt(16)\ns_barrier\nQmvaa 140,172,180\nQdv 24,63,0\ns_add_u32 m0, s62, 16512\ns_add_u32 s69, s87, 2097280\nQmvav 136,160,28\nbuffer_load_dwordx4 v62, s[52:55], s69 offen lds\n"
"Qmvaa 136,164,180\nQda 176,63,512\nQmvav 132,152,28\nQta 112,61,33024\nQmvaa 132,156,180\ns_add_u32 s69, s87, 2161784\nQmvav 128,144,28\nbuffer_load_dwordx4 v62, s[52:55], s69 offen offset:1032 lds\n"
"Qmvaa 128,148,180\nQdv 28,63,1024\nQmvav 160,144,32\nQda 180,63,1536\nQmvaa 160,148,184\nQta 114,61,33536\ns_add_u32 s69, s87, 2226288\nQmvav 164,152,32\n"
"buffer_load_dwordx4 v62, s[52:55], s69 offen offset:2064 lds\nQmvaa 164,156,184\nQta 116,61,37152\nQmvav 168,160,32\nQmvaa 168,164,184\nQta 118,61,37664\ns_add_u32 s69, s87, 2290792\nQmvav 172,168,32\n"
"buffer_load_dwordx4 v62, s[52:55], s69 offen offset:3096 lds\nQmvaa 172,172,184\nQdv 32,63,2048\nQmvav 204,168,36\nQda 184,63,2560\nQmvaa 204,172,188\nQta 120,61,33032\ns_add_u32 m0, s61, 0\n"
"Qmvav 200,160,36\ns_cbranch_vccnz .Ld0q2\nbuffer_load_dwordx4 v69, s[40:43], s90 offen offset:2048 nt lds\n.Ld0q2:\nQmvaa 200,164,188\nQta 122,61,33544\nQmvav 196,152,36\nQmvaa 196,156,188\n"
"Qta 124,61,37160\nQmvav 192,144,36\nQdv 36,63,3072\nQmvaa 192,148,188\nQda 188,63,3584\nQmvav 224,144,40\nQta 126,61,37672\nQmvaa 224,148,192\n"
"Qmvav 228,152,40\nQta 128,61,33280\nQmvaa 228,156,192\nQmvav 232,160,40\nQta 130,61,33792\nQmvaa 232,164,192\nQmvav 236,168,40\nQdv 40,63,4096\n"
"Qmvaa 236,172,192\nQda 192,63,4608\nQmaav 220,168,44\nQta 132,61,37408\nQmaaa 220,172,196\nQmaav 216,160,44\nQta 134,61,37920\nQmaaa 216,164,196\n"
"Qmaav 212,152,44\nQta 136,61,33288\nQmaaa 212,156,196\nQmaav 208,144,44\nQdv 44,63,5120\nQmaaa 208,148,196\nQda 196,63,5632\nQmaav 240,144,48\n"
"Qta 138,61,33800\nQmaaa 240,148,200\nQmaav 244,152,48\nQta 140,61,37416\nQmaaa 244,156,200\nQmaav 248,160,48\nQta 142,61,37928\nQmaaa 248,164,200\n"
"Qmaav 252,168,48\nQdv 48,63,6144\nQmaaa 252,172,200\nQda 200,63,6656\ns_waitcnt lgkmcnt(2)\nQmvav 80,112,24\nQmvaa 80,116,176\ns_add_u32 s68, s71, 0\n"
"Qmvav 84,120,24\ns_cbranch_vccz .Lst15\nbuffer_store_dwordx4 a[48:51], v79, s[56:59], s68 offen offset:3072 sc1\n.Lst15:\nQmvaa 84,124,176\nQmvav 88,128,24\ns_add_u32 s68, s71, 16384\nQmvaa 88,132,176\n"
"s_cbranch_vccz .Lst14\nbuffer_store_dwordx4 a[52:55], v79, s[56:59], s68 offen offset:3072 sc1\n.Lst14:\nQmvav 92,136,24\nQmvaa 92,140,176\ns_waitcnt lgkmcnt(0)\nQmvav 124,136,28\ns_waitcnt vmcnt(17)\n"
"s_barrier\nQmvaa 124,140,180\nQta 144,61,49536\ns_add_u32 m0, s62, 33024\ns_add_u32 s69, s87, 2621440\ns_mov_b32 s89, 0\nQmvav 120,128,28\nbuffer_load_dwordx4 v62, s[52:55], s69 offen lds\n"
"s_cbranch_vccnz .Lpb0\ns_load_dword s89, s[30:31], 0x0\n.Lpb0:\nQmvaa 120,132,180\nQta 146,61,50048\nQmvav 116,120,28\nQmvaa 116,124,180\nQta 148,61,53664\n"
"s_add_u32 s69, s87, 2685944\nQmvav 112,112,28\nbuffer_load_dwordx4 v62, s[52:55], s69 offen offset:1032 lds\ns_add_u32 s77, s77, s89\nQmvaa 112,116,180\nQta 150,61,54176\nQmvav 144,112,32\ns_add_u32 s77, s77, s89\n"
"Qmvaa 144,116,184\nQta 152,61,49544\ns_add_u32 s69, s87, 2750448\nQmvav 148,120,32\nbuffer_load_dwordx4 v62, s[52:55], s69 offen offset:2064 lds\ns_add_u32 s77, s77, s89\nQmvaa 148,124,184\nQta 154,61,50056\n"
"Qmvav 152,128,32\ns_add_u32 s77, s77, s89\nQmvaa 152,132,184\nQta 156,61,53672\ns_add_u32 s69, s87, 2814952\nQmvav 156,136,32\nbuffer_load_dwordx4 v62, s[52:55], s69 offen offset:3096 lds\ns_add_u32 s77, s77, s89\n"
"Qmvaa 156,140,184\nQta 158,61,54184\nQmvav 188,136,36\ns_add_u32 s77, s77, s89\nQmvaa 188,140,188\nQta 160,61,49792\ns_add_u32 m0, s63, 0\ns_add_u32 s88, s86, 640\n"
"Qmvav 184,128,36\nbuffer_load_dwordx4 v64, s[48:51], s88 offen lds\ns_add_u32 s77, s77, s89\nQmvaa 184,132,188\nQta 162,61,50304\nQmvav 180,120,36\ns_add_u32 s77, s77, s89\nQmvaa 180,124,188\n"
"Qta 164,61,53920\ns_add_u32 s88, s86, 20096\nQmvav 176,112,36\nbuffer_load_dwordx4 v64, s[48:51], s88 offen offset:1024 lds\ns_add_u32 s77, s77, s89\nQmvaa 176,116,188\nQta 166,61,54432\nQmvav 208,112,40\n"
"s_add_u32 s77, s77, s89\nQmvaa 208,116,192\n.Lpg1:\ns_cbranch_execz .Ltr0\nQta 168,61,49800\ns_add_u32 s88, s86, 39552\nQmvav 212,120,40\nbuffer_load_dwordx4 v64, s[48:51], s88 offen offset:2048 lds\n"
"s_add_u32 s77, s77, s89\nQmvaa 212,124,192\nQta 170,61,50312\nQmvav 216,128,40\ns_add_u32 s77, s77, s89\nQmvaa 216,132,192\nQta 172,61,53928\ns_add_u32 s88, s86, 59008\n"
"Qmvav 220,136,40\nbuffer_load_dwordx4 v64, s[48:51], s88 offen offset:3072 lds\ns_add_u32 s77, s77, s89\nQmvaa 220,140,192\nQta 174,61,54440\nQmvav 252,136,44\ns_add_u32 s77, s77, s89\nQmvaa 252,140,196\n"
"s_add_u32 m0, s63, 4096\ns_add_u32 s88, s86, 82560\nQmvav 248,128,44\nbuffer_load_dwordx4 v64, s[48:51], s88 offen lds\ns_add_u32 s77, s77, s89\nQmvaa 248,132,196\nQmvav 244,120,44\ns_add_u32 s77, s77, s89\n"
"Qmvaa 244,124,196\ns_add_u32 s88, s86, 102016\nQmvav 240,112,44\nbuffer_load_dwordx4 v64, s[48:51], s88 offen offset:1024 lds\ns_add_u32 s77, s77, s89\nQmvaa 240,116,196\nQmaav 224,112,48\ns_add_u32 s77, s77, s89\n"
"Qmaaa 224,116,200\ns_add_u32 s88, s86, 121472\nQmaav 228,120,48\nbuffer_load_dwordx4 v64, s[48:51], s88 offen offset:2048 lds\ns_add_u32 s77, s77, s89\nQmaaa 228,124,200\nQmaav 232,128,48\ns_add_u32 s77, s77, s89\n"
"Qmaaa 232,132,200\nQmaav 236,136,48\ns_add_u32 s77, s77, s89\nQmaaa 236,140,200\ns_waitcnt lgkmcnt(0)\nQmvav 96,144,24\nQmvaa 96,148,176\ns_add_u32 s68, s71, 131072\n"
"Qmvav 100,152,24\ns_cbranch_vccz .Lst13\nbuffer_store_dwordx4 a[56:59], v79, s[56:59], s68 offen offset:3072 sc1\n.Lst13:\nQmvaa 100,156,176\nQmvav 104,160,24\ns_add_u32 s68, s71, 147456\nQmvaa 104,164,176\n"
"s_cbranch_vccz .Lst12\nbuffer_store_dwordx4 a[60:63], v79, s[56:59], s68 offen offset:3072 sc1\n.Lst12:\nQmvav 108,168,24\nQmvaa 108,172,176\ns_waitcnt lgkmcnt(0)\nQmvav 140,168,28\ns_waitcnt vmcnt(16)\n"
"s_barrier\nQmvaa 140,172,180\nQdv 24,63,33024\ns_add_u32 m0, s62, 49536\ns_add_u32 s69, s87, 2621568\nQmvav 136,160,28\nbuffer_load_dwordx4 v62, s[52:55], s69 offen lds\nQmvaa 136,164,180\n"
"Qda 176,63,33536\nQmvav 132,152,28\nQta 112,61,0\nQmvaa 132,156,180\ns_add_u32 s69, s87, 2686072\nQmvav 128,144,28\nbuffer_load_dwordx4 v62, s[52:55], s69 offen offset:1032 lds\nQmvaa 128,148,180\n"
"Qdv 28,63,34048\nQmvav 160,144,32\nQda 180,63,34560\nQmvaa 160,148,184\nQta 114,61,512\ns_add_u32 s69, s87, 2750576\nQmvav 164,152,32\nbuffer_load_dwordx4 v62, s[52:55], s69 offen offset:2064 lds\n"
"Qmvaa 164,156,184\nQta 116,61,4128\nQmvav 168,160,32\nQmvaa 168,164,184\nQta 118,61,4640\ns_add_u32 s69, s87, 2815080\nQmvav 172,168,32\nbuffer_load_dwordx4 v62, s[52:55], s69 offen offset:3096 lds\n"
"Qmvaa 172,172,184\nQdv 32,63,35072\nQmvav 204,168,36\nQda 184,63,35584\nQmvaa 204,172,188\nQta 120,61,8\ns_add_u32 m0, s61, 0\nQmvav 200,160,36\n"
"s_cbranch_vccnz .Ld0q3\nbuffer_load_dwordx4 v69, s[40:43], s90 offen offset:3072 nt lds\n.Ld0q3:\nQmvaa 200,164,188\nQta 122,61,520\nQmvav 196,152,36\nQmvaa 196,156,188\nQta 124,61,4136\n"
"Qmvav 192,144,36\nQdv 36,63,36096\nQmvaa 192,148,188\nQda 188,63,36608\nQmvav 224,144,40\nQta 126,61,4648\nQmvaa 224,148,192\nQmvav 228,152,40\n"
"Qta 128,61,256\nQmvaa 228,156,192\nQmvav 232,160,40\nQta 130,61,768\nQmvaa 232,164,192\nQmvav 236,168,40\nQdv 40,63,37120\nQmvaa 236,172,192\n"
"Qda 192,63,37632\nQmaav 220,168,44\nQta 132,61,4384\nQmaaa 220,172,196\nQmaav 216,160,44\nQta 134,61,4896\nQmaaa 216,164,196\nQmaav 212,152,44\n"
"Qta 136,61,264\nQmaaa 212,156,196\nQmaav 208,144,44\nQdv 44,63,38144\nQmaaa 208,148,196\nQda 196,63,38656\nQmaav 240,144,48\nQta 138,61,776\n"
"Qmaaa 240,148,200\nQmaav 244,152,48\nQta 140,61,4392\nQmaaa 244,156,200\nQmaav 248,160,48\nQta 142,61,4904\nQmaaa 248,164,200\nQmaav 252,168,48\n"
"Qdv 48,63,39168\nQmaaa 252,172,200\nQda 200,63,39680\ns_waitcnt lgkmcnt(2)\nQmvav 80,112,24\nQmvaa 80,116,176\ns_add_u32 s68, s71, 4096\nQmvav 84,120,24\n"
"s_cbranch_vccz .Lst11\nbuffer_store_dwordx4 a[64:67], v79, s[56:59], s68 offen offset:0 sc1\n.Lst11:\nQmvaa 84,124,176\nQmvav 88,128,24\ns_add_u32 s68, s71, 20480\nQmvaa 88,132,176\ns_cbranch_vccz .Lst10\n"
"buffer_store_dwordx4 a[68:71], v79, s[56:59], s68 offen offset:0 sc1\n.Lst10:\nQmvav 92,136,24\nQmvaa 92,140,176\ns_waitcnt lgkmcnt(0)\nQmvav 124,136,28\ns_waitcnt vmcnt(17)\ns_barrier\n"
"Qmvaa 124,140,180\nQta 144,61,16512\ns_add_u32 m0, s62, 0\ns_add_u32 s69, s87, 3145728\ns_mov_b32 s89, 0\nQmvav 120,128,28\nbuffer_load_dwordx4 v62, s[52:55], s69 offen lds\ns_cbranch_vccnz .Lpb1\n"
"s_load_dword s89, s[30:31], 0x2000\n.Lpb1:\nQmvaa 120,132,180\nQta 146,61,17024\nQmvav 116,120,28\nQmvaa 116,124,180\nQta 148,61,20640\ns_add_u32 s69, s87, 3210232\n"
"Qmvav 112,112,28\nbuffer_load_dwordx4 v62, s[52:55], s69 offen offset:1032 lds\ns_add_u32 s77, s77, s89\nQmvaa 112,116,180\nQta 150,61,21152\nQmvav 144,112,32\ns_add_u32 s77, s77, s89\nQmvaa 144,116,184\n"
"Qta 152,61,16520\ns_add_u32 s69, s87, 3274736\nQmvav 148,120,32\nbuffer_load_dwordx4 v62, s[52:55], s69 offen offset:2064 lds\ns_add_u32 s77, s77, s89\nQmvaa 148,124,184\nQta 154,61,17032\nQmvav 152,128,32\n"
"s_add_u32 s77, s77, s89\nQmvaa 152,132,184\nQta 156,61,20648\ns_add_u32 s69, s87, 3339240\nQmvav 156,136,32\nbuffer_load_dwordx4 v62, s[52:55], s69 offen offset:3096 lds\ns_add_u32 s77, s77, s89\nQmvaa 156,140,184\n"
"Qta 158,61,21160\nQmvav 188,136,36\ns_add_u32 s77, s77, s89\nQmvaa 188,140,188\nQta 160,61,16768\ns_add_u32 m0, s63, 33024\ns_add_u32 s88, s86, 768\nQmvav 184,128,36\n"
"buffer_load_dwordx4 v64, s[48:51], s88 offen lds\ns_add_u32 s77, s77, s89\nQmvaa 184,132,188\nQta 162,61,17280\nQmvav 180,120,36\ns_add_u32 s77, s77, s89\nQmvaa 180,124,188\nQta 164,61,20896\n"
"s_add_u32 s88, s86, 20224\nQmvav 176,112,36\nbuffer_load_dwordx4 v64, s[48:51], s88 offen offset:1024 lds\ns_add_u32 s77, s77, s89\nQmvaa 176,116,188\nQta 166,61,21408\nQmvav 208,112,40\ns_add_u32 s77, s77, s89\n"
"Qmvaa 208,116,192\nQta 168,61,16776\ns_add_u32 s88, s86, 39680\nQmvav 212,120,40\nbuffer_load_dwordx4 v64, s[48:51], s88 offen offset:2048 lds\ns_add_u32 s77, s77, s89\nQmvaa 212,124,192\nQta 170,61,17288\n"
"Qmvav 216,128,40\ns_add_u32 s77, s77, s89\nQmvaa 216,132,192\nQta 172,61,20904\ns_add_u32 s88, s86, 59136\nQmvav 220,136,40\nbuffer_load_dwordx4 v64, s[48:51], s88 offen offset:3072 lds\ns_add_u32 s77, s77, s89\n"
"Qmvaa 220,140,192\nQta 174,61,21416\nQmvav 252,136,44\ns_add_u32 s77, s77, s89\nQmvaa 252,140,196\ns_add_u32 m0, s63, 37120\ns_add_u32 s88, s86, 82688\nQmvav 248,128,44\n"
"buffer_load_dwordx4 v64, s[48:51], s88 offen lds\ns_add_u32 s77, s77, s89\nQmvaa 248,132,196\nQmvav 244,120,44\ns_add_u32 s77, s77, s89\nQmvaa 244,124,196\ns_add_u32 s88, s86, 102144\nQmvav 240,112,44\n"
"buffer_load_dwordx4 v64, s[48:51], s88 offen offset:1024 lds\ns_add_u32 s77, s77, s89\nQmvaa 240,116,196\nQmaav 224,112,48\ns_add_u32 s77, s77, s89\nQmaaa 224,116,200\ns_add_u32 s88, s86, 121600\nQmaav 228,120,48\n"
"buffer_load_dwordx4 v64, s[48:51], s88 offen offset:2048 lds\ns_add_u32 s77, s77, s89\nQmaaa 228,124,200\nQmaav 232,128,48\ns_add_u32 s77, s77, s89\nQmaaa 232,132,200\nQmaav 236,136,48\ns_add_u32 s77, s77, s89\n"
"Qmaaa 236,140,200\ns_waitcnt lgkmcnt(0)\nQmvav 96,144,24\nQmvaa 96,148,176\ns_add_u32 s68, s71, 135168\nQmvav 100,152,24\ns_cbranch_vccz .Lst9\nbuffer_store_dwordx4 a[72:75], v79, s[56:59], s68 offen offset:0 sc1\n"
".Lst9:\nQmvaa 100,156,176\nQmvav 104,160,24\ns_add_u32 s68, s71, 151552\nQmvaa 104,164,176\ns_cbranch_vccz .Lst8\nbuffer_store_dwordx4 a[76:79], v79, s[56:59], s68 offen offset:0 sc1\n.Lst8:\n"
"Qmvav 108,168,24\nQmvaa 108,172,176\ns_waitcnt lgkmcnt(0)\nQmvav 140,168,28\ns_waitcnt vmcnt(16)\ns_barrier\nQmvaa 140,172,180\nQdv 24,63,0\n"
"s_add_u32 m0, s62, 16512\ns_add_u32 s69, s87, 3145856\nQmvav 136,160,28\nbuffer_load_dwordx4 v62, s[52:55], s69 offen lds\nQmvaa 136,164,180\nQda 176,63,512\nQmvav 132,152,28\nQta 112,61,33024\n"
"Qmvaa 132,156,180\ns_add_u32 s69, s87, 3210360\nQmvav 128,144,28\nbuffer_load_dwordx4 v62, s[52:55], s69 offen offset:1032 lds\nQmvaa 128,148,180\nQdv 28,63,1024\nQmvav 160,144,32\nQda 180,63,1536\n"
"Qmvaa 160,148,184\nQta 114,61,33536\ns_add_u32 s69, s87, 3274864\nQmvav 164,152,32\nbuffer_load_dwordx4 v62, s[52:55], s69 offen offset:2064 lds\nQmvaa 164,156,184\nQta 116,61,37152\nQmvav 168,160,32\n"
"Qmvaa 168,164,184\nQta 118,61,37664\ns_add_u32 s69, s87, 3339368\nQmvav 172,168,32\nbuffer_load_dwordx4 v62, s[52:55], s69 offen offset:3096 lds\nQmvaa 172,172,184\nQdv 32,63,2048\nQmvav 204,168,36\n"
"Qda 184,63,2560\nQmvaa 204,172,188\nQta 120,61,33032\ns_add_u32 m0, s64, 0\nQmvav 200,160,36\ns_cbranch_vccnz .Ld0q4\nbuffer_load_dwordx4 v72, s[44:47], s60 offen nt lds\n.Ld0q4:\n"
"Qmvaa 200,164,188\nQta 122,61,33544\nQmvav 196,152,36\nQmvaa 196,156,188\nQta 124,61,37160\nQmvav 192,144,36\nQdv 36,63,3072\nQmvaa 192,148,188\n"
"Qda 188,63,3584\nQmvav 224,144,40\nQta 126,61,37672\nQmvaa 224,148,192\nQmvav 228,152,40\nQta 128,61,33280\nQmvaa 228,156,192\nQmvav 232,160,40\n"
"Qta 130,61,33792\nQmvaa 232,164,192\nQmvav 236,168,40\nQdv 40,63,4096\nQmvaa 236,172,192\nQda 192,63,4608\nQmaav 220,168,44\nQta 132,61,37408\n"
"Qmaaa 220,172,196\nQmaav 216,160,44\nQta 134,61,37920\nQmaaa 216,164,196\nQmaav 212,152,44\nQta 136,61,33288\nQmaaa 212,156,196\nQmaav 208,144,44\n"
"Qdv 44,63,5120\nQmaaa 208,148,196\nQda 196,63,5632\nQmaav 240,144,48\nQta 138,61,33800\nQmaaa 240,148,200\nQmaav 244,152,48\nQta 140,61,37416\n"
"Qmaaa 244,156,200\nQmaav 248,160,48\nQta 142,61,37928\nQmaaa 248,164,200\nQmaav 252,168,48\nQdv 48,63,6144\nQmaaa 252,172,200\nQda 200,63,6656\n"
"s_waitcnt lgkmcnt(2)\nQmvav 80,112,24\nQmvaa 80,116,176\ns_add_u32 s68, s71, 4096\nQmvav 84,120,24\ns_cbranch_vccz .Lst7\nbuffer_store_dwordx4 a[80:83], v79, s[56:59], s68 offen offset:1024 sc1\n.Lst7:\n"
"Qmvaa 84,124,176\nQmvav 88,128,24\ns_add_u32 s68, s71, 20480\nQmvaa 88,132,176\ns_cbranch_vccz .Lst6\nbuffer_store_dwordx4 a[84:87], v79, s[56:59], s68 offen offset:1024 sc1\n.Lst6:\nQmvav 92,136,24\n"
"Qmvaa 92,140,176\ns_waitcnt lgkmcnt(0)\nQmvav 124,136,28\ns_waitcnt vmcnt(17)\ns_barrier\nQmvaa 124,140,180\nQta 144,61,49536\ns_add_u32 m0, s62, 33024\n"
"s_add_u32 s69, s87, 3670016\ns_mov_b32 s89, 0\nQmvav 120,128,28\nbuffer_load_dwordx4 v62, s[52:55], s69 offen lds\ns_cbranch_vccnz .Lpb2\ns_load_dword s89, s[30:31], 0x6000\n.Lpb2:\nQmvaa 120,132,180\n"
"Qta 146,61,50048\nQmvav 116,120,28\nQmvaa 116,124,180\nQta 148,61,53664\ns_add_u32 s69, s87, 3734520\nQmvav 112,112,28\nbuffer_load_dwordx4 v62, s[52:55], s69 offen offset:1032 lds\ns_add_u32 s77, s77, s89\n"
"Qmvaa 112,116,180\nQta 150,61,54176\nQmvav 144,112,32\ns_add_u32 s77, s77, s89\nQmvaa 144,116,184\nQta 152,61,49544\ns_add_u32 s69, s87, 3799024\nQmvav 148,120,32\n"
"buffer_load_dwordx4 v62, s[52:55], s69 offen offset:2064 lds\ns_add_u32 s77, s77, s89\nQmvaa 148,124,184\nQta 154,61,50056\nQmvav 152,128,32\ns_add_u32 s77, s77, s89\nQmvaa 152,132,184\nQta 156,61,53672\n"
"s_add_u32 s69, s87, 3863528\nQmvav 156,136,32\nbuffer_load_dwordx4 v62, s[52:55], s69 offen offset:3096 lds\ns_add_u32 s77, s77, s89\nQmvaa 156,140,184\nQta 158,61,54184\nQmvav 188,136,36\ns_add_u32 s77, s77, s89\n"
"Qmvaa 188,140,188\nQta 160,61,49792\ns_add_u32 m0, s63, 0\ns_add_u32 s88, s86, 896\nQmvav 184,128,36\nbuffer_load_dwordx4 v64, s[48:51], s88 offen lds\ns_add_u32 s77, s77, s89\nQmvaa 184,132,188\n"
"Qta 162,61,50304\nQmvav 180,120,36\ns_add_u32 s77, s77, s89\nQmvaa 180,124,188\nQta 164,61,53920\ns_add_u32 s88, s86, 20352\nQmvav 176,112,36\nbuffer_load_dwordx4 v64, s[48:51], s88 offen offset:1024 lds\n"
"s_add_u32 s77, s77, s89\nQmvaa 176,116,188\nQta 166,61,54432\nQmvav 208,112,40\ns_add_u32 s77, s77, s89\nQmvaa 208,116,192\nQta 168,61,49800\ns_add_u32 s88, s86, 39808\n"
"Qmvav 212,120,40\nbuffer_load_dwordx4 v64, s[48:51], s88 offen offset:2048 lds\ns_add_u32 s77, s77, s89\nQmvaa 212,124,192\nQta 170,61,50312\nQmvav 216,128,40\ns_add_u32 s77, s77, s89\nQmvaa 216,132,192\n"
"Qta 172,61,53928\ns_add_u32 s88, s86, 59264\nQmvav 220,136,40\nbuffer_load_dwordx4 v64, s[48:51], s88 offen offset:3072 lds\ns_add_u32 s77, s77, s89\nQmvaa 220,140,192\nQta 174,61,54440\nQmvav 252,136,44\n"
"s_add_u32 s77, s77, s89\nQmvaa 252,140,196\ns_add_u32 m0, s63, 4096\ns_add_u32 s88, s86, 82816\nQmvav 248,128,44\nbuffer_load_dwordx4 v64, s[48:51], s88 offen lds\ns_add_u32 s77, s77, s89\nQmvaa 248,132,196\n"
"Qmvav 244,120,44\ns_add_u32 s77, s77, s89\nQmvaa 244,124,196\ns_add_u32 s88, s86, 102272\nQmvav 240,112,44\nbuffer_load_dwordx4 v64, s[48:51], s88 offen offset:1024 lds\ns_add_u32 s77, s77, s89\nQmvaa 240,116,196\n"
"Qmaav 224,112,48\ns_add_u32 s77, s77, s89\nQmaaa 224,116,200\ns_add_u32 s88, s86, 121728\nQmaav 228,120,48\nbuffer_load_dwordx4 v64, s[48:51], s88 offen offset:2048 lds\ns_add_u32 s77, s77, s89\nQmaaa 228,124,200\n"
"Qmaav 232,128,48\ns_add_u32 s77, s77, s89\nQmaaa 232,132,200\nQmaav 236,136,48\ns_add_u32 s77, s77, s89\nQmaaa 236,140,200\ns_waitcnt lgkmcnt(0)\nQmvav 96,144,24\n"
"Qmvaa 96,148,176\ns_add_u32 s68, s71, 135168\nQmvav 100,152,24\ns_cbranch_vccz .Lst5\nbuffer_store_dwordx4 a[88:91], v79, s[56:59], s68 offen offset:1024 sc1\n.Lst5:\nQmvaa 100,156,176\nQmvav 104,160,24\n"
"s_add_u32 s68, s71, 151552\nQmvaa 104,164,176\ns_cbranch_vccz .Lst4\nbuffer_store_dwordx4 a[92:95], v79, s[56:59], s68 offen offset:1024 sc1\n.Lst4:\nQmvav 108,168,24\nQmvaa 108,172,176\ns_waitcnt lgkmcnt(0)\n"
"Qmvav 140,168,28\ns_waitcnt vmcnt(16)\ns_barrier\nQmvaa 140,172,180\nQdv 24,63,33024\ns_add_u32 m0, s62, 49536\ns_add_u32 s69, s87, 3670144\nQmvav 136,160,28\n"
"buffer_load_dwordx4 v62, s[52:55], s69 offen lds\nQmvaa 136,164,180\nQda 176,63,33536\nQmvav 132,152,28\nQta 112,61,0\nQmvaa 132,156,180\ns_add_u32 s69, s87, 3734648\nQmvav 128,144,28\n"
".Lpg2:\ns_cbranch_execz .Ltr0\nbuffer_load_dwordx4 v62, s[52:55], s69 offen offset:1032 lds\nQmvaa 128,148,180\nQdv 28,63,34048\nQmvav 160,144,32\nQda 180,63,34560\nQmvaa 160,148,184\n"
"Qta 114,61,512\ns_add_u32 s69, s87, 3799152\nQmvav 164,152,32\nbuffer_load_dwordx4 v62, s[52:55], s69 offen offset:2064 lds\nQmvaa 164,156,184\nQta 116,61,4128\nQmvav 168,160,32\nQmvaa 168,164,184\n"
"Qta 118,61,4640\ns_add_u32 s69, s87, 3863656\nQmvav 172,168,32\nbuffer_load_dwordx4 v62, s[52:55], s69 offen offset:3096 lds\nQmvaa 172,172,184\nQdv 32,63,35072\nQmvav 204,168,36\nQda 184,63,35584\n"
"Qmvaa 204,172,188\nQta 120,61,8\ns_add_u32 m0, s64, 0\nQmvav 200,160,36\ns_cbranch_vccnz .Ld0q5\nbuffer_load_dwordx4 v72, s[44:47], s60 offen offset:1024 nt lds\n.Ld0q5:\nQmvaa 200,164,188\n"
"Qta 122,61,520\nQmvav 196,152,36\nQmvaa 196,156,188\nQta 124,61,4136\nQmvav 192,144,36\nQdv 36,63,36096\nQmvaa 192,148,188\nQda 188,63,36608\n"
"Qmvav 224,144,40\nQta 126,61,4648\nQmvaa 224,148,192\nQmvav 228,152,40\nQta 128,61,256\nQmvaa 228,156,192\nQmvav 232,160,40\nQta 130,61,768\n"
"Qmvaa 232,164,192\nQmvav 236,168,40\nQdv 40,63,37120\nQmvaa 236,172,192\nQda 192,63,37632\nQmaav 220,168,44\nQta 132,61,4384\nQmaaa 220,172,196\n"
"Qmaav 216,160,44\nQta 134,61,4896\nQmaaa 216,164,196\nQmaav 212,152,44\nQta 136,61,264\nQmaaa 212,156,196\nQmaav 208,144,44\nQdv 44,63,38144\n"
"Qmaaa 208,148,196\nQda 196,63,38656\nQmaav 240,144,48\nQta 138,61,776\nQmaaa 240,148,200\nQmaav 244,152,48\nQta 140,61,4392\nQmaaa 244,156,200\n"
"Qmaav 248,160,48\nQta 142,61,4904\nQmaaa 248,164,200\nQmaav 252,168,48\nQdv 48,63,39168\nQmaaa 252,172,200\nQda 200,63,39680\ns_waitcnt lgkmcnt(2)\n"
"Qmvav 80,112,24\nQmvaa 80,116,176\ns_add_u32 s68, s71, 4096\nQmvav 84,120,24\ns_cbranch_vccz .Lst3\nbuffer_store_dwordx4 a[96:99], v79, s[56:59], s68 offen offset:2048 sc1\n.Lst3:\nQmvaa 84,124,176\n"
"Qmvav 88,128,24\ns_add_u32 s68, s71, 20480\nQmvaa 88,132,176\ns_cbranch_vccz .Lst2\nbuffer_store_dwordx4 a[100:103], v79, s[56:59], s68 offen offset:2048 sc1\n.Lst2:\nQmvav 92,136,24\nQmvaa 92,140,176\n"
"s_waitcnt lgkmcnt(0)\nQmvav 124,136,28\ns_waitcnt vmcnt(17)\ns_barrier\nQmvaa 124,140,180\nQta 144,61,16512\ns_add_u32 m0, s62, 0\ns_add_u32 s69, s87, 4194304\n"
"s_mov_b32 s89, 0\nQmvav 120,128,28\nbuffer_load_dwordx4 v62, s[52:55], s69 offen lds\ns_cbranch_vccnz .Lpb3\ns_load_dword s89, s[30:31], 0x4000\n.Lpb3:\nQmvaa 120,132,180\nQta 146,61,17024\n"
"Qmvav 116,120,28\nQmvaa 116,124,180\nQta 148,61,20640\ns_add_u32 s69, s87, 4258808\nQmvav 112,112,28\nbuffer_load_dwordx4 v62, s[52:55], s69 offen offset:1032 lds\ns_add_u32 s77, s77, s89\nQmvaa 112,116,180\n"
"Qta 150,61,21152\nQmvav 144,112,32\ns_add_u32 s77, s77, s89\nQmvaa 144,116,184\nQta 152,61,16520\ns_add_u32 s69, s87, 4323312\nQmvav 148,120,32\nbuffer_load_dwordx4 v62, s[52:55], s69 offen offset:2064 lds\n"
"s_add_u32 s77, s77, s89\nQmvaa 148,124,184\nQta 154,61,17032\nQmvav 152,128,32\ns_add_u32 s77, s77, s89\nQmvaa 152,132,184\nQta 156,61,20648\ns_add_u32 s69, s87, 4387816\n"
"Qmvav 156,136,32\nbuffer_load_dwordx4 v62, s[52:55], s69 offen offset:3096 lds\ns_add_u32 s77, s77, s89\nQmvaa 156,140,184\nQta 158,61,21160\nQmvav 188,136,36\ns_add_u32 s77, s77, s89\nQmvaa 188,140,188\n"
"Qta 160,61,16768\ns_add_u32 m0, s63, 33024\ns_add_u32 s88, s86, 1024\nQmvav 184,128,36\nbuffer_load_dwordx4 v64, s[48:51], s88 offen lds\ns_add_u32 s77, s77, s89\nQmvaa 184,132,188\nQta 162,61,17280\n"
"Qmvav 180,120,36\ns_add_u32 s77, s77, s89\nQmvaa 180,124,188\nQta 164,61,20896\ns_add_u32 s88, s86, 20480\nQmvav 176,112,36\nbuffer_load_dwordx4 v64, s[48:51], s88 offen offset:1024 lds\ns_add_u32 s77, s77, s89\n"
"Qmvaa 176,116,188\nQta 166,61,21408\nQmvav 208,112,40\ns_add_u32 s77, s77, s89\nQmvaa 208,116,192\nQta 168,61,16776\ns_add_u32 s88, s86, 39936\nQmvav 212,120,40\n"
"buffer_load_dwordx4 v64, s[48:51], s88 offen offset:2048 lds\ns_add_u32 s77, s77, s89\nQmvaa 212,124,192\nQta 170,61,17288\nQmvav 216,128,40\ns_add_u32 s77, s77, s89\nQmvaa 216,132,192\nQta 172,61,20904\n"
"s_add_u32 s88, s86, 59392\nQmvav 220,136,40\nbuffer_load_dwordx4 v64, s[48:51], s88 offen offset:3072 lds\ns_add_u32 s77, s77, s89\nQmvaa 220,140,192\nQta 174,61,21416\nQmvav 252,136,44\ns_add_u32 s77, s77, s89\n"
"Qmvaa 252,140,196\ns_add_u32 m0, s63, 37120\ns_add_u32 s88, s86, 82944\nQmvav 248,128,44\nbuffer_load_dwordx4 v64, s[48:51], s88 offen lds\ns_add_u32 s77, s77, s89\nQmvaa 248,132,196\nQmvav 244,120,44\n"
"s_add_u32 s77, s77, s89\nQmvaa 244,124,196\ns_add_u32 s88, s86, 102400\nQmvav 240,112,44\nbuffer_load_dwordx4 v64, s[48:51], s88 offen offset:1024 lds\ns_add_u32 s77, s77, s89\nQmvaa 240,116,196\nQmaav 224,112,48\n"
"s_add_u32 s77, s77, s89\nQmaaa 224,116,200\ns_add_u32 s88, s86, 121856\nQmaav 228,120,48\nbuffer_load_dwordx4 v64, s[48:51], s88 offen offset:2048 lds\ns_add_u32 s77, s77, s89\nQmaaa 228,124,200\nQmaav 232,128,48\n"
"s_add_u32 s77, s77, s89\nQmaaa 232,132,200\nQmaav 236,136,48\ns_add_u32 s77, s77, s89\nQmaaa 236,140,200\ns_waitcnt lgkmcnt(0)\nQmvav 96,144,24\nQmvaa 96,148,176\n"
"s_add_u32 s68, s71, 135168\nQmvav 100,152,24\ns_cbranch_vccz .Lst1\nbuffer_store_dwordx4 a[104:107], v79, s[56:59], s68 offen offset:2048 sc1\n.Lst1:\nQmvaa 100,156,176\nQmvav 104,160,24\ns_add_u32 s68, s71, 151552\n"
"Qmvaa 104,164,176\ns_cbranch_vccz .Lst0\nbuffer_store_dwordx4 a[108:111], v79, s[56:59], s68 offen offset:2048 sc1\n.Lst0:\nQmvav 108,168,24\nQmvaa 108,172,176\ns_waitcnt lgkmcnt(0)\nQmvav 140,168,28\n"
"s_waitcnt vmcnt(16)\ns_barrier\nQmvaa 140,172,180\nQdv 24,63,0\ns_add_u32 m0, s62, 16512\ns_add_u32 s69, s87, 4194432\nQmvav 136,160,28\nbuffer_load_dwordx4 v62, s[52:55], s69 offen lds\n"
"Qmvaa 136,164,180\nQda 176,63,512\nQmvav 132,152,28\nQta 112,61,33024\nQmvaa 132,156,180\ns_add_u32 s69, s87, 4258936\nQmvav 128,144,28\nbuffer_load_dwordx4 v62, s[52:55], s69 offen offset:1032 lds\n"
"Qmvaa 128,148,180\nQdv 28,63,1024\nQmvav 160,144,32\nQda 180,63,1536\nQmvaa 160,148,184\nQta 114,61,33536\ns_add_u32 s69, s87, 4323440\nQmvav 164,152,32\n"
"buffer_load_dwordx4 v62, s[52:55], s69 offen offset:2064 lds\nQmvaa 164,156,184\nQta 116,61,37152\nQmvav 168,160,32\nQmvaa 168,164,184\nQta 118,61,37664\ns_add_u32 s69, s87, 4387944\nQmvav 172,168,32\n"
"buffer_load_dwordx4 v62, s[52:55], s69 offen offset:3096 lds\nQmvaa 172,172,184\nQdv 32,63,2048\nQmvav 204,168,36\nQda 184,63,2560\nQmvaa 204,172,188\nQta 120,61,33032\ns_add_u32 m0, s64, 0\n"
"Qmvav 200,160,36\ns_cbranch_vccnz .Ld0q6\nbuffer_load_dwordx4 v72, s[44:47], s90 offen offset:2048 nt lds\n.Ld0q6:\nQmvaa 200,164,188\nQta 122,61,33544\nQmvav 196,152,36\nQmvaa 196,156,188\n"
"Qta 124,61,37160\nQmvav 192,144,36\nQdv 36,63,3072\nQmvaa 192,148,188\nQda 188,63,3584\nQmvav 224,144,40\nQta 126,61,37672\nQmvaa 224,148,192\n"
"Qmvav 228,152,40\nQta 128,61,33280\nQmvaa 228,156,192\nQmvav 232,160,40\nQta 130,61,33792\nQmvaa 232,164,192\nQmvav 236,168,40\nQdv 40,63,4096\n"
"Qmvaa 236,172,192\nQda 192,63,4608\nQmaav 220,168,44\nQta 132,61,37408\nQmaaa 220,172,196\nQmaav 216,160,44\nQta 134,61,37920\nQmaaa 216,164,196\n"
"Qmaav 212,152,44\nQta 136,61,33288\nQmaaa 212,156,196\nQmaav 208,144,44\nQdv 44,63,5120\nQmaaa 208,148,196\nQda 196,63,5632\nQmaav 240,144,48\n"
"Qta 138,61,33800\nQmaaa 240,148,200\nQmaav 244,152,48\nQta 140,61,37416\nQmaaa 244,156,200\nQmaav 248,160,48\nQta 142,61,37928\nQmaaa 248,164,200\n"
"Qmaav 252,168,48\nQdv 48,63,6144\nQmaaa 252,172,200\nQda 200,63,6656\ns_waitcnt lgkmcnt(2)\nQmvav 80,112,24\nQmvaa 80,116,176\nQmvav 84,120,24\n"
"Qmvaa 84,124,176\nQmvav 88,128,24\nQmvaa 88,132,176\nQmvav 92,136,24\nQmvaa 92,140,176\ns_waitcnt lgkmcnt(0)\nQmvav 124,136,28\ns_waitcnt vmcnt(17)\n"
"s_barrier\nQmvaa 124,140,180\nQta 144,61,49536\ns_add_u32 m0, s62, 33024\ns_add_u32 s69, s87, 4718592\nQmvav 120,128,28\nbuffer_load_dwordx4 v62, s[52:55], s69 offen lds\nQmvaa 120,132,180\n"
"Qta 146,61,50048\nQmvav 116,120,28\nQmvaa 116,124,180\nQta 148,61,53664\ns_add_u32 s69, s87, 4783096\nQmvav 112,112,28\nbuffer_load_dwordx4 v62, s[52:55], s69 offen offset:1032 lds\nQmvaa 112,116,180\n"
"Qta 150,61,54176\nQmvav 144,112,32\nQmvaa 144,116,184\nQta 152,61,49544\ns_add_u32 s69, s87, 4847600\nQmvav 148,120,32\nbuffer_load_dwordx4 v62, s[52:55], s69 offen offset:2064 lds\nQmvaa 148,124,184\n"
"Qta 154,61,50056\nQmvav 152,128,32\nQmvaa 152,132,184\nQta 156,61,53672\ns_add_u32 s69, s87, 4912104\nQmvav 156,136,32\nbuffer_load_dwordx4 v62, s[52:55], s69 offen offset:3096 lds\nQmvaa 156,140,184\n"
"Qta 158,61,54184\nQmvav 188,136,36\nQmvaa 188,140,188\nQta 160,61,49792\ns_add_u32 m0, s63, 0\ns_add_u32 s88, s86, 1152\nQmvav 184,128,36\nbuffer_load_dwordx4 v64, s[48:51], s88 offen lds\n"
"Qmvaa 184,132,188\nQta 162,61,50304\nQmvav 180,120,36\nQmvaa 180,124,188\nQta 164,61,53920\ns_add_u32 s88, s86, 20608\nQmvav 176,112,36\nbuffer_load_dwordx4 v64, s[48:51], s88 offen offset:1024 lds\n"
"Qmvaa 176,116,188\nQta 166,61,54432\nQmvav 208,112,40\nQmvaa 208,116,192\nQta 168,61,49800\ns_add_u32 s88, s86, 40064\nQmvav 212,120,40\nbuffer_load_dwordx4 v64, s[48:51], s88 offen offset:2048 lds\n"
"Qmvaa 212,124,192\nQta 170,61,50312\nQmvav 216,128,40\nQmvaa 216,132,192\nQta 172,61,53928\ns_add_u32 s88, s86, 59520\nQmvav 220,136,40\nbuffer_load_dwordx4 v64, s[48:51], s88 offen offset:3072 lds\n"
"Qmvaa 220,140,192\nQta 174,61,54440\nQmvav 252,136,44\nQmvaa 252,140,196\ns_add_u32 m0, s63, 4096\ns_add_u32 s88, s86, 83072\nQmvav 248,128,44\nbuffer_load_dwordx4 v64, s[48:51], s88 offen lds\n"
"Qmvaa 248,132,196\nv_mov_b32 v66, s77\nQmvav 244,120,44\nds_write_b32 v73, v66 offset:16\nQmvaa 244,124,196\ns_add_u32 s88, s86, 102528\nQmvav 240,112,44\nbuffer_load_dwordx4 v64, s[48:51], s88 offen offset:1024 lds\n"
"Qmvaa 240,116,196\nQmaav 224,112,48\nQmaaa 224,116,200\ns_add_u32 s88, s86, 121984\nQmaav 228,120,48\nbuffer_load_dwordx4 v64, s[48:51], s88 offen offset:2048 lds\nQmaaa 228,124,200\nQmaav 232,128,48\n"
"Qmaaa 232,132,200\nQmaav 236,136,48\nQmaaa 236,140,200\ns_waitcnt lgkmcnt(1)\nQmvav 96,144,24\nQmvaa 96,148,176\nQmvav 100,152,24\nQmvaa 100,156,176\n"
"Qmvav 104,160,24\nQmvaa 104,164,176\nQmvav 108,168,24\nQmvaa 108,172,176\ns_waitcnt lgkmcnt(0)\nQmvav 140,168,28\ns_waitcnt vmcnt(16)\ns_barrier\n"
"Qmvaa 140,172,180\nQdv 24,63,33024\ns_add_u32 m0, s62, 49536\ns_add_u32 s69, s87, 4718720\nQmvav 136,160,28\nbuffer_load_dwordx4 v62, s[52:55], s69 offen lds\nQmvaa 136,164,180\nQda 176,63,33536\n"
"Qmvav 132,152,28\nQta 112,61,0\nQmvaa 132,156,180\ns_add_u32 s69, s87, 4783224\nQmvav 128,144,28\nbuffer_load_dwordx4 v62, s[52:55], s69 offen offset:1032 lds\nQmvaa 128,148,180\nQdv 28,63,34048\n"
"Qmvav 160,144,32\nQda 180,63,34560\nQmvaa 160,148,184\nQta 114,61,512\ns_add_u32 s69, s87, 4847728\nQmvav 164,152,32\nbuffer_load_dwordx4 v62, s[52:55], s69 offen offset:2064 lds\nQmvaa 164,156,184\n"
"Qta 116,61,4128\nQmvav 168,160,32\nQmvaa 168,164,184\nQta 118,61,4640\ns_add_u32 s69, s87, 4912232\nQmvav 172,168,32\nbuffer_load_dwordx4 v62, s[52:55], s69 offen offset:3096 lds\nQmvaa 172,172,184\n"
"Qdv 32,63,35072\nQmvav 204,168,36\nQda 184,63,35584\nQmvaa 204,172,188\nQta 120,61,8\ns_add_u32 m0, s64, 0\nQmvav 200,160,36\ns_cbranch_vccnz .Ld0q7\n"
"buffer_load_dwordx4 v72, s[44:47], s90 offen offset:3072 nt lds\ns_add_u32 s65, s65, 2048\ns_xor_b32 s60, s65, %21\ns_add_u32 s90, s60, 260096\n.Ld0q7:\nQmvaa 200,164,188\nQta 122,61,520\nQmvav 196,152,36\n"
"Qmvaa 196,156,188\nQta 124,61,4136\nQmvav 192,144,36\nQdv 36,63,36096\nQmvaa 192,148,188\nQda 188,63,36608\nQmvav 224,144,40\nQta 126,61,4648\n"
"Qmvaa 224,148,192\nQmvav 228,152,40\nQta 128,61,256\nQmvaa 228,156,192\nQmvav 232,160,40\nQta 130,61,768\nQmvaa 232,164,192\nQmvav 236,168,40\n"
"Qdv 40,63,37120\nQmvaa 236,172,192\nQda 192,63,37632\nQmaav 220,168,44\nQta 132,61,4384\nQmaaa 220,172,196\nQmaav 216,160,44\nQta 134,61,4896\n"
"Qmaaa 216,164,196\nQmaav 212,152,44\nQta 136,61,264\nQmaaa 212,156,196\nQmaav 208,144,44\nQdv 44,63,38144\nQmaaa 208,148,196\nQda 196,63,38656\n"
"Qmaav 240,144,48\nQta 138,61,776\nQmaaa 240,148,200\nQmaav 244,152,48\nQta 140,61,4392\nQmaaa 244,156,200\nQmaav 248,160,48\nQta 142,61,4904\n"
"Qmaaa 248,164,200\nQmaav 252,168,48\nQdv 48,63,39168\nQmaaa 252,172,200\nQda 200,63,39680\ns_waitcnt lgkmcnt(2)\nQmvav 80,112,24\nQmvaa 80,116,176\n"
"Qmvav 84,120,24\nQmvaa 84,124,176\nQmvav 88,128,24\nQmvaa 88,132,176\nQmvav 92,136,24\nQmvaa 92,140,176\ns_waitcnt lgkmcnt(0)\nQmvav 124,136,28\n"
"s_waitcnt vmcnt(15)\ns_barrier\nQmvaa 124,140,180\nQta 144,61,16512\ns_mov_b32 s56, s94\ns_mov_b32 s57, s95\ns_mov_b32 s58, s75\ns_mov_b32 s94, 0\n"
".Lpg3:\ns_cbranch_execz .Ltr0\ns_add_u32 m0, %10, 160768\nQmvav 120,128,28\nbuffer_load_dwordx4 v60, s[56:59], s94 offen lds\nQmvaa 120,132,180\nQta 146,61,17024\nQmvav 116,120,28\n"
"Qmvaa 116,124,180\nQta 148,61,20640\ns_add_u32 s94, s94, 4096\nQmvav 112,112,28\nbuffer_load_dwordx4 v60, s[56:59], s94 offen lds\nQmvaa 112,116,180\nQta 150,61,21152\nQmvav 144,112,32\n"
"Qmvaa 144,116,184\nQta 152,61,16520\ns_add_u32 s94, s94, 4096\nQmvav 148,120,32\nbuffer_load_dwordx4 v60, s[56:59], s94 offen lds\nQmvaa 148,124,184\nQta 154,61,17032\nQmvav 152,128,32\n"
"Qmvaa 152,132,184\nQta 156,61,20648\ns_add_u32 s94, s94, 4096\nQmvav 156,136,32\nbuffer_load_dwordx4 v60, s[56:59], s94 offen lds\nQmvaa 156,140,184\nQta 158,61,21160\nQmvav 188,136,36\n"
"Qmvaa 188,140,188\nQta 160,61,16768\ns_add_u32 s94, s94, 4096\nQmvav 184,128,36\nbuffer_load_dwordx4 v60, s[56:59], s94 offen lds\nQmvaa 184,132,188\nQta 162,61,17280\nQmvav 180,120,36\n"
"Qmvaa 180,124,188\nQta 164,61,20896\ns_add_u32 s94, s94, 4096\nQmvav 176,112,36\nbuffer_load_dwordx4 v60, s[56:59], s94 offen lds\nQmvaa 176,116,188\nQta 166,61,21408\nQmvav 208,112,40\n"
"Qmvaa 208,116,192\nQta 168,61,16776\ns_add_u32 s94, s94, 4096\nQmvav 212,120,40\nbuffer_load_dwordx4 v60, s[56:59], s94 offen lds\nQmvaa 212,124,192\nQta 170,61,17288\nQmvav 216,128,40\n"
"Qmvaa 216,132,192\nQta 172,61,20904\ns_add_u32 s94, s94, 4096\nQmvav 220,136,40\nbuffer_load_dwordx4 v60, s[56:59], s94 offen lds\nQmvaa 220,140,192\nQta 174,61,21416\nQmvav 252,136,44\n"
"Qmvaa 252,140,196\ns_add_u32 s94, s94, 4096\nQmvav 248,128,44\nbuffer_load_dwordx4 v60, s[56:59], s94 offen lds\nQmvaa 248,132,196\nQmvav 244,120,44\nQmvaa 244,124,196\ns_add_u32 s94, s94, 4096\n"
"Qmvav 240,112,44\nbuffer_load_dwordx4 v60, s[56:59], s94 offen lds\nQmvaa 240,116,196\nQmaav 224,112,48\nQmaaa 224,116,200\nQmaav 228,120,48\nQmaaa 228,124,200\nQmaav 232,128,48\n"
"Qmaaa 232,132,200\nQmaav 236,136,48\nQmaaa 236,140,200\ns_waitcnt lgkmcnt(0)\nQmvav 96,144,24\nQmvaa 96,148,176\nQmvav 100,152,24\nQmvaa 100,156,176\n"
"Qmvav 104,160,24\nQmvaa 104,164,176\nQmvav 108,168,24\nQmvaa 108,172,176\ns_waitcnt lgkmcnt(0)\nQmvav 140,168,28\ns_waitcnt vmcnt(14)\ns_barrier\n"
"Qmvaa 140,172,180\nQdv 24,63,0\ns_add_u32 m0, s61, 33024\nQmvav 136,160,28\nbuffer_load_dwordx4 v69, s[40:43], s60 offen nt lds\nQmvaa 136,164,180\nQda 176,63,512\nQmvav 132,152,28\n"
"Qta 112,61,33024\nQmvaa 132,156,180\nQmvav 128,144,28\nbuffer_load_dwordx4 v69, s[40:43], s60 offen offset:1024 nt lds\nQmvaa 128,148,180\nQdv 28,63,1024\nQmvav 160,144,32\nQda 180,63,1536\n"
"Qmvaa 160,148,184\nQta 114,61,33536\nQmvav 164,152,32\nbuffer_load_dwordx4 v69, s[40:43], s90 offen offset:2048 nt lds\nQmvaa 164,156,184\nQta 116,61,37152\nQmvav 168,160,32\nQmvaa 168,164,184\n"
"Qta 118,61,37664\nQmvav 172,168,32\nbuffer_load_dwordx4 v69, s[40:43], s90 offen offset:3072 nt lds\nQmvaa 172,172,184\nQdv 32,63,2048\nQmvav 204,168,36\nQda 184,63,2560\nQmvaa 204,172,188\n"
"Qta 120,61,33032\ns_add_u32 m0, s64, 33024\nQmvav 200,160,36\nbuffer_load_dwordx4 v72, s[44:47], s60 offen nt lds\nQmvaa 200,164,188\nQta 122,61,33544\nQmvav 196,152,36\nQmvaa 196,156,188\n"
"Qta 124,61,37160\nQmvav 192,144,36\nbuffer_load_dwordx4 v72, s[44:47], s60 offen offset:1024 nt lds\nQmvaa 192,148,188\nQdv 36,63,3072\nQmvav 224,144,40\nQda 188,63,3584\nQmvaa 224,148,192\n"
"Qta 126,61,37672\nQmvav 228,152,40\nbuffer_load_dwordx4 v72, s[44:47], s90 offen offset:2048 nt lds\nQmvaa 228,156,192\nQta 128,61,33280\nQmvav 232,160,40\nQmvaa 232,164,192\nQta 130,61,33792\n"
"Qmvav 236,168,40\nbuffer_load_dwordx4 v72, s[44:47], s90 offen offset:3072 nt lds\ns_add_u32 s65, s65, 2048\ns_xor_b32 s60, s65, %21\ns_add_u32 s90, s60, 260096\nQmvaa 236,172,192\nQdv 40,63,4096\nQmaav 220,168,44\n"
"Qda 192,63,4608\nQmaaa 220,172,196\nQta 132,61,37408\nQmaav 216,160,44\nQmaaa 216,164,196\nQta 134,61,37920\nQmaav 212,152,44\nQmaaa 212,156,196\n"
"Qta 136,61,33288\nQmaav 208,144,44\nQdv 44,63,5120\nQmaaa 208,148,196\nQda 196,63,5632\nQmaav 240,144,48\nQta 138,61,33800\nQmaaa 240,148,200\n"
"Qmaav 244,152,48\nQta 140,61,37416\nQmaaa 244,156,200\nQmaav 248,160,48\nQta 142,61,37928\nQmaaa 248,164,200\nQmaav 252,168,48\nQdv 48,63,6144\n"
"Qmaaa 252,172,200\nQda 200,63,6656\ns_waitcnt lgkmcnt(2)\nQmvav 80,112,24\nQmvaa 80,116,176\nQmvav 84,120,24\nQmvaa 84,124,176\nQmvav 88,128,24\n"
"Qmvaa 88,132,176\nQmvav 92,136,24\nQmvaa 92,140,176\ns_waitcnt lgkmcnt(0)\nQmvav 124,136,28\ns_waitcnt vmcnt(18)\ns_barrier\nQmvaa 124,140,180\n"
"Qta 144,61,49536\nQmvav 120,128,28\nQmvaa 120,132,180\nQta 146,61,50048\nQmvav 116,120,28\nQmvaa 116,124,180\nQta 148,61,53664\nQmvav 112,112,28\n"
"Qmvaa 112,116,180\nQta 150,61,54176\nQmvav 144,112,32\nQmvaa 144,116,184\nQta 152,61,49544\nQmvav 148,120,32\nQmvaa 148,124,184\nQta 154,61,50056\n"
"Qmvav 152,128,32\nQmvaa 152,132,184\nQta 156,61,53672\nQmvav 156,136,32\nQmvaa 156,140,184\nQta 158,61,54184\nQmvav 188,136,36\nQmvaa 188,140,188\n"
"Qta 160,61,49792\nQmvav 184,128,36\nQmvaa 184,132,188\nQta 162,61,50304\nQmvav 180,120,36\nQmvaa 180,124,188\nQta 164,61,53920\nQmvav 176,112,36\n"
"Qmvaa 176,116,188\nQta 166,61,54432\nQmvav 208,112,40\nQmvaa 208,116,192\nQta 168,61,49800\nQmvav 212,120,40\nQmvaa 212,124,192\nQta 170,61,50312\n"
"Qmvav 216,128,40\nQmvaa 216,132,192\nQta 172,61,53928\nQmvav 220,136,40\nQmvaa 220,140,192\nQta 174,61,54440\nQmvav 252,136,44\nQmvaa 252,140,196\n"
"Qmvav 248,128,44\nQmvaa 248,132,196\nQmvav 244,120,44\nQmvaa 244,124,196\nQmvav 240,112,44\nQmvaa 240,116,196\nQmaav 224,112,48\nQmaaa 224,116,200\n"
"Qmaav 228,120,48\nQmaaa 228,124,200\nQmaav 232,128,48\nQmaaa 232,132,200\nQmaav 236,136,48\nQmaaa 236,140,200\ns_waitcnt lgkmcnt(0)\nQmvav 96,144,24\n"
"Qmvaa 96,148,176\nQmvav 100,152,24\nQmvaa 100,156,176\nQmvav 104,160,24\nQmvaa 104,164,176\nQmvav 108,168,24\nQmvaa 108,172,176\ns_waitcnt lgkmcnt(0)\n"
"Qmvav 140,168,28\ns_waitcnt vmcnt(18)\ns_barrier\nQmvaa 140,172,180\nQda 112,68,0\ns_add_u32 m0, s61, 66048\nv_accvgpr_write_b32 a176, 0\nQmvav 136,160,28\n"
"buffer_load_dwordx4 v69, s[40:43], s60 offen nt lds\nQmvaa 136,164,180\nQda 116,68,512\nv_accvgpr_write_b32 a177, 0\nQmvav 132,152,28\nQmvaa 132,156,180\nQda 120,68,256\nv_accvgpr_write_b32 a178, 0\n"
"Qmvav 128,144,28\nbuffer_load_dwordx4 v69, s[40:43], s60 offen offset:1024 nt lds\nQmvaa 128,148,180\nQda 124,68,768\nv_accvgpr_write_b32 a179, 0\nQmvav 160,144,32\nQmvaa 160,148,184\nQda 128,68,1024\n"
"v_accvgpr_write_b32 a180, 0\nQmvav 164,152,32\nbuffer_load_dwordx4 v69, s[40:43], s90 offen offset:2048 nt lds\nQmvaa 164,156,184\nQda 132,68,1536\nv_accvgpr_write_b32 a181, 0\nQmvav 168,160,32\nQmvaa 168,164,184\n"
"Qda 136,68,1280\nv_accvgpr_write_b32 a182, 0\nQmvav 172,168,32\nbuffer_load_dwordx4 v69, s[40:43], s90 offen offset:3072 nt lds\nQmvaa 172,172,184\nQda 140,68,1792\nv_accvgpr_write_b32 a183, 0\nQmvav 204,168,36\n"
"Qmvaa 204,172,188\ns_add_u32 m0, s64, 66048\nv_accvgpr_write_b32 a184, 0\nQmvav 200,160,36\nbuffer_load_dwordx4 v72, s[44:47], s60 offen nt lds\nQmvaa 200,164,188\nv_accvgpr_write_b32 a185, 0\nQmvav 196,152,36\n"
"Qmvaa 196,156,188\nv_accvgpr_write_b32 a186, 0\nQmvav 192,144,36\nbuffer_load_dwordx4 v72, s[44:47], s60 offen offset:1024 nt lds\nQmvaa 192,148,188\nv_accvgpr_write_b32 a187, 0\nQmvav 224,144,40\nQmvaa 224,148,192\n"
"v_accvgpr_write_b32 a188, 0\nQmvav 228,152,40\nbuffer_load_dwordx4 v72, s[44:47], s90 offen offset:2048 nt lds\nQmvaa 228,156,192\nv_accvgpr_write_b32 a189, 0\nQmvav 232,160,40\nQmvaa 232,164,192\nv_accvgpr_write_b32 a190, 0\n"
"Qmvav 236,168,40\nbuffer_load_dwordx4 v72, s[44:47], s90 offen offset:3072 nt lds\ns_add_u32 s65, s65, 2048\ns_xor_b32 s60, s65, %21\ns_add_u32 s90, s60, 260096\nQmvaa 236,172,192\nv_accvgpr_write_b32 a191, 0\nQmaav 220,168,44\n"
"Qmaaa 220,172,196\nv_accvgpr_write_b32 a192, 0\nQmaav 216,160,44\nQmaaa 216,164,196\nv_accvgpr_write_b32 a193, 0\nQmaav 212,152,44\nQmaaa 212,156,196\nv_accvgpr_write_b32 a194, 0\n"
"Qmaav 208,144,44\nQmaaa 208,148,196\nv_accvgpr_write_b32 a195, 0\nQmaav 240,144,48\nQmaaa 240,148,200\nv_accvgpr_write_b32 a196, 0\nQmaav 244,152,48\nQmaaa 244,156,200\n"
"v_accvgpr_write_b32 a197, 0\nQmaav 248,160,48\nQmaaa 248,164,200\nv_accvgpr_write_b32 a198, 0\nQmaav 252,168,48\nQmaaa 252,172,200\nv_accvgpr_write_b32 a199, 0\nQta 148,70,128\n"
"Qta 150,70,1152\nQta 156,70,384\nQta 158,70,1408\nQta 164,70,640\nQta 166,70,1664\nQta 172,70,896\nQta 174,70,1920\nv_pk_mul_f32 v[80:81], v[80:81], s[72:73] op_sel_hi:[1,0]\n"
"v_pk_mul_f32 v[82:83], v[82:83], s[72:73] op_sel_hi:[1,0]\nv_pk_mul_f32 v[84:85], v[84:85], s[72:73] op_sel_hi:[1,0]\nv_pk_mul_f32 v[86:87], v[86:87], s[72:73] op_sel_hi:[1,0]\nQk 80,80,81\nQk 81,82,83\nQk 82,84,85\nQk 83,86,87\nv_accvgpr_write_b32 a0, v80\n"
"v_accvgpr_write_b32 a1, v81\nv_accvgpr_write_b32 a2, v82\nv_accvgpr_write_b32 a3, v83\nv_pk_mul_f32 v[88:89], v[88:89], s[72:73] op_sel_hi:[1,0]\nv_pk_mul_f32 v[90:91], v[90:91], s[72:73] op_sel_hi:[1,0]\nv_pk_mul_f32 v[92:93], v[92:93], s[72:73] op_sel_hi:[1,0]\nv_pk_mul_f32 v[94:95], v[94:95], s[72:73] op_sel_hi:[1,0]\nQk 88,88,89\n"
"Qk 89,90,91\nQk 90,92,93\nQk 91,94,95\nv_accvgpr_write_b32 a4, v88\nv_accvgpr_write_b32 a5, v89\nv_accvgpr_write_b32 a6, v90\nv_accvgpr_write_b32 a7, v91\nv_pk_mul_f32 v[96:97], v[96:97], s[72:73] op_sel_hi:[1,0]\n"
"v_pk_mul_f32 v[98:99], v[98:99], s[72:73] op_sel_hi:[1,0]\nv_pk_mul_f32 v[100:101], v[100:101], s[72:73] op_sel_hi:[1,0]\nv_pk_mul_f32 v[102:103], v[102:103], s[72:73] op_sel_hi:[1,0]\nQk 96,96,97\nQk 97,98,99\nQk 98,100,101\nQk 99,102,103\nv_accvgpr_write_b32 a8, v96\n"
"v_accvgpr_write_b32 a9, v97\nv_accvgpr_write_b32 a10, v98\nv_accvgpr_write_b32 a11, v99\nv_pk_mul_f32 v[104:105], v[104:105], s[72:73] op_sel_hi:[1,0]\nv_pk_mul_f32 v[106:107], v[106:107], s[72:73] op_sel_hi:[1,0]\nv_pk_mul_f32 v[108:109], v[108:109], s[72:73] op_sel_hi:[1,0]\nv_pk_mul_f32 v[110:111], v[110:111], s[72:73] op_sel_hi:[1,0]\nQk 104,104,105\n"
"Qk 105,106,107\nQk 106,108,109\nQk 107,110,111\nv_accvgpr_write_b32 a12, v104\nv_accvgpr_write_b32 a13, v105\nv_accvgpr_write_b32 a14, v106\nv_accvgpr_write_b32 a15, v107\nv_pk_mul_f32 v[112:113], v[112:113], s[72:73] op_sel_hi:[1,0]\n"
"v_pk_mul_f32 v[114:115], v[114:115], s[72:73] op_sel_hi:[1,0]\nv_pk_mul_f32 v[116:117], v[116:117], s[72:73] op_sel_hi:[1,0]\nv_pk_mul_f32 v[118:119], v[118:119], s[72:73] op_sel_hi:[1,0]\nQk 112,112,113\nQk 113,114,115\nQk 114,116,117\nQk 115,118,119\nv_accvgpr_write_b32 a16, v112\n"
"v_accvgpr_write_b32 a17, v113\nv_accvgpr_write_b32 a18, v114\nv_accvgpr_write_b32 a19, v115\nv_pk_mul_f32 v[120:121], v[120:121], s[72:73] op_sel_hi:[1,0]\nv_pk_mul_f32 v[122:123], v[122:123], s[72:73] op_sel_hi:[1,0]\nv_pk_mul_f32 v[124:125], v[124:125], s[72:73] op_sel_hi:[1,0]\nv_pk_mul_f32 v[126:127], v[126:127], s[72:73] op_sel_hi:[1,0]\nQk 120,120,121\n"
"Qk 121,122,123\nQk 122,124,125\nQk 123,126,127\nv_accvgpr_write_b32 a20, v120\nv_accvgpr_write_b32 a21, v121\nv_accvgpr_write_b32 a22, v122\nv_accvgpr_write_b32 a23, v123\nv_pk_mul_f32 v[128:129], v[128:129], s[72:73] op_sel_hi:[1,0]\n"
"v_pk_mul_f32 v[130:131], v[130:131], s[72:73] op_sel_hi:[1,0]\nv_pk_mul_f32 v[132:133], v[132:133], s[72:73] op_sel_hi:[1,0]\nv_pk_mul_f32 v[134:135], v[134:135], s[72:73] op_sel_hi:[1,0]\nQk 128,128,129\nQk 129,130,131\nQk 130,132,133\nQk 131,134,135\nv_accvgpr_write_b32 a24, v128\n"
"v_accvgpr_write_b32 a25, v129\nv_accvgpr_write_b32 a26, v130\nv_accvgpr_write_b32 a27, v131\nv_pk_mul_f32 v[136:137], v[136:137], s[72:73] op_sel_hi:[1,0]\nv_pk_mul_f32 v[138:139], v[138:139], s[72:73] op_sel_hi:[1,0]\nv_pk_mul_f32 v[140:141], v[140:141], s[72:73] op_sel_hi:[1,0]\nv_pk_mul_f32 v[142:143], v[142:143], s[72:73] op_sel_hi:[1,0]\nQk 136,136,137\n"
"Qk 137,138,139\nQk 138,140,141\nQk 139,142,143\nv_accvgpr_write_b32 a28, v136\nv_accvgpr_write_b32 a29, v137\nv_accvgpr_write_b32 a30, v138\nv_accvgpr_write_b32 a31, v139\nv_pk_mul_f32 v[144:145], v[144:145], s[72:73] op_sel_hi:[1,0]\n"
"v_pk_mul_f32 v[146:147], v[146:147], s[72:73] op_sel_hi:[1,0]\nv_pk_mul_f32 v[148:149], v[148:149], s[72:73] op_sel_hi:[1,0]\nv_pk_mul_f32 v[150:151], v[150:151], s[72:73] op_sel_hi:[1,0]\nQk 144,144,145\nQk 145,146,147\nQk 146,148,149\nQk 147,150,151\nv_accvgpr_write_b32 a32, v144\n"
"v_accvgpr_write_b32 a33, v145\nv_accvgpr_write_b32 a34, v146\nv_accvgpr_write_b32 a35, v147\nv_pk_mul_f32 v[152:153], v[152:153], s[72:73] op_sel_hi:[1,0]\nv_pk_mul_f32 v[154:155], v[154:155], s[72:73] op_sel_hi:[1,0]\nv_pk_mul_f32 v[156:157], v[156:157], s[72:73] op_sel_hi:[1,0]\nv_pk_mul_f32 v[158:159], v[158:159], s[72:73] op_sel_hi:[1,0]\nQk 152,152,153\n"
"Qk 153,154,155\nQk 154,156,157\nQk 155,158,159\nv_accvgpr_write_b32 a36, v152\nv_accvgpr_write_b32 a37, v153\nv_accvgpr_write_b32 a38, v154\nv_accvgpr_write_b32 a39, v155\nv_pk_mul_f32 v[160:161], v[160:161], s[72:73] op_sel_hi:[1,0]\n"
"v_pk_mul_f32 v[162:163], v[162:163], s[72:73] op_sel_hi:[1,0]\nv_pk_mul_f32 v[164:165], v[164:165], s[72:73] op_sel_hi:[1,0]\nv_pk_mul_f32 v[166:167], v[166:167], s[72:73] op_sel_hi:[1,0]\nQk 160,160,161\nQk 161,162,163\nQk 162,164,165\nQk 163,166,167\nv_accvgpr_write_b32 a40, v160\n"
"v_accvgpr_write_b32 a41, v161\nv_accvgpr_write_b32 a42, v162\nv_accvgpr_write_b32 a43, v163\nv_pk_mul_f32 v[168:169], v[168:169], s[72:73] op_sel_hi:[1,0]\nv_pk_mul_f32 v[170:171], v[170:171], s[72:73] op_sel_hi:[1,0]\nv_pk_mul_f32 v[172:173], v[172:173], s[72:73] op_sel_hi:[1,0]\nv_pk_mul_f32 v[174:175], v[174:175], s[72:73] op_sel_hi:[1,0]\nQk 168,168,169\n"
"Qk 169,170,171\nQk 170,172,173\nQk 171,174,175\nv_accvgpr_write_b32 a44, v168\nv_accvgpr_write_b32 a45, v169\nv_accvgpr_write_b32 a46, v170\nv_accvgpr_write_b32 a47, v171\nv_pk_mul_f32 v[176:177], v[176:177], s[72:73] op_sel_hi:[1,0]\n"
"v_pk_mul_f32 v[178:179], v[178:179], s[72:73] op_sel_hi:[1,0]\n.Lpg4:\ns_cbranch_execz .Ltr9\nv_pk_mul_f32 v[180:181], v[180:181], s[72:73] op_sel_hi:[1,0]\nv_pk_mul_f32 v[182:183], v[182:183], s[72:73] op_sel_hi:[1,0]\nQk 176,176,177\nQk 177,178,179\nQk 178,180,181\n"
"Qk 179,182,183\nv_accvgpr_write_b32 a48, v176\nv_accvgpr_write_b32 a49, v177\nv_accvgpr_write_b32 a50, v178\nv_accvgpr_write_b32 a51, v179\nv_pk_mul_f32 v[184:185], v[184:185], s[72:73] op_sel_hi:[1,0]\nv_pk_mul_f32 v[186:187], v[186:187], s[72:73] op_sel_hi:[1,0]\nv_pk_mul_f32 v[188:189], v[188:189], s[72:73] op_sel_hi:[1,0]\n"
"v_pk_mul_f32 v[190:191], v[190:191], s[72:73] op_sel_hi:[1,0]\nQk 184,184,185\nQk 185,186,187\nQk 186,188,189\nQk 187,190,191\nv_accvgpr_write_b32 a52, v184\nv_accvgpr_write_b32 a53, v185\nv_accvgpr_write_b32 a54, v186\n"
"v_accvgpr_write_b32 a55, v187\nv_pk_mul_f32 v[192:193], v[192:193], s[72:73] op_sel_hi:[1,0]\nv_pk_mul_f32 v[194:195], v[194:195], s[72:73] op_sel_hi:[1,0]\nv_pk_mul_f32 v[196:197], v[196:197], s[72:73] op_sel_hi:[1,0]\nv_pk_mul_f32 v[198:199], v[198:199], s[72:73] op_sel_hi:[1,0]\nQk 192,192,193\nQk 193,194,195\nQk 194,196,197\n"
"Qk 195,198,199\nv_accvgpr_write_b32 a56, v192\nv_accvgpr_write_b32 a57, v193\nv_accvgpr_write_b32 a58, v194\nv_accvgpr_write_b32 a59, v195\nv_pk_mul_f32 v[200:201], v[200:201], s[72:73] op_sel_hi:[1,0]\nv_pk_mul_f32 v[202:203], v[202:203], s[72:73] op_sel_hi:[1,0]\nv_pk_mul_f32 v[204:205], v[204:205], s[72:73] op_sel_hi:[1,0]\n"
"v_pk_mul_f32 v[206:207], v[206:207], s[72:73] op_sel_hi:[1,0]\nQk 200,200,201\nQk 201,202,203\nQk 202,204,205\nQk 203,206,207\nv_accvgpr_write_b32 a60, v200\nv_accvgpr_write_b32 a61, v201\nv_accvgpr_write_b32 a62, v202\n"
"v_accvgpr_write_b32 a63, v203\nv_pk_mul_f32 v[208:209], v[208:209], s[72:73] op_sel_hi:[1,0]\nv_pk_mul_f32 v[210:211], v[210:211], s[72:73] op_sel_hi:[1,0]\nv_pk_mul_f32 v[212:213], v[212:213], s[72:73] op_sel_hi:[1,0]\nv_pk_mul_f32 v[214:215], v[214:215], s[72:73] op_sel_hi:[1,0]\nQk 208,208,209\nQk 209,210,211\nQk 210,212,213\n"
"Qk 211,214,215\nv_accvgpr_write_b32 a64, v208\nv_accvgpr_write_b32 a65, v209\nv_accvgpr_write_b32 a66, v210\nv_accvgpr_write_b32 a67, v211\nv_pk_mul_f32 v[216:217], v[216:217], s[72:73] op_sel_hi:[1,0]\nv_pk_mul_f32 v[218:219], v[218:219], s[72:73] op_sel_hi:[1,0]\nv_pk_mul_f32 v[220:221], v[220:221], s[72:73] op_sel_hi:[1,0]\n"
"v_pk_mul_f32 v[222:223], v[222:223], s[72:73] op_sel_hi:[1,0]\nQk 216,216,217\nQk 217,218,219\nQk 218,220,221\nQk 219,222,223\nv_accvgpr_write_b32 a68, v216\nv_accvgpr_write_b32 a69, v217\nv_accvgpr_write_b32 a70, v218\n"
"v_accvgpr_write_b32 a71, v219\nv_pk_mul_f32 v[224:225], v[224:225], s[72:73] op_sel_hi:[1,0]\nv_pk_mul_f32 v[226:227], v[226:227], s[72:73] op_sel_hi:[1,0]\nv_pk_mul_f32 v[228:229], v[228:229], s[72:73] op_sel_hi:[1,0]\nv_pk_mul_f32 v[230:231], v[230:231], s[72:73] op_sel_hi:[1,0]\nQk 224,224,225\nQk 225,226,227\nQk 226,228,229\n"
"Qk 227,230,231\nv_accvgpr_write_b32 a72, v224\nv_accvgpr_write_b32 a73, v225\nv_accvgpr_write_b32 a74, v226\nv_accvgpr_write_b32 a75, v227\nv_pk_mul_f32 v[232:233], v[232:233], s[72:73] op_sel_hi:[1,0]\nv_pk_mul_f32 v[234:235], v[234:235], s[72:73] op_sel_hi:[1,0]\nv_pk_mul_f32 v[236:237], v[236:237], s[72:73] op_sel_hi:[1,0]\n"
"v_pk_mul_f32 v[238:239], v[238:239], s[72:73] op_sel_hi:[1,0]\nQk 232,232,233\nQk 233,234,235\nQk 234,236,237\nQk 235,238,239\nv_accvgpr_write_b32 a76, v232\nv_accvgpr_write_b32 a77, v233\nv_accvgpr_write_b32 a78, v234\n"
"v_accvgpr_write_b32 a79, v235\nv_pk_mul_f32 v[240:241], v[240:241], s[72:73] op_sel_hi:[1,0]\nv_pk_mul_f32 v[242:243], v[242:243], s[72:73] op_sel_hi:[1,0]\nv_pk_mul_f32 v[244:245], v[244:245], s[72:73] op_sel_hi:[1,0]\nv_pk_mul_f32 v[246:247], v[246:247], s[72:73] op_sel_hi:[1,0]\nQk 240,240,241\nQk 241,242,243\nQk 242,244,245\n"
"Qk 243,246,247\nv_accvgpr_write_b32 a80, v240\nv_accvgpr_write_b32 a81, v241\nv_accvgpr_write_b32 a82, v242\nv_accvgpr_write_b32 a83, v243\nv_pk_mul_f32 v[248:249], v[248:249], s[72:73] op_sel_hi:[1,0]\nv_pk_mul_f32 v[250:251], v[250:251], s[72:73] op_sel_hi:[1,0]\nv_pk_mul_f32 v[252:253], v[252:253], s[72:73] op_sel_hi:[1,0]\n"
"v_pk_mul_f32 v[254:255], v[254:255], s[72:73] op_sel_hi:[1,0]\nQk 248,248,249\nQk 249,250,251\nQk 250,252,253\nQk 251,254,255\nv_accvgpr_write_b32 a84, v248\nv_accvgpr_write_b32 a85, v249\nv_accvgpr_write_b32 a86, v250\n"
"v_accvgpr_write_b32 a87, v251\nv_accvgpr_read_b32 v0, a208\nv_accvgpr_read_b32 v1, a209\nv_accvgpr_read_b32 v2, a210\nv_accvgpr_read_b32 v3, a211\nv_accvgpr_read_b32 v4, a212\nv_accvgpr_read_b32 v5, a213\nv_accvgpr_read_b32 v6, a214\n"
"v_accvgpr_read_b32 v7, a215\nv_pk_mul_f32 v[0:1], v[0:1], s[72:73] op_sel_hi:[1,0]\nv_pk_mul_f32 v[2:3], v[2:3], s[72:73] op_sel_hi:[1,0]\nv_pk_mul_f32 v[4:5], v[4:5], s[72:73] op_sel_hi:[1,0]\nv_pk_mul_f32 v[6:7], v[6:7], s[72:73] op_sel_hi:[1,0]\nQk 0,0,1\nQk 1,2,3\nQk 2,4,5\n"
"Qk 3,6,7\nv_accvgpr_write_b32 a88, v0\nv_accvgpr_write_b32 a89, v1\nv_accvgpr_write_b32 a90, v2\nv_accvgpr_write_b32 a91, v3\nv_accvgpr_read_b32 v8, a216\nv_accvgpr_read_b32 v9, a217\nv_accvgpr_read_b32 v10, a218\n"
"v_accvgpr_read_b32 v11, a219\nv_accvgpr_read_b32 v12, a220\nv_accvgpr_read_b32 v13, a221\nv_accvgpr_read_b32 v14, a222\nv_accvgpr_read_b32 v15, a223\nv_pk_mul_f32 v[8:9], v[8:9], s[72:73] op_sel_hi:[1,0]\nv_pk_mul_f32 v[10:11], v[10:11], s[72:73] op_sel_hi:[1,0]\nv_pk_mul_f32 v[12:13], v[12:13], s[72:73] op_sel_hi:[1,0]\n"
"v_pk_mul_f32 v[14:15], v[14:15], s[72:73] op_sel_hi:[1,0]\nQk 8,8,9\nQk 9,10,11\nQk 10,12,13\nQk 11,14,15\nv_accvgpr_write_b32 a92, v8\nv_accvgpr_write_b32 a93, v9\nv_accvgpr_write_b32 a94, v10\n"
"v_accvgpr_write_b32 a95, v11\nv_accvgpr_read_b32 v0, a224\nv_accvgpr_read_b32 v1, a225\nv_accvgpr_read_b32 v2, a226\nv_accvgpr_read_b32 v3, a227\nv_accvgpr_read_b32 v4, a228\nv_accvgpr_read_b32 v5, a229\nv_accvgpr_read_b32 v6, a230\n"
"v_accvgpr_read_b32 v7, a231\nv_pk_mul_f32 v[0:1], v[0:1], s[72:73] op_sel_hi:[1,0]\nv_pk_mul_f32 v[2:3], v[2:3], s[72:73] op_sel_hi:[1,0]\nv_pk_mul_f32 v[4:5], v[4:5], s[72:73] op_sel_hi:[1,0]\nv_pk_mul_f32 v[6:7], v[6:7], s[72:73] op_sel_hi:[1,0]\nQk 0,0,1\nQk 1,2,3\nQk 2,4,5\n"
"Qk 3,6,7\nv_accvgpr_write_b32 a96, v0\nv_accvgpr_write_b32 a97, v1\nv_accvgpr_write_b32 a98, v2\nv_accvgpr_write_b32 a99, v3\nv_accvgpr_read_b32 v8, a232\nv_accvgpr_read_b32 v9, a233\nv_accvgpr_read_b32 v10, a234\n"
"v_accvgpr_read_b32 v11, a235\nv_accvgpr_read_b32 v12, a236\nv_accvgpr_read_b32 v13, a237\nv_accvgpr_read_b32 v14, a238\nv_accvgpr_read_b32 v15, a239\nv_pk_mul_f32 v[8:9], v[8:9], s[72:73] op_sel_hi:[1,0]\nv_pk_mul_f32 v[10:11], v[10:11], s[72:73] op_sel_hi:[1,0]\nv_pk_mul_f32 v[12:13], v[12:13], s[72:73] op_sel_hi:[1,0]\n"
"v_pk_mul_f32 v[14:15], v[14:15], s[72:73] op_sel_hi:[1,0]\nQk 8,8,9\nQk 9,10,11\nQk 10,12,13\nQk 11,14,15\nv_accvgpr_write_b32 a100, v8\nv_accvgpr_write_b32 a101, v9\nv_accvgpr_write_b32 a102, v10\n"
"v_accvgpr_write_b32 a103, v11\nv_accvgpr_read_b32 v0, a240\nv_accvgpr_read_b32 v1, a241\nv_accvgpr_read_b32 v2, a242\nv_accvgpr_read_b32 v3, a243\nv_accvgpr_read_b32 v4, a244\nv_accvgpr_read_b32 v5, a245\nv_accvgpr_read_b32 v6, a246\n"
"v_accvgpr_read_b32 v7, a247\nv_pk_mul_f32 v[0:1], v[0:1], s[72:73] op_sel_hi:[1,0]\nv_pk_mul_f32 v[2:3], v[2:3], s[72:73] op_sel_hi:[1,0]\nv_pk_mul_f32 v[4:5], v[4:5], s[72:73] op_sel_hi:[1,0]\nv_pk_mul_f32 v[6:7], v[6:7], s[72:73] op_sel_hi:[1,0]\nQk 0,0,1\nQk 1,2,3\nQk 2,4,5\n"
"Qk 3,6,7\nv_accvgpr_write_b32 a104, v0\nv_accvgpr_write_b32 a105, v1\nv_accvgpr_write_b32 a106, v2\nv_accvgpr_write_b32 a107, v3\nv_accvgpr_read_b32 v8, a248\nv_accvgpr_read_b32 v9, a249\nv_accvgpr_read_b32 v10, a250\n"
"v_accvgpr_read_b32 v11, a251\nv_accvgpr_read_b32 v12, a252\nv_accvgpr_read_b32 v13, a253\nv_accvgpr_read_b32 v14, a254\nv_accvgpr_read_b32 v15, a255\nv_pk_mul_f32 v[8:9], v[8:9], s[72:73] op_sel_hi:[1,0]\nv_pk_mul_f32 v[10:11], v[10:11], s[72:73] op_sel_hi:[1,0]\nv_pk_mul_f32 v[12:13], v[12:13], s[72:73] op_sel_hi:[1,0]\n"
"v_pk_mul_f32 v[14:15], v[14:15], s[72:73] op_sel_hi:[1,0]\nQk 8,8,9\nQk 9,10,11\nQk 10,12,13\nQk 11,14,15\nv_accvgpr_write_b32 a108, v8\nv_accvgpr_write_b32 a109, v9\nv_accvgpr_write_b32 a110, v10\n"
"v_accvgpr_write_b32 a111, v11\nv_mov_b32 v66, 0x3f803f80\nv_accvgpr_write_b32 a200, 0\nv_accvgpr_write_b32 a201, 0\nv_accvgpr_write_b32 a202, 0\nv_accvgpr_write_b32 a203, 0\ns_mov_b32 vcc_lo, 0x33333333\ns_mov_b32 vcc_hi, 0xcccccccc\n"
"v_cndmask_b32 v66, 0, v66, vcc\nv_accvgpr_write_b32 a204, v66\nv_accvgpr_write_b32 a205, v66\nv_accvgpr_write_b32 a206, v66\nv_accvgpr_write_b32 a207, v66\ns_add_u32 s87, s87, 2048\ns_waitcnt lgkmcnt(8)\nQzvaa 0,112,0\n"
"Qmvaa 0,116,4\nQmvaa 0,120,8\nQmvaa 0,124,12\nQzvaa 4,140,12\nQmvaa 4,136,8\nQmvaa 4,132,4\nQmvaa 4,128,0\ns_nop 3\n"
"v_max3_f32 v66, v0, v1, v2\ns_nop 2\nv_max3_f32 v66, v66, v3, v4\nv_max3_f32 v66, v66, v5, v6\nv_max_f32 v66, v66, v7\nQzvaa 8,112,16\nv_mov_b32 v67, v66\nQmvaa 8,116,20\n"
"s_nop 0\nv_permlane32_swap_b32 v66, v67\nv_max_f32 v66, v66, v67\nQmvaa 8,120,24\nv_mov_b32 v67, v66\nQmvaa 8,124,28\ns_nop 0\nv_permlane16_swap_b32 v66, v67\n"
"v_max_f32 v66, v66, v67\nQzvaa 12,140,28\ns_nop 0\nv_max_f32_dpp v66, v66, v66 row_ror:8 row_mask:0xf bank_mask:0xf\ns_nop 1\nv_max_f32_dpp v66, v66, v66 row_ror:4 row_mask:0xf bank_mask:0xf\nQmvaa 12,136,24\ns_nop 0\n"
"v_max_f32_dpp v66, v66, v66 row_ror:2 row_mask:0xf bank_mask:0xf\ns_nop 1\nv_max_f32_dpp v66, v66, v66 row_ror:1 row_mask:0xf bank_mask:0xf\nQmvaa 12,132,20\nv_sub_f32 v56, 0xc1980000, v66\nv_mov_b32 v57, v56\nQmvaa 12,128,16\nv_mov_b32 v58, v56\n"
"v_mov_b32 v59, v56\nQa 0,0,56\nQa 1,1,56\nQa 2,2,56\nQa 3,3,56\nQe 0\nQe 1\nQe 2\n"
"Qe 3\nQk 16,0,1\nQk 17,2,3\nQa 4,4,56\nQa 5,5,56\nQa 6,6,56\nQa 7,7,56\nQcvaa 0,112,32,56\n"
"Qe 4\nQmvaa 0,116,36\nQe 5\nQmvaa 0,120,40\nQe 6\nQe 7\nQmvaa 0,124,44\nQk 18,4,5\n"
"Qk 19,6,7\nQcvaa 4,140,44,56\nQa 8,8,56\nQa 9,9,56\nQmvaa 4,136,40\nQa 10,10,56\nQa 11,11,56\nQmvaa 4,132,36\n"
"Qa 12,12,56\nQa 13,13,56\nQmvaa 4,128,32\ns_waitcnt lgkmcnt(0)\nQa 14,14,56\nQzvva 96,16,148\nQa 15,15,56\nQzvva 100,16,156\n"
"Qe 8\nQzvva 104,16,164\nQe 9\nQe 10\nQe 11\nQe 12\nQe 13\nQe 14\n"
"Qe 15\nQzvva 108,16,172\nQk 20,8,9\nQk 21,10,11\nQk 22,12,13\nQk 23,14,15\ns_nop 1\nQzvva 140,20,172\n"
"Qe 0\nQzvva 136,20,164\nQe 1\nQzvva 132,20,156\nQe 2\nQzvva 128,20,148\nQe 3\nv_smfmac_f32_16x16x64_bf16 a[176:179], a[204:207], v[16:23], v77\n"
"Qk 24,0,1\nQcvaa 8,112,48,56\ns_add_u32 m0, s61, 99072\nQk 25,2,3\nQmvaa 8,116,52\nbuffer_load_dwordx4 v69, s[40:43], s60 offen nt lds\nQmvaa 8,120,56\nQe 4\n"
"Qmvaa 8,124,60\ns_or_b32 s65, s65, 0x1d8000\ns_add_u32 s65, s65, 2048\nQcvaa 12,140,60,56\ns_andn2_b32 s65, s65, 0x1d8000\nQmvaa 12,136,56\nQe 5\nQmvaa 12,132,52\n"
"buffer_load_dwordx4 v69, s[40:43], s60 offen offset:1024 nt lds\nQmvaa 12,128,48\nQe 6\nQcvaa 0,128,64,56\ns_memrealtime s[74:75]\nQk 26,4,5\nQmvaa 0,132,68\nQe 7\n"
"Qmvaa 0,136,72\nQk 27,6,7\nQmvaa 0,140,76\nbuffer_load_dwordx4 v69, s[40:43], s90 offen offset:2048 nt lds\nQcvaa 4,124,76,56\nQe 8\nQmvaa 4,120,72\nQe 9\n"
"Qmvaa 4,116,68\nQe 10\nQmvaa 4,112,64\nQe 11\nbuffer_load_dwordx4 v69, s[40:43], s90 offen offset:3072 nt lds\nQzvva 160,24,148\nQe 12\nQzvva 164,24,156\n"
"Qe 13\ns_add_u32 m0, s64, 99072\nQzvva 168,24,164\nbuffer_load_dwordx4 v72, s[44:47], s60 offen nt lds\nQe 14\nQe 15\nQzvva 172,24,172\nQk 28,8,9\n"
"Qk 29,10,11\nQk 30,12,13\nQk 31,14,15\ns_nop 1\nQzvva 204,28,172\nQe 0\nbuffer_load_dwordx4 v72, s[44:47], s60 offen offset:1024 nt lds\nQzvva 200,28,164\n"
"Qe 1\nQzvva 196,28,156\nQe 2\nQzvva 192,28,148\nbuffer_load_dwordx4 v72, s[44:47], s90 offen offset:2048 nt lds\nv_smfmac_f32_16x16x64_bf16 a[180:183], a[204:207], v[24:31], v77\nQk 34,0,1\nQcvaa 8,112,80,56\n"
"Qe 3\nQmvaa 8,116,84\nQk 35,2,3\nQmvaa 8,120,88\ns_waitcnt vmcnt(15)\ns_barrier\nQmvaa 8,124,92\nbuffer_load_dwordx4 v72, s[44:47], s90 offen offset:3072 nt lds\n"
"Qcvaa 12,140,92,56\ns_waitcnt lgkmcnt(0)\nQmvaa 12,136,88\nQmvaa 12,132,84\ns_add_u32 s77, s74, 270\nQmvaa 12,128,80\ns_xor_b32 s60, s65, %21\ns_add_u32 s90, s60, 260096\n"
"Qcvaa 0,128,96,56\nQda 128,68,34048\nQmvaa 0,132,100\nQda 132,68,34560\nQmvaa 0,136,104\nQda 136,68,34304\nQe 4\nQe 5\n"
"Qe 6\nQe 7\nQmvaa 0,140,108\nQda 140,68,34816\nQk 32,4,5\nQk 33,6,7\nQcvaa 4,124,108,56\nQda 124,68,33792\n"
"Qmvaa 4,120,104\nQda 120,68,33280\nQmvaa 4,116,100\nQda 116,68,33536\nQmvaa 4,112,96\nQda 112,68,33024\nQzvva 224,32,148\nQe 8\n"
"Qzvva 228,32,156\nQe 9\nQzvva 232,32,164\nQe 10\nQe 11\nQzvva 236,32,172\ns_waitcnt lgkmcnt(0)\nQk 36,8,9\n"
"Qk 37,10,11\nQcvaa 8,112,0,56\nQe 12\nQmvaa 8,116,4\nv_add_u32 v68, 66048, v68\nQmvaa 8,120,8\nQe 13\nQe 14\n"
"Qe 15\nQmvaa 8,124,12\nQk 38,12,13\nQk 39,14,15\nQcvaa 12,140,12,56\nQe 0\nQmvaa 12,136,8\nQe 1\n"
"Qmvaa 12,132,4\nQe 2\nQmvaa 12,128,0\nQe 3\nv_smfmac_f32_16x16x64_bf16 a[184:187], a[204:207], v[32:39], v77\nQk 42,0,1\nQzava 208,36,148\nQe 4\n"
"Qzava 212,36,156\nQe 5\nQzava 216,36,164\nQe 6\nQe 8\nQe 7\nQzava 220,36,172\nQe 9\n"
"Qk 43,2,3\nQk 40,4,5\nQk 41,6,7\nQta 168,70,768\nQta 170,70,1792\nQzava 252,40,172\nQe 10\nQta 172,70,33792\n"
"Qta 174,70,34816\nQzava 248,40,164\nQe 11\nQta 160,70,512\nQta 162,70,1536\nQzava 244,40,156\nQk 44,8,9\nQk 45,10,11\n"
"Qta 164,70,33536\nQta 166,70,34560\nQzava 240,40,148\nQe 12\nQcvaa 0,112,16,56\nQta 152,70,256\nQta 154,70,1280\nQmvaa 0,116,20\n"
"Qe 13\nQmvaa 0,120,24\nQta 156,70,33280\nQta 158,70,34304\nQmvaa 0,124,28\nQe 14\nQcvaa 4,140,28,56\nQta 144,70,0\n"
"Qta 146,70,1024\nQmvaa 4,136,24\nQe 15\nQmvaa 4,132,20\nQta 148,70,33024\nQta 150,70,34048\nQmvaa 4,128,16\nQk 46,12,13\n"
"Qk 47,14,15\nQcvaa 8,128,32,56\nv_add_u32 v75, 1, v75\nQmvaa 8,132,36\ns_waitcnt vmcnt(8)\nds_write_b32 v73, v75\nQmvaa 8,136,40\nQe 0\n"
"Qmvaa 8,140,44\nQe 1\nQcvaa 12,124,44,56\nQe 2\nQmvaa 12,120,40\nQe 3\nQmvaa 12,116,36\nQe 4\n"
"Qmvaa 12,112,32\ns_waitcnt lgkmcnt(1)\nQk 48,0,1\nv_smfmac_f32_16x16x64_bf16 a[188:191], a[204:207], v[40:47], v77\nQk 49,2,3\nQzvva 80,16,144\nQe 5\nQmvva 80,44,148\n"
"Qe 6\nQzvva 84,16,152\nQe 7\nQmvva 84,44,156\nQk 50,4,5\nQk 51,6,7\n.Lpg5:\ns_cbranch_execz .Ltr9\n"
"Qzvva 88,16,160\nQe 8\nQmvva 88,44,164\nQe 9\nQzvva 92,16,168\nQe 10\nQmvva 92,44,172\nQe 11\n"
"Qzvva 124,20,168\nQk 54,8,9\nQk 55,10,11\nQmvva 124,48,172\nds_read_b32 v66, v74\nQzvva 120,20,160\nQe 12\nQmvva 120,48,164\n"
"Qe 13\nQzvva 116,20,152\nQe 14\nQmvva 116,48,156\nQe 15\nQzvva 112,20,144\ns_waitcnt lgkmcnt(0)\nv_cmp_gt_u32 vcc, v75, v66\n"
"Qmvva 112,48,148\ns_cbranch_vccnz .Lsa1\n.Lra1:\nQk 52,12,13\nQcvaa 0,112,48,56\ns_add_u32 m0, s61, 0\nQk 53,14,15\nQmvaa 0,116,52\n"
"buffer_load_dwordx4 v69, s[40:43], s60 offen nt lds\nQmvaa 0,120,56\nQmvaa 0,124,60\ns_add_u32 s65, s65, 2048\nQcvaa 4,140,60,56\nQmvaa 4,136,56\nQmvaa 4,132,52\nbuffer_load_dwordx4 v69, s[40:43], s60 offen offset:1024 nt lds\n"
"Qmvaa 4,128,48\nQcvaa 8,128,64,56\ns_nop 0\nQmvaa 8,132,68\nQe 0\nQmvaa 8,136,72\nQe 1\nQmvaa 8,140,76\n"
"buffer_load_dwordx4 v69, s[40:43], s90 offen offset:2048 nt lds\nQcvaa 12,124,76,56\nQe 2\nQmvaa 12,120,72\nQe 3\nQmvaa 12,116,68\nQe 4\nQmvaa 12,112,64\n"
"Qe 5\nv_smfmac_f32_16x16x64_bf16 a[192:195], a[204:207], v[48:55], v77\nQk 20,0,1\nQzvva 144,24,144\nbuffer_load_dwordx4 v69, s[40:43], s90 offen offset:3072 nt lds\nQmvva 144,52,148\nQe 6\nQzvva 148,24,152\n"
"Qe 7\nQmvva 148,52,156\ns_add_u32 m0, s64, 0\nQk 21,2,3\nQzvva 152,24,160\nbuffer_load_dwordx4 v72, s[44:47], s60 offen nt lds\nQmvva 152,52,164\nQk 22,4,5\n"
"Qk 23,6,7\nQzvva 156,24,168\nQe 8\nQmvva 156,52,172\nQe 9\nQzvva 188,28,168\nQe 10\nQmvva 188,20,172\n"
"buffer_load_dwordx4 v72, s[44:47], s60 offen offset:1024 nt lds\nQzvva 184,28,160\nQe 11\nQmvva 184,20,164\nQk 26,8,9\nQk 27,10,11\nQzvva 180,28,152\nQe 12\n"
"Qmvva 180,20,156\nQe 13\nQzvva 176,28,144\nbuffer_load_dwordx4 v72, s[44:47], s90 offen offset:2048 nt lds\nQmvva 176,20,148\nQe 14\nQcvaa 0,112,80,56\nQe 15\n"
"Qmvaa 0,116,84\nQk 24,12,13\nQk 25,14,15\nQmvaa 0,120,88\nQmvaa 0,124,92\nbuffer_load_dwordx4 v72, s[44:47], s90 offen offset:3072 nt lds\nQcvaa 4,140,92,56\nQmvaa 4,136,88\n"
"Qmvaa 4,132,84\nQmvaa 4,128,80\ns_xor_b32 s60, s65, %21\ns_add_u32 s90, s60, 260096\nQcvaa 8,128,96,56\nQda 128,68,1024\nQmvaa 8,132,100\nQda 132,68,1536\n"
"Qmvaa 8,136,104\nQda 136,68,1280\nQmvaa 8,140,108\nQda 140,68,1792\nQcvaa 12,124,108,56\nQda 124,68,768\nQmvaa 12,120,104\nQda 120,68,256\n"
"Qmvaa 12,116,100\nQda 116,68,512\nQmvaa 12,112,96\nQda 112,68,0\nv_smfmac_f32_16x16x64_bf16 a[196:199], a[204:207], v[20:27], v77\nQzvva 208,32,144\nQe 0\nQmvva 208,24,148\n"
"Qe 1\nQzvva 212,32,152\nQe 2\nQmvva 212,24,156\nQe 3\nQzvva 216,32,160\nQk 28,0,1\nQk 29,2,3\n"
"Qmvva 216,24,164\nQe 4\nQzvva 220,32,168\nQe 5\nQmvva 220,24,172\ns_waitcnt lgkmcnt(0)\nQk 30,4,5\nQcvaa 0,112,0,56\n"
"Qe 6\nQmvaa 0,116,4\nQe 7\nQmvaa 0,120,8\nQk 31,6,7\nQmvaa 0,124,12\nQe 8\nQcvaa 4,140,12,56\n"
"Qe 9\nQmvaa 4,136,8\nQe 10\nQmvaa 4,132,4\nQe 11\nQmvaa 4,128,0\nQe 12\nQzvva 240,36,144\n"
"Qe 13\nQmvva 240,28,148\nQe 14\nQzvva 244,36,152\nQe 15\nQmvva 244,28,156\nQk 34,8,9\nQk 35,10,11\n"
"Qzvva 248,36,160\nQk 32,12,13\nQk 33,14,15\nQmvva 248,28,164\nQe 0\nQzvva 252,36,168\nQe 1\nQmvva 252,28,172\n"
"Qe 2\nQzava 236,40,168\nQta 168,70,33920\nQta 170,70,34944\nQmava 236,32,172\nQe 3\nQzava 232,40,160\nQta 172,71,896\n"
"Qta 174,71,1920\nQmava 232,32,164\nQk 36,0,1\nQk 37,2,3\nQzava 228,40,152\nQta 160,70,33664\nQta 162,70,34688\nQmava 228,32,156\n"
"Qe 4\nQzava 224,40,144\nQta 164,71,640\nQta 166,71,1664\nQmava 224,32,148\nQe 5\nv_smfmac_f32_16x16x64_bf16 a[200:203], a[204:207], v[28:35], v77\nQta 152,70,33408\n"
"Qta 154,70,34432\nQcvaa 8,112,16,56\nQe 6\nQmvaa 8,116,20\nQta 156,71,384\nQta 158,71,1408\nQmvaa 8,120,24\nQe 7\n"
"Qmvaa 8,124,28\nQta 144,70,33152\nQta 146,70,34176\nQcvaa 12,140,28,56\nQk 38,4,5\nQk 39,6,7\nQmvaa 12,136,24\nQta 148,71,128\n"
"Qta 150,71,1152\nQmvaa 12,132,20\nQmvaa 12,128,16\ns_lshl_b32 s66, s67, 3\n.Lloop:\ns_bitcmp1_b32 s99, s66\ns_cbranch_scc1 .Lhook\n.Lhookret:\n"
"Qcvaa 0,128,32,56\nv_add_u32 v75, 1, v75\nQmvaa 0,132,36\ns_waitcnt vmcnt(8)\nds_write_b32 v73, v75\nQmvaa 0,136,40\nQe 8\nQmvaa 0,140,44\n"
"Qe 9\nQcvaa 4,124,44,56\nQe 10\nQmvaa 4,120,40\nQe 11\nQmvaa 4,116,36\nQe 12\nQmvaa 4,112,32\n"
"s_waitcnt lgkmcnt(1)\nQk 40,8,9\nQmvva 96,44,144\nQe 13\nQmvva 96,36,148\nQe 14\nQmvva 100,44,152\nQe 15\n"
"Qmvva 100,36,156\nQk 41,10,11\nQk 42,12,13\nQmvva 104,44,160\nQk 43,14,15\nQmvva 104,36,164\nQe 0\nQmvva 108,44,168\n"
"Qe 1\nQmvva 108,36,172\nQe 2\nQmvva 140,48,168\nQe 3\nQmvva 140,40,172\nQk 46,0,1\nQk 47,2,3\n"
"Qmvva 136,48,160\nds_read_b32 v66, v74\nQmvva 136,40,164\nQe 4\nQmvva 132,48,152\nQe 5\nQmvva 132,40,156\nQe 6\n"
"Qmvva 128,48,144\nQe 7\nQmvva 128,40,148\ns_waitcnt lgkmcnt(0)\nv_cmp_gt_u32 vcc, v75, v66\nv_smfmac_f32_16x16x64_bf16 a[176:179], a[204:207], v[36:43], v77\ns_cbranch_vccnz .Lsa2\n.Lra2:\n"
"Qcvaa 8,112,48,56\ns_add_u32 m0, s61, 33024\nQk 44,4,5\nQmvaa 8,116,52\nbuffer_load_dwordx4 v69, s[40:43], s60 offen nt lds\nQmvaa 8,120,56\nQk 45,6,7\nQmvaa 8,124,60\n"
"s_add_u32 s65, s65, 2048\nQcvaa 12,140,60,56\nQmvaa 12,136,56\nQmvaa 12,132,52\nbuffer_load_dwordx4 v69, s[40:43], s60 offen offset:1024 nt lds\nQmvaa 12,128,48\nQcvaa 0,128,64,56\ns_memrealtime s[74:75]\n"
"Qmvaa 0,132,68\nQe 8\nQmvaa 0,136,72\nQe 9\nQmvaa 0,140,76\nbuffer_load_dwordx4 v69, s[40:43], s90 offen offset:2048 nt lds\nQcvaa 4,124,76,56\nQe 10\n"
"Qmvaa 4,120,72\nQe 11\nQmvaa 4,116,68\nQe 12\nQmvaa 4,112,64\nQe 13\nQmvva 160,52,144\nbuffer_load_dwordx4 v69, s[40:43], s90 offen offset:3072 nt lds\n"
"Qmvva 160,44,148\nQe 14\nQmvva 164,52,152\nQe 15\nQmvva 164,44,156\nQk 48,8,9\nQk 49,10,11\nQmvva 168,52,160\n"
"s_add_u32 m0, s64, 33024\nQk 50,12,13\nQmvva 168,44,164\nbuffer_load_dwordx4 v72, s[44:47], s60 offen nt lds\nQmvva 172,52,168\nQk 51,14,15\nQmvva 172,44,172\nQe 0\n"
"Qmvva 204,20,168\nQe 1\nQmvva 204,48,172\nQe 2\nQmvva 200,20,160\nbuffer_load_dwordx4 v72, s[44:47], s60 offen offset:1024 nt lds\nQmvva 200,48,164\nQe 3\n"
"Qmvva 196,20,152\nQk 18,0,1\nQk 19,2,3\nQmvva 196,48,156\nQe 4\nQmvva 192,20,144\nQe 5\nQmvva 192,48,148\n"
"buffer_load_dwordx4 v72, s[44:47], s90 offen offset:2048 nt lds\nv_smfmac_f32_16x16x64_bf16 a[180:183], a[204:207], v[44:51], v77\nQk 16,4,5\nQcvaa 8,112,80,56\nQe 6\nQmvaa 8,116,84\nQe 7\nQmvaa 8,120,88\n"
"Qk 17,6,7\nQmvaa 8,124,92\nbuffer_load_dwordx4 v72, s[44:47], s90 offen offset:3072 nt lds\nQcvaa 12,140,92,56\ns_waitcnt lgkmcnt(0)\nQmvaa 12,136,88\ns_cmp_gt_u32 s77, s74\ns_cbranch_scc1 .Lsp2\n"
".Lrp2:\nQmvaa 12,132,84\ns_add_u32 s77, s74, 239\nQmvaa 12,128,80\ns_xor_b32 s60, s65, %21\ns_add_u32 s90, s60, 260096\nQcvaa 0,128,96,56\nQda 128,68,34048\n"
"Qmvaa 0,132,100\nQda 132,68,34560\nQmvaa 0,136,104\nQda 136,68,34304\nQmvaa 0,140,108\nQda 140,68,34816\nQcvaa 4,124,108,56\nQda 124,68,33792\n"
"Qmvaa 4,120,104\nQda 120,68,33280\nQmvaa 4,116,100\nQda 116,68,33536\nQmvaa 4,112,96\nQda 112,68,33024\nQmvva 224,24,144\nQe 8\n"
"Qmvva 224,16,148\nQe 9\nQmvva 228,24,152\nQe 10\nQmvva 228,16,156\nQe 11\nQmvva 232,24,160\nQk 20,8,9\n"
"Qk 21,10,11\nQmvva 232,16,164\nQe 12\nQmvva 236,24,168\nQe 13\nQmvva 236,16,172\ns_waitcnt lgkmcnt(0)\nQk 22,12,13\n"
"Qcvaa 8,112,0,56\nQe 14\nQmvaa 8,116,4\nv_subrev_u32 v68, 66048, v68\nQmvaa 8,120,8\nQe 15\nQmvaa 8,124,12\nQk 23,14,15\n"
"Qcvaa 12,140,12,56\nQe 0\nQmvaa 12,136,8\nQe 1\nQmvaa 12,132,4\nQe 2\nQmvaa 12,128,0\nQe 3\n"
"v_smfmac_f32_16x16x64_bf16 a[184:187], a[204:207], v[16:23], v77\nQk 26,0,1\nQmava 208,28,144\nQe 4\nQmava 208,20,148\nQe 5\nQmava 212,28,152\nQe 6\n"
"Qmava 212,20,156\nQe 7\nQmava 216,28,160\nQk 27,2,3\nQk 24,4,5\nQmava 216,20,164\nQk 25,6,7\nQmava 220,28,168\n"
"Qe 8\nQmava 220,20,172\nQe 9\nQmava 252,32,168\nQta 168,71,768\nQta 170,71,1792\nQmava 252,24,172\nQe 10\n"
"Qmava 248,32,160\nQta 172,71,33792\nQta 174,71,34816\nQmava 248,24,164\nQe 11\nQmava 244,32,152\nQta 160,71,512\nQta 162,71,1536\n"
"Qmava 244,24,156\nQk 28,8,9\nQk 29,10,11\nQmava 240,32,144\nQta 164,71,33536\nQta 166,71,34560\nQmava 240,24,148\nQe 12\n"
"Qcvaa 0,112,16,56\nQta 152,71,256\nQta 154,71,1280\nQmvaa 0,116,20\nQe 13\nQmvaa 0,120,24\nQta 156,71,33280\nQta 158,71,34304\n"
"Qmvaa 0,124,28\nQe 14\nQcvaa 4,140,28,56\nQta 144,71,0\nQta 146,71,1024\nQmvaa 4,136,24\nQe 15\nQmvaa 4,132,20\n"
"Qta 148,71,33024\nQta 150,71,34048\nQmvaa 4,128,16\nQk 30,12,13\nQk 31,14,15\nQcvaa 8,128,32,56\nv_add_u32 v75, 1, v75\nQmvaa 8,132,36\n"
"s_waitcnt vmcnt(8)\nds_write_b32 v73, v75\nQmvaa 8,136,40\nQe 0\nQmvaa 8,140,44\nQe 1\nQcvaa 12,124,44,56\nQe 2\n"
"Qmvaa 12,120,40\nQe 3\nQmvaa 12,116,36\nQe 4\nQmvaa 12,112,32\ns_waitcnt lgkmcnt(1)\nQk 32,0,1\nv_smfmac_f32_16x16x64_bf16 a[188:191], a[204:207], v[24:31], v77\n"
"Qk 33,2,3\nQmvva 80,36,144\nQe 5\nQmvva 80,28,148\nQe 6\nQmvva 84,36,152\nQe 7\nQmvva 84,28,156\n"
"Qk 34,4,5\nQk 35,6,7\nQmvva 88,36,160\nQe 8\nQmvva 88,28,164\nQe 9\nQmvva 92,36,168\nQe 10\n"
"Qmvva 92,28,172\nQe 11\nQmvva 124,40,168\nQk 38,8,9\nQk 39,10,11\nQmvva 124,32,172\nds_read_b32 v66, v74\nQmvva 120,40,160\n"
"Qe 12\nQmvva 120,32,164\nQe 13\nQmvva 116,40,152\nQe 14\nQmvva 116,32,156\nQe 15\nQmvva 112,40,144\n"
"s_waitcnt lgkmcnt(0)\nv_cmp_gt_u32 vcc, v75, v66\nQmvva 112,32,148\ns_cbranch_vccnz .Lsa3\n.Lra3:\nQk 36,12,13\nQcvaa 0,112,48,56\ns_add_u32 m0, s61, 66048\n"
"Qk 37,14,15\nQmvaa 0,116,52\nbuffer_load_dwordx4 v69, s[40:43], s60 offen nt lds\nQmvaa 0,120,56\nQmvaa 0,124,60\ns_add_u32 s65, s65, 2048\nQcvaa 4,140,60,56\nQmvaa 4,136,56\n"
"Qmvaa 4,132,52\nbuffer_load_dwordx4 v69, s[40:43], s60 offen offset:1024 nt lds\nQmvaa 4,128,48\nQcvaa 8,128,64,56\ns_nop 0\nQmvaa 8,132,68\nQe 0\nQmvaa 8,136,72\n"
"Qe 1\nQmvaa 8,140,76\nbuffer_load_dwordx4 v69, s[40:43], s90 offen offset:2048 nt lds\nQcvaa 12,124,76,56\nQe 2\nQmvaa 12,120,72\nQe 3\nQmvaa 12,116,68\n"
"Qe 4\nQmvaa 12,112,64\nQe 5\nv_smfmac_f32_16x16x64_bf16 a[192:195], a[204:207], v[32:39], v77\nQk 40,0,1\nQmvva 144,44,144\nbuffer_load_dwordx4 v69, s[40:43], s90 offen offset:3072 nt lds\nQmvva 144,36,148\n"
"Qe 6\nQmvva 148,44,152\nQe 7\nQmvva 148,36,156\ns_add_u32 m0, s64, 66048\nQk 41,2,3\nQmvva 152,44,160\nbuffer_load_dwordx4 v72, s[44:47], s60 offen nt lds\n"
"Qmvva 152,36,164\nQk 42,4,5\nQk 43,6,7\nQmvva 156,44,168\nQe 8\nQmvva 156,36,172\nQe 9\nQmvva 188,48,168\n"
"Qe 10\nQmvva 188,40,172\nbuffer_load_dwordx4 v72, s[44:47], s60 offen offset:1024 nt lds\nQmvva 184,48,160\nQe 11\nQmvva 184,40,164\nQk 46,8,9\nQk 47,10,11\n"
"Qmvva 180,48,152\nQe 12\nQmvva 180,40,156\nQe 13\nQmvva 176,48,144\nbuffer_load_dwordx4 v72, s[44:47], s90 offen offset:2048 nt lds\nQmvva 176,40,148\n.Lpg6:\n"
"s_cbranch_execz .Ltr9\nQe 14\nQcvaa 0,112,80,56\nQe 15\nQmvaa 0,116,84\nQk 44,12,13\nQk 45,14,15\nQmvaa 0,120,88\n"
"Qmvaa 0,124,92\nbuffer_load_dwordx4 v72, s[44:47], s90 offen offset:3072 nt lds\nQcvaa 4,140,92,56\nQmvaa 4,136,88\nQmvaa 4,132,84\nQmvaa 4,128,80\ns_xor_b32 s60, s65, %21\ns_add_u32 s90, s60, 260096\n"
"Qcvaa 8,128,96,56\nQda 128,68,1024\nQmvaa 8,132,100\nQda 132,68,1536\nQmvaa 8,136,104\nQda 136,68,1280\nQmvaa 8,140,108\nQda 140,68,1792\n"
"Qcvaa 12,124,108,56\nQda 124,68,768\nQmvaa 12,120,104\nQda 120,68,256\nQmvaa 12,116,100\nQda 116,68,512\nQmvaa 12,112,96\nQda 112,68,0\n"
"v_smfmac_f32_16x16x64_bf16 a[196:199], a[204:207], v[40:47], v77\nQmvva 208,16,144\nQe 0\nQmvva 208,44,148\nQe 1\nQmvva 212,16,152\nQe 2\nQmvva 212,44,156\n"
"Qe 3\nQmvva 216,16,160\nQk 48,0,1\nQk 49,2,3\nQmvva 216,44,164\nQe 4\nQmvva 220,16,168\nQe 5\n"
"Qmvva 220,44,172\ns_waitcnt lgkmcnt(0)\nQk 50,4,5\nQcvaa 0,112,0,56\nQe 6\nQmvaa 0,116,4\nQe 7\nQmvaa 0,120,8\n"
"Qk 51,6,7\nQmvaa 0,124,12\nQe 8\nQcvaa 4,140,12,56\nQe 9\nQmvaa 4,136,8\nQe 10\nQmvaa 4,132,4\n"
"Qe 11\nQmvaa 4,128,0\nQe 12\nQmvva 240,20,144\nQe 13\nQmvva 240,48,148\nQe 14\nQmvva 244,20,152\n"
"Qe 15\nQmvva 244,48,156\nQk 54,8,9\nQk 55,10,11\nQmvva 248,20,160\nQk 52,12,13\nQk 53,14,15\nQmvva 248,48,164\n"
"Qe 0\nQmvva 252,20,168\nQe 1\nQmvva 252,48,172\nQe 2\nQmava 236,24,168\nQta 168,71,33920\nQta 170,71,34944\n"
"Qmava 236,52,172\nQe 3\nQmava 232,24,160\nQta 172,70,896\nQta 174,70,1920\nQmava 232,52,164\nQk 16,0,1\nQk 17,2,3\n"
"Qmava 228,24,152\nQta 160,71,33664\nQta 162,71,34688\nQmava 228,52,156\nQe 4\nQmava 224,24,144\nQta 164,70,640\nQta 166,70,1664\n"
"Qmava 224,52,148\nQe 5\nv_smfmac_f32_16x16x64_bf16 a[200:203], a[204:207], v[48:55], v77\nQta 152,71,33408\nQta 154,71,34432\nQcvaa 8,112,16,56\nQe 6\nQmvaa 8,116,20\n"
"Qta 156,70,384\nQta 158,70,1408\nQmvaa 8,120,24\nQe 7\nQmvaa 8,124,28\nQta 144,71,33152\nQta 146,71,34176\nQcvaa 12,140,28,56\n"
"Qk 18,4,5\nQk 19,6,7\nQmvaa 12,136,24\nQta 148,70,128\nQta 150,70,1152\nQmvaa 12,132,20\nQmvaa 12,128,16\nQcvaa 0,128,32,56\n"
"v_add_u32 v75, 1, v75\nQmvaa 0,132,36\ns_waitcnt vmcnt(8)\nds_write_b32 v73, v75\nQmvaa 0,136,40\nQe 8\nQmvaa 0,140,44\nQe 9\n"
"Qcvaa 4,124,44,56\nQe 10\nQmvaa 4,120,40\nQe 11\nQmvaa 4,116,36\nQe 12\nQmvaa 4,112,32\ns_waitcnt lgkmcnt(1)\n"
"Qk 20,8,9\nQmvva 96,28,144\nQe 13\nQmvva 96,16,148\nQe 14\nQmvva 100,28,152\nQe 15\nQmvva 100,16,156\n"
"Qk 21,10,11\nQk 22,12,13\nQmvva 104,28,160\nQk 23,14,15\nQmvva 104,16,164\nQe 0\nQmvva 108,28,168\nQe 1\n"
"Qmvva 108,16,172\nQe 2\nQmvva 140,32,168\nQe 3\nQmvva 140,20,172\nQk 26,0,1\nQk 27,2,3\nQmvva 136,32,160\n"
"ds_read_b32 v66, v74\nQmvva 136,20,164\nQe 4\nQmvva 132,32,152\nQe 5\nQmvva 132,20,156\nQe 6\nQmvva 128,32,144\n"
"Qe 7\nQmvva 128,20,148\ns_waitcnt lgkmcnt(0)\nv_cmp_gt_u32 vcc, v75, v66\nv_smfmac_f32_16x16x64_bf16 a[176:179], a[204:207], v[16:23], v77\ns_cbranch_vccnz .Lsa4\n.Lra4:\nQcvaa 8,112,48,56\n"
"s_add_u32 m0, s61, 99072\nQk 24,4,5\nQmvaa 8,116,52\nbuffer_load_dwordx4 v69, s[40:43], s60 offen nt lds\nQmvaa 8,120,56\nQk 25,6,7\nQmvaa 8,124,60\ns_or_b32 s65, s65, 0x1d8000\n"
"s_add_u32 s65, s65, 2048\nQcvaa 12,140,60,56\ns_andn2_b32 s65, s65, 0x1d8000\nQmvaa 12,136,56\nQmvaa 12,132,52\nbuffer_load_dwordx4 v69, s[40:43], s60 offen offset:1024 nt lds\nQmvaa 12,128,48\nQcvaa 0,128,64,56\n"
"s_memrealtime s[74:75]\nQmvaa 0,132,68\nQe 8\nQmvaa 0,136,72\nQe 9\nQmvaa 0,140,76\nbuffer_load_dwordx4 v69, s[40:43], s90 offen offset:2048 nt lds\nQcvaa 4,124,76,56\n"
"Qe 10\nQmvaa 4,120,72\nQe 11\nQmvaa 4,116,68\nQe 12\nQmvaa 4,112,64\nQe 13\nQmvva 160,36,144\n"
"buffer_load_dwordx4 v69, s[40:43], s90 offen offset:3072 nt lds\nQmvva 160,24,148\nQe 14\nQmvva 164,36,152\nQe 15\nQmvva 164,24,156\nQk 28,8,9\nQk 29,10,11\n"
"Qmvva 168,36,160\ns_add_u32 m0, s64, 99072\nQk 30,12,13\nQmvva 168,24,164\nbuffer_load_dwordx4 v72, s[44:47], s60 offen nt lds\nQmvva 172,36,168\nQk 31,14,15\nQmvva 172,24,172\n"
"Qe 0\nQmvva 204,40,168\nQe 1\nQmvva 204,28,172\nQe 2\nQmvva 200,40,160\nbuffer_load_dwordx4 v72, s[44:47], s60 offen offset:1024 nt lds\nQmvva 200,28,164\n"
"Qe 3\nQmvva 196,40,152\nQk 34,0,1\nQk 35,2,3\nQmvva 196,28,156\nQe 4\nQmvva 192,40,144\nQe 5\n"
"Qmvva 192,28,148\nbuffer_load_dwordx4 v72, s[44:47], s90 offen offset:2048 nt lds\nv_smfmac_f32_16x16x64_bf16 a[180:183], a[204:207], v[24:31], v77\nQk 32,4,5\nQcvaa 8,112,80,56\nQe 6\nQmvaa 8,116,84\nQe 7\n"
"Qmvaa 8,120,88\nQk 33,6,7\nQmvaa 8,124,92\nbuffer_load_dwordx4 v72, s[44:47], s90 offen offset:3072 nt lds\nQcvaa 12,140,92,56\ns_waitcnt lgkmcnt(0)\nQmvaa 12,136,88\ns_cmp_gt_u32 s77, s74\n"
"s_cbranch_scc1 .Lsp4\n.Lrp4:\nQmvaa 12,132,84\ns_add_u32 s77, s74, 239\nQmvaa 12,128,80\ns_xor_b32 s60, s65, %21\ns_add_u32 s90, s60, 260096\nQcvaa 0,128,96,56\n"
"Qda 128,68,34048\nQmvaa 0,132,100\nQda 132,68,34560\nQmvaa 0,136,104\nQda 136,68,34304\nQmvaa 0,140,108\nQda 140,68,34816\nQcvaa 4,124,108,56\n"
"Qda 124,68,33792\nQmvaa 4,120,104\nQda 120,68,33280\nQmvaa 4,116,100\nQda 116,68,33536\nQmvaa 4,112,96\nQda 112,68,33024\nQmvva 224,44,144\n"
"Qe 8\nQmvva 224,32,148\nQe 9\nQmvva 228,44,152\nQe 10\nQmvva 228,32,156\nQe 11\nQmvva 232,44,160\n"
"Qk 36,8,9\nQk 37,10,11\nQmvva 232,32,164\nQe 12\nQmvva 236,44,168\nQe 13\nQmvva 236,32,172\ns_waitcnt lgkmcnt(0)\n"
"Qk 38,12,13\nQcvaa 8,112,0,56\nQe 14\nQmvaa 8,116,4\nv_add_u32 v68, 66048, v68\nQmvaa 8,120,8\nQe 15\nQmvaa 8,124,12\n"
"Qk 39,14,15\nQcvaa 12,140,12,56\nQe 0\nQmvaa 12,136,8\nQe 1\nQmvaa 12,132,4\nQe 2\nQmvaa 12,128,0\n"
"Qe 3\nv_smfmac_f32_16x16x64_bf16 a[184:187], a[204:207], v[32:39], v77\nQk 42,0,1\nQmava 208,48,144\nQe 4\nQmava 208,36,148\nQe 5\nQmava 212,48,152\n"
"Qe 6\nQmava 212,36,156\nQe 7\nQmava 216,48,160\nQk 43,2,3\nQk 40,4,5\nQmava 216,36,164\nQk 41,6,7\n"
"Qmava 220,48,168\nQe 8\nQmava 220,36,172\nQe 9\nQmava 252,52,168\nQta 168,70,768\nQta 170,70,1792\nQmava 252,40,172\n"
"Qe 10\nQmava 248,52,160\nQta 172,70,33792\nQta 174,70,34816\nQmava 248,40,164\nQe 11\nQmava 244,52,152\nQta 160,70,512\n"
"Qta 162,70,1536\nQmava 244,40,156\nQk 44,8,9\nQk 45,10,11\nQmava 240,52,144\nQta 164,70,33536\nQta 166,70,34560\nQmava 240,40,148\n"
"Qe 12\nQcvaa 0,112,16,56\nQta 152,70,256\nQta 154,70,1280\nQmvaa 0,116,20\nQe 13\nQmvaa 0,120,24\nQta 156,70,33280\n"
"Qta 158,70,34304\nQmvaa 0,124,28\nQe 14\nQcvaa 4,140,28,56\nQta 144,70,0\nQta 146,70,1024\nQmvaa 4,136,24\nQe 15\n"
"Qmvaa 4,132,20\nQta 148,70,33024\nQta 150,70,34048\nQmvaa 4,128,16\nQk 46,12,13\nQk 47,14,15\nQcvaa 8,128,32,56\nv_add_u32 v75, 1, v75\n"
"Qmvaa 8,132,36\ns_waitcnt vmcnt(8)\nds_write_b32 v73, v75\nQmvaa 8,136,40\nQe 0\nQmvaa 8,140,44\nQe 1\nQcvaa 12,124,44,56\n"
"Qe 2\nQmvaa 12,120,40\nQe 3\nQmvaa 12,116,36\nQe 4\nQmvaa 12,112,32\ns_waitcnt lgkmcnt(1)\nQk 48,0,1\n"
"v_smfmac_f32_16x16x64_bf16 a[188:191], a[204:207], v[40:47], v77\nQk 49,2,3\nQmvva 80,16,144\nQe 5\nQmvva 80,44,148\nQe 6\nQmvva 84,16,152\nQe 7\n"
"Qmvva 84,44,156\nQk 50,4,5\nQk 51,6,7\nQmvva 88,16,160\nQe 8\nQmvva 88,44,164\nQe 9\nQmvva 92,16,168\n"
"Qe 10\nQmvva 92,44,172\nQe 11\nQmvva 124,20,168\nQk 54,8,9\nQk 55,10,11\nQmvva 124,48,172\nds_read_b32 v66, v74\n"
"Qmvva 120,20,160\nQe 12\nQmvva 120,48,164\nQe 13\nQmvva 116,20,152\nQe 14\nQmvva 116,48,156\nQe 15\n"
"Qmvva 112,20,144\ns_waitcnt lgkmcnt(0)\nv_cmp_gt_u32 vcc, v75, v66\nQmvva 112,48,148\ns_cbranch_vccnz .Lsa5\n.Lra5:\nQk 52,12,13\nQcvaa 0,112,48,56\n"
"s_add_u32 m0, s61, 0\nQk 53,14,15\nQmvaa 0,116,52\nbuffer_load_dwordx4 v69, s[40:43], s60 offen nt lds\nQmvaa 0,120,56\nQmvaa 0,124,60\ns_add_u32 s65, s65, 2048\nQcvaa 4,140,60,56\n"
"Qmvaa 4,136,56\nQmvaa 4,132,52\nbuffer_load_dwordx4 v69, s[40:43], s60 offen offset:1024 nt lds\nQmvaa 4,128,48\nQcvaa 8,128,64,56\ns_nop 0\nQmvaa 8,132,68\nQe 0\n"
"Qmvaa 8,136,72\nQe 1\nQmvaa 8,140,76\nbuffer_load_dwordx4 v69, s[40:43], s90 offen offset:2048 nt lds\nQcvaa 12,124,76,56\nQe 2\nQmvaa 12,120,72\nQe 3\n"
"Qmvaa 12,116,68\nQe 4\nQmvaa 12,112,64\nQe 5\nv_smfmac_f32_16x16x64_bf16 a[192:195], a[204:207], v[48:55], v77\nQk 20,0,1\nQmvva 144,24,144\nbuffer_load_dwordx4 v69, s[40:43], s90 offen offset:3072 nt lds\n"
"Qmvva 144,52,148\nQe 6\nQmvva 148,24,152\nQe 7\nQmvva 148,52,156\ns_add_u32 m0, s64, 0\nQk 21,2,3\nQmvva 152,24,160\n"
"buffer_load_dwordx4 v72, s[44:47], s60 offen nt lds\nQmvva 152,52,164\nQk 22,4,5\nQk 23,6,7\nQmvva 156,24,168\nQe 8\nQmvva 156,52,172\nQe 9\n"
"Qmvva 188,28,168\nQe 10\nQmvva 188,20,172\nbuffer_load_dwordx4 v72, s[44:47], s60 offen offset:1024 nt lds\nQmvva 184,28,160\nQe 11\nQmvva 184,20,164\nQk 26,8,9\n"
"Qk 27,10,11\nQmvva 180,28,152\nQe 12\nQmvva 180,20,156\nQe 13\nQmvva 176,28,144\nbuffer_load_dwordx4 v72, s[44:47], s90 offen offset:2048 nt lds\nQmvva 176,20,148\n"
"Qe 14\nQcvaa 0,112,80,56\nQe 15\nQmvaa 0,116,84\nQk 24,12,13\nQk 25,14,15\nQmvaa 0,120,88\nQmvaa 0,124,92\n"
"buffer_load_dwordx4 v72, s[44:47], s90 offen offset:3072 nt lds\nQcvaa 4,140,92,56\nQmvaa 4,136,88\nQmvaa 4,132,84\nQmvaa 4,128,80\ns_xor_b32 s60, s65, %21\ns_add_u32 s90, s60, 260096\nQcvaa 8,128,96,56\n"
"Qda 128,68,1024\nQmvaa 8,132,100\nQda 132,68,1536\nQmvaa 8,136,104\nQda 136,68,1280\nQmvaa 8,140,108\nQda 140,68,1792\nQcvaa 12,124,108,56\n"
"Qda 124,68,768\nQmvaa 12,120,104\nQda 120,68,256\nQmvaa 12,116,100\nQda 116,68,512\nQmvaa 12,112,96\nQda 112,68,0\nv_smfmac_f32_16x16x64_bf16 a[196:199], a[204:207], v[20:27], v77\n"
"Qmvva 208,32,144\nQe 0\nQmvva 208,24,148\nQe 1\nQmvva 212,32,152\nQe 2\nQmvva 212,24,156\nQe 3\n"
"Qmvva 216,32,160\nQk 28,0,1\nQk 29,2,3\nQmvva 216,24,164\nQe 4\nQmvva 220,32,168\nQe 5\nQmvva 220,24,172\n"
"s_waitcnt lgkmcnt(0)\nQk 30,4,5\nQcvaa 0,112,0,56\nQe 6\nQmvaa 0,116,4\nQe 7\nQmvaa 0,120,8\nQk 31,6,7\n"
"Qmvaa 0,124,12\nQe 8\nQcvaa 4,140,12,56\nQe 9\nQmvaa 4,136,8\nQe 10\nQmvaa 4,132,4\nQe 11\n"
"Qmvaa 4,128,0\nQe 12\nQmvva 240,36,144\nQe 13\nQmvva 240,28,148\nQe 14\nQmvva 244,36,152\nQe 15\n"
"Qmvva 244,28,156\nQk 34,8,9\nQk 35,10,11\nQmvva 248,36,160\nQk 32,12,13\nQk 33,14,15\nQmvva 248,28,164\nQe 0\n"
"Qmvva 252,36,168\nQe 1\nQmvva 252,28,172\nQe 2\nQmava 236,40,168\nQta 168,70,33920\nQta 170,70,34944\nQmava 236,32,172\n"
"Qe 3\nQmava 232,40,160\n.Lpg7:\ns_cbranch_execz .Ltr9\nQta 172,71,896\nQta 174,71,1920\nQmava 232,32,164\nQk 36,0,1\n"
"Qk 37,2,3\nQmava 228,40,152\nQta 160,70,33664\nQta 162,70,34688\nQmava 228,32,156\nQe 4\nQmava 224,40,144\nQta 164,71,640\n"
"Qta 166,71,1664\nQmava 224,32,148\nQe 5\nv_smfmac_f32_16x16x64_bf16 a[200:203], a[204:207], v[28:35], v77\nQta 152,70,33408\nQta 154,70,34432\nQcvaa 8,112,16,56\nQe 6\n"
"Qmvaa 8,116,20\nQta 156,71,384\nQta 158,71,1408\nQmvaa 8,120,24\nQe 7\nQmvaa 8,124,28\nQta 144,70,33152\nQta 146,70,34176\n"
"Qcvaa 12,140,28,56\nQk 38,4,5\nQk 39,6,7\nQmvaa 12,136,24\nQta 148,71,128\nQta 150,71,1152\nQmvaa 12,132,20\nQmvaa 12,128,16\n"
"s_add_u32 s66, s66, 1\ns_bitcmp1_b32 0x7f7f, s66\ns_cbranch_scc1 .Lloop\ns_cmp_lg_u32 s67, 0\ns_cbranch_scc1 .Ltail1\nQcvaa 0,128,32,56\nv_add_u32 v75, 1, v75\nQmvaa 0,132,36\n"
"s_waitcnt vmcnt(8)\nds_write_b32 v73, v75\nQmvaa 0,136,40\nQe 8\nQmvaa 0,140,44\nQe 9\nQcvaa 4,124,44,56\nQe 10\n"
"Qmvaa 4,120,40\nQe 11\nQmvaa 4,116,36\nQe 12\nQmvaa 4,112,32\ns_waitcnt lgkmcnt(1)\nQk 40,8,9\nQmvva 96,44,144\n"
"Qe 13\nQmvva 96,36,148\nQe 14\nQmvva 100,44,152\nQe 15\nQmvva 100,36,156\nQk 41,10,11\nQk 42,12,13\n"
"Qmvva 104,44,160\nQk 43,14,15\nQmvva 104,36,164\nQe 0\nQmvva 108,44,168\nQe 1\nQmvva 108,36,172\nQe 2\n"
"Qmvva 140,48,168\nQe 3\nQmvva 140,40,172\nQk 46,0,1\nQk 47,2,3\nQmvva 136,48,160\nds_read_b32 v66, v74\nQmvva 136,40,164\n"
"Qe 4\nQmvva 132,48,152\nQe 5\nQmvva 132,40,156\nQe 6\nQmvva 128,48,144\nQe 7\nQmvva 128,40,148\n"
"s_waitcnt lgkmcnt(0)\nv_cmp_gt_u32 vcc, v75, v66\nv_smfmac_f32_16x16x64_bf16 a[176:179], a[204:207], v[36:43], v77\ns_cbranch_vccnz .Lsa30\n.Lra30:\nQcvaa 8,112,48,56\ns_add_u32 m0, s62, 0\ns_add_u32 s69, s87, 0\n"
"Qk 44,4,5\nQmvaa 8,116,52\nbuffer_load_dwordx4 v62, s[52:55], s69 offen lds\nQmvaa 8,120,56\nQk 45,6,7\nQmvaa 8,124,60\nQcvaa 12,140,60,56\nQmvaa 12,136,56\n"
"s_add_u32 s69, s87, 64504\nQmvaa 12,132,52\nbuffer_load_dwordx4 v62, s[52:55], s69 offen offset:1032 lds\nQmvaa 12,128,48\nQcvaa 0,128,64,56\nQmvaa 0,132,68\nQe 8\nQmvaa 0,136,72\n"
"s_add_u32 s69, s87, 129008\nQe 9\nQmvaa 0,140,76\nbuffer_load_dwordx4 v62, s[52:55], s69 offen offset:2064 lds\nQcvaa 4,124,76,56\nQe 10\nQmvaa 4,120,72\nQe 11\n"
"Qmvaa 4,116,68\nQe 12\nQmvaa 4,112,64\ns_add_u32 s69, s87, 193512\nQe 13\nQmvva 160,52,144\nbuffer_load_dwordx4 v62, s[52:55], s69 offen offset:3096 lds\nQmvva 160,44,148\n"
"Qe 14\nQmvva 164,52,152\nQe 15\nQmvva 164,44,156\nQk 48,8,9\nQk 49,10,11\nQmvva 168,52,160\ns_add_u32 m0, s62, 16512\n"
"s_add_u32 s69, s87, 128\nQk 50,12,13\nQmvva 168,44,164\nbuffer_load_dwordx4 v62, s[52:55], s69 offen lds\nQmvva 172,52,168\nQk 51,14,15\nQmvva 172,44,172\nQe 0\n"
"Qmvva 204,20,168\nQe 1\nQmvva 204,48,172\ns_add_u32 s69, s87, 64632\nQe 2\nQmvva 200,20,160\nbuffer_load_dwordx4 v62, s[52:55], s69 offen offset:1032 lds\nQmvva 200,48,164\n"
"Qe 3\nQmvva 196,20,152\nQk 18,0,1\nQk 19,2,3\nQmvva 196,48,156\nQe 4\nQmvva 192,20,144\ns_add_u32 s69, s87, 129136\n"
"Qe 5\nQmvva 192,48,148\nbuffer_load_dwordx4 v62, s[52:55], s69 offen offset:2064 lds\nv_smfmac_f32_16x16x64_bf16 a[180:183], a[204:207], v[44:51], v77\nQk 16,4,5\nQcvaa 8,112,80,56\nQe 6\nQmvaa 8,116,84\n"
"Qe 7\nQmvaa 8,120,88\ns_add_u32 s69, s87, 193640\nQk 17,6,7\nQmvaa 8,124,92\nbuffer_load_dwordx4 v62, s[52:55], s69 offen offset:3096 lds\nQcvaa 12,140,92,56\nQmvaa 12,136,88\n"
"Qmvaa 12,132,84\nQmvaa 12,128,80\nQcvaa 0,128,96,56\nQda 128,68,34048\nQmvaa 0,132,100\nQda 132,68,34560\nQmvaa 0,136,104\nQda 136,68,34304\n"
"Qmvaa 0,140,108\nQda 140,68,34816\nQcvaa 4,124,108,56\nQda 124,68,33792\nQmvaa 4,120,104\nQda 120,68,33280\nQmvaa 4,116,100\nQda 116,68,33536\n"
"Qmvaa 4,112,96\nQda 112,68,33024\nQmvva 224,24,144\nQe 8\nQmvva 224,16,148\ns_add_u32 m0, s63, 33024\ns_add_u32 s88, s86, 0\nQe 9\n"
"Qmvva 228,24,152\nbuffer_load_dwordx4 v64, s[48:51], s88 offen lds\nQe 10\nQmvva 228,16,156\nQe 11\nQmvva 232,24,160\nQk 20,8,9\nQk 21,10,11\n"
"Qmvva 232,16,164\ns_add_u32 s88, s86, 19456\nQe 12\nQmvva 236,24,168\nbuffer_load_dwordx4 v64, s[48:51], s88 offen offset:1024 lds\nQe 13\nQmvva 236,16,172\ns_waitcnt lgkmcnt(0)\n"
"Qk 22,12,13\nQcvaa 8,112,0,56\nQe 14\nQmvaa 8,116,4\nv_subrev_u32 v68, 66048, v68\ns_add_u32 s88, s86, 38912\nQmvaa 8,120,8\nbuffer_load_dwordx4 v64, s[48:51], s88 offen offset:2048 lds\n"
"Qe 15\nQmvaa 8,124,12\nQk 23,14,15\nQcvaa 12,140,12,56\nQe 0\nQmvaa 12,136,8\ns_add_u32 s88, s86, 58368\nQe 1\n"
"Qmvaa 12,132,4\nbuffer_load_dwordx4 v64, s[48:51], s88 offen offset:3072 lds\nQe 2\nQmvaa 12,128,0\nQe 3\nv_smfmac_f32_16x16x64_bf16 a[184:187], a[204:207], v[16:23], v77\nQk 26,0,1\nQmava 208,28,144\n"
"s_add_u32 m0, s63, 37120\ns_add_u32 s88, s86, 81920\nQe 4\nQmava 208,20,148\nbuffer_load_dwordx4 v64, s[48:51], s88 offen lds\nQe 5\nQmava 212,28,152\nQe 6\n"
"Qmava 212,20,156\nQe 7\nQmava 216,28,160\ns_add_u32 s88, s86, 101376\nQk 27,2,3\nQk 24,4,5\nQmava 216,20,164\nbuffer_load_dwordx4 v64, s[48:51], s88 offen offset:1024 lds\n"
"Qk 25,6,7\nQmava 220,28,168\nQe 8\nQmava 220,20,172\nQe 9\nQmava 252,32,168\nQta 168,71,768\nQta 170,71,1792\n"
"s_add_u32 s88, s86, 120832\nQmava 252,24,172\nbuffer_load_dwordx4 v64, s[48:51], s88 offen offset:2048 lds\nQe 10\nQmava 248,32,160\nQta 172,71,33792\nQta 174,71,34816\nQmava 248,24,164\n"
"Qe 11\nQmava 244,32,152\nQta 160,71,512\nQta 162,71,1536\nQmava 244,24,156\nQk 28,8,9\nQk 29,10,11\nQmava 240,32,144\n"
"Qta 164,71,33536\nQta 166,71,34560\nQmava 240,24,148\nQe 12\nQcvaa 0,112,16,56\nQta 152,71,256\nQta 154,71,1280\nQmvaa 0,116,20\n"
"Qe 13\nQmvaa 0,120,24\nQta 156,71,33280\nQta 158,71,34304\nQmvaa 0,124,28\nQe 14\nQcvaa 4,140,28,56\nQta 144,71,0\n"
"Qta 146,71,1024\nQmvaa 4,136,24\nQe 15\nQmvaa 4,132,20\nQta 148,71,33024\nQta 150,71,34048\nQmvaa 4,128,16\nQk 30,12,13\n"
"Qk 31,14,15\nQcvaa 8,128,32,56\nv_add_u32 v75, 1, v75\nQmvaa 8,132,36\nds_write_b32 v73, v75\nQmvaa 8,136,40\nQe 0\nQmvaa 8,140,44\n"
"Qe 1\nQcvaa 12,124,44,56\nQe 2\nQmvaa 12,120,40\nQe 3\nQmvaa 12,116,36\nQe 4\nQmvaa 12,112,32\n"
"s_waitcnt lgkmcnt(1)\nQk 32,0,1\nv_smfmac_f32_16x16x64_bf16 a[188:191], a[204:207], v[24:31], v77\nQk 33,2,3\nQmvva 80,36,144\nQe 5\nQmvva 80,28,148\nQe 6\n"
"Qmvva 84,36,152\nQe 7\nQmvva 84,28,156\nQk 34,4,5\nQk 35,6,7\nQmvva 88,36,160\nQe 8\nQmvva 88,28,164\n"
"Qe 9\nQmvva 92,36,168\nQe 10\nQmvva 92,28,172\nQe 11\nQmvva 124,40,168\nQk 38,8,9\nQk 39,10,11\n"
"Qmvva 124,32,172\nds_read_b32 v66, v74\nQmvva 120,40,160\nQe 12\nQmvva 120,32,164\nQe 13\nQmvva 116,40,152\nQe 14\n"
"Qmvva 116,32,156\nQe 15\nQmvva 112,40,144\ns_waitcnt lgkmcnt(0)\nv_cmp_gt_u32 vcc, v75, v66\nQmvva 112,32,148\ns_cbranch_vccnz .Lsa31\n.Lra31:\n"
"Qk 36,12,13\nQcvaa 0,112,48,56\ns_add_u32 m0, s62, 33024\ns_add_u32 s69, s87, 524288\nQk 37,14,15\nQmvaa 0,116,52\nbuffer_load_dwordx4 v62, s[52:55], s69 offen lds\nQmvaa 0,120,56\n"
"Qmvaa 0,124,60\nQcvaa 4,140,60,56\nQmvaa 4,136,56\ns_add_u32 s69, s87, 588792\nQmvaa 4,132,52\nbuffer_load_dwordx4 v62, s[52:55], s69 offen offset:1032 lds\nQmvaa 4,128,48\nQcvaa 8,128,64,56\n"
"Qmvaa 8,132,68\nQe 0\nQmvaa 8,136,72\ns_add_u32 s69, s87, 653296\nQe 1\nQmvaa 8,140,76\nbuffer_load_dwordx4 v62, s[52:55], s69 offen offset:2064 lds\nQcvaa 12,124,76,56\n"
"Qe 2\nQmvaa 12,120,72\nQe 3\nQmvaa 12,116,68\nQe 4\nQmvaa 12,112,64\nQe 5\nv_smfmac_f32_16x16x64_bf16 a[192:195], a[204:207], v[32:39], v77\n"
"s_add_u32 s69, s87, 717800\nQk 40,0,1\nQmvva 144,44,144\nbuffer_load_dwordx4 v62, s[52:55], s69 offen offset:3096 lds\nQmvva 144,36,148\nQe 6\nQmvva 148,44,152\nQe 7\n"
"Qmvva 148,36,156\ns_add_u32 m0, s62, 49536\ns_add_u32 s69, s87, 524416\nQk 41,2,3\nQmvva 152,44,160\nbuffer_load_dwordx4 v62, s[52:55], s69 offen lds\nQmvva 152,36,164\nQk 42,4,5\n"
"Qk 43,6,7\nQmvva 156,44,168\nQe 8\nQmvva 156,36,172\nQe 9\nQmvva 188,48,168\ns_add_u32 s69, s87, 588920\nQe 10\n"
"Qmvva 188,40,172\nbuffer_load_dwordx4 v62, s[52:55], s69 offen offset:1032 lds\nQmvva 184,48,160\nQe 11\nQmvva 184,40,164\nQk 46,8,9\nQk 47,10,11\nQmvva 180,48,152\n"
"Qe 12\nQmvva 180,40,156\ns_add_u32 s69, s87, 653424\nQe 13\nQmvva 176,48,144\nbuffer_load_dwordx4 v62, s[52:55], s69 offen offset:2064 lds\nQmvva 176,40,148\nQe 14\n"
"Qcvaa 0,112,80,56\nQe 15\nQmvaa 0,116,84\nQk 44,12,13\nQk 45,14,15\nQmvaa 0,120,88\ns_add_u32 s69, s87, 717928\nQmvaa 0,124,92\n"
"buffer_load_dwordx4 v62, s[52:55], s69 offen offset:3096 lds\nQcvaa 4,140,92,56\nQmvaa 4,136,88\nQmvaa 4,132,84\nQmvaa 4,128,80\nQcvaa 8,128,96,56\nQmvaa 8,132,100\nQmvaa 8,136,104\n"
"Qmvaa 8,140,108\nQcvaa 12,124,108,56\nQmvaa 12,120,104\nQmvaa 12,116,100\nQmvaa 12,112,96\nv_smfmac_f32_16x16x64_bf16 a[196:199], a[204:207], v[40:47], v77\nQmvva 208,16,144\nQta 112,71,33152\n"
"Qe 0\nQmvva 208,44,148\nQta 114,71,34176\nQe 1\nQmvva 212,16,152\nQta 116,71,33408\nQe 2\nQmvva 212,44,156\n"
"Qta 118,71,34432\nQe 3\nQmvva 216,16,160\nQta 120,71,33664\nQk 48,0,1\nQk 49,2,3\nQmvva 216,44,164\nQta 122,71,34688\n"
"Qe 4\nQmvva 220,16,168\nQta 124,71,33920\nQe 5\nQmvva 220,44,172\nQta 126,71,34944\nQk 50,4,5\nQe 6\n"
"Qe 7\ns_nop 0\nQk 51,6,7\nQe 8\nQe 9\nQe 10\nQe 11\nQe 12\n"
"Qmvva 240,20,144\nv_add_u32 v75, 1, v75\nQe 13\nQmvva 240,48,148\nds_write_b32 v73, v75\nQe 14\nQmvva 244,20,152\nQe 15\n"
"Qmvva 244,48,156\nQk 54,8,9\nQk 55,10,11\nQmvva 248,20,160\nQk 52,12,13\nQk 53,14,15\nQmvva 248,48,164\nQmvva 252,20,168\n"
"Qmvva 252,48,172\nv_smfmac_f32_16x16x64_bf16 a[200:203], a[204:207], v[48:55], v77\nQmava 224,24,144\nds_read_b32 v66, v74\nv_accvgpr_read_b32 v0, a176\nQmava 224,52,148\nv_accvgpr_read_b32 v1, a177\nv_accvgpr_read_b32 v6, a178\n"
"Qmava 228,24,152\nv_accvgpr_read_b32 v7, a179\nv_accvgpr_read_b32 v8, a180\nQmava 228,52,156\nv_accvgpr_read_b32 v9, a181\nv_accvgpr_read_b32 v14, a182\nQmava 232,24,160\nv_accvgpr_read_b32 v15, a183\n"
"v_accvgpr_read_b32 v16, a184\nQmava 232,52,164\ns_waitcnt lgkmcnt(0)\nv_cmp_gt_u32 vcc, v75, v66\nQmava 236,24,168\ns_cbranch_vccnz .Lsa32\n.Lra32:\nv_accvgpr_read_b32 v17, a185\n"
"Qmava 236,52,172\ns_waitcnt lgkmcnt(0)\nv_accvgpr_read_b32 v22, a186\nQmvva 96,28,112\ns_add_u32 m0, s63, 0\ns_add_u32 s88, s86, 128\nv_accvgpr_read_b32 v23, a187\nQmvva 100,28,116\n"
"buffer_load_dwordx4 v64, s[48:51], s88 offen lds\nQmvva 104,28,120\nv_accvgpr_read_b32 v24, a188\nv_accvgpr_read_b32 v25, a189\nQmvva 108,28,124\nv_accvgpr_read_b32 v2, a190\nv_accvgpr_read_b32 v3, a191\nQmvva 128,32,112\n"
"s_add_u32 s88, s86, 19584\nv_accvgpr_read_b32 v4, a192\nQmvva 132,32,116\nbuffer_load_dwordx4 v64, s[48:51], s88 offen offset:1024 lds\nQmvva 136,32,120\nv_accvgpr_read_b32 v5, a193\nv_accvgpr_read_b32 v10, a194\nQmvva 140,32,124\n"
"v_accvgpr_read_b32 v11, a195\nv_accvgpr_read_b32 v12, a196\nQmvva 160,36,112\ns_add_u32 s88, s86, 39040\nv_accvgpr_read_b32 v13, a197\nQmvva 164,36,116\nbuffer_load_dwordx4 v64, s[48:51], s88 offen offset:2048 lds\nQmvva 168,36,120\n"
"v_accvgpr_read_b32 v18, a198\nv_accvgpr_read_b32 v19, a199\nQmvva 172,36,124\nv_accvgpr_read_b32 v20, a200\nv_accvgpr_read_b32 v21, a201\nQmvva 192,40,112\ns_add_u32 s88, s86, 58496\nv_accvgpr_read_b32 v26, a202\n"
"Qmvva 196,40,116\nbuffer_load_dwordx4 v64, s[48:51], s88 offen offset:3072 lds\nQmvva 200,40,120\nv_accvgpr_read_b32 v27, a203\nQa 8,8,9\nQmvva 204,40,124\nQa 10,10,11\nQa 8,8,10\n"
"Qmvva 224,44,112\ns_add_u32 m0, s63, 4096\ns_add_u32 s88, s86, 82048\n.Lpg8:\ns_cbranch_execz .Ltr10\nQa 16,16,17\nQmvva 228,44,116\nbuffer_load_dwordx4 v64, s[48:51], s88 offen lds\n"
"Qmvva 232,44,120\nQa 18,18,19\nQa 16,16,18\nQmvva 236,44,124\nQa 4,4,5\nQa 6,6,7\nQmava 208,48,112\ns_add_u32 s88, s86, 101504\n"
"Qa 4,4,6\nQmava 212,48,116\nbuffer_load_dwordx4 v64, s[48:51], s88 offen offset:1024 lds\nQmava 216,48,120\nQa 12,12,13\nQa 14,14,15\nQmava 220,48,124\nQa 12,12,14\n"
"Qa 0,0,1\nQmava 240,52,112\ns_add_u32 s88, s86, 120960\nQa 2,2,3\nQmava 244,52,116\nbuffer_load_dwordx4 v64, s[48:51], s88 offen offset:2048 lds\nQmava 248,52,120\nQa 0,0,2\n"
"Qa 24,24,25\nQmava 252,52,124\nQa 26,26,27\nQa 24,24,26\nQa 20,20,21\nQa 22,22,23\nQa 20,20,22\nv_max3_u32 v9, v8, v16, v4\n"
"v_max3_u32 v9, v9, v12, v0\nv_max3_u32 v9, v9, v24, v20\nv_min3_u32 v11, v8, v16, v4\nv_min3_u32 v11, v11, v12, v0\nv_min3_u32 v11, v11, v24, v20\nv_cmp_lt_u32 vcc, 0x44800000, v9\ns_mov_b64 s[74:75], vcc\nv_cmp_gt_u32 vcc, 0x2b800000, v11\n"
"s_or_b64 s[74:75], s[74:75], vcc\ns_cmp_lg_u64 s[74:75], 0\ns_cbranch_scc1 .Lfb\n.Lfbret0:\nv_rcp_f32 v2, v0\nv_rcp_f32 v6, v4\nv_rcp_f32 v10, v8\nv_rcp_f32 v14, v12\n"
"v_rcp_f32 v18, v16\nv_rcp_f32 v22, v20\nv_rcp_f32 v26, v24\nv_and_b32 v66, 16, v60\nv_mov_b32 v67, 0xffff\nv_lshlrev_b32 v66, v66, v67\ns_mov_b32 vcc_lo, 0x00c0000c\ns_mov_b32 vcc_hi, 0xc0000c00\n"
"v_cndmask_b32 v67, 0, v66, vcc\ns_lshr_b64 vcc, vcc, 2\nv_cndmask_b32 v66, 0, v66, vcc\nv_mov_b64 v[0:1], 0\nQk 2,2,2\nv_and_b32 v3, v2, v67\nv_and_b32 v2, v2, v66\nv_mov_b64 v[4:5], 0\n"
"Qk 6,6,6\nv_and_b32 v7, v6, v67\nv_and_b32 v6, v6, v66\nv_mov_b64 v[8:9], 0\nQk 10,10,10\nv_and_b32 v11, v10, v67\nv_and_b32 v10, v10, v66\nv_mov_b64 v[12:13], 0\n"
"Qk 14,14,14\nv_and_b32 v15, v14, v67\nv_and_b32 v14, v14, v66\nv_mov_b64 v[16:17], 0\nQk 18,18,18\nv_and_b32 v19, v18, v67\nv_and_b32 v18, v18, v66\nv_mov_b64 v[20:21], 0\n"
"Qk 22,22,22\nv_and_b32 v23, v22, v67\nv_and_b32 v22, v22, v66\nv_mov_b64 v[24:25], 0\nQk 26,26,26\nv_and_b32 v27, v26, v67\nv_and_b32 v26, v26, v66\nv_mov_b64 v[28:29], 0\n"
"Qk 80,80,81\nQk 81,82,83\nQk 82,84,85\nQk 83,86,87\ns_nop 1\nQzvvv 84,80,0\nQzvvv 80,80,2\nQk 88,88,89\n"
"Qk 89,90,91\nQk 90,92,93\nQk 91,94,95\ns_nop 1\nQzvvv 92,88,0\nQzvvv 88,88,2\nQk 96,96,97\nQk 97,98,99\n"
"Qk 98,100,101\nQk 99,102,103\nQk 80,80,81\nQk 81,82,83\nQzvvv 100,96,0\nQk 82,84,85\nQk 83,86,87\nQzvvv 96,96,2\n"
"v_accvgpr_write_b32 a0, v80\nv_accvgpr_write_b32 a1, v81\nv_accvgpr_write_b32 a2, v82\nv_accvgpr_write_b32 a3, v83\nQk 104,104,105\nQk 105,106,107\nQk 106,108,109\nQk 107,110,111\n"
"Qk 88,88,89\nQk 89,90,91\nQzvvv 108,104,0\nQk 90,92,93\nQk 91,94,95\nQzvvv 104,104,2\nv_accvgpr_write_b32 a4, v88\nv_accvgpr_write_b32 a5, v89\n"
"v_accvgpr_write_b32 a6, v90\nv_accvgpr_write_b32 a7, v91\nQk 112,112,113\nQk 113,114,115\nQk 114,116,117\nQk 115,118,119\nQk 96,96,97\nQk 97,98,99\n"
"Qzvvv 116,112,4\nQk 98,100,101\nQk 99,102,103\nQzvvv 112,112,6\nv_accvgpr_write_b32 a8, v96\nv_accvgpr_write_b32 a9, v97\nv_accvgpr_write_b32 a10, v98\nv_accvgpr_write_b32 a11, v99\n"
"Qk 120,120,121\nQk 121,122,123\nQk 122,124,125\nQk 123,126,127\nQk 104,104,105\nQk 105,106,107\nQzvvv 124,120,4\nQk 106,108,109\n"
"Qk 107,110,111\nQzvvv 120,120,6\nv_accvgpr_write_b32 a12, v104\nv_accvgpr_write_b32 a13, v105\nv_accvgpr_write_b32 a14, v106\nv_accvgpr_write_b32 a15, v107\nQk 128,128,129\nQk 129,130,131\n"
"Qk 130,132,133\nQk 131,134,135\nQk 112,112,113\nQk 113,114,115\nQzvvv 132,128,4\nQk 114,116,117\nQk 115,118,119\nQzvvv 128,128,6\n"
"v_accvgpr_write_b32 a16, v112\nv_accvgpr_write_b32 a17, v113\nv_accvgpr_write_b32 a18, v114\nv_accvgpr_write_b32 a19, v115\nQk 136,136,137\nQk 137,138,139\nQk 138,140,141\nQk 139,142,143\n"
"Qk 120,120,121\nQk 121,122,123\nQzvvv 140,136,4\nQk 122,124,125\nQk 123,126,127\nQzvvv 136,136,6\nv_accvgpr_write_b32 a20, v120\nv_accvgpr_write_b32 a21, v121\n"
"v_accvgpr_write_b32 a22, v122\nv_accvgpr_write_b32 a23, v123\nQk 144,144,145\nQk 145,146,147\nQk 146,148,149\nQk 147,150,151\nQk 128,128,129\nQk 129,130,131\n"
"Qzvvv 148,144,8\nQk 130,132,133\nQk 131,134,135\nQzvvv 144,144,10\nv_accvgpr_write_b32 a24, v128\nv_accvgpr_write_b32 a25, v129\nv_accvgpr_write_b32 a26, v130\nv_accvgpr_write_b32 a27, v131\n"
"Qk 152,152,153\nQk 153,154,155\nQk 154,156,157\nQk 155,158,159\nQk 136,136,137\nQk 137,138,139\nQzvvv 156,152,8\nQk 138,140,141\n"
"Qk 139,142,143\nQzvvv 152,152,10\nv_accvgpr_write_b32 a28, v136\nv_accvgpr_write_b32 a29, v137\nv_accvgpr_write_b32 a30, v138\nv_accvgpr_write_b32 a31, v139\nQk 160,160,161\nQk 161,162,163\n"
"Qk 162,164,165\nQk 163,166,167\nQk 144,144,145\nQk 145,146,147\nQzvvv 164,160,8\nQk 146,148,149\nQk 147,150,151\nQzvvv 160,160,10\n"
"v_accvgpr_write_b32 a32, v144\nv_accvgpr_write_b32 a33, v145\nv_accvgpr_write_b32 a34, v146\nv_accvgpr_write_b32 a35, v147\nQk 168,168,169\nQk 169,170,171\nQk 170,172,173\nQk 171,174,175\n"
"Qk 152,152,153\nQk 153,154,155\nQzvvv 172,168,8\nQk 154,156,157\nQk 155,158,159\nQzvvv 168,168,10\nv_accvgpr_write_b32 a36, v152\nv_accvgpr_write_b32 a37, v153\n"
"v_accvgpr_write_b32 a38, v154\nv_accvgpr_write_b32 a39, v155\nQk 176,176,177\nQk 177,178,179\nQk 178,180,181\nQk 179,182,183\nQk 160,160,161\nQk 161,162,163\n"
"Qzvvv 180,176,12\nQk 162,164,165\nQk 163,166,167\nQzvvv 176,176,14\nv_accvgpr_write_b32 a40, v160\nv_accvgpr_write_b32 a41, v161\nv_accvgpr_write_b32 a42, v162\nv_accvgpr_write_b32 a43, v163\n"
"Qk 184,184,185\nQk 185,186,187\nQk 186,188,189\nQk 187,190,191\nQk 168,168,169\nQk 169,170,171\nQzvvv 188,184,12\nQk 170,172,173\n"
"Qk 171,174,175\nQzvvv 184,184,14\nv_accvgpr_write_b32 a44, v168\nv_accvgpr_write_b32 a45, v169\nv_accvgpr_write_b32 a46, v170\nv_accvgpr_write_b32 a47, v171\nQk 192,192,193\nQk 193,194,195\n"
"Qk 194,196,197\nQk 195,198,199\nQk 176,176,177\nQk 177,178,179\nQzvvv 196,192,12\nQk 178,180,181\nQk 179,182,183\nQzvvv 192,192,14\n"
"v_accvgpr_write_b32 a48, v176\nv_accvgpr_write_b32 a49, v177\nv_accvgpr_write_b32 a50, v178\nv_accvgpr_write_b32 a51, v179\nQk 200,200,201\nQk 201,202,203\nQk 202,204,205\nQk 203,206,207\n"
"Qk 184,184,185\nQk 185,186,187\nQzvvv 204,200,12\nQk 186,188,189\nQk 187,190,191\nQzvvv 200,200,14\nv_accvgpr_write_b32 a52, v184\nv_accvgpr_write_b32 a53, v185\n"
"v_accvgpr_write_b32 a54, v186\nv_accvgpr_write_b32 a55, v187\nQk 208,208,209\nQk 209,210,211\nQk 210,212,213\nQk 211,214,215\nQk 192,192,193\nQk 193,194,195\n"
"Qzvvv 212,208,16\nQk 194,196,197\nQk 195,198,199\nQzvvv 208,208,18\nv_accvgpr_write_b32 a56, v192\nv_accvgpr_write_b32 a57, v193\nv_accvgpr_write_b32 a58, v194\nv_accvgpr_write_b32 a59, v195\n"
"Qk 216,216,217\nQk 217,218,219\nQk 218,220,221\nQk 219,222,223\nQk 200,200,201\nQk 201,202,203\nQzvvv 220,216,16\nQk 202,204,205\n"
"Qk 203,206,207\nQzvvv 216,216,18\nv_accvgpr_write_b32 a60, v200\nv_accvgpr_write_b32 a61, v201\nv_accvgpr_write_b32 a62, v202\nv_accvgpr_write_b32 a63, v203\nQk 224,224,225\nQk 225,226,227\n"
"Qk 226,228,229\nQk 227,230,231\nQk 208,208,209\nQk 209,210,211\nQzvvv 228,224,16\nQk 210,212,213\nQk 211,214,215\nQzvvv 224,224,18\n"
"v_accvgpr_write_b32 a64, v208\nv_accvgpr_write_b32 a65, v209\nv_accvgpr_write_b32 a66, v210\nv_accvgpr_write_b32 a67, v211\nQk 232,232,233\nQk 233,234,235\nQk 234,236,237\nQk 235,238,239\n"
"Qk 216,216,217\nQk 217,218,219\nQzvvv 236,232,16\nQk 218,220,221\nQk 219,222,223\nQzvvv 232,232,18\nv_accvgpr_write_b32 a68, v216\nv_accvgpr_write_b32 a69, v217\n"
"v_accvgpr_write_b32 a70, v218\nv_accvgpr_write_b32 a71, v219\nQk 240,240,241\nQk 241,242,243\nQk 242,244,245\nQk 243,246,247\nQk 224,224,225\nQk 225,226,227\n"
"Qzvvv 244,240,20\nQk 226,228,229\nQk 227,230,231\nQzvvv 240,240,22\nv_accvgpr_write_b32 a72, v224\nv_accvgpr_write_b32 a73, v225\nv_accvgpr_write_b32 a74, v226\nv_accvgpr_write_b32 a75, v227\n"
"Qk 248,248,249\nQk 249,250,251\nQk 250,252,253\nQk 251,254,255\nQk 232,232,233\nQk 233,234,235\nQzvvv 252,248,20\nQk 234,236,237\n"
"Qk 235,238,239\nQzvvv 248,248,22\nv_accvgpr_write_b32 a76, v232\nv_accvgpr_write_b32 a77, v233\nv_accvgpr_write_b32 a78, v234\nv_accvgpr_write_b32 a79, v235\nv_accvgpr_read_b32 v48, a208\nv_accvgpr_read_b32 v49, a209\n"
"v_accvgpr_read_b32 v50, a210\nv_accvgpr_read_b32 v51, a211\nv_accvgpr_read_b32 v36, a212\nv_accvgpr_read_b32 v37, a213\nv_accvgpr_read_b32 v38, a214\nv_accvgpr_read_b32 v39, a215\nQk 48,48,49\nQk 49,50,51\n"
"Qk 50,36,37\nQk 51,38,39\nQk 240,240,241\nQk 241,242,243\nQzvvv 36,48,20\nQk 242,244,245\nQk 243,246,247\nQzvvv 48,48,22\n"
"v_accvgpr_write_b32 a80, v240\nv_accvgpr_write_b32 a81, v241\nv_accvgpr_write_b32 a82, v242\nv_accvgpr_write_b32 a83, v243\nv_accvgpr_read_b32 v52, a216\nv_accvgpr_read_b32 v53, a217\nv_accvgpr_read_b32 v54, a218\nv_accvgpr_read_b32 v55, a219\n"
"v_accvgpr_read_b32 v40, a220\nv_accvgpr_read_b32 v41, a221\nv_accvgpr_read_b32 v42, a222\nv_accvgpr_read_b32 v43, a223\nQk 52,52,53\nQk 53,54,55\nQk 54,40,41\nQk 55,42,43\n"
"Qk 248,248,249\nQk 249,250,251\nQzvvv 40,52,20\nQk 250,252,253\nQk 251,254,255\nQzvvv 52,52,22\nv_accvgpr_write_b32 a84, v248\nv_accvgpr_write_b32 a85, v249\n"
"v_accvgpr_write_b32 a86, v250\nv_accvgpr_write_b32 a87, v251\nv_accvgpr_read_b32 v44, a224\nv_accvgpr_read_b32 v45, a225\nv_accvgpr_read_b32 v46, a226\nv_accvgpr_read_b32 v47, a227\nv_accvgpr_read_b32 v32, a228\nv_accvgpr_read_b32 v33, a229\n"
"v_accvgpr_read_b32 v34, a230\nv_accvgpr_read_b32 v35, a231\nQk 44,44,45\nQk 45,46,47\nQk 46,32,33\nQk 47,34,35\nQk 48,48,49\nQk 49,50,51\n"
"Qzvvv 32,44,24\nQk 50,36,37\nQk 51,38,39\nQzvvv 44,44,26\nv_accvgpr_write_b32 a88, v48\nv_accvgpr_write_b32 a89, v49\nv_accvgpr_write_b32 a90, v50\nv_accvgpr_write_b32 a91, v51\n"
"v_accvgpr_read_b32 v48, a232\nv_accvgpr_read_b32 v49, a233\nv_accvgpr_read_b32 v50, a234\nv_accvgpr_read_b32 v51, a235\nv_accvgpr_read_b32 v36, a236\nv_accvgpr_read_b32 v37, a237\nv_accvgpr_read_b32 v38, a238\nv_accvgpr_read_b32 v39, a239\n"
"Qk 48,48,49\nQk 49,50,51\nQk 50,36,37\nQk 51,38,39\nQk 52,52,53\nQk 53,54,55\nQzvvv 36,48,24\nQk 54,40,41\n"
"Qk 55,42,43\nQzvvv 48,48,26\nv_accvgpr_write_b32 a92, v52\nv_accvgpr_write_b32 a93, v53\nv_accvgpr_write_b32 a94, v54\nv_accvgpr_write_b32 a95, v55\nv_accvgpr_read_b32 v52, a240\nv_accvgpr_read_b32 v53, a241\n"
"v_accvgpr_read_b32 v54, a242\nv_accvgpr_read_b32 v55, a243\nv_accvgpr_read_b32 v40, a244\nv_accvgpr_read_b32 v41, a245\nv_accvgpr_read_b32 v42, a246\nv_accvgpr_read_b32 v43, a247\nQk 52,52,53\nQk 53,54,55\n"
"Qk 54,40,41\nQk 55,42,43\nQk 44,44,45\nQk 45,46,47\nQzvvv 40,52,24\nQk 46,32,33\nQk 47,34,35\nQzvvv 52,52,26\n"
"v_accvgpr_write_b32 a96, v44\nv_accvgpr_write_b32 a97, v45\nv_accvgpr_write_b32 a98, v46\nv_accvgpr_write_b32 a99, v47\nv_accvgpr_read_b32 v44, a248\nv_accvgpr_read_b32 v45, a249\nv_accvgpr_read_b32 v46, a250\nv_accvgpr_read_b32 v47, a251\n"
"v_accvgpr_read_b32 v32, a252\nv_accvgpr_read_b32 v33, a253\nv_accvgpr_read_b32 v34, a254\nv_accvgpr_read_b32 v35, a255\nQk 44,44,45\nQk 45,46,47\nQk 46,32,33\nQk 47,34,35\n"
"Qk 48,48,49\nQk 49,50,51\nQzvvv 32,44,24\nQk 50,36,37\nQk 51,38,39\nQzvvv 44,44,26\nv_accvgpr_write_b32 a100, v48\nv_accvgpr_write_b32 a101, v49\n"
"v_accvgpr_write_b32 a102, v50\nv_accvgpr_write_b32 a103, v51\nQk 52,52,53\nQk 53,54,55\nQk 54,40,41\nQk 55,42,43\nv_accvgpr_write_b32 a104, v52\nv_accvgpr_write_b32 a105, v53\n"
"v_accvgpr_write_b32 a106, v54\nv_accvgpr_write_b32 a107, v55\nQk 44,44,45\nQk 45,46,47\nQk 46,32,33\nQk 47,34,35\nv_accvgpr_write_b32 a108, v44\nv_accvgpr_write_b32 a109, v45\n"
"v_accvgpr_write_b32 a110, v46\nv_accvgpr_write_b32 a111, v47\ns_mov_b32 s56, %8\ns_and_b32 s57, %9, 0xffff\ns_mov_b32 s94, 0x7ffff000\ns_mul_i32 s68, %20, 40960\ns_lshl_b32 s69, s70, 10\ns_add_u32 s68, s68, s69\n"
"s_add_u32 s68, s68, 4096\ns_add_u32 s94, %15, s68\ns_addc_u32 s95, %16, 0\ns_and_b32 s95, s95, 0xffff\ns_mov_b32 s75, 37888\n.Lpg9:\ns_cbranch_execz .Ltr10\ns_mov_b32 s58, 4194304\n"
"s_mov_b32 s67, 1\ns_mov_b64 exec, 0\ns_cmp_eq_u32 s70, 0\ns_cbranch_scc1 .Lpg12\ns_cmp_eq_u32 s70, 1\ns_cbranch_scc1 .Lpg13\ns_cmp_eq_u32 s70, 2\ns_cbranch_scc1 .Lpg14\n"
"s_branch .Lpg15\n.Ltr6:\ns_mov_b64 exec, -1\ns_branch .Lround\n.Lhook:\n.Lpg10:\ns_cbranch_execz .Ltr10\ns_cmp_eq_u32 s66, 1\n"
"s_cbranch_scc1 .Lh1\ns_cmp_eq_u32 s66, 5\ns_cbranch_scc1 .Lh5\ns_cmp_eq_u32 s66, 6\ns_cbranch_scc1 .Lh6\ns_cmp_eq_u32 s66, 9\ns_cbranch_scc1 .Lh9\ns_lshr_b32 s68, s99, s66\n"
"s_cmp_eq_u32 s68, 1\ns_cbranch_scc1 .Lh12\ns_branch .Lh11\n.Lh1:\nds_read_b32 v78, v74 offset:16\ns_cmp_eq_u32 s70, 0\ns_cselect_b32 s68, 128, 0\nv_mov_b32 v66, 0x7ffff000\n"
"v_cmp_gt_u32 vcc, s68, v60\nv_not_b32 v67, v65\ns_lshl_b32 s68, %19, 2\nv_cndmask_b32 v66, v66, v76, vcc\nv_readlane_b32 s30, v67, %19\ns_waitcnt lgkmcnt(0)\ns_nop 1\nv_add_u32_dpp v78, v78, v78 quad_perm:[1,0,3,2] row_mask:0xf bank_mask:0xf\n"
"s_nop 1\nv_add_u32_dpp v78, v78, v78 quad_perm:[2,3,0,1] row_mask:0xf bank_mask:0xf\nv_ashrrev_i32 v78, 2, v78\nv_med3_i32 v78, v78, -7, 7\nv_add_lshl_u32 v78, v78, 8, 28\nv_xor_b32 v67, v67, v78\nbuffer_store_dword v67, v66, s[24:27], s68 offen offset:1024 sc1\ns_branch .Lhookret\n"
".Lh9:\ns_lshl_b32 s68, %19, 4\ns_cmp_eq_u32 s70, 0\ns_cselect_b32 s68, s68, -1\nv_mov_b32 v67, 0x7ffff000\nv_cmp_eq_u32 vcc, s68, v60\ns_nop 1\nv_cndmask_b32 v67, v67, v76, vcc\n"
"v_add_u32 v66, 1, v65\nbuffer_store_dword v66, v67, s[24:27], 0 offen sc1\ns_branch .Lhookret\n.Lh5:\nv_bfe_u32 v66, v60, 4, 3\ns_lshl_b32 s68, %19, 7\nv_lshlrev_b32 v66, 2, v66\ns_add_u32 s68, s68, 5242880\n"
"buffer_load_dword v78, v66, s[24:27], s68 offen offset:1024 sc1\ns_branch .Lhookret\n.Lh6:\ns_waitcnt vmcnt(32)\ns_mov_b32 s97, 0\n.Lack_l:\nv_xor_b32 v78, s30, v78\nv_and_b32 v66, 0x0fffffff, v78\n"
"v_cmp_ne_u32 vcc, 0, v66\ns_cmp_eq_u64 vcc, 0\ns_cbranch_scc1 .Lack_ok\ns_sleep 1\nv_bfe_u32 v66, v60, 4, 3\ns_lshl_b32 s68, %19, 7\nv_lshlrev_b32 v66, 2, v66\ns_add_u32 s68, s68, 5242880\n"
"buffer_load_dword v78, v66, s[24:27], s68 offen offset:1024 sc1\ns_waitcnt vmcnt(0)\ns_add_u32 s97, s97, 1\ns_cmp_lt_u32 s97, 1048576\ns_cbranch_scc1 .Lack_l\n.Lack_ok:\nv_lshrrev_b32 v78, 28, v78\ns_nop 1\n"
"v_add_u32_dpp v78, v78, v78 quad_perm:[1,0,3,2] row_mask:0xf bank_mask:0xf\ns_nop 1\nv_add_u32_dpp v78, v78, v78 quad_perm:[2,3,0,1] row_mask:0xf bank_mask:0xf\ns_nop 1\nv_readlane_b32 s68, v78, %19\nv_readlane_b32 s69, v78, 4\ns_cmp_lt_u32 s68, 32\ns_cselect_b32 s68, 8192, 0\n"
"s_xor_b32 s68, s68, s98\ns_add_u32 s71, s71, s68\ns_cmp_lt_u32 s69, 32\ns_cselect_b32 s69, 8, 0\ns_xor_b32 s69, s69, %20\ns_and_b32 s69, s69, 8\ns_lshl_b32 s98, s69, 10\ns_branch .Lhookret\n"
".Lh11:\nbuffer_load_dword v78, v76, s[24:27], 0 offen sc1\ns_branch .Lhookret\n.Lh12:\ns_waitcnt vmcnt(32)\nv_sub_u32 v66, v78, v65\nv_cmp_gt_u32 vcc, 1, v66\ns_and_b64 vcc, vcc, s[28:29]\n"
"s_cbranch_scc1 .Lf1_w\n.Lf1_r:\ns_cmp_lg_u32 s70, 3\ns_cbranch_scc1 .Lf1_n\nbuffer_inv sc1\n.Lf1_n:\ns_lshr_b32 s88, %19, 2\ns_lshl_b32 s88, s88, 4\n"
"s_add_u32 s88, s88, s93\ns_andn2_b32 s68, s71, 786432\ns_add_u32 s48, %8, s68\ns_addc_u32 s49, %9, 0\ns_and_b32 s49, s49, 0xffff\ns_sub_u32 s50, 4194304, s68\ns_mul_i32 s68, s88, 163840\ns_and_b32 s69, %19, 3\n"
"s_mul_i32 s69, s69, 320\ns_add_u32 s68, s68, s69\ns_add_u32 s52, %15, s68\ns_addc_u32 s53, %16, 0\ns_and_b32 s53, s53, 0xffff\ns_sub_u32 s54, 5246976, s68\ns_add_u32 s71, s71, 1048576\ns_and_b32 s87, %19, 3\n"
"s_mul_i32 s87, s87, 327680\ns_add_u32 s87, s87, 1310720\ns_mul_i32 s69, s92, 10240\nv_bfe_u32 v66, v60, 5, 3\nv_mul_u32_u24 v66, 1280, v66\nv_and_b32 v67, 16, v60\nv_add3_u32 v67, v67, v66, s69\ns_and_b32 s69, %19, 1\n"
"v_lshrrev_b32 v66, 8, v60\ns_lshl_b32 s68, s93, 1\nv_xor_b32 v66, s68, v66\nv_lshl_add_u32 v66, v66, 5, v67\ns_lshr_b32 s68, 128, s69\nv_add_u32 v61, s68, v66\ns_mul_i32 s68, s69, 192\nv_add_u32 v63, s68, v66\n"
"v_bfe_u32 v66, v60, 8, 1\nv_xor_b32 v66, s93, v66\nv_lshl_add_u32 v67, v66, 5, v67\nv_lshrrev_b32 v66, 9, v60\nv_mul_u32_u24 v66, 20480, v66\ns_xor_b32 s69, s69, 1\ns_lshl_b32 s69, s69, 8\nv_add3_u32 v62, v66, v67, s69\n"
"s_branch .Lhookret\n.Lf1_w:\ns_mov_b32 s97, 0\n.Lf1_l:\ns_sleep 1\nbuffer_load_dword v78, v76, s[24:27], 0 offen sc1\ns_waitcnt vmcnt(0)\ns_add_u32 s97, s97, 1\n"
"s_cmp_gt_u32 s97, 1048576\ns_cbranch_scc1 .Lf1_r\nv_sub_u32 v66, v78, v65\nv_cmp_gt_u32 vcc, 1, v66\ns_and_b64 vcc, vcc, s[28:29]\ns_cbranch_scc1 .Lf1_l\ns_branch .Lf1_r\n.Lsa1:\n"
"ds_read_b32 v66, v74\ns_waitcnt lgkmcnt(0)\nv_cmp_gt_u32 vcc, v75, v66\ns_cbranch_vccnz .Lsa1\ns_branch .Lra1\n.Lsa2:\nds_read_b32 v66, v74\ns_waitcnt lgkmcnt(0)\n"
"v_cmp_gt_u32 vcc, v75, v66\ns_cbranch_vccnz .Lsa2\ns_branch .Lra2\n.Lsp2:\ns_sub_u32 s75, s77, s74\ns_cmp_lt_u32 s75, 1024\ns_cbranch_scc0 .Lrp2\ns_memrealtime s[74:75]\n"
"s_waitcnt lgkmcnt(0)\ns_branch .Lsp2\n.Lsa3:\nds_read_b32 v66, v74\ns_waitcnt lgkmcnt(0)\nv_cmp_gt_u32 vcc, v75, v66\ns_cbranch_vccnz .Lsa3\ns_branch .Lra3\n"
".Lsa4:\nds_read_b32 v66, v74\ns_waitcnt lgkmcnt(0)\nv_cmp_gt_u32 vcc, v75, v66\ns_cbranch_vccnz .Lsa4\ns_branch .Lra4\n.Lsp4:\ns_sub_u32 s75, s77, s74\n"
"s_cmp_lt_u32 s75, 1024\ns_cbranch_scc0 .Lrp4\ns_memrealtime s[74:75]\ns_waitcnt lgkmcnt(0)\ns_branch .Lsp4\n.Lsa5:\nds_read_b32 v66, v74\ns_waitcnt lgkmcnt(0)\n"
"v_cmp_gt_u32 vcc, v75, v66\ns_cbranch_vccnz .Lsa5\ns_branch .Lra5\n.Lsa30:\nds_read_b32 v66, v74\ns_waitcnt lgkmcnt(0)\nv_cmp_gt_u32 vcc, v75, v66\ns_cbranch_vccnz .Lsa30\n"
"s_branch .Lra30\n.Lsa31:\nds_read_b32 v66, v74\ns_waitcnt lgkmcnt(0)\nv_cmp_gt_u32 vcc, v75, v66\ns_cbranch_vccnz .Lsa31\ns_branch .Lra31\n.Lsa32:\n"
"ds_read_b32 v66, v74\ns_waitcnt lgkmcnt(0)\nv_cmp_gt_u32 vcc, v75, v66\ns_cbranch_vccnz .Lsa32\ns_branch .Lra32\n.Lsa30x:\nds_read_b32 v66, v74\ns_waitcnt lgkmcnt(0)\n"
"v_cmp_gt_u32 vcc, v75, v66\ns_cbranch_vccnz .Lsa30x\ns_branch .Lra30x\n.Lsa31x:\nds_read_b32 v66, v74\ns_waitcnt lgkmcnt(0)\nv_cmp_gt_u32 vcc, v75, v66\ns_cbranch_vccnz .Lsa31x\n"
"s_branch .Lra31x\n.Lso40:\nds_read_b32 v66, v74\ns_waitcnt lgkmcnt(0)\nv_cmp_gt_u32 vcc, v75, v66\ns_cbranch_vccnz .Lso40\ns_branch .Lro40\n.Lso180:\n"
"ds_read_b32 v66, v74\ns_waitcnt lgkmcnt(0)\nv_cmp_gt_u32 vcc, v75, v66\ns_cbranch_vccnz .Lso180\ns_branch .Lro180\n.Lso320:\nds_read_b32 v66, v74\ns_waitcnt lgkmcnt(0)\n"
"v_cmp_gt_u32 vcc, v75, v66\ns_cbranch_vccnz .Lso320\ns_branch .Lro320\n.Lso460:\nds_read_b32 v66, v74\ns_waitcnt lgkmcnt(0)\nv_cmp_gt_u32 vcc, v75, v66\ns_cbranch_vccnz .Lso460\n"
"s_branch .Lro460\n.Lso600:\nds_read_b32 v66, v74\ns_waitcnt lgkmcnt(0)\nv_cmp_gt_u32 vcc, v75, v66\ns_cbranch_vccnz .Lso600\ns_branch .Lro600\n.Lso740:\n"
"ds_read_b32 v66, v74\ns_waitcnt lgkmcnt(0)\nv_cmp_gt_u32 vcc, v75, v66\ns_cbranch_vccnz .Lso740\ns_branch .Lro740\n.Lso880:\nds_read_b32 v66, v74\ns_waitcnt lgkmcnt(0)\n"
"v_cmp_gt_u32 vcc, v75, v66\ns_cbranch_vccnz .Lso880\ns_branch .Lro880\n.Lso1020:\nds_read_b32 v66, v74\ns_waitcnt lgkmcnt(0)\nv_cmp_gt_u32 vcc, v75, v66\ns_cbranch_vccnz .Lso1020\n"
"s_branch .Lro1020\n.Lso1160:\nds_read_b32 v66, v74\ns_waitcnt lgkmcnt(0)\nv_cmp_gt_u32 vcc, v75, v66\ns_cbranch_vccnz .Lso1160\ns_branch .Lro1160\n.Lso1300:\n"
"ds_read_b32 v66, v74\ns_waitcnt lgkmcnt(0)\nv_cmp_gt_u32 vcc, v75, v66\ns_cbranch_vccnz .Lso1300\ns_branch .Lro1300\n.Lso1440:\nds_read_b32 v66, v74\ns_waitcnt lgkmcnt(0)\n"
"v_cmp_gt_u32 vcc, v75, v66\ns_cbranch_vccnz .Lso1440\ns_branch .Lro1440\n.Lso1455:\nds_read_b32 v66, v74\ns_waitcnt lgkmcnt(0)\nv_cmp_gt_u32 vcc, v75, v66\ns_cbranch_vccnz .Lso1455\n"
"s_branch .Lro1455\n.Lso1525r0:\nds_read_b32 v66, v74\ns_waitcnt lgkmcnt(0)\nv_cmp_gt_u32 vcc, v75, v66\ns_cbranch_vccnz .Lso1525r0\ns_branch .Lro1525r0\n.Lso1595r0:\n"
"ds_read_b32 v66, v74\ns_waitcnt lgkmcnt(0)\nv_cmp_gt_u32 vcc, v75, v66\ns_cbranch_vccnz .Lso1595r0\ns_branch .Lro1595r0\n.Lso1665r0:\nds_read_b32 v66, v74\ns_waitcnt lgkmcnt(0)\n"
"v_cmp_gt_u32 vcc, v75, v66\ns_cbranch_vccnz .Lso1665r0\ns_branch .Lro1665r0\n.Lso1735r0:\nds_read_b32 v66, v74\ns_waitcnt lgkmcnt(0)\nv_cmp_gt_u32 vcc, v75, v66\ns_cbranch_vccnz .Lso1735r0\n"
"s_branch .Lro1735r0\n.Lso1805r0:\nds_read_b32 v66, v74\ns_waitcnt lgkmcnt(0)\nv_cmp_gt_u32 vcc, v75, v66\ns_cbranch_vccnz .Lso1805r0\ns_branch .Lro1805r0\n.Lso1878r0:\n"
"ds_read_b32 v66, v74\ns_waitcnt lgkmcnt(0)\nv_cmp_gt_u32 vcc, v75, v66\ns_cbranch_vccnz .Lso1878r0\ns_branch .Lro1878r0\n.Lso1920r0:\nds_read_b32 v66, v74\ns_waitcnt lgkmcnt(0)\n"
"v_cmp_gt_u32 vcc, v75, v66\ns_cbranch_vccnz .Lso1920r0\ns_branch .Lro1920r0\n.Lso1960r0:\nds_read_b32 v66, v74\ns_waitcnt lgkmcnt(0)\nv_cmp_gt_u32 vcc, v75, v66\ns_cbranch_vccnz .Lso1960r0\n"
"s_branch .Lro1960r0\n.Lso2046r0:\nds_read_b32 v66, v74\ns_waitcnt lgkmcnt(0)\nv_cmp_gt_u32 vcc, v75, v66\ns_cbranch_vccnz .Lso2046r0\ns_branch .Lro2046r0\n.Lso2088r0:\n"
"ds_read_b32 v66, v74\ns_waitcnt lgkmcnt(0)\nv_cmp_gt_u32 vcc, v75, v66\ns_cbranch_vccnz .Lso2088r0\ns_branch .Lro2088r0\n.Lso2172r0:\nds_read_b32 v66, v74\ns_waitcnt lgkmcnt(0)\n"
"v_cmp_gt_u32 vcc, v75, v66\ns_cbranch_vccnz .Lso2172r0\ns_branch .Lro2172r0\n.Lso2222r0:\nds_read_b32 v66, v74\ns_waitcnt lgkmcnt(0)\nv_cmp_gt_u32 vcc, v75, v66\ns_cbranch_vccnz .Lso2222r0\n"
"s_branch .Lro2222r0\n.Lso1525r1:\nds_read_b32 v66, v74\ns_waitcnt lgkmcnt(0)\nv_cmp_gt_u32 vcc, v75, v66\ns_cbranch_vccnz .Lso1525r1\ns_branch .Lro1525r1\n.Lso1595r1:\n"
"ds_read_b32 v66, v74\ns_waitcnt lgkmcnt(0)\nv_cmp_gt_u32 vcc, v75, v66\ns_cbranch_vccnz .Lso1595r1\ns_branch .Lro1595r1\n.Lso1665r1:\nds_read_b32 v66, v74\ns_waitcnt lgkmcnt(0)\n"
"v_cmp_gt_u32 vcc, v75, v66\ns_cbranch_vccnz .Lso1665r1\ns_branch .Lro1665r1\n.Lso1735r1:\nds_read_b32 v66, v74\ns_waitcnt lgkmcnt(0)\nv_cmp_gt_u32 vcc, v75, v66\ns_cbranch_vccnz .Lso1735r1\n"
"s_branch .Lro1735r1\n.Lso1805r1:\nds_read_b32 v66, v74\ns_waitcnt lgkmcnt(0)\nv_cmp_gt_u32 vcc, v75, v66\ns_cbranch_vccnz .Lso1805r1\ns_branch .Lro1805r1\n.Lso1840r1:\n"
"ds_read_b32 v66, v74\ns_waitcnt lgkmcnt(0)\nv_cmp_gt_u32 vcc, v75, v66\ns_cbranch_vccnz .Lso1840r1\ns_branch .Lro1840r1\n.Lso1878r1:\nds_read_b32 v66, v74\ns_waitcnt lgkmcnt(0)\n"
"v_cmp_gt_u32 vcc, v75, v66\ns_cbranch_vccnz .Lso1878r1\ns_branch .Lro1878r1\n.Lso1920r1:\nds_read_b32 v66, v74\ns_waitcnt lgkmcnt(0)\nv_cmp_gt_u32 vcc, v75, v66\ns_cbranch_vccnz .Lso1920r1\n"
"s_branch .Lro1920r1\n.Lso1945r1:\nds_read_b32 v66, v74\ns_waitcnt lgkmcnt(0)\nv_cmp_gt_u32 vcc, v75, v66\ns_cbranch_vccnz .Lso1945r1\ns_branch .Lro1945r1\n.Lso2004r1:\n"
"ds_read_b32 v66, v74\ns_waitcnt lgkmcnt(0)\nv_cmp_gt_u32 vcc, v75, v66\ns_cbranch_vccnz .Lso2004r1\ns_branch .Lro2004r1\n.Lso2045r1:\nds_read_b32 v66, v74\ns_waitcnt lgkmcnt(0)\n"
"v_cmp_gt_u32 vcc, v75, v66\ns_cbranch_vccnz .Lso2045r1\ns_branch .Lro2045r1\n.Lso2130r1:\nds_read_b32 v66, v74\ns_waitcnt lgkmcnt(0)\nv_cmp_gt_u32 vcc, v75, v66\ns_cbranch_vccnz .Lso2130r1\n"
"s_branch .Lro2130r1\n.Lso2172r1:\nds_read_b32 v66, v74\ns_waitcnt lgkmcnt(0)\nv_cmp_gt_u32 vcc, v75, v66\ns_cbranch_vccnz .Lso2172r1\ns_branch .Lro2172r1\n.Lso2225r1:\n"
"ds_read_b32 v66, v74\ns_waitcnt lgkmcnt(0)\nv_cmp_gt_u32 vcc, v75, v66\ns_cbranch_vccnz .Lso2225r1\ns_branch .Lro2225r1\n.Ltail1:\n.Lpg11:\ns_cbranch_execz .Ltr10\n"
"Qcvaa 0,128,32,56\nv_add_u32 v75, 1, v75\nQmvaa 0,132,36\ns_waitcnt vmcnt(0)\nds_write_b32 v73, v75\nQmvaa 0,136,40\nQe 8\nQmvaa 0,140,44\n"
"Qe 9\nQcvaa 4,124,44,56\nQe 10\nQmvaa 4,120,40\nQe 11\nQmvaa 4,116,36\nQe 12\nQmvaa 4,112,32\n"
"s_waitcnt lgkmcnt(1)\nQk 40,8,9\nQmvva 96,44,144\nQe 13\nQmvva 96,36,148\nQe 14\nQmvva 100,44,152\nQe 15\n"
"Qmvva 100,36,156\nQk 41,10,11\nQk 42,12,13\nQmvva 104,44,160\nQk 43,14,15\nQmvva 104,36,164\nQe 0\nQmvva 108,44,168\n"
"Qe 1\nQmvva 108,36,172\nQe 2\nQmvva 140,48,168\nQe 3\nQmvva 140,40,172\nQk 46,0,1\nQk 47,2,3\n"
"Qmvva 136,48,160\nds_read_b32 v66, v74\nQmvva 136,40,164\nQe 4\nQmvva 132,48,152\nQe 5\nQmvva 132,40,156\nQe 6\n"
"Qmvva 128,48,144\nQe 7\nQmvva 128,40,148\ns_waitcnt lgkmcnt(0)\nv_cmp_gt_u32 vcc, v75, v66\nv_smfmac_f32_16x16x64_bf16 a[176:179], a[204:207], v[36:43], v77\ns_cbranch_vccnz .Lsa30x\n.Lra30x:\n"
"Qcvaa 8,112,48,56\ns_add_u32 m0, s96, 0\ns_add_u32 s95, s87, 4096\nQk 44,4,5\nQmvaa 8,116,52\nbuffer_load_dwordx4 v61, s[52:55], s95 offen lds\nQmvaa 8,120,56\nQk 45,6,7\n"
"Qmvaa 8,124,60\nQcvaa 12,140,60,56\nQmvaa 12,136,56\ns_add_u32 s95, s87, 23552\nQmvaa 12,132,52\nbuffer_load_dwordx4 v61, s[52:55], s95 offen offset:1024 lds\nQmvaa 12,128,48\nQcvaa 0,128,64,56\n"
"Qmvaa 0,132,68\nQe 8\nQmvaa 0,136,72\ns_add_u32 m0, s96, 8192\ns_add_u32 s95, s87, 45056\nQe 9\nQmvaa 0,140,76\nbuffer_load_dwordx4 v61, s[52:55], s95 offen lds\n"
"Qcvaa 4,124,76,56\nQe 10\nQmvaa 4,120,72\nQe 11\nQmvaa 4,116,68\nQe 12\nQmvaa 4,112,64\ns_add_u32 s95, s87, 64512\n"
"Qe 13\nQmvva 160,52,144\nbuffer_load_dwordx4 v61, s[52:55], s95 offen offset:1024 lds\nQmvva 160,44,148\nQe 14\nQmvva 164,52,152\nQe 15\nQmvva 164,44,156\n"
"Qk 48,8,9\nQk 49,10,11\nQmvva 168,52,160\ns_add_u32 m0, s96, 16384\ns_add_u32 s95, s87, 4096\nQk 50,12,13\nQmvva 168,44,164\nbuffer_load_dwordx4 v62, s[52:55], s95 offen lds\n"
"Qmvva 172,52,168\nQk 51,14,15\nQmvva 172,44,172\nQe 0\nQmvva 204,20,168\nQe 1\nQmvva 204,48,172\ns_add_u32 s95, s87, 44032\n"
"Qe 2\nQmvva 200,20,160\nbuffer_load_dwordx4 v62, s[52:55], s95 offen offset:1024 lds\nQmvva 200,48,164\nQe 3\nQmvva 196,20,152\nQk 18,0,1\nQk 19,2,3\n"
"Qmvva 196,48,156\nQe 4\nQmvva 192,20,144\ns_add_u32 m0, s96, 24576\ns_add_u32 s95, s87, 4096\nQe 5\nQmvva 192,48,148\nbuffer_load_dwordx4 v63, s[52:55], s95 offen lds\n"
"v_smfmac_f32_16x16x64_bf16 a[180:183], a[204:207], v[44:51], v77\nQk 16,4,5\nQcvaa 8,112,80,56\nQe 6\nQmvaa 8,116,84\nQe 7\nQmvaa 8,120,88\ns_add_u32 s95, s87, 23552\n"
"Qk 17,6,7\nQmvaa 8,124,92\nbuffer_load_dwordx4 v63, s[52:55], s95 offen offset:1024 lds\nQcvaa 12,140,92,56\nQmvaa 12,136,88\nQmvaa 12,132,84\nQmvaa 12,128,80\ns_add_u32 m0, s96, 32768\n"
"s_add_u32 s95, s87, 45056\nQcvaa 0,128,96,56\nQda 128,68,34048\nbuffer_load_dwordx4 v63, s[52:55], s95 offen lds\nQmvaa 0,132,100\nQda 132,68,34560\nQmvaa 0,136,104\nQda 136,68,34304\n"
"Qmvaa 0,140,108\nQda 140,68,34816\nQcvaa 4,124,108,56\nQda 124,68,33792\ns_add_u32 s95, s87, 64512\nQmvaa 4,120,104\nQda 120,68,33280\nbuffer_load_dwordx4 v63, s[52:55], s95 offen offset:1024 lds\n"
"Qmvaa 4,116,100\nQda 116,68,33536\nQmvaa 4,112,96\nQda 112,68,33024\nQmvva 224,24,144\nQe 8\nQmvva 224,16,148\ns_add_u32 m0, s96, 40960\n"
"s_mov_b32 s95, 4096\nQe 9\nQmvva 228,24,152\nbuffer_load_dwordx4 v61, s[52:55], s95 offen lds\nQe 10\nQmvva 228,16,156\nQe 11\nQmvva 232,24,160\n"
"Qk 20,8,9\nQk 21,10,11\nQmvva 232,16,164\nQe 12\nQmvva 236,24,168\ns_mov_b32 s95, 23552\nQe 13\nQmvva 236,16,172\n"
"buffer_load_dwordx4 v61, s[52:55], s95 offen offset:1024 lds\ns_waitcnt lgkmcnt(0)\nQk 22,12,13\nQcvaa 8,112,0,56\nQe 14\nQmvaa 8,116,4\nv_subrev_u32 v68, 66048, v68\nQmvaa 8,120,8\n"
"Qe 15\nQmvaa 8,124,12\ns_add_u32 m0, s96, 49152\ns_mov_b32 s95, 45056\nQk 23,14,15\nQcvaa 12,140,12,56\nbuffer_load_dwordx4 v61, s[52:55], s95 offen lds\nQe 0\n"
"Qmvaa 12,136,8\nQe 1\nQmvaa 12,132,4\nQe 2\nQmvaa 12,128,0\nQe 3\nv_smfmac_f32_16x16x64_bf16 a[184:187], a[204:207], v[16:23], v77\ns_mov_b32 s95, 64512\n"
"Qk 26,0,1\nQmava 208,28,144\nbuffer_load_dwordx4 v61, s[52:55], s95 offen offset:1024 lds\nQe 4\nQmava 208,20,148\nQe 5\nQmava 212,28,152\nQe 6\n"
"Qmava 212,20,156\nQe 7\nQmava 216,28,160\ns_add_u32 m0, s96, 57344\ns_mov_b32 s95, 4096\nQk 27,2,3\nQk 24,4,5\nQmava 216,20,164\n"
"buffer_load_dwordx4 v62, s[52:55], s95 offen lds\nQk 25,6,7\nQmava 220,28,168\nQe 8\nQmava 220,20,172\nQe 9\nQmava 252,32,168\nQta 168,71,768\n"
"Qta 170,71,1792\nQmava 252,24,172\ns_mov_b32 s95, 44032\nQe 10\nQmava 248,32,160\nQta 172,71,33792\nQta 174,71,34816\nbuffer_load_dwordx4 v62, s[52:55], s95 offen offset:1024 lds\n"
"Qmava 248,24,164\nQe 11\nQmava 244,32,152\nQta 160,71,512\nQta 162,71,1536\nQmava 244,24,156\nQk 28,8,9\nQk 29,10,11\n"
"Qmava 240,32,144\nQta 164,71,33536\nQta 166,71,34560\ns_add_u32 m0, s96, 132096\ns_mov_b32 s95, 4096\nQmava 240,24,148\nbuffer_load_dwordx4 v63, s[52:55], s95 offen lds\nQe 12\n"
"Qcvaa 0,112,16,56\nQta 152,71,256\nQta 154,71,1280\nQmvaa 0,116,20\nQe 13\nQmvaa 0,120,24\nQta 156,71,33280\nQta 158,71,34304\n"
"Qmvaa 0,124,28\ns_mov_b32 s95, 23552\nQe 14\nQcvaa 4,140,28,56\nQta 144,71,0\nQta 146,71,1024\nbuffer_load_dwordx4 v63, s[52:55], s95 offen offset:1024 lds\nQmvaa 4,136,24\n"
"Qe 15\nQmvaa 4,132,20\nQta 148,71,33024\nQta 150,71,34048\nQmvaa 4,128,16\nQk 30,12,13\nQk 31,14,15\nQcvaa 8,128,32,56\n"
"v_add_u32 v75, 1, v75\nQmvaa 8,132,36\nds_write_b32 v73, v75\nQmvaa 8,136,40\nQe 0\nQmvaa 8,140,44\nQe 1\nQcvaa 12,124,44,56\n"
".Lpg12:\ns_cbranch_execz .Ltr6\nQe 2\nQmvaa 12,120,40\nQe 3\nQmvaa 12,116,36\nQe 4\nQmvaa 12,112,32\n"
"s_waitcnt lgkmcnt(1)\nQk 32,0,1\nv_smfmac_f32_16x16x64_bf16 a[188:191], a[204:207], v[24:31], v77\nQk 33,2,3\nQmvva 80,36,144\nQe 5\nQmvva 80,28,148\nQe 6\n"
"Qmvva 84,36,152\nQe 7\nQmvva 84,28,156\nQk 34,4,5\nQk 35,6,7\nQmvva 88,36,160\nQe 8\nQmvva 88,28,164\n"
"Qe 9\nQmvva 92,36,168\nQe 10\nQmvva 92,28,172\nQe 11\nQmvva 124,40,168\nQk 38,8,9\nQk 39,10,11\n"
"Qmvva 124,32,172\nds_read_b32 v66, v74\nQmvva 120,40,160\nQe 12\nQmvva 120,32,164\nQe 13\nQmvva 116,40,152\nQe 14\n"
"Qmvva 116,32,156\nQe 15\nQmvva 112,40,144\ns_waitcnt lgkmcnt(0)\nv_cmp_gt_u32 vcc, v75, v66\nQmvva 112,32,148\ns_cbranch_vccnz .Lsa31x\n.Lra31x:\n"
"Qk 36,12,13\nQcvaa 0,112,48,56\ns_add_u32 m0, s96, 140288\ns_mov_b32 s95, 45056\nQk 37,14,15\nQmvaa 0,116,52\nbuffer_load_dwordx4 v63, s[52:55], s95 offen lds\nQmvaa 0,120,56\n"
"Qmvaa 0,124,60\nQcvaa 4,140,60,56\nQmvaa 4,136,56\ns_mov_b32 s95, 64512\nQmvaa 4,132,52\nbuffer_load_dwordx4 v63, s[52:55], s95 offen offset:1024 lds\nQmvaa 4,128,48\nQcvaa 8,128,64,56\n"
"Qmvaa 8,132,68\nQe 0\nQmvaa 8,136,72\ns_add_u32 m0, s96, 148480\ns_add_u32 s95, s87, 86016\nQe 1\nQmvaa 8,140,76\nbuffer_load_dwordx4 v61, s[52:55], s95 offen lds\n"
"Qcvaa 12,124,76,56\nQe 2\nQmvaa 12,120,72\nQe 3\nQmvaa 12,116,68\nQe 4\nQmvaa 12,112,64\nQe 5\n"
"v_smfmac_f32_16x16x64_bf16 a[192:195], a[204:207], v[32:39], v77\ns_add_u32 s95, s87, 105472\nQk 40,0,1\nQmvva 144,44,144\nbuffer_load_dwordx4 v61, s[52:55], s95 offen offset:1024 lds\nQmvva 144,36,148\nQe 6\nQmvva 148,44,152\n"
"Qe 7\nQmvva 148,36,156\ns_add_u32 m0, s96, 65536\ns_add_u32 s95, s87, 126976\nQk 41,2,3\nQmvva 152,44,160\nbuffer_load_dwordx4 v61, s[52:55], s95 offen lds\nQmvva 152,36,164\n"
"Qk 42,4,5\nQk 43,6,7\nQmvva 156,44,168\nQe 8\nQmvva 156,36,172\nQe 9\nQmvva 188,48,168\ns_add_u32 s95, s87, 146432\n"
"Qe 10\nQmvva 188,40,172\nbuffer_load_dwordx4 v61, s[52:55], s95 offen offset:1024 lds\nQmvva 184,48,160\nQe 11\nQmvva 184,40,164\nQk 46,8,9\nQk 47,10,11\n"
"Qmvva 180,48,152\nQe 12\nQmvva 180,40,156\ns_add_u32 m0, s96, 73728\ns_add_u32 s95, s87, 86016\nQe 13\nQmvva 176,48,144\nbuffer_load_dwordx4 v62, s[52:55], s95 offen lds\n"
"Qmvva 176,40,148\nQe 14\nQcvaa 0,112,80,56\nQe 15\nQmvaa 0,116,84\nQk 44,12,13\nQk 45,14,15\nQmvaa 0,120,88\n"
"s_add_u32 s95, s87, 125952\nQmvaa 0,124,92\nbuffer_load_dwordx4 v62, s[52:55], s95 offen offset:1024 lds\nQcvaa 4,140,92,56\nQmvaa 4,136,88\nQmvaa 4,132,84\nQmvaa 4,128,80\ns_add_u32 m0, s96, 81920\n"
"s_add_u32 s95, s87, 86016\nQcvaa 8,128,96,56\nbuffer_load_dwordx4 v63, s[52:55], s95 offen lds\nQmvaa 8,132,100\nQmvaa 8,136,104\nQmvaa 8,140,108\nQcvaa 12,124,108,56\ns_add_u32 s95, s87, 105472\n"
"Qmvaa 12,120,104\nbuffer_load_dwordx4 v63, s[52:55], s95 offen offset:1024 lds\nQmvaa 12,116,100\nQmvaa 12,112,96\nv_smfmac_f32_16x16x64_bf16 a[196:199], a[204:207], v[40:47], v77\nQmvva 208,16,144\nQta 112,71,33152\ns_add_u32 m0, s96, 90112\n"
"s_add_u32 s95, s87, 126976\nQe 0\nQmvva 208,44,148\nQta 114,71,34176\nbuffer_load_dwordx4 v63, s[52:55], s95 offen lds\nQe 1\nQmvva 212,16,152\nQta 116,71,33408\n"
"Qe 2\nQmvva 212,44,156\nQta 118,71,34432\nQe 3\nQmvva 216,16,160\nQta 120,71,33664\nQk 48,0,1\nQk 49,2,3\n"
"Qmvva 216,44,164\nQta 122,71,34688\ns_add_u32 s95, s87, 146432\nQe 4\nQmvva 220,16,168\nQta 124,71,33920\nbuffer_load_dwordx4 v63, s[52:55], s95 offen offset:1024 lds\nQe 5\n"
"Qmvva 220,44,172\nQta 126,71,34944\nQk 50,4,5\nQe 6\nQe 7\ns_nop 0\nQk 51,6,7\nQe 8\n"
"Qe 9\nQe 10\nQe 11\nQe 12\nQmvva 240,20,144\nQe 13\nQmvva 240,48,148\nQe 14\n"
"Qmvva 244,20,152\nQe 15\nQmvva 244,48,156\nQk 54,8,9\nQk 55,10,11\nQmvva 248,20,160\nQk 52,12,13\nQk 53,14,15\n"
"Qmvva 248,48,164\nQmvva 252,20,168\nQmvva 252,48,172\nv_smfmac_f32_16x16x64_bf16 a[200:203], a[204:207], v[48:55], v77\nQmava 224,24,144\nv_accvgpr_read_b32 v0, a176\nv_accvgpr_read_b32 v1, a177\nQmava 224,52,148\n"
"v_accvgpr_read_b32 v6, a178\nv_accvgpr_read_b32 v7, a179\nQmava 228,24,152\nv_accvgpr_read_b32 v8, a180\nv_accvgpr_read_b32 v9, a181\nQmava 228,52,156\nv_accvgpr_read_b32 v14, a182\nv_accvgpr_read_b32 v15, a183\n"
"Qmava 232,24,160\nv_accvgpr_read_b32 v16, a184\nv_accvgpr_read_b32 v17, a185\nQmava 232,52,164\nv_accvgpr_read_b32 v22, a186\nv_accvgpr_read_b32 v23, a187\nQmava 236,24,168\nv_accvgpr_read_b32 v2, a190\n"
"v_accvgpr_read_b32 v3, a191\nQmava 236,52,172\ns_waitcnt lgkmcnt(0)\nv_accvgpr_read_b32 v24, a188\nQmvva 96,28,112\nv_accvgpr_read_b32 v25, a189\nv_accvgpr_read_b32 v4, a192\nQmvva 100,28,116\n"
"v_accvgpr_read_b32 v5, a193\nv_accvgpr_read_b32 v10, a194\nQmvva 104,28,120\nv_accvgpr_read_b32 v11, a195\nv_accvgpr_read_b32 v12, a196\nQmvva 108,28,124\nv_accvgpr_read_b32 v13, a197\nv_accvgpr_read_b32 v18, a198\n"
"Qmvva 128,32,112\nv_accvgpr_read_b32 v19, a199\nv_accvgpr_read_b32 v20, a200\nQmvva 132,32,116\nv_accvgpr_read_b32 v21, a201\nv_accvgpr_read_b32 v26, a202\nQmvva 136,32,120\nv_accvgpr_read_b32 v27, a203\n"
"Qa 8,8,9\nQmvva 140,32,124\nQa 10,10,11\nQa 8,8,10\nQmvva 160,36,112\nQa 16,16,17\nQa 18,18,19\nQmvva 164,36,116\n"
"Qa 16,16,18\nQa 4,4,5\nQmvva 168,36,120\nQa 6,6,7\nQa 4,4,6\nQmvva 172,36,124\nQa 12,12,13\nQa 14,14,15\n"
"Qmvva 192,40,112\nQa 12,12,14\nQa 0,0,1\nQmvva 196,40,116\nQa 2,2,3\nQa 0,0,2\nQmvva 200,40,120\nQa 24,24,25\n"
"Qa 26,26,27\nQmvva 204,40,124\nQa 24,24,26\nQa 20,20,21\nQmvva 224,44,112\nQa 22,22,23\nQa 20,20,22\nQmvva 228,44,116\n"
"v_max3_u32 v9, v8, v16, v4\nv_max3_u32 v9, v9, v12, v0\nQmvva 232,44,120\nv_max3_u32 v9, v9, v24, v20\nv_min3_u32 v11, v8, v16, v4\nQmvva 236,44,124\nv_min3_u32 v11, v11, v12, v0\nv_min3_u32 v11, v11, v24, v20\n"
"Qmava 208,48,112\nQmava 212,48,116\nQmava 216,48,120\nQmava 220,48,124\nQmava 240,52,112\nQmava 244,52,116\nQmava 248,52,120\nQmava 252,52,124\n"
"v_cmp_lt_u32 vcc, 0x44800000, v9\ns_mov_b64 s[74:75], vcc\nv_cmp_gt_u32 vcc, 0x2b800000, v11\ns_or_b64 s[74:75], s[74:75], vcc\ns_cmp_lg_u64 s[74:75], 0\ns_cbranch_scc1 .Lfb\n.Lfbret1:\ns_mov_b32 s56, %8\n"
"s_and_b32 s57, %9, 0xffff\ns_mov_b32 s58, 4194304\ns_lshl_b32 s68, %19, 4\ns_cmp_eq_u32 s70, 0\ns_cselect_b32 s68, s68, -1\nv_mov_b32 v77, 0x7ffff000\nv_cmp_eq_u32 vcc, s68, v60\ns_nop 1\n"
"v_cndmask_b32 v77, v77, v76, vcc\ns_lshl_b32 s68, s93, 12\ns_add_u32 s68, s68, %10\nv_lshrrev_b32 v66, 9, v60\nv_lshlrev_b32 v66, 11, v66\nv_bfe_u32 v67, v60, 1, 8\nv_add3_u32 v68, v66, v67, s68\nv_add_u32 v69, 65536, v68\n"
"v_add_u32 v70, 131072, v68\ns_mov_b64 exec, 0\ns_cmp_eq_u32 s70, 0\ns_cbranch_scc1 .Lpg16\ns_cmp_eq_u32 s70, 1\ns_cbranch_scc1 .Lpg17\ns_cmp_eq_u32 s70, 2\ns_cbranch_scc1 .Lpg18\n"
"s_branch .Lpg19\n.Ltr8:\ns_mov_b64 exec, -1\nv_rcp_f32 v2, v0\nv_rcp_f32 v6, v4\nv_rcp_f32 v10, v8\nv_rcp_f32 v14, v12\nv_rcp_f32 v18, v16\n"
"v_rcp_f32 v22, v20\nv_rcp_f32 v26, v24\nv_and_b32 v66, 16, v60\nv_mov_b32 v67, 0xffff\nv_lshlrev_b32 v66, v66, v67\ns_mov_b32 vcc_lo, 0x00c0000c\ns_mov_b32 vcc_hi, 0xc0000c00\nv_cndmask_b32 v67, 0, v66, vcc\n"
"s_lshr_b64 vcc, vcc, 2\nv_cndmask_b32 v66, 0, v66, vcc\nv_mov_b64 v[0:1], 0\nQk 2,2,2\nv_and_b32 v3, v2, v67\nv_and_b32 v2, v2, v66\nv_mov_b64 v[4:5], 0\nQk 6,6,6\n"
"v_and_b32 v7, v6, v67\nv_and_b32 v6, v6, v66\nv_mov_b64 v[8:9], 0\nQk 10,10,10\nv_and_b32 v11, v10, v67\nv_and_b32 v10, v10, v66\nv_mov_b64 v[12:13], 0\nQk 14,14,14\n"
"v_and_b32 v15, v14, v67\nv_and_b32 v14, v14, v66\nv_mov_b64 v[16:17], 0\nQk 18,18,18\nv_and_b32 v19, v18, v67\nv_and_b32 v18, v18, v66\nv_mov_b64 v[20:21], 0\nQk 22,22,22\n"
"v_and_b32 v23, v22, v67\nv_and_b32 v22, v22, v66\nv_mov_b64 v[24:25], 0\nQk 26,26,26\nv_and_b32 v27, v26, v67\nv_and_b32 v26, v26, v66\nv_mov_b64 v[28:29], 0\nQk 80,80,81\n"
"Qk 81,82,83\nQk 82,84,85\nQk 83,86,87\ns_nop 1\nQzvvv 84,80,0\nQzvvv 80,80,2\nQk 112,112,113\nQk 113,114,115\n"
"Qk 114,116,117\nQk 115,118,119\ns_nop 1\nQzvvv 116,112,4\nQzvvv 112,112,6\nQk 144,144,145\nQk 145,146,147\nQk 146,148,149\n"
"Qk 147,150,151\nQk 80,80,81\nQk 81,82,83\nQzvvv 148,144,8\nQk 82,84,85\nQk 83,86,87\nQzvvv 144,144,10\nQk 176,176,177\n"
"Qk 177,178,179\nQk 178,180,181\nQk 179,182,183\nQk 112,112,113\nQk 113,114,115\nQzvvv 180,176,12\nQk 114,116,117\nQk 115,118,119\n"
"Qzvvv 176,176,14\nQk 208,208,209\nQk 209,210,211\nQk 210,212,213\nQk 211,214,215\nQk 144,144,145\nQk 145,146,147\nQzvvv 212,208,16\n"
"Qk 146,148,149\nQk 147,150,151\nQzvvv 208,208,18\nQk 240,240,241\nQk 241,242,243\nQk 242,244,245\nQk 243,246,247\nQk 176,176,177\n"
"Qk 177,178,179\nQzvvv 244,240,20\nQk 178,180,181\nQk 179,182,183\nQzvvv 240,240,22\nv_accvgpr_read_b32 v84, a224\nv_accvgpr_read_b32 v85, a225\nv_accvgpr_read_b32 v86, a226\n"
"v_accvgpr_read_b32 v87, a227\nv_accvgpr_read_b32 v32, a228\nv_accvgpr_read_b32 v33, a229\nv_accvgpr_read_b32 v34, a230\nv_accvgpr_read_b32 v35, a231\nQk 84,84,85\nQk 85,86,87\nQk 86,32,33\n"
"Qk 87,34,35\nQk 208,208,209\nQk 209,210,211\nQzvvv 32,84,24\nQk 210,212,213\nQk 211,214,215\nQzvvv 84,84,26\nQk 88,88,89\n"
"Qk 89,90,91\nQk 90,92,93\nQk 91,94,95\nQk 240,240,241\nQk 241,242,243\nQzvvv 92,88,0\nQk 242,244,245\nQk 243,246,247\n"
"Qzvvv 88,88,2\nQk 120,120,121\nQk 121,122,123\nQk 122,124,125\nQk 123,126,127\nQk 84,84,85\nQk 85,86,87\nQzvvv 124,120,4\n"
"Qk 86,32,33\nQk 87,34,35\nQzvvv 120,120,6\nQk 152,152,153\nQk 153,154,155\nQk 154,156,157\nQk 155,158,159\nQk 88,88,89\n"
"Qk 89,90,91\nQzvvv 156,152,8\nQk 90,92,93\nQk 91,94,95\nQzvvv 152,152,10\ns_waitcnt vmcnt(20)\ns_barrier\nQk 184,184,185\n"
"Qk 185,186,187\nQk 186,188,189\nQk 187,190,191\nQk 120,120,121\nQk 121,122,123\nQzvvv 188,184,12\nQk 122,124,125\nQk 123,126,127\n"
"Qzvvv 184,184,14\nQta 56,68,0\nQk 216,216,217\nQk 217,218,219\nQk 218,220,221\nQk 219,222,223\nQk 152,152,153\nQk 153,154,155\n"
"Qzvvv 220,216,16\nQk 154,156,157\nQk 155,158,159\nQzvvv 216,216,18\nQta 58,68,1024\nQk 248,248,249\nQk 249,250,251\nQk 250,252,253\n"
"Qk 251,254,255\nQk 184,184,185\nQk 185,186,187\nQzvvv 252,248,20\nQk 186,188,189\nQk 187,190,191\nQzvvv 248,248,22\nQta 60,68,8192\n"
"v_accvgpr_read_b32 v116, a232\nv_accvgpr_read_b32 v117, a233\nv_accvgpr_read_b32 v118, a234\nv_accvgpr_read_b32 v119, a235\nv_accvgpr_read_b32 v36, a236\nv_accvgpr_read_b32 v37, a237\nv_accvgpr_read_b32 v38, a238\nv_accvgpr_read_b32 v39, a239\n"
"Qk 116,116,117\nQk 117,118,119\nQk 118,36,37\nQk 119,38,39\nQk 216,216,217\nQk 217,218,219\nQzvvv 36,116,24\nQk 218,220,221\n"
"Qk 219,222,223\nQzvvv 116,116,26\nQta 62,68,9216\ns_mov_b32 s31, 0\nQk 96,96,97\nQk 97,98,99\nQk 98,100,101\nQk 99,102,103\n"
"Qk 248,248,249\nQk 249,250,251\nQzvvv 100,96,0\nQk 250,252,253\nQk 251,254,255\nQzvvv 96,96,2\nbuffer_load_dwordx4 a[0:3], v79, s[48:51], s31 offen offset:0\nQta 64,68,256\n"
"Qk 128,128,129\nQk 129,130,131\nQk 130,132,133\nQk 131,134,135\nQk 116,116,117\nQk 117,118,119\nQzvvv 132,128,4\nQk 118,36,37\n"
"Qk 119,38,39\nQzvvv 128,128,6\nbuffer_load_dwordx4 a[4:7], v79, s[48:51], s31 offen offset:1024\nQta 66,68,1280\ns_waitcnt lgkmcnt(2)\nQzvav 56,56,80\nQmvav 56,60,88\nQk 160,160,161\n"
"Qk 161,162,163\nQk 162,164,165\nQk 163,166,167\nQk 96,96,97\nQk 97,98,99\nQzvvv 164,160,8\nQk 98,100,101\nQk 99,102,103\n"
"Qzvvv 160,160,10\nbuffer_load_dwordx4 a[8:11], v79, s[48:51], s31 offen offset:2048\nQta 68,68,8448\nQk 192,192,193\nQk 193,194,195\nQk 194,196,197\nQk 195,198,199\nQk 128,128,129\n"
"Qk 129,130,131\nQzvvv 196,192,12\nQk 130,132,133\nQk 131,134,135\nQzvvv 192,192,14\nbuffer_load_dwordx4 a[12:15], v79, s[48:51], s31 offen offset:3072\nQta 70,68,9472\nv_max3_f32 v66, |v56|, |v57|, |v58|\n"
"v_max_f32 v66, v66, |v59|\n.Lpg13:\ns_cbranch_execz .Ltr6\nv_mov_b32 v67, v66\ns_mov_b32 s31, 4096\nQk 224,224,225\nQk 225,226,227\nQk 226,228,229\n"
"Qk 227,230,231\nQk 160,160,161\nQk 161,162,163\nQzvvv 228,224,16\nQk 162,164,165\nQk 163,166,167\nQzvvv 224,224,18\nbuffer_load_dwordx4 a[16:19], v79, s[48:51], s31 offen offset:0\n"
"v_permlane32_swap_b32 v66, v67\nv_max_f32 v66, v66, v67\nv_mov_b32 v67, v66\nv_accvgpr_read_b32 v148, a208\nv_accvgpr_read_b32 v149, a209\nv_accvgpr_read_b32 v150, a210\nv_accvgpr_read_b32 v151, a211\nv_accvgpr_read_b32 v36, a212\n"
"v_accvgpr_read_b32 v37, a213\nv_accvgpr_read_b32 v38, a214\nv_accvgpr_read_b32 v39, a215\nQk 148,148,149\nQk 149,150,151\nQk 150,36,37\nQk 151,38,39\nQk 192,192,193\n"
"Qk 193,194,195\nQzvvv 36,148,20\nQk 194,196,197\nQk 195,198,199\nQzvvv 148,148,22\nbuffer_load_dwordx4 a[20:23], v79, s[48:51], s31 offen offset:1024\nv_permlane16_swap_b32 v66, v67\nv_max_f32 v66, v66, v67\n"
"v_accvgpr_read_b32 v180, a240\nv_accvgpr_read_b32 v181, a241\nv_accvgpr_read_b32 v182, a242\nv_accvgpr_read_b32 v183, a243\nv_accvgpr_read_b32 v40, a244\nv_accvgpr_read_b32 v41, a245\nv_accvgpr_read_b32 v42, a246\nv_accvgpr_read_b32 v43, a247\n"
"Qk 180,180,181\nQk 181,182,183\nQk 182,40,41\nQk 183,42,43\nQk 224,224,225\nQk 225,226,227\nQzvvv 40,180,24\nQk 226,228,229\n"
"Qk 227,230,231\nQzvvv 180,180,26\nbuffer_load_dwordx4 a[24:27], v79, s[48:51], s31 offen offset:2048\nv_max_f32_dpp v66, v66, v66 row_ror:8 row_mask:0xf bank_mask:0xf\ns_mov_b32 s31, 16384\nQk 104,104,105\nQk 105,106,107\nQk 106,108,109\n"
"Qk 107,110,111\nQk 148,148,149\nQk 149,150,151\nQzvvv 108,104,0\nQk 150,36,37\nQk 151,38,39\nQzvvv 104,104,2\nbuffer_load_dwordx4 a[28:31], v79, s[48:51], s31 offen offset:0\n"
"v_max_f32_dpp v66, v66, v66 row_ror:4 row_mask:0xf bank_mask:0xf\nQk 136,136,137\nQk 137,138,139\nQk 138,140,141\nQk 139,142,143\nQk 180,180,181\nQk 181,182,183\nQzvvv 140,136,4\n"
"Qk 182,40,41\nQk 183,42,43\nQzvvv 136,136,6\nbuffer_load_dwordx4 a[32:35], v79, s[48:51], s31 offen offset:1024\nv_max_f32_dpp v66, v66, v66 row_ror:2 row_mask:0xf bank_mask:0xf\nQk 168,168,169\nQk 169,170,171\nQk 170,172,173\n"
"Qk 171,174,175\nQk 104,104,105\nQk 105,106,107\nQzvvv 172,168,8\nQk 106,108,109\nQk 107,110,111\nQzvvv 168,168,10\nbuffer_load_dwordx4 a[36:39], v79, s[48:51], s31 offen offset:2048\n"
"v_max_f32_dpp v66, v66, v66 row_ror:1 row_mask:0xf bank_mask:0xf\nv_and_b32 v66, 0x7f800000, v66\nv_add_u32 v66, 0x2000000, v66\nQk 200,200,201\nQk 201,202,203\nQk 202,204,205\nQk 203,206,207\nQk 136,136,137\n"
"Qk 137,138,139\nQzvvv 204,200,12\nQk 138,140,141\nQk 139,142,143\nQzvvv 200,200,14\nbuffer_load_dwordx4 a[40:43], v79, s[48:51], s31 offen offset:3072\nv_min_u32 v66, 0x7e000000, v66\nv_or_b32 v66, 0x400000, v66\n"
"ds_write_b32 v73, v66 offset:16\nv_xor_b32 v67, 0x80000000, v66\ns_mov_b32 s31, 20480\nQk 232,232,233\nQk 233,234,235\nQk 234,236,237\nQk 235,238,239\nQk 168,168,169\n"
"Qk 169,170,171\nQzvvv 236,232,16\nQk 170,172,173\nQk 171,174,175\nQzvvv 232,232,18\nbuffer_load_dwordx4 a[44:47], v79, s[48:51], s31 offen offset:0\nv_accvgpr_write_b32 a112, v67\nv_accvgpr_write_b32 a113, v67\n"
"v_accvgpr_write_b32 a114, v67\nv_accvgpr_write_b32 a115, v67\nv_accvgpr_read_b32 v212, a216\nv_accvgpr_read_b32 v213, a217\nv_accvgpr_read_b32 v214, a218\nv_accvgpr_read_b32 v215, a219\nv_accvgpr_read_b32 v40, a220\nv_accvgpr_read_b32 v41, a221\n"
"v_accvgpr_read_b32 v42, a222\nv_accvgpr_read_b32 v43, a223\nQk 212,212,213\nQk 213,214,215\nQk 214,40,41\nQk 215,42,43\nQk 200,200,201\nQk 201,202,203\n"
"Qzvvv 40,212,20\nQk 202,204,205\nQk 203,206,207\nQzvvv 212,212,22\nbuffer_load_dwordx4 a[48:51], v79, s[48:51], s31 offen offset:1024\nv_accvgpr_read_b32 v244, a248\nv_accvgpr_read_b32 v245, a249\nv_accvgpr_read_b32 v246, a250\n"
"v_accvgpr_read_b32 v247, a251\nv_accvgpr_read_b32 v32, a252\nv_accvgpr_read_b32 v33, a253\nv_accvgpr_read_b32 v34, a254\nv_accvgpr_read_b32 v35, a255\nQk 244,244,245\nQk 245,246,247\nQk 246,32,33\n"
"Qk 247,34,35\nQk 232,232,233\nQk 233,234,235\nQzvvv 32,244,24\nQk 234,236,237\nQk 235,238,239\nQzvvv 244,244,26\nbuffer_load_dwordx4 a[52:55], v79, s[48:51], s31 offen offset:2048\n"
"Qk 212,212,213\nQk 213,214,215\nQk 214,40,41\nQk 215,42,43\ns_nop 2\nQk 244,244,245\nQk 245,246,247\nQk 246,32,33\n"
"Qk 247,34,35\ns_waitcnt lgkmcnt(5)\nQcaav 140,56,84,112\ns_add_u32 m0, s96, 98304\ns_mov_b32 s95, 86016\nQmaav 140,60,116\nbuffer_load_dwordx4 v61, s[52:55], s95 offen lds\nQcaav 136,56,240,112\n"
"Qmaav 136,60,248\nQcaav 132,56,208,112\nQta 72,68,16384\ns_mov_b32 s95, 105472\nQmaav 132,60,216\nbuffer_load_dwordx4 v61, s[52:55], s95 offen offset:1024 lds\nQcaav 128,56,176,112\nQmaav 128,60,184\n"
"Qta 74,68,16896\nQcaav 124,56,144,112\nQmaav 124,60,152\nQcaav 120,56,112,112\nQta 76,68,17408\nQmaav 120,60,120\ns_add_u32 s88, s71, 0\nQcaav 116,56,80,112\n"
"buffer_store_dwordx4 v[80:83], v79, s[56:59], s88 offen offset:0 sc1\nQmaav 116,60,88\nQta 78,68,17920\ns_waitcnt lgkmcnt(5)\nQcaav 144,64,80,112\nQmaav 144,68,88\nQcaav 148,64,112,112\nQmaav 148,68,120\n"
"Qcaav 152,64,144,112\nQta 56,68,24576\nQmaav 152,68,152\nQcaav 156,64,176,112\nQmaav 156,68,184\nQta 58,68,25600\ns_add_u32 m0, s96, 106496\ns_mov_b32 s95, 126976\n"
"Qcaav 160,64,208,112\nbuffer_load_dwordx4 v61, s[52:55], s95 offen lds\nQmaav 160,68,216\nQcaav 164,64,240,112\nQta 60,68,32768\nQmaav 164,68,248\ns_mov_b32 s95, 146432\nQcaav 168,64,84,112\n"
"buffer_load_dwordx4 v61, s[52:55], s95 offen offset:1024 lds\nQmaav 168,68,116\nQta 62,68,33792\ns_waitcnt lgkmcnt(4)\nQcaav 196,72,84,112\nQmaav 196,76,116\nQcaav 192,72,240,112\nQmaav 192,76,248\n"
"Qcaav 188,72,208,112\nQta 64,68,24832\nQmaav 188,76,216\nbuffer_store_dwordx4 v[112:115], v79, s[56:59], s88 offen offset:1024 sc1\nQcaav 184,72,176,112\nQmaav 184,76,184\nQta 66,68,25856\nQcaav 180,72,144,112\n"
"Qmaav 180,76,152\nQcaav 176,72,112,112\nQta 68,68,33024\nv_add_u32 v75, 1, v75\nQmaav 176,76,120\ns_waitcnt vmcnt(34)\nds_write_b32 v73, v75\nQcaav 172,72,80,112\n"
"Qmaav 172,76,88\nQta 70,68,34048\ns_waitcnt lgkmcnt(5)\nQcaav 200,56,80,112\ns_add_u32 m0, s96, 114688\ns_mov_b32 s95, 86016\nQmaav 200,60,88\nbuffer_load_dwordx4 v62, s[52:55], s95 offen lds\n"
"Qcaav 204,56,112,112\nQmaav 204,60,120\nQcaav 208,56,144,112\nQta 72,68,512\nQmaav 208,60,152\nds_read_b32 v66, v74\ns_mov_b32 s95, 125952\nQcaav 212,56,176,112\n"
"buffer_load_dwordx4 v62, s[52:55], s95 offen offset:1024 lds\nQmaav 212,60,184\nQta 74,68,1536\nQcaav 216,56,208,112\nQmaav 216,60,216\ns_waitcnt lgkmcnt(1)\nv_cmp_gt_u32 vcc, v75, v66\nQcaav 220,56,240,112\n"
"s_cbranch_vccnz .Lso40\n.Lro40:\nQmaav 220,60,248\nQta 76,68,8704\nQcaav 224,56,84,112\nbuffer_store_dwordx4 v[144:147], v79, s[56:59], s88 offen offset:2048 sc1\nv_xor_b32 v56, 8, v73\nQmaav 224,60,116\n"
"ds_read_b32 v56, v56 offset:16\ns_waitcnt lgkmcnt(5)\nQcaav 252,64,84,112\nQta 78,68,9728\nQmaav 252,68,116\nQcaav 248,64,240,112\nQmaav 248,68,248\nQcaav 244,64,208,112\n"
"Qta 56,68,768\nQmaav 244,68,216\nQcaav 240,64,176,112\ns_waitcnt lgkmcnt(2)\nv_mov_b32 v57, v56\nv_mov_b32 v58, v56\nQmaav 240,68,184\nQta 58,68,1792\n"
"s_add_u32 m0, s96, 122880\ns_mov_b32 s95, 86016\nQcaav 236,64,144,112\nbuffer_load_dwordx4 v63, s[52:55], s95 offen lds\nQmaav 236,68,152\nv_mov_b32 v59, v56\nv_accvgpr_write_b32 a80, v56\nQcaav 232,64,112,112\n"
"Qta 60,68,8960\nv_accvgpr_write_b32 a81, v56\nQmaav 232,68,120\ns_mov_b32 s95, 105472\nv_accvgpr_write_b32 a82, v56\nQcaav 228,64,80,112\nbuffer_load_dwordx4 v63, s[52:55], s95 offen offset:1024 lds\nQmaav 228,68,88\n"
"Qta 62,68,9984\nv_accvgpr_write_b32 a83, v56\ns_waitcnt lgkmcnt(4)\nQcvav 0,72,80,56\nQmvav 0,76,88\nQcvav 4,72,112,56\nbuffer_store_dwordx4 v[176:179], v79, s[56:59], s88 offen offset:3072 sc1\nQmvav 4,76,120\n"
"Qcvav 8,72,144,56\nQta 64,68,16640\nQmvav 8,76,152\nQcvav 12,72,176,56\nQmvav 12,76,184\nQta 66,68,17152\nQcvav 16,72,208,56\nQmvav 16,76,216\n"
"Qcvav 20,72,240,56\nQta 68,68,17664\nQmvav 20,76,248\nQcvav 24,72,84,56\nQmvav 24,76,116\nQta 70,68,18176\ns_waitcnt lgkmcnt(4)\nQcvav 52,56,84,56\n"
"Qmvav 52,60,116\nQcvav 48,56,240,56\nQmvav 48,60,248\nQcvav 44,56,208,56\nQta 72,68,25088\nQmvav 44,60,216\nQcvav 40,56,176,56\nQmvav 40,60,184\n"
"Qta 74,68,26112\ns_add_u32 s88, s71, 4096\nQcvav 36,56,144,56\nbuffer_store_dwordx4 v[208:211], v79, s[56:59], s88 offen offset:0 sc1\nQmvav 36,60,152\nQcvav 32,56,112,56\nQta 76,68,33280\nQmvav 32,60,120\n"
"Qcvav 28,56,80,56\nQmvav 28,60,88\nQta 78,68,34304\ns_waitcnt lgkmcnt(4)\nQcvav 92,64,80,56\nQmvav 92,68,88\nQcvav 124,64,112,56\nQmvav 124,68,120\n"
"Qcvav 156,64,144,56\nQta 56,68,25344\nQmvav 156,68,152\nQcvav 188,64,176,56\nQmvav 188,68,184\nQta 58,68,26368\nQcvav 220,64,208,56\nQmvav 220,68,216\n"
"Qcvav 252,64,240,56\nQta 60,68,33536\nQmvav 252,68,248\nQcvav 100,64,84,56\nQmvav 100,68,116\nQta 62,68,34560\ns_waitcnt lgkmcnt(4)\nQcvav 172,72,84,56\n"
"buffer_store_dwordx4 v[240:243], v79, s[56:59], s88 offen offset:1024 sc1\nQmvav 172,76,116\nQcvav 140,72,240,56\nQmvav 140,76,248\nQcvav 108,72,208,56\nQta 64,68,40960\nQmvav 108,76,216\nQcvav 228,72,176,56\n"
"Qmvav 228,76,184\nQta 66,68,41984\nQcvav 196,72,144,56\nQmvav 196,76,152\nQcvav 164,72,112,56\nQta 68,68,49152\nQmvav 164,76,120\nQcvav 132,72,80,56\n"
"Qmvav 132,76,88\nQta 70,68,50176\ns_waitcnt lgkmcnt(4)\nQcvav 204,56,80,56\nQmvav 204,60,88\nQcvav 236,56,112,56\nQmvav 236,60,120\nQcaav 96,56,144,80\n"
"Qta 72,68,41216\nQmaav 96,60,152\nQcaav 100,56,176,80\nbuffer_store_dwordx4 v[84:87], v79, s[56:59], s88 offen offset:2048 sc1\nQmaav 100,60,184\nQta 74,68,42240\nQcaav 104,56,208,80\nQmaav 104,60,216\n"
"Qcaav 108,56,240,80\nQta 76,68,49408\nQmaav 108,60,248\nQcaav 112,56,84,80\nQmaav 112,60,116\nQta 78,68,50432\ns_waitcnt vmcnt(15)\ns_waitcnt lgkmcnt(4)\n"
"Qmaaa 140,64,24\nQmaaa 140,68,52\nQmaaa 136,64,20\nQmaaa 136,68,48\nQmaaa 132,64,16\nQta 56,68,41472\nQmaaa 132,68,44\nQmaaa 128,64,12\n"
"Qmaaa 128,68,40\nQta 58,68,42496\nQmaaa 124,64,8\nQmaaa 124,68,36\nQmaaa 120,64,4\nQta 60,68,49664\ns_and_b32 s68, %19, 3\nQmaaa 120,68,32\n"
"s_add_u32 s88, s71, 16384\nQmaaa 116,64,0\nbuffer_store_dwordx4 v[88:91], v79, s[56:59], s88 offen offset:0 sc1\nQmaaa 116,68,28\nQta 62,68,50688\ns_waitcnt lgkmcnt(4)\nQmaaa 144,72,0\ns_cmp_lt_u32 s68, 1\n"
"s_cselect_b32 s68, 40, 32\nQmaaa 144,76,28\ns_mul_i32 s67, s68, 40960\ns_mul_i32 s74, s68, 32768\nQmaaa 148,72,4\ns_and_b32 s68, %19, 3\nQmaaa 148,76,32\ns_cmp_lt_u32 s68, 2\n"
"s_cselect_b32 s68, 48, 40\nQmaaa 152,72,8\nQta 64,68,41728\ns_mul_i32 s72, s68, 40960\nQmaaa 152,76,36\ns_mul_i32 s75, s68, 32768\ns_and_b32 s68, %19, 3\nQmaaa 156,72,12\n"
"s_cmp_lt_u32 s68, 3\ns_cselect_b32 s68, 56, 48\nQmaaa 156,76,40\nQta 66,68,42752\ns_mul_i32 s73, s68, 40960\nQmaaa 160,72,16\ns_mul_i32 s77, s68, 32768\nv_add_u32 v58, s91, v60\n"
"Qmaaa 160,76,44\ns_mul_i32 s68, s93, 28672\ns_sub_u32 s68, 14336, s68\nQmaaa 164,72,20\nQta 68,68,49920\nv_add_u32 v59, s68, v58\nQmaaa 164,76,48\nQmaaa 168,72,24\n"
"Qmaaa 168,76,52\nQta 70,68,50944\ns_waitcnt lgkmcnt(4)\nQmvaa 24,56,24\nQmvaa 24,60,52\nQmvaa 20,56,20\nQmvaa 20,60,48\nQmvaa 16,56,16\n"
"Qta 72,68,57344\nQmvaa 16,60,44\nbuffer_store_dwordx4 v[120:123], v79, s[56:59], s88 offen offset:1024 sc1\nQmvaa 12,56,12\nQmvaa 12,60,40\nQta 74,68,57856\nQmvaa 8,56,8\nQmvaa 8,60,36\n"
"Qmvaa 4,56,4\nQta 76,68,58368\nv_add_u32 v75, 1, v75\nQmvaa 4,60,32\ns_waitcnt vmcnt(35)\nds_write_b32 v73, v75\nQmvaa 0,56,0\nQmvaa 0,60,28\n"
"Qta 78,68,58880\ns_waitcnt lgkmcnt(5)\nQmvaa 28,64,0\nQmvaa 28,68,28\nQmvaa 32,64,4\nQmvaa 32,68,32\nQmvaa 36,64,8\nQta 56,68,57600\n"
"Qmvaa 36,68,36\nds_read_b32 v66, v74\nQmvaa 40,64,12\nQmvaa 40,68,40\nQta 58,68,58112\nQmvaa 44,64,16\nQmvaa 44,68,44\ns_waitcnt lgkmcnt(1)\n"
"v_cmp_gt_u32 vcc, v75, v66\nQmvaa 48,64,20\ns_cbranch_vccnz .Lso180\n.Lro180:\nQmvaa 48,68,48\nQta 60,68,58624\nQmvaa 52,64,24\nbuffer_store_dwordx4 v[152:155], v79, s[56:59], s88 offen offset:2048 sc1\n"
"Qmvaa 52,68,52\ns_waitcnt lgkmcnt(4)\nQmaaa 196,72,24\nQta 62,68,59136\nQmaaa 196,76,52\nQmaaa 192,72,20\nQmaaa 192,76,48\nQmaaa 188,72,16\n"
"Qta 64,70,1024\nQmaaa 188,76,44\nQmaaa 184,72,12\nQmaaa 184,76,40\nQta 66,70,2048\ns_add_u32 m0, s96, 0\ns_mov_b32 s95, 126976\nQmaaa 180,72,8\n"
"buffer_load_dwordx4 v63, s[52:55], s95 offen lds\nQmaaa 180,76,36\nQmaaa 176,72,4\nQta 68,70,9216\nQmaaa 176,76,32\ns_mov_b32 s95, 146432\n.Lpg14:\ns_cbranch_execz .Ltr6\n"
"Qmaaa 172,72,0\nbuffer_load_dwordx4 v63, s[52:55], s95 offen offset:1024 lds\nQmaaa 172,76,28\nQta 70,70,10240\ns_waitcnt lgkmcnt(4)\nQmvaa 92,56,0\nQmvaa 92,60,28\nQmvaa 124,56,4\n"
"buffer_store_dwordx4 v[184:187], v79, s[56:59], s88 offen offset:3072 sc1\nQmvaa 124,60,32\nQmvaa 156,56,8\nQta 72,70,1280\nQmvaa 156,60,36\nQmvaa 188,56,12\nQmvaa 188,60,40\nQta 74,70,2304\n"
"Qmvaa 220,56,16\nQmvaa 220,60,44\nQmvaa 252,56,20\nQta 76,70,9472\nQmvaa 252,60,48\ns_add_u32 m0, s96, 8192\ns_mov_b32 s95, 331776\nQmvaa 100,56,24\n"
"buffer_load_dwordx4 v61, s[52:55], s95 offen lds\nQmvaa 100,60,52\nQta 78,70,10496\ns_waitcnt lgkmcnt(4)\nQmaaa 224,64,24\nQmaaa 224,68,52\ns_mov_b32 s95, 351232\nQmaaa 220,64,20\n"
"buffer_load_dwordx4 v61, s[52:55], s95 offen offset:1024 lds\nQmaaa 220,68,48\nQmaaa 216,64,16\nQta 56,70,1536\nQmaaa 216,68,44\nQmaaa 212,64,12\nQmaaa 212,68,40\nQta 58,70,2560\n"
"s_add_u32 s88, s71, 20480\nQmaaa 208,64,8\nbuffer_store_dwordx4 v[216:219], v79, s[56:59], s88 offen offset:0 sc1\nQmaaa 208,68,36\nQmaaa 204,64,4\nQta 60,70,9728\nQmaaa 204,68,32\nQmaaa 200,64,0\n"
"Qmaaa 200,68,28\nQta 62,70,10752\ns_waitcnt lgkmcnt(4)\nQmaaa 228,72,0\nQmaaa 228,76,28\nQmaaa 232,72,4\nQmaaa 232,76,32\nQmaaa 236,72,8\n"
"Qta 64,70,1792\ns_add_u32 m0, s96, 16384\ns_mov_b32 s95, 372736\nQmaaa 236,76,36\nbuffer_load_dwordx4 v61, s[52:55], s95 offen lds\nQmaaa 240,72,12\nQmaaa 240,76,40\nQta 66,70,2816\n"
"Qmaaa 244,72,16\ns_mov_b32 s95, 392192\nQmaaa 244,76,44\nbuffer_load_dwordx4 v61, s[52:55], s95 offen offset:1024 lds\nQmaaa 248,72,20\nQta 68,70,9984\nQmaaa 248,76,48\nQmaaa 252,72,24\n"
"Qmaaa 252,76,52\nQta 70,70,11008\ns_waitcnt lgkmcnt(4)\nQmvaa 172,56,24\nbuffer_store_dwordx4 v[248:251], v79, s[56:59], s88 offen offset:1024 sc1\nQmvaa 172,60,52\nQmvaa 140,56,20\nQmvaa 140,60,48\n"
"Qmvaa 108,56,16\nQta 72,70,17408\nQmvaa 108,60,44\nQmvaa 228,56,12\nQmvaa 228,60,40\nQta 74,70,18432\nQmvaa 196,56,8\nQmvaa 196,60,36\n"
"Qmvaa 164,56,4\nQta 76,69,0\ns_add_u32 m0, s96, 24576\ns_mov_b32 s95, 331776\nQmvaa 164,60,32\nbuffer_load_dwordx4 v62, s[52:55], s95 offen lds\nQmvaa 132,56,0\nQmvaa 132,60,28\n"
"Qta 78,69,1024\ns_waitcnt lgkmcnt(4)\nQmvaa 204,64,0\ns_mov_b32 s95, 371712\nQmvaa 204,68,28\nbuffer_load_dwordx4 v62, s[52:55], s95 offen offset:1024 lds\nQmvaa 236,64,4\nQmvaa 236,68,32\n"
"Qmaaa 96,64,8\nQta 56,70,17664\nQmaaa 96,68,36\nQmaaa 100,64,12\nbuffer_store_dwordx4 v[116:119], v79, s[56:59], s88 offen offset:2048 sc1\nQmaaa 100,68,40\nQta 58,70,18688\nQmaaa 104,64,16\n"
"Qmaaa 104,68,44\nQmaaa 108,64,20\nQta 60,69,256\nQmaaa 108,68,48\nQmaaa 112,64,24\nQmaaa 112,68,52\nQta 62,69,1280\ns_waitcnt lgkmcnt(4)\n"
"Qmaav 140,72,180\nQmaav 140,76,244\ns_mov_b32 s31, 131072\nQmaav 136,72,148\nbuffer_load_dwordx4 a[0:3], v79, s[48:51], s31 offen offset:0\nQmaav 136,76,212\nQmaav 132,72,224\nQta 64,70,17920\n"
"Qmaav 132,76,232\nQmaav 128,72,192\nbuffer_load_dwordx4 a[4:7], v79, s[48:51], s31 offen offset:1024\nQmaav 128,76,200\nQta 66,70,18944\nQmaav 124,72,160\nQmaav 124,76,168\nQmaav 120,72,128\n"
"Qta 68,69,512\nQmaav 120,76,136\nbuffer_load_dwordx4 a[8:11], v79, s[48:51], s31 offen offset:2048\nQmaav 116,72,96\nQmaav 116,76,104\nQta 70,69,1536\ns_waitcnt lgkmcnt(4)\nQmaav 144,56,96\n"
"Qmaav 144,60,104\nbuffer_load_dwordx4 a[12:15], v79, s[48:51], s31 offen offset:3072\nQmaav 148,56,128\nQmaav 148,60,136\nQmaav 152,56,160\nQta 72,70,18176\ns_mov_b32 s31, 135168\nQmaav 152,60,168\n"
"buffer_load_dwordx4 a[16:19], v79, s[48:51], s31 offen offset:0\nQmaav 156,56,192\nQmaav 156,60,200\nQta 74,70,19200\nQmaav 160,56,224\nQmaav 160,60,232\nbuffer_load_dwordx4 a[20:23], v79, s[48:51], s31 offen offset:1024\nQmaav 164,56,148\n"
"Qta 76,69,768\nQmaav 164,60,212\nQmaav 168,56,180\nQmaav 168,60,244\nQta 78,69,1792\ns_waitcnt lgkmcnt(4)\nQmvav 24,64,180\nbuffer_load_dwordx4 a[24:27], v79, s[48:51], s31 offen offset:2048\n"
"Qmvav 24,68,244\nQmvav 20,64,148\nQmvav 20,68,212\nQmvav 16,64,224\nQta 56,69,8192\ns_mov_b32 s31, 147456\nQmvav 16,68,232\nbuffer_load_dwordx4 a[28:31], v79, s[48:51], s31 offen offset:0\n"
"Qmvav 12,64,192\nQmvav 12,68,200\nQta 58,69,8704\nQmvav 8,64,160\nQmvav 8,68,168\nbuffer_load_dwordx4 a[32:35], v79, s[48:51], s31 offen offset:1024\nQmvav 4,64,128\nQta 60,69,9216\n"
"v_add_u32 v75, 1, v75\nQmvav 4,68,136\ns_waitcnt vmcnt(31)\nds_write_b32 v73, v75\nQmvav 0,64,96\nQmvav 0,68,104\nQta 62,69,9728\ns_waitcnt lgkmcnt(5)\n"
"Qmvav 28,72,96\nbuffer_load_dwordx4 a[36:39], v79, s[48:51], s31 offen offset:2048\nQmvav 28,76,104\nQmvav 32,72,128\nQmvav 32,76,136\nQmvav 36,72,160\nQta 64,69,8448\nQmvav 36,76,168\n"
"ds_read_b32 v66, v74\nQmvav 40,72,192\nbuffer_load_dwordx4 a[40:43], v79, s[48:51], s31 offen offset:3072\nQmvav 40,76,200\nQta 66,69,8960\nQmvav 44,72,224\nQmvav 44,76,232\ns_waitcnt lgkmcnt(1)\n"
"v_cmp_gt_u32 vcc, v75, v66\nQmvav 48,72,148\ns_cbranch_vccnz .Lso320\n.Lro320:\nQmvav 48,76,212\nQta 68,69,9472\ns_mov_b32 s31, 151552\nQmvav 52,72,180\n"
"buffer_load_dwordx4 a[44:47], v79, s[48:51], s31 offen offset:0\nQmvav 52,76,244\ns_waitcnt lgkmcnt(4)\nQmaav 196,56,180\nQta 70,69,9984\nQmaav 196,60,244\nQmaav 192,56,148\nbuffer_load_dwordx4 a[48:51], v79, s[48:51], s31 offen offset:1024\n"
"Qmaav 192,60,212\nQmaav 188,56,224\nQta 72,69,16384\nQmaav 188,60,232\nQmaav 184,56,192\nbuffer_load_dwordx4 a[52:55], v79, s[48:51], s31 offen offset:2048\nQmaav 184,60,200\nQta 74,69,17408\n"
"Qmaav 180,56,160\nQmaav 180,60,168\nQmaav 176,56,128\nQta 76,69,24576\ns_mov_b32 s31, 262144\nQmaav 176,60,136\nbuffer_load_dwordx4 v[80:83], v79, s[48:51], s31 offen offset:0\nQmaav 172,56,96\n"
"Qmaav 172,60,104\nQta 78,69,25600\ns_waitcnt lgkmcnt(4)\nQmvav 92,64,96\nQmvav 92,68,104\nbuffer_load_dwordx4 v[112:115], v79, s[48:51], s31 offen offset:1024\nQmvav 124,64,128\nQmvav 124,68,136\n"
"Qmvav 156,64,160\nQta 56,69,16640\nQmvav 156,68,168\nbuffer_load_dwordx4 v[144:147], v79, s[48:51], s31 offen offset:2048\nQmvav 188,64,192\nQmvav 188,68,200\nQta 58,69,17664\nQmvav 220,64,224\n"
"Qmvav 220,68,232\nbuffer_load_dwordx4 v[176:179], v79, s[48:51], s31 offen offset:3072\nQmvav 252,64,148\nQta 60,69,24832\nQmvav 252,68,212\nQmvav 100,64,180\nQmvav 100,68,244\nQta 62,69,25856\n"
"s_mov_b32 s31, 266240\ns_waitcnt lgkmcnt(4)\nQmaav 224,72,180\nbuffer_load_dwordx4 v[208:211], v79, s[48:51], s31 offen offset:0\nQmaav 224,76,244\nQmaav 220,72,148\nQmaav 220,76,212\nQmaav 216,72,224\n"
"Qta 64,69,16896\nQmaav 216,76,232\nbuffer_load_dwordx4 v[240:243], v79, s[48:51], s31 offen offset:1024\nQmaav 212,72,192\nQmaav 212,76,200\nQta 66,69,17920\nQmaav 208,72,160\nQmaav 208,76,168\n"
"buffer_load_dwordx4 v[84:87], v79, s[48:51], s31 offen offset:2048\nQmaav 204,72,128\nQta 68,69,25088\nQmaav 204,76,136\nQmaav 200,72,96\nQmaav 200,76,104\nQta 70,69,26112\ns_mov_b32 s31, 278528\n"
"s_waitcnt lgkmcnt(4)\nQmaav 228,56,96\nbuffer_load_dwordx4 v[88:91], v79, s[48:51], s31 offen offset:0\nQmaav 228,60,104\nQmaav 232,56,128\nQmaav 232,60,136\nQmaav 236,56,160\nQta 72,69,17152\n"
"Qmaav 236,60,168\nbuffer_load_dwordx4 v[120:123], v79, s[48:51], s31 offen offset:1024\nQmaav 240,56,192\nQmaav 240,60,200\nQta 74,69,18176\nQmaav 244,56,224\nQmaav 244,60,232\nbuffer_load_dwordx4 v[152:155], v79, s[48:51], s31 offen offset:2048\n"
"Qmaav 248,56,148\nQta 76,69,25344\nQmaav 248,60,212\nQmaav 252,56,180\nQmaav 252,60,244\nQta 78,69,26368\ns_waitcnt lgkmcnt(4)\nQmvav 172,64,180\n"
"buffer_load_dwordx4 v[184:187], v79, s[48:51], s31 offen offset:3072\nQmvav 172,68,244\nQmvav 140,64,148\nQmvav 140,68,212\nQmvav 108,64,224\nQta 56,69,32768\ns_mov_b32 s31, 282624\nQmvav 108,68,232\n"
"buffer_load_dwordx4 v[216:219], v79, s[48:51], s31 offen offset:0\nQmvav 228,64,192\nQmvav 228,68,200\nQta 58,69,33792\nQmvav 196,64,160\nQmvav 196,68,168\nbuffer_load_dwordx4 v[248:251], v79, s[48:51], s31 offen offset:1024\nQmvav 164,64,128\n"
"Qta 60,69,40960\nQmvav 164,68,136\nQmvav 132,64,96\nQmvav 132,68,104\nQta 62,69,41984\ns_waitcnt lgkmcnt(4)\nQmvav 204,72,96\nbuffer_load_dwordx4 v[116:119], v79, s[48:51], s31 offen offset:2048\n"
"Qmvav 204,76,104\nQmvav 236,72,128\nQmvav 236,76,136\nQmaav 96,72,160\nQta 64,69,33024\ns_add_u32 m0, s96, 32768\ns_mov_b32 s95, 331776\nQmaav 96,76,168\n"
"buffer_load_dwordx4 v63, s[52:55], s95 offen lds\nQmaav 100,72,192\nQmaav 100,76,200\nQta 66,69,34048\nQmaav 104,72,224\ns_mov_b32 s95, 351232\nQmaav 104,76,232\nbuffer_load_dwordx4 v63, s[52:55], s95 offen offset:1024 lds\n"
"Qmaav 108,72,148\nQta 68,69,41216\nQmaav 108,76,212\nQmaav 112,72,180\nQmaav 112,76,244\nQta 70,69,42240\ns_add_u32 m0, s96, 40960\ns_mov_b32 s95, 372736\n"
"s_waitcnt vmcnt(16)\ns_waitcnt lgkmcnt(4)\nQmaaa 140,56,24\nbuffer_load_dwordx4 v63, s[52:55], s95 offen lds\nQmaaa 140,60,52\nQmaaa 136,56,20\nQmaaa 136,60,48\nQmaaa 132,56,16\n"
"Qta 72,69,33280\ns_mov_b32 s95, 392192\nQmaaa 132,60,44\nbuffer_load_dwordx4 v63, s[52:55], s95 offen offset:1024 lds\nQmaaa 128,56,12\nQmaaa 128,60,40\nQta 74,69,34304\nQmaaa 124,56,8\n"
"Qmaaa 124,60,36\nQmaaa 120,56,4\nQta 76,69,41472\ns_add_u32 s88, s71, 131072\nQmaaa 120,60,32\nbuffer_store_dwordx4 v[96:99], v79, s[56:59], s88 offen offset:0 sc1\nQmaaa 116,56,0\nQmaaa 116,60,28\n"
"Qta 78,69,42496\ns_waitcnt lgkmcnt(4)\nQmaaa 144,64,0\nQmaaa 144,68,28\nQmaaa 148,64,4\nQmaaa 148,68,32\nQmaaa 152,64,8\nQta 56,69,33536\n"
"Qmaaa 152,68,36\nQmaaa 156,64,12\nQmaaa 156,68,40\nQta 58,69,34560\ns_add_u32 m0, s96, 49152\ns_mov_b32 s95, 413696\nQmaaa 160,64,16\nbuffer_load_dwordx4 v61, s[52:55], s95 offen lds\n"
"Qmaaa 160,68,44\nQmaaa 164,64,20\nQta 60,69,41728\nQmaaa 164,68,48\ns_mov_b32 s95, 433152\nQmaaa 168,64,24\nbuffer_load_dwordx4 v61, s[52:55], s95 offen offset:1024 lds\nQmaaa 168,68,52\n"
"Qta 62,69,42752\ns_waitcnt lgkmcnt(4)\nQmvaa 24,72,24\nQmvaa 24,76,52\nQmvaa 20,72,20\nbuffer_store_dwordx4 v[128:131], v79, s[56:59], s88 offen offset:1024 sc1\nQmvaa 20,76,48\nQmvaa 16,72,16\n"
"Qta 64,69,49152\nQmvaa 16,76,44\nQmvaa 12,72,12\nQmvaa 12,76,40\nQta 66,69,49664\nQmvaa 8,72,8\nQmvaa 8,76,36\nQmvaa 4,72,4\n"
"Qta 68,69,50176\nv_add_u32 v75, 1, v75\nQmvaa 4,76,32\ns_waitcnt vmcnt(36)\nds_write_b32 v73, v75\ns_add_u32 m0, s96, 57344\ns_mov_b32 s95, 454656\nQmvaa 0,72,0\n"
"buffer_load_dwordx4 v61, s[52:55], s95 offen lds\nQmvaa 0,76,28\nQta 70,69,50688\ns_waitcnt lgkmcnt(5)\nQmvaa 28,56,0\nQmvaa 28,60,28\ns_mov_b32 s95, 474112\nQmvaa 32,56,4\n"
"buffer_load_dwordx4 v61, s[52:55], s95 offen offset:1024 lds\nQmvaa 32,60,32\nQmvaa 36,56,8\nQta 72,69,49408\nQmvaa 36,60,36\nds_read_b32 v66, v74\nQmvaa 40,56,12\nQmvaa 40,60,40\n"
"Qta 74,69,49920\nQmvaa 44,56,16\nbuffer_store_dwordx4 v[160:163], v79, s[56:59], s88 offen offset:2048 sc1\nv_add_u32 v67, 2, v65\nQmvaa 44,60,44\ns_waitcnt lgkmcnt(1)\nv_cmp_gt_u32 vcc, v75, v66\nQmvaa 48,56,20\n"
"s_cbranch_vccnz .Lso460\n.Lro460:\nQmvaa 48,60,48\nQta 76,69,50432\nQmvaa 52,56,24\nbuffer_store_dword v67, v77, s[24:27], 0 offen sc1\nQmvaa 52,60,52\ns_waitcnt lgkmcnt(4)\n"
"Qmaaa 196,64,24\nQta 78,69,50944\nQmaaa 196,68,52\nQmaaa 192,64,20\nQmaaa 192,68,48\nQmaaa 188,64,16\nQta 56,69,57344\ns_add_u32 m0, s96, 132096\n"
"s_mov_b32 s95, 413696\nQmaaa 188,68,44\nbuffer_load_dwordx4 v62, s[52:55], s95 offen lds\nQmaaa 184,64,12\nQmaaa 184,68,40\nQta 58,69,58368\nQmaaa 180,64,8\ns_mov_b32 s95, 453632\n"
"Qmaaa 180,68,36\nbuffer_load_dwordx4 v62, s[52:55], s95 offen offset:1024 lds\nQmaaa 176,64,4\nQta 60,68,0\nQmaaa 176,68,32\nQmaaa 172,64,0\nQmaaa 172,68,28\nQta 62,68,1024\n"
"s_waitcnt lgkmcnt(4)\nQmvaa 92,72,0\nbuffer_store_dwordx4 v[192:195], v79, s[56:59], s88 offen offset:3072 sc1\nQmvaa 92,76,28\nQmvaa 124,72,4\nQmvaa 124,76,32\nQmvaa 156,72,8\nQta 64,69,57600\n"
"Qmvaa 156,76,36\nQmvaa 188,72,12\nQmvaa 188,76,40\nQta 66,69,58624\nQmvaa 220,72,16\nQmvaa 220,76,44\nQmvaa 252,72,20\nQta 68,68,256\n"
"s_add_u32 m0, s96, 140288\ns_mov_b32 s95, 413696\nQmvaa 252,76,48\nbuffer_load_dwordx4 v63, s[52:55], s95 offen lds\nQmvaa 100,72,24\nQmvaa 100,76,52\nQta 70,68,1280\ns_waitcnt lgkmcnt(4)\n"
"Qmaaa 224,56,24\ns_mov_b32 s95, 433152\nQmaaa 224,60,52\nbuffer_load_dwordx4 v63, s[52:55], s95 offen offset:1024 lds\nQmaaa 220,56,20\nQmaaa 220,60,48\nQmaaa 216,56,16\nQta 72,69,57856\n"
"Qmaaa 216,60,44\ns_add_u32 s88, s71, 135168\nQmaaa 212,56,12\nbuffer_store_dwordx4 v[224:227], v79, s[56:59], s88 offen offset:0 sc1\nQmaaa 212,60,40\nQta 74,69,58880\nQmaaa 208,56,8\nQmaaa 208,60,36\n"
"Qmaaa 204,56,4\nQta 76,68,512\nQmaaa 204,60,32\n.Lpg15:\ns_cbranch_execz .Ltr6\nQmaaa 200,56,0\nQmaaa 200,60,28\nQta 78,68,1536\n"
"s_waitcnt lgkmcnt(4)\nQmaaa 228,64,0\nQmaaa 228,68,28\ns_add_u32 m0, s96, 148480\ns_mov_b32 s95, 454656\nQmaaa 232,64,4\nbuffer_load_dwordx4 v63, s[52:55], s95 offen lds\nQmaaa 232,68,32\n"
"Qmaaa 236,64,8\nQta 56,69,58112\nQmaaa 236,68,36\ns_mov_b32 s95, 474112\nQmaaa 240,64,12\nbuffer_load_dwordx4 v63, s[52:55], s95 offen offset:1024 lds\nQmaaa 240,68,40\nQta 58,69,59136\n"
"Qmaaa 244,64,16\nQmaaa 244,68,44\nQmaaa 248,64,20\nQta 60,68,768\nQmaaa 248,68,48\nQmaaa 252,64,24\nbuffer_store_dwordx4 v[148:151], v79, s[56:59], s88 offen offset:1024 sc1\nQmaaa 252,68,52\n"
"Qta 62,68,1792\ns_waitcnt lgkmcnt(4)\nQmvaa 172,72,24\nQmvaa 172,76,52\ns_mov_b32 s40, %17\ns_and_b32 s41, %18, 0xffff\nQmvaa 140,72,20\ns_mov_b32 s42, 571200\n"
"s_lshl_b32 s66, %19, 17\nQmvaa 140,76,48\ns_add_u32 s69, s98, 4194304\ns_add_u32 s98, s66, s69\nQmvaa 108,72,16\nQta 64,68,8192\ns_xor_b32 s66, s66, 524288\nQmvaa 108,76,44\n"
"s_add_u32 s64, s66, s69\ns_mul_i32 s66, s92, 33792\nQmvaa 228,72,12\ns_add_u32 s66, s66, %10\ns_add_u32 s66, s66, 98304\nQmvaa 228,76,40\nQta 66,68,9216\ns_add_u32 m0, s96, 65536\n"
"s_mov_b32 s95, 659456\nQmvaa 196,72,8\nbuffer_load_dwordx4 v61, s[52:55], s95 offen lds\nQmvaa 196,76,36\ns_mul_i32 s68, s93, 96\ns_sub_u32 s68, s66, s68\nQmvaa 164,72,4\nQta 68,68,16384\n"
"s_add_u32 s68, s68, 96\nQmvaa 164,76,32\ns_mov_b32 s95, 678912\nv_bfe_u32 v148, v60, 4, 4\nQmvaa 132,72,0\nbuffer_load_dwordx4 v61, s[52:55], s95 offen offset:1024 lds\nQmvaa 132,76,28\nQta 70,68,17408\n"
"v_mul_u32_u24 v148, 208, v148\ns_waitcnt lgkmcnt(4)\nQmvaa 204,56,0\nv_lshrrev_b32 v149, 8, v60\nv_lshl_add_u32 v148, v149, 3, v148\nQmvaa 204,60,28\nv_add_u32 v56, s68, v148\ns_lshl_b32 s68, s93, 3\n"
"Qmvaa 236,56,4\nv_lshrrev_b32 v148, 7, v60\nv_add_u32 v148, s68, v148\nQmvaa 236,60,32\nv_and_b32 v149, 127, v60\nv_and_b32 v150, 64, v60\nQmaaa 96,56,8\nQta 72,68,8448\n"
"v_lshrrev_b32 v150, 1, v150\nQmaaa 96,60,36\nbuffer_store_dwordx4 v[180:183], v79, s[56:59], s88 offen offset:2048 sc1\nQmaaa 100,56,12\nv_mul_u32_u24 v57, 208, v148\nv_add3_u32 v57, v57, v149, s66\nQmaaa 100,60,40\nQta 74,68,9472\n"
"v_add_u32 v57, v57, v150\nQmaaa 104,56,16\ns_and_b32 s65, %19, 1\ns_and_b32 s68, %19, 4\nQmaaa 104,60,44\ns_lshl_b32 s68, s68, 2\ns_lshl_b32 s69, s65, 3\nQmaaa 108,56,20\n"
"Qta 76,68,16640\ns_add_u32 s68, s68, s69\nQmaaa 108,60,48\ns_lshr_b32 s68, 0xc0004080, s68\ns_and_b32 s68, s68, 255\nQmaaa 112,56,24\ns_mul_i32 s69, s92, 286720\ns_add_u32 s68, s68, s69\n"
"Qmaaa 112,60,52\nQta 78,68,17664\nv_mul_u32_u24 v64, 2560, v148\ns_waitcnt vmcnt(24)\ns_waitcnt lgkmcnt(4)\nQmaav 140,64,84\ns_mov_b32 s31, 393216\nv_add3_u32 v64, v64, v149, s68\n"
"Qmaav 140,68,116\nbuffer_load_dwordx4 a[0:3], v79, s[48:51], s31 offen offset:0\nQmaav 136,64,240\ns_lshl_b32 s68, s93, 4\nv_lshrrev_b32 v148, 6, v60\nQmaav 136,68,248\nv_add_u32 v148, s68, v148\nv_and_b32 v149, 63, v60\n"
"Qmaav 132,64,208\nQta 56,68,8704\nv_and_b32 v150, 32, v60\nQmaav 132,68,216\nbuffer_load_dwordx4 a[4:7], v79, s[48:51], s31 offen offset:1024\nQmaav 128,64,176\nv_lshlrev_b32 v150, 1, v150\nv_mul_u32_u24 v71, 208, v148\n"
"Qmaav 128,68,184\nQta 58,68,9728\nv_add3_u32 v71, v71, v149, s66\nQmaav 124,64,144\nv_add_u32 v71, v71, v150\ns_xor_b32 s65, s65, 1\nQmaav 124,68,152\nbuffer_load_dwordx4 a[8:11], v79, s[48:51], s31 offen offset:2048\n"
"Qmaav 120,64,112\nQta 60,68,16896\ns_lshl_b32 s65, s65, 8\nQmaav 120,68,120\ns_add_u32 s68, s65, s69\nv_mul_u32_u24 v72, 2560, v148\nQmaav 116,64,80\nv_add3_u32 v72, v72, v149, s68\n"
"Qmaav 116,68,88\nQta 62,68,17920\ns_waitcnt lgkmcnt(4)\nQmaav 144,72,80\nbuffer_load_dwordx4 a[12:15], v79, s[48:51], s31 offen offset:3072\nQmaav 144,76,88\nQmaav 148,72,112\nQmaav 148,76,120\n"
"Qmaav 152,72,144\nQta 64,68,8960\ns_mov_b32 s31, 397312\nQmaav 152,76,152\nbuffer_load_dwordx4 a[16:19], v79, s[48:51], s31 offen offset:0\nQmaav 156,72,176\nQmaav 156,76,184\nQta 66,68,9984\n"
"Qmaav 160,72,208\nQmaav 160,76,216\nbuffer_load_dwordx4 a[20:23], v79, s[48:51], s31 offen offset:1024\nQmaav 164,72,240\nQta 68,68,17152\nQmaav 164,76,248\nQmaav 168,72,84\nQmaav 168,76,116\n"
"Qta 70,68,18176\ns_waitcnt lgkmcnt(4)\nQmvav 24,56,84\nbuffer_load_dwordx4 a[24:27], v79, s[48:51], s31 offen offset:2048\nQmvav 24,60,116\nQmvav 20,56,240\nQmvav 20,60,248\nQmvav 16,56,208\n"
"Qta 72,68,24576\ns_mov_b32 s31, 409600\nQmvav 16,60,216\nbuffer_load_dwordx4 a[28:31], v79, s[48:51], s31 offen offset:0\nQmvav 12,56,176\nQmvav 12,60,184\nQta 74,68,25088\nQmvav 8,56,144\n"
"Qmvav 8,60,152\nbuffer_load_dwordx4 a[32:35], v79, s[48:51], s31 offen offset:1024\nQmvav 4,56,112\nQta 76,68,25600\nv_add_u32 v75, 1, v75\nQmvav 4,60,120\ns_waitcnt vmcnt(19)\nds_write_b32 v73, v75\n"
"Qmvav 0,56,80\nQmvav 0,60,88\nQta 78,68,26112\ns_waitcnt lgkmcnt(5)\nQmvav 28,64,80\nbuffer_load_dwordx4 a[36:39], v79, s[48:51], s31 offen offset:2048\nQmvav 28,68,88\nQmvav 32,64,112\n"
"Qmvav 32,68,120\nQmvav 36,64,144\nQta 56,68,24832\nQmvav 36,68,152\nds_read_b32 v66, v74\nQmvav 40,64,176\nbuffer_load_dwordx4 a[40:43], v79, s[48:51], s31 offen offset:3072\nQmvav 40,68,184\n"
"Qta 58,68,25344\nQmvav 44,64,208\nQmvav 44,68,216\ns_waitcnt lgkmcnt(1)\nv_cmp_gt_u32 vcc, v75, v66\nQmvav 48,64,240\ns_cbranch_vccnz .Lso600\n.Lro600:\n"
"Qmvav 48,68,248\nQta 60,68,25856\ns_mov_b32 s31, 413696\nQmvav 52,64,84\nbuffer_load_dwordx4 a[44:47], v79, s[48:51], s31 offen offset:0\nQmvav 52,68,116\ns_waitcnt lgkmcnt(4)\nQmaav 196,72,84\n"
"Qta 62,68,26368\nQmaav 196,76,116\nQmaav 192,72,240\nbuffer_load_dwordx4 a[48:51], v79, s[48:51], s31 offen offset:1024\nQmaav 192,76,248\nQmaav 188,72,208\nQta 64,68,32768\nQmaav 188,76,216\n"
"Qmaav 184,72,176\nbuffer_load_dwordx4 a[52:55], v79, s[48:51], s31 offen offset:2048\nQmaav 184,76,184\nQta 66,68,33792\nQmaav 180,72,144\nQmaav 180,76,152\nQmaav 176,72,112\nQta 68,68,40960\n"
"s_add_u32 m0, s96, 73728\ns_mov_b32 s95, 700416\nQmaav 176,76,120\nbuffer_load_dwordx4 v61, s[52:55], s95 offen lds\nQmaav 172,72,80\nQmaav 172,76,88\nQta 70,68,41984\ns_waitcnt lgkmcnt(4)\n"
"Qmvav 92,56,80\ns_mov_b32 s95, 719872\nQmvav 92,60,88\nbuffer_load_dwordx4 v61, s[52:55], s95 offen offset:1024 lds\nQmvav 124,56,112\nQmvav 124,60,120\nQmvav 156,56,144\nQta 72,68,33024\n"
"s_add_u32 m0, s96, 81920\ns_mov_b32 s95, 659456\nQmvav 156,60,152\nbuffer_load_dwordx4 v62, s[52:55], s95 offen lds\nQmvav 188,56,176\nQmvav 188,60,184\nQta 74,68,34048\nQmvav 220,56,208\n"
"s_mov_b32 s95, 699392\nQmvav 220,60,216\nbuffer_load_dwordx4 v62, s[52:55], s95 offen offset:1024 lds\nQmvav 252,56,240\nQta 76,68,41216\nQmvav 252,60,248\nQmvav 100,56,84\nQmvav 100,60,116\n"
"Qta 78,68,42240\ns_add_u32 m0, s96, 90112\ns_mov_b32 s95, 659456\ns_waitcnt lgkmcnt(4)\nQmaav 224,64,84\nbuffer_load_dwordx4 v63, s[52:55], s95 offen lds\nQmaav 224,68,116\nQmaav 220,64,240\n"
"Qmaav 220,68,248\nQmaav 216,64,208\nQta 56,68,33280\ns_mov_b32 s95, 678912\nQmaav 216,68,216\nbuffer_load_dwordx4 v63, s[52:55], s95 offen offset:1024 lds\nQmaav 212,64,176\nQmaav 212,68,184\n"
"Qta 58,68,34304\nQmaav 208,64,144\ns_add_u32 m0, s96, 98304\ns_mov_b32 s95, 700416\nQmaav 208,68,152\nbuffer_load_dwordx4 v63, s[52:55], s95 offen lds\nQmaav 204,64,112\nQta 60,68,41472\n"
"Qmaav 204,68,120\nQmaav 200,64,80\nQmaav 200,68,88\nQta 62,68,42496\ns_waitcnt lgkmcnt(4)\nQmaav 228,72,80\nQmaav 228,76,88\ns_add_u32 s88, s71, 147456\n"
"Qmaav 232,72,112\nbuffer_store_dwordx4 v[104:107], v79, s[56:59], s88 offen offset:0 sc1\nQmaav 232,76,120\nQmaav 236,72,144\nQta 64,68,33536\nQmaav 236,76,152\nQmaav 240,72,176\nQmaav 240,76,184\n"
"Qta 66,68,34560\nQmaav 244,72,208\nQmaav 244,76,216\nQmaav 248,72,240\nQta 68,68,41728\nQmaav 248,76,248\nQmaav 252,72,84\nQmaav 252,76,116\n"
"Qta 70,68,42752\ns_waitcnt lgkmcnt(4)\nQmvav 172,56,84\nbuffer_store_dwordx4 v[136:139], v79, s[56:59], s88 offen offset:1024 sc1\nQmvav 172,60,116\nQmvav 140,56,240\nQmvav 140,60,248\nQmvav 108,56,208\n"
"Qta 72,68,49152\nQmvav 108,60,216\nQmvav 228,56,176\nQmvav 228,60,184\nQta 74,68,50176\nQmvav 196,56,144\nQmvav 196,60,152\nQmvav 164,56,112\n"
"Qta 76,68,57344\ns_mov_b32 s95, 719872\nQmvav 164,60,120\nbuffer_load_dwordx4 v63, s[52:55], s95 offen offset:1024 lds\nQmvav 132,56,80\nQmvav 132,60,88\nQta 78,68,58368\ns_waitcnt lgkmcnt(4)\n"
"Qmvav 204,64,80\ns_mov_b32 s31, 524288\nQmvav 204,68,88\nbuffer_load_dwordx4 v[80:83], v79, s[48:51], s31 offen offset:0\nQmvav 236,64,112\nQmvav 236,68,120\nQmaav 96,64,144\nQta 56,68,49408\n"
"Qmaav 96,68,152\nbuffer_load_dwordx4 v[112:115], v79, s[48:51], s31 offen offset:1024\nQmaav 100,64,176\nQmaav 100,68,184\nQta 58,68,50432\nQmaav 104,64,208\nQmaav 104,68,216\nbuffer_load_dwordx4 v[144:147], v79, s[48:51], s31 offen offset:2048\n"
"Qmaav 108,64,240\nQta 60,68,57600\nQmaav 108,68,248\nQmaav 112,64,84\nQmaav 112,68,116\nQta 62,68,58624\ns_waitcnt vmcnt(13)\ns_waitcnt lgkmcnt(4)\n"
"Qmaaa 140,72,24\nbuffer_load_dwordx4 v[176:179], v79, s[48:51], s31 offen offset:3072\nQmaaa 140,76,52\nQmaaa 136,72,20\nQmaaa 136,76,48\nQmaaa 132,72,16\nQta 64,68,49664\ns_mov_b32 s31, 528384\n"
"Qmaaa 132,76,44\nbuffer_load_dwordx4 v[208:211], v79, s[48:51], s31 offen offset:0\nQmaaa 128,72,12\nQmaaa 128,76,40\nQta 66,68,50688\nQmaaa 124,72,8\nQmaaa 124,76,36\nbuffer_load_dwordx4 v[240:243], v79, s[48:51], s31 offen offset:1024\n"
"Qmaaa 120,72,4\nQta 68,68,57856\nQmaaa 120,76,32\nQmaaa 116,72,0\nQmaaa 116,76,28\nQta 70,68,58880\ns_waitcnt lgkmcnt(4)\nQmaaa 144,56,0\n"
"buffer_load_dwordx4 v[84:87], v79, s[48:51], s31 offen offset:2048\nQmaaa 144,60,28\nQmaaa 148,56,4\nQmaaa 148,60,32\nQmaaa 152,56,8\nQta 72,68,49920\ns_mov_b32 s31, 540672\nQmaaa 152,60,36\n"
"buffer_load_dwordx4 v[88:91], v79, s[48:51], s31 offen offset:0\nQmaaa 156,56,12\nQmaaa 156,60,40\nQta 74,68,50944\nQmaaa 160,56,16\nQmaaa 160,60,44\nbuffer_load_dwordx4 v[120:123], v79, s[48:51], s31 offen offset:1024\nQmaaa 164,56,20\n"
"Qta 76,68,58112\nQmaaa 164,60,48\nQmaaa 168,56,24\nQmaaa 168,60,52\nQta 78,68,59136\ns_waitcnt lgkmcnt(4)\nQmvaa 24,64,24\nbuffer_load_dwordx4 v[152:155], v79, s[48:51], s31 offen offset:2048\n"
"Qmvaa 24,68,52\nQmvaa 20,64,20\nQmvaa 20,68,48\nQmvaa 16,64,16\nQta 56,70,1024\nQmvaa 16,68,44\nbuffer_load_dwordx4 v[184:187], v79, s[48:51], s31 offen offset:3072\nQmvaa 12,64,12\n"
"Qmvaa 12,68,40\nQta 58,70,1536\nQmvaa 8,64,8\ns_mov_b32 s31, 544768\nQmvaa 8,68,36\nbuffer_load_dwordx4 v[216:219], v79, s[48:51], s31 offen offset:0\nQmvaa 4,64,4\nQta 60,70,2048\n"
"v_add_u32 v75, 1, v75\nQmvaa 4,68,32\ns_waitcnt vmcnt(18)\nds_write_b32 v73, v75\nQmvaa 0,64,0\nQmvaa 0,68,28\nQta 62,70,2560\ns_waitcnt lgkmcnt(5)\n"
"Qmvaa 28,72,0\nbuffer_load_dwordx4 v[248:251], v79, s[48:51], s31 offen offset:1024\nQmvaa 28,76,28\nQmvaa 32,72,4\nQmvaa 32,76,32\nQmvaa 36,72,8\nQta 64,70,1280\nQmvaa 36,76,36\n"
"ds_read_b32 v66, v74\nQmvaa 40,72,12\nbuffer_load_dwordx4 v[116:119], v79, s[48:51], s31 offen offset:2048\nQmvaa 40,76,40\nQta 66,70,1792\nQmvaa 44,72,16\nQmvaa 44,76,44\ns_waitcnt lgkmcnt(1)\n"
"v_cmp_gt_u32 vcc, v75, v66\nQmvaa 48,72,20\ns_cbranch_vccnz .Lso740\n.Lro740:\nQmvaa 48,76,48\nQta 68,70,2304\ns_add_u32 m0, s96, 106496\ns_mov_b32 s95, 741376\n"
"Qmvaa 52,72,24\nbuffer_load_dwordx4 v61, s[52:55], s95 offen lds\nQmvaa 52,76,52\ns_waitcnt lgkmcnt(4)\nQmaaa 196,56,24\nQta 70,70,2816\nQmaaa 196,60,52\ns_mov_b32 s95, 760832\n"
"Qmaaa 192,56,20\nbuffer_load_dwordx4 v61, s[52:55], s95 offen offset:1024 lds\nQmaaa 192,60,48\nQmaaa 188,56,16\nQta 72,70,9216\nQmaaa 188,60,44\ns_add_u32 m0, s96, 114688\ns_mov_b32 s95, 782336\n"
"Qmaaa 184,56,12\nbuffer_load_dwordx4 v61, s[52:55], s95 offen lds\nQmaaa 184,60,40\nQta 74,70,10240\nQmaaa 180,56,8\nQmaaa 180,60,36\nQmaaa 176,56,4\nQta 76,70,17408\n"
"s_mov_b32 s95, 801792\nQmaaa 176,60,32\nbuffer_load_dwordx4 v61, s[52:55], s95 offen offset:1024 lds\nQmaaa 172,56,0\nQmaaa 172,60,28\nQta 78,70,18432\ns_waitcnt lgkmcnt(4)\nQmvaa 92,64,0\n"
"s_add_u32 m0, s96, 122880\ns_mov_b32 s95, 741376\nQmvaa 92,68,28\nbuffer_load_dwordx4 v62, s[52:55], s95 offen lds\nQmvaa 124,64,4\nQmvaa 124,68,32\nQmvaa 156,64,8\nQta 56,70,9472\n"
"s_mov_b32 s95, 781312\nQmvaa 156,68,36\nbuffer_load_dwordx4 v62, s[52:55], s95 offen offset:1024 lds\nQmvaa 188,64,12\nQmvaa 188,68,40\nQta 58,70,10496\nQmvaa 220,64,16\ns_add_u32 m0, s96, 0\n"
"s_mov_b32 s95, 741376\nQmvaa 220,68,44\nbuffer_load_dwordx4 v63, s[52:55], s95 offen lds\nQmvaa 252,64,20\nQta 60,70,17664\nQmvaa 252,68,48\nQmvaa 100,64,24\nQmvaa 100,68,52\n"
"Qta 62,70,18688\ns_mov_b32 s95, 760832\ns_waitcnt lgkmcnt(4)\nQmaaa 224,72,24\nbuffer_load_dwordx4 v63, s[52:55], s95 offen offset:1024 lds\nQmaaa 224,76,52\nQmaaa 220,72,20\nQmaaa 220,76,48\n"
"Qmaaa 216,72,16\nQta 64,70,9728\ns_add_u32 m0, s96, 8192\ns_mov_b32 s95, 782336\nQmaaa 216,76,44\nbuffer_load_dwordx4 v63, s[52:55], s95 offen lds\nQmaaa 212,72,12\nQmaaa 212,76,40\n"
"Qta 66,70,10752\nQmaaa 208,72,8\ns_mov_b32 s95, 801792\nQmaaa 208,76,36\nbuffer_load_dwordx4 v63, s[52:55], s95 offen offset:1024 lds\nQmaaa 204,72,4\n.Lpg16:\ns_cbranch_execz .Lpg20\n"
"Qta 68,70,17920\nQmaaa 204,76,32\nQmaaa 200,72,0\nQmaaa 200,76,28\nQta 70,70,18944\ns_waitcnt lgkmcnt(4)\nQmaaa 228,56,0\nQmaaa 228,60,28\n"
"Qmaaa 232,56,4\nbuffer_store_dwordx4 v[168:171], v79, s[56:59], s88 offen offset:2048 sc1\nQmaaa 232,60,32\nQmaaa 236,56,8\nQta 72,70,9984\nQmaaa 236,60,36\nQmaaa 240,56,12\nQmaaa 240,60,40\n"
"Qta 74,70,11008\nQmaaa 244,56,16\nQmaaa 244,60,44\nQmaaa 248,56,20\nQta 76,70,18176\nQmaaa 248,60,48\nQmaaa 252,56,24\nQmaaa 252,60,52\n"
"Qta 78,70,19200\ns_waitcnt lgkmcnt(4)\nQmvaa 172,64,24\nbuffer_store_dwordx4 v[200:203], v79, s[56:59], s88 offen offset:3072 sc1\nQmvaa 172,68,52\nQmvaa 140,64,20\nQmvaa 140,68,48\nQmvaa 108,64,16\n"
"Qta 56,69,0\nQmvaa 108,68,44\nQmvaa 228,64,12\nQmvaa 228,68,40\nQta 58,69,1024\nQmvaa 196,64,8\nQmvaa 196,68,36\nQmvaa 164,64,4\n"
"Qta 60,69,8192\ns_add_u32 m0, s96, 16384\ns_mov_b32 s95, 987136\nQmvaa 164,68,32\nbuffer_load_dwordx4 v61, s[52:55], s95 offen lds\nQmvaa 132,64,0\nQmvaa 132,68,28\nQta 62,69,9216\n"
"s_waitcnt lgkmcnt(4)\nQmvaa 204,72,0\ns_mov_b32 s95, 1006592\nQmvaa 204,76,28\nbuffer_load_dwordx4 v61, s[52:55], s95 offen offset:1024 lds\nQmvaa 236,72,4\nQmvaa 236,76,32\nQmaaa 96,72,8\n"
"Qta 64,69,256\ns_mov_b32 s31, 655360\nQmaaa 96,76,36\nbuffer_load_dwordx4 a[0:3], v79, s[48:51], s31 offen offset:0\nQmaaa 100,72,12\nQmaaa 100,76,40\nQta 66,69,1280\nQmaaa 104,72,16\n"
"Qmaaa 104,76,44\nbuffer_load_dwordx4 a[4:7], v79, s[48:51], s31 offen offset:1024\nQmaaa 108,72,20\nQta 68,69,8448\nQmaaa 108,76,48\nQmaaa 112,72,24\nQmaaa 112,76,52\nQta 70,69,9472\n"
"s_waitcnt vmcnt(16)\ns_waitcnt lgkmcnt(4)\nQmaav 140,56,84\nbuffer_load_dwordx4 a[8:11], v79, s[48:51], s31 offen offset:2048\nQmaav 140,60,116\nQmaav 136,56,240\nQmaav 136,60,248\nQmaav 132,56,208\n"
"Qta 72,69,512\nQmaav 132,60,216\nbuffer_load_dwordx4 a[12:15], v79, s[48:51], s31 offen offset:3072\nQmaav 128,56,176\nQmaav 128,60,184\nQta 74,69,1536\nQmaav 124,56,144\ns_mov_b32 s31, 659456\n"
"Qmaav 124,60,152\nbuffer_load_dwordx4 a[16:19], v79, s[48:51], s31 offen offset:0\nQmaav 120,56,112\nQta 76,69,8704\nQmaav 120,60,120\nQmaav 116,56,80\nQmaav 116,60,88\nQta 78,69,9728\n"
"s_waitcnt lgkmcnt(4)\nQmaav 144,64,80\nbuffer_load_dwordx4 a[20:23], v79, s[48:51], s31 offen offset:1024\nQmaav 144,68,88\nQmaav 148,64,112\nQmaav 148,68,120\nQmaav 152,64,144\nQta 56,69,768\n"
"Qmaav 152,68,152\nbuffer_load_dwordx4 a[24:27], v79, s[48:51], s31 offen offset:2048\nQmaav 156,64,176\nQmaav 156,68,184\nQta 58,69,1792\nQmaav 160,64,208\ns_mov_b32 s31, 671744\nQmaav 160,68,216\n"
"buffer_load_dwordx4 a[28:31], v79, s[48:51], s31 offen offset:0\nQmaav 164,64,240\nQta 60,69,8960\nQmaav 164,68,248\nQmaav 168,64,84\nQmaav 168,68,116\nQta 62,69,9984\ns_waitcnt lgkmcnt(4)\n"
"Qmvav 24,72,84\nbuffer_load_dwordx4 a[32:35], v79, s[48:51], s31 offen offset:1024\nQmvav 24,76,116\nQmvav 20,72,240\nQmvav 20,76,248\nQmvav 16,72,208\nQta 64,69,16384\nQmvav 16,76,216\n"
"buffer_load_dwordx4 a[36:39], v79, s[48:51], s31 offen offset:2048\nQmvav 12,72,176\nQmvav 12,76,184\nQta 66,69,16896\nQmvav 8,72,144\nQmvav 8,76,152\nbuffer_load_dwordx4 a[40:43], v79, s[48:51], s31 offen offset:3072\nQmvav 4,72,112\n"
"Qta 68,69,17408\nv_add_u32 v75, 1, v75\nQmvav 4,76,120\ns_waitcnt vmcnt(19)\nds_write_b32 v73, v75\nQmvav 0,72,80\nQmvav 0,76,88\nQta 70,69,17920\n"
"s_mov_b32 s31, 675840\ns_waitcnt lgkmcnt(5)\nQmvav 28,56,80\nbuffer_load_dwordx4 a[44:47], v79, s[48:51], s31 offen offset:0\nQmvav 28,60,88\nQmvav 32,56,112\nQmvav 32,60,120\nQmvav 36,56,144\n"
"Qta 72,69,16640\nQmvav 36,60,152\nds_read_b32 v66, v74\nQmvav 40,56,176\nbuffer_load_dwordx4 a[48:51], v79, s[48:51], s31 offen offset:1024\nQmvav 40,60,184\nQta 74,69,17152\nQmvav 44,56,208\n"
"Qmvav 44,60,216\ns_waitcnt lgkmcnt(1)\nv_cmp_gt_u32 vcc, v75, v66\nQmvav 48,56,240\ns_cbranch_vccnz .Lso880\n.Lro880:\nQmvav 48,60,248\nQta 76,69,17664\n"
"Qmvav 52,56,84\nbuffer_load_dwordx4 a[52:55], v79, s[48:51], s31 offen offset:2048\nQmvav 52,60,116\ns_waitcnt lgkmcnt(4)\nQmaav 196,64,84\nQta 78,69,18176\nQmaav 196,68,116\ns_add_u32 m0, s96, 24576\n"
"s_mov_b32 s95, 1028096\nQmaav 192,64,240\nbuffer_load_dwordx4 v61, s[52:55], s95 offen lds\nQmaav 192,68,248\nQmaav 188,64,208\nQta 56,69,24576\nQmaav 188,68,216\ns_mov_b32 s95, 1047552\n"
"Qmaav 184,64,176\nbuffer_load_dwordx4 v61, s[52:55], s95 offen offset:1024 lds\nQmaav 184,68,184\nQta 58,69,25600\nQmaav 180,64,144\nQmaav 180,68,152\nQmaav 176,64,112\nQta 60,69,32768\n"
"s_add_u32 m0, s96, 32768\ns_mov_b32 s95, 987136\nQmaav 176,68,120\nbuffer_load_dwordx4 v62, s[52:55], s95 offen lds\nQmaav 172,64,80\nQmaav 172,68,88\nQta 62,69,33792\ns_waitcnt lgkmcnt(4)\n"
"Qmvav 92,72,80\ns_mov_b32 s95, 1027072\nQmvav 92,76,88\nbuffer_load_dwordx4 v62, s[52:55], s95 offen offset:1024 lds\nQmvav 124,72,112\nQmvav 124,76,120\nQmvav 156,72,144\nQta 64,69,24832\n"
"s_add_u32 m0, s96, 40960\ns_mov_b32 s95, 987136\nQmvav 156,76,152\nbuffer_load_dwordx4 v63, s[52:55], s95 offen lds\nQmvav 188,72,176\nQmvav 188,76,184\nQta 66,69,25856\nQmvav 220,72,208\n"
"s_mov_b32 s95, 1006592\nQmvav 220,76,216\nbuffer_load_dwordx4 v63, s[52:55], s95 offen offset:1024 lds\nQmvav 252,72,240\nQta 68,69,33024\nQmvav 252,76,248\nQmvav 100,72,84\nQmvav 100,76,116\n"
"Qta 70,69,34048\ns_add_u32 m0, s96, 49152\ns_mov_b32 s95, 1028096\ns_waitcnt lgkmcnt(4)\nQmaav 224,56,84\nbuffer_load_dwordx4 v63, s[52:55], s95 offen lds\nQmaav 224,60,116\nQmaav 220,56,240\n"
"Qmaav 220,60,248\nQmaav 216,56,208\nQta 72,69,25088\ns_mov_b32 s95, 1047552\nQmaav 216,60,216\nbuffer_load_dwordx4 v63, s[52:55], s95 offen offset:1024 lds\nQmaav 212,56,176\nQmaav 212,60,184\n"
"Qta 74,69,26112\nQmaav 208,56,144\ns_add_u32 m0, s96, 57344\ns_mov_b32 s95, 1069056\nQmaav 208,60,152\nbuffer_load_dwordx4 v61, s[52:55], s95 offen lds\nQmaav 204,56,112\nQta 76,69,33280\n"
"Qmaav 204,60,120\nQmaav 200,56,80\nQmaav 200,60,88\nQta 78,69,34304\ns_waitcnt lgkmcnt(4)\nQmaav 228,64,80\nQmaav 228,68,88\ns_add_u32 s88, s71, 151552\n"
"Qmaav 232,64,112\nbuffer_store_dwordx4 v[232:235], v79, s[56:59], s88 offen offset:0 sc1\nQmaav 232,68,120\nQmaav 236,64,144\nQta 56,69,25344\nQmaav 236,68,152\nQmaav 240,64,176\nQmaav 240,68,184\n"
"Qta 58,69,26368\nQmaav 244,64,208\nQmaav 244,68,216\nQmaav 248,64,240\nQta 60,69,33536\nQmaav 248,68,248\nQmaav 252,64,84\nQmaav 252,68,116\n"
"Qta 62,69,34560\ns_waitcnt lgkmcnt(4)\nQmvav 172,72,84\nbuffer_store_dwordx4 v[212:215], v79, s[56:59], s88 offen offset:1024 sc1\nQmvav 172,76,116\nQmvav 140,72,240\nQmvav 140,76,248\nQmvav 108,72,208\n"
"Qta 64,69,40960\nQmvav 108,76,216\nQmvav 228,72,176\nQmvav 228,76,184\nQta 66,69,41984\nQmvav 196,72,144\nQmvav 196,76,152\nQmvav 164,72,112\n"
"Qta 68,69,49152\ns_mov_b32 s95, 1088512\nQmvav 164,76,120\nbuffer_load_dwordx4 v61, s[52:55], s95 offen offset:1024 lds\nQmvav 132,72,80\nQmvav 132,76,88\nQta 70,69,50176\ns_waitcnt lgkmcnt(4)\n"
"Qmvav 204,56,80\ns_mov_b32 s31, 786432\nQmvav 204,60,88\nbuffer_load_dwordx4 v[80:83], v79, s[48:51], s31 offen offset:0\nQmvav 236,56,112\nQmvav 236,60,120\nQmaav 96,56,144\nQta 72,69,41216\n"
"Qmaav 96,60,152\nbuffer_load_dwordx4 v[112:115], v79, s[48:51], s31 offen offset:1024\nQmaav 100,56,176\nQmaav 100,60,184\nQta 74,69,42240\nQmaav 104,56,208\nQmaav 104,60,216\nbuffer_load_dwordx4 v[144:147], v79, s[48:51], s31 offen offset:2048\n"
"Qmaav 108,56,240\nQta 76,69,49408\nQmaav 108,60,248\nQmaav 112,56,84\nv_accvgpr_read_b32 v96, a96\nv_accvgpr_read_b32 v97, a97\nQmaav 112,60,116\nQta 78,69,50432\n"
"v_accvgpr_read_b32 v98, a98\ns_waitcnt vmcnt(15)\ns_waitcnt lgkmcnt(4)\nQmaaa 140,64,24\nbuffer_load_dwordx4 v[176:179], v79, s[48:51], s31 offen offset:3072\nQmaaa 140,68,52\nv_accvgpr_read_b32 v99, a99\nv_accvgpr_read_b32 v128, a100\n"
"Qmaaa 136,64,20\nv_accvgpr_read_b32 v129, a101\nv_accvgpr_read_b32 v130, a102\nQmaaa 136,68,48\nv_accvgpr_read_b32 v131, a103\nv_accvgpr_read_b32 v160, a104\nQmaaa 132,64,16\nQta 56,69,41472\n"
"s_mov_b32 s31, 790528\nQmaaa 132,68,44\nbuffer_load_dwordx4 v[208:211], v79, s[48:51], s31 offen offset:0\nQmaaa 128,64,12\nv_accvgpr_read_b32 v161, a105\nv_accvgpr_read_b32 v162, a106\nQmaaa 128,68,40\nQta 58,69,42496\n"
"v_accvgpr_read_b32 v163, a107\nQmaaa 124,64,8\nv_accvgpr_read_b32 v192, a108\nv_accvgpr_read_b32 v193, a109\nQmaaa 124,68,36\nbuffer_load_dwordx4 v[240:243], v79, s[48:51], s31 offen offset:1024\nQmaaa 120,64,4\nQta 60,69,49664\n"
"v_accvgpr_read_b32 v194, a110\nQmaaa 120,68,32\nv_accvgpr_read_b32 v195, a111\nv_accvgpr_read_b32 v224, a112\nQmaaa 116,64,0\nv_accvgpr_read_b32 v225, a113\nv_accvgpr_read_b32 v226, a114\nQmaaa 116,68,28\n"
"Qta 62,69,50688\nv_accvgpr_read_b32 v227, a115\ns_waitcnt lgkmcnt(4)\nQmaaa 144,72,0\nbuffer_load_dwordx4 v[84:87], v79, s[48:51], s31 offen offset:2048\nQmaaa 144,76,28\nQmaaa 148,72,4\nQmaaa 148,76,32\n"
"Qmaaa 152,72,8\nQta 64,69,41728\ns_mov_b32 s31, 802816\nQmaaa 152,76,36\nbuffer_load_dwordx4 v[88:91], v79, s[48:51], s31 offen offset:0\nQmaaa 156,72,12\nQmaaa 156,76,40\nQta 66,69,42752\n"
"Qmaaa 160,72,16\nQmaaa 160,76,44\nbuffer_load_dwordx4 v[120:123], v79, s[48:51], s31 offen offset:1024\nQmaaa 164,72,20\nQta 68,69,49920\nQmaaa 164,76,48\nQmaaa 168,72,24\nQmaaa 168,76,52\n"
"Qta 70,69,50944\ns_waitcnt lgkmcnt(4)\nQmvaa 24,56,24\nbuffer_load_dwordx4 v[152:155], v79, s[48:51], s31 offen offset:2048\nQmvaa 24,60,52\nQmvaa 20,56,20\nQmvaa 20,60,48\nQmvaa 16,56,16\n"
"Qta 72,69,57344\nQmvaa 16,60,44\nbuffer_load_dwordx4 v[184:187], v79, s[48:51], s31 offen offset:3072\nQmvaa 12,56,12\nQmvaa 12,60,40\nQta 74,69,57856\nQmvaa 8,56,8\ns_mov_b32 s31, 806912\n"
"Qmvaa 8,60,36\nbuffer_load_dwordx4 v[216:219], v79, s[48:51], s31 offen offset:0\nQmvaa 4,56,4\nQta 76,69,58368\nv_add_u32 v75, 1, v75\nQmvaa 4,60,32\ns_waitcnt vmcnt(20)\nds_write_b32 v73, v75\n"
"Qmvaa 0,56,0\nQmvaa 0,60,28\nQta 78,69,58880\ns_waitcnt lgkmcnt(5)\nQmvaa 28,64,0\nbuffer_load_dwordx4 v[248:251], v79, s[48:51], s31 offen offset:1024\nQmvaa 28,68,28\nQmvaa 32,64,4\n"
"Qmvaa 32,68,32\nQmvaa 36,64,8\nQta 56,69,57600\nQmvaa 36,68,36\nds_read_b32 v66, v74\nQmvaa 40,64,12\nbuffer_load_dwordx4 v[116:119], v79, s[48:51], s31 offen offset:2048\nQmvaa 40,68,40\n"
"Qta 58,69,58112\nQmvaa 44,64,16\nQmvaa 44,68,44\ns_waitcnt lgkmcnt(1)\nv_cmp_gt_u32 vcc, v75, v66\nQmvaa 48,64,20\ns_cbranch_vccnz .Lso1020\n.Lro1020:\n"
"Qmvaa 48,68,48\nQta 60,69,58624\ns_add_u32 m0, s96, 132096\ns_mov_b32 s95, 1110016\nQmvaa 52,64,24\nbuffer_load_dwordx4 v61, s[52:55], s95 offen lds\nQmvaa 52,68,52\ns_waitcnt lgkmcnt(4)\n"
"Qmaaa 196,72,24\nQta 62,69,59136\nQmaaa 196,76,52\ns_mov_b32 s95, 1129472\nQmaaa 192,72,20\nbuffer_load_dwordx4 v61, s[52:55], s95 offen offset:1024 lds\nQmaaa 192,76,48\nQmaaa 188,72,16\n"
"Qta 64,68,0\nQmaaa 188,76,44\ns_add_u32 m0, s96, 140288\ns_mov_b32 s95, 1069056\nQmaaa 184,72,12\nbuffer_load_dwordx4 v62, s[52:55], s95 offen lds\nQmaaa 184,76,40\nQta 66,68,1024\n"
"Qmaaa 180,72,8\nQmaaa 180,76,36\nQmaaa 176,72,4\nQta 68,68,8192\ns_mov_b32 s95, 1108992\nQmaaa 176,76,32\nbuffer_load_dwordx4 v62, s[52:55], s95 offen offset:1024 lds\nQmaaa 172,72,0\n"
"Qmaaa 172,76,28\nQta 70,68,9216\ns_waitcnt lgkmcnt(4)\nQmvaa 92,56,0\ns_add_u32 m0, s96, 148480\ns_mov_b32 s95, 1069056\nQmvaa 92,60,28\nbuffer_load_dwordx4 v63, s[52:55], s95 offen lds\n"
"Qmvaa 124,56,4\nQmvaa 124,60,32\nQmvaa 156,56,8\nQta 72,68,256\ns_mov_b32 s95, 1088512\nQmvaa 156,60,36\nbuffer_load_dwordx4 v63, s[52:55], s95 offen offset:1024 lds\nQmvaa 188,56,12\n"
"Qmvaa 188,60,40\nQta 74,68,1280\nQmvaa 220,56,16\ns_add_u32 m0, s96, 65536\ns_mov_b32 s95, 1110016\nQmvaa 220,60,44\nbuffer_load_dwordx4 v63, s[52:55], s95 offen lds\nQmvaa 252,56,20\n"
"Qta 76,68,8448\nQmvaa 252,60,48\nQmvaa 100,56,24\nQmvaa 100,60,52\nQta 78,68,9472\ns_mov_b32 s95, 1129472\ns_waitcnt lgkmcnt(4)\nQmaaa 224,64,24\n"
"buffer_load_dwordx4 v63, s[52:55], s95 offen offset:1024 lds\nQmaaa 224,68,52\nQmaaa 220,64,20\nQmaaa 220,68,48\nQmaaa 216,64,16\nQta 56,68,512\ns_add_u32 m0, s96, 73728\ns_add_u32 s95, s67, 4096\n"
"Qmaaa 216,68,44\nbuffer_load_dwordx4 v61, s[52:55], s95 offen lds\nQmaaa 212,64,12\nQmaaa 212,68,40\nQta 58,68,1536\nQmaaa 208,64,8\ns_add_u32 s95, s67, 23552\nQmaaa 208,68,36\n"
"buffer_load_dwordx4 v61, s[52:55], s95 offen offset:1024 lds\nQmaaa 204,64,4\nQta 60,68,8704\nQmaaa 204,68,32\nQmaaa 200,64,0\nQmaaa 200,68,28\nQta 62,68,9728\ns_waitcnt lgkmcnt(4)\n"
"Qmaaa 228,72,0\nQmaaa 228,76,28\nQmaaa 232,72,4\nbuffer_store_dwordx4 v[244:247], v79, s[56:59], s88 offen offset:2048 sc1\nQmaaa 232,76,32\nQmaaa 236,72,8\nQta 64,68,768\nQmaaa 236,76,36\n"
"Qmaaa 240,72,12\nQmaaa 240,76,40\n.Lpg17:\ns_cbranch_execz .Lpg21\nQta 66,68,1792\nQmaaa 244,72,16\nQmaaa 244,76,44\nQmaaa 248,72,20\n"
"Qta 68,68,8960\nQmaaa 248,76,48\ns_add_u32 m0, s96, 81920\ns_add_u32 s95, s67, 45056\nQmaaa 252,72,24\nbuffer_load_dwordx4 v61, s[52:55], s95 offen lds\nQmaaa 252,76,52\nQta 70,68,9984\n"
"s_waitcnt lgkmcnt(4)\nQmvaa 172,56,24\nQmvaa 172,60,52\ns_add_u32 s95, s67, 64512\nQmvaa 140,56,20\nbuffer_load_dwordx4 v61, s[52:55], s95 offen offset:1024 lds\nQmvaa 140,60,48\nQmvaa 108,56,16\n"
"Qta 72,68,16384\nQmvaa 108,60,44\ns_add_u32 m0, s96, 90112\ns_add_u32 s95, s67, 4096\nQmvaa 228,56,12\nbuffer_load_dwordx4 v62, s[52:55], s95 offen lds\nQmvaa 228,60,40\nQta 74,68,17408\n"
"Qmvaa 196,56,8\nQmvaa 196,60,36\nQmvaa 164,56,4\nQta 76,68,24576\ns_add_u32 s95, s67, 44032\nQmvaa 164,60,32\nbuffer_load_dwordx4 v62, s[52:55], s95 offen offset:1024 lds\nQmvaa 132,56,0\n"
"Qmvaa 132,60,28\nQta 78,68,25600\ns_waitcnt lgkmcnt(4)\nQmvaa 204,64,0\nbuffer_load_dword v78, v76, s[24:27], 0 offen sc1\ns_mov_b32 s31, 917504\nQmvaa 204,68,28\nbuffer_load_dwordx4 a[0:3], v79, s[48:51], s31 offen offset:0\n"
"Qmvaa 236,64,4\nQmvaa 236,68,32\nQmvaa 96,64,8\nQta 56,68,16640\nQmvaa 96,68,36\nbuffer_load_dwordx4 a[4:7], v79, s[48:51], s31 offen offset:1024\nQmvaa 128,64,12\nQmvaa 128,68,40\n"
"Qta 58,68,17664\nQmvaa 160,64,16\nQmvaa 160,68,44\nbuffer_load_dwordx4 a[8:11], v79, s[48:51], s31 offen offset:2048\nQmvaa 192,64,20\nQta 60,68,24832\nQmvaa 192,68,48\nQmvaa 224,64,24\n"
"Qmvaa 224,68,52\nQta 62,68,25856\ns_waitcnt vmcnt(19)\ns_waitcnt lgkmcnt(4)\nQmaav 140,72,84\nbuffer_load_dwordx4 a[12:15], v79, s[48:51], s31 offen offset:3072\nQmaav 140,76,116\nQmaav 136,72,240\n"
"Qmaav 136,76,248\nQmaav 132,72,208\nQta 64,68,16896\ns_mov_b32 s31, 921600\nQmaav 132,76,216\nbuffer_load_dwordx4 a[16:19], v79, s[48:51], s31 offen offset:0\nQmaav 128,72,176\nQmaav 128,76,184\n"
"Qta 66,68,17920\nQmaav 124,72,144\nQmaav 124,76,152\nbuffer_load_dwordx4 a[20:23], v79, s[48:51], s31 offen offset:1024\nQmaav 120,72,112\nQta 68,68,25088\nQmaav 120,76,120\nQmaav 116,72,80\n"
"Qmaav 116,76,88\nQta 70,68,26112\ns_waitcnt lgkmcnt(4)\nQmaav 144,56,80\nbuffer_load_dwordx4 a[24:27], v79, s[48:51], s31 offen offset:2048\nQmaav 144,60,88\nQmaav 148,56,112\nQmaav 148,60,120\n"
"Qmaav 152,56,144\nQta 72,68,17152\ns_mov_b32 s31, 933888\nQmaav 152,60,152\nbuffer_load_dwordx4 a[28:31], v79, s[48:51], s31 offen offset:0\nQmaav 156,56,176\nQmaav 156,60,184\nQta 74,68,18176\n"
"Qmaav 160,56,208\nQmaav 160,60,216\nbuffer_load_dwordx4 a[32:35], v79, s[48:51], s31 offen offset:1024\nQmaav 164,56,240\nQta 76,68,25344\nQmaav 164,60,248\nQmaav 168,56,84\nQmaav 168,60,116\n"
"Qta 78,68,26368\ns_waitcnt lgkmcnt(4)\nQmvav 24,64,84\nbuffer_load_dwordx4 a[36:39], v79, s[48:51], s31 offen offset:2048\nQmvav 24,68,116\nQmvav 20,64,240\nQmvav 20,68,248\nQmvav 16,64,208\n"
"Qta 56,68,32768\nQmvav 16,68,216\nbuffer_load_dwordx4 a[40:43], v79, s[48:51], s31 offen offset:3072\nQmvav 12,64,176\nQmvav 12,68,184\nQta 58,68,33280\nQmvav 8,64,144\ns_mov_b32 s31, 937984\n"
"Qmvav 8,68,152\nbuffer_load_dwordx4 a[44:47], v79, s[48:51], s31 offen offset:0\nQmvav 4,64,112\nQta 60,68,33792\nv_add_u32 v75, 1, v75\nQmvav 4,68,120\ns_waitcnt vmcnt(24)\nds_write_b32 v73, v75\n"
"Qmvav 0,64,80\nQmvav 0,68,88\nQta 62,68,34304\ns_waitcnt lgkmcnt(5)\nQmvav 28,72,80\nbuffer_load_dwordx4 a[48:51], v79, s[48:51], s31 offen offset:1024\nQmvav 28,76,88\nQmvav 32,72,112\n"
"Qmvav 32,76,120\ns_waitcnt vmcnt(13)\nv_sub_u32 v66, v78, v65\nv_cmp_gt_u32 vcc, 2, v66\ns_and_b64 vcc, vcc, s[28:29]\ns_cbranch_scc1 .Lf2_w\n.Lf2_r:\nQmvav 36,72,144\n"
"Qta 64,68,33024\nQmvav 36,76,152\nds_read_b32 v66, v74\nQmvav 40,72,176\nbuffer_load_dwordx4 a[52:55], v79, s[48:51], s31 offen offset:2048\nQmvav 40,76,184\nQta 66,68,33536\nQmvav 44,72,208\n"
"Qmvav 44,76,216\ns_waitcnt lgkmcnt(1)\nv_cmp_gt_u32 vcc, v75, v66\nQmvav 48,72,240\ns_cbranch_vccnz .Lso1160\n.Lro1160:\nQmvav 48,76,248\nQta 68,68,34048\n"
"s_add_u32 s31, s74, 0\nQmvav 52,72,84\nbuffer_load_dwordx4 a[80:83], v79, s[48:51], s31 offen offset:0 sc1\nQmvav 52,76,116\ns_waitcnt lgkmcnt(4)\nQmaav 196,56,84\nQta 70,68,34560\nQmaav 196,60,116\n"
"Qmaav 192,56,240\nbuffer_load_dwordx4 a[84:87], v79, s[48:51], s31 offen offset:1024 sc1\nQmaav 192,60,248\nQmaav 188,56,208\nQta 72,68,40960\nQmaav 188,60,216\nQmaav 184,56,176\nbuffer_load_dwordx4 a[88:91], v79, s[48:51], s31 offen offset:2048 sc1\n"
"Qmaav 184,60,184\nQta 74,68,41984\nQmaav 180,56,144\nQmaav 180,60,152\nQmaav 176,56,112\nQta 76,68,49152\nQmaav 176,60,120\nbuffer_load_dwordx4 a[92:95], v79, s[48:51], s31 offen offset:3072 sc1\n"
"Qmaav 172,56,80\nQmaav 172,60,88\nQta 78,68,50176\ns_waitcnt lgkmcnt(4)\nQmvav 92,64,80\ns_add_u32 s31, s74, 4096\nQmvav 92,68,88\nbuffer_load_dwordx4 a[96:99], v79, s[48:51], s31 offen offset:0 sc1\n"
"Qmvav 124,64,112\nQmvav 124,68,120\nQmvav 156,64,144\nQta 56,68,41216\nQmvav 156,68,152\nbuffer_load_dwordx4 a[100:103], v79, s[48:51], s31 offen offset:1024 sc1\nQmvav 188,64,176\nQmvav 188,68,184\n"
"Qta 58,68,42240\nQmvav 220,64,208\nQmvav 220,68,216\nbuffer_load_dwordx4 a[104:107], v79, s[48:51], s31 offen offset:2048 sc1\nQmvav 252,64,240\nQta 60,68,49408\nQmvav 252,68,248\nQmvav 100,64,84\n"
"Qmvav 100,68,116\nQta 62,68,50432\ns_add_u32 s31, s74, 16384\ns_waitcnt lgkmcnt(4)\nQmaav 224,72,84\nbuffer_load_dwordx4 a[108:111], v79, s[48:51], s31 offen offset:0 sc1\nQmaav 224,76,116\nQmaav 220,72,240\n"
"Qmaav 220,76,248\nQmaav 216,72,208\nQta 64,68,41472\nQmaav 216,76,216\nbuffer_load_dwordx4 a[112:115], v79, s[48:51], s31 offen offset:1024 sc1\nQmaav 212,72,176\nQmaav 212,76,184\nQta 66,68,42496\n"
"Qmaav 208,72,144\nQmaav 208,76,152\nbuffer_load_dwordx4 v[168:171], v79, s[48:51], s31 offen offset:2048 sc1\nQmaav 204,72,112\nQta 68,68,49664\nQmaav 204,76,120\nQmaav 200,72,80\nQmaav 200,76,88\n"
"Qta 70,68,50688\ns_waitcnt lgkmcnt(4)\nQmaav 228,56,80\nbuffer_load_dwordx4 v[200:203], v79, s[48:51], s31 offen offset:3072 sc1\nQmaav 228,60,88\nQmaav 232,56,112\nQmaav 232,60,120\nQmaav 236,56,144\n"
"Qta 72,68,41728\ns_add_u32 s31, s74, 20480\nQmaav 236,60,152\nbuffer_load_dwordx4 v[232:235], v79, s[48:51], s31 offen offset:0 sc1\nQmaav 240,56,176\nQmaav 240,60,184\nQta 74,68,42752\nQmaav 244,56,208\n"
"Qmaav 244,60,216\nbuffer_load_dwordx4 v[212:215], v79, s[48:51], s31 offen offset:1024 sc1\nQmaav 248,56,240\nQta 76,68,49920\nQmaav 248,60,248\nQmaav 252,56,84\nQmaav 252,60,116\nQta 78,68,50944\n"
"s_waitcnt lgkmcnt(4)\nQmvav 172,64,84\nbuffer_load_dwordx4 v[244:247], v79, s[48:51], s31 offen offset:2048 sc1\nQmvav 172,68,116\nQmvav 140,64,240\nQmvav 140,68,248\nQmvav 108,64,208\nQta 56,68,57344\n"
"s_add_u32 m0, s96, 98304\ns_add_u32 s95, s67, 4096\nQmvav 108,68,216\nbuffer_load_dwordx4 v63, s[52:55], s95 offen lds\nQmvav 228,64,176\nQmvav 228,68,184\nQta 58,68,58368\nQmvav 196,64,144\n"
"s_add_u32 s95, s67, 23552\nQmvav 196,68,152\nbuffer_load_dwordx4 v63, s[52:55], s95 offen offset:1024 lds\nQmvav 164,64,112\nQta 60,70,1024\nQmvav 164,68,120\nQmvav 132,64,80\nQmvav 132,68,88\n"
"Qta 62,70,2048\ns_add_u32 m0, s96, 106496\ns_add_u32 s95, s67, 45056\ns_waitcnt lgkmcnt(4)\nQmvav 204,72,80\nbuffer_load_dwordx4 v63, s[52:55], s95 offen lds\nQmvav 204,76,88\nQmvav 236,72,112\n"
"Qmvav 236,76,120\nQmvav 96,72,144\nQta 64,68,57600\ns_add_u32 s95, s67, 64512\nQmvav 96,76,152\nbuffer_load_dwordx4 v63, s[52:55], s95 offen offset:1024 lds\nQmvav 128,72,176\nQmvav 128,76,184\n"
"Qta 66,68,58624\nQmvav 160,72,208\ns_add_u32 m0, s96, 114688\ns_add_u32 s95, s72, 4096\nQmvav 160,76,216\nbuffer_load_dwordx4 v61, s[52:55], s95 offen lds\nQmvav 192,72,240\nQta 68,70,1280\n"
"Qmvav 192,76,248\nQmvav 224,72,84\nQmvav 224,76,116\nQta 70,70,2304\ns_add_u32 s95, s72, 23552\ns_waitcnt vmcnt(19)\ns_waitcnt lgkmcnt(4)\nQmaaa 140,56,24\n"
"buffer_load_dwordx4 v61, s[52:55], s95 offen offset:1024 lds\nQmaaa 140,60,52\nQmaaa 136,56,20\nQmaaa 136,60,48\nQmaaa 132,56,16\nQta 72,68,57856\ns_add_u32 m0, s96, 122880\ns_add_u32 s95, s72, 45056\n"
"Qmaaa 132,60,44\nbuffer_load_dwordx4 v61, s[52:55], s95 offen lds\nQmaaa 128,56,12\nQmaaa 128,60,40\nQta 74,68,58880\nQmaaa 124,56,8\ns_add_u32 s95, s72, 64512\nQmaaa 124,60,36\n"
"buffer_load_dwordx4 v61, s[52:55], s95 offen offset:1024 lds\nQmaaa 120,56,4\nQta 76,70,1536\nQmaaa 120,60,32\nQmaaa 116,56,0\nQmaaa 116,60,28\nQta 78,70,2560\ns_add_u32 m0, s96, 0\n"
"s_add_u32 s95, s72, 4096\ns_waitcnt lgkmcnt(4)\nQmaaa 144,64,0\nbuffer_load_dwordx4 v62, s[52:55], s95 offen lds\nQmaaa 144,68,28\nQmaaa 148,64,4\nQmaaa 148,68,32\nQmaaa 152,64,8\n"
"Qta 56,68,58112\ns_add_u32 s95, s72, 44032\nQmaaa 152,68,36\nbuffer_load_dwordx4 v62, s[52:55], s95 offen offset:1024 lds\nQmaaa 156,64,12\nQmaaa 156,68,40\nQta 58,68,59136\nQmaaa 160,64,16\n"
"s_add_u32 m0, s96, 8192\ns_add_u32 s95, s72, 4096\nQmaaa 160,68,44\nbuffer_load_dwordx4 v63, s[52:55], s95 offen lds\nQmaaa 164,64,20\nQta 60,70,1792\nQmaaa 164,68,48\nQmaaa 168,64,24\n"
"Qmaaa 168,68,52\nQta 62,70,2816\ns_add_u32 s95, s72, 23552\ns_waitcnt lgkmcnt(4)\nQmvaa 24,72,24\nbuffer_load_dwordx4 v63, s[52:55], s95 offen offset:1024 lds\nQmvaa 24,76,52\nQmvaa 20,72,20\n"
"Qmvaa 20,76,48\nQmvaa 16,72,16\nQta 64,70,9216\ns_add_u32 s31, s75, 0\nQmvaa 16,76,44\nbuffer_load_dwordx4 v[80:83], v79, s[48:51], s31 offen offset:0 sc1\nQmvaa 12,72,12\nQmvaa 12,76,40\n"
"Qta 66,70,9728\nQmvaa 8,72,8\nQmvaa 8,76,36\nbuffer_load_dwordx4 v[112:115], v79, s[48:51], s31 offen offset:1024 sc1\nQmvaa 4,72,4\nQta 68,70,10240\nv_add_u32 v75, 1, v75\nQmvaa 4,76,32\n"
"s_waitcnt vmcnt(43)\nds_write_b32 v73, v75\nQmvaa 0,72,0\nQmvaa 0,76,28\nQta 70,70,10752\ns_waitcnt lgkmcnt(5)\nQmvaa 28,56,0\nbuffer_load_dwordx4 v[144:147], v79, s[48:51], s31 offen offset:2048 sc1\n"
"Qmvaa 28,60,28\nQmvaa 32,56,4\nQmvaa 32,60,32\nQmvaa 36,56,8\nQta 72,70,9472\nQmvaa 36,60,36\nds_read_b32 v66, v74\nQmvaa 40,56,12\n"
"buffer_load_dwordx4 v[176:179], v79, s[48:51], s31 offen offset:3072 sc1\nQmvaa 40,60,40\nQta 74,70,9984\nQmvaa 44,56,16\nv_add_u32 v67, 3, v65\nQmvaa 44,60,44\ns_waitcnt lgkmcnt(1)\nv_cmp_gt_u32 vcc, v75, v66\n"
"Qmvaa 48,56,20\ns_cbranch_vccnz .Lso1300\n.Lro1300:\nQmvaa 48,60,48\nQta 76,70,10496\ns_add_u32 s31, s75, 4096\nQmvaa 52,56,24\nbuffer_load_dwordx4 v[208:211], v79, s[48:51], s31 offen offset:0 sc1\n"
"Qmvaa 52,60,52\nbuffer_store_dword v67, v77, s[24:27], 0 offen sc1\ns_waitcnt lgkmcnt(4)\nQmaaa 196,64,24\nQta 78,70,11008\nQmaaa 196,68,52\nQmaaa 192,64,20\nbuffer_load_dwordx4 v[240:243], v79, s[48:51], s31 offen offset:1024 sc1\n"
"Qmaaa 192,68,48\nQmaaa 188,64,16\nQta 56,70,17408\nQmaaa 188,68,44\nQmaaa 184,64,12\nbuffer_load_dwordx4 v[84:87], v79, s[48:51], s31 offen offset:2048 sc1\nQmaaa 184,68,40\nQta 58,70,18432\n"
"Qmaaa 180,64,8\nQmaaa 180,68,36\nQmaaa 176,64,4\nQta 60,69,0\ns_add_u32 s31, s75, 16384\nQmaaa 176,68,32\nbuffer_load_dwordx4 v[88:91], v79, s[48:51], s31 offen offset:0 sc1\nQmaaa 172,64,0\n"
"Qmaaa 172,68,28\nQta 62,69,1024\ns_waitcnt lgkmcnt(4)\nQmvaa 92,72,0\nQmvaa 92,76,28\nbuffer_load_dwordx4 v[120:123], v79, s[48:51], s31 offen offset:1024 sc1\nQmvaa 124,72,4\nQmvaa 124,76,32\n"
"Qmvaa 156,72,8\nQta 64,70,17664\nQmvaa 156,76,36\nbuffer_load_dwordx4 v[152:155], v79, s[48:51], s31 offen offset:2048 sc1\nQmvaa 188,72,12\nQmvaa 188,76,40\nQta 66,70,18688\nQmvaa 220,72,16\n"
"Qmvaa 220,76,44\nbuffer_load_dwordx4 v[184:187], v79, s[48:51], s31 offen offset:3072 sc1\nQmvaa 252,72,20\nQta 68,69,256\nQmvaa 252,76,48\nQmvaa 100,72,24\nQmvaa 100,76,52\nQta 70,69,1280\n"
"s_add_u32 s31, s75, 20480\ns_waitcnt lgkmcnt(4)\nQmaaa 224,56,24\nbuffer_load_dwordx4 v[216:219], v79, s[48:51], s31 offen offset:0 sc1\nQmaaa 224,60,52\nQmaaa 220,56,20\nQmaaa 220,60,48\nQmaaa 216,56,16\n"
"Qta 72,70,17920\nQmaaa 216,60,44\nbuffer_load_dwordx4 v[248:251], v79, s[48:51], s31 offen offset:1024 sc1\nQmaaa 212,56,12\nQmaaa 212,60,40\nQta 74,70,18944\nQmaaa 208,56,8\nQmaaa 208,60,36\n"
"buffer_load_dwordx4 v[116:119], v79, s[48:51], s31 offen offset:2048 sc1\nQmaaa 204,56,4\nQta 76,69,512\nQmaaa 204,60,32\nQmaaa 200,56,0\nQmaaa 200,60,28\nQta 78,69,1536\ns_add_u32 m0, s96, 16384\n"
"s_add_u32 s95, s72, 45056\ns_waitcnt lgkmcnt(4)\nQmaaa 228,64,0\nbuffer_load_dwordx4 v63, s[52:55], s95 offen lds\nQmaaa 228,68,28\nQmaaa 232,64,4\nQmaaa 232,68,32\nQmaaa 236,64,8\n"
"Qta 56,70,18176\ns_add_u32 s95, s72, 64512\nQmaaa 236,68,36\nbuffer_load_dwordx4 v63, s[52:55], s95 offen offset:1024 lds\nQmaaa 240,64,12\nQmaaa 240,68,40\nQta 58,70,19200\nQmaaa 244,64,16\n"
"s_add_u32 m0, s96, 24576\ns_add_u32 s95, s73, 4096\nQmaaa 244,68,44\nbuffer_load_dwordx4 v61, s[52:55], s95 offen lds\nQmaaa 248,64,20\nQta 60,69,768\nQmaaa 248,68,48\nQmaaa 252,64,24\n"
"Qmaaa 252,68,52\nQta 62,69,1792\ns_add_u32 s95, s73, 23552\ns_waitcnt lgkmcnt(4)\nQmvaa 172,72,24\nbuffer_load_dwordx4 v61, s[52:55], s95 offen offset:1024 lds\nQmvaa 172,76,52\nQmvaa 140,72,20\n"
"Qmvaa 140,76,48\nQmvaa 108,72,16\nQta 64,69,8192\ns_add_u32 m0, s96, 32768\ns_add_u32 s95, s73, 45056\nQmvaa 108,76,44\nbuffer_load_dwordx4 v61, s[52:55], s95 offen lds\nQmvaa 228,72,12\n"
"Qmvaa 228,76,40\nQta 66,69,9216\nQmvaa 196,72,8\n.Lpg18:\ns_cbranch_execz .Lpg22\ns_add_u32 s95, s73, 64512\nQmvaa 196,76,36\nbuffer_load_dwordx4 v61, s[52:55], s95 offen offset:1024 lds\n"
"Qmvaa 164,72,4\nQta 68,69,16384\nQmvaa 164,76,32\nQmvaa 132,72,0\nQmvaa 132,76,28\nQta 70,69,17408\ns_add_u32 m0, s96, 40960\ns_add_u32 s95, s73, 4096\n"
"s_waitcnt lgkmcnt(4)\nQmvaa 204,56,0\nbuffer_load_dwordx4 v62, s[52:55], s95 offen lds\nQmvaa 204,60,28\nQmvaa 236,56,4\nQmvaa 236,60,32\nQmvaa 96,56,8\nQta 72,69,8448\n"
"s_add_u32 s95, s73, 44032\nQmvaa 96,60,36\nbuffer_load_dwordx4 v62, s[52:55], s95 offen offset:1024 lds\nQmvaa 128,56,12\nQmvaa 128,60,40\nQta 74,69,9472\nQmvaa 160,56,16\ns_add_u32 m0, s96, 49152\n"
"s_add_u32 s95, s73, 4096\nQmvaa 160,60,44\nbuffer_load_dwordx4 v63, s[52:55], s95 offen lds\nQmvaa 192,56,20\nQta 76,69,16640\nQmvaa 192,60,48\nQmvaa 224,56,24\nQmvaa 224,60,52\n"
"Qta 78,69,17664\ns_add_u32 s95, s73, 23552\ns_waitcnt vmcnt(36)\ns_waitcnt lgkmcnt(4)\nQmaaa 140,64,104\nbuffer_load_dwordx4 v63, s[52:55], s95 offen offset:1024 lds\nQmaav 140,68,244\nQmaaa 136,64,100\n"
"Qmaav 136,68,212\nQmaaa 132,64,96\nQta 56,69,8704\ns_add_u32 s31, s77, 0\nQmaav 132,68,232\nbuffer_load_dwordx4 a[0:3], v79, s[48:51], s31 offen offset:0 sc1\nQmaaa 128,64,92\nQmaav 128,68,200\n"
"Qta 58,69,9728\nQmaaa 124,64,88\nQmaav 124,68,168\nbuffer_load_dwordx4 a[4:7], v79, s[48:51], s31 offen offset:1024 sc1\nQmaaa 120,64,84\nQta 60,69,16896\nQmaaa 120,68,112\nQmaaa 116,64,80\n"
"Qmaaa 116,68,108\nQta 62,69,17920\ns_waitcnt lgkmcnt(4)\nQmaaa 144,72,80\nbuffer_load_dwordx4 a[8:11], v79, s[48:51], s31 offen offset:2048 sc1\nQmaaa 144,76,108\nQmaaa 148,72,84\nQmaaa 148,76,112\n"
"Qmaaa 152,72,88\nQta 64,69,8960\nQmaav 152,76,168\nbuffer_load_dwordx4 a[12:15], v79, s[48:51], s31 offen offset:3072 sc1\nQmaaa 156,72,92\nQmaav 156,76,200\nQta 66,69,9984\nQmaaa 160,72,96\n"
"s_add_u32 s31, s77, 4096\nQmaav 160,76,232\nbuffer_load_dwordx4 a[16:19], v79, s[48:51], s31 offen offset:0 sc1\nQmaaa 164,72,100\nQta 68,69,17152\nQmaav 164,76,212\nQmaaa 168,72,104\nQmaav 168,76,244\n"
"Qta 70,69,18176\ns_waitcnt lgkmcnt(4)\nQmvaa 24,56,104\nbuffer_load_dwordx4 a[20:23], v79, s[48:51], s31 offen offset:1024 sc1\nQmvav 24,60,244\nQmvaa 20,56,100\nQmvav 20,60,212\nQmvaa 16,56,96\n"
"Qta 72,69,24576\nQmvav 16,60,232\nbuffer_load_dwordx4 a[24:27], v79, s[48:51], s31 offen offset:2048 sc1\nQmvaa 12,56,92\nQmvav 12,60,200\nQta 74,69,25088\nQmvaa 8,56,88\ns_add_u32 s31, s77, 16384\n"
"Qmvav 8,60,168\nbuffer_load_dwordx4 a[28:31], v79, s[48:51], s31 offen offset:0 sc1\nQmvaa 4,56,84\nQta 76,69,25600\nv_add_u32 v75, 1, v75\nQmvaa 4,60,112\ns_waitcnt vmcnt(43)\nds_write_b32 v73, v75\n"
"Qmvaa 0,56,80\nQmvaa 0,60,108\nQta 78,69,26112\ns_waitcnt lgkmcnt(5)\nQmvaa 28,64,80\nbuffer_load_dwordx4 a[32:35], v79, s[48:51], s31 offen offset:1024 sc1\nQmvaa 28,68,108\nQmvaa 32,64,84\n"
"Qmvaa 32,68,112\nQmvaa 36,64,88\nQta 56,69,24832\nQmvav 36,68,168\nds_read_b32 v66, v74\nQmvaa 40,64,92\nbuffer_load_dwordx4 a[36:39], v79, s[48:51], s31 offen offset:2048 sc1\nQmvav 40,68,200\n"
"Qta 58,69,25344\nQmvaa 44,64,96\nQmvav 44,68,232\ns_waitcnt lgkmcnt(1)\nv_cmp_gt_u32 vcc, v75, v66\nQmvaa 48,64,100\ns_cbranch_vccnz .Lso1440\n.Lro1440:\n"
"Qmvav 48,68,212\nQta 60,69,25856\nv_add_u32 v75, 1, v75\nQmvaa 52,64,104\ns_waitcnt vmcnt(39)\nds_write_b32 v73, v75\nQmvav 52,68,244\nbuffer_load_dwordx4 a[40:43], v79, s[48:51], s31 offen offset:3072 sc1\n"
"s_waitcnt lgkmcnt(5)\nQmaaa 196,72,104\nQta 62,69,26368\nQmaav 196,76,244\nQmaaa 192,72,100\ns_add_u32 s31, s77, 20480\nQmaav 192,76,212\nbuffer_load_dwordx4 a[44:47], v79, s[48:51], s31 offen offset:0 sc1\n"
"Qmaaa 188,72,96\nQta 64,69,32768\nQmaav 188,76,232\nQmaaa 184,72,92\nds_read_b32 v66, v74\nQmaav 184,76,200\nQta 66,69,33792\nQmaaa 180,72,88\n"
"buffer_load_dwordx4 a[48:51], v79, s[48:51], s31 offen offset:1024 sc1\nQmaav 180,76,168\nbuffer_load_dword v78, v76, s[24:27], 0 offen sc1\nQmaaa 176,72,84\ns_waitcnt lgkmcnt(1)\nv_cmp_gt_u32 vcc, v75, v66\nQmaaa 176,76,112\ns_cbranch_vccnz .Lso1455\n"
".Lro1455:\nQmaaa 172,72,80\nQta 68,69,40960\nQmaaa 172,76,108\nbuffer_load_dwordx4 a[52:55], v79, s[48:51], s31 offen offset:2048 sc1\ns_waitcnt lgkmcnt(4)\nQmvaa 92,56,80\nQmvaa 92,60,108\n"
"Qta 70,69,41984\nQmvaa 124,56,84\ns_add_u32 m0, s96, 57344\ns_add_u32 s95, s73, 45056\nQmvaa 124,60,112\nbuffer_load_dwordx4 v63, s[52:55], s95 offen lds\nQmvaa 156,56,88\nQta 72,69,33024\n"
"Qmvav 156,60,168\nQmvaa 188,56,92\nQmvav 188,60,200\nQta 74,69,34048\ns_add_u32 s95, s73, 64512\nQmvaa 220,56,96\nbuffer_load_dwordx4 v63, s[52:55], s95 offen offset:1024 lds\nQmvav 220,60,232\n"
"Qmvaa 252,56,100\nQta 76,69,41216\nQmvav 252,60,212\nQmvaa 100,56,104\nQmvav 100,60,244\nQta 78,69,42240\ns_waitcnt lgkmcnt(4)\nQmaaa 224,64,104\n"
"Qmaav 224,68,244\nQmaaa 220,64,100\nQmaav 220,68,212\nQmaaa 216,64,96\nQta 56,69,33280\nQmaav 216,68,232\nQmaaa 212,64,92\nQmaav 212,68,200\n"
"Qta 58,69,34304\nQmaaa 208,64,88\nQmaav 208,68,168\nQmaaa 204,64,84\nQta 60,69,41472\nQmaaa 204,68,112\nQmaaa 200,64,80\nQmaaa 200,68,108\n"
"Qta 62,69,42496\ns_waitcnt lgkmcnt(4)\nQmaaa 228,72,80\nQmaaa 228,76,108\nQmaaa 232,72,84\nQmaaa 232,76,112\nQmaaa 236,72,88\nQta 64,69,33536\n"
"Qmaav 236,76,168\nQmaaa 240,72,92\nQmaav 240,76,200\nQta 66,69,34560\nQmaaa 244,72,96\nQmaav 244,76,232\nQmaaa 248,72,100\nQta 68,69,41728\n"
"Qmaav 248,76,212\nQmaaa 252,72,104\nQmaav 252,76,244\nQta 70,69,42752\ns_waitcnt lgkmcnt(4)\nQmvaa 172,56,104\nQmvav 172,60,244\nQmvaa 140,56,100\n"
"Qmvav 140,60,212\nQmvaa 108,56,96\nQta 72,69,49152\nQmvav 108,60,232\nQmvaa 228,56,92\nQmvav 228,60,200\nQta 74,69,50176\nQmvaa 196,56,88\n"
"Qmvav 196,60,168\nQmvaa 164,56,84\nQta 76,69,57344\nQmvaa 164,60,112\ns_waitcnt vmcnt(3)\nv_sub_u32 v66, v78, v65\nv_cmp_gt_u32 vcc, 3, v66\ns_and_b64 vcc, vcc, s[28:29]\n"
"s_cbranch_scc1 .Lf3_w\n.Lf3_r:\nv_add_u32 v75, 1, v75\nQmvaa 132,56,80\ns_waitcnt vmcnt(42)\nds_write_b32 v73, v75\nQmvaa 132,60,108\nQta 78,69,58368\n"
"s_waitcnt lgkmcnt(5)\nQmvaa 204,64,80\ns_add_u32 s31, s74, 131072\nQmvaa 204,68,108\nbuffer_load_dwordx4 a[80:83], v79, s[48:51], s31 offen offset:0 sc1\nQmvaa 236,64,84\nQmvaa 236,68,112\ns_bitcmp1_b32 %19, 2\n"
"s_cbranch_scc1 .Lrole1\nQmvaa 96,64,88\nQta 56,69,49408\ns_lshl_b32 s46, %19, 4\nQmvav 96,68,168\nbuffer_load_dwordx4 a[84:87], v79, s[48:51], s31 offen offset:1024 sc1\nQmvaa 128,64,92\nds_read_b32 v66, v74\n"
"s_add_u32 s46, s46, 128\nQmvav 128,68,200\nQta 58,69,50432\nv_mov_b32 v77, 0x7ffff000\nQmvaa 160,64,96\nQmvav 160,68,232\nbuffer_load_dwordx4 a[88:91], v79, s[48:51], s31 offen offset:2048 sc1\nQmvaa 192,64,100\n"
"s_waitcnt lgkmcnt(1)\nv_cmp_gt_u32 vcc, v75, v66\nQmvav 192,68,212\ns_cbranch_vccnz .Lso1525r0\n.Lro1525r0:\nQmvaa 224,64,104\nQta 60,69,57600\nQmvav 224,68,244\n"
"buffer_load_dwordx4 a[92:95], v79, s[48:51], s31 offen offset:3072 sc1\ns_waitcnt vmcnt(31)\ns_waitcnt lgkmcnt(4)\nQmaav 140,72,84\nQmaav 140,76,116\nQta 62,69,58624\nQmaav 136,72,240\ns_add_u32 m0, s96, 65536\n"
"s_add_u32 s95, s67, 86016\nQmaav 136,76,248\nbuffer_load_dwordx4 v63, s[52:55], s95 offen lds\nQmaav 132,72,208\nQta 64,69,49664\nQmaav 132,76,216\nQmaav 128,72,176\nQmaav 128,76,184\n"
"Qta 66,69,50688\ns_add_u32 s95, s67, 105472\nQmaav 124,72,144\nbuffer_load_dwordx4 v63, s[52:55], s95 offen offset:1024 lds\nQmaav 124,76,152\nQmaav 120,72,112\nQta 68,69,57856\nQmaav 120,76,120\n"
"s_add_u32 m0, s96, 73728\ns_add_u32 s95, s67, 126976\nQmaav 116,72,80\nbuffer_load_dwordx4 v63, s[52:55], s95 offen lds\nQmaav 116,76,88\nQta 70,69,58880\ns_waitcnt lgkmcnt(4)\nQmaav 144,56,80\n"
"Qmaav 144,60,88\ns_add_u32 s95, s67, 146432\nQmaav 148,56,112\nbuffer_load_dwordx4 v63, s[52:55], s95 offen offset:1024 lds\nQmaav 148,60,120\nQmaav 152,56,144\nQta 72,69,49920\nQmaav 152,60,152\n"
"s_add_u32 m0, s96, 81920\ns_add_u32 s95, s72, 86016\nQmaav 156,56,176\nbuffer_load_dwordx4 v63, s[52:55], s95 offen lds\nQmaav 156,60,184\nQta 74,69,50944\nQmaav 160,56,208\nQmaav 160,60,216\n"
"Qmaav 164,56,240\nQta 76,69,58112\ns_add_u32 s95, s72, 105472\nQmaav 164,60,248\nbuffer_load_dwordx4 v63, s[52:55], s95 offen offset:1024 lds\nQmaav 168,56,84\nQmaav 168,60,116\nQta 78,69,59136\n"
"s_waitcnt lgkmcnt(4)\nQmvav 24,64,84\ns_add_u32 m0, s96, 90112\ns_add_u32 s95, s72, 126976\nQmvav 24,68,116\nbuffer_load_dwordx4 v63, s[52:55], s95 offen lds\nQmvav 20,64,240\nQmvav 20,68,248\n"
"Qmvav 16,64,208\nQta 56,68,0\ns_add_u32 s95, s72, 146432\nQmvav 16,68,216\nbuffer_load_dwordx4 v63, s[52:55], s95 offen offset:1024 lds\nQmvav 12,64,176\nQmvav 12,68,184\nQta 58,68,512\n"
"Qmvav 8,64,144\ns_add_u32 s31, s74, 135168\nQmvav 8,68,152\nbuffer_load_dwordx4 a[96:99], v79, s[48:51], s31 offen offset:0 sc1\nQmvav 4,64,112\nQta 60,68,1024\nQmvav 4,68,120\nQmvav 0,64,80\n"
"Qmvav 0,68,88\nQta 62,68,1536\ns_waitcnt lgkmcnt(4)\nQmvav 28,72,80\nbuffer_load_dwordx4 a[100:103], v79, s[48:51], s31 offen offset:1024 sc1\nQmvav 28,76,88\nQmvav 32,72,112\nQmvav 32,76,120\n"
"Qmvav 36,72,144\nQta 64,68,256\nQmvav 36,76,152\nbuffer_load_dwordx4 a[104:107], v79, s[48:51], s31 offen offset:2048 sc1\nQmvav 40,72,176\nQmvav 40,76,184\nQta 66,68,768\nQmvav 44,72,208\n"
"s_add_u32 s31, s74, 147456\nv_cmp_eq_u32 s[44:45], s46, v60\ns_nop 1\nv_cndmask_b32 v77, v77, v76, s[44:45]\nv_add_u32 v67, 1, v65\ns_xor_b32 s46, %19, 12\ns_lshl_b32 s28, 1, s46\nQmvav 44,76,216\n"
"buffer_load_dwordx4 a[108:111], v79, s[48:51], s31 offen offset:0 sc1\nQmvav 48,72,240\nQta 68,68,1280\nQmvav 48,76,248\nv_add_u32 v75, 1, v75\nQmvav 52,72,84\ns_waitcnt vmcnt(37)\nds_write_b32 v73, v75\n"
"Qmvav 52,76,116\nQta 70,68,1792\ns_waitcnt lgkmcnt(5)\nQmaav 196,56,84\nbuffer_load_dwordx4 a[112:115], v79, s[48:51], s31 offen offset:1024 sc1\nQmaav 196,60,116\nQmaav 192,56,240\nQmaav 192,60,248\n"
"Qmaav 188,56,208\nQta 72,68,8192\nQmaav 188,60,216\nbuffer_load_dwordx4 v[168:171], v79, s[48:51], s31 offen offset:2048 sc1\nQmaav 184,56,176\nds_read_b32 v66, v74\nQmaav 184,60,184\nQta 74,68,9216\n"
"Qmaav 180,56,144\nQmaav 180,60,152\nbuffer_load_dwordx4 v[200:203], v79, s[48:51], s31 offen offset:3072 sc1\nQmaav 176,56,112\ns_waitcnt lgkmcnt(1)\nv_cmp_gt_u32 vcc, v75, v66\nQmaav 176,60,120\ns_cbranch_vccnz .Lso1595r0\n"
".Lro1595r0:\nQmaav 172,56,80\nQta 76,68,16384\ns_add_u32 s31, s74, 151552\nQmaav 172,60,88\nbuffer_load_dwordx4 v[232:235], v79, s[48:51], s31 offen offset:0 sc1\ns_waitcnt lgkmcnt(4)\nQmvav 92,64,80\n"
"Qmvav 92,68,88\nQta 78,68,17408\nQmvav 124,64,112\nQmvav 124,68,120\nbuffer_load_dwordx4 v[212:215], v79, s[48:51], s31 offen offset:1024 sc1\nQmvav 156,64,144\nQta 56,68,8448\nQmvav 156,68,152\n"
"Qmvav 188,64,176\nQmvav 188,68,184\nQta 58,68,9472\nQmvav 220,64,208\nbuffer_load_dwordx4 v[244:247], v79, s[48:51], s31 offen offset:2048 sc1\nQmvav 220,68,216\nQmvav 252,64,240\nQta 60,68,16640\n"
"Qmvav 252,68,248\ns_add_u32 m0, s91, 0\ns_add_u32 s31, s77, 131072\nQmvav 100,64,84\nbuffer_load_dwordx4 v79, s[48:51], s31 offen sc1 lds\nQmvav 100,68,116\nQta 62,68,17664\ns_waitcnt lgkmcnt(4)\n"
"Qmaav 224,72,84\nQmaav 224,76,116\nQmaav 220,72,240\nbuffer_load_dwordx4 v79, s[48:51], s31 offen offset:1024 sc1 lds\nQmaav 220,76,248\nQmaav 216,72,208\nQta 64,68,8704\nQmaav 216,76,216\n"
"Qmaav 212,72,176\nbuffer_load_dwordx4 v79, s[48:51], s31 offen offset:2048 sc1 lds\nQmaav 212,76,184\nQta 66,68,9728\nQmaav 208,72,144\nQmaav 208,76,152\nQmaav 204,72,112\nQta 68,68,16896\n"
"Qmaav 204,76,120\nbuffer_load_dwordx4 v79, s[48:51], s31 offen offset:3072 sc1 lds\nQmaav 200,72,80\nQmaav 200,76,88\nQta 70,68,17920\ns_waitcnt lgkmcnt(4)\nQmaav 228,56,80\ns_add_u32 m0, s91, 4096\n"
"s_add_u32 s31, s77, 135168\nQmaav 228,60,88\nbuffer_load_dwordx4 v79, s[48:51], s31 offen sc1 lds\nQmaav 232,56,112\nQmaav 232,60,120\nQmaav 236,56,144\nQta 72,68,8960\nQmaav 236,60,152\n"
"buffer_load_dwordx4 v79, s[48:51], s31 offen offset:1024 sc1 lds\nQmaav 240,56,176\nQmaav 240,60,184\nQta 74,68,9984\nQmaav 244,56,208\nQmaav 244,60,216\nbuffer_load_dwordx4 v79, s[48:51], s31 offen offset:2048 sc1 lds\nQmaav 248,56,240\n"
"Qta 76,68,17152\nQmaav 248,60,248\nQmaav 252,56,84\nQmaav 252,60,116\nQta 78,68,18176\ns_add_u32 m0, s91, 33792\ns_add_u32 s31, s77, 147456\ns_waitcnt lgkmcnt(4)\n"
"Qmvav 172,64,84\nbuffer_load_dwordx4 v79, s[48:51], s31 offen sc1 lds\nQmvav 172,68,116\nQmvav 140,64,240\nQmvav 140,68,248\nQmvav 108,64,208\nQta 56,68,24576\nQmvav 108,68,216\n"
"buffer_load_dwordx4 v79, s[48:51], s31 offen offset:1024 sc1 lds\nQmvav 228,64,176\nQmvav 228,68,184\nQta 58,68,25600\nQmvav 196,64,144\nQmvav 196,68,152\nbuffer_load_dwordx4 v79, s[48:51], s31 offen offset:2048 sc1 lds\nQmvav 164,64,112\n"
"Qta 60,68,32768\nQmvav 164,68,120\nv_add_u32 v75, 1, v75\nQmvav 132,64,80\ns_waitcnt vmcnt(49)\nds_write_b32 v73, v75\nQmvav 132,68,88\nQta 62,68,33792\n"
"s_waitcnt lgkmcnt(5)\nQmvav 204,72,80\nbuffer_load_dwordx4 v79, s[48:51], s31 offen offset:3072 sc1 lds\nQmvav 204,76,88\nQmvav 236,72,112\nQmvav 236,76,120\nQmvav 96,72,144\nQta 64,68,24832\n"
"s_add_u32 m0, s91, 37888\ns_add_u32 s31, s77, 151552\nQmvav 96,76,152\nbuffer_load_dwordx4 v79, s[48:51], s31 offen sc1 lds\nQmvav 128,72,176\nds_read_b32 v66, v74\nQmvav 128,76,184\n.Lpg19:\n"
"s_cbranch_execz .Lpg23\nQta 66,68,25856\nQmvav 160,72,208\nQmvav 160,76,216\nbuffer_load_dwordx4 v79, s[48:51], s31 offen offset:1024 sc1 lds\nQmvav 192,72,240\ns_waitcnt lgkmcnt(1)\nv_cmp_gt_u32 vcc, v75, v66\n"
"Qmvav 192,76,248\ns_cbranch_vccnz .Lso1665r0\n.Lro1665r0:\nQmvav 224,72,84\nQta 68,68,33024\nQmvav 224,76,116\nbuffer_load_dwordx4 v79, s[48:51], s31 offen offset:2048 sc1 lds\ns_waitcnt vmcnt(38)\n"
"s_waitcnt lgkmcnt(4)\nQmaaa 140,56,24\nQmaaa 140,60,52\nQta 70,68,34048\nQmaaa 136,56,20\ns_add_u32 m0, s96, 0\ns_add_u32 s95, s73, 86016\nQmaaa 136,60,48\n"
"buffer_load_dwordx4 v63, s[52:55], s95 offen lds\nQmaaa 132,56,16\nQta 72,68,25088\nQmaaa 132,60,44\nQmaaa 128,56,12\nQmaaa 128,60,40\nQta 74,68,26112\ns_add_u32 s95, s73, 105472\n"
"Qmaaa 124,56,8\nbuffer_load_dwordx4 v63, s[52:55], s95 offen offset:1024 lds\nQmaaa 124,60,36\nQmaaa 120,56,4\nQta 76,68,33280\nQmaaa 120,60,32\ns_add_u32 m0, s96, 8192\ns_add_u32 s95, s73, 126976\n"
"Qmaaa 116,56,0\nbuffer_load_dwordx4 v63, s[52:55], s95 offen lds\nQmaaa 116,60,28\nQta 78,68,34304\ns_waitcnt lgkmcnt(4)\nQmaaa 144,64,0\nQmaaa 144,68,28\ns_add_u32 s95, s73, 146432\n"
"Qmaaa 148,64,4\nbuffer_load_dwordx4 v63, s[52:55], s95 offen offset:1024 lds\nQmaaa 148,68,32\nQmaaa 152,64,8\nQta 56,68,25344\nQmaaa 152,68,36\ns_add_u32 s31, s75, 131072\nQmaaa 156,64,12\n"
"buffer_load_dwordx4 v[80:83], v79, s[48:51], s31 offen offset:0 sc1\nQmaaa 156,68,40\nQta 58,68,26368\nQmaaa 160,64,16\nQmaaa 160,68,44\nQmaaa 164,64,20\nQta 60,68,33536\nQmaaa 164,68,48\n"
"buffer_load_dwordx4 v[112:115], v79, s[48:51], s31 offen offset:1024 sc1\nQmaaa 168,64,24\nQmaaa 168,68,52\nQta 62,68,34560\ns_waitcnt lgkmcnt(4)\nQmvaa 24,72,24\nQmvaa 24,76,52\nbuffer_load_dwordx4 v[144:147], v79, s[48:51], s31 offen offset:2048 sc1\n"
"Qmvaa 20,72,20\nQmvaa 20,76,48\nQmvaa 16,72,16\nQta 64,68,40960\nQmvaa 16,76,44\nbuffer_load_dwordx4 v[176:179], v79, s[48:51], s31 offen offset:3072 sc1\nQmvaa 12,72,12\nQmvaa 12,76,40\n"
"Qta 66,68,41472\nQmvaa 8,72,8\ns_add_u32 s31, s75, 135168\nQmvaa 8,76,36\nbuffer_load_dwordx4 v[208:211], v79, s[48:51], s31 offen offset:0 sc1\nQmvaa 4,72,4\nQta 68,68,41984\nQmvaa 4,76,32\n"
"Qmvaa 0,72,0\nQmvaa 0,76,28\nQta 70,68,42496\ns_waitcnt lgkmcnt(4)\nQmvaa 28,56,0\nbuffer_load_dwordx4 v[240:243], v79, s[48:51], s31 offen offset:1024 sc1\nQmvaa 28,60,28\nQmvaa 32,56,4\n"
"Qmvaa 32,60,32\nQmvaa 36,56,8\nQta 72,68,41216\nQmvaa 36,60,36\nbuffer_load_dwordx4 v[84:87], v79, s[48:51], s31 offen offset:2048 sc1\nQmvaa 40,56,12\nQmvaa 40,60,40\nQta 74,68,41728\n"
"Qmvaa 44,56,16\ns_add_u32 s31, s75, 147456\nQmvaa 44,60,44\nbuffer_load_dwordx4 v[88:91], v79, s[48:51], s31 offen offset:0 sc1\nQmvaa 48,56,20\nQta 76,68,42240\nQmvaa 48,60,48\nv_add_u32 v75, 1, v75\n"
"Qmvaa 52,56,24\ns_waitcnt vmcnt(38)\nds_write_b32 v73, v75\nQmvaa 52,60,52\nQta 78,68,42752\ns_waitcnt lgkmcnt(5)\nQmaaa 196,64,24\nbuffer_load_dwordx4 v[120:123], v79, s[48:51], s31 offen offset:1024 sc1\n"
"Qmaaa 196,68,52\nQmaaa 192,64,20\nQmaaa 192,68,48\nQmaaa 188,64,16\nQta 56,68,49152\nQmaaa 188,68,44\nbuffer_load_dwordx4 v[152:155], v79, s[48:51], s31 offen offset:2048 sc1\nQmaaa 184,64,12\n"
"ds_read_b32 v66, v74\nQmaaa 184,68,40\nQta 58,68,50176\nQmaaa 180,64,8\nQmaaa 180,68,36\nbuffer_load_dwordx4 v[184:187], v79, s[48:51], s31 offen offset:3072 sc1\nQmaaa 176,64,4\ns_waitcnt lgkmcnt(1)\n"
"v_cmp_gt_u32 vcc, v75, v66\nQmaaa 176,68,32\ns_cbranch_vccnz .Lso1735r0\n.Lro1735r0:\nQmaaa 172,64,0\nQta 60,68,57344\ns_add_u32 s31, s75, 151552\nQmaaa 172,68,28\n"
"buffer_load_dwordx4 v[216:219], v79, s[48:51], s31 offen offset:0 sc1\ns_waitcnt lgkmcnt(4)\nQmvaa 92,72,0\nQmvaa 92,76,28\nQta 62,68,58368\nQmvaa 124,72,4\nQmvaa 124,76,32\nbuffer_load_dwordx4 v[248:251], v79, s[48:51], s31 offen offset:1024 sc1\n"
"Qmvaa 156,72,8\nQta 64,68,49408\nQmvaa 156,76,36\nQmvaa 188,72,12\nQmvaa 188,76,40\nQta 66,68,50432\nQmvaa 220,72,16\nbuffer_load_dwordx4 v[116:119], v79, s[48:51], s31 offen offset:2048 sc1\n"
"Qmvaa 220,76,44\nQmvaa 252,72,20\nQta 68,68,57600\nQmvaa 252,76,48\ns_add_u32 m0, s96, 16384\ns_add_u32 s95, s67, 86016\nQmvaa 100,72,24\nbuffer_load_dwordx4 v61, s[52:55], s95 offen lds\n"
"Qmvaa 100,76,52\nQta 70,68,58624\ns_waitcnt lgkmcnt(4)\nQmaaa 224,56,24\nQmaaa 224,60,52\ns_add_u32 s95, s67, 105472\nQmaaa 220,56,20\nbuffer_load_dwordx4 v61, s[52:55], s95 offen offset:1024 lds\n"
"Qmaaa 220,60,48\nQmaaa 216,56,16\nQta 72,68,49664\nQmaaa 216,60,44\ns_add_u32 m0, s96, 24576\ns_add_u32 s95, s67, 126976\nQmaaa 212,56,12\nbuffer_load_dwordx4 v61, s[52:55], s95 offen lds\n"
"Qmaaa 212,60,40\nQta 74,68,50688\nQmaaa 208,56,8\nQmaaa 208,60,36\nQmaaa 204,56,4\nQta 76,68,57856\ns_add_u32 s95, s67, 146432\nQmaaa 204,60,32\n"
"buffer_load_dwordx4 v61, s[52:55], s95 offen offset:1024 lds\nQmaaa 200,56,0\nQmaaa 200,60,28\nQta 78,68,58880\ns_waitcnt lgkmcnt(4)\nQmaaa 228,64,0\ns_add_u32 m0, s96, 32768\ns_add_u32 s95, s72, 86016\n"
"Qmaaa 228,68,28\nbuffer_load_dwordx4 v61, s[52:55], s95 offen lds\nQmaaa 232,64,4\nQmaaa 232,68,32\nQmaaa 236,64,8\nQta 56,68,49920\ns_add_u32 s95, s72, 105472\nQmaaa 236,68,36\n"
"buffer_load_dwordx4 v61, s[52:55], s95 offen offset:1024 lds\nQmaaa 240,64,12\nQmaaa 240,68,40\nQta 58,68,50944\nQmaaa 244,64,16\nQmaaa 244,68,44\nQmaaa 248,64,20\nQta 60,68,58112\n"
"Qmaaa 248,68,48\nQmaaa 252,64,24\nQmaaa 252,68,52\nQta 62,68,59136\ns_waitcnt lgkmcnt(4)\nQmvaa 172,72,24\nQmvaa 172,76,52\nQmvaa 140,72,20\n"
"Qmvaa 140,76,48\nQmvaa 108,72,16\nQta 64,69,0\nQmvaa 108,76,44\nQmvaa 228,72,12\nQmvaa 228,76,40\nQta 66,69,1024\nQmvaa 196,72,8\n"
"Qmvaa 196,76,36\nQmvaa 164,72,4\nQta 68,69,8192\nQmvaa 164,76,32\nv_add_u32 v75, 1, v75\nQmvaa 132,72,0\ns_waitcnt vmcnt(20)\nds_write_b32 v73, v75\n"
"Qmvaa 132,76,28\nQta 70,69,9216\ns_waitcnt lgkmcnt(5)\nQmvaa 204,56,0\ns_waitcnt vmcnt(24)\nQda 0,58,0\nQmvaa 204,60,28\nQmvaa 236,56,4\n"
"Qda 28,58,33792\nQmvaa 236,60,32\nQmvaa 96,56,8\nQta 72,69,16384\nQmvaa 96,60,36\nQda 4,58,1024\nQmvaa 128,56,12\nds_read_b32 v66, v74\n"
"Qmvaa 128,60,40\nQta 74,69,17408\nQmvaa 160,56,16\nQda 32,58,34816\nQmvaa 160,60,44\nQmvaa 192,56,20\ns_waitcnt lgkmcnt(2)\nv_cmp_gt_u32 vcc, v75, v66\n"
"Qmvaa 192,60,48\ns_cbranch_vccnz .Lso1805r0\n.Lro1805r0:\nQmvaa 224,56,24\nQta 76,69,24576\ns_add_u32 m0, s96, 40960\ns_add_u32 s95, s72, 126976\nQmvaa 224,60,52\n"
"buffer_load_dwordx4 v61, s[52:55], s95 offen lds\ns_waitcnt vmcnt(39)\ns_waitcnt lgkmcnt(8)\nQmaaa 200,64,80\nQda 8,58,2048\nQmaaa 200,68,108\nQta 78,69,25600\nQmaaa 204,64,84\n"
"Qda 36,58,35840\ns_add_u32 s95, s72, 146432\nQmaaa 204,68,112\nbuffer_load_dwordx4 v61, s[52:55], s95 offen offset:1024 lds\nQmaaa 208,64,88\nQta 56,68,0\nQmaav 208,68,168\nQda 12,58,3072\n"
"Qmaaa 212,64,92\nQmaav 212,68,200\nQta 58,68,1024\ns_add_u32 m0, s96, 49152\ns_add_u32 s95, s73, 86016\nQmaaa 216,64,96\nbuffer_load_dwordx4 v61, s[52:55], s95 offen lds\nQmaav 216,68,232\n"
"Qda 40,58,36864\nQmaaa 220,64,100\nQta 60,68,8192\nQmaav 220,68,212\nQda 16,58,4096\ns_add_u32 s95, s73, 105472\nQmaaa 224,64,104\nbuffer_load_dwordx4 v61, s[52:55], s95 offen offset:1024 lds\n"
"Qmaav 224,68,244\nQta 62,68,9216\ns_waitcnt vmcnt(10)\ns_waitcnt lgkmcnt(8)\nQmaav 200,72,80\nQda 44,58,37888\nQmaav 200,76,88\ns_add_u32 m0, s96, 57344\n"
"s_add_u32 s95, s73, 126976\nQmaav 204,72,112\nbuffer_load_dwordx4 v61, s[52:55], s95 offen lds\nQmaav 204,76,120\nQda 20,58,5120\nQmaav 208,72,144\nQta 64,69,256\nQmaav 208,76,152\n"
"Qda 48,58,38912\ns_add_u32 s95, s73, 146432\nQmaav 212,72,176\nbuffer_load_dwordx4 v61, s[52:55], s95 offen offset:1024 lds\nQmaav 212,76,184\nQta 66,69,1280\nQmaav 216,72,208\nQda 24,58,6144\n"
"Qmaav 216,76,216\nQmaav 220,72,240\nQta 68,69,8448\nQmaav 220,76,248\nQda 52,58,39936\nQmaav 224,72,84\nQmaav 224,76,116\nQta 70,69,9472\n"
"s_waitcnt lgkmcnt(9)\nQmaaa 200,56,0\nQmaaa 200,60,28\nQmaaa 204,56,4\nQmaaa 204,60,32\ns_waitcnt lgkmcnt(9)\nQmaaa 208,56,8\nQta 72,69,16640\n"
"Qmaaa 208,60,36\ns_nop 1\nds_write_b128 v58, a[200:203] offset:0\nQmaaa 212,56,12\nQmaaa 212,60,40\nQta 74,69,17664\ns_waitcnt lgkmcnt(4)\nQmaaa 216,56,16\n"
"ds_write_b128 v58, a[204:207] offset:1024\nQmaaa 216,60,44\nQmaaa 220,56,20\nQta 76,69,24832\nQmaaa 220,60,48\nds_write_b128 v58, a[208:211] offset:2048\nQmaaa 224,56,24\nQmaaa 224,60,52\n"
"Qta 78,69,25856\ns_waitcnt lgkmcnt(7)\nQmaaa 228,64,80\nds_write_b128 v58, a[212:215] offset:3072\nQmaaa 228,68,108\nQmaaa 232,64,84\nQmaaa 232,68,112\nds_write_b128 v58, a[216:219] offset:4096\n"
"Qmaaa 236,64,88\nQta 56,68,256\nQmaav 236,68,168\nQmaaa 240,64,92\nds_write_b128 v58, a[220:223] offset:5120\nQmaav 240,68,200\nQta 58,68,1280\nQmaaa 244,64,96\n"
"Qmaav 244,68,232\nds_write_b128 v58, a[224:227] offset:6144\nQmaaa 248,64,100\nQta 60,68,8448\nQmaav 248,68,212\nQmaaa 252,64,104\nQmaav 252,68,244\nQta 62,68,9472\n"
"s_waitcnt lgkmcnt(8)\nQmaav 228,72,80\nv_add_u32 v75, 1, v75\nQmaav 228,76,88\nds_write_b32 v73, v75\nQmaav 232,72,112\nQmaav 232,76,120\nQmaav 236,72,144\n"
"Qta 64,69,512\nQmaav 236,76,152\nQmaav 240,72,176\nQmaav 240,76,184\nQta 66,69,1536\nQmaav 244,72,208\nQmaav 244,76,216\nds_read_b32 v66, v74\n"
"Qmaav 248,72,240\nQta 68,69,8704\nQmaav 248,76,248\nQmaav 252,72,84\nQmaav 252,76,116\ns_waitcnt lgkmcnt(1)\nv_cmp_gt_u32 vcc, v75, v66\ns_waitcnt lgkmcnt(5)\n"
"Qmaaa 228,56,0\ns_cbranch_vccnz .Lso1878r0\n.Lro1878r0:\nQmaaa 228,60,28\nQta 70,69,9728\nQmaaa 232,56,4\nQmaaa 232,60,32\nQdv 148,59,2048\n"
"Qmaaa 236,56,8\nQta 72,69,16896\nQmaaa 236,60,36\ns_nop 0\nds_write_b128 v58, a[228:231] offset:33792\nQmaaa 240,56,12\nQdv 180,59,3072\nQmaaa 240,60,40\n"
"Qta 74,69,17920\nQmaaa 244,56,16\nds_write_b128 v58, a[232:235] offset:34816\nQmaaa 244,60,44\nQdv 104,59,5120\nQmaaa 248,56,20\nQta 76,69,25088\nQmaaa 248,60,48\n"
"ds_write_b128 v58, a[236:239] offset:35840\nQmaaa 252,56,24\nQdv 136,59,0\ns_waitcnt lgkmcnt(9)\nQa 196,196,148\nQmaaa 252,60,52\nQta 78,69,26112\nQa 197,197,149\n"
"s_waitcnt lgkmcnt(10)\nQmvaa 132,64,80\nds_write_b128 v58, a[240:243] offset:36864\nQmvaa 132,68,108\nQa 198,198,150\nQa 199,199,151\nQmvaa 164,64,84\ns_waitcnt lgkmcnt(8)\n"
"Qa 228,228,180\nQa 229,229,181\nQmvaa 164,68,112\nds_write_b128 v58, a[244:247] offset:37888\nQmvaa 196,64,88\nQta 56,68,512\nQa 230,230,182\nQa 231,231,183\n"
"Qmvav 196,68,168\nQdv 148,59,4096\ns_waitcnt lgkmcnt(8)\nQa 140,140,104\nQmvaa 228,64,92\nds_write_b128 v58, a[248:251] offset:38912\nQmvav 228,68,200\nQta 58,68,1536\n"
"Qa 141,141,105\nQmvaa 108,64,96\nQa 142,142,106\nQa 143,143,107\nQmvav 108,68,232\nQdv 180,59,6144\ns_waitcnt lgkmcnt(8)\nQa 132,132,136\n"
"Qmvaa 140,64,100\nQta 60,68,8704\nQa 133,133,137\nQmvav 140,68,212\nds_write_b128 v58, a[252:255] offset:39936\nQmvaa 172,64,104\nQa 134,134,138\nQa 135,135,139\n"
"Qmvav 172,68,244\nQta 62,68,9728\ns_waitcnt lgkmcnt(6)\nQa 108,108,148\ns_waitcnt lgkmcnt(6)\nQmvav 132,72,80\nQdv 104,59,1024\nv_add_u32 v75, 1, v75\n"
"Qmvav 132,76,88\ns_waitcnt vmcnt(10)\nds_write_b32 v73, v75\nQmvav 164,72,112\nQa 109,109,149\nQa 110,110,150\nQmvav 164,76,120\nQa 111,111,151\n"
"s_waitcnt lgkmcnt(5)\nQa 172,172,180\nQmvav 196,72,144\nQta 64,69,768\nQa 173,173,181\nQmvav 196,76,152\nQa 174,174,182\nQa 175,175,183\n"
"Qmvav 228,72,176\nQmvav 228,76,184\nQta 66,69,1792\ns_waitcnt lgkmcnt(3)\nQa 164,164,104\nQmvav 108,72,208\nQa 165,165,105\nQa 166,166,106\n"
"Qmvav 108,76,216\nds_read_b32 v66, v74\nQa 167,167,107\nQmvav 140,72,240\nQta 68,69,8960\nQmvav 140,76,248\nQmvav 172,72,84\nQmvav 172,76,116\n"
"s_waitcnt lgkmcnt(1)\nv_cmp_gt_u32 vcc, v75, v66\ns_waitcnt lgkmcnt(6)\nQmvaa 132,56,0\ns_cbranch_vccnz .Lso1920r0\n.Lro1920r0:\ns_add_u32 s68, s64, 0\nQmvaa 132,60,28\n"
"Qta 70,69,9984\nQmvaa 164,56,4\nQmvaa 164,60,32\nQdv 136,59,35840\nQmvaa 196,56,8\nQta 72,69,17152\ns_nop 1\nQk 132,132,133\n"
"Qmvaa 196,60,36\nQk 133,134,135\nQmvaa 228,56,12\nQdv 148,59,36864\nQk 134,164,165\nQmvaa 228,60,40\nQta 74,69,18176\nQk 135,166,167\n"
"Qmvaa 108,56,16\nbuffer_store_dwordx4 v[132:135], v79, s[24:27], s68 offen offset:0 sc1\nQk 196,196,197\nQmvaa 108,60,44\nQdv 180,59,38912\nQk 197,198,199\nQmvaa 140,56,20\nQta 76,69,25344\n"
"Qk 198,228,229\nQmvaa 140,60,48\ns_waitcnt lgkmcnt(5)\nQa 96,96,136\nQa 97,97,137\nQmvaa 172,56,24\nQdv 104,59,33792\nQa 98,98,138\n"
"Qmvaa 172,60,52\nQta 78,69,26368\nQa 99,99,139\ns_waitcnt lgkmcnt(11)\n.Lpg20:\ns_cbranch_execz .Lpg24\nQmvaa 204,64,80\ns_waitcnt lgkmcnt(5)\n"
"Qa 128,128,148\nQa 129,129,149\nQmvaa 204,68,108\nQa 130,130,150\nQa 131,131,151\nQmvaa 236,64,84\nQk 199,230,231\nQk 108,108,109\n"
"Qmvaa 236,68,112\ns_waitcnt lgkmcnt(3)\nQa 192,192,180\nQa 193,193,181\nQmvaa 96,64,88\nQta 56,68,768\nQa 194,194,182\nQmvav 96,68,168\n"
"Qdv 136,59,37888\nQa 195,195,183\nQmvaa 128,64,92\ns_waitcnt lgkmcnt(3)\nQa 204,204,104\nQa 205,205,105\nQmvav 128,68,200\nQta 58,68,1792\n"
"Qa 206,206,106\nQmvaa 160,64,96\nbuffer_store_dwordx4 v[196:199], v79, s[24:27], s68 offen offset:1024 sc1\nQa 207,207,107\nQmvav 160,68,232\nQdv 148,59,39936\nQk 109,110,111\nQmvaa 192,64,100\n"
"Qta 60,68,8960\nQk 110,140,141\nQmvav 192,68,212\nQk 111,142,143\nQk 172,172,173\nQmvaa 224,64,104\nbuffer_store_dwordx4 v[108:111], v79, s[24:27], s68 offen offset:2048 sc1\nv_add_u32 v75, 1, v75\n"
"Qmvav 224,68,244\ns_waitcnt vmcnt(3)\nds_write_b32 v73, v75\ns_waitcnt lgkmcnt(10)\nQmvav 204,72,80\nQta 62,68,9984\ns_waitcnt lgkmcnt(5)\nQa 160,160,136\n"
"Qmvav 204,76,88\nQdv 180,59,34816\nQa 161,161,137\nQmvav 236,72,112\nQa 162,162,138\nQa 163,163,139\nQmvav 236,76,120\ns_waitcnt lgkmcnt(4)\n"
"Qa 224,224,148\nQa 225,225,149\nQmvav 96,72,144\nQta 64,68,16384\nQa 226,226,150\nQmvav 96,76,152\nQa 227,227,151\nQk 173,174,175\n"
"Qmvav 128,72,176\nQmvav 128,76,184\nds_read_b32 v66, v74\ns_waitcnt lgkmcnt(2)\nQa 236,236,180\nQmvav 160,72,208\nQta 66,68,17408\nQa 237,237,181\n"
"Qmvav 160,76,216\nQa 238,238,182\nQa 239,239,183\nQmvav 192,72,240\nQmvav 192,76,248\ns_waitcnt lgkmcnt(1)\nv_cmp_gt_u32 vcc, v75, v66\nQmvav 224,72,84\n"
"s_cbranch_vccnz .Lso1960r0\n.Lro1960r0:\nQmvav 224,76,116\nQta 68,68,24576\ns_add_u32 m0, s96, 65536\ns_add_u32 s95, s67, 86016\ns_waitcnt lgkmcnt(5)\nQmvaa 204,56,0\n"
"buffer_load_dwordx4 v62, s[52:55], s95 offen lds\nQmvaa 204,60,28\nQmvaa 236,56,4\nQta 70,68,25600\nQmvaa 236,60,32\ns_add_u32 s95, s67, 125952\nQmvaa 96,56,8\nbuffer_load_dwordx4 v62, s[52:55], s95 offen offset:1024 lds\n"
"Qmvaa 96,60,36\nQta 72,68,32768\nQk 174,204,205\nQmvaa 128,56,12\nQk 175,206,207\nQk 236,236,237\nQmvaa 128,60,40\nbuffer_store_dwordx4 v[172:175], v79, s[24:27], s68 offen offset:3072 sc1\n"
"Qk 237,238,239\nQmvaa 160,56,16\nQta 74,68,33792\ns_add_u32 m0, s96, 73728\ns_add_u32 s95, s72, 86016\nQmvaa 160,60,44\nbuffer_load_dwordx4 v62, s[52:55], s95 offen lds\nQmvaa 192,56,20\n"
"Qk 238,96,97\nQk 239,98,99\nQmvaa 192,60,48\nQta 76,68,40960\ns_add_u32 s68, s64, 4096\nQmvaa 224,56,24\nbuffer_store_dwordx4 v[236:239], v79, s[24:27], s68 offen offset:0 sc1\ns_add_u32 s95, s72, 125952\n"
"Qmvaa 224,60,52\nbuffer_load_dwordx4 v62, s[52:55], s95 offen offset:1024 lds\ns_waitcnt lgkmcnt(3)\nQmaaa 116,64,80\nQta 78,68,41984\nQk 128,128,129\nQmaaa 116,68,108\nQk 129,130,131\n"
"Qk 130,160,161\nQmaaa 120,64,84\ns_add_u32 m0, s96, 81920\ns_add_u32 s95, s73, 86016\nQmaaa 120,68,112\nbuffer_load_dwordx4 v62, s[52:55], s95 offen lds\nQmaaa 124,64,88\nQta 56,68,49152\n"
"Qk 131,162,163\nQmaav 124,68,168\nbuffer_store_dwordx4 v[128:131], v79, s[24:27], s68 offen offset:1024 sc1\nQk 192,192,193\nQmaaa 128,64,92\nQk 193,194,195\nQk 194,224,225\nQmaav 128,68,200\n"
"Qta 58,68,50176\ns_add_u32 s95, s73, 125952\nQmaaa 132,64,96\nbuffer_load_dwordx4 v62, s[52:55], s95 offen offset:1024 lds\nQmaav 132,68,232\nQk 195,226,227\nQmaaa 136,64,100\nQta 60,68,57344\n"
"v_bfe_u32 v61, v60, 6, 2\nQmaav 136,68,212\nv_lshrrev_b32 v62, 8, v60\nv_xor_b32 v61, v61, v62\nQmaaa 140,64,104\nv_bfe_u32 v62, v60, 5, 1\nv_lshl_or_b32 v61, v61, 1, v62\nQmaav 140,68,244\n"
"Qta 62,68,58368\nv_and_b32 v62, 16, v60\ns_waitcnt lgkmcnt(4)\nQmaav 116,72,80\nv_mov_b32 v63, 0x3f80\nv_lshlrev_b32 v63, v62, v63\nQmaav 116,76,88\nv_min_u32 v62, 1, v61\n"
"v_add_u32 v62, -1, v62\nQmaav 120,72,112\nv_and_b32 v62, v62, v63\nv_xor_b32 v61, 1, v61\nQmaav 120,76,120\nv_min_u32 v61, 1, v61\nv_add_u32 v61, -1, v61\nQmaav 124,72,144\n"
"Qta 64,68,16640\nv_and_b32 v61, v61, v63\nQmaav 124,76,152\nv_accvgpr_write_b32 a202, v62\nv_accvgpr_write_b32 a203, v61\nQmaav 128,72,176\nv_accvgpr_write_b32 a200, 0\nv_accvgpr_write_b32 a201, 0\n"
"Qmaav 128,76,184\nQta 66,68,17664\nv_accvgpr_write_b32 a204, 0\nQmaav 132,72,208\nbuffer_store_dwordx4 v[192:195], v79, s[24:27], s68 offen offset:2048 sc1\nv_accvgpr_write_b32 a205, 0\nQmaav 132,76,216\nQmaav 136,72,240\n"
"Qta 68,68,24832\nQmaav 136,76,248\nQmaav 140,72,84\nQmaav 140,76,116\nQta 70,68,25856\ns_waitcnt lgkmcnt(4)\nQmaaa 116,56,0\nQmaaa 116,60,28\n"
"Qmaaa 120,56,4\nQmaaa 120,60,32\nQmaaa 124,56,8\nQta 72,68,33024\nQmaaa 124,60,36\ns_nop 2\nds_write_b128 v58, a[116:119] offset:0\nQmaaa 128,56,12\n"
"Qmaaa 128,60,40\nQta 74,68,34048\nQmaaa 132,56,16\nds_write_b128 v58, a[120:123] offset:1024\nQmaaa 132,60,44\nQmaaa 136,56,20\nQta 76,68,41216\nQmaaa 136,60,48\n"
"ds_write_b128 v58, a[124:127] offset:2048\nQmaaa 140,56,24\nQmaaa 140,60,52\nQta 78,68,42240\ns_waitcnt lgkmcnt(7)\nQmaaa 144,64,80\nds_write_b128 v58, a[128:131] offset:3072\nQmaaa 144,68,108\n"
"Qmaaa 148,64,84\nQmaaa 148,68,112\nds_write_b128 v58, a[132:135] offset:4096\nQmaaa 152,64,88\nQta 56,68,49408\nQmaav 152,68,168\nQmaaa 156,64,92\nds_write_b128 v58, a[136:139] offset:5120\n"
"Qmaav 156,68,200\nQta 58,68,50432\nQmaaa 160,64,96\nQmaav 160,68,232\nds_write_b128 v58, a[140:143] offset:6144\nQmaaa 164,64,100\nQta 60,68,57600\nQmaav 164,68,212\n"
"Qmaaa 168,64,104\nQmaav 168,68,244\nQta 62,68,58624\ns_waitcnt lgkmcnt(8)\nQmaav 144,72,80\nv_add_u32 v75, 1, v75\nQmaav 144,76,88\ns_waitcnt vmcnt(8)\n"
"ds_write_b32 v73, v75\nQmaav 148,72,112\nQmaav 148,76,120\nQmaav 152,72,144\nQta 64,68,16896\nQmaav 152,76,152\nQmaav 156,72,176\nQmaav 156,76,184\n"
"Qta 66,68,17920\nQmaav 160,72,208\nQmaav 160,76,216\nds_read_b32 v66, v74\nQmaav 164,72,240\nQta 68,68,25088\nQmaav 164,76,248\nQmaav 168,72,84\n"
"Qmaav 168,76,116\ns_waitcnt lgkmcnt(1)\nv_cmp_gt_u32 vcc, v75, v66\ns_waitcnt lgkmcnt(5)\nQmaaa 144,56,0\ns_cbranch_vccnz .Lso2046r0\n.Lro2046r0:\nQmaaa 144,60,28\n"
"Qta 70,68,26112\nQmaaa 148,56,4\nQmaaa 148,60,32\nQdv 104,59,2048\nQmaaa 152,56,8\nQta 72,68,33280\nQmaaa 152,60,36\ns_nop 0\n"
"ds_write_b128 v58, a[144:147] offset:33792\nQmaaa 156,56,12\nQdv 136,59,3072\nQmaaa 156,60,40\nQta 74,68,34304\nQmaaa 160,56,16\nds_write_b128 v58, a[148:151] offset:34816\nQmaaa 160,60,44\n"
"Qdv 148,59,5120\nQmaaa 164,56,20\nQta 76,68,41472\nQmaaa 164,60,48\nds_write_b128 v58, a[152:155] offset:35840\nQmaaa 168,56,24\nQdv 180,59,0\ns_waitcnt lgkmcnt(9)\n"
"Qa 8,8,104\nQmaaa 168,60,52\nQta 78,68,42496\nQa 9,9,105\ns_waitcnt lgkmcnt(10)\nQmvaa 0,64,80\nds_write_b128 v58, a[156:159] offset:36864\nQmvaa 0,68,108\n"
"Qa 10,10,106\nQa 11,11,107\nQmvaa 4,64,84\ns_waitcnt lgkmcnt(8)\nQa 12,12,136\nQa 13,13,137\nQmvaa 4,68,112\nds_write_b128 v58, a[160:163] offset:37888\n"
"Qmvaa 8,64,88\nQta 56,68,49664\nQa 14,14,138\nQa 15,15,139\nQmvav 8,68,168\nQdv 104,59,4096\ns_waitcnt lgkmcnt(8)\nQa 20,20,148\n"
"Qmvaa 12,64,92\nds_write_b128 v58, a[164:167] offset:38912\nQmvav 12,68,200\nQta 58,68,50688\nQa 21,21,149\nQmvaa 16,64,96\nQa 22,22,150\nQa 23,23,151\n"
"Qmvav 16,68,232\nQdv 136,59,6144\ns_waitcnt lgkmcnt(8)\nQa 0,0,180\nQmvaa 20,64,100\nQta 60,68,57856\nQa 1,1,181\nQmvav 20,68,212\n"
"ds_write_b128 v58, a[168:171] offset:39936\nQmvaa 24,64,104\nQa 2,2,182\nQa 3,3,183\nQmvav 24,68,244\nQta 62,68,58880\ns_waitcnt lgkmcnt(6)\nQa 16,16,104\n"
"s_waitcnt lgkmcnt(6)\nQmvav 0,72,80\nQdv 148,59,1024\nv_add_u32 v75, 1, v75\nQmvav 0,76,88\ns_waitcnt vmcnt(1)\nds_write_b32 v73, v75\nQmvav 4,72,112\n"
"Qa 17,17,105\nQa 18,18,106\nQmvav 4,76,120\nQa 19,19,107\ns_waitcnt lgkmcnt(5)\nQa 24,24,136\nQmvav 8,72,144\nQta 64,69,0\n"
"Qa 25,25,137\nQmvav 8,76,152\nQa 26,26,138\nQa 27,27,139\nQmvav 12,72,176\ns_waitcnt vmcnt(0)\nbuffer_store_dword v67, v77, s[24:27], 0 offen sc1\nQmvav 12,76,184\n"
"Qta 66,69,512\ns_waitcnt lgkmcnt(3)\nQa 4,4,148\nQmvav 16,72,208\nQa 5,5,149\nQa 6,6,150\nQmvav 16,76,216\nds_read_b32 v66, v74\n"
"Qa 7,7,151\nQmvav 20,72,240\nQta 68,69,1024\nQmvav 20,76,248\nQmvav 24,72,84\nQmvav 24,76,116\ns_waitcnt lgkmcnt(1)\nv_cmp_gt_u32 vcc, v75, v66\n"
"s_waitcnt lgkmcnt(6)\nQmvaa 0,56,0\ns_cbranch_vccnz .Lso2088r0\n.Lro2088r0:\nQmvaa 0,60,28\nQta 70,69,1536\nQmvaa 4,56,4\nQmvaa 4,60,32\n"
"Qdv 180,59,33792\nQmvaa 8,56,8\nQta 72,69,8192\nQmvaa 8,60,36\nQmvaa 12,56,12\nQdv 104,59,34816\nQmvaa 12,60,40\nQta 74,69,8704\n"
"Qmvaa 16,56,16\nQmvaa 16,60,44\nQdv 136,59,35840\nQmvaa 20,56,20\nQta 76,69,9216\nQmvaa 20,60,48\ns_waitcnt lgkmcnt(5)\nQa 28,28,180\n"
"Qa 29,29,181\nQmvaa 24,56,24\nQdv 148,59,36864\nQa 30,30,182\nQmvaa 24,60,52\nQta 78,69,9728\nQa 31,31,183\ns_waitcnt lgkmcnt(11)\n"
"Qmaaa 172,64,80\ns_waitcnt lgkmcnt(5)\nQa 32,32,104\nQa 33,33,105\nQmaaa 172,68,108\nQa 34,34,106\nQa 35,35,107\nQmaaa 176,64,84\n"
"Qmaaa 176,68,112\ns_waitcnt lgkmcnt(3)\nQa 36,36,136\nQa 37,37,137\nQmaaa 180,64,88\nQta 56,69,16384\nQa 38,38,138\nQmaav 180,68,168\n"
"Qdv 180,59,37888\nQa 39,39,139\nQmaaa 184,64,92\ns_waitcnt lgkmcnt(3)\nQa 40,40,148\nQa 41,41,149\nQmaav 184,68,200\nQta 58,69,16896\n"
"Qa 42,42,150\nQmaaa 188,64,96\nQdv 104,59,38912\nQa 43,43,151\nQmaav 188,68,232\nQmaaa 192,64,100\nQta 60,69,17408\nQmaav 192,68,212\n"
"Qdv 136,59,39936\nQmaaa 196,64,104\ns_waitcnt lgkmcnt(4)\nQa 44,44,180\nQa 45,45,181\nQmaav 196,68,244\nQta 62,69,17920\nQa 46,46,182\n"
"s_waitcnt lgkmcnt(5)\nQmaav 172,72,80\nQa 47,47,183\nQmaav 172,76,88\nQmaav 176,72,112\ns_waitcnt lgkmcnt(3)\nQa 48,48,104\nQa 49,49,105\n"
"Qmaav 176,76,120\nQa 50,50,106\nQa 51,51,107\nQmaav 180,72,144\nQta 64,68,17152\nQmaav 180,76,152\ns_waitcnt lgkmcnt(2)\nQa 52,52,136\n"
"Qa 53,53,137\nQmaav 184,72,176\nQa 54,54,138\nQa 55,55,139\nQmaav 184,76,184\nQta 66,68,18176\nQmaav 188,72,208\nQmaav 188,76,216\n"
"Qmaav 192,72,240\nQta 68,68,25344\nQmaav 192,76,248\nQmaav 196,72,84\nQmaav 196,76,116\nQta 70,68,26368\ns_waitcnt lgkmcnt(4)\nQmaaa 172,56,0\n"
"Qmaaa 172,60,28\nQmaaa 176,56,4\nQmaaa 176,60,32\nQmaaa 180,56,8\nQta 72,68,33536\nQmaaa 180,60,36\ns_nop 2\nds_write_b128 v58, a[172:175] offset:0\n"
"Qmaaa 184,56,12\nQmaaa 184,60,40\nQta 74,68,34560\nQmaaa 188,56,16\nds_write_b128 v58, a[176:179] offset:1024\nQmaaa 188,60,44\nQmaaa 192,56,20\nQta 76,68,41728\n"
"Qmaaa 192,60,48\nds_write_b128 v58, a[180:183] offset:2048\nQmaaa 196,56,24\nQmaaa 196,60,52\nQta 78,68,42752\ns_waitcnt lgkmcnt(7)\nQmvaa 28,64,80\nds_write_b128 v58, a[184:187] offset:3072\n"
"Qmvaa 28,68,108\nQmvaa 32,64,84\nQmvaa 32,68,112\nds_write_b128 v58, a[188:191] offset:4096\nQmvaa 36,64,88\nQta 56,68,49920\nQmvav 36,68,168\nQmvaa 40,64,92\n"
"ds_write_b128 v58, a[192:195] offset:5120\nQmvav 40,68,200\nQta 58,68,50944\nQmvaa 44,64,96\nQmvav 44,68,232\nbuffer_load_dword v78, v76, s[24:27], 0 offen sc1\nQmvaa 48,64,100\nQta 60,68,58112\n"
"Qmvav 48,68,212\nds_write_b128 v58, a[196:199] offset:6144\nQmvaa 52,64,104\nQmvav 52,68,244\nQta 62,68,59136\ns_waitcnt lgkmcnt(8)\nQmvav 28,72,80\nv_add_u32 v75, 1, v75\n"
"Qmvav 28,76,88\nds_write_b32 v73, v75\nQmvav 32,72,112\nQmvav 32,76,120\nQmvav 36,72,144\nQta 64,69,256\nQmvav 36,76,152\nQmvav 40,72,176\n"
"Qmvav 40,76,184\nQta 66,69,768\nQmvav 44,72,208\nQmvav 44,76,216\nds_read_b32 v66, v74\nQmvav 48,72,240\nQta 68,69,1280\nQmvav 48,76,248\n"
"Qmvav 52,72,84\nQmvav 52,76,116\ns_waitcnt lgkmcnt(1)\nv_cmp_gt_u32 vcc, v75, v66\ns_waitcnt lgkmcnt(5)\nQmvaa 28,56,0\ns_cbranch_vccnz .Lso2172r0\n.Lro2172r0:\n"
"Qmvaa 28,60,28\nQta 70,69,1792\nQmvaa 32,56,4\nQmvaa 32,60,32\nQdv 148,59,2048\nQmvaa 36,56,8\nQta 72,69,8448\nQmvaa 36,60,36\n"
"Qmvaa 40,56,12\nQdv 180,59,3072\nQmvaa 40,60,40\nQta 74,69,8960\nQmvaa 44,56,16\nQmvaa 44,60,44\nQdv 104,59,5120\nQmvaa 48,56,20\n"
"Qta 76,69,9472\nQmvaa 48,60,48\ns_waitcnt lgkmcnt(5)\nQa 156,156,148\nQa 157,157,149\nQmvaa 52,56,24\nQdv 136,59,0\nQa 158,158,150\n"
"Qmvaa 52,60,52\nQta 78,69,9984\nQa 159,159,151\ns_waitcnt lgkmcnt(11)\nQmvaa 92,64,80\ns_waitcnt lgkmcnt(5)\nQa 188,188,180\nQa 189,189,181\n"
"Qmvaa 92,68,108\nQa 190,190,182\n.Lpg21:\ns_cbranch_execz .Lpg25\nQa 191,191,183\nQmvaa 124,64,84\ns_waitcnt vmcnt(0)\nv_sub_u32 v66, v78, v65\n"
"v_cmp_gt_u32 vcc, 1, v66\ns_and_b64 vcc, vcc, s[28:29]\ns_cbranch_scc1 .Lx0_w\n.Lx0_r:\nQmvaa 124,68,112\ns_add_u32 s69, s98, 0\nbuffer_load_dwordx4 a[208:211], v79, s[24:27], s69 offen offset:0 sc1\nQmvaa 156,64,88\n"
"Qta 56,69,16640\ns_waitcnt lgkmcnt(4)\nQa 252,252,104\nQmvav 156,68,168\nbuffer_load_dwordx4 a[212:215], v79, s[24:27], s69 offen offset:1024 sc1\nQmvaa 188,64,92\nQdv 148,59,4096\nQa 253,253,105\n"
"Qmvav 188,68,200\nbuffer_load_dwordx4 a[216:219], v79, s[24:27], s69 offen offset:2048 sc1\nQmvaa 220,64,96\nQta 58,69,17152\nQa 254,254,106\nQa 255,255,107\nQmvav 220,68,232\nbuffer_load_dwordx4 a[220:223], v79, s[24:27], s69 offen offset:3072 sc1\n"
"Qmvaa 252,64,100\nQdv 180,59,6144\ns_waitcnt lgkmcnt(5)\nQa 92,92,136\nQmvav 252,68,212\ns_add_u32 s69, s98, 4096\nbuffer_load_dwordx4 a[224:227], v79, s[24:27], s69 offen offset:0 sc1\nQmvaa 100,64,104\n"
"Qta 60,69,17664\nQa 93,93,137\nQa 94,94,138\nQa 95,95,139\nQmvav 100,68,244\nbuffer_load_dwordx4 a[228:231], v79, s[24:27], s69 offen offset:1024 sc1\ns_waitcnt lgkmcnt(9)\nQmvav 92,72,80\n"
"Qdv 104,59,1024\ns_waitcnt lgkmcnt(4)\nQa 220,220,148\nQmvav 92,76,88\nbuffer_load_dwordx4 a[232:235], v79, s[24:27], s69 offen offset:2048 sc1\nQmvav 124,72,112\nQta 62,69,18176\nQa 221,221,149\n"
"Qmvav 124,76,120\nbuffer_load_dwordx4 a[236:239], v79, s[24:27], s69 offen offset:3072 sc1\nQmvav 156,72,144\nQa 222,222,150\nQa 223,223,151\nQmvav 156,76,152\ns_add_u32 s69, s98, 16384\nbuffer_load_dwordx4 a[240:243], v79, s[24:27], s69 offen offset:0 sc1\n"
"Qmvav 188,72,176\ns_waitcnt lgkmcnt(3)\nQa 100,100,180\nQa 101,101,181\nQmvav 188,76,184\nbuffer_load_dwordx4 a[244:247], v79, s[24:27], s69 offen offset:1024 sc1\nQmvav 220,72,208\nv_add_u32 v75, 1, v75\n"
"Qa 102,102,182\nQmvav 220,76,216\nds_write_b32 v73, v75\nQa 103,103,183\nQmvav 252,72,240\nbuffer_load_dwordx4 a[248:251], v79, s[24:27], s69 offen offset:2048 sc1\nQmvav 252,76,248\ns_waitcnt lgkmcnt(2)\n"
"Qa 124,124,104\nQa 125,125,105\nQmvav 100,72,84\nQa 126,126,106\nQa 127,127,107\nQmvav 100,76,116\ns_waitcnt lgkmcnt(1)\nQmvaa 92,56,0\n"
"Qmvaa 92,60,28\nQmvaa 124,56,4\nQmvaa 124,60,32\nds_read_b32 v66, v74\nQmvaa 156,56,8\nQmvaa 156,60,36\nQmvaa 188,56,12\nQmvaa 188,60,40\n"
"s_waitcnt lgkmcnt(0)\nv_cmp_gt_u32 vcc, v75, v66\nQmvaa 220,56,16\ns_cbranch_vccnz .Lso2222r0\n.Lro2222r0:\nQmvaa 220,60,44\nQmvaa 252,56,20\nQmvaa 252,60,48\n"
"Qmvaa 100,56,24\nQmvaa 100,60,52\ns_waitcnt vmcnt(10)\nQmvaa 92,202,208\nQmvaa 124,200,208\ns_waitcnt vmcnt(9)\nQmvaa 156,202,212\nQmvaa 188,200,212\n"
"s_nop 3\nQk 92,92,93\nQk 93,94,95\nds_write_b64 v56, v[92:93] offset:64\ns_waitcnt vmcnt(8)\nQmvaa 220,202,216\nQk 124,124,125\nQk 125,126,127\n"
"ds_write_b64 v56, v[124:125] offset:3392\nQmvaa 252,200,216\nQk 156,156,157\nQk 157,158,159\nds_write_b64 v56, v[156:157] offset:6720\ns_waitcnt vmcnt(7)\nQmvaa 100,202,220\nQk 188,188,189\n"
"Qk 189,190,191\nds_write_b64 v56, v[188:189] offset:10048\nQmvaa 0,200,220\nQk 220,220,221\nQk 221,222,223\nds_write_b64 v56, v[220:221] offset:13376\ns_waitcnt vmcnt(6)\nQmvaa 4,202,224\n"
"Qk 252,252,253\nQk 253,254,255\nds_write_b64 v56, v[252:253] offset:16704\nQmvaa 8,200,224\nQk 100,100,101\nQk 101,102,103\nds_write_b64 v56, v[100:101] offset:20032\ns_waitcnt vmcnt(5)\n"
"Qmvaa 12,202,228\nQk 0,0,1\nQk 1,2,3\nds_write_b64 v56, v[0:1] offset:0\nQmvaa 16,200,228\nQk 4,4,5\nQk 5,6,7\nds_write_b64 v56, v[4:5] offset:3328\n"
"s_waitcnt vmcnt(4)\nQmvaa 20,202,232\nQk 8,8,9\nQk 9,10,11\nds_write_b64 v56, v[8:9] offset:6656\nQmvaa 24,200,232\nQk 12,12,13\nQk 13,14,15\n"
"ds_write_b64 v56, v[12:13] offset:9984\ns_waitcnt vmcnt(3)\nQmvaa 28,202,236\nQk 16,16,17\nQk 17,18,19\nds_write_b64 v56, v[16:17] offset:13312\nQmvaa 32,200,236\nQk 20,20,21\n"
"Qk 21,22,23\nds_write_b64 v56, v[20:21] offset:16640\ns_waitcnt vmcnt(2)\nQmvaa 36,202,240\nQk 24,24,25\nQk 25,26,27\nds_write_b64 v56, v[24:25] offset:19968\nQmvaa 40,200,240\n"
"Qk 28,28,29\nQk 29,30,31\nds_write_b64 v56, v[28:29] offset:32\ns_waitcnt vmcnt(1)\nQmvaa 44,202,244\nQk 32,32,33\nQk 33,34,35\nds_write_b64 v56, v[32:33] offset:3360\n"
"Qmvaa 48,200,244\nQk 36,36,37\nQk 37,38,39\nds_write_b64 v56, v[36:37] offset:6688\ns_waitcnt vmcnt(0)\nQmvaa 52,202,248\nQk 40,40,41\nQk 41,42,43\n"
"ds_write_b64 v56, v[40:41] offset:10016\nQk 44,44,45\nQk 45,46,47\nds_write_b64 v56, v[44:45] offset:13344\nQk 48,48,49\nQk 49,50,51\nds_write_b64 v56, v[48:49] offset:16672\nQk 52,52,53\n"
"Qk 53,54,55\nds_write_b64 v56, v[52:53] offset:20000\ns_waitcnt lgkmcnt(0)\ns_barrier\nQda 0,57,0\nQda 4,57,3328\nQda 8,57,6656\nQda 12,57,9984\n"
"Qda 16,57,13312\nQda 20,57,16640\nQda 24,57,19968\nQda 28,71,64\nQda 32,71,6720\nQda 36,71,13376\nQda 40,71,20032\ns_mov_b32 s68, 0\n"
"s_waitcnt lgkmcnt(10)\nbuffer_store_dwordx4 a[0:3], v64, s[40:43], s68 offen offset:0 sc1\ns_mov_b32 s68, 40960\ns_waitcnt lgkmcnt(9)\nbuffer_store_dwordx4 a[4:7], v64, s[40:43], s68 offen offset:0 sc1\ns_mov_b32 s68, 81920\ns_waitcnt lgkmcnt(8)\nbuffer_store_dwordx4 a[8:11], v64, s[40:43], s68 offen offset:0 sc1\n"
"s_mov_b32 s68, 122880\ns_waitcnt lgkmcnt(7)\nbuffer_store_dwordx4 a[12:15], v64, s[40:43], s68 offen offset:0 sc1\ns_mov_b32 s68, 163840\ns_waitcnt lgkmcnt(6)\nbuffer_store_dwordx4 a[16:19], v64, s[40:43], s68 offen offset:0 sc1\ns_mov_b32 s68, 204800\ns_waitcnt lgkmcnt(5)\n"
"buffer_store_dwordx4 a[20:23], v64, s[40:43], s68 offen offset:0 sc1\ns_mov_b32 s68, 245760\ns_waitcnt lgkmcnt(4)\nbuffer_store_dwordx4 a[24:27], v64, s[40:43], s68 offen offset:0 sc1\ns_mov_b32 s68, 0\ns_waitcnt lgkmcnt(3)\nbuffer_store_dwordx4 a[28:31], v72, s[40:43], s68 offen offset:0 sc1\ns_mov_b32 s68, 81920\n"
"s_waitcnt lgkmcnt(2)\nbuffer_store_dwordx4 a[32:35], v72, s[40:43], s68 offen offset:0 sc1\ns_mov_b32 s68, 163840\ns_waitcnt lgkmcnt(1)\nbuffer_store_dwordx4 a[36:39], v72, s[40:43], s68 offen offset:0 sc1\ns_mov_b32 s68, 245760\ns_cmp_eq_u32 s93, 1\ns_cselect_b32 s68, 0x7ffff000, s68\n"
"s_waitcnt lgkmcnt(0)\nbuffer_store_dwordx4 a[40:43], v72, s[40:43], s68 offen offset:0 sc1\ns_branch .Lfin\n.Lrole1:\n.Lpg22:\ns_cbranch_execz .Lpg26\nQmvaa 96,64,88\nQta 56,69,49408\n"
"s_lshl_b32 s46, %19, 4\nQmvav 96,68,168\nbuffer_load_dwordx4 a[84:87], v79, s[48:51], s31 offen offset:1024 sc1\nQmvaa 128,64,92\nds_read_b32 v66, v74\ns_add_u32 s46, s46, 128\nQmvav 128,68,200\nQta 58,69,50432\n"
"v_mov_b32 v77, 0x7ffff000\nQmvaa 160,64,96\nQmvav 160,68,232\nbuffer_load_dwordx4 a[88:91], v79, s[48:51], s31 offen offset:2048 sc1\nQmvaa 192,64,100\ns_waitcnt lgkmcnt(1)\nv_cmp_gt_u32 vcc, v75, v66\nQmvav 192,68,212\n"
"s_cbranch_vccnz .Lso1525r1\n.Lro1525r1:\nQmvaa 224,64,104\nQta 60,69,57600\nQmvav 224,68,244\nbuffer_load_dwordx4 a[92:95], v79, s[48:51], s31 offen offset:3072 sc1\ns_waitcnt vmcnt(31)\ns_waitcnt lgkmcnt(4)\n"
"Qmaav 140,72,84\nQmaav 140,76,116\nQta 62,69,58624\nQmaav 136,72,240\ns_add_u32 m0, s96, 65536\ns_add_u32 s95, s67, 86016\nQmaav 136,76,248\nbuffer_load_dwordx4 v62, s[52:55], s95 offen lds\n"
"Qmaav 132,72,208\nQta 64,69,49664\nQmaav 132,76,216\nQmaav 128,72,176\nQmaav 128,76,184\nQta 66,69,50688\ns_add_u32 s95, s67, 125952\nQmaav 124,72,144\n"
"buffer_load_dwordx4 v62, s[52:55], s95 offen offset:1024 lds\nQmaav 124,76,152\nQmaav 120,72,112\nQta 68,69,57856\nQmaav 120,76,120\ns_add_u32 m0, s96, 73728\ns_add_u32 s95, s72, 86016\nQmaav 116,72,80\n"
"buffer_load_dwordx4 v62, s[52:55], s95 offen lds\nQmaav 116,76,88\nQta 70,69,58880\ns_waitcnt lgkmcnt(4)\nQmaav 144,56,80\nQmaav 144,60,88\ns_add_u32 s95, s72, 125952\nQmaav 148,56,112\n"
"buffer_load_dwordx4 v62, s[52:55], s95 offen offset:1024 lds\nQmaav 148,60,120\nQmaav 152,56,144\nQta 72,69,49920\nQmaav 152,60,152\ns_add_u32 m0, s96, 81920\ns_add_u32 s95, s73, 86016\nQmaav 156,56,176\n"
"buffer_load_dwordx4 v62, s[52:55], s95 offen lds\nQmaav 156,60,184\nQta 74,69,50944\nQmaav 160,56,208\nQmaav 160,60,216\nQmaav 164,56,240\nQta 76,69,58112\ns_add_u32 s95, s73, 125952\n"
"Qmaav 164,60,248\nbuffer_load_dwordx4 v62, s[52:55], s95 offen offset:1024 lds\nQmaav 168,56,84\nQmaav 168,60,116\nQta 78,69,59136\ns_waitcnt lgkmcnt(4)\nQmvav 24,64,84\ns_add_u32 m0, s96, 90112\n"
"s_add_u32 s95, s67, 86016\nQmvav 24,68,116\nbuffer_load_dwordx4 v61, s[52:55], s95 offen lds\nQmvav 20,64,240\nQmvav 20,68,248\nQmvav 16,64,208\nQta 56,68,0\ns_add_u32 s95, s67, 105472\n"
"Qmvav 16,68,216\nbuffer_load_dwordx4 v61, s[52:55], s95 offen offset:1024 lds\nQmvav 12,64,176\nQmvav 12,68,184\nQta 58,68,512\nQmvav 8,64,144\ns_add_u32 s31, s74, 135168\nQmvav 8,68,152\n"
"buffer_load_dwordx4 a[96:99], v79, s[48:51], s31 offen offset:0 sc1\nQmvav 4,64,112\nQta 60,68,1024\nQmvav 4,68,120\nQmvav 0,64,80\nQmvav 0,68,88\nQta 62,68,1536\ns_waitcnt lgkmcnt(4)\n"
"Qmvav 28,72,80\nbuffer_load_dwordx4 a[100:103], v79, s[48:51], s31 offen offset:1024 sc1\nQmvav 28,76,88\nQmvav 32,72,112\nQmvav 32,76,120\nQmvav 36,72,144\nQta 64,68,256\nQmvav 36,76,152\n"
"buffer_load_dwordx4 a[104:107], v79, s[48:51], s31 offen offset:2048 sc1\nQmvav 40,72,176\nQmvav 40,76,184\nQta 66,68,768\nQmvav 44,72,208\ns_add_u32 s31, s74, 147456\nv_cmp_eq_u32 s[44:45], s46, v60\ns_nop 1\n"
"v_cndmask_b32 v77, v77, v76, s[44:45]\nv_add_u32 v67, 1, v65\ns_xor_b32 s46, %19, 12\ns_lshl_b32 s28, 1, s46\nQmvav 44,76,216\nbuffer_load_dwordx4 a[108:111], v79, s[48:51], s31 offen offset:0 sc1\nQmvav 48,72,240\nQta 68,68,1280\n"
"Qmvav 48,76,248\nv_add_u32 v75, 1, v75\nQmvav 52,72,84\ns_waitcnt vmcnt(37)\nds_write_b32 v73, v75\nQmvav 52,76,116\nQta 70,68,1792\ns_waitcnt lgkmcnt(5)\n"
"Qmaav 196,56,84\nbuffer_load_dwordx4 a[112:115], v79, s[48:51], s31 offen offset:1024 sc1\nQmaav 196,60,116\nQmaav 192,56,240\nQmaav 192,60,248\nQmaav 188,56,208\nQta 72,68,8192\nQmaav 188,60,216\n"
"buffer_load_dwordx4 v[168:171], v79, s[48:51], s31 offen offset:2048 sc1\nQmaav 184,56,176\nds_read_b32 v66, v74\nQmaav 184,60,184\nQta 74,68,9216\nQmaav 180,56,144\nQmaav 180,60,152\nbuffer_load_dwordx4 v[200:203], v79, s[48:51], s31 offen offset:3072 sc1\n"
"Qmaav 176,56,112\ns_waitcnt lgkmcnt(1)\nv_cmp_gt_u32 vcc, v75, v66\nQmaav 176,60,120\ns_cbranch_vccnz .Lso1595r1\n.Lro1595r1:\nQmaav 172,56,80\nQta 76,68,16384\n"
"s_add_u32 s31, s74, 151552\nQmaav 172,60,88\nbuffer_load_dwordx4 v[232:235], v79, s[48:51], s31 offen offset:0 sc1\ns_waitcnt lgkmcnt(4)\nQmvav 92,64,80\nQmvav 92,68,88\nQta 78,68,17408\nQmvav 124,64,112\n"
"Qmvav 124,68,120\nbuffer_load_dwordx4 v[212:215], v79, s[48:51], s31 offen offset:1024 sc1\nQmvav 156,64,144\nQta 56,68,8448\nQmvav 156,68,152\nQmvav 188,64,176\nQmvav 188,68,184\nQta 58,68,9472\n"
"Qmvav 220,64,208\nbuffer_load_dwordx4 v[244:247], v79, s[48:51], s31 offen offset:2048 sc1\nQmvav 220,68,216\nQmvav 252,64,240\nQta 60,68,16640\nQmvav 252,68,248\ns_add_u32 m0, s91, 0\ns_add_u32 s31, s77, 131072\n"
"Qmvav 100,64,84\nbuffer_load_dwordx4 v79, s[48:51], s31 offen sc1 lds\nQmvav 100,68,116\nQta 62,68,17664\ns_waitcnt lgkmcnt(4)\nQmaav 224,72,84\nQmaav 224,76,116\nQmaav 220,72,240\n"
"buffer_load_dwordx4 v79, s[48:51], s31 offen offset:1024 sc1 lds\nQmaav 220,76,248\nQmaav 216,72,208\nQta 64,68,8704\nQmaav 216,76,216\nQmaav 212,72,176\nbuffer_load_dwordx4 v79, s[48:51], s31 offen offset:2048 sc1 lds\nQmaav 212,76,184\n"
"Qta 66,68,9728\nQmaav 208,72,144\nQmaav 208,76,152\nQmaav 204,72,112\nQta 68,68,16896\nQmaav 204,76,120\nbuffer_load_dwordx4 v79, s[48:51], s31 offen offset:3072 sc1 lds\nQmaav 200,72,80\n"
"Qmaav 200,76,88\nQta 70,68,17920\ns_waitcnt lgkmcnt(4)\nQmaav 228,56,80\ns_add_u32 m0, s91, 4096\ns_add_u32 s31, s77, 135168\nQmaav 228,60,88\nbuffer_load_dwordx4 v79, s[48:51], s31 offen sc1 lds\n"
"Qmaav 232,56,112\nQmaav 232,60,120\nQmaav 236,56,144\nQta 72,68,8960\nQmaav 236,60,152\nbuffer_load_dwordx4 v79, s[48:51], s31 offen offset:1024 sc1 lds\nQmaav 240,56,176\nQmaav 240,60,184\n"
"Qta 74,68,9984\nQmaav 244,56,208\nQmaav 244,60,216\nbuffer_load_dwordx4 v79, s[48:51], s31 offen offset:2048 sc1 lds\nQmaav 248,56,240\nQta 76,68,17152\nQmaav 248,60,248\nQmaav 252,56,84\n"
"Qmaav 252,60,116\nQta 78,68,18176\ns_add_u32 m0, s91, 33792\ns_add_u32 s31, s77, 147456\ns_waitcnt lgkmcnt(4)\nQmvav 172,64,84\nbuffer_load_dwordx4 v79, s[48:51], s31 offen sc1 lds\nQmvav 172,68,116\n"
"Qmvav 140,64,240\nQmvav 140,68,248\nQmvav 108,64,208\nQta 56,68,24576\nQmvav 108,68,216\nbuffer_load_dwordx4 v79, s[48:51], s31 offen offset:1024 sc1 lds\nQmvav 228,64,176\nQmvav 228,68,184\n"
"Qta 58,68,25600\nQmvav 196,64,144\nQmvav 196,68,152\nbuffer_load_dwordx4 v79, s[48:51], s31 offen offset:2048 sc1 lds\nQmvav 164,64,112\nQta 60,68,32768\nQmvav 164,68,120\nv_add_u32 v75, 1, v75\n"
"Qmvav 132,64,80\ns_waitcnt vmcnt(49)\nds_write_b32 v73, v75\nQmvav 132,68,88\nQta 62,68,33792\ns_waitcnt lgkmcnt(5)\nQmvav 204,72,80\nbuffer_load_dwordx4 v79, s[48:51], s31 offen offset:3072 sc1 lds\n"
"Qmvav 204,76,88\nQmvav 236,72,112\nQmvav 236,76,120\nQmvav 96,72,144\nQta 64,68,24832\ns_add_u32 m0, s91, 37888\ns_add_u32 s31, s77, 151552\nQmvav 96,76,152\n"
"buffer_load_dwordx4 v79, s[48:51], s31 offen sc1 lds\nQmvav 128,72,176\nds_read_b32 v66, v74\nQmvav 128,76,184\nQta 66,68,25856\nQmvav 160,72,208\nQmvav 160,76,216\nbuffer_load_dwordx4 v79, s[48:51], s31 offen offset:1024 sc1 lds\n"
"Qmvav 192,72,240\ns_waitcnt lgkmcnt(1)\nv_cmp_gt_u32 vcc, v75, v66\nQmvav 192,76,248\ns_cbranch_vccnz .Lso1665r1\n.Lro1665r1:\nQmvav 224,72,84\nQta 68,68,33024\n"
"Qmvav 224,76,116\nbuffer_load_dwordx4 v79, s[48:51], s31 offen offset:2048 sc1 lds\ns_waitcnt vmcnt(38)\ns_waitcnt lgkmcnt(4)\nQmaaa 140,56,24\nQmaaa 140,60,52\nQta 70,68,34048\nQmaaa 136,56,20\n"
"s_add_u32 m0, s96, 0\ns_add_u32 s95, s67, 126976\nQmaaa 136,60,48\nbuffer_load_dwordx4 v61, s[52:55], s95 offen lds\nQmaaa 132,56,16\nQta 72,68,25088\nQmaaa 132,60,44\nQmaaa 128,56,12\n"
"Qmaaa 128,60,40\nQta 74,68,26112\ns_add_u32 s95, s67, 146432\nQmaaa 124,56,8\nbuffer_load_dwordx4 v61, s[52:55], s95 offen offset:1024 lds\nQmaaa 124,60,36\nQmaaa 120,56,4\nQta 76,68,33280\n"
"Qmaaa 120,60,32\ns_add_u32 s31, s75, 131072\nQmaaa 116,56,0\nbuffer_load_dwordx4 v[80:83], v79, s[48:51], s31 offen offset:0 sc1\nQmaaa 116,60,28\n.Lpg23:\ns_cbranch_execz .Ltr8\nQta 78,68,34304\n"
"s_waitcnt lgkmcnt(4)\nQmaaa 144,64,0\nQmaaa 144,68,28\nQmaaa 148,64,4\nbuffer_load_dwordx4 v[112:115], v79, s[48:51], s31 offen offset:1024 sc1\nQmaaa 148,68,32\nQmaaa 152,64,8\nQta 56,68,25344\n"
"Qmaaa 152,68,36\nQmaaa 156,64,12\nbuffer_load_dwordx4 v[144:147], v79, s[48:51], s31 offen offset:2048 sc1\nQmaaa 156,68,40\nQta 58,68,26368\nQmaaa 160,64,16\nQmaaa 160,68,44\nQmaaa 164,64,20\n"
"Qta 60,68,33536\nQmaaa 164,68,48\nbuffer_load_dwordx4 v[176:179], v79, s[48:51], s31 offen offset:3072 sc1\nQmaaa 168,64,24\nQmaaa 168,68,52\nQta 62,68,34560\ns_waitcnt lgkmcnt(4)\nQmvaa 24,72,24\n"
"s_add_u32 s31, s75, 135168\nQmvaa 24,76,52\nbuffer_load_dwordx4 v[208:211], v79, s[48:51], s31 offen offset:0 sc1\nQmvaa 20,72,20\nQmvaa 20,76,48\nQmvaa 16,72,16\nQta 64,68,40960\nQmvaa 16,76,44\n"
"buffer_load_dwordx4 v[240:243], v79, s[48:51], s31 offen offset:1024 sc1\nQmvaa 12,72,12\nQmvaa 12,76,40\nQta 66,68,41472\nQmvaa 8,72,8\nQmvaa 8,76,36\nbuffer_load_dwordx4 v[84:87], v79, s[48:51], s31 offen offset:2048 sc1\nQmvaa 4,72,4\n"
"Qta 68,68,41984\nQmvaa 4,76,32\nQmvaa 0,72,0\nQmvaa 0,76,28\nQta 70,68,42496\ns_add_u32 s31, s75, 147456\ns_waitcnt lgkmcnt(4)\nQmvaa 28,56,0\n"
"buffer_load_dwordx4 v[88:91], v79, s[48:51], s31 offen offset:0 sc1\nQmvaa 28,60,28\nQmvaa 32,56,4\nQmvaa 32,60,32\nQmvaa 36,56,8\nQta 72,68,41216\nQmvaa 36,60,36\nbuffer_load_dwordx4 v[120:123], v79, s[48:51], s31 offen offset:1024 sc1\n"
"Qmvaa 40,56,12\nQmvaa 40,60,40\nQta 74,68,41728\nQmvaa 44,56,16\nQmvaa 44,60,44\nbuffer_load_dwordx4 v[152:155], v79, s[48:51], s31 offen offset:2048 sc1\nQmvaa 48,56,20\nQta 76,68,42240\n"
"Qmvaa 48,60,48\nv_add_u32 v75, 1, v75\nQmvaa 52,56,24\ns_waitcnt vmcnt(40)\nds_write_b32 v73, v75\nQmvaa 52,60,52\nQta 78,68,42752\ns_waitcnt lgkmcnt(5)\n"
"Qmaaa 196,64,24\nbuffer_load_dwordx4 v[184:187], v79, s[48:51], s31 offen offset:3072 sc1\nQmaaa 196,68,52\nQmaaa 192,64,20\nQmaaa 192,68,48\nQmaaa 188,64,16\nQta 56,68,49152\ns_add_u32 s31, s75, 151552\n"
"Qmaaa 188,68,44\nbuffer_load_dwordx4 v[216:219], v79, s[48:51], s31 offen offset:0 sc1\nQmaaa 184,64,12\nds_read_b32 v66, v74\nQmaaa 184,68,40\nQta 58,68,50176\nQmaaa 180,64,8\nQmaaa 180,68,36\n"
"buffer_load_dwordx4 v[248:251], v79, s[48:51], s31 offen offset:1024 sc1\nQmaaa 176,64,4\ns_waitcnt lgkmcnt(1)\nv_cmp_gt_u32 vcc, v75, v66\nQmaaa 176,68,32\ns_cbranch_vccnz .Lso1735r1\n.Lro1735r1:\nQmaaa 172,64,0\n"
"Qta 60,68,57344\nQmaaa 172,68,28\nbuffer_load_dwordx4 v[116:119], v79, s[48:51], s31 offen offset:2048 sc1\ns_waitcnt lgkmcnt(4)\nQmvaa 92,72,0\nQmvaa 92,76,28\nQta 62,68,58368\nQmvaa 124,72,4\n"
"s_add_u32 m0, s96, 8192\ns_add_u32 s95, s72, 86016\nQmvaa 124,76,32\nbuffer_load_dwordx4 v61, s[52:55], s95 offen lds\nQmvaa 156,72,8\nQta 64,68,49408\nQmvaa 156,76,36\nQmvaa 188,72,12\n"
"Qmvaa 188,76,40\nQta 66,68,50432\ns_add_u32 s95, s72, 105472\nQmvaa 220,72,16\nbuffer_load_dwordx4 v61, s[52:55], s95 offen offset:1024 lds\nQmvaa 220,76,44\nQmvaa 252,72,20\nQta 68,68,57600\n"
"Qmvaa 252,76,48\ns_add_u32 m0, s96, 16384\ns_add_u32 s95, s72, 126976\nQmvaa 100,72,24\nbuffer_load_dwordx4 v61, s[52:55], s95 offen lds\nQmvaa 100,76,52\nQta 70,68,58624\ns_waitcnt lgkmcnt(4)\n"
"Qmaaa 224,56,24\nQmaaa 224,60,52\ns_add_u32 s95, s72, 146432\nQmaaa 220,56,20\nbuffer_load_dwordx4 v61, s[52:55], s95 offen offset:1024 lds\nQmaaa 220,60,48\nQmaaa 216,56,16\nQta 72,68,49664\n"
"Qmaaa 216,60,44\ns_add_u32 m0, s96, 24576\ns_add_u32 s95, s73, 86016\nQmaaa 212,56,12\nbuffer_load_dwordx4 v61, s[52:55], s95 offen lds\nQmaaa 212,60,40\nQta 74,68,50688\nQmaaa 208,56,8\n"
"Qmaaa 208,60,36\nQmaaa 204,56,4\nQta 76,68,57856\ns_add_u32 s95, s73, 105472\nQmaaa 204,60,32\nbuffer_load_dwordx4 v61, s[52:55], s95 offen offset:1024 lds\nQmaaa 200,56,0\nQmaaa 200,60,28\n"
"Qta 78,68,58880\ns_waitcnt lgkmcnt(4)\nQmaaa 228,64,0\ns_add_u32 m0, s96, 32768\ns_add_u32 s95, s73, 126976\nQmaaa 228,68,28\nbuffer_load_dwordx4 v61, s[52:55], s95 offen lds\nQmaaa 232,64,4\n"
"Qmaaa 232,68,32\nQmaaa 236,64,8\nQta 56,68,49920\ns_add_u32 s95, s73, 146432\nQmaaa 236,68,36\nbuffer_load_dwordx4 v61, s[52:55], s95 offen offset:1024 lds\nQmaaa 240,64,12\nQmaaa 240,68,40\n"
"Qta 58,68,50944\nQmaaa 244,64,16\nQmaaa 244,68,44\nQmaaa 248,64,20\nQta 60,68,58112\nQmaaa 248,68,48\nQmaaa 252,64,24\nQmaaa 252,68,52\n"
"Qta 62,68,59136\ns_waitcnt lgkmcnt(4)\nQmvaa 172,72,24\nQmvaa 172,76,52\nQmvaa 140,72,20\nQmvaa 140,76,48\nQmvaa 108,72,16\nQta 64,69,0\n"
"Qmvaa 108,76,44\nQmvaa 228,72,12\nQmvaa 228,76,40\nQta 66,69,512\nQmvaa 196,72,8\nQmvaa 196,76,36\nQmvaa 164,72,4\nQta 68,69,1024\n"
"Qmvaa 164,76,32\nv_add_u32 v75, 1, v75\nQmvaa 132,72,0\ns_waitcnt vmcnt(22)\nds_write_b32 v73, v75\nQmvaa 132,76,28\nQta 70,69,1536\ns_waitcnt lgkmcnt(5)\n"
"Qmvaa 204,56,0\ns_waitcnt vmcnt(24)\nQda 0,58,0\nQmvaa 204,60,28\nQmvaa 236,56,4\nQda 28,58,33792\nQmvaa 236,60,32\nQmvaa 96,56,8\n"
"Qta 72,69,8192\nQmvaa 96,60,36\nQda 4,58,1024\nQmvaa 128,56,12\nds_read_b32 v66, v74\nQmvaa 128,60,40\nQta 74,69,8704\nQmvaa 160,56,16\n"
"Qda 32,58,34816\nQmvaa 160,60,44\nQmvaa 192,56,20\ns_waitcnt lgkmcnt(2)\nv_cmp_gt_u32 vcc, v75, v66\nQmvaa 192,60,48\ns_cbranch_vccnz .Lso1805r1\n.Lro1805r1:\n"
"Qmvaa 224,56,24\nQta 76,69,9216\ns_add_u32 m0, s96, 40960\ns_add_u32 s95, s67, 86016\nQmvaa 224,60,52\nbuffer_load_dwordx4 v63, s[52:55], s95 offen lds\ns_waitcnt vmcnt(39)\ns_waitcnt lgkmcnt(8)\n"
"Qmaaa 172,64,80\nQda 8,58,2048\nQmaaa 172,68,108\nQta 78,69,9728\nQmaaa 176,64,84\nQda 36,58,35840\ns_add_u32 s95, s67, 105472\nQmaaa 176,68,112\n"
"buffer_load_dwordx4 v63, s[52:55], s95 offen offset:1024 lds\nQmaaa 180,64,88\nQta 56,69,16384\nQmaav 180,68,168\nQda 12,58,3072\nQmaaa 184,64,92\nQmaav 184,68,200\nQta 58,69,16896\n"
"s_add_u32 m0, s96, 49152\ns_add_u32 s95, s67, 126976\nQmaaa 188,64,96\nbuffer_load_dwordx4 v63, s[52:55], s95 offen lds\nQmaav 188,68,232\nQda 40,58,36864\nQmaaa 192,64,100\nQta 60,69,17408\n"
"Qmaav 192,68,212\nQda 16,58,4096\ns_add_u32 s95, s67, 146432\nQmaaa 196,64,104\nbuffer_load_dwordx4 v63, s[52:55], s95 offen offset:1024 lds\nQmaav 196,68,244\nQta 62,69,17920\ns_waitcnt vmcnt(12)\n"
"s_waitcnt lgkmcnt(8)\nQmaav 172,72,80\nQda 44,58,37888\nQmaav 172,76,88\ns_add_u32 m0, s96, 57344\ns_add_u32 s95, s72, 86016\nQmaav 176,72,112\nbuffer_load_dwordx4 v63, s[52:55], s95 offen lds\n"
"Qmaav 176,76,120\nQda 20,58,5120\nQmaav 180,72,144\nQta 64,69,24576\nv_add_u32 v75, 1, v75\nQmaav 180,76,152\ns_waitcnt vmcnt(5)\nds_write_b32 v73, v75\n"
"s_add_u32 s95, s72, 105472\nQmaav 184,72,176\nbuffer_load_dwordx4 v63, s[52:55], s95 offen offset:1024 lds\nQmaav 184,76,184\nQta 66,69,25600\nQmaav 188,72,208\nQda 48,58,38912\nQmaav 188,76,216\n"
"Qmaav 192,72,240\nQta 68,68,0\nQmaav 192,76,248\nQda 24,58,6144\nQmaav 196,72,84\nQmaav 196,76,116\nds_read_b32 v66, v74\ns_waitcnt lgkmcnt(9)\n"
"Qmaaa 172,56,0\nQta 70,68,1024\nQmaaa 172,60,28\nQda 52,58,39936\nQmaaa 176,56,4\nQmaaa 176,60,32\ns_waitcnt lgkmcnt(2)\nv_cmp_gt_u32 vcc, v75, v66\n"
"s_waitcnt lgkmcnt(11)\nQmaaa 180,56,8\ns_cbranch_vccnz .Lso1840r1\n.Lro1840r1:\nQmaaa 180,60,36\nQta 72,68,8192\nQmaaa 184,56,12\nds_write_b128 v58, a[172:175] offset:0\n"
"Qmaaa 184,60,40\ns_waitcnt lgkmcnt(2)\nQmaaa 188,56,16\nQta 74,68,9216\nQmaaa 188,60,44\nds_write_b128 v58, a[176:179] offset:1024\nQmaaa 192,56,20\nQmaaa 192,60,48\n"
"Qta 76,68,16384\nQmaaa 196,56,24\nds_write_b128 v58, a[180:183] offset:2048\nQmaaa 196,60,52\ns_waitcnt lgkmcnt(7)\nQmaaa 116,64,80\nQta 78,68,17408\nQmaaa 116,68,108\n"
"ds_write_b128 v58, a[184:187] offset:3072\nQmaaa 120,64,84\nQmaaa 120,68,112\nQmaaa 124,64,88\nQta 56,68,24576\nQmaav 124,68,168\nds_write_b128 v58, a[188:191] offset:4096\nQmaaa 128,64,92\n"
"Qmaav 128,68,200\nQta 58,68,25600\nQmaaa 132,64,96\nds_write_b128 v58, a[192:195] offset:5120\nQmaav 132,68,232\nQmaaa 136,64,100\nQta 60,68,32768\nQmaav 136,68,212\n"
"ds_write_b128 v58, a[196:199] offset:6144\nQmaaa 140,64,104\nQmaav 140,68,244\nQta 62,68,33792\ns_waitcnt lgkmcnt(8)\nQmaav 116,72,80\nv_add_u32 v75, 1, v75\nQmaav 116,76,88\n"
"ds_write_b32 v73, v75\nQmaav 120,72,112\nQmaav 120,76,120\nQmaav 124,72,144\nQta 64,69,256\nQmaav 124,76,152\nQmaav 128,72,176\nQmaav 128,76,184\n"
"Qta 66,69,768\nQmaav 132,72,208\nQmaav 132,76,216\nds_read_b32 v66, v74\nQmaav 136,72,240\nQta 68,69,1280\nQmaav 136,76,248\nQmaav 140,72,84\n"
"Qmaav 140,76,116\ns_waitcnt lgkmcnt(1)\nv_cmp_gt_u32 vcc, v75, v66\ns_waitcnt lgkmcnt(5)\nQmaaa 116,56,0\ns_cbranch_vccnz .Lso1878r1\n.Lro1878r1:\nQmaaa 116,60,28\n"
"Qta 70,69,1792\nQmaaa 120,56,4\nQmaaa 120,60,32\nQdv 148,59,2048\nQmaaa 124,56,8\nQta 72,69,8448\nQmaaa 124,60,36\ns_nop 0\n"
"ds_write_b128 v58, a[116:119] offset:33792\nQmaaa 128,56,12\nQdv 180,59,3072\nQmaaa 128,60,40\nQta 74,69,8960\nQmaaa 132,56,16\nds_write_b128 v58, a[120:123] offset:34816\nQmaaa 132,60,44\n"
"Qdv 104,59,5120\nQmaaa 136,56,20\nQta 76,69,9472\nQmaaa 136,60,48\nds_write_b128 v58, a[124:127] offset:35840\nQmaaa 140,56,24\nQdv 136,59,0\ns_waitcnt lgkmcnt(9)\n"
"Qa 156,156,148\nQmaaa 140,60,52\nQta 78,69,9984\nQa 157,157,149\ns_waitcnt lgkmcnt(10)\nQmvaa 92,64,80\nds_write_b128 v58, a[128:131] offset:36864\nQmvaa 92,68,108\n"
"Qa 158,158,150\nQa 159,159,151\nQmvaa 124,64,84\ns_waitcnt lgkmcnt(8)\nQa 188,188,180\nQa 189,189,181\nQmvaa 124,68,112\nds_write_b128 v58, a[132:135] offset:37888\n"
"Qmvaa 156,64,88\nQta 56,69,16640\nQa 190,190,182\nQa 191,191,183\nQmvav 156,68,168\nQdv 148,59,4096\ns_waitcnt lgkmcnt(8)\nQa 252,252,104\n"
"Qmvaa 188,64,92\nds_write_b128 v58, a[136:139] offset:38912\nQmvav 188,68,200\nQta 58,69,17152\nQa 253,253,105\nQmvaa 220,64,96\nQa 254,254,106\nQa 255,255,107\n"
"Qmvav 220,68,232\nQdv 180,59,6144\ns_waitcnt lgkmcnt(8)\nQa 92,92,136\nQmvaa 252,64,100\nQta 60,69,17664\nQa 93,93,137\nQmvav 252,68,212\n"
"ds_write_b128 v58, a[140:143] offset:39936\nQmvaa 100,64,104\nQa 94,94,138\nQa 95,95,139\nQmvav 100,68,244\nQta 62,69,18176\ns_waitcnt lgkmcnt(6)\nQa 220,220,148\n"
"s_waitcnt lgkmcnt(6)\nQmvav 92,72,80\nQdv 104,59,1024\nv_add_u32 v75, 1, v75\nQmvav 92,76,88\nds_write_b32 v73, v75\nQa 221,221,149\nQmvav 124,72,112\n"
"Qa 222,222,150\nQa 223,223,151\nQmvav 124,76,120\ns_waitcnt lgkmcnt(5)\nQa 100,100,180\nQa 101,101,181\nQmvav 156,72,144\nQta 64,69,24832\n"
"Qa 102,102,182\nQmvav 156,76,152\nQa 103,103,183\nQmvav 188,72,176\nQmvav 188,76,184\nQta 66,69,25856\ns_waitcnt lgkmcnt(3)\nQa 124,124,104\n"
"Qmvav 220,72,208\nQa 125,125,105\nQa 126,126,106\nQmvav 220,76,216\nds_read_b32 v66, v74\nQa 127,127,107\nQmvav 252,72,240\nQta 68,68,256\n"
"Qmvav 252,76,248\nQmvav 100,72,84\nQmvav 100,76,116\ns_waitcnt lgkmcnt(1)\nv_cmp_gt_u32 vcc, v75, v66\ns_waitcnt lgkmcnt(6)\nQmvaa 92,56,0\ns_cbranch_vccnz .Lso1920r1\n"
".Lro1920r1:\ns_add_u32 s68, s64, 0\nQmvaa 92,60,28\nQta 70,68,1280\ns_add_u32 m0, s96, 65536\ns_add_u32 s95, s72, 126976\nQmvaa 124,56,4\nbuffer_load_dwordx4 v63, s[52:55], s95 offen lds\n"
"Qmvaa 124,60,32\nQdv 136,59,33792\nQmvaa 156,56,8\nQta 72,68,8448\nQk 92,92,93\nQmvaa 156,60,36\ns_add_u32 s95, s72, 146432\nQk 93,94,95\n"
"Qmvaa 188,56,12\nbuffer_load_dwordx4 v63, s[52:55], s95 offen offset:1024 lds\nQmvaa 188,60,40\nQta 74,68,9472\nQk 94,124,125\nQmvaa 220,56,16\nQdv 148,59,34816\nQk 95,126,127\n"
"Qmvaa 220,60,44\nbuffer_store_dwordx4 v[92:95], v79, s[24:27], s68 offen offset:0 sc1\nQk 156,156,157\nQmvaa 252,56,20\nQta 76,68,16640\ns_add_u32 m0, s96, 73728\ns_add_u32 s95, s73, 86016\nQmvaa 252,60,48\n"
"buffer_load_dwordx4 v63, s[52:55], s95 offen lds\nv_add_u32 v75, 1, v75\nQmvaa 100,56,24\nds_write_b32 v73, v75\ns_waitcnt lgkmcnt(5)\nQa 0,0,136\nQmvaa 100,60,52\nQta 78,68,17664\n"
"Qa 1,1,137\ns_waitcnt lgkmcnt(6)\nQmaaa 144,64,80\nQdv 180,59,35840\ns_add_u32 s95, s73, 105472\nQmaaa 144,68,108\nbuffer_load_dwordx4 v63, s[52:55], s95 offen offset:1024 lds\nQmaaa 148,64,84\n"
"Qa 2,2,138\nQa 3,3,139\nQmaaa 148,68,112\nQdv 104,59,36864\ns_waitcnt lgkmcnt(5)\nQa 4,4,148\nQmaaa 152,64,88\nQta 56,68,24832\n"
"s_add_u32 m0, s96, 81920\ns_add_u32 s95, s73, 126976\nQmaav 152,68,168\nbuffer_load_dwordx4 v63, s[52:55], s95 offen lds\nQmaaa 156,64,92\nds_read_b32 v66, v74\nQa 5,5,149\nQa 6,6,150\n"
"Qa 7,7,151\nQmaav 156,68,200\nQta 58,68,25856\ns_waitcnt lgkmcnt(4)\nQa 8,8,180\nQmaaa 160,64,96\nQdv 136,59,37888\ns_add_u32 s95, s73, 146432\n"
"Qmaav 160,68,232\nbuffer_load_dwordx4 v63, s[52:55], s95 offen offset:1024 lds\nQmaaa 164,64,100\ns_waitcnt lgkmcnt(2)\nv_cmp_gt_u32 vcc, v75, v66\n.Lpg24:\ns_cbranch_execz .Ltr8\nQmaav 164,68,212\n"
"s_cbranch_vccnz .Lso1945r1\n.Lro1945r1:\nQa 9,9,181\nQa 10,10,182\nQa 11,11,183\nQmaaa 168,64,104\nQta 60,68,33024\ns_waitcnt lgkmcnt(5)\n"
"Qa 12,12,104\nQmaav 168,68,244\nQdv 148,59,38912\nQa 13,13,105\ns_waitcnt lgkmcnt(6)\nQmaav 144,72,80\nQa 14,14,106\nQa 15,15,107\n"
"Qmaav 144,76,88\nQta 62,68,34048\ns_waitcnt lgkmcnt(3)\nQa 16,16,136\nQmaav 148,72,112\nQdv 180,59,39936\nQa 17,17,137\nQmaav 148,76,120\n"
"Qa 18,18,138\nQa 19,19,139\nQmaav 152,72,144\nQta 64,69,25088\nv_bfe_u32 v61, v60, 6, 2\nQmaav 152,76,152\ns_waitcnt lgkmcnt(3)\nQa 20,20,148\n"
"Qa 21,21,149\nQmaav 156,72,176\nQa 22,22,150\nQa 23,23,151\nQmaav 156,76,184\nQta 66,69,26112\nv_lshrrev_b32 v62, 8, v60\nQmaav 160,72,208\n"
"v_xor_b32 v61, v61, v62\nv_bfe_u32 v62, v60, 5, 1\nQmaav 160,76,216\nv_lshl_or_b32 v61, v61, 1, v62\nv_and_b32 v62, 16, v60\nQmaav 164,72,240\nQta 68,68,512\ns_waitcnt lgkmcnt(3)\n"
"Qa 24,24,180\nQmaav 164,76,248\nQa 25,25,181\nQa 26,26,182\nQmaav 168,72,84\nQa 27,27,183\nv_mov_b32 v63, 0x3f80\nQmaav 168,76,116\n"
"Qta 70,68,1536\nv_lshlrev_b32 v63, v62, v63\ns_waitcnt lgkmcnt(5)\nQmaaa 144,56,0\nv_min_u32 v62, 1, v61\nv_add_u32 v62, -1, v62\nQmaaa 144,60,28\nv_and_b32 v62, v62, v63\n"
"v_xor_b32 v61, 1, v61\nQmaaa 148,56,4\nv_min_u32 v61, 1, v61\nv_add_u32 v61, -1, v61\nQmaaa 148,60,32\nv_and_b32 v61, v61, v63\nv_accvgpr_write_b32 a174, v62\nQmaaa 152,56,8\n"
"Qta 72,68,8704\nv_accvgpr_write_b32 a175, v61\nQmaaa 152,60,36\nds_write_b128 v58, a[144:147] offset:0\nQmaaa 156,56,12\nv_accvgpr_write_b32 a172, 0\nv_accvgpr_write_b32 a173, 0\nQmaaa 156,60,40\n"
"Qta 74,68,9728\nv_accvgpr_write_b32 a176, 0\nQmaaa 160,56,16\nds_write_b128 v58, a[148:151] offset:1024\nQmaaa 160,60,44\nv_accvgpr_write_b32 a177, 0\nQk 157,158,159\nQmaaa 164,56,20\n"
"Qta 76,68,16896\nQk 158,188,189\nQmaaa 164,60,48\nds_write_b128 v58, a[152:155] offset:2048\nQmaaa 168,56,24\nQk 159,190,191\nQk 220,220,221\nQmaaa 168,60,52\n"
"Qta 78,68,17920\nbuffer_store_dwordx4 v[156:159], v79, s[24:27], s68 offen offset:1024 sc1\ns_waitcnt lgkmcnt(7)\nQmvaa 0,64,80\nds_write_b128 v58, a[156:159] offset:3072\nQmvaa 0,68,108\nQk 221,222,223\nQk 222,252,253\n"
"Qmvaa 4,64,84\nQk 223,254,255\nQk 100,100,101\nQmvaa 4,68,112\nds_write_b128 v58, a[160:163] offset:4096\nQmvaa 8,64,88\nQta 56,68,25088\nbuffer_store_dwordx4 v[220:223], v79, s[24:27], s68 offen offset:2048 sc1\n"
"Qmvav 8,68,168\nQk 101,102,103\nQmvaa 12,64,92\nds_write_b128 v58, a[164:167] offset:5120\nQmvav 12,68,200\nQta 58,68,26112\nQmvaa 16,64,96\nQmvav 16,68,232\n"
"ds_write_b128 v58, a[168:171] offset:6144\nQmvaa 20,64,100\nQta 60,68,33280\nQmvav 20,68,212\nQmvaa 24,64,104\nQmvav 24,68,244\nQta 62,68,34304\ns_waitcnt lgkmcnt(8)\n"
"Qmvav 0,72,80\nv_add_u32 v75, 1, v75\nQmvav 0,76,88\ns_waitcnt vmcnt(11)\nds_write_b32 v73, v75\nQmvav 4,72,112\nQmvav 4,76,120\nQmvav 8,72,144\n"
"Qta 64,69,25344\nQmvav 8,76,152\nQmvav 12,72,176\nQmvav 12,76,184\nQta 66,69,26368\nQmvav 16,72,208\nQmvav 16,76,216\nds_read_b32 v66, v74\n"
"Qmvav 20,72,240\nQta 68,68,768\nQmvav 20,76,248\nQmvav 24,72,84\nQmvav 24,76,116\ns_waitcnt lgkmcnt(1)\nv_cmp_gt_u32 vcc, v75, v66\ns_waitcnt lgkmcnt(5)\n"
"Qmvaa 0,56,0\ns_cbranch_vccnz .Lso2004r1\n.Lro2004r1:\nQmvaa 0,60,28\nQta 70,68,1792\nQmvaa 4,56,4\nQmvaa 4,60,32\nQdv 104,59,2048\n"
"Qmvaa 8,56,8\nQta 72,68,8960\ns_nop 1\nQk 102,0,1\nQmvaa 8,60,36\nQk 103,2,3\nQmvaa 12,56,12\nQdv 136,59,3072\n"
"buffer_store_dwordx4 v[100:103], v79, s[24:27], s68 offen offset:3072 sc1\nQmvaa 12,60,40\nQta 74,68,9984\nQk 4,4,5\nQmvaa 16,56,16\nQk 5,6,7\nQk 6,8,9\nQmvaa 16,60,44\n"
"Qdv 148,59,5120\nQk 7,10,11\nQmvaa 20,56,20\nQta 76,68,17152\ns_add_u32 s68, s64, 4096\nQmvaa 20,60,48\ns_waitcnt lgkmcnt(5)\nQa 36,36,104\n"
"Qa 37,37,105\nQmvaa 24,56,24\nQdv 180,59,0\nQa 38,38,106\nQmvaa 24,60,52\nQta 78,68,18176\nQa 39,39,107\ns_waitcnt lgkmcnt(11)\n"
"Qmvaa 28,64,80\ns_waitcnt lgkmcnt(5)\nQa 40,40,136\nQa 41,41,137\nQmvaa 28,68,108\nQa 42,42,138\nQa 43,43,139\nQmvaa 32,64,84\n"
"buffer_store_dwordx4 v[4:7], v79, s[24:27], s68 offen offset:0 sc1\nQk 12,12,13\nQmvaa 32,68,112\ns_waitcnt lgkmcnt(3)\nQa 48,48,148\nQa 49,49,149\nQmvaa 36,64,88\nQta 56,68,25344\n"
"Qa 50,50,150\nQmvav 36,68,168\nQdv 104,59,4096\nQa 51,51,151\nQmvaa 40,64,92\ns_waitcnt lgkmcnt(3)\nQa 28,28,180\nQa 29,29,181\n"
"Qmvav 40,68,200\nQta 58,68,26368\nQa 30,30,182\nQmvaa 44,64,96\nQa 31,31,183\nQk 13,14,15\nQmvav 44,68,232\nQdv 136,59,6144\n"
"Qk 14,16,17\nQmvaa 48,64,100\nQta 60,68,33536\nQk 15,18,19\nQmvav 48,68,212\nbuffer_store_dwordx4 v[12:15], v79, s[24:27], s68 offen offset:1024 sc1\nQk 20,20,21\nQmvaa 52,64,104\n"
"Qk 21,22,23\nQk 22,24,25\nQmvav 52,68,244\nQta 62,68,34560\nv_add_u32 v75, 1, v75\ns_waitcnt lgkmcnt(6)\nQmvav 28,72,80\ns_waitcnt vmcnt(5)\n"
"ds_write_b32 v73, v75\nQmvav 28,76,88\nQdv 148,59,1024\ns_waitcnt lgkmcnt(6)\nQa 44,44,104\nQmvav 32,72,112\nQa 45,45,105\nQa 46,46,106\n"
"Qmvav 32,76,120\nQa 47,47,107\ns_waitcnt lgkmcnt(4)\nQa 52,52,136\nQmvav 36,72,144\nQta 64,68,40960\nQa 53,53,137\nQmvav 36,76,152\n"
"Qa 54,54,138\nQa 55,55,139\nQmvav 40,72,176\nQk 23,26,27\nQmvav 40,76,184\nQta 66,68,41984\ns_waitcnt lgkmcnt(2)\nQa 32,32,148\n"
"Qmvav 44,72,208\nds_read_b32 v66, v74\nQa 33,33,149\nQmvav 44,76,216\nQa 34,34,150\nQa 35,35,151\nQmvav 48,72,240\nQta 68,68,49152\n"
"buffer_store_dwordx4 v[20:23], v79, s[24:27], s68 offen offset:2048 sc1\nQmvav 48,76,248\nQmvav 52,72,84\ns_waitcnt lgkmcnt(1)\nv_cmp_gt_u32 vcc, v75, v66\nQmvav 52,76,116\ns_cbranch_vccnz .Lso2045r1\n.Lro2045r1:\n"
"s_waitcnt lgkmcnt(6)\nQmvaa 28,56,0\nQta 70,68,50176\nQmvaa 28,60,28\nQmvaa 32,56,4\nQmvaa 32,60,32\nQmvaa 36,56,8\nQta 72,68,57344\n"
"s_nop 3\nQk 28,28,29\nQmvaa 36,60,36\nQk 29,30,31\nQmvaa 40,56,12\nQk 30,32,33\nQk 31,34,35\nQmvaa 40,60,40\n"
"Qta 74,68,58368\nbuffer_store_dwordx4 v[28:31], v79, s[24:27], s68 offen offset:3072 sc1\nQmvaa 44,56,16\nQk 36,36,37\nQk 37,38,39\nQmvaa 44,60,44\ns_add_u32 s68, s64, 16384\nQmvaa 48,56,20\n"
"Qta 76,69,0\nQk 38,40,41\nQmvaa 48,60,48\nQk 39,42,43\nQmvaa 52,56,24\nbuffer_store_dwordx4 v[36:39], v79, s[24:27], s68 offen offset:0 sc1\nQk 44,44,45\nQmvaa 52,60,52\n"
"Qta 78,69,1024\nQk 45,46,47\ns_waitcnt lgkmcnt(4)\nQmaaa 200,64,80\nQk 46,48,49\nQk 47,50,51\nQmaaa 200,68,108\nbuffer_store_dwordx4 v[44:47], v79, s[24:27], s68 offen offset:1024 sc1\n"
"Qmaaa 204,64,84\nQk 52,52,53\nQk 53,54,55\nQmaaa 204,68,112\nv_mov_b32 v54, 0\nv_mov_b32 v55, 0\nQmaaa 208,64,88\nQta 56,69,8192\n"
"buffer_store_dwordx4 v[52:55], v79, s[24:27], s68 offen offset:2048 sc1\nQmaav 208,68,168\nQmaaa 212,64,92\nQmaav 212,68,200\nQta 58,69,9216\nQmaaa 216,64,96\nQmaav 216,68,232\nQmaaa 220,64,100\n"
"Qta 60,69,16384\nQmaav 220,68,212\nQmaaa 224,64,104\nQmaav 224,68,244\nQta 62,69,17408\ns_waitcnt lgkmcnt(4)\nQmaav 200,72,80\nQmaav 200,76,88\n"
"Qmaav 204,72,112\nQmaav 204,76,120\nQmaav 208,72,144\nQta 64,68,41216\nQmaav 208,76,152\nQmaav 212,72,176\nQmaav 212,76,184\nQta 66,68,42240\n"
"Qmaav 216,72,208\nQmaav 216,76,216\nQmaav 220,72,240\nQta 68,68,49408\nQmaav 220,76,248\nQmaav 224,72,84\nQmaav 224,76,116\nQta 70,68,50432\n"
"s_waitcnt lgkmcnt(4)\nQmaaa 200,56,0\nQmaaa 200,60,28\nQmaaa 204,56,4\nQmaaa 204,60,32\nQmaaa 208,56,8\nQta 72,68,57600\nQmaaa 208,60,36\n"
"s_nop 2\nds_write_b128 v58, a[200:203] offset:33792\nQmaaa 212,56,12\nQmaaa 212,60,40\nQta 74,68,58624\nQmaaa 216,56,16\nds_write_b128 v58, a[204:207] offset:34816\nQmaaa 216,60,44\n"
"Qmaaa 220,56,20\nQta 76,69,256\nQmaaa 220,60,48\nds_write_b128 v58, a[208:211] offset:35840\nQmaaa 224,56,24\nQmaaa 224,60,52\nQta 78,69,1280\ns_waitcnt lgkmcnt(7)\n"
"Qmaaa 228,64,80\nds_write_b128 v58, a[212:215] offset:36864\nQmaaa 228,68,108\nQmaaa 232,64,84\ns_waitcnt vmcnt(0)\nbuffer_store_dword v67, v77, s[24:27], 0 offen sc1\nQmaaa 232,68,112\nds_write_b128 v58, a[216:219] offset:37888\n"
"Qmaaa 236,64,88\nQta 56,69,8448\nQmaav 236,68,168\nQmaaa 240,64,92\nds_write_b128 v58, a[220:223] offset:38912\nQmaav 240,68,200\nQta 58,69,9472\nQmaaa 244,64,96\n"
"Qmaav 244,68,232\nds_write_b128 v58, a[224:227] offset:39936\nQmaaa 248,64,100\nQta 60,69,16640\nQmaav 248,68,212\nQmaaa 252,64,104\nQmaav 252,68,244\nQta 62,69,17664\n"
"s_waitcnt lgkmcnt(8)\nQmaav 228,72,80\nv_add_u32 v75, 1, v75\nQmaav 228,76,88\nds_write_b32 v73, v75\nQmaav 232,72,112\nQmaav 232,76,120\nQmaav 236,72,144\n"
"Qta 64,68,41472\nQmaav 236,76,152\nQmaav 240,72,176\nQmaav 240,76,184\nQta 66,68,42496\nQmaav 244,72,208\nQmaav 244,76,216\nds_read_b32 v66, v74\n"
"Qmaav 248,72,240\nQta 68,68,49664\nQmaav 248,76,248\nQmaav 252,72,84\nbuffer_load_dword v78, v76, s[24:27], 0 offen sc1\nQmaav 252,76,116\ns_waitcnt lgkmcnt(1)\nv_cmp_gt_u32 vcc, v75, v66\n"
"s_waitcnt lgkmcnt(5)\nQmaaa 228,56,0\ns_cbranch_vccnz .Lso2130r1\n.Lro2130r1:\nQmaaa 228,60,28\nQta 70,68,50688\nQmaaa 232,56,4\nQmaaa 232,60,32\n"
"Qdv 180,59,35840\nQmaaa 236,56,8\nQta 72,68,57856\nQmaaa 236,60,36\ns_nop 0\nds_write_b128 v58, a[228:231] offset:0\nQmaaa 240,56,12\nQdv 104,59,36864\n"
"Qmaaa 240,60,40\nQta 74,68,58880\nQmaaa 244,56,16\nds_write_b128 v58, a[232:235] offset:1024\nQmaaa 244,60,44\nQdv 136,59,38912\nQmaaa 248,56,20\nQta 76,69,512\n"
"Qmaaa 248,60,48\nds_write_b128 v58, a[236:239] offset:2048\nQmaaa 252,56,24\nQdv 148,59,33792\ns_waitcnt lgkmcnt(9)\nQa 196,196,180\nQmaaa 252,60,52\nQta 78,69,1536\n"
"Qa 197,197,181\ns_waitcnt lgkmcnt(10)\nQmvaa 132,64,80\nds_write_b128 v58, a[240:243] offset:3072\nQmvaa 132,68,108\nQa 198,198,182\nQa 199,199,183\nQmvaa 164,64,84\n"
"s_waitcnt lgkmcnt(8)\nQa 228,228,104\nQa 229,229,105\nQmvaa 164,68,112\nds_write_b128 v58, a[244:247] offset:4096\nQmvaa 196,64,88\nQta 56,69,8704\nQa 230,230,106\n"
"Qa 231,231,107\nQmvav 196,68,168\nQdv 180,59,37888\ns_waitcnt lgkmcnt(8)\nQa 140,140,136\nQmvaa 228,64,92\nds_write_b128 v58, a[248:251] offset:5120\nQmvav 228,68,200\n"
"Qta 58,69,9728\nQa 141,141,137\nQmvaa 108,64,96\nQa 142,142,138\nQa 143,143,139\nQmvav 108,68,232\nQdv 104,59,39936\ns_waitcnt lgkmcnt(8)\n"
"Qa 132,132,148\nQmvaa 140,64,100\nQta 60,69,16896\nQa 133,133,149\nQmvav 140,68,212\nds_write_b128 v58, a[252:255] offset:6144\nQmvaa 172,64,104\nQa 134,134,150\n"
"Qa 135,135,151\nQmvav 172,68,244\nQta 62,69,17920\ns_waitcnt lgkmcnt(6)\nQa 108,108,180\ns_waitcnt lgkmcnt(6)\nQmvav 132,72,80\nQdv 136,59,34816\n"
"v_add_u32 v75, 1, v75\nQmvav 132,76,88\nds_write_b32 v73, v75\nQa 109,109,181\nQmvav 164,72,112\nQa 110,110,182\nQa 111,111,183\nQmvav 164,76,120\n"
"s_waitcnt lgkmcnt(5)\nQa 172,172,104\nQa 173,173,105\nQmvav 196,72,144\nQta 64,68,41728\nQa 174,174,106\nQmvav 196,76,152\nQa 175,175,107\n"
"Qmvav 228,72,176\nQmvav 228,76,184\nQta 66,68,42752\ns_waitcnt lgkmcnt(3)\nQa 164,164,136\nQmvav 108,72,208\nQa 165,165,137\nQa 166,166,138\n"
"Qmvav 108,76,216\nds_read_b32 v66, v74\nQa 167,167,139\nQmvav 140,72,240\nQta 68,68,49920\nQmvav 140,76,248\nQmvav 172,72,84\nQmvav 172,76,116\n"
"s_waitcnt lgkmcnt(1)\nv_cmp_gt_u32 vcc, v75, v66\ns_waitcnt lgkmcnt(6)\nQmvaa 132,56,0\ns_cbranch_vccnz .Lso2172r1\n.Lro2172r1:\nQmvaa 132,60,28\ns_waitcnt vmcnt(0)\n"
"v_sub_u32 v66, v78, v65\nv_cmp_gt_u32 vcc, 1, v66\ns_and_b64 vcc, vcc, s[28:29]\ns_cbranch_scc1 .Lx1_w\n.Lx1_r:\nQmvaa 164,56,4\ns_add_u32 s69, s98, 0\nbuffer_load_dwordx4 a[180:183], v79, s[24:27], s69 offen offset:0 sc1\n"
"Qmvaa 164,60,32\nQta 70,68,50944\nQmvaa 196,56,8\nbuffer_load_dwordx4 a[184:187], v79, s[24:27], s69 offen offset:1024 sc1\nQmvaa 196,60,36\nQdv 148,59,2048\nQmvaa 228,56,12\nbuffer_load_dwordx4 a[188:191], v79, s[24:27], s69 offen offset:2048 sc1\n"
"Qmvaa 228,60,40\nQta 72,68,58112\nQmvaa 108,56,16\nbuffer_load_dwordx4 a[192:195], v79, s[24:27], s69 offen offset:3072 sc1\nQmvaa 108,60,44\nQdv 180,59,4096\nQmvaa 140,56,20\ns_add_u32 s69, s98, 4096\n"
"buffer_load_dwordx4 a[196:199], v79, s[24:27], s69 offen offset:0 sc1\nQmvaa 140,60,48\nQta 74,68,59136\ns_waitcnt lgkmcnt(3)\nQa 96,96,148\nQmvaa 172,56,24\nbuffer_load_dwordx4 a[116:119], v79, s[24:27], s69 offen offset:1024 sc1\nQmvaa 172,60,52\n"
"Qdv 104,59,0\nQa 97,97,149\ns_waitcnt lgkmcnt(4)\nQmvaa 204,64,80\nbuffer_load_dwordx4 a[120:123], v79, s[24:27], s69 offen offset:2048 sc1\nQmvaa 204,68,108\nQta 76,69,768\nQa 98,98,150\n"
"Qmvaa 236,64,84\nQdv 136,59,1024\nQa 99,99,151\nQmvaa 236,68,112\n.Lpg25:\ns_cbranch_execz .Ltr8\ns_waitcnt lgkmcnt(4)\nQa 160,160,180\n"
"Qa 161,161,181\nQmvaa 96,64,88\nQta 78,69,1792\nQa 162,162,182\nQmvav 96,68,168\nQdv 148,59,3072\nQa 163,163,183\nQmvaa 128,64,92\n"
"s_waitcnt lgkmcnt(4)\nQa 204,204,104\nQa 205,205,105\nQmvav 128,68,200\nQta 56,69,8960\nQa 206,206,106\nQmvaa 160,64,96\nQdv 180,59,5120\n"
"Qa 207,207,107\nQmvav 160,68,232\nQmvaa 192,64,100\nQta 58,69,9984\ns_waitcnt lgkmcnt(5)\nQa 236,236,136\nQmvav 192,68,212\nQa 237,237,137\n"
"Qa 238,238,138\nQmvaa 224,64,104\nQa 239,239,139\nQmvav 224,68,244\nQta 60,69,17152\ns_waitcnt lgkmcnt(4)\nQa 128,128,148\ns_waitcnt lgkmcnt(4)\n"
"Qmvav 204,72,80\nQa 129,129,149\nQa 130,130,150\nQmvav 204,76,88\nQa 131,131,151\nQmvav 236,72,112\nQta 62,69,18176\ns_waitcnt lgkmcnt(3)\n"
"Qa 192,192,180\nQmvav 236,76,120\nQa 193,193,181\nQa 194,194,182\nQmvav 96,72,144\nQa 195,195,183\nQmvav 96,76,152\nQmvav 128,72,176\n"
"Qmvav 128,76,184\nQmvav 160,72,208\nQmvav 160,76,216\nQdv 104,59,6144\nQmvav 192,72,240\nQmvav 192,76,248\nv_add_u32 v75, 1, v75\nQmvav 224,72,84\n"
"ds_write_b32 v73, v75\nQmvav 224,76,116\ns_waitcnt lgkmcnt(2)\nQmvaa 204,56,0\nQmvaa 204,60,28\nQmvaa 236,56,4\nQmvaa 236,60,32\ns_waitcnt lgkmcnt(1)\n"
"s_nop 1\nQa 224,224,104\nQa 225,225,105\nQmvaa 96,56,8\nQa 226,226,106\nQa 227,227,107\nQmvaa 96,60,36\nQmvaa 128,56,12\n"
"ds_read_b32 v66, v74\nQmvaa 128,60,40\nQmvaa 160,56,16\nQmvaa 160,60,44\nQmvaa 192,56,20\ns_waitcnt lgkmcnt(0)\nv_cmp_gt_u32 vcc, v75, v66\nQmvaa 192,60,48\n"
"s_cbranch_vccnz .Lso2225r1\n.Lro2225r1:\nQmvaa 224,56,24\nQmvaa 224,60,52\ns_waitcnt vmcnt(6)\nQmvaa 132,174,180\nQmvaa 164,172,180\ns_waitcnt vmcnt(5)\n"
"Qmvaa 196,174,184\nQmvaa 228,172,184\ns_nop 3\nQk 132,132,133\nQk 133,134,135\nds_write_b64 v56, v[132:133] offset:0\ns_waitcnt vmcnt(4)\nQmvaa 108,174,188\n"
"Qk 164,164,165\nQk 165,166,167\nds_write_b64 v56, v[164:165] offset:3328\nQmvaa 140,172,188\nQk 196,196,197\nQk 197,198,199\nds_write_b64 v56, v[196:197] offset:6656\ns_waitcnt vmcnt(3)\n"
"Qmvaa 172,174,192\nQk 228,228,229\nQk 229,230,231\nds_write_b64 v56, v[228:229] offset:9984\nQmvaa 204,172,192\nQk 108,108,109\nQk 109,110,111\nds_write_b64 v56, v[108:109] offset:13312\n"
"s_waitcnt vmcnt(2)\nQmvaa 236,174,196\nQk 140,140,141\nQk 141,142,143\nds_write_b64 v56, v[140:141] offset:16640\nQmvaa 96,172,196\nQk 172,172,173\nQk 173,174,175\n"
"ds_write_b64 v56, v[172:173] offset:19968\ns_waitcnt vmcnt(1)\nQmvaa 128,174,116\nQk 204,204,205\nQk 205,206,207\nds_write_b64 v56, v[204:205] offset:32\nQmvaa 160,172,116\nQk 236,236,237\n"
"Qk 237,238,239\nds_write_b64 v56, v[236:237] offset:3360\ns_waitcnt vmcnt(0)\nQmvaa 192,174,120\nQk 96,96,97\nQk 97,98,99\nds_write_b64 v56, v[96:97] offset:6688\nQmvaa 224,172,120\n"
"Qk 128,128,129\nQk 129,130,131\nds_write_b64 v56, v[128:129] offset:10016\nQk 160,160,161\nQk 161,162,163\nds_write_b64 v56, v[160:161] offset:13344\nQk 192,192,193\nQk 193,194,195\n"
"ds_write_b64 v56, v[192:193] offset:16672\nQk 224,224,225\nQk 225,226,227\nds_write_b64 v56, v[224:225] offset:20000\ns_waitcnt lgkmcnt(0)\ns_barrier\nQda 0,57,0\nQda 4,57,3328\n"
"Qda 8,57,6656\nQda 12,57,9984\nQda 16,57,13312\nQda 20,57,16640\nQda 24,57,19968\ns_mov_b32 s68, 0\ns_waitcnt lgkmcnt(6)\nbuffer_store_dwordx4 a[0:3], v64, s[40:43], s68 offen offset:0 sc1\n"
"s_mov_b32 s68, 40960\ns_waitcnt lgkmcnt(5)\nbuffer_store_dwordx4 a[4:7], v64, s[40:43], s68 offen offset:0 sc1\ns_mov_b32 s68, 81920\ns_waitcnt lgkmcnt(4)\nbuffer_store_dwordx4 a[8:11], v64, s[40:43], s68 offen offset:0 sc1\ns_mov_b32 s68, 122880\ns_waitcnt lgkmcnt(3)\n"
"buffer_store_dwordx4 a[12:15], v64, s[40:43], s68 offen offset:0 sc1\ns_mov_b32 s68, 163840\ns_waitcnt lgkmcnt(2)\nbuffer_store_dwordx4 a[16:19], v64, s[40:43], s68 offen offset:0 sc1\ns_mov_b32 s68, 204800\ns_waitcnt lgkmcnt(1)\nbuffer_store_dwordx4 a[20:23], v64, s[40:43], s68 offen offset:0 sc1\ns_mov_b32 s68, 245760\n"
"s_waitcnt lgkmcnt(0)\nbuffer_store_dwordx4 a[24:27], v64, s[40:43], s68 offen offset:0 sc1\n.Lfin:\n.Lpg26:\ns_cbranch_execz .Ltr8\ns_waitcnt vmcnt(0) lgkmcnt(0)\ns_branch .Lexit\n.Lend:\n"
".Lfb:\ns_waitcnt vmcnt(0) lgkmcnt(0)\nv_lshrrev_b32 v52, 4, v60\nv_and_b32 v53, 15, v52\nv_lshrrev_b32 v52, 4, v52\nv_lshlrev_b32 v66, 8, v53\nv_lshl_add_u32 v66, v52, 4, v66\nv_add_u32 v66, 1024, v66\n"
"v_lshlrev_b32 v67, 10, v52\nv_lshl_add_u32 v67, v53, 1, v67\nv_add_u32 v67, 1024, v67\nv_mov_b32 v52, 0x3f803f80\nv_accvgpr_write_b32 a204, v52\nv_accvgpr_write_b32 a205, v52\nv_accvgpr_write_b32 a206, v52\nv_accvgpr_write_b32 a207, v52\n"
"v_mov_b32 v24, 0xff800000\nv_mov_b32 v28, 0xff800000\nv_mov_b32 v32, 0xff800000\nv_mov_b32 v36, 0xff800000\nv_mov_b32 v40, 0xff800000\nv_mov_b32 v44, 0xff800000\nv_mov_b32 v48, 0xff800000\ns_lshl_b32 s89, s67, 21\n"
"s_lshl_b32 s68, s93, 18\ns_add_u32 s89, s89, s68\ns_mov_b32 s66, 0\n.Lfb1:\ns_add_u32 s68, s89, 4096\nbuffer_load_dwordx4 a[112:115], v66, s[40:43], s89 offen offset:0\nbuffer_load_dwordx4 a[116:119], v66, s[40:43], s89 offen offset:64\nbuffer_load_dwordx4 a[120:123], v66, s[40:43], s89 offen offset:128\n"
"buffer_load_dwordx4 a[124:127], v66, s[40:43], s89 offen offset:192\nbuffer_load_dwordx4 a[128:131], v66, s[40:43], s68 offen offset:0\nbuffer_load_dwordx4 a[132:135], v66, s[40:43], s68 offen offset:64\nbuffer_load_dwordx4 a[136:139], v66, s[40:43], s68 offen offset:128\nbuffer_load_dwordx4 a[140:143], v66, s[40:43], s68 offen offset:192\ns_waitcnt vmcnt(0)\nQzvaa 0,112,0\nQmvaa 0,116,4\n"
"Qmvaa 0,120,8\nQmvaa 0,124,12\nQzvaa 4,128,0\nQmvaa 4,132,4\nQmvaa 4,136,8\nQmvaa 4,140,12\ns_nop 7\nv_max3_f32 v24, v24, v0, v1\n"
"v_max3_f32 v24, v24, v2, v3\nv_max3_f32 v24, v24, v4, v5\nv_max3_f32 v24, v24, v6, v7\nQzvaa 0,112,16\nQmvaa 0,116,20\nQmvaa 0,120,24\nQmvaa 0,124,28\nQzvaa 4,128,16\n"
"Qmvaa 4,132,20\nQmvaa 4,136,24\nQmvaa 4,140,28\ns_nop 7\nv_max3_f32 v28, v28, v0, v1\nv_max3_f32 v28, v28, v2, v3\nv_max3_f32 v28, v28, v4, v5\nv_max3_f32 v28, v28, v6, v7\n"
"Qzvaa 0,112,32\nQmvaa 0,116,36\nQmvaa 0,120,40\nQmvaa 0,124,44\nQzvaa 4,128,32\nQmvaa 4,132,36\nQmvaa 4,136,40\nQmvaa 4,140,44\n"
"s_nop 7\nv_max3_f32 v32, v32, v0, v1\nv_max3_f32 v32, v32, v2, v3\nv_max3_f32 v32, v32, v4, v5\nv_max3_f32 v32, v32, v6, v7\nQzvaa 0,112,48\nQmvaa 0,116,52\nQmvaa 0,120,56\n"
"Qmvaa 0,124,60\nQzvaa 4,128,48\nQmvaa 4,132,52\nQmvaa 4,136,56\nQmvaa 4,140,60\ns_nop 7\nv_max3_f32 v36, v36, v0, v1\nv_max3_f32 v36, v36, v2, v3\n"
"v_max3_f32 v36, v36, v4, v5\nv_max3_f32 v36, v36, v6, v7\nQzvaa 0,112,64\nQmvaa 0,116,68\nQmvaa 0,120,72\nQmvaa 0,124,76\nQzvaa 4,128,64\nQmvaa 4,132,68\n"
"Qmvaa 4,136,72\nQmvaa 4,140,76\ns_nop 7\nv_max3_f32 v40, v40, v0, v1\nv_max3_f32 v40, v40, v2, v3\nv_max3_f32 v40, v40, v4, v5\nv_max3_f32 v40, v40, v6, v7\nQzvaa 0,112,80\n"
"Qmvaa 0,116,84\nQmvaa 0,120,88\nQmvaa 0,124,92\nQzvaa 4,128,80\nQmvaa 4,132,84\nQmvaa 4,136,88\nQmvaa 4,140,92\ns_nop 7\n"
"v_max3_f32 v44, v44, v0, v1\nv_max3_f32 v44, v44, v2, v3\nv_max3_f32 v44, v44, v4, v5\nv_max3_f32 v44, v44, v6, v7\nQzvaa 0,112,96\nQmvaa 0,116,100\nQmvaa 0,120,104\nQmvaa 0,124,108\n"
"Qzvaa 4,128,96\nQmvaa 4,132,100\nQmvaa 4,136,104\nQmvaa 4,140,108\ns_nop 7\nv_max3_f32 v48, v48, v0, v1\nv_max3_f32 v48, v48, v2, v3\nv_max3_f32 v48, v48, v4, v5\n"
"v_max3_f32 v48, v48, v6, v7\ns_add_u32 s89, s89, 8192\ns_add_u32 s66, s66, 1\ns_cmp_lt_u32 s66, 32\ns_cbranch_scc1 .Lfb1\nv_mov_b32 v25, v24\ns_nop 1\nv_permlane32_swap_b32 v24, v25\n"
"v_max_f32 v24, v24, v25\nv_mov_b32 v25, v24\ns_nop 1\nv_permlane16_swap_b32 v24, v25\nv_max_f32 v24, v24, v25\nv_sub_f32 v24, 0, v24\nv_mov_b32 v25, v24\nv_mov_b32 v26, v24\n"
"v_mov_b32 v27, v24\nv_mov_b32 v29, v28\ns_nop 1\nv_permlane32_swap_b32 v28, v29\nv_max_f32 v28, v28, v29\nv_mov_b32 v29, v28\ns_nop 1\nv_permlane16_swap_b32 v28, v29\n"
"v_max_f32 v28, v28, v29\nv_sub_f32 v28, 0, v28\nv_mov_b32 v29, v28\nv_mov_b32 v30, v28\nv_mov_b32 v31, v28\nv_mov_b32 v33, v32\ns_nop 1\nv_permlane32_swap_b32 v32, v33\n"
"v_max_f32 v32, v32, v33\nv_mov_b32 v33, v32\ns_nop 1\nv_permlane16_swap_b32 v32, v33\nv_max_f32 v32, v32, v33\nv_sub_f32 v32, 0, v32\nv_mov_b32 v33, v32\nv_mov_b32 v34, v32\n"
"v_mov_b32 v35, v32\nv_mov_b32 v37, v36\ns_nop 1\nv_permlane32_swap_b32 v36, v37\nv_max_f32 v36, v36, v37\nv_mov_b32 v37, v36\ns_nop 1\nv_permlane16_swap_b32 v36, v37\n"
"v_max_f32 v36, v36, v37\nv_sub_f32 v36, 0, v36\nv_mov_b32 v37, v36\nv_mov_b32 v38, v36\nv_mov_b32 v39, v36\nv_mov_b32 v41, v40\ns_nop 1\nv_permlane32_swap_b32 v40, v41\n"
"v_max_f32 v40, v40, v41\nv_mov_b32 v41, v40\ns_nop 1\nv_permlane16_swap_b32 v40, v41\nv_max_f32 v40, v40, v41\nv_sub_f32 v40, 0, v40\nv_mov_b32 v41, v40\nv_mov_b32 v42, v40\n"
"v_mov_b32 v43, v40\nv_mov_b32 v45, v44\ns_nop 1\nv_permlane32_swap_b32 v44, v45\nv_max_f32 v44, v44, v45\nv_mov_b32 v45, v44\ns_nop 1\nv_permlane16_swap_b32 v44, v45\n"
"v_max_f32 v44, v44, v45\nv_sub_f32 v44, 0, v44\nv_mov_b32 v45, v44\nv_mov_b32 v46, v44\nv_mov_b32 v47, v44\nv_mov_b32 v49, v48\ns_nop 1\nv_permlane32_swap_b32 v48, v49\n"
"v_max_f32 v48, v48, v49\nv_mov_b32 v49, v48\ns_nop 1\nv_permlane16_swap_b32 v48, v49\nv_max_f32 v48, v48, v49\nv_sub_f32 v48, 0, v48\nv_mov_b32 v49, v48\nv_mov_b32 v50, v48\n"
"v_mov_b32 v51, v48\nv_mov_b32 v52, 0\nv_mov_b32 v80, 0\nv_mov_b32 v81, 0\nv_mov_b32 v82, 0\nv_mov_b32 v83, 0\nv_mov_b32 v84, 0\nv_mov_b32 v85, 0\n"
"v_mov_b32 v86, 0\nv_mov_b32 v87, 0\nv_mov_b32 v88, 0\nv_mov_b32 v89, 0\nv_mov_b32 v90, 0\nv_mov_b32 v91, 0\nv_mov_b32 v92, 0\nv_mov_b32 v93, 0\n"
"v_mov_b32 v94, 0\nv_mov_b32 v95, 0\nv_mov_b32 v96, 0\nv_mov_b32 v97, 0\nv_mov_b32 v98, 0\nv_mov_b32 v99, 0\nv_mov_b32 v100, 0\nv_mov_b32 v101, 0\n"
"v_mov_b32 v102, 0\nv_mov_b32 v103, 0\nv_mov_b32 v104, 0\nv_mov_b32 v105, 0\nv_mov_b32 v106, 0\nv_mov_b32 v107, 0\nv_mov_b32 v108, 0\nv_mov_b32 v109, 0\n"
"v_mov_b32 v110, 0\nv_mov_b32 v111, 0\nv_accvgpr_write_b32 a176, v52\nv_accvgpr_write_b32 a177, v52\nv_accvgpr_write_b32 a178, v52\nv_accvgpr_write_b32 a179, v52\nv_mov_b32 v112, 0\nv_mov_b32 v113, 0\n"
"v_mov_b32 v114, 0\nv_mov_b32 v115, 0\nv_mov_b32 v116, 0\nv_mov_b32 v117, 0\nv_mov_b32 v118, 0\nv_mov_b32 v119, 0\nv_mov_b32 v120, 0\nv_mov_b32 v121, 0\n"
"v_mov_b32 v122, 0\nv_mov_b32 v123, 0\nv_mov_b32 v124, 0\nv_mov_b32 v125, 0\nv_mov_b32 v126, 0\nv_mov_b32 v127, 0\nv_mov_b32 v128, 0\nv_mov_b32 v129, 0\n"
"v_mov_b32 v130, 0\nv_mov_b32 v131, 0\nv_mov_b32 v132, 0\nv_mov_b32 v133, 0\nv_mov_b32 v134, 0\nv_mov_b32 v135, 0\nv_mov_b32 v136, 0\nv_mov_b32 v137, 0\n"
"v_mov_b32 v138, 0\nv_mov_b32 v139, 0\nv_mov_b32 v140, 0\nv_mov_b32 v141, 0\nv_mov_b32 v142, 0\nv_mov_b32 v143, 0\nv_accvgpr_write_b32 a180, v52\nv_accvgpr_write_b32 a181, v52\n"
"v_accvgpr_write_b32 a182, v52\nv_accvgpr_write_b32 a183, v52\nv_mov_b32 v144, 0\nv_mov_b32 v145, 0\nv_mov_b32 v146, 0\nv_mov_b32 v147, 0\nv_mov_b32 v148, 0\nv_mov_b32 v149, 0\n"
"v_mov_b32 v150, 0\nv_mov_b32 v151, 0\nv_mov_b32 v152, 0\nv_mov_b32 v153, 0\nv_mov_b32 v154, 0\nv_mov_b32 v155, 0\nv_mov_b32 v156, 0\nv_mov_b32 v157, 0\n"
"v_mov_b32 v158, 0\nv_mov_b32 v159, 0\nv_mov_b32 v160, 0\nv_mov_b32 v161, 0\nv_mov_b32 v162, 0\nv_mov_b32 v163, 0\nv_mov_b32 v164, 0\nv_mov_b32 v165, 0\n"
"v_mov_b32 v166, 0\nv_mov_b32 v167, 0\nv_mov_b32 v168, 0\nv_mov_b32 v169, 0\nv_mov_b32 v170, 0\nv_mov_b32 v171, 0\nv_mov_b32 v172, 0\nv_mov_b32 v173, 0\n"
"v_mov_b32 v174, 0\nv_mov_b32 v175, 0\nv_accvgpr_write_b32 a184, v52\nv_accvgpr_write_b32 a185, v52\nv_accvgpr_write_b32 a186, v52\nv_accvgpr_write_b32 a187, v52\nv_mov_b32 v176, 0\nv_mov_b32 v177, 0\n"
"v_mov_b32 v178, 0\nv_mov_b32 v179, 0\nv_mov_b32 v180, 0\nv_mov_b32 v181, 0\nv_mov_b32 v182, 0\nv_mov_b32 v183, 0\nv_mov_b32 v184, 0\nv_mov_b32 v185, 0\n"
"v_mov_b32 v186, 0\nv_mov_b32 v187, 0\nv_mov_b32 v188, 0\nv_mov_b32 v189, 0\nv_mov_b32 v190, 0\nv_mov_b32 v191, 0\nv_mov_b32 v192, 0\nv_mov_b32 v193, 0\n"
"v_mov_b32 v194, 0\nv_mov_b32 v195, 0\nv_mov_b32 v196, 0\nv_mov_b32 v197, 0\nv_mov_b32 v198, 0\nv_mov_b32 v199, 0\nv_mov_b32 v200, 0\nv_mov_b32 v201, 0\n"
"v_mov_b32 v202, 0\nv_mov_b32 v203, 0\nv_mov_b32 v204, 0\nv_mov_b32 v205, 0\nv_mov_b32 v206, 0\nv_mov_b32 v207, 0\nv_accvgpr_write_b32 a188, v52\nv_accvgpr_write_b32 a189, v52\n"
"v_accvgpr_write_b32 a190, v52\nv_accvgpr_write_b32 a191, v52\nv_mov_b32 v208, 0\nv_mov_b32 v209, 0\nv_mov_b32 v210, 0\nv_mov_b32 v211, 0\nv_mov_b32 v212, 0\nv_mov_b32 v213, 0\n"
"v_mov_b32 v214, 0\nv_mov_b32 v215, 0\nv_mov_b32 v216, 0\nv_mov_b32 v217, 0\nv_mov_b32 v218, 0\nv_mov_b32 v219, 0\nv_mov_b32 v220, 0\nv_mov_b32 v221, 0\n"
"v_mov_b32 v222, 0\nv_mov_b32 v223, 0\nv_mov_b32 v224, 0\nv_mov_b32 v225, 0\nv_mov_b32 v226, 0\nv_mov_b32 v227, 0\nv_mov_b32 v228, 0\nv_mov_b32 v229, 0\n"
"v_mov_b32 v230, 0\nv_mov_b32 v231, 0\nv_mov_b32 v232, 0\nv_mov_b32 v233, 0\nv_mov_b32 v234, 0\nv_mov_b32 v235, 0\nv_mov_b32 v236, 0\nv_mov_b32 v237, 0\n"
"v_mov_b32 v238, 0\nv_mov_b32 v239, 0\nv_accvgpr_write_b32 a192, v52\nv_accvgpr_write_b32 a193, v52\nv_accvgpr_write_b32 a194, v52\nv_accvgpr_write_b32 a195, v52\nv_mov_b32 v240, 0\nv_mov_b32 v241, 0\n"
"v_mov_b32 v242, 0\nv_mov_b32 v243, 0\nv_mov_b32 v244, 0\nv_mov_b32 v245, 0\nv_mov_b32 v246, 0\nv_mov_b32 v247, 0\nv_mov_b32 v248, 0\nv_mov_b32 v249, 0\n"
"v_mov_b32 v250, 0\nv_mov_b32 v251, 0\nv_mov_b32 v252, 0\nv_mov_b32 v253, 0\nv_mov_b32 v254, 0\nv_mov_b32 v255, 0\nv_accvgpr_write_b32 a208, v52\nv_accvgpr_write_b32 a209, v52\n"
"v_accvgpr_write_b32 a210, v52\nv_accvgpr_write_b32 a211, v52\nv_accvgpr_write_b32 a212, v52\nv_accvgpr_write_b32 a213, v52\nv_accvgpr_write_b32 a214, v52\nv_accvgpr_write_b32 a215, v52\nv_accvgpr_write_b32 a216, v52\nv_accvgpr_write_b32 a217, v52\n"
"v_accvgpr_write_b32 a218, v52\nv_accvgpr_write_b32 a219, v52\nv_accvgpr_write_b32 a220, v52\nv_accvgpr_write_b32 a221, v52\nv_accvgpr_write_b32 a222, v52\nv_accvgpr_write_b32 a223, v52\nv_accvgpr_write_b32 a196, v52\nv_accvgpr_write_b32 a197, v52\n"
"v_accvgpr_write_b32 a198, v52\nv_accvgpr_write_b32 a199, v52\nv_accvgpr_write_b32 a224, v52\nv_accvgpr_write_b32 a225, v52\nv_accvgpr_write_b32 a226, v52\nv_accvgpr_write_b32 a227, v52\nv_accvgpr_write_b32 a228, v52\nv_accvgpr_write_b32 a229, v52\n"
"v_accvgpr_write_b32 a230, v52\nv_accvgpr_write_b32 a231, v52\nv_accvgpr_write_b32 a232, v52\nv_accvgpr_write_b32 a233, v52\nv_accvgpr_write_b32 a234, v52\nv_accvgpr_write_b32 a235, v52\nv_accvgpr_write_b32 a236, v52\nv_accvgpr_write_b32 a237, v52\n"
"v_accvgpr_write_b32 a238, v52\nv_accvgpr_write_b32 a239, v52\nv_accvgpr_write_b32 a240, v52\nv_accvgpr_write_b32 a241, v52\nv_accvgpr_write_b32 a242, v52\nv_accvgpr_write_b32 a243, v52\nv_accvgpr_write_b32 a244, v52\nv_accvgpr_write_b32 a245, v52\n"
"v_accvgpr_write_b32 a246, v52\nv_accvgpr_write_b32 a247, v52\nv_accvgpr_write_b32 a248, v52\nv_accvgpr_write_b32 a249, v52\nv_accvgpr_write_b32 a250, v52\nv_accvgpr_write_b32 a251, v52\nv_accvgpr_write_b32 a252, v52\nv_accvgpr_write_b32 a253, v52\n"
"v_accvgpr_write_b32 a254, v52\nv_accvgpr_write_b32 a255, v52\nv_accvgpr_write_b32 a200, v52\nv_accvgpr_write_b32 a201, v52\nv_accvgpr_write_b32 a202, v52\nv_accvgpr_write_b32 a203, v52\ns_lshl_b32 s89, s67, 21\ns_lshl_b32 s68, s93, 18\n"
"s_add_u32 s89, s89, s68\ns_mov_b32 s66, 0\n.Lfb2:\ns_add_u32 s68, s89, 4096\nbuffer_load_dwordx4 a[112:115], v66, s[40:43], s89 offen offset:0\nbuffer_load_dwordx4 a[116:119], v66, s[40:43], s89 offen offset:64\nbuffer_load_dwordx4 a[120:123], v66, s[40:43], s89 offen offset:128\nbuffer_load_dwordx4 a[124:127], v66, s[40:43], s89 offen offset:192\n"
"buffer_load_dwordx4 a[128:131], v66, s[40:43], s68 offen offset:0\nbuffer_load_dwordx4 a[132:135], v66, s[40:43], s68 offen offset:64\nbuffer_load_dwordx4 a[136:139], v66, s[40:43], s68 offen offset:128\nbuffer_load_dwordx4 a[140:143], v66, s[40:43], s68 offen offset:192\nbuffer_load_ushort v8, v67, s[44:47], s89 offen offset:0\nbuffer_load_ushort v9, v67, s[44:47], s89 offen offset:256\nbuffer_load_ushort v10, v67, s[44:47], s89 offen offset:512\nbuffer_load_ushort v11, v67, s[44:47], s89 offen offset:768\n"
"buffer_load_ushort v12, v67, s[44:47], s68 offen offset:0\nbuffer_load_ushort v13, v67, s[44:47], s68 offen offset:256\nbuffer_load_ushort v14, v67, s[44:47], s68 offen offset:512\nbuffer_load_ushort v15, v67, s[44:47], s68 offen offset:768\ns_waitcnt vmcnt(0)\nv_lshl_or_b32 v8, v9, 16, v8\nv_accvgpr_write_b32 a144, v8\nv_lshl_or_b32 v10, v11, 16, v10\n"
"v_accvgpr_write_b32 a145, v10\nv_lshl_or_b32 v12, v13, 16, v12\nv_accvgpr_write_b32 a146, v12\nv_lshl_or_b32 v14, v15, 16, v14\nv_accvgpr_write_b32 a147, v14\nbuffer_load_ushort v8, v67, s[44:47], s89 offen offset:32\nbuffer_load_ushort v9, v67, s[44:47], s89 offen offset:288\nbuffer_load_ushort v10, v67, s[44:47], s89 offen offset:544\n"
"buffer_load_ushort v11, v67, s[44:47], s89 offen offset:800\nbuffer_load_ushort v12, v67, s[44:47], s68 offen offset:32\nbuffer_load_ushort v13, v67, s[44:47], s68 offen offset:288\nbuffer_load_ushort v14, v67, s[44:47], s68 offen offset:544\nbuffer_load_ushort v15, v67, s[44:47], s68 offen offset:800\ns_waitcnt vmcnt(0)\nv_lshl_or_b32 v8, v9, 16, v8\nv_accvgpr_write_b32 a148, v8\n"
"v_lshl_or_b32 v10, v11, 16, v10\nv_accvgpr_write_b32 a149, v10\nv_lshl_or_b32 v12, v13, 16, v12\nv_accvgpr_write_b32 a150, v12\nv_lshl_or_b32 v14, v15, 16, v14\nv_accvgpr_write_b32 a151, v14\nbuffer_load_ushort v8, v67, s[44:47], s89 offen offset:64\nbuffer_load_ushort v9, v67, s[44:47], s89 offen offset:320\n"
"buffer_load_ushort v10, v67, s[44:47], s89 offen offset:576\nbuffer_load_ushort v11, v67, s[44:47], s89 offen offset:832\nbuffer_load_ushort v12, v67, s[44:47], s68 offen offset:64\nbuffer_load_ushort v13, v67, s[44:47], s68 offen offset:320\nbuffer_load_ushort v14, v67, s[44:47], s68 offen offset:576\nbuffer_load_ushort v15, v67, s[44:47], s68 offen offset:832\ns_waitcnt vmcnt(0)\nv_lshl_or_b32 v8, v9, 16, v8\n"
"v_accvgpr_write_b32 a152, v8\nv_lshl_or_b32 v10, v11, 16, v10\nv_accvgpr_write_b32 a153, v10\nv_lshl_or_b32 v12, v13, 16, v12\nv_accvgpr_write_b32 a154, v12\nv_lshl_or_b32 v14, v15, 16, v14\nv_accvgpr_write_b32 a155, v14\nbuffer_load_ushort v8, v67, s[44:47], s89 offen offset:96\n"
"buffer_load_ushort v9, v67, s[44:47], s89 offen offset:352\nbuffer_load_ushort v10, v67, s[44:47], s89 offen offset:608\nbuffer_load_ushort v11, v67, s[44:47], s89 offen offset:864\nbuffer_load_ushort v12, v67, s[44:47], s68 offen offset:96\nbuffer_load_ushort v13, v67, s[44:47], s68 offen offset:352\nbuffer_load_ushort v14, v67, s[44:47], s68 offen offset:608\nbuffer_load_ushort v15, v67, s[44:47], s68 offen offset:864\ns_waitcnt vmcnt(0)\n"
"v_lshl_or_b32 v8, v9, 16, v8\nv_accvgpr_write_b32 a156, v8\nv_lshl_or_b32 v10, v11, 16, v10\nv_accvgpr_write_b32 a157, v10\nv_lshl_or_b32 v12, v13, 16, v12\nv_accvgpr_write_b32 a158, v12\nv_lshl_or_b32 v14, v15, 16, v14\nv_accvgpr_write_b32 a159, v14\n"
"buffer_load_ushort v8, v67, s[44:47], s89 offen offset:128\nbuffer_load_ushort v9, v67, s[44:47], s89 offen offset:384\nbuffer_load_ushort v10, v67, s[44:47], s89 offen offset:640\nbuffer_load_ushort v11, v67, s[44:47], s89 offen offset:896\nbuffer_load_ushort v12, v67, s[44:47], s68 offen offset:128\nbuffer_load_ushort v13, v67, s[44:47], s68 offen offset:384\nbuffer_load_ushort v14, v67, s[44:47], s68 offen offset:640\nbuffer_load_ushort v15, v67, s[44:47], s68 offen offset:896\n"
"s_waitcnt vmcnt(0)\nv_lshl_or_b32 v8, v9, 16, v8\nv_accvgpr_write_b32 a160, v8\nv_lshl_or_b32 v10, v11, 16, v10\nv_accvgpr_write_b32 a161, v10\nv_lshl_or_b32 v12, v13, 16, v12\nv_accvgpr_write_b32 a162, v12\nv_lshl_or_b32 v14, v15, 16, v14\n"
"v_accvgpr_write_b32 a163, v14\nbuffer_load_ushort v8, v67, s[44:47], s89 offen offset:160\nbuffer_load_ushort v9, v67, s[44:47], s89 offen offset:416\nbuffer_load_ushort v10, v67, s[44:47], s89 offen offset:672\nbuffer_load_ushort v11, v67, s[44:47], s89 offen offset:928\nbuffer_load_ushort v12, v67, s[44:47], s68 offen offset:160\nbuffer_load_ushort v13, v67, s[44:47], s68 offen offset:416\nbuffer_load_ushort v14, v67, s[44:47], s68 offen offset:672\n"
"buffer_load_ushort v15, v67, s[44:47], s68 offen offset:928\ns_waitcnt vmcnt(0)\nv_lshl_or_b32 v8, v9, 16, v8\nv_accvgpr_write_b32 a164, v8\nv_lshl_or_b32 v10, v11, 16, v10\nv_accvgpr_write_b32 a165, v10\nv_lshl_or_b32 v12, v13, 16, v12\nv_accvgpr_write_b32 a166, v12\n"
"v_lshl_or_b32 v14, v15, 16, v14\nv_accvgpr_write_b32 a167, v14\nbuffer_load_ushort v8, v67, s[44:47], s89 offen offset:192\nbuffer_load_ushort v9, v67, s[44:47], s89 offen offset:448\nbuffer_load_ushort v10, v67, s[44:47], s89 offen offset:704\nbuffer_load_ushort v11, v67, s[44:47], s89 offen offset:960\nbuffer_load_ushort v12, v67, s[44:47], s68 offen offset:192\nbuffer_load_ushort v13, v67, s[44:47], s68 offen offset:448\n"
"buffer_load_ushort v14, v67, s[44:47], s68 offen offset:704\nbuffer_load_ushort v15, v67, s[44:47], s68 offen offset:960\ns_waitcnt vmcnt(0)\nv_lshl_or_b32 v8, v9, 16, v8\nv_accvgpr_write_b32 a168, v8\nv_lshl_or_b32 v10, v11, 16, v10\nv_accvgpr_write_b32 a169, v10\nv_lshl_or_b32 v12, v13, 16, v12\n"
"v_accvgpr_write_b32 a170, v12\nv_lshl_or_b32 v14, v15, 16, v14\nv_accvgpr_write_b32 a171, v14\nbuffer_load_ushort v8, v67, s[44:47], s89 offen offset:224\nbuffer_load_ushort v9, v67, s[44:47], s89 offen offset:480\nbuffer_load_ushort v10, v67, s[44:47], s89 offen offset:736\nbuffer_load_ushort v11, v67, s[44:47], s89 offen offset:992\nbuffer_load_ushort v12, v67, s[44:47], s68 offen offset:224\n"
"buffer_load_ushort v13, v67, s[44:47], s68 offen offset:480\nbuffer_load_ushort v14, v67, s[44:47], s68 offen offset:736\nbuffer_load_ushort v15, v67, s[44:47], s68 offen offset:992\ns_waitcnt vmcnt(0)\nv_lshl_or_b32 v8, v9, 16, v8\nv_accvgpr_write_b32 a172, v8\nv_lshl_or_b32 v10, v11, 16, v10\nv_accvgpr_write_b32 a173, v10\n"
"v_lshl_or_b32 v12, v13, 16, v12\nv_accvgpr_write_b32 a174, v12\nv_lshl_or_b32 v14, v15, 16, v14\nv_accvgpr_write_b32 a175, v14\nQcvaa 0,112,0,24\nQmvaa 0,116,4\nQmvaa 0,120,8\nQmvaa 0,124,12\n"
"Qcvaa 4,128,0,24\nQmvaa 4,132,4\nQmvaa 4,136,8\nQmvaa 4,140,12\ns_nop 7\nQe 0\nQe 1\nQe 2\n"
"Qe 3\nQe 4\nQe 5\nQe 6\nQe 7\ns_nop 0\nQk 16,0,1\nQk 17,2,3\n"
"Qk 18,4,5\nQk 19,6,7\ns_nop 1\nQmvva 80,16,144\nQmvva 84,16,148\nQmvva 88,16,152\nQmvva 92,16,156\nQmvva 96,16,160\n"
"Qmvva 100,16,164\nQmvva 104,16,168\nQmvva 108,16,172\nQmaav 176,204,16\nQcvaa 0,112,16,28\nQmvaa 0,116,20\nQmvaa 0,120,24\nQmvaa 0,124,28\n"
"Qcvaa 4,128,16,28\nQmvaa 4,132,20\nQmvaa 4,136,24\nQmvaa 4,140,28\ns_nop 7\nQe 0\nQe 1\nQe 2\n"
"Qe 3\nQe 4\nQe 5\nQe 6\nQe 7\ns_nop 0\nQk 16,0,1\nQk 17,2,3\n"
"Qk 18,4,5\nQk 19,6,7\ns_nop 1\nQmvva 112,16,144\nQmvva 116,16,148\nQmvva 120,16,152\nQmvva 124,16,156\nQmvva 128,16,160\n"
"Qmvva 132,16,164\nQmvva 136,16,168\nQmvva 140,16,172\nQmaav 180,204,16\nQcvaa 0,112,32,32\nQmvaa 0,116,36\nQmvaa 0,120,40\nQmvaa 0,124,44\n"
"Qcvaa 4,128,32,32\nQmvaa 4,132,36\nQmvaa 4,136,40\nQmvaa 4,140,44\ns_nop 7\nQe 0\nQe 1\nQe 2\n"
"Qe 3\nQe 4\nQe 5\nQe 6\nQe 7\ns_nop 0\nQk 16,0,1\nQk 17,2,3\n"
"Qk 18,4,5\nQk 19,6,7\ns_nop 1\nQmvva 144,16,144\nQmvva 148,16,148\nQmvva 152,16,152\nQmvva 156,16,156\nQmvva 160,16,160\n"
"Qmvva 164,16,164\nQmvva 168,16,168\nQmvva 172,16,172\nQmaav 184,204,16\nQcvaa 0,112,48,36\nQmvaa 0,116,52\nQmvaa 0,120,56\nQmvaa 0,124,60\n"
"Qcvaa 4,128,48,36\nQmvaa 4,132,52\nQmvaa 4,136,56\nQmvaa 4,140,60\ns_nop 7\nQe 0\nQe 1\nQe 2\n"
"Qe 3\nQe 4\nQe 5\nQe 6\nQe 7\ns_nop 0\nQk 16,0,1\nQk 17,2,3\n"
"Qk 18,4,5\nQk 19,6,7\ns_nop 1\nQmvva 176,16,144\nQmvva 180,16,148\nQmvva 184,16,152\nQmvva 188,16,156\nQmvva 192,16,160\n"
"Qmvva 196,16,164\nQmvva 200,16,168\nQmvva 204,16,172\nQmaav 188,204,16\nQcvaa 0,112,64,40\nQmvaa 0,116,68\nQmvaa 0,120,72\nQmvaa 0,124,76\n"
"Qcvaa 4,128,64,40\nQmvaa 4,132,68\nQmvaa 4,136,72\nQmvaa 4,140,76\ns_nop 7\nQe 0\nQe 1\nQe 2\n"
"Qe 3\nQe 4\nQe 5\nQe 6\nQe 7\ns_nop 0\nQk 16,0,1\nQk 17,2,3\n"
"Qk 18,4,5\nQk 19,6,7\ns_nop 1\nQmvva 208,16,144\nQmvva 212,16,148\nQmvva 216,16,152\nQmvva 220,16,156\nQmvva 224,16,160\n"
"Qmvva 228,16,164\nQmvva 232,16,168\nQmvva 236,16,172\nQmaav 192,204,16\nQcvaa 0,112,80,44\nQmvaa 0,116,84\nQmvaa 0,120,88\nQmvaa 0,124,92\n"
"Qcvaa 4,128,80,44\nQmvaa 4,132,84\nQmvaa 4,136,88\nQmvaa 4,140,92\ns_nop 7\nQe 0\nQe 1\nQe 2\n"
"Qe 3\nQe 4\nQe 5\nQe 6\nQe 7\ns_nop 0\nQk 16,0,1\nQk 17,2,3\n"
"Qk 18,4,5\nQk 19,6,7\ns_nop 1\nQmvva 240,16,144\nQmvva 244,16,148\nQmvva 248,16,152\nQmvva 252,16,156\nQmava 208,16,160\n"
"Qmava 212,16,164\nQmava 216,16,168\nQmava 220,16,172\nQmaav 196,204,16\nQcvaa 0,112,96,48\nQmvaa 0,116,100\nQmvaa 0,120,104\nQmvaa 0,124,108\n"
"Qcvaa 4,128,96,48\nQmvaa 4,132,100\nQmvaa 4,136,104\nQmvaa 4,140,108\ns_nop 7\nQe 0\nQe 1\nQe 2\n"
"Qe 3\nQe 4\nQe 5\nQe 6\nQe 7\ns_nop 0\nQk 16,0,1\nQk 17,2,3\n"
"Qk 18,4,5\nQk 19,6,7\ns_nop 1\nQmava 224,16,144\nQmava 228,16,148\nQmava 232,16,152\nQmava 236,16,156\nQmava 240,16,160\n"
"Qmava 244,16,164\nQmava 248,16,168\nQmava 252,16,172\nQmaav 200,204,16\ns_add_u32 s89, s89, 8192\ns_add_u32 s66, s66, 1\ns_cmp_lt_u32 s66, 32\ns_cbranch_scc1 .Lfb2\n"
"s_nop 7\nv_accvgpr_read_b32 v0, a176\nv_accvgpr_read_b32 v4, a180\nv_accvgpr_read_b32 v8, a184\nv_accvgpr_read_b32 v12, a188\nv_accvgpr_read_b32 v16, a192\nv_accvgpr_read_b32 v20, a196\nv_accvgpr_read_b32 v24, a200\n"
"s_cmp_eq_u32 s67, 0\ns_cbranch_scc1 .Lfbret0\ns_branch .Lfbret1\n.Lf2_w:\ns_mov_b32 s97, 0\n.Lf2_l:\ns_sleep 1\nbuffer_load_dword v78, v76, s[24:27], 0 offen sc1\n"
"s_waitcnt vmcnt(0)\ns_add_u32 s97, s97, 1\ns_cmp_gt_u32 s97, 1048576\ns_cbranch_scc1 .Lf2_r\nv_sub_u32 v66, v78, v65\nv_cmp_gt_u32 vcc, 2, v66\ns_and_b64 vcc, vcc, s[28:29]\ns_cbranch_scc1 .Lf2_l\n"
"s_branch .Lf2_r\n.Lf3_w:\ns_mov_b32 s97, 0\n.Lf3_l:\ns_sleep 1\nbuffer_load_dword v78, v76, s[24:27], 0 offen sc1\ns_waitcnt vmcnt(0)\ns_add_u32 s97, s97, 1\n"
"s_cmp_gt_u32 s97, 1048576\ns_cbranch_scc1 .Lf3_r\nv_sub_u32 v66, v78, v65\nv_cmp_gt_u32 vcc, 3, v66\ns_and_b64 vcc, vcc, s[28:29]\ns_cbranch_scc1 .Lf3_l\ns_branch .Lf3_r\n.Lx0_w:\n"
"s_mov_b32 s97, 0\n.Lx0_l:\ns_sleep 1\nbuffer_load_dword v78, v76, s[24:27], 0 offen sc1\ns_waitcnt vmcnt(0)\ns_add_u32 s97, s97, 1\ns_cmp_gt_u32 s97, 1048576\ns_cbranch_scc1 .Lx0_r\n"
"v_sub_u32 v66, v78, v65\nv_cmp_gt_u32 vcc, 1, v66\ns_and_b64 vcc, vcc, s[28:29]\ns_cbranch_scc1 .Lx0_l\ns_branch .Lx0_r\n.Lx1_w:\ns_mov_b32 s97, 0\n.Lx1_l:\n"
"s_sleep 1\nbuffer_load_dword v78, v76, s[24:27], 0 offen sc1\ns_waitcnt vmcnt(0)\ns_add_u32 s97, s97, 1\ns_cmp_gt_u32 s97, 1048576\ns_cbranch_scc1 .Lx1_r\nv_sub_u32 v66, v78, v65\nv_cmp_gt_u32 vcc, 1, v66\n"
"s_and_b64 vcc, vcc, s[28:29]\ns_cbranch_scc1 .Lx1_l\ns_branch .Lx1_r\n.Lexit:\n"
  :: "s"((unsigned)kp), "s"((unsigned)(kp >> 32)), "s"((unsigned)vp), "s"((unsigned)(vp >> 32)), "s"((unsigned)xp), "s"((unsigned)(xp >> 32)),
     "s"((unsigned)wp), "s"((unsigned)(wp >> 32)), "s"((unsigned)op), "s"((unsigned)(op >> 32)), "s"(ldsb), "s"(wid), "s"(head0),
     "s"((unsigned)dp), "s"((unsigned)(dp >> 32)), "s"((unsigned)wop), "s"((unsigned)(wop >> 32)), "s"((unsigned)yp), "s"((unsigned)(yp >> 32)),
     "s"(hg), "s"(b * 8u + hg), "s"(kx)
  : "v0", "v1", "v2", "v3", "v4", "v5", "v6", "v7", "v8", "v9", "v10", "v11", "v12", "v13", "v14", "v15", "v16", "v17", "v18", "v19", "v20", "v21", "v22", "v23", "v24", "v25", "v26", "v27", "v28", "v29", "v30", "v31", "v32", "v33", "v34", "v35", "v36", "v37", "v38", "v39", "v40", "v41", "v42", "v43", "v44", "v45", "v46", "v47", "v48", "v49", "v50", "v51", "v52", "v53", "v54", "v55", "v56", "v57", "v58", "v59", "v60", "v61", "v62", "v63", "v64", "v65", "v66", "v67", "v68", "v69", "v70", "v71", "v72", "v73", "v74", "v75", "v76", "v77", "v78", "v79", "v80", "v81", "v82", "v83", "v84", "v85", "v86", "v87", "v88", "v89", "v90", "v91", "v92", "v93", "v94", "v95", "v96", "v97", "v98", "v99", "v100", "v101", "v102", "v103", "v104", "v105", "v106", "v107", "v108", "v109", "v110", "v111", "v112", "v113", "v114", "v115", "v116", "v117", "v118", "v119", "v120", "v121", "v122", "v123", "v124", "v125", "v126", "v127", "v128", "v129", "v130", "v131", "v132", "v133", "v134", "v135", "v136", "v137", "v138", "v139", "v140", "v141", "v142", "v143", "v144", "v145", "v146", "v147", "v148", "v149", "v150", "v151", "v152", "v153", "v154", "v155", "v156", "v157", "v158", "v159", "v160", "v161", "v162", "v163", "v164", "v165", "v166", "v167", "v168", "v169", "v170", "v171", "v172", "v173", "v174", "v175", "v176", "v177", "v178", "v179", "v180", "v181", "v182", "v183", "v184", "v185", "v186", "v187", "v188", "v189", "v190", "v191", "v192", "v193", "v194", "v195", "v196", "v197", "v198", "v199", "v200", "v201", "v202", "v203", "v204", "v205", "v206", "v207", "v208", "v209", "v210", "v211", "v212", "v213", "v214", "v215", "v216", "v217", "v218", "v219", "v220", "v221", "v222", "v223", "v224", "v225", "v226", "v227", "v228", "v229", "v230", "v231", "v232", "v233", "v234", "v235", "v236", "v237", "v238", "v239", "v240", "v241", "v242", "v243", "v244", "v245", "v246", "v247", "v248", "v249", "v250", "v251", "v252", "v253", "v254", "v255", "a0", "a1", "a2", "a3", "a4", "a5", "a6", "a7", "a8", "a9", "a10", "a11", "a12", "a13", "a14", "a15", "a16", "a17", "a18", "a19", "a20", "a21", "a22", "a23", "a24", "a25", "a26", "a27", "a28", "a29", "a30", "a31", "a32", "a33", "a34", "a35", "a36", "a37", "a38", "a39", "a40", "a41", "a42", "a43", "a44", "a45", "a46", "a47", "a48", "a49", "a50", "a51", "a52", "a53", "a54", "a55", "a56", "a57", "a58", "a59", "a60", "a61", "a62", "a63", "a64", "a65", "a66", "a67", "a68", "a69", "a70", "a71", "a72", "a73", "a74", "a75", "a76", "a77", "a78", "a79", "a80", "a81", "a82", "a83", "a84", "a85", "a86", "a87", "a88", "a89", "a90", "a91", "a92", "a93", "a94", "a95", "a96", "a97", "a98", "a99", "a100", "a101", "a102", "a103", "a104", "a105", "a106", "a107", "a108", "a109", "a110", "a111", "a112", "a113", "a114", "a115", "a116", "a117", "a118", "a119", "a120", "a121", "a122", "a123", "a124", "a125", "a126", "a127", "a128", "a129", "a130", "a131", "a132", "a133", "a134", "a135", "a136", "a137", "a138", "a139", "a140", "a141", "a142", "a143", "a144", "a145", "a146", "a147", "a148", "a149", "a150", "a151", "a152", "a153", "a154", "a155", "a156", "a157", "a158", "a159", "a160", "a161", "a162", "a163", "a164", "a165", "a166", "a167", "a168", "a169", "a170", "a171", "a172", "a173", "a174", "a175", "a176", "a177", "a178", "a179", "a180", "a181", "a182", "a183", "a184", "a185", "a186", "a187", "a188", "a189", "a190", "a191", "a192", "a193", "a194", "a195", "a196", "a197", "a198", "a199", "a200", "a201", "a202", "a203", "a204", "a205", "a206", "a207", "a208", "a209", "a210", "a211", "a212", "a213", "a214", "a215", "a216", "a217", "a218", "a219", "a220", "a221", "a222", "a223", "a224", "a225", "a226", "a227", "a228", "a229", "a230", "a231", "a232", "a233", "a234", "a235", "a236", "a237", "a238", "a239", "a240", "a241", "a242", "a243", "a244", "a245", "a246", "a247", "a248", "a249", "a250", "a251", "a252", "a253", "a254", "a255", "s24", "s25", "s26", "s27", "s28", "s29", "s30", "s31", "s40", "s41", "s42", "s43", "s44", "s45", "s46", "s47", "s48", "s49", "s50", "s51", "s52", "s53", "s54", "s55", "s56", "s57", "s58", "s59", "s60", "s61", "s62", "s63", "s64", "s65", "s66", "s67", "s68", "s69", "s70", "s71", "s72", "s73", "s74", "s75", "s76", "s77", "s78", "s79", "s80", "s81", "s82", "s83", "s84", "s85", "s86", "s87", "s88", "s89", "s90", "s91", "s92", "s93", "s94", "s95", "s96", "s97", "s98", "s99", "vcc", "scc", "memory");
}

extern "C" void kernel_launch(void* const* d_in, const int* in_sizes, int n_in, void* d_out, int out_size, void* d_ws, size_t ws_size, hipStream_t stream) {
  typedef const unsigned short* P;
  (void)hipFuncSetAttribute((const void*)mha2_asm, hipFuncAttributeMaxDynamicSharedMemorySize, 163840);
  hipLaunchKernelGGL(mha2_asm, dim3(256), dim3(256), 163840, stream, (P)d_in[0], (P)d_in[5], (P)d_in[3], (P)d_in[8], (P)d_in[1], (P)d_in[6], (P)d_in[2], (P)d_in[7],
                     (P)d_in[4], (P)d_in[9], (char*)d_ws, (unsigned short*)d_out);
}
```

```cpp
#include <hip/hip_runtime.h>
#include <stdint.h>

__global__ __launch_bounds__(256, 1) __attribute__((aligned(4096)))
void mha2_asm(const unsigned short* __restrict__ X0, const unsigned short* __restrict__ X1, const unsigned short* __restrict__ W0, const unsigned short* __restrict__ W1,
              const unsigned short* __restrict__ K0, const unsigned short* __restrict__ K1,
              const unsigned short* __restrict__ V0, const unsigned short* __restrict__ V1,
              const unsigned short* __restrict__ WO0, const unsigned short* __restrict__ WO1, char* ws, unsigned short* out) {
  extern __shared__ __attribute__((aligned(16))) char lds[];
  const unsigned c = blockIdx.x;
  const unsigned xcd = c & 7u, idx = c >> 3;
  const unsigned hg = idx & 7u, bb = xcd * 4u + (idx >> 3);
  const unsigned bk = bb >> 4, b = bb & 15u;
  const unsigned wid = __builtin_amdgcn_readfirstlane(threadIdx.x >> 6);
  const unsigned head0 = (hg >> 2) * 16u + (hg & 3u) * 2u + (wid >> 1);
  const unsigned long long kp = (unsigned long long)(bk ? K1 : K0) + ((unsigned long long)b * 32u + (head0 & ~1u)) * 262144ull - 1024ull;
  const unsigned long long vp = (unsigned long long)(bk ? V1 : V0) + ((unsigned long long)b * 32u + (head0 & ~1u)) * 262144ull - 1024ull;
  const unsigned long long xp = (unsigned long long)(bk ? X1 : X0) + (unsigned long long)b * (224u * 1280u);
  const unsigned long long wp = (unsigned long long)(bk ? W1 : W0);
  const unsigned long long op = (unsigned long long)ws + 0ull + (unsigned long long)bb * 6291456ull;
  const unsigned ldsb = (unsigned)(size_t)lds;
  const unsigned long long wop = (unsigned long long)(bk ? WO1 : WO0) - 4096ull;
  const unsigned long long yp = (unsigned long long)out + (unsigned long long)b * (224ull * 2560ull) + bk * 1280u + (hg & 3u) * 320u;
  const unsigned long long dp = (unsigned long long)ws + (300ull << 20) + (c * 4u + wid) * 256u;
  const unsigned hd = wid >> 1, ky = hg & 4u;
  const unsigned kx = (ky >> 0 & 1u) << 11 | (ky >> 1 & 1u) << 12 | (ky >> 2 & 1u) << 13 | (ky >> 3 & 1u) << 14 | (ky >> 4 & 1u) << 17;
  asm volatile(
".macro Qta d,v,o\nds_read_b64_tr_b16 a[\\d:\\d+1], v\\v offset:\\o\n.endm\n.macro Qdv d,v,o\nds_read_b128 v[\\d:\\d+3], v\\v offset:\\o\n.endm\n.macro Qda d,v,o\nds_read_b128 a[\\d:\\d+3], v\\v offset:\\o\n"
".endm\n.macro Qzvav d,a,b\nv_mfma_f32_16x16x32_bf16 v[\\d:\\d+3], a[\\a:\\a+3], v[\\b:\\b+3], 0\n.endm\n.macro Qcvav d,a,b,c\nv_mfma_f32_16x16x32_bf16 v[\\d:\\d+3], a[\\a:\\a+3], v[\\b:\\b+3], v[\\c:\\c+3]\n.endm\n.macro Qmvaa d,a,b\n"
"v_mfma_f32_16x16x32_bf16 v[\\d:\\d+3], a[\\a:\\a+3], a[\\b:\\b+3], v[\\d:\\d+3]\n.endm\n.macro Qcaav d,a,b,c\nv_mfma_f32_16x16x32_bf16 a[\\d:\\d+3], a[\\a:\\a+3], v[\\b:\\b+3], a[\\c:\\c+3]\n.endm\n.macro Qmaaa d,a,b\nv_mfma_f32_16x16x32_bf16 a[\\d:\\d+3], a[\\a:\\a+3], a[\\b:\\b+3], a[\\d:\\d+3]\n.endm\n"
".macro Qmvav d,a,b\nv_mfma_f32_16x16x32_bf16 v[\\d:\\d+3], a[\\a:\\a+3], v[\\b:\\b+3], v[\\d:\\d+3]\n.endm\n.macro Qmaav d,a,b\nv_mfma_f32_16x16x32_bf16 a[\\d:\\d+3], a[\\a:\\a+3], v[\\b:\\b+3], a[\\d:\\d+3]\n.endm\n.macro Qk d,a,b\nv_cvt_pk_bf16_f32 v\\d, v\\a, v\\b\n"
".endm\n.macro Qzvaa d,a,b\nv_mfma_f32_16x16x32_bf16 v[\\d:\\d+3], a[\\a:\\a+3], a[\\b:\\b+3], 0\n.endm\n.macro Qa d,a,b\nv_add_f32 v\\d, v\\a, v\\b\n.endm\n.macro Qe d\n"
"v_exp_f32 v\\d, v\\d\n.endm\n.macro Qcvaa d,a,b,c\nv_mfma_f32_16x16x32_bf16 v[\\d:\\d+3], a[\\a:\\a+3], a[\\b:\\b+3], v[\\c:\\c+3]\n.endm\n.macro Qmvva d,a,b\nv_mfma_f32_16x16x32_bf16 v[\\d:\\d+3], v[\\a:\\a+3], a[\\b:\\b+3], v[\\d:\\d+3]\n.endm\n"
".macro Qmava d,a,b\nv_mfma_f32_16x16x32_bf16 a[\\d:\\d+3], v[\\a:\\a+3], a[\\b:\\b+3], a[\\d:\\d+3]\n.endm\n.macro Qzvvv d,a,b\nv_mfma_f32_16x16x32_bf16 v[\\d:\\d+3], v[\\a:\\a+3], v[\\b:\\b+3], 0\n.endm\n.p2align 7\nv_mbcnt_lo_u32_b32 v0, -1, 0\n"
"v_mbcnt_hi_u32_b32 v0, -1, v0\nv_lshlrev_b32 v60, 4, v0\ns_add_u32 m0, %10, 160768\ns_getpc_b64 s[56:57]\n.Lpc0:\ns_and_b32 s56, s56, 0xffffff80\ns_and_b32 s57, s57, 0xffff\ns_mov_b32 s58, .Lend-.Lpc0\n"
"s_mov_b32 s59, 0x20000\ns_and_b32 s94, %20, 31\ns_lshl_b32 s94, s94, 2\ns_add_u32 s94, s94, %11\ns_lshl_b32 s94, s94, 10\nbuffer_load_dwordx4 v60, s[56:59], s94 offen lds\ns_mov_b32 s52, %6\ns_and_b32 s53, %7, 0xffff\n"
"s_mov_b32 s54, 5242880\ns_mov_b32 s55, 0x20000\ns_mov_b32 s48, %4\ns_and_b32 s49, %5, 0xffff\ns_mov_b32 s50, 286720\ns_mov_b32 s51, 0x20000\ns_mov_b32 s70, %11\ns_and_b32 s92, s70, 1\n"
"s_lshr_b32 s93, s70, 1\nv_lshrrev_b32 v2, 4, v0\ns_lshl_b32 s68, s92, 18\nv_and_b32 v4, 3, v0\nv_lshlrev_b32 v4, 4, v4\nv_and_b32 v1, 1, v2\nv_lshl_add_u32 v4, v1, 6, v4\nv_lshrrev_b32 v1, 5, v0\n"
"v_lshl_add_u32 v4, v1, 15, v4\nv_bfe_u32 v1, v0, 2, 2\nv_lshl_add_u32 v4, v1, 13, v4\nv_add_u32 v62, s68, v4\ns_lshl_b32 s87, %12, 8\ns_mul_i32 s62, s70, 4128\ns_add_u32 s62, s62, %10\ns_add_u32 s62, s62, 33024\n"
"s_add_u32 m0, s62, 0\ns_add_u32 s66, s87, 0\ns_nop 0\nbuffer_load_dwordx4 v62, s[52:55], s66 offen lds\ns_add_u32 s66, s87, 64504\ns_nop 0\nbuffer_load_dwordx4 v62, s[52:55], s66 offen offset:1032 lds\ns_add_u32 s66, s87, 129008\n"
"s_nop 0\nbuffer_load_dwordx4 v62, s[52:55], s66 offen offset:2064 lds\ns_add_u32 s66, s87, 193512\ns_nop 0\nbuffer_load_dwordx4 v62, s[52:55], s66 offen offset:3096 lds\ns_lshl_b32 s69, s92, 1\nv_lshrrev_b32 v3, 5, v0\nv_and_b32 v4, 3, v0\n"
"v_xor_b32 v4, s69, v4\nv_lshlrev_b32 v4, 4, v4\nv_lshl_add_u32 v4, v3, 6, v4\nv_bfe_u32 v1, v0, 2, 3\nv_mul_u32_u24 v64, 1280, v1\nv_add_u32 v64, v64, v4\ns_mul_i32 s86, s93, 143360\ns_mul_i32 s68, s92, 10240\n"
"s_add_u32 s86, s86, s68\ns_mul_i32 s63, s92, 14336\ns_mul_i32 s68, s93, 7168\ns_add_u32 s63, s63, s68\ns_add_u32 s63, s63, %10\ns_add_u32 s63, s63, 99072\ns_add_u32 m0, s63, 33024\ns_add_u32 s97, s86, 0\n"
"s_nop 0\nbuffer_load_dwordx4 v64, s[48:51], s97 offen lds\ns_add_u32 s97, s86, 19456\ns_nop 0\nbuffer_load_dwordx4 v64, s[48:51], s97 offen offset:1024 lds\ns_add_u32 s97, s86, 38912\ns_nop 0\nbuffer_load_dwordx4 v64, s[48:51], s97 offen offset:2048 lds\n"
"s_add_u32 s97, s86, 58368\ns_nop 0\nbuffer_load_dwordx4 v64, s[48:51], s97 offen offset:3072 lds\ns_add_u32 m0, s63, 37120\ns_add_u32 s97, s86, 81920\ns_nop 0\nbuffer_load_dwordx4 v64, s[48:51], s97 offen lds\ns_add_u32 s97, s86, 101376\n"
"s_nop 0\nbuffer_load_dwordx4 v64, s[48:51], s97 offen offset:1024 lds\ns_add_u32 s97, s86, 120832\ns_nop 0\nbuffer_load_dwordx4 v64, s[48:51], s97 offen offset:2048 lds\ns_add_u32 m0, s62, 16512\ns_add_u32 s66, s87, 128\ns_nop 0\n"
"buffer_load_dwordx4 v62, s[52:55], s66 offen lds\ns_mov_b32 s24, %8\ns_and_b32 s25, %9, 0xffff\ns_mov_b32 s26, 6291456\ns_mov_b32 s27, 0x20000\nv_and_b32 v5, 7, v0\nv_lshlrev_b32 v5, 7, v5\ns_add_u32 s66, s87, 64632\n"
"s_nop 0\nbuffer_load_dwordx4 v62, s[52:55], s66 offen offset:1032 lds\nv_add_u32 v76, 5242880, v5\ns_lshl_b32 s68, s70, 2\ns_add_u32 s68, s68, 4\nv_bfe_u32 v5, v0, 3, 1\nv_mad_u32_u24 v76, v5, s68, v76\nbuffer_load_dword v65, v76, s[24:27], 0 offen sc1\n"
"s_add_u32 s66, s87, 129136\ns_nop 0\nbuffer_load_dwordx4 v62, s[52:55], s66 offen offset:2064 lds\ns_mov_b32 s40, %0\ns_and_b32 s41, %1, 0xffff\ns_mov_b32 s42, 2622464\ns_mov_b32 s43, 0x20000\ns_mov_b32 s44, %2\n"
"s_and_b32 s45, %3, 0xffff\ns_add_u32 s66, s87, 193640\ns_nop 0\nbuffer_load_dwordx4 v62, s[52:55], s66 offen offset:3096 lds\ns_mov_b32 s46, 2622464\ns_mov_b32 s47, 0x20000\ns_mov_b32 s56, %8\ns_and_b32 s57, %9, 0xffff\n"
"s_mov_b32 s58, 0\ns_mov_b32 s59, 0x20000\ns_mov_b64 exec, 0\ns_cmp_eq_u32 s70, 0\ns_cbranch_scc1 .Lpg0\ns_cmp_eq_u32 s70, 1\ns_cbranch_scc1 .Lpg1\ns_cmp_eq_u32 s70, 2\n"
"s_cbranch_scc1 .Lpg2\ns_branch .Lpg3\n.Ltr0:\ns_mov_b64 exec, -1\ns_mov_b32 s65, 0\ns_mov_b32 s60, %21\ns_add_u32 s90, s60, 260096\ns_mov_b32 s67, 0\n"
"s_mov_b32 s72, 0x3e0293ee\ns_lshl_b32 s68, s70, 8\ns_add_u32 m0, s63, 0\ns_add_u32 s97, s86, 128\ns_nop 0\nbuffer_load_dwordx4 v64, s[48:51], s97 offen lds\ns_lshl_b32 s69, s93, 5\ns_or_b32 s88, s68, s69\n"
"v_bfe_u32 v1, v0, 2, 2\nv_mul_u32_u24 v4, 0x8100, v1\nv_and_b32 v1, 3, v0\nv_lshl_or_b32 v4, v1, 4, v4\ns_add_u32 s97, s86, 19584\ns_nop 0\nbuffer_load_dwordx4 v64, s[48:51], s97 offen offset:1024 lds\nv_and_b32 v1, 1, v2\n"
"v_lshl_or_b32 v4, v1, 7, v4\nv_lshl_or_b32 v4, v3, 6, v4\nv_xor_b32 v4, s88, v4\nv_add_u32 v69, 1024, v4\nv_bfe_u32 v1, v0, 1, 2\ns_add_u32 s97, s86, 39040\ns_nop 0\nbuffer_load_dwordx4 v64, s[48:51], s97 offen offset:2048 lds\n"
"v_mul_u32_u24 v4, 0x8100, v1\nv_and_b32 v1, 9, v0\nv_lshl_or_b32 v4, v1, 4, v4\nv_lshl_or_b32 v4, v2, 5, v4\nv_xor_b32 v4, s68, v4\nv_add_u32 v72, 1024, v4\ns_add_u32 s97, s86, 58496\ns_nop 0\n"
"buffer_load_dwordx4 v64, s[48:51], s97 offen offset:3072 lds\ns_lshl_b32 s68, s70, 12\ns_add_u32 s61, s68, %10\ns_mul_i32 s68, s92, 8320\ns_lshl_b32 s69, s93, 12\ns_add_u32 s68, s68, s69\ns_add_u32 s68, s68, %10\ns_add_u32 m0, s63, 4096\n"
"s_add_u32 s97, s86, 82048\ns_nop 0\nbuffer_load_dwordx4 v64, s[48:51], s97 offen lds\ns_add_u32 s64, s68, 16384\ns_lshl_b32 s69, s93, 11\ns_add_u32 s69, s69, %10\nv_bfe_u32 v3, v0, 3, 1\nv_lshlrev_b32 v4, 1, v3\n"
"v_xor_b32 v4, v4, v2\ns_add_u32 s97, s86, 101504\ns_nop 0\nbuffer_load_dwordx4 v64, s[48:51], s97 offen offset:1024 lds\nv_and_b32 v1, 7, v0\nv_lshl_add_u32 v4, v1, 2, v4\nv_lshlrev_b32 v4, 4, v4\nv_bfe_u32 v1, v0, 2, 2\n"
"v_and_b32 v5, 255, v4\nv_lshl_or_b32 v1, v1, 12, v5\ns_add_u32 s97, s86, 120960\ns_nop 0\nbuffer_load_dwordx4 v64, s[48:51], s97 offen offset:2048 lds\nv_add_u32 v68, s69, v1\ns_mul_i32 s68, s92, 7168\ns_add_u32 s68, s68, %10\n"
"s_add_u32 s68, s68, 99072\nv_mul_u32_u24 v1, 14336, v3\nv_add3_u32 v63, v1, v4, s68\ns_add_u32 m0, s62, 33024\ns_add_u32 s66, s87, 524288\ns_nop 0\nbuffer_load_dwordx4 v62, s[52:55], s66 offen lds\nv_lshrrev_b32 v3, 5, v0\n"
"v_and_b32 v4, 31, v0\nv_lshlrev_b32 v4, 3, v4\nv_and_b32 v1, 1, v2\nv_lshl_add_u32 v4, v1, 13, v4\nv_lshl_add_u32 v4, v3, 12, v4\ns_add_u32 s66, s87, 588792\ns_nop 0\nbuffer_load_dwordx4 v62, s[52:55], s66 offen offset:1032 lds\n"
"v_add_u32 v4, 16384, v4\nv_add_u32 v70, s69, v4\nv_add_u32 v71, 66048, v70\ns_mul_i32 s68, s93, 8256\ns_add_u32 s68, s68, %10\ns_add_u32 s68, s68, 33024\ns_add_u32 s66, s87, 653296\ns_nop 0\n"
"buffer_load_dwordx4 v62, s[52:55], s66 offen offset:2064 lds\nv_mul_u32_u24 v3, 1032, v2\nv_and_b32 v4, 15, v0\nv_lshlrev_b32 v4, 4, v4\nv_add3_u32 v61, v3, v4, s68\ns_lshl_b32 s68, s70, 2\ns_add_u32 s69, %10, 131968\ns_add_u32 s66, s87, 717800\n"
"s_nop 0\nbuffer_load_dwordx4 v62, s[52:55], s66 offen offset:3096 lds\ns_add_u32 s68, s68, s69\nv_mov_b32 v73, s68\nv_and_b32 v3, 3, v0\nv_lshl_add_u32 v74, v3, 2, s69\nv_mov_b32 v75, 0\nds_write_b32 v73, v75\n"
"s_mov_b64 exec, 0\ns_cmp_eq_u32 s70, 0\ns_cbranch_scc1 .Lpg4\ns_cmp_eq_u32 s70, 1\ns_cbranch_scc1 .Lpg5\ns_cmp_eq_u32 s70, 2\ns_cbranch_scc1 .Lpg6\ns_branch .Lpg7\n"
".Ltr9:\ns_mov_b64 exec, -1\ns_waitcnt lgkmcnt(0)\ns_mul_i32 s91, s70, 7168\ns_add_u32 s91, s91, %10\ns_add_u32 s91, s91, 98304\ns_lshl_b32 s96, s70, 11\ns_add_u32 s96, s96, %10\n"
"s_add_u32 m0, s62, 49536\ns_add_u32 s66, s87, 524416\ns_nop 0\nbuffer_load_dwordx4 v62, s[52:55], s66 offen lds\nv_and_b32 v3, 1, v0\nv_mul_u32_u24 v3, 0xaaaa, v3\nv_or_b32 v77, 0x4444, v3\ns_and_b32 s68, %19, 4\n"
"s_lshl_b32 s28, 15, s68\ns_mov_b32 s29, 0\ns_add_u32 s66, s87, 588920\ns_nop 0\nbuffer_load_dwordx4 v62, s[52:55], s66 offen offset:1032 lds\ns_and_b32 s68, %19, 4\ns_add_u32 s68, s68, %19\ns_lshl_b32 s71, s68, 18\n"
"s_mul_i32 s69, s70, 0x80100\ns_and_b32 s69, s69, 0x100100\ns_add_u32 s68, s71, s69\ns_add_u32 s66, s87, 653424\ns_nop 0\nbuffer_load_dwordx4 v62, s[52:55], s66 offen offset:2064 lds\ns_lshr_b32 s98, %20, 1\ns_xor_b32 s98, s98, %20\n"
"s_lshl_b32 s98, s98, 11\ns_and_b32 s98, s98, 8192\ns_add_u32 s68, s68, s98\ns_add_u32 s68, s68, 7168\ns_add_u32 s66, s87, 717928\ns_nop 0\nbuffer_load_dwordx4 v62, s[52:55], s66 offen offset:3096 lds\ns_add_u32 s30, %8, s68\n"
"s_addc_u32 s31, %9, 0\ns_mov_b32 s77, 0\nv_bfe_i32 v8, v0, 3, 1\nv_lshlrev_b32 v4, 10, v0\nv_and_b32 v4, 0x6000, v4\ns_mov_b64 exec, 0\ns_cmp_eq_u32 s70, 0\ns_cbranch_scc1 .Lpg8\n"
"s_cmp_eq_u32 s70, 1\ns_cbranch_scc1 .Lpg9\ns_cmp_eq_u32 s70, 2\ns_cbranch_scc1 .Lpg10\ns_branch .Lpg11\n.Ltr10:\ns_mov_b64 exec, -1\nv_lshrrev_b32 v3, 5, v0\n"
"v_lshl_or_b32 v4, v3, 16, v4\nv_and_b32 v3, 0x70, v60\nv_or_b32 v4, v3, v4\nv_or_b32 v8, 1, v8\nv_mov_b32 v9, v8\nv_mov_b32 v10, v8\nv_mov_b32 v11, v8\nbuffer_store_dwordx4 v[8:11], v4, s[24:27], s68 offen offset:0 sc1\n"
"s_nop 1\ns_lshl_b32 s68, s70, 8\nv_lshl_or_b32 v3, v2, 15, v60\nv_xor_b32 v79, s68, v3\ns_and_b32 s68, %19, 3\ns_lshl_b32 s99, 3072, s68\ns_or_b32 s99, s99, 0x262\ns_mov_b32 s94, 0x7ffff000\n"
"s_mul_i32 s68, %20, 40960\ns_mul_i32 s69, s93, 20480\ns_add_u32 s68, s68, s69\ns_lshl_b32 s69, s92, 10\ns_add_u32 s68, s68, s69\ns_add_u32 s68, s68, 2048\ns_add_u32 s94, %6, s68\ns_addc_u32 s95, %7, 0\n"
"s_and_b32 s95, s95, 0xffff\ns_mov_b32 s75, 17408\n.Lround:\ns_cmp_lg_u32 s67, 0\ns_cselect_b64 vcc, -1, 0\ns_waitcnt vmcnt(15)\ns_barrier\nQta 112,61,0\n"
"Qta 114,61,512\nQdv 24,63,33024\nQdv 28,63,34048\nQdv 32,63,35072\nQdv 36,63,36096\nQdv 40,63,37120\nQdv 44,63,38144\nQdv 48,63,39168\n"
"Qda 176,63,33536\nQta 116,61,4128\nQta 118,61,4640\nQta 120,61,8\nQta 122,61,520\nQta 124,61,4136\nQta 126,61,4648\nQta 128,61,256\n"
"Qta 130,61,768\nQta 132,61,4384\nQta 134,61,4896\nQta 136,61,264\nQta 138,61,776\nQta 140,61,4392\nQta 142,61,4904\ns_waitcnt lgkmcnt(15)\n"
"Qzvav 0,112,24\nQzvav 4,112,28\nQda 180,63,34560\nQzvav 8,112,32\nQda 184,63,35584\nQzvav 12,112,36\nQda 188,63,36608\nQzvav 16,112,40\n"
"Qda 192,63,37632\nQzvav 20,112,44\nQda 196,63,38656\nQzvav 52,112,48\nQda 200,63,39680\nv_max3_f32 v0, |v0|, |v1|, |v2|\nv_max3_f32 v4, |v4|, |v5|, |v6|\nv_max3_f32 v8, |v8|, |v9|, |v10|\n"
"v_max3_f32 v12, |v12|, |v13|, |v14|\nv_max3_f32 v16, |v16|, |v17|, |v18|\nv_max3_f32 v20, |v20|, |v21|, |v22|\ns_nop 0\nv_max3_f32 v52, |v52|, |v53|, |v54|\nv_min3_u32 v56, v0, v4, v8\nv_min3_u32 v56, v56, v12, v16\nv_min3_u32 v56, v56, v20, v52\n"
"v_mov_b32 v57, v56\ns_nop 1\nv_permlane32_swap_b32 v56, v57\nv_max_f32 v56, v56, v57\nv_mov_b32 v57, v56\ns_nop 1\nv_permlane16_swap_b32 v56, v57\nv_max_f32 v56, v56, v57\n"
"s_nop 1\nv_min_f32_dpp v56, v56, v56 row_ror:8 row_mask:0xf bank_mask:0xf\ns_nop 1\nv_min_f32_dpp v56, v56, v56 row_ror:4 row_mask:0xf bank_mask:0xf\ns_nop 1\nv_min_f32_dpp v56, v56, v56 row_ror:2 row_mask:0xf bank_mask:0xf\ns_nop 1\nv_min_f32_dpp v56, v56, v56 row_ror:1 row_mask:0xf bank_mask:0xf\n"
"v_and_b32 v56, 0x7f800000, v56\nv_min_u32 v56, 0x78000000, v56\nv_add_u32 v56, 0x5400000, v56\nv_mov_b32 v57, v56\nv_mov_b64 v[58:59], v[56:57]\nv_mul_f32 v52, -v56, s72\ns_waitcnt lgkmcnt(6)\nQcvav 80,112,24,56\n"
"Qmvaa 80,116,176\ns_add_u32 s68, s71, 0\nQcvav 84,120,24,56\ns_cbranch_vccz .Lst27\nbuffer_store_dwordx4 a[0:3], v79, s[56:59], s68 offen offset:0 sc1\n.Lst27:\nQmvaa 84,124,176\nQcvav 88,128,24,56\n"
"s_add_u32 s68, s71, 16384\nQmvaa 88,132,176\ns_cbranch_vccz .Lst26\nbuffer_store_dwordx4 a[4:7], v79, s[56:59], s68 offen offset:0 sc1\n.Lst26:\nQcvav 92,136,24,56\nQmvaa 92,140,176\ns_waitcnt lgkmcnt(0)\n"
"Qcvav 124,136,28,56\ns_waitcnt vmcnt(16)\ns_barrier\nQmvaa 124,140,180\nQta 144,61,16512\ns_add_u32 m0, s62, 0\ns_add_u32 s69, s87, 1048576\nQcvav 120,128,28,56\n"
"buffer_load_dwordx4 v62, s[52:55], s69 offen lds\nQmvaa 120,132,180\nQta 146,61,17024\nQcvav 116,120,28,56\nv_accvgpr_write_b32 a204, v56\nQmvaa 116,124,180\nQta 148,61,20640\ns_add_u32 s69, s87, 1113080\n"
"Qcvav 112,112,28,56\nbuffer_load_dwordx4 v62, s[52:55], s69 offen offset:1032 lds\nQmvaa 112,116,180\nQta 150,61,21152\nQcvav 144,112,32,56\nv_accvgpr_write_b32 a205, v56\nQmvaa 144,116,184\nQta 152,61,16520\n"
"s_add_u32 s69, s87, 1177584\nQcvav 148,120,32,56\nbuffer_load_dwordx4 v62, s[52:55], s69 offen offset:2064 lds\nQmvaa 148,124,184\nQta 154,61,17032\nQcvav 152,128,32,56\nv_accvgpr_write_b32 a206, v56\nQmvaa 152,132,184\n"
"Qta 156,61,20648\ns_add_u32 s69, s87, 1242088\nQcvav 156,136,32,56\nbuffer_load_dwordx4 v62, s[52:55], s69 offen offset:3096 lds\nQmvaa 156,140,184\nQta 158,61,21160\nQcvav 188,136,36,56\nv_accvgpr_write_b32 a207, v56\n"
"Qmvaa 188,140,188\nQta 160,61,16768\ns_add_u32 m0, s63, 33024\ns_add_u32 s88, s86, 256\nQcvav 184,128,36,56\nbuffer_load_dwordx4 v64, s[48:51], s88 offen lds\nQmvaa 184,132,188\nQta 162,61,17280\n"
"Qcvav 180,120,36,56\nQmvaa 180,124,188\nQta 164,61,20896\ns_add_u32 s88, s86, 19712\nQcvav 176,112,36,56\nbuffer_load_dwordx4 v64, s[48:51], s88 offen offset:1024 lds\nQmvaa 176,116,188\nQta 166,61,21408\n"
"Qcvav 208,112,40,56\nQmvaa 208,116,192\nQta 168,61,16776\ns_add_u32 s88, s86, 39168\nQcvav 212,120,40,56\nbuffer_load_dwordx4 v64, s[48:51], s88 offen offset:2048 lds\nQmvaa 212,124,192\nQta 170,61,17288\n"
"Qcvav 216,128,40,56\nQmvaa 216,132,192\nQta 172,61,20904\ns_add_u32 s88, s86, 58624\nQcvav 220,136,40,56\nbuffer_load_dwordx4 v64, s[48:51], s88 offen offset:3072 lds\nQmvaa 220,140,192\nQta 174,61,21416\n"
"Qcvav 252,136,44,56\nQmvaa 252,140,196\ns_add_u32 m0, s63, 37120\ns_add_u32 s88, s86, 82176\nQcvav 248,128,44,56\nbuffer_load_dwordx4 v64, s[48:51], s88 offen lds\nQmvaa 248,132,196\nQcvav 244,120,44,56\n"
"Qmvaa 244,124,196\ns_add_u32 s88, s86, 101632\nQcvav 240,112,44,56\nbuffer_load_dwordx4 v64, s[48:51], s88 offen offset:1024 lds\nQmvaa 240,116,196\nQcaav 224,112,48,204\nQmaaa 224,116,200\ns_add_u32 s88, s86, 121088\n"
"Qcaav 228,120,48,204\nbuffer_load_dwordx4 v64, s[48:51], s88 offen offset:2048 lds\nQmaaa 228,124,200\nQcaav 232,128,48,204\nQmaaa 232,132,200\nQcaav 236,136,48,204\nQmaaa 236,140,200\ns_waitcnt lgkmcnt(0)\n"
"Qcvav 96,144,24,56\nQmvaa 96,148,176\ns_add_u32 s68, s71, 131072\nQcvav 100,152,24,56\ns_cbranch_vccz .Lst25\nbuffer_store_dwordx4 a[8:11], v79, s[56:59], s68 offen offset:0 sc1\n.Lst25:\nQmvaa 100,156,176\n"
"Qcvav 104,160,24,56\ns_add_u32 s68, s71, 147456\nQmvaa 104,164,176\ns_cbranch_vccz .Lst24\nbuffer_store_dwordx4 a[12:15], v79, s[56:59], s68 offen offset:0 sc1\n.Lst24:\nQcvav 108,168,24,56\nQmvaa 108,172,176\n"
"s_waitcnt lgkmcnt(0)\nQcvav 140,168,28,56\ns_waitcnt vmcnt(15)\ns_barrier\nQmvaa 140,172,180\nQdv 24,63,0\ns_add_u32 m0, s62, 16512\ns_add_u32 s69, s87, 1048704\n"
"Qcvav 136,160,28,56\nbuffer_load_dwordx4 v62, s[52:55], s69 offen lds\nQmvaa 136,164,180\nQda 176,63,512\nQcvav 132,152,28,56\nQta 112,61,33024\nQmvaa 132,156,180\ns_add_u32 s69, s87, 1113208\n"
"Qcvav 128,144,28,56\nbuffer_load_dwordx4 v62, s[52:55], s69 offen offset:1032 lds\nQmvaa 128,148,180\nQdv 28,63,1024\nQcvav 160,144,32,56\nQda 180,63,1536\nQmvaa 160,148,184\nQta 114,61,33536\n"
"s_add_u32 s69, s87, 1177712\nQcvav 164,152,32,56\nbuffer_load_dwordx4 v62, s[52:55], s69 offen offset:2064 lds\nQmvaa 164,156,184\nQta 116,61,37152\nQcvav 168,160,32,56\nQmvaa 168,164,184\n.Lpg0:\n"
"s_cbranch_execz .Ltr0\nQta 118,61,37664\ns_add_u32 s69, s87, 1242216\nQcvav 172,168,32,56\nbuffer_load_dwordx4 v62, s[52:55], s69 offen offset:3096 lds\nQmvaa 172,172,184\nQdv 32,63,2048\nQcvav 204,168,36,56\n"
"Qda 184,63,2560\nQmvaa 204,172,188\nQta 120,61,33032\ns_add_u32 m0, s61, 0\nQcvav 200,160,36,56\ns_cbranch_vccnz .Ld0q0\nbuffer_load_dwordx4 v69, s[40:43], s60 offen nt lds\n.Ld0q0:\n"
"Qmvaa 200,164,188\nQta 122,61,33544\nQcvav 196,152,36,56\nQmvaa 196,156,188\nQta 124,61,37160\nQcvav 192,144,36,56\nQdv 36,63,3072\nQmvaa 192,148,188\n"
"Qda 188,63,3584\nQcvav 224,144,40,56\nQta 126,61,37672\nQmvaa 224,148,192\nQcvav 228,152,40,56\nQta 128,61,33280\nQmvaa 228,156,192\nQcvav 232,160,40,56\n"
"Qta 130,61,33792\nQmvaa 232,164,192\nQcvav 236,168,40,56\nQdv 40,63,4096\nQmvaa 236,172,192\nQda 192,63,4608\nQcaav 220,168,44,204\nQta 132,61,37408\n"
"Qmaaa 220,172,196\nQcaav 216,160,44,204\nQta 134,61,37920\nQmaaa 216,164,196\nQcaav 212,152,44,204\nQta 136,61,33288\nQmaaa 212,156,196\nQcaav 208,144,44,204\n"
"Qdv 44,63,5120\nQmaaa 208,148,196\nQda 196,63,5632\nQcaav 240,144,48,204\nQta 138,61,33800\nQmaaa 240,148,200\nQcaav 244,152,48,204\nQta 140,61,37416\n"
"Qmaaa 244,156,200\nQcaav 248,160,48,204\nQta 142,61,37928\nQmaaa 248,164,200\nQcaav 252,168,48,204\nQdv 48,63,6144\nQmaaa 252,172,200\nQda 200,63,6656\n"
"s_waitcnt lgkmcnt(2)\nQmvav 80,112,24\nQmvaa 80,116,176\ns_add_u32 s68, s71, 0\nQmvav 84,120,24\ns_cbranch_vccz .Lst23\nbuffer_store_dwordx4 a[16:19], v79, s[56:59], s68 offen offset:1024 sc1\n.Lst23:\n"
"Qmvaa 84,124,176\nQmvav 88,128,24\ns_add_u32 s68, s71, 16384\nQmvaa 88,132,176\ns_cbranch_vccz .Lst22\nbuffer_store_dwordx4 a[20:23], v79, s[56:59], s68 offen offset:1024 sc1\n.Lst22:\nQmvav 92,136,24\n"
"Qmvaa 92,140,176\ns_waitcnt lgkmcnt(0)\nQmvav 124,136,28\ns_waitcnt vmcnt(17)\ns_barrier\nQmvaa 124,140,180\nQta 144,61,49536\ns_add_u32 m0, s62, 33024\n"
"s_add_u32 s69, s87, 1572864\nQmvav 120,128,28\nbuffer_load_dwordx4 v62, s[52:55], s69 offen lds\nQmvaa 120,132,180\nQta 146,61,50048\nQmvav 116,120,28\nQmvaa 116,124,180\nQta 148,61,53664\n"
"s_add_u32 s69, s87, 1637368\nQmvav 112,112,28\nbuffer_load_dwordx4 v62, s[52:55], s69 offen offset:1032 lds\nQmvaa 112,116,180\nQta 150,61,54176\nQmvav 144,112,32\nQmvaa 144,116,184\nQta 152,61,49544\n"
"s_add_u32 s69, s87, 1701872\nQmvav 148,120,32\nbuffer_load_dwordx4 v62, s[52:55], s69 offen offset:2064 lds\nQmvaa 148,124,184\nQta 154,61,50056\nQmvav 152,128,32\nQmvaa 152,132,184\nQta 156,61,53672\n"
"s_add_u32 s69, s87, 1766376\nQmvav 156,136,32\nbuffer_load_dwordx4 v62, s[52:55], s69 offen offset:3096 lds\nQmvaa 156,140,184\nQta 158,61,54184\nQmvav 188,136,36\nQmvaa 188,140,188\nQta 160,61,49792\n"
"s_add_u32 m0, s63, 0\ns_add_u32 s88, s86, 384\nQmvav 184,128,36\nbuffer_load_dwordx4 v64, s[48:51], s88 offen lds\nQmvaa 184,132,188\nQta 162,61,50304\nQmvav 180,120,36\nQmvaa 180,124,188\n"
"Qta 164,61,53920\ns_add_u32 s88, s86, 19840\nQmvav 176,112,36\nbuffer_load_dwordx4 v64, s[48:51], s88 offen offset:1024 lds\nQmvaa 176,116,188\nQta 166,61,54432\nQmvav 208,112,40\nQmvaa 208,116,192\n"
"Qta 168,61,49800\ns_add_u32 s88, s86, 39296\nQmvav 212,120,40\nbuffer_load_dwordx4 v64, s[48:51], s88 offen offset:2048 lds\nQmvaa 212,124,192\nQta 170,61,50312\nQmvav 216,128,40\nQmvaa 216,132,192\n"
"Qta 172,61,53928\ns_add_u32 s88, s86, 58752\nQmvav 220,136,40\nbuffer_load_dwordx4 v64, s[48:51], s88 offen offset:3072 lds\nQmvaa 220,140,192\nQta 174,61,54440\nQmvav 252,136,44\nQmvaa 252,140,196\n"
"s_add_u32 m0, s63, 4096\ns_add_u32 s88, s86, 82304\nQmvav 248,128,44\nbuffer_load_dwordx4 v64, s[48:51], s88 offen lds\nQmvaa 248,132,196\nQmvav 244,120,44\nQmvaa 244,124,196\ns_add_u32 s88, s86, 101760\n"
"Qmvav 240,112,44\nbuffer_load_dwordx4 v64, s[48:51], s88 offen offset:1024 lds\nQmvaa 240,116,196\nQmaav 224,112,48\nQmaaa 224,116,200\ns_add_u32 s88, s86, 121216\nQmaav 228,120,48\nbuffer_load_dwordx4 v64, s[48:51], s88 offen offset:2048 lds\n"
"Qmaaa 228,124,200\nQmaav 232,128,48\nQmaaa 232,132,200\nQmaav 236,136,48\nQmaaa 236,140,200\ns_waitcnt lgkmcnt(0)\nQmvav 96,144,24\nQmvaa 96,148,176\n"
"s_add_u32 s68, s71, 131072\nQmvav 100,152,24\ns_cbranch_vccz .Lst21\nbuffer_store_dwordx4 a[24:27], v79, s[56:59], s68 offen offset:1024 sc1\n.Lst21:\nQmvaa 100,156,176\nQmvav 104,160,24\ns_add_u32 s68, s71, 147456\n"
"Qmvaa 104,164,176\ns_cbranch_vccz .Lst20\nbuffer_store_dwordx4 a[28:31], v79, s[56:59], s68 offen offset:1024 sc1\n.Lst20:\nQmvav 108,168,24\nQmvaa 108,172,176\ns_waitcnt lgkmcnt(0)\nQmvav 140,168,28\n"
"s_waitcnt vmcnt(16)\ns_barrier\nQmvaa 140,172,180\nQdv 24,63,33024\ns_add_u32 m0, s62, 49536\ns_add_u32 s69, s87, 1572992\nQmvav 136,160,28\nbuffer_load_dwordx4 v62, s[52:55], s69 offen lds\n"
"Qmvaa 136,164,180\nQda 176,63,33536\nQmvav 132,152,28\nQta 112,61,0\nQmvaa 132,156,180\ns_add_u32 s69, s87, 1637496\nQmvav 128,144,28\nbuffer_load_dwordx4 v62, s[52:55], s69 offen offset:1032 lds\n"
"Qmvaa 128,148,180\nQdv 28,63,34048\nQmvav 160,144,32\nQda 180,63,34560\nQmvaa 160,148,184\nQta 114,61,512\ns_add_u32 s69, s87, 1702000\nQmvav 164,152,32\n"
"buffer_load_dwordx4 v62, s[52:55], s69 offen offset:2064 lds\nQmvaa 164,156,184\nQta 116,61,4128\nQmvav 168,160,32\nQmvaa 168,164,184\nQta 118,61,4640\ns_add_u32 s69, s87, 1766504\nQmvav 172,168,32\n"
"buffer_load_dwordx4 v62, s[52:55], s69 offen offset:3096 lds\nQmvaa 172,172,184\nQdv 32,63,35072\nQmvav 204,168,36\nQda 184,63,35584\nQmvaa 204,172,188\nQta 120,61,8\ns_add_u32 m0, s61, 0\n"
"Qmvav 200,160,36\ns_cbranch_vccnz .Ld0q1\nbuffer_load_dwordx4 v69, s[40:43], s60 offen offset:1024 nt lds\n.Ld0q1:\nQmvaa 200,164,188\nQta 122,61,520\nQmvav 196,152,36\nQmvaa 196,156,188\n"
"Qta 124,61,4136\nQmvav 192,144,36\nQdv 36,63,36096\nQmvaa 192,148,188\nQda 188,63,36608\nQmvav 224,144,40\nQta 126,61,4648\nQmvaa 224,148,192\n"
"Qmvav 228,152,40\nQta 128,61,256\nQmvaa 228,156,192\nQmvav 232,160,40\nQta 130,61,768\nQmvaa 232,164,192\nQmvav 236,168,40\nQdv 40,63,37120\n"
"Qmvaa 236,172,192\nQda 192,63,37632\nQmaav 220,168,44\nQta 132,61,4384\nQmaaa 220,172,196\nQmaav 216,160,44\nQta 134,61,4896\nQmaaa 216,164,196\n"
"Qmaav 212,152,44\nQta 136,61,264\nQmaaa 212,156,196\nQmaav 208,144,44\nQdv 44,63,38144\nQmaaa 208,148,196\nQda 196,63,38656\nQmaav 240,144,48\n"
"Qta 138,61,776\nQmaaa 240,148,200\nQmaav 244,152,48\nQta 140,61,4392\nQmaaa 244,156,200\nQmaav 248,160,48\nQta 142,61,4904\nQmaaa 248,164,200\n"
"Qmaav 252,168,48\nQdv 48,63,39168\nQmaaa 252,172,200\nQda 200,63,39680\ns_waitcnt lgkmcnt(2)\nQmvav 80,112,24\nQmvaa 80,116,176\ns_add_u32 s68, s71, 0\n"
"Qmvav 84,120,24\ns_cbranch_vccz .Lst19\nbuffer_store_dwordx4 a[32:35], v79, s[56:59], s68 offen offset:2048 sc1\n.Lst19:\nQmvaa 84,124,176\nQmvav 88,128,24\ns_add_u32 s68, s71, 16384\nQmvaa 88,132,176\n"
"s_cbranch_vccz .Lst18\nbuffer_store_dwordx4 a[36:39], v79, s[56:59], s68 offen offset:2048 sc1\n.Lst18:\nQmvav 92,136,24\nQmvaa 92,140,176\ns_waitcnt lgkmcnt(0)\nQmvav 124,136,28\ns_waitcnt vmcnt(17)\n"
"s_barrier\nQmvaa 124,140,180\nQta 144,61,16512\ns_add_u32 m0, s62, 0\ns_add_u32 s69, s87, 2097152\nQmvav 120,128,28\nbuffer_load_dwordx4 v62, s[52:55], s69 offen lds\nQmvaa 120,132,180\n"
"Qta 146,61,17024\nQmvav 116,120,28\nQmvaa 116,124,180\nQta 148,61,20640\ns_add_u32 s69, s87, 2161656\nQmvav 112,112,28\nbuffer_load_dwordx4 v62, s[52:55], s69 offen offset:1032 lds\nQmvaa 112,116,180\n"
"Qta 150,61,21152\nQmvav 144,112,32\nQmvaa 144,116,184\nQta 152,61,16520\ns_add_u32 s69, s87, 2226160\nQmvav 148,120,32\nbuffer_load_dwordx4 v62, s[52:55], s69 offen offset:2064 lds\nQmvaa 148,124,184\n"
"Qta 154,61,17032\nQmvav 152,128,32\nQmvaa 152,132,184\nQta 156,61,20648\ns_add_u32 s69, s87, 2290664\nQmvav 156,136,32\nbuffer_load_dwordx4 v62, s[52:55], s69 offen offset:3096 lds\nQmvaa 156,140,184\n"
"Qta 158,61,21160\nQmvav 188,136,36\nQmvaa 188,140,188\nQta 160,61,16768\ns_add_u32 m0, s63, 33024\ns_add_u32 s88, s86, 512\nQmvav 184,128,36\nbuffer_load_dwordx4 v64, s[48:51], s88 offen lds\n"
"Qmvaa 184,132,188\nQta 162,61,17280\nQmvav 180,120,36\nQmvaa 180,124,188\nQta 164,61,20896\ns_add_u32 s88, s86, 19968\nQmvav 176,112,36\nbuffer_load_dwordx4 v64, s[48:51], s88 offen offset:1024 lds\n"
"Qmvaa 176,116,188\nQta 166,61,21408\nQmvav 208,112,40\nQmvaa 208,116,192\nQta 168,61,16776\ns_add_u32 s88, s86, 39424\nQmvav 212,120,40\nbuffer_load_dwordx4 v64, s[48:51], s88 offen offset:2048 lds\n"
"Qmvaa 212,124,192\nQta 170,61,17288\nQmvav 216,128,40\nQmvaa 216,132,192\nQta 172,61,20904\ns_add_u32 s88, s86, 58880\nQmvav 220,136,40\nbuffer_load_dwordx4 v64, s[48:51], s88 offen offset:3072 lds\n"
"Qmvaa 220,140,192\nQta 174,61,21416\nQmvav 252,136,44\nQmvaa 252,140,196\ns_add_u32 m0, s63, 37120\ns_add_u32 s88, s86, 82432\nQmvav 248,128,44\nbuffer_load_dwordx4 v64, s[48:51], s88 offen lds\n"
"Qmvaa 248,132,196\nQmvav 244,120,44\nQmvaa 244,124,196\ns_add_u32 s88, s86, 101888\nQmvav 240,112,44\nbuffer_load_dwordx4 v64, s[48:51], s88 offen offset:1024 lds\nQmvaa 240,116,196\nQmaav 224,112,48\n"
"Qmaaa 224,116,200\ns_add_u32 s88, s86, 121344\nQmaav 228,120,48\nbuffer_load_dwordx4 v64, s[48:51], s88 offen offset:2048 lds\nQmaaa 228,124,200\nQmaav 232,128,48\nQmaaa 232,132,200\nQmaav 236,136,48\n"
"Qmaaa 236,140,200\ns_waitcnt lgkmcnt(0)\nQmvav 96,144,24\nQmvaa 96,148,176\ns_add_u32 s68, s71, 131072\nQmvav 100,152,24\ns_cbranch_vccz .Lst17\nbuffer_store_dwordx4 a[40:43], v79, s[56:59], s68 offen offset:2048 sc1\n"
".Lst17:\nQmvaa 100,156,176\nQmvav 104,160,24\ns_add_u32 s68, s71, 147456\nQmvaa 104,164,176\ns_cbranch_vccz .Lst16\nbuffer_store_dwordx4 a[44:47], v79, s[56:59], s68 offen offset:2048 sc1\n.Lst16:\n"
"Qmvav 108,168,24\nQmvaa 108,172,176\ns_waitcnt lgkmcnt(0)\nQmvav 140,168,28\ns_waitcnt vmcnt(16)\ns_barrier\nQmvaa 140,172,180\nQdv 24,63,0\n"
"s_add_u32 m0, s62, 16512\ns_add_u32 s69, s87, 2097280\nQmvav 136,160,28\nbuffer_load_dwordx4 v62, s[52:55], s69 offen lds\nQmvaa 136,164,180\nQda 176,63,512\nQmvav 132,152,28\nQta 112,61,33024\n"
"Qmvaa 132,156,180\ns_add_u32 s69, s87, 2161784\nQmvav 128,144,28\nbuffer_load_dwordx4 v62, s[52:55], s69 offen offset:1032 lds\nQmvaa 128,148,180\nQdv 28,63,1024\nQmvav 160,144,32\nQda 180,63,1536\n"
"Qmvaa 160,148,184\nQta 114,61,33536\ns_add_u32 s69, s87, 2226288\nQmvav 164,152,32\nbuffer_load_dwordx4 v62, s[52:55], s69 offen offset:2064 lds\nQmvaa 164,156,184\nQta 116,61,37152\nQmvav 168,160,32\n"
"Qmvaa 168,164,184\nQta 118,61,37664\ns_add_u32 s69, s87, 2290792\nQmvav 172,168,32\nbuffer_load_dwordx4 v62, s[52:55], s69 offen offset:3096 lds\nQmvaa 172,172,184\nQdv 32,63,2048\nQmvav 204,168,36\n"
"Qda 184,63,2560\nQmvaa 204,172,188\nQta 120,61,33032\ns_add_u32 m0, s61, 0\nQmvav 200,160,36\ns_cbranch_vccnz .Ld0q2\nbuffer_load_dwordx4 v69, s[40:43], s90 offen offset:2048 nt lds\n.Ld0q2:\n"
"Qmvaa 200,164,188\nQta 122,61,33544\nQmvav 196,152,36\nQmvaa 196,156,188\nQta 124,61,37160\nQmvav 192,144,36\nQdv 36,63,3072\nQmvaa 192,148,188\n"
"Qda 188,63,3584\nQmvav 224,144,40\nQta 126,61,37672\nQmvaa 224,148,192\nQmvav 228,152,40\nQta 128,61,33280\nQmvaa 228,156,192\nQmvav 232,160,40\n"
"Qta 130,61,33792\nQmvaa 232,164,192\nQmvav 236,168,40\nQdv 40,63,4096\nQmvaa 236,172,192\nQda 192,63,4608\nQmaav 220,168,44\nQta 132,61,37408\n"
"Qmaaa 220,172,196\nQmaav 216,160,44\nQta 134,61,37920\nQmaaa 216,164,196\nQmaav 212,152,44\nQta 136,61,33288\nQmaaa 212,156,196\nQmaav 208,144,44\n"
"Qdv 44,63,5120\nQmaaa 208,148,196\nQda 196,63,5632\nQmaav 240,144,48\nQta 138,61,33800\nQmaaa 240,148,200\nQmaav 244,152,48\nQta 140,61,37416\n"
"Qmaaa 244,156,200\nQmaav 248,160,48\nQta 142,61,37928\nQmaaa 248,164,200\nQmaav 252,168,48\nQdv 48,63,6144\nQmaaa 252,172,200\nQda 200,63,6656\n"
"s_waitcnt lgkmcnt(2)\nQmvav 80,112,24\nQmvaa 80,116,176\ns_add_u32 s68, s71, 0\nQmvav 84,120,24\ns_cbranch_vccz .Lst15\nbuffer_store_dwordx4 a[48:51], v79, s[56:59], s68 offen offset:3072 sc1\n.Lst15:\n"
"Qmvaa 84,124,176\nQmvav 88,128,24\ns_add_u32 s68, s71, 16384\nQmvaa 88,132,176\ns_cbranch_vccz .Lst14\nbuffer_store_dwordx4 a[52:55], v79, s[56:59], s68 offen offset:3072 sc1\n.Lst14:\nQmvav 92,136,24\n"
"Qmvaa 92,140,176\ns_waitcnt lgkmcnt(0)\nQmvav 124,136,28\ns_waitcnt vmcnt(17)\ns_barrier\nQmvaa 124,140,180\nQta 144,61,49536\ns_add_u32 m0, s62, 33024\n"
"s_add_u32 s69, s87, 2621440\ns_mov_b32 s89, 0\nQmvav 120,128,28\nbuffer_load_dwordx4 v62, s[52:55], s69 offen lds\ns_cbranch_vccnz .Lpb0\ns_load_dword s89, s[30:31], 0x0\n.Lpb0:\nQmvaa 120,132,180\n"
"Qta 146,61,50048\nQmvav 116,120,28\nQmvaa 116,124,180\nQta 148,61,53664\ns_add_u32 s69, s87, 2685944\nQmvav 112,112,28\nbuffer_load_dwordx4 v62, s[52:55], s69 offen offset:1032 lds\ns_add_u32 s77, s77, s89\n"
"Qmvaa 112,116,180\nQta 150,61,54176\nQmvav 144,112,32\ns_add_u32 s77, s77, s89\nQmvaa 144,116,184\n.Lpg1:\ns_cbranch_execz .Ltr0\nQta 152,61,49544\n"
"s_add_u32 s69, s87, 2750448\nQmvav 148,120,32\nbuffer_load_dwordx4 v62, s[52:55], s69 offen offset:2064 lds\ns_add_u32 s77, s77, s89\nQmvaa 148,124,184\nQta 154,61,50056\nQmvav 152,128,32\ns_add_u32 s77, s77, s89\n"
"Qmvaa 152,132,184\nQta 156,61,53672\ns_add_u32 s69, s87, 2814952\nQmvav 156,136,32\nbuffer_load_dwordx4 v62, s[52:55], s69 offen offset:3096 lds\ns_add_u32 s77, s77, s89\nQmvaa 156,140,184\nQta 158,61,54184\n"
"Qmvav 188,136,36\ns_add_u32 s77, s77, s89\nQmvaa 188,140,188\nQta 160,61,49792\ns_add_u32 m0, s63, 0\ns_add_u32 s88, s86, 640\nQmvav 184,128,36\nbuffer_load_dwordx4 v64, s[48:51], s88 offen lds\n"
"s_add_u32 s77, s77, s89\nQmvaa 184,132,188\nQta 162,61,50304\nQmvav 180,120,36\ns_add_u32 s77, s77, s89\nQmvaa 180,124,188\nQta 164,61,53920\ns_add_u32 s88, s86, 20096\n"
"Qmvav 176,112,36\nbuffer_load_dwordx4 v64, s[48:51], s88 offen offset:1024 lds\ns_add_u32 s77, s77, s89\nQmvaa 176,116,188\nQta 166,61,54432\nQmvav 208,112,40\ns_add_u32 s77, s77, s89\nQmvaa 208,116,192\n"
"Qta 168,61,49800\ns_add_u32 s88, s86, 39552\nQmvav 212,120,40\nbuffer_load_dwordx4 v64, s[48:51], s88 offen offset:2048 lds\ns_add_u32 s77, s77, s89\nQmvaa 212,124,192\nQta 170,61,50312\nQmvav 216,128,40\n"
"s_add_u32 s77, s77, s89\nQmvaa 216,132,192\nQta 172,61,53928\ns_add_u32 s88, s86, 59008\nQmvav 220,136,40\nbuffer_load_dwordx4 v64, s[48:51], s88 offen offset:3072 lds\ns_add_u32 s77, s77, s89\nQmvaa 220,140,192\n"
"Qta 174,61,54440\nQmvav 252,136,44\ns_add_u32 s77, s77, s89\nQmvaa 252,140,196\ns_add_u32 m0, s63, 4096\ns_add_u32 s88, s86, 82560\nQmvav 248,128,44\nbuffer_load_dwordx4 v64, s[48:51], s88 offen lds\n"
"s_add_u32 s77, s77, s89\nQmvaa 248,132,196\nQmvav 244,120,44\ns_add_u32 s77, s77, s89\nQmvaa 244,124,196\ns_add_u32 s88, s86, 102016\nQmvav 240,112,44\nbuffer_load_dwordx4 v64, s[48:51], s88 offen offset:1024 lds\n"
"s_add_u32 s77, s77, s89\nQmvaa 240,116,196\nQmaav 224,112,48\ns_add_u32 s77, s77, s89\nQmaaa 224,116,200\ns_add_u32 s88, s86, 121472\nQmaav 228,120,48\nbuffer_load_dwordx4 v64, s[48:51], s88 offen offset:2048 lds\n"
"s_add_u32 s77, s77, s89\nQmaaa 228,124,200\nQmaav 232,128,48\ns_add_u32 s77, s77, s89\nQmaaa 232,132,200\nQmaav 236,136,48\ns_add_u32 s77, s77, s89\nQmaaa 236,140,200\n"
"s_waitcnt lgkmcnt(0)\nQmvav 96,144,24\nQmvaa 96,148,176\ns_add_u32 s68, s71, 131072\nQmvav 100,152,24\ns_cbranch_vccz .Lst13\nbuffer_store_dwordx4 a[56:59], v79, s[56:59], s68 offen offset:3072 sc1\n.Lst13:\n"
"Qmvaa 100,156,176\nQmvav 104,160,24\ns_add_u32 s68, s71, 147456\nQmvaa 104,164,176\ns_cbranch_vccz .Lst12\nbuffer_store_dwordx4 a[60:63], v79, s[56:59], s68 offen offset:3072 sc1\n.Lst12:\nQmvav 108,168,24\n"
"Qmvaa 108,172,176\ns_waitcnt lgkmcnt(0)\nQmvav 140,168,28\ns_waitcnt vmcnt(16)\ns_barrier\nQmvaa 140,172,180\nQdv 24,63,33024\ns_add_u32 m0, s62, 49536\n"
"s_add_u32 s69, s87, 2621568\nQmvav 136,160,28\nbuffer_load_dwordx4 v62, s[52:55], s69 offen lds\nQmvaa 136,164,180\nQda 176,63,33536\nQmvav 132,152,28\nQta 112,61,0\nQmvaa 132,156,180\n"
"s_add_u32 s69, s87, 2686072\nQmvav 128,144,28\nbuffer_load_dwordx4 v62, s[52:55], s69 offen offset:1032 lds\nQmvaa 128,148,180\nQdv 28,63,34048\nQmvav 160,144,32\nQda 180,63,34560\nQmvaa 160,148,184\n"
"Qta 114,61,512\ns_add_u32 s69, s87, 2750576\nQmvav 164,152,32\nbuffer_load_dwordx4 v62, s[52:55], s69 offen offset:2064 lds\nQmvaa 164,156,184\nQta 116,61,4128\nQmvav 168,160,32\nQmvaa 168,164,184\n"
"Qta 118,61,4640\ns_add_u32 s69, s87, 2815080\nQmvav 172,168,32\nbuffer_load_dwordx4 v62, s[52:55], s69 offen offset:3096 lds\nQmvaa 172,172,184\nQdv 32,63,35072\nQmvav 204,168,36\nQda 184,63,35584\n"
"Qmvaa 204,172,188\nQta 120,61,8\ns_add_u32 m0, s61, 0\nQmvav 200,160,36\ns_cbranch_vccnz .Ld0q3\nbuffer_load_dwordx4 v69, s[40:43], s90 offen offset:3072 nt lds\n.Ld0q3:\nQmvaa 200,164,188\n"
"Qta 122,61,520\nQmvav 196,152,36\nQmvaa 196,156,188\nQta 124,61,4136\nQmvav 192,144,36\nQdv 36,63,36096\nQmvaa 192,148,188\nQda 188,63,36608\n"
"Qmvav 224,144,40\nQta 126,61,4648\nQmvaa 224,148,192\nQmvav 228,152,40\nQta 128,61,256\nQmvaa 228,156,192\nQmvav 232,160,40\nQta 130,61,768\n"
"Qmvaa 232,164,192\nQmvav 236,168,40\nQdv 40,63,37120\nQmvaa 236,172,192\nQda 192,63,37632\nQmaav 220,168,44\nQta 132,61,4384\nQmaaa 220,172,196\n"
"Qmaav 216,160,44\nQta 134,61,4896\nQmaaa 216,164,196\nQmaav 212,152,44\nQta 136,61,264\nQmaaa 212,156,196\nQmaav 208,144,44\nQdv 44,63,38144\n"
"Qmaaa 208,148,196\nQda 196,63,38656\nQmaav 240,144,48\nQta 138,61,776\nQmaaa 240,148,200\nQmaav 244,152,48\nQta 140,61,4392\nQmaaa 244,156,200\n"
"Qmaav 248,160,48\nQta 142,61,4904\nQmaaa 248,164,200\nQmaav 252,168,48\nQdv 48,63,39168\nQmaaa 252,172,200\nQda 200,63,39680\ns_waitcnt lgkmcnt(2)\n"
"Qmvav 80,112,24\nQmvaa 80,116,176\ns_add_u32 s68, s71, 4096\nQmvav 84,120,24\ns_cbranch_vccz .Lst11\nbuffer_store_dwordx4 a[64:67], v79, s[56:59], s68 offen offset:0 sc1\n.Lst11:\nQmvaa 84,124,176\n"
"Qmvav 88,128,24\ns_add_u32 s68, s71, 20480\nQmvaa 88,132,176\ns_cbranch_vccz .Lst10\nbuffer_store_dwordx4 a[68:71], v79, s[56:59], s68 offen offset:0 sc1\n.Lst10:\nQmvav 92,136,24\nQmvaa 92,140,176\n"
"s_waitcnt lgkmcnt(0)\nQmvav 124,136,28\ns_waitcnt vmcnt(17)\ns_barrier\nQmvaa 124,140,180\nQta 144,61,16512\ns_add_u32 m0, s62, 0\ns_add_u32 s69, s87, 3145728\n"
"s_mov_b32 s89, 0\nQmvav 120,128,28\nbuffer_load_dwordx4 v62, s[52:55], s69 offen lds\ns_cbranch_vccnz .Lpb1\ns_load_dword s89, s[30:31], 0x2000\n.Lpb1:\nQmvaa 120,132,180\nQta 146,61,17024\n"
"Qmvav 116,120,28\nQmvaa 116,124,180\nQta 148,61,20640\ns_add_u32 s69, s87, 3210232\nQmvav 112,112,28\nbuffer_load_dwordx4 v62, s[52:55], s69 offen offset:1032 lds\ns_add_u32 s77, s77, s89\nQmvaa 112,116,180\n"
"Qta 150,61,21152\nQmvav 144,112,32\ns_add_u32 s77, s77, s89\nQmvaa 144,116,184\nQta 152,61,16520\ns_add_u32 s69, s87, 3274736\nQmvav 148,120,32\nbuffer_load_dwordx4 v62, s[52:55], s69 offen offset:2064 lds\n"
"s_add_u32 s77, s77, s89\nQmvaa 148,124,184\nQta 154,61,17032\nQmvav 152,128,32\ns_add_u32 s77, s77, s89\nQmvaa 152,132,184\nQta 156,61,20648\ns_add_u32 s69, s87, 3339240\n"
"Qmvav 156,136,32\nbuffer_load_dwordx4 v62, s[52:55], s69 offen offset:3096 lds\ns_add_u32 s77, s77, s89\nQmvaa 156,140,184\nQta 158,61,21160\nQmvav 188,136,36\ns_add_u32 s77, s77, s89\nQmvaa 188,140,188\n"
"Qta 160,61,16768\ns_add_u32 m0, s63, 33024\ns_add_u32 s88, s86, 768\nQmvav 184,128,36\nbuffer_load_dwordx4 v64, s[48:51], s88 offen lds\ns_add_u32 s77, s77, s89\nQmvaa 184,132,188\nQta 162,61,17280\n"
"Qmvav 180,120,36\ns_add_u32 s77, s77, s89\nQmvaa 180,124,188\nQta 164,61,20896\ns_add_u32 s88, s86, 20224\nQmvav 176,112,36\nbuffer_load_dwordx4 v64, s[48:51], s88 offen offset:1024 lds\ns_add_u32 s77, s77, s89\n"
"Qmvaa 176,116,188\nQta 166,61,21408\nQmvav 208,112,40\ns_add_u32 s77, s77, s89\nQmvaa 208,116,192\nQta 168,61,16776\ns_add_u32 s88, s86, 39680\nQmvav 212,120,40\n"
"buffer_load_dwordx4 v64, s[48:51], s88 offen offset:2048 lds\ns_add_u32 s77, s77, s89\nQmvaa 212,124,192\nQta 170,61,17288\nQmvav 216,128,40\ns_add_u32 s77, s77, s89\nQmvaa 216,132,192\nQta 172,61,20904\n"
"s_add_u32 s88, s86, 59136\nQmvav 220,136,40\nbuffer_load_dwordx4 v64, s[48:51], s88 offen offset:3072 lds\ns_add_u32 s77, s77, s89\nQmvaa 220,140,192\nQta 174,61,21416\nQmvav 252,136,44\ns_add_u32 s77, s77, s89\n"
"Qmvaa 252,140,196\ns_add_u32 m0, s63, 37120\ns_add_u32 s88, s86, 82688\nQmvav 248,128,44\nbuffer_load_dwordx4 v64, s[48:51], s88 offen lds\ns_add_u32 s77, s77, s89\nQmvaa 248,132,196\nQmvav 244,120,44\n"
"s_add_u32 s77, s77, s89\nQmvaa 244,124,196\ns_add_u32 s88, s86, 102144\nQmvav 240,112,44\nbuffer_load_dwordx4 v64, s[48:51], s88 offen offset:1024 lds\ns_add_u32 s77, s77, s89\nQmvaa 240,116,196\nQmaav 224,112,48\n"
"s_add_u32 s77, s77, s89\nQmaaa 224,116,200\ns_add_u32 s88, s86, 121600\nQmaav 228,120,48\nbuffer_load_dwordx4 v64, s[48:51], s88 offen offset:2048 lds\ns_add_u32 s77, s77, s89\nQmaaa 228,124,200\nQmaav 232,128,48\n"
"s_add_u32 s77, s77, s89\nQmaaa 232,132,200\nQmaav 236,136,48\ns_add_u32 s77, s77, s89\nQmaaa 236,140,200\ns_waitcnt lgkmcnt(0)\nQmvav 96,144,24\nQmvaa 96,148,176\n"
"s_add_u32 s68, s71, 135168\nQmvav 100,152,24\ns_cbranch_vccz .Lst9\nbuffer_store_dwordx4 a[72:75], v79, s[56:59], s68 offen offset:0 sc1\n.Lst9:\nQmvaa 100,156,176\nQmvav 104,160,24\ns_add_u32 s68, s71, 151552\n"
"Qmvaa 104,164,176\ns_cbranch_vccz .Lst8\nbuffer_store_dwordx4 a[76:79], v79, s[56:59], s68 offen offset:0 sc1\n.Lst8:\nQmvav 108,168,24\nQmvaa 108,172,176\ns_waitcnt lgkmcnt(0)\nQmvav 140,168,28\n"
"s_waitcnt vmcnt(16)\ns_barrier\nQmvaa 140,172,180\nQdv 24,63,0\ns_add_u32 m0, s62, 16512\ns_add_u32 s69, s87, 3145856\nQmvav 136,160,28\nbuffer_load_dwordx4 v62, s[52:55], s69 offen lds\n"
"Qmvaa 136,164,180\nQda 176,63,512\nQmvav 132,152,28\nQta 112,61,33024\nQmvaa 132,156,180\ns_add_u32 s69, s87, 3210360\nQmvav 128,144,28\nbuffer_load_dwordx4 v62, s[52:55], s69 offen offset:1032 lds\n"
"Qmvaa 128,148,180\nQdv 28,63,1024\nQmvav 160,144,32\nQda 180,63,1536\nQmvaa 160,148,184\nQta 114,61,33536\ns_add_u32 s69, s87, 3274864\nQmvav 164,152,32\n"
"buffer_load_dwordx4 v62, s[52:55], s69 offen offset:2064 lds\nQmvaa 164,156,184\nQta 116,61,37152\nQmvav 168,160,32\nQmvaa 168,164,184\nQta 118,61,37664\ns_add_u32 s69, s87, 3339368\nQmvav 172,168,32\n"
"buffer_load_dwordx4 v62, s[52:55], s69 offen offset:3096 lds\nQmvaa 172,172,184\nQdv 32,63,2048\nQmvav 204,168,36\nQda 184,63,2560\nQmvaa 204,172,188\nQta 120,61,33032\ns_add_u32 m0, s64, 0\n"
"Qmvav 200,160,36\ns_cbranch_vccnz .Ld0q4\nbuffer_load_dwordx4 v72, s[44:47], s60 offen nt lds\n.Ld0q4:\nQmvaa 200,164,188\nQta 122,61,33544\nQmvav 196,152,36\nQmvaa 196,156,188\n"
"Qta 124,61,37160\nQmvav 192,144,36\nQdv 36,63,3072\nQmvaa 192,148,188\nQda 188,63,3584\nQmvav 224,144,40\nQta 126,61,37672\nQmvaa 224,148,192\n"
"Qmvav 228,152,40\nQta 128,61,33280\nQmvaa 228,156,192\nQmvav 232,160,40\nQta 130,61,33792\nQmvaa 232,164,192\nQmvav 236,168,40\nQdv 40,63,4096\n"
"Qmvaa 236,172,192\nQda 192,63,4608\nQmaav 220,168,44\nQta 132,61,37408\nQmaaa 220,172,196\nQmaav 216,160,44\nQta 134,61,37920\nQmaaa 216,164,196\n"
"Qmaav 212,152,44\nQta 136,61,33288\nQmaaa 212,156,196\nQmaav 208,144,44\nQdv 44,63,5120\nQmaaa 208,148,196\nQda 196,63,5632\nQmaav 240,144,48\n"
"Qta 138,61,33800\nQmaaa 240,148,200\nQmaav 244,152,48\nQta 140,61,37416\nQmaaa 244,156,200\nQmaav 248,160,48\nQta 142,61,37928\nQmaaa 248,164,200\n"
"Qmaav 252,168,48\nQdv 48,63,6144\nQmaaa 252,172,200\nQda 200,63,6656\ns_waitcnt lgkmcnt(2)\nQmvav 80,112,24\nQmvaa 80,116,176\ns_add_u32 s68, s71, 4096\n"
"Qmvav 84,120,24\ns_cbranch_vccz .Lst7\nbuffer_store_dwordx4 a[80:83], v79, s[56:59], s68 offen offset:1024 sc1\n.Lst7:\nQmvaa 84,124,176\nQmvav 88,128,24\ns_add_u32 s68, s71, 20480\nQmvaa 88,132,176\n"
"s_cbranch_vccz .Lst6\nbuffer_store_dwordx4 a[84:87], v79, s[56:59], s68 offen offset:1024 sc1\n.Lst6:\nQmvav 92,136,24\nQmvaa 92,140,176\ns_waitcnt lgkmcnt(0)\nQmvav 124,136,28\ns_waitcnt vmcnt(17)\n"
"s_barrier\nQmvaa 124,140,180\nQta 144,61,49536\ns_add_u32 m0, s62, 33024\ns_add_u32 s69, s87, 3670016\ns_mov_b32 s89, 0\nQmvav 120,128,28\nbuffer_load_dwordx4 v62, s[52:55], s69 offen lds\n"
"s_cbranch_vccnz .Lpb2\ns_load_dword s89, s[30:31], 0x6000\n.Lpb2:\nQmvaa 120,132,180\nQta 146,61,50048\nQmvav 116,120,28\nQmvaa 116,124,180\nQta 148,61,53664\n"
"s_add_u32 s69, s87, 3734520\nQmvav 112,112,28\nbuffer_load_dwordx4 v62, s[52:55], s69 offen offset:1032 lds\ns_add_u32 s77, s77, s89\nQmvaa 112,116,180\nQta 150,61,54176\nQmvav 144,112,32\ns_add_u32 s77, s77, s89\n"
"Qmvaa 144,116,184\nQta 152,61,49544\ns_add_u32 s69, s87, 3799024\nQmvav 148,120,32\nbuffer_load_dwordx4 v62, s[52:55], s69 offen offset:2064 lds\ns_add_u32 s77, s77, s89\nQmvaa 148,124,184\nQta 154,61,50056\n"
"Qmvav 152,128,32\ns_add_u32 s77, s77, s89\nQmvaa 152,132,184\nQta 156,61,53672\ns_add_u32 s69, s87, 3863528\nQmvav 156,136,32\nbuffer_load_dwordx4 v62, s[52:55], s69 offen offset:3096 lds\ns_add_u32 s77, s77, s89\n"
"Qmvaa 156,140,184\nQta 158,61,54184\nQmvav 188,136,36\ns_add_u32 s77, s77, s89\nQmvaa 188,140,188\nQta 160,61,49792\ns_add_u32 m0, s63, 0\ns_add_u32 s88, s86, 896\n"
"Qmvav 184,128,36\nbuffer_load_dwordx4 v64, s[48:51], s88 offen lds\ns_add_u32 s77, s77, s89\nQmvaa 184,132,188\nQta 162,61,50304\nQmvav 180,120,36\ns_add_u32 s77, s77, s89\nQmvaa 180,124,188\n"
"Qta 164,61,53920\ns_add_u32 s88, s86, 20352\nQmvav 176,112,36\nbuffer_load_dwordx4 v64, s[48:51], s88 offen offset:1024 lds\ns_add_u32 s77, s77, s89\nQmvaa 176,116,188\nQta 166,61,54432\nQmvav 208,112,40\n"
"s_add_u32 s77, s77, s89\nQmvaa 208,116,192\nQta 168,61,49800\ns_add_u32 s88, s86, 39808\nQmvav 212,120,40\nbuffer_load_dwordx4 v64, s[48:51], s88 offen offset:2048 lds\ns_add_u32 s77, s77, s89\nQmvaa 212,124,192\n"
"Qta 170,61,50312\nQmvav 216,128,40\ns_add_u32 s77, s77, s89\nQmvaa 216,132,192\nQta 172,61,53928\ns_add_u32 s88, s86, 59264\nQmvav 220,136,40\nbuffer_load_dwordx4 v64, s[48:51], s88 offen offset:3072 lds\n"
"s_add_u32 s77, s77, s89\nQmvaa 220,140,192\nQta 174,61,54440\nQmvav 252,136,44\ns_add_u32 s77, s77, s89\nQmvaa 252,140,196\ns_add_u32 m0, s63, 4096\ns_add_u32 s88, s86, 82816\n"
"Qmvav 248,128,44\nbuffer_load_dwordx4 v64, s[48:51], s88 offen lds\ns_add_u32 s77, s77, s89\nQmvaa 248,132,196\nQmvav 244,120,44\ns_add_u32 s77, s77, s89\nQmvaa 244,124,196\ns_add_u32 s88, s86, 102272\n"
"Qmvav 240,112,44\nbuffer_load_dwordx4 v64, s[48:51], s88 offen offset:1024 lds\ns_add_u32 s77, s77, s89\nQmvaa 240,116,196\nQmaav 224,112,48\ns_add_u32 s77, s77, s89\nQmaaa 224,116,200\ns_add_u32 s88, s86, 121728\n"
"Qmaav 228,120,48\n.Lpg2:\ns_cbranch_execz .Ltr0\nbuffer_load_dwordx4 v64, s[48:51], s88 offen offset:2048 lds\ns_add_u32 s77, s77, s89\nQmaaa 228,124,200\nQmaav 232,128,48\ns_add_u32 s77, s77, s89\n"
"Qmaaa 232,132,200\nQmaav 236,136,48\ns_add_u32 s77, s77, s89\nQmaaa 236,140,200\ns_waitcnt lgkmcnt(0)\nQmvav 96,144,24\nQmvaa 96,148,176\ns_add_u32 s68, s71, 135168\n"
"Qmvav 100,152,24\ns_cbranch_vccz .Lst5\nbuffer_store_dwordx4 a[88:91], v79, s[56:59], s68 offen offset:1024 sc1\n.Lst5:\nQmvaa 100,156,176\nQmvav 104,160,24\ns_add_u32 s68, s71, 151552\nQmvaa 104,164,176\n"
"s_cbranch_vccz .Lst4\nbuffer_store_dwordx4 a[92:95], v79, s[56:59], s68 offen offset:1024 sc1\n.Lst4:\nQmvav 108,168,24\nQmvaa 108,172,176\ns_waitcnt lgkmcnt(0)\nQmvav 140,168,28\ns_waitcnt vmcnt(16)\n"
"s_barrier\nQmvaa 140,172,180\nQdv 24,63,33024\ns_add_u32 m0, s62, 49536\ns_add_u32 s69, s87, 3670144\nQmvav 136,160,28\nbuffer_load_dwordx4 v62, s[52:55], s69 offen lds\nQmvaa 136,164,180\n"
"Qda 176,63,33536\nQmvav 132,152,28\nQta 112,61,0\nQmvaa 132,156,180\ns_add_u32 s69, s87, 3734648\nQmvav 128,144,28\nbuffer_load_dwordx4 v62, s[52:55], s69 offen offset:1032 lds\nQmvaa 128,148,180\n"
"Qdv 28,63,34048\nQmvav 160,144,32\nQda 180,63,34560\nQmvaa 160,148,184\nQta 114,61,512\ns_add_u32 s69, s87, 3799152\nQmvav 164,152,32\nbuffer_load_dwordx4 v62, s[52:55], s69 offen offset:2064 lds\n"
"Qmvaa 164,156,184\nQta 116,61,4128\nQmvav 168,160,32\nQmvaa 168,164,184\nQta 118,61,4640\ns_add_u32 s69, s87, 3863656\nQmvav 172,168,32\nbuffer_load_dwordx4 v62, s[52:55], s69 offen offset:3096 lds\n"
"Qmvaa 172,172,184\nQdv 32,63,35072\nQmvav 204,168,36\nQda 184,63,35584\nQmvaa 204,172,188\nQta 120,61,8\ns_add_u32 m0, s64, 0\nQmvav 200,160,36\n"
"s_cbranch_vccnz .Ld0q5\nbuffer_load_dwordx4 v72, s[44:47], s60 offen offset:1024 nt lds\n.Ld0q5:\nQmvaa 200,164,188\nQta 122,61,520\nQmvav 196,152,36\nQmvaa 196,156,188\nQta 124,61,4136\n"
"Qmvav 192,144,36\nQdv 36,63,36096\nQmvaa 192,148,188\nQda 188,63,36608\nQmvav 224,144,40\nQta 126,61,4648\nQmvaa 224,148,192\nQmvav 228,152,40\n"
"Qta 128,61,256\nQmvaa 228,156,192\nQmvav 232,160,40\nQta 130,61,768\nQmvaa 232,164,192\nQmvav 236,168,40\nQdv 40,63,37120\nQmvaa 236,172,192\n"
"Qda 192,63,37632\nQmaav 220,168,44\nQta 132,61,4384\nQmaaa 220,172,196\nQmaav 216,160,44\nQta 134,61,4896\nQmaaa 216,164,196\nQmaav 212,152,44\n"
"Qta 136,61,264\nQmaaa 212,156,196\nQmaav 208,144,44\nQdv 44,63,38144\nQmaaa 208,148,196\nQda 196,63,38656\nQmaav 240,144,48\nQta 138,61,776\n"
"Qmaaa 240,148,200\nQmaav 244,152,48\nQta 140,61,4392\nQmaaa 244,156,200\nQmaav 248,160,48\nQta 142,61,4904\nQmaaa 248,164,200\nQmaav 252,168,48\n"
"Qdv 48,63,39168\nQmaaa 252,172,200\nQda 200,63,39680\ns_waitcnt lgkmcnt(2)\nQmvav 80,112,24\nQmvaa 80,116,176\ns_add_u32 s68, s71, 4096\nQmvav 84,120,24\n"
"s_cbranch_vccz .Lst3\nbuffer_store_dwordx4 a[96:99], v79, s[56:59], s68 offen offset:2048 sc1\n.Lst3:\nQmvaa 84,124,176\nQmvav 88,128,24\ns_add_u32 s68, s71, 20480\nQmvaa 88,132,176\ns_cbranch_vccz .Lst2\n"
"buffer_store_dwordx4 a[100:103], v79, s[56:59], s68 offen offset:2048 sc1\n.Lst2:\nQmvav 92,136,24\nQmvaa 92,140,176\ns_waitcnt lgkmcnt(0)\nQmvav 124,136,28\ns_waitcnt vmcnt(17)\ns_barrier\n"
"Qmvaa 124,140,180\nQta 144,61,16512\ns_add_u32 m0, s62, 0\ns_add_u32 s69, s87, 4194304\ns_mov_b32 s89, 0\nQmvav 120,128,28\nbuffer_load_dwordx4 v62, s[52:55], s69 offen lds\ns_cbranch_vccnz .Lpb3\n"
"s_load_dword s89, s[30:31], 0x4000\n.Lpb3:\nQmvaa 120,132,180\nQta 146,61,17024\nQmvav 116,120,28\nQmvaa 116,124,180\nQta 148,61,20640\ns_add_u32 s69, s87, 4258808\n"
"Qmvav 112,112,28\nbuffer_load_dwordx4 v62, s[52:55], s69 offen offset:1032 lds\ns_add_u32 s77, s77, s89\nQmvaa 112,116,180\nQta 150,61,21152\nQmvav 144,112,32\ns_add_u32 s77, s77, s89\nQmvaa 144,116,184\n"
"Qta 152,61,16520\ns_add_u32 s69, s87, 4323312\nQmvav 148,120,32\nbuffer_load_dwordx4 v62, s[52:55], s69 offen offset:2064 lds\ns_add_u32 s77, s77, s89\nQmvaa 148,124,184\nQta 154,61,17032\nQmvav 152,128,32\n"
"s_add_u32 s77, s77, s89\nQmvaa 152,132,184\nQta 156,61,20648\ns_add_u32 s69, s87, 4387816\nQmvav 156,136,32\nbuffer_load_dwordx4 v62, s[52:55], s69 offen offset:3096 lds\ns_add_u32 s77, s77, s89\nQmvaa 156,140,184\n"
"Qta 158,61,21160\nQmvav 188,136,36\ns_add_u32 s77, s77, s89\nQmvaa 188,140,188\nQta 160,61,16768\ns_add_u32 m0, s63, 33024\ns_add_u32 s88, s86, 1024\nQmvav 184,128,36\n"
"buffer_load_dwordx4 v64, s[48:51], s88 offen lds\ns_add_u32 s77, s77, s89\nQmvaa 184,132,188\nQta 162,61,17280\nQmvav 180,120,36\ns_add_u32 s77, s77, s89\nQmvaa 180,124,188\nQta 164,61,20896\n"
"s_add_u32 s88, s86, 20480\nQmvav 176,112,36\nbuffer_load_dwordx4 v64, s[48:51], s88 offen offset:1024 lds\ns_add_u32 s77, s77, s89\nQmvaa 176,116,188\nQta 166,61,21408\nQmvav 208,112,40\ns_add_u32 s77, s77, s89\n"
"Qmvaa 208,116,192\nQta 168,61,16776\ns_add_u32 s88, s86, 39936\nQmvav 212,120,40\nbuffer_load_dwordx4 v64, s[48:51], s88 offen offset:2048 lds\ns_add_u32 s77, s77, s89\nQmvaa 212,124,192\nQta 170,61,17288\n"
"Qmvav 216,128,40\ns_add_u32 s77, s77, s89\nQmvaa 216,132,192\nQta 172,61,20904\ns_add_u32 s88, s86, 59392\nQmvav 220,136,40\nbuffer_load_dwordx4 v64, s[48:51], s88 offen offset:3072 lds\ns_add_u32 s77, s77, s89\n"
"Qmvaa 220,140,192\nQta 174,61,21416\nQmvav 252,136,44\ns_add_u32 s77, s77, s89\nQmvaa 252,140,196\ns_add_u32 m0, s63, 37120\ns_add_u32 s88, s86, 82944\nQmvav 248,128,44\n"
"buffer_load_dwordx4 v64, s[48:51], s88 offen lds\ns_add_u32 s77, s77, s89\nQmvaa 248,132,196\nQmvav 244,120,44\ns_add_u32 s77, s77, s89\nQmvaa 244,124,196\ns_add_u32 s88, s86, 102400\nQmvav 240,112,44\n"
"buffer_load_dwordx4 v64, s[48:51], s88 offen offset:1024 lds\ns_add_u32 s77, s77, s89\nQmvaa 240,116,196\nQmaav 224,112,48\ns_add_u32 s77, s77, s89\nQmaaa 224,116,200\ns_add_u32 s88, s86, 121856\nQmaav 228,120,48\n"
"buffer_load_dwordx4 v64, s[48:51], s88 offen offset:2048 lds\ns_add_u32 s77, s77, s89\nQmaaa 228,124,200\nQmaav 232,128,48\ns_add_u32 s77, s77, s89\nQmaaa 232,132,200\nQmaav 236,136,48\ns_add_u32 s77, s77, s89\n"
"Qmaaa 236,140,200\ns_waitcnt lgkmcnt(0)\nQmvav 96,144,24\nQmvaa 96,148,176\ns_add_u32 s68, s71, 135168\nQmvav 100,152,24\ns_cbranch_vccz .Lst1\nbuffer_store_dwordx4 a[104:107], v79, s[56:59], s68 offen offset:2048 sc1\n"
".Lst1:\nQmvaa 100,156,176\nQmvav 104,160,24\ns_add_u32 s68, s71, 151552\nQmvaa 104,164,176\ns_cbranch_vccz .Lst0\nbuffer_store_dwordx4 a[108:111], v79, s[56:59], s68 offen offset:2048 sc1\n.Lst0:\n"
"Qmvav 108,168,24\nQmvaa 108,172,176\ns_waitcnt lgkmcnt(0)\nQmvav 140,168,28\ns_waitcnt vmcnt(16)\ns_barrier\nQmvaa 140,172,180\nQdv 24,63,0\n"
"s_add_u32 m0, s62, 16512\ns_add_u32 s69, s87, 4194432\nQmvav 136,160,28\nbuffer_load_dwordx4 v62, s[52:55], s69 offen lds\nQmvaa 136,164,180\nQda 176,63,512\nQmvav 132,152,28\nQta 112,61,33024\n"
"Qmvaa 132,156,180\ns_add_u32 s69, s87, 4258936\nQmvav 128,144,28\nbuffer_load_dwordx4 v62, s[52:55], s69 offen offset:1032 lds\nQmvaa 128,148,180\nQdv 28,63,1024\nQmvav 160,144,32\nQda 180,63,1536\n"
"Qmvaa 160,148,184\nQta 114,61,33536\ns_add_u32 s69, s87, 4323440\nQmvav 164,152,32\nbuffer_load_dwordx4 v62, s[52:55], s69 offen offset:2064 lds\nQmvaa 164,156,184\nQta 116,61,37152\nQmvav 168,160,32\n"
"Qmvaa 168,164,184\nQta 118,61,37664\ns_add_u32 s69, s87, 4387944\nQmvav 172,168,32\nbuffer_load_dwordx4 v62, s[52:55], s69 offen offset:3096 lds\nQmvaa 172,172,184\nQdv 32,63,2048\nQmvav 204,168,36\n"
"Qda 184,63,2560\nQmvaa 204,172,188\nQta 120,61,33032\ns_add_u32 m0, s64, 0\nQmvav 200,160,36\ns_cbranch_vccnz .Ld0q6\nbuffer_load_dwordx4 v72, s[44:47], s90 offen offset:2048 nt lds\n.Ld0q6:\n"
"Qmvaa 200,164,188\nQta 122,61,33544\nQmvav 196,152,36\nQmvaa 196,156,188\nQta 124,61,37160\nQmvav 192,144,36\nQdv 36,63,3072\nQmvaa 192,148,188\n"
"Qda 188,63,3584\nQmvav 224,144,40\nQta 126,61,37672\nQmvaa 224,148,192\nQmvav 228,152,40\nQta 128,61,33280\nQmvaa 228,156,192\nQmvav 232,160,40\n"
"Qta 130,61,33792\nQmvaa 232,164,192\nQmvav 236,168,40\nQdv 40,63,4096\nQmvaa 236,172,192\nQda 192,63,4608\nQmaav 220,168,44\nQta 132,61,37408\n"
"Qmaaa 220,172,196\nQmaav 216,160,44\nQta 134,61,37920\nQmaaa 216,164,196\nQmaav 212,152,44\nQta 136,61,33288\nQmaaa 212,156,196\nQmaav 208,144,44\n"
"Qdv 44,63,5120\nQmaaa 208,148,196\nQda 196,63,5632\nQmaav 240,144,48\nQta 138,61,33800\nQmaaa 240,148,200\nQmaav 244,152,48\nQta 140,61,37416\n"
"Qmaaa 244,156,200\nQmaav 248,160,48\nQta 142,61,37928\nQmaaa 248,164,200\nQmaav 252,168,48\nQdv 48,63,6144\nQmaaa 252,172,200\nQda 200,63,6656\n"
"s_waitcnt lgkmcnt(2)\nQmvav 80,112,24\nQmvaa 80,116,176\nQmvav 84,120,24\nQmvaa 84,124,176\nQmvav 88,128,24\nQmvaa 88,132,176\nQmvav 92,136,24\n"
"Qmvaa 92,140,176\ns_waitcnt lgkmcnt(0)\nQmvav 124,136,28\ns_waitcnt vmcnt(17)\ns_barrier\nQmvaa 124,140,180\nQta 144,61,49536\ns_add_u32 m0, s62, 33024\n"
"s_add_u32 s69, s87, 4718592\nQmvav 120,128,28\nbuffer_load_dwordx4 v62, s[52:55], s69 offen lds\nQmvaa 120,132,180\nQta 146,61,50048\nQmvav 116,120,28\nQmvaa 116,124,180\nQta 148,61,53664\n"
"s_add_u32 s69, s87, 4783096\nQmvav 112,112,28\nbuffer_load_dwordx4 v62, s[52:55], s69 offen offset:1032 lds\nQmvaa 112,116,180\nQta 150,61,54176\nQmvav 144,112,32\nQmvaa 144,116,184\nQta 152,61,49544\n"
"s_add_u32 s69, s87, 4847600\nQmvav 148,120,32\nbuffer_load_dwordx4 v62, s[52:55], s69 offen offset:2064 lds\nQmvaa 148,124,184\nQta 154,61,50056\nQmvav 152,128,32\nQmvaa 152,132,184\nQta 156,61,53672\n"
"s_add_u32 s69, s87, 4912104\nQmvav 156,136,32\nbuffer_load_dwordx4 v62, s[52:55], s69 offen offset:3096 lds\nQmvaa 156,140,184\nQta 158,61,54184\nQmvav 188,136,36\nQmvaa 188,140,188\nQta 160,61,49792\n"
"s_add_u32 m0, s63, 0\ns_add_u32 s88, s86, 1152\nQmvav 184,128,36\nbuffer_load_dwordx4 v64, s[48:51], s88 offen lds\nQmvaa 184,132,188\nQta 162,61,50304\nQmvav 180,120,36\nQmvaa 180,124,188\n"
"Qta 164,61,53920\ns_add_u32 s88, s86, 20608\nQmvav 176,112,36\nbuffer_load_dwordx4 v64, s[48:51], s88 offen offset:1024 lds\nQmvaa 176,116,188\nQta 166,61,54432\nQmvav 208,112,40\nQmvaa 208,116,192\n"
"Qta 168,61,49800\ns_add_u32 s88, s86, 40064\nQmvav 212,120,40\nbuffer_load_dwordx4 v64, s[48:51], s88 offen offset:2048 lds\nQmvaa 212,124,192\nQta 170,61,50312\nQmvav 216,128,40\nQmvaa 216,132,192\n"
"Qta 172,61,53928\ns_add_u32 s88, s86, 59520\nQmvav 220,136,40\nbuffer_load_dwordx4 v64, s[48:51], s88 offen offset:3072 lds\nQmvaa 220,140,192\nQta 174,61,54440\nQmvav 252,136,44\nQmvaa 252,140,196\n"
"s_add_u32 m0, s63, 4096\ns_add_u32 s88, s86, 83072\nQmvav 248,128,44\nbuffer_load_dwordx4 v64, s[48:51], s88 offen lds\nQmvaa 248,132,196\nv_mov_b32 v66, s77\nQmvav 244,120,44\nds_write_b32 v73, v66 offset:16\n"
"Qmvaa 244,124,196\ns_add_u32 s88, s86, 102528\nQmvav 240,112,44\nbuffer_load_dwordx4 v64, s[48:51], s88 offen offset:1024 lds\nQmvaa 240,116,196\nQmaav 224,112,48\nQmaaa 224,116,200\ns_add_u32 s88, s86, 121984\n"
"Qmaav 228,120,48\nbuffer_load_dwordx4 v64, s[48:51], s88 offen offset:2048 lds\nQmaaa 228,124,200\nQmaav 232,128,48\nQmaaa 232,132,200\nQmaav 236,136,48\nQmaaa 236,140,200\ns_waitcnt lgkmcnt(1)\n"
"Qmvav 96,144,24\nQmvaa 96,148,176\nQmvav 100,152,24\nQmvaa 100,156,176\nQmvav 104,160,24\nQmvaa 104,164,176\nQmvav 108,168,24\nQmvaa 108,172,176\n"
"s_waitcnt lgkmcnt(0)\nQmvav 140,168,28\ns_waitcnt vmcnt(16)\ns_barrier\nQmvaa 140,172,180\nQdv 24,63,33024\ns_add_u32 m0, s62, 49536\ns_add_u32 s69, s87, 4718720\n"
"Qmvav 136,160,28\nbuffer_load_dwordx4 v62, s[52:55], s69 offen lds\nQmvaa 136,164,180\nQda 176,63,33536\nQmvav 132,152,28\nQta 112,61,0\nQmvaa 132,156,180\ns_add_u32 s69, s87, 4783224\n"
"Qmvav 128,144,28\nbuffer_load_dwordx4 v62, s[52:55], s69 offen offset:1032 lds\nQmvaa 128,148,180\nQdv 28,63,34048\nQmvav 160,144,32\nQda 180,63,34560\nQmvaa 160,148,184\nQta 114,61,512\n"
"s_add_u32 s69, s87, 4847728\nQmvav 164,152,32\nbuffer_load_dwordx4 v62, s[52:55], s69 offen offset:2064 lds\nQmvaa 164,156,184\nQta 116,61,4128\nQmvav 168,160,32\nQmvaa 168,164,184\nQta 118,61,4640\n"
"s_add_u32 s69, s87, 4912232\nQmvav 172,168,32\nbuffer_load_dwordx4 v62, s[52:55], s69 offen offset:3096 lds\nQmvaa 172,172,184\nQdv 32,63,35072\nQmvav 204,168,36\nQda 184,63,35584\nQmvaa 204,172,188\n"
"Qta 120,61,8\ns_add_u32 m0, s64, 0\nQmvav 200,160,36\ns_cbranch_vccnz .Ld0q7\nbuffer_load_dwordx4 v72, s[44:47], s90 offen offset:3072 nt lds\ns_add_u32 s65, s65, 2048\ns_xor_b32 s60, s65, %21\ns_add_u32 s90, s60, 260096\n"
".Ld0q7:\nQmvaa 200,164,188\nQta 122,61,520\nQmvav 196,152,36\nQmvaa 196,156,188\nQta 124,61,4136\nQmvav 192,144,36\nQdv 36,63,36096\n"
"Qmvaa 192,148,188\nQda 188,63,36608\nQmvav 224,144,40\nQta 126,61,4648\nQmvaa 224,148,192\nQmvav 228,152,40\nQta 128,61,256\nQmvaa 228,156,192\n"
"Qmvav 232,160,40\nQta 130,61,768\nQmvaa 232,164,192\nQmvav 236,168,40\nQdv 40,63,37120\nQmvaa 236,172,192\nQda 192,63,37632\nQmaav 220,168,44\n"
"Qta 132,61,4384\nQmaaa 220,172,196\nQmaav 216,160,44\n.Lpg3:\ns_cbranch_execz .Ltr0\nQta 134,61,4896\nQmaaa 216,164,196\nQmaav 212,152,44\n"
"Qta 136,61,264\nQmaaa 212,156,196\nQmaav 208,144,44\nQdv 44,63,38144\nQmaaa 208,148,196\nQda 196,63,38656\nQmaav 240,144,48\nQta 138,61,776\n"
"Qmaaa 240,148,200\nQmaav 244,152,48\nQta 140,61,4392\nQmaaa 244,156,200\nQmaav 248,160,48\nQta 142,61,4904\nQmaaa 248,164,200\nQmaav 252,168,48\n"
"Qdv 48,63,39168\nQmaaa 252,172,200\nQda 200,63,39680\ns_waitcnt lgkmcnt(2)\nQmvav 80,112,24\nQmvaa 80,116,176\nQmvav 84,120,24\nQmvaa 84,124,176\n"
"Qmvav 88,128,24\nQmvaa 88,132,176\nQmvav 92,136,24\nQmvaa 92,140,176\ns_waitcnt lgkmcnt(0)\nQmvav 124,136,28\ns_waitcnt vmcnt(15)\ns_barrier\n"
"Qmvaa 124,140,180\nQta 144,61,16512\ns_mov_b32 s56, s94\ns_mov_b32 s57, s95\ns_mov_b32 s58, s75\ns_mov_b32 s94, 0\ns_add_u32 m0, %10, 160768\nQmvav 120,128,28\n"
"buffer_load_dwordx4 v60, s[56:59], s94 offen lds\nQmvaa 120,132,180\nQta 146,61,17024\nQmvav 116,120,28\nQmvaa 116,124,180\nQta 148,61,20640\ns_add_u32 s94, s94, 4096\nQmvav 112,112,28\n"
"buffer_load_dwordx4 v60, s[56:59], s94 offen lds\nQmvaa 112,116,180\nQta 150,61,21152\nQmvav 144,112,32\nQmvaa 144,116,184\nQta 152,61,16520\ns_add_u32 s94, s94, 4096\nQmvav 148,120,32\n"
"buffer_load_dwordx4 v60, s[56:59], s94 offen lds\nQmvaa 148,124,184\nQta 154,61,17032\nQmvav 152,128,32\nQmvaa 152,132,184\nQta 156,61,20648\ns_add_u32 s94, s94, 4096\nQmvav 156,136,32\n"
"buffer_load_dwordx4 v60, s[56:59], s94 offen lds\nQmvaa 156,140,184\nQta 158,61,21160\nQmvav 188,136,36\nQmvaa 188,140,188\nQta 160,61,16768\ns_add_u32 s94, s94, 4096\nQmvav 184,128,36\n"
"buffer_load_dwordx4 v60, s[56:59], s94 offen lds\nQmvaa 184,132,188\nQta 162,61,17280\nQmvav 180,120,36\nQmvaa 180,124,188\nQta 164,61,20896\ns_add_u32 s94, s94, 4096\nQmvav 176,112,36\n"
"buffer_load_dwordx4 v60, s[56:59], s94 offen lds\nQmvaa 176,116,188\nQta 166,61,21408\nQmvav 208,112,40\nQmvaa 208,116,192\nQta 168,61,16776\ns_add_u32 s94, s94, 4096\nQmvav 212,120,40\n"
"buffer_load_dwordx4 v60, s[56:59], s94 offen lds\nQmvaa 212,124,192\nQta 170,61,17288\nQmvav 216,128,40\nQmvaa 216,132,192\nQta 172,61,20904\ns_add_u32 s94, s94, 4096\nQmvav 220,136,40\n"
"buffer_load_dwordx4 v60, s[56:59], s94 offen lds\nQmvaa 220,140,192\nQta 174,61,21416\nQmvav 252,136,44\nQmvaa 252,140,196\ns_add_u32 s94, s94, 4096\nQmvav 248,128,44\nbuffer_load_dwordx4 v60, s[56:59], s94 offen lds\n"
"Qmvaa 248,132,196\nQmvav 244,120,44\nQmvaa 244,124,196\ns_add_u32 s94, s94, 4096\nQmvav 240,112,44\nbuffer_load_dwordx4 v60, s[56:59], s94 offen lds\nQmvaa 240,116,196\nQmaav 224,112,48\n"
"Qmaaa 224,116,200\nQmaav 228,120,48\nQmaaa 228,124,200\nQmaav 232,128,48\nQmaaa 232,132,200\nQmaav 236,136,48\nQmaaa 236,140,200\ns_waitcnt lgkmcnt(0)\n"
"Qmvav 96,144,24\nQmvaa 96,148,176\nQmvav 100,152,24\nQmvaa 100,156,176\nQmvav 104,160,24\nQmvaa 104,164,176\nQmvav 108,168,24\nQmvaa 108,172,176\n"
"s_waitcnt lgkmcnt(0)\nQmvav 140,168,28\ns_waitcnt vmcnt(14)\ns_barrier\nQmvaa 140,172,180\nQdv 24,63,0\ns_add_u32 m0, s61, 33024\nQmvav 136,160,28\n"
"buffer_load_dwordx4 v69, s[40:43], s60 offen nt lds\nQmvaa 136,164,180\nQda 176,63,512\nQmvav 132,152,28\nQta 112,61,33024\nQmvaa 132,156,180\nQmvav 128,144,28\nbuffer_load_dwordx4 v69, s[40:43], s60 offen offset:1024 nt lds\n"
"Qmvaa 128,148,180\nQdv 28,63,1024\nQmvav 160,144,32\nQda 180,63,1536\nQmvaa 160,148,184\nQta 114,61,33536\nQmvav 164,152,32\nbuffer_load_dwordx4 v69, s[40:43], s90 offen offset:2048 nt lds\n"
"Qmvaa 164,156,184\nQta 116,61,37152\nQmvav 168,160,32\nQmvaa 168,164,184\nQta 118,61,37664\nQmvav 172,168,32\nbuffer_load_dwordx4 v69, s[40:43], s90 offen offset:3072 nt lds\nQmvaa 172,172,184\n"
"Qdv 32,63,2048\nQmvav 204,168,36\nQda 184,63,2560\nQmvaa 204,172,188\nQta 120,61,33032\ns_add_u32 m0, s64, 33024\nQmvav 200,160,36\nbuffer_load_dwordx4 v72, s[44:47], s60 offen nt lds\n"
"Qmvaa 200,164,188\nQta 122,61,33544\nQmvav 196,152,36\nQmvaa 196,156,188\nQta 124,61,37160\nQmvav 192,144,36\nbuffer_load_dwordx4 v72, s[44:47], s60 offen offset:1024 nt lds\nQmvaa 192,148,188\n"
"Qdv 36,63,3072\nQmvav 224,144,40\nQda 188,63,3584\nQmvaa 224,148,192\nQta 126,61,37672\nQmvav 228,152,40\nbuffer_load_dwordx4 v72, s[44:47], s90 offen offset:2048 nt lds\nQmvaa 228,156,192\n"
"Qta 128,61,33280\nQmvav 232,160,40\nQmvaa 232,164,192\nQta 130,61,33792\nQmvav 236,168,40\nbuffer_load_dwordx4 v72, s[44:47], s90 offen offset:3072 nt lds\ns_add_u32 s65, s65, 2048\ns_xor_b32 s60, s65, %21\n"
"s_add_u32 s90, s60, 260096\nQmvaa 236,172,192\nQdv 40,63,4096\nQmaav 220,168,44\nQda 192,63,4608\nQmaaa 220,172,196\nQta 132,61,37408\nQmaav 216,160,44\n"
"Qmaaa 216,164,196\nQta 134,61,37920\nQmaav 212,152,44\nQmaaa 212,156,196\nQta 136,61,33288\nQmaav 208,144,44\nQdv 44,63,5120\nQmaaa 208,148,196\n"
"Qda 196,63,5632\nQmaav 240,144,48\nQta 138,61,33800\nQmaaa 240,148,200\nQmaav 244,152,48\nQta 140,61,37416\nQmaaa 244,156,200\nQmaav 248,160,48\n"
"Qta 142,61,37928\nQmaaa 248,164,200\nQmaav 252,168,48\nQdv 48,63,6144\nQmaaa 252,172,200\nQda 200,63,6656\ns_waitcnt lgkmcnt(2)\nQmvav 80,112,24\n"
"Qmvaa 80,116,176\nQmvav 84,120,24\nQmvaa 84,124,176\nQmvav 88,128,24\nQmvaa 88,132,176\nQmvav 92,136,24\nQmvaa 92,140,176\ns_waitcnt lgkmcnt(0)\n"
"Qmvav 124,136,28\ns_waitcnt vmcnt(18)\ns_barrier\nQmvaa 124,140,180\nQta 144,61,49536\nQmvav 120,128,28\nQmvaa 120,132,180\nQta 146,61,50048\n"
"Qmvav 116,120,28\nQmvaa 116,124,180\nQta 148,61,53664\nQmvav 112,112,28\nQmvaa 112,116,180\nQta 150,61,54176\nQmvav 144,112,32\nQmvaa 144,116,184\n"
"Qta 152,61,49544\nQmvav 148,120,32\nQmvaa 148,124,184\nQta 154,61,50056\nQmvav 152,128,32\nQmvaa 152,132,184\nQta 156,61,53672\nQmvav 156,136,32\n"
"Qmvaa 156,140,184\nQta 158,61,54184\nQmvav 188,136,36\nQmvaa 188,140,188\nQta 160,61,49792\nQmvav 184,128,36\nQmvaa 184,132,188\nQta 162,61,50304\n"
"Qmvav 180,120,36\nQmvaa 180,124,188\nQta 164,61,53920\nQmvav 176,112,36\nQmvaa 176,116,188\nQta 166,61,54432\nQmvav 208,112,40\nQmvaa 208,116,192\n"
"Qta 168,61,49800\nQmvav 212,120,40\nQmvaa 212,124,192\nQta 170,61,50312\nQmvav 216,128,40\nQmvaa 216,132,192\nQta 172,61,53928\nQmvav 220,136,40\n"
"Qmvaa 220,140,192\nQta 174,61,54440\nQmvav 252,136,44\nQmvaa 252,140,196\nQmvav 248,128,44\nQmvaa 248,132,196\nQmvav 244,120,44\nQmvaa 244,124,196\n"
"Qmvav 240,112,44\nQmvaa 240,116,196\nQmaav 224,112,48\nQmaaa 224,116,200\nQmaav 228,120,48\nQmaaa 228,124,200\nQmaav 232,128,48\nQmaaa 232,132,200\n"
"Qmaav 236,136,48\nQmaaa 236,140,200\ns_waitcnt lgkmcnt(0)\nQmvav 96,144,24\nQmvaa 96,148,176\nQmvav 100,152,24\nQmvaa 100,156,176\nQmvav 104,160,24\n"
"Qmvaa 104,164,176\nQmvav 108,168,24\nQmvaa 108,172,176\ns_waitcnt lgkmcnt(0)\nQmvav 140,168,28\ns_waitcnt vmcnt(18)\ns_barrier\nQmvaa 140,172,180\n"
"Qda 112,68,0\ns_add_u32 m0, s61, 66048\nv_accvgpr_write_b32 a176, 0\nQmvav 136,160,28\nbuffer_load_dwordx4 v69, s[40:43], s60 offen nt lds\nQmvaa 136,164,180\nQda 116,68,512\nv_accvgpr_write_b32 a177, 0\n"
"Qmvav 132,152,28\nQmvaa 132,156,180\nQda 120,68,256\nv_accvgpr_write_b32 a178, 0\nQmvav 128,144,28\nbuffer_load_dwordx4 v69, s[40:43], s60 offen offset:1024 nt lds\nQmvaa 128,148,180\nQda 124,68,768\n"
"v_accvgpr_write_b32 a179, 0\nQmvav 160,144,32\nQmvaa 160,148,184\nQda 128,68,1024\nv_accvgpr_write_b32 a180, 0\nQmvav 164,152,32\nbuffer_load_dwordx4 v69, s[40:43], s90 offen offset:2048 nt lds\nQmvaa 164,156,184\n"
"Qda 132,68,1536\nv_accvgpr_write_b32 a181, 0\nQmvav 168,160,32\nQmvaa 168,164,184\nQda 136,68,1280\nv_accvgpr_write_b32 a182, 0\nQmvav 172,168,32\nbuffer_load_dwordx4 v69, s[40:43], s90 offen offset:3072 nt lds\n"
"Qmvaa 172,172,184\nQda 140,68,1792\nv_accvgpr_write_b32 a183, 0\nQmvav 204,168,36\nQmvaa 204,172,188\ns_add_u32 m0, s64, 66048\nv_accvgpr_write_b32 a184, 0\nQmvav 200,160,36\n"
"buffer_load_dwordx4 v72, s[44:47], s60 offen nt lds\nQmvaa 200,164,188\nv_accvgpr_write_b32 a185, 0\nQmvav 196,152,36\nQmvaa 196,156,188\nv_accvgpr_write_b32 a186, 0\nQmvav 192,144,36\nbuffer_load_dwordx4 v72, s[44:47], s60 offen offset:1024 nt lds\n"
"Qmvaa 192,148,188\nv_accvgpr_write_b32 a187, 0\nQmvav 224,144,40\nQmvaa 224,148,192\nv_accvgpr_write_b32 a188, 0\nQmvav 228,152,40\nbuffer_load_dwordx4 v72, s[44:47], s90 offen offset:2048 nt lds\nQmvaa 228,156,192\n"
"v_accvgpr_write_b32 a189, 0\nQmvav 232,160,40\nQmvaa 232,164,192\nv_accvgpr_write_b32 a190, 0\nQmvav 236,168,40\nbuffer_load_dwordx4 v72, s[44:47], s90 offen offset:3072 nt lds\ns_add_u32 s65, s65, 2048\ns_xor_b32 s60, s65, %21\n"
"s_add_u32 s90, s60, 260096\nQmvaa 236,172,192\nv_accvgpr_write_b32 a191, 0\nQmaav 220,168,44\nQmaaa 220,172,196\nv_accvgpr_write_b32 a192, 0\nQmaav 216,160,44\nQmaaa 216,164,196\n"
"v_accvgpr_write_b32 a193, 0\nQmaav 212,152,44\nQmaaa 212,156,196\nv_accvgpr_write_b32 a194, 0\nQmaav 208,144,44\nQmaaa 208,148,196\nv_accvgpr_write_b32 a195, 0\nQmaav 240,144,48\n"
"Qmaaa 240,148,200\nv_accvgpr_write_b32 a196, 0\nQmaav 244,152,48\nQmaaa 244,156,200\nv_accvgpr_write_b32 a197, 0\nQmaav 248,160,48\nQmaaa 248,164,200\nv_accvgpr_write_b32 a198, 0\n"
"Qmaav 252,168,48\nQmaaa 252,172,200\nv_accvgpr_write_b32 a199, 0\nQta 148,70,128\nQta 150,70,1152\nQta 156,70,384\nQta 158,70,1408\nQta 164,70,640\n"
"Qta 166,70,1664\nQta 172,70,896\nQta 174,70,1920\nv_pk_fma_f32 v[80:81], v[80:81], s[72:73], v[52:53] op_sel_hi:[1,0,0]\nv_pk_fma_f32 v[82:83], v[82:83], s[72:73], v[52:53] op_sel_hi:[1,0,0]\nv_pk_fma_f32 v[84:85], v[84:85], s[72:73], v[52:53] op_sel_hi:[1,0,0]\nv_pk_fma_f32 v[86:87], v[86:87], s[72:73], v[52:53] op_sel_hi:[1,0,0]\nQk 80,80,81\n"
"Qk 81,82,83\nQk 82,84,85\nQk 83,86,87\nv_accvgpr_write_b32 a0, v80\nv_accvgpr_write_b32 a1, v81\nv_accvgpr_write_b32 a2, v82\nv_accvgpr_write_b32 a3, v83\nv_pk_fma_f32 v[88:89], v[88:89], s[72:73], v[52:53] op_sel_hi:[1,0,0]\n"
"v_pk_fma_f32 v[90:91], v[90:91], s[72:73], v[52:53] op_sel_hi:[1,0,0]\nv_pk_fma_f32 v[92:93], v[92:93], s[72:73], v[52:53] op_sel_hi:[1,0,0]\nv_pk_fma_f32 v[94:95], v[94:95], s[72:73], v[52:53] op_sel_hi:[1,0,0]\nQk 88,88,89\nQk 89,90,91\nQk 90,92,93\nQk 91,94,95\nv_accvgpr_write_b32 a4, v88\n"
"v_accvgpr_write_b32 a5, v89\nv_accvgpr_write_b32 a6, v90\nv_accvgpr_write_b32 a7, v91\nv_pk_fma_f32 v[96:97], v[96:97], s[72:73], v[52:53] op_sel_hi:[1,0,0]\nv_pk_fma_f32 v[98:99], v[98:99], s[72:73], v[52:53] op_sel_hi:[1,0,0]\nv_pk_fma_f32 v[100:101], v[100:101], s[72:73], v[52:53] op_sel_hi:[1,0,0]\nv_pk_fma_f32 v[102:103], v[102:103], s[72:73], v[52:53] op_sel_hi:[1,0,0]\nQk 96,96,97\n"
"Qk 97,98,99\nQk 98,100,101\nQk 99,102,103\nv_accvgpr_write_b32 a8, v96\nv_accvgpr_write_b32 a9, v97\nv_accvgpr_write_b32 a10, v98\ns_waitcnt lgkmcnt(6)\nv_accvgpr_write_b32 a11, v99\n"
"v_pk_fma_f32 v[104:105], v[104:105], s[72:73], v[52:53] op_sel_hi:[1,0,0]\nv_accvgpr_read_b32 v66, a148\nv_pk_fma_f32 v[106:107], v[106:107], s[72:73], v[52:53] op_sel_hi:[1,0,0]\nv_pk_fma_f32 v[108:109], v[108:109], s[72:73], v[52:53] op_sel_hi:[1,0,0]\nv_pk_fma_f32 v[110:111], v[110:111], s[72:73], v[52:53] op_sel_hi:[1,0,0]\nv_accvgpr_read_b32 v67, a150\nQk 104,104,105\nQk 105,106,107\n"
"Qk 106,108,109\nv_max_f32 v66, |v66|, |v67|\nQk 107,110,111\nv_accvgpr_write_b32 a12, v104\nv_accvgpr_write_b32 a13, v105\nv_mov_b32 v67, v66\nv_accvgpr_write_b32 a14, v106\nv_accvgpr_write_b32 a15, v107\n"
"v_pk_fma_f32 v[112:113], v[112:113], s[72:73], v[52:53] op_sel_hi:[1,0,0]\nv_pk_fma_f32 v[114:115], v[114:115], s[72:73], v[52:53] op_sel_hi:[1,0,0]\nv_pk_fma_f32 v[116:117], v[116:117], s[72:73], v[52:53] op_sel_hi:[1,0,0]\nv_permlane32_swap_b32 v66, v67\nv_pk_fma_f32 v[118:119], v[118:119], s[72:73], v[52:53] op_sel_hi:[1,0,0]\nQk 112,112,113\nQk 113,114,115\nv_max_f32 v66, v66, v67\n"
"Qk 114,116,117\nQk 115,118,119\nv_accvgpr_write_b32 a16, v112\nv_mov_b32 v67, v66\nv_accvgpr_write_b32 a17, v113\nv_accvgpr_write_b32 a18, v114\nv_accvgpr_write_b32 a19, v115\nv_permlane16_swap_b32 v66, v67\n"
"v_pk_fma_f32 v[120:121], v[120:121], s[72:73], v[52:53] op_sel_hi:[1,0,0]\nv_pk_fma_f32 v[122:123], v[122:123], s[72:73], v[52:53] op_sel_hi:[1,0,0]\nv_max_f32 v66, v66, v67\nv_pk_fma_f32 v[124:125], v[124:125], s[72:73], v[52:53] op_sel_hi:[1,0,0]\nv_pk_fma_f32 v[126:127], v[126:127], s[72:73], v[52:53] op_sel_hi:[1,0,0]\nQk 120,120,121\nv_min_f32_dpp v66, v66, v66 row_ror:8 row_mask:0xf bank_mask:0xf\nQk 121,122,123\n"
"Qk 122,124,125\nQk 123,126,127\nv_min_f32_dpp v66, v66, v66 row_ror:4 row_mask:0xf bank_mask:0xf\nv_accvgpr_write_b32 a20, v120\nv_accvgpr_write_b32 a21, v121\nv_accvgpr_write_b32 a22, v122\nv_min_f32_dpp v66, v66, v66 row_ror:2 row_mask:0xf bank_mask:0xf\nv_accvgpr_write_b32 a23, v123\n"
"v_pk_fma_f32 v[128:129], v[128:129], s[72:73], v[52:53] op_sel_hi:[1,0,0]\nv_min_f32_dpp v66, v66, v66 row_ror:1 row_mask:0xf bank_mask:0xf\nv_pk_fma_f32 v[130:131], v[130:131], s[72:73], v[52:53] op_sel_hi:[1,0,0]\nv_pk_fma_f32 v[132:133], v[132:133], s[72:73], v[52:53] op_sel_hi:[1,0,0]\nv_pk_fma_f32 v[134:135], v[134:135], s[72:73], v[52:53] op_sel_hi:[1,0,0]\nv_and_b32 v66, 0x7f000000, v66\nQk 128,128,129\nQk 129,130,131\n"
"Qk 130,132,133\nv_or_b32 v66, 0x400000, v66\nQk 131,134,135\nv_accvgpr_write_b32 a24, v128\nv_accvgpr_write_b32 a25, v129\nv_mov_b32 v67, 0\nv_accvgpr_write_b32 a26, v130\nv_accvgpr_write_b32 a27, v131\n"
"v_pk_fma_f32 v[136:137], v[136:137], s[72:73], v[52:53] op_sel_hi:[1,0,0]\nv_pk_fma_f32 v[138:139], v[138:139], s[72:73], v[52:53] op_sel_hi:[1,0,0]\nv_pk_fma_f32 v[140:141], v[140:141], s[72:73], v[52:53] op_sel_hi:[1,0,0]\nv_mov_b32 v58, 0x39800000\nv_pk_fma_f32 v[142:143], v[142:143], s[72:73], v[52:53] op_sel_hi:[1,0,0]\nQk 136,136,137\nQk 137,138,139\nv_mov_b32 v59, 0\n"
"Qk 138,140,141\nQk 139,142,143\nv_accvgpr_write_b32 a28, v136\nv_readfirstlane_b32 s73, v66\nv_accvgpr_write_b32 a29, v137\n.Lpg4:\ns_cbranch_execz .Ltr9\nv_accvgpr_write_b32 a30, v138\n"
"v_accvgpr_write_b32 a31, v139\nv_pk_fma_f32 v[144:145], v[144:145], s[72:73], v[52:53] op_sel_hi:[1,0,0]\nv_pk_fma_f32 v[146:147], v[146:147], s[72:73], v[52:53] op_sel_hi:[1,0,0]\nv_pk_fma_f32 v[148:149], v[148:149], s[72:73], v[52:53] op_sel_hi:[1,0,0]\nv_pk_fma_f32 v[150:151], v[150:151], s[72:73], v[52:53] op_sel_hi:[1,0,0]\nv_mfma_f32_16x16x4_4b_bf16 v[80:95], v[66:67], v[58:59], 0\nQk 144,144,145\nQk 145,146,147\n"
"Qk 146,148,149\nQk 147,150,151\nv_accvgpr_write_b32 a32, v144\nv_accvgpr_write_b32 a33, v145\nv_accvgpr_write_b32 a34, v146\nv_accvgpr_write_b32 a35, v147\nv_pk_fma_f32 v[152:153], v[152:153], s[72:73], v[52:53] op_sel_hi:[1,0,0]\nv_pk_fma_f32 v[154:155], v[154:155], s[72:73], v[52:53] op_sel_hi:[1,0,0]\n"
"v_pk_fma_f32 v[156:157], v[156:157], s[72:73], v[52:53] op_sel_hi:[1,0,0]\nv_pk_fma_f32 v[158:159], v[158:159], s[72:73], v[52:53] op_sel_hi:[1,0,0]\nv_mfma_f32_16x16x4_4b_bf16 v[96:111], v[66:67], v[58:59], 0\nQk 152,152,153\nQk 153,154,155\nQk 154,156,157\nQk 155,158,159\nv_accvgpr_write_b32 a36, v152\n"
"v_accvgpr_write_b32 a37, v153\nv_accvgpr_write_b32 a38, v154\nv_accvgpr_write_b32 a39, v155\nv_pk_fma_f32 v[160:161], v[160:161], s[72:73], v[52:53] op_sel_hi:[1,0,0]\nv_pk_fma_f32 v[162:163], v[162:163], s[72:73], v[52:53] op_sel_hi:[1,0,0]\nv_pk_fma_f32 v[164:165], v[164:165], s[72:73], v[52:53] op_sel_hi:[1,0,0]\nv_pk_fma_f32 v[166:167], v[166:167], s[72:73], v[52:53] op_sel_hi:[1,0,0]\nv_mfma_f32_16x16x4_4b_bf16 v[112:127], v[66:67], v[58:59], 0\n"
"Qk 160,160,161\nQk 161,162,163\nQk 162,164,165\nQk 163,166,167\nv_accvgpr_write_b32 a40, v160\nv_accvgpr_write_b32 a41, v161\nv_accvgpr_write_b32 a42, v162\nv_accvgpr_write_b32 a43, v163\n"
"v_pk_fma_f32 v[168:169], v[168:169], s[72:73], v[52:53] op_sel_hi:[1,0,0]\nv_pk_fma_f32 v[170:171], v[170:171], s[72:73], v[52:53] op_sel_hi:[1,0,0]\nv_pk_fma_f32 v[172:173], v[172:173], s[72:73], v[52:53] op_sel_hi:[1,0,0]\nv_pk_fma_f32 v[174:175], v[174:175], s[72:73], v[52:53] op_sel_hi:[1,0,0]\nv_mfma_f32_16x16x4_4b_bf16 v[128:143], v[66:67], v[58:59], 0\nQk 168,168,169\nQk 169,170,171\nQk 170,172,173\n"
"Qk 171,174,175\nv_accvgpr_write_b32 a44, v168\nv_accvgpr_write_b32 a45, v169\nv_accvgpr_write_b32 a46, v170\nv_accvgpr_write_b32 a47, v171\nv_pk_fma_f32 v[176:177], v[176:177], s[72:73], v[52:53] op_sel_hi:[1,0,0]\nv_pk_fma_f32 v[178:179], v[178:179], s[72:73], v[52:53] op_sel_hi:[1,0,0]\nv_pk_fma_f32 v[180:181], v[180:181], s[72:73], v[52:53] op_sel_hi:[1,0,0]\n"
"v_pk_fma_f32 v[182:183], v[182:183], s[72:73], v[52:53] op_sel_hi:[1,0,0]\nv_mfma_f32_16x16x4_4b_bf16 v[144:159], v[66:67], v[58:59], 0\nQk 176,176,177\nQk 177,178,179\nQk 178,180,181\nQk 179,182,183\nv_accvgpr_write_b32 a48, v176\nv_accvgpr_write_b32 a49, v177\n"
"v_accvgpr_write_b32 a50, v178\nv_accvgpr_write_b32 a51, v179\nv_pk_fma_f32 v[184:185], v[184:185], s[72:73], v[52:53] op_sel_hi:[1,0,0]\nv_pk_fma_f32 v[186:187], v[186:187], s[72:73], v[52:53] op_sel_hi:[1,0,0]\nv_pk_fma_f32 v[188:189], v[188:189], s[72:73], v[52:53] op_sel_hi:[1,0,0]\nv_pk_fma_f32 v[190:191], v[190:191], s[72:73], v[52:53] op_sel_hi:[1,0,0]\nv_mfma_f32_16x16x4_4b_bf16 v[160:175], v[66:67], v[58:59], 0\nQk 184,184,185\n"
"Qk 185,186,187\nQk 186,188,189\nQk 187,190,191\nv_accvgpr_write_b32 a52, v184\nv_accvgpr_write_b32 a53, v185\nv_accvgpr_write_b32 a54, v186\nv_accvgpr_write_b32 a55, v187\nv_pk_fma_f32 v[192:193], v[192:193], s[72:73], v[52:53] op_sel_hi:[1,0,0]\n"
"v_pk_fma_f32 v[194:195], v[194:195], s[72:73], v[52:53] op_sel_hi:[1,0,0]\nv_pk_fma_f32 v[196:197], v[196:197], s[72:73], v[52:53] op_sel_hi:[1,0,0]\nv_pk_fma_f32 v[198:199], v[198:199], s[72:73], v[52:53] op_sel_hi:[1,0,0]\nv_mfma_f32_16x16x4_4b_bf16 v[176:191], v[66:67], v[58:59], 0\nQk 192,192,193\nQk 193,194,195\nQk 194,196,197\nQk 195,198,199\n"
"v_accvgpr_write_b32 a56, v192\nv_accvgpr_write_b32 a57, v193\nv_accvgpr_write_b32 a58, v194\nv_accvgpr_write_b32 a59, v195\nv_pk_fma_f32 v[200:201], v[200:201], s[72:73], v[52:53] op_sel_hi:[1,0,0]\nv_pk_fma_f32 v[202:203], v[202:203], s[72:73], v[52:53] op_sel_hi:[1,0,0]\nv_pk_fma_f32 v[204:205], v[204:205], s[72:73], v[52:53] op_sel_hi:[1,0,0]\nv_pk_fma_f32 v[206:207], v[206:207], s[72:73], v[52:53] op_sel_hi:[1,0,0]\n"
"Qk 200,200,201\nQk 201,202,203\nQk 202,204,205\nQk 203,206,207\nv_accvgpr_write_b32 a60, v200\nv_accvgpr_write_b32 a61, v201\nv_accvgpr_write_b32 a62, v202\nv_accvgpr_write_b32 a63, v203\n"
"v_pk_fma_f32 v[208:209], v[208:209], s[72:73], v[52:53] op_sel_hi:[1,0,0]\nv_pk_fma_f32 v[210:211], v[210:211], s[72:73], v[52:53] op_sel_hi:[1,0,0]\nv_pk_fma_f32 v[212:213], v[212:213], s[72:73], v[52:53] op_sel_hi:[1,0,0]\nv_pk_fma_f32 v[214:215], v[214:215], s[72:73], v[52:53] op_sel_hi:[1,0,0]\nv_mfma_f32_16x16x4_4b_bf16 v[192:207], v[66:67], v[58:59], 0\nQk 208,208,209\nQk 209,210,211\nQk 210,212,213\n"
"Qk 211,214,215\nv_accvgpr_write_b32 a64, v208\nv_accvgpr_write_b32 a65, v209\nv_accvgpr_write_b32 a66, v210\nv_accvgpr_write_b32 a67, v211\nv_pk_fma_f32 v[216:217], v[216:217], s[72:73], v[52:53] op_sel_hi:[1,0,0]\nv_pk_fma_f32 v[218:219], v[218:219], s[72:73], v[52:53] op_sel_hi:[1,0,0]\nv_pk_fma_f32 v[220:221], v[220:221], s[72:73], v[52:53] op_sel_hi:[1,0,0]\n"
"v_pk_fma_f32 v[222:223], v[222:223], s[72:73], v[52:53] op_sel_hi:[1,0,0]\nQk 216,216,217\nQk 217,218,219\nQk 218,220,221\nQk 219,222,223\nv_accvgpr_write_b32 a68, v216\nv_accvgpr_write_b32 a69, v217\nv_accvgpr_write_b32 a70, v218\n"
"v_accvgpr_write_b32 a71, v219\nv_pk_fma_f32 v[224:225], v[224:225], s[72:73], v[52:53] op_sel_hi:[1,0,0]\nv_pk_fma_f32 v[226:227], v[226:227], s[72:73], v[52:53] op_sel_hi:[1,0,0]\nv_pk_fma_f32 v[228:229], v[228:229], s[72:73], v[52:53] op_sel_hi:[1,0,0]\nv_pk_fma_f32 v[230:231], v[230:231], s[72:73], v[52:53] op_sel_hi:[1,0,0]\nv_mfma_f32_16x16x4_4b_bf16 v[208:223], v[66:67], v[58:59], 0\nQk 224,224,225\nQk 225,226,227\n"
"Qk 226,228,229\nQk 227,230,231\nv_accvgpr_write_b32 a72, v224\nv_accvgpr_write_b32 a73, v225\nv_accvgpr_write_b32 a74, v226\nv_accvgpr_write_b32 a75, v227\nv_pk_fma_f32 v[232:233], v[232:233], s[72:73], v[52:53] op_sel_hi:[1,0,0]\nv_pk_fma_f32 v[234:235], v[234:235], s[72:73], v[52:53] op_sel_hi:[1,0,0]\n"
"v_pk_fma_f32 v[236:237], v[236:237], s[72:73], v[52:53] op_sel_hi:[1,0,0]\nv_pk_fma_f32 v[238:239], v[238:239], s[72:73], v[52:53] op_sel_hi:[1,0,0]\nQk 232,232,233\nQk 233,234,235\nQk 234,236,237\nQk 235,238,239\nv_accvgpr_write_b32 a76, v232\nv_accvgpr_write_b32 a77, v233\n"
"v_accvgpr_write_b32 a78, v234\nv_accvgpr_write_b32 a79, v235\nv_pk_fma_f32 v[240:241], v[240:241], s[72:73], v[52:53] op_sel_hi:[1,0,0]\nv_pk_fma_f32 v[242:243], v[242:243], s[72:73], v[52:53] op_sel_hi:[1,0,0]\nv_pk_fma_f32 v[244:245], v[244:245], s[72:73], v[52:53] op_sel_hi:[1,0,0]\nv_pk_fma_f32 v[246:247], v[246:247], s[72:73], v[52:53] op_sel_hi:[1,0,0]\nv_mfma_f32_16x16x4_4b_bf16 v[224:239], v[66:67], v[58:59], 0\nQk 240,240,241\n"
"Qk 241,242,243\nQk 242,244,245\nQk 243,246,247\nv_accvgpr_write_b32 a80, v240\nv_accvgpr_write_b32 a81, v241\nv_accvgpr_write_b32 a82, v242\nv_accvgpr_write_b32 a83, v243\nv_pk_fma_f32 v[248:249], v[248:249], s[72:73], v[52:53] op_sel_hi:[1,0,0]\n"
"v_pk_fma_f32 v[250:251], v[250:251], s[72:73], v[52:53] op_sel_hi:[1,0,0]\nv_pk_fma_f32 v[252:253], v[252:253], s[72:73], v[52:53] op_sel_hi:[1,0,0]\nv_pk_fma_f32 v[254:255], v[254:255], s[72:73], v[52:53] op_sel_hi:[1,0,0]\nQk 248,248,249\nQk 249,250,251\nQk 250,252,253\nQk 251,254,255\nv_accvgpr_write_b32 a84, v248\n"
"v_accvgpr_write_b32 a85, v249\nv_accvgpr_write_b32 a86, v250\nv_accvgpr_write_b32 a87, v251\nv_accvgpr_read_b32 v0, a208\nv_accvgpr_read_b32 v1, a209\nv_accvgpr_read_b32 v2, a210\nv_accvgpr_read_b32 v3, a211\nv_accvgpr_read_b32 v4, a212\n"
"v_accvgpr_read_b32 v5, a213\nv_accvgpr_read_b32 v6, a214\nv_accvgpr_read_b32 v7, a215\nv_pk_fma_f32 v[0:1], v[0:1], s[72:73], v[52:53] op_sel_hi:[1,0,0]\nv_pk_fma_f32 v[2:3], v[2:3], s[72:73], v[52:53] op_sel_hi:[1,0,0]\nv_pk_fma_f32 v[4:5], v[4:5], s[72:73], v[52:53] op_sel_hi:[1,0,0]\nv_pk_fma_f32 v[6:7], v[6:7], s[72:73], v[52:53] op_sel_hi:[1,0,0]\nv_mfma_f32_16x16x4_4b_bf16 v[240:255], v[66:67], v[58:59], 0\n"
"Qk 0,0,1\nQk 1,2,3\nQk 2,4,5\nQk 3,6,7\nv_accvgpr_write_b32 a88, v0\nv_accvgpr_write_b32 a89, v1\nv_accvgpr_write_b32 a90, v2\nv_accvgpr_write_b32 a91, v3\n"
"v_accvgpr_read_b32 v8, a216\nv_accvgpr_read_b32 v9, a217\nv_accvgpr_read_b32 v10, a218\nv_accvgpr_read_b32 v11, a219\nv_accvgpr_read_b32 v12, a220\nv_accvgpr_read_b32 v13, a221\nv_accvgpr_read_b32 v14, a222\nv_accvgpr_read_b32 v15, a223\n"
"v_pk_fma_f32 v[8:9], v[8:9], s[72:73], v[52:53] op_sel_hi:[1,0,0]\nv_pk_fma_f32 v[10:11], v[10:11], s[72:73], v[52:53] op_sel_hi:[1,0,0]\nv_pk_fma_f32 v[12:13], v[12:13], s[72:73], v[52:53] op_sel_hi:[1,0,0]\nv_pk_fma_f32 v[14:15], v[14:15], s[72:73], v[52:53] op_sel_hi:[1,0,0]\nv_mfma_f32_16x16x4_4b_bf16 a[208:223], v[66:67], v[58:59], 0\nQk 8,8,9\nQk 9,10,11\nQk 10,12,13\n"
"Qk 11,14,15\nv_accvgpr_write_b32 a92, v8\nv_accvgpr_write_b32 a93, v9\nv_accvgpr_write_b32 a94, v10\nv_accvgpr_write_b32 a95, v11\nv_accvgpr_read_b32 v0, a224\nv_accvgpr_read_b32 v1, a225\nv_accvgpr_read_b32 v2, a226\n"
"v_accvgpr_read_b32 v3, a227\nv_accvgpr_read_b32 v4, a228\nv_accvgpr_read_b32 v5, a229\nv_accvgpr_read_b32 v6, a230\nv_accvgpr_read_b32 v7, a231\nv_pk_fma_f32 v[0:1], v[0:1], s[72:73], v[52:53] op_sel_hi:[1,0,0]\nv_pk_fma_f32 v[2:3], v[2:3], s[72:73], v[52:53] op_sel_hi:[1,0,0]\nv_pk_fma_f32 v[4:5], v[4:5], s[72:73], v[52:53] op_sel_hi:[1,0,0]\n"
"v_pk_fma_f32 v[6:7], v[6:7], s[72:73], v[52:53] op_sel_hi:[1,0,0]\nQk 0,0,1\nQk 1,2,3\nQk 2,4,5\nQk 3,6,7\nv_accvgpr_write_b32 a96, v0\nv_accvgpr_write_b32 a97, v1\nv_accvgpr_write_b32 a98, v2\n"
"v_accvgpr_write_b32 a99, v3\nv_accvgpr_read_b32 v8, a232\nv_accvgpr_read_b32 v9, a233\nv_accvgpr_read_b32 v10, a234\nv_accvgpr_read_b32 v11, a235\nv_accvgpr_read_b32 v12, a236\nv_accvgpr_read_b32 v13, a237\nv_accvgpr_read_b32 v14, a238\n"
"v_accvgpr_read_b32 v15, a239\nv_pk_fma_f32 v[8:9], v[8:9], s[72:73], v[52:53] op_sel_hi:[1,0,0]\nv_pk_fma_f32 v[10:11], v[10:11], s[72:73], v[52:53] op_sel_hi:[1,0,0]\nv_pk_fma_f32 v[12:13], v[12:13], s[72:73], v[52:53] op_sel_hi:[1,0,0]\nv_pk_fma_f32 v[14:15], v[14:15], s[72:73], v[52:53] op_sel_hi:[1,0,0]\nv_mfma_f32_16x16x4_4b_bf16 a[224:239], v[66:67], v[58:59], 0\nQk 8,8,9\nQk 9,10,11\n"
"Qk 10,12,13\nQk 11,14,15\nv_accvgpr_write_b32 a100, v8\nv_accvgpr_write_b32 a101, v9\nv_accvgpr_write_b32 a102, v10\nv_accvgpr_write_b32 a103, v11\nv_accvgpr_read_b32 v0, a240\nv_accvgpr_read_b32 v1, a241\n"
"v_accvgpr_read_b32 v2, a242\nv_accvgpr_read_b32 v3, a243\nv_accvgpr_read_b32 v4, a244\nv_accvgpr_read_b32 v5, a245\nv_accvgpr_read_b32 v6, a246\nv_accvgpr_read_b32 v7, a247\nv_pk_fma_f32 v[0:1], v[0:1], s[72:73], v[52:53] op_sel_hi:[1,0,0]\nv_pk_fma_f32 v[2:3], v[2:3], s[72:73], v[52:53] op_sel_hi:[1,0,0]\n"
"v_pk_fma_f32 v[4:5], v[4:5], s[72:73], v[52:53] op_sel_hi:[1,0,0]\nv_pk_fma_f32 v[6:7], v[6:7], s[72:73], v[52:53] op_sel_hi:[1,0,0]\nQk 0,0,1\nQk 1,2,3\nQk 2,4,5\nQk 3,6,7\nv_accvgpr_write_b32 a104, v0\nv_accvgpr_write_b32 a105, v1\n"
"v_accvgpr_write_b32 a106, v2\nv_accvgpr_write_b32 a107, v3\nv_accvgpr_read_b32 v8, a248\nv_accvgpr_read_b32 v9, a249\nv_accvgpr_read_b32 v10, a250\nv_accvgpr_read_b32 v11, a251\nv_accvgpr_read_b32 v12, a252\nv_accvgpr_read_b32 v13, a253\n"
"v_accvgpr_read_b32 v14, a254\nv_accvgpr_read_b32 v15, a255\nv_pk_fma_f32 v[8:9], v[8:9], s[72:73], v[52:53] op_sel_hi:[1,0,0]\nv_pk_fma_f32 v[10:11], v[10:11], s[72:73], v[52:53] op_sel_hi:[1,0,0]\nv_pk_fma_f32 v[12:13], v[12:13], s[72:73], v[52:53] op_sel_hi:[1,0,0]\nv_pk_fma_f32 v[14:15], v[14:15], s[72:73], v[52:53] op_sel_hi:[1,0,0]\nv_mfma_f32_16x16x4_4b_bf16 a[240:255], v[66:67], v[58:59], 0\nQk 8,8,9\n"
"Qk 9,10,11\nQk 10,12,13\nQk 11,14,15\nv_accvgpr_write_b32 a108, v8\nv_accvgpr_write_b32 a109, v9\nv_accvgpr_write_b32 a110, v10\nv_accvgpr_write_b32 a111, v11\nv_mov_b32 v66, 0x3f803f80\n"
"v_accvgpr_write_b32 a200, 0\nv_accvgpr_write_b32 a201, 0\nv_accvgpr_write_b32 a202, 0\nv_accvgpr_write_b32 a203, 0\ns_mov_b32 vcc_lo, 0x33333333\ns_mov_b32 vcc_hi, 0xcccccccc\nv_cndmask_b32 v66, 0, v66, vcc\nv_accvgpr_write_b32 a204, v66\n"
"v_accvgpr_write_b32 a205, v66\nv_accvgpr_write_b32 a206, v66\nv_accvgpr_write_b32 a207, v66\ns_add_u32 s87, s87, 2048\ns_waitcnt lgkmcnt(8)\nQzvaa 0,112,0\nQmvaa 0,116,4\nQmvaa 0,120,8\n"
"Qmvaa 0,124,12\nQzvaa 4,140,12\nQmvaa 4,136,8\nQmvaa 4,132,4\nQmvaa 4,128,0\ns_nop 3\nv_max3_f32 v66, v0, v1, v2\ns_nop 2\n"
"v_max3_f32 v66, v66, v3, v4\nv_max3_f32 v66, v66, v5, v6\nv_max_f32 v66, v66, v7\nQzvaa 8,112,16\nv_mov_b32 v67, v66\nQmvaa 8,116,20\ns_nop 0\nv_permlane32_swap_b32 v66, v67\n"
"v_max_f32 v66, v66, v67\nQmvaa 8,120,24\nv_mov_b32 v67, v66\nQmvaa 8,124,28\ns_nop 0\nv_permlane16_swap_b32 v66, v67\nv_max_f32 v66, v66, v67\nQzvaa 12,140,28\n"
"s_nop 0\nv_min_f32_dpp v66, v66, v66 row_ror:8 row_mask:0xf bank_mask:0xf\ns_nop 1\nv_min_f32_dpp v66, v66, v66 row_ror:4 row_mask:0xf bank_mask:0xf\nQmvaa 12,136,24\ns_nop 0\nv_min_f32_dpp v66, v66, v66 row_ror:2 row_mask:0xf bank_mask:0xf\ns_nop 1\n"
"v_min_f32_dpp v66, v66, v66 row_ror:1 row_mask:0xf bank_mask:0xf\nQmvaa 12,132,20\nv_sub_f32 v56, 0xc1a00000, v66\nv_mov_b32 v57, v56\nQmvaa 12,128,16\nv_mov_b32 v58, v56\nv_mov_b32 v59, v56\nQa 0,0,56\n"
"Qa 1,1,56\nQa 2,2,56\nQa 3,3,56\nQe 0\nQe 1\nQe 2\nQe 3\nQk 16,0,1\n"
"Qk 17,2,3\nQa 4,4,56\nQa 5,5,56\nQa 6,6,56\nQa 7,7,56\nQcvaa 0,112,32,56\nQe 4\nQmvaa 0,116,36\n"
"Qe 5\nQmvaa 0,120,40\nQe 6\nQe 7\nQmvaa 0,124,44\nQk 18,4,5\nQk 19,6,7\nQcvaa 4,140,44,56\n"
"Qa 8,8,56\nQa 9,9,56\nQmvaa 4,136,40\nQa 10,10,56\nQa 11,11,56\nQmvaa 4,132,36\nQa 12,12,56\nQa 13,13,56\n"
"Qmvaa 4,128,32\ns_waitcnt lgkmcnt(0)\nQa 14,14,56\nQmvva 96,16,148\nQa 15,15,56\nQmvva 100,16,156\nQe 8\nQmvva 104,16,164\n"
"Qe 9\nQe 10\nQe 11\nQe 12\nQe 13\nQe 14\nQe 15\nQmvva 108,16,172\n"
"Qk 20,8,9\nQk 21,10,11\nQk 22,12,13\nQk 23,14,15\ns_nop 1\nQmvva 140,20,172\nQe 0\nQmvva 136,20,164\n"
"Qe 1\nQmvva 132,20,156\nQe 2\nQmvva 128,20,148\nQe 3\nv_smfmac_f32_16x16x64_bf16 a[176:179], a[204:207], v[16:23], v77\nQk 24,0,1\nQcvaa 8,112,48,56\n"
"s_add_u32 m0, s61, 99072\nQk 25,2,3\nQmvaa 8,116,52\nbuffer_load_dwordx4 v69, s[40:43], s60 offen nt lds\nQmvaa 8,120,56\nQe 4\nQmvaa 8,124,60\ns_or_b32 s65, s65, 0x1d8000\n"
"s_add_u32 s65, s65, 2048\nQcvaa 12,140,60,56\ns_andn2_b32 s65, s65, 0x1d8000\nQmvaa 12,136,56\nQe 5\nQmvaa 12,132,52\nbuffer_load_dwordx4 v69, s[40:43], s60 offen offset:1024 nt lds\nQmvaa 12,128,48\n"
"Qe 6\nQcvaa 0,128,64,56\ns_memrealtime s[74:75]\nQk 26,4,5\nQmvaa 0,132,68\nQe 7\nQmvaa 0,136,72\nQk 27,6,7\n"
"Qmvaa 0,140,76\nbuffer_load_dwordx4 v69, s[40:43], s90 offen offset:2048 nt lds\nQcvaa 4,124,76,56\nQe 8\nQmvaa 4,120,72\nQe 9\nQmvaa 4,116,68\nQe 10\n"
"Qmvaa 4,112,64\nQe 11\nbuffer_load_dwordx4 v69, s[40:43], s90 offen offset:3072 nt lds\nQmvva 160,24,148\nQe 12\nQmvva 164,24,156\nQe 13\ns_add_u32 m0, s64, 99072\n"
"Qmvva 168,24,164\nbuffer_load_dwordx4 v72, s[44:47], s60 offen nt lds\nQe 14\nQe 15\nQmvva 172,24,172\nQk 28,8,9\nQk 29,10,11\nQk 30,12,13\n"
"Qk 31,14,15\ns_nop 1\nQmvva 204,28,172\nQe 0\nbuffer_load_dwordx4 v72, s[44:47], s60 offen offset:1024 nt lds\nQmvva 200,28,164\nQe 1\nQmvva 196,28,156\n"
"Qe 2\nQmvva 192,28,148\nbuffer_load_dwordx4 v72, s[44:47], s90 offen offset:2048 nt lds\nv_smfmac_f32_16x16x64_bf16 a[180:183], a[204:207], v[24:31], v77\nQk 34,0,1\nQcvaa 8,112,80,56\nQe 3\nQmvaa 8,116,84\n"
"Qk 35,2,3\nQmvaa 8,120,88\ns_waitcnt vmcnt(15)\ns_barrier\nQmvaa 8,124,92\nbuffer_load_dwordx4 v72, s[44:47], s90 offen offset:3072 nt lds\nQcvaa 12,140,92,56\ns_waitcnt lgkmcnt(0)\n"
"Qmvaa 12,136,88\nQmvaa 12,132,84\ns_add_u32 s77, s74, 270\nQmvaa 12,128,80\ns_xor_b32 s60, s65, %21\ns_add_u32 s90, s60, 260096\nQcvaa 0,128,96,56\nQda 128,68,34048\n"
"Qmvaa 0,132,100\nQda 132,68,34560\nQmvaa 0,136,104\nQda 136,68,34304\nQe 4\nQe 5\nQe 6\nQe 7\n"
"Qmvaa 0,140,108\nQda 140,68,34816\nQk 32,4,5\nQk 33,6,7\nQcvaa 4,124,108,56\nQda 124,68,33792\nQmvaa 4,120,104\nQda 120,68,33280\n"
"Qmvaa 4,116,100\nQda 116,68,33536\nQmvaa 4,112,96\nQda 112,68,33024\nQmvva 224,32,148\nQe 8\nQmvva 228,32,156\nQe 9\n"
"Qmvva 232,32,164\nQe 10\nQe 11\nQmvva 236,32,172\ns_waitcnt lgkmcnt(0)\nQk 36,8,9\nQk 37,10,11\nQcvaa 8,112,0,56\n"
"Qe 12\nQmvaa 8,116,4\nv_add_u32 v68, 66048, v68\nQmvaa 8,120,8\nQe 13\nQe 14\nQe 15\nQmvaa 8,124,12\n"
"Qk 38,12,13\nQk 39,14,15\nQcvaa 12,140,12,56\nQe 0\nQmvaa 12,136,8\nQe 1\nQmvaa 12,132,4\nQe 2\n"
"Qmvaa 12,128,0\nQe 3\nv_smfmac_f32_16x16x64_bf16 a[184:187], a[204:207], v[32:39], v77\nQk 42,0,1\nQmava 208,36,148\nQe 4\nQmava 212,36,156\n.Lpg5:\n"
"s_cbranch_execz .Ltr9\nQe 5\nQmava 216,36,164\nQe 6\nQe 8\nQe 7\nQmava 220,36,172\nQe 9\n"
"Qk 43,2,3\nQk 40,4,5\nQk 41,6,7\nQta 168,70,768\nQta 170,70,1792\nQmava 252,40,172\nQe 10\nQta 172,70,33792\n"
"Qta 174,70,34816\nQmava 248,40,164\nQe 11\nQta 160,70,512\nQta 162,70,1536\nQmava 244,40,156\nQk 44,8,9\nQk 45,10,11\n"
"Qta 164,70,33536\nQta 166,70,34560\nQmava 240,40,148\nQe 12\nQcvaa 0,112,16,56\nQta 152,70,256\nQta 154,70,1280\nQmvaa 0,116,20\n"
"Qe 13\nQmvaa 0,120,24\nQta 156,70,33280\nQta 158,70,34304\nQmvaa 0,124,28\nQe 14\nQcvaa 4,140,28,56\nQta 144,70,0\n"
"Qta 146,70,1024\nQmvaa 4,136,24\nQe 15\nQmvaa 4,132,20\nQta 148,70,33024\nQta 150,70,34048\nQmvaa 4,128,16\nQk 46,12,13\n"
"Qk 47,14,15\nQcvaa 8,128,32,56\nv_add_u32 v75, 1, v75\nQmvaa 8,132,36\ns_waitcnt vmcnt(8)\nds_write_b32 v73, v75\nQmvaa 8,136,40\nQe 0\n"
"Qmvaa 8,140,44\nQe 1\nQcvaa 12,124,44,56\nQe 2\nQmvaa 12,120,40\nQe 3\nQmvaa 12,116,36\nQe 4\n"
"Qmvaa 12,112,32\ns_waitcnt lgkmcnt(1)\nQk 48,0,1\nv_smfmac_f32_16x16x64_bf16 a[188:191], a[204:207], v[40:47], v77\nQk 49,2,3\nQmvva 80,16,144\nQe 5\nQmvva 80,44,148\n"
"Qe 6\nQmvva 84,16,152\nQe 7\nQmvva 84,44,156\nQk 50,4,5\nQk 51,6,7\nQmvva 88,16,160\nQe 8\n"
"Qmvva 88,44,164\nQe 9\nQmvva 92,16,168\nQe 10\nQmvva 92,44,172\nQe 11\nQmvva 124,20,168\nQk 54,8,9\n"
"Qk 55,10,11\nQmvva 124,48,172\nds_read_b32 v66, v74\nQmvva 120,20,160\nQe 12\nQmvva 120,48,164\nQe 13\nQmvva 116,20,152\n"
"Qe 14\nQmvva 116,48,156\nQe 15\nQmvva 112,20,144\ns_waitcnt lgkmcnt(0)\nv_cmp_gt_u32 vcc, v75, v66\nQmvva 112,48,148\ns_cbranch_vccnz .Lsa1\n"
".Lra1:\nQk 52,12,13\nQcvaa 0,112,48,56\ns_add_u32 m0, s61, 0\nQk 53,14,15\nQmvaa 0,116,52\nbuffer_load_dwordx4 v69, s[40:43], s60 offen nt lds\nQmvaa 0,120,56\n"
"Qmvaa 0,124,60\ns_add_u32 s65, s65, 2048\nQcvaa 4,140,60,56\nQmvaa 4,136,56\nQmvaa 4,132,52\nbuffer_load_dwordx4 v69, s[40:43], s60 offen offset:1024 nt lds\nQmvaa 4,128,48\nQcvaa 8,128,64,56\n"
"s_nop 0\nQmvaa 8,132,68\nQe 0\nQmvaa 8,136,72\nQe 1\nQmvaa 8,140,76\nbuffer_load_dwordx4 v69, s[40:43], s90 offen offset:2048 nt lds\nQcvaa 12,124,76,56\n"
"Qe 2\nQmvaa 12,120,72\nQe 3\nQmvaa 12,116,68\nQe 4\nQmvaa 12,112,64\nQe 5\nv_smfmac_f32_16x16x64_bf16 a[192:195], a[204:207], v[48:55], v77\n"
"Qk 20,0,1\nQmvva 144,24,144\nbuffer_load_dwordx4 v69, s[40:43], s90 offen offset:3072 nt lds\nQmvva 144,52,148\nQe 6\nQmvva 148,24,152\nQe 7\nQmvva 148,52,156\n"
"s_add_u32 m0, s64, 0\nQk 21,2,3\nQmvva 152,24,160\nbuffer_load_dwordx4 v72, s[44:47], s60 offen nt lds\nQmvva 152,52,164\nQk 22,4,5\nQk 23,6,7\nQmvva 156,24,168\n"
"Qe 8\nQmvva 156,52,172\nQe 9\nQmvva 188,28,168\nQe 10\nQmvva 188,20,172\nbuffer_load_dwordx4 v72, s[44:47], s60 offen offset:1024 nt lds\nQmvva 184,28,160\n"
"Qe 11\nQmvva 184,20,164\nQk 26,8,9\nQk 27,10,11\nQmvva 180,28,152\nQe 12\nQmvva 180,20,156\nQe 13\n"
"Qmvva 176,28,144\nbuffer_load_dwordx4 v72, s[44:47], s90 offen offset:2048 nt lds\nQmvva 176,20,148\nQe 14\nQcvaa 0,112,80,56\nQe 15\nQmvaa 0,116,84\nQk 24,12,13\n"
"Qk 25,14,15\nQmvaa 0,120,88\nQmvaa 0,124,92\nbuffer_load_dwordx4 v72, s[44:47], s90 offen offset:3072 nt lds\nQcvaa 4,140,92,56\nQmvaa 4,136,88\nQmvaa 4,132,84\nQmvaa 4,128,80\n"
"s_xor_b32 s60, s65, %21\ns_add_u32 s90, s60, 260096\nQcvaa 8,128,96,56\nQda 128,68,1024\nQmvaa 8,132,100\nQda 132,68,1536\nQmvaa 8,136,104\nQda 136,68,1280\n"
"Qmvaa 8,140,108\nQda 140,68,1792\nQcvaa 12,124,108,56\nQda 124,68,768\nQmvaa 12,120,104\nQda 120,68,256\nQmvaa 12,116,100\nQda 116,68,512\n"
"Qmvaa 12,112,96\nQda 112,68,0\nv_smfmac_f32_16x16x64_bf16 a[196:199], a[204:207], v[20:27], v77\nQmvva 208,32,144\nQe 0\nQmvva 208,24,148\nQe 1\nQmvva 212,32,152\n"
"Qe 2\nQmvva 212,24,156\nQe 3\nQmvva 216,32,160\nQk 28,0,1\nQk 29,2,3\nQmvva 216,24,164\nQe 4\n"
"Qmvva 220,32,168\nQe 5\nQmvva 220,24,172\ns_waitcnt lgkmcnt(0)\nQk 30,4,5\nQcvaa 0,112,0,56\nQe 6\nQmvaa 0,116,4\n"
"Qe 7\nQmvaa 0,120,8\nQk 31,6,7\nQmvaa 0,124,12\nQe 8\nQcvaa 4,140,12,56\nQe 9\nQmvaa 4,136,8\n"
"Qe 10\nQmvaa 4,132,4\nQe 11\nQmvaa 4,128,0\nQe 12\nQmvva 240,36,144\nQe 13\nQmvva 240,28,148\n"
"Qe 14\nQmvva 244,36,152\nQe 15\nQmvva 244,28,156\nQk 34,8,9\nQk 35,10,11\nQmvva 248,36,160\nQk 32,12,13\n"
"Qk 33,14,15\nQmvva 248,28,164\nQe 0\nQmvva 252,36,168\nQe 1\nQmvva 252,28,172\nQe 2\nQmava 236,40,168\n"
"Qta 168,70,33920\nQta 170,70,34944\nQmava 236,32,172\nQe 3\nQmava 232,40,160\nQta 172,71,896\nQta 174,71,1920\nQmava 232,32,164\n"
"Qk 36,0,1\nQk 37,2,3\nQmava 228,40,152\nQta 160,70,33664\nQta 162,70,34688\nQmava 228,32,156\nQe 4\nQmava 224,40,144\n"
"Qta 164,71,640\nQta 166,71,1664\nQmava 224,32,148\nQe 5\nv_smfmac_f32_16x16x64_bf16 a[200:203], a[204:207], v[28:35], v77\nQta 152,70,33408\nQta 154,70,34432\nQcvaa 8,112,16,56\n"
"Qe 6\nQmvaa 8,116,20\nQta 156,71,384\nQta 158,71,1408\nQmvaa 8,120,24\nQe 7\nQmvaa 8,124,28\nQta 144,70,33152\n"
"Qta 146,70,34176\nQcvaa 12,140,28,56\nQk 38,4,5\nQk 39,6,7\nQmvaa 12,136,24\nQta 148,71,128\nQta 150,71,1152\nQmvaa 12,132,20\n"
"Qmvaa 12,128,16\ns_lshl_b32 s66, s67, 3\n.Lloop:\ns_bitcmp1_b32 s99, s66\ns_cbranch_scc1 .Lhook\n.Lhookret:\nQcvaa 0,128,32,56\nv_add_u32 v75, 1, v75\n"
"Qmvaa 0,132,36\ns_waitcnt vmcnt(8)\nds_write_b32 v73, v75\nQmvaa 0,136,40\nQe 8\nQmvaa 0,140,44\nQe 9\nQcvaa 4,124,44,56\n"
"Qe 10\nQmvaa 4,120,40\nQe 11\nQmvaa 4,116,36\nQe 12\nQmvaa 4,112,32\ns_waitcnt lgkmcnt(1)\nQk 40,8,9\n"
"Qmvva 96,44,144\nQe 13\nQmvva 96,36,148\nQe 14\nQmvva 100,44,152\nQe 15\nQmvva 100,36,156\nQk 41,10,11\n"
"Qk 42,12,13\nQmvva 104,44,160\nQk 43,14,15\nQmvva 104,36,164\nQe 0\nQmvva 108,44,168\nQe 1\nQmvva 108,36,172\n"
"Qe 2\nQmvva 140,48,168\nQe 3\nQmvva 140,40,172\nQk 46,0,1\nQk 47,2,3\nQmvva 136,48,160\nds_read_b32 v66, v74\n"
"Qmvva 136,40,164\nQe 4\nQmvva 132,48,152\nQe 5\nQmvva 132,40,156\nQe 6\nQmvva 128,48,144\nQe 7\n"
"Qmvva 128,40,148\ns_waitcnt lgkmcnt(0)\nv_cmp_gt_u32 vcc, v75, v66\nv_smfmac_f32_16x16x64_bf16 a[176:179], a[204:207], v[36:43], v77\ns_cbranch_vccnz .Lsa2\n.Lra2:\nQcvaa 8,112,48,56\ns_add_u32 m0, s61, 33024\n"
"Qk 44,4,5\nQmvaa 8,116,52\nbuffer_load_dwordx4 v69, s[40:43], s60 offen nt lds\nQmvaa 8,120,56\nQk 45,6,7\nQmvaa 8,124,60\ns_add_u32 s65, s65, 2048\nQcvaa 12,140,60,56\n"
"Qmvaa 12,136,56\nQmvaa 12,132,52\nbuffer_load_dwordx4 v69, s[40:43], s60 offen offset:1024 nt lds\nQmvaa 12,128,48\nQcvaa 0,128,64,56\ns_memrealtime s[74:75]\nQmvaa 0,132,68\nQe 8\n"
"Qmvaa 0,136,72\nQe 9\nQmvaa 0,140,76\nbuffer_load_dwordx4 v69, s[40:43], s90 offen offset:2048 nt lds\nQcvaa 4,124,76,56\nQe 10\nQmvaa 4,120,72\nQe 11\n"
"Qmvaa 4,116,68\nQe 12\nQmvaa 4,112,64\nQe 13\nQmvva 160,52,144\nbuffer_load_dwordx4 v69, s[40:43], s90 offen offset:3072 nt lds\nQmvva 160,44,148\nQe 14\n"
"Qmvva 164,52,152\nQe 15\nQmvva 164,44,156\nQk 48,8,9\nQk 49,10,11\nQmvva 168,52,160\ns_add_u32 m0, s64, 33024\nQk 50,12,13\n"
"Qmvva 168,44,164\nbuffer_load_dwordx4 v72, s[44:47], s60 offen nt lds\nQmvva 172,52,168\nQk 51,14,15\nQmvva 172,44,172\nQe 0\nQmvva 204,20,168\nQe 1\n"
"Qmvva 204,48,172\nQe 2\nQmvva 200,20,160\nbuffer_load_dwordx4 v72, s[44:47], s60 offen offset:1024 nt lds\nQmvva 200,48,164\nQe 3\nQmvva 196,20,152\nQk 18,0,1\n"
"Qk 19,2,3\nQmvva 196,48,156\nQe 4\nQmvva 192,20,144\nQe 5\nQmvva 192,48,148\nbuffer_load_dwordx4 v72, s[44:47], s90 offen offset:2048 nt lds\nv_smfmac_f32_16x16x64_bf16 a[180:183], a[204:207], v[44:51], v77\n"
"Qk 16,4,5\nQcvaa 8,112,80,56\nQe 6\nQmvaa 8,116,84\nQe 7\nQmvaa 8,120,88\nQk 17,6,7\nQmvaa 8,124,92\n"
"buffer_load_dwordx4 v72, s[44:47], s90 offen offset:3072 nt lds\nQcvaa 12,140,92,56\ns_waitcnt lgkmcnt(0)\nQmvaa 12,136,88\ns_cmp_gt_u32 s77, s74\ns_cbranch_scc1 .Lsp2\n.Lrp2:\nQmvaa 12,132,84\n"
"s_add_u32 s77, s74, 239\nQmvaa 12,128,80\ns_xor_b32 s60, s65, %21\ns_add_u32 s90, s60, 260096\nQcvaa 0,128,96,56\nQda 128,68,34048\nQmvaa 0,132,100\nQda 132,68,34560\n"
"Qmvaa 0,136,104\nQda 136,68,34304\nQmvaa 0,140,108\nQda 140,68,34816\nQcvaa 4,124,108,56\nQda 124,68,33792\nQmvaa 4,120,104\nQda 120,68,33280\n"
"Qmvaa 4,116,100\nQda 116,68,33536\nQmvaa 4,112,96\nQda 112,68,33024\nQmvva 224,24,144\nQe 8\nQmvva 224,16,148\nQe 9\n"
"Qmvva 228,24,152\nQe 10\nQmvva 228,16,156\nQe 11\nQmvva 232,24,160\nQk 20,8,9\nQk 21,10,11\nQmvva 232,16,164\n"
"Qe 12\nQmvva 236,24,168\nQe 13\nQmvva 236,16,172\ns_waitcnt lgkmcnt(0)\nQk 22,12,13\nQcvaa 8,112,0,56\nQe 14\n"
"Qmvaa 8,116,4\nv_subrev_u32 v68, 66048, v68\nQmvaa 8,120,8\nQe 15\nQmvaa 8,124,12\nQk 23,14,15\nQcvaa 12,140,12,56\nQe 0\n"
"Qmvaa 12,136,8\nQe 1\nQmvaa 12,132,4\nQe 2\nQmvaa 12,128,0\nQe 3\nv_smfmac_f32_16x16x64_bf16 a[184:187], a[204:207], v[16:23], v77\nQk 26,0,1\n"
"Qmava 208,28,144\nQe 4\nQmava 208,20,148\nQe 5\nQmava 212,28,152\nQe 6\nQmava 212,20,156\nQe 7\n"
"Qmava 216,28,160\nQk 27,2,3\nQk 24,4,5\nQmava 216,20,164\nQk 25,6,7\nQmava 220,28,168\nQe 8\nQmava 220,20,172\n"
"Qe 9\nQmava 252,32,168\nQta 168,71,768\nQta 170,71,1792\nQmava 252,24,172\nQe 10\nQmava 248,32,160\nQta 172,71,33792\n"
"Qta 174,71,34816\nQmava 248,24,164\nQe 11\nQmava 244,32,152\nQta 160,71,512\nQta 162,71,1536\nQmava 244,24,156\nQk 28,8,9\n"
"Qk 29,10,11\nQmava 240,32,144\nQta 164,71,33536\nQta 166,71,34560\nQmava 240,24,148\nQe 12\nQcvaa 0,112,16,56\nQta 152,71,256\n"
"Qta 154,71,1280\nQmvaa 0,116,20\nQe 13\nQmvaa 0,120,24\nQta 156,71,33280\nQta 158,71,34304\nQmvaa 0,124,28\nQe 14\n"
"Qcvaa 4,140,28,56\nQta 144,71,0\nQta 146,71,1024\nQmvaa 4,136,24\nQe 15\nQmvaa 4,132,20\nQta 148,71,33024\nQta 150,71,34048\n"
"Qmvaa 4,128,16\nQk 30,12,13\nQk 31,14,15\nQcvaa 8,128,32,56\nv_add_u32 v75, 1, v75\nQmvaa 8,132,36\ns_waitcnt vmcnt(8)\nds_write_b32 v73, v75\n"
"Qmvaa 8,136,40\nQe 0\nQmvaa 8,140,44\nQe 1\nQcvaa 12,124,44,56\nQe 2\nQmvaa 12,120,40\nQe 3\n"
"Qmvaa 12,116,36\nQe 4\nQmvaa 12,112,32\ns_waitcnt lgkmcnt(1)\nQk 32,0,1\nv_smfmac_f32_16x16x64_bf16 a[188:191], a[204:207], v[24:31], v77\nQk 33,2,3\nQmvva 80,36,144\n"
"Qe 5\nQmvva 80,28,148\nQe 6\nQmvva 84,36,152\nQe 7\nQmvva 84,28,156\nQk 34,4,5\nQk 35,6,7\n"
"Qmvva 88,36,160\nQe 8\nQmvva 88,28,164\nQe 9\nQmvva 92,36,168\nQe 10\nQmvva 92,28,172\nQe 11\n"
"Qmvva 124,40,168\nQk 38,8,9\nQk 39,10,11\nQmvva 124,32,172\nds_read_b32 v66, v74\nQmvva 120,40,160\nQe 12\n.Lpg6:\n"
"s_cbranch_execz .Ltr9\nQmvva 120,32,164\nQe 13\nQmvva 116,40,152\nQe 14\nQmvva 116,32,156\nQe 15\nQmvva 112,40,144\n"
"s_waitcnt lgkmcnt(0)\nv_cmp_gt_u32 vcc, v75, v66\nQmvva 112,32,148\ns_cbranch_vccnz .Lsa3\n.Lra3:\nQk 36,12,13\nQcvaa 0,112,48,56\ns_add_u32 m0, s61, 66048\n"
"Qk 37,14,15\nQmvaa 0,116,52\nbuffer_load_dwordx4 v69, s[40:43], s60 offen nt lds\nQmvaa 0,120,56\nQmvaa 0,124,60\ns_add_u32 s65, s65, 2048\nQcvaa 4,140,60,56\nQmvaa 4,136,56\n"
"Qmvaa 4,132,52\nbuffer_load_dwordx4 v69, s[40:43], s60 offen offset:1024 nt lds\nQmvaa 4,128,48\nQcvaa 8,128,64,56\ns_nop 0\nQmvaa 8,132,68\nQe 0\nQmvaa 8,136,72\n"
"Qe 1\nQmvaa 8,140,76\nbuffer_load_dwordx4 v69, s[40:43], s90 offen offset:2048 nt lds\nQcvaa 12,124,76,56\nQe 2\nQmvaa 12,120,72\nQe 3\nQmvaa 12,116,68\n"
"Qe 4\nQmvaa 12,112,64\nQe 5\nv_smfmac_f32_16x16x64_bf16 a[192:195], a[204:207], v[32:39], v77\nQk 40,0,1\nQmvva 144,44,144\nbuffer_load_dwordx4 v69, s[40:43], s90 offen offset:3072 nt lds\nQmvva 144,36,148\n"
"Qe 6\nQmvva 148,44,152\nQe 7\nQmvva 148,36,156\ns_add_u32 m0, s64, 66048\nQk 41,2,3\nQmvva 152,44,160\nbuffer_load_dwordx4 v72, s[44:47], s60 offen nt lds\n"
"Qmvva 152,36,164\nQk 42,4,5\nQk 43,6,7\nQmvva 156,44,168\nQe 8\nQmvva 156,36,172\nQe 9\nQmvva 188,48,168\n"
"Qe 10\nQmvva 188,40,172\nbuffer_load_dwordx4 v72, s[44:47], s60 offen offset:1024 nt lds\nQmvva 184,48,160\nQe 11\nQmvva 184,40,164\nQk 46,8,9\nQk 47,10,11\n"
"Qmvva 180,48,152\nQe 12\nQmvva 180,40,156\nQe 13\nQmvva 176,48,144\nbuffer_load_dwordx4 v72, s[44:47], s90 offen offset:2048 nt lds\nQmvva 176,40,148\nQe 14\n"
"Qcvaa 0,112,80,56\nQe 15\nQmvaa 0,116,84\nQk 44,12,13\nQk 45,14,15\nQmvaa 0,120,88\nQmvaa 0,124,92\nbuffer_load_dwordx4 v72, s[44:47], s90 offen offset:3072 nt lds\n"
"Qcvaa 4,140,92,56\nQmvaa 4,136,88\nQmvaa 4,132,84\nQmvaa 4,128,80\ns_xor_b32 s60, s65, %21\ns_add_u32 s90, s60, 260096\nQcvaa 8,128,96,56\nQda 128,68,1024\n"
"Qmvaa 8,132,100\nQda 132,68,1536\nQmvaa 8,136,104\nQda 136,68,1280\nQmvaa 8,140,108\nQda 140,68,1792\nQcvaa 12,124,108,56\nQda 124,68,768\n"
"Qmvaa 12,120,104\nQda 120,68,256\nQmvaa 12,116,100\nQda 116,68,512\nQmvaa 12,112,96\nQda 112,68,0\nv_smfmac_f32_16x16x64_bf16 a[196:199], a[204:207], v[40:47], v77\nQmvva 208,16,144\n"
"Qe 0\nQmvva 208,44,148\nQe 1\nQmvva 212,16,152\nQe 2\nQmvva 212,44,156\nQe 3\nQmvva 216,16,160\n"
"Qk 48,0,1\nQk 49,2,3\nQmvva 216,44,164\nQe 4\nQmvva 220,16,168\nQe 5\nQmvva 220,44,172\ns_waitcnt lgkmcnt(0)\n"
"Qk 50,4,5\nQcvaa 0,112,0,56\nQe 6\nQmvaa 0,116,4\nQe 7\nQmvaa 0,120,8\nQk 51,6,7\nQmvaa 0,124,12\n"
"Qe 8\nQcvaa 4,140,12,56\nQe 9\nQmvaa 4,136,8\nQe 10\nQmvaa 4,132,4\nQe 11\nQmvaa 4,128,0\n"
"Qe 12\nQmvva 240,20,144\nQe 13\nQmvva 240,48,148\nQe 14\nQmvva 244,20,152\nQe 15\nQmvva 244,48,156\n"
"Qk 54,8,9\nQk 55,10,11\nQmvva 248,20,160\nQk 52,12,13\nQk 53,14,15\nQmvva 248,48,164\nQe 0\nQmvva 252,20,168\n"
"Qe 1\nQmvva 252,48,172\nQe 2\nQmava 236,24,168\nQta 168,71,33920\nQta 170,71,34944\nQmava 236,52,172\nQe 3\n"
"Qmava 232,24,160\nQta 172,70,896\nQta 174,70,1920\nQmava 232,52,164\nQk 16,0,1\nQk 17,2,3\nQmava 228,24,152\nQta 160,71,33664\n"
"Qta 162,71,34688\nQmava 228,52,156\nQe 4\nQmava 224,24,144\nQta 164,70,640\nQta 166,70,1664\nQmava 224,52,148\nQe 5\n"
"v_smfmac_f32_16x16x64_bf16 a[200:203], a[204:207], v[48:55], v77\nQta 152,71,33408\nQta 154,71,34432\nQcvaa 8,112,16,56\nQe 6\nQmvaa 8,116,20\nQta 156,70,384\nQta 158,70,1408\n"
"Qmvaa 8,120,24\nQe 7\nQmvaa 8,124,28\nQta 144,71,33152\nQta 146,71,34176\nQcvaa 12,140,28,56\nQk 18,4,5\nQk 19,6,7\n"
"Qmvaa 12,136,24\nQta 148,70,128\nQta 150,70,1152\nQmvaa 12,132,20\nQmvaa 12,128,16\nQcvaa 0,128,32,56\nv_add_u32 v75, 1, v75\nQmvaa 0,132,36\n"
"s_waitcnt vmcnt(8)\nds_write_b32 v73, v75\nQmvaa 0,136,40\nQe 8\nQmvaa 0,140,44\nQe 9\nQcvaa 4,124,44,56\nQe 10\n"
"Qmvaa 4,120,40\nQe 11\nQmvaa 4,116,36\nQe 12\nQmvaa 4,112,32\ns_waitcnt lgkmcnt(1)\nQk 20,8,9\nQmvva 96,28,144\n"
"Qe 13\nQmvva 96,16,148\nQe 14\nQmvva 100,28,152\nQe 15\nQmvva 100,16,156\nQk 21,10,11\nQk 22,12,13\n"
"Qmvva 104,28,160\nQk 23,14,15\nQmvva 104,16,164\nQe 0\nQmvva 108,28,168\nQe 1\nQmvva 108,16,172\nQe 2\n"
"Qmvva 140,32,168\nQe 3\nQmvva 140,20,172\nQk 26,0,1\nQk 27,2,3\nQmvva 136,32,160\nds_read_b32 v66, v74\nQmvva 136,20,164\n"
"Qe 4\nQmvva 132,32,152\nQe 5\nQmvva 132,20,156\nQe 6\nQmvva 128,32,144\nQe 7\nQmvva 128,20,148\n"
"s_waitcnt lgkmcnt(0)\nv_cmp_gt_u32 vcc, v75, v66\nv_smfmac_f32_16x16x64_bf16 a[176:179], a[204:207], v[16:23], v77\ns_cbranch_vccnz .Lsa4\n.Lra4:\nQcvaa 8,112,48,56\ns_add_u32 m0, s61, 99072\nQk 24,4,5\n"
"Qmvaa 8,116,52\nbuffer_load_dwordx4 v69, s[40:43], s60 offen nt lds\nQmvaa 8,120,56\nQk 25,6,7\nQmvaa 8,124,60\ns_or_b32 s65, s65, 0x1d8000\ns_add_u32 s65, s65, 2048\nQcvaa 12,140,60,56\n"
"s_andn2_b32 s65, s65, 0x1d8000\nQmvaa 12,136,56\nQmvaa 12,132,52\nbuffer_load_dwordx4 v69, s[40:43], s60 offen offset:1024 nt lds\nQmvaa 12,128,48\nQcvaa 0,128,64,56\ns_memrealtime s[74:75]\nQmvaa 0,132,68\n"
"Qe 8\nQmvaa 0,136,72\nQe 9\nQmvaa 0,140,76\nbuffer_load_dwordx4 v69, s[40:43], s90 offen offset:2048 nt lds\nQcvaa 4,124,76,56\nQe 10\nQmvaa 4,120,72\n"
"Qe 11\nQmvaa 4,116,68\nQe 12\nQmvaa 4,112,64\nQe 13\nQmvva 160,36,144\nbuffer_load_dwordx4 v69, s[40:43], s90 offen offset:3072 nt lds\nQmvva 160,24,148\n"
"Qe 14\nQmvva 164,36,152\nQe 15\nQmvva 164,24,156\nQk 28,8,9\nQk 29,10,11\nQmvva 168,36,160\ns_add_u32 m0, s64, 99072\n"
"Qk 30,12,13\nQmvva 168,24,164\nbuffer_load_dwordx4 v72, s[44:47], s60 offen nt lds\nQmvva 172,36,168\nQk 31,14,15\nQmvva 172,24,172\nQe 0\nQmvva 204,40,168\n"
"Qe 1\nQmvva 204,28,172\nQe 2\nQmvva 200,40,160\nbuffer_load_dwordx4 v72, s[44:47], s60 offen offset:1024 nt lds\nQmvva 200,28,164\nQe 3\nQmvva 196,40,152\n"
"Qk 34,0,1\nQk 35,2,3\nQmvva 196,28,156\nQe 4\nQmvva 192,40,144\nQe 5\nQmvva 192,28,148\nbuffer_load_dwordx4 v72, s[44:47], s90 offen offset:2048 nt lds\n"
"v_smfmac_f32_16x16x64_bf16 a[180:183], a[204:207], v[24:31], v77\nQk 32,4,5\nQcvaa 8,112,80,56\nQe 6\nQmvaa 8,116,84\nQe 7\nQmvaa 8,120,88\nQk 33,6,7\n"
"Qmvaa 8,124,92\nbuffer_load_dwordx4 v72, s[44:47], s90 offen offset:3072 nt lds\nQcvaa 12,140,92,56\ns_waitcnt lgkmcnt(0)\nQmvaa 12,136,88\ns_cmp_gt_u32 s77, s74\ns_cbranch_scc1 .Lsp4\n.Lrp4:\n"
"Qmvaa 12,132,84\ns_add_u32 s77, s74, 239\nQmvaa 12,128,80\ns_xor_b32 s60, s65, %21\ns_add_u32 s90, s60, 260096\nQcvaa 0,128,96,56\nQda 128,68,34048\nQmvaa 0,132,100\n"
"Qda 132,68,34560\nQmvaa 0,136,104\nQda 136,68,34304\nQmvaa 0,140,108\nQda 140,68,34816\nQcvaa 4,124,108,56\nQda 124,68,33792\nQmvaa 4,120,104\n"
"Qda 120,68,33280\nQmvaa 4,116,100\nQda 116,68,33536\nQmvaa 4,112,96\nQda 112,68,33024\nQmvva 224,44,144\nQe 8\nQmvva 224,32,148\n"
"Qe 9\nQmvva 228,44,152\nQe 10\nQmvva 228,32,156\nQe 11\nQmvva 232,44,160\nQk 36,8,9\nQk 37,10,11\n"
"Qmvva 232,32,164\nQe 12\nQmvva 236,44,168\nQe 13\nQmvva 236,32,172\ns_waitcnt lgkmcnt(0)\nQk 38,12,13\nQcvaa 8,112,0,56\n"
"Qe 14\nQmvaa 8,116,4\nv_add_u32 v68, 66048, v68\nQmvaa 8,120,8\nQe 15\nQmvaa 8,124,12\nQk 39,14,15\nQcvaa 12,140,12,56\n"
"Qe 0\nQmvaa 12,136,8\nQe 1\nQmvaa 12,132,4\nQe 2\nQmvaa 12,128,0\nQe 3\nv_smfmac_f32_16x16x64_bf16 a[184:187], a[204:207], v[32:39], v77\n"
"Qk 42,0,1\nQmava 208,48,144\nQe 4\nQmava 208,36,148\nQe 5\nQmava 212,48,152\nQe 6\nQmava 212,36,156\n"
"Qe 7\nQmava 216,48,160\nQk 43,2,3\nQk 40,4,5\nQmava 216,36,164\nQk 41,6,7\nQmava 220,48,168\nQe 8\n"
"Qmava 220,36,172\nQe 9\nQmava 252,52,168\nQta 168,70,768\nQta 170,70,1792\nQmava 252,40,172\nQe 10\nQmava 248,52,160\n"
"Qta 172,70,33792\nQta 174,70,34816\nQmava 248,40,164\nQe 11\nQmava 244,52,152\nQta 160,70,512\nQta 162,70,1536\nQmava 244,40,156\n"
"Qk 44,8,9\nQk 45,10,11\nQmava 240,52,144\nQta 164,70,33536\nQta 166,70,34560\nQmava 240,40,148\nQe 12\nQcvaa 0,112,16,56\n"
"Qta 152,70,256\nQta 154,70,1280\nQmvaa 0,116,20\nQe 13\nQmvaa 0,120,24\nQta 156,70,33280\nQta 158,70,34304\nQmvaa 0,124,28\n"
"Qe 14\nQcvaa 4,140,28,56\nQta 144,70,0\nQta 146,70,1024\nQmvaa 4,136,24\nQe 15\nQmvaa 4,132,20\nQta 148,70,33024\n"
"Qta 150,70,34048\nQmvaa 4,128,16\nQk 46,12,13\nQk 47,14,15\nQcvaa 8,128,32,56\nv_add_u32 v75, 1, v75\nQmvaa 8,132,36\ns_waitcnt vmcnt(8)\n"
"ds_write_b32 v73, v75\nQmvaa 8,136,40\nQe 0\nQmvaa 8,140,44\nQe 1\nQcvaa 12,124,44,56\nQe 2\nQmvaa 12,120,40\n"
"Qe 3\nQmvaa 12,116,36\nQe 4\nQmvaa 12,112,32\ns_waitcnt lgkmcnt(1)\nQk 48,0,1\nv_smfmac_f32_16x16x64_bf16 a[188:191], a[204:207], v[40:47], v77\nQk 49,2,3\n"
"Qmvva 80,16,144\nQe 5\nQmvva 80,44,148\nQe 6\nQmvva 84,16,152\nQe 7\nQmvva 84,44,156\nQk 50,4,5\n"
"Qk 51,6,7\nQmvva 88,16,160\nQe 8\nQmvva 88,44,164\nQe 9\nQmvva 92,16,168\nQe 10\nQmvva 92,44,172\n"
"Qe 11\nQmvva 124,20,168\nQk 54,8,9\nQk 55,10,11\nQmvva 124,48,172\nds_read_b32 v66, v74\nQmvva 120,20,160\nQe 12\n"
"Qmvva 120,48,164\nQe 13\nQmvva 116,20,152\nQe 14\nQmvva 116,48,156\nQe 15\nQmvva 112,20,144\ns_waitcnt lgkmcnt(0)\n"
"v_cmp_gt_u32 vcc, v75, v66\nQmvva 112,48,148\ns_cbranch_vccnz .Lsa5\n.Lra5:\nQk 52,12,13\nQcvaa 0,112,48,56\ns_add_u32 m0, s61, 0\nQk 53,14,15\n"
"Qmvaa 0,116,52\nbuffer_load_dwordx4 v69, s[40:43], s60 offen nt lds\nQmvaa 0,120,56\nQmvaa 0,124,60\ns_add_u32 s65, s65, 2048\nQcvaa 4,140,60,56\nQmvaa 4,136,56\nQmvaa 4,132,52\n"
"buffer_load_dwordx4 v69, s[40:43], s60 offen offset:1024 nt lds\nQmvaa 4,128,48\nQcvaa 8,128,64,56\ns_nop 0\nQmvaa 8,132,68\nQe 0\nQmvaa 8,136,72\nQe 1\n"
"Qmvaa 8,140,76\nbuffer_load_dwordx4 v69, s[40:43], s90 offen offset:2048 nt lds\nQcvaa 12,124,76,56\nQe 2\nQmvaa 12,120,72\nQe 3\nQmvaa 12,116,68\nQe 4\n"
"Qmvaa 12,112,64\nQe 5\nv_smfmac_f32_16x16x64_bf16 a[192:195], a[204:207], v[48:55], v77\nQk 20,0,1\nQmvva 144,24,144\nbuffer_load_dwordx4 v69, s[40:43], s90 offen offset:3072 nt lds\nQmvva 144,52,148\nQe 6\n"
"Qmvva 148,24,152\nQe 7\nQmvva 148,52,156\ns_add_u32 m0, s64, 0\nQk 21,2,3\nQmvva 152,24,160\nbuffer_load_dwordx4 v72, s[44:47], s60 offen nt lds\nQmvva 152,52,164\n"
"Qk 22,4,5\nQk 23,6,7\nQmvva 156,24,168\nQe 8\nQmvva 156,52,172\nQe 9\nQmvva 188,28,168\nQe 10\n"
"Qmvva 188,20,172\nbuffer_load_dwordx4 v72, s[44:47], s60 offen offset:1024 nt lds\nQmvva 184,28,160\nQe 11\nQmvva 184,20,164\nQk 26,8,9\nQk 27,10,11\nQmvva 180,28,152\n"
"Qe 12\nQmvva 180,20,156\nQe 13\nQmvva 176,28,144\nbuffer_load_dwordx4 v72, s[44:47], s90 offen offset:2048 nt lds\nQmvva 176,20,148\nQe 14\nQcvaa 0,112,80,56\n"
"Qe 15\nQmvaa 0,116,84\nQk 24,12,13\nQk 25,14,15\nQmvaa 0,120,88\nQmvaa 0,124,92\nbuffer_load_dwordx4 v72, s[44:47], s90 offen offset:3072 nt lds\nQcvaa 4,140,92,56\n"
"Qmvaa 4,136,88\nQmvaa 4,132,84\nQmvaa 4,128,80\n.Lpg7:\ns_cbranch_execz .Ltr9\ns_xor_b32 s60, s65, %21\ns_add_u32 s90, s60, 260096\nQcvaa 8,128,96,56\n"
"Qda 128,68,1024\nQmvaa 8,132,100\nQda 132,68,1536\nQmvaa 8,136,104\nQda 136,68,1280\nQmvaa 8,140,108\nQda 140,68,1792\nQcvaa 12,124,108,56\n"
"Qda 124,68,768\nQmvaa 12,120,104\nQda 120,68,256\nQmvaa 12,116,100\nQda 116,68,512\nQmvaa 12,112,96\nQda 112,68,0\nv_smfmac_f32_16x16x64_bf16 a[196:199], a[204:207], v[20:27], v77\n"
"Qmvva 208,32,144\nQe 0\nQmvva 208,24,148\nQe 1\nQmvva 212,32,152\nQe 2\nQmvva 212,24,156\nQe 3\n"
"Qmvva 216,32,160\nQk 28,0,1\nQk 29,2,3\nQmvva 216,24,164\nQe 4\nQmvva 220,32,168\nQe 5\nQmvva 220,24,172\n"
"s_waitcnt lgkmcnt(0)\nQk 30,4,5\nQcvaa 0,112,0,56\nQe 6\nQmvaa 0,116,4\nQe 7\nQmvaa 0,120,8\nQk 31,6,7\n"
"Qmvaa 0,124,12\nQe 8\nQcvaa 4,140,12,56\nQe 9\nQmvaa 4,136,8\nQe 10\nQmvaa 4,132,4\nQe 11\n"
"Qmvaa 4,128,0\nQe 12\nQmvva 240,36,144\nQe 13\nQmvva 240,28,148\nQe 14\nQmvva 244,36,152\nQe 15\n"
"Qmvva 244,28,156\nQk 34,8,9\nQk 35,10,11\nQmvva 248,36,160\nQk 32,12,13\nQk 33,14,15\nQmvva 248,28,164\nQe 0\n"
"Qmvva 252,36,168\nQe 1\nQmvva 252,28,172\nQe 2\nQmava 236,40,168\nQta 168,70,33920\nQta 170,70,34944\nQmava 236,32,172\n"
"Qe 3\nQmava 232,40,160\nQta 172,71,896\nQta 174,71,1920\nQmava 232,32,164\nQk 36,0,1\nQk 37,2,3\nQmava 228,40,152\n"
"Qta 160,70,33664\nQta 162,70,34688\nQmava 228,32,156\nQe 4\nQmava 224,40,144\nQta 164,71,640\nQta 166,71,1664\nQmava 224,32,148\n"
"Qe 5\nv_smfmac_f32_16x16x64_bf16 a[200:203], a[204:207], v[28:35], v77\nQta 152,70,33408\nQta 154,70,34432\nQcvaa 8,112,16,56\nQe 6\nQmvaa 8,116,20\nQta 156,71,384\n"
"Qta 158,71,1408\nQmvaa 8,120,24\nQe 7\nQmvaa 8,124,28\nQta 144,70,33152\nQta 146,70,34176\nQcvaa 12,140,28,56\nQk 38,4,5\n"
"Qk 39,6,7\nQmvaa 12,136,24\nQta 148,71,128\nQta 150,71,1152\nQmvaa 12,132,20\nQmvaa 12,128,16\ns_add_u32 s66, s66, 1\ns_bitcmp1_b32 0x7f7f, s66\n"
"s_cbranch_scc1 .Lloop\ns_cmp_lg_u32 s67, 0\ns_cbranch_scc1 .Ltail1\nQcvaa 0,128,32,56\nv_add_u32 v75, 1, v75\nQmvaa 0,132,36\ns_waitcnt vmcnt(8)\nds_write_b32 v73, v75\n"
"Qmvaa 0,136,40\nQe 8\nQmvaa 0,140,44\nQe 9\nQcvaa 4,124,44,56\nQe 10\nQmvaa 4,120,40\nQe 11\n"
"Qmvaa 4,116,36\nQe 12\nQmvaa 4,112,32\ns_waitcnt lgkmcnt(1)\nQk 40,8,9\nQmvva 96,44,144\nQe 13\nQmvva 96,36,148\n"
"Qe 14\nQmvva 100,44,152\nQe 15\nQmvva 100,36,156\nQk 41,10,11\nQk 42,12,13\nQmvva 104,44,160\nQk 43,14,15\n"
"Qmvva 104,36,164\nQe 0\nQmvva 108,44,168\nQe 1\nQmvva 108,36,172\nQe 2\nQmvva 140,48,168\nQe 3\n"
"Qmvva 140,40,172\nQk 46,0,1\nQk 47,2,3\nQmvva 136,48,160\nds_read_b32 v66, v74\nQmvva 136,40,164\nQe 4\nQmvva 132,48,152\n"
"Qe 5\nQmvva 132,40,156\nQe 6\nQmvva 128,48,144\nQe 7\nQmvva 128,40,148\ns_waitcnt lgkmcnt(0)\nv_cmp_gt_u32 vcc, v75, v66\n"
"v_smfmac_f32_16x16x64_bf16 a[176:179], a[204:207], v[36:43], v77\ns_cbranch_vccnz .Lsa30\n.Lra30:\nQcvaa 8,112,48,56\ns_add_u32 m0, s62, 0\ns_add_u32 s69, s87, 0\nQk 44,4,5\nQmvaa 8,116,52\n"
"buffer_load_dwordx4 v62, s[52:55], s69 offen lds\nQmvaa 8,120,56\nQk 45,6,7\nQmvaa 8,124,60\nQcvaa 12,140,60,56\nQmvaa 12,136,56\ns_add_u32 s69, s87, 64504\nQmvaa 12,132,52\n"
"buffer_load_dwordx4 v62, s[52:55], s69 offen offset:1032 lds\nQmvaa 12,128,48\nQcvaa 0,128,64,56\nQmvaa 0,132,68\nQe 8\nQmvaa 0,136,72\ns_add_u32 s69, s87, 129008\nQe 9\n"
"Qmvaa 0,140,76\nbuffer_load_dwordx4 v62, s[52:55], s69 offen offset:2064 lds\nQcvaa 4,124,76,56\nQe 10\nQmvaa 4,120,72\nQe 11\nQmvaa 4,116,68\nQe 12\n"
"Qmvaa 4,112,64\ns_add_u32 s69, s87, 193512\nQe 13\nQmvva 160,52,144\nbuffer_load_dwordx4 v62, s[52:55], s69 offen offset:3096 lds\nQmvva 160,44,148\nQe 14\nQmvva 164,52,152\n"
"Qe 15\nQmvva 164,44,156\nQk 48,8,9\nQk 49,10,11\nQmvva 168,52,160\ns_add_u32 m0, s62, 16512\ns_add_u32 s69, s87, 128\nQk 50,12,13\n"
"Qmvva 168,44,164\nbuffer_load_dwordx4 v62, s[52:55], s69 offen lds\nQmvva 172,52,168\nQk 51,14,15\nQmvva 172,44,172\nQe 0\nQmvva 204,20,168\nQe 1\n"
"Qmvva 204,48,172\ns_add_u32 s69, s87, 64632\nQe 2\nQmvva 200,20,160\nbuffer_load_dwordx4 v62, s[52:55], s69 offen offset:1032 lds\nQmvva 200,48,164\nQe 3\nQmvva 196,20,152\n"
"Qk 18,0,1\nQk 19,2,3\nQmvva 196,48,156\nQe 4\nQmvva 192,20,144\ns_add_u32 s69, s87, 129136\nQe 5\nQmvva 192,48,148\n"
"buffer_load_dwordx4 v62, s[52:55], s69 offen offset:2064 lds\nv_smfmac_f32_16x16x64_bf16 a[180:183], a[204:207], v[44:51], v77\nQk 16,4,5\nQcvaa 8,112,80,56\nQe 6\nQmvaa 8,116,84\nQe 7\nQmvaa 8,120,88\n"
"s_add_u32 s69, s87, 193640\nQk 17,6,7\nQmvaa 8,124,92\nbuffer_load_dwordx4 v62, s[52:55], s69 offen offset:3096 lds\nQcvaa 12,140,92,56\nQmvaa 12,136,88\nQmvaa 12,132,84\nQmvaa 12,128,80\n"
"Qcvaa 0,128,96,56\nQda 128,68,34048\nQmvaa 0,132,100\nQda 132,68,34560\nQmvaa 0,136,104\nQda 136,68,34304\nQmvaa 0,140,108\nQda 140,68,34816\n"
"Qcvaa 4,124,108,56\nQda 124,68,33792\nQmvaa 4,120,104\nQda 120,68,33280\nQmvaa 4,116,100\nQda 116,68,33536\nQmvaa 4,112,96\nQda 112,68,33024\n"
"Qmvva 224,24,144\nQe 8\nQmvva 224,16,148\ns_add_u32 m0, s63, 33024\ns_add_u32 s88, s86, 0\nQe 9\nQmvva 228,24,152\nbuffer_load_dwordx4 v64, s[48:51], s88 offen lds\n"
"Qe 10\nQmvva 228,16,156\nQe 11\nQmvva 232,24,160\nQk 20,8,9\nQk 21,10,11\nQmvva 232,16,164\ns_add_u32 s88, s86, 19456\n"
"Qe 12\nQmvva 236,24,168\nbuffer_load_dwordx4 v64, s[48:51], s88 offen offset:1024 lds\nQe 13\nQmvva 236,16,172\ns_waitcnt lgkmcnt(0)\nQk 22,12,13\nQcvaa 8,112,0,56\n"
"Qe 14\nQmvaa 8,116,4\nv_subrev_u32 v68, 66048, v68\ns_add_u32 s88, s86, 38912\nQmvaa 8,120,8\nbuffer_load_dwordx4 v64, s[48:51], s88 offen offset:2048 lds\nQe 15\nQmvaa 8,124,12\n"
"Qk 23,14,15\nQcvaa 12,140,12,56\nQe 0\nQmvaa 12,136,8\ns_add_u32 s88, s86, 58368\nQe 1\nQmvaa 12,132,4\nbuffer_load_dwordx4 v64, s[48:51], s88 offen offset:3072 lds\n"
"Qe 2\nQmvaa 12,128,0\nQe 3\nv_smfmac_f32_16x16x64_bf16 a[184:187], a[204:207], v[16:23], v77\nQk 26,0,1\nQmava 208,28,144\ns_add_u32 m0, s63, 37120\ns_add_u32 s88, s86, 81920\n"
"Qe 4\nQmava 208,20,148\nbuffer_load_dwordx4 v64, s[48:51], s88 offen lds\nQe 5\nQmava 212,28,152\nQe 6\nQmava 212,20,156\nQe 7\n"
"Qmava 216,28,160\ns_add_u32 s88, s86, 101376\nQk 27,2,3\nQk 24,4,5\nQmava 216,20,164\nbuffer_load_dwordx4 v64, s[48:51], s88 offen offset:1024 lds\nQk 25,6,7\nQmava 220,28,168\n"
"Qe 8\nQmava 220,20,172\nQe 9\nQmava 252,32,168\nQta 168,71,768\nQta 170,71,1792\ns_add_u32 s88, s86, 120832\nQmava 252,24,172\n"
"buffer_load_dwordx4 v64, s[48:51], s88 offen offset:2048 lds\nQe 10\nQmava 248,32,160\nQta 172,71,33792\nQta 174,71,34816\nQmava 248,24,164\nQe 11\nQmava 244,32,152\n"
"Qta 160,71,512\nQta 162,71,1536\nQmava 244,24,156\nQk 28,8,9\nQk 29,10,11\nQmava 240,32,144\nQta 164,71,33536\nQta 166,71,34560\n"
"Qmava 240,24,148\nQe 12\nQcvaa 0,112,16,56\nQta 152,71,256\nQta 154,71,1280\nQmvaa 0,116,20\nQe 13\nQmvaa 0,120,24\n"
"Qta 156,71,33280\nQta 158,71,34304\nQmvaa 0,124,28\nQe 14\nQcvaa 4,140,28,56\nQta 144,71,0\nQta 146,71,1024\nQmvaa 4,136,24\n"
"Qe 15\nQmvaa 4,132,20\nQta 148,71,33024\nQta 150,71,34048\nQmvaa 4,128,16\nQk 30,12,13\nQk 31,14,15\nQcvaa 8,128,32,56\n"
"v_add_u32 v75, 1, v75\nQmvaa 8,132,36\nds_write_b32 v73, v75\nQmvaa 8,136,40\nQe 0\nQmvaa 8,140,44\nQe 1\nQcvaa 12,124,44,56\n"
"Qe 2\nQmvaa 12,120,40\nQe 3\nQmvaa 12,116,36\nQe 4\nQmvaa 12,112,32\ns_waitcnt lgkmcnt(1)\nQk 32,0,1\n"
"v_smfmac_f32_16x16x64_bf16 a[188:191], a[204:207], v[24:31], v77\nQk 33,2,3\nQmvva 80,36,144\nQe 5\nQmvva 80,28,148\nQe 6\nQmvva 84,36,152\nQe 7\n"
"Qmvva 84,28,156\nQk 34,4,5\nQk 35,6,7\nQmvva 88,36,160\nQe 8\nQmvva 88,28,164\nQe 9\nQmvva 92,36,168\n"
"Qe 10\nQmvva 92,28,172\nQe 11\nQmvva 124,40,168\nQk 38,8,9\nQk 39,10,11\nQmvva 124,32,172\nds_read_b32 v66, v74\n"
"Qmvva 120,40,160\nQe 12\nQmvva 120,32,164\nQe 13\nQmvva 116,40,152\nQe 14\nQmvva 116,32,156\nQe 15\n"
"Qmvva 112,40,144\ns_waitcnt lgkmcnt(0)\nv_cmp_gt_u32 vcc, v75, v66\nQmvva 112,32,148\ns_cbranch_vccnz .Lsa31\n.Lra31:\nQk 36,12,13\nQcvaa 0,112,48,56\n"
"s_add_u32 m0, s62, 33024\ns_add_u32 s69, s87, 524288\nQk 37,14,15\nQmvaa 0,116,52\nbuffer_load_dwordx4 v62, s[52:55], s69 offen lds\nQmvaa 0,120,56\nQmvaa 0,124,60\nQcvaa 4,140,60,56\n"
"Qmvaa 4,136,56\ns_add_u32 s69, s87, 588792\nQmvaa 4,132,52\nbuffer_load_dwordx4 v62, s[52:55], s69 offen offset:1032 lds\nQmvaa 4,128,48\nQcvaa 8,128,64,56\nQmvaa 8,132,68\nQe 0\n"
"Qmvaa 8,136,72\ns_add_u32 s69, s87, 653296\nQe 1\nQmvaa 8,140,76\nbuffer_load_dwordx4 v62, s[52:55], s69 offen offset:2064 lds\nQcvaa 12,124,76,56\nQe 2\nQmvaa 12,120,72\n"
"Qe 3\nQmvaa 12,116,68\nQe 4\nQmvaa 12,112,64\nQe 5\nv_smfmac_f32_16x16x64_bf16 a[192:195], a[204:207], v[32:39], v77\ns_add_u32 s69, s87, 717800\nQk 40,0,1\n"
"Qmvva 144,44,144\nbuffer_load_dwordx4 v62, s[52:55], s69 offen offset:3096 lds\nQmvva 144,36,148\nQe 6\nQmvva 148,44,152\nQe 7\nQmvva 148,36,156\ns_add_u32 m0, s62, 49536\n"
"s_add_u32 s69, s87, 524416\nQk 41,2,3\nQmvva 152,44,160\nbuffer_load_dwordx4 v62, s[52:55], s69 offen lds\nQmvva 152,36,164\nQk 42,4,5\nQk 43,6,7\nQmvva 156,44,168\n"
"Qe 8\nQmvva 156,36,172\nQe 9\nQmvva 188,48,168\ns_add_u32 s69, s87, 588920\nQe 10\nQmvva 188,40,172\nbuffer_load_dwordx4 v62, s[52:55], s69 offen offset:1032 lds\n"
"Qmvva 184,48,160\nQe 11\nQmvva 184,40,164\nQk 46,8,9\nQk 47,10,11\nQmvva 180,48,152\nQe 12\nQmvva 180,40,156\n"
"s_add_u32 s69, s87, 653424\nQe 13\nQmvva 176,48,144\nbuffer_load_dwordx4 v62, s[52:55], s69 offen offset:2064 lds\nQmvva 176,40,148\nQe 14\nQcvaa 0,112,80,56\nQe 15\n"
"Qmvaa 0,116,84\nQk 44,12,13\nQk 45,14,15\nQmvaa 0,120,88\ns_add_u32 s69, s87, 717928\nQmvaa 0,124,92\nbuffer_load_dwordx4 v62, s[52:55], s69 offen offset:3096 lds\nQcvaa 4,140,92,56\n"
"Qmvaa 4,136,88\nQmvaa 4,132,84\nQmvaa 4,128,80\nQcvaa 8,128,96,56\nQmvaa 8,132,100\nQmvaa 8,136,104\nQmvaa 8,140,108\nQcvaa 12,124,108,56\n"
"Qmvaa 12,120,104\nQmvaa 12,116,100\nQmvaa 12,112,96\nv_smfmac_f32_16x16x64_bf16 a[196:199], a[204:207], v[40:47], v77\nQmvva 208,16,144\nQta 112,71,33152\nQe 0\nQmvva 208,44,148\n"
"Qta 114,71,34176\nQe 1\nQmvva 212,16,152\nQta 116,71,33408\nQe 2\nQmvva 212,44,156\nQta 118,71,34432\nQe 3\n"
"Qmvva 216,16,160\nQta 120,71,33664\nQk 48,0,1\nQk 49,2,3\nQmvva 216,44,164\nQta 122,71,34688\nQe 4\nQmvva 220,16,168\n"
"Qta 124,71,33920\nQe 5\nQmvva 220,44,172\nQta 126,71,34944\nQk 50,4,5\nQe 6\nQe 7\ns_nop 0\n"
"Qk 51,6,7\nQe 8\nQe 9\nQe 10\nQe 11\nQe 12\nQmvva 240,20,144\nv_add_u32 v75, 1, v75\n"
"Qe 13\nQmvva 240,48,148\nds_write_b32 v73, v75\nQe 14\nQmvva 244,20,152\nQe 15\nQmvva 244,48,156\nQk 54,8,9\n"
"Qk 55,10,11\nQmvva 248,20,160\nQk 52,12,13\nQk 53,14,15\nQmvva 248,48,164\nQmvva 252,20,168\nQmvva 252,48,172\nv_smfmac_f32_16x16x64_bf16 a[200:203], a[204:207], v[48:55], v77\n"
"Qmava 224,24,144\nds_read_b32 v66, v74\n.Lpg8:\ns_cbranch_execz .Ltr10\nv_accvgpr_read_b32 v0, a176\nQmava 224,52,148\nv_accvgpr_read_b32 v1, a177\nv_accvgpr_read_b32 v6, a178\n"
"Qmava 228,24,152\nv_accvgpr_read_b32 v7, a179\nv_accvgpr_read_b32 v8, a180\nQmava 228,52,156\nv_accvgpr_read_b32 v9, a181\nv_accvgpr_read_b32 v14, a182\nQmava 232,24,160\nv_accvgpr_read_b32 v15, a183\n"
"v_accvgpr_read_b32 v16, a184\nQmava 232,52,164\ns_waitcnt lgkmcnt(0)\nv_cmp_gt_u32 vcc, v75, v66\nQmava 236,24,168\ns_cbranch_vccnz .Lsa32\n.Lra32:\nv_accvgpr_read_b32 v17, a185\n"
"Qmava 236,52,172\ns_waitcnt lgkmcnt(0)\nv_accvgpr_read_b32 v22, a186\nQmvva 96,28,112\ns_add_u32 m0, s63, 0\ns_add_u32 s88, s86, 128\nv_accvgpr_read_b32 v23, a187\nQmvva 100,28,116\n"
"buffer_load_dwordx4 v64, s[48:51], s88 offen lds\nQmvva 104,28,120\nv_accvgpr_read_b32 v24, a188\nv_accvgpr_read_b32 v25, a189\nQmvva 108,28,124\nv_accvgpr_read_b32 v2, a190\nv_accvgpr_read_b32 v3, a191\nQmvva 128,32,112\n"
"s_add_u32 s88, s86, 19584\nv_accvgpr_read_b32 v4, a192\nQmvva 132,32,116\nbuffer_load_dwordx4 v64, s[48:51], s88 offen offset:1024 lds\nQmvva 136,32,120\nv_accvgpr_read_b32 v5, a193\nv_accvgpr_read_b32 v10, a194\nQmvva 140,32,124\n"
"v_accvgpr_read_b32 v11, a195\nv_accvgpr_read_b32 v12, a196\nQmvva 160,36,112\ns_add_u32 s88, s86, 39040\nv_accvgpr_read_b32 v13, a197\nQmvva 164,36,116\nbuffer_load_dwordx4 v64, s[48:51], s88 offen offset:2048 lds\nQmvva 168,36,120\n"
"v_accvgpr_read_b32 v18, a198\nv_accvgpr_read_b32 v19, a199\nQmvva 172,36,124\nv_accvgpr_read_b32 v20, a200\nv_accvgpr_read_b32 v21, a201\nQmvva 192,40,112\ns_add_u32 s88, s86, 58496\nv_accvgpr_read_b32 v26, a202\n"
"Qmvva 196,40,116\nbuffer_load_dwordx4 v64, s[48:51], s88 offen offset:3072 lds\nQmvva 200,40,120\nv_accvgpr_read_b32 v27, a203\nQa 8,8,9\nQmvva 204,40,124\nQa 10,10,11\nQa 8,8,10\n"
"Qmvva 224,44,112\ns_add_u32 m0, s63, 4096\ns_add_u32 s88, s86, 82048\nQa 16,16,17\nQmvva 228,44,116\nbuffer_load_dwordx4 v64, s[48:51], s88 offen lds\nQmvva 232,44,120\nQa 18,18,19\n"
"Qa 16,16,18\nQmvva 236,44,124\nQa 4,4,5\nQa 6,6,7\nQmava 208,48,112\ns_add_u32 s88, s86, 101504\nQa 4,4,6\nQmava 212,48,116\n"
"buffer_load_dwordx4 v64, s[48:51], s88 offen offset:1024 lds\nQmava 216,48,120\nQa 12,12,13\nQa 14,14,15\nQmava 220,48,124\nQa 12,12,14\nQa 0,0,1\nQmava 240,52,112\n"
"s_add_u32 s88, s86, 120960\nQa 2,2,3\nQmava 244,52,116\nbuffer_load_dwordx4 v64, s[48:51], s88 offen offset:2048 lds\nQmava 248,52,120\nQa 0,0,2\nQa 24,24,25\nQmava 252,52,124\n"
"Qa 26,26,27\nQa 24,24,26\nQa 20,20,21\nQa 22,22,23\nQa 20,20,22\nv_max3_u32 v9, v8, v16, v4\nv_max3_u32 v9, v9, v12, v0\nv_max3_u32 v9, v9, v24, v20\n"
"v_min3_u32 v11, v8, v16, v4\nv_min3_u32 v11, v11, v12, v0\nv_min3_u32 v11, v11, v24, v20\nv_cmp_lt_u32 vcc, 0x4e800000, v9\ns_mov_b64 s[74:75], vcc\nv_cmp_gt_u32 vcc, 0x35800000, v11\ns_or_b64 s[74:75], s[74:75], vcc\ns_cmp_lg_u64 s[74:75], 0\n"
"s_cbranch_scc1 .Lfb\n.Lfbret0:\nv_mov_b32 v30, s73\nv_mov_b32 v31, 0\nv_mov_b32 v58, 0xb9800000\nv_mov_b32 v59, 0\nv_rcp_f32 v2, v0\nv_rcp_f32 v6, v4\n"
"v_mfma_f32_16x16x4_4b_bf16 v[80:95], v[30:31], v[58:59], v[80:95]\nv_rcp_f32 v10, v8\nv_rcp_f32 v14, v12\nv_rcp_f32 v18, v16\nv_rcp_f32 v22, v20\nv_rcp_f32 v26, v24\nv_and_b32 v66, 16, v60\nv_mfma_f32_16x16x4_4b_bf16 v[96:111], v[30:31], v[58:59], v[96:111]\n"
"v_mov_b32 v67, 0xffff\nv_lshlrev_b32 v66, v66, v67\ns_mov_b32 vcc_lo, 0x00c0000c\ns_mov_b32 vcc_hi, 0xc0000c00\nv_cndmask_b32 v67, 0, v66, vcc\ns_lshr_b64 vcc, vcc, 2\nv_mfma_f32_16x16x4_4b_bf16 v[112:127], v[30:31], v[58:59], v[112:127]\nv_cndmask_b32 v66, 0, v66, vcc\n"
"v_mov_b64 v[0:1], 0\nQk 2,2,2\nv_and_b32 v3, v2, v67\nv_and_b32 v2, v2, v66\nv_mov_b64 v[4:5], 0\nv_mfma_f32_16x16x4_4b_bf16 v[128:143], v[30:31], v[58:59], v[128:143]\nQk 6,6,6\nv_and_b32 v7, v6, v67\n"
"v_and_b32 v6, v6, v66\nv_mov_b64 v[8:9], 0\nQk 10,10,10\nv_and_b32 v11, v10, v67\nv_mfma_f32_16x16x4_4b_bf16 v[144:159], v[30:31], v[58:59], v[144:159]\nv_and_b32 v10, v10, v66\nv_mov_b64 v[12:13], 0\nQk 14,14,14\n"
"v_and_b32 v15, v14, v67\nv_and_b32 v14, v14, v66\nv_mov_b64 v[16:17], 0\nv_mfma_f32_16x16x4_4b_bf16 v[160:175], v[30:31], v[58:59], v[160:175]\nQk 18,18,18\nv_and_b32 v19, v18, v67\nv_and_b32 v18, v18, v66\nv_mov_b64 v[20:21], 0\n"
"Qk 22,22,22\nv_and_b32 v23, v22, v67\nv_mfma_f32_16x16x4_4b_bf16 v[176:191], v[30:31], v[58:59], v[176:191]\nv_and_b32 v22, v22, v66\nv_mov_b64 v[24:25], 0\nQk 26,26,26\nv_and_b32 v27, v26, v67\nv_and_b32 v26, v26, v66\n"
"v_mov_b64 v[28:29], 0\nv_mfma_f32_16x16x4_4b_bf16 v[192:207], v[30:31], v[58:59], v[192:207]\nQk 80,80,81\nQk 81,82,83\nQk 82,84,85\nQk 83,86,87\ns_nop 1\nQzvvv 84,80,0\n"
"Qzvvv 80,80,2\nQk 88,88,89\nQk 89,90,91\nQk 90,92,93\nQk 91,94,95\ns_nop 1\nQzvvv 92,88,0\nQzvvv 88,88,2\n"
"v_mfma_f32_16x16x4_4b_bf16 v[208:223], v[30:31], v[58:59], v[208:223]\nQk 96,96,97\nQk 97,98,99\nQk 98,100,101\nQk 99,102,103\nQk 80,80,81\nQk 81,82,83\nQzvvv 100,96,0\n"
"Qk 82,84,85\nQk 83,86,87\nQzvvv 96,96,2\nv_accvgpr_write_b32 a0, v80\nv_accvgpr_write_b32 a1, v81\nv_accvgpr_write_b32 a2, v82\nv_accvgpr_write_b32 a3, v83\nv_mfma_f32_16x16x4_4b_bf16 v[224:239], v[30:31], v[58:59], v[224:239]\n"
"Qk 104,104,105\nQk 105,106,107\nQk 106,108,109\nQk 107,110,111\nQk 88,88,89\nQk 89,90,91\nQzvvv 108,104,0\nQk 90,92,93\n"
"Qk 91,94,95\nQzvvv 104,104,2\nv_accvgpr_write_b32 a4, v88\nv_accvgpr_write_b32 a5, v89\nv_accvgpr_write_b32 a6, v90\nv_accvgpr_write_b32 a7, v91\nv_mfma_f32_16x16x4_4b_bf16 v[240:255], v[30:31], v[58:59], v[240:255]\nQk 112,112,113\n"
"Qk 113,114,115\nQk 114,116,117\nQk 115,118,119\nQk 96,96,97\nQk 97,98,99\nQzvvv 116,112,4\nQk 98,100,101\nQk 99,102,103\n"
"Qzvvv 112,112,6\nv_accvgpr_write_b32 a8, v96\nv_accvgpr_write_b32 a9, v97\nv_accvgpr_write_b32 a10, v98\nv_accvgpr_write_b32 a11, v99\nv_mfma_f32_16x16x4_4b_bf16 a[208:223], v[30:31], v[58:59], a[208:223]\nQk 120,120,121\nQk 121,122,123\n"
"Qk 122,124,125\nQk 123,126,127\nQk 104,104,105\nQk 105,106,107\nQzvvv 124,120,4\nQk 106,108,109\nQk 107,110,111\nQzvvv 120,120,6\n"
"v_accvgpr_write_b32 a12, v104\nv_accvgpr_write_b32 a13, v105\nv_accvgpr_write_b32 a14, v106\nv_accvgpr_write_b32 a15, v107\nv_mfma_f32_16x16x4_4b_bf16 a[224:239], v[30:31], v[58:59], a[224:239]\nQk 128,128,129\nQk 129,130,131\nQk 130,132,133\n"
"Qk 131,134,135\nQk 112,112,113\nQk 113,114,115\nQzvvv 132,128,4\nQk 114,116,117\nQk 115,118,119\nQzvvv 128,128,6\nv_accvgpr_write_b32 a16, v112\n"
"v_accvgpr_write_b32 a17, v113\nv_accvgpr_write_b32 a18, v114\nv_accvgpr_write_b32 a19, v115\nv_mfma_f32_16x16x4_4b_bf16 a[240:255], v[30:31], v[58:59], a[240:255]\nQk 136,136,137\nQk 137,138,139\nQk 138,140,141\nQk 139,142,143\n"
"Qk 120,120,121\nQk 121,122,123\nQzvvv 140,136,4\nQk 122,124,125\nQk 123,126,127\nQzvvv 136,136,6\nv_accvgpr_write_b32 a20, v120\nv_accvgpr_write_b32 a21, v121\n"
"v_accvgpr_write_b32 a22, v122\nv_accvgpr_write_b32 a23, v123\nQk 144,144,145\nQk 145,146,147\nQk 146,148,149\nQk 147,150,151\nQk 128,128,129\nQk 129,130,131\n"
"Qzvvv 148,144,8\nQk 130,132,133\nQk 131,134,135\nQzvvv 144,144,10\nv_accvgpr_write_b32 a24, v128\nv_accvgpr_write_b32 a25, v129\nv_accvgpr_write_b32 a26, v130\nv_accvgpr_write_b32 a27, v131\n"
"Qk 152,152,153\nQk 153,154,155\nQk 154,156,157\nQk 155,158,159\nQk 136,136,137\nQk 137,138,139\nQzvvv 156,152,8\nQk 138,140,141\n"
"Qk 139,142,143\nQzvvv 152,152,10\nv_accvgpr_write_b32 a28, v136\nv_accvgpr_write_b32 a29, v137\nv_accvgpr_write_b32 a30, v138\nv_accvgpr_write_b32 a31, v139\nQk 160,160,161\nQk 161,162,163\n"
"Qk 162,164,165\nQk 163,166,167\nQk 144,144,145\nQk 145,146,147\nQzvvv 164,160,8\nQk 146,148,149\nQk 147,150,151\nQzvvv 160,160,10\n"
"v_accvgpr_write_b32 a32, v144\nv_accvgpr_write_b32 a33, v145\nv_accvgpr_write_b32 a34, v146\nv_accvgpr_write_b32 a35, v147\nQk 168,168,169\nQk 169,170,171\nQk 170,172,173\nQk 171,174,175\n"
"Qk 152,152,153\nQk 153,154,155\nQzvvv 172,168,8\nQk 154,156,157\nQk 155,158,159\nQzvvv 168,168,10\nv_accvgpr_write_b32 a36, v152\nv_accvgpr_write_b32 a37, v153\n"
"v_accvgpr_write_b32 a38, v154\nv_accvgpr_write_b32 a39, v155\nQk 176,176,177\nQk 177,178,179\nQk 178,180,181\nQk 179,182,183\nQk 160,160,161\nQk 161,162,163\n"
"Qzvvv 180,176,12\nQk 162,164,165\nQk 163,166,167\nQzvvv 176,176,14\nv_accvgpr_write_b32 a40, v160\nv_accvgpr_write_b32 a41, v161\nv_accvgpr_write_b32 a42, v162\nv_accvgpr_write_b32 a43, v163\n"
"Qk 184,184,185\nQk 185,186,187\nQk 186,188,189\nQk 187,190,191\nQk 168,168,169\nQk 169,170,171\nQzvvv 188,184,12\nQk 170,172,173\n"
"Qk 171,174,175\nQzvvv 184,184,14\nv_accvgpr_write_b32 a44, v168\nv_accvgpr_write_b32 a45, v169\nv_accvgpr_write_b32 a46, v170\nv_accvgpr_write_b32 a47, v171\nQk 192,192,193\nQk 193,194,195\n"
"Qk 194,196,197\nQk 195,198,199\nQk 176,176,177\nQk 177,178,179\nQzvvv 196,192,12\nQk 178,180,181\nQk 179,182,183\nQzvvv 192,192,14\n"
"v_accvgpr_write_b32 a48, v176\nv_accvgpr_write_b32 a49, v177\nv_accvgpr_write_b32 a50, v178\nv_accvgpr_write_b32 a51, v179\nQk 200,200,201\nQk 201,202,203\nQk 202,204,205\nQk 203,206,207\n"
"Qk 184,184,185\nQk 185,186,187\nQzvvv 204,200,12\nQk 186,188,189\nQk 187,190,191\nQzvvv 200,200,14\nv_accvgpr_write_b32 a52, v184\nv_accvgpr_write_b32 a53, v185\n"
"v_accvgpr_write_b32 a54, v186\nv_accvgpr_write_b32 a55, v187\nQk 208,208,209\nQk 209,210,211\nQk 210,212,213\nQk 211,214,215\nQk 192,192,193\nQk 193,194,195\n"
"Qzvvv 212,208,16\nQk 194,196,197\nQk 195,198,199\nQzvvv 208,208,18\nv_accvgpr_write_b32 a56, v192\nv_accvgpr_write_b32 a57, v193\nv_accvgpr_write_b32 a58, v194\nv_accvgpr_write_b32 a59, v195\n"
"Qk 216,216,217\nQk 217,218,219\nQk 218,220,221\nQk 219,222,223\nQk 200,200,201\nQk 201,202,203\nQzvvv 220,216,16\nQk 202,204,205\n"
"Qk 203,206,207\nQzvvv 216,216,18\nv_accvgpr_write_b32 a60, v200\nv_accvgpr_write_b32 a61, v201\nv_accvgpr_write_b32 a62, v202\nv_accvgpr_write_b32 a63, v203\nQk 224,224,225\nQk 225,226,227\n"
"Qk 226,228,229\nQk 227,230,231\nQk 208,208,209\nQk 209,210,211\nQzvvv 228,224,16\nQk 210,212,213\nQk 211,214,215\nQzvvv 224,224,18\n"
"v_accvgpr_write_b32 a64, v208\nv_accvgpr_write_b32 a65, v209\nv_accvgpr_write_b32 a66, v210\nv_accvgpr_write_b32 a67, v211\nQk 232,232,233\nQk 233,234,235\nQk 234,236,237\nQk 235,238,239\n"
"Qk 216,216,217\nQk 217,218,219\nQzvvv 236,232,16\nQk 218,220,221\nQk 219,222,223\nQzvvv 232,232,18\nv_accvgpr_write_b32 a68, v216\nv_accvgpr_write_b32 a69, v217\n"
"v_accvgpr_write_b32 a70, v218\nv_accvgpr_write_b32 a71, v219\nQk 240,240,241\nQk 241,242,243\nQk 242,244,245\nQk 243,246,247\nQk 224,224,225\nQk 225,226,227\n"
"Qzvvv 244,240,20\nQk 226,228,229\nQk 227,230,231\nQzvvv 240,240,22\nv_accvgpr_write_b32 a72, v224\nv_accvgpr_write_b32 a73, v225\nv_accvgpr_write_b32 a74, v226\nv_accvgpr_write_b32 a75, v227\n"
"Qk 248,248,249\nQk 249,250,251\nQk 250,252,253\nQk 251,254,255\nQk 232,232,233\nQk 233,234,235\nQzvvv 252,248,20\nQk 234,236,237\n"
"Qk 235,238,239\nQzvvv 248,248,22\nv_accvgpr_write_b32 a76, v232\nv_accvgpr_write_b32 a77, v233\nv_accvgpr_write_b32 a78, v234\nv_accvgpr_write_b32 a79, v235\nv_accvgpr_read_b32 v48, a208\nv_accvgpr_read_b32 v49, a209\n"
"v_accvgpr_read_b32 v50, a210\nv_accvgpr_read_b32 v51, a211\nv_accvgpr_read_b32 v36, a212\nv_accvgpr_read_b32 v37, a213\nv_accvgpr_read_b32 v38, a214\nv_accvgpr_read_b32 v39, a215\nQk 48,48,49\nQk 49,50,51\n"
"Qk 50,36,37\nQk 51,38,39\nQk 240,240,241\nQk 241,242,243\nQzvvv 36,48,20\nQk 242,244,245\nQk 243,246,247\nQzvvv 48,48,22\n"
"v_accvgpr_write_b32 a80, v240\nv_accvgpr_write_b32 a81, v241\nv_accvgpr_write_b32 a82, v242\nv_accvgpr_write_b32 a83, v243\nv_accvgpr_read_b32 v52, a216\nv_accvgpr_read_b32 v53, a217\nv_accvgpr_read_b32 v54, a218\nv_accvgpr_read_b32 v55, a219\n"
"v_accvgpr_read_b32 v40, a220\nv_accvgpr_read_b32 v41, a221\nv_accvgpr_read_b32 v42, a222\nv_accvgpr_read_b32 v43, a223\nQk 52,52,53\nQk 53,54,55\nQk 54,40,41\nQk 55,42,43\n"
"Qk 248,248,249\nQk 249,250,251\nQzvvv 40,52,20\nQk 250,252,253\nQk 251,254,255\nQzvvv 52,52,22\nv_accvgpr_write_b32 a84, v248\nv_accvgpr_write_b32 a85, v249\n"
"v_accvgpr_write_b32 a86, v250\nv_accvgpr_write_b32 a87, v251\nv_accvgpr_read_b32 v44, a224\nv_accvgpr_read_b32 v45, a225\nv_accvgpr_read_b32 v46, a226\nv_accvgpr_read_b32 v47, a227\nv_accvgpr_read_b32 v32, a228\nv_accvgpr_read_b32 v33, a229\n"
"v_accvgpr_read_b32 v34, a230\nv_accvgpr_read_b32 v35, a231\nQk 44,44,45\nQk 45,46,47\nQk 46,32,33\nQk 47,34,35\nQk 48,48,49\nQk 49,50,51\n"
"Qzvvv 32,44,24\nQk 50,36,37\nQk 51,38,39\nQzvvv 44,44,26\nv_accvgpr_write_b32 a88, v48\nv_accvgpr_write_b32 a89, v49\nv_accvgpr_write_b32 a90, v50\nv_accvgpr_write_b32 a91, v51\n"
"v_accvgpr_read_b32 v48, a232\nv_accvgpr_read_b32 v49, a233\nv_accvgpr_read_b32 v50, a234\nv_accvgpr_read_b32 v51, a235\nv_accvgpr_read_b32 v36, a236\nv_accvgpr_read_b32 v37, a237\nv_accvgpr_read_b32 v38, a238\nv_accvgpr_read_b32 v39, a239\n"
".Lpg9:\ns_cbranch_execz .Ltr10\nQk 48,48,49\nQk 49,50,51\nQk 50,36,37\nQk 51,38,39\nQk 52,52,53\nQk 53,54,55\n"
"Qzvvv 36,48,24\nQk 54,40,41\nQk 55,42,43\nQzvvv 48,48,26\nv_accvgpr_write_b32 a92, v52\nv_accvgpr_write_b32 a93, v53\nv_accvgpr_write_b32 a94, v54\nv_accvgpr_write_b32 a95, v55\n"
"v_accvgpr_read_b32 v52, a240\nv_accvgpr_read_b32 v53, a241\nv_accvgpr_read_b32 v54, a242\nv_accvgpr_read_b32 v55, a243\nv_accvgpr_read_b32 v40, a244\nv_accvgpr_read_b32 v41, a245\nv_accvgpr_read_b32 v42, a246\nv_accvgpr_read_b32 v43, a247\n"
"Qk 52,52,53\nQk 53,54,55\nQk 54,40,41\nQk 55,42,43\nQk 44,44,45\nQk 45,46,47\nQzvvv 40,52,24\nQk 46,32,33\n"
"Qk 47,34,35\nQzvvv 52,52,26\nv_accvgpr_write_b32 a96, v44\nv_accvgpr_write_b32 a97, v45\nv_accvgpr_write_b32 a98, v46\nv_accvgpr_write_b32 a99, v47\nv_accvgpr_read_b32 v44, a248\nv_accvgpr_read_b32 v45, a249\n"
"v_accvgpr_read_b32 v46, a250\nv_accvgpr_read_b32 v47, a251\nv_accvgpr_read_b32 v32, a252\nv_accvgpr_read_b32 v33, a253\nv_accvgpr_read_b32 v34, a254\nv_accvgpr_read_b32 v35, a255\nQk 44,44,45\nQk 45,46,47\n"
"Qk 46,32,33\nQk 47,34,35\nQk 48,48,49\nQk 49,50,51\nQzvvv 32,44,24\nQk 50,36,37\nQk 51,38,39\nQzvvv 44,44,26\n"
"v_accvgpr_write_b32 a100, v48\nv_accvgpr_write_b32 a101, v49\nv_accvgpr_write_b32 a102, v50\nv_accvgpr_write_b32 a103, v51\nQk 52,52,53\nQk 53,54,55\nQk 54,40,41\nQk 55,42,43\n"
"v_accvgpr_write_b32 a104, v52\nv_accvgpr_write_b32 a105, v53\nv_accvgpr_write_b32 a106, v54\nv_accvgpr_write_b32 a107, v55\nQk 44,44,45\nQk 45,46,47\nQk 46,32,33\nQk 47,34,35\n"
"v_accvgpr_write_b32 a108, v44\nv_accvgpr_write_b32 a109, v45\nv_accvgpr_write_b32 a110, v46\nv_accvgpr_write_b32 a111, v47\ns_mov_b32 s56, %8\ns_and_b32 s57, %9, 0xffff\ns_mov_b32 s94, 0x7ffff000\ns_mul_i32 s68, %20, 40960\n"
"s_lshl_b32 s69, s70, 10\ns_add_u32 s68, s68, s69\ns_add_u32 s68, s68, 4096\ns_add_u32 s94, %15, s68\ns_addc_u32 s95, %16, 0\ns_and_b32 s95, s95, 0xffff\ns_mov_b32 s75, 37888\ns_mov_b32 s58, 4194304\n"
"s_mov_b32 s67, 1\ns_mov_b64 exec, 0\ns_cmp_eq_u32 s70, 0\ns_cbranch_scc1 .Lpg12\ns_cmp_eq_u32 s70, 1\ns_cbranch_scc1 .Lpg13\ns_cmp_eq_u32 s70, 2\ns_cbranch_scc1 .Lpg14\n"
"s_branch .Lpg15\n.Ltr6:\ns_mov_b64 exec, -1\ns_branch .Lround\n.Lhook:\n.Lpg10:\ns_cbranch_execz .Ltr10\ns_cmp_eq_u32 s66, 1\n"
"s_cbranch_scc1 .Lh1\ns_cmp_eq_u32 s66, 5\ns_cbranch_scc1 .Lh5\ns_cmp_eq_u32 s66, 6\ns_cbranch_scc1 .Lh6\ns_cmp_eq_u32 s66, 9\ns_cbranch_scc1 .Lh9\ns_lshr_b32 s68, s99, s66\n"
"s_cmp_eq_u32 s68, 1\ns_cbranch_scc1 .Lh12\ns_branch .Lh11\n.Lh1:\nds_read_b32 v78, v74 offset:16\ns_cmp_eq_u32 s70, 0\ns_cselect_b32 s68, 128, 0\nv_mov_b32 v66, 0x7ffff000\n"
"v_cmp_gt_u32 vcc, s68, v60\nv_not_b32 v67, v65\ns_lshl_b32 s68, %19, 2\nv_cndmask_b32 v66, v66, v76, vcc\nv_readlane_b32 s30, v67, %19\ns_waitcnt lgkmcnt(0)\ns_nop 1\nv_add_u32_dpp v78, v78, v78 quad_perm:[1,0,3,2] row_mask:0xf bank_mask:0xf\n"
"s_nop 1\nv_add_u32_dpp v78, v78, v78 quad_perm:[2,3,0,1] row_mask:0xf bank_mask:0xf\nv_ashrrev_i32 v78, 2, v78\nv_med3_i32 v78, v78, -7, 7\nv_add_lshl_u32 v78, v78, 8, 28\nv_xor_b32 v67, v67, v78\nbuffer_store_dword v67, v66, s[24:27], s68 offen offset:1024 sc1\ns_branch .Lhookret\n"
".Lh9:\ns_lshl_b32 s68, %19, 4\ns_cmp_eq_u32 s70, 0\ns_cselect_b32 s68, s68, -1\nv_mov_b32 v67, 0x7ffff000\nv_cmp_eq_u32 vcc, s68, v60\ns_nop 1\nv_cndmask_b32 v67, v67, v76, vcc\n"
"v_add_u32 v66, 1, v65\nbuffer_store_dword v66, v67, s[24:27], 0 offen sc1\ns_branch .Lhookret\n.Lh5:\nv_bfe_u32 v66, v60, 4, 3\ns_lshl_b32 s68, %19, 7\nv_lshlrev_b32 v66, 2, v66\ns_add_u32 s68, s68, 5242880\n"
"buffer_load_dword v78, v66, s[24:27], s68 offen offset:1024 sc1\ns_branch .Lhookret\n.Lh6:\ns_waitcnt vmcnt(32)\ns_mov_b32 s97, 0\n.Lack_l:\nv_xor_b32 v78, s30, v78\nv_and_b32 v66, 0x0fffffff, v78\n"
"v_cmp_ne_u32 vcc, 0, v66\ns_cmp_eq_u64 vcc, 0\ns_cbranch_scc1 .Lack_ok\ns_sleep 1\nv_bfe_u32 v66, v60, 4, 3\ns_lshl_b32 s68, %19, 7\nv_lshlrev_b32 v66, 2, v66\ns_add_u32 s68, s68, 5242880\n"
"buffer_load_dword v78, v66, s[24:27], s68 offen offset:1024 sc1\ns_waitcnt vmcnt(0)\ns_add_u32 s97, s97, 1\ns_cmp_lt_u32 s97, 1048576\ns_cbranch_scc1 .Lack_l\n.Lack_ok:\nv_lshrrev_b32 v78, 28, v78\ns_nop 1\n"
"v_add_u32_dpp v78, v78, v78 quad_perm:[1,0,3,2] row_mask:0xf bank_mask:0xf\ns_nop 1\nv_add_u32_dpp v78, v78, v78 quad_perm:[2,3,0,1] row_mask:0xf bank_mask:0xf\ns_nop 1\nv_readlane_b32 s68, v78, %19\nv_readlane_b32 s69, v78, 4\ns_cmp_lt_u32 s68, 32\ns_cselect_b32 s68, 8192, 0\n"
"s_xor_b32 s68, s68, s98\ns_add_u32 s71, s71, s68\ns_cmp_lt_u32 s69, 32\ns_cselect_b32 s69, 8, 0\ns_xor_b32 s69, s69, %20\ns_and_b32 s69, s69, 8\ns_lshl_b32 s98, s69, 10\ns_branch .Lhookret\n"
".Lh11:\nbuffer_load_dword v78, v76, s[24:27], 0 offen sc1\ns_branch .Lhookret\n.Lh12:\ns_waitcnt vmcnt(32)\nv_sub_u32 v66, v78, v65\nv_cmp_gt_u32 vcc, 1, v66\ns_and_b64 vcc, vcc, s[28:29]\n"
"s_cbranch_scc1 .Lf1_w\n.Lf1_r:\ns_cmp_lg_u32 s70, 3\ns_cbranch_scc1 .Lf1_n\nbuffer_inv sc1\n.Lf1_n:\ns_lshr_b32 s88, %19, 2\ns_lshl_b32 s88, s88, 4\n"
"s_add_u32 s88, s88, s93\ns_andn2_b32 s68, s71, 786432\ns_add_u32 s48, %8, s68\ns_addc_u32 s49, %9, 0\ns_and_b32 s49, s49, 0xffff\ns_sub_u32 s50, 4194304, s68\ns_mul_i32 s68, s88, 163840\ns_and_b32 s69, %19, 3\n"
"s_mul_i32 s69, s69, 320\ns_add_u32 s68, s68, s69\ns_add_u32 s52, %15, s68\ns_addc_u32 s53, %16, 0\ns_and_b32 s53, s53, 0xffff\ns_sub_u32 s54, 5246976, s68\ns_add_u32 s71, s71, 1048576\ns_and_b32 s87, %19, 3\n"
"s_mul_i32 s87, s87, 327680\ns_add_u32 s87, s87, 1310720\ns_mul_i32 s69, s92, 10240\nv_bfe_u32 v66, v60, 5, 3\nv_mul_u32_u24 v66, 1280, v66\nv_and_b32 v67, 16, v60\nv_add3_u32 v67, v67, v66, s69\ns_and_b32 s69, %19, 1\n"
"v_lshrrev_b32 v66, 8, v60\ns_lshl_b32 s68, s93, 1\nv_xor_b32 v66, s68, v66\nv_lshl_add_u32 v66, v66, 5, v67\ns_lshr_b32 s68, 128, s69\nv_add_u32 v61, s68, v66\ns_mul_i32 s68, s69, 192\nv_add_u32 v63, s68, v66\n"
"v_bfe_u32 v66, v60, 8, 1\nv_xor_b32 v66, s93, v66\nv_lshl_add_u32 v67, v66, 5, v67\nv_lshrrev_b32 v66, 9, v60\nv_mul_u32_u24 v66, 20480, v66\ns_xor_b32 s69, s69, 1\ns_lshl_b32 s69, s69, 8\nv_add3_u32 v62, v66, v67, s69\n"
"s_branch .Lhookret\n.Lf1_w:\ns_mov_b32 s97, 0\n.Lf1_l:\ns_sleep 1\nbuffer_load_dword v78, v76, s[24:27], 0 offen sc1\ns_waitcnt vmcnt(0)\ns_add_u32 s97, s97, 1\n"
"s_cmp_gt_u32 s97, 1048576\ns_cbranch_scc1 .Lf1_r\nv_sub_u32 v66, v78, v65\nv_cmp_gt_u32 vcc, 1, v66\ns_and_b64 vcc, vcc, s[28:29]\ns_cbranch_scc1 .Lf1_l\ns_branch .Lf1_r\n.Lsa1:\n"
"ds_read_b32 v66, v74\ns_waitcnt lgkmcnt(0)\nv_cmp_gt_u32 vcc, v75, v66\ns_cbranch_vccnz .Lsa1\ns_branch .Lra1\n.Lsa2:\nds_read_b32 v66, v74\ns_waitcnt lgkmcnt(0)\n"
"v_cmp_gt_u32 vcc, v75, v66\ns_cbranch_vccnz .Lsa2\ns_branch .Lra2\n.Lsp2:\ns_sub_u32 s75, s77, s74\ns_cmp_lt_u32 s75, 1024\ns_cbranch_scc0 .Lrp2\ns_memrealtime s[74:75]\n"
"s_waitcnt lgkmcnt(0)\ns_branch .Lsp2\n.Lsa3:\nds_read_b32 v66, v74\ns_waitcnt lgkmcnt(0)\nv_cmp_gt_u32 vcc, v75, v66\ns_cbranch_vccnz .Lsa3\ns_branch .Lra3\n"
".Lsa4:\nds_read_b32 v66, v74\ns_waitcnt lgkmcnt(0)\nv_cmp_gt_u32 vcc, v75, v66\ns_cbranch_vccnz .Lsa4\ns_branch .Lra4\n.Lsp4:\ns_sub_u32 s75, s77, s74\n"
"s_cmp_lt_u32 s75, 1024\ns_cbranch_scc0 .Lrp4\ns_memrealtime s[74:75]\ns_waitcnt lgkmcnt(0)\ns_branch .Lsp4\n.Lsa5:\nds_read_b32 v66, v74\ns_waitcnt lgkmcnt(0)\n"
"v_cmp_gt_u32 vcc, v75, v66\ns_cbranch_vccnz .Lsa5\ns_branch .Lra5\n.Lsa30:\nds_read_b32 v66, v74\ns_waitcnt lgkmcnt(0)\nv_cmp_gt_u32 vcc, v75, v66\ns_cbranch_vccnz .Lsa30\n"
"s_branch .Lra30\n.Lsa31:\nds_read_b32 v66, v74\ns_waitcnt lgkmcnt(0)\nv_cmp_gt_u32 vcc, v75, v66\ns_cbranch_vccnz .Lsa31\ns_branch .Lra31\n.Lsa32:\n"
"ds_read_b32 v66, v74\ns_waitcnt lgkmcnt(0)\nv_cmp_gt_u32 vcc, v75, v66\ns_cbranch_vccnz .Lsa32\ns_branch .Lra32\n.Lsa30x:\nds_read_b32 v66, v74\ns_waitcnt lgkmcnt(0)\n"
"v_cmp_gt_u32 vcc, v75, v66\ns_cbranch_vccnz .Lsa30x\ns_branch .Lra30x\n.Lsa31x:\nds_read_b32 v66, v74\ns_waitcnt lgkmcnt(0)\nv_cmp_gt_u32 vcc, v75, v66\ns_cbranch_vccnz .Lsa31x\n"
"s_branch .Lra31x\n.Lso40:\nds_read_b32 v66, v74\ns_waitcnt lgkmcnt(0)\nv_cmp_gt_u32 vcc, v75, v66\ns_cbranch_vccnz .Lso40\ns_branch .Lro40\n.Lso180:\n"
"ds_read_b32 v66, v74\ns_waitcnt lgkmcnt(0)\nv_cmp_gt_u32 vcc, v75, v66\ns_cbranch_vccnz .Lso180\ns_branch .Lro180\n.Lso320:\nds_read_b32 v66, v74\ns_waitcnt lgkmcnt(0)\n"
"v_cmp_gt_u32 vcc, v75, v66\ns_cbranch_vccnz .Lso320\ns_branch .Lro320\n.Lso460:\nds_read_b32 v66, v74\ns_waitcnt lgkmcnt(0)\nv_cmp_gt_u32 vcc, v75, v66\ns_cbranch_vccnz .Lso460\n"
"s_branch .Lro460\n.Lso600:\nds_read_b32 v66, v74\ns_waitcnt lgkmcnt(0)\nv_cmp_gt_u32 vcc, v75, v66\ns_cbranch_vccnz .Lso600\ns_branch .Lro600\n.Lso740:\n"
"ds_read_b32 v66, v74\ns_waitcnt lgkmcnt(0)\nv_cmp_gt_u32 vcc, v75, v66\ns_cbranch_vccnz .Lso740\ns_branch .Lro740\n.Lso880:\nds_read_b32 v66, v74\ns_waitcnt lgkmcnt(0)\n"
"v_cmp_gt_u32 vcc, v75, v66\ns_cbranch_vccnz .Lso880\ns_branch .Lro880\n.Lso1020:\nds_read_b32 v66, v74\ns_waitcnt lgkmcnt(0)\nv_cmp_gt_u32 vcc, v75, v66\ns_cbranch_vccnz .Lso1020\n"
"s_branch .Lro1020\n.Lso1160:\nds_read_b32 v66, v74\ns_waitcnt lgkmcnt(0)\nv_cmp_gt_u32 vcc, v75, v66\ns_cbranch_vccnz .Lso1160\ns_branch .Lro1160\n.Lso1300:\n"
"ds_read_b32 v66, v74\ns_waitcnt lgkmcnt(0)\nv_cmp_gt_u32 vcc, v75, v66\ns_cbranch_vccnz .Lso1300\ns_branch .Lro1300\n.Lso1440:\nds_read_b32 v66, v74\ns_waitcnt lgkmcnt(0)\n"
"v_cmp_gt_u32 vcc, v75, v66\ns_cbranch_vccnz .Lso1440\ns_branch .Lro1440\n.Lso1455:\nds_read_b32 v66, v74\ns_waitcnt lgkmcnt(0)\nv_cmp_gt_u32 vcc, v75, v66\ns_cbranch_vccnz .Lso1455\n"
"s_branch .Lro1455\n.Lso1525r0:\nds_read_b32 v66, v74\ns_waitcnt lgkmcnt(0)\nv_cmp_gt_u32 vcc, v75, v66\ns_cbranch_vccnz .Lso1525r0\ns_branch .Lro1525r0\n.Lso1595r0:\n"
"ds_read_b32 v66, v74\ns_waitcnt lgkmcnt(0)\nv_cmp_gt_u32 vcc, v75, v66\ns_cbranch_vccnz .Lso1595r0\ns_branch .Lro1595r0\n.Lso1665r0:\nds_read_b32 v66, v74\ns_waitcnt lgkmcnt(0)\n"
"v_cmp_gt_u32 vcc, v75, v66\ns_cbranch_vccnz .Lso1665r0\ns_branch .Lro1665r0\n.Lso1735r0:\nds_read_b32 v66, v74\ns_waitcnt lgkmcnt(0)\nv_cmp_gt_u32 vcc, v75, v66\ns_cbranch_vccnz .Lso1735r0\n"
"s_branch .Lro1735r0\n.Lso1805r0:\nds_read_b32 v66, v74\ns_waitcnt lgkmcnt(0)\nv_cmp_gt_u32 vcc, v75, v66\ns_cbranch_vccnz .Lso1805r0\ns_branch .Lro1805r0\n.Lso1878r0:\n"
"ds_read_b32 v66, v74\ns_waitcnt lgkmcnt(0)\nv_cmp_gt_u32 vcc, v75, v66\ns_cbranch_vccnz .Lso1878r0\ns_branch .Lro1878r0\n.Lso1920r0:\nds_read_b32 v66, v74\ns_waitcnt lgkmcnt(0)\n"
"v_cmp_gt_u32 vcc, v75, v66\ns_cbranch_vccnz .Lso1920r0\ns_branch .Lro1920r0\n.Lso1960r0:\nds_read_b32 v66, v74\ns_waitcnt lgkmcnt(0)\nv_cmp_gt_u32 vcc, v75, v66\ns_cbranch_vccnz .Lso1960r0\n"
"s_branch .Lro1960r0\n.Lso2046r0:\nds_read_b32 v66, v74\ns_waitcnt lgkmcnt(0)\nv_cmp_gt_u32 vcc, v75, v66\ns_cbranch_vccnz .Lso2046r0\ns_branch .Lro2046r0\n.Lso2088r0:\n"
"ds_read_b32 v66, v74\ns_waitcnt lgkmcnt(0)\nv_cmp_gt_u32 vcc, v75, v66\ns_cbranch_vccnz .Lso2088r0\ns_branch .Lro2088r0\n.Lso2172r0:\nds_read_b32 v66, v74\ns_waitcnt lgkmcnt(0)\n"
"v_cmp_gt_u32 vcc, v75, v66\ns_cbranch_vccnz .Lso2172r0\ns_branch .Lro2172r0\n.Lso2222r0:\nds_read_b32 v66, v74\ns_waitcnt lgkmcnt(0)\nv_cmp_gt_u32 vcc, v75, v66\ns_cbranch_vccnz .Lso2222r0\n"
"s_branch .Lro2222r0\n.Lso1525r1:\nds_read_b32 v66, v74\ns_waitcnt lgkmcnt(0)\nv_cmp_gt_u32 vcc, v75, v66\ns_cbranch_vccnz .Lso1525r1\ns_branch .Lro1525r1\n.Lso1595r1:\n"
"ds_read_b32 v66, v74\ns_waitcnt lgkmcnt(0)\nv_cmp_gt_u32 vcc, v75, v66\ns_cbranch_vccnz .Lso1595r1\ns_branch .Lro1595r1\n.Lso1665r1:\nds_read_b32 v66, v74\ns_waitcnt lgkmcnt(0)\n"
"v_cmp_gt_u32 vcc, v75, v66\ns_cbranch_vccnz .Lso1665r1\ns_branch .Lro1665r1\n.Lso1735r1:\nds_read_b32 v66, v74\ns_waitcnt lgkmcnt(0)\nv_cmp_gt_u32 vcc, v75, v66\ns_cbranch_vccnz .Lso1735r1\n"
"s_branch .Lro1735r1\n.Lso1805r1:\nds_read_b32 v66, v74\ns_waitcnt lgkmcnt(0)\nv_cmp_gt_u32 vcc, v75, v66\ns_cbranch_vccnz .Lso1805r1\ns_branch .Lro1805r1\n.Lso1840r1:\n"
"ds_read_b32 v66, v74\ns_waitcnt lgkmcnt(0)\nv_cmp_gt_u32 vcc, v75, v66\ns_cbranch_vccnz .Lso1840r1\ns_branch .Lro1840r1\n.Lso1878r1:\nds_read_b32 v66, v74\ns_waitcnt lgkmcnt(0)\n"
"v_cmp_gt_u32 vcc, v75, v66\ns_cbranch_vccnz .Lso1878r1\ns_branch .Lro1878r1\n.Lso1920r1:\nds_read_b32 v66, v74\ns_waitcnt lgkmcnt(0)\nv_cmp_gt_u32 vcc, v75, v66\ns_cbranch_vccnz .Lso1920r1\n"
"s_branch .Lro1920r1\n.Lso1945r1:\nds_read_b32 v66, v74\ns_waitcnt lgkmcnt(0)\nv_cmp_gt_u32 vcc, v75, v66\ns_cbranch_vccnz .Lso1945r1\ns_branch .Lro1945r1\n.Lso2004r1:\n"
"ds_read_b32 v66, v74\ns_waitcnt lgkmcnt(0)\nv_cmp_gt_u32 vcc, v75, v66\ns_cbranch_vccnz .Lso2004r1\ns_branch .Lro2004r1\n.Lso2045r1:\nds_read_b32 v66, v74\ns_waitcnt lgkmcnt(0)\n"
"v_cmp_gt_u32 vcc, v75, v66\ns_cbranch_vccnz .Lso2045r1\ns_branch .Lro2045r1\n.Lso2130r1:\nds_read_b32 v66, v74\ns_waitcnt lgkmcnt(0)\nv_cmp_gt_u32 vcc, v75, v66\ns_cbranch_vccnz .Lso2130r1\n"
"s_branch .Lro2130r1\n.Lso2172r1:\nds_read_b32 v66, v74\ns_waitcnt lgkmcnt(0)\nv_cmp_gt_u32 vcc, v75, v66\ns_cbranch_vccnz .Lso2172r1\ns_branch .Lro2172r1\n.Lso2225r1:\n"
"ds_read_b32 v66, v74\ns_waitcnt lgkmcnt(0)\nv_cmp_gt_u32 vcc, v75, v66\ns_cbranch_vccnz .Lso2225r1\ns_branch .Lro2225r1\n.Ltail1:\n.Lpg11:\ns_cbranch_execz .Ltr10\n"
"Qcvaa 0,128,32,56\nv_add_u32 v75, 1, v75\nQmvaa 0,132,36\ns_waitcnt vmcnt(0)\nds_write_b32 v73, v75\nQmvaa 0,136,40\nQe 8\nQmvaa 0,140,44\n"
"Qe 9\nQcvaa 4,124,44,56\nQe 10\nQmvaa 4,120,40\nQe 11\nQmvaa 4,116,36\nQe 12\nQmvaa 4,112,32\n"
"s_waitcnt lgkmcnt(1)\nQk 40,8,9\nQmvva 96,44,144\nQe 13\nQmvva 96,36,148\nQe 14\nQmvva 100,44,152\nQe 15\n"
"Qmvva 100,36,156\nQk 41,10,11\nQk 42,12,13\nQmvva 104,44,160\nQk 43,14,15\nQmvva 104,36,164\nQe 0\nQmvva 108,44,168\n"
"Qe 1\nQmvva 108,36,172\nQe 2\nQmvva 140,48,168\nQe 3\nQmvva 140,40,172\nQk 46,0,1\nQk 47,2,3\n"
"Qmvva 136,48,160\nds_read_b32 v66, v74\nQmvva 136,40,164\nQe 4\nQmvva 132,48,152\nQe 5\nQmvva 132,40,156\nQe 6\n"
"Qmvva 128,48,144\nQe 7\nQmvva 128,40,148\ns_waitcnt lgkmcnt(0)\nv_cmp_gt_u32 vcc, v75, v66\nv_smfmac_f32_16x16x64_bf16 a[176:179], a[204:207], v[36:43], v77\ns_cbranch_vccnz .Lsa30x\n.Lra30x:\n"
"Qcvaa 8,112,48,56\ns_add_u32 m0, s96, 0\ns_add_u32 s95, s87, 4096\nQk 44,4,5\nQmvaa 8,116,52\nbuffer_load_dwordx4 v61, s[52:55], s95 offen lds\nQmvaa 8,120,56\nQk 45,6,7\n"
"Qmvaa 8,124,60\nQcvaa 12,140,60,56\nQmvaa 12,136,56\ns_add_u32 s95, s87, 23552\nQmvaa 12,132,52\nbuffer_load_dwordx4 v61, s[52:55], s95 offen offset:1024 lds\nQmvaa 12,128,48\nQcvaa 0,128,64,56\n"
"Qmvaa 0,132,68\nQe 8\nQmvaa 0,136,72\ns_add_u32 m0, s96, 8192\ns_add_u32 s95, s87, 45056\nQe 9\nQmvaa 0,140,76\nbuffer_load_dwordx4 v61, s[52:55], s95 offen lds\n"
"Qcvaa 4,124,76,56\nQe 10\nQmvaa 4,120,72\nQe 11\nQmvaa 4,116,68\nQe 12\nQmvaa 4,112,64\ns_add_u32 s95, s87, 64512\n"
"Qe 13\nQmvva 160,52,144\nbuffer_load_dwordx4 v61, s[52:55], s95 offen offset:1024 lds\nQmvva 160,44,148\nQe 14\nQmvva 164,52,152\nQe 15\nQmvva 164,44,156\n"
"Qk 48,8,9\nQk 49,10,11\nQmvva 168,52,160\ns_add_u32 m0, s96, 16384\ns_add_u32 s95, s87, 4096\nQk 50,12,13\nQmvva 168,44,164\nbuffer_load_dwordx4 v62, s[52:55], s95 offen lds\n"
"Qmvva 172,52,168\nQk 51,14,15\nQmvva 172,44,172\nQe 0\nQmvva 204,20,168\nQe 1\nQmvva 204,48,172\ns_add_u32 s95, s87, 44032\n"
"Qe 2\nQmvva 200,20,160\nbuffer_load_dwordx4 v62, s[52:55], s95 offen offset:1024 lds\nQmvva 200,48,164\nQe 3\nQmvva 196,20,152\nQk 18,0,1\nQk 19,2,3\n"
"Qmvva 196,48,156\nQe 4\nQmvva 192,20,144\ns_add_u32 m0, s96, 24576\ns_add_u32 s95, s87, 4096\nQe 5\nQmvva 192,48,148\nbuffer_load_dwordx4 v63, s[52:55], s95 offen lds\n"
"v_smfmac_f32_16x16x64_bf16 a[180:183], a[204:207], v[44:51], v77\nQk 16,4,5\nQcvaa 8,112,80,56\nQe 6\nQmvaa 8,116,84\nQe 7\nQmvaa 8,120,88\ns_add_u32 s95, s87, 23552\n"
"Qk 17,6,7\nQmvaa 8,124,92\nbuffer_load_dwordx4 v63, s[52:55], s95 offen offset:1024 lds\nQcvaa 12,140,92,56\nQmvaa 12,136,88\nQmvaa 12,132,84\nQmvaa 12,128,80\ns_add_u32 m0, s96, 32768\n"
"s_add_u32 s95, s87, 45056\nQcvaa 0,128,96,56\nQda 128,68,34048\nbuffer_load_dwordx4 v63, s[52:55], s95 offen lds\nQmvaa 0,132,100\nQda 132,68,34560\nQmvaa 0,136,104\nQda 136,68,34304\n"
"Qmvaa 0,140,108\nQda 140,68,34816\nQcvaa 4,124,108,56\nQda 124,68,33792\ns_add_u32 s95, s87, 64512\nQmvaa 4,120,104\nQda 120,68,33280\nbuffer_load_dwordx4 v63, s[52:55], s95 offen offset:1024 lds\n"
"Qmvaa 4,116,100\nQda 116,68,33536\nQmvaa 4,112,96\nQda 112,68,33024\nQmvva 224,24,144\nQe 8\nQmvva 224,16,148\ns_add_u32 m0, s96, 40960\n"
"s_mov_b32 s95, 4096\nQe 9\nQmvva 228,24,152\nbuffer_load_dwordx4 v61, s[52:55], s95 offen lds\nQe 10\nQmvva 228,16,156\nQe 11\nQmvva 232,24,160\n"
"Qk 20,8,9\nQk 21,10,11\nQmvva 232,16,164\nQe 12\nQmvva 236,24,168\ns_mov_b32 s95, 23552\nQe 13\nQmvva 236,16,172\n"
"buffer_load_dwordx4 v61, s[52:55], s95 offen offset:1024 lds\ns_waitcnt lgkmcnt(0)\nQk 22,12,13\nQcvaa 8,112,0,56\nQe 14\nQmvaa 8,116,4\nv_subrev_u32 v68, 66048, v68\nQmvaa 8,120,8\n"
"Qe 15\nQmvaa 8,124,12\ns_add_u32 m0, s96, 49152\ns_mov_b32 s95, 45056\n.Lpg12:\ns_cbranch_execz .Ltr6\nQk 23,14,15\nQcvaa 12,140,12,56\n"
"buffer_load_dwordx4 v61, s[52:55], s95 offen lds\nQe 0\nQmvaa 12,136,8\nQe 1\nQmvaa 12,132,4\nQe 2\nQmvaa 12,128,0\nQe 3\n"
"v_smfmac_f32_16x16x64_bf16 a[184:187], a[204:207], v[16:23], v77\ns_mov_b32 s95, 64512\nQk 26,0,1\nQmava 208,28,144\nbuffer_load_dwordx4 v61, s[52:55], s95 offen offset:1024 lds\nQe 4\nQmava 208,20,148\nQe 5\n"
"Qmava 212,28,152\nQe 6\nQmava 212,20,156\nQe 7\nQmava 216,28,160\ns_add_u32 m0, s96, 57344\ns_mov_b32 s95, 4096\nQk 27,2,3\n"
"Qk 24,4,5\nQmava 216,20,164\nbuffer_load_dwordx4 v62, s[52:55], s95 offen lds\nQk 25,6,7\nQmava 220,28,168\nQe 8\nQmava 220,20,172\nQe 9\n"
"Qmava 252,32,168\nQta 168,71,768\nQta 170,71,1792\nQmava 252,24,172\ns_mov_b32 s95, 44032\nQe 10\nQmava 248,32,160\nQta 172,71,33792\n"
"Qta 174,71,34816\nbuffer_load_dwordx4 v62, s[52:55], s95 offen offset:1024 lds\nQmava 248,24,164\nQe 11\nQmava 244,32,152\nQta 160,71,512\nQta 162,71,1536\nQmava 244,24,156\n"
"Qk 28,8,9\nQk 29,10,11\nQmava 240,32,144\nQta 164,71,33536\nQta 166,71,34560\ns_add_u32 m0, s96, 132096\ns_mov_b32 s95, 4096\nQmava 240,24,148\n"
"buffer_load_dwordx4 v63, s[52:55], s95 offen lds\nQe 12\nQcvaa 0,112,16,56\nQta 152,71,256\nQta 154,71,1280\nQmvaa 0,116,20\nQe 13\nQmvaa 0,120,24\n"
"Qta 156,71,33280\nQta 158,71,34304\nQmvaa 0,124,28\ns_mov_b32 s95, 23552\nQe 14\nQcvaa 4,140,28,56\nQta 144,71,0\nQta 146,71,1024\n"
"buffer_load_dwordx4 v63, s[52:55], s95 offen offset:1024 lds\nQmvaa 4,136,24\nQe 15\nQmvaa 4,132,20\nQta 148,71,33024\nQta 150,71,34048\nQmvaa 4,128,16\nQk 30,12,13\n"
"Qk 31,14,15\nQcvaa 8,128,32,56\nv_add_u32 v75, 1, v75\nQmvaa 8,132,36\nds_write_b32 v73, v75\nQmvaa 8,136,40\nQe 0\nQmvaa 8,140,44\n"
"Qe 1\nQcvaa 12,124,44,56\nQe 2\nQmvaa 12,120,40\nQe 3\nQmvaa 12,116,36\nQe 4\nQmvaa 12,112,32\n"
"s_waitcnt lgkmcnt(1)\nQk 32,0,1\nv_smfmac_f32_16x16x64_bf16 a[188:191], a[204:207], v[24:31], v77\nQk 33,2,3\nQmvva 80,36,144\nQe 5\nQmvva 80,28,148\nQe 6\n"
"Qmvva 84,36,152\nQe 7\nQmvva 84,28,156\nQk 34,4,5\nQk 35,6,7\nQmvva 88,36,160\nQe 8\nQmvva 88,28,164\n"
"Qe 9\nQmvva 92,36,168\nQe 10\nQmvva 92,28,172\nQe 11\nQmvva 124,40,168\nQk 38,8,9\nQk 39,10,11\n"
"Qmvva 124,32,172\nds_read_b32 v66, v74\nQmvva 120,40,160\nQe 12\nQmvva 120,32,164\nQe 13\nQmvva 116,40,152\nQe 14\n"
"Qmvva 116,32,156\nQe 15\nQmvva 112,40,144\ns_waitcnt lgkmcnt(0)\nv_cmp_gt_u32 vcc, v75, v66\nQmvva 112,32,148\ns_cbranch_vccnz .Lsa31x\n.Lra31x:\n"
"Qk 36,12,13\nQcvaa 0,112,48,56\ns_add_u32 m0, s96, 140288\ns_mov_b32 s95, 45056\nQk 37,14,15\nQmvaa 0,116,52\nbuffer_load_dwordx4 v63, s[52:55], s95 offen lds\nQmvaa 0,120,56\n"
"Qmvaa 0,124,60\nQcvaa 4,140,60,56\nQmvaa 4,136,56\ns_mov_b32 s95, 64512\nQmvaa 4,132,52\nbuffer_load_dwordx4 v63, s[52:55], s95 offen offset:1024 lds\nQmvaa 4,128,48\nQcvaa 8,128,64,56\n"
"Qmvaa 8,132,68\nQe 0\nQmvaa 8,136,72\ns_add_u32 m0, s96, 148480\ns_add_u32 s95, s87, 86016\nQe 1\nQmvaa 8,140,76\nbuffer_load_dwordx4 v61, s[52:55], s95 offen lds\n"
"Qcvaa 12,124,76,56\nQe 2\nQmvaa 12,120,72\nQe 3\nQmvaa 12,116,68\nQe 4\nQmvaa 12,112,64\nQe 5\n"
"v_smfmac_f32_16x16x64_bf16 a[192:195], a[204:207], v[32:39], v77\ns_add_u32 s95, s87, 105472\nQk 40,0,1\nQmvva 144,44,144\nbuffer_load_dwordx4 v61, s[52:55], s95 offen offset:1024 lds\nQmvva 144,36,148\nQe 6\nQmvva 148,44,152\n"
"Qe 7\nQmvva 148,36,156\ns_add_u32 m0, s96, 65536\ns_add_u32 s95, s87, 126976\nQk 41,2,3\nQmvva 152,44,160\nbuffer_load_dwordx4 v61, s[52:55], s95 offen lds\nQmvva 152,36,164\n"
"Qk 42,4,5\nQk 43,6,7\nQmvva 156,44,168\nQe 8\nQmvva 156,36,172\nQe 9\nQmvva 188,48,168\ns_add_u32 s95, s87, 146432\n"
"Qe 10\nQmvva 188,40,172\nbuffer_load_dwordx4 v61, s[52:55], s95 offen offset:1024 lds\nQmvva 184,48,160\nQe 11\nQmvva 184,40,164\nQk 46,8,9\nQk 47,10,11\n"
"Qmvva 180,48,152\nQe 12\nQmvva 180,40,156\ns_add_u32 m0, s96, 73728\ns_add_u32 s95, s87, 86016\nQe 13\nQmvva 176,48,144\nbuffer_load_dwordx4 v62, s[52:55], s95 offen lds\n"
"Qmvva 176,40,148\nQe 14\nQcvaa 0,112,80,56\nQe 15\nQmvaa 0,116,84\nQk 44,12,13\nQk 45,14,15\nQmvaa 0,120,88\n"
"s_add_u32 s95, s87, 125952\nQmvaa 0,124,92\nbuffer_load_dwordx4 v62, s[52:55], s95 offen offset:1024 lds\nQcvaa 4,140,92,56\nQmvaa 4,136,88\nQmvaa 4,132,84\nQmvaa 4,128,80\ns_add_u32 m0, s96, 81920\n"
"s_add_u32 s95, s87, 86016\nQcvaa 8,128,96,56\nbuffer_load_dwordx4 v63, s[52:55], s95 offen lds\nQmvaa 8,132,100\nQmvaa 8,136,104\nQmvaa 8,140,108\nQcvaa 12,124,108,56\ns_add_u32 s95, s87, 105472\n"
"Qmvaa 12,120,104\nbuffer_load_dwordx4 v63, s[52:55], s95 offen offset:1024 lds\nQmvaa 12,116,100\nQmvaa 12,112,96\nv_smfmac_f32_16x16x64_bf16 a[196:199], a[204:207], v[40:47], v77\nQmvva 208,16,144\nQta 112,71,33152\ns_add_u32 m0, s96, 90112\n"
"s_add_u32 s95, s87, 126976\nQe 0\nQmvva 208,44,148\nQta 114,71,34176\nbuffer_load_dwordx4 v63, s[52:55], s95 offen lds\nQe 1\nQmvva 212,16,152\nQta 116,71,33408\n"
"Qe 2\nQmvva 212,44,156\nQta 118,71,34432\nQe 3\nQmvva 216,16,160\nQta 120,71,33664\nQk 48,0,1\nQk 49,2,3\n"
"Qmvva 216,44,164\nQta 122,71,34688\ns_add_u32 s95, s87, 146432\nQe 4\nQmvva 220,16,168\nQta 124,71,33920\nbuffer_load_dwordx4 v63, s[52:55], s95 offen offset:1024 lds\nQe 5\n"
"Qmvva 220,44,172\nQta 126,71,34944\nQk 50,4,5\nQe 6\nQe 7\ns_nop 0\nQk 51,6,7\nQe 8\n"
"Qe 9\nQe 10\nQe 11\nQe 12\nQmvva 240,20,144\nQe 13\nQmvva 240,48,148\nQe 14\n"
"Qmvva 244,20,152\nQe 15\nQmvva 244,48,156\nQk 54,8,9\nQk 55,10,11\nQmvva 248,20,160\nQk 52,12,13\nQk 53,14,15\n"
"Qmvva 248,48,164\nQmvva 252,20,168\nQmvva 252,48,172\nv_smfmac_f32_16x16x64_bf16 a[200:203], a[204:207], v[48:55], v77\nQmava 224,24,144\nv_accvgpr_read_b32 v0, a176\nv_accvgpr_read_b32 v1, a177\nQmava 224,52,148\n"
"v_accvgpr_read_b32 v6, a178\nv_accvgpr_read_b32 v7, a179\nQmava 228,24,152\nv_accvgpr_read_b32 v8, a180\nv_accvgpr_read_b32 v9, a181\nQmava 228,52,156\nv_accvgpr_read_b32 v14, a182\nv_accvgpr_read_b32 v15, a183\n"
"Qmava 232,24,160\nv_accvgpr_read_b32 v16, a184\nv_accvgpr_read_b32 v17, a185\nQmava 232,52,164\nv_accvgpr_read_b32 v22, a186\nv_accvgpr_read_b32 v23, a187\nQmava 236,24,168\nv_accvgpr_read_b32 v2, a190\n"
"v_accvgpr_read_b32 v3, a191\nQmava 236,52,172\ns_waitcnt lgkmcnt(0)\nv_accvgpr_read_b32 v24, a188\nQmvva 96,28,112\nv_accvgpr_read_b32 v25, a189\nv_accvgpr_read_b32 v4, a192\nQmvva 100,28,116\n"
"v_accvgpr_read_b32 v5, a193\nv_accvgpr_read_b32 v10, a194\nQmvva 104,28,120\nv_accvgpr_read_b32 v11, a195\nv_accvgpr_read_b32 v12, a196\nQmvva 108,28,124\nv_accvgpr_read_b32 v13, a197\nv_accvgpr_read_b32 v18, a198\n"
"Qmvva 128,32,112\nv_accvgpr_read_b32 v19, a199\nv_accvgpr_read_b32 v20, a200\nQmvva 132,32,116\nv_accvgpr_read_b32 v21, a201\nv_accvgpr_read_b32 v26, a202\nQmvva 136,32,120\nv_accvgpr_read_b32 v27, a203\n"
"Qa 8,8,9\nQmvva 140,32,124\nQa 10,10,11\nQa 8,8,10\nQmvva 160,36,112\nQa 16,16,17\nQa 18,18,19\nQmvva 164,36,116\n"
"Qa 16,16,18\nQa 4,4,5\nQmvva 168,36,120\nQa 6,6,7\nQa 4,4,6\nQmvva 172,36,124\nQa 12,12,13\nQa 14,14,15\n"
"Qmvva 192,40,112\nQa 12,12,14\nQa 0,0,1\nQmvva 196,40,116\nQa 2,2,3\nQa 0,0,2\nQmvva 200,40,120\nQa 24,24,25\n"
"Qa 26,26,27\nQmvva 204,40,124\nQa 24,24,26\nQa 20,20,21\nQmvva 224,44,112\nQa 22,22,23\nQa 20,20,22\nQmvva 228,44,116\n"
"v_max3_u32 v9, v8, v16, v4\nv_max3_u32 v9, v9, v12, v0\nQmvva 232,44,120\nv_max3_u32 v9, v9, v24, v20\nv_min3_u32 v11, v8, v16, v4\nQmvva 236,44,124\nv_min3_u32 v11, v11, v12, v0\nv_min3_u32 v11, v11, v24, v20\n"
"Qmava 208,48,112\nQmava 212,48,116\nQmava 216,48,120\nQmava 220,48,124\nQmava 240,52,112\nQmava 244,52,116\nQmava 248,52,120\nQmava 252,52,124\n"
"v_cmp_lt_u32 vcc, 0x4e800000, v9\ns_mov_b64 s[74:75], vcc\nv_cmp_gt_u32 vcc, 0x35800000, v11\ns_or_b64 s[74:75], s[74:75], vcc\ns_cmp_lg_u64 s[74:75], 0\ns_cbranch_scc1 .Lfb\n.Lfbret1:\ns_mov_b32 s56, %8\n"
"s_and_b32 s57, %9, 0xffff\ns_mov_b32 s58, 4194304\ns_lshl_b32 s68, %19, 4\ns_cmp_eq_u32 s70, 0\ns_cselect_b32 s68, s68, -1\nv_mov_b32 v77, 0x7ffff000\nv_cmp_eq_u32 vcc, s68, v60\ns_nop 1\n"
"v_cndmask_b32 v77, v77, v76, vcc\ns_lshl_b32 s68, s93, 12\ns_add_u32 s68, s68, %10\nv_lshrrev_b32 v66, 9, v60\nv_lshlrev_b32 v66, 11, v66\nv_bfe_u32 v67, v60, 1, 8\nv_add3_u32 v68, v66, v67, s68\nv_add_u32 v69, 65536, v68\n"
"v_add_u32 v70, 131072, v68\ns_mov_b64 exec, 0\ns_cmp_eq_u32 s70, 0\ns_cbranch_scc1 .Lpg16\ns_cmp_eq_u32 s70, 1\ns_cbranch_scc1 .Lpg17\ns_cmp_eq_u32 s70, 2\ns_cbranch_scc1 .Lpg18\n"
"s_branch .Lpg19\n.Ltr8:\ns_mov_b64 exec, -1\nv_mov_b32 v30, s73\nv_mov_b32 v31, 0\nv_mov_b32 v58, 0xb9800000\nv_mov_b32 v59, 0\nv_rcp_f32 v2, v0\n"
"v_rcp_f32 v6, v4\nv_mfma_f32_16x16x4_4b_bf16 v[80:95], v[30:31], v[58:59], v[80:95]\nv_rcp_f32 v10, v8\nv_rcp_f32 v14, v12\nv_rcp_f32 v18, v16\nv_rcp_f32 v22, v20\nv_rcp_f32 v26, v24\nv_and_b32 v66, 16, v60\n"
"v_mfma_f32_16x16x4_4b_bf16 v[112:127], v[30:31], v[58:59], v[112:127]\nv_mov_b32 v67, 0xffff\nv_lshlrev_b32 v66, v66, v67\ns_mov_b32 vcc_lo, 0x00c0000c\ns_mov_b32 vcc_hi, 0xc0000c00\nv_cndmask_b32 v67, 0, v66, vcc\ns_lshr_b64 vcc, vcc, 2\nv_mfma_f32_16x16x4_4b_bf16 v[144:159], v[30:31], v[58:59], v[144:159]\n"
"v_cndmask_b32 v66, 0, v66, vcc\nv_mov_b64 v[0:1], 0\nQk 2,2,2\nv_and_b32 v3, v2, v67\nv_and_b32 v2, v2, v66\nv_mov_b64 v[4:5], 0\nv_mfma_f32_16x16x4_4b_bf16 v[176:191], v[30:31], v[58:59], v[176:191]\nQk 6,6,6\n"
"v_and_b32 v7, v6, v67\nv_and_b32 v6, v6, v66\nv_mov_b64 v[8:9], 0\nQk 10,10,10\nv_and_b32 v11, v10, v67\nv_mfma_f32_16x16x4_4b_bf16 v[208:223], v[30:31], v[58:59], v[208:223]\nv_and_b32 v10, v10, v66\nv_mov_b64 v[12:13], 0\n"
"Qk 14,14,14\nv_and_b32 v15, v14, v67\nv_and_b32 v14, v14, v66\nv_mov_b64 v[16:17], 0\nv_mfma_f32_16x16x4_4b_bf16 v[240:255], v[30:31], v[58:59], v[240:255]\nQk 18,18,18\nv_and_b32 v19, v18, v67\nv_and_b32 v18, v18, v66\n"
"v_mov_b64 v[20:21], 0\nQk 22,22,22\nv_and_b32 v23, v22, v67\nv_mfma_f32_16x16x4_4b_bf16 a[224:239], v[30:31], v[58:59], a[224:239]\nv_and_b32 v22, v22, v66\nv_mov_b64 v[24:25], 0\nQk 26,26,26\nv_and_b32 v27, v26, v67\n"
"v_and_b32 v26, v26, v66\nv_mov_b64 v[28:29], 0\nv_mfma_f32_16x16x4_4b_bf16 v[96:111], v[30:31], v[58:59], v[96:111]\nQk 80,80,81\nQk 81,82,83\nQk 82,84,85\nQk 83,86,87\ns_nop 1\n"
"Qzvvv 84,80,0\nQzvvv 80,80,2\nQk 112,112,113\nQk 113,114,115\nQk 114,116,117\nQk 115,118,119\ns_nop 1\nQzvvv 116,112,4\n"
"Qzvvv 112,112,6\nv_mfma_f32_16x16x4_4b_bf16 v[128:143], v[30:31], v[58:59], v[128:143]\nQk 144,144,145\nQk 145,146,147\nQk 146,148,149\nQk 147,150,151\nQk 80,80,81\nQk 81,82,83\n"
"Qzvvv 148,144,8\nQk 82,84,85\nQk 83,86,87\nQzvvv 144,144,10\nv_mfma_f32_16x16x4_4b_bf16 v[160:175], v[30:31], v[58:59], v[160:175]\nQk 176,176,177\nQk 177,178,179\nQk 178,180,181\n"
"Qk 179,182,183\nQk 112,112,113\nQk 113,114,115\nQzvvv 180,176,12\nQk 114,116,117\nQk 115,118,119\nQzvvv 176,176,14\nv_mfma_f32_16x16x4_4b_bf16 v[192:207], v[30:31], v[58:59], v[192:207]\n"
"Qk 208,208,209\nQk 209,210,211\nQk 210,212,213\nQk 211,214,215\nQk 144,144,145\nQk 145,146,147\nQzvvv 212,208,16\nQk 146,148,149\n"
"Qk 147,150,151\nQzvvv 208,208,18\nv_mfma_f32_16x16x4_4b_bf16 v[224:239], v[30:31], v[58:59], v[224:239]\nQk 240,240,241\nQk 241,242,243\nQk 242,244,245\nQk 243,246,247\nQk 176,176,177\n"
"Qk 177,178,179\nQzvvv 244,240,20\nQk 178,180,181\nQk 179,182,183\nQzvvv 240,240,22\nv_mfma_f32_16x16x4_4b_bf16 a[208:223], v[30:31], v[58:59], a[208:223]\nv_accvgpr_read_b32 v84, a224\nv_accvgpr_read_b32 v85, a225\n"
"v_accvgpr_read_b32 v86, a226\nv_accvgpr_read_b32 v87, a227\nv_accvgpr_read_b32 v32, a228\nv_accvgpr_read_b32 v33, a229\nv_accvgpr_read_b32 v34, a230\nv_accvgpr_read_b32 v35, a231\nQk 84,84,85\nQk 85,86,87\n"
"Qk 86,32,33\nQk 87,34,35\nQk 208,208,209\nQk 209,210,211\nQzvvv 32,84,24\nQk 210,212,213\nQk 211,214,215\nQzvvv 84,84,26\n"
"v_mfma_f32_16x16x4_4b_bf16 a[240:255], v[30:31], v[58:59], a[240:255]\nQk 88,88,89\nQk 89,90,91\nQk 90,92,93\nQk 91,94,95\nQk 240,240,241\nQk 241,242,243\nQzvvv 92,88,0\n"
"Qk 242,244,245\nQk 243,246,247\nQzvvv 88,88,2\nQk 120,120,121\nQk 121,122,123\nQk 122,124,125\nQk 123,126,127\nQk 84,84,85\n"
"Qk 85,86,87\nQzvvv 124,120,4\nQk 86,32,33\nQk 87,34,35\nQzvvv 120,120,6\nQk 152,152,153\nQk 153,154,155\nQk 154,156,157\n"
"Qk 155,158,159\nQk 88,88,89\nQk 89,90,91\nQzvvv 156,152,8\nQk 90,92,93\nQk 91,94,95\nQzvvv 152,152,10\ns_waitcnt vmcnt(20)\n"
"s_barrier\nQk 184,184,185\nQk 185,186,187\nQk 186,188,189\nQk 187,190,191\nQk 120,120,121\nQk 121,122,123\n.Lpg13:\n"
"s_cbranch_execz .Ltr6\nQzvvv 188,184,12\nQk 122,124,125\nQk 123,126,127\nQzvvv 184,184,14\nQta 56,68,0\nQk 216,216,217\nQk 217,218,219\n"
"Qk 218,220,221\nQk 219,222,223\nQk 152,152,153\nQk 153,154,155\nQzvvv 220,216,16\nQk 154,156,157\nQk 155,158,159\nQzvvv 216,216,18\n"
"Qta 58,68,1024\nQk 248,248,249\nQk 249,250,251\nQk 250,252,253\nQk 251,254,255\nQk 184,184,185\nQk 185,186,187\nQzvvv 252,248,20\n"
"Qk 186,188,189\nQk 187,190,191\nQzvvv 248,248,22\nQta 60,68,8192\nv_accvgpr_read_b32 v116, a232\nv_accvgpr_read_b32 v117, a233\nv_accvgpr_read_b32 v118, a234\nv_accvgpr_read_b32 v119, a235\n"
"v_accvgpr_read_b32 v36, a236\nv_accvgpr_read_b32 v37, a237\nv_accvgpr_read_b32 v38, a238\nv_accvgpr_read_b32 v39, a239\nQk 116,116,117\nQk 117,118,119\nQk 118,36,37\nQk 119,38,39\n"
"Qk 216,216,217\nQk 217,218,219\nQzvvv 36,116,24\nQk 218,220,221\nQk 219,222,223\nQzvvv 116,116,26\nQta 62,68,9216\ns_mov_b32 s31, 0\n"
"Qk 96,96,97\nQk 97,98,99\nQk 98,100,101\nQk 99,102,103\nQk 248,248,249\nQk 249,250,251\nQzvvv 100,96,0\nQk 250,252,253\n"
"Qk 251,254,255\nQzvvv 96,96,2\nbuffer_load_dwordx4 a[0:3], v79, s[48:51], s31 offen offset:0\nQta 64,68,256\nQk 128,128,129\nQk 129,130,131\nQk 130,132,133\nQk 131,134,135\n"
"Qk 116,116,117\nQk 117,118,119\nQzvvv 132,128,4\nQk 118,36,37\nQk 119,38,39\nQzvvv 128,128,6\nbuffer_load_dwordx4 a[4:7], v79, s[48:51], s31 offen offset:1024\nQta 66,68,1280\n"
"s_waitcnt lgkmcnt(2)\nQzvav 56,56,80\nQmvav 56,60,88\nQk 160,160,161\nQk 161,162,163\nQk 162,164,165\nQk 163,166,167\nQk 96,96,97\n"
"Qk 97,98,99\nQzvvv 164,160,8\nQk 98,100,101\nQk 99,102,103\nQzvvv 160,160,10\nbuffer_load_dwordx4 a[8:11], v79, s[48:51], s31 offen offset:2048\nQta 68,68,8448\nQk 192,192,193\n"
"Qk 193,194,195\nQk 194,196,197\nQk 195,198,199\nQk 128,128,129\nQk 129,130,131\nQzvvv 196,192,12\nQk 130,132,133\nQk 131,134,135\n"
"Qzvvv 192,192,14\nbuffer_load_dwordx4 a[12:15], v79, s[48:51], s31 offen offset:3072\nQta 70,68,9472\nv_max3_f32 v66, |v56|, |v57|, |v58|\nv_max_f32 v66, v66, |v59|\nv_mov_b32 v67, v66\ns_mov_b32 s31, 4096\nQk 224,224,225\n"
"Qk 225,226,227\nQk 226,228,229\nQk 227,230,231\nQk 160,160,161\nQk 161,162,163\nQzvvv 228,224,16\nQk 162,164,165\nQk 163,166,167\n"
"Qzvvv 224,224,18\nbuffer_load_dwordx4 a[16:19], v79, s[48:51], s31 offen offset:0\nv_permlane32_swap_b32 v66, v67\nv_max_f32 v66, v66, v67\nv_mov_b32 v67, v66\nv_accvgpr_read_b32 v148, a208\nv_accvgpr_read_b32 v149, a209\nv_accvgpr_read_b32 v150, a210\n"
"v_accvgpr_read_b32 v151, a211\nv_accvgpr_read_b32 v36, a212\nv_accvgpr_read_b32 v37, a213\nv_accvgpr_read_b32 v38, a214\nv_accvgpr_read_b32 v39, a215\nQk 148,148,149\nQk 149,150,151\nQk 150,36,37\n"
"Qk 151,38,39\nQk 192,192,193\nQk 193,194,195\nQzvvv 36,148,20\nQk 194,196,197\nQk 195,198,199\nQzvvv 148,148,22\nbuffer_load_dwordx4 a[20:23], v79, s[48:51], s31 offen offset:1024\n"
"v_permlane16_swap_b32 v66, v67\nv_max_f32 v66, v66, v67\nv_accvgpr_read_b32 v180, a240\nv_accvgpr_read_b32 v181, a241\nv_accvgpr_read_b32 v182, a242\nv_accvgpr_read_b32 v183, a243\nv_accvgpr_read_b32 v40, a244\nv_accvgpr_read_b32 v41, a245\n"
"v_accvgpr_read_b32 v42, a246\nv_accvgpr_read_b32 v43, a247\nQk 180,180,181\nQk 181,182,183\nQk 182,40,41\nQk 183,42,43\nQk 224,224,225\nQk 225,226,227\n"
"Qzvvv 40,180,24\nQk 226,228,229\nQk 227,230,231\nQzvvv 180,180,26\nbuffer_load_dwordx4 a[24:27], v79, s[48:51], s31 offen offset:2048\nv_max_f32_dpp v66, v66, v66 row_ror:8 row_mask:0xf bank_mask:0xf\ns_mov_b32 s31, 16384\nQk 104,104,105\n"
"Qk 105,106,107\nQk 106,108,109\nQk 107,110,111\nQk 148,148,149\nQk 149,150,151\nQzvvv 108,104,0\nQk 150,36,37\nQk 151,38,39\n"
"Qzvvv 104,104,2\nbuffer_load_dwordx4 a[28:31], v79, s[48:51], s31 offen offset:0\nv_max_f32_dpp v66, v66, v66 row_ror:4 row_mask:0xf bank_mask:0xf\nQk 136,136,137\nQk 137,138,139\nQk 138,140,141\nQk 139,142,143\nQk 180,180,181\n"
"Qk 181,182,183\nQzvvv 140,136,4\nQk 182,40,41\nQk 183,42,43\nQzvvv 136,136,6\nbuffer_load_dwordx4 a[32:35], v79, s[48:51], s31 offen offset:1024\nv_max_f32_dpp v66, v66, v66 row_ror:2 row_mask:0xf bank_mask:0xf\nQk 168,168,169\n"
"Qk 169,170,171\nQk 170,172,173\nQk 171,174,175\nQk 104,104,105\nQk 105,106,107\nQzvvv 172,168,8\nQk 106,108,109\nQk 107,110,111\n"
"Qzvvv 168,168,10\nbuffer_load_dwordx4 a[36:39], v79, s[48:51], s31 offen offset:2048\nv_max_f32_dpp v66, v66, v66 row_ror:1 row_mask:0xf bank_mask:0xf\nv_and_b32 v66, 0x7f800000, v66\nv_add_u32 v66, 0x3000000, v66\nQk 200,200,201\nQk 201,202,203\nQk 202,204,205\n"
"Qk 203,206,207\nQk 136,136,137\nQk 137,138,139\nQzvvv 204,200,12\nQk 138,140,141\nQk 139,142,143\nQzvvv 200,200,14\nbuffer_load_dwordx4 a[40:43], v79, s[48:51], s31 offen offset:3072\n"
"v_min_u32 v66, 0x7e000000, v66\nv_or_b32 v66, 0x400000, v66\nds_write_b32 v73, v66 offset:16\nv_xor_b32 v67, 0x80000000, v66\ns_mov_b32 s31, 20480\nQk 232,232,233\nQk 233,234,235\nQk 234,236,237\n"
"Qk 235,238,239\nQk 168,168,169\nQk 169,170,171\nQzvvv 236,232,16\nQk 170,172,173\nQk 171,174,175\nQzvvv 232,232,18\nbuffer_load_dwordx4 a[44:47], v79, s[48:51], s31 offen offset:0\n"
"v_accvgpr_write_b32 a112, v67\nv_accvgpr_write_b32 a113, v67\nv_accvgpr_write_b32 a114, v67\nv_accvgpr_write_b32 a115, v67\nv_accvgpr_read_b32 v212, a216\nv_accvgpr_read_b32 v213, a217\nv_accvgpr_read_b32 v214, a218\nv_accvgpr_read_b32 v215, a219\n"
"v_accvgpr_read_b32 v40, a220\nv_accvgpr_read_b32 v41, a221\nv_accvgpr_read_b32 v42, a222\nv_accvgpr_read_b32 v43, a223\nQk 212,212,213\nQk 213,214,215\nQk 214,40,41\nQk 215,42,43\n"
"Qk 200,200,201\nQk 201,202,203\nQzvvv 40,212,20\nQk 202,204,205\nQk 203,206,207\nQzvvv 212,212,22\nbuffer_load_dwordx4 a[48:51], v79, s[48:51], s31 offen offset:1024\nv_accvgpr_read_b32 v244, a248\n"
"v_accvgpr_read_b32 v245, a249\nv_accvgpr_read_b32 v246, a250\nv_accvgpr_read_b32 v247, a251\nv_accvgpr_read_b32 v32, a252\nv_accvgpr_read_b32 v33, a253\nv_accvgpr_read_b32 v34, a254\nv_accvgpr_read_b32 v35, a255\nQk 244,244,245\n"
"Qk 245,246,247\nQk 246,32,33\nQk 247,34,35\nQk 232,232,233\nQk 233,234,235\nQzvvv 32,244,24\nQk 234,236,237\nQk 235,238,239\n"
"Qzvvv 244,244,26\nbuffer_load_dwordx4 a[52:55], v79, s[48:51], s31 offen offset:2048\nQk 212,212,213\nQk 213,214,215\nQk 214,40,41\nQk 215,42,43\ns_nop 2\nQk 244,244,245\n"
"Qk 245,246,247\nQk 246,32,33\nQk 247,34,35\ns_waitcnt lgkmcnt(5)\nQcaav 140,56,84,112\ns_add_u32 m0, s96, 98304\ns_mov_b32 s95, 86016\nQmaav 140,60,116\n"
"buffer_load_dwordx4 v61, s[52:55], s95 offen lds\nQcaav 136,56,240,112\nQmaav 136,60,248\nQcaav 132,56,208,112\nQta 72,68,16384\ns_mov_b32 s95, 105472\nQmaav 132,60,216\nbuffer_load_dwordx4 v61, s[52:55], s95 offen offset:1024 lds\n"
"Qcaav 128,56,176,112\nQmaav 128,60,184\nQta 74,68,16896\nQcaav 124,56,144,112\nQmaav 124,60,152\nQcaav 120,56,112,112\nQta 76,68,17408\nQmaav 120,60,120\n"
"s_add_u32 s88, s71, 0\nQcaav 116,56,80,112\nbuffer_store_dwordx4 v[80:83], v79, s[56:59], s88 offen offset:0 sc1\nQmaav 116,60,88\nQta 78,68,17920\ns_waitcnt lgkmcnt(5)\nQcaav 144,64,80,112\nQmaav 144,68,88\n"
"Qcaav 148,64,112,112\nQmaav 148,68,120\nQcaav 152,64,144,112\nQta 56,68,24576\nQmaav 152,68,152\nQcaav 156,64,176,112\nQmaav 156,68,184\nQta 58,68,25600\n"
"s_add_u32 m0, s96, 106496\ns_mov_b32 s95, 126976\nQcaav 160,64,208,112\nbuffer_load_dwordx4 v61, s[52:55], s95 offen lds\nQmaav 160,68,216\nQcaav 164,64,240,112\nQta 60,68,32768\nQmaav 164,68,248\n"
"s_mov_b32 s95, 146432\nQcaav 168,64,84,112\nbuffer_load_dwordx4 v61, s[52:55], s95 offen offset:1024 lds\nQmaav 168,68,116\nQta 62,68,33792\ns_waitcnt lgkmcnt(4)\nQcaav 196,72,84,112\nQmaav 196,76,116\n"
"Qcaav 192,72,240,112\nQmaav 192,76,248\nQcaav 188,72,208,112\nQta 64,68,24832\nQmaav 188,76,216\nbuffer_store_dwordx4 v[112:115], v79, s[56:59], s88 offen offset:1024 sc1\nQcaav 184,72,176,112\nQmaav 184,76,184\n"
"Qta 66,68,25856\nQcaav 180,72,144,112\nQmaav 180,76,152\nQcaav 176,72,112,112\nQta 68,68,33024\nv_add_u32 v75, 1, v75\nQmaav 176,76,120\ns_waitcnt vmcnt(34)\n"
"ds_write_b32 v73, v75\nQcaav 172,72,80,112\nQmaav 172,76,88\nQta 70,68,34048\ns_waitcnt lgkmcnt(5)\nQcaav 200,56,80,112\ns_add_u32 m0, s96, 114688\ns_mov_b32 s95, 86016\n"
"Qmaav 200,60,88\nbuffer_load_dwordx4 v62, s[52:55], s95 offen lds\nQcaav 204,56,112,112\nQmaav 204,60,120\nQcaav 208,56,144,112\nQta 72,68,512\nQmaav 208,60,152\nds_read_b32 v66, v74\n"
"s_mov_b32 s95, 125952\nQcaav 212,56,176,112\nbuffer_load_dwordx4 v62, s[52:55], s95 offen offset:1024 lds\nQmaav 212,60,184\nQta 74,68,1536\nQcaav 216,56,208,112\nQmaav 216,60,216\ns_waitcnt lgkmcnt(1)\n"
"v_cmp_gt_u32 vcc, v75, v66\nQcaav 220,56,240,112\ns_cbranch_vccnz .Lso40\n.Lro40:\nQmaav 220,60,248\nQta 76,68,8704\nQcaav 224,56,84,112\nbuffer_store_dwordx4 v[144:147], v79, s[56:59], s88 offen offset:2048 sc1\n"
"v_xor_b32 v56, 8, v73\nQmaav 224,60,116\nds_read_b32 v56, v56 offset:16\ns_waitcnt lgkmcnt(5)\nQcaav 252,64,84,112\nQta 78,68,9728\nQmaav 252,68,116\nQcaav 248,64,240,112\n"
"Qmaav 248,68,248\nQcaav 244,64,208,112\nQta 56,68,768\nQmaav 244,68,216\nQcaav 240,64,176,112\ns_waitcnt lgkmcnt(2)\nv_mov_b32 v57, v56\nv_mov_b32 v58, v56\n"
"Qmaav 240,68,184\nQta 58,68,1792\ns_add_u32 m0, s96, 122880\ns_mov_b32 s95, 86016\nQcaav 236,64,144,112\nbuffer_load_dwordx4 v63, s[52:55], s95 offen lds\nQmaav 236,68,152\nv_mov_b32 v59, v56\n"
"v_accvgpr_write_b32 a80, v56\nQcaav 232,64,112,112\nQta 60,68,8960\nv_accvgpr_write_b32 a81, v56\nQmaav 232,68,120\ns_mov_b32 s95, 105472\nv_accvgpr_write_b32 a82, v56\nQcaav 228,64,80,112\n"
"buffer_load_dwordx4 v63, s[52:55], s95 offen offset:1024 lds\nQmaav 228,68,88\nQta 62,68,9984\nv_accvgpr_write_b32 a83, v56\ns_waitcnt lgkmcnt(4)\nQcvav 0,72,80,56\nQmvav 0,76,88\nQcvav 4,72,112,56\n"
"buffer_store_dwordx4 v[176:179], v79, s[56:59], s88 offen offset:3072 sc1\nQmvav 4,76,120\nQcvav 8,72,144,56\nQta 64,68,16640\nQmvav 8,76,152\nQcvav 12,72,176,56\nQmvav 12,76,184\nQta 66,68,17152\n"
"Qcvav 16,72,208,56\nQmvav 16,76,216\nQcvav 20,72,240,56\nQta 68,68,17664\nQmvav 20,76,248\nQcvav 24,72,84,56\nQmvav 24,76,116\nQta 70,68,18176\n"
"s_waitcnt lgkmcnt(4)\nQcvav 52,56,84,56\nQmvav 52,60,116\nQcvav 48,56,240,56\nQmvav 48,60,248\nQcvav 44,56,208,56\nQta 72,68,25088\nQmvav 44,60,216\n"
"Qcvav 40,56,176,56\nQmvav 40,60,184\nQta 74,68,26112\ns_add_u32 s88, s71, 4096\nQcvav 36,56,144,56\nbuffer_store_dwordx4 v[208:211], v79, s[56:59], s88 offen offset:0 sc1\nQmvav 36,60,152\nQcvav 32,56,112,56\n"
"Qta 76,68,33280\nQmvav 32,60,120\nQcvav 28,56,80,56\nQmvav 28,60,88\nQta 78,68,34304\ns_waitcnt lgkmcnt(4)\nQcvav 92,64,80,56\nQmvav 92,68,88\n"
"Qcvav 124,64,112,56\nQmvav 124,68,120\nQcvav 156,64,144,56\nQta 56,68,25344\nQmvav 156,68,152\nQcvav 188,64,176,56\nQmvav 188,68,184\nQta 58,68,26368\n"
"Qcvav 220,64,208,56\nQmvav 220,68,216\nQcvav 252,64,240,56\nQta 60,68,33536\nQmvav 252,68,248\nQcvav 100,64,84,56\nQmvav 100,68,116\nQta 62,68,34560\n"
"s_waitcnt lgkmcnt(4)\nQcvav 172,72,84,56\nbuffer_store_dwordx4 v[240:243], v79, s[56:59], s88 offen offset:1024 sc1\nQmvav 172,76,116\nQcvav 140,72,240,56\nQmvav 140,76,248\nQcvav 108,72,208,56\nQta 64,68,40960\n"
"Qmvav 108,76,216\nQcvav 228,72,176,56\nQmvav 228,76,184\nQta 66,68,41984\nQcvav 196,72,144,56\nQmvav 196,76,152\nQcvav 164,72,112,56\nQta 68,68,49152\n"
"Qmvav 164,76,120\nQcvav 132,72,80,56\nQmvav 132,76,88\nQta 70,68,50176\ns_waitcnt lgkmcnt(4)\nQcvav 204,56,80,56\nQmvav 204,60,88\nQcvav 236,56,112,56\n"
"Qmvav 236,60,120\nQcaav 96,56,144,80\nQta 72,68,41216\nQmaav 96,60,152\nQcaav 100,56,176,80\nbuffer_store_dwordx4 v[84:87], v79, s[56:59], s88 offen offset:2048 sc1\nQmaav 100,60,184\nQta 74,68,42240\n"
"Qcaav 104,56,208,80\nQmaav 104,60,216\nQcaav 108,56,240,80\nQta 76,68,49408\nQmaav 108,60,248\nQcaav 112,56,84,80\nQmaav 112,60,116\nQta 78,68,50432\n"
"s_waitcnt vmcnt(15)\ns_waitcnt lgkmcnt(4)\nQmaaa 140,64,24\nQmaaa 140,68,52\nQmaaa 136,64,20\nQmaaa 136,68,48\nQmaaa 132,64,16\nQta 56,68,41472\n"
"Qmaaa 132,68,44\nQmaaa 128,64,12\nQmaaa 128,68,40\nQta 58,68,42496\nQmaaa 124,64,8\nQmaaa 124,68,36\nQmaaa 120,64,4\nQta 60,68,49664\n"
"s_and_b32 s68, %19, 3\nQmaaa 120,68,32\n.Lpg14:\ns_cbranch_execz .Ltr6\ns_add_u32 s88, s71, 16384\nQmaaa 116,64,0\nbuffer_store_dwordx4 v[88:91], v79, s[56:59], s88 offen offset:0 sc1\nQmaaa 116,68,28\n"
"Qta 62,68,50688\ns_waitcnt lgkmcnt(4)\nQmaaa 144,72,0\ns_cmp_lt_u32 s68, 1\ns_cselect_b32 s68, 40, 32\nQmaaa 144,76,28\ns_mul_i32 s67, s68, 40960\ns_mul_i32 s74, s68, 32768\n"
"Qmaaa 148,72,4\ns_and_b32 s68, %19, 3\nQmaaa 148,76,32\ns_cmp_lt_u32 s68, 2\ns_cselect_b32 s68, 48, 40\nQmaaa 152,72,8\nQta 64,68,41728\ns_mul_i32 s72, s68, 40960\n"
"Qmaaa 152,76,36\ns_mul_i32 s75, s68, 32768\ns_and_b32 s68, %19, 3\nQmaaa 156,72,12\ns_cmp_lt_u32 s68, 3\ns_cselect_b32 s68, 56, 48\nQmaaa 156,76,40\nQta 66,68,42752\n"
"s_mul_i32 s73, s68, 40960\nQmaaa 160,72,16\ns_mul_i32 s77, s68, 32768\nv_add_u32 v58, s91, v60\nQmaaa 160,76,44\ns_mul_i32 s68, s93, 28672\ns_sub_u32 s68, 14336, s68\nQmaaa 164,72,20\n"
"Qta 68,68,49920\nv_add_u32 v59, s68, v58\nQmaaa 164,76,48\nQmaaa 168,72,24\nQmaaa 168,76,52\nQta 70,68,50944\ns_waitcnt lgkmcnt(4)\nQmvaa 24,56,24\n"
"Qmvaa 24,60,52\nQmvaa 20,56,20\nQmvaa 20,60,48\nQmvaa 16,56,16\nQta 72,68,57344\nQmvaa 16,60,44\nbuffer_store_dwordx4 v[120:123], v79, s[56:59], s88 offen offset:1024 sc1\nQmvaa 12,56,12\n"
"Qmvaa 12,60,40\nQta 74,68,57856\nQmvaa 8,56,8\nQmvaa 8,60,36\nQmvaa 4,56,4\nQta 76,68,58368\nv_add_u32 v75, 1, v75\nQmvaa 4,60,32\n"
"s_waitcnt vmcnt(35)\nds_write_b32 v73, v75\nQmvaa 0,56,0\nQmvaa 0,60,28\nQta 78,68,58880\ns_waitcnt lgkmcnt(5)\nQmvaa 28,64,0\nQmvaa 28,68,28\n"
"Qmvaa 32,64,4\nQmvaa 32,68,32\nQmvaa 36,64,8\nQta 56,68,57600\nQmvaa 36,68,36\nds_read_b32 v66, v74\nQmvaa 40,64,12\nQmvaa 40,68,40\n"
"Qta 58,68,58112\nQmvaa 44,64,16\nQmvaa 44,68,44\ns_waitcnt lgkmcnt(1)\nv_cmp_gt_u32 vcc, v75, v66\nQmvaa 48,64,20\ns_cbranch_vccnz .Lso180\n.Lro180:\n"
"Qmvaa 48,68,48\nQta 60,68,58624\nQmvaa 52,64,24\nbuffer_store_dwordx4 v[152:155], v79, s[56:59], s88 offen offset:2048 sc1\nQmvaa 52,68,52\ns_waitcnt lgkmcnt(4)\nQmaaa 196,72,24\nQta 62,68,59136\n"
"Qmaaa 196,76,52\nQmaaa 192,72,20\nQmaaa 192,76,48\nQmaaa 188,72,16\nQta 64,70,1024\nQmaaa 188,76,44\nQmaaa 184,72,12\nQmaaa 184,76,40\n"
"Qta 66,70,2048\ns_add_u32 m0, s96, 0\ns_mov_b32 s95, 126976\nQmaaa 180,72,8\nbuffer_load_dwordx4 v63, s[52:55], s95 offen lds\nQmaaa 180,76,36\nQmaaa 176,72,4\nQta 68,70,9216\n"
"Qmaaa 176,76,32\ns_mov_b32 s95, 146432\nQmaaa 172,72,0\nbuffer_load_dwordx4 v63, s[52:55], s95 offen offset:1024 lds\nQmaaa 172,76,28\nQta 70,70,10240\ns_waitcnt lgkmcnt(4)\nQmvaa 92,56,0\n"
"Qmvaa 92,60,28\nQmvaa 124,56,4\nbuffer_store_dwordx4 v[184:187], v79, s[56:59], s88 offen offset:3072 sc1\nQmvaa 124,60,32\nQmvaa 156,56,8\nQta 72,70,1280\nQmvaa 156,60,36\nQmvaa 188,56,12\n"
"Qmvaa 188,60,40\nQta 74,70,2304\nQmvaa 220,56,16\nQmvaa 220,60,44\nQmvaa 252,56,20\nQta 76,70,9472\nQmvaa 252,60,48\ns_add_u32 m0, s96, 8192\n"
"s_mov_b32 s95, 331776\nQmvaa 100,56,24\nbuffer_load_dwordx4 v61, s[52:55], s95 offen lds\nQmvaa 100,60,52\nQta 78,70,10496\ns_waitcnt lgkmcnt(4)\nQmaaa 224,64,24\nQmaaa 224,68,52\n"
"s_mov_b32 s95, 351232\nQmaaa 220,64,20\nbuffer_load_dwordx4 v61, s[52:55], s95 offen offset:1024 lds\nQmaaa 220,68,48\nQmaaa 216,64,16\nQta 56,70,1536\nQmaaa 216,68,44\nQmaaa 212,64,12\n"
"Qmaaa 212,68,40\nQta 58,70,2560\ns_add_u32 s88, s71, 20480\nQmaaa 208,64,8\nbuffer_store_dwordx4 v[216:219], v79, s[56:59], s88 offen offset:0 sc1\nQmaaa 208,68,36\nQmaaa 204,64,4\nQta 60,70,9728\n"
"Qmaaa 204,68,32\nQmaaa 200,64,0\nQmaaa 200,68,28\nQta 62,70,10752\ns_waitcnt lgkmcnt(4)\nQmaaa 228,72,0\nQmaaa 228,76,28\nQmaaa 232,72,4\n"
"Qmaaa 232,76,32\nQmaaa 236,72,8\nQta 64,70,1792\ns_add_u32 m0, s96, 16384\ns_mov_b32 s95, 372736\nQmaaa 236,76,36\nbuffer_load_dwordx4 v61, s[52:55], s95 offen lds\nQmaaa 240,72,12\n"
"Qmaaa 240,76,40\nQta 66,70,2816\nQmaaa 244,72,16\ns_mov_b32 s95, 392192\nQmaaa 244,76,44\nbuffer_load_dwordx4 v61, s[52:55], s95 offen offset:1024 lds\nQmaaa 248,72,20\nQta 68,70,9984\n"
"Qmaaa 248,76,48\nQmaaa 252,72,24\nQmaaa 252,76,52\nQta 70,70,11008\ns_waitcnt lgkmcnt(4)\nQmvaa 172,56,24\nbuffer_store_dwordx4 v[248:251], v79, s[56:59], s88 offen offset:1024 sc1\nQmvaa 172,60,52\n"
"Qmvaa 140,56,20\nQmvaa 140,60,48\nQmvaa 108,56,16\nQta 72,70,17408\nQmvaa 108,60,44\nQmvaa 228,56,12\nQmvaa 228,60,40\nQta 74,70,18432\n"
"Qmvaa 196,56,8\nQmvaa 196,60,36\nQmvaa 164,56,4\nQta 76,69,0\ns_add_u32 m0, s96, 24576\ns_mov_b32 s95, 331776\nQmvaa 164,60,32\nbuffer_load_dwordx4 v62, s[52:55], s95 offen lds\n"
"Qmvaa 132,56,0\nQmvaa 132,60,28\nQta 78,69,1024\ns_waitcnt lgkmcnt(4)\nQmvaa 204,64,0\ns_mov_b32 s95, 371712\nQmvaa 204,68,28\nbuffer_load_dwordx4 v62, s[52:55], s95 offen offset:1024 lds\n"
"Qmvaa 236,64,4\nQmvaa 236,68,32\nQmaaa 96,64,8\nQta 56,70,17664\nQmaaa 96,68,36\nQmaaa 100,64,12\nbuffer_store_dwordx4 v[116:119], v79, s[56:59], s88 offen offset:2048 sc1\nQmaaa 100,68,40\n"
"Qta 58,70,18688\nQmaaa 104,64,16\nQmaaa 104,68,44\nQmaaa 108,64,20\nQta 60,69,256\nQmaaa 108,68,48\nQmaaa 112,64,24\nQmaaa 112,68,52\n"
"Qta 62,69,1280\ns_waitcnt lgkmcnt(4)\nQmaav 140,72,180\nQmaav 140,76,244\ns_mov_b32 s31, 131072\nQmaav 136,72,148\nbuffer_load_dwordx4 a[0:3], v79, s[48:51], s31 offen offset:0\nQmaav 136,76,212\n"
"Qmaav 132,72,224\nQta 64,70,17920\nQmaav 132,76,232\nQmaav 128,72,192\nbuffer_load_dwordx4 a[4:7], v79, s[48:51], s31 offen offset:1024\nQmaav 128,76,200\nQta 66,70,18944\nQmaav 124,72,160\n"
"Qmaav 124,76,168\nQmaav 120,72,128\nQta 68,69,512\nQmaav 120,76,136\nbuffer_load_dwordx4 a[8:11], v79, s[48:51], s31 offen offset:2048\nQmaav 116,72,96\nQmaav 116,76,104\nQta 70,69,1536\n"
"s_waitcnt lgkmcnt(4)\nQmaav 144,56,96\nQmaav 144,60,104\nbuffer_load_dwordx4 a[12:15], v79, s[48:51], s31 offen offset:3072\nQmaav 148,56,128\nQmaav 148,60,136\nQmaav 152,56,160\nQta 72,70,18176\n"
"s_mov_b32 s31, 135168\nQmaav 152,60,168\nbuffer_load_dwordx4 a[16:19], v79, s[48:51], s31 offen offset:0\nQmaav 156,56,192\nQmaav 156,60,200\nQta 74,70,19200\nQmaav 160,56,224\nQmaav 160,60,232\n"
"buffer_load_dwordx4 a[20:23], v79, s[48:51], s31 offen offset:1024\nQmaav 164,56,148\nQta 76,69,768\nQmaav 164,60,212\nQmaav 168,56,180\nQmaav 168,60,244\nQta 78,69,1792\ns_waitcnt lgkmcnt(4)\n"
"Qmvav 24,64,180\nbuffer_load_dwordx4 a[24:27], v79, s[48:51], s31 offen offset:2048\nQmvav 24,68,244\nQmvav 20,64,148\nQmvav 20,68,212\nQmvav 16,64,224\nQta 56,69,8192\ns_mov_b32 s31, 147456\n"
"Qmvav 16,68,232\nbuffer_load_dwordx4 a[28:31], v79, s[48:51], s31 offen offset:0\nQmvav 12,64,192\nQmvav 12,68,200\nQta 58,69,8704\nQmvav 8,64,160\nQmvav 8,68,168\nbuffer_load_dwordx4 a[32:35], v79, s[48:51], s31 offen offset:1024\n"
"Qmvav 4,64,128\nQta 60,69,9216\nv_add_u32 v75, 1, v75\nQmvav 4,68,136\ns_waitcnt vmcnt(31)\nds_write_b32 v73, v75\nQmvav 0,64,96\nQmvav 0,68,104\n"
"Qta 62,69,9728\ns_waitcnt lgkmcnt(5)\nQmvav 28,72,96\nbuffer_load_dwordx4 a[36:39], v79, s[48:51], s31 offen offset:2048\nQmvav 28,76,104\nQmvav 32,72,128\nQmvav 32,76,136\nQmvav 36,72,160\n"
"Qta 64,69,8448\nQmvav 36,76,168\nds_read_b32 v66, v74\nQmvav 40,72,192\nbuffer_load_dwordx4 a[40:43], v79, s[48:51], s31 offen offset:3072\nQmvav 40,76,200\nQta 66,69,8960\nQmvav 44,72,224\n"
"Qmvav 44,76,232\ns_waitcnt lgkmcnt(1)\nv_cmp_gt_u32 vcc, v75, v66\nQmvav 48,72,148\ns_cbranch_vccnz .Lso320\n.Lro320:\nQmvav 48,76,212\nQta 68,69,9472\n"
"s_mov_b32 s31, 151552\nQmvav 52,72,180\nbuffer_load_dwordx4 a[44:47], v79, s[48:51], s31 offen offset:0\nQmvav 52,76,244\ns_waitcnt lgkmcnt(4)\nQmaav 196,56,180\nQta 70,69,9984\nQmaav 196,60,244\n"
"Qmaav 192,56,148\nbuffer_load_dwordx4 a[48:51], v79, s[48:51], s31 offen offset:1024\nQmaav 192,60,212\nQmaav 188,56,224\nQta 72,69,16384\nQmaav 188,60,232\nQmaav 184,56,192\nbuffer_load_dwordx4 a[52:55], v79, s[48:51], s31 offen offset:2048\n"
"Qmaav 184,60,200\nQta 74,69,17408\nQmaav 180,56,160\nQmaav 180,60,168\nQmaav 176,56,128\nQta 76,69,24576\ns_mov_b32 s31, 262144\nQmaav 176,60,136\n"
"buffer_load_dwordx4 v[80:83], v79, s[48:51], s31 offen offset:0\nQmaav 172,56,96\nQmaav 172,60,104\nQta 78,69,25600\ns_waitcnt lgkmcnt(4)\nQmvav 92,64,96\nQmvav 92,68,104\nbuffer_load_dwordx4 v[112:115], v79, s[48:51], s31 offen offset:1024\n"
"Qmvav 124,64,128\nQmvav 124,68,136\nQmvav 156,64,160\nQta 56,69,16640\nQmvav 156,68,168\nbuffer_load_dwordx4 v[144:147], v79, s[48:51], s31 offen offset:2048\nQmvav 188,64,192\nQmvav 188,68,200\n"
"Qta 58,69,17664\nQmvav 220,64,224\nQmvav 220,68,232\nbuffer_load_dwordx4 v[176:179], v79, s[48:51], s31 offen offset:3072\nQmvav 252,64,148\nQta 60,69,24832\nQmvav 252,68,212\nQmvav 100,64,180\n"
"Qmvav 100,68,244\nQta 62,69,25856\ns_mov_b32 s31, 266240\ns_waitcnt lgkmcnt(4)\nQmaav 224,72,180\nbuffer_load_dwordx4 v[208:211], v79, s[48:51], s31 offen offset:0\nQmaav 224,76,244\nQmaav 220,72,148\n"
"Qmaav 220,76,212\nQmaav 216,72,224\nQta 64,69,16896\nQmaav 216,76,232\nbuffer_load_dwordx4 v[240:243], v79, s[48:51], s31 offen offset:1024\nQmaav 212,72,192\nQmaav 212,76,200\nQta 66,69,17920\n"
"Qmaav 208,72,160\nQmaav 208,76,168\nbuffer_load_dwordx4 v[84:87], v79, s[48:51], s31 offen offset:2048\nQmaav 204,72,128\nQta 68,69,25088\nQmaav 204,76,136\nQmaav 200,72,96\nQmaav 200,76,104\n"
"Qta 70,69,26112\ns_mov_b32 s31, 278528\ns_waitcnt lgkmcnt(4)\nQmaav 228,56,96\nbuffer_load_dwordx4 v[88:91], v79, s[48:51], s31 offen offset:0\nQmaav 228,60,104\nQmaav 232,56,128\nQmaav 232,60,136\n"
"Qmaav 236,56,160\nQta 72,69,17152\nQmaav 236,60,168\nbuffer_load_dwordx4 v[120:123], v79, s[48:51], s31 offen offset:1024\nQmaav 240,56,192\nQmaav 240,60,200\nQta 74,69,18176\nQmaav 244,56,224\n"
"Qmaav 244,60,232\nbuffer_load_dwordx4 v[152:155], v79, s[48:51], s31 offen offset:2048\nQmaav 248,56,148\nQta 76,69,25344\nQmaav 248,60,212\nQmaav 252,56,180\nQmaav 252,60,244\nQta 78,69,26368\n"
"s_waitcnt lgkmcnt(4)\nQmvav 172,64,180\nbuffer_load_dwordx4 v[184:187], v79, s[48:51], s31 offen offset:3072\nQmvav 172,68,244\nQmvav 140,64,148\nQmvav 140,68,212\nQmvav 108,64,224\nQta 56,69,32768\n"
"s_mov_b32 s31, 282624\nQmvav 108,68,232\nbuffer_load_dwordx4 v[216:219], v79, s[48:51], s31 offen offset:0\nQmvav 228,64,192\nQmvav 228,68,200\nQta 58,69,33792\nQmvav 196,64,160\nQmvav 196,68,168\n"
"buffer_load_dwordx4 v[248:251], v79, s[48:51], s31 offen offset:1024\nQmvav 164,64,128\nQta 60,69,40960\nQmvav 164,68,136\nQmvav 132,64,96\nQmvav 132,68,104\nQta 62,69,41984\ns_waitcnt lgkmcnt(4)\n"
"Qmvav 204,72,96\nbuffer_load_dwordx4 v[116:119], v79, s[48:51], s31 offen offset:2048\nQmvav 204,76,104\nQmvav 236,72,128\nQmvav 236,76,136\nQmaav 96,72,160\nQta 64,69,33024\ns_add_u32 m0, s96, 32768\n"
"s_mov_b32 s95, 331776\nQmaav 96,76,168\nbuffer_load_dwordx4 v63, s[52:55], s95 offen lds\nQmaav 100,72,192\nQmaav 100,76,200\nQta 66,69,34048\nQmaav 104,72,224\ns_mov_b32 s95, 351232\n"
"Qmaav 104,76,232\nbuffer_load_dwordx4 v63, s[52:55], s95 offen offset:1024 lds\nQmaav 108,72,148\nQta 68,69,41216\nQmaav 108,76,212\nQmaav 112,72,180\nQmaav 112,76,244\nQta 70,69,42240\n"
"s_add_u32 m0, s96, 40960\ns_mov_b32 s95, 372736\ns_waitcnt vmcnt(16)\ns_waitcnt lgkmcnt(4)\nQmaaa 140,56,24\nbuffer_load_dwordx4 v63, s[52:55], s95 offen lds\nQmaaa 140,60,52\nQmaaa 136,56,20\n"
"Qmaaa 136,60,48\nQmaaa 132,56,16\nQta 72,69,33280\ns_mov_b32 s95, 392192\nQmaaa 132,60,44\nbuffer_load_dwordx4 v63, s[52:55], s95 offen offset:1024 lds\nQmaaa 128,56,12\nQmaaa 128,60,40\n"
"Qta 74,69,34304\nQmaaa 124,56,8\nQmaaa 124,60,36\nQmaaa 120,56,4\nQta 76,69,41472\ns_add_u32 s88, s71, 131072\nQmaaa 120,60,32\nbuffer_store_dwordx4 v[96:99], v79, s[56:59], s88 offen offset:0 sc1\n"
"Qmaaa 116,56,0\nQmaaa 116,60,28\nQta 78,69,42496\ns_waitcnt lgkmcnt(4)\nQmaaa 144,64,0\nQmaaa 144,68,28\nQmaaa 148,64,4\nQmaaa 148,68,32\n"
"Qmaaa 152,64,8\nQta 56,69,33536\nQmaaa 152,68,36\nQmaaa 156,64,12\nQmaaa 156,68,40\nQta 58,69,34560\ns_add_u32 m0, s96, 49152\ns_mov_b32 s95, 413696\n"
"Qmaaa 160,64,16\nbuffer_load_dwordx4 v61, s[52:55], s95 offen lds\nQmaaa 160,68,44\nQmaaa 164,64,20\nQta 60,69,41728\nQmaaa 164,68,48\ns_mov_b32 s95, 433152\nQmaaa 168,64,24\n"
"buffer_load_dwordx4 v61, s[52:55], s95 offen offset:1024 lds\nQmaaa 168,68,52\nQta 62,69,42752\ns_waitcnt lgkmcnt(4)\nQmvaa 24,72,24\nQmvaa 24,76,52\nQmvaa 20,72,20\nbuffer_store_dwordx4 v[128:131], v79, s[56:59], s88 offen offset:1024 sc1\n"
"Qmvaa 20,76,48\nQmvaa 16,72,16\nQta 64,69,49152\nQmvaa 16,76,44\nQmvaa 12,72,12\nQmvaa 12,76,40\nQta 66,69,49664\nQmvaa 8,72,8\n"
"Qmvaa 8,76,36\nQmvaa 4,72,4\nQta 68,69,50176\nv_add_u32 v75, 1, v75\n.Lpg15:\ns_cbranch_execz .Ltr6\nQmvaa 4,76,32\ns_waitcnt vmcnt(36)\n"
"ds_write_b32 v73, v75\ns_add_u32 m0, s96, 57344\ns_mov_b32 s95, 454656\nQmvaa 0,72,0\nbuffer_load_dwordx4 v61, s[52:55], s95 offen lds\nQmvaa 0,76,28\nQta 70,69,50688\ns_waitcnt lgkmcnt(5)\n"
"Qmvaa 28,56,0\nQmvaa 28,60,28\ns_mov_b32 s95, 474112\nQmvaa 32,56,4\nbuffer_load_dwordx4 v61, s[52:55], s95 offen offset:1024 lds\nQmvaa 32,60,32\nQmvaa 36,56,8\nQta 72,69,49408\n"
"Qmvaa 36,60,36\nds_read_b32 v66, v74\nQmvaa 40,56,12\nQmvaa 40,60,40\nQta 74,69,49920\nQmvaa 44,56,16\nbuffer_store_dwordx4 v[160:163], v79, s[56:59], s88 offen offset:2048 sc1\nv_add_u32 v67, 2, v65\n"
"Qmvaa 44,60,44\ns_waitcnt lgkmcnt(1)\nv_cmp_gt_u32 vcc, v75, v66\nQmvaa 48,56,20\ns_cbranch_vccnz .Lso460\n.Lro460:\nQmvaa 48,60,48\nQta 76,69,50432\n"
"Qmvaa 52,56,24\nbuffer_store_dword v67, v77, s[24:27], 0 offen sc1\nQmvaa 52,60,52\ns_waitcnt lgkmcnt(4)\nQmaaa 196,64,24\nQta 78,69,50944\nQmaaa 196,68,52\nQmaaa 192,64,20\n"
"Qmaaa 192,68,48\nQmaaa 188,64,16\nQta 56,69,57344\ns_add_u32 m0, s96, 132096\ns_mov_b32 s95, 413696\nQmaaa 188,68,44\nbuffer_load_dwordx4 v62, s[52:55], s95 offen lds\nQmaaa 184,64,12\n"
"Qmaaa 184,68,40\nQta 58,69,58368\nQmaaa 180,64,8\ns_mov_b32 s95, 453632\nQmaaa 180,68,36\nbuffer_load_dwordx4 v62, s[52:55], s95 offen offset:1024 lds\nQmaaa 176,64,4\nQta 60,68,0\n"
"Qmaaa 176,68,32\nQmaaa 172,64,0\nQmaaa 172,68,28\nQta 62,68,1024\ns_waitcnt lgkmcnt(4)\nQmvaa 92,72,0\nbuffer_store_dwordx4 v[192:195], v79, s[56:59], s88 offen offset:3072 sc1\nQmvaa 92,76,28\n"
"Qmvaa 124,72,4\nQmvaa 124,76,32\nQmvaa 156,72,8\nQta 64,69,57600\nQmvaa 156,76,36\nQmvaa 188,72,12\nQmvaa 188,76,40\nQta 66,69,58624\n"
"Qmvaa 220,72,16\nQmvaa 220,76,44\nQmvaa 252,72,20\nQta 68,68,256\ns_add_u32 m0, s96, 140288\ns_mov_b32 s95, 413696\nQmvaa 252,76,48\nbuffer_load_dwordx4 v63, s[52:55], s95 offen lds\n"
"Qmvaa 100,72,24\nQmvaa 100,76,52\nQta 70,68,1280\ns_waitcnt lgkmcnt(4)\nQmaaa 224,56,24\ns_mov_b32 s95, 433152\nQmaaa 224,60,52\nbuffer_load_dwordx4 v63, s[52:55], s95 offen offset:1024 lds\n"
"Qmaaa 220,56,20\nQmaaa 220,60,48\nQmaaa 216,56,16\nQta 72,69,57856\nQmaaa 216,60,44\ns_add_u32 s88, s71, 135168\nQmaaa 212,56,12\nbuffer_store_dwordx4 v[224:227], v79, s[56:59], s88 offen offset:0 sc1\n"
"Qmaaa 212,60,40\nQta 74,69,58880\nQmaaa 208,56,8\nQmaaa 208,60,36\nQmaaa 204,56,4\nQta 76,68,512\nQmaaa 204,60,32\nQmaaa 200,56,0\n"
"Qmaaa 200,60,28\nQta 78,68,1536\ns_waitcnt lgkmcnt(4)\nQmaaa 228,64,0\nQmaaa 228,68,28\ns_add_u32 m0, s96, 148480\ns_mov_b32 s95, 454656\nQmaaa 232,64,4\n"
"buffer_load_dwordx4 v63, s[52:55], s95 offen lds\nQmaaa 232,68,32\nQmaaa 236,64,8\nQta 56,69,58112\nQmaaa 236,68,36\ns_mov_b32 s95, 474112\nQmaaa 240,64,12\nbuffer_load_dwordx4 v63, s[52:55], s95 offen offset:1024 lds\n"
"Qmaaa 240,68,40\nQta 58,69,59136\nQmaaa 244,64,16\nQmaaa 244,68,44\nQmaaa 248,64,20\nQta 60,68,768\nQmaaa 248,68,48\nQmaaa 252,64,24\n"
"buffer_store_dwordx4 v[148:151], v79, s[56:59], s88 offen offset:1024 sc1\nQmaaa 252,68,52\nQta 62,68,1792\ns_waitcnt lgkmcnt(4)\nQmvaa 172,72,24\nQmvaa 172,76,52\ns_mov_b32 s40, %17\ns_and_b32 s41, %18, 0xffff\n"
"Qmvaa 140,72,20\ns_mov_b32 s42, 571200\ns_lshl_b32 s66, %19, 17\nQmvaa 140,76,48\ns_add_u32 s69, s98, 4194304\ns_add_u32 s98, s66, s69\nQmvaa 108,72,16\nQta 64,68,8192\n"
"s_xor_b32 s66, s66, 524288\nQmvaa 108,76,44\ns_add_u32 s64, s66, s69\ns_mul_i32 s66, s92, 33792\nQmvaa 228,72,12\ns_add_u32 s66, s66, %10\ns_add_u32 s66, s66, 98304\nQmvaa 228,76,40\n"
"Qta 66,68,9216\ns_add_u32 m0, s96, 65536\ns_mov_b32 s95, 659456\nQmvaa 196,72,8\nbuffer_load_dwordx4 v61, s[52:55], s95 offen lds\nQmvaa 196,76,36\ns_mul_i32 s68, s93, 96\ns_sub_u32 s68, s66, s68\n"
"Qmvaa 164,72,4\nQta 68,68,16384\ns_add_u32 s68, s68, 96\nQmvaa 164,76,32\ns_mov_b32 s95, 678912\nv_bfe_u32 v148, v60, 4, 4\nQmvaa 132,72,0\nbuffer_load_dwordx4 v61, s[52:55], s95 offen offset:1024 lds\n"
"Qmvaa 132,76,28\nQta 70,68,17408\nv_mul_u32_u24 v148, 208, v148\ns_waitcnt lgkmcnt(4)\nQmvaa 204,56,0\nv_lshrrev_b32 v149, 8, v60\nv_lshl_add_u32 v148, v149, 3, v148\nQmvaa 204,60,28\n"
"v_add_u32 v56, s68, v148\ns_lshl_b32 s68, s93, 3\nQmvaa 236,56,4\nv_lshrrev_b32 v148, 7, v60\nv_add_u32 v148, s68, v148\nQmvaa 236,60,32\nv_and_b32 v149, 127, v60\nv_and_b32 v150, 64, v60\n"
"Qmaaa 96,56,8\nQta 72,68,8448\nv_lshrrev_b32 v150, 1, v150\nQmaaa 96,60,36\nbuffer_store_dwordx4 v[180:183], v79, s[56:59], s88 offen offset:2048 sc1\nQmaaa 100,56,12\nv_mul_u32_u24 v57, 208, v148\nv_add3_u32 v57, v57, v149, s66\n"
"Qmaaa 100,60,40\nQta 74,68,9472\nv_add_u32 v57, v57, v150\nQmaaa 104,56,16\ns_and_b32 s65, %19, 1\ns_and_b32 s68, %19, 4\nQmaaa 104,60,44\ns_lshl_b32 s68, s68, 2\n"
"s_lshl_b32 s69, s65, 3\nQmaaa 108,56,20\nQta 76,68,16640\ns_add_u32 s68, s68, s69\nQmaaa 108,60,48\ns_lshr_b32 s68, 0xc0004080, s68\ns_and_b32 s68, s68, 255\nQmaaa 112,56,24\n"
"s_mul_i32 s69, s92, 286720\ns_add_u32 s68, s68, s69\nQmaaa 112,60,52\nQta 78,68,17664\nv_mul_u32_u24 v64, 2560, v148\ns_waitcnt vmcnt(24)\ns_waitcnt lgkmcnt(4)\nQmaav 140,64,84\n"
"s_mov_b32 s31, 393216\nv_add3_u32 v64, v64, v149, s68\nQmaav 140,68,116\nbuffer_load_dwordx4 a[0:3], v79, s[48:51], s31 offen offset:0\nQmaav 136,64,240\ns_lshl_b32 s68, s93, 4\nv_lshrrev_b32 v148, 6, v60\nQmaav 136,68,248\n"
"v_add_u32 v148, s68, v148\nv_and_b32 v149, 63, v60\nQmaav 132,64,208\nQta 56,68,8704\nv_and_b32 v150, 32, v60\nQmaav 132,68,216\nbuffer_load_dwordx4 a[4:7], v79, s[48:51], s31 offen offset:1024\nQmaav 128,64,176\n"
"v_lshlrev_b32 v150, 1, v150\nv_mul_u32_u24 v71, 208, v148\nQmaav 128,68,184\nQta 58,68,9728\nv_add3_u32 v71, v71, v149, s66\nQmaav 124,64,144\nv_add_u32 v71, v71, v150\ns_xor_b32 s65, s65, 1\n"
"Qmaav 124,68,152\nbuffer_load_dwordx4 a[8:11], v79, s[48:51], s31 offen offset:2048\nQmaav 120,64,112\nQta 60,68,16896\ns_lshl_b32 s65, s65, 8\nQmaav 120,68,120\ns_add_u32 s68, s65, s69\nv_mul_u32_u24 v72, 2560, v148\n"
"Qmaav 116,64,80\nv_add3_u32 v72, v72, v149, s68\nQmaav 116,68,88\nQta 62,68,17920\ns_waitcnt lgkmcnt(4)\nQmaav 144,72,80\nbuffer_load_dwordx4 a[12:15], v79, s[48:51], s31 offen offset:3072\nQmaav 144,76,88\n"
"Qmaav 148,72,112\nQmaav 148,76,120\nQmaav 152,72,144\nQta 64,68,8960\ns_mov_b32 s31, 397312\nQmaav 152,76,152\nbuffer_load_dwordx4 a[16:19], v79, s[48:51], s31 offen offset:0\nQmaav 156,72,176\n"
"Qmaav 156,76,184\nQta 66,68,9984\nQmaav 160,72,208\nQmaav 160,76,216\nbuffer_load_dwordx4 a[20:23], v79, s[48:51], s31 offen offset:1024\nQmaav 164,72,240\nQta 68,68,17152\nQmaav 164,76,248\n"
"Qmaav 168,72,84\nQmaav 168,76,116\nQta 70,68,18176\ns_waitcnt lgkmcnt(4)\nQmvav 24,56,84\nbuffer_load_dwordx4 a[24:27], v79, s[48:51], s31 offen offset:2048\nQmvav 24,60,116\nQmvav 20,56,240\n"
"Qmvav 20,60,248\nQmvav 16,56,208\nQta 72,68,24576\ns_mov_b32 s31, 409600\nQmvav 16,60,216\nbuffer_load_dwordx4 a[28:31], v79, s[48:51], s31 offen offset:0\nQmvav 12,56,176\nQmvav 12,60,184\n"
"Qta 74,68,25088\nQmvav 8,56,144\nQmvav 8,60,152\nbuffer_load_dwordx4 a[32:35], v79, s[48:51], s31 offen offset:1024\nQmvav 4,56,112\nQta 76,68,25600\nv_add_u32 v75, 1, v75\nQmvav 4,60,120\n"
"s_waitcnt vmcnt(19)\nds_write_b32 v73, v75\nQmvav 0,56,80\nQmvav 0,60,88\nQta 78,68,26112\ns_waitcnt lgkmcnt(5)\nQmvav 28,64,80\nbuffer_load_dwordx4 a[36:39], v79, s[48:51], s31 offen offset:2048\n"
"Qmvav 28,68,88\nQmvav 32,64,112\nQmvav 32,68,120\nQmvav 36,64,144\nQta 56,68,24832\nQmvav 36,68,152\nds_read_b32 v66, v74\nQmvav 40,64,176\n"
"buffer_load_dwordx4 a[40:43], v79, s[48:51], s31 offen offset:3072\nQmvav 40,68,184\nQta 58,68,25344\nQmvav 44,64,208\nQmvav 44,68,216\ns_waitcnt lgkmcnt(1)\nv_cmp_gt_u32 vcc, v75, v66\nQmvav 48,64,240\n"
"s_cbranch_vccnz .Lso600\n.Lro600:\nQmvav 48,68,248\nQta 60,68,25856\ns_mov_b32 s31, 413696\nQmvav 52,64,84\nbuffer_load_dwordx4 a[44:47], v79, s[48:51], s31 offen offset:0\nQmvav 52,68,116\n"
"s_waitcnt lgkmcnt(4)\nQmaav 196,72,84\nQta 62,68,26368\nQmaav 196,76,116\nQmaav 192,72,240\nbuffer_load_dwordx4 a[48:51], v79, s[48:51], s31 offen offset:1024\nQmaav 192,76,248\nQmaav 188,72,208\n"
"Qta 64,68,32768\nQmaav 188,76,216\nQmaav 184,72,176\nbuffer_load_dwordx4 a[52:55], v79, s[48:51], s31 offen offset:2048\nQmaav 184,76,184\nQta 66,68,33792\nQmaav 180,72,144\nQmaav 180,76,152\n"
"Qmaav 176,72,112\nQta 68,68,40960\ns_add_u32 m0, s96, 73728\ns_mov_b32 s95, 700416\nQmaav 176,76,120\nbuffer_load_dwordx4 v61, s[52:55], s95 offen lds\nQmaav 172,72,80\nQmaav 172,76,88\n"
"Qta 70,68,41984\ns_waitcnt lgkmcnt(4)\nQmvav 92,56,80\ns_mov_b32 s95, 719872\nQmvav 92,60,88\nbuffer_load_dwordx4 v61, s[52:55], s95 offen offset:1024 lds\nQmvav 124,56,112\nQmvav 124,60,120\n"
"Qmvav 156,56,144\nQta 72,68,33024\ns_add_u32 m0, s96, 81920\ns_mov_b32 s95, 659456\nQmvav 156,60,152\nbuffer_load_dwordx4 v62, s[52:55], s95 offen lds\nQmvav 188,56,176\nQmvav 188,60,184\n"
"Qta 74,68,34048\nQmvav 220,56,208\ns_mov_b32 s95, 699392\nQmvav 220,60,216\nbuffer_load_dwordx4 v62, s[52:55], s95 offen offset:1024 lds\nQmvav 252,56,240\nQta 76,68,41216\nQmvav 252,60,248\n"
"Qmvav 100,56,84\nQmvav 100,60,116\nQta 78,68,42240\ns_add_u32 m0, s96, 90112\ns_mov_b32 s95, 659456\ns_waitcnt lgkmcnt(4)\nQmaav 224,64,84\nbuffer_load_dwordx4 v63, s[52:55], s95 offen lds\n"
"Qmaav 224,68,116\nQmaav 220,64,240\nQmaav 220,68,248\nQmaav 216,64,208\nQta 56,68,33280\ns_mov_b32 s95, 678912\nQmaav 216,68,216\nbuffer_load_dwordx4 v63, s[52:55], s95 offen offset:1024 lds\n"
"Qmaav 212,64,176\nQmaav 212,68,184\nQta 58,68,34304\nQmaav 208,64,144\ns_add_u32 m0, s96, 98304\ns_mov_b32 s95, 700416\nQmaav 208,68,152\nbuffer_load_dwordx4 v63, s[52:55], s95 offen lds\n"
"Qmaav 204,64,112\nQta 60,68,41472\nQmaav 204,68,120\nQmaav 200,64,80\nQmaav 200,68,88\nQta 62,68,42496\ns_waitcnt lgkmcnt(4)\nQmaav 228,72,80\n"
"Qmaav 228,76,88\ns_add_u32 s88, s71, 147456\nQmaav 232,72,112\nbuffer_store_dwordx4 v[104:107], v79, s[56:59], s88 offen offset:0 sc1\nQmaav 232,76,120\nQmaav 236,72,144\nQta 64,68,33536\nQmaav 236,76,152\n"
"Qmaav 240,72,176\nQmaav 240,76,184\nQta 66,68,34560\nQmaav 244,72,208\nQmaav 244,76,216\nQmaav 248,72,240\nQta 68,68,41728\nQmaav 248,76,248\n"
"Qmaav 252,72,84\nQmaav 252,76,116\nQta 70,68,42752\ns_waitcnt lgkmcnt(4)\nQmvav 172,56,84\nbuffer_store_dwordx4 v[136:139], v79, s[56:59], s88 offen offset:1024 sc1\nQmvav 172,60,116\nQmvav 140,56,240\n"
"Qmvav 140,60,248\nQmvav 108,56,208\nQta 72,68,49152\nQmvav 108,60,216\nQmvav 228,56,176\nQmvav 228,60,184\nQta 74,68,50176\nQmvav 196,56,144\n"
"Qmvav 196,60,152\nQmvav 164,56,112\nQta 76,68,57344\ns_mov_b32 s95, 719872\nQmvav 164,60,120\nbuffer_load_dwordx4 v63, s[52:55], s95 offen offset:1024 lds\nQmvav 132,56,80\nQmvav 132,60,88\n"
"Qta 78,68,58368\ns_waitcnt lgkmcnt(4)\nQmvav 204,64,80\ns_mov_b32 s31, 524288\nQmvav 204,68,88\nbuffer_load_dwordx4 v[80:83], v79, s[48:51], s31 offen offset:0\nQmvav 236,64,112\nQmvav 236,68,120\n"
"Qmaav 96,64,144\nQta 56,68,49408\nQmaav 96,68,152\nbuffer_load_dwordx4 v[112:115], v79, s[48:51], s31 offen offset:1024\nQmaav 100,64,176\nQmaav 100,68,184\nQta 58,68,50432\nQmaav 104,64,208\n"
"Qmaav 104,68,216\nbuffer_load_dwordx4 v[144:147], v79, s[48:51], s31 offen offset:2048\nQmaav 108,64,240\nQta 60,68,57600\nQmaav 108,68,248\nQmaav 112,64,84\nQmaav 112,68,116\nQta 62,68,58624\n"
"s_waitcnt vmcnt(13)\ns_waitcnt lgkmcnt(4)\nQmaaa 140,72,24\nbuffer_load_dwordx4 v[176:179], v79, s[48:51], s31 offen offset:3072\nQmaaa 140,76,52\nQmaaa 136,72,20\nQmaaa 136,76,48\nQmaaa 132,72,16\n"
"Qta 64,68,49664\ns_mov_b32 s31, 528384\nQmaaa 132,76,44\nbuffer_load_dwordx4 v[208:211], v79, s[48:51], s31 offen offset:0\nQmaaa 128,72,12\nQmaaa 128,76,40\nQta 66,68,50688\nQmaaa 124,72,8\n"
"Qmaaa 124,76,36\nbuffer_load_dwordx4 v[240:243], v79, s[48:51], s31 offen offset:1024\nQmaaa 120,72,4\nQta 68,68,57856\nQmaaa 120,76,32\nQmaaa 116,72,0\nQmaaa 116,76,28\nQta 70,68,58880\n"
"s_waitcnt lgkmcnt(4)\nQmaaa 144,56,0\nbuffer_load_dwordx4 v[84:87], v79, s[48:51], s31 offen offset:2048\nQmaaa 144,60,28\nQmaaa 148,56,4\nQmaaa 148,60,32\nQmaaa 152,56,8\nQta 72,68,49920\n"
"s_mov_b32 s31, 540672\nQmaaa 152,60,36\nbuffer_load_dwordx4 v[88:91], v79, s[48:51], s31 offen offset:0\nQmaaa 156,56,12\nQmaaa 156,60,40\nQta 74,68,50944\nQmaaa 160,56,16\nQmaaa 160,60,44\n"
"buffer_load_dwordx4 v[120:123], v79, s[48:51], s31 offen offset:1024\nQmaaa 164,56,20\nQta 76,68,58112\nQmaaa 164,60,48\nQmaaa 168,56,24\nQmaaa 168,60,52\nQta 78,68,59136\ns_waitcnt lgkmcnt(4)\n"
"Qmvaa 24,64,24\nbuffer_load_dwordx4 v[152:155], v79, s[48:51], s31 offen offset:2048\nQmvaa 24,68,52\nQmvaa 20,64,20\nQmvaa 20,68,48\nQmvaa 16,64,16\nQta 56,70,1024\nQmvaa 16,68,44\n"
"buffer_load_dwordx4 v[184:187], v79, s[48:51], s31 offen offset:3072\nQmvaa 12,64,12\nQmvaa 12,68,40\nQta 58,70,1536\nQmvaa 8,64,8\ns_mov_b32 s31, 544768\nQmvaa 8,68,36\nbuffer_load_dwordx4 v[216:219], v79, s[48:51], s31 offen offset:0\n"
"Qmvaa 4,64,4\nQta 60,70,2048\nv_add_u32 v75, 1, v75\nQmvaa 4,68,32\ns_waitcnt vmcnt(18)\nds_write_b32 v73, v75\nQmvaa 0,64,0\nQmvaa 0,68,28\n"
".Lpg16:\ns_cbranch_execz .Lpg20\nQta 62,70,2560\ns_waitcnt lgkmcnt(5)\nQmvaa 28,72,0\nbuffer_load_dwordx4 v[248:251], v79, s[48:51], s31 offen offset:1024\nQmvaa 28,76,28\nQmvaa 32,72,4\n"
"Qmvaa 32,76,32\nQmvaa 36,72,8\nQta 64,70,1280\nQmvaa 36,76,36\nds_read_b32 v66, v74\nQmvaa 40,72,12\nbuffer_load_dwordx4 v[116:119], v79, s[48:51], s31 offen offset:2048\nQmvaa 40,76,40\n"
"Qta 66,70,1792\nQmvaa 44,72,16\nQmvaa 44,76,44\ns_waitcnt lgkmcnt(1)\nv_cmp_gt_u32 vcc, v75, v66\nQmvaa 48,72,20\ns_cbranch_vccnz .Lso740\n.Lro740:\n"
"Qmvaa 48,76,48\nQta 68,70,2304\ns_add_u32 m0, s96, 106496\ns_mov_b32 s95, 741376\nQmvaa 52,72,24\nbuffer_load_dwordx4 v61, s[52:55], s95 offen lds\nQmvaa 52,76,52\ns_waitcnt lgkmcnt(4)\n"
"Qmaaa 196,56,24\nQta 70,70,2816\nQmaaa 196,60,52\ns_mov_b32 s95, 760832\nQmaaa 192,56,20\nbuffer_load_dwordx4 v61, s[52:55], s95 offen offset:1024 lds\nQmaaa 192,60,48\nQmaaa 188,56,16\n"
"Qta 72,70,9216\nQmaaa 188,60,44\ns_add_u32 m0, s96, 114688\ns_mov_b32 s95, 782336\nQmaaa 184,56,12\nbuffer_load_dwordx4 v61, s[52:55], s95 offen lds\nQmaaa 184,60,40\nQta 74,70,10240\n"
"Qmaaa 180,56,8\nQmaaa 180,60,36\nQmaaa 176,56,4\nQta 76,70,17408\ns_mov_b32 s95, 801792\nQmaaa 176,60,32\nbuffer_load_dwordx4 v61, s[52:55], s95 offen offset:1024 lds\nQmaaa 172,56,0\n"
"Qmaaa 172,60,28\nQta 78,70,18432\ns_waitcnt lgkmcnt(4)\nQmvaa 92,64,0\ns_add_u32 m0, s96, 122880\ns_mov_b32 s95, 741376\nQmvaa 92,68,28\nbuffer_load_dwordx4 v62, s[52:55], s95 offen lds\n"
"Qmvaa 124,64,4\nQmvaa 124,68,32\nQmvaa 156,64,8\nQta 56,70,9472\ns_mov_b32 s95, 781312\nQmvaa 156,68,36\nbuffer_load_dwordx4 v62, s[52:55], s95 offen offset:1024 lds\nQmvaa 188,64,12\n"
"Qmvaa 188,68,40\nQta 58,70,10496\nQmvaa 220,64,16\ns_add_u32 m0, s96, 0\ns_mov_b32 s95, 741376\nQmvaa 220,68,44\nbuffer_load_dwordx4 v63, s[52:55], s95 offen lds\nQmvaa 252,64,20\n"
"Qta 60,70,17664\nQmvaa 252,68,48\nQmvaa 100,64,24\nQmvaa 100,68,52\nQta 62,70,18688\ns_mov_b32 s95, 760832\ns_waitcnt lgkmcnt(4)\nQmaaa 224,72,24\n"
"buffer_load_dwordx4 v63, s[52:55], s95 offen offset:1024 lds\nQmaaa 224,76,52\nQmaaa 220,72,20\nQmaaa 220,76,48\nQmaaa 216,72,16\nQta 64,70,9728\ns_add_u32 m0, s96, 8192\ns_mov_b32 s95, 782336\n"
"Qmaaa 216,76,44\nbuffer_load_dwordx4 v63, s[52:55], s95 offen lds\nQmaaa 212,72,12\nQmaaa 212,76,40\nQta 66,70,10752\nQmaaa 208,72,8\ns_mov_b32 s95, 801792\nQmaaa 208,76,36\n"
"buffer_load_dwordx4 v63, s[52:55], s95 offen offset:1024 lds\nQmaaa 204,72,4\nQta 68,70,17920\nQmaaa 204,76,32\nQmaaa 200,72,0\nQmaaa 200,76,28\nQta 70,70,18944\ns_waitcnt lgkmcnt(4)\n"
"Qmaaa 228,56,0\nQmaaa 228,60,28\nQmaaa 232,56,4\nbuffer_store_dwordx4 v[168:171], v79, s[56:59], s88 offen offset:2048 sc1\nQmaaa 232,60,32\nQmaaa 236,56,8\nQta 72,70,9984\nQmaaa 236,60,36\n"
"Qmaaa 240,56,12\nQmaaa 240,60,40\nQta 74,70,11008\nQmaaa 244,56,16\nQmaaa 244,60,44\nQmaaa 248,56,20\nQta 76,70,18176\nQmaaa 248,60,48\n"
"Qmaaa 252,56,24\nQmaaa 252,60,52\nQta 78,70,19200\ns_waitcnt lgkmcnt(4)\nQmvaa 172,64,24\nbuffer_store_dwordx4 v[200:203], v79, s[56:59], s88 offen offset:3072 sc1\nQmvaa 172,68,52\nQmvaa 140,64,20\n"
"Qmvaa 140,68,48\nQmvaa 108,64,16\nQta 56,69,0\nQmvaa 108,68,44\nQmvaa 228,64,12\nQmvaa 228,68,40\nQta 58,69,1024\nQmvaa 196,64,8\n"
"Qmvaa 196,68,36\nQmvaa 164,64,4\nQta 60,69,8192\ns_add_u32 m0, s96, 16384\ns_mov_b32 s95, 987136\nQmvaa 164,68,32\nbuffer_load_dwordx4 v61, s[52:55], s95 offen lds\nQmvaa 132,64,0\n"
"Qmvaa 132,68,28\nQta 62,69,9216\ns_waitcnt lgkmcnt(4)\nQmvaa 204,72,0\ns_mov_b32 s95, 1006592\nQmvaa 204,76,28\nbuffer_load_dwordx4 v61, s[52:55], s95 offen offset:1024 lds\nQmvaa 236,72,4\n"
"Qmvaa 236,76,32\nQmaaa 96,72,8\nQta 64,69,256\ns_mov_b32 s31, 655360\nQmaaa 96,76,36\nbuffer_load_dwordx4 a[0:3], v79, s[48:51], s31 offen offset:0\nQmaaa 100,72,12\nQmaaa 100,76,40\n"
"Qta 66,69,1280\nQmaaa 104,72,16\nQmaaa 104,76,44\nbuffer_load_dwordx4 a[4:7], v79, s[48:51], s31 offen offset:1024\nQmaaa 108,72,20\nQta 68,69,8448\nQmaaa 108,76,48\nQmaaa 112,72,24\n"
"Qmaaa 112,76,52\nQta 70,69,9472\ns_waitcnt vmcnt(16)\ns_waitcnt lgkmcnt(4)\nQmaav 140,56,84\nbuffer_load_dwordx4 a[8:11], v79, s[48:51], s31 offen offset:2048\nQmaav 140,60,116\nQmaav 136,56,240\n"
"Qmaav 136,60,248\nQmaav 132,56,208\nQta 72,69,512\nQmaav 132,60,216\nbuffer_load_dwordx4 a[12:15], v79, s[48:51], s31 offen offset:3072\nQmaav 128,56,176\nQmaav 128,60,184\nQta 74,69,1536\n"
"Qmaav 124,56,144\ns_mov_b32 s31, 659456\nQmaav 124,60,152\nbuffer_load_dwordx4 a[16:19], v79, s[48:51], s31 offen offset:0\nQmaav 120,56,112\nQta 76,69,8704\nQmaav 120,60,120\nQmaav 116,56,80\n"
"Qmaav 116,60,88\nQta 78,69,9728\ns_waitcnt lgkmcnt(4)\nQmaav 144,64,80\nbuffer_load_dwordx4 a[20:23], v79, s[48:51], s31 offen offset:1024\nQmaav 144,68,88\nQmaav 148,64,112\nQmaav 148,68,120\n"
"Qmaav 152,64,144\nQta 56,69,768\nQmaav 152,68,152\nbuffer_load_dwordx4 a[24:27], v79, s[48:51], s31 offen offset:2048\nQmaav 156,64,176\nQmaav 156,68,184\nQta 58,69,1792\nQmaav 160,64,208\n"
"s_mov_b32 s31, 671744\nQmaav 160,68,216\nbuffer_load_dwordx4 a[28:31], v79, s[48:51], s31 offen offset:0\nQmaav 164,64,240\nQta 60,69,8960\nQmaav 164,68,248\nQmaav 168,64,84\nQmaav 168,68,116\n"
"Qta 62,69,9984\ns_waitcnt lgkmcnt(4)\nQmvav 24,72,84\nbuffer_load_dwordx4 a[32:35], v79, s[48:51], s31 offen offset:1024\nQmvav 24,76,116\nQmvav 20,72,240\nQmvav 20,76,248\nQmvav 16,72,208\n"
"Qta 64,69,16384\nQmvav 16,76,216\nbuffer_load_dwordx4 a[36:39], v79, s[48:51], s31 offen offset:2048\nQmvav 12,72,176\nQmvav 12,76,184\nQta 66,69,16896\nQmvav 8,72,144\nQmvav 8,76,152\n"
"buffer_load_dwordx4 a[40:43], v79, s[48:51], s31 offen offset:3072\nQmvav 4,72,112\nQta 68,69,17408\nv_add_u32 v75, 1, v75\nQmvav 4,76,120\ns_waitcnt vmcnt(19)\nds_write_b32 v73, v75\nQmvav 0,72,80\n"
"Qmvav 0,76,88\nQta 70,69,17920\ns_mov_b32 s31, 675840\ns_waitcnt lgkmcnt(5)\nQmvav 28,56,80\nbuffer_load_dwordx4 a[44:47], v79, s[48:51], s31 offen offset:0\nQmvav 28,60,88\nQmvav 32,56,112\n"
"Qmvav 32,60,120\nQmvav 36,56,144\nQta 72,69,16640\nQmvav 36,60,152\nds_read_b32 v66, v74\nQmvav 40,56,176\nbuffer_load_dwordx4 a[48:51], v79, s[48:51], s31 offen offset:1024\nQmvav 40,60,184\n"
"Qta 74,69,17152\nQmvav 44,56,208\nQmvav 44,60,216\ns_waitcnt lgkmcnt(1)\nv_cmp_gt_u32 vcc, v75, v66\nQmvav 48,56,240\ns_cbranch_vccnz .Lso880\n.Lro880:\n"
"Qmvav 48,60,248\nQta 76,69,17664\nQmvav 52,56,84\nbuffer_load_dwordx4 a[52:55], v79, s[48:51], s31 offen offset:2048\nQmvav 52,60,116\ns_waitcnt lgkmcnt(4)\nQmaav 196,64,84\nQta 78,69,18176\n"
"Qmaav 196,68,116\ns_add_u32 m0, s96, 24576\ns_mov_b32 s95, 1028096\nQmaav 192,64,240\nbuffer_load_dwordx4 v61, s[52:55], s95 offen lds\nQmaav 192,68,248\nQmaav 188,64,208\nQta 56,69,24576\n"
"Qmaav 188,68,216\ns_mov_b32 s95, 1047552\nQmaav 184,64,176\nbuffer_load_dwordx4 v61, s[52:55], s95 offen offset:1024 lds\nQmaav 184,68,184\nQta 58,69,25600\nQmaav 180,64,144\nQmaav 180,68,152\n"
"Qmaav 176,64,112\nQta 60,69,32768\ns_add_u32 m0, s96, 32768\ns_mov_b32 s95, 987136\nQmaav 176,68,120\nbuffer_load_dwordx4 v62, s[52:55], s95 offen lds\nQmaav 172,64,80\nQmaav 172,68,88\n"
"Qta 62,69,33792\ns_waitcnt lgkmcnt(4)\nQmvav 92,72,80\ns_mov_b32 s95, 1027072\nQmvav 92,76,88\nbuffer_load_dwordx4 v62, s[52:55], s95 offen offset:1024 lds\nQmvav 124,72,112\nQmvav 124,76,120\n"
"Qmvav 156,72,144\nQta 64,69,24832\ns_add_u32 m0, s96, 40960\ns_mov_b32 s95, 987136\nQmvav 156,76,152\nbuffer_load_dwordx4 v63, s[52:55], s95 offen lds\nQmvav 188,72,176\nQmvav 188,76,184\n"
"Qta 66,69,25856\nQmvav 220,72,208\ns_mov_b32 s95, 1006592\nQmvav 220,76,216\nbuffer_load_dwordx4 v63, s[52:55], s95 offen offset:1024 lds\nQmvav 252,72,240\nQta 68,69,33024\nQmvav 252,76,248\n"
"Qmvav 100,72,84\nQmvav 100,76,116\nQta 70,69,34048\ns_add_u32 m0, s96, 49152\ns_mov_b32 s95, 1028096\ns_waitcnt lgkmcnt(4)\nQmaav 224,56,84\nbuffer_load_dwordx4 v63, s[52:55], s95 offen lds\n"
"Qmaav 224,60,116\nQmaav 220,56,240\nQmaav 220,60,248\nQmaav 216,56,208\nQta 72,69,25088\ns_mov_b32 s95, 1047552\nQmaav 216,60,216\nbuffer_load_dwordx4 v63, s[52:55], s95 offen offset:1024 lds\n"
"Qmaav 212,56,176\nQmaav 212,60,184\nQta 74,69,26112\nQmaav 208,56,144\ns_add_u32 m0, s96, 57344\ns_mov_b32 s95, 1069056\nQmaav 208,60,152\nbuffer_load_dwordx4 v61, s[52:55], s95 offen lds\n"
"Qmaav 204,56,112\nQta 76,69,33280\nQmaav 204,60,120\nQmaav 200,56,80\nQmaav 200,60,88\nQta 78,69,34304\ns_waitcnt lgkmcnt(4)\nQmaav 228,64,80\n"
"Qmaav 228,68,88\ns_add_u32 s88, s71, 151552\nQmaav 232,64,112\nbuffer_store_dwordx4 v[232:235], v79, s[56:59], s88 offen offset:0 sc1\nQmaav 232,68,120\nQmaav 236,64,144\nQta 56,69,25344\nQmaav 236,68,152\n"
"Qmaav 240,64,176\nQmaav 240,68,184\nQta 58,69,26368\nQmaav 244,64,208\nQmaav 244,68,216\nQmaav 248,64,240\nQta 60,69,33536\nQmaav 248,68,248\n"
"Qmaav 252,64,84\nQmaav 252,68,116\nQta 62,69,34560\ns_waitcnt lgkmcnt(4)\nQmvav 172,72,84\nbuffer_store_dwordx4 v[212:215], v79, s[56:59], s88 offen offset:1024 sc1\nQmvav 172,76,116\nQmvav 140,72,240\n"
"Qmvav 140,76,248\nQmvav 108,72,208\nQta 64,69,40960\nQmvav 108,76,216\nQmvav 228,72,176\nQmvav 228,76,184\nQta 66,69,41984\nQmvav 196,72,144\n"
"Qmvav 196,76,152\nQmvav 164,72,112\nQta 68,69,49152\ns_mov_b32 s95, 1088512\nQmvav 164,76,120\nbuffer_load_dwordx4 v61, s[52:55], s95 offen offset:1024 lds\nQmvav 132,72,80\nQmvav 132,76,88\n"
"Qta 70,69,50176\ns_waitcnt lgkmcnt(4)\nQmvav 204,56,80\ns_mov_b32 s31, 786432\nQmvav 204,60,88\nbuffer_load_dwordx4 v[80:83], v79, s[48:51], s31 offen offset:0\nQmvav 236,56,112\nQmvav 236,60,120\n"
"Qmaav 96,56,144\nQta 72,69,41216\nQmaav 96,60,152\nbuffer_load_dwordx4 v[112:115], v79, s[48:51], s31 offen offset:1024\nQmaav 100,56,176\nQmaav 100,60,184\nQta 74,69,42240\nQmaav 104,56,208\n"
"Qmaav 104,60,216\nbuffer_load_dwordx4 v[144:147], v79, s[48:51], s31 offen offset:2048\nQmaav 108,56,240\nQta 76,69,49408\nQmaav 108,60,248\nQmaav 112,56,84\nv_accvgpr_read_b32 v96, a96\nv_accvgpr_read_b32 v97, a97\n"
"Qmaav 112,60,116\nQta 78,69,50432\nv_accvgpr_read_b32 v98, a98\ns_waitcnt vmcnt(15)\ns_waitcnt lgkmcnt(4)\nQmaaa 140,64,24\nbuffer_load_dwordx4 v[176:179], v79, s[48:51], s31 offen offset:3072\nQmaaa 140,68,52\n"
"v_accvgpr_read_b32 v99, a99\nv_accvgpr_read_b32 v128, a100\nQmaaa 136,64,20\nv_accvgpr_read_b32 v129, a101\nv_accvgpr_read_b32 v130, a102\nQmaaa 136,68,48\nv_accvgpr_read_b32 v131, a103\nv_accvgpr_read_b32 v160, a104\n"
"Qmaaa 132,64,16\nQta 56,69,41472\ns_mov_b32 s31, 790528\nQmaaa 132,68,44\nbuffer_load_dwordx4 v[208:211], v79, s[48:51], s31 offen offset:0\nQmaaa 128,64,12\nv_accvgpr_read_b32 v161, a105\nv_accvgpr_read_b32 v162, a106\n"
"Qmaaa 128,68,40\nQta 58,69,42496\nv_accvgpr_read_b32 v163, a107\nQmaaa 124,64,8\nv_accvgpr_read_b32 v192, a108\nv_accvgpr_read_b32 v193, a109\nQmaaa 124,68,36\nbuffer_load_dwordx4 v[240:243], v79, s[48:51], s31 offen offset:1024\n"
"Qmaaa 120,64,4\nQta 60,69,49664\nv_accvgpr_read_b32 v194, a110\nQmaaa 120,68,32\nv_accvgpr_read_b32 v195, a111\nv_accvgpr_read_b32 v224, a112\nQmaaa 116,64,0\nv_accvgpr_read_b32 v225, a113\n"
"v_accvgpr_read_b32 v226, a114\nQmaaa 116,68,28\nQta 62,69,50688\nv_accvgpr_read_b32 v227, a115\ns_waitcnt lgkmcnt(4)\nQmaaa 144,72,0\nbuffer_load_dwordx4 v[84:87], v79, s[48:51], s31 offen offset:2048\nQmaaa 144,76,28\n"
"Qmaaa 148,72,4\nQmaaa 148,76,32\nQmaaa 152,72,8\nQta 64,69,41728\ns_mov_b32 s31, 802816\nQmaaa 152,76,36\nbuffer_load_dwordx4 v[88:91], v79, s[48:51], s31 offen offset:0\nQmaaa 156,72,12\n"
"Qmaaa 156,76,40\nQta 66,69,42752\nQmaaa 160,72,16\nQmaaa 160,76,44\nbuffer_load_dwordx4 v[120:123], v79, s[48:51], s31 offen offset:1024\nQmaaa 164,72,20\nQta 68,69,49920\nQmaaa 164,76,48\n"
"Qmaaa 168,72,24\nQmaaa 168,76,52\nQta 70,69,50944\ns_waitcnt lgkmcnt(4)\nQmvaa 24,56,24\nbuffer_load_dwordx4 v[152:155], v79, s[48:51], s31 offen offset:2048\nQmvaa 24,60,52\nQmvaa 20,56,20\n"
"Qmvaa 20,60,48\nQmvaa 16,56,16\nQta 72,69,57344\nQmvaa 16,60,44\nbuffer_load_dwordx4 v[184:187], v79, s[48:51], s31 offen offset:3072\nQmvaa 12,56,12\nQmvaa 12,60,40\nQta 74,69,57856\n"
"Qmvaa 8,56,8\ns_mov_b32 s31, 806912\nQmvaa 8,60,36\nbuffer_load_dwordx4 v[216:219], v79, s[48:51], s31 offen offset:0\nQmvaa 4,56,4\nQta 76,69,58368\nv_add_u32 v75, 1, v75\nQmvaa 4,60,32\n"
"s_waitcnt vmcnt(20)\nds_write_b32 v73, v75\nQmvaa 0,56,0\nQmvaa 0,60,28\nQta 78,69,58880\ns_waitcnt lgkmcnt(5)\nQmvaa 28,64,0\nbuffer_load_dwordx4 v[248:251], v79, s[48:51], s31 offen offset:1024\n"
"Qmvaa 28,68,28\nQmvaa 32,64,4\nQmvaa 32,68,32\nQmvaa 36,64,8\nQta 56,69,57600\nQmvaa 36,68,36\nds_read_b32 v66, v74\nQmvaa 40,64,12\n"
"buffer_load_dwordx4 v[116:119], v79, s[48:51], s31 offen offset:2048\nQmvaa 40,68,40\nQta 58,69,58112\nQmvaa 44,64,16\n.Lpg17:\ns_cbranch_execz .Lpg21\nQmvaa 44,68,44\ns_waitcnt lgkmcnt(1)\n"
"v_cmp_gt_u32 vcc, v75, v66\nQmvaa 48,64,20\ns_cbranch_vccnz .Lso1020\n.Lro1020:\nQmvaa 48,68,48\nQta 60,69,58624\ns_add_u32 m0, s96, 132096\ns_mov_b32 s95, 1110016\n"
"Qmvaa 52,64,24\nbuffer_load_dwordx4 v61, s[52:55], s95 offen lds\nQmvaa 52,68,52\ns_waitcnt lgkmcnt(4)\nQmaaa 196,72,24\nQta 62,69,59136\nQmaaa 196,76,52\ns_mov_b32 s95, 1129472\n"
"Qmaaa 192,72,20\nbuffer_load_dwordx4 v61, s[52:55], s95 offen offset:1024 lds\nQmaaa 192,76,48\nQmaaa 188,72,16\nQta 64,68,0\nQmaaa 188,76,44\ns_add_u32 m0, s96, 140288\ns_mov_b32 s95, 1069056\n"
"Qmaaa 184,72,12\nbuffer_load_dwordx4 v62, s[52:55], s95 offen lds\nQmaaa 184,76,40\nQta 66,68,1024\nQmaaa 180,72,8\nQmaaa 180,76,36\nQmaaa 176,72,4\nQta 68,68,8192\n"
"s_mov_b32 s95, 1108992\nQmaaa 176,76,32\nbuffer_load_dwordx4 v62, s[52:55], s95 offen offset:1024 lds\nQmaaa 172,72,0\nQmaaa 172,76,28\nQta 70,68,9216\ns_waitcnt lgkmcnt(4)\nQmvaa 92,56,0\n"
"s_add_u32 m0, s96, 148480\ns_mov_b32 s95, 1069056\nQmvaa 92,60,28\nbuffer_load_dwordx4 v63, s[52:55], s95 offen lds\nQmvaa 124,56,4\nQmvaa 124,60,32\nQmvaa 156,56,8\nQta 72,68,256\n"
"s_mov_b32 s95, 1088512\nQmvaa 156,60,36\nbuffer_load_dwordx4 v63, s[52:55], s95 offen offset:1024 lds\nQmvaa 188,56,12\nQmvaa 188,60,40\nQta 74,68,1280\nQmvaa 220,56,16\ns_add_u32 m0, s96, 65536\n"
"s_mov_b32 s95, 1110016\nQmvaa 220,60,44\nbuffer_load_dwordx4 v63, s[52:55], s95 offen lds\nQmvaa 252,56,20\nQta 76,68,8448\nQmvaa 252,60,48\nQmvaa 100,56,24\nQmvaa 100,60,52\n"
"Qta 78,68,9472\ns_mov_b32 s95, 1129472\ns_waitcnt lgkmcnt(4)\nQmaaa 224,64,24\nbuffer_load_dwordx4 v63, s[52:55], s95 offen offset:1024 lds\nQmaaa 224,68,52\nQmaaa 220,64,20\nQmaaa 220,68,48\n"
"Qmaaa 216,64,16\nQta 56,68,512\ns_add_u32 m0, s96, 73728\ns_add_u32 s95, s67, 4096\nQmaaa 216,68,44\nbuffer_load_dwordx4 v61, s[52:55], s95 offen lds\nQmaaa 212,64,12\nQmaaa 212,68,40\n"
"Qta 58,68,1536\nQmaaa 208,64,8\ns_add_u32 s95, s67, 23552\nQmaaa 208,68,36\nbuffer_load_dwordx4 v61, s[52:55], s95 offen offset:1024 lds\nQmaaa 204,64,4\nQta 60,68,8704\nQmaaa 204,68,32\n"
"Qmaaa 200,64,0\nQmaaa 200,68,28\nQta 62,68,9728\ns_waitcnt lgkmcnt(4)\nQmaaa 228,72,0\nQmaaa 228,76,28\nQmaaa 232,72,4\nbuffer_store_dwordx4 v[244:247], v79, s[56:59], s88 offen offset:2048 sc1\n"
"Qmaaa 232,76,32\nQmaaa 236,72,8\nQta 64,68,768\nQmaaa 236,76,36\nQmaaa 240,72,12\nQmaaa 240,76,40\nQta 66,68,1792\nQmaaa 244,72,16\n"
"Qmaaa 244,76,44\nQmaaa 248,72,20\nQta 68,68,8960\nQmaaa 248,76,48\ns_add_u32 m0, s96, 81920\ns_add_u32 s95, s67, 45056\nQmaaa 252,72,24\nbuffer_load_dwordx4 v61, s[52:55], s95 offen lds\n"
"Qmaaa 252,76,52\nQta 70,68,9984\ns_waitcnt lgkmcnt(4)\nQmvaa 172,56,24\nQmvaa 172,60,52\ns_add_u32 s95, s67, 64512\nQmvaa 140,56,20\nbuffer_load_dwordx4 v61, s[52:55], s95 offen offset:1024 lds\n"
"Qmvaa 140,60,48\nQmvaa 108,56,16\nQta 72,68,16384\nQmvaa 108,60,44\ns_add_u32 m0, s96, 90112\ns_add_u32 s95, s67, 4096\nQmvaa 228,56,12\nbuffer_load_dwordx4 v62, s[52:55], s95 offen lds\n"
"Qmvaa 228,60,40\nQta 74,68,17408\nQmvaa 196,56,8\nQmvaa 196,60,36\nQmvaa 164,56,4\nQta 76,68,24576\ns_add_u32 s95, s67, 44032\nQmvaa 164,60,32\n"
"buffer_load_dwordx4 v62, s[52:55], s95 offen offset:1024 lds\nQmvaa 132,56,0\nQmvaa 132,60,28\nQta 78,68,25600\ns_waitcnt lgkmcnt(4)\nQmvaa 204,64,0\nbuffer_load_dword v78, v76, s[24:27], 0 offen sc1\ns_mov_b32 s31, 917504\n"
"Qmvaa 204,68,28\nbuffer_load_dwordx4 a[0:3], v79, s[48:51], s31 offen offset:0\nQmvaa 236,64,4\nQmvaa 236,68,32\nQmvaa 96,64,8\nQta 56,68,16640\nQmvaa 96,68,36\nbuffer_load_dwordx4 a[4:7], v79, s[48:51], s31 offen offset:1024\n"
"Qmvaa 128,64,12\nQmvaa 128,68,40\nQta 58,68,17664\nQmvaa 160,64,16\nQmvaa 160,68,44\nbuffer_load_dwordx4 a[8:11], v79, s[48:51], s31 offen offset:2048\nQmvaa 192,64,20\nQta 60,68,24832\n"
"Qmvaa 192,68,48\nQmvaa 224,64,24\nQmvaa 224,68,52\nQta 62,68,25856\ns_waitcnt vmcnt(19)\ns_waitcnt lgkmcnt(4)\nQmaav 140,72,84\nbuffer_load_dwordx4 a[12:15], v79, s[48:51], s31 offen offset:3072\n"
"Qmaav 140,76,116\nQmaav 136,72,240\nQmaav 136,76,248\nQmaav 132,72,208\nQta 64,68,16896\ns_mov_b32 s31, 921600\nQmaav 132,76,216\nbuffer_load_dwordx4 a[16:19], v79, s[48:51], s31 offen offset:0\n"
"Qmaav 128,72,176\nQmaav 128,76,184\nQta 66,68,17920\nQmaav 124,72,144\nQmaav 124,76,152\nbuffer_load_dwordx4 a[20:23], v79, s[48:51], s31 offen offset:1024\nQmaav 120,72,112\nQta 68,68,25088\n"
"Qmaav 120,76,120\nQmaav 116,72,80\nQmaav 116,76,88\nQta 70,68,26112\ns_waitcnt lgkmcnt(4)\nQmaav 144,56,80\nbuffer_load_dwordx4 a[24:27], v79, s[48:51], s31 offen offset:2048\nQmaav 144,60,88\n"
"Qmaav 148,56,112\nQmaav 148,60,120\nQmaav 152,56,144\nQta 72,68,17152\ns_mov_b32 s31, 933888\nQmaav 152,60,152\nbuffer_load_dwordx4 a[28:31], v79, s[48:51], s31 offen offset:0\nQmaav 156,56,176\n"
"Qmaav 156,60,184\nQta 74,68,18176\nQmaav 160,56,208\nQmaav 160,60,216\nbuffer_load_dwordx4 a[32:35], v79, s[48:51], s31 offen offset:1024\nQmaav 164,56,240\nQta 76,68,25344\nQmaav 164,60,248\n"
"Qmaav 168,56,84\nQmaav 168,60,116\nQta 78,68,26368\ns_waitcnt lgkmcnt(4)\nQmvav 24,64,84\nbuffer_load_dwordx4 a[36:39], v79, s[48:51], s31 offen offset:2048\nQmvav 24,68,116\nQmvav 20,64,240\n"
"Qmvav 20,68,248\nQmvav 16,64,208\nQta 56,68,32768\nQmvav 16,68,216\nbuffer_load_dwordx4 a[40:43], v79, s[48:51], s31 offen offset:3072\nQmvav 12,64,176\nQmvav 12,68,184\nQta 58,68,33280\n"
"Qmvav 8,64,144\ns_mov_b32 s31, 937984\nQmvav 8,68,152\nbuffer_load_dwordx4 a[44:47], v79, s[48:51], s31 offen offset:0\nQmvav 4,64,112\nQta 60,68,33792\nv_add_u32 v75, 1, v75\nQmvav 4,68,120\n"
"s_waitcnt vmcnt(24)\nds_write_b32 v73, v75\nQmvav 0,64,80\nQmvav 0,68,88\nQta 62,68,34304\ns_waitcnt lgkmcnt(5)\nQmvav 28,72,80\nbuffer_load_dwordx4 a[48:51], v79, s[48:51], s31 offen offset:1024\n"
"Qmvav 28,76,88\nQmvav 32,72,112\nQmvav 32,76,120\ns_waitcnt vmcnt(13)\nv_sub_u32 v66, v78, v65\nv_cmp_gt_u32 vcc, 2, v66\ns_and_b64 vcc, vcc, s[28:29]\ns_cbranch_scc1 .Lf2_w\n"
".Lf2_r:\nQmvav 36,72,144\nQta 64,68,33024\nQmvav 36,76,152\nds_read_b32 v66, v74\nQmvav 40,72,176\nbuffer_load_dwordx4 a[52:55], v79, s[48:51], s31 offen offset:2048\nQmvav 40,76,184\n"
"Qta 66,68,33536\nQmvav 44,72,208\nQmvav 44,76,216\ns_waitcnt lgkmcnt(1)\nv_cmp_gt_u32 vcc, v75, v66\nQmvav 48,72,240\ns_cbranch_vccnz .Lso1160\n.Lro1160:\n"
"Qmvav 48,76,248\nQta 68,68,34048\ns_add_u32 s31, s74, 0\nQmvav 52,72,84\nbuffer_load_dwordx4 a[80:83], v79, s[48:51], s31 offen offset:0 sc1\nQmvav 52,76,116\ns_waitcnt lgkmcnt(4)\nQmaav 196,56,84\n"
"Qta 70,68,34560\nQmaav 196,60,116\nQmaav 192,56,240\nbuffer_load_dwordx4 a[84:87], v79, s[48:51], s31 offen offset:1024 sc1\nQmaav 192,60,248\nQmaav 188,56,208\nQta 72,68,40960\nQmaav 188,60,216\n"
"Qmaav 184,56,176\nbuffer_load_dwordx4 a[88:91], v79, s[48:51], s31 offen offset:2048 sc1\nQmaav 184,60,184\nQta 74,68,41984\nQmaav 180,56,144\nQmaav 180,60,152\nQmaav 176,56,112\nQta 76,68,49152\n"
"Qmaav 176,60,120\nbuffer_load_dwordx4 a[92:95], v79, s[48:51], s31 offen offset:3072 sc1\nQmaav 172,56,80\nQmaav 172,60,88\nQta 78,68,50176\ns_waitcnt lgkmcnt(4)\nQmvav 92,64,80\ns_add_u32 s31, s74, 4096\n"
"Qmvav 92,68,88\nbuffer_load_dwordx4 a[96:99], v79, s[48:51], s31 offen offset:0 sc1\nQmvav 124,64,112\nQmvav 124,68,120\nQmvav 156,64,144\nQta 56,68,41216\nQmvav 156,68,152\nbuffer_load_dwordx4 a[100:103], v79, s[48:51], s31 offen offset:1024 sc1\n"
"Qmvav 188,64,176\nQmvav 188,68,184\nQta 58,68,42240\nQmvav 220,64,208\nQmvav 220,68,216\nbuffer_load_dwordx4 a[104:107], v79, s[48:51], s31 offen offset:2048 sc1\nQmvav 252,64,240\nQta 60,68,49408\n"
"Qmvav 252,68,248\nQmvav 100,64,84\nQmvav 100,68,116\nQta 62,68,50432\ns_add_u32 s31, s74, 16384\ns_waitcnt lgkmcnt(4)\nQmaav 224,72,84\nbuffer_load_dwordx4 a[108:111], v79, s[48:51], s31 offen offset:0 sc1\n"
"Qmaav 224,76,116\nQmaav 220,72,240\nQmaav 220,76,248\nQmaav 216,72,208\nQta 64,68,41472\nQmaav 216,76,216\nbuffer_load_dwordx4 a[112:115], v79, s[48:51], s31 offen offset:1024 sc1\nQmaav 212,72,176\n"
"Qmaav 212,76,184\nQta 66,68,42496\nQmaav 208,72,144\nQmaav 208,76,152\nbuffer_load_dwordx4 v[168:171], v79, s[48:51], s31 offen offset:2048 sc1\nQmaav 204,72,112\nQta 68,68,49664\nQmaav 204,76,120\n"
"Qmaav 200,72,80\nQmaav 200,76,88\nQta 70,68,50688\ns_waitcnt lgkmcnt(4)\nQmaav 228,56,80\nbuffer_load_dwordx4 v[200:203], v79, s[48:51], s31 offen offset:3072 sc1\nQmaav 228,60,88\nQmaav 232,56,112\n"
"Qmaav 232,60,120\nQmaav 236,56,144\nQta 72,68,41728\ns_add_u32 s31, s74, 20480\nQmaav 236,60,152\nbuffer_load_dwordx4 v[232:235], v79, s[48:51], s31 offen offset:0 sc1\nQmaav 240,56,176\nQmaav 240,60,184\n"
"Qta 74,68,42752\nQmaav 244,56,208\nQmaav 244,60,216\nbuffer_load_dwordx4 v[212:215], v79, s[48:51], s31 offen offset:1024 sc1\nQmaav 248,56,240\nQta 76,68,49920\nQmaav 248,60,248\nQmaav 252,56,84\n"
"Qmaav 252,60,116\nQta 78,68,50944\ns_waitcnt lgkmcnt(4)\nQmvav 172,64,84\nbuffer_load_dwordx4 v[244:247], v79, s[48:51], s31 offen offset:2048 sc1\nQmvav 172,68,116\nQmvav 140,64,240\nQmvav 140,68,248\n"
"Qmvav 108,64,208\nQta 56,68,57344\ns_add_u32 m0, s96, 98304\ns_add_u32 s95, s67, 4096\nQmvav 108,68,216\nbuffer_load_dwordx4 v63, s[52:55], s95 offen lds\nQmvav 228,64,176\nQmvav 228,68,184\n"
"Qta 58,68,58368\nQmvav 196,64,144\ns_add_u32 s95, s67, 23552\nQmvav 196,68,152\nbuffer_load_dwordx4 v63, s[52:55], s95 offen offset:1024 lds\nQmvav 164,64,112\nQta 60,70,1024\nQmvav 164,68,120\n"
"Qmvav 132,64,80\nQmvav 132,68,88\nQta 62,70,2048\ns_add_u32 m0, s96, 106496\ns_add_u32 s95, s67, 45056\ns_waitcnt lgkmcnt(4)\nQmvav 204,72,80\nbuffer_load_dwordx4 v63, s[52:55], s95 offen lds\n"
"Qmvav 204,76,88\nQmvav 236,72,112\nQmvav 236,76,120\nQmvav 96,72,144\nQta 64,68,57600\ns_add_u32 s95, s67, 64512\nQmvav 96,76,152\nbuffer_load_dwordx4 v63, s[52:55], s95 offen offset:1024 lds\n"
"Qmvav 128,72,176\nQmvav 128,76,184\nQta 66,68,58624\nQmvav 160,72,208\ns_add_u32 m0, s96, 114688\ns_add_u32 s95, s72, 4096\nQmvav 160,76,216\nbuffer_load_dwordx4 v61, s[52:55], s95 offen lds\n"
"Qmvav 192,72,240\nQta 68,70,1280\nQmvav 192,76,248\nQmvav 224,72,84\nQmvav 224,76,116\nQta 70,70,2304\ns_add_u32 s95, s72, 23552\ns_waitcnt vmcnt(19)\n"
"s_waitcnt lgkmcnt(4)\nQmaaa 140,56,24\nbuffer_load_dwordx4 v61, s[52:55], s95 offen offset:1024 lds\nQmaaa 140,60,52\nQmaaa 136,56,20\nQmaaa 136,60,48\nQmaaa 132,56,16\nQta 72,68,57856\n"
"s_add_u32 m0, s96, 122880\ns_add_u32 s95, s72, 45056\nQmaaa 132,60,44\nbuffer_load_dwordx4 v61, s[52:55], s95 offen lds\nQmaaa 128,56,12\nQmaaa 128,60,40\nQta 74,68,58880\nQmaaa 124,56,8\n"
"s_add_u32 s95, s72, 64512\nQmaaa 124,60,36\nbuffer_load_dwordx4 v61, s[52:55], s95 offen offset:1024 lds\nQmaaa 120,56,4\nQta 76,70,1536\nQmaaa 120,60,32\nQmaaa 116,56,0\nQmaaa 116,60,28\n"
"Qta 78,70,2560\ns_add_u32 m0, s96, 0\ns_add_u32 s95, s72, 4096\ns_waitcnt lgkmcnt(4)\nQmaaa 144,64,0\nbuffer_load_dwordx4 v62, s[52:55], s95 offen lds\nQmaaa 144,68,28\nQmaaa 148,64,4\n"
"Qmaaa 148,68,32\nQmaaa 152,64,8\nQta 56,68,58112\ns_add_u32 s95, s72, 44032\nQmaaa 152,68,36\nbuffer_load_dwordx4 v62, s[52:55], s95 offen offset:1024 lds\nQmaaa 156,64,12\nQmaaa 156,68,40\n"
"Qta 58,68,59136\nQmaaa 160,64,16\ns_add_u32 m0, s96, 8192\ns_add_u32 s95, s72, 4096\nQmaaa 160,68,44\nbuffer_load_dwordx4 v63, s[52:55], s95 offen lds\nQmaaa 164,64,20\nQta 60,70,1792\n"
"Qmaaa 164,68,48\nQmaaa 168,64,24\nQmaaa 168,68,52\nQta 62,70,2816\ns_add_u32 s95, s72, 23552\ns_waitcnt lgkmcnt(4)\nQmvaa 24,72,24\nbuffer_load_dwordx4 v63, s[52:55], s95 offen offset:1024 lds\n"
"Qmvaa 24,76,52\nQmvaa 20,72,20\nQmvaa 20,76,48\nQmvaa 16,72,16\nQta 64,70,9216\ns_add_u32 s31, s75, 0\nQmvaa 16,76,44\nbuffer_load_dwordx4 v[80:83], v79, s[48:51], s31 offen offset:0 sc1\n"
"Qmvaa 12,72,12\nQmvaa 12,76,40\nQta 66,70,9728\nQmvaa 8,72,8\nQmvaa 8,76,36\nbuffer_load_dwordx4 v[112:115], v79, s[48:51], s31 offen offset:1024 sc1\nQmvaa 4,72,4\nQta 68,70,10240\n"
"v_add_u32 v75, 1, v75\nQmvaa 4,76,32\ns_waitcnt vmcnt(43)\nds_write_b32 v73, v75\nQmvaa 0,72,0\nQmvaa 0,76,28\nQta 70,70,10752\ns_waitcnt lgkmcnt(5)\n"
"Qmvaa 28,56,0\nbuffer_load_dwordx4 v[144:147], v79, s[48:51], s31 offen offset:2048 sc1\nQmvaa 28,60,28\nQmvaa 32,56,4\nQmvaa 32,60,32\nQmvaa 36,56,8\nQta 72,70,9472\nQmvaa 36,60,36\n"
"ds_read_b32 v66, v74\nQmvaa 40,56,12\nbuffer_load_dwordx4 v[176:179], v79, s[48:51], s31 offen offset:3072 sc1\nQmvaa 40,60,40\nQta 74,70,9984\nQmvaa 44,56,16\nv_add_u32 v67, 3, v65\nQmvaa 44,60,44\n"
"s_waitcnt lgkmcnt(1)\nv_cmp_gt_u32 vcc, v75, v66\nQmvaa 48,56,20\ns_cbranch_vccnz .Lso1300\n.Lro1300:\nQmvaa 48,60,48\nQta 76,70,10496\ns_add_u32 s31, s75, 4096\n"
"Qmvaa 52,56,24\nbuffer_load_dwordx4 v[208:211], v79, s[48:51], s31 offen offset:0 sc1\nQmvaa 52,60,52\nbuffer_store_dword v67, v77, s[24:27], 0 offen sc1\ns_waitcnt lgkmcnt(4)\nQmaaa 196,64,24\nQta 78,70,11008\nQmaaa 196,68,52\n"
"Qmaaa 192,64,20\nbuffer_load_dwordx4 v[240:243], v79, s[48:51], s31 offen offset:1024 sc1\nQmaaa 192,68,48\nQmaaa 188,64,16\nQta 56,70,17408\nQmaaa 188,68,44\nQmaaa 184,64,12\nbuffer_load_dwordx4 v[84:87], v79, s[48:51], s31 offen offset:2048 sc1\n"
".Lpg18:\ns_cbranch_execz .Lpg22\nQmaaa 184,68,40\nQta 58,70,18432\nQmaaa 180,64,8\nQmaaa 180,68,36\nQmaaa 176,64,4\nQta 60,69,0\n"
"s_add_u32 s31, s75, 16384\nQmaaa 176,68,32\nbuffer_load_dwordx4 v[88:91], v79, s[48:51], s31 offen offset:0 sc1\nQmaaa 172,64,0\nQmaaa 172,68,28\nQta 62,69,1024\ns_waitcnt lgkmcnt(4)\nQmvaa 92,72,0\n"
"Qmvaa 92,76,28\nbuffer_load_dwordx4 v[120:123], v79, s[48:51], s31 offen offset:1024 sc1\nQmvaa 124,72,4\nQmvaa 124,76,32\nQmvaa 156,72,8\nQta 64,70,17664\nQmvaa 156,76,36\nbuffer_load_dwordx4 v[152:155], v79, s[48:51], s31 offen offset:2048 sc1\n"
"Qmvaa 188,72,12\nQmvaa 188,76,40\nQta 66,70,18688\nQmvaa 220,72,16\nQmvaa 220,76,44\nbuffer_load_dwordx4 v[184:187], v79, s[48:51], s31 offen offset:3072 sc1\nQmvaa 252,72,20\nQta 68,69,256\n"
"Qmvaa 252,76,48\nQmvaa 100,72,24\nQmvaa 100,76,52\nQta 70,69,1280\ns_add_u32 s31, s75, 20480\ns_waitcnt lgkmcnt(4)\nQmaaa 224,56,24\nbuffer_load_dwordx4 v[216:219], v79, s[48:51], s31 offen offset:0 sc1\n"
"Qmaaa 224,60,52\nQmaaa 220,56,20\nQmaaa 220,60,48\nQmaaa 216,56,16\nQta 72,70,17920\nQmaaa 216,60,44\nbuffer_load_dwordx4 v[248:251], v79, s[48:51], s31 offen offset:1024 sc1\nQmaaa 212,56,12\n"
"Qmaaa 212,60,40\nQta 74,70,18944\nQmaaa 208,56,8\nQmaaa 208,60,36\nbuffer_load_dwordx4 v[116:119], v79, s[48:51], s31 offen offset:2048 sc1\nQmaaa 204,56,4\nQta 76,69,512\nQmaaa 204,60,32\n"
"Qmaaa 200,56,0\nQmaaa 200,60,28\nQta 78,69,1536\ns_add_u32 m0, s96, 16384\ns_add_u32 s95, s72, 45056\ns_waitcnt lgkmcnt(4)\nQmaaa 228,64,0\nbuffer_load_dwordx4 v63, s[52:55], s95 offen lds\n"
"Qmaaa 228,68,28\nQmaaa 232,64,4\nQmaaa 232,68,32\nQmaaa 236,64,8\nQta 56,70,18176\ns_add_u32 s95, s72, 64512\nQmaaa 236,68,36\nbuffer_load_dwordx4 v63, s[52:55], s95 offen offset:1024 lds\n"
"Qmaaa 240,64,12\nQmaaa 240,68,40\nQta 58,70,19200\nQmaaa 244,64,16\ns_add_u32 m0, s96, 24576\ns_add_u32 s95, s73, 4096\nQmaaa 244,68,44\nbuffer_load_dwordx4 v61, s[52:55], s95 offen lds\n"
"Qmaaa 248,64,20\nQta 60,69,768\nQmaaa 248,68,48\nQmaaa 252,64,24\nQmaaa 252,68,52\nQta 62,69,1792\ns_add_u32 s95, s73, 23552\ns_waitcnt lgkmcnt(4)\n"
"Qmvaa 172,72,24\nbuffer_load_dwordx4 v61, s[52:55], s95 offen offset:1024 lds\nQmvaa 172,76,52\nQmvaa 140,72,20\nQmvaa 140,76,48\nQmvaa 108,72,16\nQta 64,69,8192\ns_add_u32 m0, s96, 32768\n"
"s_add_u32 s95, s73, 45056\nQmvaa 108,76,44\nbuffer_load_dwordx4 v61, s[52:55], s95 offen lds\nQmvaa 228,72,12\nQmvaa 228,76,40\nQta 66,69,9216\nQmvaa 196,72,8\ns_add_u32 s95, s73, 64512\n"
"Qmvaa 196,76,36\nbuffer_load_dwordx4 v61, s[52:55], s95 offen offset:1024 lds\nQmvaa 164,72,4\nQta 68,69,16384\nQmvaa 164,76,32\nQmvaa 132,72,0\nQmvaa 132,76,28\nQta 70,69,17408\n"
"s_add_u32 m0, s96, 40960\ns_add_u32 s95, s73, 4096\ns_waitcnt lgkmcnt(4)\nQmvaa 204,56,0\nbuffer_load_dwordx4 v62, s[52:55], s95 offen lds\nQmvaa 204,60,28\nQmvaa 236,56,4\nQmvaa 236,60,32\n"
"Qmvaa 96,56,8\nQta 72,69,8448\ns_add_u32 s95, s73, 44032\nQmvaa 96,60,36\nbuffer_load_dwordx4 v62, s[52:55], s95 offen offset:1024 lds\nQmvaa 128,56,12\nQmvaa 128,60,40\nQta 74,69,9472\n"
"Qmvaa 160,56,16\ns_add_u32 m0, s96, 49152\ns_add_u32 s95, s73, 4096\nQmvaa 160,60,44\nbuffer_load_dwordx4 v63, s[52:55], s95 offen lds\nQmvaa 192,56,20\nQta 76,69,16640\nQmvaa 192,60,48\n"
"Qmvaa 224,56,24\nQmvaa 224,60,52\nQta 78,69,17664\ns_add_u32 s95, s73, 23552\ns_waitcnt vmcnt(36)\ns_waitcnt lgkmcnt(4)\nQmaaa 140,64,104\nbuffer_load_dwordx4 v63, s[52:55], s95 offen offset:1024 lds\n"
"Qmaav 140,68,244\nQmaaa 136,64,100\nQmaav 136,68,212\nQmaaa 132,64,96\nQta 56,69,8704\ns_add_u32 s31, s77, 0\nQmaav 132,68,232\nbuffer_load_dwordx4 a[0:3], v79, s[48:51], s31 offen offset:0 sc1\n"
"Qmaaa 128,64,92\nQmaav 128,68,200\nQta 58,69,9728\nQmaaa 124,64,88\nQmaav 124,68,168\nbuffer_load_dwordx4 a[4:7], v79, s[48:51], s31 offen offset:1024 sc1\nQmaaa 120,64,84\nQta 60,69,16896\n"
"Qmaaa 120,68,112\nQmaaa 116,64,80\nQmaaa 116,68,108\nQta 62,69,17920\ns_waitcnt lgkmcnt(4)\nQmaaa 144,72,80\nbuffer_load_dwordx4 a[8:11], v79, s[48:51], s31 offen offset:2048 sc1\nQmaaa 144,76,108\n"
"Qmaaa 148,72,84\nQmaaa 148,76,112\nQmaaa 152,72,88\nQta 64,69,8960\nQmaav 152,76,168\nbuffer_load_dwordx4 a[12:15], v79, s[48:51], s31 offen offset:3072 sc1\nQmaaa 156,72,92\nQmaav 156,76,200\n"
"Qta 66,69,9984\nQmaaa 160,72,96\ns_add_u32 s31, s77, 4096\nQmaav 160,76,232\nbuffer_load_dwordx4 a[16:19], v79, s[48:51], s31 offen offset:0 sc1\nQmaaa 164,72,100\nQta 68,69,17152\nQmaav 164,76,212\n"
"Qmaaa 168,72,104\nQmaav 168,76,244\nQta 70,69,18176\ns_waitcnt lgkmcnt(4)\nQmvaa 24,56,104\nbuffer_load_dwordx4 a[20:23], v79, s[48:51], s31 offen offset:1024 sc1\nQmvav 24,60,244\nQmvaa 20,56,100\n"
"Qmvav 20,60,212\nQmvaa 16,56,96\nQta 72,69,24576\nQmvav 16,60,232\nbuffer_load_dwordx4 a[24:27], v79, s[48:51], s31 offen offset:2048 sc1\nQmvaa 12,56,92\nQmvav 12,60,200\nQta 74,69,25088\n"
"Qmvaa 8,56,88\ns_add_u32 s31, s77, 16384\nQmvav 8,60,168\nbuffer_load_dwordx4 a[28:31], v79, s[48:51], s31 offen offset:0 sc1\nQmvaa 4,56,84\nQta 76,69,25600\nv_add_u32 v75, 1, v75\nQmvaa 4,60,112\n"
"s_waitcnt vmcnt(43)\nds_write_b32 v73, v75\nQmvaa 0,56,80\nQmvaa 0,60,108\nQta 78,69,26112\ns_waitcnt lgkmcnt(5)\nQmvaa 28,64,80\nbuffer_load_dwordx4 a[32:35], v79, s[48:51], s31 offen offset:1024 sc1\n"
"Qmvaa 28,68,108\nQmvaa 32,64,84\nQmvaa 32,68,112\nQmvaa 36,64,88\nQta 56,69,24832\nQmvav 36,68,168\nds_read_b32 v66, v74\nQmvaa 40,64,92\n"
"buffer_load_dwordx4 a[36:39], v79, s[48:51], s31 offen offset:2048 sc1\nQmvav 40,68,200\nQta 58,69,25344\nQmvaa 44,64,96\nQmvav 44,68,232\ns_waitcnt lgkmcnt(1)\nv_cmp_gt_u32 vcc, v75, v66\nQmvaa 48,64,100\n"
"s_cbranch_vccnz .Lso1440\n.Lro1440:\nQmvav 48,68,212\nQta 60,69,25856\nv_add_u32 v75, 1, v75\nQmvaa 52,64,104\ns_waitcnt vmcnt(39)\nds_write_b32 v73, v75\n"
"Qmvav 52,68,244\nbuffer_load_dwordx4 a[40:43], v79, s[48:51], s31 offen offset:3072 sc1\ns_waitcnt lgkmcnt(5)\nQmaaa 196,72,104\nQta 62,69,26368\nQmaav 196,76,244\nQmaaa 192,72,100\ns_add_u32 s31, s77, 20480\n"
"Qmaav 192,76,212\nbuffer_load_dwordx4 a[44:47], v79, s[48:51], s31 offen offset:0 sc1\nQmaaa 188,72,96\nQta 64,69,32768\nQmaav 188,76,232\nQmaaa 184,72,92\nds_read_b32 v66, v74\nQmaav 184,76,200\n"
"Qta 66,69,33792\nQmaaa 180,72,88\nbuffer_load_dwordx4 a[48:51], v79, s[48:51], s31 offen offset:1024 sc1\nQmaav 180,76,168\nbuffer_load_dword v78, v76, s[24:27], 0 offen sc1\nQmaaa 176,72,84\ns_waitcnt lgkmcnt(1)\nv_cmp_gt_u32 vcc, v75, v66\n"
"Qmaaa 176,76,112\ns_cbranch_vccnz .Lso1455\n.Lro1455:\nQmaaa 172,72,80\nQta 68,69,40960\nQmaaa 172,76,108\nbuffer_load_dwordx4 a[52:55], v79, s[48:51], s31 offen offset:2048 sc1\ns_waitcnt lgkmcnt(4)\n"
"Qmvaa 92,56,80\nQmvaa 92,60,108\nQta 70,69,41984\nQmvaa 124,56,84\ns_add_u32 m0, s96, 57344\ns_add_u32 s95, s73, 45056\nQmvaa 124,60,112\nbuffer_load_dwordx4 v63, s[52:55], s95 offen lds\n"
"Qmvaa 156,56,88\nQta 72,69,33024\nQmvav 156,60,168\nQmvaa 188,56,92\nQmvav 188,60,200\nQta 74,69,34048\ns_add_u32 s95, s73, 64512\nQmvaa 220,56,96\n"
"buffer_load_dwordx4 v63, s[52:55], s95 offen offset:1024 lds\nQmvav 220,60,232\nQmvaa 252,56,100\nQta 76,69,41216\nQmvav 252,60,212\nQmvaa 100,56,104\nQmvav 100,60,244\nQta 78,69,42240\n"
"s_waitcnt lgkmcnt(4)\nQmaaa 224,64,104\nQmaav 224,68,244\nQmaaa 220,64,100\nQmaav 220,68,212\nQmaaa 216,64,96\nQta 56,69,33280\nQmaav 216,68,232\n"
"Qmaaa 212,64,92\nQmaav 212,68,200\nQta 58,69,34304\nQmaaa 208,64,88\nQmaav 208,68,168\nQmaaa 204,64,84\nQta 60,69,41472\nQmaaa 204,68,112\n"
"Qmaaa 200,64,80\nQmaaa 200,68,108\nQta 62,69,42496\ns_waitcnt lgkmcnt(4)\nQmaaa 228,72,80\nQmaaa 228,76,108\nQmaaa 232,72,84\nQmaaa 232,76,112\n"
"Qmaaa 236,72,88\nQta 64,69,33536\nQmaav 236,76,168\nQmaaa 240,72,92\nQmaav 240,76,200\nQta 66,69,34560\nQmaaa 244,72,96\nQmaav 244,76,232\n"
"Qmaaa 248,72,100\nQta 68,69,41728\nQmaav 248,76,212\nQmaaa 252,72,104\nQmaav 252,76,244\nQta 70,69,42752\ns_waitcnt lgkmcnt(4)\nQmvaa 172,56,104\n"
"Qmvav 172,60,244\nQmvaa 140,56,100\nQmvav 140,60,212\nQmvaa 108,56,96\nQta 72,69,49152\nQmvav 108,60,232\nQmvaa 228,56,92\nQmvav 228,60,200\n"
"Qta 74,69,50176\nQmvaa 196,56,88\nQmvav 196,60,168\nQmvaa 164,56,84\nQta 76,69,57344\nQmvaa 164,60,112\ns_waitcnt vmcnt(3)\nv_sub_u32 v66, v78, v65\n"
"v_cmp_gt_u32 vcc, 3, v66\ns_and_b64 vcc, vcc, s[28:29]\ns_cbranch_scc1 .Lf3_w\n.Lf3_r:\nv_add_u32 v75, 1, v75\nQmvaa 132,56,80\ns_waitcnt vmcnt(42)\nds_write_b32 v73, v75\n"
"Qmvaa 132,60,108\nQta 78,69,58368\ns_waitcnt lgkmcnt(5)\nQmvaa 204,64,80\ns_add_u32 s31, s74, 131072\nQmvaa 204,68,108\nbuffer_load_dwordx4 a[80:83], v79, s[48:51], s31 offen offset:0 sc1\nQmvaa 236,64,84\n"
"Qmvaa 236,68,112\ns_bitcmp1_b32 %19, 2\ns_cbranch_scc1 .Lrole1\nQmvaa 96,64,88\nQta 56,69,49408\ns_lshl_b32 s46, %19, 4\nQmvav 96,68,168\nbuffer_load_dwordx4 a[84:87], v79, s[48:51], s31 offen offset:1024 sc1\n"
"Qmvaa 128,64,92\nds_read_b32 v66, v74\ns_add_u32 s46, s46, 128\nQmvav 128,68,200\nQta 58,69,50432\nv_mov_b32 v77, 0x7ffff000\nQmvaa 160,64,96\nQmvav 160,68,232\n"
"buffer_load_dwordx4 a[88:91], v79, s[48:51], s31 offen offset:2048 sc1\nQmvaa 192,64,100\ns_waitcnt lgkmcnt(1)\nv_cmp_gt_u32 vcc, v75, v66\nQmvav 192,68,212\ns_cbranch_vccnz .Lso1525r0\n.Lro1525r0:\nQmvaa 224,64,104\n"
"Qta 60,69,57600\nQmvav 224,68,244\nbuffer_load_dwordx4 a[92:95], v79, s[48:51], s31 offen offset:3072 sc1\ns_waitcnt vmcnt(31)\ns_waitcnt lgkmcnt(4)\nQmaav 140,72,84\nQmaav 140,76,116\nQta 62,69,58624\n"
"Qmaav 136,72,240\ns_add_u32 m0, s96, 65536\ns_add_u32 s95, s67, 86016\nQmaav 136,76,248\nbuffer_load_dwordx4 v63, s[52:55], s95 offen lds\nQmaav 132,72,208\nQta 64,69,49664\nQmaav 132,76,216\n"
"Qmaav 128,72,176\nQmaav 128,76,184\nQta 66,69,50688\ns_add_u32 s95, s67, 105472\nQmaav 124,72,144\nbuffer_load_dwordx4 v63, s[52:55], s95 offen offset:1024 lds\nQmaav 124,76,152\nQmaav 120,72,112\n"
"Qta 68,69,57856\nQmaav 120,76,120\ns_add_u32 m0, s96, 73728\ns_add_u32 s95, s67, 126976\nQmaav 116,72,80\nbuffer_load_dwordx4 v63, s[52:55], s95 offen lds\nQmaav 116,76,88\nQta 70,69,58880\n"
"s_waitcnt lgkmcnt(4)\nQmaav 144,56,80\nQmaav 144,60,88\ns_add_u32 s95, s67, 146432\nQmaav 148,56,112\nbuffer_load_dwordx4 v63, s[52:55], s95 offen offset:1024 lds\nQmaav 148,60,120\nQmaav 152,56,144\n"
"Qta 72,69,49920\nQmaav 152,60,152\ns_add_u32 m0, s96, 81920\ns_add_u32 s95, s72, 86016\nQmaav 156,56,176\nbuffer_load_dwordx4 v63, s[52:55], s95 offen lds\nQmaav 156,60,184\nQta 74,69,50944\n"
"Qmaav 160,56,208\nQmaav 160,60,216\nQmaav 164,56,240\nQta 76,69,58112\ns_add_u32 s95, s72, 105472\nQmaav 164,60,248\nbuffer_load_dwordx4 v63, s[52:55], s95 offen offset:1024 lds\nQmaav 168,56,84\n"
"Qmaav 168,60,116\nQta 78,69,59136\ns_waitcnt lgkmcnt(4)\nQmvav 24,64,84\ns_add_u32 m0, s96, 90112\ns_add_u32 s95, s72, 126976\nQmvav 24,68,116\nbuffer_load_dwordx4 v63, s[52:55], s95 offen lds\n"
"Qmvav 20,64,240\nQmvav 20,68,248\nQmvav 16,64,208\nQta 56,68,0\ns_add_u32 s95, s72, 146432\nQmvav 16,68,216\nbuffer_load_dwordx4 v63, s[52:55], s95 offen offset:1024 lds\nQmvav 12,64,176\n"
"Qmvav 12,68,184\nQta 58,68,512\nQmvav 8,64,144\ns_add_u32 s31, s74, 135168\nQmvav 8,68,152\nbuffer_load_dwordx4 a[96:99], v79, s[48:51], s31 offen offset:0 sc1\nQmvav 4,64,112\nQta 60,68,1024\n"
"Qmvav 4,68,120\nQmvav 0,64,80\nQmvav 0,68,88\nQta 62,68,1536\ns_waitcnt lgkmcnt(4)\nQmvav 28,72,80\nbuffer_load_dwordx4 a[100:103], v79, s[48:51], s31 offen offset:1024 sc1\nQmvav 28,76,88\n"
"Qmvav 32,72,112\nQmvav 32,76,120\nQmvav 36,72,144\nQta 64,68,256\nQmvav 36,76,152\nbuffer_load_dwordx4 a[104:107], v79, s[48:51], s31 offen offset:2048 sc1\nQmvav 40,72,176\nQmvav 40,76,184\n"
"Qta 66,68,768\nQmvav 44,72,208\ns_add_u32 s31, s74, 147456\nv_cmp_eq_u32 s[44:45], s46, v60\ns_nop 1\nv_cndmask_b32 v77, v77, v76, s[44:45]\nv_add_u32 v67, 1, v65\ns_xor_b32 s46, %19, 12\n"
"s_lshl_b32 s28, 1, s46\nQmvav 44,76,216\nbuffer_load_dwordx4 a[108:111], v79, s[48:51], s31 offen offset:0 sc1\nQmvav 48,72,240\nQta 68,68,1280\nQmvav 48,76,248\nv_add_u32 v75, 1, v75\nQmvav 52,72,84\n"
"s_waitcnt vmcnt(37)\nds_write_b32 v73, v75\nQmvav 52,76,116\nQta 70,68,1792\ns_waitcnt lgkmcnt(5)\nQmaav 196,56,84\nbuffer_load_dwordx4 a[112:115], v79, s[48:51], s31 offen offset:1024 sc1\nQmaav 196,60,116\n"
"Qmaav 192,56,240\nQmaav 192,60,248\nQmaav 188,56,208\nQta 72,68,8192\nQmaav 188,60,216\nbuffer_load_dwordx4 v[168:171], v79, s[48:51], s31 offen offset:2048 sc1\nQmaav 184,56,176\nds_read_b32 v66, v74\n"
"Qmaav 184,60,184\nQta 74,68,9216\nQmaav 180,56,144\nQmaav 180,60,152\nbuffer_load_dwordx4 v[200:203], v79, s[48:51], s31 offen offset:3072 sc1\nQmaav 176,56,112\ns_waitcnt lgkmcnt(1)\nv_cmp_gt_u32 vcc, v75, v66\n"
"Qmaav 176,60,120\ns_cbranch_vccnz .Lso1595r0\n.Lro1595r0:\nQmaav 172,56,80\nQta 76,68,16384\ns_add_u32 s31, s74, 151552\nQmaav 172,60,88\nbuffer_load_dwordx4 v[232:235], v79, s[48:51], s31 offen offset:0 sc1\n"
"s_waitcnt lgkmcnt(4)\nQmvav 92,64,80\nQmvav 92,68,88\nQta 78,68,17408\nQmvav 124,64,112\nQmvav 124,68,120\nbuffer_load_dwordx4 v[212:215], v79, s[48:51], s31 offen offset:1024 sc1\nQmvav 156,64,144\n"
"Qta 56,68,8448\nQmvav 156,68,152\nQmvav 188,64,176\n.Lpg19:\ns_cbranch_execz .Lpg23\nQmvav 188,68,184\nQta 58,68,9472\nQmvav 220,64,208\n"
"buffer_load_dwordx4 v[244:247], v79, s[48:51], s31 offen offset:2048 sc1\nQmvav 220,68,216\nQmvav 252,64,240\nQta 60,68,16640\nQmvav 252,68,248\ns_add_u32 m0, s91, 0\ns_add_u32 s31, s77, 131072\nQmvav 100,64,84\n"
"buffer_load_dwordx4 v79, s[48:51], s31 offen sc1 lds\nQmvav 100,68,116\nQta 62,68,17664\ns_waitcnt lgkmcnt(4)\nQmaav 224,72,84\nQmaav 224,76,116\nQmaav 220,72,240\nbuffer_load_dwordx4 v79, s[48:51], s31 offen offset:1024 sc1 lds\n"
"Qmaav 220,76,248\nQmaav 216,72,208\nQta 64,68,8704\nQmaav 216,76,216\nQmaav 212,72,176\nbuffer_load_dwordx4 v79, s[48:51], s31 offen offset:2048 sc1 lds\nQmaav 212,76,184\nQta 66,68,9728\n"
"Qmaav 208,72,144\nQmaav 208,76,152\nQmaav 204,72,112\nQta 68,68,16896\nQmaav 204,76,120\nbuffer_load_dwordx4 v79, s[48:51], s31 offen offset:3072 sc1 lds\nQmaav 200,72,80\nQmaav 200,76,88\n"
"Qta 70,68,17920\ns_waitcnt lgkmcnt(4)\nQmaav 228,56,80\ns_add_u32 m0, s91, 4096\ns_add_u32 s31, s77, 135168\nQmaav 228,60,88\nbuffer_load_dwordx4 v79, s[48:51], s31 offen sc1 lds\nQmaav 232,56,112\n"
"Qmaav 232,60,120\nQmaav 236,56,144\nQta 72,68,8960\nQmaav 236,60,152\nbuffer_load_dwordx4 v79, s[48:51], s31 offen offset:1024 sc1 lds\nQmaav 240,56,176\nQmaav 240,60,184\nQta 74,68,9984\n"
"Qmaav 244,56,208\nQmaav 244,60,216\nbuffer_load_dwordx4 v79, s[48:51], s31 offen offset:2048 sc1 lds\nQmaav 248,56,240\nQta 76,68,17152\nQmaav 248,60,248\nQmaav 252,56,84\nQmaav 252,60,116\n"
"Qta 78,68,18176\ns_add_u32 m0, s91, 33792\ns_add_u32 s31, s77, 147456\ns_waitcnt lgkmcnt(4)\nQmvav 172,64,84\nbuffer_load_dwordx4 v79, s[48:51], s31 offen sc1 lds\nQmvav 172,68,116\nQmvav 140,64,240\n"
"Qmvav 140,68,248\nQmvav 108,64,208\nQta 56,68,24576\nQmvav 108,68,216\nbuffer_load_dwordx4 v79, s[48:51], s31 offen offset:1024 sc1 lds\nQmvav 228,64,176\nQmvav 228,68,184\nQta 58,68,25600\n"
"Qmvav 196,64,144\nQmvav 196,68,152\nbuffer_load_dwordx4 v79, s[48:51], s31 offen offset:2048 sc1 lds\nQmvav 164,64,112\nQta 60,68,32768\nQmvav 164,68,120\nv_add_u32 v75, 1, v75\nQmvav 132,64,80\n"
"s_waitcnt vmcnt(49)\nds_write_b32 v73, v75\nQmvav 132,68,88\nQta 62,68,33792\ns_waitcnt lgkmcnt(5)\nQmvav 204,72,80\nbuffer_load_dwordx4 v79, s[48:51], s31 offen offset:3072 sc1 lds\nQmvav 204,76,88\n"
"Qmvav 236,72,112\nQmvav 236,76,120\nQmvav 96,72,144\nQta 64,68,24832\ns_add_u32 m0, s91, 37888\ns_add_u32 s31, s77, 151552\nQmvav 96,76,152\nbuffer_load_dwordx4 v79, s[48:51], s31 offen sc1 lds\n"
"Qmvav 128,72,176\nds_read_b32 v66, v74\nQmvav 128,76,184\nQta 66,68,25856\nQmvav 160,72,208\nQmvav 160,76,216\nbuffer_load_dwordx4 v79, s[48:51], s31 offen offset:1024 sc1 lds\nQmvav 192,72,240\n"
"s_waitcnt lgkmcnt(1)\nv_cmp_gt_u32 vcc, v75, v66\nQmvav 192,76,248\ns_cbranch_vccnz .Lso1665r0\n.Lro1665r0:\nQmvav 224,72,84\nQta 68,68,33024\nQmvav 224,76,116\n"
"buffer_load_dwordx4 v79, s[48:51], s31 offen offset:2048 sc1 lds\ns_waitcnt vmcnt(38)\ns_waitcnt lgkmcnt(4)\nQmaaa 140,56,24\nQmaaa 140,60,52\nQta 70,68,34048\nQmaaa 136,56,20\ns_add_u32 m0, s96, 0\n"
"s_add_u32 s95, s73, 86016\nQmaaa 136,60,48\nbuffer_load_dwordx4 v63, s[52:55], s95 offen lds\nQmaaa 132,56,16\nQta 72,68,25088\nQmaaa 132,60,44\nQmaaa 128,56,12\nQmaaa 128,60,40\n"
"Qta 74,68,26112\ns_add_u32 s95, s73, 105472\nQmaaa 124,56,8\nbuffer_load_dwordx4 v63, s[52:55], s95 offen offset:1024 lds\nQmaaa 124,60,36\nQmaaa 120,56,4\nQta 76,68,33280\nQmaaa 120,60,32\n"
"s_add_u32 m0, s96, 8192\ns_add_u32 s95, s73, 126976\nQmaaa 116,56,0\nbuffer_load_dwordx4 v63, s[52:55], s95 offen lds\nQmaaa 116,60,28\nQta 78,68,34304\ns_waitcnt lgkmcnt(4)\nQmaaa 144,64,0\n"
"Qmaaa 144,68,28\ns_add_u32 s95, s73, 146432\nQmaaa 148,64,4\nbuffer_load_dwordx4 v63, s[52:55], s95 offen offset:1024 lds\nQmaaa 148,68,32\nQmaaa 152,64,8\nQta 56,68,25344\nQmaaa 152,68,36\n"
"s_add_u32 s31, s75, 131072\nQmaaa 156,64,12\nbuffer_load_dwordx4 v[80:83], v79, s[48:51], s31 offen offset:0 sc1\nQmaaa 156,68,40\nQta 58,68,26368\nQmaaa 160,64,16\nQmaaa 160,68,44\nQmaaa 164,64,20\n"
"Qta 60,68,33536\nQmaaa 164,68,48\nbuffer_load_dwordx4 v[112:115], v79, s[48:51], s31 offen offset:1024 sc1\nQmaaa 168,64,24\nQmaaa 168,68,52\nQta 62,68,34560\ns_waitcnt lgkmcnt(4)\nQmvaa 24,72,24\n"
"Qmvaa 24,76,52\nbuffer_load_dwordx4 v[144:147], v79, s[48:51], s31 offen offset:2048 sc1\nQmvaa 20,72,20\nQmvaa 20,76,48\nQmvaa 16,72,16\nQta 64,68,40960\nQmvaa 16,76,44\nbuffer_load_dwordx4 v[176:179], v79, s[48:51], s31 offen offset:3072 sc1\n"
"Qmvaa 12,72,12\nQmvaa 12,76,40\nQta 66,68,41472\nQmvaa 8,72,8\ns_add_u32 s31, s75, 135168\nQmvaa 8,76,36\nbuffer_load_dwordx4 v[208:211], v79, s[48:51], s31 offen offset:0 sc1\nQmvaa 4,72,4\n"
"Qta 68,68,41984\nQmvaa 4,76,32\nQmvaa 0,72,0\nQmvaa 0,76,28\nQta 70,68,42496\ns_waitcnt lgkmcnt(4)\nQmvaa 28,56,0\nbuffer_load_dwordx4 v[240:243], v79, s[48:51], s31 offen offset:1024 sc1\n"
"Qmvaa 28,60,28\nQmvaa 32,56,4\nQmvaa 32,60,32\nQmvaa 36,56,8\nQta 72,68,41216\nQmvaa 36,60,36\nbuffer_load_dwordx4 v[84:87], v79, s[48:51], s31 offen offset:2048 sc1\nQmvaa 40,56,12\n"
"Qmvaa 40,60,40\nQta 74,68,41728\nQmvaa 44,56,16\ns_add_u32 s31, s75, 147456\nQmvaa 44,60,44\nbuffer_load_dwordx4 v[88:91], v79, s[48:51], s31 offen offset:0 sc1\nQmvaa 48,56,20\nQta 76,68,42240\n"
"Qmvaa 48,60,48\nv_add_u32 v75, 1, v75\nQmvaa 52,56,24\ns_waitcnt vmcnt(38)\nds_write_b32 v73, v75\nQmvaa 52,60,52\nQta 78,68,42752\ns_waitcnt lgkmcnt(5)\n"
"Qmaaa 196,64,24\nbuffer_load_dwordx4 v[120:123], v79, s[48:51], s31 offen offset:1024 sc1\nQmaaa 196,68,52\nQmaaa 192,64,20\nQmaaa 192,68,48\nQmaaa 188,64,16\nQta 56,68,49152\nQmaaa 188,68,44\n"
"buffer_load_dwordx4 v[152:155], v79, s[48:51], s31 offen offset:2048 sc1\nQmaaa 184,64,12\nds_read_b32 v66, v74\nQmaaa 184,68,40\nQta 58,68,50176\nQmaaa 180,64,8\nQmaaa 180,68,36\nbuffer_load_dwordx4 v[184:187], v79, s[48:51], s31 offen offset:3072 sc1\n"
"Qmaaa 176,64,4\ns_waitcnt lgkmcnt(1)\nv_cmp_gt_u32 vcc, v75, v66\nQmaaa 176,68,32\ns_cbranch_vccnz .Lso1735r0\n.Lro1735r0:\nQmaaa 172,64,0\nQta 60,68,57344\n"
"s_add_u32 s31, s75, 151552\nQmaaa 172,68,28\nbuffer_load_dwordx4 v[216:219], v79, s[48:51], s31 offen offset:0 sc1\ns_waitcnt lgkmcnt(4)\nQmvaa 92,72,0\nQmvaa 92,76,28\nQta 62,68,58368\nQmvaa 124,72,4\n"
"Qmvaa 124,76,32\nbuffer_load_dwordx4 v[248:251], v79, s[48:51], s31 offen offset:1024 sc1\nQmvaa 156,72,8\nQta 64,68,49408\nQmvaa 156,76,36\nQmvaa 188,72,12\nQmvaa 188,76,40\nQta 66,68,50432\n"
"Qmvaa 220,72,16\nbuffer_load_dwordx4 v[116:119], v79, s[48:51], s31 offen offset:2048 sc1\nQmvaa 220,76,44\nQmvaa 252,72,20\nQta 68,68,57600\nQmvaa 252,76,48\ns_add_u32 m0, s96, 16384\ns_add_u32 s95, s67, 86016\n"
"Qmvaa 100,72,24\nbuffer_load_dwordx4 v61, s[52:55], s95 offen lds\nQmvaa 100,76,52\nQta 70,68,58624\ns_waitcnt lgkmcnt(4)\nQmaaa 224,56,24\nQmaaa 224,60,52\ns_add_u32 s95, s67, 105472\n"
"Qmaaa 220,56,20\nbuffer_load_dwordx4 v61, s[52:55], s95 offen offset:1024 lds\nQmaaa 220,60,48\nQmaaa 216,56,16\nQta 72,68,49664\nQmaaa 216,60,44\ns_add_u32 m0, s96, 24576\ns_add_u32 s95, s67, 126976\n"
"Qmaaa 212,56,12\nbuffer_load_dwordx4 v61, s[52:55], s95 offen lds\nQmaaa 212,60,40\nQta 74,68,50688\nQmaaa 208,56,8\nQmaaa 208,60,36\nQmaaa 204,56,4\nQta 76,68,57856\n"
"s_add_u32 s95, s67, 146432\nQmaaa 204,60,32\nbuffer_load_dwordx4 v61, s[52:55], s95 offen offset:1024 lds\nQmaaa 200,56,0\nQmaaa 200,60,28\nQta 78,68,58880\ns_waitcnt lgkmcnt(4)\nQmaaa 228,64,0\n"
"s_add_u32 m0, s96, 32768\ns_add_u32 s95, s72, 86016\nQmaaa 228,68,28\nbuffer_load_dwordx4 v61, s[52:55], s95 offen lds\nQmaaa 232,64,4\nQmaaa 232,68,32\nQmaaa 236,64,8\nQta 56,68,49920\n"
"s_add_u32 s95, s72, 105472\nQmaaa 236,68,36\nbuffer_load_dwordx4 v61, s[52:55], s95 offen offset:1024 lds\nQmaaa 240,64,12\nQmaaa 240,68,40\nQta 58,68,50944\nQmaaa 244,64,16\nQmaaa 244,68,44\n"
"Qmaaa 248,64,20\nQta 60,68,58112\nQmaaa 248,68,48\nQmaaa 252,64,24\nQmaaa 252,68,52\nQta 62,68,59136\ns_waitcnt lgkmcnt(4)\nQmvaa 172,72,24\n"
"Qmvaa 172,76,52\nQmvaa 140,72,20\nQmvaa 140,76,48\nQmvaa 108,72,16\nQta 64,69,0\nQmvaa 108,76,44\nQmvaa 228,72,12\nQmvaa 228,76,40\n"
"Qta 66,69,1024\nQmvaa 196,72,8\nQmvaa 196,76,36\nQmvaa 164,72,4\nQta 68,69,8192\nQmvaa 164,76,32\nv_add_u32 v75, 1, v75\nQmvaa 132,72,0\n"
"s_waitcnt vmcnt(20)\nds_write_b32 v73, v75\nQmvaa 132,76,28\nQta 70,69,9216\ns_waitcnt lgkmcnt(5)\nQmvaa 204,56,0\ns_waitcnt vmcnt(24)\nQda 0,58,0\n"
"Qmvaa 204,60,28\nQmvaa 236,56,4\nQda 28,58,33792\nQmvaa 236,60,32\nQmvaa 96,56,8\nQta 72,69,16384\nQmvaa 96,60,36\nQda 4,58,1024\n"
"Qmvaa 128,56,12\nds_read_b32 v66, v74\nQmvaa 128,60,40\nQta 74,69,17408\nQmvaa 160,56,16\nQda 32,58,34816\nQmvaa 160,60,44\nQmvaa 192,56,20\n"
"s_waitcnt lgkmcnt(2)\nv_cmp_gt_u32 vcc, v75, v66\nQmvaa 192,60,48\ns_cbranch_vccnz .Lso1805r0\n.Lro1805r0:\nQmvaa 224,56,24\nQta 76,69,24576\ns_add_u32 m0, s96, 40960\n"
"s_add_u32 s95, s72, 126976\nQmvaa 224,60,52\nbuffer_load_dwordx4 v61, s[52:55], s95 offen lds\ns_waitcnt vmcnt(39)\ns_waitcnt lgkmcnt(8)\nQmaaa 200,64,80\nQda 8,58,2048\nQmaaa 200,68,108\n"
"Qta 78,69,25600\nQmaaa 204,64,84\nQda 36,58,35840\ns_add_u32 s95, s72, 146432\nQmaaa 204,68,112\nbuffer_load_dwordx4 v61, s[52:55], s95 offen offset:1024 lds\nQmaaa 208,64,88\nQta 56,68,0\n"
"Qmaav 208,68,168\nQda 12,58,3072\nQmaaa 212,64,92\nQmaav 212,68,200\nQta 58,68,1024\ns_add_u32 m0, s96, 49152\ns_add_u32 s95, s73, 86016\nQmaaa 216,64,96\n"
"buffer_load_dwordx4 v61, s[52:55], s95 offen lds\nQmaav 216,68,232\nQda 40,58,36864\nQmaaa 220,64,100\nQta 60,68,8192\nQmaav 220,68,212\nQda 16,58,4096\ns_add_u32 s95, s73, 105472\n"
"Qmaaa 224,64,104\nbuffer_load_dwordx4 v61, s[52:55], s95 offen offset:1024 lds\nQmaav 224,68,244\nQta 62,68,9216\ns_waitcnt vmcnt(10)\ns_waitcnt lgkmcnt(8)\nQmaav 200,72,80\nQda 44,58,37888\n"
"Qmaav 200,76,88\ns_add_u32 m0, s96, 57344\ns_add_u32 s95, s73, 126976\nQmaav 204,72,112\nbuffer_load_dwordx4 v61, s[52:55], s95 offen lds\nQmaav 204,76,120\nQda 20,58,5120\nQmaav 208,72,144\n"
"Qta 64,69,256\nQmaav 208,76,152\nQda 48,58,38912\ns_add_u32 s95, s73, 146432\nQmaav 212,72,176\nbuffer_load_dwordx4 v61, s[52:55], s95 offen offset:1024 lds\nQmaav 212,76,184\nQta 66,69,1280\n"
"Qmaav 216,72,208\nQda 24,58,6144\nQmaav 216,76,216\nQmaav 220,72,240\nQta 68,69,8448\nQmaav 220,76,248\nQda 52,58,39936\nQmaav 224,72,84\n"
"Qmaav 224,76,116\nQta 70,69,9472\ns_waitcnt lgkmcnt(9)\nQmaaa 200,56,0\nQmaaa 200,60,28\nQmaaa 204,56,4\nQmaaa 204,60,32\ns_waitcnt lgkmcnt(9)\n"
"Qmaaa 208,56,8\nQta 72,69,16640\nQmaaa 208,60,36\ns_nop 1\nds_write_b128 v58, a[200:203] offset:0\nQmaaa 212,56,12\nQmaaa 212,60,40\nQta 74,69,17664\n"
"s_waitcnt lgkmcnt(4)\nQmaaa 216,56,16\nds_write_b128 v58, a[204:207] offset:1024\nQmaaa 216,60,44\nQmaaa 220,56,20\nQta 76,69,24832\nQmaaa 220,60,48\nds_write_b128 v58, a[208:211] offset:2048\n"
"Qmaaa 224,56,24\nQmaaa 224,60,52\nQta 78,69,25856\ns_waitcnt lgkmcnt(7)\nQmaaa 228,64,80\nds_write_b128 v58, a[212:215] offset:3072\nQmaaa 228,68,108\nQmaaa 232,64,84\n"
"Qmaaa 232,68,112\nds_write_b128 v58, a[216:219] offset:4096\nQmaaa 236,64,88\nQta 56,68,256\nQmaav 236,68,168\nQmaaa 240,64,92\nds_write_b128 v58, a[220:223] offset:5120\nQmaav 240,68,200\n"
"Qta 58,68,1280\nQmaaa 244,64,96\nQmaav 244,68,232\nds_write_b128 v58, a[224:227] offset:6144\nQmaaa 248,64,100\nQta 60,68,8448\nQmaav 248,68,212\nQmaaa 252,64,104\n"
"Qmaav 252,68,244\nQta 62,68,9472\ns_waitcnt lgkmcnt(8)\nQmaav 228,72,80\nv_add_u32 v75, 1, v75\nQmaav 228,76,88\nds_write_b32 v73, v75\nQmaav 232,72,112\n"
"Qmaav 232,76,120\nQmaav 236,72,144\nQta 64,69,512\nQmaav 236,76,152\nQmaav 240,72,176\nQmaav 240,76,184\nQta 66,69,1536\nQmaav 244,72,208\n"
"Qmaav 244,76,216\nds_read_b32 v66, v74\nQmaav 248,72,240\nQta 68,69,8704\nQmaav 248,76,248\nQmaav 252,72,84\nQmaav 252,76,116\ns_waitcnt lgkmcnt(1)\n"
"v_cmp_gt_u32 vcc, v75, v66\ns_waitcnt lgkmcnt(5)\nQmaaa 228,56,0\ns_cbranch_vccnz .Lso1878r0\n.Lro1878r0:\nQmaaa 228,60,28\nQta 70,69,9728\nQmaaa 232,56,4\n"
"Qmaaa 232,60,32\nQdv 148,59,2048\nQmaaa 236,56,8\nQta 72,69,16896\nQmaaa 236,60,36\ns_nop 0\nds_write_b128 v58, a[228:231] offset:33792\nQmaaa 240,56,12\n"
"Qdv 180,59,3072\nQmaaa 240,60,40\nQta 74,69,17920\nQmaaa 244,56,16\nds_write_b128 v58, a[232:235] offset:34816\nQmaaa 244,60,44\nQdv 104,59,5120\nQmaaa 248,56,20\n"
"Qta 76,69,25088\nQmaaa 248,60,48\nds_write_b128 v58, a[236:239] offset:35840\nQmaaa 252,56,24\nQdv 136,59,0\ns_waitcnt lgkmcnt(9)\nQa 196,196,148\nQmaaa 252,60,52\n"
"Qta 78,69,26112\nQa 197,197,149\n.Lpg20:\ns_cbranch_execz .Lpg24\ns_waitcnt lgkmcnt(10)\nQmvaa 132,64,80\nds_write_b128 v58, a[240:243] offset:36864\nQmvaa 132,68,108\n"
"Qa 198,198,150\nQa 199,199,151\nQmvaa 164,64,84\ns_waitcnt lgkmcnt(8)\nQa 228,228,180\nQa 229,229,181\nQmvaa 164,68,112\nds_write_b128 v58, a[244:247] offset:37888\n"
"Qmvaa 196,64,88\nQta 56,68,512\nQa 230,230,182\nQa 231,231,183\nQmvav 196,68,168\nQdv 148,59,4096\ns_waitcnt lgkmcnt(8)\nQa 140,140,104\n"
"Qmvaa 228,64,92\nds_write_b128 v58, a[248:251] offset:38912\nQmvav 228,68,200\nQta 58,68,1536\nQa 141,141,105\nQmvaa 108,64,96\nQa 142,142,106\nQa 143,143,107\n"
"Qmvav 108,68,232\nQdv 180,59,6144\ns_waitcnt lgkmcnt(8)\nQa 132,132,136\nQmvaa 140,64,100\nQta 60,68,8704\nQa 133,133,137\nQmvav 140,68,212\n"
"ds_write_b128 v58, a[252:255] offset:39936\nQmvaa 172,64,104\nQa 134,134,138\nQa 135,135,139\nQmvav 172,68,244\nQta 62,68,9728\ns_waitcnt lgkmcnt(6)\nQa 108,108,148\n"
"s_waitcnt lgkmcnt(6)\nQmvav 132,72,80\nQdv 104,59,1024\nv_add_u32 v75, 1, v75\nQmvav 132,76,88\ns_waitcnt vmcnt(10)\nds_write_b32 v73, v75\nQmvav 164,72,112\n"
"Qa 109,109,149\nQa 110,110,150\nQmvav 164,76,120\nQa 111,111,151\ns_waitcnt lgkmcnt(5)\nQa 172,172,180\nQmvav 196,72,144\nQta 64,69,768\n"
"Qa 173,173,181\nQmvav 196,76,152\nQa 174,174,182\nQa 175,175,183\nQmvav 228,72,176\nQmvav 228,76,184\nQta 66,69,1792\ns_waitcnt lgkmcnt(3)\n"
"Qa 164,164,104\nQmvav 108,72,208\nQa 165,165,105\nQa 166,166,106\nQmvav 108,76,216\nds_read_b32 v66, v74\nQa 167,167,107\nQmvav 140,72,240\n"
"Qta 68,69,8960\nQmvav 140,76,248\nQmvav 172,72,84\nQmvav 172,76,116\ns_waitcnt lgkmcnt(1)\nv_cmp_gt_u32 vcc, v75, v66\ns_waitcnt lgkmcnt(6)\nQmvaa 132,56,0\n"
"s_cbranch_vccnz .Lso1920r0\n.Lro1920r0:\ns_add_u32 s68, s64, 0\nQmvaa 132,60,28\nQta 70,69,9984\nQmvaa 164,56,4\nQmvaa 164,60,32\nQdv 136,59,35840\n"
"Qmvaa 196,56,8\nQta 72,69,17152\ns_nop 1\nQk 132,132,133\nQmvaa 196,60,36\nQk 133,134,135\nQmvaa 228,56,12\nQdv 148,59,36864\n"
"Qk 134,164,165\nQmvaa 228,60,40\nQta 74,69,18176\nQk 135,166,167\nQmvaa 108,56,16\nbuffer_store_dwordx4 v[132:135], v79, s[24:27], s68 offen offset:0 sc1\nQk 196,196,197\nQmvaa 108,60,44\n"
"Qdv 180,59,38912\nQk 197,198,199\nQmvaa 140,56,20\nQta 76,69,25344\nQk 198,228,229\nQmvaa 140,60,48\ns_waitcnt lgkmcnt(5)\nQa 96,96,136\n"
"Qa 97,97,137\nQmvaa 172,56,24\nQdv 104,59,33792\nQa 98,98,138\nQmvaa 172,60,52\nQta 78,69,26368\nQa 99,99,139\ns_waitcnt lgkmcnt(11)\n"
"Qmvaa 204,64,80\ns_waitcnt lgkmcnt(5)\nQa 128,128,148\nQa 129,129,149\nQmvaa 204,68,108\nQa 130,130,150\nQa 131,131,151\nQmvaa 236,64,84\n"
"Qk 199,230,231\nQk 108,108,109\nQmvaa 236,68,112\ns_waitcnt lgkmcnt(3)\nQa 192,192,180\nQa 193,193,181\nQmvaa 96,64,88\nQta 56,68,768\n"
"Qa 194,194,182\nQmvav 96,68,168\nQdv 136,59,37888\nQa 195,195,183\nQmvaa 128,64,92\ns_waitcnt lgkmcnt(3)\nQa 204,204,104\nQa 205,205,105\n"
"Qmvav 128,68,200\nQta 58,68,1792\nQa 206,206,106\nQmvaa 160,64,96\nbuffer_store_dwordx4 v[196:199], v79, s[24:27], s68 offen offset:1024 sc1\nQa 207,207,107\nQmvav 160,68,232\nQdv 148,59,39936\n"
"Qk 109,110,111\nQmvaa 192,64,100\nQta 60,68,8960\nQk 110,140,141\nQmvav 192,68,212\nQk 111,142,143\nQk 172,172,173\nQmvaa 224,64,104\n"
"buffer_store_dwordx4 v[108:111], v79, s[24:27], s68 offen offset:2048 sc1\nv_add_u32 v75, 1, v75\nQmvav 224,68,244\ns_waitcnt vmcnt(3)\nds_write_b32 v73, v75\ns_waitcnt lgkmcnt(10)\nQmvav 204,72,80\nQta 62,68,9984\n"
"s_waitcnt lgkmcnt(5)\nQa 160,160,136\nQmvav 204,76,88\nQdv 180,59,34816\nQa 161,161,137\nQmvav 236,72,112\nQa 162,162,138\nQa 163,163,139\n"
"Qmvav 236,76,120\ns_waitcnt lgkmcnt(4)\nQa 224,224,148\nQa 225,225,149\nQmvav 96,72,144\nQta 64,68,16384\nQa 226,226,150\nQmvav 96,76,152\n"
"Qa 227,227,151\nQk 173,174,175\nQmvav 128,72,176\nQmvav 128,76,184\nds_read_b32 v66, v74\ns_waitcnt lgkmcnt(2)\nQa 236,236,180\nQmvav 160,72,208\n"
"Qta 66,68,17408\nQa 237,237,181\nQmvav 160,76,216\nQa 238,238,182\nQa 239,239,183\nQmvav 192,72,240\nQmvav 192,76,248\ns_waitcnt lgkmcnt(1)\n"
"v_cmp_gt_u32 vcc, v75, v66\nQmvav 224,72,84\ns_cbranch_vccnz .Lso1960r0\n.Lro1960r0:\nQmvav 224,76,116\nQta 68,68,24576\ns_add_u32 m0, s96, 65536\ns_add_u32 s95, s67, 86016\n"
"s_waitcnt lgkmcnt(5)\nQmvaa 204,56,0\nbuffer_load_dwordx4 v62, s[52:55], s95 offen lds\nQmvaa 204,60,28\nQmvaa 236,56,4\nQta 70,68,25600\nQmvaa 236,60,32\ns_add_u32 s95, s67, 125952\n"
"Qmvaa 96,56,8\nbuffer_load_dwordx4 v62, s[52:55], s95 offen offset:1024 lds\nQmvaa 96,60,36\nQta 72,68,32768\nQk 174,204,205\nQmvaa 128,56,12\nQk 175,206,207\nQk 236,236,237\n"
"Qmvaa 128,60,40\nbuffer_store_dwordx4 v[172:175], v79, s[24:27], s68 offen offset:3072 sc1\nQk 237,238,239\nQmvaa 160,56,16\nQta 74,68,33792\ns_add_u32 m0, s96, 73728\ns_add_u32 s95, s72, 86016\nQmvaa 160,60,44\n"
"buffer_load_dwordx4 v62, s[52:55], s95 offen lds\nQmvaa 192,56,20\nQk 238,96,97\nQk 239,98,99\nQmvaa 192,60,48\nQta 76,68,40960\ns_add_u32 s68, s64, 4096\nQmvaa 224,56,24\n"
"buffer_store_dwordx4 v[236:239], v79, s[24:27], s68 offen offset:0 sc1\ns_add_u32 s95, s72, 125952\nQmvaa 224,60,52\nbuffer_load_dwordx4 v62, s[52:55], s95 offen offset:1024 lds\ns_waitcnt lgkmcnt(3)\nQmaaa 116,64,80\nQta 78,68,41984\nQk 128,128,129\n"
"Qmaaa 116,68,108\nQk 129,130,131\nQk 130,160,161\nQmaaa 120,64,84\ns_add_u32 m0, s96, 81920\ns_add_u32 s95, s73, 86016\nQmaaa 120,68,112\nbuffer_load_dwordx4 v62, s[52:55], s95 offen lds\n"
"Qmaaa 124,64,88\nQta 56,68,49152\nQk 131,162,163\nQmaav 124,68,168\nbuffer_store_dwordx4 v[128:131], v79, s[24:27], s68 offen offset:1024 sc1\nQk 192,192,193\nQmaaa 128,64,92\nQk 193,194,195\n"
"Qk 194,224,225\nQmaav 128,68,200\nQta 58,68,50176\ns_add_u32 s95, s73, 125952\nQmaaa 132,64,96\nbuffer_load_dwordx4 v62, s[52:55], s95 offen offset:1024 lds\nQmaav 132,68,232\nQk 195,226,227\n"
"Qmaaa 136,64,100\nQta 60,68,57344\nv_bfe_u32 v61, v60, 6, 2\nQmaav 136,68,212\nv_lshrrev_b32 v62, 8, v60\nv_xor_b32 v61, v61, v62\nQmaaa 140,64,104\nv_bfe_u32 v62, v60, 5, 1\n"
"v_lshl_or_b32 v61, v61, 1, v62\nQmaav 140,68,244\nQta 62,68,58368\nv_and_b32 v62, 16, v60\ns_waitcnt lgkmcnt(4)\nQmaav 116,72,80\nv_mov_b32 v63, 0x3f80\nv_lshlrev_b32 v63, v62, v63\n"
"Qmaav 116,76,88\nv_min_u32 v62, 1, v61\nv_add_u32 v62, -1, v62\nQmaav 120,72,112\nv_and_b32 v62, v62, v63\nv_xor_b32 v61, 1, v61\nQmaav 120,76,120\nv_min_u32 v61, 1, v61\n"
"v_add_u32 v61, -1, v61\nQmaav 124,72,144\nQta 64,68,16640\nv_and_b32 v61, v61, v63\nQmaav 124,76,152\nv_accvgpr_write_b32 a202, v62\nv_accvgpr_write_b32 a203, v61\nQmaav 128,72,176\n"
"v_accvgpr_write_b32 a200, 0\nv_accvgpr_write_b32 a201, 0\nQmaav 128,76,184\nQta 66,68,17664\nv_accvgpr_write_b32 a204, 0\nQmaav 132,72,208\nbuffer_store_dwordx4 v[192:195], v79, s[24:27], s68 offen offset:2048 sc1\nv_accvgpr_write_b32 a205, 0\n"
"Qmaav 132,76,216\nQmaav 136,72,240\nQta 68,68,24832\nQmaav 136,76,248\nQmaav 140,72,84\nQmaav 140,76,116\nQta 70,68,25856\ns_waitcnt lgkmcnt(4)\n"
"Qmaaa 116,56,0\nQmaaa 116,60,28\nQmaaa 120,56,4\nQmaaa 120,60,32\nQmaaa 124,56,8\nQta 72,68,33024\nQmaaa 124,60,36\ns_nop 2\n"
"ds_write_b128 v58, a[116:119] offset:0\nQmaaa 128,56,12\nQmaaa 128,60,40\nQta 74,68,34048\nQmaaa 132,56,16\nds_write_b128 v58, a[120:123] offset:1024\nQmaaa 132,60,44\nQmaaa 136,56,20\n"
"Qta 76,68,41216\nQmaaa 136,60,48\nds_write_b128 v58, a[124:127] offset:2048\nQmaaa 140,56,24\nQmaaa 140,60,52\nQta 78,68,42240\ns_waitcnt lgkmcnt(7)\nQmaaa 144,64,80\n"
"ds_write_b128 v58, a[128:131] offset:3072\nQmaaa 144,68,108\nQmaaa 148,64,84\nQmaaa 148,68,112\nds_write_b128 v58, a[132:135] offset:4096\nQmaaa 152,64,88\nQta 56,68,49408\nQmaav 152,68,168\n"
"Qmaaa 156,64,92\nds_write_b128 v58, a[136:139] offset:5120\nQmaav 156,68,200\nQta 58,68,50432\nQmaaa 160,64,96\nQmaav 160,68,232\nds_write_b128 v58, a[140:143] offset:6144\nQmaaa 164,64,100\n"
"Qta 60,68,57600\nQmaav 164,68,212\nQmaaa 168,64,104\nQmaav 168,68,244\nQta 62,68,58624\ns_waitcnt lgkmcnt(8)\nQmaav 144,72,80\nv_add_u32 v75, 1, v75\n"
"Qmaav 144,76,88\ns_waitcnt vmcnt(8)\nds_write_b32 v73, v75\nQmaav 148,72,112\nQmaav 148,76,120\nQmaav 152,72,144\nQta 64,68,16896\nQmaav 152,76,152\n"
"Qmaav 156,72,176\nQmaav 156,76,184\nQta 66,68,17920\nQmaav 160,72,208\nQmaav 160,76,216\nds_read_b32 v66, v74\nQmaav 164,72,240\nQta 68,68,25088\n"
"Qmaav 164,76,248\nQmaav 168,72,84\nQmaav 168,76,116\ns_waitcnt lgkmcnt(1)\nv_cmp_gt_u32 vcc, v75, v66\ns_waitcnt lgkmcnt(5)\nQmaaa 144,56,0\ns_cbranch_vccnz .Lso2046r0\n"
".Lro2046r0:\nQmaaa 144,60,28\nQta 70,68,26112\nQmaaa 148,56,4\nQmaaa 148,60,32\nQdv 104,59,2048\nQmaaa 152,56,8\nQta 72,68,33280\n"
"Qmaaa 152,60,36\ns_nop 0\nds_write_b128 v58, a[144:147] offset:33792\nQmaaa 156,56,12\nQdv 136,59,3072\nQmaaa 156,60,40\nQta 74,68,34304\nQmaaa 160,56,16\n"
"ds_write_b128 v58, a[148:151] offset:34816\nQmaaa 160,60,44\nQdv 148,59,5120\nQmaaa 164,56,20\nQta 76,68,41472\nQmaaa 164,60,48\nds_write_b128 v58, a[152:155] offset:35840\nQmaaa 168,56,24\n"
"Qdv 180,59,0\ns_waitcnt lgkmcnt(9)\nQa 8,8,104\nQmaaa 168,60,52\nQta 78,68,42496\nQa 9,9,105\ns_waitcnt lgkmcnt(10)\nQmvaa 0,64,80\n"
"ds_write_b128 v58, a[156:159] offset:36864\nQmvaa 0,68,108\nQa 10,10,106\nQa 11,11,107\nQmvaa 4,64,84\ns_waitcnt lgkmcnt(8)\nQa 12,12,136\nQa 13,13,137\n"
"Qmvaa 4,68,112\nds_write_b128 v58, a[160:163] offset:37888\nQmvaa 8,64,88\nQta 56,68,49664\nQa 14,14,138\nQa 15,15,139\nQmvav 8,68,168\nQdv 104,59,4096\n"
"s_waitcnt lgkmcnt(8)\nQa 20,20,148\nQmvaa 12,64,92\nds_write_b128 v58, a[164:167] offset:38912\nQmvav 12,68,200\nQta 58,68,50688\nQa 21,21,149\nQmvaa 16,64,96\n"
"Qa 22,22,150\nQa 23,23,151\nQmvav 16,68,232\nQdv 136,59,6144\ns_waitcnt lgkmcnt(8)\nQa 0,0,180\nQmvaa 20,64,100\nQta 60,68,57856\n"
"Qa 1,1,181\nQmvav 20,68,212\nds_write_b128 v58, a[168:171] offset:39936\nQmvaa 24,64,104\nQa 2,2,182\nQa 3,3,183\nQmvav 24,68,244\nQta 62,68,58880\n"
"s_waitcnt lgkmcnt(6)\nQa 16,16,104\ns_waitcnt lgkmcnt(6)\nQmvav 0,72,80\nQdv 148,59,1024\nv_add_u32 v75, 1, v75\nQmvav 0,76,88\ns_waitcnt vmcnt(1)\n"
"ds_write_b32 v73, v75\nQmvav 4,72,112\nQa 17,17,105\nQa 18,18,106\nQmvav 4,76,120\nQa 19,19,107\ns_waitcnt lgkmcnt(5)\nQa 24,24,136\n"
"Qmvav 8,72,144\nQta 64,69,0\nQa 25,25,137\nQmvav 8,76,152\nQa 26,26,138\nQa 27,27,139\nQmvav 12,72,176\ns_waitcnt vmcnt(0)\n"
"buffer_store_dword v67, v77, s[24:27], 0 offen sc1\nQmvav 12,76,184\nQta 66,69,512\ns_waitcnt lgkmcnt(3)\nQa 4,4,148\nQmvav 16,72,208\nQa 5,5,149\nQa 6,6,150\n"
"Qmvav 16,76,216\nds_read_b32 v66, v74\nQa 7,7,151\nQmvav 20,72,240\nQta 68,69,1024\nQmvav 20,76,248\nQmvav 24,72,84\nQmvav 24,76,116\n"
"s_waitcnt lgkmcnt(1)\nv_cmp_gt_u32 vcc, v75, v66\ns_waitcnt lgkmcnt(6)\nQmvaa 0,56,0\ns_cbranch_vccnz .Lso2088r0\n.Lro2088r0:\nQmvaa 0,60,28\nQta 70,69,1536\n"
"Qmvaa 4,56,4\nQmvaa 4,60,32\nQdv 180,59,33792\nQmvaa 8,56,8\nQta 72,69,8192\nQmvaa 8,60,36\nQmvaa 12,56,12\nQdv 104,59,34816\n"
"Qmvaa 12,60,40\nQta 74,69,8704\nQmvaa 16,56,16\nQmvaa 16,60,44\nQdv 136,59,35840\nQmvaa 20,56,20\nQta 76,69,9216\nQmvaa 20,60,48\n"
"s_waitcnt lgkmcnt(5)\nQa 28,28,180\nQa 29,29,181\nQmvaa 24,56,24\nQdv 148,59,36864\nQa 30,30,182\nQmvaa 24,60,52\nQta 78,69,9728\n"
"Qa 31,31,183\ns_waitcnt lgkmcnt(11)\nQmaaa 172,64,80\ns_waitcnt lgkmcnt(5)\nQa 32,32,104\nQa 33,33,105\nQmaaa 172,68,108\nQa 34,34,106\n"
"Qa 35,35,107\nQmaaa 176,64,84\nQmaaa 176,68,112\ns_waitcnt lgkmcnt(3)\nQa 36,36,136\nQa 37,37,137\nQmaaa 180,64,88\nQta 56,69,16384\n"
"Qa 38,38,138\nQmaav 180,68,168\nQdv 180,59,37888\nQa 39,39,139\nQmaaa 184,64,92\ns_waitcnt lgkmcnt(3)\nQa 40,40,148\nQa 41,41,149\n"
"Qmaav 184,68,200\nQta 58,69,16896\nQa 42,42,150\nQmaaa 188,64,96\nQdv 104,59,38912\nQa 43,43,151\nQmaav 188,68,232\nQmaaa 192,64,100\n"
"Qta 60,69,17408\nQmaav 192,68,212\nQdv 136,59,39936\nQmaaa 196,64,104\ns_waitcnt lgkmcnt(4)\nQa 44,44,180\nQa 45,45,181\nQmaav 196,68,244\n"
"Qta 62,69,17920\nQa 46,46,182\ns_waitcnt lgkmcnt(5)\nQmaav 172,72,80\nQa 47,47,183\nQmaav 172,76,88\nQmaav 176,72,112\ns_waitcnt lgkmcnt(3)\n"
"Qa 48,48,104\nQa 49,49,105\nQmaav 176,76,120\nQa 50,50,106\nQa 51,51,107\nQmaav 180,72,144\nQta 64,68,17152\nQmaav 180,76,152\n"
"s_waitcnt lgkmcnt(2)\nQa 52,52,136\nQa 53,53,137\nQmaav 184,72,176\nQa 54,54,138\nQa 55,55,139\nQmaav 184,76,184\nQta 66,68,18176\n"
"Qmaav 188,72,208\nQmaav 188,76,216\nQmaav 192,72,240\nQta 68,68,25344\nQmaav 192,76,248\nQmaav 196,72,84\nQmaav 196,76,116\n.Lpg21:\n"
"s_cbranch_execz .Lpg25\nQta 70,68,26368\ns_waitcnt lgkmcnt(4)\nQmaaa 172,56,0\nQmaaa 172,60,28\nQmaaa 176,56,4\nQmaaa 176,60,32\nQmaaa 180,56,8\n"
"Qta 72,68,33536\nQmaaa 180,60,36\ns_nop 2\nds_write_b128 v58, a[172:175] offset:0\nQmaaa 184,56,12\nQmaaa 184,60,40\nQta 74,68,34560\nQmaaa 188,56,16\n"
"ds_write_b128 v58, a[176:179] offset:1024\nQmaaa 188,60,44\nQmaaa 192,56,20\nQta 76,68,41728\nQmaaa 192,60,48\nds_write_b128 v58, a[180:183] offset:2048\nQmaaa 196,56,24\nQmaaa 196,60,52\n"
"Qta 78,68,42752\ns_waitcnt lgkmcnt(7)\nQmvaa 28,64,80\nds_write_b128 v58, a[184:187] offset:3072\nQmvaa 28,68,108\nQmvaa 32,64,84\nQmvaa 32,68,112\nds_write_b128 v58, a[188:191] offset:4096\n"
"Qmvaa 36,64,88\nQta 56,68,49920\nQmvav 36,68,168\nQmvaa 40,64,92\nds_write_b128 v58, a[192:195] offset:5120\nQmvav 40,68,200\nQta 58,68,50944\nQmvaa 44,64,96\n"
"Qmvav 44,68,232\nbuffer_load_dword v78, v76, s[24:27], 0 offen sc1\nQmvaa 48,64,100\nQta 60,68,58112\nQmvav 48,68,212\nds_write_b128 v58, a[196:199] offset:6144\nQmvaa 52,64,104\nQmvav 52,68,244\n"
"Qta 62,68,59136\ns_waitcnt lgkmcnt(8)\nQmvav 28,72,80\nv_add_u32 v75, 1, v75\nQmvav 28,76,88\nds_write_b32 v73, v75\nQmvav 32,72,112\nQmvav 32,76,120\n"
"Qmvav 36,72,144\nQta 64,69,256\nQmvav 36,76,152\nQmvav 40,72,176\nQmvav 40,76,184\nQta 66,69,768\nQmvav 44,72,208\nQmvav 44,76,216\n"
"ds_read_b32 v66, v74\nQmvav 48,72,240\nQta 68,69,1280\nQmvav 48,76,248\nQmvav 52,72,84\nQmvav 52,76,116\ns_waitcnt lgkmcnt(1)\nv_cmp_gt_u32 vcc, v75, v66\n"
"s_waitcnt lgkmcnt(5)\nQmvaa 28,56,0\ns_cbranch_vccnz .Lso2172r0\n.Lro2172r0:\nQmvaa 28,60,28\nQta 70,69,1792\nQmvaa 32,56,4\nQmvaa 32,60,32\n"
"Qdv 148,59,2048\nQmvaa 36,56,8\nQta 72,69,8448\nQmvaa 36,60,36\nQmvaa 40,56,12\nQdv 180,59,3072\nQmvaa 40,60,40\nQta 74,69,8960\n"
"Qmvaa 44,56,16\nQmvaa 44,60,44\nQdv 104,59,5120\nQmvaa 48,56,20\nQta 76,69,9472\nQmvaa 48,60,48\ns_waitcnt lgkmcnt(5)\nQa 156,156,148\n"
"Qa 157,157,149\nQmvaa 52,56,24\nQdv 136,59,0\nQa 158,158,150\nQmvaa 52,60,52\nQta 78,69,9984\nQa 159,159,151\ns_waitcnt lgkmcnt(11)\n"
"Qmvaa 92,64,80\ns_waitcnt lgkmcnt(5)\nQa 188,188,180\nQa 189,189,181\nQmvaa 92,68,108\nQa 190,190,182\nQa 191,191,183\nQmvaa 124,64,84\n"
"s_waitcnt vmcnt(0)\nv_sub_u32 v66, v78, v65\nv_cmp_gt_u32 vcc, 1, v66\ns_and_b64 vcc, vcc, s[28:29]\ns_cbranch_scc1 .Lx0_w\n.Lx0_r:\nQmvaa 124,68,112\ns_add_u32 s69, s98, 0\n"
"buffer_load_dwordx4 a[208:211], v79, s[24:27], s69 offen offset:0 sc1\nQmvaa 156,64,88\nQta 56,69,16640\ns_waitcnt lgkmcnt(4)\nQa 252,252,104\nQmvav 156,68,168\nbuffer_load_dwordx4 a[212:215], v79, s[24:27], s69 offen offset:1024 sc1\nQmvaa 188,64,92\n"
"Qdv 148,59,4096\nQa 253,253,105\nQmvav 188,68,200\nbuffer_load_dwordx4 a[216:219], v79, s[24:27], s69 offen offset:2048 sc1\nQmvaa 220,64,96\nQta 58,69,17152\nQa 254,254,106\nQa 255,255,107\n"
"Qmvav 220,68,232\nbuffer_load_dwordx4 a[220:223], v79, s[24:27], s69 offen offset:3072 sc1\nQmvaa 252,64,100\nQdv 180,59,6144\ns_waitcnt lgkmcnt(5)\nQa 92,92,136\nQmvav 252,68,212\ns_add_u32 s69, s98, 4096\n"
"buffer_load_dwordx4 a[224:227], v79, s[24:27], s69 offen offset:0 sc1\nQmvaa 100,64,104\nQta 60,69,17664\nQa 93,93,137\nQa 94,94,138\nQa 95,95,139\nQmvav 100,68,244\nbuffer_load_dwordx4 a[228:231], v79, s[24:27], s69 offen offset:1024 sc1\n"
"s_waitcnt lgkmcnt(9)\nQmvav 92,72,80\nQdv 104,59,1024\ns_waitcnt lgkmcnt(4)\nQa 220,220,148\nQmvav 92,76,88\nbuffer_load_dwordx4 a[232:235], v79, s[24:27], s69 offen offset:2048 sc1\nQmvav 124,72,112\n"
"Qta 62,69,18176\nQa 221,221,149\nQmvav 124,76,120\nbuffer_load_dwordx4 a[236:239], v79, s[24:27], s69 offen offset:3072 sc1\nQmvav 156,72,144\nQa 222,222,150\nQa 223,223,151\nQmvav 156,76,152\n"
"s_add_u32 s69, s98, 16384\nbuffer_load_dwordx4 a[240:243], v79, s[24:27], s69 offen offset:0 sc1\nQmvav 188,72,176\ns_waitcnt lgkmcnt(3)\nQa 100,100,180\nQa 101,101,181\nQmvav 188,76,184\nbuffer_load_dwordx4 a[244:247], v79, s[24:27], s69 offen offset:1024 sc1\n"
"Qmvav 220,72,208\nv_add_u32 v75, 1, v75\nQa 102,102,182\nQmvav 220,76,216\nds_write_b32 v73, v75\nQa 103,103,183\nQmvav 252,72,240\nbuffer_load_dwordx4 a[248:251], v79, s[24:27], s69 offen offset:2048 sc1\n"
"Qmvav 252,76,248\ns_waitcnt lgkmcnt(2)\nQa 124,124,104\nQa 125,125,105\nQmvav 100,72,84\nQa 126,126,106\nQa 127,127,107\nQmvav 100,76,116\n"
"s_waitcnt lgkmcnt(1)\nQmvaa 92,56,0\nQmvaa 92,60,28\nQmvaa 124,56,4\nQmvaa 124,60,32\nds_read_b32 v66, v74\nQmvaa 156,56,8\nQmvaa 156,60,36\n"
"Qmvaa 188,56,12\nQmvaa 188,60,40\ns_waitcnt lgkmcnt(0)\nv_cmp_gt_u32 vcc, v75, v66\nQmvaa 220,56,16\ns_cbranch_vccnz .Lso2222r0\n.Lro2222r0:\nQmvaa 220,60,44\n"
"Qmvaa 252,56,20\nQmvaa 252,60,48\nQmvaa 100,56,24\nQmvaa 100,60,52\ns_waitcnt vmcnt(10)\nQmvaa 92,202,208\nQmvaa 124,200,208\ns_waitcnt vmcnt(9)\n"
"Qmvaa 156,202,212\nQmvaa 188,200,212\ns_nop 3\nQk 92,92,93\nQk 93,94,95\nds_write_b64 v56, v[92:93] offset:64\ns_waitcnt vmcnt(8)\nQmvaa 220,202,216\n"
"Qk 124,124,125\nQk 125,126,127\nds_write_b64 v56, v[124:125] offset:3392\nQmvaa 252,200,216\nQk 156,156,157\nQk 157,158,159\nds_write_b64 v56, v[156:157] offset:6720\ns_waitcnt vmcnt(7)\n"
"Qmvaa 100,202,220\nQk 188,188,189\nQk 189,190,191\nds_write_b64 v56, v[188:189] offset:10048\nQmvaa 0,200,220\nQk 220,220,221\nQk 221,222,223\nds_write_b64 v56, v[220:221] offset:13376\n"
"s_waitcnt vmcnt(6)\nQmvaa 4,202,224\nQk 252,252,253\nQk 253,254,255\nds_write_b64 v56, v[252:253] offset:16704\nQmvaa 8,200,224\nQk 100,100,101\nQk 101,102,103\n"
"ds_write_b64 v56, v[100:101] offset:20032\ns_waitcnt vmcnt(5)\nQmvaa 12,202,228\nQk 0,0,1\nQk 1,2,3\nds_write_b64 v56, v[0:1] offset:0\nQmvaa 16,200,228\nQk 4,4,5\n"
"Qk 5,6,7\nds_write_b64 v56, v[4:5] offset:3328\ns_waitcnt vmcnt(4)\nQmvaa 20,202,232\nQk 8,8,9\nQk 9,10,11\nds_write_b64 v56, v[8:9] offset:6656\nQmvaa 24,200,232\n"
"Qk 12,12,13\nQk 13,14,15\nds_write_b64 v56, v[12:13] offset:9984\ns_waitcnt vmcnt(3)\nQmvaa 28,202,236\nQk 16,16,17\nQk 17,18,19\nds_write_b64 v56, v[16:17] offset:13312\n"
"Qmvaa 32,200,236\nQk 20,20,21\nQk 21,22,23\nds_write_b64 v56, v[20:21] offset:16640\ns_waitcnt vmcnt(2)\nQmvaa 36,202,240\nQk 24,24,25\nQk 25,26,27\n"
"ds_write_b64 v56, v[24:25] offset:19968\nQmvaa 40,200,240\nQk 28,28,29\nQk 29,30,31\nds_write_b64 v56, v[28:29] offset:32\ns_waitcnt vmcnt(1)\nQmvaa 44,202,244\nQk 32,32,33\n"
"Qk 33,34,35\nds_write_b64 v56, v[32:33] offset:3360\nQmvaa 48,200,244\nQk 36,36,37\nQk 37,38,39\nds_write_b64 v56, v[36:37] offset:6688\ns_waitcnt vmcnt(0)\nQmvaa 52,202,248\n"
"Qk 40,40,41\nQk 41,42,43\nds_write_b64 v56, v[40:41] offset:10016\nQk 44,44,45\nQk 45,46,47\nds_write_b64 v56, v[44:45] offset:13344\nQk 48,48,49\nQk 49,50,51\n"
"ds_write_b64 v56, v[48:49] offset:16672\nQk 52,52,53\nQk 53,54,55\nds_write_b64 v56, v[52:53] offset:20000\ns_waitcnt lgkmcnt(0)\ns_barrier\nQda 0,57,0\nQda 4,57,3328\n"
"Qda 8,57,6656\nQda 12,57,9984\nQda 16,57,13312\nQda 20,57,16640\nQda 24,57,19968\nQda 28,71,64\nQda 32,71,6720\nQda 36,71,13376\n"
"Qda 40,71,20032\ns_mov_b32 s68, 0\ns_waitcnt lgkmcnt(10)\nbuffer_store_dwordx4 a[0:3], v64, s[40:43], s68 offen offset:0 sc1\ns_mov_b32 s68, 40960\ns_waitcnt lgkmcnt(9)\nbuffer_store_dwordx4 a[4:7], v64, s[40:43], s68 offen offset:0 sc1\ns_mov_b32 s68, 81920\n"
"s_waitcnt lgkmcnt(8)\nbuffer_store_dwordx4 a[8:11], v64, s[40:43], s68 offen offset:0 sc1\ns_mov_b32 s68, 122880\ns_waitcnt lgkmcnt(7)\nbuffer_store_dwordx4 a[12:15], v64, s[40:43], s68 offen offset:0 sc1\ns_mov_b32 s68, 163840\ns_waitcnt lgkmcnt(6)\nbuffer_store_dwordx4 a[16:19], v64, s[40:43], s68 offen offset:0 sc1\n"
"s_mov_b32 s68, 204800\ns_waitcnt lgkmcnt(5)\nbuffer_store_dwordx4 a[20:23], v64, s[40:43], s68 offen offset:0 sc1\ns_mov_b32 s68, 245760\ns_waitcnt lgkmcnt(4)\nbuffer_store_dwordx4 a[24:27], v64, s[40:43], s68 offen offset:0 sc1\ns_mov_b32 s68, 0\ns_waitcnt lgkmcnt(3)\n"
"buffer_store_dwordx4 a[28:31], v72, s[40:43], s68 offen offset:0 sc1\ns_mov_b32 s68, 81920\ns_waitcnt lgkmcnt(2)\nbuffer_store_dwordx4 a[32:35], v72, s[40:43], s68 offen offset:0 sc1\ns_mov_b32 s68, 163840\ns_waitcnt lgkmcnt(1)\nbuffer_store_dwordx4 a[36:39], v72, s[40:43], s68 offen offset:0 sc1\ns_mov_b32 s68, 245760\n"
"s_cmp_eq_u32 s93, 1\ns_cselect_b32 s68, 0x7ffff000, s68\ns_waitcnt lgkmcnt(0)\nbuffer_store_dwordx4 a[40:43], v72, s[40:43], s68 offen offset:0 sc1\ns_branch .Lfin\n.Lrole1:\n.Lpg22:\ns_cbranch_execz .Lpg26\n"
"Qmvaa 96,64,88\nQta 56,69,49408\ns_lshl_b32 s46, %19, 4\nQmvav 96,68,168\nbuffer_load_dwordx4 a[84:87], v79, s[48:51], s31 offen offset:1024 sc1\nQmvaa 128,64,92\nds_read_b32 v66, v74\ns_add_u32 s46, s46, 128\n"
"Qmvav 128,68,200\nQta 58,69,50432\nv_mov_b32 v77, 0x7ffff000\nQmvaa 160,64,96\nQmvav 160,68,232\nbuffer_load_dwordx4 a[88:91], v79, s[48:51], s31 offen offset:2048 sc1\nQmvaa 192,64,100\ns_waitcnt lgkmcnt(1)\n"
"v_cmp_gt_u32 vcc, v75, v66\nQmvav 192,68,212\ns_cbranch_vccnz .Lso1525r1\n.Lro1525r1:\nQmvaa 224,64,104\nQta 60,69,57600\nQmvav 224,68,244\nbuffer_load_dwordx4 a[92:95], v79, s[48:51], s31 offen offset:3072 sc1\n"
"s_waitcnt vmcnt(31)\ns_waitcnt lgkmcnt(4)\nQmaav 140,72,84\nQmaav 140,76,116\nQta 62,69,58624\nQmaav 136,72,240\ns_add_u32 m0, s96, 65536\ns_add_u32 s95, s67, 86016\n"
"Qmaav 136,76,248\nbuffer_load_dwordx4 v62, s[52:55], s95 offen lds\nQmaav 132,72,208\nQta 64,69,49664\nQmaav 132,76,216\nQmaav 128,72,176\nQmaav 128,76,184\nQta 66,69,50688\n"
"s_add_u32 s95, s67, 125952\nQmaav 124,72,144\nbuffer_load_dwordx4 v62, s[52:55], s95 offen offset:1024 lds\nQmaav 124,76,152\nQmaav 120,72,112\nQta 68,69,57856\nQmaav 120,76,120\ns_add_u32 m0, s96, 73728\n"
"s_add_u32 s95, s72, 86016\nQmaav 116,72,80\nbuffer_load_dwordx4 v62, s[52:55], s95 offen lds\nQmaav 116,76,88\nQta 70,69,58880\ns_waitcnt lgkmcnt(4)\nQmaav 144,56,80\nQmaav 144,60,88\n"
"s_add_u32 s95, s72, 125952\nQmaav 148,56,112\nbuffer_load_dwordx4 v62, s[52:55], s95 offen offset:1024 lds\nQmaav 148,60,120\nQmaav 152,56,144\nQta 72,69,49920\nQmaav 152,60,152\ns_add_u32 m0, s96, 81920\n"
"s_add_u32 s95, s73, 86016\nQmaav 156,56,176\nbuffer_load_dwordx4 v62, s[52:55], s95 offen lds\nQmaav 156,60,184\nQta 74,69,50944\nQmaav 160,56,208\nQmaav 160,60,216\nQmaav 164,56,240\n"
"Qta 76,69,58112\ns_add_u32 s95, s73, 125952\nQmaav 164,60,248\nbuffer_load_dwordx4 v62, s[52:55], s95 offen offset:1024 lds\nQmaav 168,56,84\nQmaav 168,60,116\nQta 78,69,59136\ns_waitcnt lgkmcnt(4)\n"
"Qmvav 24,64,84\ns_add_u32 m0, s96, 90112\ns_add_u32 s95, s67, 86016\nQmvav 24,68,116\nbuffer_load_dwordx4 v61, s[52:55], s95 offen lds\nQmvav 20,64,240\nQmvav 20,68,248\nQmvav 16,64,208\n"
"Qta 56,68,0\ns_add_u32 s95, s67, 105472\nQmvav 16,68,216\nbuffer_load_dwordx4 v61, s[52:55], s95 offen offset:1024 lds\nQmvav 12,64,176\nQmvav 12,68,184\nQta 58,68,512\nQmvav 8,64,144\n"
"s_add_u32 s31, s74, 135168\nQmvav 8,68,152\nbuffer_load_dwordx4 a[96:99], v79, s[48:51], s31 offen offset:0 sc1\nQmvav 4,64,112\nQta 60,68,1024\nQmvav 4,68,120\nQmvav 0,64,80\nQmvav 0,68,88\n"
"Qta 62,68,1536\ns_waitcnt lgkmcnt(4)\nQmvav 28,72,80\nbuffer_load_dwordx4 a[100:103], v79, s[48:51], s31 offen offset:1024 sc1\nQmvav 28,76,88\nQmvav 32,72,112\nQmvav 32,76,120\nQmvav 36,72,144\n"
"Qta 64,68,256\nQmvav 36,76,152\nbuffer_load_dwordx4 a[104:107], v79, s[48:51], s31 offen offset:2048 sc1\nQmvav 40,72,176\nQmvav 40,76,184\nQta 66,68,768\nQmvav 44,72,208\ns_add_u32 s31, s74, 147456\n"
"v_cmp_eq_u32 s[44:45], s46, v60\ns_nop 1\nv_cndmask_b32 v77, v77, v76, s[44:45]\nv_add_u32 v67, 1, v65\ns_xor_b32 s46, %19, 12\ns_lshl_b32 s28, 1, s46\nQmvav 44,76,216\nbuffer_load_dwordx4 a[108:111], v79, s[48:51], s31 offen offset:0 sc1\n"
"Qmvav 48,72,240\nQta 68,68,1280\nQmvav 48,76,248\nv_add_u32 v75, 1, v75\nQmvav 52,72,84\ns_waitcnt vmcnt(37)\nds_write_b32 v73, v75\nQmvav 52,76,116\n"
"Qta 70,68,1792\ns_waitcnt lgkmcnt(5)\nQmaav 196,56,84\nbuffer_load_dwordx4 a[112:115], v79, s[48:51], s31 offen offset:1024 sc1\nQmaav 196,60,116\nQmaav 192,56,240\nQmaav 192,60,248\nQmaav 188,56,208\n"
"Qta 72,68,8192\nQmaav 188,60,216\nbuffer_load_dwordx4 v[168:171], v79, s[48:51], s31 offen offset:2048 sc1\nQmaav 184,56,176\nds_read_b32 v66, v74\nQmaav 184,60,184\nQta 74,68,9216\nQmaav 180,56,144\n"
"Qmaav 180,60,152\nbuffer_load_dwordx4 v[200:203], v79, s[48:51], s31 offen offset:3072 sc1\nQmaav 176,56,112\ns_waitcnt lgkmcnt(1)\nv_cmp_gt_u32 vcc, v75, v66\nQmaav 176,60,120\ns_cbranch_vccnz .Lso1595r1\n.Lro1595r1:\n"
"Qmaav 172,56,80\nQta 76,68,16384\ns_add_u32 s31, s74, 151552\nQmaav 172,60,88\nbuffer_load_dwordx4 v[232:235], v79, s[48:51], s31 offen offset:0 sc1\ns_waitcnt lgkmcnt(4)\nQmvav 92,64,80\nQmvav 92,68,88\n"
"Qta 78,68,17408\nQmvav 124,64,112\nQmvav 124,68,120\nbuffer_load_dwordx4 v[212:215], v79, s[48:51], s31 offen offset:1024 sc1\nQmvav 156,64,144\nQta 56,68,8448\nQmvav 156,68,152\nQmvav 188,64,176\n"
"Qmvav 188,68,184\nQta 58,68,9472\nQmvav 220,64,208\nbuffer_load_dwordx4 v[244:247], v79, s[48:51], s31 offen offset:2048 sc1\nQmvav 220,68,216\nQmvav 252,64,240\nQta 60,68,16640\nQmvav 252,68,248\n"
"s_add_u32 m0, s91, 0\ns_add_u32 s31, s77, 131072\nQmvav 100,64,84\nbuffer_load_dwordx4 v79, s[48:51], s31 offen sc1 lds\nQmvav 100,68,116\nQta 62,68,17664\ns_waitcnt lgkmcnt(4)\nQmaav 224,72,84\n"
"Qmaav 224,76,116\nQmaav 220,72,240\nbuffer_load_dwordx4 v79, s[48:51], s31 offen offset:1024 sc1 lds\nQmaav 220,76,248\nQmaav 216,72,208\nQta 64,68,8704\nQmaav 216,76,216\nQmaav 212,72,176\n"
"buffer_load_dwordx4 v79, s[48:51], s31 offen offset:2048 sc1 lds\nQmaav 212,76,184\nQta 66,68,9728\nQmaav 208,72,144\nQmaav 208,76,152\nQmaav 204,72,112\nQta 68,68,16896\nQmaav 204,76,120\n"
"buffer_load_dwordx4 v79, s[48:51], s31 offen offset:3072 sc1 lds\nQmaav 200,72,80\nQmaav 200,76,88\nQta 70,68,17920\ns_waitcnt lgkmcnt(4)\nQmaav 228,56,80\n.Lpg23:\ns_cbranch_execz .Ltr8\n"
"s_add_u32 m0, s91, 4096\ns_add_u32 s31, s77, 135168\nQmaav 228,60,88\nbuffer_load_dwordx4 v79, s[48:51], s31 offen sc1 lds\nQmaav 232,56,112\nQmaav 232,60,120\nQmaav 236,56,144\nQta 72,68,8960\n"
"Qmaav 236,60,152\nbuffer_load_dwordx4 v79, s[48:51], s31 offen offset:1024 sc1 lds\nQmaav 240,56,176\nQmaav 240,60,184\nQta 74,68,9984\nQmaav 244,56,208\nQmaav 244,60,216\nbuffer_load_dwordx4 v79, s[48:51], s31 offen offset:2048 sc1 lds\n"
"Qmaav 248,56,240\nQta 76,68,17152\nQmaav 248,60,248\nQmaav 252,56,84\nQmaav 252,60,116\nQta 78,68,18176\ns_add_u32 m0, s91, 33792\ns_add_u32 s31, s77, 147456\n"
"s_waitcnt lgkmcnt(4)\nQmvav 172,64,84\nbuffer_load_dwordx4 v79, s[48:51], s31 offen sc1 lds\nQmvav 172,68,116\nQmvav 140,64,240\nQmvav 140,68,248\nQmvav 108,64,208\nQta 56,68,24576\n"
"Qmvav 108,68,216\nbuffer_load_dwordx4 v79, s[48:51], s31 offen offset:1024 sc1 lds\nQmvav 228,64,176\nQmvav 228,68,184\nQta 58,68,25600\nQmvav 196,64,144\nQmvav 196,68,152\nbuffer_load_dwordx4 v79, s[48:51], s31 offen offset:2048 sc1 lds\n"
"Qmvav 164,64,112\nQta 60,68,32768\nQmvav 164,68,120\nv_add_u32 v75, 1, v75\nQmvav 132,64,80\ns_waitcnt vmcnt(49)\nds_write_b32 v73, v75\nQmvav 132,68,88\n"
"Qta 62,68,33792\ns_waitcnt lgkmcnt(5)\nQmvav 204,72,80\nbuffer_load_dwordx4 v79, s[48:51], s31 offen offset:3072 sc1 lds\nQmvav 204,76,88\nQmvav 236,72,112\nQmvav 236,76,120\nQmvav 96,72,144\n"
"Qta 64,68,24832\ns_add_u32 m0, s91, 37888\ns_add_u32 s31, s77, 151552\nQmvav 96,76,152\nbuffer_load_dwordx4 v79, s[48:51], s31 offen sc1 lds\nQmvav 128,72,176\nds_read_b32 v66, v74\nQmvav 128,76,184\n"
"Qta 66,68,25856\nQmvav 160,72,208\nQmvav 160,76,216\nbuffer_load_dwordx4 v79, s[48:51], s31 offen offset:1024 sc1 lds\nQmvav 192,72,240\ns_waitcnt lgkmcnt(1)\nv_cmp_gt_u32 vcc, v75, v66\nQmvav 192,76,248\n"
"s_cbranch_vccnz .Lso1665r1\n.Lro1665r1:\nQmvav 224,72,84\nQta 68,68,33024\nQmvav 224,76,116\nbuffer_load_dwordx4 v79, s[48:51], s31 offen offset:2048 sc1 lds\ns_waitcnt vmcnt(38)\ns_waitcnt lgkmcnt(4)\n"
"Qmaaa 140,56,24\nQmaaa 140,60,52\nQta 70,68,34048\nQmaaa 136,56,20\ns_add_u32 m0, s96, 0\ns_add_u32 s95, s67, 126976\nQmaaa 136,60,48\nbuffer_load_dwordx4 v61, s[52:55], s95 offen lds\n"
"Qmaaa 132,56,16\nQta 72,68,25088\nQmaaa 132,60,44\nQmaaa 128,56,12\nQmaaa 128,60,40\nQta 74,68,26112\ns_add_u32 s95, s67, 146432\nQmaaa 124,56,8\n"
"buffer_load_dwordx4 v61, s[52:55], s95 offen offset:1024 lds\nQmaaa 124,60,36\nQmaaa 120,56,4\nQta 76,68,33280\nQmaaa 120,60,32\ns_add_u32 s31, s75, 131072\nQmaaa 116,56,0\nbuffer_load_dwordx4 v[80:83], v79, s[48:51], s31 offen offset:0 sc1\n"
"Qmaaa 116,60,28\nQta 78,68,34304\ns_waitcnt lgkmcnt(4)\nQmaaa 144,64,0\nQmaaa 144,68,28\nQmaaa 148,64,4\nbuffer_load_dwordx4 v[112:115], v79, s[48:51], s31 offen offset:1024 sc1\nQmaaa 148,68,32\n"
"Qmaaa 152,64,8\nQta 56,68,25344\nQmaaa 152,68,36\nQmaaa 156,64,12\nbuffer_load_dwordx4 v[144:147], v79, s[48:51], s31 offen offset:2048 sc1\nQmaaa 156,68,40\nQta 58,68,26368\nQmaaa 160,64,16\n"
"Qmaaa 160,68,44\nQmaaa 164,64,20\nQta 60,68,33536\nQmaaa 164,68,48\nbuffer_load_dwordx4 v[176:179], v79, s[48:51], s31 offen offset:3072 sc1\nQmaaa 168,64,24\nQmaaa 168,68,52\nQta 62,68,34560\n"
"s_waitcnt lgkmcnt(4)\nQmvaa 24,72,24\ns_add_u32 s31, s75, 135168\nQmvaa 24,76,52\nbuffer_load_dwordx4 v[208:211], v79, s[48:51], s31 offen offset:0 sc1\nQmvaa 20,72,20\nQmvaa 20,76,48\nQmvaa 16,72,16\n"
"Qta 64,68,40960\nQmvaa 16,76,44\nbuffer_load_dwordx4 v[240:243], v79, s[48:51], s31 offen offset:1024 sc1\nQmvaa 12,72,12\nQmvaa 12,76,40\nQta 66,68,41472\nQmvaa 8,72,8\nQmvaa 8,76,36\n"
"buffer_load_dwordx4 v[84:87], v79, s[48:51], s31 offen offset:2048 sc1\nQmvaa 4,72,4\nQta 68,68,41984\nQmvaa 4,76,32\nQmvaa 0,72,0\nQmvaa 0,76,28\nQta 70,68,42496\ns_add_u32 s31, s75, 147456\n"
"s_waitcnt lgkmcnt(4)\nQmvaa 28,56,0\nbuffer_load_dwordx4 v[88:91], v79, s[48:51], s31 offen offset:0 sc1\nQmvaa 28,60,28\nQmvaa 32,56,4\nQmvaa 32,60,32\nQmvaa 36,56,8\nQta 72,68,41216\n"
"Qmvaa 36,60,36\nbuffer_load_dwordx4 v[120:123], v79, s[48:51], s31 offen offset:1024 sc1\nQmvaa 40,56,12\nQmvaa 40,60,40\nQta 74,68,41728\nQmvaa 44,56,16\nQmvaa 44,60,44\nbuffer_load_dwordx4 v[152:155], v79, s[48:51], s31 offen offset:2048 sc1\n"
"Qmvaa 48,56,20\nQta 76,68,42240\nQmvaa 48,60,48\nv_add_u32 v75, 1, v75\nQmvaa 52,56,24\ns_waitcnt vmcnt(40)\nds_write_b32 v73, v75\nQmvaa 52,60,52\n"
"Qta 78,68,42752\ns_waitcnt lgkmcnt(5)\nQmaaa 196,64,24\nbuffer_load_dwordx4 v[184:187], v79, s[48:51], s31 offen offset:3072 sc1\nQmaaa 196,68,52\nQmaaa 192,64,20\nQmaaa 192,68,48\nQmaaa 188,64,16\n"
"Qta 56,68,49152\ns_add_u32 s31, s75, 151552\nQmaaa 188,68,44\nbuffer_load_dwordx4 v[216:219], v79, s[48:51], s31 offen offset:0 sc1\nQmaaa 184,64,12\nds_read_b32 v66, v74\nQmaaa 184,68,40\nQta 58,68,50176\n"
"Qmaaa 180,64,8\nQmaaa 180,68,36\nbuffer_load_dwordx4 v[248:251], v79, s[48:51], s31 offen offset:1024 sc1\nQmaaa 176,64,4\ns_waitcnt lgkmcnt(1)\nv_cmp_gt_u32 vcc, v75, v66\nQmaaa 176,68,32\ns_cbranch_vccnz .Lso1735r1\n"
".Lro1735r1:\nQmaaa 172,64,0\nQta 60,68,57344\nQmaaa 172,68,28\nbuffer_load_dwordx4 v[116:119], v79, s[48:51], s31 offen offset:2048 sc1\ns_waitcnt lgkmcnt(4)\nQmvaa 92,72,0\nQmvaa 92,76,28\n"
"Qta 62,68,58368\nQmvaa 124,72,4\ns_add_u32 m0, s96, 8192\ns_add_u32 s95, s72, 86016\nQmvaa 124,76,32\nbuffer_load_dwordx4 v61, s[52:55], s95 offen lds\nQmvaa 156,72,8\nQta 64,68,49408\n"
"Qmvaa 156,76,36\nQmvaa 188,72,12\nQmvaa 188,76,40\nQta 66,68,50432\ns_add_u32 s95, s72, 105472\nQmvaa 220,72,16\nbuffer_load_dwordx4 v61, s[52:55], s95 offen offset:1024 lds\nQmvaa 220,76,44\n"
"Qmvaa 252,72,20\nQta 68,68,57600\nQmvaa 252,76,48\ns_add_u32 m0, s96, 16384\ns_add_u32 s95, s72, 126976\nQmvaa 100,72,24\nbuffer_load_dwordx4 v61, s[52:55], s95 offen lds\nQmvaa 100,76,52\n"
"Qta 70,68,58624\ns_waitcnt lgkmcnt(4)\nQmaaa 224,56,24\nQmaaa 224,60,52\ns_add_u32 s95, s72, 146432\nQmaaa 220,56,20\nbuffer_load_dwordx4 v61, s[52:55], s95 offen offset:1024 lds\nQmaaa 220,60,48\n"
"Qmaaa 216,56,16\nQta 72,68,49664\nQmaaa 216,60,44\ns_add_u32 m0, s96, 24576\ns_add_u32 s95, s73, 86016\nQmaaa 212,56,12\nbuffer_load_dwordx4 v61, s[52:55], s95 offen lds\nQmaaa 212,60,40\n"
"Qta 74,68,50688\nQmaaa 208,56,8\nQmaaa 208,60,36\nQmaaa 204,56,4\nQta 76,68,57856\ns_add_u32 s95, s73, 105472\nQmaaa 204,60,32\nbuffer_load_dwordx4 v61, s[52:55], s95 offen offset:1024 lds\n"
"Qmaaa 200,56,0\nQmaaa 200,60,28\nQta 78,68,58880\ns_waitcnt lgkmcnt(4)\nQmaaa 228,64,0\ns_add_u32 m0, s96, 32768\ns_add_u32 s95, s73, 126976\nQmaaa 228,68,28\n"
"buffer_load_dwordx4 v61, s[52:55], s95 offen lds\nQmaaa 232,64,4\nQmaaa 232,68,32\nQmaaa 236,64,8\nQta 56,68,49920\ns_add_u32 s95, s73, 146432\nQmaaa 236,68,36\nbuffer_load_dwordx4 v61, s[52:55], s95 offen offset:1024 lds\n"
"Qmaaa 240,64,12\nQmaaa 240,68,40\nQta 58,68,50944\nQmaaa 244,64,16\nQmaaa 244,68,44\nQmaaa 248,64,20\nQta 60,68,58112\nQmaaa 248,68,48\n"
"Qmaaa 252,64,24\nQmaaa 252,68,52\nQta 62,68,59136\ns_waitcnt lgkmcnt(4)\nQmvaa 172,72,24\nQmvaa 172,76,52\nQmvaa 140,72,20\nQmvaa 140,76,48\n"
"Qmvaa 108,72,16\nQta 64,69,0\nQmvaa 108,76,44\nQmvaa 228,72,12\nQmvaa 228,76,40\nQta 66,69,512\nQmvaa 196,72,8\nQmvaa 196,76,36\n"
"Qmvaa 164,72,4\nQta 68,69,1024\nQmvaa 164,76,32\nv_add_u32 v75, 1, v75\nQmvaa 132,72,0\ns_waitcnt vmcnt(22)\nds_write_b32 v73, v75\nQmvaa 132,76,28\n"
"Qta 70,69,1536\ns_waitcnt lgkmcnt(5)\nQmvaa 204,56,0\ns_waitcnt vmcnt(24)\nQda 0,58,0\nQmvaa 204,60,28\nQmvaa 236,56,4\nQda 28,58,33792\n"
"Qmvaa 236,60,32\nQmvaa 96,56,8\nQta 72,69,8192\nQmvaa 96,60,36\nQda 4,58,1024\nQmvaa 128,56,12\nds_read_b32 v66, v74\nQmvaa 128,60,40\n"
"Qta 74,69,8704\nQmvaa 160,56,16\nQda 32,58,34816\nQmvaa 160,60,44\nQmvaa 192,56,20\ns_waitcnt lgkmcnt(2)\nv_cmp_gt_u32 vcc, v75, v66\nQmvaa 192,60,48\n"
"s_cbranch_vccnz .Lso1805r1\n.Lro1805r1:\nQmvaa 224,56,24\nQta 76,69,9216\ns_add_u32 m0, s96, 40960\ns_add_u32 s95, s67, 86016\nQmvaa 224,60,52\nbuffer_load_dwordx4 v63, s[52:55], s95 offen lds\n"
"s_waitcnt vmcnt(39)\ns_waitcnt lgkmcnt(8)\nQmaaa 172,64,80\nQda 8,58,2048\nQmaaa 172,68,108\nQta 78,69,9728\nQmaaa 176,64,84\nQda 36,58,35840\n"
"s_add_u32 s95, s67, 105472\nQmaaa 176,68,112\nbuffer_load_dwordx4 v63, s[52:55], s95 offen offset:1024 lds\nQmaaa 180,64,88\nQta 56,69,16384\nQmaav 180,68,168\nQda 12,58,3072\nQmaaa 184,64,92\n"
"Qmaav 184,68,200\nQta 58,69,16896\ns_add_u32 m0, s96, 49152\ns_add_u32 s95, s67, 126976\nQmaaa 188,64,96\nbuffer_load_dwordx4 v63, s[52:55], s95 offen lds\nQmaav 188,68,232\nQda 40,58,36864\n"
"Qmaaa 192,64,100\nQta 60,69,17408\nQmaav 192,68,212\nQda 16,58,4096\ns_add_u32 s95, s67, 146432\nQmaaa 196,64,104\nbuffer_load_dwordx4 v63, s[52:55], s95 offen offset:1024 lds\nQmaav 196,68,244\n"
"Qta 62,69,17920\ns_waitcnt vmcnt(12)\ns_waitcnt lgkmcnt(8)\nQmaav 172,72,80\nQda 44,58,37888\nQmaav 172,76,88\ns_add_u32 m0, s96, 57344\ns_add_u32 s95, s72, 86016\n"
"Qmaav 176,72,112\nbuffer_load_dwordx4 v63, s[52:55], s95 offen lds\nQmaav 176,76,120\nQda 20,58,5120\nQmaav 180,72,144\nQta 64,69,24576\nv_add_u32 v75, 1, v75\nQmaav 180,76,152\n"
"s_waitcnt vmcnt(5)\nds_write_b32 v73, v75\ns_add_u32 s95, s72, 105472\nQmaav 184,72,176\nbuffer_load_dwordx4 v63, s[52:55], s95 offen offset:1024 lds\nQmaav 184,76,184\nQta 66,69,25600\nQmaav 188,72,208\n"
"Qda 48,58,38912\nQmaav 188,76,216\nQmaav 192,72,240\nQta 68,68,0\nQmaav 192,76,248\nQda 24,58,6144\nQmaav 196,72,84\nQmaav 196,76,116\n"
"ds_read_b32 v66, v74\ns_waitcnt lgkmcnt(9)\nQmaaa 172,56,0\nQta 70,68,1024\nQmaaa 172,60,28\nQda 52,58,39936\nQmaaa 176,56,4\nQmaaa 176,60,32\n"
"s_waitcnt lgkmcnt(2)\nv_cmp_gt_u32 vcc, v75, v66\ns_waitcnt lgkmcnt(11)\nQmaaa 180,56,8\ns_cbranch_vccnz .Lso1840r1\n.Lro1840r1:\nQmaaa 180,60,36\nQta 72,68,8192\n"
"Qmaaa 184,56,12\nds_write_b128 v58, a[172:175] offset:0\nQmaaa 184,60,40\ns_waitcnt lgkmcnt(2)\nQmaaa 188,56,16\nQta 74,68,9216\nQmaaa 188,60,44\nds_write_b128 v58, a[176:179] offset:1024\n"
"Qmaaa 192,56,20\nQmaaa 192,60,48\nQta 76,68,16384\nQmaaa 196,56,24\nds_write_b128 v58, a[180:183] offset:2048\nQmaaa 196,60,52\ns_waitcnt lgkmcnt(7)\nQmaaa 116,64,80\n"
"Qta 78,68,17408\nQmaaa 116,68,108\nds_write_b128 v58, a[184:187] offset:3072\nQmaaa 120,64,84\nQmaaa 120,68,112\nQmaaa 124,64,88\nQta 56,68,24576\nQmaav 124,68,168\n"
"ds_write_b128 v58, a[188:191] offset:4096\nQmaaa 128,64,92\nQmaav 128,68,200\nQta 58,68,25600\nQmaaa 132,64,96\nds_write_b128 v58, a[192:195] offset:5120\nQmaav 132,68,232\nQmaaa 136,64,100\n"
"Qta 60,68,32768\nQmaav 136,68,212\nds_write_b128 v58, a[196:199] offset:6144\nQmaaa 140,64,104\nQmaav 140,68,244\nQta 62,68,33792\ns_waitcnt lgkmcnt(8)\nQmaav 116,72,80\n"
"v_add_u32 v75, 1, v75\nQmaav 116,76,88\nds_write_b32 v73, v75\nQmaav 120,72,112\nQmaav 120,76,120\nQmaav 124,72,144\nQta 64,69,256\nQmaav 124,76,152\n"
"Qmaav 128,72,176\nQmaav 128,76,184\nQta 66,69,768\nQmaav 132,72,208\nQmaav 132,76,216\nds_read_b32 v66, v74\nQmaav 136,72,240\nQta 68,69,1280\n"
"Qmaav 136,76,248\nQmaav 140,72,84\nQmaav 140,76,116\ns_waitcnt lgkmcnt(1)\nv_cmp_gt_u32 vcc, v75, v66\ns_waitcnt lgkmcnt(5)\nQmaaa 116,56,0\ns_cbranch_vccnz .Lso1878r1\n"
".Lro1878r1:\nQmaaa 116,60,28\nQta 70,69,1792\nQmaaa 120,56,4\nQmaaa 120,60,32\nQdv 148,59,2048\nQmaaa 124,56,8\nQta 72,69,8448\n"
"Qmaaa 124,60,36\ns_nop 0\nds_write_b128 v58, a[116:119] offset:33792\nQmaaa 128,56,12\nQdv 180,59,3072\nQmaaa 128,60,40\nQta 74,69,8960\nQmaaa 132,56,16\n"
"ds_write_b128 v58, a[120:123] offset:34816\nQmaaa 132,60,44\nQdv 104,59,5120\nQmaaa 136,56,20\nQta 76,69,9472\nQmaaa 136,60,48\nds_write_b128 v58, a[124:127] offset:35840\nQmaaa 140,56,24\n"
"Qdv 136,59,0\ns_waitcnt lgkmcnt(9)\nQa 156,156,148\nQmaaa 140,60,52\nQta 78,69,9984\nQa 157,157,149\ns_waitcnt lgkmcnt(10)\nQmvaa 92,64,80\n"
"ds_write_b128 v58, a[128:131] offset:36864\nQmvaa 92,68,108\nQa 158,158,150\nQa 159,159,151\nQmvaa 124,64,84\ns_waitcnt lgkmcnt(8)\nQa 188,188,180\nQa 189,189,181\n"
"Qmvaa 124,68,112\nds_write_b128 v58, a[132:135] offset:37888\nQmvaa 156,64,88\nQta 56,69,16640\nQa 190,190,182\nQa 191,191,183\nQmvav 156,68,168\nQdv 148,59,4096\n"
"s_waitcnt lgkmcnt(8)\nQa 252,252,104\nQmvaa 188,64,92\nds_write_b128 v58, a[136:139] offset:38912\nQmvav 188,68,200\nQta 58,69,17152\nQa 253,253,105\nQmvaa 220,64,96\n"
"Qa 254,254,106\nQa 255,255,107\nQmvav 220,68,232\nQdv 180,59,6144\ns_waitcnt lgkmcnt(8)\nQa 92,92,136\nQmvaa 252,64,100\nQta 60,69,17664\n"
"Qa 93,93,137\nQmvav 252,68,212\nds_write_b128 v58, a[140:143] offset:39936\nQmvaa 100,64,104\nQa 94,94,138\nQa 95,95,139\nQmvav 100,68,244\nQta 62,69,18176\n"
".Lpg24:\ns_cbranch_execz .Ltr8\ns_waitcnt lgkmcnt(6)\nQa 220,220,148\ns_waitcnt lgkmcnt(6)\nQmvav 92,72,80\nQdv 104,59,1024\nv_add_u32 v75, 1, v75\n"
"Qmvav 92,76,88\nds_write_b32 v73, v75\nQa 221,221,149\nQmvav 124,72,112\nQa 222,222,150\nQa 223,223,151\nQmvav 124,76,120\ns_waitcnt lgkmcnt(5)\n"
"Qa 100,100,180\nQa 101,101,181\nQmvav 156,72,144\nQta 64,69,24832\nQa 102,102,182\nQmvav 156,76,152\nQa 103,103,183\nQmvav 188,72,176\n"
"Qmvav 188,76,184\nQta 66,69,25856\ns_waitcnt lgkmcnt(3)\nQa 124,124,104\nQmvav 220,72,208\nQa 125,125,105\nQa 126,126,106\nQmvav 220,76,216\n"
"ds_read_b32 v66, v74\nQa 127,127,107\nQmvav 252,72,240\nQta 68,68,256\nQmvav 252,76,248\nQmvav 100,72,84\nQmvav 100,76,116\ns_waitcnt lgkmcnt(1)\n"
"v_cmp_gt_u32 vcc, v75, v66\ns_waitcnt lgkmcnt(6)\nQmvaa 92,56,0\ns_cbranch_vccnz .Lso1920r1\n.Lro1920r1:\ns_add_u32 s68, s64, 0\nQmvaa 92,60,28\nQta 70,68,1280\n"
"s_add_u32 m0, s96, 65536\ns_add_u32 s95, s72, 126976\nQmvaa 124,56,4\nbuffer_load_dwordx4 v63, s[52:55], s95 offen lds\nQmvaa 124,60,32\nQdv 136,59,33792\nQmvaa 156,56,8\nQta 72,68,8448\n"
"Qk 92,92,93\nQmvaa 156,60,36\ns_add_u32 s95, s72, 146432\nQk 93,94,95\nQmvaa 188,56,12\nbuffer_load_dwordx4 v63, s[52:55], s95 offen offset:1024 lds\nQmvaa 188,60,40\nQta 74,68,9472\n"
"Qk 94,124,125\nQmvaa 220,56,16\nQdv 148,59,34816\nQk 95,126,127\nQmvaa 220,60,44\nbuffer_store_dwordx4 v[92:95], v79, s[24:27], s68 offen offset:0 sc1\nQk 156,156,157\nQmvaa 252,56,20\n"
"Qta 76,68,16640\ns_add_u32 m0, s96, 73728\ns_add_u32 s95, s73, 86016\nQmvaa 252,60,48\nbuffer_load_dwordx4 v63, s[52:55], s95 offen lds\nv_add_u32 v75, 1, v75\nQmvaa 100,56,24\nds_write_b32 v73, v75\n"
"s_waitcnt lgkmcnt(5)\nQa 0,0,136\nQmvaa 100,60,52\nQta 78,68,17664\nQa 1,1,137\ns_waitcnt lgkmcnt(6)\nQmaaa 144,64,80\nQdv 180,59,35840\n"
"s_add_u32 s95, s73, 105472\nQmaaa 144,68,108\nbuffer_load_dwordx4 v63, s[52:55], s95 offen offset:1024 lds\nQmaaa 148,64,84\nQa 2,2,138\nQa 3,3,139\nQmaaa 148,68,112\nQdv 104,59,36864\n"
"s_waitcnt lgkmcnt(5)\nQa 4,4,148\nQmaaa 152,64,88\nQta 56,68,24832\ns_add_u32 m0, s96, 81920\ns_add_u32 s95, s73, 126976\nQmaav 152,68,168\nbuffer_load_dwordx4 v63, s[52:55], s95 offen lds\n"
"Qmaaa 156,64,92\nds_read_b32 v66, v74\nQa 5,5,149\nQa 6,6,150\nQa 7,7,151\nQmaav 156,68,200\nQta 58,68,25856\ns_waitcnt lgkmcnt(4)\n"
"Qa 8,8,180\nQmaaa 160,64,96\nQdv 136,59,37888\ns_add_u32 s95, s73, 146432\nQmaav 160,68,232\nbuffer_load_dwordx4 v63, s[52:55], s95 offen offset:1024 lds\nQmaaa 164,64,100\ns_waitcnt lgkmcnt(2)\n"
"v_cmp_gt_u32 vcc, v75, v66\nQmaav 164,68,212\ns_cbranch_vccnz .Lso1945r1\n.Lro1945r1:\nQa 9,9,181\nQa 10,10,182\nQa 11,11,183\nQmaaa 168,64,104\n"
"Qta 60,68,33024\ns_waitcnt lgkmcnt(5)\nQa 12,12,104\nQmaav 168,68,244\nQdv 148,59,38912\nQa 13,13,105\ns_waitcnt lgkmcnt(6)\nQmaav 144,72,80\n"
"Qa 14,14,106\nQa 15,15,107\nQmaav 144,76,88\nQta 62,68,34048\ns_waitcnt lgkmcnt(3)\nQa 16,16,136\nQmaav 148,72,112\nQdv 180,59,39936\n"
"Qa 17,17,137\nQmaav 148,76,120\nQa 18,18,138\nQa 19,19,139\nQmaav 152,72,144\nQta 64,69,25088\nv_bfe_u32 v61, v60, 6, 2\nQmaav 152,76,152\n"
"s_waitcnt lgkmcnt(3)\nQa 20,20,148\nQa 21,21,149\nQmaav 156,72,176\nQa 22,22,150\nQa 23,23,151\nQmaav 156,76,184\nQta 66,69,26112\n"
"v_lshrrev_b32 v62, 8, v60\nQmaav 160,72,208\nv_xor_b32 v61, v61, v62\nv_bfe_u32 v62, v60, 5, 1\nQmaav 160,76,216\nv_lshl_or_b32 v61, v61, 1, v62\nv_and_b32 v62, 16, v60\nQmaav 164,72,240\n"
"Qta 68,68,512\ns_waitcnt lgkmcnt(3)\nQa 24,24,180\nQmaav 164,76,248\nQa 25,25,181\nQa 26,26,182\nQmaav 168,72,84\nQa 27,27,183\n"
"v_mov_b32 v63, 0x3f80\nQmaav 168,76,116\nQta 70,68,1536\nv_lshlrev_b32 v63, v62, v63\ns_waitcnt lgkmcnt(5)\nQmaaa 144,56,0\nv_min_u32 v62, 1, v61\nv_add_u32 v62, -1, v62\n"
"Qmaaa 144,60,28\nv_and_b32 v62, v62, v63\nv_xor_b32 v61, 1, v61\nQmaaa 148,56,4\nv_min_u32 v61, 1, v61\nv_add_u32 v61, -1, v61\nQmaaa 148,60,32\nv_and_b32 v61, v61, v63\n"
"v_accvgpr_write_b32 a174, v62\nQmaaa 152,56,8\nQta 72,68,8704\nv_accvgpr_write_b32 a175, v61\nQmaaa 152,60,36\nds_write_b128 v58, a[144:147] offset:0\nQmaaa 156,56,12\nv_accvgpr_write_b32 a172, 0\n"
"v_accvgpr_write_b32 a173, 0\nQmaaa 156,60,40\nQta 74,68,9728\nv_accvgpr_write_b32 a176, 0\nQmaaa 160,56,16\nds_write_b128 v58, a[148:151] offset:1024\nQmaaa 160,60,44\nv_accvgpr_write_b32 a177, 0\n"
"Qk 157,158,159\nQmaaa 164,56,20\nQta 76,68,16896\nQk 158,188,189\nQmaaa 164,60,48\nds_write_b128 v58, a[152:155] offset:2048\nQmaaa 168,56,24\nQk 159,190,191\n"
"Qk 220,220,221\nQmaaa 168,60,52\nQta 78,68,17920\nbuffer_store_dwordx4 v[156:159], v79, s[24:27], s68 offen offset:1024 sc1\ns_waitcnt lgkmcnt(7)\nQmvaa 0,64,80\nds_write_b128 v58, a[156:159] offset:3072\nQmvaa 0,68,108\n"
"Qk 221,222,223\nQk 222,252,253\nQmvaa 4,64,84\nQk 223,254,255\nQk 100,100,101\nQmvaa 4,68,112\nds_write_b128 v58, a[160:163] offset:4096\nQmvaa 8,64,88\n"
"Qta 56,68,25088\nbuffer_store_dwordx4 v[220:223], v79, s[24:27], s68 offen offset:2048 sc1\nQmvav 8,68,168\nQk 101,102,103\nQmvaa 12,64,92\nds_write_b128 v58, a[164:167] offset:5120\nQmvav 12,68,200\nQta 58,68,26112\n"
"Qmvaa 16,64,96\nQmvav 16,68,232\nds_write_b128 v58, a[168:171] offset:6144\nQmvaa 20,64,100\nQta 60,68,33280\nQmvav 20,68,212\nQmvaa 24,64,104\nQmvav 24,68,244\n"
"Qta 62,68,34304\ns_waitcnt lgkmcnt(8)\nQmvav 0,72,80\nv_add_u32 v75, 1, v75\nQmvav 0,76,88\ns_waitcnt vmcnt(11)\nds_write_b32 v73, v75\nQmvav 4,72,112\n"
"Qmvav 4,76,120\nQmvav 8,72,144\nQta 64,69,25344\nQmvav 8,76,152\nQmvav 12,72,176\nQmvav 12,76,184\nQta 66,69,26368\nQmvav 16,72,208\n"
"Qmvav 16,76,216\nds_read_b32 v66, v74\nQmvav 20,72,240\nQta 68,68,768\nQmvav 20,76,248\nQmvav 24,72,84\nQmvav 24,76,116\ns_waitcnt lgkmcnt(1)\n"
"v_cmp_gt_u32 vcc, v75, v66\ns_waitcnt lgkmcnt(5)\nQmvaa 0,56,0\ns_cbranch_vccnz .Lso2004r1\n.Lro2004r1:\nQmvaa 0,60,28\nQta 70,68,1792\nQmvaa 4,56,4\n"
"Qmvaa 4,60,32\nQdv 104,59,2048\nQmvaa 8,56,8\nQta 72,68,8960\ns_nop 1\nQk 102,0,1\nQmvaa 8,60,36\nQk 103,2,3\n"
"Qmvaa 12,56,12\nQdv 136,59,3072\nbuffer_store_dwordx4 v[100:103], v79, s[24:27], s68 offen offset:3072 sc1\nQmvaa 12,60,40\nQta 74,68,9984\nQk 4,4,5\nQmvaa 16,56,16\nQk 5,6,7\n"
"Qk 6,8,9\nQmvaa 16,60,44\nQdv 148,59,5120\nQk 7,10,11\nQmvaa 20,56,20\nQta 76,68,17152\ns_add_u32 s68, s64, 4096\nQmvaa 20,60,48\n"
"s_waitcnt lgkmcnt(5)\nQa 36,36,104\nQa 37,37,105\nQmvaa 24,56,24\nQdv 180,59,0\nQa 38,38,106\nQmvaa 24,60,52\nQta 78,68,18176\n"
"Qa 39,39,107\ns_waitcnt lgkmcnt(11)\nQmvaa 28,64,80\ns_waitcnt lgkmcnt(5)\nQa 40,40,136\nQa 41,41,137\nQmvaa 28,68,108\nQa 42,42,138\n"
"Qa 43,43,139\nQmvaa 32,64,84\nbuffer_store_dwordx4 v[4:7], v79, s[24:27], s68 offen offset:0 sc1\nQk 12,12,13\nQmvaa 32,68,112\ns_waitcnt lgkmcnt(3)\nQa 48,48,148\nQa 49,49,149\n"
"Qmvaa 36,64,88\nQta 56,68,25344\nQa 50,50,150\nQmvav 36,68,168\nQdv 104,59,4096\nQa 51,51,151\nQmvaa 40,64,92\ns_waitcnt lgkmcnt(3)\n"
"Qa 28,28,180\nQa 29,29,181\nQmvav 40,68,200\nQta 58,68,26368\nQa 30,30,182\nQmvaa 44,64,96\nQa 31,31,183\nQk 13,14,15\n"
"Qmvav 44,68,232\nQdv 136,59,6144\nQk 14,16,17\nQmvaa 48,64,100\nQta 60,68,33536\nQk 15,18,19\nQmvav 48,68,212\nbuffer_store_dwordx4 v[12:15], v79, s[24:27], s68 offen offset:1024 sc1\n"
"Qk 20,20,21\nQmvaa 52,64,104\nQk 21,22,23\nQk 22,24,25\nQmvav 52,68,244\nQta 62,68,34560\nv_add_u32 v75, 1, v75\ns_waitcnt lgkmcnt(6)\n"
"Qmvav 28,72,80\ns_waitcnt vmcnt(5)\nds_write_b32 v73, v75\nQmvav 28,76,88\nQdv 148,59,1024\ns_waitcnt lgkmcnt(6)\nQa 44,44,104\nQmvav 32,72,112\n"
"Qa 45,45,105\nQa 46,46,106\nQmvav 32,76,120\nQa 47,47,107\ns_waitcnt lgkmcnt(4)\nQa 52,52,136\nQmvav 36,72,144\nQta 64,68,40960\n"
"Qa 53,53,137\nQmvav 36,76,152\nQa 54,54,138\nQa 55,55,139\nQmvav 40,72,176\nQk 23,26,27\nQmvav 40,76,184\nQta 66,68,41984\n"
"s_waitcnt lgkmcnt(2)\nQa 32,32,148\nQmvav 44,72,208\nds_read_b32 v66, v74\nQa 33,33,149\nQmvav 44,76,216\nQa 34,34,150\nQa 35,35,151\n"
"Qmvav 48,72,240\nQta 68,68,49152\nbuffer_store_dwordx4 v[20:23], v79, s[24:27], s68 offen offset:2048 sc1\nQmvav 48,76,248\nQmvav 52,72,84\ns_waitcnt lgkmcnt(1)\nv_cmp_gt_u32 vcc, v75, v66\nQmvav 52,76,116\n"
"s_cbranch_vccnz .Lso2045r1\n.Lro2045r1:\ns_waitcnt lgkmcnt(6)\nQmvaa 28,56,0\nQta 70,68,50176\nQmvaa 28,60,28\nQmvaa 32,56,4\nQmvaa 32,60,32\n"
"Qmvaa 36,56,8\nQta 72,68,57344\ns_nop 3\nQk 28,28,29\nQmvaa 36,60,36\nQk 29,30,31\nQmvaa 40,56,12\nQk 30,32,33\n"
"Qk 31,34,35\nQmvaa 40,60,40\nQta 74,68,58368\nbuffer_store_dwordx4 v[28:31], v79, s[24:27], s68 offen offset:3072 sc1\nQmvaa 44,56,16\nQk 36,36,37\nQk 37,38,39\nQmvaa 44,60,44\n"
"s_add_u32 s68, s64, 16384\nQmvaa 48,56,20\nQta 76,69,0\nQk 38,40,41\nQmvaa 48,60,48\nQk 39,42,43\nQmvaa 52,56,24\nbuffer_store_dwordx4 v[36:39], v79, s[24:27], s68 offen offset:0 sc1\n"
"Qk 44,44,45\nQmvaa 52,60,52\nQta 78,69,1024\nQk 45,46,47\ns_waitcnt lgkmcnt(4)\nQmaaa 200,64,80\nQk 46,48,49\nQk 47,50,51\n"
"Qmaaa 200,68,108\nbuffer_store_dwordx4 v[44:47], v79, s[24:27], s68 offen offset:1024 sc1\nQmaaa 204,64,84\nQk 52,52,53\nQk 53,54,55\nQmaaa 204,68,112\nv_mov_b32 v54, 0\nv_mov_b32 v55, 0\n"
"Qmaaa 208,64,88\nQta 56,69,8192\nbuffer_store_dwordx4 v[52:55], v79, s[24:27], s68 offen offset:2048 sc1\nQmaav 208,68,168\nQmaaa 212,64,92\nQmaav 212,68,200\nQta 58,69,9216\nQmaaa 216,64,96\n"
"Qmaav 216,68,232\nQmaaa 220,64,100\nQta 60,69,16384\nQmaav 220,68,212\nQmaaa 224,64,104\nQmaav 224,68,244\nQta 62,69,17408\ns_waitcnt lgkmcnt(4)\n"
"Qmaav 200,72,80\nQmaav 200,76,88\nQmaav 204,72,112\nQmaav 204,76,120\nQmaav 208,72,144\nQta 64,68,41216\nQmaav 208,76,152\nQmaav 212,72,176\n"
"Qmaav 212,76,184\nQta 66,68,42240\nQmaav 216,72,208\nQmaav 216,76,216\nQmaav 220,72,240\nQta 68,68,49408\nQmaav 220,76,248\nQmaav 224,72,84\n"
"Qmaav 224,76,116\nQta 70,68,50432\ns_waitcnt lgkmcnt(4)\nQmaaa 200,56,0\nQmaaa 200,60,28\nQmaaa 204,56,4\nQmaaa 204,60,32\nQmaaa 208,56,8\n"
"Qta 72,68,57600\nQmaaa 208,60,36\ns_nop 2\nds_write_b128 v58, a[200:203] offset:33792\nQmaaa 212,56,12\nQmaaa 212,60,40\nQta 74,68,58624\nQmaaa 216,56,16\n"
"ds_write_b128 v58, a[204:207] offset:34816\nQmaaa 216,60,44\nQmaaa 220,56,20\nQta 76,69,256\nQmaaa 220,60,48\nds_write_b128 v58, a[208:211] offset:35840\nQmaaa 224,56,24\nQmaaa 224,60,52\n"
"Qta 78,69,1280\ns_waitcnt lgkmcnt(7)\nQmaaa 228,64,80\nds_write_b128 v58, a[212:215] offset:36864\nQmaaa 228,68,108\nQmaaa 232,64,84\ns_waitcnt vmcnt(0)\nbuffer_store_dword v67, v77, s[24:27], 0 offen sc1\n"
"Qmaaa 232,68,112\nds_write_b128 v58, a[216:219] offset:37888\nQmaaa 236,64,88\nQta 56,69,8448\nQmaav 236,68,168\nQmaaa 240,64,92\nds_write_b128 v58, a[220:223] offset:38912\nQmaav 240,68,200\n"
"Qta 58,69,9472\nQmaaa 244,64,96\nQmaav 244,68,232\nds_write_b128 v58, a[224:227] offset:39936\nQmaaa 248,64,100\nQta 60,69,16640\nQmaav 248,68,212\nQmaaa 252,64,104\n"
"Qmaav 252,68,244\nQta 62,69,17664\ns_waitcnt lgkmcnt(8)\nQmaav 228,72,80\nv_add_u32 v75, 1, v75\nQmaav 228,76,88\nds_write_b32 v73, v75\nQmaav 232,72,112\n"
"Qmaav 232,76,120\nQmaav 236,72,144\nQta 64,68,41472\nQmaav 236,76,152\nQmaav 240,72,176\nQmaav 240,76,184\nQta 66,68,42496\nQmaav 244,72,208\n"
"Qmaav 244,76,216\nds_read_b32 v66, v74\nQmaav 248,72,240\nQta 68,68,49664\nQmaav 248,76,248\nQmaav 252,72,84\nbuffer_load_dword v78, v76, s[24:27], 0 offen sc1\nQmaav 252,76,116\n"
"s_waitcnt lgkmcnt(1)\nv_cmp_gt_u32 vcc, v75, v66\ns_waitcnt lgkmcnt(5)\nQmaaa 228,56,0\ns_cbranch_vccnz .Lso2130r1\n.Lro2130r1:\nQmaaa 228,60,28\nQta 70,68,50688\n"
"Qmaaa 232,56,4\nQmaaa 232,60,32\nQdv 180,59,35840\nQmaaa 236,56,8\nQta 72,68,57856\nQmaaa 236,60,36\ns_nop 0\nds_write_b128 v58, a[228:231] offset:0\n"
"Qmaaa 240,56,12\nQdv 104,59,36864\nQmaaa 240,60,40\nQta 74,68,58880\nQmaaa 244,56,16\nds_write_b128 v58, a[232:235] offset:1024\nQmaaa 244,60,44\nQdv 136,59,38912\n"
"Qmaaa 248,56,20\nQta 76,69,512\nQmaaa 248,60,48\nds_write_b128 v58, a[236:239] offset:2048\nQmaaa 252,56,24\nQdv 148,59,33792\ns_waitcnt lgkmcnt(9)\nQa 196,196,180\n"
"Qmaaa 252,60,52\nQta 78,69,1536\nQa 197,197,181\ns_waitcnt lgkmcnt(10)\nQmvaa 132,64,80\nds_write_b128 v58, a[240:243] offset:3072\nQmvaa 132,68,108\nQa 198,198,182\n"
"Qa 199,199,183\n.Lpg25:\ns_cbranch_execz .Ltr8\nQmvaa 164,64,84\ns_waitcnt lgkmcnt(8)\nQa 228,228,104\nQa 229,229,105\nQmvaa 164,68,112\n"
"ds_write_b128 v58, a[244:247] offset:4096\nQmvaa 196,64,88\nQta 56,69,8704\nQa 230,230,106\nQa 231,231,107\nQmvav 196,68,168\nQdv 180,59,37888\ns_waitcnt lgkmcnt(8)\n"
"Qa 140,140,136\nQmvaa 228,64,92\nds_write_b128 v58, a[248:251] offset:5120\nQmvav 228,68,200\nQta 58,69,9728\nQa 141,141,137\nQmvaa 108,64,96\nQa 142,142,138\n"
"Qa 143,143,139\nQmvav 108,68,232\nQdv 104,59,39936\ns_waitcnt lgkmcnt(8)\nQa 132,132,148\nQmvaa 140,64,100\nQta 60,69,16896\nQa 133,133,149\n"
"Qmvav 140,68,212\nds_write_b128 v58, a[252:255] offset:6144\nQmvaa 172,64,104\nQa 134,134,150\nQa 135,135,151\nQmvav 172,68,244\nQta 62,69,17920\ns_waitcnt lgkmcnt(6)\n"
"Qa 108,108,180\ns_waitcnt lgkmcnt(6)\nQmvav 132,72,80\nQdv 136,59,34816\nv_add_u32 v75, 1, v75\nQmvav 132,76,88\nds_write_b32 v73, v75\nQa 109,109,181\n"
"Qmvav 164,72,112\nQa 110,110,182\nQa 111,111,183\nQmvav 164,76,120\ns_waitcnt lgkmcnt(5)\nQa 172,172,104\nQa 173,173,105\nQmvav 196,72,144\n"
"Qta 64,68,41728\nQa 174,174,106\nQmvav 196,76,152\nQa 175,175,107\nQmvav 228,72,176\nQmvav 228,76,184\nQta 66,68,42752\ns_waitcnt lgkmcnt(3)\n"
"Qa 164,164,136\nQmvav 108,72,208\nQa 165,165,137\nQa 166,166,138\nQmvav 108,76,216\nds_read_b32 v66, v74\nQa 167,167,139\nQmvav 140,72,240\n"
"Qta 68,68,49920\nQmvav 140,76,248\nQmvav 172,72,84\nQmvav 172,76,116\ns_waitcnt lgkmcnt(1)\nv_cmp_gt_u32 vcc, v75, v66\ns_waitcnt lgkmcnt(6)\nQmvaa 132,56,0\n"
"s_cbranch_vccnz .Lso2172r1\n.Lro2172r1:\nQmvaa 132,60,28\ns_waitcnt vmcnt(0)\nv_sub_u32 v66, v78, v65\nv_cmp_gt_u32 vcc, 1, v66\ns_and_b64 vcc, vcc, s[28:29]\ns_cbranch_scc1 .Lx1_w\n"
".Lx1_r:\nQmvaa 164,56,4\ns_add_u32 s69, s98, 0\nbuffer_load_dwordx4 a[180:183], v79, s[24:27], s69 offen offset:0 sc1\nQmvaa 164,60,32\nQta 70,68,50944\nQmvaa 196,56,8\nbuffer_load_dwordx4 a[184:187], v79, s[24:27], s69 offen offset:1024 sc1\n"
"Qmvaa 196,60,36\nQdv 148,59,2048\nQmvaa 228,56,12\nbuffer_load_dwordx4 a[188:191], v79, s[24:27], s69 offen offset:2048 sc1\nQmvaa 228,60,40\nQta 72,68,58112\nQmvaa 108,56,16\nbuffer_load_dwordx4 a[192:195], v79, s[24:27], s69 offen offset:3072 sc1\n"
"Qmvaa 108,60,44\nQdv 180,59,4096\nQmvaa 140,56,20\ns_add_u32 s69, s98, 4096\nbuffer_load_dwordx4 a[196:199], v79, s[24:27], s69 offen offset:0 sc1\nQmvaa 140,60,48\nQta 74,68,59136\ns_waitcnt lgkmcnt(3)\n"
"Qa 96,96,148\nQmvaa 172,56,24\nbuffer_load_dwordx4 a[116:119], v79, s[24:27], s69 offen offset:1024 sc1\nQmvaa 172,60,52\nQdv 104,59,0\nQa 97,97,149\ns_waitcnt lgkmcnt(4)\nQmvaa 204,64,80\n"
"buffer_load_dwordx4 a[120:123], v79, s[24:27], s69 offen offset:2048 sc1\nQmvaa 204,68,108\nQta 76,69,768\nQa 98,98,150\nQmvaa 236,64,84\nQdv 136,59,1024\nQa 99,99,151\nQmvaa 236,68,112\n"
"s_waitcnt lgkmcnt(4)\nQa 160,160,180\nQa 161,161,181\nQmvaa 96,64,88\nQta 78,69,1792\nQa 162,162,182\nQmvav 96,68,168\nQdv 148,59,3072\n"
"Qa 163,163,183\nQmvaa 128,64,92\ns_waitcnt lgkmcnt(4)\nQa 204,204,104\nQa 205,205,105\nQmvav 128,68,200\nQta 56,69,8960\nQa 206,206,106\n"
"Qmvaa 160,64,96\nQdv 180,59,5120\nQa 207,207,107\nQmvav 160,68,232\nQmvaa 192,64,100\nQta 58,69,9984\ns_waitcnt lgkmcnt(5)\nQa 236,236,136\n"
"Qmvav 192,68,212\nQa 237,237,137\nQa 238,238,138\nQmvaa 224,64,104\nQa 239,239,139\nQmvav 224,68,244\nQta 60,69,17152\ns_waitcnt lgkmcnt(4)\n"
"Qa 128,128,148\ns_waitcnt lgkmcnt(4)\nQmvav 204,72,80\nQa 129,129,149\nQa 130,130,150\nQmvav 204,76,88\nQa 131,131,151\nQmvav 236,72,112\n"
"Qta 62,69,18176\ns_waitcnt lgkmcnt(3)\nQa 192,192,180\nQmvav 236,76,120\nQa 193,193,181\nQa 194,194,182\nQmvav 96,72,144\nQa 195,195,183\n"
"Qmvav 96,76,152\nQmvav 128,72,176\nQmvav 128,76,184\nQmvav 160,72,208\nQmvav 160,76,216\nQdv 104,59,6144\nQmvav 192,72,240\nQmvav 192,76,248\n"
"v_add_u32 v75, 1, v75\nQmvav 224,72,84\nds_write_b32 v73, v75\nQmvav 224,76,116\ns_waitcnt lgkmcnt(2)\nQmvaa 204,56,0\nQmvaa 204,60,28\nQmvaa 236,56,4\n"
"Qmvaa 236,60,32\ns_waitcnt lgkmcnt(1)\ns_nop 1\nQa 224,224,104\nQa 225,225,105\nQmvaa 96,56,8\nQa 226,226,106\nQa 227,227,107\n"
"Qmvaa 96,60,36\nQmvaa 128,56,12\nds_read_b32 v66, v74\nQmvaa 128,60,40\nQmvaa 160,56,16\nQmvaa 160,60,44\nQmvaa 192,56,20\ns_waitcnt lgkmcnt(0)\n"
"v_cmp_gt_u32 vcc, v75, v66\nQmvaa 192,60,48\ns_cbranch_vccnz .Lso2225r1\n.Lro2225r1:\nQmvaa 224,56,24\nQmvaa 224,60,52\ns_waitcnt vmcnt(6)\nQmvaa 132,174,180\n"
"Qmvaa 164,172,180\ns_waitcnt vmcnt(5)\nQmvaa 196,174,184\nQmvaa 228,172,184\ns_nop 3\nQk 132,132,133\nQk 133,134,135\nds_write_b64 v56, v[132:133] offset:0\n"
"s_waitcnt vmcnt(4)\nQmvaa 108,174,188\nQk 164,164,165\nQk 165,166,167\nds_write_b64 v56, v[164:165] offset:3328\nQmvaa 140,172,188\nQk 196,196,197\nQk 197,198,199\n"
"ds_write_b64 v56, v[196:197] offset:6656\ns_waitcnt vmcnt(3)\nQmvaa 172,174,192\nQk 228,228,229\nQk 229,230,231\nds_write_b64 v56, v[228:229] offset:9984\nQmvaa 204,172,192\nQk 108,108,109\n"
"Qk 109,110,111\nds_write_b64 v56, v[108:109] offset:13312\ns_waitcnt vmcnt(2)\nQmvaa 236,174,196\nQk 140,140,141\nQk 141,142,143\nds_write_b64 v56, v[140:141] offset:16640\nQmvaa 96,172,196\n"
"Qk 172,172,173\nQk 173,174,175\nds_write_b64 v56, v[172:173] offset:19968\ns_waitcnt vmcnt(1)\nQmvaa 128,174,116\nQk 204,204,205\nQk 205,206,207\nds_write_b64 v56, v[204:205] offset:32\n"
"Qmvaa 160,172,116\nQk 236,236,237\nQk 237,238,239\nds_write_b64 v56, v[236:237] offset:3360\ns_waitcnt vmcnt(0)\nQmvaa 192,174,120\nQk 96,96,97\nQk 97,98,99\n"
"ds_write_b64 v56, v[96:97] offset:6688\nQmvaa 224,172,120\nQk 128,128,129\nQk 129,130,131\nds_write_b64 v56, v[128:129] offset:10016\nQk 160,160,161\nQk 161,162,163\nds_write_b64 v56, v[160:161] offset:13344\n"
"Qk 192,192,193\nQk 193,194,195\nds_write_b64 v56, v[192:193] offset:16672\nQk 224,224,225\nQk 225,226,227\nds_write_b64 v56, v[224:225] offset:20000\ns_waitcnt lgkmcnt(0)\ns_barrier\n"
"Qda 0,57,0\nQda 4,57,3328\nQda 8,57,6656\nQda 12,57,9984\nQda 16,57,13312\nQda 20,57,16640\nQda 24,57,19968\ns_mov_b32 s68, 0\n"
"s_waitcnt lgkmcnt(6)\nbuffer_store_dwordx4 a[0:3], v64, s[40:43], s68 offen offset:0 sc1\ns_mov_b32 s68, 40960\ns_waitcnt lgkmcnt(5)\nbuffer_store_dwordx4 a[4:7], v64, s[40:43], s68 offen offset:0 sc1\ns_mov_b32 s68, 81920\ns_waitcnt lgkmcnt(4)\nbuffer_store_dwordx4 a[8:11], v64, s[40:43], s68 offen offset:0 sc1\n"
"s_mov_b32 s68, 122880\ns_waitcnt lgkmcnt(3)\nbuffer_store_dwordx4 a[12:15], v64, s[40:43], s68 offen offset:0 sc1\ns_mov_b32 s68, 163840\ns_waitcnt lgkmcnt(2)\nbuffer_store_dwordx4 a[16:19], v64, s[40:43], s68 offen offset:0 sc1\ns_mov_b32 s68, 204800\ns_waitcnt lgkmcnt(1)\n"
"buffer_store_dwordx4 a[20:23], v64, s[40:43], s68 offen offset:0 sc1\ns_mov_b32 s68, 245760\ns_waitcnt lgkmcnt(0)\nbuffer_store_dwordx4 a[24:27], v64, s[40:43], s68 offen offset:0 sc1\n.Lfin:\n.Lpg26:\ns_cbranch_execz .Ltr8\ns_waitcnt vmcnt(0) lgkmcnt(0)\n"
"s_branch .Lexit\n.Lend:\n.Lfb:\ns_waitcnt vmcnt(0) lgkmcnt(0)\nv_lshrrev_b32 v52, 4, v60\nv_and_b32 v53, 15, v52\nv_lshrrev_b32 v52, 4, v52\nv_lshlrev_b32 v66, 8, v53\n"
"v_lshl_add_u32 v66, v52, 4, v66\nv_add_u32 v66, 1024, v66\nv_lshlrev_b32 v67, 10, v52\nv_lshl_add_u32 v67, v53, 1, v67\nv_add_u32 v67, 1024, v67\nv_mov_b32 v52, 0x3f803f80\nv_accvgpr_write_b32 a204, v52\nv_accvgpr_write_b32 a205, v52\n"
"v_accvgpr_write_b32 a206, v52\nv_accvgpr_write_b32 a207, v52\nv_mov_b32 v24, 0xff800000\nv_mov_b32 v28, 0xff800000\nv_mov_b32 v32, 0xff800000\nv_mov_b32 v36, 0xff800000\nv_mov_b32 v40, 0xff800000\nv_mov_b32 v44, 0xff800000\n"
"v_mov_b32 v48, 0xff800000\ns_lshl_b32 s89, s67, 21\ns_lshl_b32 s68, s93, 18\ns_add_u32 s89, s89, s68\ns_mov_b32 s66, 0\n.Lfb1:\ns_add_u32 s68, s89, 4096\nbuffer_load_dwordx4 a[112:115], v66, s[40:43], s89 offen offset:0\n"
"buffer_load_dwordx4 a[116:119], v66, s[40:43], s89 offen offset:64\nbuffer_load_dwordx4 a[120:123], v66, s[40:43], s89 offen offset:128\nbuffer_load_dwordx4 a[124:127], v66, s[40:43], s89 offen offset:192\nbuffer_load_dwordx4 a[128:131], v66, s[40:43], s68 offen offset:0\nbuffer_load_dwordx4 a[132:135], v66, s[40:43], s68 offen offset:64\nbuffer_load_dwordx4 a[136:139], v66, s[40:43], s68 offen offset:128\nbuffer_load_dwordx4 a[140:143], v66, s[40:43], s68 offen offset:192\ns_waitcnt vmcnt(0)\n"
"Qzvaa 0,112,0\nQmvaa 0,116,4\nQmvaa 0,120,8\nQmvaa 0,124,12\nQzvaa 4,128,0\nQmvaa 4,132,4\nQmvaa 4,136,8\nQmvaa 4,140,12\n"
"s_nop 7\nv_max3_f32 v24, v24, v0, v1\nv_max3_f32 v24, v24, v2, v3\nv_max3_f32 v24, v24, v4, v5\nv_max3_f32 v24, v24, v6, v7\nQzvaa 0,112,16\nQmvaa 0,116,20\nQmvaa 0,120,24\n"
"Qmvaa 0,124,28\nQzvaa 4,128,16\nQmvaa 4,132,20\nQmvaa 4,136,24\nQmvaa 4,140,28\ns_nop 7\nv_max3_f32 v28, v28, v0, v1\nv_max3_f32 v28, v28, v2, v3\n"
"v_max3_f32 v28, v28, v4, v5\nv_max3_f32 v28, v28, v6, v7\nQzvaa 0,112,32\nQmvaa 0,116,36\nQmvaa 0,120,40\nQmvaa 0,124,44\nQzvaa 4,128,32\nQmvaa 4,132,36\n"
"Qmvaa 4,136,40\nQmvaa 4,140,44\ns_nop 7\nv_max3_f32 v32, v32, v0, v1\nv_max3_f32 v32, v32, v2, v3\nv_max3_f32 v32, v32, v4, v5\nv_max3_f32 v32, v32, v6, v7\nQzvaa 0,112,48\n"
"Qmvaa 0,116,52\nQmvaa 0,120,56\nQmvaa 0,124,60\nQzvaa 4,128,48\nQmvaa 4,132,52\nQmvaa 4,136,56\nQmvaa 4,140,60\ns_nop 7\n"
"v_max3_f32 v36, v36, v0, v1\nv_max3_f32 v36, v36, v2, v3\nv_max3_f32 v36, v36, v4, v5\nv_max3_f32 v36, v36, v6, v7\nQzvaa 0,112,64\nQmvaa 0,116,68\nQmvaa 0,120,72\nQmvaa 0,124,76\n"
"Qzvaa 4,128,64\nQmvaa 4,132,68\nQmvaa 4,136,72\nQmvaa 4,140,76\ns_nop 7\nv_max3_f32 v40, v40, v0, v1\nv_max3_f32 v40, v40, v2, v3\nv_max3_f32 v40, v40, v4, v5\n"
"v_max3_f32 v40, v40, v6, v7\nQzvaa 0,112,80\nQmvaa 0,116,84\nQmvaa 0,120,88\nQmvaa 0,124,92\nQzvaa 4,128,80\nQmvaa 4,132,84\nQmvaa 4,136,88\n"
"Qmvaa 4,140,92\ns_nop 7\nv_max3_f32 v44, v44, v0, v1\nv_max3_f32 v44, v44, v2, v3\nv_max3_f32 v44, v44, v4, v5\nv_max3_f32 v44, v44, v6, v7\nQzvaa 0,112,96\nQmvaa 0,116,100\n"
"Qmvaa 0,120,104\nQmvaa 0,124,108\nQzvaa 4,128,96\nQmvaa 4,132,100\nQmvaa 4,136,104\nQmvaa 4,140,108\ns_nop 7\nv_max3_f32 v48, v48, v0, v1\n"
"v_max3_f32 v48, v48, v2, v3\nv_max3_f32 v48, v48, v4, v5\nv_max3_f32 v48, v48, v6, v7\ns_add_u32 s89, s89, 8192\ns_add_u32 s66, s66, 1\ns_cmp_lt_u32 s66, 32\ns_cbranch_scc1 .Lfb1\nv_mov_b32 v25, v24\n"
"s_nop 1\nv_permlane32_swap_b32 v24, v25\nv_max_f32 v24, v24, v25\nv_mov_b32 v25, v24\ns_nop 1\nv_permlane16_swap_b32 v24, v25\nv_max_f32 v24, v24, v25\nv_sub_f32 v24, 0, v24\n"
"v_mov_b32 v25, v24\nv_mov_b32 v26, v24\nv_mov_b32 v27, v24\nv_mov_b32 v29, v28\ns_nop 1\nv_permlane32_swap_b32 v28, v29\nv_max_f32 v28, v28, v29\nv_mov_b32 v29, v28\n"
"s_nop 1\nv_permlane16_swap_b32 v28, v29\nv_max_f32 v28, v28, v29\nv_sub_f32 v28, 0, v28\nv_mov_b32 v29, v28\nv_mov_b32 v30, v28\nv_mov_b32 v31, v28\nv_mov_b32 v33, v32\n"
"s_nop 1\nv_permlane32_swap_b32 v32, v33\nv_max_f32 v32, v32, v33\nv_mov_b32 v33, v32\ns_nop 1\nv_permlane16_swap_b32 v32, v33\nv_max_f32 v32, v32, v33\nv_sub_f32 v32, 0, v32\n"
"v_mov_b32 v33, v32\nv_mov_b32 v34, v32\nv_mov_b32 v35, v32\nv_mov_b32 v37, v36\ns_nop 1\nv_permlane32_swap_b32 v36, v37\nv_max_f32 v36, v36, v37\nv_mov_b32 v37, v36\n"
"s_nop 1\nv_permlane16_swap_b32 v36, v37\nv_max_f32 v36, v36, v37\nv_sub_f32 v36, 0, v36\nv_mov_b32 v37, v36\nv_mov_b32 v38, v36\nv_mov_b32 v39, v36\nv_mov_b32 v41, v40\n"
"s_nop 1\nv_permlane32_swap_b32 v40, v41\nv_max_f32 v40, v40, v41\nv_mov_b32 v41, v40\ns_nop 1\nv_permlane16_swap_b32 v40, v41\nv_max_f32 v40, v40, v41\nv_sub_f32 v40, 0, v40\n"
"v_mov_b32 v41, v40\nv_mov_b32 v42, v40\nv_mov_b32 v43, v40\nv_mov_b32 v45, v44\ns_nop 1\nv_permlane32_swap_b32 v44, v45\nv_max_f32 v44, v44, v45\nv_mov_b32 v45, v44\n"
"s_nop 1\nv_permlane16_swap_b32 v44, v45\nv_max_f32 v44, v44, v45\nv_sub_f32 v44, 0, v44\nv_mov_b32 v45, v44\nv_mov_b32 v46, v44\nv_mov_b32 v47, v44\nv_mov_b32 v49, v48\n"
"s_nop 1\nv_permlane32_swap_b32 v48, v49\nv_max_f32 v48, v48, v49\nv_mov_b32 v49, v48\ns_nop 1\nv_permlane16_swap_b32 v48, v49\nv_max_f32 v48, v48, v49\nv_sub_f32 v48, 0, v48\n"
"v_mov_b32 v49, v48\nv_mov_b32 v50, v48\nv_mov_b32 v51, v48\nv_mov_b32 v52, 0\nv_mov_b32 v80, 0\nv_mov_b32 v81, 0\nv_mov_b32 v82, 0\nv_mov_b32 v83, 0\n"
"v_mov_b32 v84, 0\nv_mov_b32 v85, 0\nv_mov_b32 v86, 0\nv_mov_b32 v87, 0\nv_mov_b32 v88, 0\nv_mov_b32 v89, 0\nv_mov_b32 v90, 0\nv_mov_b32 v91, 0\n"
"v_mov_b32 v92, 0\nv_mov_b32 v93, 0\nv_mov_b32 v94, 0\nv_mov_b32 v95, 0\nv_mov_b32 v96, 0\nv_mov_b32 v97, 0\nv_mov_b32 v98, 0\nv_mov_b32 v99, 0\n"
"v_mov_b32 v100, 0\nv_mov_b32 v101, 0\nv_mov_b32 v102, 0\nv_mov_b32 v103, 0\nv_mov_b32 v104, 0\nv_mov_b32 v105, 0\nv_mov_b32 v106, 0\nv_mov_b32 v107, 0\n"
"v_mov_b32 v108, 0\nv_mov_b32 v109, 0\nv_mov_b32 v110, 0\nv_mov_b32 v111, 0\nv_accvgpr_write_b32 a176, v52\nv_accvgpr_write_b32 a177, v52\nv_accvgpr_write_b32 a178, v52\nv_accvgpr_write_b32 a179, v52\n"
"v_mov_b32 v112, 0\nv_mov_b32 v113, 0\nv_mov_b32 v114, 0\nv_mov_b32 v115, 0\nv_mov_b32 v116, 0\nv_mov_b32 v117, 0\nv_mov_b32 v118, 0\nv_mov_b32 v119, 0\n"
"v_mov_b32 v120, 0\nv_mov_b32 v121, 0\nv_mov_b32 v122, 0\nv_mov_b32 v123, 0\nv_mov_b32 v124, 0\nv_mov_b32 v125, 0\nv_mov_b32 v126, 0\nv_mov_b32 v127, 0\n"
"v_mov_b32 v128, 0\nv_mov_b32 v129, 0\nv_mov_b32 v130, 0\nv_mov_b32 v131, 0\nv_mov_b32 v132, 0\nv_mov_b32 v133, 0\nv_mov_b32 v134, 0\nv_mov_b32 v135, 0\n"
"v_mov_b32 v136, 0\nv_mov_b32 v137, 0\nv_mov_b32 v138, 0\nv_mov_b32 v139, 0\nv_mov_b32 v140, 0\nv_mov_b32 v141, 0\nv_mov_b32 v142, 0\nv_mov_b32 v143, 0\n"
"v_accvgpr_write_b32 a180, v52\nv_accvgpr_write_b32 a181, v52\nv_accvgpr_write_b32 a182, v52\nv_accvgpr_write_b32 a183, v52\nv_mov_b32 v144, 0\nv_mov_b32 v145, 0\nv_mov_b32 v146, 0\nv_mov_b32 v147, 0\n"
"v_mov_b32 v148, 0\nv_mov_b32 v149, 0\nv_mov_b32 v150, 0\nv_mov_b32 v151, 0\nv_mov_b32 v152, 0\nv_mov_b32 v153, 0\nv_mov_b32 v154, 0\nv_mov_b32 v155, 0\n"
"v_mov_b32 v156, 0\nv_mov_b32 v157, 0\nv_mov_b32 v158, 0\nv_mov_b32 v159, 0\nv_mov_b32 v160, 0\nv_mov_b32 v161, 0\nv_mov_b32 v162, 0\nv_mov_b32 v163, 0\n"
"v_mov_b32 v164, 0\nv_mov_b32 v165, 0\nv_mov_b32 v166, 0\nv_mov_b32 v167, 0\nv_mov_b32 v168, 0\nv_mov_b32 v169, 0\nv_mov_b32 v170, 0\nv_mov_b32 v171, 0\n"
"v_mov_b32 v172, 0\nv_mov_b32 v173, 0\nv_mov_b32 v174, 0\nv_mov_b32 v175, 0\nv_accvgpr_write_b32 a184, v52\nv_accvgpr_write_b32 a185, v52\nv_accvgpr_write_b32 a186, v52\nv_accvgpr_write_b32 a187, v52\n"
"v_mov_b32 v176, 0\nv_mov_b32 v177, 0\nv_mov_b32 v178, 0\nv_mov_b32 v179, 0\nv_mov_b32 v180, 0\nv_mov_b32 v181, 0\nv_mov_b32 v182, 0\nv_mov_b32 v183, 0\n"
"v_mov_b32 v184, 0\nv_mov_b32 v185, 0\nv_mov_b32 v186, 0\nv_mov_b32 v187, 0\nv_mov_b32 v188, 0\nv_mov_b32 v189, 0\nv_mov_b32 v190, 0\nv_mov_b32 v191, 0\n"
"v_mov_b32 v192, 0\nv_mov_b32 v193, 0\nv_mov_b32 v194, 0\nv_mov_b32 v195, 0\nv_mov_b32 v196, 0\nv_mov_b32 v197, 0\nv_mov_b32 v198, 0\nv_mov_b32 v199, 0\n"
"v_mov_b32 v200, 0\nv_mov_b32 v201, 0\nv_mov_b32 v202, 0\nv_mov_b32 v203, 0\nv_mov_b32 v204, 0\nv_mov_b32 v205, 0\nv_mov_b32 v206, 0\nv_mov_b32 v207, 0\n"
"v_accvgpr_write_b32 a188, v52\nv_accvgpr_write_b32 a189, v52\nv_accvgpr_write_b32 a190, v52\nv_accvgpr_write_b32 a191, v52\nv_mov_b32 v208, 0\nv_mov_b32 v209, 0\nv_mov_b32 v210, 0\nv_mov_b32 v211, 0\n"
"v_mov_b32 v212, 0\nv_mov_b32 v213, 0\nv_mov_b32 v214, 0\nv_mov_b32 v215, 0\nv_mov_b32 v216, 0\nv_mov_b32 v217, 0\nv_mov_b32 v218, 0\nv_mov_b32 v219, 0\n"
"v_mov_b32 v220, 0\nv_mov_b32 v221, 0\nv_mov_b32 v222, 0\nv_mov_b32 v223, 0\nv_mov_b32 v224, 0\nv_mov_b32 v225, 0\nv_mov_b32 v226, 0\nv_mov_b32 v227, 0\n"
"v_mov_b32 v228, 0\nv_mov_b32 v229, 0\nv_mov_b32 v230, 0\nv_mov_b32 v231, 0\nv_mov_b32 v232, 0\nv_mov_b32 v233, 0\nv_mov_b32 v234, 0\nv_mov_b32 v235, 0\n"
"v_mov_b32 v236, 0\nv_mov_b32 v237, 0\nv_mov_b32 v238, 0\nv_mov_b32 v239, 0\nv_accvgpr_write_b32 a192, v52\nv_accvgpr_write_b32 a193, v52\nv_accvgpr_write_b32 a194, v52\nv_accvgpr_write_b32 a195, v52\n"
"v_mov_b32 v240, 0\nv_mov_b32 v241, 0\nv_mov_b32 v242, 0\nv_mov_b32 v243, 0\nv_mov_b32 v244, 0\nv_mov_b32 v245, 0\nv_mov_b32 v246, 0\nv_mov_b32 v247, 0\n"
"v_mov_b32 v248, 0\nv_mov_b32 v249, 0\nv_mov_b32 v250, 0\nv_mov_b32 v251, 0\nv_mov_b32 v252, 0\nv_mov_b32 v253, 0\nv_mov_b32 v254, 0\nv_mov_b32 v255, 0\n"
"v_accvgpr_write_b32 a208, v52\nv_accvgpr_write_b32 a209, v52\nv_accvgpr_write_b32 a210, v52\nv_accvgpr_write_b32 a211, v52\nv_accvgpr_write_b32 a212, v52\nv_accvgpr_write_b32 a213, v52\nv_accvgpr_write_b32 a214, v52\nv_accvgpr_write_b32 a215, v52\n"
"v_accvgpr_write_b32 a216, v52\nv_accvgpr_write_b32 a217, v52\nv_accvgpr_write_b32 a218, v52\nv_accvgpr_write_b32 a219, v52\nv_accvgpr_write_b32 a220, v52\nv_accvgpr_write_b32 a221, v52\nv_accvgpr_write_b32 a222, v52\nv_accvgpr_write_b32 a223, v52\n"
"v_accvgpr_write_b32 a196, v52\nv_accvgpr_write_b32 a197, v52\nv_accvgpr_write_b32 a198, v52\nv_accvgpr_write_b32 a199, v52\nv_accvgpr_write_b32 a224, v52\nv_accvgpr_write_b32 a225, v52\nv_accvgpr_write_b32 a226, v52\nv_accvgpr_write_b32 a227, v52\n"
"v_accvgpr_write_b32 a228, v52\nv_accvgpr_write_b32 a229, v52\nv_accvgpr_write_b32 a230, v52\nv_accvgpr_write_b32 a231, v52\nv_accvgpr_write_b32 a232, v52\nv_accvgpr_write_b32 a233, v52\nv_accvgpr_write_b32 a234, v52\nv_accvgpr_write_b32 a235, v52\n"
"v_accvgpr_write_b32 a236, v52\nv_accvgpr_write_b32 a237, v52\nv_accvgpr_write_b32 a238, v52\nv_accvgpr_write_b32 a239, v52\nv_accvgpr_write_b32 a240, v52\nv_accvgpr_write_b32 a241, v52\nv_accvgpr_write_b32 a242, v52\nv_accvgpr_write_b32 a243, v52\n"
"v_accvgpr_write_b32 a244, v52\nv_accvgpr_write_b32 a245, v52\nv_accvgpr_write_b32 a246, v52\nv_accvgpr_write_b32 a247, v52\nv_accvgpr_write_b32 a248, v52\nv_accvgpr_write_b32 a249, v52\nv_accvgpr_write_b32 a250, v52\nv_accvgpr_write_b32 a251, v52\n"
"v_accvgpr_write_b32 a252, v52\nv_accvgpr_write_b32 a253, v52\nv_accvgpr_write_b32 a254, v52\nv_accvgpr_write_b32 a255, v52\nv_accvgpr_write_b32 a200, v52\nv_accvgpr_write_b32 a201, v52\nv_accvgpr_write_b32 a202, v52\nv_accvgpr_write_b32 a203, v52\n"
"s_lshl_b32 s89, s67, 21\ns_lshl_b32 s68, s93, 18\ns_add_u32 s89, s89, s68\ns_mov_b32 s66, 0\n.Lfb2:\ns_add_u32 s68, s89, 4096\nbuffer_load_dwordx4 a[112:115], v66, s[40:43], s89 offen offset:0\nbuffer_load_dwordx4 a[116:119], v66, s[40:43], s89 offen offset:64\n"
"buffer_load_dwordx4 a[120:123], v66, s[40:43], s89 offen offset:128\nbuffer_load_dwordx4 a[124:127], v66, s[40:43], s89 offen offset:192\nbuffer_load_dwordx4 a[128:131], v66, s[40:43], s68 offen offset:0\nbuffer_load_dwordx4 a[132:135], v66, s[40:43], s68 offen offset:64\nbuffer_load_dwordx4 a[136:139], v66, s[40:43], s68 offen offset:128\nbuffer_load_dwordx4 a[140:143], v66, s[40:43], s68 offen offset:192\nbuffer_load_ushort v8, v67, s[44:47], s89 offen offset:0\nbuffer_load_ushort v9, v67, s[44:47], s89 offen offset:256\n"
"buffer_load_ushort v10, v67, s[44:47], s89 offen offset:512\nbuffer_load_ushort v11, v67, s[44:47], s89 offen offset:768\nbuffer_load_ushort v12, v67, s[44:47], s68 offen offset:0\nbuffer_load_ushort v13, v67, s[44:47], s68 offen offset:256\nbuffer_load_ushort v14, v67, s[44:47], s68 offen offset:512\nbuffer_load_ushort v15, v67, s[44:47], s68 offen offset:768\ns_waitcnt vmcnt(0)\nv_lshl_or_b32 v8, v9, 16, v8\n"
"v_accvgpr_write_b32 a144, v8\nv_lshl_or_b32 v10, v11, 16, v10\nv_accvgpr_write_b32 a145, v10\nv_lshl_or_b32 v12, v13, 16, v12\nv_accvgpr_write_b32 a146, v12\nv_lshl_or_b32 v14, v15, 16, v14\nv_accvgpr_write_b32 a147, v14\nbuffer_load_ushort v8, v67, s[44:47], s89 offen offset:32\n"
"buffer_load_ushort v9, v67, s[44:47], s89 offen offset:288\nbuffer_load_ushort v10, v67, s[44:47], s89 offen offset:544\nbuffer_load_ushort v11, v67, s[44:47], s89 offen offset:800\nbuffer_load_ushort v12, v67, s[44:47], s68 offen offset:32\nbuffer_load_ushort v13, v67, s[44:47], s68 offen offset:288\nbuffer_load_ushort v14, v67, s[44:47], s68 offen offset:544\nbuffer_load_ushort v15, v67, s[44:47], s68 offen offset:800\ns_waitcnt vmcnt(0)\n"
"v_lshl_or_b32 v8, v9, 16, v8\nv_accvgpr_write_b32 a148, v8\nv_lshl_or_b32 v10, v11, 16, v10\nv_accvgpr_write_b32 a149, v10\nv_lshl_or_b32 v12, v13, 16, v12\nv_accvgpr_write_b32 a150, v12\nv_lshl_or_b32 v14, v15, 16, v14\nv_accvgpr_write_b32 a151, v14\n"
"buffer_load_ushort v8, v67, s[44:47], s89 offen offset:64\nbuffer_load_ushort v9, v67, s[44:47], s89 offen offset:320\nbuffer_load_ushort v10, v67, s[44:47], s89 offen offset:576\nbuffer_load_ushort v11, v67, s[44:47], s89 offen offset:832\nbuffer_load_ushort v12, v67, s[44:47], s68 offen offset:64\nbuffer_load_ushort v13, v67, s[44:47], s68 offen offset:320\nbuffer_load_ushort v14, v67, s[44:47], s68 offen offset:576\nbuffer_load_ushort v15, v67, s[44:47], s68 offen offset:832\n"
"s_waitcnt vmcnt(0)\nv_lshl_or_b32 v8, v9, 16, v8\nv_accvgpr_write_b32 a152, v8\nv_lshl_or_b32 v10, v11, 16, v10\nv_accvgpr_write_b32 a153, v10\nv_lshl_or_b32 v12, v13, 16, v12\nv_accvgpr_write_b32 a154, v12\nv_lshl_or_b32 v14, v15, 16, v14\n"
"v_accvgpr_write_b32 a155, v14\nbuffer_load_ushort v8, v67, s[44:47], s89 offen offset:96\nbuffer_load_ushort v9, v67, s[44:47], s89 offen offset:352\nbuffer_load_ushort v10, v67, s[44:47], s89 offen offset:608\nbuffer_load_ushort v11, v67, s[44:47], s89 offen offset:864\nbuffer_load_ushort v12, v67, s[44:47], s68 offen offset:96\nbuffer_load_ushort v13, v67, s[44:47], s68 offen offset:352\nbuffer_load_ushort v14, v67, s[44:47], s68 offen offset:608\n"
"buffer_load_ushort v15, v67, s[44:47], s68 offen offset:864\ns_waitcnt vmcnt(0)\nv_lshl_or_b32 v8, v9, 16, v8\nv_accvgpr_write_b32 a156, v8\nv_lshl_or_b32 v10, v11, 16, v10\nv_accvgpr_write_b32 a157, v10\nv_lshl_or_b32 v12, v13, 16, v12\nv_accvgpr_write_b32 a158, v12\n"
"v_lshl_or_b32 v14, v15, 16, v14\nv_accvgpr_write_b32 a159, v14\nbuffer_load_ushort v8, v67, s[44:47], s89 offen offset:128\nbuffer_load_ushort v9, v67, s[44:47], s89 offen offset:384\nbuffer_load_ushort v10, v67, s[44:47], s89 offen offset:640\nbuffer_load_ushort v11, v67, s[44:47], s89 offen offset:896\nbuffer_load_ushort v12, v67, s[44:47], s68 offen offset:128\nbuffer_load_ushort v13, v67, s[44:47], s68 offen offset:384\n"
"buffer_load_ushort v14, v67, s[44:47], s68 offen offset:640\nbuffer_load_ushort v15, v67, s[44:47], s68 offen offset:896\ns_waitcnt vmcnt(0)\nv_lshl_or_b32 v8, v9, 16, v8\nv_accvgpr_write_b32 a160, v8\nv_lshl_or_b32 v10, v11, 16, v10\nv_accvgpr_write_b32 a161, v10\nv_lshl_or_b32 v12, v13, 16, v12\n"
"v_accvgpr_write_b32 a162, v12\nv_lshl_or_b32 v14, v15, 16, v14\nv_accvgpr_write_b32 a163, v14\nbuffer_load_ushort v8, v67, s[44:47], s89 offen offset:160\nbuffer_load_ushort v9, v67, s[44:47], s89 offen offset:416\nbuffer_load_ushort v10, v67, s[44:47], s89 offen offset:672\nbuffer_load_ushort v11, v67, s[44:47], s89 offen offset:928\nbuffer_load_ushort v12, v67, s[44:47], s68 offen offset:160\n"
"buffer_load_ushort v13, v67, s[44:47], s68 offen offset:416\nbuffer_load_ushort v14, v67, s[44:47], s68 offen offset:672\nbuffer_load_ushort v15, v67, s[44:47], s68 offen offset:928\ns_waitcnt vmcnt(0)\nv_lshl_or_b32 v8, v9, 16, v8\nv_accvgpr_write_b32 a164, v8\nv_lshl_or_b32 v10, v11, 16, v10\nv_accvgpr_write_b32 a165, v10\n"
"v_lshl_or_b32 v12, v13, 16, v12\nv_accvgpr_write_b32 a166, v12\nv_lshl_or_b32 v14, v15, 16, v14\nv_accvgpr_write_b32 a167, v14\nbuffer_load_ushort v8, v67, s[44:47], s89 offen offset:192\nbuffer_load_ushort v9, v67, s[44:47], s89 offen offset:448\nbuffer_load_ushort v10, v67, s[44:47], s89 offen offset:704\nbuffer_load_ushort v11, v67, s[44:47], s89 offen offset:960\n"
"buffer_load_ushort v12, v67, s[44:47], s68 offen offset:192\nbuffer_load_ushort v13, v67, s[44:47], s68 offen offset:448\nbuffer_load_ushort v14, v67, s[44:47], s68 offen offset:704\nbuffer_load_ushort v15, v67, s[44:47], s68 offen offset:960\ns_waitcnt vmcnt(0)\nv_lshl_or_b32 v8, v9, 16, v8\nv_accvgpr_write_b32 a168, v8\nv_lshl_or_b32 v10, v11, 16, v10\n"
"v_accvgpr_write_b32 a169, v10\nv_lshl_or_b32 v12, v13, 16, v12\nv_accvgpr_write_b32 a170, v12\nv_lshl_or_b32 v14, v15, 16, v14\nv_accvgpr_write_b32 a171, v14\nbuffer_load_ushort v8, v67, s[44:47], s89 offen offset:224\nbuffer_load_ushort v9, v67, s[44:47], s89 offen offset:480\nbuffer_load_ushort v10, v67, s[44:47], s89 offen offset:736\n"
"buffer_load_ushort v11, v67, s[44:47], s89 offen offset:992\nbuffer_load_ushort v12, v67, s[44:47], s68 offen offset:224\nbuffer_load_ushort v13, v67, s[44:47], s68 offen offset:480\nbuffer_load_ushort v14, v67, s[44:47], s68 offen offset:736\nbuffer_load_ushort v15, v67, s[44:47], s68 offen offset:992\ns_waitcnt vmcnt(0)\nv_lshl_or_b32 v8, v9, 16, v8\nv_accvgpr_write_b32 a172, v8\n"
"v_lshl_or_b32 v10, v11, 16, v10\nv_accvgpr_write_b32 a173, v10\nv_lshl_or_b32 v12, v13, 16, v12\nv_accvgpr_write_b32 a174, v12\nv_lshl_or_b32 v14, v15, 16, v14\nv_accvgpr_write_b32 a175, v14\nQcvaa 0,112,0,24\nQmvaa 0,116,4\n"
"Qmvaa 0,120,8\nQmvaa 0,124,12\nQcvaa 4,128,0,24\nQmvaa 4,132,4\nQmvaa 4,136,8\nQmvaa 4,140,12\ns_nop 7\nQe 0\n"
"Qe 1\nQe 2\nQe 3\nQe 4\nQe 5\nQe 6\nQe 7\ns_nop 0\n"
"Qk 16,0,1\nQk 17,2,3\nQk 18,4,5\nQk 19,6,7\ns_nop 1\nQmvva 80,16,144\nQmvva 84,16,148\nQmvva 88,16,152\n"
"Qmvva 92,16,156\nQmvva 96,16,160\nQmvva 100,16,164\nQmvva 104,16,168\nQmvva 108,16,172\nQmaav 176,204,16\nQcvaa 0,112,16,28\nQmvaa 0,116,20\n"
"Qmvaa 0,120,24\nQmvaa 0,124,28\nQcvaa 4,128,16,28\nQmvaa 4,132,20\nQmvaa 4,136,24\nQmvaa 4,140,28\ns_nop 7\nQe 0\n"
"Qe 1\nQe 2\nQe 3\nQe 4\nQe 5\nQe 6\nQe 7\ns_nop 0\n"
"Qk 16,0,1\nQk 17,2,3\nQk 18,4,5\nQk 19,6,7\ns_nop 1\nQmvva 112,16,144\nQmvva 116,16,148\nQmvva 120,16,152\n"
"Qmvva 124,16,156\nQmvva 128,16,160\nQmvva 132,16,164\nQmvva 136,16,168\nQmvva 140,16,172\nQmaav 180,204,16\nQcvaa 0,112,32,32\nQmvaa 0,116,36\n"
"Qmvaa 0,120,40\nQmvaa 0,124,44\nQcvaa 4,128,32,32\nQmvaa 4,132,36\nQmvaa 4,136,40\nQmvaa 4,140,44\ns_nop 7\nQe 0\n"
"Qe 1\nQe 2\nQe 3\nQe 4\nQe 5\nQe 6\nQe 7\ns_nop 0\n"
"Qk 16,0,1\nQk 17,2,3\nQk 18,4,5\nQk 19,6,7\ns_nop 1\nQmvva 144,16,144\nQmvva 148,16,148\nQmvva 152,16,152\n"
"Qmvva 156,16,156\nQmvva 160,16,160\nQmvva 164,16,164\nQmvva 168,16,168\nQmvva 172,16,172\nQmaav 184,204,16\nQcvaa 0,112,48,36\nQmvaa 0,116,52\n"
"Qmvaa 0,120,56\nQmvaa 0,124,60\nQcvaa 4,128,48,36\nQmvaa 4,132,52\nQmvaa 4,136,56\nQmvaa 4,140,60\ns_nop 7\nQe 0\n"
"Qe 1\nQe 2\nQe 3\nQe 4\nQe 5\nQe 6\nQe 7\ns_nop 0\n"
"Qk 16,0,1\nQk 17,2,3\nQk 18,4,5\nQk 19,6,7\ns_nop 1\nQmvva 176,16,144\nQmvva 180,16,148\nQmvva 184,16,152\n"
"Qmvva 188,16,156\nQmvva 192,16,160\nQmvva 196,16,164\nQmvva 200,16,168\nQmvva 204,16,172\nQmaav 188,204,16\nQcvaa 0,112,64,40\nQmvaa 0,116,68\n"
"Qmvaa 0,120,72\nQmvaa 0,124,76\nQcvaa 4,128,64,40\nQmvaa 4,132,68\nQmvaa 4,136,72\nQmvaa 4,140,76\ns_nop 7\nQe 0\n"
"Qe 1\nQe 2\nQe 3\nQe 4\nQe 5\nQe 6\nQe 7\ns_nop 0\n"
"Qk 16,0,1\nQk 17,2,3\nQk 18,4,5\nQk 19,6,7\ns_nop 1\nQmvva 208,16,144\nQmvva 212,16,148\nQmvva 216,16,152\n"
"Qmvva 220,16,156\nQmvva 224,16,160\nQmvva 228,16,164\nQmvva 232,16,168\nQmvva 236,16,172\nQmaav 192,204,16\nQcvaa 0,112,80,44\nQmvaa 0,116,84\n"
"Qmvaa 0,120,88\nQmvaa 0,124,92\nQcvaa 4,128,80,44\nQmvaa 4,132,84\nQmvaa 4,136,88\nQmvaa 4,140,92\ns_nop 7\nQe 0\n"
"Qe 1\nQe 2\nQe 3\nQe 4\nQe 5\nQe 6\nQe 7\ns_nop 0\n"
"Qk 16,0,1\nQk 17,2,3\nQk 18,4,5\nQk 19,6,7\ns_nop 1\nQmvva 240,16,144\nQmvva 244,16,148\nQmvva 248,16,152\n"
"Qmvva 252,16,156\nQmava 208,16,160\nQmava 212,16,164\nQmava 216,16,168\nQmava 220,16,172\nQmaav 196,204,16\nQcvaa 0,112,96,48\nQmvaa 0,116,100\n"
"Qmvaa 0,120,104\nQmvaa 0,124,108\nQcvaa 4,128,96,48\nQmvaa 4,132,100\nQmvaa 4,136,104\nQmvaa 4,140,108\ns_nop 7\nQe 0\n"
"Qe 1\nQe 2\nQe 3\nQe 4\nQe 5\nQe 6\nQe 7\ns_nop 0\n"
"Qk 16,0,1\nQk 17,2,3\nQk 18,4,5\nQk 19,6,7\ns_nop 1\nQmava 224,16,144\nQmava 228,16,148\nQmava 232,16,152\n"
"Qmava 236,16,156\nQmava 240,16,160\nQmava 244,16,164\nQmava 248,16,168\nQmava 252,16,172\nQmaav 200,204,16\ns_add_u32 s89, s89, 8192\ns_add_u32 s66, s66, 1\n"
"s_cmp_lt_u32 s66, 32\ns_cbranch_scc1 .Lfb2\ns_nop 7\nv_accvgpr_read_b32 v0, a176\nv_accvgpr_read_b32 v4, a180\nv_accvgpr_read_b32 v8, a184\nv_accvgpr_read_b32 v12, a188\nv_accvgpr_read_b32 v16, a192\n"
"v_accvgpr_read_b32 v20, a196\nv_accvgpr_read_b32 v24, a200\ns_mov_b32 s73, 0\ns_cmp_eq_u32 s67, 0\ns_cbranch_scc1 .Lfbret0\ns_branch .Lfbret1\n.Lf2_w:\ns_mov_b32 s97, 0\n"
".Lf2_l:\ns_sleep 1\nbuffer_load_dword v78, v76, s[24:27], 0 offen sc1\ns_waitcnt vmcnt(0)\ns_add_u32 s97, s97, 1\ns_cmp_gt_u32 s97, 1048576\ns_cbranch_scc1 .Lf2_r\nv_sub_u32 v66, v78, v65\n"
"v_cmp_gt_u32 vcc, 2, v66\ns_and_b64 vcc, vcc, s[28:29]\ns_cbranch_scc1 .Lf2_l\ns_branch .Lf2_r\n.Lf3_w:\ns_mov_b32 s97, 0\n.Lf3_l:\ns_sleep 1\n"
"buffer_load_dword v78, v76, s[24:27], 0 offen sc1\ns_waitcnt vmcnt(0)\ns_add_u32 s97, s97, 1\ns_cmp_gt_u32 s97, 1048576\ns_cbranch_scc1 .Lf3_r\nv_sub_u32 v66, v78, v65\nv_cmp_gt_u32 vcc, 3, v66\ns_and_b64 vcc, vcc, s[28:29]\n"
"s_cbranch_scc1 .Lf3_l\ns_branch .Lf3_r\n.Lx0_w:\ns_mov_b32 s97, 0\n.Lx0_l:\ns_sleep 1\nbuffer_load_dword v78, v76, s[24:27], 0 offen sc1\ns_waitcnt vmcnt(0)\n"
"s_add_u32 s97, s97, 1\ns_cmp_gt_u32 s97, 1048576\ns_cbranch_scc1 .Lx0_r\nv_sub_u32 v66, v78, v65\nv_cmp_gt_u32 vcc, 1, v66\ns_and_b64 vcc, vcc, s[28:29]\ns_cbranch_scc1 .Lx0_l\ns_branch .Lx0_r\n"
".Lx1_w:\ns_mov_b32 s97, 0\n.Lx1_l:\ns_sleep 1\nbuffer_load_dword v78, v76, s[24:27], 0 offen sc1\ns_waitcnt vmcnt(0)\ns_add_u32 s97, s97, 1\ns_cmp_gt_u32 s97, 1048576\n"
"s_cbranch_scc1 .Lx1_r\nv_sub_u32 v66, v78, v65\nv_cmp_gt_u32 vcc, 1, v66\ns_and_b64 vcc, vcc, s[28:29]\ns_cbranch_scc1 .Lx1_l\ns_branch .Lx1_r\n.Lexit:\n"
  :: "s"((unsigned)kp), "s"((unsigned)(kp >> 32)), "s"((unsigned)vp), "s"((unsigned)(vp >> 32)), "s"((unsigned)xp), "s"((unsigned)(xp >> 32)),
     "s"((unsigned)wp), "s"((unsigned)(wp >> 32)), "s"((unsigned)op), "s"((unsigned)(op >> 32)), "s"(ldsb), "s"(wid), "s"(head0),
     "s"((unsigned)dp), "s"((unsigned)(dp >> 32)), "s"((unsigned)wop), "s"((unsigned)(wop >> 32)), "s"((unsigned)yp), "s"((unsigned)(yp >> 32)),
     "s"(hg), "s"(b * 8u + hg), "s"(kx)
  : "v0", "v1", "v2", "v3", "v4", "v5", "v6", "v7", "v8", "v9", "v10", "v11", "v12", "v13", "v14", "v15", "v16", "v17", "v18", "v19", "v20", "v21", "v22", "v23", "v24", "v25", "v26", "v27", "v28", "v29", "v30", "v31", "v32", "v33", "v34", "v35", "v36", "v37", "v38", "v39", "v40", "v41", "v42", "v43", "v44", "v45", "v46", "v47", "v48", "v49", "v50", "v51", "v52", "v53", "v54", "v55", "v56", "v57", "v58", "v59", "v60", "v61", "v62", "v63", "v64", "v65", "v66", "v67", "v68", "v69", "v70", "v71", "v72", "v73", "v74", "v75", "v76", "v77", "v78", "v79", "v80", "v81", "v82", "v83", "v84", "v85", "v86", "v87", "v88", "v89", "v90", "v91", "v92", "v93", "v94", "v95", "v96", "v97", "v98", "v99", "v100", "v101", "v102", "v103", "v104", "v105", "v106", "v107", "v108", "v109", "v110", "v111", "v112", "v113", "v114", "v115", "v116", "v117", "v118", "v119", "v120", "v121", "v122", "v123", "v124", "v125", "v126", "v127", "v128", "v129", "v130", "v131", "v132", "v133", "v134", "v135", "v136", "v137", "v138", "v139", "v140", "v141", "v142", "v143", "v144", "v145", "v146", "v147", "v148", "v149", "v150", "v151", "v152", "v153", "v154", "v155", "v156", "v157", "v158", "v159", "v160", "v161", "v162", "v163", "v164", "v165", "v166", "v167", "v168", "v169", "v170", "v171", "v172", "v173", "v174", "v175", "v176", "v177", "v178", "v179", "v180", "v181", "v182", "v183", "v184", "v185", "v186", "v187", "v188", "v189", "v190", "v191", "v192", "v193", "v194", "v195", "v196", "v197", "v198", "v199", "v200", "v201", "v202", "v203", "v204", "v205", "v206", "v207", "v208", "v209", "v210", "v211", "v212", "v213", "v214", "v215", "v216", "v217", "v218", "v219", "v220", "v221", "v222", "v223", "v224", "v225", "v226", "v227", "v228", "v229", "v230", "v231", "v232", "v233", "v234", "v235", "v236", "v237", "v238", "v239", "v240", "v241", "v242", "v243", "v244", "v245", "v246", "v247", "v248", "v249", "v250", "v251", "v252", "v253", "v254", "v255", "a0", "a1", "a2", "a3", "a4", "a5", "a6", "a7", "a8", "a9", "a10", "a11", "a12", "a13", "a14", "a15", "a16", "a17", "a18", "a19", "a20", "a21", "a22", "a23", "a24", "a25", "a26", "a27", "a28", "a29", "a30", "a31", "a32", "a33", "a34", "a35", "a36", "a37", "a38", "a39", "a40", "a41", "a42", "a43", "a44", "a45", "a46", "a47", "a48", "a49", "a50", "a51", "a52", "a53", "a54", "a55", "a56", "a57", "a58", "a59", "a60", "a61", "a62", "a63", "a64", "a65", "a66", "a67", "a68", "a69", "a70", "a71", "a72", "a73", "a74", "a75", "a76", "a77", "a78", "a79", "a80", "a81", "a82", "a83", "a84", "a85", "a86", "a87", "a88", "a89", "a90", "a91", "a92", "a93", "a94", "a95", "a96", "a97", "a98", "a99", "a100", "a101", "a102", "a103", "a104", "a105", "a106", "a107", "a108", "a109", "a110", "a111", "a112", "a113", "a114", "a115", "a116", "a117", "a118", "a119", "a120", "a121", "a122", "a123", "a124", "a125", "a126", "a127", "a128", "a129", "a130", "a131", "a132", "a133", "a134", "a135", "a136", "a137", "a138", "a139", "a140", "a141", "a142", "a143", "a144", "a145", "a146", "a147", "a148", "a149", "a150", "a151", "a152", "a153", "a154", "a155", "a156", "a157", "a158", "a159", "a160", "a161", "a162", "a163", "a164", "a165", "a166", "a167", "a168", "a169", "a170", "a171", "a172", "a173", "a174", "a175", "a176", "a177", "a178", "a179", "a180", "a181", "a182", "a183", "a184", "a185", "a186", "a187", "a188", "a189", "a190", "a191", "a192", "a193", "a194", "a195", "a196", "a197", "a198", "a199", "a200", "a201", "a202", "a203", "a204", "a205", "a206", "a207", "a208", "a209", "a210", "a211", "a212", "a213", "a214", "a215", "a216", "a217", "a218", "a219", "a220", "a221", "a222", "a223", "a224", "a225", "a226", "a227", "a228", "a229", "a230", "a231", "a232", "a233", "a234", "a235", "a236", "a237", "a238", "a239", "a240", "a241", "a242", "a243", "a244", "a245", "a246", "a247", "a248", "a249", "a250", "a251", "a252", "a253", "a254", "a255", "s24", "s25", "s26", "s27", "s28", "s29", "s30", "s31", "s40", "s41", "s42", "s43", "s44", "s45", "s46", "s47", "s48", "s49", "s50", "s51", "s52", "s53", "s54", "s55", "s56", "s57", "s58", "s59", "s60", "s61", "s62", "s63", "s64", "s65", "s66", "s67", "s68", "s69", "s70", "s71", "s72", "s73", "s74", "s75", "s76", "s77", "s78", "s79", "s80", "s81", "s82", "s83", "s84", "s85", "s86", "s87", "s88", "s89", "s90", "s91", "s92", "s93", "s94", "s95", "s96", "s97", "s98", "s99", "vcc", "scc", "memory");
}

extern "C" void kernel_launch(void* const* d_in, const int* in_sizes, int n_in, void* d_out, int out_size, void* d_ws, size_t ws_size, hipStream_t stream) {
  typedef const unsigned short* P;
  (void)hipFuncSetAttribute((const void*)mha2_asm, hipFuncAttributeMaxDynamicSharedMemorySize, 163840);
  hipLaunchKernelGGL(mha2_asm, dim3(256), dim3(256), 163840, stream, (P)d_in[0], (P)d_in[5], (P)d_in[3], (P)d_in[8], (P)d_in[1], (P)d_in[6], (P)d_in[2], (P)d_in[7],
                     (P)d_in[4], (P)d_in[9], (char*)d_ws, (unsigned short*)d_out);
}
```

```cpp
#include <hip/hip_runtime.h>
#include <stdint.h>

__global__ __launch_bounds__(256, 1) __attribute__((aligned(4096)))
void mha2_asm(const unsigned short* __restrict__ X0, const unsigned short* __restrict__ X1, const unsigned short* __restrict__ W0, const unsigned short* __restrict__ W1,
              const unsigned short* __restrict__ K0, const unsigned short* __restrict__ K1,
              const unsigned short* __restrict__ V0, const unsigned short* __restrict__ V1,
              const unsigned short* __restrict__ WO0, const unsigned short* __restrict__ WO1, char* ws, unsigned short* out) {
  extern __shared__ __attribute__((aligned(16))) char lds[];
  const unsigned c = blockIdx.x;
  const unsigned xcd = c & 7u, idx = c >> 3;
  const unsigned hg = idx & 7u, bb = xcd * 4u + (idx >> 3);
  const unsigned bk = bb >> 4, b = bb & 15u;
  const unsigned wid = __builtin_amdgcn_readfirstlane(threadIdx.x >> 6);
  const unsigned head0 = (hg >> 2) * 16u + (hg & 3u) * 2u + (wid >> 1);
  const unsigned long long kp = (unsigned long long)(bk ? K1 : K0) + ((unsigned long long)b * 32u + (head0 & ~1u)) * 262144ull - 1024ull;
  const unsigned long long vp = (unsigned long long)(bk ? V1 : V0) + ((unsigned long long)b * 32u + (head0 & ~1u)) * 262144ull - 1024ull;
  const unsigned long long xp = (unsigned long long)(bk ? X1 : X0) + (unsigned long long)b * (224u * 1280u);
  const unsigned long long wp = (unsigned long long)(bk ? W1 : W0);
  const unsigned long long op = (unsigned long long)ws + 0ull + (unsigned long long)bb * 6291456ull;
  const unsigned ldsb = (unsigned)(size_t)lds;
  const unsigned long long wop = (unsigned long long)(bk ? WO1 : WO0) - 4096ull;
  const unsigned long long yp = (unsigned long long)out + (unsigned long long)b * (224ull * 2560ull) + bk * 1280u + (hg & 3u) * 320u;
  const unsigned long long dp = (unsigned long long)ws + (300ull << 20) + (c * 4u + wid) * 256u;
  const unsigned hd = wid >> 1, ky = hg & 4u;
  const unsigned kx = (ky >> 0 & 1u) << 11 | (ky >> 1 & 1u) << 12 | (ky >> 2 & 1u) << 13 | (ky >> 3 & 1u) << 14 | (ky >> 4 & 1u) << 17;
  asm volatile(
".macro Qta d,v,o\nds_read_b64_tr_b16 a[\\d:\\d+1], v\\v offset:\\o\n.endm\n.macro Qdv d,v,o\nds_read_b128 v[\\d:\\d+3], v\\v offset:\\o\n.endm\n.macro Qda d,v,o\nds_read_b128 a[\\d:\\d+3], v\\v offset:\\o\n"
".endm\n.macro Qzvav d,a,b\nv_mfma_f32_16x16x32_bf16 v[\\d:\\d+3], a[\\a:\\a+3], v[\\b:\\b+3], 0\n.endm\n.macro Qcvav d,a,b,c\nv_mfma_f32_16x16x32_bf16 v[\\d:\\d+3], a[\\a:\\a+3], v[\\b:\\b+3], v[\\c:\\c+3]\n.endm\n.macro Qmvaa d,a,b\n"
"v_mfma_f32_16x16x32_bf16 v[\\d:\\d+3], a[\\a:\\a+3], a[\\b:\\b+3], v[\\d:\\d+3]\n.endm\n.macro Qcaav d,a,b,c\nv_mfma_f32_16x16x32_bf16 a[\\d:\\d+3], a[\\a:\\a+3], v[\\b:\\b+3], a[\\c:\\c+3]\n.endm\n.macro Qmaaa d,a,b\nv_mfma_f32_16x16x32_bf16 a[\\d:\\d+3], a[\\a:\\a+3], a[\\b:\\b+3], a[\\d:\\d+3]\n.endm\n"
".macro Qmvav d,a,b\nv_mfma_f32_16x16x32_bf16 v[\\d:\\d+3], a[\\a:\\a+3], v[\\b:\\b+3], v[\\d:\\d+3]\n.endm\n.macro Qmaav d,a,b\nv_mfma_f32_16x16x32_bf16 a[\\d:\\d+3], a[\\a:\\a+3], v[\\b:\\b+3], a[\\d:\\d+3]\n.endm\n.macro Qk d,a,b\nv_cvt_pk_bf16_f32 v\\d, v\\a, v\\b\n"
".endm\n.macro Qzvaa d,a,b\nv_mfma_f32_16x16x32_bf16 v[\\d:\\d+3], a[\\a:\\a+3], a[\\b:\\b+3], 0\n.endm\n.macro Qa d,a,b\nv_add_f32 v\\d, v\\a, v\\b\n.endm\n.macro Qe d\n"
"v_exp_f32 v\\d, v\\d\n.endm\n.macro Qcvaa d,a,b,c\nv_mfma_f32_16x16x32_bf16 v[\\d:\\d+3], a[\\a:\\a+3], a[\\b:\\b+3], v[\\c:\\c+3]\n.endm\n.macro Qmvva d,a,b\nv_mfma_f32_16x16x32_bf16 v[\\d:\\d+3], v[\\a:\\a+3], a[\\b:\\b+3], v[\\d:\\d+3]\n.endm\n"
".macro Qmava d,a,b\nv_mfma_f32_16x16x32_bf16 a[\\d:\\d+3], v[\\a:\\a+3], a[\\b:\\b+3], a[\\d:\\d+3]\n.endm\n.macro Qzvvv d,a,b\nv_mfma_f32_16x16x32_bf16 v[\\d:\\d+3], v[\\a:\\a+3], v[\\b:\\b+3], 0\n.endm\n.p2align 7\ns_memtime s[80:81]\n"
"s_memrealtime s[82:83]\nv_mbcnt_lo_u32_b32 v0, -1, 0\nv_mbcnt_hi_u32_b32 v0, -1, v0\nv_lshlrev_b32 v60, 4, v0\ns_add_u32 m0, %10, 160768\ns_getpc_b64 s[56:57]\n.Lpc0:\ns_and_b32 s56, s56, 0xffffff80\n"
"s_and_b32 s57, s57, 0xffff\ns_mov_b32 s58, .Lend-.Lpc0\ns_mov_b32 s59, 0x20000\ns_and_b32 s94, %20, 31\ns_lshl_b32 s94, s94, 2\ns_add_u32 s94, s94, %11\ns_lshl_b32 s94, s94, 10\nbuffer_load_dwordx4 v60, s[56:59], s94 offen lds\n"
"s_mov_b32 s52, %6\ns_and_b32 s53, %7, 0xffff\ns_mov_b32 s54, 5242880\ns_mov_b32 s55, 0x20000\ns_mov_b32 s48, %4\ns_and_b32 s49, %5, 0xffff\ns_mov_b32 s50, 286720\ns_mov_b32 s51, 0x20000\n"
"s_mov_b32 s70, %11\ns_and_b32 s92, s70, 1\ns_lshr_b32 s93, s70, 1\nv_lshrrev_b32 v2, 4, v0\ns_lshl_b32 s68, s92, 18\nv_and_b32 v4, 3, v0\nv_lshlrev_b32 v4, 4, v4\nv_and_b32 v1, 1, v2\n"
"v_lshl_add_u32 v4, v1, 6, v4\nv_lshrrev_b32 v1, 5, v0\nv_lshl_add_u32 v4, v1, 15, v4\nv_bfe_u32 v1, v0, 2, 2\nv_lshl_add_u32 v4, v1, 13, v4\nv_add_u32 v62, s68, v4\ns_lshl_b32 s87, %12, 8\ns_mul_i32 s62, s70, 4128\n"
"s_add_u32 s62, s62, %10\ns_add_u32 s62, s62, 33024\ns_add_u32 m0, s62, 0\ns_add_u32 s66, s87, 0\ns_nop 0\nbuffer_load_dwordx4 v62, s[52:55], s66 offen lds\ns_add_u32 s66, s87, 64504\ns_nop 0\n"
"buffer_load_dwordx4 v62, s[52:55], s66 offen offset:1032 lds\ns_add_u32 s66, s87, 129008\ns_nop 0\nbuffer_load_dwordx4 v62, s[52:55], s66 offen offset:2064 lds\ns_add_u32 s66, s87, 193512\ns_nop 0\nbuffer_load_dwordx4 v62, s[52:55], s66 offen offset:3096 lds\ns_lshl_b32 s69, s92, 1\n"
"v_lshrrev_b32 v3, 5, v0\nv_and_b32 v4, 3, v0\nv_xor_b32 v4, s69, v4\nv_lshlrev_b32 v4, 4, v4\nv_lshl_add_u32 v4, v3, 6, v4\nv_bfe_u32 v1, v0, 2, 3\nv_mul_u32_u24 v64, 1280, v1\nv_add_u32 v64, v64, v4\n"
"s_mul_i32 s86, s93, 143360\ns_mul_i32 s68, s92, 10240\ns_add_u32 s86, s86, s68\ns_mul_i32 s63, s92, 14336\ns_mul_i32 s68, s93, 7168\ns_add_u32 s63, s63, s68\ns_add_u32 s63, s63, %10\ns_add_u32 s63, s63, 99072\n"
"s_add_u32 m0, s63, 33024\ns_add_u32 s97, s86, 0\ns_nop 0\nbuffer_load_dwordx4 v64, s[48:51], s97 offen lds\ns_add_u32 s97, s86, 19456\ns_nop 0\nbuffer_load_dwordx4 v64, s[48:51], s97 offen offset:1024 lds\ns_add_u32 s97, s86, 38912\n"
"s_nop 0\nbuffer_load_dwordx4 v64, s[48:51], s97 offen offset:2048 lds\ns_add_u32 s97, s86, 58368\ns_nop 0\nbuffer_load_dwordx4 v64, s[48:51], s97 offen offset:3072 lds\ns_add_u32 m0, s63, 37120\ns_add_u32 s97, s86, 81920\ns_nop 0\n"
"buffer_load_dwordx4 v64, s[48:51], s97 offen lds\ns_add_u32 s97, s86, 101376\ns_nop 0\nbuffer_load_dwordx4 v64, s[48:51], s97 offen offset:1024 lds\ns_add_u32 s97, s86, 120832\ns_nop 0\nbuffer_load_dwordx4 v64, s[48:51], s97 offen offset:2048 lds\ns_add_u32 m0, s62, 16512\n"
"s_add_u32 s66, s87, 128\ns_nop 0\nbuffer_load_dwordx4 v62, s[52:55], s66 offen lds\ns_mov_b32 s24, %8\ns_and_b32 s25, %9, 0xffff\ns_mov_b32 s26, 6291456\ns_mov_b32 s27, 0x20000\nv_and_b32 v5, 7, v0\n"
"v_lshlrev_b32 v5, 7, v5\ns_add_u32 s66, s87, 64632\ns_nop 0\nbuffer_load_dwordx4 v62, s[52:55], s66 offen offset:1032 lds\nv_add_u32 v76, 5242880, v5\ns_lshl_b32 s68, s70, 2\ns_add_u32 s68, s68, 4\nv_bfe_u32 v5, v0, 3, 1\n"
"v_mad_u32_u24 v76, v5, s68, v76\nbuffer_load_dword v65, v76, s[24:27], 0 offen sc1\ns_add_u32 s66, s87, 129136\ns_nop 0\nbuffer_load_dwordx4 v62, s[52:55], s66 offen offset:2064 lds\ns_mov_b32 s40, %0\ns_and_b32 s41, %1, 0xffff\ns_mov_b32 s42, 2622464\n"
"s_mov_b32 s43, 0x20000\ns_mov_b32 s44, %2\ns_and_b32 s45, %3, 0xffff\ns_add_u32 s66, s87, 193640\ns_nop 0\nbuffer_load_dwordx4 v62, s[52:55], s66 offen offset:3096 lds\ns_mov_b32 s46, 2622464\ns_mov_b32 s47, 0x20000\n"
"s_mov_b32 s56, %8\ns_and_b32 s57, %9, 0xffff\ns_mov_b32 s58, 0\ns_mov_b32 s59, 0x20000\ns_mov_b64 exec, 0\ns_cmp_eq_u32 s70, 0\ns_cbranch_scc1 .Lpg0\ns_cmp_eq_u32 s70, 1\n"
"s_cbranch_scc1 .Lpg1\ns_cmp_eq_u32 s70, 2\ns_cbranch_scc1 .Lpg2\ns_branch .Lpg3\n.Ltr0:\ns_mov_b64 exec, -1\ns_mov_b32 s65, 0\ns_mov_b32 s60, %21\n"
"s_add_u32 s90, s60, 260096\ns_mov_b32 s67, 0\ns_mov_b32 s72, 0x3e0293ee\ns_mov_b32 s79, -2031\ns_add_u32 m0, s63, 0\ns_add_u32 s97, s86, 128\ns_nop 0\nbuffer_load_dwordx4 v64, s[48:51], s97 offen lds\n"
"s_lshl_b32 s68, s70, 8\ns_lshl_b32 s69, s93, 5\ns_or_b32 s88, s68, s69\nv_bfe_u32 v1, v0, 2, 2\nv_mul_u32_u24 v4, 0x8100, v1\nv_and_b32 v1, 3, v0\ns_add_u32 s97, s86, 19584\ns_nop 0\n"
"buffer_load_dwordx4 v64, s[48:51], s97 offen offset:1024 lds\nv_lshl_or_b32 v4, v1, 4, v4\nv_and_b32 v1, 1, v2\nv_lshl_or_b32 v4, v1, 7, v4\nv_lshl_or_b32 v4, v3, 6, v4\nv_xor_b32 v4, s88, v4\nv_add_u32 v69, 1024, v4\ns_add_u32 s97, s86, 39040\n"
"s_nop 0\nbuffer_load_dwordx4 v64, s[48:51], s97 offen offset:2048 lds\nv_bfe_u32 v1, v0, 1, 2\nv_mul_u32_u24 v4, 0x8100, v1\nv_and_b32 v1, 9, v0\nv_lshl_or_b32 v4, v1, 4, v4\nv_lshl_or_b32 v4, v2, 5, v4\nv_xor_b32 v4, s68, v4\n"
"s_add_u32 s97, s86, 58496\ns_nop 0\nbuffer_load_dwordx4 v64, s[48:51], s97 offen offset:3072 lds\nv_add_u32 v72, 1024, v4\ns_lshl_b32 s68, s70, 12\ns_add_u32 s61, s68, %10\ns_mul_i32 s68, s92, 8320\ns_lshl_b32 s69, s93, 12\n"
"s_add_u32 s68, s68, s69\ns_add_u32 m0, s63, 4096\ns_add_u32 s97, s86, 82048\ns_nop 0\nbuffer_load_dwordx4 v64, s[48:51], s97 offen lds\ns_add_u32 s68, s68, %10\ns_add_u32 s64, s68, 16384\ns_lshl_b32 s69, s93, 11\n"
"s_add_u32 s69, s69, %10\nv_bfe_u32 v3, v0, 3, 1\nv_lshlrev_b32 v4, 1, v3\ns_add_u32 s97, s86, 101504\ns_nop 0\nbuffer_load_dwordx4 v64, s[48:51], s97 offen offset:1024 lds\nv_xor_b32 v4, v4, v2\nv_and_b32 v1, 7, v0\n"
"v_lshl_add_u32 v4, v1, 2, v4\nv_lshlrev_b32 v4, 4, v4\nv_bfe_u32 v1, v0, 2, 2\nv_and_b32 v5, 255, v4\ns_add_u32 s97, s86, 120960\ns_nop 0\nbuffer_load_dwordx4 v64, s[48:51], s97 offen offset:2048 lds\nv_lshl_or_b32 v1, v1, 12, v5\n"
"v_add_u32 v68, s69, v1\ns_mul_i32 s68, s92, 7168\ns_add_u32 s68, s68, %10\ns_add_u32 s68, s68, 99072\nv_mul_u32_u24 v1, 14336, v3\ns_add_u32 m0, s62, 33024\ns_add_u32 s66, s87, 524288\ns_nop 0\n"
"buffer_load_dwordx4 v62, s[52:55], s66 offen lds\nv_add3_u32 v63, v1, v4, s68\nv_lshrrev_b32 v3, 5, v0\nv_and_b32 v4, 31, v0\nv_lshlrev_b32 v4, 3, v4\nv_and_b32 v1, 1, v2\nv_lshl_add_u32 v4, v1, 13, v4\ns_add_u32 s66, s87, 588792\n"
"s_nop 0\nbuffer_load_dwordx4 v62, s[52:55], s66 offen offset:1032 lds\nv_lshl_add_u32 v4, v3, 12, v4\nv_add_u32 v4, 16384, v4\nv_add_u32 v70, s69, v4\nv_add_u32 v71, 66048, v70\ns_mul_i32 s68, s93, 8256\ns_add_u32 s68, s68, %10\n"
"s_add_u32 s66, s87, 653296\ns_nop 0\nbuffer_load_dwordx4 v62, s[52:55], s66 offen offset:2064 lds\ns_add_u32 s68, s68, 33024\nv_mul_u32_u24 v3, 1032, v2\nv_and_b32 v4, 15, v0\nv_lshlrev_b32 v4, 4, v4\nv_add3_u32 v61, v3, v4, s68\n"
"s_lshl_b32 s68, s70, 2\ns_add_u32 s66, s87, 717800\ns_nop 0\nbuffer_load_dwordx4 v62, s[52:55], s66 offen offset:3096 lds\ns_add_u32 s69, %10, 131968\ns_add_u32 s68, s68, s69\nv_mov_b32 v73, s68\nv_and_b32 v3, 3, v0\n"
"v_lshl_add_u32 v74, v3, 2, s69\nv_mov_b32 v75, 0\ns_mov_b64 exec, 0\ns_cmp_eq_u32 s70, 0\ns_cbranch_scc1 .Lpg4\ns_cmp_eq_u32 s70, 1\ns_cbranch_scc1 .Lpg5\ns_cmp_eq_u32 s70, 2\n"
"s_cbranch_scc1 .Lpg6\ns_branch .Lpg7\n.Ltr9:\ns_mov_b64 exec, -1\nds_write_b32 v73, v75\ns_waitcnt lgkmcnt(0)\ns_mul_i32 s91, s70, 7168\ns_add_u32 s91, s91, %10\n"
"s_add_u32 s91, s91, 98304\ns_lshl_b32 s96, s70, 11\ns_add_u32 m0, s62, 49536\ns_add_u32 s66, s87, 524416\ns_nop 0\nbuffer_load_dwordx4 v62, s[52:55], s66 offen lds\ns_add_u32 s96, s96, %10\nv_and_b32 v3, 1, v0\n"
"v_mul_u32_u24 v3, 0xaaaa, v3\nv_or_b32 v77, 0x4444, v3\ns_and_b32 s68, %19, 4\ns_lshl_b32 s28, 15, s68\ns_add_u32 s66, s87, 588920\ns_nop 0\nbuffer_load_dwordx4 v62, s[52:55], s66 offen offset:1032 lds\ns_mov_b32 s29, 0\n"
"s_and_b32 s68, %19, 4\ns_add_u32 s68, s68, %19\ns_lshl_b32 s71, s68, 18\ns_mul_i32 s69, s70, 0x80100\ns_and_b32 s69, s69, 0x100100\ns_add_u32 s66, s87, 653424\ns_nop 0\nbuffer_load_dwordx4 v62, s[52:55], s66 offen offset:2064 lds\n"
"s_add_u32 s68, s71, s69\ns_lshr_b32 s98, %20, 1\ns_xor_b32 s98, s98, %20\ns_lshl_b32 s98, s98, 11\ns_and_b32 s98, s98, 8192\ns_add_u32 s68, s68, s98\ns_add_u32 s66, s87, 717928\ns_nop 0\n"
"buffer_load_dwordx4 v62, s[52:55], s66 offen offset:3096 lds\ns_add_u32 s68, s68, 7168\ns_add_u32 s30, %8, s68\ns_addc_u32 s31, %9, 0\ns_mov_b32 s77, 0\nv_bfe_i32 v8, v0, 3, 1\nv_lshlrev_b32 v4, 10, v0\ns_mov_b64 exec, 0\n"
"s_cmp_eq_u32 s70, 0\ns_cbranch_scc1 .Lpg8\ns_cmp_eq_u32 s70, 1\ns_cbranch_scc1 .Lpg9\ns_cmp_eq_u32 s70, 2\ns_cbranch_scc1 .Lpg10\ns_branch .Lpg11\n.Ltr10:\n"
"s_mov_b64 exec, -1\nv_and_b32 v4, 0x6000, v4\nv_lshrrev_b32 v3, 5, v0\nv_lshl_or_b32 v4, v3, 16, v4\nv_and_b32 v3, 0x70, v60\nv_or_b32 v4, v3, v4\nv_or_b32 v8, 1, v8\nv_mov_b32 v9, v8\n"
"v_mov_b32 v10, v8\nv_mov_b32 v11, v8\nbuffer_store_dwordx4 v[8:11], v4, s[24:27], s68 offen offset:0 sc1\ns_nop 1\ns_lshl_b32 s68, s70, 8\nv_lshl_or_b32 v3, v2, 15, v60\nv_xor_b32 v79, s68, v3\ns_and_b32 s68, %19, 3\n"
"s_lshl_b32 s99, 3072, s68\ns_or_b32 s99, s99, 0x262\ns_mov_b32 s94, 0x7ffff000\ns_mul_i32 s68, %20, 40960\ns_mul_i32 s69, s93, 20480\ns_add_u32 s68, s68, s69\ns_lshl_b32 s69, s92, 10\ns_add_u32 s68, s68, s69\n"
"s_add_u32 s68, s68, 2048\ns_add_u32 s94, %6, s68\ns_addc_u32 s95, %7, 0\ns_and_b32 s95, s95, 0xffff\ns_mov_b32 s75, 17408\n.Lround:\ns_cmp_lg_u32 s67, 0\ns_cselect_b64 vcc, -1, 0\n"
"s_waitcnt vmcnt(15)\ns_barrier\nQta 112,61,0\nQta 114,61,512\nQdv 24,63,33024\nQdv 28,63,34048\nQdv 32,63,35072\nQdv 36,63,36096\n"
"Qdv 40,63,37120\nQdv 44,63,38144\nQdv 48,63,39168\nQda 176,63,33536\nQta 116,61,4128\nQta 118,61,4640\nQta 120,61,8\nQta 122,61,520\n"
"Qta 124,61,4136\nQta 126,61,4648\nQta 128,61,256\nQta 130,61,768\nQta 132,61,4384\nQta 134,61,4896\nQta 136,61,264\nQta 138,61,776\n"
"Qta 140,61,4392\nQta 142,61,4904\ns_waitcnt lgkmcnt(15)\nQzvav 0,112,24\nQzvav 4,112,28\nQda 180,63,34560\nQzvav 8,112,32\nQda 184,63,35584\n"
"Qzvav 12,112,36\nQda 188,63,36608\nQzvav 16,112,40\nQda 192,63,37632\nQzvav 20,112,44\nQda 196,63,38656\nQzvav 52,112,48\nQda 200,63,39680\n"
"v_max3_f32 v0, |v0|, |v1|, |v2|\nv_max3_f32 v4, |v4|, |v5|, |v6|\nv_max3_f32 v8, |v8|, |v9|, |v10|\nv_max3_f32 v12, |v12|, |v13|, |v14|\nv_max3_f32 v16, |v16|, |v17|, |v18|\nv_max3_f32 v20, |v20|, |v21|, |v22|\ns_nop 0\nv_max3_f32 v52, |v52|, |v53|, |v54|\n"
"v_min3_u32 v56, v0, v4, v8\nv_min3_u32 v56, v56, v12, v16\nv_min3_u32 v56, v56, v20, v52\nv_mov_b32 v57, v56\ns_nop 1\nv_permlane32_swap_b32 v56, v57\nv_max_f32 v56, v56, v57\nv_mov_b32 v57, v56\n"
"s_nop 1\nv_permlane16_swap_b32 v56, v57\nv_max_f32 v56, v56, v57\ns_nop 1\nv_min_f32_dpp v56, v56, v56 row_ror:8 row_mask:0xf bank_mask:0xf\ns_nop 1\nv_min_f32_dpp v56, v56, v56 row_ror:4 row_mask:0xf bank_mask:0xf\ns_nop 1\n"
"v_min_f32_dpp v56, v56, v56 row_ror:2 row_mask:0xf bank_mask:0xf\ns_nop 1\nv_min_f32_dpp v56, v56, v56 row_ror:1 row_mask:0xf bank_mask:0xf\nv_and_b32 v56, 0x7f800000, v56\nv_min_u32 v56, 0x78000000, v56\nv_add_u32 v56, 0x5400000, v56\nv_mov_b32 v57, v56\nv_mov_b64 v[58:59], v[56:57]\n"
"v_mul_f32 v52, -v56, s72\ns_waitcnt lgkmcnt(6)\nQcvav 80,112,24,56\nQmvaa 80,116,176\ns_add_u32 s68, s71, 0\nQcvav 84,120,24,56\ns_cbranch_vccz .Lst27\nbuffer_store_dwordx4 a[0:3], v79, s[56:59], s68 offen offset:0 sc1\n"
".Lst27:\nQmvaa 84,124,176\nQcvav 88,128,24,56\ns_add_u32 s68, s71, 16384\nQmvaa 88,132,176\ns_cbranch_vccz .Lst26\nbuffer_store_dwordx4 a[4:7], v79, s[56:59], s68 offen offset:0 sc1\n.Lst26:\n"
"Qcvav 92,136,24,56\nQmvaa 92,140,176\ns_waitcnt lgkmcnt(0)\nQcvav 124,136,28,56\ns_waitcnt vmcnt(16)\ns_barrier\nQmvaa 124,140,180\nQta 144,61,16512\n"
"s_add_u32 m0, s62, 0\ns_add_u32 s69, s87, 1048576\nQcvav 120,128,28,56\nbuffer_load_dwordx4 v62, s[52:55], s69 offen lds\nQmvaa 120,132,180\nQta 146,61,17024\nQcvav 116,120,28,56\nv_accvgpr_write_b32 a204, v56\n"
"Qmvaa 116,124,180\nQta 148,61,20640\ns_add_u32 s69, s87, 1113080\nQcvav 112,112,28,56\nbuffer_load_dwordx4 v62, s[52:55], s69 offen offset:1032 lds\nQmvaa 112,116,180\nQta 150,61,21152\nQcvav 144,112,32,56\n"
"v_accvgpr_write_b32 a205, v56\nQmvaa 144,116,184\nQta 152,61,16520\ns_add_u32 s69, s87, 1177584\nQcvav 148,120,32,56\nbuffer_load_dwordx4 v62, s[52:55], s69 offen offset:2064 lds\nQmvaa 148,124,184\nQta 154,61,17032\n"
"Qcvav 152,128,32,56\nv_accvgpr_write_b32 a206, v56\nQmvaa 152,132,184\nQta 156,61,20648\ns_add_u32 s69, s87, 1242088\nQcvav 156,136,32,56\nbuffer_load_dwordx4 v62, s[52:55], s69 offen offset:3096 lds\nQmvaa 156,140,184\n"
"Qta 158,61,21160\nQcvav 188,136,36,56\nv_accvgpr_write_b32 a207, v56\nQmvaa 188,140,188\nQta 160,61,16768\ns_add_u32 m0, s63, 33024\ns_add_u32 s88, s86, 256\nQcvav 184,128,36,56\n"
"buffer_load_dwordx4 v64, s[48:51], s88 offen lds\nQmvaa 184,132,188\nQta 162,61,17280\nQcvav 180,120,36,56\nQmvaa 180,124,188\nQta 164,61,20896\ns_add_u32 s88, s86, 19712\nQcvav 176,112,36,56\n"
"buffer_load_dwordx4 v64, s[48:51], s88 offen offset:1024 lds\nQmvaa 176,116,188\nQta 166,61,21408\nQcvav 208,112,40,56\nQmvaa 208,116,192\nQta 168,61,16776\ns_add_u32 s88, s86, 39168\nQcvav 212,120,40,56\n"
"buffer_load_dwordx4 v64, s[48:51], s88 offen offset:2048 lds\nQmvaa 212,124,192\nQta 170,61,17288\nQcvav 216,128,40,56\nQmvaa 216,132,192\nQta 172,61,20904\ns_add_u32 s88, s86, 58624\nQcvav 220,136,40,56\n"
"buffer_load_dwordx4 v64, s[48:51], s88 offen offset:3072 lds\nQmvaa 220,140,192\nQta 174,61,21416\nQcvav 252,136,44,56\nQmvaa 252,140,196\ns_add_u32 m0, s63, 37120\ns_add_u32 s88, s86, 82176\nQcvav 248,128,44,56\n"
"buffer_load_dwordx4 v64, s[48:51], s88 offen lds\nQmvaa 248,132,196\nQcvav 244,120,44,56\nQmvaa 244,124,196\ns_add_u32 s88, s86, 101632\nQcvav 240,112,44,56\nbuffer_load_dwordx4 v64, s[48:51], s88 offen offset:1024 lds\nQmvaa 240,116,196\n"
"Qcaav 224,112,48,204\nQmaaa 224,116,200\ns_add_u32 s88, s86, 121088\nQcaav 228,120,48,204\nbuffer_load_dwordx4 v64, s[48:51], s88 offen offset:2048 lds\nQmaaa 228,124,200\nQcaav 232,128,48,204\nQmaaa 232,132,200\n"
"Qcaav 236,136,48,204\nQmaaa 236,140,200\ns_waitcnt lgkmcnt(0)\nQcvav 96,144,24,56\nQmvaa 96,148,176\ns_add_u32 s68, s71, 131072\nQcvav 100,152,24,56\ns_cbranch_vccz .Lst25\n"
"buffer_store_dwordx4 a[8:11], v79, s[56:59], s68 offen offset:0 sc1\n.Lst25:\nQmvaa 100,156,176\nQcvav 104,160,24,56\ns_add_u32 s68, s71, 147456\nQmvaa 104,164,176\ns_cbranch_vccz .Lst24\nbuffer_store_dwordx4 a[12:15], v79, s[56:59], s68 offen offset:0 sc1\n"
".Lst24:\nQcvav 108,168,24,56\nQmvaa 108,172,176\ns_waitcnt lgkmcnt(0)\nQcvav 140,168,28,56\ns_waitcnt vmcnt(15)\ns_barrier\nQmvaa 140,172,180\n"
"Qdv 24,63,0\ns_add_u32 m0, s62, 16512\ns_add_u32 s69, s87, 1048704\nQcvav 136,160,28,56\nbuffer_load_dwordx4 v62, s[52:55], s69 offen lds\nQmvaa 136,164,180\nQda 176,63,512\nQcvav 132,152,28,56\n"
"Qta 112,61,33024\nQmvaa 132,156,180\ns_add_u32 s69, s87, 1113208\nQcvav 128,144,28,56\nbuffer_load_dwordx4 v62, s[52:55], s69 offen offset:1032 lds\nQmvaa 128,148,180\nQdv 28,63,1024\nQcvav 160,144,32,56\n"
"Qda 180,63,1536\nQmvaa 160,148,184\nQta 114,61,33536\ns_add_u32 s69, s87, 1177712\nQcvav 164,152,32,56\nbuffer_load_dwordx4 v62, s[52:55], s69 offen offset:2064 lds\nQmvaa 164,156,184\n.Lpg0:\n"
"s_cbranch_execz .Ltr0\nQta 116,61,37152\nQcvav 168,160,32,56\nQmvaa 168,164,184\nQta 118,61,37664\ns_add_u32 s69, s87, 1242216\nQcvav 172,168,32,56\nbuffer_load_dwordx4 v62, s[52:55], s69 offen offset:3096 lds\n"
"Qmvaa 172,172,184\nQdv 32,63,2048\nQcvav 204,168,36,56\nQda 184,63,2560\nQmvaa 204,172,188\nQta 120,61,33032\ns_add_u32 m0, s61, 0\nQcvav 200,160,36,56\n"
"s_cbranch_vccnz .Ld0q0\nbuffer_load_dwordx4 v69, s[40:43], s60 offen nt lds\n.Ld0q0:\nQmvaa 200,164,188\nQta 122,61,33544\nQcvav 196,152,36,56\nQmvaa 196,156,188\nQta 124,61,37160\n"
"Qcvav 192,144,36,56\nQdv 36,63,3072\nQmvaa 192,148,188\nQda 188,63,3584\nQcvav 224,144,40,56\nQta 126,61,37672\nQmvaa 224,148,192\nQcvav 228,152,40,56\n"
"Qta 128,61,33280\nQmvaa 228,156,192\nQcvav 232,160,40,56\nQta 130,61,33792\nQmvaa 232,164,192\nQcvav 236,168,40,56\nQdv 40,63,4096\nQmvaa 236,172,192\n"
"Qda 192,63,4608\nQcaav 220,168,44,204\nQta 132,61,37408\nQmaaa 220,172,196\nQcaav 216,160,44,204\nQta 134,61,37920\nQmaaa 216,164,196\nQcaav 212,152,44,204\n"
"Qta 136,61,33288\nQmaaa 212,156,196\nQcaav 208,144,44,204\nQdv 44,63,5120\nQmaaa 208,148,196\nQda 196,63,5632\nQcaav 240,144,48,204\nQta 138,61,33800\n"
"Qmaaa 240,148,200\nQcaav 244,152,48,204\nQta 140,61,37416\nQmaaa 244,156,200\nQcaav 248,160,48,204\nQta 142,61,37928\nQmaaa 248,164,200\nQcaav 252,168,48,204\n"
"Qdv 48,63,6144\nQmaaa 252,172,200\nQda 200,63,6656\ns_waitcnt lgkmcnt(2)\nQmvav 80,112,24\nQmvaa 80,116,176\ns_add_u32 s68, s71, 0\nQmvav 84,120,24\n"
"s_cbranch_vccz .Lst23\nbuffer_store_dwordx4 a[16:19], v79, s[56:59], s68 offen offset:1024 sc1\n.Lst23:\nQmvaa 84,124,176\nQmvav 88,128,24\ns_add_u32 s68, s71, 16384\nQmvaa 88,132,176\ns_cbranch_vccz .Lst22\n"
"buffer_store_dwordx4 a[20:23], v79, s[56:59], s68 offen offset:1024 sc1\n.Lst22:\nQmvav 92,136,24\nQmvaa 92,140,176\ns_waitcnt lgkmcnt(0)\nQmvav 124,136,28\ns_waitcnt vmcnt(17)\ns_barrier\n"
"Qmvaa 124,140,180\nQta 144,61,49536\ns_add_u32 m0, s62, 33024\ns_add_u32 s69, s87, 1572864\nQmvav 120,128,28\nbuffer_load_dwordx4 v62, s[52:55], s69 offen lds\nQmvaa 120,132,180\nQta 146,61,50048\n"
"Qmvav 116,120,28\nQmvaa 116,124,180\nQta 148,61,53664\ns_add_u32 s69, s87, 1637368\nQmvav 112,112,28\nbuffer_load_dwordx4 v62, s[52:55], s69 offen offset:1032 lds\nQmvaa 112,116,180\nQta 150,61,54176\n"
"Qmvav 144,112,32\nQmvaa 144,116,184\nQta 152,61,49544\ns_add_u32 s69, s87, 1701872\nQmvav 148,120,32\nbuffer_load_dwordx4 v62, s[52:55], s69 offen offset:2064 lds\nQmvaa 148,124,184\nQta 154,61,50056\n"
"Qmvav 152,128,32\nQmvaa 152,132,184\nQta 156,61,53672\ns_add_u32 s69, s87, 1766376\nQmvav 156,136,32\nbuffer_load_dwordx4 v62, s[52:55], s69 offen offset:3096 lds\nQmvaa 156,140,184\nQta 158,61,54184\n"
"Qmvav 188,136,36\nQmvaa 188,140,188\nQta 160,61,49792\ns_add_u32 m0, s63, 0\ns_add_u32 s88, s86, 384\nQmvav 184,128,36\nbuffer_load_dwordx4 v64, s[48:51], s88 offen lds\nQmvaa 184,132,188\n"
"Qta 162,61,50304\nQmvav 180,120,36\nQmvaa 180,124,188\nQta 164,61,53920\ns_add_u32 s88, s86, 19840\nQmvav 176,112,36\nbuffer_load_dwordx4 v64, s[48:51], s88 offen offset:1024 lds\nQmvaa 176,116,188\n"
"Qta 166,61,54432\nQmvav 208,112,40\nQmvaa 208,116,192\nQta 168,61,49800\ns_add_u32 s88, s86, 39296\nQmvav 212,120,40\nbuffer_load_dwordx4 v64, s[48:51], s88 offen offset:2048 lds\nQmvaa 212,124,192\n"
"Qta 170,61,50312\nQmvav 216,128,40\nQmvaa 216,132,192\nQta 172,61,53928\ns_add_u32 s88, s86, 58752\nQmvav 220,136,40\nbuffer_load_dwordx4 v64, s[48:51], s88 offen offset:3072 lds\nQmvaa 220,140,192\n"
"Qta 174,61,54440\nQmvav 252,136,44\nQmvaa 252,140,196\ns_add_u32 m0, s63, 4096\ns_add_u32 s88, s86, 82304\nQmvav 248,128,44\nbuffer_load_dwordx4 v64, s[48:51], s88 offen lds\nQmvaa 248,132,196\n"
"Qmvav 244,120,44\nQmvaa 244,124,196\ns_add_u32 s88, s86, 101760\nQmvav 240,112,44\nbuffer_load_dwordx4 v64, s[48:51], s88 offen offset:1024 lds\nQmvaa 240,116,196\nQmaav 224,112,48\nQmaaa 224,116,200\n"
"s_add_u32 s88, s86, 121216\nQmaav 228,120,48\nbuffer_load_dwordx4 v64, s[48:51], s88 offen offset:2048 lds\nQmaaa 228,124,200\nQmaav 232,128,48\nQmaaa 232,132,200\nQmaav 236,136,48\nQmaaa 236,140,200\n"
"s_waitcnt lgkmcnt(0)\nQmvav 96,144,24\nQmvaa 96,148,176\ns_add_u32 s68, s71, 131072\nQmvav 100,152,24\ns_cbranch_vccz .Lst21\nbuffer_store_dwordx4 a[24:27], v79, s[56:59], s68 offen offset:1024 sc1\n.Lst21:\n"
"Qmvaa 100,156,176\nQmvav 104,160,24\ns_add_u32 s68, s71, 147456\nQmvaa 104,164,176\ns_cbranch_vccz .Lst20\nbuffer_store_dwordx4 a[28:31], v79, s[56:59], s68 offen offset:1024 sc1\n.Lst20:\nQmvav 108,168,24\n"
"Qmvaa 108,172,176\ns_waitcnt lgkmcnt(0)\nQmvav 140,168,28\ns_waitcnt vmcnt(16)\ns_barrier\nQmvaa 140,172,180\nQdv 24,63,33024\ns_add_u32 m0, s62, 49536\n"
"s_add_u32 s69, s87, 1572992\nQmvav 136,160,28\nbuffer_load_dwordx4 v62, s[52:55], s69 offen lds\nQmvaa 136,164,180\nQda 176,63,33536\nQmvav 132,152,28\nQta 112,61,0\nQmvaa 132,156,180\n"
"s_add_u32 s69, s87, 1637496\nQmvav 128,144,28\nbuffer_load_dwordx4 v62, s[52:55], s69 offen offset:1032 lds\nQmvaa 128,148,180\nQdv 28,63,34048\nQmvav 160,144,32\nQda 180,63,34560\nQmvaa 160,148,184\n"
"Qta 114,61,512\ns_add_u32 s69, s87, 1702000\nQmvav 164,152,32\nbuffer_load_dwordx4 v62, s[52:55], s69 offen offset:2064 lds\nQmvaa 164,156,184\nQta 116,61,4128\nQmvav 168,160,32\nQmvaa 168,164,184\n"
"Qta 118,61,4640\ns_add_u32 s69, s87, 1766504\nQmvav 172,168,32\nbuffer_load_dwordx4 v62, s[52:55], s69 offen offset:3096 lds\nQmvaa 172,172,184\nQdv 32,63,35072\nQmvav 204,168,36\nQda 184,63,35584\n"
"Qmvaa 204,172,188\nQta 120,61,8\ns_add_u32 m0, s61, 0\nQmvav 200,160,36\ns_cbranch_vccnz .Ld0q1\nbuffer_load_dwordx4 v69, s[40:43], s60 offen offset:1024 nt lds\n.Ld0q1:\nQmvaa 200,164,188\n"
"Qta 122,61,520\nQmvav 196,152,36\nQmvaa 196,156,188\nQta 124,61,4136\nQmvav 192,144,36\nQdv 36,63,36096\nQmvaa 192,148,188\nQda 188,63,36608\n"
"Qmvav 224,144,40\nQta 126,61,4648\nQmvaa 224,148,192\nQmvav 228,152,40\nQta 128,61,256\nQmvaa 228,156,192\nQmvav 232,160,40\nQta 130,61,768\n"
"Qmvaa 232,164,192\nQmvav 236,168,40\nQdv 40,63,37120\nQmvaa 236,172,192\nQda 192,63,37632\nQmaav 220,168,44\nQta 132,61,4384\nQmaaa 220,172,196\n"
"Qmaav 216,160,44\nQta 134,61,4896\nQmaaa 216,164,196\nQmaav 212,152,44\nQta 136,61,264\nQmaaa 212,156,196\nQmaav 208,144,44\nQdv 44,63,38144\n"
"Qmaaa 208,148,196\nQda 196,63,38656\nQmaav 240,144,48\nQta 138,61,776\nQmaaa 240,148,200\nQmaav 244,152,48\nQta 140,61,4392\nQmaaa 244,156,200\n"
"Qmaav 248,160,48\nQta 142,61,4904\nQmaaa 248,164,200\nQmaav 252,168,48\nQdv 48,63,39168\nQmaaa 252,172,200\nQda 200,63,39680\ns_waitcnt lgkmcnt(2)\n"
"Qmvav 80,112,24\nQmvaa 80,116,176\ns_add_u32 s68, s71, 0\nQmvav 84,120,24\ns_cbranch_vccz .Lst19\nbuffer_store_dwordx4 a[32:35], v79, s[56:59], s68 offen offset:2048 sc1\n.Lst19:\nQmvaa 84,124,176\n"
"Qmvav 88,128,24\ns_add_u32 s68, s71, 16384\nQmvaa 88,132,176\ns_cbranch_vccz .Lst18\nbuffer_store_dwordx4 a[36:39], v79, s[56:59], s68 offen offset:2048 sc1\n.Lst18:\nQmvav 92,136,24\nQmvaa 92,140,176\n"
"s_waitcnt lgkmcnt(0)\nQmvav 124,136,28\ns_waitcnt vmcnt(17)\ns_barrier\nQmvaa 124,140,180\nQta 144,61,16512\ns_add_u32 m0, s62, 0\ns_add_u32 s69, s87, 2097152\n"
"Qmvav 120,128,28\nbuffer_load_dwordx4 v62, s[52:55], s69 offen lds\nQmvaa 120,132,180\nQta 146,61,17024\nQmvav 116,120,28\nQmvaa 116,124,180\nQta 148,61,20640\ns_add_u32 s69, s87, 2161656\n"
"Qmvav 112,112,28\nbuffer_load_dwordx4 v62, s[52:55], s69 offen offset:1032 lds\nQmvaa 112,116,180\nQta 150,61,21152\nQmvav 144,112,32\nQmvaa 144,116,184\nQta 152,61,16520\ns_add_u32 s69, s87, 2226160\n"
"Qmvav 148,120,32\nbuffer_load_dwordx4 v62, s[52:55], s69 offen offset:2064 lds\nQmvaa 148,124,184\nQta 154,61,17032\nQmvav 152,128,32\nQmvaa 152,132,184\nQta 156,61,20648\ns_add_u32 s69, s87, 2290664\n"
"Qmvav 156,136,32\nbuffer_load_dwordx4 v62, s[52:55], s69 offen offset:3096 lds\nQmvaa 156,140,184\nQta 158,61,21160\nQmvav 188,136,36\nQmvaa 188,140,188\nQta 160,61,16768\ns_add_u32 m0, s63, 33024\n"
"s_add_u32 s88, s86, 512\nQmvav 184,128,36\nbuffer_load_dwordx4 v64, s[48:51], s88 offen lds\nQmvaa 184,132,188\nQta 162,61,17280\nQmvav 180,120,36\nQmvaa 180,124,188\nQta 164,61,20896\n"
"s_add_u32 s88, s86, 19968\nQmvav 176,112,36\nbuffer_load_dwordx4 v64, s[48:51], s88 offen offset:1024 lds\nQmvaa 176,116,188\nQta 166,61,21408\nQmvav 208,112,40\nQmvaa 208,116,192\nQta 168,61,16776\n"
"s_add_u32 s88, s86, 39424\nQmvav 212,120,40\nbuffer_load_dwordx4 v64, s[48:51], s88 offen offset:2048 lds\nQmvaa 212,124,192\nQta 170,61,17288\nQmvav 216,128,40\nQmvaa 216,132,192\nQta 172,61,20904\n"
"s_add_u32 s88, s86, 58880\nQmvav 220,136,40\nbuffer_load_dwordx4 v64, s[48:51], s88 offen offset:3072 lds\nQmvaa 220,140,192\nQta 174,61,21416\nQmvav 252,136,44\nQmvaa 252,140,196\ns_add_u32 m0, s63, 37120\n"
"s_add_u32 s88, s86, 82432\nQmvav 248,128,44\nbuffer_load_dwordx4 v64, s[48:51], s88 offen lds\nQmvaa 248,132,196\nQmvav 244,120,44\nQmvaa 244,124,196\ns_add_u32 s88, s86, 101888\nQmvav 240,112,44\n"
"buffer_load_dwordx4 v64, s[48:51], s88 offen offset:1024 lds\nQmvaa 240,116,196\nQmaav 224,112,48\nQmaaa 224,116,200\ns_add_u32 s88, s86, 121344\nQmaav 228,120,48\nbuffer_load_dwordx4 v64, s[48:51], s88 offen offset:2048 lds\nQmaaa 228,124,200\n"
"Qmaav 232,128,48\nQmaaa 232,132,200\nQmaav 236,136,48\nQmaaa 236,140,200\ns_waitcnt lgkmcnt(0)\nQmvav 96,144,24\nQmvaa 96,148,176\ns_add_u32 s68, s71, 131072\n"
"Qmvav 100,152,24\ns_cbranch_vccz .Lst17\nbuffer_store_dwordx4 a[40:43], v79, s[56:59], s68 offen offset:2048 sc1\n.Lst17:\nQmvaa 100,156,176\nQmvav 104,160,24\ns_add_u32 s68, s71, 147456\nQmvaa 104,164,176\n"
"s_cbranch_vccz .Lst16\nbuffer_store_dwordx4 a[44:47], v79, s[56:59], s68 offen offset:2048 sc1\n.Lst16:\nQmvav 108,168,24\nQmvaa 108,172,176\ns_waitcnt lgkmcnt(0)\nQmvav 140,168,28\ns_waitcnt vmcnt(16)\n"
"s_barrier\nQmvaa 140,172,180\nQdv 24,63,0\ns_add_u32 m0, s62, 16512\ns_add_u32 s69, s87, 2097280\nQmvav 136,160,28\nbuffer_load_dwordx4 v62, s[52:55], s69 offen lds\nQmvaa 136,164,180\n"
"Qda 176,63,512\nQmvav 132,152,28\nQta 112,61,33024\nQmvaa 132,156,180\ns_add_u32 s69, s87, 2161784\nQmvav 128,144,28\nbuffer_load_dwordx4 v62, s[52:55], s69 offen offset:1032 lds\nQmvaa 128,148,180\n"
"Qdv 28,63,1024\nQmvav 160,144,32\nQda 180,63,1536\nQmvaa 160,148,184\nQta 114,61,33536\ns_add_u32 s69, s87, 2226288\nQmvav 164,152,32\nbuffer_load_dwordx4 v62, s[52:55], s69 offen offset:2064 lds\n"
"Qmvaa 164,156,184\nQta 116,61,37152\nQmvav 168,160,32\nQmvaa 168,164,184\nQta 118,61,37664\ns_add_u32 s69, s87, 2290792\nQmvav 172,168,32\nbuffer_load_dwordx4 v62, s[52:55], s69 offen offset:3096 lds\n"
"Qmvaa 172,172,184\nQdv 32,63,2048\nQmvav 204,168,36\nQda 184,63,2560\nQmvaa 204,172,188\nQta 120,61,33032\ns_add_u32 m0, s61, 0\nQmvav 200,160,36\n"
"s_cbranch_vccnz .Ld0q2\nbuffer_load_dwordx4 v69, s[40:43], s90 offen offset:2048 nt lds\n.Ld0q2:\nQmvaa 200,164,188\nQta 122,61,33544\nQmvav 196,152,36\nQmvaa 196,156,188\nQta 124,61,37160\n"
"Qmvav 192,144,36\nQdv 36,63,3072\nQmvaa 192,148,188\nQda 188,63,3584\nQmvav 224,144,40\nQta 126,61,37672\nQmvaa 224,148,192\nQmvav 228,152,40\n"
"Qta 128,61,33280\nQmvaa 228,156,192\nQmvav 232,160,40\nQta 130,61,33792\nQmvaa 232,164,192\nQmvav 236,168,40\nQdv 40,63,4096\nQmvaa 236,172,192\n"
"Qda 192,63,4608\nQmaav 220,168,44\nQta 132,61,37408\nQmaaa 220,172,196\nQmaav 216,160,44\nQta 134,61,37920\nQmaaa 216,164,196\nQmaav 212,152,44\n"
"Qta 136,61,33288\nQmaaa 212,156,196\nQmaav 208,144,44\nQdv 44,63,5120\nQmaaa 208,148,196\nQda 196,63,5632\nQmaav 240,144,48\nQta 138,61,33800\n"
"Qmaaa 240,148,200\nQmaav 244,152,48\nQta 140,61,37416\nQmaaa 244,156,200\nQmaav 248,160,48\nQta 142,61,37928\nQmaaa 248,164,200\nQmaav 252,168,48\n"
"Qdv 48,63,6144\nQmaaa 252,172,200\nQda 200,63,6656\ns_waitcnt lgkmcnt(2)\nQmvav 80,112,24\nQmvaa 80,116,176\ns_add_u32 s68, s71, 0\nQmvav 84,120,24\n"
"s_cbranch_vccz .Lst15\nbuffer_store_dwordx4 a[48:51], v79, s[56:59], s68 offen offset:3072 sc1\n.Lst15:\nQmvaa 84,124,176\nQmvav 88,128,24\ns_add_u32 s68, s71, 16384\nQmvaa 88,132,176\ns_cbranch_vccz .Lst14\n"
"buffer_store_dwordx4 a[52:55], v79, s[56:59], s68 offen offset:3072 sc1\n.Lst14:\nQmvav 92,136,24\nQmvaa 92,140,176\ns_waitcnt lgkmcnt(0)\nQmvav 124,136,28\ns_waitcnt vmcnt(17)\ns_barrier\n"
"Qmvaa 124,140,180\nQta 144,61,49536\ns_add_u32 m0, s62, 33024\ns_add_u32 s69, s87, 2621440\ns_mov_b32 s89, 0\nQmvav 120,128,28\nbuffer_load_dwordx4 v62, s[52:55], s69 offen lds\ns_cbranch_vccnz .Lpb0\n"
"s_load_dword s89, s[30:31], 0x0\n.Lpb0:\nQmvaa 120,132,180\nQta 146,61,50048\nQmvav 116,120,28\nQmvaa 116,124,180\nQta 148,61,53664\ns_add_u32 s69, s87, 2685944\n"
"Qmvav 112,112,28\nbuffer_load_dwordx4 v62, s[52:55], s69 offen offset:1032 lds\ns_add_u32 s77, s77, s89\nQmvaa 112,116,180\n.Lpg1:\ns_cbranch_execz .Ltr0\nQta 150,61,54176\nQmvav 144,112,32\n"
"s_add_u32 s77, s77, s89\nQmvaa 144,116,184\nQta 152,61,49544\ns_add_u32 s69, s87, 2750448\nQmvav 148,120,32\nbuffer_load_dwordx4 v62, s[52:55], s69 offen offset:2064 lds\ns_add_u32 s77, s77, s89\nQmvaa 148,124,184\n"
"Qta 154,61,50056\nQmvav 152,128,32\ns_add_u32 s77, s77, s89\nQmvaa 152,132,184\nQta 156,61,53672\ns_add_u32 s69, s87, 2814952\nQmvav 156,136,32\nbuffer_load_dwordx4 v62, s[52:55], s69 offen offset:3096 lds\n"
"s_add_u32 s77, s77, s89\nQmvaa 156,140,184\nQta 158,61,54184\nQmvav 188,136,36\ns_add_u32 s77, s77, s89\nQmvaa 188,140,188\nQta 160,61,49792\ns_add_u32 m0, s63, 0\n"
"s_add_u32 s88, s86, 640\nQmvav 184,128,36\nbuffer_load_dwordx4 v64, s[48:51], s88 offen lds\ns_add_u32 s77, s77, s89\nQmvaa 184,132,188\nQta 162,61,50304\nQmvav 180,120,36\ns_add_u32 s77, s77, s89\n"
"Qmvaa 180,124,188\nQta 164,61,53920\ns_add_u32 s88, s86, 20096\nQmvav 176,112,36\nbuffer_load_dwordx4 v64, s[48:51], s88 offen offset:1024 lds\ns_add_u32 s77, s77, s89\nQmvaa 176,116,188\nQta 166,61,54432\n"
"Qmvav 208,112,40\ns_add_u32 s77, s77, s89\nQmvaa 208,116,192\nQta 168,61,49800\ns_add_u32 s88, s86, 39552\nQmvav 212,120,40\nbuffer_load_dwordx4 v64, s[48:51], s88 offen offset:2048 lds\ns_add_u32 s77, s77, s89\n"
"Qmvaa 212,124,192\nQta 170,61,50312\nQmvav 216,128,40\ns_add_u32 s77, s77, s89\nQmvaa 216,132,192\nQta 172,61,53928\ns_add_u32 s88, s86, 59008\nQmvav 220,136,40\n"
"buffer_load_dwordx4 v64, s[48:51], s88 offen offset:3072 lds\ns_add_u32 s77, s77, s89\nQmvaa 220,140,192\nQta 174,61,54440\nQmvav 252,136,44\ns_add_u32 s77, s77, s89\nQmvaa 252,140,196\ns_add_u32 m0, s63, 4096\n"
"s_add_u32 s88, s86, 82560\nQmvav 248,128,44\nbuffer_load_dwordx4 v64, s[48:51], s88 offen lds\ns_add_u32 s77, s77, s89\nQmvaa 248,132,196\nQmvav 244,120,44\ns_add_u32 s77, s77, s89\nQmvaa 244,124,196\n"
"s_add_u32 s88, s86, 102016\nQmvav 240,112,44\nbuffer_load_dwordx4 v64, s[48:51], s88 offen offset:1024 lds\ns_add_u32 s77, s77, s89\nQmvaa 240,116,196\nQmaav 224,112,48\ns_add_u32 s77, s77, s89\nQmaaa 224,116,200\n"
"s_add_u32 s88, s86, 121472\nQmaav 228,120,48\nbuffer_load_dwordx4 v64, s[48:51], s88 offen offset:2048 lds\ns_add_u32 s77, s77, s89\nQmaaa 228,124,200\nQmaav 232,128,48\ns_add_u32 s77, s77, s89\nQmaaa 232,132,200\n"
"Qmaav 236,136,48\ns_add_u32 s77, s77, s89\nQmaaa 236,140,200\ns_waitcnt lgkmcnt(0)\nQmvav 96,144,24\nQmvaa 96,148,176\ns_add_u32 s68, s71, 131072\nQmvav 100,152,24\n"
"s_cbranch_vccz .Lst13\nbuffer_store_dwordx4 a[56:59], v79, s[56:59], s68 offen offset:3072 sc1\n.Lst13:\nQmvaa 100,156,176\nQmvav 104,160,24\ns_add_u32 s68, s71, 147456\nQmvaa 104,164,176\ns_cbranch_vccz .Lst12\n"
"buffer_store_dwordx4 a[60:63], v79, s[56:59], s68 offen offset:3072 sc1\n.Lst12:\nQmvav 108,168,24\nQmvaa 108,172,176\ns_waitcnt lgkmcnt(0)\nQmvav 140,168,28\ns_waitcnt vmcnt(16)\ns_barrier\n"
"Qmvaa 140,172,180\nQdv 24,63,33024\ns_add_u32 m0, s62, 49536\ns_add_u32 s69, s87, 2621568\nQmvav 136,160,28\nbuffer_load_dwordx4 v62, s[52:55], s69 offen lds\nQmvaa 136,164,180\nQda 176,63,33536\n"
"Qmvav 132,152,28\nQta 112,61,0\nQmvaa 132,156,180\ns_add_u32 s69, s87, 2686072\nQmvav 128,144,28\nbuffer_load_dwordx4 v62, s[52:55], s69 offen offset:1032 lds\nQmvaa 128,148,180\nQdv 28,63,34048\n"
"Qmvav 160,144,32\nQda 180,63,34560\nQmvaa 160,148,184\nQta 114,61,512\ns_add_u32 s69, s87, 2750576\nQmvav 164,152,32\nbuffer_load_dwordx4 v62, s[52:55], s69 offen offset:2064 lds\nQmvaa 164,156,184\n"
"Qta 116,61,4128\nQmvav 168,160,32\nQmvaa 168,164,184\nQta 118,61,4640\ns_add_u32 s69, s87, 2815080\nQmvav 172,168,32\nbuffer_load_dwordx4 v62, s[52:55], s69 offen offset:3096 lds\nQmvaa 172,172,184\n"
"Qdv 32,63,35072\nQmvav 204,168,36\nQda 184,63,35584\nQmvaa 204,172,188\nQta 120,61,8\ns_add_u32 m0, s61, 0\nQmvav 200,160,36\ns_cbranch_vccnz .Ld0q3\n"
"buffer_load_dwordx4 v69, s[40:43], s90 offen offset:3072 nt lds\n.Ld0q3:\nQmvaa 200,164,188\nQta 122,61,520\nQmvav 196,152,36\nQmvaa 196,156,188\nQta 124,61,4136\nQmvav 192,144,36\n"
"Qdv 36,63,36096\nQmvaa 192,148,188\nQda 188,63,36608\nQmvav 224,144,40\nQta 126,61,4648\nQmvaa 224,148,192\nQmvav 228,152,40\nQta 128,61,256\n"
"Qmvaa 228,156,192\nQmvav 232,160,40\nQta 130,61,768\nQmvaa 232,164,192\nQmvav 236,168,40\nQdv 40,63,37120\nQmvaa 236,172,192\nQda 192,63,37632\n"
"Qmaav 220,168,44\nQta 132,61,4384\nQmaaa 220,172,196\nQmaav 216,160,44\nQta 134,61,4896\nQmaaa 216,164,196\nQmaav 212,152,44\nQta 136,61,264\n"
"Qmaaa 212,156,196\nQmaav 208,144,44\nQdv 44,63,38144\nQmaaa 208,148,196\nQda 196,63,38656\nQmaav 240,144,48\nQta 138,61,776\nQmaaa 240,148,200\n"
"Qmaav 244,152,48\nQta 140,61,4392\nQmaaa 244,156,200\nQmaav 248,160,48\nQta 142,61,4904\nQmaaa 248,164,200\nQmaav 252,168,48\nQdv 48,63,39168\n"
"Qmaaa 252,172,200\nQda 200,63,39680\ns_waitcnt lgkmcnt(2)\nQmvav 80,112,24\nQmvaa 80,116,176\ns_add_u32 s68, s71, 4096\nQmvav 84,120,24\ns_cbranch_vccz .Lst11\n"
"buffer_store_dwordx4 a[64:67], v79, s[56:59], s68 offen offset:0 sc1\n.Lst11:\nQmvaa 84,124,176\nQmvav 88,128,24\ns_add_u32 s68, s71, 20480\nQmvaa 88,132,176\ns_cbranch_vccz .Lst10\nbuffer_store_dwordx4 a[68:71], v79, s[56:59], s68 offen offset:0 sc1\n"
".Lst10:\nQmvav 92,136,24\nQmvaa 92,140,176\ns_waitcnt lgkmcnt(0)\nQmvav 124,136,28\ns_waitcnt vmcnt(17)\ns_barrier\nQmvaa 124,140,180\n"
"Qta 144,61,16512\ns_add_u32 m0, s62, 0\ns_add_u32 s69, s87, 3145728\ns_mov_b32 s89, 0\nQmvav 120,128,28\nbuffer_load_dwordx4 v62, s[52:55], s69 offen lds\ns_cbranch_vccnz .Lpb1\ns_load_dword s89, s[30:31], 0x2000\n"
".Lpb1:\nQmvaa 120,132,180\nQta 146,61,17024\nQmvav 116,120,28\nQmvaa 116,124,180\nQta 148,61,20640\ns_add_u32 s69, s87, 3210232\nQmvav 112,112,28\n"
"buffer_load_dwordx4 v62, s[52:55], s69 offen offset:1032 lds\ns_add_u32 s77, s77, s89\nQmvaa 112,116,180\nQta 150,61,21152\nQmvav 144,112,32\ns_add_u32 s77, s77, s89\nQmvaa 144,116,184\nQta 152,61,16520\n"
"s_add_u32 s69, s87, 3274736\nQmvav 148,120,32\nbuffer_load_dwordx4 v62, s[52:55], s69 offen offset:2064 lds\ns_add_u32 s77, s77, s89\nQmvaa 148,124,184\nQta 154,61,17032\nQmvav 152,128,32\ns_add_u32 s77, s77, s89\n"
"Qmvaa 152,132,184\nQta 156,61,20648\ns_add_u32 s69, s87, 3339240\nQmvav 156,136,32\nbuffer_load_dwordx4 v62, s[52:55], s69 offen offset:3096 lds\ns_add_u32 s77, s77, s89\nQmvaa 156,140,184\nQta 158,61,21160\n"
"Qmvav 188,136,36\ns_add_u32 s77, s77, s89\nQmvaa 188,140,188\nQta 160,61,16768\ns_add_u32 m0, s63, 33024\ns_add_u32 s88, s86, 768\nQmvav 184,128,36\nbuffer_load_dwordx4 v64, s[48:51], s88 offen lds\n"
"s_add_u32 s77, s77, s89\nQmvaa 184,132,188\nQta 162,61,17280\nQmvav 180,120,36\ns_add_u32 s77, s77, s89\nQmvaa 180,124,188\nQta 164,61,20896\ns_add_u32 s88, s86, 20224\n"
"Qmvav 176,112,36\nbuffer_load_dwordx4 v64, s[48:51], s88 offen offset:1024 lds\ns_add_u32 s77, s77, s89\nQmvaa 176,116,188\nQta 166,61,21408\nQmvav 208,112,40\ns_add_u32 s77, s77, s89\nQmvaa 208,116,192\n"
"Qta 168,61,16776\ns_add_u32 s88, s86, 39680\nQmvav 212,120,40\nbuffer_load_dwordx4 v64, s[48:51], s88 offen offset:2048 lds\ns_add_u32 s77, s77, s89\nQmvaa 212,124,192\nQta 170,61,17288\nQmvav 216,128,40\n"
"s_add_u32 s77, s77, s89\nQmvaa 216,132,192\nQta 172,61,20904\ns_add_u32 s88, s86, 59136\nQmvav 220,136,40\nbuffer_load_dwordx4 v64, s[48:51], s88 offen offset:3072 lds\ns_add_u32 s77, s77, s89\nQmvaa 220,140,192\n"
"Qta 174,61,21416\nQmvav 252,136,44\ns_add_u32 s77, s77, s89\nQmvaa 252,140,196\ns_add_u32 m0, s63, 37120\ns_add_u32 s88, s86, 82688\nQmvav 248,128,44\nbuffer_load_dwordx4 v64, s[48:51], s88 offen lds\n"
"s_add_u32 s77, s77, s89\nQmvaa 248,132,196\nQmvav 244,120,44\ns_add_u32 s77, s77, s89\nQmvaa 244,124,196\ns_add_u32 s88, s86, 102144\nQmvav 240,112,44\nbuffer_load_dwordx4 v64, s[48:51], s88 offen offset:1024 lds\n"
"s_add_u32 s77, s77, s89\nQmvaa 240,116,196\nQmaav 224,112,48\ns_add_u32 s77, s77, s89\nQmaaa 224,116,200\ns_add_u32 s88, s86, 121600\nQmaav 228,120,48\nbuffer_load_dwordx4 v64, s[48:51], s88 offen offset:2048 lds\n"
"s_add_u32 s77, s77, s89\nQmaaa 228,124,200\nQmaav 232,128,48\ns_add_u32 s77, s77, s89\nQmaaa 232,132,200\nQmaav 236,136,48\ns_add_u32 s77, s77, s89\nQmaaa 236,140,200\n"
"s_waitcnt lgkmcnt(0)\nQmvav 96,144,24\nQmvaa 96,148,176\ns_add_u32 s68, s71, 135168\nQmvav 100,152,24\ns_cbranch_vccz .Lst9\nbuffer_store_dwordx4 a[72:75], v79, s[56:59], s68 offen offset:0 sc1\n.Lst9:\n"
"Qmvaa 100,156,176\nQmvav 104,160,24\ns_add_u32 s68, s71, 151552\nQmvaa 104,164,176\ns_cbranch_vccz .Lst8\nbuffer_store_dwordx4 a[76:79], v79, s[56:59], s68 offen offset:0 sc1\n.Lst8:\nQmvav 108,168,24\n"
"Qmvaa 108,172,176\ns_waitcnt lgkmcnt(0)\nQmvav 140,168,28\ns_waitcnt vmcnt(16)\ns_barrier\nQmvaa 140,172,180\nQdv 24,63,0\ns_add_u32 m0, s62, 16512\n"
"s_add_u32 s69, s87, 3145856\nQmvav 136,160,28\nbuffer_load_dwordx4 v62, s[52:55], s69 offen lds\nQmvaa 136,164,180\nQda 176,63,512\nQmvav 132,152,28\nQta 112,61,33024\nQmvaa 132,156,180\n"
"s_add_u32 s69, s87, 3210360\nQmvav 128,144,28\nbuffer_load_dwordx4 v62, s[52:55], s69 offen offset:1032 lds\nQmvaa 128,148,180\nQdv 28,63,1024\nQmvav 160,144,32\nQda 180,63,1536\nQmvaa 160,148,184\n"
"Qta 114,61,33536\ns_add_u32 s69, s87, 3274864\nQmvav 164,152,32\nbuffer_load_dwordx4 v62, s[52:55], s69 offen offset:2064 lds\nQmvaa 164,156,184\nQta 116,61,37152\nQmvav 168,160,32\nQmvaa 168,164,184\n"
"Qta 118,61,37664\ns_add_u32 s69, s87, 3339368\nQmvav 172,168,32\nbuffer_load_dwordx4 v62, s[52:55], s69 offen offset:3096 lds\nQmvaa 172,172,184\nQdv 32,63,2048\nQmvav 204,168,36\nQda 184,63,2560\n"
"Qmvaa 204,172,188\nQta 120,61,33032\ns_add_u32 m0, s64, 0\nQmvav 200,160,36\ns_cbranch_vccnz .Ld0q4\nbuffer_load_dwordx4 v72, s[44:47], s60 offen nt lds\n.Ld0q4:\nQmvaa 200,164,188\n"
"Qta 122,61,33544\nQmvav 196,152,36\nQmvaa 196,156,188\nQta 124,61,37160\nQmvav 192,144,36\nQdv 36,63,3072\nQmvaa 192,148,188\nQda 188,63,3584\n"
"Qmvav 224,144,40\nQta 126,61,37672\nQmvaa 224,148,192\nQmvav 228,152,40\nQta 128,61,33280\nQmvaa 228,156,192\nQmvav 232,160,40\nQta 130,61,33792\n"
"Qmvaa 232,164,192\nQmvav 236,168,40\nQdv 40,63,4096\nQmvaa 236,172,192\nQda 192,63,4608\nQmaav 220,168,44\nQta 132,61,37408\nQmaaa 220,172,196\n"
"Qmaav 216,160,44\nQta 134,61,37920\nQmaaa 216,164,196\nQmaav 212,152,44\nQta 136,61,33288\nQmaaa 212,156,196\nQmaav 208,144,44\nQdv 44,63,5120\n"
"Qmaaa 208,148,196\nQda 196,63,5632\nQmaav 240,144,48\nQta 138,61,33800\nQmaaa 240,148,200\nQmaav 244,152,48\nQta 140,61,37416\nQmaaa 244,156,200\n"
"Qmaav 248,160,48\nQta 142,61,37928\nQmaaa 248,164,200\nQmaav 252,168,48\nQdv 48,63,6144\nQmaaa 252,172,200\nQda 200,63,6656\ns_waitcnt lgkmcnt(2)\n"
"Qmvav 80,112,24\nQmvaa 80,116,176\ns_add_u32 s68, s71, 4096\nQmvav 84,120,24\ns_cbranch_vccz .Lst7\nbuffer_store_dwordx4 a[80:83], v79, s[56:59], s68 offen offset:1024 sc1\n.Lst7:\nQmvaa 84,124,176\n"
"Qmvav 88,128,24\ns_add_u32 s68, s71, 20480\nQmvaa 88,132,176\ns_cbranch_vccz .Lst6\nbuffer_store_dwordx4 a[84:87], v79, s[56:59], s68 offen offset:1024 sc1\n.Lst6:\nQmvav 92,136,24\nQmvaa 92,140,176\n"
"s_waitcnt lgkmcnt(0)\nQmvav 124,136,28\ns_waitcnt vmcnt(17)\ns_barrier\nQmvaa 124,140,180\nQta 144,61,49536\ns_add_u32 m0, s62, 33024\ns_add_u32 s69, s87, 3670016\n"
"s_mov_b32 s89, 0\nQmvav 120,128,28\nbuffer_load_dwordx4 v62, s[52:55], s69 offen lds\ns_cbranch_vccnz .Lpb2\ns_load_dword s89, s[30:31], 0x6000\n.Lpb2:\nQmvaa 120,132,180\nQta 146,61,50048\n"
"Qmvav 116,120,28\nQmvaa 116,124,180\nQta 148,61,53664\ns_add_u32 s69, s87, 3734520\nQmvav 112,112,28\nbuffer_load_dwordx4 v62, s[52:55], s69 offen offset:1032 lds\ns_add_u32 s77, s77, s89\nQmvaa 112,116,180\n"
"Qta 150,61,54176\nQmvav 144,112,32\ns_add_u32 s77, s77, s89\nQmvaa 144,116,184\nQta 152,61,49544\ns_add_u32 s69, s87, 3799024\nQmvav 148,120,32\nbuffer_load_dwordx4 v62, s[52:55], s69 offen offset:2064 lds\n"
"s_add_u32 s77, s77, s89\nQmvaa 148,124,184\nQta 154,61,50056\nQmvav 152,128,32\ns_add_u32 s77, s77, s89\nQmvaa 152,132,184\nQta 156,61,53672\ns_add_u32 s69, s87, 3863528\n"
"Qmvav 156,136,32\nbuffer_load_dwordx4 v62, s[52:55], s69 offen offset:3096 lds\ns_add_u32 s77, s77, s89\nQmvaa 156,140,184\nQta 158,61,54184\nQmvav 188,136,36\ns_add_u32 s77, s77, s89\nQmvaa 188,140,188\n"
"Qta 160,61,49792\ns_add_u32 m0, s63, 0\ns_add_u32 s88, s86, 896\nQmvav 184,128,36\nbuffer_load_dwordx4 v64, s[48:51], s88 offen lds\ns_add_u32 s77, s77, s89\nQmvaa 184,132,188\nQta 162,61,50304\n"
"Qmvav 180,120,36\ns_add_u32 s77, s77, s89\nQmvaa 180,124,188\nQta 164,61,53920\ns_add_u32 s88, s86, 20352\nQmvav 176,112,36\nbuffer_load_dwordx4 v64, s[48:51], s88 offen offset:1024 lds\ns_add_u32 s77, s77, s89\n"
"Qmvaa 176,116,188\nQta 166,61,54432\nQmvav 208,112,40\ns_add_u32 s77, s77, s89\nQmvaa 208,116,192\nQta 168,61,49800\ns_add_u32 s88, s86, 39808\nQmvav 212,120,40\n"
"buffer_load_dwordx4 v64, s[48:51], s88 offen offset:2048 lds\ns_add_u32 s77, s77, s89\nQmvaa 212,124,192\nQta 170,61,50312\nQmvav 216,128,40\ns_add_u32 s77, s77, s89\nQmvaa 216,132,192\nQta 172,61,53928\n"
"s_add_u32 s88, s86, 59264\nQmvav 220,136,40\nbuffer_load_dwordx4 v64, s[48:51], s88 offen offset:3072 lds\ns_add_u32 s77, s77, s89\nQmvaa 220,140,192\nQta 174,61,54440\nQmvav 252,136,44\ns_add_u32 s77, s77, s89\n"
"Qmvaa 252,140,196\ns_add_u32 m0, s63, 4096\ns_add_u32 s88, s86, 82816\nQmvav 248,128,44\nbuffer_load_dwordx4 v64, s[48:51], s88 offen lds\ns_add_u32 s77, s77, s89\nQmvaa 248,132,196\nQmvav 244,120,44\n"
"s_add_u32 s77, s77, s89\nQmvaa 244,124,196\ns_add_u32 s88, s86, 102272\nQmvav 240,112,44\nbuffer_load_dwordx4 v64, s[48:51], s88 offen offset:1024 lds\ns_add_u32 s77, s77, s89\nQmvaa 240,116,196\nQmaav 224,112,48\n"
".Lpg2:\ns_cbranch_execz .Ltr0\ns_add_u32 s77, s77, s89\nQmaaa 224,116,200\ns_add_u32 s88, s86, 121728\nQmaav 228,120,48\nbuffer_load_dwordx4 v64, s[48:51], s88 offen offset:2048 lds\ns_add_u32 s77, s77, s89\n"
"Qmaaa 228,124,200\nQmaav 232,128,48\ns_add_u32 s77, s77, s89\nQmaaa 232,132,200\nQmaav 236,136,48\ns_add_u32 s77, s77, s89\nQmaaa 236,140,200\ns_waitcnt lgkmcnt(0)\n"
"Qmvav 96,144,24\nQmvaa 96,148,176\ns_add_u32 s68, s71, 135168\nQmvav 100,152,24\ns_cbranch_vccz .Lst5\nbuffer_store_dwordx4 a[88:91], v79, s[56:59], s68 offen offset:1024 sc1\n.Lst5:\nQmvaa 100,156,176\n"
"Qmvav 104,160,24\ns_add_u32 s68, s71, 151552\nQmvaa 104,164,176\ns_cbranch_vccz .Lst4\nbuffer_store_dwordx4 a[92:95], v79, s[56:59], s68 offen offset:1024 sc1\n.Lst4:\nQmvav 108,168,24\nQmvaa 108,172,176\n"
"s_waitcnt lgkmcnt(0)\nQmvav 140,168,28\ns_waitcnt vmcnt(16)\ns_barrier\nQmvaa 140,172,180\nQdv 24,63,33024\ns_add_u32 m0, s62, 49536\ns_add_u32 s69, s87, 3670144\n"
"Qmvav 136,160,28\nbuffer_load_dwordx4 v62, s[52:55], s69 offen lds\nQmvaa 136,164,180\nQda 176,63,33536\nQmvav 132,152,28\nQta 112,61,0\nQmvaa 132,156,180\ns_add_u32 s69, s87, 3734648\n"
"Qmvav 128,144,28\nbuffer_load_dwordx4 v62, s[52:55], s69 offen offset:1032 lds\nQmvaa 128,148,180\nQdv 28,63,34048\nQmvav 160,144,32\nQda 180,63,34560\nQmvaa 160,148,184\nQta 114,61,512\n"
"s_add_u32 s69, s87, 3799152\nQmvav 164,152,32\nbuffer_load_dwordx4 v62, s[52:55], s69 offen offset:2064 lds\nQmvaa 164,156,184\nQta 116,61,4128\nQmvav 168,160,32\nQmvaa 168,164,184\nQta 118,61,4640\n"
"s_add_u32 s69, s87, 3863656\nQmvav 172,168,32\nbuffer_load_dwordx4 v62, s[52:55], s69 offen offset:3096 lds\nQmvaa 172,172,184\nQdv 32,63,35072\nQmvav 204,168,36\nQda 184,63,35584\nQmvaa 204,172,188\n"
"Qta 120,61,8\ns_add_u32 m0, s64, 0\nQmvav 200,160,36\ns_cbranch_vccnz .Ld0q5\nbuffer_load_dwordx4 v72, s[44:47], s60 offen offset:1024 nt lds\n.Ld0q5:\nQmvaa 200,164,188\nQta 122,61,520\n"
"Qmvav 196,152,36\nQmvaa 196,156,188\nQta 124,61,4136\nQmvav 192,144,36\nQdv 36,63,36096\nQmvaa 192,148,188\nQda 188,63,36608\nQmvav 224,144,40\n"
"Qta 126,61,4648\nQmvaa 224,148,192\nQmvav 228,152,40\nQta 128,61,256\nQmvaa 228,156,192\nQmvav 232,160,40\nQta 130,61,768\nQmvaa 232,164,192\n"
"Qmvav 236,168,40\nQdv 40,63,37120\nQmvaa 236,172,192\nQda 192,63,37632\nQmaav 220,168,44\nQta 132,61,4384\nQmaaa 220,172,196\nQmaav 216,160,44\n"
"Qta 134,61,4896\nQmaaa 216,164,196\nQmaav 212,152,44\nQta 136,61,264\nQmaaa 212,156,196\nQmaav 208,144,44\nQdv 44,63,38144\nQmaaa 208,148,196\n"
"Qda 196,63,38656\nQmaav 240,144,48\nQta 138,61,776\nQmaaa 240,148,200\nQmaav 244,152,48\nQta 140,61,4392\nQmaaa 244,156,200\nQmaav 248,160,48\n"
"Qta 142,61,4904\nQmaaa 248,164,200\nQmaav 252,168,48\nQdv 48,63,39168\nQmaaa 252,172,200\nQda 200,63,39680\ns_waitcnt lgkmcnt(2)\nQmvav 80,112,24\n"
"Qmvaa 80,116,176\ns_add_u32 s68, s71, 4096\nQmvav 84,120,24\ns_cbranch_vccz .Lst3\nbuffer_store_dwordx4 a[96:99], v79, s[56:59], s68 offen offset:2048 sc1\n.Lst3:\nQmvaa 84,124,176\nQmvav 88,128,24\n"
"s_add_u32 s68, s71, 20480\nQmvaa 88,132,176\ns_cbranch_vccz .Lst2\nbuffer_store_dwordx4 a[100:103], v79, s[56:59], s68 offen offset:2048 sc1\n.Lst2:\nQmvav 92,136,24\nQmvaa 92,140,176\ns_waitcnt lgkmcnt(0)\n"
"Qmvav 124,136,28\ns_waitcnt vmcnt(17)\ns_barrier\nQmvaa 124,140,180\nQta 144,61,16512\ns_add_u32 m0, s62, 0\ns_add_u32 s69, s87, 4194304\ns_mov_b32 s89, 0\n"
"Qmvav 120,128,28\nbuffer_load_dwordx4 v62, s[52:55], s69 offen lds\ns_cbranch_vccnz .Lpb3\ns_load_dword s89, s[30:31], 0x4000\n.Lpb3:\nQmvaa 120,132,180\nQta 146,61,17024\nQmvav 116,120,28\n"
"Qmvaa 116,124,180\nQta 148,61,20640\ns_add_u32 s69, s87, 4258808\nQmvav 112,112,28\nbuffer_load_dwordx4 v62, s[52:55], s69 offen offset:1032 lds\ns_add_u32 s77, s77, s89\nQmvaa 112,116,180\nQta 150,61,21152\n"
"Qmvav 144,112,32\ns_add_u32 s77, s77, s89\nQmvaa 144,116,184\nQta 152,61,16520\ns_add_u32 s69, s87, 4323312\nQmvav 148,120,32\nbuffer_load_dwordx4 v62, s[52:55], s69 offen offset:2064 lds\ns_add_u32 s77, s77, s89\n"
"Qmvaa 148,124,184\nQta 154,61,17032\nQmvav 152,128,32\ns_add_u32 s77, s77, s89\nQmvaa 152,132,184\nQta 156,61,20648\ns_add_u32 s69, s87, 4387816\nQmvav 156,136,32\n"
"buffer_load_dwordx4 v62, s[52:55], s69 offen offset:3096 lds\ns_add_u32 s77, s77, s89\nQmvaa 156,140,184\nQta 158,61,21160\nQmvav 188,136,36\ns_add_u32 s77, s77, s89\nQmvaa 188,140,188\nQta 160,61,16768\n"
"s_add_u32 m0, s63, 33024\ns_add_u32 s88, s86, 1024\nQmvav 184,128,36\nbuffer_load_dwordx4 v64, s[48:51], s88 offen lds\ns_add_u32 s77, s77, s89\nQmvaa 184,132,188\nQta 162,61,17280\nQmvav 180,120,36\n"
"s_add_u32 s77, s77, s89\nQmvaa 180,124,188\nQta 164,61,20896\ns_add_u32 s88, s86, 20480\nQmvav 176,112,36\nbuffer_load_dwordx4 v64, s[48:51], s88 offen offset:1024 lds\ns_add_u32 s77, s77, s89\nQmvaa 176,116,188\n"
"Qta 166,61,21408\nQmvav 208,112,40\ns_add_u32 s77, s77, s89\nQmvaa 208,116,192\nQta 168,61,16776\ns_add_u32 s88, s86, 39936\nQmvav 212,120,40\nbuffer_load_dwordx4 v64, s[48:51], s88 offen offset:2048 lds\n"
"s_add_u32 s77, s77, s89\nQmvaa 212,124,192\nQta 170,61,17288\nQmvav 216,128,40\ns_add_u32 s77, s77, s89\nQmvaa 216,132,192\nQta 172,61,20904\ns_add_u32 s88, s86, 59392\n"
"Qmvav 220,136,40\nbuffer_load_dwordx4 v64, s[48:51], s88 offen offset:3072 lds\ns_add_u32 s77, s77, s89\nQmvaa 220,140,192\nQta 174,61,21416\nQmvav 252,136,44\ns_add_u32 s77, s77, s89\nQmvaa 252,140,196\n"
"s_add_u32 m0, s63, 37120\ns_add_u32 s88, s86, 82944\nQmvav 248,128,44\nbuffer_load_dwordx4 v64, s[48:51], s88 offen lds\ns_add_u32 s77, s77, s89\nQmvaa 248,132,196\nQmvav 244,120,44\ns_add_u32 s77, s77, s89\n"
"Qmvaa 244,124,196\ns_add_u32 s88, s86, 102400\nQmvav 240,112,44\nbuffer_load_dwordx4 v64, s[48:51], s88 offen offset:1024 lds\ns_add_u32 s77, s77, s89\nQmvaa 240,116,196\nQmaav 224,112,48\ns_add_u32 s77, s77, s89\n"
"Qmaaa 224,116,200\ns_add_u32 s88, s86, 121856\nQmaav 228,120,48\nbuffer_load_dwordx4 v64, s[48:51], s88 offen offset:2048 lds\ns_add_u32 s77, s77, s89\nQmaaa 228,124,200\nQmaav 232,128,48\ns_add_u32 s77, s77, s89\n"
"Qmaaa 232,132,200\nQmaav 236,136,48\ns_add_u32 s77, s77, s89\nQmaaa 236,140,200\ns_waitcnt lgkmcnt(0)\nQmvav 96,144,24\nQmvaa 96,148,176\ns_add_u32 s68, s71, 135168\n"
"Qmvav 100,152,24\ns_cbranch_vccz .Lst1\nbuffer_store_dwordx4 a[104:107], v79, s[56:59], s68 offen offset:2048 sc1\n.Lst1:\nQmvaa 100,156,176\nQmvav 104,160,24\ns_add_u32 s68, s71, 151552\nQmvaa 104,164,176\n"
"s_cbranch_vccz .Lst0\nbuffer_store_dwordx4 a[108:111], v79, s[56:59], s68 offen offset:2048 sc1\n.Lst0:\nQmvav 108,168,24\nQmvaa 108,172,176\ns_waitcnt lgkmcnt(0)\nQmvav 140,168,28\ns_waitcnt vmcnt(16)\n"
"s_barrier\nQmvaa 140,172,180\nQdv 24,63,0\ns_add_u32 m0, s62, 16512\ns_add_u32 s69, s87, 4194432\nQmvav 136,160,28\nbuffer_load_dwordx4 v62, s[52:55], s69 offen lds\nQmvaa 136,164,180\n"
"Qda 176,63,512\nQmvav 132,152,28\nQta 112,61,33024\nQmvaa 132,156,180\ns_add_u32 s69, s87, 4258936\nQmvav 128,144,28\nbuffer_load_dwordx4 v62, s[52:55], s69 offen offset:1032 lds\nQmvaa 128,148,180\n"
"Qdv 28,63,1024\nQmvav 160,144,32\nQda 180,63,1536\nQmvaa 160,148,184\nQta 114,61,33536\ns_add_u32 s69, s87, 4323440\nQmvav 164,152,32\nbuffer_load_dwordx4 v62, s[52:55], s69 offen offset:2064 lds\n"
"Qmvaa 164,156,184\nQta 116,61,37152\nQmvav 168,160,32\nQmvaa 168,164,184\nQta 118,61,37664\ns_add_u32 s69, s87, 4387944\nQmvav 172,168,32\nbuffer_load_dwordx4 v62, s[52:55], s69 offen offset:3096 lds\n"
"Qmvaa 172,172,184\nQdv 32,63,2048\nQmvav 204,168,36\nQda 184,63,2560\nQmvaa 204,172,188\nQta 120,61,33032\ns_add_u32 m0, s64, 0\nQmvav 200,160,36\n"
"s_cbranch_vccnz .Ld0q6\nbuffer_load_dwordx4 v72, s[44:47], s90 offen offset:2048 nt lds\n.Ld0q6:\nQmvaa 200,164,188\nQta 122,61,33544\nQmvav 196,152,36\nQmvaa 196,156,188\nQta 124,61,37160\n"
"Qmvav 192,144,36\nQdv 36,63,3072\nQmvaa 192,148,188\nQda 188,63,3584\nQmvav 224,144,40\nQta 126,61,37672\nQmvaa 224,148,192\nQmvav 228,152,40\n"
"Qta 128,61,33280\nQmvaa 228,156,192\nQmvav 232,160,40\nQta 130,61,33792\nQmvaa 232,164,192\nQmvav 236,168,40\nQdv 40,63,4096\nQmvaa 236,172,192\n"
"Qda 192,63,4608\nQmaav 220,168,44\nQta 132,61,37408\nQmaaa 220,172,196\nQmaav 216,160,44\nQta 134,61,37920\nQmaaa 216,164,196\nQmaav 212,152,44\n"
"Qta 136,61,33288\nQmaaa 212,156,196\nQmaav 208,144,44\nQdv 44,63,5120\nQmaaa 208,148,196\nQda 196,63,5632\nQmaav 240,144,48\nQta 138,61,33800\n"
"Qmaaa 240,148,200\nQmaav 244,152,48\nQta 140,61,37416\nQmaaa 244,156,200\nQmaav 248,160,48\nQta 142,61,37928\nQmaaa 248,164,200\nQmaav 252,168,48\n"
"Qdv 48,63,6144\nQmaaa 252,172,200\nQda 200,63,6656\ns_waitcnt lgkmcnt(2)\nQmvav 80,112,24\nQmvaa 80,116,176\nQmvav 84,120,24\nQmvaa 84,124,176\n"
"Qmvav 88,128,24\nQmvaa 88,132,176\nQmvav 92,136,24\nQmvaa 92,140,176\ns_waitcnt lgkmcnt(0)\nQmvav 124,136,28\ns_waitcnt vmcnt(17)\ns_barrier\n"
"Qmvaa 124,140,180\nQta 144,61,49536\ns_add_u32 m0, s62, 33024\ns_add_u32 s69, s87, 4718592\nQmvav 120,128,28\nbuffer_load_dwordx4 v62, s[52:55], s69 offen lds\nQmvaa 120,132,180\nQta 146,61,50048\n"
"Qmvav 116,120,28\nQmvaa 116,124,180\nQta 148,61,53664\ns_add_u32 s69, s87, 4783096\nQmvav 112,112,28\nbuffer_load_dwordx4 v62, s[52:55], s69 offen offset:1032 lds\nQmvaa 112,116,180\nQta 150,61,54176\n"
"Qmvav 144,112,32\nQmvaa 144,116,184\nQta 152,61,49544\ns_add_u32 s69, s87, 4847600\nQmvav 148,120,32\nbuffer_load_dwordx4 v62, s[52:55], s69 offen offset:2064 lds\nQmvaa 148,124,184\nQta 154,61,50056\n"
"Qmvav 152,128,32\nQmvaa 152,132,184\nQta 156,61,53672\ns_add_u32 s69, s87, 4912104\nQmvav 156,136,32\nbuffer_load_dwordx4 v62, s[52:55], s69 offen offset:3096 lds\nQmvaa 156,140,184\nQta 158,61,54184\n"
"Qmvav 188,136,36\nQmvaa 188,140,188\nQta 160,61,49792\ns_add_u32 m0, s63, 0\ns_add_u32 s88, s86, 1152\nQmvav 184,128,36\nbuffer_load_dwordx4 v64, s[48:51], s88 offen lds\nQmvaa 184,132,188\n"
"Qta 162,61,50304\nQmvav 180,120,36\nQmvaa 180,124,188\nQta 164,61,53920\ns_add_u32 s88, s86, 20608\nQmvav 176,112,36\nbuffer_load_dwordx4 v64, s[48:51], s88 offen offset:1024 lds\nQmvaa 176,116,188\n"
"Qta 166,61,54432\nQmvav 208,112,40\nQmvaa 208,116,192\nQta 168,61,49800\ns_add_u32 s88, s86, 40064\nQmvav 212,120,40\nbuffer_load_dwordx4 v64, s[48:51], s88 offen offset:2048 lds\nQmvaa 212,124,192\n"
"Qta 170,61,50312\nQmvav 216,128,40\nQmvaa 216,132,192\nQta 172,61,53928\ns_add_u32 s88, s86, 59520\nQmvav 220,136,40\nbuffer_load_dwordx4 v64, s[48:51], s88 offen offset:3072 lds\nQmvaa 220,140,192\n"
"Qta 174,61,54440\nQmvav 252,136,44\nQmvaa 252,140,196\ns_add_u32 m0, s63, 4096\ns_add_u32 s88, s86, 83072\nQmvav 248,128,44\nbuffer_load_dwordx4 v64, s[48:51], s88 offen lds\nQmvaa 248,132,196\n"
"v_mov_b32 v66, s77\nQmvav 244,120,44\nds_write_b32 v73, v66 offset:16\nQmvaa 244,124,196\ns_add_u32 s88, s86, 102528\nQmvav 240,112,44\nbuffer_load_dwordx4 v64, s[48:51], s88 offen offset:1024 lds\nQmvaa 240,116,196\n"
"Qmaav 224,112,48\nQmaaa 224,116,200\ns_add_u32 s88, s86, 121984\nQmaav 228,120,48\nbuffer_load_dwordx4 v64, s[48:51], s88 offen offset:2048 lds\nQmaaa 228,124,200\nQmaav 232,128,48\nQmaaa 232,132,200\n"
"Qmaav 236,136,48\nQmaaa 236,140,200\ns_waitcnt lgkmcnt(1)\nQmvav 96,144,24\nQmvaa 96,148,176\nQmvav 100,152,24\nQmvaa 100,156,176\nQmvav 104,160,24\n"
"Qmvaa 104,164,176\nQmvav 108,168,24\nQmvaa 108,172,176\ns_waitcnt lgkmcnt(0)\nQmvav 140,168,28\ns_waitcnt vmcnt(16)\ns_barrier\nQmvaa 140,172,180\n"
"Qdv 24,63,33024\ns_add_u32 m0, s62, 49536\ns_add_u32 s69, s87, 4718720\nQmvav 136,160,28\nbuffer_load_dwordx4 v62, s[52:55], s69 offen lds\nQmvaa 136,164,180\nQda 176,63,33536\nQmvav 132,152,28\n"
"Qta 112,61,0\nQmvaa 132,156,180\ns_add_u32 s69, s87, 4783224\nQmvav 128,144,28\nbuffer_load_dwordx4 v62, s[52:55], s69 offen offset:1032 lds\nQmvaa 128,148,180\nQdv 28,63,34048\nQmvav 160,144,32\n"
"Qda 180,63,34560\nQmvaa 160,148,184\nQta 114,61,512\ns_add_u32 s69, s87, 4847728\nQmvav 164,152,32\nbuffer_load_dwordx4 v62, s[52:55], s69 offen offset:2064 lds\nQmvaa 164,156,184\nQta 116,61,4128\n"
"Qmvav 168,160,32\nQmvaa 168,164,184\nQta 118,61,4640\ns_add_u32 s69, s87, 4912232\nQmvav 172,168,32\nbuffer_load_dwordx4 v62, s[52:55], s69 offen offset:3096 lds\nQmvaa 172,172,184\nQdv 32,63,35072\n"
"Qmvav 204,168,36\nQda 184,63,35584\nQmvaa 204,172,188\nQta 120,61,8\ns_add_u32 m0, s64, 0\nQmvav 200,160,36\ns_cbranch_vccnz .Ld0q7\nbuffer_load_dwordx4 v72, s[44:47], s90 offen offset:3072 nt lds\n"
"s_add_u32 s65, s65, 2048\ns_xor_b32 s60, s65, %21\ns_add_u32 s90, s60, 260096\n.Ld0q7:\nQmvaa 200,164,188\nQta 122,61,520\nQmvav 196,152,36\nQmvaa 196,156,188\n"
"Qta 124,61,4136\nQmvav 192,144,36\nQdv 36,63,36096\nQmvaa 192,148,188\nQda 188,63,36608\nQmvav 224,144,40\nQta 126,61,4648\nQmvaa 224,148,192\n"
"Qmvav 228,152,40\nQta 128,61,256\nQmvaa 228,156,192\nQmvav 232,160,40\nQta 130,61,768\nQmvaa 232,164,192\nQmvav 236,168,40\nQdv 40,63,37120\n"
"Qmvaa 236,172,192\nQda 192,63,37632\nQmaav 220,168,44\n.Lpg3:\ns_cbranch_execz .Ltr0\nQta 132,61,4384\nQmaaa 220,172,196\nQmaav 216,160,44\n"
"Qta 134,61,4896\nQmaaa 216,164,196\nQmaav 212,152,44\nQta 136,61,264\nQmaaa 212,156,196\nQmaav 208,144,44\nQdv 44,63,38144\nQmaaa 208,148,196\n"
"Qda 196,63,38656\nQmaav 240,144,48\nQta 138,61,776\nQmaaa 240,148,200\nQmaav 244,152,48\nQta 140,61,4392\nQmaaa 244,156,200\nQmaav 248,160,48\n"
"Qta 142,61,4904\nQmaaa 248,164,200\nQmaav 252,168,48\nQdv 48,63,39168\nQmaaa 252,172,200\nQda 200,63,39680\ns_waitcnt lgkmcnt(2)\nQmvav 80,112,24\n"
"Qmvaa 80,116,176\nQmvav 84,120,24\nQmvaa 84,124,176\nQmvav 88,128,24\nQmvaa 88,132,176\nQmvav 92,136,24\nQmvaa 92,140,176\ns_waitcnt lgkmcnt(0)\n"
"Qmvav 124,136,28\ns_waitcnt vmcnt(15)\ns_barrier\nQmvaa 124,140,180\nQta 144,61,16512\ns_mov_b32 s56, s94\ns_mov_b32 s57, s95\ns_mov_b32 s58, s75\n"
"s_mov_b32 s94, 0\ns_add_u32 m0, %10, 160768\nQmvav 120,128,28\nbuffer_load_dwordx4 v60, s[56:59], s94 offen lds\nQmvaa 120,132,180\nQta 146,61,17024\nQmvav 116,120,28\nQmvaa 116,124,180\n"
"Qta 148,61,20640\ns_add_u32 s94, s94, 4096\nQmvav 112,112,28\nbuffer_load_dwordx4 v60, s[56:59], s94 offen lds\nQmvaa 112,116,180\nQta 150,61,21152\nQmvav 144,112,32\nQmvaa 144,116,184\n"
"Qta 152,61,16520\ns_add_u32 s94, s94, 4096\nQmvav 148,120,32\nbuffer_load_dwordx4 v60, s[56:59], s94 offen lds\nQmvaa 148,124,184\nQta 154,61,17032\nQmvav 152,128,32\nQmvaa 152,132,184\n"
"Qta 156,61,20648\ns_add_u32 s94, s94, 4096\nQmvav 156,136,32\nbuffer_load_dwordx4 v60, s[56:59], s94 offen lds\nQmvaa 156,140,184\nQta 158,61,21160\nQmvav 188,136,36\nQmvaa 188,140,188\n"
"Qta 160,61,16768\ns_add_u32 s94, s94, 4096\nQmvav 184,128,36\nbuffer_load_dwordx4 v60, s[56:59], s94 offen lds\nQmvaa 184,132,188\nQta 162,61,17280\nQmvav 180,120,36\nQmvaa 180,124,188\n"
"Qta 164,61,20896\ns_add_u32 s94, s94, 4096\nQmvav 176,112,36\nbuffer_load_dwordx4 v60, s[56:59], s94 offen lds\nQmvaa 176,116,188\nQta 166,61,21408\nQmvav 208,112,40\nQmvaa 208,116,192\n"
"Qta 168,61,16776\ns_add_u32 s94, s94, 4096\nQmvav 212,120,40\nbuffer_load_dwordx4 v60, s[56:59], s94 offen lds\nQmvaa 212,124,192\nQta 170,61,17288\nQmvav 216,128,40\nQmvaa 216,132,192\n"
"Qta 172,61,20904\ns_add_u32 s94, s94, 4096\nQmvav 220,136,40\nbuffer_load_dwordx4 v60, s[56:59], s94 offen lds\nQmvaa 220,140,192\nQta 174,61,21416\nQmvav 252,136,44\nQmvaa 252,140,196\n"
"s_add_u32 s94, s94, 4096\nQmvav 248,128,44\nbuffer_load_dwordx4 v60, s[56:59], s94 offen lds\nQmvaa 248,132,196\nQmvav 244,120,44\nQmvaa 244,124,196\ns_add_u32 s94, s94, 4096\nQmvav 240,112,44\n"
"buffer_load_dwordx4 v60, s[56:59], s94 offen lds\nQmvaa 240,116,196\nQmaav 224,112,48\nQmaaa 224,116,200\nQmaav 228,120,48\nQmaaa 228,124,200\nQmaav 232,128,48\nQmaaa 232,132,200\n"
"Qmaav 236,136,48\nQmaaa 236,140,200\ns_waitcnt lgkmcnt(0)\nQmvav 96,144,24\nQmvaa 96,148,176\nQmvav 100,152,24\nQmvaa 100,156,176\nQmvav 104,160,24\n"
"Qmvaa 104,164,176\nQmvav 108,168,24\nQmvaa 108,172,176\ns_waitcnt lgkmcnt(0)\nQmvav 140,168,28\ns_waitcnt vmcnt(14)\ns_barrier\nQmvaa 140,172,180\n"
"Qdv 24,63,0\ns_add_u32 m0, s61, 33024\nQmvav 136,160,28\nbuffer_load_dwordx4 v69, s[40:43], s60 offen nt lds\nQmvaa 136,164,180\nQda 176,63,512\nQmvav 132,152,28\nQta 112,61,33024\n"
"Qmvaa 132,156,180\nQmvav 128,144,28\nbuffer_load_dwordx4 v69, s[40:43], s60 offen offset:1024 nt lds\nQmvaa 128,148,180\nQdv 28,63,1024\nQmvav 160,144,32\nQda 180,63,1536\nQmvaa 160,148,184\n"
"Qta 114,61,33536\nQmvav 164,152,32\nbuffer_load_dwordx4 v69, s[40:43], s90 offen offset:2048 nt lds\nQmvaa 164,156,184\nQta 116,61,37152\nQmvav 168,160,32\nQmvaa 168,164,184\nQta 118,61,37664\n"
"Qmvav 172,168,32\nbuffer_load_dwordx4 v69, s[40:43], s90 offen offset:3072 nt lds\nQmvaa 172,172,184\nQdv 32,63,2048\nQmvav 204,168,36\nQda 184,63,2560\nQmvaa 204,172,188\nQta 120,61,33032\n"
"s_add_u32 m0, s64, 33024\nQmvav 200,160,36\nbuffer_load_dwordx4 v72, s[44:47], s60 offen nt lds\nQmvaa 200,164,188\nQta 122,61,33544\nQmvav 196,152,36\nQmvaa 196,156,188\nQta 124,61,37160\n"
"Qmvav 192,144,36\nbuffer_load_dwordx4 v72, s[44:47], s60 offen offset:1024 nt lds\nQmvaa 192,148,188\nQdv 36,63,3072\nQmvav 224,144,40\nQda 188,63,3584\nQmvaa 224,148,192\nQta 126,61,37672\n"
"Qmvav 228,152,40\nbuffer_load_dwordx4 v72, s[44:47], s90 offen offset:2048 nt lds\nQmvaa 228,156,192\nQta 128,61,33280\nQmvav 232,160,40\nQmvaa 232,164,192\nQta 130,61,33792\nQmvav 236,168,40\n"
"buffer_load_dwordx4 v72, s[44:47], s90 offen offset:3072 nt lds\ns_add_u32 s65, s65, 2048\ns_xor_b32 s60, s65, %21\ns_add_u32 s90, s60, 260096\nQmvaa 236,172,192\nQdv 40,63,4096\nQmaav 220,168,44\nQda 192,63,4608\n"
"Qmaaa 220,172,196\nQta 132,61,37408\nQmaav 216,160,44\nQmaaa 216,164,196\nQta 134,61,37920\nQmaav 212,152,44\nQmaaa 212,156,196\nQta 136,61,33288\n"
"Qmaav 208,144,44\nQdv 44,63,5120\nQmaaa 208,148,196\nQda 196,63,5632\nQmaav 240,144,48\nQta 138,61,33800\nQmaaa 240,148,200\nQmaav 244,152,48\n"
"Qta 140,61,37416\nQmaaa 244,156,200\nQmaav 248,160,48\nQta 142,61,37928\nQmaaa 248,164,200\nQmaav 252,168,48\nQdv 48,63,6144\nQmaaa 252,172,200\n"
"Qda 200,63,6656\ns_waitcnt lgkmcnt(2)\nQmvav 80,112,24\nQmvaa 80,116,176\nQmvav 84,120,24\nQmvaa 84,124,176\nQmvav 88,128,24\nQmvaa 88,132,176\n"
"Qmvav 92,136,24\nQmvaa 92,140,176\ns_waitcnt lgkmcnt(0)\nQmvav 124,136,28\ns_waitcnt vmcnt(18)\ns_barrier\nQmvaa 124,140,180\nQta 144,61,49536\n"
"Qmvav 120,128,28\nQmvaa 120,132,180\nQta 146,61,50048\nQmvav 116,120,28\nQmvaa 116,124,180\nQta 148,61,53664\nQmvav 112,112,28\nQmvaa 112,116,180\n"
"Qta 150,61,54176\nQmvav 144,112,32\nQmvaa 144,116,184\nQta 152,61,49544\nQmvav 148,120,32\nQmvaa 148,124,184\nQta 154,61,50056\nQmvav 152,128,32\n"
"Qmvaa 152,132,184\nQta 156,61,53672\nQmvav 156,136,32\nQmvaa 156,140,184\nQta 158,61,54184\nQmvav 188,136,36\nQmvaa 188,140,188\nQta 160,61,49792\n"
"Qmvav 184,128,36\nQmvaa 184,132,188\nQta 162,61,50304\nQmvav 180,120,36\nQmvaa 180,124,188\nQta 164,61,53920\nQmvav 176,112,36\nQmvaa 176,116,188\n"
"Qta 166,61,54432\nQmvav 208,112,40\nQmvaa 208,116,192\nQta 168,61,49800\nQmvav 212,120,40\nQmvaa 212,124,192\nQta 170,61,50312\nQmvav 216,128,40\n"
"Qmvaa 216,132,192\nQta 172,61,53928\nQmvav 220,136,40\nQmvaa 220,140,192\nQta 174,61,54440\nQmvav 252,136,44\nQmvaa 252,140,196\nQmvav 248,128,44\n"
"Qmvaa 248,132,196\nQmvav 244,120,44\nQmvaa 244,124,196\nQmvav 240,112,44\nQmvaa 240,116,196\nQmaav 224,112,48\nQmaaa 224,116,200\nQmaav 228,120,48\n"
"Qmaaa 228,124,200\nQmaav 232,128,48\nQmaaa 232,132,200\nQmaav 236,136,48\nQmaaa 236,140,200\ns_waitcnt lgkmcnt(0)\nQmvav 96,144,24\nQmvaa 96,148,176\n"
"Qmvav 100,152,24\nQmvaa 100,156,176\nQmvav 104,160,24\nQmvaa 104,164,176\nQmvav 108,168,24\nQmvaa 108,172,176\ns_waitcnt lgkmcnt(0)\nQmvav 140,168,28\n"
"s_waitcnt vmcnt(18)\ns_barrier\nQmvaa 140,172,180\nQda 112,68,0\ns_add_u32 m0, s61, 66048\nv_accvgpr_write_b32 a176, 0\nQmvav 136,160,28\nbuffer_load_dwordx4 v69, s[40:43], s60 offen nt lds\n"
"Qmvaa 136,164,180\nQda 116,68,512\nv_accvgpr_write_b32 a177, 0\nQmvav 132,152,28\nQmvaa 132,156,180\nQda 120,68,256\nv_accvgpr_write_b32 a178, 0\nQmvav 128,144,28\n"
"buffer_load_dwordx4 v69, s[40:43], s60 offen offset:1024 nt lds\nQmvaa 128,148,180\nQda 124,68,768\nv_accvgpr_write_b32 a179, 0\nQmvav 160,144,32\nQmvaa 160,148,184\nQda 128,68,1024\nv_accvgpr_write_b32 a180, 0\n"
"Qmvav 164,152,32\nbuffer_load_dwordx4 v69, s[40:43], s90 offen offset:2048 nt lds\nQmvaa 164,156,184\nQda 132,68,1536\nv_accvgpr_write_b32 a181, 0\nQmvav 168,160,32\nQmvaa 168,164,184\nQda 136,68,1280\n"
"v_accvgpr_write_b32 a182, 0\nQmvav 172,168,32\nbuffer_load_dwordx4 v69, s[40:43], s90 offen offset:3072 nt lds\nQmvaa 172,172,184\nQda 140,68,1792\nv_accvgpr_write_b32 a183, 0\nQmvav 204,168,36\nQmvaa 204,172,188\n"
"s_add_u32 m0, s64, 66048\nv_accvgpr_write_b32 a184, 0\nQmvav 200,160,36\nbuffer_load_dwordx4 v72, s[44:47], s60 offen nt lds\nQmvaa 200,164,188\nv_accvgpr_write_b32 a185, 0\nQmvav 196,152,36\nQmvaa 196,156,188\n"
"v_accvgpr_write_b32 a186, 0\nQmvav 192,144,36\nbuffer_load_dwordx4 v72, s[44:47], s60 offen offset:1024 nt lds\nQmvaa 192,148,188\nv_accvgpr_write_b32 a187, 0\nQmvav 224,144,40\nQmvaa 224,148,192\nv_accvgpr_write_b32 a188, 0\n"
"Qmvav 228,152,40\nbuffer_load_dwordx4 v72, s[44:47], s90 offen offset:2048 nt lds\nQmvaa 228,156,192\nv_accvgpr_write_b32 a189, 0\nQmvav 232,160,40\nQmvaa 232,164,192\nv_accvgpr_write_b32 a190, 0\nQmvav 236,168,40\n"
"buffer_load_dwordx4 v72, s[44:47], s90 offen offset:3072 nt lds\ns_add_u32 s65, s65, 2048\ns_xor_b32 s60, s65, %21\ns_add_u32 s90, s60, 260096\nQmvaa 236,172,192\nv_accvgpr_write_b32 a191, 0\nQmaav 220,168,44\nQmaaa 220,172,196\n"
"v_accvgpr_write_b32 a192, 0\nQmaav 216,160,44\nQmaaa 216,164,196\nv_accvgpr_write_b32 a193, 0\nQmaav 212,152,44\nQmaaa 212,156,196\nv_accvgpr_write_b32 a194, 0\nQmaav 208,144,44\n"
"Qmaaa 208,148,196\nv_accvgpr_write_b32 a195, 0\nQmaav 240,144,48\nQmaaa 240,148,200\nv_accvgpr_write_b32 a196, 0\nQmaav 244,152,48\nQmaaa 244,156,200\nv_accvgpr_write_b32 a197, 0\n"
"Qmaav 248,160,48\nQmaaa 248,164,200\nv_accvgpr_write_b32 a198, 0\nQmaav 252,168,48\nQmaaa 252,172,200\nv_accvgpr_write_b32 a199, 0\nQta 148,70,128\nQta 150,70,1152\n"
"Qta 156,70,384\nQta 158,70,1408\nQta 164,70,640\nQta 166,70,1664\nQta 172,70,896\nQta 174,70,1920\nv_pk_fma_f32 v[80:81], v[80:81], s[72:73], v[52:53] op_sel_hi:[1,0,0]\nv_pk_fma_f32 v[82:83], v[82:83], s[72:73], v[52:53] op_sel_hi:[1,0,0]\n"
"v_pk_fma_f32 v[84:85], v[84:85], s[72:73], v[52:53] op_sel_hi:[1,0,0]\nv_pk_fma_f32 v[86:87], v[86:87], s[72:73], v[52:53] op_sel_hi:[1,0,0]\nQk 80,80,81\nQk 81,82,83\nQk 82,84,85\nQk 83,86,87\nv_accvgpr_write_b32 a0, v80\nv_accvgpr_write_b32 a1, v81\n"
"v_accvgpr_write_b32 a2, v82\nv_accvgpr_write_b32 a3, v83\nv_pk_fma_f32 v[88:89], v[88:89], s[72:73], v[52:53] op_sel_hi:[1,0,0]\nv_pk_fma_f32 v[90:91], v[90:91], s[72:73], v[52:53] op_sel_hi:[1,0,0]\nv_pk_fma_f32 v[92:93], v[92:93], s[72:73], v[52:53] op_sel_hi:[1,0,0]\nv_pk_fma_f32 v[94:95], v[94:95], s[72:73], v[52:53] op_sel_hi:[1,0,0]\nQk 88,88,89\nQk 89,90,91\n"
"Qk 90,92,93\nQk 91,94,95\nv_accvgpr_write_b32 a4, v88\nv_accvgpr_write_b32 a5, v89\nv_accvgpr_write_b32 a6, v90\nv_accvgpr_write_b32 a7, v91\nv_pk_fma_f32 v[96:97], v[96:97], s[72:73], v[52:53] op_sel_hi:[1,0,0]\nv_pk_fma_f32 v[98:99], v[98:99], s[72:73], v[52:53] op_sel_hi:[1,0,0]\n"
"v_pk_fma_f32 v[100:101], v[100:101], s[72:73], v[52:53] op_sel_hi:[1,0,0]\nv_pk_fma_f32 v[102:103], v[102:103], s[72:73], v[52:53] op_sel_hi:[1,0,0]\nQk 96,96,97\nQk 97,98,99\nQk 98,100,101\nQk 99,102,103\nv_accvgpr_write_b32 a8, v96\nv_accvgpr_write_b32 a9, v97\n"
"v_accvgpr_write_b32 a10, v98\ns_waitcnt lgkmcnt(6)\nv_accvgpr_write_b32 a11, v99\nv_pk_fma_f32 v[104:105], v[104:105], s[72:73], v[52:53] op_sel_hi:[1,0,0]\nv_accvgpr_read_b32 v66, a148\nv_pk_fma_f32 v[106:107], v[106:107], s[72:73], v[52:53] op_sel_hi:[1,0,0]\nv_pk_fma_f32 v[108:109], v[108:109], s[72:73], v[52:53] op_sel_hi:[1,0,0]\nv_pk_fma_f32 v[110:111], v[110:111], s[72:73], v[52:53] op_sel_hi:[1,0,0]\n"
"v_accvgpr_read_b32 v67, a150\nQk 104,104,105\nQk 105,106,107\nQk 106,108,109\nv_max_f32 v66, |v66|, |v67|\nQk 107,110,111\nv_accvgpr_write_b32 a12, v104\nv_accvgpr_write_b32 a13, v105\n"
"v_mov_b32 v67, v66\nv_accvgpr_write_b32 a14, v106\nv_accvgpr_write_b32 a15, v107\nv_pk_fma_f32 v[112:113], v[112:113], s[72:73], v[52:53] op_sel_hi:[1,0,0]\nv_pk_fma_f32 v[114:115], v[114:115], s[72:73], v[52:53] op_sel_hi:[1,0,0]\nv_pk_fma_f32 v[116:117], v[116:117], s[72:73], v[52:53] op_sel_hi:[1,0,0]\nv_permlane32_swap_b32 v66, v67\nv_pk_fma_f32 v[118:119], v[118:119], s[72:73], v[52:53] op_sel_hi:[1,0,0]\n"
"Qk 112,112,113\nQk 113,114,115\nv_max_f32 v66, v66, v67\nQk 114,116,117\nQk 115,118,119\nv_accvgpr_write_b32 a16, v112\nv_mov_b32 v67, v66\nv_accvgpr_write_b32 a17, v113\n"
"v_accvgpr_write_b32 a18, v114\nv_accvgpr_write_b32 a19, v115\nv_permlane16_swap_b32 v66, v67\nv_pk_fma_f32 v[120:121], v[120:121], s[72:73], v[52:53] op_sel_hi:[1,0,0]\nv_pk_fma_f32 v[122:123], v[122:123], s[72:73], v[52:53] op_sel_hi:[1,0,0]\nv_max_f32 v66, v66, v67\nv_pk_fma_f32 v[124:125], v[124:125], s[72:73], v[52:53] op_sel_hi:[1,0,0]\nv_pk_fma_f32 v[126:127], v[126:127], s[72:73], v[52:53] op_sel_hi:[1,0,0]\n"
"Qk 120,120,121\nv_min_f32_dpp v66, v66, v66 row_ror:8 row_mask:0xf bank_mask:0xf\nQk 121,122,123\nQk 122,124,125\nQk 123,126,127\nv_min_f32_dpp v66, v66, v66 row_ror:4 row_mask:0xf bank_mask:0xf\nv_accvgpr_write_b32 a20, v120\nv_accvgpr_write_b32 a21, v121\n"
"v_accvgpr_write_b32 a22, v122\nv_min_f32_dpp v66, v66, v66 row_ror:2 row_mask:0xf bank_mask:0xf\nv_accvgpr_write_b32 a23, v123\nv_pk_fma_f32 v[128:129], v[128:129], s[72:73], v[52:53] op_sel_hi:[1,0,0]\nv_min_f32_dpp v66, v66, v66 row_ror:1 row_mask:0xf bank_mask:0xf\nv_pk_fma_f32 v[130:131], v[130:131], s[72:73], v[52:53] op_sel_hi:[1,0,0]\nv_pk_fma_f32 v[132:133], v[132:133], s[72:73], v[52:53] op_sel_hi:[1,0,0]\nv_pk_fma_f32 v[134:135], v[134:135], s[72:73], v[52:53] op_sel_hi:[1,0,0]\n"
"v_and_b32 v66, 0x7f000000, v66\nQk 128,128,129\nQk 129,130,131\nQk 130,132,133\nv_or_b32 v66, 0x400000, v66\nQk 131,134,135\nv_accvgpr_write_b32 a24, v128\nv_accvgpr_write_b32 a25, v129\n"
"v_mov_b32 v67, 0\nv_accvgpr_write_b32 a26, v130\nv_accvgpr_write_b32 a27, v131\nv_pk_fma_f32 v[136:137], v[136:137], s[72:73], v[52:53] op_sel_hi:[1,0,0]\nv_pk_fma_f32 v[138:139], v[138:139], s[72:73], v[52:53] op_sel_hi:[1,0,0]\nv_pk_fma_f32 v[140:141], v[140:141], s[72:73], v[52:53] op_sel_hi:[1,0,0]\nv_mov_b32 v58, 0x39800000\nv_pk_fma_f32 v[142:143], v[142:143], s[72:73], v[52:53] op_sel_hi:[1,0,0]\n"
"Qk 136,136,137\nQk 137,138,139\nv_mov_b32 v59, 0\nQk 138,140,141\n.Lpg4:\ns_cbranch_execz .Ltr9\nQk 139,142,143\nv_accvgpr_write_b32 a28, v136\n"
"v_readfirstlane_b32 s73, v66\nv_accvgpr_write_b32 a29, v137\nv_accvgpr_write_b32 a30, v138\nv_accvgpr_write_b32 a31, v139\nv_pk_fma_f32 v[144:145], v[144:145], s[72:73], v[52:53] op_sel_hi:[1,0,0]\nv_pk_fma_f32 v[146:147], v[146:147], s[72:73], v[52:53] op_sel_hi:[1,0,0]\nv_pk_fma_f32 v[148:149], v[148:149], s[72:73], v[52:53] op_sel_hi:[1,0,0]\nv_pk_fma_f32 v[150:151], v[150:151], s[72:73], v[52:53] op_sel_hi:[1,0,0]\n"
"v_mfma_f32_16x16x4_4b_bf16 v[80:95], v[66:67], v[58:59], 0\nQk 144,144,145\nQk 145,146,147\nQk 146,148,149\nQk 147,150,151\nv_accvgpr_write_b32 a32, v144\nv_accvgpr_write_b32 a33, v145\nv_accvgpr_write_b32 a34, v146\n"
"v_accvgpr_write_b32 a35, v147\nv_pk_fma_f32 v[152:153], v[152:153], s[72:73], v[52:53] op_sel_hi:[1,0,0]\nv_pk_fma_f32 v[154:155], v[154:155], s[72:73], v[52:53] op_sel_hi:[1,0,0]\nv_pk_fma_f32 v[156:157], v[156:157], s[72:73], v[52:53] op_sel_hi:[1,0,0]\nv_pk_fma_f32 v[158:159], v[158:159], s[72:73], v[52:53] op_sel_hi:[1,0,0]\nv_mfma_f32_16x16x4_4b_bf16 v[96:111], v[66:67], v[58:59], 0\nQk 152,152,153\nQk 153,154,155\n"
"Qk 154,156,157\nQk 155,158,159\nv_accvgpr_write_b32 a36, v152\nv_accvgpr_write_b32 a37, v153\nv_accvgpr_write_b32 a38, v154\nv_accvgpr_write_b32 a39, v155\nv_pk_fma_f32 v[160:161], v[160:161], s[72:73], v[52:53] op_sel_hi:[1,0,0]\nv_pk_fma_f32 v[162:163], v[162:163], s[72:73], v[52:53] op_sel_hi:[1,0,0]\n"
"v_pk_fma_f32 v[164:165], v[164:165], s[72:73], v[52:53] op_sel_hi:[1,0,0]\nv_pk_fma_f32 v[166:167], v[166:167], s[72:73], v[52:53] op_sel_hi:[1,0,0]\nv_mfma_f32_16x16x4_4b_bf16 v[112:127], v[66:67], v[58:59], 0\nQk 160,160,161\nQk 161,162,163\nQk 162,164,165\nQk 163,166,167\nv_accvgpr_write_b32 a40, v160\n"
"v_accvgpr_write_b32 a41, v161\nv_accvgpr_write_b32 a42, v162\nv_accvgpr_write_b32 a43, v163\nv_pk_fma_f32 v[168:169], v[168:169], s[72:73], v[52:53] op_sel_hi:[1,0,0]\nv_pk_fma_f32 v[170:171], v[170:171], s[72:73], v[52:53] op_sel_hi:[1,0,0]\nv_pk_fma_f32 v[172:173], v[172:173], s[72:73], v[52:53] op_sel_hi:[1,0,0]\nv_pk_fma_f32 v[174:175], v[174:175], s[72:73], v[52:53] op_sel_hi:[1,0,0]\nv_mfma_f32_16x16x4_4b_bf16 v[128:143], v[66:67], v[58:59], 0\n"
"Qk 168,168,169\nQk 169,170,171\nQk 170,172,173\nQk 171,174,175\nv_accvgpr_write_b32 a44, v168\nv_accvgpr_write_b32 a45, v169\nv_accvgpr_write_b32 a46, v170\nv_accvgpr_write_b32 a47, v171\n"
"v_pk_fma_f32 v[176:177], v[176:177], s[72:73], v[52:53] op_sel_hi:[1,0,0]\nv_pk_fma_f32 v[178:179], v[178:179], s[72:73], v[52:53] op_sel_hi:[1,0,0]\nv_pk_fma_f32 v[180:181], v[180:181], s[72:73], v[52:53] op_sel_hi:[1,0,0]\nv_pk_fma_f32 v[182:183], v[182:183], s[72:73], v[52:53] op_sel_hi:[1,0,0]\nv_mfma_f32_16x16x4_4b_bf16 v[144:159], v[66:67], v[58:59], 0\nQk 176,176,177\nQk 177,178,179\nQk 178,180,181\n"
"Qk 179,182,183\nv_accvgpr_write_b32 a48, v176\nv_accvgpr_write_b32 a49, v177\nv_accvgpr_write_b32 a50, v178\nv_accvgpr_write_b32 a51, v179\nv_pk_fma_f32 v[184:185], v[184:185], s[72:73], v[52:53] op_sel_hi:[1,0,0]\nv_pk_fma_f32 v[186:187], v[186:187], s[72:73], v[52:53] op_sel_hi:[1,0,0]\nv_pk_fma_f32 v[188:189], v[188:189], s[72:73], v[52:53] op_sel_hi:[1,0,0]\n"
"v_pk_fma_f32 v[190:191], v[190:191], s[72:73], v[52:53] op_sel_hi:[1,0,0]\nv_mfma_f32_16x16x4_4b_bf16 v[160:175], v[66:67], v[58:59], 0\nQk 184,184,185\nQk 185,186,187\nQk 186,188,189\nQk 187,190,191\nv_accvgpr_write_b32 a52, v184\nv_accvgpr_write_b32 a53, v185\n"
"v_accvgpr_write_b32 a54, v186\nv_accvgpr_write_b32 a55, v187\nv_pk_fma_f32 v[192:193], v[192:193], s[72:73], v[52:53] op_sel_hi:[1,0,0]\nv_pk_fma_f32 v[194:195], v[194:195], s[72:73], v[52:53] op_sel_hi:[1,0,0]\nv_pk_fma_f32 v[196:197], v[196:197], s[72:73], v[52:53] op_sel_hi:[1,0,0]\nv_pk_fma_f32 v[198:199], v[198:199], s[72:73], v[52:53] op_sel_hi:[1,0,0]\nv_mfma_f32_16x16x4_4b_bf16 v[176:191], v[66:67], v[58:59], 0\nQk 192,192,193\n"
"Qk 193,194,195\nQk 194,196,197\nQk 195,198,199\nv_accvgpr_write_b32 a56, v192\nv_accvgpr_write_b32 a57, v193\nv_accvgpr_write_b32 a58, v194\nv_accvgpr_write_b32 a59, v195\nv_pk_fma_f32 v[200:201], v[200:201], s[72:73], v[52:53] op_sel_hi:[1,0,0]\n"
"v_pk_fma_f32 v[202:203], v[202:203], s[72:73], v[52:53] op_sel_hi:[1,0,0]\nv_pk_fma_f32 v[204:205], v[204:205], s[72:73], v[52:53] op_sel_hi:[1,0,0]\nv_pk_fma_f32 v[206:207], v[206:207], s[72:73], v[52:53] op_sel_hi:[1,0,0]\nQk 200,200,201\nQk 201,202,203\nQk 202,204,205\nQk 203,206,207\nv_accvgpr_write_b32 a60, v200\n"
"v_accvgpr_write_b32 a61, v201\nv_accvgpr_write_b32 a62, v202\nv_accvgpr_write_b32 a63, v203\nv_pk_fma_f32 v[208:209], v[208:209], s[72:73], v[52:53] op_sel_hi:[1,0,0]\nv_pk_fma_f32 v[210:211], v[210:211], s[72:73], v[52:53] op_sel_hi:[1,0,0]\nv_pk_fma_f32 v[212:213], v[212:213], s[72:73], v[52:53] op_sel_hi:[1,0,0]\nv_pk_fma_f32 v[214:215], v[214:215], s[72:73], v[52:53] op_sel_hi:[1,0,0]\nv_mfma_f32_16x16x4_4b_bf16 v[192:207], v[66:67], v[58:59], 0\n"
"Qk 208,208,209\nQk 209,210,211\nQk 210,212,213\nQk 211,214,215\nv_accvgpr_write_b32 a64, v208\nv_accvgpr_write_b32 a65, v209\nv_accvgpr_write_b32 a66, v210\nv_accvgpr_write_b32 a67, v211\n"
"v_pk_fma_f32 v[216:217], v[216:217], s[72:73], v[52:53] op_sel_hi:[1,0,0]\nv_pk_fma_f32 v[218:219], v[218:219], s[72:73], v[52:53] op_sel_hi:[1,0,0]\nv_pk_fma_f32 v[220:221], v[220:221], s[72:73], v[52:53] op_sel_hi:[1,0,0]\nv_pk_fma_f32 v[222:223], v[222:223], s[72:73], v[52:53] op_sel_hi:[1,0,0]\nQk 216,216,217\nQk 217,218,219\nQk 218,220,221\nQk 219,222,223\n"
"v_accvgpr_write_b32 a68, v216\nv_accvgpr_write_b32 a69, v217\nv_accvgpr_write_b32 a70, v218\nv_accvgpr_write_b32 a71, v219\nv_pk_fma_f32 v[224:225], v[224:225], s[72:73], v[52:53] op_sel_hi:[1,0,0]\nv_pk_fma_f32 v[226:227], v[226:227], s[72:73], v[52:53] op_sel_hi:[1,0,0]\nv_pk_fma_f32 v[228:229], v[228:229], s[72:73], v[52:53] op_sel_hi:[1,0,0]\nv_pk_fma_f32 v[230:231], v[230:231], s[72:73], v[52:53] op_sel_hi:[1,0,0]\n"
"v_mfma_f32_16x16x4_4b_bf16 v[208:223], v[66:67], v[58:59], 0\nQk 224,224,225\nQk 225,226,227\nQk 226,228,229\nQk 227,230,231\nv_accvgpr_write_b32 a72, v224\nv_accvgpr_write_b32 a73, v225\nv_accvgpr_write_b32 a74, v226\n"
"v_accvgpr_write_b32 a75, v227\nv_pk_fma_f32 v[232:233], v[232:233], s[72:73], v[52:53] op_sel_hi:[1,0,0]\nv_pk_fma_f32 v[234:235], v[234:235], s[72:73], v[52:53] op_sel_hi:[1,0,0]\nv_pk_fma_f32 v[236:237], v[236:237], s[72:73], v[52:53] op_sel_hi:[1,0,0]\nv_pk_fma_f32 v[238:239], v[238:239], s[72:73], v[52:53] op_sel_hi:[1,0,0]\nQk 232,232,233\nQk 233,234,235\nQk 234,236,237\n"
"Qk 235,238,239\nv_accvgpr_write_b32 a76, v232\nv_accvgpr_write_b32 a77, v233\nv_accvgpr_write_b32 a78, v234\nv_accvgpr_write_b32 a79, v235\nv_pk_fma_f32 v[240:241], v[240:241], s[72:73], v[52:53] op_sel_hi:[1,0,0]\nv_pk_fma_f32 v[242:243], v[242:243], s[72:73], v[52:53] op_sel_hi:[1,0,0]\nv_pk_fma_f32 v[244:245], v[244:245], s[72:73], v[52:53] op_sel_hi:[1,0,0]\n"
"v_pk_fma_f32 v[246:247], v[246:247], s[72:73], v[52:53] op_sel_hi:[1,0,0]\nv_mfma_f32_16x16x4_4b_bf16 v[224:239], v[66:67], v[58:59], 0\nQk 240,240,241\nQk 241,242,243\nQk 242,244,245\nQk 243,246,247\nv_accvgpr_write_b32 a80, v240\nv_accvgpr_write_b32 a81, v241\n"
"v_accvgpr_write_b32 a82, v242\nv_accvgpr_write_b32 a83, v243\nv_pk_fma_f32 v[248:249], v[248:249], s[72:73], v[52:53] op_sel_hi:[1,0,0]\nv_pk_fma_f32 v[250:251], v[250:251], s[72:73], v[52:53] op_sel_hi:[1,0,0]\nv_pk_fma_f32 v[252:253], v[252:253], s[72:73], v[52:53] op_sel_hi:[1,0,0]\nv_pk_fma_f32 v[254:255], v[254:255], s[72:73], v[52:53] op_sel_hi:[1,0,0]\nQk 248,248,249\nQk 249,250,251\n"
"Qk 250,252,253\nQk 251,254,255\nv_accvgpr_write_b32 a84, v248\nv_accvgpr_write_b32 a85, v249\nv_accvgpr_write_b32 a86, v250\nv_accvgpr_write_b32 a87, v251\nv_accvgpr_read_b32 v0, a208\nv_accvgpr_read_b32 v1, a209\n"
"v_accvgpr_read_b32 v2, a210\nv_accvgpr_read_b32 v3, a211\nv_accvgpr_read_b32 v4, a212\nv_accvgpr_read_b32 v5, a213\nv_accvgpr_read_b32 v6, a214\nv_accvgpr_read_b32 v7, a215\nv_pk_fma_f32 v[0:1], v[0:1], s[72:73], v[52:53] op_sel_hi:[1,0,0]\nv_pk_fma_f32 v[2:3], v[2:3], s[72:73], v[52:53] op_sel_hi:[1,0,0]\n"
"v_pk_fma_f32 v[4:5], v[4:5], s[72:73], v[52:53] op_sel_hi:[1,0,0]\nv_pk_fma_f32 v[6:7], v[6:7], s[72:73], v[52:53] op_sel_hi:[1,0,0]\nv_mfma_f32_16x16x4_4b_bf16 v[240:255], v[66:67], v[58:59], 0\nQk 0,0,1\nQk 1,2,3\nQk 2,4,5\nQk 3,6,7\nv_accvgpr_write_b32 a88, v0\n"
"v_accvgpr_write_b32 a89, v1\nv_accvgpr_write_b32 a90, v2\nv_accvgpr_write_b32 a91, v3\nv_accvgpr_read_b32 v8, a216\nv_accvgpr_read_b32 v9, a217\nv_accvgpr_read_b32 v10, a218\nv_accvgpr_read_b32 v11, a219\nv_accvgpr_read_b32 v12, a220\n"
"v_accvgpr_read_b32 v13, a221\nv_accvgpr_read_b32 v14, a222\nv_accvgpr_read_b32 v15, a223\nv_pk_fma_f32 v[8:9], v[8:9], s[72:73], v[52:53] op_sel_hi:[1,0,0]\nv_pk_fma_f32 v[10:11], v[10:11], s[72:73], v[52:53] op_sel_hi:[1,0,0]\nv_pk_fma_f32 v[12:13], v[12:13], s[72:73], v[52:53] op_sel_hi:[1,0,0]\nv_pk_fma_f32 v[14:15], v[14:15], s[72:73], v[52:53] op_sel_hi:[1,0,0]\nv_mfma_f32_16x16x4_4b_bf16 a[208:223], v[66:67], v[58:59], 0\n"
"Qk 8,8,9\nQk 9,10,11\nQk 10,12,13\nQk 11,14,15\nv_accvgpr_write_b32 a92, v8\nv_accvgpr_write_b32 a93, v9\nv_accvgpr_write_b32 a94, v10\nv_accvgpr_write_b32 a95, v11\n"
"v_accvgpr_read_b32 v0, a224\nv_accvgpr_read_b32 v1, a225\nv_accvgpr_read_b32 v2, a226\nv_accvgpr_read_b32 v3, a227\nv_accvgpr_read_b32 v4, a228\nv_accvgpr_read_b32 v5, a229\nv_accvgpr_read_b32 v6, a230\nv_accvgpr_read_b32 v7, a231\n"
"v_pk_fma_f32 v[0:1], v[0:1], s[72:73], v[52:53] op_sel_hi:[1,0,0]\nv_pk_fma_f32 v[2:3], v[2:3], s[72:73], v[52:53] op_sel_hi:[1,0,0]\nv_pk_fma_f32 v[4:5], v[4:5], s[72:73], v[52:53] op_sel_hi:[1,0,0]\nv_pk_fma_f32 v[6:7], v[6:7], s[72:73], v[52:53] op_sel_hi:[1,0,0]\nQk 0,0,1\nQk 1,2,3\nQk 2,4,5\nQk 3,6,7\n"
"v_accvgpr_write_b32 a96, v0\nv_accvgpr_write_b32 a97, v1\nv_accvgpr_write_b32 a98, v2\nv_accvgpr_write_b32 a99, v3\nv_accvgpr_read_b32 v8, a232\nv_accvgpr_read_b32 v9, a233\nv_accvgpr_read_b32 v10, a234\nv_accvgpr_read_b32 v11, a235\n"
"v_accvgpr_read_b32 v12, a236\nv_accvgpr_read_b32 v13, a237\nv_accvgpr_read_b32 v14, a238\nv_accvgpr_read_b32 v15, a239\nv_pk_fma_f32 v[8:9], v[8:9], s[72:73], v[52:53] op_sel_hi:[1,0,0]\nv_pk_fma_f32 v[10:11], v[10:11], s[72:73], v[52:53] op_sel_hi:[1,0,0]\nv_pk_fma_f32 v[12:13], v[12:13], s[72:73], v[52:53] op_sel_hi:[1,0,0]\nv_pk_fma_f32 v[14:15], v[14:15], s[72:73], v[52:53] op_sel_hi:[1,0,0]\n"
"v_mfma_f32_16x16x4_4b_bf16 a[224:239], v[66:67], v[58:59], 0\nQk 8,8,9\nQk 9,10,11\nQk 10,12,13\nQk 11,14,15\nv_accvgpr_write_b32 a100, v8\nv_accvgpr_write_b32 a101, v9\nv_accvgpr_write_b32 a102, v10\n"
"v_accvgpr_write_b32 a103, v11\nv_accvgpr_read_b32 v0, a240\nv_accvgpr_read_b32 v1, a241\nv_accvgpr_read_b32 v2, a242\nv_accvgpr_read_b32 v3, a243\nv_accvgpr_read_b32 v4, a244\nv_accvgpr_read_b32 v5, a245\nv_accvgpr_read_b32 v6, a246\n"
"v_accvgpr_read_b32 v7, a247\nv_pk_fma_f32 v[0:1], v[0:1], s[72:73], v[52:53] op_sel_hi:[1,0,0]\nv_pk_fma_f32 v[2:3], v[2:3], s[72:73], v[52:53] op_sel_hi:[1,0,0]\nv_pk_fma_f32 v[4:5], v[4:5], s[72:73], v[52:53] op_sel_hi:[1,0,0]\nv_pk_fma_f32 v[6:7], v[6:7], s[72:73], v[52:53] op_sel_hi:[1,0,0]\nQk 0,0,1\nQk 1,2,3\nQk 2,4,5\n"
"Qk 3,6,7\nv_accvgpr_write_b32 a104, v0\nv_accvgpr_write_b32 a105, v1\nv_accvgpr_write_b32 a106, v2\nv_accvgpr_write_b32 a107, v3\nv_accvgpr_read_b32 v8, a248\nv_accvgpr_read_b32 v9, a249\nv_accvgpr_read_b32 v10, a250\n"
"v_accvgpr_read_b32 v11, a251\nv_accvgpr_read_b32 v12, a252\nv_accvgpr_read_b32 v13, a253\nv_accvgpr_read_b32 v14, a254\nv_accvgpr_read_b32 v15, a255\nv_pk_fma_f32 v[8:9], v[8:9], s[72:73], v[52:53] op_sel_hi:[1,0,0]\nv_pk_fma_f32 v[10:11], v[10:11], s[72:73], v[52:53] op_sel_hi:[1,0,0]\nv_pk_fma_f32 v[12:13], v[12:13], s[72:73], v[52:53] op_sel_hi:[1,0,0]\n"
"v_pk_fma_f32 v[14:15], v[14:15], s[72:73], v[52:53] op_sel_hi:[1,0,0]\nv_mfma_f32_16x16x4_4b_bf16 a[240:255], v[66:67], v[58:59], 0\nQk 8,8,9\nQk 9,10,11\nQk 10,12,13\nQk 11,14,15\nv_accvgpr_write_b32 a108, v8\nv_accvgpr_write_b32 a109, v9\n"
"v_accvgpr_write_b32 a110, v10\nv_accvgpr_write_b32 a111, v11\nv_mov_b32 v66, 0x3f803f80\nv_accvgpr_write_b32 a200, 0\nv_accvgpr_write_b32 a201, 0\nv_accvgpr_write_b32 a202, 0\nv_accvgpr_write_b32 a203, 0\ns_mov_b32 vcc_lo, 0x33333333\n"
"s_mov_b32 vcc_hi, 0xcccccccc\nv_cndmask_b32 v66, 0, v66, vcc\nv_accvgpr_write_b32 a204, v66\nv_accvgpr_write_b32 a205, v66\nv_accvgpr_write_b32 a206, v66\nv_accvgpr_write_b32 a207, v66\ns_add_u32 s87, s87, 2048\ns_waitcnt lgkmcnt(8)\n"
"Qzvaa 0,112,0\nQmvaa 0,116,4\ns_memrealtime s[74:75]\ns_memtime s[68:69]\nQmvaa 0,120,8\nQmvaa 0,124,12\nQzvaa 4,140,12\nQmvaa 4,136,8\n"
"Qmvaa 4,132,4\nQmvaa 4,128,0\ns_nop 3\nv_max3_f32 v66, v0, v1, v2\ns_nop 2\nv_max3_f32 v66, v66, v3, v4\nv_max3_f32 v66, v66, v5, v6\nv_max_f32 v66, v66, v7\n"
"Qzvaa 8,112,16\ns_waitcnt lgkmcnt(0)\nv_mov_b32 v67, v66\nQmvaa 8,116,20\ns_sub_u32 s68, s68, s80\ns_sub_u32 s69, s74, s82\nQmvaa 8,120,24\ns_mul_i32 s69, s69, 73\n"
"s_lshl_b32 s68, s68, 2\nQmvaa 8,124,28\ns_sub_u32 s75, s67, 1\ns_cmp_gt_u32 s68, s69\ns_cselect_b32 s68, s75, 0\ns_cselect_b32 s31, 4, 0\ns_add_u32 s31, s31, 240\nQzvaa 12,140,28\n"
"s_and_b32 s75, s68, 25\ns_add_u32 s77, s74, 60\nQmvaa 12,136,24\ns_add_u32 s77, s77, s75\nv_permlane32_swap_b32 v66, v67\nQmvaa 12,132,20\nv_max_f32 v66, v66, v67\nv_mov_b32 v67, v66\n"
"Qmvaa 12,128,16\ns_nop 0\nv_permlane16_swap_b32 v66, v67\nv_max_f32 v66, v66, v67\ns_nop 1\nv_min_f32_dpp v66, v66, v66 row_ror:8 row_mask:0xf bank_mask:0xf\ns_nop 1\nv_min_f32_dpp v66, v66, v66 row_ror:4 row_mask:0xf bank_mask:0xf\n"
"s_nop 1\nv_min_f32_dpp v66, v66, v66 row_ror:2 row_mask:0xf bank_mask:0xf\ns_nop 1\nv_min_f32_dpp v66, v66, v66 row_ror:1 row_mask:0xf bank_mask:0xf\nv_sub_f32 v56, 0xc1a00000, v66\nv_mov_b32 v57, v56\nv_mov_b32 v58, v56\nv_mov_b32 v59, v56\n"
"Qa 0,0,56\nQa 1,1,56\nQa 2,2,56\nQa 3,3,56\nQe 0\nQe 1\nQe 2\nQe 3\n"
"Qk 16,0,1\nQk 17,2,3\nQcvaa 0,112,32,56\nQa 4,4,56\nQa 5,5,56\nQmvaa 0,116,36\nQa 6,6,56\nQa 7,7,56\n"
"Qmvaa 0,120,40\nQe 4\nQe 5\nQe 6\nQe 7\nQmvaa 0,124,44\nQk 18,4,5\nQk 19,6,7\n"
"Qcvaa 4,140,44,56\nQa 8,8,56\nQa 9,9,56\nQmvaa 4,136,40\nQa 10,10,56\nQa 11,11,56\nQmvaa 4,132,36\nQa 12,12,56\n"
"Qa 13,13,56\nQmvaa 4,128,32\ns_waitcnt lgkmcnt(0)\nQa 14,14,56\nQmvva 96,16,148\nQa 15,15,56\nQmvva 100,16,156\nQe 8\n"
"Qmvva 104,16,164\nQe 9\nQe 10\nQe 11\nQe 12\nQe 13\nQe 14\nQe 15\n"
"Qmvva 108,16,172\nQk 20,8,9\nQk 21,10,11\nQk 22,12,13\nQk 23,14,15\ns_nop 1\nQmvva 140,20,172\nQe 0\n"
"Qmvva 136,20,164\nQe 1\nQmvva 132,20,156\nQe 2\nQmvva 128,20,148\nQe 3\nv_smfmac_f32_16x16x64_bf16 a[176:179], a[204:207], v[16:23], v77\nQk 24,0,1\n"
"Qcvaa 8,112,48,56\ns_add_u32 m0, s61, 99072\nQk 25,2,3\nQmvaa 8,116,52\nbuffer_load_dwordx4 v69, s[40:43], s60 offen nt lds\nQmvaa 8,120,56\nQe 4\nQmvaa 8,124,60\n"
"s_or_b32 s65, s65, 0x1d8000\ns_add_u32 s65, s65, 2048\nQcvaa 12,140,60,56\ns_andn2_b32 s65, s65, 0x1d8000\nQmvaa 12,136,56\nQe 5\nQmvaa 12,132,52\nbuffer_load_dwordx4 v69, s[40:43], s60 offen offset:1024 nt lds\n"
"Qmvaa 12,128,48\nQe 6\nQcvaa 0,128,64,56\ns_memrealtime s[74:75]\nQk 26,4,5\nQmvaa 0,132,68\nQe 7\nQmvaa 0,136,72\n"
"Qk 27,6,7\nQmvaa 0,140,76\nbuffer_load_dwordx4 v69, s[40:43], s90 offen offset:2048 nt lds\nQcvaa 4,124,76,56\nQe 8\nQmvaa 4,120,72\nQe 9\nQmvaa 4,116,68\n"
"Qe 10\nQmvaa 4,112,64\nQe 11\nbuffer_load_dwordx4 v69, s[40:43], s90 offen offset:3072 nt lds\nQmvva 160,24,148\nQe 12\nQmvva 164,24,156\nQe 13\n"
"s_add_u32 m0, s64, 99072\nQmvva 168,24,164\nbuffer_load_dwordx4 v72, s[44:47], s60 offen nt lds\nQe 14\nQe 15\nQmvva 172,24,172\nQk 28,8,9\nQk 29,10,11\n"
"Qk 30,12,13\nQk 31,14,15\ns_nop 1\nQmvva 204,28,172\nQe 0\nbuffer_load_dwordx4 v72, s[44:47], s60 offen offset:1024 nt lds\nQmvva 200,28,164\nQe 1\n"
"Qmvva 196,28,156\nQe 2\nQmvva 192,28,148\nbuffer_load_dwordx4 v72, s[44:47], s90 offen offset:2048 nt lds\nv_smfmac_f32_16x16x64_bf16 a[180:183], a[204:207], v[24:31], v77\nQk 34,0,1\nQcvaa 8,112,80,56\nQe 3\n"
"Qmvaa 8,116,84\nQk 35,2,3\nQmvaa 8,120,88\ns_waitcnt vmcnt(15)\ns_barrier\nQmvaa 8,124,92\nbuffer_load_dwordx4 v72, s[44:47], s90 offen offset:3072 nt lds\nQcvaa 12,140,92,56\n"
"s_waitcnt lgkmcnt(0)\nQmvaa 12,136,88\ns_cmp_gt_u32 s77, s74\ns_cbranch_scc1 .Lsp0\n.Lrp0:\nQmvaa 12,132,84\ns_and_b32 s75, s68, 30\ns_add_u32 s77, s74, 90\n"
"Qmvaa 12,128,80\ns_add_u32 s77, s77, s75\ns_xor_b32 s60, s65, %21\ns_add_u32 s90, s60, 260096\nQcvaa 0,128,96,56\nQda 128,68,34048\nQmvaa 0,132,100\nQda 132,68,34560\n"
"Qmvaa 0,136,104\nQda 136,68,34304\nQe 4\nQe 5\nQe 6\nQe 7\nQmvaa 0,140,108\nQda 140,68,34816\n"
"Qk 32,4,5\nQk 33,6,7\nQcvaa 4,124,108,56\nQda 124,68,33792\nQmvaa 4,120,104\nQda 120,68,33280\nQmvaa 4,116,100\nQda 116,68,33536\n"
"Qmvaa 4,112,96\nQda 112,68,33024\nQmvva 224,32,148\nQe 8\nQmvva 228,32,156\nQe 9\nQmvva 232,32,164\nQe 10\n"
"Qe 11\nQmvva 236,32,172\ns_waitcnt lgkmcnt(0)\nQk 36,8,9\nQk 37,10,11\nQcvaa 8,112,0,56\nQe 12\nQmvaa 8,116,4\n"
"v_add_u32 v68, 66048, v68\nQmvaa 8,120,8\nQe 13\n.Lpg5:\ns_cbranch_execz .Ltr9\nQe 14\nQe 15\nQmvaa 8,124,12\n"
"Qk 38,12,13\nQk 39,14,15\nQcvaa 12,140,12,56\nQe 0\nQmvaa 12,136,8\nQe 1\nQmvaa 12,132,4\nQe 2\n"
"Qmvaa 12,128,0\nQe 3\nv_smfmac_f32_16x16x64_bf16 a[184:187], a[204:207], v[32:39], v77\nQk 42,0,1\nQmava 208,36,148\nQe 4\nQmava 212,36,156\nQe 5\n"
"Qmava 216,36,164\nQe 6\nQe 8\nQe 7\nQmava 220,36,172\nQe 9\nQk 43,2,3\nQk 40,4,5\n"
"Qk 41,6,7\nQta 168,70,768\nQta 170,70,1792\nQmava 252,40,172\nQe 10\nQta 172,70,33792\nQta 174,70,34816\nQmava 248,40,164\n"
"Qe 11\nQta 160,70,512\nQta 162,70,1536\nQmava 244,40,156\nQk 44,8,9\nQk 45,10,11\nQta 164,70,33536\nQta 166,70,34560\n"
"Qmava 240,40,148\nQe 12\nQcvaa 0,112,16,56\nQta 152,70,256\nQta 154,70,1280\nQmvaa 0,116,20\nQe 13\nQmvaa 0,120,24\n"
"Qta 156,70,33280\nQta 158,70,34304\nQmvaa 0,124,28\nQe 14\nQcvaa 4,140,28,56\nQta 144,70,0\nQta 146,70,1024\nQmvaa 4,136,24\n"
"Qe 15\nQmvaa 4,132,20\nQta 148,70,33024\nQta 150,70,34048\nQmvaa 4,128,16\nQk 46,12,13\nQk 47,14,15\nQcvaa 8,128,32,56\n"
"v_add_u32 v75, 1, v75\nQmvaa 8,132,36\ns_waitcnt vmcnt(8)\nds_write_b32 v73, v75\nQmvaa 8,136,40\nQe 0\nQmvaa 8,140,44\nQe 1\n"
"Qcvaa 12,124,44,56\nQe 2\nQmvaa 12,120,40\nQe 3\nQmvaa 12,116,36\nQe 4\nQmvaa 12,112,32\ns_waitcnt lgkmcnt(1)\n"
"Qk 48,0,1\nv_smfmac_f32_16x16x64_bf16 a[188:191], a[204:207], v[40:47], v77\nQk 49,2,3\nQmvva 80,16,144\nQe 5\nQmvva 80,44,148\nQe 6\nQmvva 84,16,152\n"
"Qe 7\nQmvva 84,44,156\nQk 50,4,5\nQk 51,6,7\nQmvva 88,16,160\nQe 8\nQmvva 88,44,164\nQe 9\n"
"Qmvva 92,16,168\nQe 10\nQmvva 92,44,172\nQe 11\nQmvva 124,20,168\nQk 54,8,9\nQk 55,10,11\nQmvva 124,48,172\n"
"ds_read_b32 v66, v74\nQmvva 120,20,160\nQe 12\nQmvva 120,48,164\nQe 13\nQmvva 116,20,152\nQe 14\nQmvva 116,48,156\n"
"Qe 15\nQmvva 112,20,144\ns_waitcnt lgkmcnt(0)\nv_cmp_gt_u32 vcc, v75, v66\nQmvva 112,48,148\ns_cbranch_vccnz .Lsa1\n.Lra1:\nQk 52,12,13\n"
"Qcvaa 0,112,48,56\ns_add_u32 m0, s61, 0\nQk 53,14,15\nQmvaa 0,116,52\nbuffer_load_dwordx4 v69, s[40:43], s60 offen nt lds\nQmvaa 0,120,56\nQmvaa 0,124,60\ns_add_u32 s65, s65, 2048\n"
"Qcvaa 4,140,60,56\nQmvaa 4,136,56\nQmvaa 4,132,52\nbuffer_load_dwordx4 v69, s[40:43], s60 offen offset:1024 nt lds\nQmvaa 4,128,48\nQcvaa 8,128,64,56\ns_memrealtime s[74:75]\nQmvaa 8,132,68\n"
"Qe 0\nQmvaa 8,136,72\nQe 1\nQmvaa 8,140,76\nbuffer_load_dwordx4 v69, s[40:43], s90 offen offset:2048 nt lds\nQcvaa 12,124,76,56\nQe 2\nQmvaa 12,120,72\n"
"Qe 3\nQmvaa 12,116,68\nQe 4\nQmvaa 12,112,64\nQe 5\nv_smfmac_f32_16x16x64_bf16 a[192:195], a[204:207], v[48:55], v77\nQk 20,0,1\nQmvva 144,24,144\n"
"buffer_load_dwordx4 v69, s[40:43], s90 offen offset:3072 nt lds\nQmvva 144,52,148\nQe 6\nQmvva 148,24,152\nQe 7\nQmvva 148,52,156\ns_add_u32 m0, s64, 0\nQk 21,2,3\n"
"Qmvva 152,24,160\nbuffer_load_dwordx4 v72, s[44:47], s60 offen nt lds\nQmvva 152,52,164\nQk 22,4,5\nQk 23,6,7\nQmvva 156,24,168\nQe 8\nQmvva 156,52,172\n"
"Qe 9\nQmvva 188,28,168\nQe 10\nQmvva 188,20,172\nbuffer_load_dwordx4 v72, s[44:47], s60 offen offset:1024 nt lds\nQmvva 184,28,160\nQe 11\nQmvva 184,20,164\n"
"Qk 26,8,9\nQk 27,10,11\nQmvva 180,28,152\nQe 12\nQmvva 180,20,156\nQe 13\nQmvva 176,28,144\nbuffer_load_dwordx4 v72, s[44:47], s90 offen offset:2048 nt lds\n"
"Qmvva 176,20,148\nQe 14\nQcvaa 0,112,80,56\nQe 15\nQmvaa 0,116,84\nQk 24,12,13\nQk 25,14,15\nQmvaa 0,120,88\n"
"Qmvaa 0,124,92\nbuffer_load_dwordx4 v72, s[44:47], s90 offen offset:3072 nt lds\nQcvaa 4,140,92,56\ns_waitcnt lgkmcnt(0)\nQmvaa 4,136,88\ns_cmp_gt_u32 s77, s74\ns_cbranch_scc1 .Lsp1\n.Lrp1:\n"
"Qmvaa 4,132,84\ns_and_b32 s75, s68, 45\ns_add_u32 s77, s74, 110\nQmvaa 4,128,80\ns_add_u32 s77, s77, s75\ns_xor_b32 s60, s65, %21\ns_add_u32 s90, s60, 260096\nQcvaa 8,128,96,56\n"
"Qda 128,68,1024\ns_add_u32 s79, s79, 2030\nQmvaa 8,132,100\nQda 132,68,1536\ns_min_u32 s79, s79, s77\nQmvaa 8,136,104\nQda 136,68,1280\nQmvaa 8,140,108\n"
"Qda 140,68,1792\nQcvaa 12,124,108,56\nQda 124,68,768\nQmvaa 12,120,104\nQda 120,68,256\nQmvaa 12,116,100\nQda 116,68,512\nQmvaa 12,112,96\n"
"Qda 112,68,0\nv_smfmac_f32_16x16x64_bf16 a[196:199], a[204:207], v[20:27], v77\nQmvva 208,32,144\nQe 0\nQmvva 208,24,148\nQe 1\nQmvva 212,32,152\nQe 2\n"
"Qmvva 212,24,156\nQe 3\nQmvva 216,32,160\nQk 28,0,1\nQk 29,2,3\nQmvva 216,24,164\nQe 4\nQmvva 220,32,168\n"
"Qe 5\nQmvva 220,24,172\ns_waitcnt lgkmcnt(0)\nQk 30,4,5\nQcvaa 0,112,0,56\nQe 6\nQmvaa 0,116,4\nQe 7\n"
"Qmvaa 0,120,8\nQk 31,6,7\nQmvaa 0,124,12\nQe 8\nQcvaa 4,140,12,56\nQe 9\nQmvaa 4,136,8\nQe 10\n"
"Qmvaa 4,132,4\nQe 11\nQmvaa 4,128,0\nQe 12\nQmvva 240,36,144\nQe 13\nQmvva 240,28,148\nQe 14\n"
"Qmvva 244,36,152\nQe 15\nQmvva 244,28,156\nQk 34,8,9\nQk 35,10,11\nQmvva 248,36,160\nQk 32,12,13\nQk 33,14,15\n"
"Qmvva 248,28,164\nQe 0\nQmvva 252,36,168\nQe 1\nQmvva 252,28,172\nQe 2\nQmava 236,40,168\nQta 168,70,33920\n"
"Qta 170,70,34944\nQmava 236,32,172\nQe 3\nQmava 232,40,160\nQta 172,71,896\nQta 174,71,1920\nQmava 232,32,164\nQk 36,0,1\n"
"Qk 37,2,3\nQmava 228,40,152\nQta 160,70,33664\nQta 162,70,34688\nQmava 228,32,156\nQe 4\nQmava 224,40,144\nQta 164,71,640\n"
"Qta 166,71,1664\nQmava 224,32,148\nQe 5\nv_smfmac_f32_16x16x64_bf16 a[200:203], a[204:207], v[28:35], v77\nQta 152,70,33408\nQta 154,70,34432\nQcvaa 8,112,16,56\nQe 6\n"
"Qmvaa 8,116,20\nQta 156,71,384\nQta 158,71,1408\nQmvaa 8,120,24\nQe 7\nQmvaa 8,124,28\nQta 144,70,33152\nQta 146,70,34176\n"
"Qcvaa 12,140,28,56\nQk 38,4,5\nQk 39,6,7\nQmvaa 12,136,24\nQta 148,71,128\nQta 150,71,1152\nQmvaa 12,132,20\nQmvaa 12,128,16\n"
"s_lshl_b32 s66, s67, 3\n.Lloop:\ns_bitcmp1_b32 s99, s66\ns_cbranch_scc1 .Lhook\n.Lhookret:\nQcvaa 0,128,32,56\nv_add_u32 v75, 1, v75\nQmvaa 0,132,36\n"
"s_waitcnt vmcnt(8)\nds_write_b32 v73, v75\nQmvaa 0,136,40\nQe 8\nQmvaa 0,140,44\nQe 9\nQcvaa 4,124,44,56\nQe 10\n"
"Qmvaa 4,120,40\nQe 11\nQmvaa 4,116,36\nQe 12\nQmvaa 4,112,32\ns_waitcnt lgkmcnt(1)\nQk 40,8,9\nQmvva 96,44,144\n"
"Qe 13\nQmvva 96,36,148\nQe 14\nQmvva 100,44,152\nQe 15\nQmvva 100,36,156\nQk 41,10,11\nQk 42,12,13\n"
"Qmvva 104,44,160\nQk 43,14,15\nQmvva 104,36,164\nQe 0\nQmvva 108,44,168\nQe 1\nQmvva 108,36,172\nQe 2\n"
"Qmvva 140,48,168\nQe 3\nQmvva 140,40,172\nQk 46,0,1\nQk 47,2,3\nQmvva 136,48,160\nds_read_b32 v66, v74\nQmvva 136,40,164\n"
"Qe 4\nQmvva 132,48,152\nQe 5\nQmvva 132,40,156\nQe 6\nQmvva 128,48,144\nQe 7\nQmvva 128,40,148\n"
"s_waitcnt lgkmcnt(0)\nv_cmp_gt_u32 vcc, v75, v66\nv_smfmac_f32_16x16x64_bf16 a[176:179], a[204:207], v[36:43], v77\ns_cbranch_vccnz .Lsa2\n.Lra2:\nQcvaa 8,112,48,56\ns_add_u32 m0, s61, 33024\nQk 44,4,5\n"
"Qmvaa 8,116,52\nbuffer_load_dwordx4 v69, s[40:43], s60 offen nt lds\nQmvaa 8,120,56\nQk 45,6,7\nQmvaa 8,124,60\ns_add_u32 s65, s65, 2048\nQcvaa 12,140,60,56\nQmvaa 12,136,56\n"
"Qmvaa 12,132,52\nbuffer_load_dwordx4 v69, s[40:43], s60 offen offset:1024 nt lds\nQmvaa 12,128,48\nQcvaa 0,128,64,56\ns_memrealtime s[74:75]\nQmvaa 0,132,68\nQe 8\nQmvaa 0,136,72\n"
"Qe 9\nQmvaa 0,140,76\nbuffer_load_dwordx4 v69, s[40:43], s90 offen offset:2048 nt lds\nQcvaa 4,124,76,56\nQe 10\nQmvaa 4,120,72\nQe 11\nQmvaa 4,116,68\n"
"Qe 12\nQmvaa 4,112,64\nQe 13\nQmvva 160,52,144\nbuffer_load_dwordx4 v69, s[40:43], s90 offen offset:3072 nt lds\nQmvva 160,44,148\nQe 14\nQmvva 164,52,152\n"
"Qe 15\nQmvva 164,44,156\nQk 48,8,9\nQk 49,10,11\nQmvva 168,52,160\ns_add_u32 m0, s64, 33024\nQk 50,12,13\nQmvva 168,44,164\n"
"buffer_load_dwordx4 v72, s[44:47], s60 offen nt lds\nQmvva 172,52,168\nQk 51,14,15\nQmvva 172,44,172\nQe 0\nQmvva 204,20,168\nQe 1\nQmvva 204,48,172\n"
"Qe 2\nQmvva 200,20,160\nbuffer_load_dwordx4 v72, s[44:47], s60 offen offset:1024 nt lds\nQmvva 200,48,164\nQe 3\nQmvva 196,20,152\nQk 18,0,1\nQk 19,2,3\n"
"Qmvva 196,48,156\nQe 4\nQmvva 192,20,144\nQe 5\nQmvva 192,48,148\nbuffer_load_dwordx4 v72, s[44:47], s90 offen offset:2048 nt lds\nv_smfmac_f32_16x16x64_bf16 a[180:183], a[204:207], v[44:51], v77\nQk 16,4,5\n"
"Qcvaa 8,112,80,56\nQe 6\nQmvaa 8,116,84\nQe 7\nQmvaa 8,120,88\nQk 17,6,7\nQmvaa 8,124,92\nbuffer_load_dwordx4 v72, s[44:47], s90 offen offset:3072 nt lds\n"
"Qcvaa 12,140,92,56\ns_waitcnt lgkmcnt(0)\nQmvaa 12,136,88\ns_cmp_gt_u32 s77, s74\ns_cbranch_scc1 .Lsp2\n.Lrp2:\nQmvaa 12,132,84\ns_add_u32 s77, s74, 220\n"
"Qmvaa 12,128,80\ns_xor_b32 s60, s65, %21\ns_add_u32 s90, s60, 260096\nQcvaa 0,128,96,56\nQda 128,68,34048\ns_add_u32 s79, s79, s31\nQmvaa 0,132,100\nQda 132,68,34560\n"
"s_max_u32 s77, s77, s79\nQmvaa 0,136,104\nQda 136,68,34304\nQmvaa 0,140,108\nQda 140,68,34816\nQcvaa 4,124,108,56\nQda 124,68,33792\nQmvaa 4,120,104\n"
"Qda 120,68,33280\nQmvaa 4,116,100\nQda 116,68,33536\nQmvaa 4,112,96\nQda 112,68,33024\nQmvva 224,24,144\nQe 8\nQmvva 224,16,148\n"
"Qe 9\nQmvva 228,24,152\nQe 10\nQmvva 228,16,156\nQe 11\nQmvva 232,24,160\nQk 20,8,9\nQk 21,10,11\n"
"Qmvva 232,16,164\nQe 12\nQmvva 236,24,168\nQe 13\nQmvva 236,16,172\ns_waitcnt lgkmcnt(0)\nQk 22,12,13\nQcvaa 8,112,0,56\n"
"Qe 14\nQmvaa 8,116,4\nv_subrev_u32 v68, 66048, v68\nQmvaa 8,120,8\nQe 15\nQmvaa 8,124,12\nQk 23,14,15\nQcvaa 12,140,12,56\n"
"Qe 0\nQmvaa 12,136,8\nQe 1\nQmvaa 12,132,4\nQe 2\nQmvaa 12,128,0\nQe 3\nv_smfmac_f32_16x16x64_bf16 a[184:187], a[204:207], v[16:23], v77\n"
"Qk 26,0,1\nQmava 208,28,144\nQe 4\nQmava 208,20,148\nQe 5\nQmava 212,28,152\nQe 6\nQmava 212,20,156\n"
"Qe 7\nQmava 216,28,160\nQk 27,2,3\nQk 24,4,5\nQmava 216,20,164\nQk 25,6,7\nQmava 220,28,168\nQe 8\n"
"Qmava 220,20,172\nQe 9\nQmava 252,32,168\nQta 168,71,768\nQta 170,71,1792\nQmava 252,24,172\nQe 10\nQmava 248,32,160\n"
"Qta 172,71,33792\nQta 174,71,34816\nQmava 248,24,164\nQe 11\nQmava 244,32,152\nQta 160,71,512\nQta 162,71,1536\nQmava 244,24,156\n"
"Qk 28,8,9\nQk 29,10,11\nQmava 240,32,144\nQta 164,71,33536\nQta 166,71,34560\nQmava 240,24,148\nQe 12\nQcvaa 0,112,16,56\n"
"Qta 152,71,256\nQta 154,71,1280\nQmvaa 0,116,20\nQe 13\nQmvaa 0,120,24\nQta 156,71,33280\nQta 158,71,34304\nQmvaa 0,124,28\n"
"Qe 14\nQcvaa 4,140,28,56\nQta 144,71,0\nQta 146,71,1024\nQmvaa 4,136,24\nQe 15\nQmvaa 4,132,20\nQta 148,71,33024\n"
"Qta 150,71,34048\nQmvaa 4,128,16\nQk 30,12,13\nQk 31,14,15\nQcvaa 8,128,32,56\nv_add_u32 v75, 1, v75\nQmvaa 8,132,36\ns_waitcnt vmcnt(8)\n"
"ds_write_b32 v73, v75\nQmvaa 8,136,40\nQe 0\nQmvaa 8,140,44\nQe 1\nQcvaa 12,124,44,56\nQe 2\nQmvaa 12,120,40\n"
"Qe 3\nQmvaa 12,116,36\nQe 4\nQmvaa 12,112,32\ns_waitcnt lgkmcnt(1)\nQk 32,0,1\nv_smfmac_f32_16x16x64_bf16 a[188:191], a[204:207], v[24:31], v77\nQk 33,2,3\n"
".Lpg6:\ns_cbranch_execz .Ltr9\nQmvva 80,36,144\nQe 5\nQmvva 80,28,148\nQe 6\nQmvva 84,36,152\nQe 7\n"
"Qmvva 84,28,156\nQk 34,4,5\nQk 35,6,7\nQmvva 88,36,160\nQe 8\nQmvva 88,28,164\nQe 9\nQmvva 92,36,168\n"
"Qe 10\nQmvva 92,28,172\nQe 11\nQmvva 124,40,168\nQk 38,8,9\nQk 39,10,11\nQmvva 124,32,172\nds_read_b32 v66, v74\n"
"Qmvva 120,40,160\nQe 12\nQmvva 120,32,164\nQe 13\nQmvva 116,40,152\nQe 14\nQmvva 116,32,156\nQe 15\n"
"Qmvva 112,40,144\ns_waitcnt lgkmcnt(0)\nv_cmp_gt_u32 vcc, v75, v66\nQmvva 112,32,148\ns_cbranch_vccnz .Lsa3\n.Lra3:\nQk 36,12,13\nQcvaa 0,112,48,56\n"
"s_add_u32 m0, s61, 66048\nQk 37,14,15\nQmvaa 0,116,52\nbuffer_load_dwordx4 v69, s[40:43], s60 offen nt lds\nQmvaa 0,120,56\nQmvaa 0,124,60\ns_add_u32 s65, s65, 2048\nQcvaa 4,140,60,56\n"
"Qmvaa 4,136,56\nQmvaa 4,132,52\nbuffer_load_dwordx4 v69, s[40:43], s60 offen offset:1024 nt lds\nQmvaa 4,128,48\nQcvaa 8,128,64,56\ns_nop 0\nQmvaa 8,132,68\nQe 0\n"
"Qmvaa 8,136,72\nQe 1\nQmvaa 8,140,76\nbuffer_load_dwordx4 v69, s[40:43], s90 offen offset:2048 nt lds\nQcvaa 12,124,76,56\nQe 2\nQmvaa 12,120,72\nQe 3\n"
"Qmvaa 12,116,68\nQe 4\nQmvaa 12,112,64\nQe 5\nv_smfmac_f32_16x16x64_bf16 a[192:195], a[204:207], v[32:39], v77\nQk 40,0,1\nQmvva 144,44,144\nbuffer_load_dwordx4 v69, s[40:43], s90 offen offset:3072 nt lds\n"
"Qmvva 144,36,148\nQe 6\nQmvva 148,44,152\nQe 7\nQmvva 148,36,156\ns_add_u32 m0, s64, 66048\nQk 41,2,3\nQmvva 152,44,160\n"
"buffer_load_dwordx4 v72, s[44:47], s60 offen nt lds\nQmvva 152,36,164\nQk 42,4,5\nQk 43,6,7\nQmvva 156,44,168\nQe 8\nQmvva 156,36,172\nQe 9\n"
"Qmvva 188,48,168\nQe 10\nQmvva 188,40,172\nbuffer_load_dwordx4 v72, s[44:47], s60 offen offset:1024 nt lds\nQmvva 184,48,160\nQe 11\nQmvva 184,40,164\nQk 46,8,9\n"
"Qk 47,10,11\nQmvva 180,48,152\nQe 12\nQmvva 180,40,156\nQe 13\nQmvva 176,48,144\nbuffer_load_dwordx4 v72, s[44:47], s90 offen offset:2048 nt lds\nQmvva 176,40,148\n"
"Qe 14\nQcvaa 0,112,80,56\nQe 15\nQmvaa 0,116,84\nQk 44,12,13\nQk 45,14,15\nQmvaa 0,120,88\nQmvaa 0,124,92\n"
"buffer_load_dwordx4 v72, s[44:47], s90 offen offset:3072 nt lds\nQcvaa 4,140,92,56\nQmvaa 4,136,88\nQmvaa 4,132,84\nQmvaa 4,128,80\ns_xor_b32 s60, s65, %21\ns_add_u32 s90, s60, 260096\nQcvaa 8,128,96,56\n"
"Qda 128,68,1024\nQmvaa 8,132,100\nQda 132,68,1536\nQmvaa 8,136,104\nQda 136,68,1280\nQmvaa 8,140,108\nQda 140,68,1792\nQcvaa 12,124,108,56\n"
"Qda 124,68,768\nQmvaa 12,120,104\nQda 120,68,256\nQmvaa 12,116,100\nQda 116,68,512\nQmvaa 12,112,96\nQda 112,68,0\nv_smfmac_f32_16x16x64_bf16 a[196:199], a[204:207], v[40:47], v77\n"
"Qmvva 208,16,144\nQe 0\nQmvva 208,44,148\nQe 1\nQmvva 212,16,152\nQe 2\nQmvva 212,44,156\nQe 3\n"
"Qmvva 216,16,160\nQk 48,0,1\nQk 49,2,3\nQmvva 216,44,164\nQe 4\nQmvva 220,16,168\nQe 5\nQmvva 220,44,172\n"
"s_waitcnt lgkmcnt(0)\nQk 50,4,5\nQcvaa 0,112,0,56\nQe 6\nQmvaa 0,116,4\nQe 7\nQmvaa 0,120,8\nQk 51,6,7\n"
"Qmvaa 0,124,12\nQe 8\nQcvaa 4,140,12,56\nQe 9\nQmvaa 4,136,8\nQe 10\nQmvaa 4,132,4\nQe 11\n"
"Qmvaa 4,128,0\nQe 12\nQmvva 240,20,144\nQe 13\nQmvva 240,48,148\nQe 14\nQmvva 244,20,152\nQe 15\n"
"Qmvva 244,48,156\nQk 54,8,9\nQk 55,10,11\nQmvva 248,20,160\nQk 52,12,13\nQk 53,14,15\nQmvva 248,48,164\nQe 0\n"
"Qmvva 252,20,168\nQe 1\nQmvva 252,48,172\nQe 2\nQmava 236,24,168\nQta 168,71,33920\nQta 170,71,34944\nQmava 236,52,172\n"
"Qe 3\nQmava 232,24,160\nQta 172,70,896\nQta 174,70,1920\nQmava 232,52,164\nQk 16,0,1\nQk 17,2,3\nQmava 228,24,152\n"
"Qta 160,71,33664\nQta 162,71,34688\nQmava 228,52,156\nQe 4\nQmava 224,24,144\nQta 164,70,640\nQta 166,70,1664\nQmava 224,52,148\n"
"Qe 5\nv_smfmac_f32_16x16x64_bf16 a[200:203], a[204:207], v[48:55], v77\nQta 152,71,33408\nQta 154,71,34432\nQcvaa 8,112,16,56\nQe 6\nQmvaa 8,116,20\nQta 156,70,384\n"
"Qta 158,70,1408\nQmvaa 8,120,24\nQe 7\nQmvaa 8,124,28\nQta 144,71,33152\nQta 146,71,34176\nQcvaa 12,140,28,56\nQk 18,4,5\n"
"Qk 19,6,7\nQmvaa 12,136,24\nQta 148,70,128\nQta 150,70,1152\nQmvaa 12,132,20\nQmvaa 12,128,16\nQcvaa 0,128,32,56\nv_add_u32 v75, 1, v75\n"
"Qmvaa 0,132,36\ns_waitcnt vmcnt(8)\nds_write_b32 v73, v75\nQmvaa 0,136,40\nQe 8\nQmvaa 0,140,44\nQe 9\nQcvaa 4,124,44,56\n"
"Qe 10\nQmvaa 4,120,40\nQe 11\nQmvaa 4,116,36\nQe 12\nQmvaa 4,112,32\ns_waitcnt lgkmcnt(1)\nQk 20,8,9\n"
"Qmvva 96,28,144\nQe 13\nQmvva 96,16,148\nQe 14\nQmvva 100,28,152\nQe 15\nQmvva 100,16,156\nQk 21,10,11\n"
"Qk 22,12,13\nQmvva 104,28,160\nQk 23,14,15\nQmvva 104,16,164\nQe 0\nQmvva 108,28,168\nQe 1\nQmvva 108,16,172\n"
"Qe 2\nQmvva 140,32,168\nQe 3\nQmvva 140,20,172\nQk 26,0,1\nQk 27,2,3\nQmvva 136,32,160\nds_read_b32 v66, v74\n"
"Qmvva 136,20,164\nQe 4\nQmvva 132,32,152\nQe 5\nQmvva 132,20,156\nQe 6\nQmvva 128,32,144\nQe 7\n"
"Qmvva 128,20,148\ns_waitcnt lgkmcnt(0)\nv_cmp_gt_u32 vcc, v75, v66\nv_smfmac_f32_16x16x64_bf16 a[176:179], a[204:207], v[16:23], v77\ns_cbranch_vccnz .Lsa4\n.Lra4:\nQcvaa 8,112,48,56\ns_add_u32 m0, s61, 99072\n"
"Qk 24,4,5\nQmvaa 8,116,52\nbuffer_load_dwordx4 v69, s[40:43], s60 offen nt lds\nQmvaa 8,120,56\nQk 25,6,7\nQmvaa 8,124,60\ns_or_b32 s65, s65, 0x1d8000\ns_add_u32 s65, s65, 2048\n"
"Qcvaa 12,140,60,56\ns_andn2_b32 s65, s65, 0x1d8000\nQmvaa 12,136,56\nQmvaa 12,132,52\nbuffer_load_dwordx4 v69, s[40:43], s60 offen offset:1024 nt lds\nQmvaa 12,128,48\nQcvaa 0,128,64,56\ns_memrealtime s[74:75]\n"
"Qmvaa 0,132,68\nQe 8\nQmvaa 0,136,72\nQe 9\nQmvaa 0,140,76\nbuffer_load_dwordx4 v69, s[40:43], s90 offen offset:2048 nt lds\nQcvaa 4,124,76,56\nQe 10\n"
"Qmvaa 4,120,72\nQe 11\nQmvaa 4,116,68\nQe 12\nQmvaa 4,112,64\nQe 13\nQmvva 160,36,144\nbuffer_load_dwordx4 v69, s[40:43], s90 offen offset:3072 nt lds\n"
"Qmvva 160,24,148\nQe 14\nQmvva 164,36,152\nQe 15\nQmvva 164,24,156\nQk 28,8,9\nQk 29,10,11\nQmvva 168,36,160\n"
"s_add_u32 m0, s64, 99072\nQk 30,12,13\nQmvva 168,24,164\nbuffer_load_dwordx4 v72, s[44:47], s60 offen nt lds\nQmvva 172,36,168\nQk 31,14,15\nQmvva 172,24,172\nQe 0\n"
"Qmvva 204,40,168\nQe 1\nQmvva 204,28,172\nQe 2\nQmvva 200,40,160\nbuffer_load_dwordx4 v72, s[44:47], s60 offen offset:1024 nt lds\nQmvva 200,28,164\nQe 3\n"
"Qmvva 196,40,152\nQk 34,0,1\nQk 35,2,3\nQmvva 196,28,156\nQe 4\nQmvva 192,40,144\nQe 5\nQmvva 192,28,148\n"
"buffer_load_dwordx4 v72, s[44:47], s90 offen offset:2048 nt lds\nv_smfmac_f32_16x16x64_bf16 a[180:183], a[204:207], v[24:31], v77\nQk 32,4,5\nQcvaa 8,112,80,56\nQe 6\nQmvaa 8,116,84\nQe 7\nQmvaa 8,120,88\n"
"Qk 33,6,7\nQmvaa 8,124,92\nbuffer_load_dwordx4 v72, s[44:47], s90 offen offset:3072 nt lds\nQcvaa 12,140,92,56\ns_waitcnt lgkmcnt(0)\nQmvaa 12,136,88\ns_cmp_gt_u32 s77, s74\ns_cbranch_scc1 .Lsp4\n"
".Lrp4:\nQmvaa 12,132,84\ns_add_u32 s77, s74, 220\nQmvaa 12,128,80\ns_xor_b32 s60, s65, %21\ns_add_u32 s90, s60, 260096\nQcvaa 0,128,96,56\nQda 128,68,34048\n"
"s_add_u32 s79, s79, s31\nQmvaa 0,132,100\nQda 132,68,34560\ns_max_u32 s77, s77, s79\nQmvaa 0,136,104\nQda 136,68,34304\nQmvaa 0,140,108\nQda 140,68,34816\n"
"Qcvaa 4,124,108,56\nQda 124,68,33792\nQmvaa 4,120,104\nQda 120,68,33280\nQmvaa 4,116,100\nQda 116,68,33536\nQmvaa 4,112,96\nQda 112,68,33024\n"
"Qmvva 224,44,144\nQe 8\nQmvva 224,32,148\nQe 9\nQmvva 228,44,152\nQe 10\nQmvva 228,32,156\nQe 11\n"
"Qmvva 232,44,160\nQk 36,8,9\nQk 37,10,11\nQmvva 232,32,164\nQe 12\nQmvva 236,44,168\nQe 13\nQmvva 236,32,172\n"
"s_waitcnt lgkmcnt(0)\nQk 38,12,13\nQcvaa 8,112,0,56\nQe 14\nQmvaa 8,116,4\nv_add_u32 v68, 66048, v68\nQmvaa 8,120,8\nQe 15\n"
"Qmvaa 8,124,12\nQk 39,14,15\nQcvaa 12,140,12,56\nQe 0\nQmvaa 12,136,8\nQe 1\nQmvaa 12,132,4\nQe 2\n"
"Qmvaa 12,128,0\nQe 3\nv_smfmac_f32_16x16x64_bf16 a[184:187], a[204:207], v[32:39], v77\nQk 42,0,1\nQmava 208,48,144\nQe 4\nQmava 208,36,148\nQe 5\n"
"Qmava 212,48,152\nQe 6\nQmava 212,36,156\nQe 7\nQmava 216,48,160\nQk 43,2,3\nQk 40,4,5\nQmava 216,36,164\n"
"Qk 41,6,7\nQmava 220,48,168\nQe 8\nQmava 220,36,172\nQe 9\nQmava 252,52,168\nQta 168,70,768\nQta 170,70,1792\n"
"Qmava 252,40,172\nQe 10\nQmava 248,52,160\nQta 172,70,33792\nQta 174,70,34816\nQmava 248,40,164\nQe 11\nQmava 244,52,152\n"
"Qta 160,70,512\nQta 162,70,1536\nQmava 244,40,156\nQk 44,8,9\nQk 45,10,11\nQmava 240,52,144\nQta 164,70,33536\nQta 166,70,34560\n"
"Qmava 240,40,148\nQe 12\nQcvaa 0,112,16,56\nQta 152,70,256\nQta 154,70,1280\nQmvaa 0,116,20\nQe 13\nQmvaa 0,120,24\n"
"Qta 156,70,33280\nQta 158,70,34304\nQmvaa 0,124,28\nQe 14\nQcvaa 4,140,28,56\nQta 144,70,0\nQta 146,70,1024\nQmvaa 4,136,24\n"
"Qe 15\nQmvaa 4,132,20\nQta 148,70,33024\nQta 150,70,34048\nQmvaa 4,128,16\nQk 46,12,13\nQk 47,14,15\nQcvaa 8,128,32,56\n"
"v_add_u32 v75, 1, v75\nQmvaa 8,132,36\ns_waitcnt vmcnt(8)\nds_write_b32 v73, v75\nQmvaa 8,136,40\nQe 0\nQmvaa 8,140,44\nQe 1\n"
"Qcvaa 12,124,44,56\nQe 2\nQmvaa 12,120,40\nQe 3\nQmvaa 12,116,36\nQe 4\nQmvaa 12,112,32\ns_waitcnt lgkmcnt(1)\n"
"Qk 48,0,1\nv_smfmac_f32_16x16x64_bf16 a[188:191], a[204:207], v[40:47], v77\nQk 49,2,3\nQmvva 80,16,144\nQe 5\nQmvva 80,44,148\nQe 6\nQmvva 84,16,152\n"
"Qe 7\nQmvva 84,44,156\nQk 50,4,5\nQk 51,6,7\nQmvva 88,16,160\nQe 8\nQmvva 88,44,164\nQe 9\n"
"Qmvva 92,16,168\nQe 10\nQmvva 92,44,172\nQe 11\nQmvva 124,20,168\nQk 54,8,9\nQk 55,10,11\nQmvva 124,48,172\n"
"ds_read_b32 v66, v74\nQmvva 120,20,160\nQe 12\nQmvva 120,48,164\nQe 13\nQmvva 116,20,152\nQe 14\nQmvva 116,48,156\n"
"Qe 15\nQmvva 112,20,144\ns_waitcnt lgkmcnt(0)\nv_cmp_gt_u32 vcc, v75, v66\nQmvva 112,48,148\ns_cbranch_vccnz .Lsa5\n.Lra5:\nQk 52,12,13\n"
"Qcvaa 0,112,48,56\ns_add_u32 m0, s61, 0\nQk 53,14,15\nQmvaa 0,116,52\nbuffer_load_dwordx4 v69, s[40:43], s60 offen nt lds\nQmvaa 0,120,56\nQmvaa 0,124,60\ns_add_u32 s65, s65, 2048\n"
"Qcvaa 4,140,60,56\nQmvaa 4,136,56\nQmvaa 4,132,52\nbuffer_load_dwordx4 v69, s[40:43], s60 offen offset:1024 nt lds\nQmvaa 4,128,48\nQcvaa 8,128,64,56\ns_nop 0\nQmvaa 8,132,68\n"
"Qe 0\nQmvaa 8,136,72\nQe 1\nQmvaa 8,140,76\nbuffer_load_dwordx4 v69, s[40:43], s90 offen offset:2048 nt lds\nQcvaa 12,124,76,56\nQe 2\nQmvaa 12,120,72\n"
"Qe 3\nQmvaa 12,116,68\nQe 4\nQmvaa 12,112,64\nQe 5\nv_smfmac_f32_16x16x64_bf16 a[192:195], a[204:207], v[48:55], v77\nQk 20,0,1\nQmvva 144,24,144\n"
"buffer_load_dwordx4 v69, s[40:43], s90 offen offset:3072 nt lds\nQmvva 144,52,148\nQe 6\nQmvva 148,24,152\nQe 7\nQmvva 148,52,156\ns_add_u32 m0, s64, 0\nQk 21,2,3\n"
"Qmvva 152,24,160\nbuffer_load_dwordx4 v72, s[44:47], s60 offen nt lds\nQmvva 152,52,164\nQk 22,4,5\nQk 23,6,7\nQmvva 156,24,168\nQe 8\nQmvva 156,52,172\n"
"Qe 9\nQmvva 188,28,168\nQe 10\nQmvva 188,20,172\nbuffer_load_dwordx4 v72, s[44:47], s60 offen offset:1024 nt lds\nQmvva 184,28,160\n.Lpg7:\ns_cbranch_execz .Ltr9\n"
"Qe 11\nQmvva 184,20,164\nQk 26,8,9\nQk 27,10,11\nQmvva 180,28,152\nQe 12\nQmvva 180,20,156\nQe 13\n"
"Qmvva 176,28,144\nbuffer_load_dwordx4 v72, s[44:47], s90 offen offset:2048 nt lds\nQmvva 176,20,148\nQe 14\nQcvaa 0,112,80,56\nQe 15\nQmvaa 0,116,84\nQk 24,12,13\n"
"Qk 25,14,15\nQmvaa 0,120,88\nQmvaa 0,124,92\nbuffer_load_dwordx4 v72, s[44:47], s90 offen offset:3072 nt lds\nQcvaa 4,140,92,56\nQmvaa 4,136,88\nQmvaa 4,132,84\nQmvaa 4,128,80\n"
"s_xor_b32 s60, s65, %21\ns_add_u32 s90, s60, 260096\nQcvaa 8,128,96,56\nQda 128,68,1024\nQmvaa 8,132,100\nQda 132,68,1536\nQmvaa 8,136,104\nQda 136,68,1280\n"
"Qmvaa 8,140,108\nQda 140,68,1792\nQcvaa 12,124,108,56\nQda 124,68,768\nQmvaa 12,120,104\nQda 120,68,256\nQmvaa 12,116,100\nQda 116,68,512\n"
"Qmvaa 12,112,96\nQda 112,68,0\nv_smfmac_f32_16x16x64_bf16 a[196:199], a[204:207], v[20:27], v77\nQmvva 208,32,144\nQe 0\nQmvva 208,24,148\nQe 1\nQmvva 212,32,152\n"
"Qe 2\nQmvva 212,24,156\nQe 3\nQmvva 216,32,160\nQk 28,0,1\nQk 29,2,3\nQmvva 216,24,164\nQe 4\n"
"Qmvva 220,32,168\nQe 5\nQmvva 220,24,172\ns_waitcnt lgkmcnt(0)\nQk 30,4,5\nQcvaa 0,112,0,56\nQe 6\nQmvaa 0,116,4\n"
"Qe 7\nQmvaa 0,120,8\nQk 31,6,7\nQmvaa 0,124,12\nQe 8\nQcvaa 4,140,12,56\nQe 9\nQmvaa 4,136,8\n"
"Qe 10\nQmvaa 4,132,4\nQe 11\nQmvaa 4,128,0\nQe 12\nQmvva 240,36,144\nQe 13\nQmvva 240,28,148\n"
"Qe 14\nQmvva 244,36,152\nQe 15\nQmvva 244,28,156\nQk 34,8,9\nQk 35,10,11\nQmvva 248,36,160\nQk 32,12,13\n"
"Qk 33,14,15\nQmvva 248,28,164\nQe 0\nQmvva 252,36,168\nQe 1\nQmvva 252,28,172\nQe 2\nQmava 236,40,168\n"
"Qta 168,70,33920\nQta 170,70,34944\nQmava 236,32,172\nQe 3\nQmava 232,40,160\nQta 172,71,896\nQta 174,71,1920\nQmava 232,32,164\n"
"Qk 36,0,1\nQk 37,2,3\nQmava 228,40,152\nQta 160,70,33664\nQta 162,70,34688\nQmava 228,32,156\nQe 4\nQmava 224,40,144\n"
"Qta 164,71,640\nQta 166,71,1664\nQmava 224,32,148\nQe 5\nv_smfmac_f32_16x16x64_bf16 a[200:203], a[204:207], v[28:35], v77\nQta 152,70,33408\nQta 154,70,34432\nQcvaa 8,112,16,56\n"
"Qe 6\nQmvaa 8,116,20\nQta 156,71,384\nQta 158,71,1408\nQmvaa 8,120,24\nQe 7\nQmvaa 8,124,28\nQta 144,70,33152\n"
"Qta 146,70,34176\nQcvaa 12,140,28,56\nQk 38,4,5\nQk 39,6,7\nQmvaa 12,136,24\nQta 148,71,128\nQta 150,71,1152\nQmvaa 12,132,20\n"
"Qmvaa 12,128,16\ns_add_u32 s66, s66, 1\ns_bitcmp1_b32 0x7f7f, s66\ns_cbranch_scc1 .Lloop\ns_cmp_lg_u32 s67, 0\ns_cbranch_scc1 .Ltail1\nQcvaa 0,128,32,56\nv_add_u32 v75, 1, v75\n"
"Qmvaa 0,132,36\ns_waitcnt vmcnt(8)\nds_write_b32 v73, v75\nQmvaa 0,136,40\nQe 8\nQmvaa 0,140,44\nQe 9\nQcvaa 4,124,44,56\n"
"Qe 10\nQmvaa 4,120,40\nQe 11\nQmvaa 4,116,36\nQe 12\nQmvaa 4,112,32\ns_waitcnt lgkmcnt(1)\nQk 40,8,9\n"
"Qmvva 96,44,144\nQe 13\nQmvva 96,36,148\nQe 14\nQmvva 100,44,152\nQe 15\nQmvva 100,36,156\nQk 41,10,11\n"
"Qk 42,12,13\nQmvva 104,44,160\nQk 43,14,15\nQmvva 104,36,164\nQe 0\nQmvva 108,44,168\nQe 1\nQmvva 108,36,172\n"
"Qe 2\nQmvva 140,48,168\nQe 3\nQmvva 140,40,172\nQk 46,0,1\nQk 47,2,3\nQmvva 136,48,160\nds_read_b32 v66, v74\n"
"Qmvva 136,40,164\nQe 4\nQmvva 132,48,152\nQe 5\nQmvva 132,40,156\nQe 6\nQmvva 128,48,144\nQe 7\n"
"Qmvva 128,40,148\ns_waitcnt lgkmcnt(0)\nv_cmp_gt_u32 vcc, v75, v66\nv_smfmac_f32_16x16x64_bf16 a[176:179], a[204:207], v[36:43], v77\ns_cbranch_vccnz .Lsa30\n.Lra30:\nQcvaa 8,112,48,56\ns_add_u32 m0, s62, 0\n"
"s_add_u32 s69, s87, 0\nQk 44,4,5\nQmvaa 8,116,52\nbuffer_load_dwordx4 v62, s[52:55], s69 offen lds\nQmvaa 8,120,56\nQk 45,6,7\nQmvaa 8,124,60\nQcvaa 12,140,60,56\n"
"Qmvaa 12,136,56\ns_add_u32 s69, s87, 64504\nQmvaa 12,132,52\nbuffer_load_dwordx4 v62, s[52:55], s69 offen offset:1032 lds\nQmvaa 12,128,48\nQcvaa 0,128,64,56\nQmvaa 0,132,68\nQe 8\n"
"Qmvaa 0,136,72\ns_add_u32 s69, s87, 129008\nQe 9\nQmvaa 0,140,76\nbuffer_load_dwordx4 v62, s[52:55], s69 offen offset:2064 lds\nQcvaa 4,124,76,56\nQe 10\nQmvaa 4,120,72\n"
"Qe 11\nQmvaa 4,116,68\nQe 12\nQmvaa 4,112,64\ns_add_u32 s69, s87, 193512\nQe 13\nQmvva 160,52,144\nbuffer_load_dwordx4 v62, s[52:55], s69 offen offset:3096 lds\n"
"Qmvva 160,44,148\nQe 14\nQmvva 164,52,152\nQe 15\nQmvva 164,44,156\nQk 48,8,9\nQk 49,10,11\nQmvva 168,52,160\n"
"s_add_u32 m0, s62, 16512\ns_add_u32 s69, s87, 128\nQk 50,12,13\nQmvva 168,44,164\nbuffer_load_dwordx4 v62, s[52:55], s69 offen lds\nQmvva 172,52,168\nQk 51,14,15\nQmvva 172,44,172\n"
"Qe 0\nQmvva 204,20,168\nQe 1\nQmvva 204,48,172\ns_add_u32 s69, s87, 64632\nQe 2\nQmvva 200,20,160\nbuffer_load_dwordx4 v62, s[52:55], s69 offen offset:1032 lds\n"
"Qmvva 200,48,164\nQe 3\nQmvva 196,20,152\nQk 18,0,1\nQk 19,2,3\nQmvva 196,48,156\nQe 4\nQmvva 192,20,144\n"
"s_add_u32 s69, s87, 129136\nQe 5\nQmvva 192,48,148\nbuffer_load_dwordx4 v62, s[52:55], s69 offen offset:2064 lds\nv_smfmac_f32_16x16x64_bf16 a[180:183], a[204:207], v[44:51], v77\nQk 16,4,5\nQcvaa 8,112,80,56\nQe 6\n"
"Qmvaa 8,116,84\nQe 7\nQmvaa 8,120,88\ns_add_u32 s69, s87, 193640\nQk 17,6,7\nQmvaa 8,124,92\nbuffer_load_dwordx4 v62, s[52:55], s69 offen offset:3096 lds\nQcvaa 12,140,92,56\n"
"Qmvaa 12,136,88\nQmvaa 12,132,84\nQmvaa 12,128,80\nQcvaa 0,128,96,56\nQda 128,68,34048\nQmvaa 0,132,100\nQda 132,68,34560\nQmvaa 0,136,104\n"
"Qda 136,68,34304\nQmvaa 0,140,108\nQda 140,68,34816\nQcvaa 4,124,108,56\nQda 124,68,33792\nQmvaa 4,120,104\nQda 120,68,33280\nQmvaa 4,116,100\n"
"Qda 116,68,33536\nQmvaa 4,112,96\nQda 112,68,33024\nQmvva 224,24,144\nQe 8\nQmvva 224,16,148\ns_add_u32 m0, s63, 33024\ns_add_u32 s88, s86, 0\n"
"Qe 9\nQmvva 228,24,152\nbuffer_load_dwordx4 v64, s[48:51], s88 offen lds\nQe 10\nQmvva 228,16,156\nQe 11\nQmvva 232,24,160\nQk 20,8,9\n"
"Qk 21,10,11\nQmvva 232,16,164\ns_add_u32 s88, s86, 19456\nQe 12\nQmvva 236,24,168\nbuffer_load_dwordx4 v64, s[48:51], s88 offen offset:1024 lds\nQe 13\nQmvva 236,16,172\n"
"s_waitcnt lgkmcnt(0)\nQk 22,12,13\nQcvaa 8,112,0,56\nQe 14\nQmvaa 8,116,4\nv_subrev_u32 v68, 66048, v68\ns_add_u32 s88, s86, 38912\nQmvaa 8,120,8\n"
"buffer_load_dwordx4 v64, s[48:51], s88 offen offset:2048 lds\nQe 15\nQmvaa 8,124,12\nQk 23,14,15\nQcvaa 12,140,12,56\nQe 0\nQmvaa 12,136,8\ns_add_u32 s88, s86, 58368\n"
"Qe 1\nQmvaa 12,132,4\nbuffer_load_dwordx4 v64, s[48:51], s88 offen offset:3072 lds\nQe 2\nQmvaa 12,128,0\nQe 3\nv_smfmac_f32_16x16x64_bf16 a[184:187], a[204:207], v[16:23], v77\nQk 26,0,1\n"
"Qmava 208,28,144\ns_add_u32 m0, s63, 37120\ns_add_u32 s88, s86, 81920\nQe 4\nQmava 208,20,148\nbuffer_load_dwordx4 v64, s[48:51], s88 offen lds\nQe 5\nQmava 212,28,152\n"
"Qe 6\nQmava 212,20,156\nQe 7\nQmava 216,28,160\ns_add_u32 s88, s86, 101376\nQk 27,2,3\nQk 24,4,5\nQmava 216,20,164\n"
"buffer_load_dwordx4 v64, s[48:51], s88 offen offset:1024 lds\nQk 25,6,7\nQmava 220,28,168\nQe 8\nQmava 220,20,172\nQe 9\nQmava 252,32,168\nQta 168,71,768\n"
"Qta 170,71,1792\ns_add_u32 s88, s86, 120832\nQmava 252,24,172\nbuffer_load_dwordx4 v64, s[48:51], s88 offen offset:2048 lds\nQe 10\nQmava 248,32,160\nQta 172,71,33792\nQta 174,71,34816\n"
"Qmava 248,24,164\nQe 11\nQmava 244,32,152\nQta 160,71,512\nQta 162,71,1536\nQmava 244,24,156\nQk 28,8,9\nQk 29,10,11\n"
"Qmava 240,32,144\nQta 164,71,33536\nQta 166,71,34560\nQmava 240,24,148\nQe 12\nQcvaa 0,112,16,56\nQta 152,71,256\nQta 154,71,1280\n"
"Qmvaa 0,116,20\nQe 13\nQmvaa 0,120,24\nQta 156,71,33280\nQta 158,71,34304\nQmvaa 0,124,28\nQe 14\nQcvaa 4,140,28,56\n"
"Qta 144,71,0\nQta 146,71,1024\nQmvaa 4,136,24\nQe 15\nQmvaa 4,132,20\nQta 148,71,33024\nQta 150,71,34048\nQmvaa 4,128,16\n"
"Qk 30,12,13\nQk 31,14,15\nQcvaa 8,128,32,56\nv_add_u32 v75, 1, v75\nQmvaa 8,132,36\nds_write_b32 v73, v75\nQmvaa 8,136,40\nQe 0\n"
"Qmvaa 8,140,44\nQe 1\nQcvaa 12,124,44,56\nQe 2\nQmvaa 12,120,40\nQe 3\nQmvaa 12,116,36\nQe 4\n"
"Qmvaa 12,112,32\ns_waitcnt lgkmcnt(1)\nQk 32,0,1\nv_smfmac_f32_16x16x64_bf16 a[188:191], a[204:207], v[24:31], v77\nQk 33,2,3\nQmvva 80,36,144\nQe 5\nQmvva 80,28,148\n"
"Qe 6\nQmvva 84,36,152\nQe 7\nQmvva 84,28,156\nQk 34,4,5\nQk 35,6,7\nQmvva 88,36,160\nQe 8\n"
"Qmvva 88,28,164\nQe 9\nQmvva 92,36,168\nQe 10\nQmvva 92,28,172\nQe 11\nQmvva 124,40,168\nQk 38,8,9\n"
"Qk 39,10,11\nQmvva 124,32,172\nds_read_b32 v66, v74\nQmvva 120,40,160\nQe 12\nQmvva 120,32,164\nQe 13\nQmvva 116,40,152\n"
"Qe 14\nQmvva 116,32,156\nQe 15\nQmvva 112,40,144\ns_waitcnt lgkmcnt(0)\nv_cmp_gt_u32 vcc, v75, v66\nQmvva 112,32,148\ns_cbranch_vccnz .Lsa31\n"
".Lra31:\nQk 36,12,13\nQcvaa 0,112,48,56\ns_add_u32 m0, s62, 33024\ns_add_u32 s69, s87, 524288\nQk 37,14,15\nQmvaa 0,116,52\nbuffer_load_dwordx4 v62, s[52:55], s69 offen lds\n"
"Qmvaa 0,120,56\nQmvaa 0,124,60\nQcvaa 4,140,60,56\nQmvaa 4,136,56\ns_add_u32 s69, s87, 588792\nQmvaa 4,132,52\nbuffer_load_dwordx4 v62, s[52:55], s69 offen offset:1032 lds\nQmvaa 4,128,48\n"
"Qcvaa 8,128,64,56\nQmvaa 8,132,68\nQe 0\nQmvaa 8,136,72\ns_add_u32 s69, s87, 653296\nQe 1\nQmvaa 8,140,76\nbuffer_load_dwordx4 v62, s[52:55], s69 offen offset:2064 lds\n"
"Qcvaa 12,124,76,56\nQe 2\nQmvaa 12,120,72\nQe 3\nQmvaa 12,116,68\nQe 4\nQmvaa 12,112,64\nQe 5\n"
"v_smfmac_f32_16x16x64_bf16 a[192:195], a[204:207], v[32:39], v77\ns_add_u32 s69, s87, 717800\nQk 40,0,1\nQmvva 144,44,144\nbuffer_load_dwordx4 v62, s[52:55], s69 offen offset:3096 lds\nQmvva 144,36,148\nQe 6\nQmvva 148,44,152\n"
"Qe 7\nQmvva 148,36,156\ns_add_u32 m0, s62, 49536\ns_add_u32 s69, s87, 524416\nQk 41,2,3\nQmvva 152,44,160\nbuffer_load_dwordx4 v62, s[52:55], s69 offen lds\nQmvva 152,36,164\n"
"Qk 42,4,5\nQk 43,6,7\nQmvva 156,44,168\nQe 8\nQmvva 156,36,172\nQe 9\nQmvva 188,48,168\ns_add_u32 s69, s87, 588920\n"
"Qe 10\nQmvva 188,40,172\nbuffer_load_dwordx4 v62, s[52:55], s69 offen offset:1032 lds\nQmvva 184,48,160\nQe 11\nQmvva 184,40,164\nQk 46,8,9\nQk 47,10,11\n"
"Qmvva 180,48,152\nQe 12\nQmvva 180,40,156\ns_add_u32 s69, s87, 653424\nQe 13\nQmvva 176,48,144\nbuffer_load_dwordx4 v62, s[52:55], s69 offen offset:2064 lds\nQmvva 176,40,148\n"
"Qe 14\nQcvaa 0,112,80,56\nQe 15\nQmvaa 0,116,84\nQk 44,12,13\nQk 45,14,15\nQmvaa 0,120,88\ns_add_u32 s69, s87, 717928\n"
"Qmvaa 0,124,92\nbuffer_load_dwordx4 v62, s[52:55], s69 offen offset:3096 lds\nQcvaa 4,140,92,56\nQmvaa 4,136,88\nQmvaa 4,132,84\nQmvaa 4,128,80\nQcvaa 8,128,96,56\nQmvaa 8,132,100\n"
"Qmvaa 8,136,104\nQmvaa 8,140,108\nQcvaa 12,124,108,56\nQmvaa 12,120,104\nQmvaa 12,116,100\nQmvaa 12,112,96\nv_smfmac_f32_16x16x64_bf16 a[196:199], a[204:207], v[40:47], v77\nQmvva 208,16,144\n"
"Qta 112,71,33152\nQe 0\nQmvva 208,44,148\nQta 114,71,34176\nQe 1\nQmvva 212,16,152\nQta 116,71,33408\nQe 2\n"
"Qmvva 212,44,156\nQta 118,71,34432\nQe 3\nQmvva 216,16,160\nQta 120,71,33664\nQk 48,0,1\nQk 49,2,3\nQmvva 216,44,164\n"
"Qta 122,71,34688\nQe 4\nQmvva 220,16,168\nQta 124,71,33920\nQe 5\nQmvva 220,44,172\nQta 126,71,34944\nQk 50,4,5\n"
"Qe 6\nQe 7\ns_nop 0\n.Lpg8:\ns_cbranch_execz .Ltr10\nQk 51,6,7\nQe 8\nQe 9\n"
"Qe 10\nQe 11\nQe 12\nQmvva 240,20,144\nv_add_u32 v75, 1, v75\nQe 13\nQmvva 240,48,148\nds_write_b32 v73, v75\n"
"Qe 14\nQmvva 244,20,152\nQe 15\nQmvva 244,48,156\nQk 54,8,9\nQk 55,10,11\nQmvva 248,20,160\nQk 52,12,13\n"
"Qk 53,14,15\nQmvva 248,48,164\nQmvva 252,20,168\nQmvva 252,48,172\nv_smfmac_f32_16x16x64_bf16 a[200:203], a[204:207], v[48:55], v77\nQmava 224,24,144\nds_read_b32 v66, v74\nv_accvgpr_read_b32 v0, a176\n"
"Qmava 224,52,148\nv_accvgpr_read_b32 v1, a177\nv_accvgpr_read_b32 v6, a178\nQmava 228,24,152\nv_accvgpr_read_b32 v7, a179\nv_accvgpr_read_b32 v8, a180\nQmava 228,52,156\nv_accvgpr_read_b32 v9, a181\n"
"v_accvgpr_read_b32 v14, a182\nQmava 232,24,160\nv_accvgpr_read_b32 v15, a183\nv_accvgpr_read_b32 v16, a184\nQmava 232,52,164\ns_waitcnt lgkmcnt(0)\nv_cmp_gt_u32 vcc, v75, v66\nQmava 236,24,168\n"
"s_cbranch_vccnz .Lsa32\n.Lra32:\nv_accvgpr_read_b32 v17, a185\nQmava 236,52,172\ns_waitcnt lgkmcnt(0)\nv_accvgpr_read_b32 v22, a186\nQmvva 96,28,112\ns_add_u32 m0, s63, 0\n"
"s_add_u32 s88, s86, 128\nv_accvgpr_read_b32 v23, a187\nQmvva 100,28,116\nbuffer_load_dwordx4 v64, s[48:51], s88 offen lds\nQmvva 104,28,120\nv_accvgpr_read_b32 v24, a188\nv_accvgpr_read_b32 v25, a189\nQmvva 108,28,124\n"
"v_accvgpr_read_b32 v2, a190\nv_accvgpr_read_b32 v3, a191\nQmvva 128,32,112\ns_add_u32 s88, s86, 19584\nv_accvgpr_read_b32 v4, a192\nQmvva 132,32,116\nbuffer_load_dwordx4 v64, s[48:51], s88 offen offset:1024 lds\nQmvva 136,32,120\n"
"v_accvgpr_read_b32 v5, a193\nv_accvgpr_read_b32 v10, a194\nQmvva 140,32,124\nv_accvgpr_read_b32 v11, a195\nv_accvgpr_read_b32 v12, a196\nQmvva 160,36,112\ns_add_u32 s88, s86, 39040\nv_accvgpr_read_b32 v13, a197\n"
"Qmvva 164,36,116\nbuffer_load_dwordx4 v64, s[48:51], s88 offen offset:2048 lds\nQmvva 168,36,120\nv_accvgpr_read_b32 v18, a198\nv_accvgpr_read_b32 v19, a199\nQmvva 172,36,124\nv_accvgpr_read_b32 v20, a200\nv_accvgpr_read_b32 v21, a201\n"
"Qmvva 192,40,112\ns_add_u32 s88, s86, 58496\nv_accvgpr_read_b32 v26, a202\nQmvva 196,40,116\nbuffer_load_dwordx4 v64, s[48:51], s88 offen offset:3072 lds\nQmvva 200,40,120\nv_accvgpr_read_b32 v27, a203\nQa 8,8,9\n"
"Qmvva 204,40,124\nQa 10,10,11\nQa 8,8,10\nQmvva 224,44,112\ns_add_u32 m0, s63, 4096\ns_add_u32 s88, s86, 82048\nQa 16,16,17\nQmvva 228,44,116\n"
"buffer_load_dwordx4 v64, s[48:51], s88 offen lds\nQmvva 232,44,120\nQa 18,18,19\nQa 16,16,18\nQmvva 236,44,124\nQa 4,4,5\nQa 6,6,7\nQmava 208,48,112\n"
"s_add_u32 s88, s86, 101504\nQa 4,4,6\nQmava 212,48,116\nbuffer_load_dwordx4 v64, s[48:51], s88 offen offset:1024 lds\nQmava 216,48,120\nQa 12,12,13\nQa 14,14,15\nQmava 220,48,124\n"
"Qa 12,12,14\nQa 0,0,1\nQmava 240,52,112\ns_add_u32 s88, s86, 120960\nQa 2,2,3\nQmava 244,52,116\nbuffer_load_dwordx4 v64, s[48:51], s88 offen offset:2048 lds\nQmava 248,52,120\n"
"Qa 0,0,2\nQa 24,24,25\nQmava 252,52,124\nQa 26,26,27\nQa 24,24,26\nQa 20,20,21\nQa 22,22,23\nQa 20,20,22\n"
"v_max3_u32 v9, v8, v16, v4\nv_max3_u32 v9, v9, v12, v0\nv_max3_u32 v9, v9, v24, v20\nv_min3_u32 v11, v8, v16, v4\nv_min3_u32 v11, v11, v12, v0\nv_min3_u32 v11, v11, v24, v20\nv_cmp_lt_u32 vcc, 0x4e800000, v9\ns_mov_b64 s[74:75], vcc\n"
"v_cmp_gt_u32 vcc, 0x35800000, v11\ns_or_b64 s[74:75], s[74:75], vcc\ns_cmp_lg_u64 s[74:75], 0\ns_cbranch_scc1 .Lfb\n.Lfbret0:\nv_mov_b32 v30, s73\nv_mov_b32 v31, 0\nv_mov_b32 v58, 0xb9800000\n"
"v_mov_b32 v59, 0\nv_rcp_f32 v2, v0\nv_rcp_f32 v6, v4\nv_mfma_f32_16x16x4_4b_bf16 v[80:95], v[30:31], v[58:59], v[80:95]\nv_rcp_f32 v10, v8\nv_rcp_f32 v14, v12\nv_rcp_f32 v18, v16\nv_rcp_f32 v22, v20\n"
"v_rcp_f32 v26, v24\nv_and_b32 v66, 16, v60\nv_mfma_f32_16x16x4_4b_bf16 v[96:111], v[30:31], v[58:59], v[96:111]\nv_mov_b32 v67, 0xffff\nv_lshlrev_b32 v66, v66, v67\ns_mov_b32 vcc_lo, 0x00c0000c\ns_mov_b32 vcc_hi, 0xc0000c00\nv_cndmask_b32 v67, 0, v66, vcc\n"
"s_lshr_b64 vcc, vcc, 2\nv_mfma_f32_16x16x4_4b_bf16 v[112:127], v[30:31], v[58:59], v[112:127]\nv_cndmask_b32 v66, 0, v66, vcc\nv_mov_b64 v[0:1], 0\nQk 2,2,2\nv_and_b32 v3, v2, v67\nv_and_b32 v2, v2, v66\nv_mov_b64 v[4:5], 0\n"
"v_mfma_f32_16x16x4_4b_bf16 v[128:143], v[30:31], v[58:59], v[128:143]\nQk 6,6,6\nv_and_b32 v7, v6, v67\nv_and_b32 v6, v6, v66\nv_mov_b64 v[8:9], 0\nQk 10,10,10\nv_and_b32 v11, v10, v67\nv_mfma_f32_16x16x4_4b_bf16 v[144:159], v[30:31], v[58:59], v[144:159]\n"
"v_and_b32 v10, v10, v66\nv_mov_b64 v[12:13], 0\nQk 14,14,14\nv_and_b32 v15, v14, v67\nv_and_b32 v14, v14, v66\nv_mov_b64 v[16:17], 0\nv_mfma_f32_16x16x4_4b_bf16 v[160:175], v[30:31], v[58:59], v[160:175]\nQk 18,18,18\n"
"v_and_b32 v19, v18, v67\nv_and_b32 v18, v18, v66\nv_mov_b64 v[20:21], 0\nQk 22,22,22\nv_and_b32 v23, v22, v67\nv_mfma_f32_16x16x4_4b_bf16 v[176:191], v[30:31], v[58:59], v[176:191]\nv_and_b32 v22, v22, v66\nv_mov_b64 v[24:25], 0\n"
"Qk 26,26,26\nv_and_b32 v27, v26, v67\nv_and_b32 v26, v26, v66\nv_mov_b64 v[28:29], 0\nv_mfma_f32_16x16x4_4b_bf16 v[192:207], v[30:31], v[58:59], v[192:207]\nQk 80,80,81\nQk 81,82,83\nQk 82,84,85\n"
"Qk 83,86,87\ns_nop 1\nQzvvv 84,80,0\nQzvvv 80,80,2\nQk 88,88,89\nQk 89,90,91\nQk 90,92,93\nQk 91,94,95\n"
"s_nop 1\nQzvvv 92,88,0\nQzvvv 88,88,2\nv_mfma_f32_16x16x4_4b_bf16 v[208:223], v[30:31], v[58:59], v[208:223]\nQk 96,96,97\nQk 97,98,99\nQk 98,100,101\nQk 99,102,103\n"
"Qk 80,80,81\nQk 81,82,83\nQzvvv 100,96,0\nQk 82,84,85\nQk 83,86,87\nQzvvv 96,96,2\nv_accvgpr_write_b32 a0, v80\nv_accvgpr_write_b32 a1, v81\n"
"v_accvgpr_write_b32 a2, v82\nv_accvgpr_write_b32 a3, v83\nv_mfma_f32_16x16x4_4b_bf16 v[224:239], v[30:31], v[58:59], v[224:239]\nQk 104,104,105\nQk 105,106,107\nQk 106,108,109\nQk 107,110,111\nQk 88,88,89\n"
"Qk 89,90,91\nQzvvv 108,104,0\nQk 90,92,93\nQk 91,94,95\nQzvvv 104,104,2\nv_accvgpr_write_b32 a4, v88\nv_accvgpr_write_b32 a5, v89\nv_accvgpr_write_b32 a6, v90\n"
"v_accvgpr_write_b32 a7, v91\nv_mfma_f32_16x16x4_4b_bf16 v[240:255], v[30:31], v[58:59], v[240:255]\nQk 112,112,113\nQk 113,114,115\nQk 114,116,117\nQk 115,118,119\nQk 96,96,97\nQk 97,98,99\n"
"Qzvvv 116,112,4\nQk 98,100,101\nQk 99,102,103\nQzvvv 112,112,6\nv_accvgpr_write_b32 a8, v96\nv_accvgpr_write_b32 a9, v97\nv_accvgpr_write_b32 a10, v98\nv_accvgpr_write_b32 a11, v99\n"
"v_mfma_f32_16x16x4_4b_bf16 a[208:223], v[30:31], v[58:59], a[208:223]\nQk 120,120,121\nQk 121,122,123\nQk 122,124,125\nQk 123,126,127\nQk 104,104,105\nQk 105,106,107\nQzvvv 124,120,4\n"
"Qk 106,108,109\nQk 107,110,111\nQzvvv 120,120,6\nv_accvgpr_write_b32 a12, v104\nv_accvgpr_write_b32 a13, v105\nv_accvgpr_write_b32 a14, v106\nv_accvgpr_write_b32 a15, v107\nv_mfma_f32_16x16x4_4b_bf16 a[224:239], v[30:31], v[58:59], a[224:239]\n"
"Qk 128,128,129\nQk 129,130,131\nQk 130,132,133\nQk 131,134,135\nQk 112,112,113\nQk 113,114,115\nQzvvv 132,128,4\nQk 114,116,117\n"
"Qk 115,118,119\nQzvvv 128,128,6\nv_accvgpr_write_b32 a16, v112\nv_accvgpr_write_b32 a17, v113\nv_accvgpr_write_b32 a18, v114\nv_accvgpr_write_b32 a19, v115\nv_mfma_f32_16x16x4_4b_bf16 a[240:255], v[30:31], v[58:59], a[240:255]\nQk 136,136,137\n"
"Qk 137,138,139\nQk 138,140,141\nQk 139,142,143\nQk 120,120,121\nQk 121,122,123\nQzvvv 140,136,4\nQk 122,124,125\nQk 123,126,127\n"
"Qzvvv 136,136,6\nv_accvgpr_write_b32 a20, v120\nv_accvgpr_write_b32 a21, v121\nv_accvgpr_write_b32 a22, v122\nv_accvgpr_write_b32 a23, v123\nQk 144,144,145\nQk 145,146,147\nQk 146,148,149\n"
"Qk 147,150,151\nQk 128,128,129\nQk 129,130,131\nQzvvv 148,144,8\nQk 130,132,133\nQk 131,134,135\nQzvvv 144,144,10\nv_accvgpr_write_b32 a24, v128\n"
"v_accvgpr_write_b32 a25, v129\nv_accvgpr_write_b32 a26, v130\nv_accvgpr_write_b32 a27, v131\nQk 152,152,153\nQk 153,154,155\nQk 154,156,157\nQk 155,158,159\nQk 136,136,137\n"
"Qk 137,138,139\nQzvvv 156,152,8\nQk 138,140,141\nQk 139,142,143\nQzvvv 152,152,10\nv_accvgpr_write_b32 a28, v136\nv_accvgpr_write_b32 a29, v137\nv_accvgpr_write_b32 a30, v138\n"
"v_accvgpr_write_b32 a31, v139\nQk 160,160,161\nQk 161,162,163\nQk 162,164,165\nQk 163,166,167\nQk 144,144,145\nQk 145,146,147\nQzvvv 164,160,8\n"
"Qk 146,148,149\nQk 147,150,151\nQzvvv 160,160,10\nv_accvgpr_write_b32 a32, v144\nv_accvgpr_write_b32 a33, v145\nv_accvgpr_write_b32 a34, v146\nv_accvgpr_write_b32 a35, v147\nQk 168,168,169\n"
"Qk 169,170,171\nQk 170,172,173\nQk 171,174,175\nQk 152,152,153\nQk 153,154,155\nQzvvv 172,168,8\nQk 154,156,157\nQk 155,158,159\n"
"Qzvvv 168,168,10\nv_accvgpr_write_b32 a36, v152\nv_accvgpr_write_b32 a37, v153\nv_accvgpr_write_b32 a38, v154\nv_accvgpr_write_b32 a39, v155\nQk 176,176,177\nQk 177,178,179\nQk 178,180,181\n"
"Qk 179,182,183\nQk 160,160,161\nQk 161,162,163\nQzvvv 180,176,12\nQk 162,164,165\nQk 163,166,167\nQzvvv 176,176,14\nv_accvgpr_write_b32 a40, v160\n"
"v_accvgpr_write_b32 a41, v161\nv_accvgpr_write_b32 a42, v162\nv_accvgpr_write_b32 a43, v163\nQk 184,184,185\nQk 185,186,187\nQk 186,188,189\nQk 187,190,191\nQk 168,168,169\n"
"Qk 169,170,171\nQzvvv 188,184,12\nQk 170,172,173\nQk 171,174,175\nQzvvv 184,184,14\nv_accvgpr_write_b32 a44, v168\nv_accvgpr_write_b32 a45, v169\nv_accvgpr_write_b32 a46, v170\n"
"v_accvgpr_write_b32 a47, v171\nQk 192,192,193\nQk 193,194,195\nQk 194,196,197\nQk 195,198,199\nQk 176,176,177\nQk 177,178,179\nQzvvv 196,192,12\n"
"Qk 178,180,181\nQk 179,182,183\nQzvvv 192,192,14\nv_accvgpr_write_b32 a48, v176\nv_accvgpr_write_b32 a49, v177\nv_accvgpr_write_b32 a50, v178\nv_accvgpr_write_b32 a51, v179\nQk 200,200,201\n"
"Qk 201,202,203\nQk 202,204,205\nQk 203,206,207\nQk 184,184,185\nQk 185,186,187\nQzvvv 204,200,12\nQk 186,188,189\nQk 187,190,191\n"
"Qzvvv 200,200,14\nv_accvgpr_write_b32 a52, v184\nv_accvgpr_write_b32 a53, v185\nv_accvgpr_write_b32 a54, v186\nv_accvgpr_write_b32 a55, v187\nQk 208,208,209\nQk 209,210,211\nQk 210,212,213\n"
"Qk 211,214,215\nQk 192,192,193\nQk 193,194,195\nQzvvv 212,208,16\nQk 194,196,197\nQk 195,198,199\nQzvvv 208,208,18\nv_accvgpr_write_b32 a56, v192\n"
"v_accvgpr_write_b32 a57, v193\nv_accvgpr_write_b32 a58, v194\nv_accvgpr_write_b32 a59, v195\nQk 216,216,217\nQk 217,218,219\nQk 218,220,221\nQk 219,222,223\nQk 200,200,201\n"
"Qk 201,202,203\nQzvvv 220,216,16\nQk 202,204,205\nQk 203,206,207\nQzvvv 216,216,18\nv_accvgpr_write_b32 a60, v200\nv_accvgpr_write_b32 a61, v201\nv_accvgpr_write_b32 a62, v202\n"
"v_accvgpr_write_b32 a63, v203\nQk 224,224,225\nQk 225,226,227\nQk 226,228,229\nQk 227,230,231\nQk 208,208,209\nQk 209,210,211\nQzvvv 228,224,16\n"
"Qk 210,212,213\nQk 211,214,215\nQzvvv 224,224,18\nv_accvgpr_write_b32 a64, v208\nv_accvgpr_write_b32 a65, v209\nv_accvgpr_write_b32 a66, v210\nv_accvgpr_write_b32 a67, v211\nQk 232,232,233\n"
"Qk 233,234,235\nQk 234,236,237\nQk 235,238,239\nQk 216,216,217\nQk 217,218,219\nQzvvv 236,232,16\nQk 218,220,221\nQk 219,222,223\n"
"Qzvvv 232,232,18\nv_accvgpr_write_b32 a68, v216\nv_accvgpr_write_b32 a69, v217\nv_accvgpr_write_b32 a70, v218\nv_accvgpr_write_b32 a71, v219\nQk 240,240,241\nQk 241,242,243\nQk 242,244,245\n"
"Qk 243,246,247\nQk 224,224,225\nQk 225,226,227\nQzvvv 244,240,20\nQk 226,228,229\nQk 227,230,231\nQzvvv 240,240,22\nv_accvgpr_write_b32 a72, v224\n"
"v_accvgpr_write_b32 a73, v225\nv_accvgpr_write_b32 a74, v226\nv_accvgpr_write_b32 a75, v227\nQk 248,248,249\nQk 249,250,251\nQk 250,252,253\nQk 251,254,255\nQk 232,232,233\n"
"Qk 233,234,235\nQzvvv 252,248,20\nQk 234,236,237\nQk 235,238,239\nQzvvv 248,248,22\nv_accvgpr_write_b32 a76, v232\nv_accvgpr_write_b32 a77, v233\nv_accvgpr_write_b32 a78, v234\n"
"v_accvgpr_write_b32 a79, v235\nv_accvgpr_read_b32 v48, a208\nv_accvgpr_read_b32 v49, a209\nv_accvgpr_read_b32 v50, a210\nv_accvgpr_read_b32 v51, a211\nv_accvgpr_read_b32 v36, a212\nv_accvgpr_read_b32 v37, a213\nv_accvgpr_read_b32 v38, a214\n"
"v_accvgpr_read_b32 v39, a215\nQk 48,48,49\nQk 49,50,51\nQk 50,36,37\nQk 51,38,39\nQk 240,240,241\nQk 241,242,243\nQzvvv 36,48,20\n"
"Qk 242,244,245\nQk 243,246,247\nQzvvv 48,48,22\nv_accvgpr_write_b32 a80, v240\nv_accvgpr_write_b32 a81, v241\nv_accvgpr_write_b32 a82, v242\nv_accvgpr_write_b32 a83, v243\nv_accvgpr_read_b32 v52, a216\n"
"v_accvgpr_read_b32 v53, a217\nv_accvgpr_read_b32 v54, a218\nv_accvgpr_read_b32 v55, a219\nv_accvgpr_read_b32 v40, a220\nv_accvgpr_read_b32 v41, a221\nv_accvgpr_read_b32 v42, a222\nv_accvgpr_read_b32 v43, a223\nQk 52,52,53\n"
"Qk 53,54,55\nQk 54,40,41\nQk 55,42,43\nQk 248,248,249\nQk 249,250,251\nQzvvv 40,52,20\nQk 250,252,253\nQk 251,254,255\n"
"Qzvvv 52,52,22\nv_accvgpr_write_b32 a84, v248\nv_accvgpr_write_b32 a85, v249\nv_accvgpr_write_b32 a86, v250\nv_accvgpr_write_b32 a87, v251\nv_accvgpr_read_b32 v44, a224\nv_accvgpr_read_b32 v45, a225\nv_accvgpr_read_b32 v46, a226\n"
"v_accvgpr_read_b32 v47, a227\nv_accvgpr_read_b32 v32, a228\nv_accvgpr_read_b32 v33, a229\nv_accvgpr_read_b32 v34, a230\nv_accvgpr_read_b32 v35, a231\nQk 44,44,45\n.Lpg9:\ns_cbranch_execz .Ltr10\n"
"Qk 45,46,47\nQk 46,32,33\nQk 47,34,35\nQk 48,48,49\nQk 49,50,51\nQzvvv 32,44,24\nQk 50,36,37\nQk 51,38,39\n"
"Qzvvv 44,44,26\nv_accvgpr_write_b32 a88, v48\nv_accvgpr_write_b32 a89, v49\nv_accvgpr_write_b32 a90, v50\nv_accvgpr_write_b32 a91, v51\nv_accvgpr_read_b32 v48, a232\nv_accvgpr_read_b32 v49, a233\nv_accvgpr_read_b32 v50, a234\n"
"v_accvgpr_read_b32 v51, a235\nv_accvgpr_read_b32 v36, a236\nv_accvgpr_read_b32 v37, a237\nv_accvgpr_read_b32 v38, a238\nv_accvgpr_read_b32 v39, a239\nQk 48,48,49\nQk 49,50,51\nQk 50,36,37\n"
"Qk 51,38,39\nQk 52,52,53\nQk 53,54,55\nQzvvv 36,48,24\nQk 54,40,41\nQk 55,42,43\nQzvvv 48,48,26\nv_accvgpr_write_b32 a92, v52\n"
"v_accvgpr_write_b32 a93, v53\nv_accvgpr_write_b32 a94, v54\nv_accvgpr_write_b32 a95, v55\nv_accvgpr_read_b32 v52, a240\nv_accvgpr_read_b32 v53, a241\nv_accvgpr_read_b32 v54, a242\nv_accvgpr_read_b32 v55, a243\nv_accvgpr_read_b32 v40, a244\n"
"v_accvgpr_read_b32 v41, a245\nv_accvgpr_read_b32 v42, a246\nv_accvgpr_read_b32 v43, a247\nQk 52,52,53\nQk 53,54,55\nQk 54,40,41\nQk 55,42,43\nQk 44,44,45\n"
"Qk 45,46,47\nQzvvv 40,52,24\nQk 46,32,33\nQk 47,34,35\nQzvvv 52,52,26\nv_accvgpr_write_b32 a96, v44\nv_accvgpr_write_b32 a97, v45\nv_accvgpr_write_b32 a98, v46\n"
"v_accvgpr_write_b32 a99, v47\nv_accvgpr_read_b32 v44, a248\nv_accvgpr_read_b32 v45, a249\nv_accvgpr_read_b32 v46, a250\nv_accvgpr_read_b32 v47, a251\nv_accvgpr_read_b32 v32, a252\nv_accvgpr_read_b32 v33, a253\nv_accvgpr_read_b32 v34, a254\n"
"v_accvgpr_read_b32 v35, a255\nQk 44,44,45\nQk 45,46,47\nQk 46,32,33\nQk 47,34,35\nQk 48,48,49\nQk 49,50,51\nQzvvv 32,44,24\n"
"Qk 50,36,37\nQk 51,38,39\nQzvvv 44,44,26\nv_accvgpr_write_b32 a100, v48\nv_accvgpr_write_b32 a101, v49\nv_accvgpr_write_b32 a102, v50\nv_accvgpr_write_b32 a103, v51\nQk 52,52,53\n"
"Qk 53,54,55\nQk 54,40,41\nQk 55,42,43\nv_accvgpr_write_b32 a104, v52\nv_accvgpr_write_b32 a105, v53\nv_accvgpr_write_b32 a106, v54\nv_accvgpr_write_b32 a107, v55\nQk 44,44,45\n"
"Qk 45,46,47\nQk 46,32,33\nQk 47,34,35\nv_accvgpr_write_b32 a108, v44\nv_accvgpr_write_b32 a109, v45\nv_accvgpr_write_b32 a110, v46\nv_accvgpr_write_b32 a111, v47\ns_mov_b32 s56, %8\n"
"s_and_b32 s57, %9, 0xffff\ns_mov_b32 s94, 0x7ffff000\ns_mul_i32 s68, %20, 40960\ns_lshl_b32 s69, s70, 10\ns_add_u32 s68, s68, s69\ns_add_u32 s68, s68, 4096\ns_add_u32 s94, %15, s68\ns_addc_u32 s95, %16, 0\n"
"s_and_b32 s95, s95, 0xffff\ns_mov_b32 s75, 37888\ns_mov_b32 s58, 4194304\ns_mov_b32 s67, 1\ns_mov_b64 exec, 0\ns_cmp_eq_u32 s70, 0\ns_cbranch_scc1 .Lpg12\ns_cmp_eq_u32 s70, 1\n"
"s_cbranch_scc1 .Lpg13\ns_cmp_eq_u32 s70, 2\ns_cbranch_scc1 .Lpg14\ns_branch .Lpg15\n.Ltr6:\ns_mov_b64 exec, -1\ns_branch .Lround\n.Lhook:\n"
".Lpg10:\ns_cbranch_execz .Ltr10\ns_cmp_eq_u32 s66, 1\ns_cbranch_scc1 .Lh1\ns_cmp_eq_u32 s66, 5\ns_cbranch_scc1 .Lh5\ns_cmp_eq_u32 s66, 6\ns_cbranch_scc1 .Lh6\n"
"s_cmp_eq_u32 s66, 9\ns_cbranch_scc1 .Lh9\ns_lshr_b32 s68, s99, s66\ns_cmp_eq_u32 s68, 1\ns_cbranch_scc1 .Lh12\ns_branch .Lh11\n.Lh1:\nds_read_b32 v78, v74 offset:16\n"
"s_cmp_eq_u32 s70, 0\ns_cselect_b32 s68, 128, 0\nv_mov_b32 v66, 0x7ffff000\nv_cmp_gt_u32 vcc, s68, v60\nv_not_b32 v67, v65\ns_lshl_b32 s68, %19, 2\nv_cndmask_b32 v66, v66, v76, vcc\nv_readlane_b32 s30, v67, %19\n"
"s_waitcnt lgkmcnt(0)\ns_nop 1\nv_add_u32_dpp v78, v78, v78 quad_perm:[1,0,3,2] row_mask:0xf bank_mask:0xf\ns_nop 1\nv_add_u32_dpp v78, v78, v78 quad_perm:[2,3,0,1] row_mask:0xf bank_mask:0xf\nv_ashrrev_i32 v78, 2, v78\nv_med3_i32 v78, v78, -7, 7\nv_add_lshl_u32 v78, v78, 8, 28\n"
"v_xor_b32 v67, v67, v78\nbuffer_store_dword v67, v66, s[24:27], s68 offen offset:1024 sc1\ns_branch .Lhookret\n.Lh9:\ns_lshl_b32 s68, %19, 4\ns_cmp_eq_u32 s70, 0\ns_cselect_b32 s68, s68, -1\nv_mov_b32 v67, 0x7ffff000\n"
"v_cmp_eq_u32 vcc, s68, v60\ns_nop 1\nv_cndmask_b32 v67, v67, v76, vcc\nv_add_u32 v66, 1, v65\nbuffer_store_dword v66, v67, s[24:27], 0 offen sc1\ns_branch .Lhookret\n.Lh5:\nv_bfe_u32 v66, v60, 4, 3\n"
"s_lshl_b32 s68, %19, 7\nv_lshlrev_b32 v66, 2, v66\ns_add_u32 s68, s68, 5242880\nbuffer_load_dword v78, v66, s[24:27], s68 offen offset:1024 sc1\ns_branch .Lhookret\n.Lh6:\ns_waitcnt vmcnt(32)\ns_mov_b32 s97, 0\n"
".Lack_l:\nv_xor_b32 v78, s30, v78\nv_and_b32 v66, 0x0fffffff, v78\nv_cmp_ne_u32 vcc, 0, v66\ns_cmp_eq_u64 vcc, 0\ns_cbranch_scc1 .Lack_ok\ns_sleep 1\nv_bfe_u32 v66, v60, 4, 3\n"
"s_lshl_b32 s68, %19, 7\nv_lshlrev_b32 v66, 2, v66\ns_add_u32 s68, s68, 5242880\nbuffer_load_dword v78, v66, s[24:27], s68 offen offset:1024 sc1\ns_waitcnt vmcnt(0)\ns_add_u32 s97, s97, 1\ns_cmp_lt_u32 s97, 1048576\ns_cbranch_scc1 .Lack_l\n"
".Lack_ok:\nv_lshrrev_b32 v78, 28, v78\ns_nop 1\nv_add_u32_dpp v78, v78, v78 quad_perm:[1,0,3,2] row_mask:0xf bank_mask:0xf\ns_nop 1\nv_add_u32_dpp v78, v78, v78 quad_perm:[2,3,0,1] row_mask:0xf bank_mask:0xf\ns_nop 1\nv_readlane_b32 s68, v78, %19\n"
"v_readlane_b32 s69, v78, 4\ns_cmp_lt_u32 s68, 32\ns_cselect_b32 s68, 8192, 0\ns_xor_b32 s68, s68, s98\ns_add_u32 s71, s71, s68\ns_cmp_lt_u32 s69, 32\ns_cselect_b32 s69, 8, 0\ns_xor_b32 s69, s69, %20\n"
"s_and_b32 s69, s69, 8\ns_lshl_b32 s98, s69, 10\ns_branch .Lhookret\n.Lh11:\nbuffer_load_dword v78, v76, s[24:27], 0 offen sc1\ns_branch .Lhookret\n.Lh12:\ns_waitcnt vmcnt(32)\n"
"v_sub_u32 v66, v78, v65\nv_cmp_gt_u32 vcc, 1, v66\ns_and_b64 vcc, vcc, s[28:29]\ns_cbranch_scc1 .Lf1_w\n.Lf1_r:\ns_cmp_lg_u32 s70, 3\ns_cbranch_scc1 .Lf1_n\nbuffer_inv sc1\n"
".Lf1_n:\ns_lshr_b32 s88, %19, 2\ns_lshl_b32 s88, s88, 4\ns_add_u32 s88, s88, s93\ns_andn2_b32 s68, s71, 786432\ns_add_u32 s48, %8, s68\ns_addc_u32 s49, %9, 0\ns_and_b32 s49, s49, 0xffff\n"
"s_sub_u32 s50, 4194304, s68\ns_mul_i32 s68, s88, 163840\ns_and_b32 s69, %19, 3\ns_mul_i32 s69, s69, 320\ns_add_u32 s68, s68, s69\ns_add_u32 s52, %15, s68\ns_addc_u32 s53, %16, 0\ns_and_b32 s53, s53, 0xffff\n"
"s_sub_u32 s54, 5246976, s68\ns_add_u32 s71, s71, 1048576\ns_and_b32 s87, %19, 3\ns_mul_i32 s87, s87, 327680\ns_add_u32 s87, s87, 1310720\ns_mul_i32 s69, s92, 10240\nv_bfe_u32 v66, v60, 5, 3\nv_mul_u32_u24 v66, 1280, v66\n"
"v_and_b32 v67, 16, v60\nv_add3_u32 v67, v67, v66, s69\ns_and_b32 s69, %19, 1\nv_lshrrev_b32 v66, 8, v60\ns_lshl_b32 s68, s93, 1\nv_xor_b32 v66, s68, v66\nv_lshl_add_u32 v66, v66, 5, v67\ns_lshr_b32 s68, 128, s69\n"
"v_add_u32 v61, s68, v66\ns_mul_i32 s68, s69, 192\nv_add_u32 v63, s68, v66\nv_bfe_u32 v66, v60, 8, 1\nv_xor_b32 v66, s93, v66\nv_lshl_add_u32 v67, v66, 5, v67\nv_lshrrev_b32 v66, 9, v60\nv_mul_u32_u24 v66, 20480, v66\n"
"s_xor_b32 s69, s69, 1\ns_lshl_b32 s69, s69, 8\nv_add3_u32 v62, v66, v67, s69\ns_branch .Lhookret\n.Lf1_w:\ns_mov_b32 s97, 0\n.Lf1_l:\ns_sleep 1\n"
"buffer_load_dword v78, v76, s[24:27], 0 offen sc1\ns_waitcnt vmcnt(0)\ns_add_u32 s97, s97, 1\ns_cmp_gt_u32 s97, 1048576\ns_cbranch_scc1 .Lf1_r\nv_sub_u32 v66, v78, v65\nv_cmp_gt_u32 vcc, 1, v66\ns_and_b64 vcc, vcc, s[28:29]\n"
"s_cbranch_scc1 .Lf1_l\ns_branch .Lf1_r\n.Lsp0:\ns_sub_u32 s75, s77, s74\ns_cmp_lt_u32 s75, 1024\ns_cbranch_scc0 .Lrp0\ns_memrealtime s[74:75]\ns_waitcnt lgkmcnt(0)\n"
"s_branch .Lsp0\n.Lsa1:\nds_read_b32 v66, v74\ns_waitcnt lgkmcnt(0)\nv_cmp_gt_u32 vcc, v75, v66\ns_cbranch_vccnz .Lsa1\ns_branch .Lra1\n.Lsp1:\n"
"s_sub_u32 s75, s77, s74\ns_cmp_lt_u32 s75, 1024\ns_cbranch_scc0 .Lrp1\ns_memrealtime s[74:75]\ns_waitcnt lgkmcnt(0)\ns_branch .Lsp1\n.Lsa2:\nds_read_b32 v66, v74\n"
"s_waitcnt lgkmcnt(0)\nv_cmp_gt_u32 vcc, v75, v66\ns_cbranch_vccnz .Lsa2\ns_branch .Lra2\n.Lsp2:\ns_sub_u32 s75, s77, s74\ns_cmp_lt_u32 s75, 1024\ns_cbranch_scc0 .Lrp2\n"
"s_memrealtime s[74:75]\ns_waitcnt lgkmcnt(0)\ns_branch .Lsp2\n.Lsa3:\nds_read_b32 v66, v74\ns_waitcnt lgkmcnt(0)\nv_cmp_gt_u32 vcc, v75, v66\ns_cbranch_vccnz .Lsa3\n"
"s_branch .Lra3\n.Lsa4:\nds_read_b32 v66, v74\ns_waitcnt lgkmcnt(0)\nv_cmp_gt_u32 vcc, v75, v66\ns_cbranch_vccnz .Lsa4\ns_branch .Lra4\n.Lsp4:\n"
"s_sub_u32 s75, s77, s74\ns_cmp_lt_u32 s75, 1024\ns_cbranch_scc0 .Lrp4\ns_memrealtime s[74:75]\ns_waitcnt lgkmcnt(0)\ns_branch .Lsp4\n.Lsa5:\nds_read_b32 v66, v74\n"
"s_waitcnt lgkmcnt(0)\nv_cmp_gt_u32 vcc, v75, v66\ns_cbranch_vccnz .Lsa5\ns_branch .Lra5\n.Lsa30:\nds_read_b32 v66, v74\ns_waitcnt lgkmcnt(0)\nv_cmp_gt_u32 vcc, v75, v66\n"
"s_cbranch_vccnz .Lsa30\ns_branch .Lra30\n.Lsa31:\nds_read_b32 v66, v74\ns_waitcnt lgkmcnt(0)\nv_cmp_gt_u32 vcc, v75, v66\ns_cbranch_vccnz .Lsa31\ns_branch .Lra31\n"
".Lsa32:\nds_read_b32 v66, v74\ns_waitcnt lgkmcnt(0)\nv_cmp_gt_u32 vcc, v75, v66\ns_cbranch_vccnz .Lsa32\ns_branch .Lra32\n.Lsa30x:\nds_read_b32 v66, v74\n"
"s_waitcnt lgkmcnt(0)\nv_cmp_gt_u32 vcc, v75, v66\ns_cbranch_vccnz .Lsa30x\ns_branch .Lra30x\n.Lsa31x:\nds_read_b32 v66, v74\ns_waitcnt lgkmcnt(0)\nv_cmp_gt_u32 vcc, v75, v66\n"
"s_cbranch_vccnz .Lsa31x\ns_branch .Lra31x\n.Lso40:\nds_read_b32 v66, v74\ns_waitcnt lgkmcnt(0)\nv_cmp_gt_u32 vcc, v75, v66\ns_cbranch_vccnz .Lso40\ns_branch .Lro40\n"
".Lso180:\nds_read_b32 v66, v74\ns_waitcnt lgkmcnt(0)\nv_cmp_gt_u32 vcc, v75, v66\ns_cbranch_vccnz .Lso180\ns_branch .Lro180\n.Lso320:\nds_read_b32 v66, v74\n"
"s_waitcnt lgkmcnt(0)\nv_cmp_gt_u32 vcc, v75, v66\ns_cbranch_vccnz .Lso320\ns_branch .Lro320\n.Lso460:\nds_read_b32 v66, v74\ns_waitcnt lgkmcnt(0)\nv_cmp_gt_u32 vcc, v75, v66\n"
"s_cbranch_vccnz .Lso460\ns_branch .Lro460\n.Lso600:\nds_read_b32 v66, v74\ns_waitcnt lgkmcnt(0)\nv_cmp_gt_u32 vcc, v75, v66\ns_cbranch_vccnz .Lso600\ns_branch .Lro600\n"
".Lso740:\nds_read_b32 v66, v74\ns_waitcnt lgkmcnt(0)\nv_cmp_gt_u32 vcc, v75, v66\ns_cbranch_vccnz .Lso740\ns_branch .Lro740\n.Lso880:\nds_read_b32 v66, v74\n"
"s_waitcnt lgkmcnt(0)\nv_cmp_gt_u32 vcc, v75, v66\ns_cbranch_vccnz .Lso880\ns_branch .Lro880\n.Lso1020:\nds_read_b32 v66, v74\ns_waitcnt lgkmcnt(0)\nv_cmp_gt_u32 vcc, v75, v66\n"
"s_cbranch_vccnz .Lso1020\ns_branch .Lro1020\n.Lso1160:\nds_read_b32 v66, v74\ns_waitcnt lgkmcnt(0)\nv_cmp_gt_u32 vcc, v75, v66\ns_cbranch_vccnz .Lso1160\ns_branch .Lro1160\n"
".Lso1300:\nds_read_b32 v66, v74\ns_waitcnt lgkmcnt(0)\nv_cmp_gt_u32 vcc, v75, v66\ns_cbranch_vccnz .Lso1300\ns_branch .Lro1300\n.Lso1440:\nds_read_b32 v66, v74\n"
"s_waitcnt lgkmcnt(0)\nv_cmp_gt_u32 vcc, v75, v66\ns_cbranch_vccnz .Lso1440\ns_branch .Lro1440\n.Lso1455:\nds_read_b32 v66, v74\ns_waitcnt lgkmcnt(0)\nv_cmp_gt_u32 vcc, v75, v66\n"
"s_cbranch_vccnz .Lso1455\ns_branch .Lro1455\n.Lso1525r0:\nds_read_b32 v66, v74\ns_waitcnt lgkmcnt(0)\nv_cmp_gt_u32 vcc, v75, v66\ns_cbranch_vccnz .Lso1525r0\ns_branch .Lro1525r0\n"
".Lso1595r0:\nds_read_b32 v66, v74\ns_waitcnt lgkmcnt(0)\nv_cmp_gt_u32 vcc, v75, v66\ns_cbranch_vccnz .Lso1595r0\ns_branch .Lro1595r0\n.Lso1665r0:\nds_read_b32 v66, v74\n"
"s_waitcnt lgkmcnt(0)\nv_cmp_gt_u32 vcc, v75, v66\ns_cbranch_vccnz .Lso1665r0\ns_branch .Lro1665r0\n.Lso1735r0:\nds_read_b32 v66, v74\ns_waitcnt lgkmcnt(0)\nv_cmp_gt_u32 vcc, v75, v66\n"
"s_cbranch_vccnz .Lso1735r0\ns_branch .Lro1735r0\n.Lso1805r0:\nds_read_b32 v66, v74\ns_waitcnt lgkmcnt(0)\nv_cmp_gt_u32 vcc, v75, v66\ns_cbranch_vccnz .Lso1805r0\ns_branch .Lro1805r0\n"
".Lso1878r0:\nds_read_b32 v66, v74\ns_waitcnt lgkmcnt(0)\nv_cmp_gt_u32 vcc, v75, v66\ns_cbranch_vccnz .Lso1878r0\ns_branch .Lro1878r0\n.Lso1920r0:\nds_read_b32 v66, v74\n"
"s_waitcnt lgkmcnt(0)\nv_cmp_gt_u32 vcc, v75, v66\ns_cbranch_vccnz .Lso1920r0\ns_branch .Lro1920r0\n.Lso1960r0:\nds_read_b32 v66, v74\ns_waitcnt lgkmcnt(0)\nv_cmp_gt_u32 vcc, v75, v66\n"
"s_cbranch_vccnz .Lso1960r0\ns_branch .Lro1960r0\n.Lso2046r0:\nds_read_b32 v66, v74\ns_waitcnt lgkmcnt(0)\nv_cmp_gt_u32 vcc, v75, v66\ns_cbranch_vccnz .Lso2046r0\ns_branch .Lro2046r0\n"
".Lso2088r0:\nds_read_b32 v66, v74\ns_waitcnt lgkmcnt(0)\nv_cmp_gt_u32 vcc, v75, v66\ns_cbranch_vccnz .Lso2088r0\ns_branch .Lro2088r0\n.Lso2172r0:\nds_read_b32 v66, v74\n"
"s_waitcnt lgkmcnt(0)\nv_cmp_gt_u32 vcc, v75, v66\ns_cbranch_vccnz .Lso2172r0\ns_branch .Lro2172r0\n.Lso2222r0:\nds_read_b32 v66, v74\ns_waitcnt lgkmcnt(0)\nv_cmp_gt_u32 vcc, v75, v66\n"
"s_cbranch_vccnz .Lso2222r0\ns_branch .Lro2222r0\n.Lso1525r1:\nds_read_b32 v66, v74\ns_waitcnt lgkmcnt(0)\nv_cmp_gt_u32 vcc, v75, v66\ns_cbranch_vccnz .Lso1525r1\ns_branch .Lro1525r1\n"
".Lso1595r1:\nds_read_b32 v66, v74\ns_waitcnt lgkmcnt(0)\nv_cmp_gt_u32 vcc, v75, v66\ns_cbranch_vccnz .Lso1595r1\ns_branch .Lro1595r1\n.Lso1665r1:\nds_read_b32 v66, v74\n"
"s_waitcnt lgkmcnt(0)\nv_cmp_gt_u32 vcc, v75, v66\ns_cbranch_vccnz .Lso1665r1\ns_branch .Lro1665r1\n.Lso1735r1:\nds_read_b32 v66, v74\ns_waitcnt lgkmcnt(0)\nv_cmp_gt_u32 vcc, v75, v66\n"
"s_cbranch_vccnz .Lso1735r1\ns_branch .Lro1735r1\n.Lso1805r1:\nds_read_b32 v66, v74\ns_waitcnt lgkmcnt(0)\nv_cmp_gt_u32 vcc, v75, v66\ns_cbranch_vccnz .Lso1805r1\ns_branch .Lro1805r1\n"
".Lso1840r1:\nds_read_b32 v66, v74\ns_waitcnt lgkmcnt(0)\nv_cmp_gt_u32 vcc, v75, v66\ns_cbranch_vccnz .Lso1840r1\ns_branch .Lro1840r1\n.Lso1878r1:\nds_read_b32 v66, v74\n"
"s_waitcnt lgkmcnt(0)\nv_cmp_gt_u32 vcc, v75, v66\ns_cbranch_vccnz .Lso1878r1\ns_branch .Lro1878r1\n.Lso1920r1:\nds_read_b32 v66, v74\ns_waitcnt lgkmcnt(0)\nv_cmp_gt_u32 vcc, v75, v66\n"
"s_cbranch_vccnz .Lso1920r1\ns_branch .Lro1920r1\n.Lso1945r1:\nds_read_b32 v66, v74\ns_waitcnt lgkmcnt(0)\nv_cmp_gt_u32 vcc, v75, v66\ns_cbranch_vccnz .Lso1945r1\ns_branch .Lro1945r1\n"
".Lso2004r1:\nds_read_b32 v66, v74\ns_waitcnt lgkmcnt(0)\nv_cmp_gt_u32 vcc, v75, v66\ns_cbranch_vccnz .Lso2004r1\ns_branch .Lro2004r1\n.Lso2045r1:\nds_read_b32 v66, v74\n"
"s_waitcnt lgkmcnt(0)\nv_cmp_gt_u32 vcc, v75, v66\ns_cbranch_vccnz .Lso2045r1\ns_branch .Lro2045r1\n.Lso2130r1:\nds_read_b32 v66, v74\ns_waitcnt lgkmcnt(0)\nv_cmp_gt_u32 vcc, v75, v66\n"
"s_cbranch_vccnz .Lso2130r1\ns_branch .Lro2130r1\n.Lso2172r1:\nds_read_b32 v66, v74\ns_waitcnt lgkmcnt(0)\nv_cmp_gt_u32 vcc, v75, v66\ns_cbranch_vccnz .Lso2172r1\ns_branch .Lro2172r1\n"
".Lso2225r1:\nds_read_b32 v66, v74\ns_waitcnt lgkmcnt(0)\nv_cmp_gt_u32 vcc, v75, v66\ns_cbranch_vccnz .Lso2225r1\ns_branch .Lro2225r1\n.Ltail1:\n.Lpg11:\n"
"s_cbranch_execz .Ltr10\nQcvaa 0,128,32,56\nv_add_u32 v75, 1, v75\nQmvaa 0,132,36\ns_waitcnt vmcnt(0)\nds_write_b32 v73, v75\nQmvaa 0,136,40\nQe 8\n"
"Qmvaa 0,140,44\nQe 9\nQcvaa 4,124,44,56\nQe 10\nQmvaa 4,120,40\nQe 11\nQmvaa 4,116,36\nQe 12\n"
"Qmvaa 4,112,32\ns_waitcnt lgkmcnt(1)\nQk 40,8,9\nQmvva 96,44,144\nQe 13\nQmvva 96,36,148\nQe 14\nQmvva 100,44,152\n"
"Qe 15\nQmvva 100,36,156\nQk 41,10,11\nQk 42,12,13\nQmvva 104,44,160\nQk 43,14,15\nQmvva 104,36,164\nQe 0\n"
"Qmvva 108,44,168\nQe 1\nQmvva 108,36,172\nQe 2\nQmvva 140,48,168\nQe 3\nQmvva 140,40,172\nQk 46,0,1\n"
"Qk 47,2,3\nQmvva 136,48,160\nds_read_b32 v66, v74\nQmvva 136,40,164\nQe 4\nQmvva 132,48,152\nQe 5\nQmvva 132,40,156\n"
"Qe 6\nQmvva 128,48,144\nQe 7\nQmvva 128,40,148\ns_waitcnt lgkmcnt(0)\nv_cmp_gt_u32 vcc, v75, v66\nv_smfmac_f32_16x16x64_bf16 a[176:179], a[204:207], v[36:43], v77\ns_cbranch_vccnz .Lsa30x\n"
".Lra30x:\nQcvaa 8,112,48,56\ns_add_u32 m0, s96, 0\ns_add_u32 s95, s87, 4096\nQk 44,4,5\nQmvaa 8,116,52\nbuffer_load_dwordx4 v61, s[52:55], s95 offen lds\nQmvaa 8,120,56\n"
"Qk 45,6,7\nQmvaa 8,124,60\nQcvaa 12,140,60,56\nQmvaa 12,136,56\ns_add_u32 s95, s87, 23552\nQmvaa 12,132,52\nbuffer_load_dwordx4 v61, s[52:55], s95 offen offset:1024 lds\nQmvaa 12,128,48\n"
"Qcvaa 0,128,64,56\nQmvaa 0,132,68\nQe 8\nQmvaa 0,136,72\ns_add_u32 m0, s96, 8192\ns_add_u32 s95, s87, 45056\nQe 9\nQmvaa 0,140,76\n"
"buffer_load_dwordx4 v61, s[52:55], s95 offen lds\nQcvaa 4,124,76,56\nQe 10\nQmvaa 4,120,72\nQe 11\nQmvaa 4,116,68\nQe 12\nQmvaa 4,112,64\n"
"s_add_u32 s95, s87, 64512\nQe 13\nQmvva 160,52,144\nbuffer_load_dwordx4 v61, s[52:55], s95 offen offset:1024 lds\nQmvva 160,44,148\nQe 14\nQmvva 164,52,152\nQe 15\n"
"Qmvva 164,44,156\nQk 48,8,9\nQk 49,10,11\nQmvva 168,52,160\ns_add_u32 m0, s96, 16384\ns_add_u32 s95, s87, 4096\nQk 50,12,13\nQmvva 168,44,164\n"
"buffer_load_dwordx4 v62, s[52:55], s95 offen lds\nQmvva 172,52,168\nQk 51,14,15\nQmvva 172,44,172\nQe 0\nQmvva 204,20,168\nQe 1\nQmvva 204,48,172\n"
"s_add_u32 s95, s87, 44032\nQe 2\nQmvva 200,20,160\nbuffer_load_dwordx4 v62, s[52:55], s95 offen offset:1024 lds\nQmvva 200,48,164\nQe 3\nQmvva 196,20,152\nQk 18,0,1\n"
"Qk 19,2,3\nQmvva 196,48,156\nQe 4\nQmvva 192,20,144\ns_add_u32 m0, s96, 24576\ns_add_u32 s95, s87, 4096\nQe 5\nQmvva 192,48,148\n"
"buffer_load_dwordx4 v63, s[52:55], s95 offen lds\nv_smfmac_f32_16x16x64_bf16 a[180:183], a[204:207], v[44:51], v77\nQk 16,4,5\nQcvaa 8,112,80,56\nQe 6\nQmvaa 8,116,84\nQe 7\nQmvaa 8,120,88\n"
"s_add_u32 s95, s87, 23552\nQk 17,6,7\nQmvaa 8,124,92\nbuffer_load_dwordx4 v63, s[52:55], s95 offen offset:1024 lds\nQcvaa 12,140,92,56\nQmvaa 12,136,88\nQmvaa 12,132,84\nQmvaa 12,128,80\n"
"s_add_u32 m0, s96, 32768\ns_add_u32 s95, s87, 45056\nQcvaa 0,128,96,56\nQda 128,68,34048\nbuffer_load_dwordx4 v63, s[52:55], s95 offen lds\nQmvaa 0,132,100\nQda 132,68,34560\nQmvaa 0,136,104\n"
"Qda 136,68,34304\nQmvaa 0,140,108\nQda 140,68,34816\nQcvaa 4,124,108,56\nQda 124,68,33792\ns_add_u32 s95, s87, 64512\nQmvaa 4,120,104\nQda 120,68,33280\n"
"buffer_load_dwordx4 v63, s[52:55], s95 offen offset:1024 lds\nQmvaa 4,116,100\nQda 116,68,33536\n.Lpg12:\ns_cbranch_execz .Ltr6\nQmvaa 4,112,96\nQda 112,68,33024\nQmvva 224,24,144\n"
"Qe 8\nQmvva 224,16,148\ns_add_u32 m0, s96, 40960\ns_mov_b32 s95, 4096\nQe 9\nQmvva 228,24,152\nbuffer_load_dwordx4 v61, s[52:55], s95 offen lds\nQe 10\n"
"Qmvva 228,16,156\nQe 11\nQmvva 232,24,160\nQk 20,8,9\nQk 21,10,11\nQmvva 232,16,164\nQe 12\nQmvva 236,24,168\n"
"s_mov_b32 s95, 23552\nQe 13\nQmvva 236,16,172\nbuffer_load_dwordx4 v61, s[52:55], s95 offen offset:1024 lds\ns_waitcnt lgkmcnt(0)\nQk 22,12,13\nQcvaa 8,112,0,56\nQe 14\n"
"Qmvaa 8,116,4\nv_subrev_u32 v68, 66048, v68\nQmvaa 8,120,8\nQe 15\nQmvaa 8,124,12\ns_add_u32 m0, s96, 49152\ns_mov_b32 s95, 45056\nQk 23,14,15\n"
"Qcvaa 12,140,12,56\nbuffer_load_dwordx4 v61, s[52:55], s95 offen lds\nQe 0\nQmvaa 12,136,8\nQe 1\nQmvaa 12,132,4\nQe 2\nQmvaa 12,128,0\n"
"Qe 3\nv_smfmac_f32_16x16x64_bf16 a[184:187], a[204:207], v[16:23], v77\ns_mov_b32 s95, 64512\nQk 26,0,1\nQmava 208,28,144\nbuffer_load_dwordx4 v61, s[52:55], s95 offen offset:1024 lds\nQe 4\nQmava 208,20,148\n"
"Qe 5\nQmava 212,28,152\nQe 6\nQmava 212,20,156\nQe 7\nQmava 216,28,160\ns_add_u32 m0, s96, 57344\ns_mov_b32 s95, 4096\n"
"Qk 27,2,3\nQk 24,4,5\nQmava 216,20,164\nbuffer_load_dwordx4 v62, s[52:55], s95 offen lds\nQk 25,6,7\nQmava 220,28,168\nQe 8\nQmava 220,20,172\n"
"Qe 9\nQmava 252,32,168\nQta 168,71,768\nQta 170,71,1792\nQmava 252,24,172\ns_mov_b32 s95, 44032\nQe 10\nQmava 248,32,160\n"
"Qta 172,71,33792\nQta 174,71,34816\nbuffer_load_dwordx4 v62, s[52:55], s95 offen offset:1024 lds\nQmava 248,24,164\nQe 11\nQmava 244,32,152\nQta 160,71,512\nQta 162,71,1536\n"
"Qmava 244,24,156\nQk 28,8,9\nQk 29,10,11\nQmava 240,32,144\nQta 164,71,33536\nQta 166,71,34560\ns_add_u32 m0, s96, 132096\ns_mov_b32 s95, 4096\n"
"Qmava 240,24,148\nbuffer_load_dwordx4 v63, s[52:55], s95 offen lds\nQe 12\nQcvaa 0,112,16,56\nQta 152,71,256\nQta 154,71,1280\nQmvaa 0,116,20\nQe 13\n"
"Qmvaa 0,120,24\nQta 156,71,33280\nQta 158,71,34304\nQmvaa 0,124,28\ns_mov_b32 s95, 23552\nQe 14\nQcvaa 4,140,28,56\nQta 144,71,0\n"
"Qta 146,71,1024\nbuffer_load_dwordx4 v63, s[52:55], s95 offen offset:1024 lds\nQmvaa 4,136,24\nQe 15\nQmvaa 4,132,20\nQta 148,71,33024\nQta 150,71,34048\nQmvaa 4,128,16\n"
"Qk 30,12,13\nQk 31,14,15\nQcvaa 8,128,32,56\nv_add_u32 v75, 1, v75\nQmvaa 8,132,36\nds_write_b32 v73, v75\nQmvaa 8,136,40\nQe 0\n"
"Qmvaa 8,140,44\nQe 1\nQcvaa 12,124,44,56\nQe 2\nQmvaa 12,120,40\nQe 3\nQmvaa 12,116,36\nQe 4\n"
"Qmvaa 12,112,32\ns_waitcnt lgkmcnt(1)\nQk 32,0,1\nv_smfmac_f32_16x16x64_bf16 a[188:191], a[204:207], v[24:31], v77\nQk 33,2,3\nQmvva 80,36,144\nQe 5\nQmvva 80,28,148\n"
"Qe 6\nQmvva 84,36,152\nQe 7\nQmvva 84,28,156\nQk 34,4,5\nQk 35,6,7\nQmvva 88,36,160\nQe 8\n"
"Qmvva 88,28,164\nQe 9\nQmvva 92,36,168\nQe 10\nQmvva 92,28,172\nQe 11\nQmvva 124,40,168\nQk 38,8,9\n"
"Qk 39,10,11\nQmvva 124,32,172\nds_read_b32 v66, v74\nQmvva 120,40,160\nQe 12\nQmvva 120,32,164\nQe 13\nQmvva 116,40,152\n"
"Qe 14\nQmvva 116,32,156\nQe 15\nQmvva 112,40,144\ns_waitcnt lgkmcnt(0)\nv_cmp_gt_u32 vcc, v75, v66\nQmvva 112,32,148\ns_cbranch_vccnz .Lsa31x\n"
".Lra31x:\nQk 36,12,13\nQcvaa 0,112,48,56\ns_add_u32 m0, s96, 140288\ns_mov_b32 s95, 45056\nQk 37,14,15\nQmvaa 0,116,52\nbuffer_load_dwordx4 v63, s[52:55], s95 offen lds\n"
"Qmvaa 0,120,56\nQmvaa 0,124,60\nQcvaa 4,140,60,56\nQmvaa 4,136,56\ns_mov_b32 s95, 64512\nQmvaa 4,132,52\nbuffer_load_dwordx4 v63, s[52:55], s95 offen offset:1024 lds\nQmvaa 4,128,48\n"
"Qcvaa 8,128,64,56\nQmvaa 8,132,68\nQe 0\nQmvaa 8,136,72\ns_add_u32 m0, s96, 148480\ns_add_u32 s95, s87, 86016\nQe 1\nQmvaa 8,140,76\n"
"buffer_load_dwordx4 v61, s[52:55], s95 offen lds\nQcvaa 12,124,76,56\nQe 2\nQmvaa 12,120,72\nQe 3\nQmvaa 12,116,68\nQe 4\nQmvaa 12,112,64\n"
"Qe 5\nv_smfmac_f32_16x16x64_bf16 a[192:195], a[204:207], v[32:39], v77\ns_add_u32 s95, s87, 105472\nQk 40,0,1\nQmvva 144,44,144\nbuffer_load_dwordx4 v61, s[52:55], s95 offen offset:1024 lds\nQmvva 144,36,148\nQe 6\n"
"Qmvva 148,44,152\nQe 7\nQmvva 148,36,156\ns_add_u32 m0, s96, 65536\ns_add_u32 s95, s87, 126976\nQk 41,2,3\nQmvva 152,44,160\nbuffer_load_dwordx4 v61, s[52:55], s95 offen lds\n"
"Qmvva 152,36,164\nQk 42,4,5\nQk 43,6,7\nQmvva 156,44,168\nQe 8\nQmvva 156,36,172\nQe 9\nQmvva 188,48,168\n"
"s_add_u32 s95, s87, 146432\nQe 10\nQmvva 188,40,172\nbuffer_load_dwordx4 v61, s[52:55], s95 offen offset:1024 lds\nQmvva 184,48,160\nQe 11\nQmvva 184,40,164\nQk 46,8,9\n"
"Qk 47,10,11\nQmvva 180,48,152\nQe 12\nQmvva 180,40,156\ns_add_u32 m0, s96, 73728\ns_add_u32 s95, s87, 86016\nQe 13\nQmvva 176,48,144\n"
"buffer_load_dwordx4 v62, s[52:55], s95 offen lds\nQmvva 176,40,148\nQe 14\nQcvaa 0,112,80,56\nQe 15\nQmvaa 0,116,84\nQk 44,12,13\nQk 45,14,15\n"
"Qmvaa 0,120,88\ns_add_u32 s95, s87, 125952\nQmvaa 0,124,92\nbuffer_load_dwordx4 v62, s[52:55], s95 offen offset:1024 lds\nQcvaa 4,140,92,56\nQmvaa 4,136,88\nQmvaa 4,132,84\nQmvaa 4,128,80\n"
"s_add_u32 m0, s96, 81920\ns_add_u32 s95, s87, 86016\nQcvaa 8,128,96,56\nbuffer_load_dwordx4 v63, s[52:55], s95 offen lds\nQmvaa 8,132,100\nQmvaa 8,136,104\nQmvaa 8,140,108\nQcvaa 12,124,108,56\n"
"s_add_u32 s95, s87, 105472\nQmvaa 12,120,104\nbuffer_load_dwordx4 v63, s[52:55], s95 offen offset:1024 lds\nQmvaa 12,116,100\nQmvaa 12,112,96\nv_smfmac_f32_16x16x64_bf16 a[196:199], a[204:207], v[40:47], v77\nQmvva 208,16,144\nQta 112,71,33152\n"
"s_add_u32 m0, s96, 90112\ns_add_u32 s95, s87, 126976\nQe 0\nQmvva 208,44,148\nQta 114,71,34176\nbuffer_load_dwordx4 v63, s[52:55], s95 offen lds\nQe 1\nQmvva 212,16,152\n"
"Qta 116,71,33408\nQe 2\nQmvva 212,44,156\nQta 118,71,34432\nQe 3\nQmvva 216,16,160\nQta 120,71,33664\nQk 48,0,1\n"
"Qk 49,2,3\nQmvva 216,44,164\nQta 122,71,34688\ns_add_u32 s95, s87, 146432\nQe 4\nQmvva 220,16,168\nQta 124,71,33920\nbuffer_load_dwordx4 v63, s[52:55], s95 offen offset:1024 lds\n"
"Qe 5\nQmvva 220,44,172\nQta 126,71,34944\nQk 50,4,5\nQe 6\nQe 7\ns_nop 0\nQk 51,6,7\n"
"Qe 8\nQe 9\nQe 10\nQe 11\nQe 12\nQmvva 240,20,144\nQe 13\nQmvva 240,48,148\n"
"Qe 14\nQmvva 244,20,152\nQe 15\nQmvva 244,48,156\nQk 54,8,9\nQk 55,10,11\nQmvva 248,20,160\nQk 52,12,13\n"
"Qk 53,14,15\nQmvva 248,48,164\nQmvva 252,20,168\nQmvva 252,48,172\nv_smfmac_f32_16x16x64_bf16 a[200:203], a[204:207], v[48:55], v77\nQmava 224,24,144\nv_accvgpr_read_b32 v0, a176\nv_accvgpr_read_b32 v1, a177\n"
"Qmava 224,52,148\nv_accvgpr_read_b32 v6, a178\nv_accvgpr_read_b32 v7, a179\nQmava 228,24,152\nv_accvgpr_read_b32 v8, a180\nv_accvgpr_read_b32 v9, a181\nQmava 228,52,156\nv_accvgpr_read_b32 v14, a182\n"
"v_accvgpr_read_b32 v15, a183\nQmava 232,24,160\nv_accvgpr_read_b32 v16, a184\nv_accvgpr_read_b32 v17, a185\nQmava 232,52,164\nv_accvgpr_read_b32 v22, a186\nv_accvgpr_read_b32 v23, a187\nQmava 236,24,168\n"
"v_accvgpr_read_b32 v2, a190\nv_accvgpr_read_b32 v3, a191\nQmava 236,52,172\ns_waitcnt lgkmcnt(0)\nv_accvgpr_read_b32 v24, a188\nQmvva 96,28,112\nv_accvgpr_read_b32 v25, a189\nv_accvgpr_read_b32 v4, a192\n"
"Qmvva 100,28,116\nv_accvgpr_read_b32 v5, a193\nv_accvgpr_read_b32 v10, a194\nQmvva 104,28,120\nv_accvgpr_read_b32 v11, a195\nv_accvgpr_read_b32 v12, a196\nQmvva 108,28,124\nv_accvgpr_read_b32 v13, a197\n"
"v_accvgpr_read_b32 v18, a198\nQmvva 128,32,112\nv_accvgpr_read_b32 v19, a199\nv_accvgpr_read_b32 v20, a200\nQmvva 132,32,116\nv_accvgpr_read_b32 v21, a201\nv_accvgpr_read_b32 v26, a202\nQmvva 136,32,120\n"
"v_accvgpr_read_b32 v27, a203\nQa 8,8,9\nQmvva 140,32,124\nQa 10,10,11\nQa 8,8,10\nQmvva 160,36,112\nQa 16,16,17\nQa 18,18,19\n"
"Qmvva 164,36,116\nQa 16,16,18\nQa 4,4,5\nQmvva 168,36,120\nQa 6,6,7\nQa 4,4,6\nQmvva 172,36,124\nQa 12,12,13\n"
"Qa 14,14,15\nQmvva 192,40,112\nQa 12,12,14\nQa 0,0,1\nQmvva 196,40,116\nQa 2,2,3\nQa 0,0,2\nQmvva 200,40,120\n"
"Qa 24,24,25\nQa 26,26,27\nQmvva 204,40,124\nQa 24,24,26\nQa 20,20,21\nQmvva 224,44,112\nQa 22,22,23\nQa 20,20,22\n"
"Qmvva 228,44,116\nv_max3_u32 v9, v8, v16, v4\nv_max3_u32 v9, v9, v12, v0\nQmvva 232,44,120\nv_max3_u32 v9, v9, v24, v20\nv_min3_u32 v11, v8, v16, v4\nQmvva 236,44,124\nv_min3_u32 v11, v11, v12, v0\n"
"v_min3_u32 v11, v11, v24, v20\nQmava 208,48,112\nQmava 212,48,116\nQmava 216,48,120\nQmava 220,48,124\nQmava 240,52,112\nQmava 244,52,116\nQmava 248,52,120\n"
"Qmava 252,52,124\nv_cmp_lt_u32 vcc, 0x4e800000, v9\ns_mov_b64 s[74:75], vcc\nv_cmp_gt_u32 vcc, 0x35800000, v11\ns_or_b64 s[74:75], s[74:75], vcc\ns_cmp_lg_u64 s[74:75], 0\ns_cbranch_scc1 .Lfb\n.Lfbret1:\n"
"s_mov_b32 s56, %8\ns_and_b32 s57, %9, 0xffff\ns_mov_b32 s58, 4194304\ns_lshl_b32 s68, %19, 4\ns_cmp_eq_u32 s70, 0\ns_cselect_b32 s68, s68, -1\nv_mov_b32 v77, 0x7ffff000\nv_cmp_eq_u32 vcc, s68, v60\n"
"s_nop 1\nv_cndmask_b32 v77, v77, v76, vcc\ns_lshl_b32 s68, s93, 12\ns_add_u32 s68, s68, %10\nv_lshrrev_b32 v66, 9, v60\nv_lshlrev_b32 v66, 11, v66\nv_bfe_u32 v67, v60, 1, 8\nv_add3_u32 v68, v66, v67, s68\n"
"v_add_u32 v69, 65536, v68\nv_add_u32 v70, 131072, v68\ns_mov_b64 exec, 0\ns_cmp_eq_u32 s70, 0\ns_cbranch_scc1 .Lpg16\ns_cmp_eq_u32 s70, 1\ns_cbranch_scc1 .Lpg17\ns_cmp_eq_u32 s70, 2\n"
"s_cbranch_scc1 .Lpg18\ns_branch .Lpg19\n.Ltr8:\ns_mov_b64 exec, -1\nv_mov_b32 v30, s73\nv_mov_b32 v31, 0\nv_mov_b32 v58, 0xb9800000\nv_mov_b32 v59, 0\n"
"v_rcp_f32 v2, v0\nv_rcp_f32 v6, v4\nv_mfma_f32_16x16x4_4b_bf16 v[80:95], v[30:31], v[58:59], v[80:95]\nv_rcp_f32 v10, v8\nv_rcp_f32 v14, v12\nv_rcp_f32 v18, v16\nv_rcp_f32 v22, v20\nv_rcp_f32 v26, v24\n"
"v_and_b32 v66, 16, v60\nv_mfma_f32_16x16x4_4b_bf16 v[112:127], v[30:31], v[58:59], v[112:127]\nv_mov_b32 v67, 0xffff\nv_lshlrev_b32 v66, v66, v67\ns_mov_b32 vcc_lo, 0x00c0000c\ns_mov_b32 vcc_hi, 0xc0000c00\nv_cndmask_b32 v67, 0, v66, vcc\ns_lshr_b64 vcc, vcc, 2\n"
"v_mfma_f32_16x16x4_4b_bf16 v[144:159], v[30:31], v[58:59], v[144:159]\nv_cndmask_b32 v66, 0, v66, vcc\nv_mov_b64 v[0:1], 0\nQk 2,2,2\nv_and_b32 v3, v2, v67\nv_and_b32 v2, v2, v66\nv_mov_b64 v[4:5], 0\nv_mfma_f32_16x16x4_4b_bf16 v[176:191], v[30:31], v[58:59], v[176:191]\n"
"Qk 6,6,6\nv_and_b32 v7, v6, v67\nv_and_b32 v6, v6, v66\nv_mov_b64 v[8:9], 0\nQk 10,10,10\nv_and_b32 v11, v10, v67\nv_mfma_f32_16x16x4_4b_bf16 v[208:223], v[30:31], v[58:59], v[208:223]\nv_and_b32 v10, v10, v66\n"
"v_mov_b64 v[12:13], 0\nQk 14,14,14\nv_and_b32 v15, v14, v67\nv_and_b32 v14, v14, v66\nv_mov_b64 v[16:17], 0\nv_mfma_f32_16x16x4_4b_bf16 v[240:255], v[30:31], v[58:59], v[240:255]\nQk 18,18,18\nv_and_b32 v19, v18, v67\n"
"v_and_b32 v18, v18, v66\nv_mov_b64 v[20:21], 0\nQk 22,22,22\nv_and_b32 v23, v22, v67\nv_mfma_f32_16x16x4_4b_bf16 a[224:239], v[30:31], v[58:59], a[224:239]\nv_and_b32 v22, v22, v66\nv_mov_b64 v[24:25], 0\nQk 26,26,26\n"
"v_and_b32 v27, v26, v67\nv_and_b32 v26, v26, v66\nv_mov_b64 v[28:29], 0\nv_mfma_f32_16x16x4_4b_bf16 v[96:111], v[30:31], v[58:59], v[96:111]\nQk 80,80,81\nQk 81,82,83\nQk 82,84,85\nQk 83,86,87\n"
"s_nop 1\nQzvvv 84,80,0\nQzvvv 80,80,2\nQk 112,112,113\nQk 113,114,115\nQk 114,116,117\nQk 115,118,119\ns_nop 1\n"
"Qzvvv 116,112,4\nQzvvv 112,112,6\nv_mfma_f32_16x16x4_4b_bf16 v[128:143], v[30:31], v[58:59], v[128:143]\nQk 144,144,145\nQk 145,146,147\nQk 146,148,149\nQk 147,150,151\nQk 80,80,81\n"
"Qk 81,82,83\nQzvvv 148,144,8\nQk 82,84,85\nQk 83,86,87\nQzvvv 144,144,10\nv_mfma_f32_16x16x4_4b_bf16 v[160:175], v[30:31], v[58:59], v[160:175]\nQk 176,176,177\nQk 177,178,179\n"
"Qk 178,180,181\nQk 179,182,183\nQk 112,112,113\nQk 113,114,115\nQzvvv 180,176,12\nQk 114,116,117\nQk 115,118,119\nQzvvv 176,176,14\n"
"v_mfma_f32_16x16x4_4b_bf16 v[192:207], v[30:31], v[58:59], v[192:207]\nQk 208,208,209\nQk 209,210,211\nQk 210,212,213\nQk 211,214,215\nQk 144,144,145\nQk 145,146,147\nQzvvv 212,208,16\n"
"Qk 146,148,149\nQk 147,150,151\nQzvvv 208,208,18\nv_mfma_f32_16x16x4_4b_bf16 v[224:239], v[30:31], v[58:59], v[224:239]\nQk 240,240,241\nQk 241,242,243\nQk 242,244,245\nQk 243,246,247\n"
"Qk 176,176,177\nQk 177,178,179\nQzvvv 244,240,20\nQk 178,180,181\nQk 179,182,183\nQzvvv 240,240,22\nv_mfma_f32_16x16x4_4b_bf16 a[208:223], v[30:31], v[58:59], a[208:223]\nv_accvgpr_read_b32 v84, a224\n"
"v_accvgpr_read_b32 v85, a225\nv_accvgpr_read_b32 v86, a226\nv_accvgpr_read_b32 v87, a227\nv_accvgpr_read_b32 v32, a228\nv_accvgpr_read_b32 v33, a229\nv_accvgpr_read_b32 v34, a230\nv_accvgpr_read_b32 v35, a231\nQk 84,84,85\n"
"Qk 85,86,87\nQk 86,32,33\nQk 87,34,35\nQk 208,208,209\nQk 209,210,211\nQzvvv 32,84,24\nQk 210,212,213\nQk 211,214,215\n"
"Qzvvv 84,84,26\nv_mfma_f32_16x16x4_4b_bf16 a[240:255], v[30:31], v[58:59], a[240:255]\nQk 88,88,89\nQk 89,90,91\nQk 90,92,93\nQk 91,94,95\nQk 240,240,241\nQk 241,242,243\n"
"Qzvvv 92,88,0\nQk 242,244,245\n.Lpg13:\ns_cbranch_execz .Ltr6\nQk 243,246,247\nQzvvv 88,88,2\nQk 120,120,121\nQk 121,122,123\n"
"Qk 122,124,125\nQk 123,126,127\nQk 84,84,85\nQk 85,86,87\nQzvvv 124,120,4\nQk 86,32,33\nQk 87,34,35\nQzvvv 120,120,6\n"
"Qk 152,152,153\nQk 153,154,155\nQk 154,156,157\nQk 155,158,159\nQk 88,88,89\nQk 89,90,91\nQzvvv 156,152,8\nQk 90,92,93\n"
"Qk 91,94,95\nQzvvv 152,152,10\ns_waitcnt vmcnt(20)\ns_barrier\nQk 184,184,185\nQk 185,186,187\nQk 186,188,189\nQk 187,190,191\n"
"Qk 120,120,121\nQk 121,122,123\nQzvvv 188,184,12\nQk 122,124,125\nQk 123,126,127\nQzvvv 184,184,14\nQta 56,68,0\nQk 216,216,217\n"
"Qk 217,218,219\nQk 218,220,221\nQk 219,222,223\nQk 152,152,153\nQk 153,154,155\nQzvvv 220,216,16\nQk 154,156,157\nQk 155,158,159\n"
"Qzvvv 216,216,18\nQta 58,68,1024\nQk 248,248,249\nQk 249,250,251\nQk 250,252,253\nQk 251,254,255\nQk 184,184,185\nQk 185,186,187\n"
"Qzvvv 252,248,20\nQk 186,188,189\nQk 187,190,191\nQzvvv 248,248,22\nQta 60,68,8192\nv_accvgpr_read_b32 v116, a232\nv_accvgpr_read_b32 v117, a233\nv_accvgpr_read_b32 v118, a234\n"
"v_accvgpr_read_b32 v119, a235\nv_accvgpr_read_b32 v36, a236\nv_accvgpr_read_b32 v37, a237\nv_accvgpr_read_b32 v38, a238\nv_accvgpr_read_b32 v39, a239\nQk 116,116,117\nQk 117,118,119\nQk 118,36,37\n"
"Qk 119,38,39\nQk 216,216,217\nQk 217,218,219\nQzvvv 36,116,24\nQk 218,220,221\nQk 219,222,223\nQzvvv 116,116,26\nQta 62,68,9216\n"
"s_mov_b32 s31, 0\nQk 96,96,97\nQk 97,98,99\nQk 98,100,101\nQk 99,102,103\nQk 248,248,249\nQk 249,250,251\nQzvvv 100,96,0\n"
"Qk 250,252,253\nQk 251,254,255\nQzvvv 96,96,2\nbuffer_load_dwordx4 a[0:3], v79, s[48:51], s31 offen offset:0\nQta 64,68,256\nQk 128,128,129\nQk 129,130,131\nQk 130,132,133\n"
"Qk 131,134,135\nQk 116,116,117\nQk 117,118,119\nQzvvv 132,128,4\nQk 118,36,37\nQk 119,38,39\nQzvvv 128,128,6\nbuffer_load_dwordx4 a[4:7], v79, s[48:51], s31 offen offset:1024\n"
"Qta 66,68,1280\ns_waitcnt lgkmcnt(2)\nQzvav 56,56,80\nQmvav 56,60,88\nQk 160,160,161\nQk 161,162,163\nQk 162,164,165\nQk 163,166,167\n"
"Qk 96,96,97\nQk 97,98,99\nQzvvv 164,160,8\nQk 98,100,101\nQk 99,102,103\nQzvvv 160,160,10\nbuffer_load_dwordx4 a[8:11], v79, s[48:51], s31 offen offset:2048\nQta 68,68,8448\n"
"Qk 192,192,193\nQk 193,194,195\nQk 194,196,197\nQk 195,198,199\nQk 128,128,129\nQk 129,130,131\nQzvvv 196,192,12\nQk 130,132,133\n"
"Qk 131,134,135\nQzvvv 192,192,14\nbuffer_load_dwordx4 a[12:15], v79, s[48:51], s31 offen offset:3072\nQta 70,68,9472\nv_max3_f32 v66, |v56|, |v57|, |v58|\nv_max_f32 v66, v66, |v59|\nv_mov_b32 v67, v66\ns_mov_b32 s31, 4096\n"
"Qk 224,224,225\nQk 225,226,227\nQk 226,228,229\nQk 227,230,231\nQk 160,160,161\nQk 161,162,163\nQzvvv 228,224,16\nQk 162,164,165\n"
"Qk 163,166,167\nQzvvv 224,224,18\nbuffer_load_dwordx4 a[16:19], v79, s[48:51], s31 offen offset:0\nv_permlane32_swap_b32 v66, v67\nv_max_f32 v66, v66, v67\nv_mov_b32 v67, v66\nv_accvgpr_read_b32 v148, a208\nv_accvgpr_read_b32 v149, a209\n"
"v_accvgpr_read_b32 v150, a210\nv_accvgpr_read_b32 v151, a211\nv_accvgpr_read_b32 v36, a212\nv_accvgpr_read_b32 v37, a213\nv_accvgpr_read_b32 v38, a214\nv_accvgpr_read_b32 v39, a215\nQk 148,148,149\nQk 149,150,151\n"
"Qk 150,36,37\nQk 151,38,39\nQk 192,192,193\nQk 193,194,195\nQzvvv 36,148,20\nQk 194,196,197\nQk 195,198,199\nQzvvv 148,148,22\n"
"buffer_load_dwordx4 a[20:23], v79, s[48:51], s31 offen offset:1024\nv_permlane16_swap_b32 v66, v67\nv_max_f32 v66, v66, v67\nv_accvgpr_read_b32 v180, a240\nv_accvgpr_read_b32 v181, a241\nv_accvgpr_read_b32 v182, a242\nv_accvgpr_read_b32 v183, a243\nv_accvgpr_read_b32 v40, a244\n"
"v_accvgpr_read_b32 v41, a245\nv_accvgpr_read_b32 v42, a246\nv_accvgpr_read_b32 v43, a247\nQk 180,180,181\nQk 181,182,183\nQk 182,40,41\nQk 183,42,43\nQk 224,224,225\n"
"Qk 225,226,227\nQzvvv 40,180,24\nQk 226,228,229\nQk 227,230,231\nQzvvv 180,180,26\nbuffer_load_dwordx4 a[24:27], v79, s[48:51], s31 offen offset:2048\nv_max_f32_dpp v66, v66, v66 row_ror:8 row_mask:0xf bank_mask:0xf\ns_mov_b32 s31, 16384\n"
"Qk 104,104,105\nQk 105,106,107\nQk 106,108,109\nQk 107,110,111\nQk 148,148,149\nQk 149,150,151\nQzvvv 108,104,0\nQk 150,36,37\n"
"Qk 151,38,39\nQzvvv 104,104,2\nbuffer_load_dwordx4 a[28:31], v79, s[48:51], s31 offen offset:0\nv_max_f32_dpp v66, v66, v66 row_ror:4 row_mask:0xf bank_mask:0xf\nQk 136,136,137\nQk 137,138,139\nQk 138,140,141\nQk 139,142,143\n"
"Qk 180,180,181\nQk 181,182,183\nQzvvv 140,136,4\nQk 182,40,41\nQk 183,42,43\nQzvvv 136,136,6\nbuffer_load_dwordx4 a[32:35], v79, s[48:51], s31 offen offset:1024\nv_max_f32_dpp v66, v66, v66 row_ror:2 row_mask:0xf bank_mask:0xf\n"
"Qk 168,168,169\nQk 169,170,171\nQk 170,172,173\nQk 171,174,175\nQk 104,104,105\nQk 105,106,107\nQzvvv 172,168,8\nQk 106,108,109\n"
"Qk 107,110,111\nQzvvv 168,168,10\nbuffer_load_dwordx4 a[36:39], v79, s[48:51], s31 offen offset:2048\nv_max_f32_dpp v66, v66, v66 row_ror:1 row_mask:0xf bank_mask:0xf\nv_and_b32 v66, 0x7f800000, v66\nv_add_u32 v66, 0x3000000, v66\nQk 200,200,201\nQk 201,202,203\n"
"Qk 202,204,205\nQk 203,206,207\nQk 136,136,137\nQk 137,138,139\nQzvvv 204,200,12\nQk 138,140,141\nQk 139,142,143\nQzvvv 200,200,14\n"
"buffer_load_dwordx4 a[40:43], v79, s[48:51], s31 offen offset:3072\nv_min_u32 v66, 0x7e000000, v66\nv_or_b32 v66, 0x400000, v66\nds_write_b32 v73, v66 offset:16\nv_xor_b32 v67, 0x80000000, v66\ns_mov_b32 s31, 20480\nQk 232,232,233\nQk 233,234,235\n"
"Qk 234,236,237\nQk 235,238,239\nQk 168,168,169\nQk 169,170,171\nQzvvv 236,232,16\nQk 170,172,173\nQk 171,174,175\nQzvvv 232,232,18\n"
"buffer_load_dwordx4 a[44:47], v79, s[48:51], s31 offen offset:0\nv_accvgpr_write_b32 a112, v67\nv_accvgpr_write_b32 a113, v67\nv_accvgpr_write_b32 a114, v67\nv_accvgpr_write_b32 a115, v67\nv_accvgpr_read_b32 v212, a216\nv_accvgpr_read_b32 v213, a217\nv_accvgpr_read_b32 v214, a218\n"
"v_accvgpr_read_b32 v215, a219\nv_accvgpr_read_b32 v40, a220\nv_accvgpr_read_b32 v41, a221\nv_accvgpr_read_b32 v42, a222\nv_accvgpr_read_b32 v43, a223\nQk 212,212,213\nQk 213,214,215\nQk 214,40,41\n"
"Qk 215,42,43\nQk 200,200,201\nQk 201,202,203\nQzvvv 40,212,20\nQk 202,204,205\nQk 203,206,207\nQzvvv 212,212,22\nbuffer_load_dwordx4 a[48:51], v79, s[48:51], s31 offen offset:1024\n"
"v_accvgpr_read_b32 v244, a248\nv_accvgpr_read_b32 v245, a249\nv_accvgpr_read_b32 v246, a250\nv_accvgpr_read_b32 v247, a251\nv_accvgpr_read_b32 v32, a252\nv_accvgpr_read_b32 v33, a253\nv_accvgpr_read_b32 v34, a254\nv_accvgpr_read_b32 v35, a255\n"
"Qk 244,244,245\nQk 245,246,247\nQk 246,32,33\nQk 247,34,35\nQk 232,232,233\nQk 233,234,235\nQzvvv 32,244,24\nQk 234,236,237\n"
"Qk 235,238,239\nQzvvv 244,244,26\nbuffer_load_dwordx4 a[52:55], v79, s[48:51], s31 offen offset:2048\nQk 212,212,213\nQk 213,214,215\nQk 214,40,41\nQk 215,42,43\ns_nop 2\n"
"Qk 244,244,245\nQk 245,246,247\nQk 246,32,33\nQk 247,34,35\ns_waitcnt lgkmcnt(5)\nQcaav 140,56,84,112\ns_add_u32 m0, s96, 98304\ns_mov_b32 s95, 86016\n"
"Qmaav 140,60,116\nbuffer_load_dwordx4 v61, s[52:55], s95 offen lds\nQcaav 136,56,240,112\nQmaav 136,60,248\nQcaav 132,56,208,112\nQta 72,68,16384\ns_mov_b32 s95, 105472\nQmaav 132,60,216\n"
"buffer_load_dwordx4 v61, s[52:55], s95 offen offset:1024 lds\nQcaav 128,56,176,112\nQmaav 128,60,184\nQta 74,68,16896\nQcaav 124,56,144,112\nQmaav 124,60,152\nQcaav 120,56,112,112\nQta 76,68,17408\n"
"Qmaav 120,60,120\ns_add_u32 s88, s71, 0\nQcaav 116,56,80,112\nbuffer_store_dwordx4 v[80:83], v79, s[56:59], s88 offen offset:0 sc1\nQmaav 116,60,88\nQta 78,68,17920\ns_waitcnt lgkmcnt(5)\nQcaav 144,64,80,112\n"
"Qmaav 144,68,88\nQcaav 148,64,112,112\nQmaav 148,68,120\nQcaav 152,64,144,112\nQta 56,68,24576\nQmaav 152,68,152\nQcaav 156,64,176,112\nQmaav 156,68,184\n"
"Qta 58,68,25600\ns_add_u32 m0, s96, 106496\ns_mov_b32 s95, 126976\nQcaav 160,64,208,112\nbuffer_load_dwordx4 v61, s[52:55], s95 offen lds\nQmaav 160,68,216\nQcaav 164,64,240,112\nQta 60,68,32768\n"
"Qmaav 164,68,248\ns_mov_b32 s95, 146432\nQcaav 168,64,84,112\nbuffer_load_dwordx4 v61, s[52:55], s95 offen offset:1024 lds\nQmaav 168,68,116\nQta 62,68,33792\ns_waitcnt lgkmcnt(4)\nQcaav 196,72,84,112\n"
"Qmaav 196,76,116\nQcaav 192,72,240,112\nQmaav 192,76,248\nQcaav 188,72,208,112\nQta 64,68,24832\nQmaav 188,76,216\nbuffer_store_dwordx4 v[112:115], v79, s[56:59], s88 offen offset:1024 sc1\nQcaav 184,72,176,112\n"
"Qmaav 184,76,184\nQta 66,68,25856\nQcaav 180,72,144,112\nQmaav 180,76,152\nQcaav 176,72,112,112\nQta 68,68,33024\nv_add_u32 v75, 1, v75\nQmaav 176,76,120\n"
"s_waitcnt vmcnt(34)\nds_write_b32 v73, v75\nQcaav 172,72,80,112\nQmaav 172,76,88\nQta 70,68,34048\ns_waitcnt lgkmcnt(5)\nQcaav 200,56,80,112\ns_add_u32 m0, s96, 114688\n"
"s_mov_b32 s95, 86016\nQmaav 200,60,88\nbuffer_load_dwordx4 v62, s[52:55], s95 offen lds\nQcaav 204,56,112,112\nQmaav 204,60,120\nQcaav 208,56,144,112\nQta 72,68,512\nQmaav 208,60,152\n"
"ds_read_b32 v66, v74\ns_mov_b32 s95, 125952\nQcaav 212,56,176,112\nbuffer_load_dwordx4 v62, s[52:55], s95 offen offset:1024 lds\nQmaav 212,60,184\nQta 74,68,1536\nQcaav 216,56,208,112\nQmaav 216,60,216\n"
"s_waitcnt lgkmcnt(1)\nv_cmp_gt_u32 vcc, v75, v66\nQcaav 220,56,240,112\ns_cbranch_vccnz .Lso40\n.Lro40:\nQmaav 220,60,248\nQta 76,68,8704\nQcaav 224,56,84,112\n"
"buffer_store_dwordx4 v[144:147], v79, s[56:59], s88 offen offset:2048 sc1\nv_xor_b32 v56, 8, v73\nQmaav 224,60,116\nds_read_b32 v56, v56 offset:16\ns_waitcnt lgkmcnt(5)\nQcaav 252,64,84,112\nQta 78,68,9728\nQmaav 252,68,116\n"
"Qcaav 248,64,240,112\nQmaav 248,68,248\nQcaav 244,64,208,112\nQta 56,68,768\nQmaav 244,68,216\nQcaav 240,64,176,112\ns_waitcnt lgkmcnt(2)\nv_mov_b32 v57, v56\n"
"v_mov_b32 v58, v56\nQmaav 240,68,184\nQta 58,68,1792\ns_add_u32 m0, s96, 122880\ns_mov_b32 s95, 86016\nQcaav 236,64,144,112\nbuffer_load_dwordx4 v63, s[52:55], s95 offen lds\nQmaav 236,68,152\n"
"v_mov_b32 v59, v56\nv_accvgpr_write_b32 a80, v56\nQcaav 232,64,112,112\nQta 60,68,8960\nv_accvgpr_write_b32 a81, v56\nQmaav 232,68,120\ns_mov_b32 s95, 105472\nv_accvgpr_write_b32 a82, v56\n"
"Qcaav 228,64,80,112\nbuffer_load_dwordx4 v63, s[52:55], s95 offen offset:1024 lds\nQmaav 228,68,88\nQta 62,68,9984\nv_accvgpr_write_b32 a83, v56\ns_waitcnt lgkmcnt(4)\nQcvav 0,72,80,56\nQmvav 0,76,88\n"
"Qcvav 4,72,112,56\nbuffer_store_dwordx4 v[176:179], v79, s[56:59], s88 offen offset:3072 sc1\nQmvav 4,76,120\nQcvav 8,72,144,56\nQta 64,68,16640\nQmvav 8,76,152\nQcvav 12,72,176,56\nQmvav 12,76,184\n"
"Qta 66,68,17152\nQcvav 16,72,208,56\nQmvav 16,76,216\nQcvav 20,72,240,56\nQta 68,68,17664\nQmvav 20,76,248\nQcvav 24,72,84,56\nQmvav 24,76,116\n"
"Qta 70,68,18176\ns_waitcnt lgkmcnt(4)\nQcvav 52,56,84,56\nQmvav 52,60,116\nQcvav 48,56,240,56\nQmvav 48,60,248\nQcvav 44,56,208,56\nQta 72,68,25088\n"
"Qmvav 44,60,216\nQcvav 40,56,176,56\nQmvav 40,60,184\nQta 74,68,26112\ns_add_u32 s88, s71, 4096\nQcvav 36,56,144,56\nbuffer_store_dwordx4 v[208:211], v79, s[56:59], s88 offen offset:0 sc1\nQmvav 36,60,152\n"
"Qcvav 32,56,112,56\nQta 76,68,33280\nQmvav 32,60,120\nQcvav 28,56,80,56\nQmvav 28,60,88\nQta 78,68,34304\ns_waitcnt lgkmcnt(4)\nQcvav 92,64,80,56\n"
"Qmvav 92,68,88\nQcvav 124,64,112,56\nQmvav 124,68,120\nQcvav 156,64,144,56\nQta 56,68,25344\nQmvav 156,68,152\nQcvav 188,64,176,56\nQmvav 188,68,184\n"
"Qta 58,68,26368\nQcvav 220,64,208,56\nQmvav 220,68,216\nQcvav 252,64,240,56\nQta 60,68,33536\nQmvav 252,68,248\nQcvav 100,64,84,56\nQmvav 100,68,116\n"
"Qta 62,68,34560\ns_waitcnt lgkmcnt(4)\nQcvav 172,72,84,56\nbuffer_store_dwordx4 v[240:243], v79, s[56:59], s88 offen offset:1024 sc1\nQmvav 172,76,116\nQcvav 140,72,240,56\nQmvav 140,76,248\nQcvav 108,72,208,56\n"
"Qta 64,68,40960\nQmvav 108,76,216\nQcvav 228,72,176,56\nQmvav 228,76,184\nQta 66,68,41984\nQcvav 196,72,144,56\nQmvav 196,76,152\nQcvav 164,72,112,56\n"
"Qta 68,68,49152\nQmvav 164,76,120\nQcvav 132,72,80,56\nQmvav 132,76,88\nQta 70,68,50176\ns_waitcnt lgkmcnt(4)\nQcvav 204,56,80,56\nQmvav 204,60,88\n"
"Qcvav 236,56,112,56\nQmvav 236,60,120\nQcaav 96,56,144,80\nQta 72,68,41216\n.Lpg14:\ns_cbranch_execz .Ltr6\nQmaav 96,60,152\nQcaav 100,56,176,80\n"
"buffer_store_dwordx4 v[84:87], v79, s[56:59], s88 offen offset:2048 sc1\nQmaav 100,60,184\nQta 74,68,42240\nQcaav 104,56,208,80\nQmaav 104,60,216\nQcaav 108,56,240,80\nQta 76,68,49408\nQmaav 108,60,248\n"
"Qcaav 112,56,84,80\nQmaav 112,60,116\nQta 78,68,50432\ns_waitcnt vmcnt(15)\ns_waitcnt lgkmcnt(4)\nQmaaa 140,64,24\nQmaaa 140,68,52\nQmaaa 136,64,20\n"
"Qmaaa 136,68,48\nQmaaa 132,64,16\nQta 56,68,41472\nQmaaa 132,68,44\nQmaaa 128,64,12\nQmaaa 128,68,40\nQta 58,68,42496\nQmaaa 124,64,8\n"
"Qmaaa 124,68,36\nQmaaa 120,64,4\nQta 60,68,49664\ns_and_b32 s68, %19, 3\nQmaaa 120,68,32\ns_add_u32 s88, s71, 16384\nQmaaa 116,64,0\nbuffer_store_dwordx4 v[88:91], v79, s[56:59], s88 offen offset:0 sc1\n"
"Qmaaa 116,68,28\nQta 62,68,50688\ns_waitcnt lgkmcnt(4)\nQmaaa 144,72,0\ns_cmp_lt_u32 s68, 1\ns_cselect_b32 s68, 40, 32\nQmaaa 144,76,28\ns_mul_i32 s67, s68, 40960\n"
"s_mul_i32 s74, s68, 32768\nQmaaa 148,72,4\ns_and_b32 s68, %19, 3\nQmaaa 148,76,32\ns_cmp_lt_u32 s68, 2\ns_cselect_b32 s68, 48, 40\nQmaaa 152,72,8\nQta 64,68,41728\n"
"s_mul_i32 s72, s68, 40960\nQmaaa 152,76,36\ns_mul_i32 s75, s68, 32768\ns_and_b32 s68, %19, 3\nQmaaa 156,72,12\ns_cmp_lt_u32 s68, 3\ns_cselect_b32 s68, 56, 48\nQmaaa 156,76,40\n"
"Qta 66,68,42752\ns_mul_i32 s73, s68, 40960\nQmaaa 160,72,16\ns_mul_i32 s77, s68, 32768\nv_add_u32 v58, s91, v60\nQmaaa 160,76,44\ns_mul_i32 s68, s93, 28672\ns_sub_u32 s68, 14336, s68\n"
"Qmaaa 164,72,20\nQta 68,68,49920\nv_add_u32 v59, s68, v58\nQmaaa 164,76,48\nQmaaa 168,72,24\nQmaaa 168,76,52\nQta 70,68,50944\ns_waitcnt lgkmcnt(4)\n"
"Qmvaa 24,56,24\nQmvaa 24,60,52\nQmvaa 20,56,20\nQmvaa 20,60,48\nQmvaa 16,56,16\nQta 72,68,57344\nQmvaa 16,60,44\nbuffer_store_dwordx4 v[120:123], v79, s[56:59], s88 offen offset:1024 sc1\n"
"Qmvaa 12,56,12\nQmvaa 12,60,40\nQta 74,68,57856\nQmvaa 8,56,8\nQmvaa 8,60,36\nQmvaa 4,56,4\nQta 76,68,58368\nv_add_u32 v75, 1, v75\n"
"Qmvaa 4,60,32\ns_waitcnt vmcnt(35)\nds_write_b32 v73, v75\nQmvaa 0,56,0\nQmvaa 0,60,28\nQta 78,68,58880\ns_waitcnt lgkmcnt(5)\nQmvaa 28,64,0\n"
"Qmvaa 28,68,28\nQmvaa 32,64,4\nQmvaa 32,68,32\nQmvaa 36,64,8\nQta 56,68,57600\nQmvaa 36,68,36\nds_read_b32 v66, v74\nQmvaa 40,64,12\n"
"Qmvaa 40,68,40\nQta 58,68,58112\nQmvaa 44,64,16\nQmvaa 44,68,44\ns_waitcnt lgkmcnt(1)\nv_cmp_gt_u32 vcc, v75, v66\nQmvaa 48,64,20\ns_cbranch_vccnz .Lso180\n"
".Lro180:\nQmvaa 48,68,48\nQta 60,68,58624\nQmvaa 52,64,24\nbuffer_store_dwordx4 v[152:155], v79, s[56:59], s88 offen offset:2048 sc1\nQmvaa 52,68,52\ns_waitcnt lgkmcnt(4)\nQmaaa 196,72,24\n"
"Qta 62,68,59136\nQmaaa 196,76,52\nQmaaa 192,72,20\nQmaaa 192,76,48\nQmaaa 188,72,16\nQta 64,70,1024\nQmaaa 188,76,44\nQmaaa 184,72,12\n"
"Qmaaa 184,76,40\nQta 66,70,2048\ns_add_u32 m0, s96, 0\ns_mov_b32 s95, 126976\nQmaaa 180,72,8\nbuffer_load_dwordx4 v63, s[52:55], s95 offen lds\nQmaaa 180,76,36\nQmaaa 176,72,4\n"
"Qta 68,70,9216\nQmaaa 176,76,32\ns_mov_b32 s95, 146432\nQmaaa 172,72,0\nbuffer_load_dwordx4 v63, s[52:55], s95 offen offset:1024 lds\nQmaaa 172,76,28\nQta 70,70,10240\ns_waitcnt lgkmcnt(4)\n"
"Qmvaa 92,56,0\nQmvaa 92,60,28\nQmvaa 124,56,4\nbuffer_store_dwordx4 v[184:187], v79, s[56:59], s88 offen offset:3072 sc1\nQmvaa 124,60,32\nQmvaa 156,56,8\nQta 72,70,1280\nQmvaa 156,60,36\n"
"Qmvaa 188,56,12\nQmvaa 188,60,40\nQta 74,70,2304\nQmvaa 220,56,16\nQmvaa 220,60,44\nQmvaa 252,56,20\nQta 76,70,9472\nQmvaa 252,60,48\n"
"s_add_u32 m0, s96, 8192\ns_mov_b32 s95, 331776\nQmvaa 100,56,24\nbuffer_load_dwordx4 v61, s[52:55], s95 offen lds\nQmvaa 100,60,52\nQta 78,70,10496\ns_waitcnt lgkmcnt(4)\nQmaaa 224,64,24\n"
"Qmaaa 224,68,52\ns_mov_b32 s95, 351232\nQmaaa 220,64,20\nbuffer_load_dwordx4 v61, s[52:55], s95 offen offset:1024 lds\nQmaaa 220,68,48\nQmaaa 216,64,16\nQta 56,70,1536\nQmaaa 216,68,44\n"
"Qmaaa 212,64,12\nQmaaa 212,68,40\nQta 58,70,2560\ns_add_u32 s88, s71, 20480\nQmaaa 208,64,8\nbuffer_store_dwordx4 v[216:219], v79, s[56:59], s88 offen offset:0 sc1\nQmaaa 208,68,36\nQmaaa 204,64,4\n"
"Qta 60,70,9728\nQmaaa 204,68,32\nQmaaa 200,64,0\nQmaaa 200,68,28\nQta 62,70,10752\ns_waitcnt lgkmcnt(4)\nQmaaa 228,72,0\nQmaaa 228,76,28\n"
"Qmaaa 232,72,4\nQmaaa 232,76,32\nQmaaa 236,72,8\nQta 64,70,1792\ns_add_u32 m0, s96, 16384\ns_mov_b32 s95, 372736\nQmaaa 236,76,36\nbuffer_load_dwordx4 v61, s[52:55], s95 offen lds\n"
"Qmaaa 240,72,12\nQmaaa 240,76,40\nQta 66,70,2816\nQmaaa 244,72,16\ns_mov_b32 s95, 392192\nQmaaa 244,76,44\nbuffer_load_dwordx4 v61, s[52:55], s95 offen offset:1024 lds\nQmaaa 248,72,20\n"
"Qta 68,70,9984\nQmaaa 248,76,48\nQmaaa 252,72,24\nQmaaa 252,76,52\nQta 70,70,11008\ns_waitcnt lgkmcnt(4)\nQmvaa 172,56,24\nbuffer_store_dwordx4 v[248:251], v79, s[56:59], s88 offen offset:1024 sc1\n"
"Qmvaa 172,60,52\nQmvaa 140,56,20\nQmvaa 140,60,48\nQmvaa 108,56,16\nQta 72,70,17408\nQmvaa 108,60,44\nQmvaa 228,56,12\nQmvaa 228,60,40\n"
"Qta 74,70,18432\nQmvaa 196,56,8\nQmvaa 196,60,36\nQmvaa 164,56,4\nQta 76,69,0\ns_add_u32 m0, s96, 24576\ns_mov_b32 s95, 331776\nQmvaa 164,60,32\n"
"buffer_load_dwordx4 v62, s[52:55], s95 offen lds\nQmvaa 132,56,0\nQmvaa 132,60,28\nQta 78,69,1024\ns_waitcnt lgkmcnt(4)\nQmvaa 204,64,0\ns_mov_b32 s95, 371712\nQmvaa 204,68,28\n"
"buffer_load_dwordx4 v62, s[52:55], s95 offen offset:1024 lds\nQmvaa 236,64,4\nQmvaa 236,68,32\nQmaaa 96,64,8\nQta 56,70,17664\nQmaaa 96,68,36\nQmaaa 100,64,12\nbuffer_store_dwordx4 v[116:119], v79, s[56:59], s88 offen offset:2048 sc1\n"
"Qmaaa 100,68,40\nQta 58,70,18688\nQmaaa 104,64,16\nQmaaa 104,68,44\nQmaaa 108,64,20\nQta 60,69,256\nQmaaa 108,68,48\nQmaaa 112,64,24\n"
"Qmaaa 112,68,52\nQta 62,69,1280\ns_waitcnt lgkmcnt(4)\nQmaav 140,72,180\nQmaav 140,76,244\ns_mov_b32 s31, 131072\nQmaav 136,72,148\nbuffer_load_dwordx4 a[0:3], v79, s[48:51], s31 offen offset:0\n"
"Qmaav 136,76,212\nQmaav 132,72,224\nQta 64,70,17920\nQmaav 132,76,232\nQmaav 128,72,192\nbuffer_load_dwordx4 a[4:7], v79, s[48:51], s31 offen offset:1024\nQmaav 128,76,200\nQta 66,70,18944\n"
"Qmaav 124,72,160\nQmaav 124,76,168\nQmaav 120,72,128\nQta 68,69,512\nQmaav 120,76,136\nbuffer_load_dwordx4 a[8:11], v79, s[48:51], s31 offen offset:2048\nQmaav 116,72,96\nQmaav 116,76,104\n"
"Qta 70,69,1536\ns_waitcnt lgkmcnt(4)\nQmaav 144,56,96\nQmaav 144,60,104\nbuffer_load_dwordx4 a[12:15], v79, s[48:51], s31 offen offset:3072\nQmaav 148,56,128\nQmaav 148,60,136\nQmaav 152,56,160\n"
"Qta 72,70,18176\ns_mov_b32 s31, 135168\nQmaav 152,60,168\nbuffer_load_dwordx4 a[16:19], v79, s[48:51], s31 offen offset:0\nQmaav 156,56,192\nQmaav 156,60,200\nQta 74,70,19200\nQmaav 160,56,224\n"
"Qmaav 160,60,232\nbuffer_load_dwordx4 a[20:23], v79, s[48:51], s31 offen offset:1024\nQmaav 164,56,148\nQta 76,69,768\nQmaav 164,60,212\nQmaav 168,56,180\nQmaav 168,60,244\nQta 78,69,1792\n"
"s_waitcnt lgkmcnt(4)\nQmvav 24,64,180\nbuffer_load_dwordx4 a[24:27], v79, s[48:51], s31 offen offset:2048\nQmvav 24,68,244\nQmvav 20,64,148\nQmvav 20,68,212\nQmvav 16,64,224\nQta 56,69,8192\n"
"s_mov_b32 s31, 147456\nQmvav 16,68,232\nbuffer_load_dwordx4 a[28:31], v79, s[48:51], s31 offen offset:0\nQmvav 12,64,192\nQmvav 12,68,200\nQta 58,69,8704\nQmvav 8,64,160\nQmvav 8,68,168\n"
"buffer_load_dwordx4 a[32:35], v79, s[48:51], s31 offen offset:1024\nQmvav 4,64,128\nQta 60,69,9216\nv_add_u32 v75, 1, v75\nQmvav 4,68,136\ns_waitcnt vmcnt(31)\nds_write_b32 v73, v75\nQmvav 0,64,96\n"
"Qmvav 0,68,104\nQta 62,69,9728\ns_waitcnt lgkmcnt(5)\nQmvav 28,72,96\nbuffer_load_dwordx4 a[36:39], v79, s[48:51], s31 offen offset:2048\nQmvav 28,76,104\nQmvav 32,72,128\nQmvav 32,76,136\n"
"Qmvav 36,72,160\nQta 64,69,8448\nQmvav 36,76,168\nds_read_b32 v66, v74\nQmvav 40,72,192\nbuffer_load_dwordx4 a[40:43], v79, s[48:51], s31 offen offset:3072\nQmvav 40,76,200\nQta 66,69,8960\n"
"Qmvav 44,72,224\nQmvav 44,76,232\ns_waitcnt lgkmcnt(1)\nv_cmp_gt_u32 vcc, v75, v66\nQmvav 48,72,148\ns_cbranch_vccnz .Lso320\n.Lro320:\nQmvav 48,76,212\n"
"Qta 68,69,9472\ns_mov_b32 s31, 151552\nQmvav 52,72,180\nbuffer_load_dwordx4 a[44:47], v79, s[48:51], s31 offen offset:0\nQmvav 52,76,244\ns_waitcnt lgkmcnt(4)\nQmaav 196,56,180\nQta 70,69,9984\n"
"Qmaav 196,60,244\nQmaav 192,56,148\nbuffer_load_dwordx4 a[48:51], v79, s[48:51], s31 offen offset:1024\nQmaav 192,60,212\nQmaav 188,56,224\nQta 72,69,16384\nQmaav 188,60,232\nQmaav 184,56,192\n"
"buffer_load_dwordx4 a[52:55], v79, s[48:51], s31 offen offset:2048\nQmaav 184,60,200\nQta 74,69,17408\nQmaav 180,56,160\nQmaav 180,60,168\nQmaav 176,56,128\nQta 76,69,24576\ns_mov_b32 s31, 262144\n"
"Qmaav 176,60,136\nbuffer_load_dwordx4 v[80:83], v79, s[48:51], s31 offen offset:0\nQmaav 172,56,96\nQmaav 172,60,104\nQta 78,69,25600\ns_waitcnt lgkmcnt(4)\nQmvav 92,64,96\nQmvav 92,68,104\n"
"buffer_load_dwordx4 v[112:115], v79, s[48:51], s31 offen offset:1024\nQmvav 124,64,128\nQmvav 124,68,136\nQmvav 156,64,160\nQta 56,69,16640\nQmvav 156,68,168\nbuffer_load_dwordx4 v[144:147], v79, s[48:51], s31 offen offset:2048\nQmvav 188,64,192\n"
"Qmvav 188,68,200\nQta 58,69,17664\nQmvav 220,64,224\nQmvav 220,68,232\nbuffer_load_dwordx4 v[176:179], v79, s[48:51], s31 offen offset:3072\nQmvav 252,64,148\nQta 60,69,24832\nQmvav 252,68,212\n"
"Qmvav 100,64,180\nQmvav 100,68,244\nQta 62,69,25856\ns_mov_b32 s31, 266240\ns_waitcnt lgkmcnt(4)\nQmaav 224,72,180\nbuffer_load_dwordx4 v[208:211], v79, s[48:51], s31 offen offset:0\nQmaav 224,76,244\n"
"Qmaav 220,72,148\nQmaav 220,76,212\nQmaav 216,72,224\nQta 64,69,16896\nQmaav 216,76,232\nbuffer_load_dwordx4 v[240:243], v79, s[48:51], s31 offen offset:1024\nQmaav 212,72,192\nQmaav 212,76,200\n"
"Qta 66,69,17920\nQmaav 208,72,160\nQmaav 208,76,168\nbuffer_load_dwordx4 v[84:87], v79, s[48:51], s31 offen offset:2048\nQmaav 204,72,128\nQta 68,69,25088\nQmaav 204,76,136\nQmaav 200,72,96\n"
"Qmaav 200,76,104\nQta 70,69,26112\ns_mov_b32 s31, 278528\ns_waitcnt lgkmcnt(4)\nQmaav 228,56,96\nbuffer_load_dwordx4 v[88:91], v79, s[48:51], s31 offen offset:0\nQmaav 228,60,104\nQmaav 232,56,128\n"
"Qmaav 232,60,136\nQmaav 236,56,160\nQta 72,69,17152\nQmaav 236,60,168\nbuffer_load_dwordx4 v[120:123], v79, s[48:51], s31 offen offset:1024\nQmaav 240,56,192\nQmaav 240,60,200\nQta 74,69,18176\n"
"Qmaav 244,56,224\nQmaav 244,60,232\nbuffer_load_dwordx4 v[152:155], v79, s[48:51], s31 offen offset:2048\nQmaav 248,56,148\nQta 76,69,25344\nQmaav 248,60,212\nQmaav 252,56,180\nQmaav 252,60,244\n"
"Qta 78,69,26368\ns_waitcnt lgkmcnt(4)\nQmvav 172,64,180\nbuffer_load_dwordx4 v[184:187], v79, s[48:51], s31 offen offset:3072\nQmvav 172,68,244\nQmvav 140,64,148\nQmvav 140,68,212\nQmvav 108,64,224\n"
"Qta 56,69,32768\ns_mov_b32 s31, 282624\nQmvav 108,68,232\nbuffer_load_dwordx4 v[216:219], v79, s[48:51], s31 offen offset:0\nQmvav 228,64,192\nQmvav 228,68,200\nQta 58,69,33792\nQmvav 196,64,160\n"
"Qmvav 196,68,168\nbuffer_load_dwordx4 v[248:251], v79, s[48:51], s31 offen offset:1024\nQmvav 164,64,128\nQta 60,69,40960\nQmvav 164,68,136\nQmvav 132,64,96\nQmvav 132,68,104\nQta 62,69,41984\n"
"s_waitcnt lgkmcnt(4)\nQmvav 204,72,96\nbuffer_load_dwordx4 v[116:119], v79, s[48:51], s31 offen offset:2048\nQmvav 204,76,104\nQmvav 236,72,128\nQmvav 236,76,136\nQmaav 96,72,160\nQta 64,69,33024\n"
"s_add_u32 m0, s96, 32768\ns_mov_b32 s95, 331776\nQmaav 96,76,168\nbuffer_load_dwordx4 v63, s[52:55], s95 offen lds\nQmaav 100,72,192\nQmaav 100,76,200\nQta 66,69,34048\nQmaav 104,72,224\n"
"s_mov_b32 s95, 351232\nQmaav 104,76,232\nbuffer_load_dwordx4 v63, s[52:55], s95 offen offset:1024 lds\nQmaav 108,72,148\nQta 68,69,41216\nQmaav 108,76,212\nQmaav 112,72,180\nQmaav 112,76,244\n"
"Qta 70,69,42240\ns_add_u32 m0, s96, 40960\ns_mov_b32 s95, 372736\ns_waitcnt vmcnt(16)\ns_waitcnt lgkmcnt(4)\nQmaaa 140,56,24\nbuffer_load_dwordx4 v63, s[52:55], s95 offen lds\nQmaaa 140,60,52\n"
"Qmaaa 136,56,20\nQmaaa 136,60,48\nQmaaa 132,56,16\nQta 72,69,33280\ns_mov_b32 s95, 392192\nQmaaa 132,60,44\nbuffer_load_dwordx4 v63, s[52:55], s95 offen offset:1024 lds\nQmaaa 128,56,12\n"
"Qmaaa 128,60,40\nQta 74,69,34304\nQmaaa 124,56,8\nQmaaa 124,60,36\nQmaaa 120,56,4\nQta 76,69,41472\ns_add_u32 s88, s71, 131072\nQmaaa 120,60,32\n"
"buffer_store_dwordx4 v[96:99], v79, s[56:59], s88 offen offset:0 sc1\nQmaaa 116,56,0\nQmaaa 116,60,28\nQta 78,69,42496\ns_waitcnt lgkmcnt(4)\nQmaaa 144,64,0\nQmaaa 144,68,28\nQmaaa 148,64,4\n"
"Qmaaa 148,68,32\nQmaaa 152,64,8\nQta 56,69,33536\nQmaaa 152,68,36\nQmaaa 156,64,12\nQmaaa 156,68,40\nQta 58,69,34560\n.Lpg15:\n"
"s_cbranch_execz .Ltr6\ns_add_u32 m0, s96, 49152\ns_mov_b32 s95, 413696\nQmaaa 160,64,16\nbuffer_load_dwordx4 v61, s[52:55], s95 offen lds\nQmaaa 160,68,44\nQmaaa 164,64,20\nQta 60,69,41728\n"
"Qmaaa 164,68,48\ns_mov_b32 s95, 433152\nQmaaa 168,64,24\nbuffer_load_dwordx4 v61, s[52:55], s95 offen offset:1024 lds\nQmaaa 168,68,52\nQta 62,69,42752\ns_waitcnt lgkmcnt(4)\nQmvaa 24,72,24\n"
"Qmvaa 24,76,52\nQmvaa 20,72,20\nbuffer_store_dwordx4 v[128:131], v79, s[56:59], s88 offen offset:1024 sc1\nQmvaa 20,76,48\nQmvaa 16,72,16\nQta 64,69,49152\nQmvaa 16,76,44\nQmvaa 12,72,12\n"
"Qmvaa 12,76,40\nQta 66,69,49664\nQmvaa 8,72,8\nQmvaa 8,76,36\nQmvaa 4,72,4\nQta 68,69,50176\nv_add_u32 v75, 1, v75\nQmvaa 4,76,32\n"
"s_waitcnt vmcnt(36)\nds_write_b32 v73, v75\ns_add_u32 m0, s96, 57344\ns_mov_b32 s95, 454656\nQmvaa 0,72,0\nbuffer_load_dwordx4 v61, s[52:55], s95 offen lds\nQmvaa 0,76,28\nQta 70,69,50688\n"
"s_waitcnt lgkmcnt(5)\nQmvaa 28,56,0\nQmvaa 28,60,28\ns_mov_b32 s95, 474112\nQmvaa 32,56,4\nbuffer_load_dwordx4 v61, s[52:55], s95 offen offset:1024 lds\nQmvaa 32,60,32\nQmvaa 36,56,8\n"
"Qta 72,69,49408\nQmvaa 36,60,36\nds_read_b32 v66, v74\nQmvaa 40,56,12\nQmvaa 40,60,40\nQta 74,69,49920\nQmvaa 44,56,16\nbuffer_store_dwordx4 v[160:163], v79, s[56:59], s88 offen offset:2048 sc1\n"
"v_add_u32 v67, 2, v65\nQmvaa 44,60,44\ns_waitcnt lgkmcnt(1)\nv_cmp_gt_u32 vcc, v75, v66\nQmvaa 48,56,20\ns_cbranch_vccnz .Lso460\n.Lro460:\nQmvaa 48,60,48\n"
"Qta 76,69,50432\nQmvaa 52,56,24\nbuffer_store_dword v67, v77, s[24:27], 0 offen sc1\nQmvaa 52,60,52\ns_waitcnt lgkmcnt(4)\nQmaaa 196,64,24\nQta 78,69,50944\nQmaaa 196,68,52\n"
"Qmaaa 192,64,20\nQmaaa 192,68,48\nQmaaa 188,64,16\nQta 56,69,57344\ns_add_u32 m0, s96, 132096\ns_mov_b32 s95, 413696\nQmaaa 188,68,44\nbuffer_load_dwordx4 v62, s[52:55], s95 offen lds\n"
"Qmaaa 184,64,12\nQmaaa 184,68,40\nQta 58,69,58368\nQmaaa 180,64,8\ns_mov_b32 s95, 453632\nQmaaa 180,68,36\nbuffer_load_dwordx4 v62, s[52:55], s95 offen offset:1024 lds\nQmaaa 176,64,4\n"
"Qta 60,68,0\nQmaaa 176,68,32\nQmaaa 172,64,0\nQmaaa 172,68,28\nQta 62,68,1024\ns_waitcnt lgkmcnt(4)\nQmvaa 92,72,0\nbuffer_store_dwordx4 v[192:195], v79, s[56:59], s88 offen offset:3072 sc1\n"
"Qmvaa 92,76,28\nQmvaa 124,72,4\nQmvaa 124,76,32\nQmvaa 156,72,8\nQta 64,69,57600\nQmvaa 156,76,36\nQmvaa 188,72,12\nQmvaa 188,76,40\n"
"Qta 66,69,58624\nQmvaa 220,72,16\nQmvaa 220,76,44\nQmvaa 252,72,20\nQta 68,68,256\ns_add_u32 m0, s96, 140288\ns_mov_b32 s95, 413696\nQmvaa 252,76,48\n"
"buffer_load_dwordx4 v63, s[52:55], s95 offen lds\nQmvaa 100,72,24\nQmvaa 100,76,52\nQta 70,68,1280\ns_waitcnt lgkmcnt(4)\nQmaaa 224,56,24\ns_mov_b32 s95, 433152\nQmaaa 224,60,52\n"
"buffer_load_dwordx4 v63, s[52:55], s95 offen offset:1024 lds\nQmaaa 220,56,20\nQmaaa 220,60,48\nQmaaa 216,56,16\nQta 72,69,57856\nQmaaa 216,60,44\ns_add_u32 s88, s71, 135168\nQmaaa 212,56,12\n"
"buffer_store_dwordx4 v[224:227], v79, s[56:59], s88 offen offset:0 sc1\nQmaaa 212,60,40\nQta 74,69,58880\nQmaaa 208,56,8\nQmaaa 208,60,36\nQmaaa 204,56,4\nQta 76,68,512\nQmaaa 204,60,32\n"
"Qmaaa 200,56,0\nQmaaa 200,60,28\nQta 78,68,1536\ns_waitcnt lgkmcnt(4)\nQmaaa 228,64,0\nQmaaa 228,68,28\ns_add_u32 m0, s96, 148480\ns_mov_b32 s95, 454656\n"
"Qmaaa 232,64,4\nbuffer_load_dwordx4 v63, s[52:55], s95 offen lds\nQmaaa 232,68,32\nQmaaa 236,64,8\nQta 56,69,58112\nQmaaa 236,68,36\ns_mov_b32 s95, 474112\nQmaaa 240,64,12\n"
"buffer_load_dwordx4 v63, s[52:55], s95 offen offset:1024 lds\nQmaaa 240,68,40\nQta 58,69,59136\nQmaaa 244,64,16\nQmaaa 244,68,44\nQmaaa 248,64,20\nQta 60,68,768\nQmaaa 248,68,48\n"
"Qmaaa 252,64,24\nbuffer_store_dwordx4 v[148:151], v79, s[56:59], s88 offen offset:1024 sc1\nQmaaa 252,68,52\nQta 62,68,1792\ns_waitcnt lgkmcnt(4)\nQmvaa 172,72,24\nQmvaa 172,76,52\ns_mov_b32 s40, %17\n"
"s_and_b32 s41, %18, 0xffff\nQmvaa 140,72,20\ns_mov_b32 s42, 571200\ns_lshl_b32 s66, %19, 17\nQmvaa 140,76,48\ns_add_u32 s69, s98, 4194304\ns_add_u32 s98, s66, s69\nQmvaa 108,72,16\n"
"Qta 64,68,8192\ns_xor_b32 s66, s66, 524288\nQmvaa 108,76,44\ns_add_u32 s64, s66, s69\ns_mul_i32 s66, s92, 33792\nQmvaa 228,72,12\ns_add_u32 s66, s66, %10\ns_add_u32 s66, s66, 98304\n"
"Qmvaa 228,76,40\nQta 66,68,9216\ns_add_u32 m0, s96, 65536\ns_mov_b32 s95, 659456\nQmvaa 196,72,8\nbuffer_load_dwordx4 v61, s[52:55], s95 offen lds\nQmvaa 196,76,36\ns_mul_i32 s68, s93, 96\n"
"s_sub_u32 s68, s66, s68\nQmvaa 164,72,4\nQta 68,68,16384\ns_add_u32 s68, s68, 96\nQmvaa 164,76,32\ns_mov_b32 s95, 678912\nv_bfe_u32 v148, v60, 4, 4\nQmvaa 132,72,0\n"
"buffer_load_dwordx4 v61, s[52:55], s95 offen offset:1024 lds\nQmvaa 132,76,28\nQta 70,68,17408\nv_mul_u32_u24 v148, 208, v148\ns_waitcnt lgkmcnt(4)\nQmvaa 204,56,0\nv_lshrrev_b32 v149, 8, v60\nv_lshl_add_u32 v148, v149, 3, v148\n"
"Qmvaa 204,60,28\nv_add_u32 v56, s68, v148\ns_lshl_b32 s68, s93, 3\nQmvaa 236,56,4\nv_lshrrev_b32 v148, 7, v60\nv_add_u32 v148, s68, v148\nQmvaa 236,60,32\nv_and_b32 v149, 127, v60\n"
"v_and_b32 v150, 64, v60\nQmaaa 96,56,8\nQta 72,68,8448\nv_lshrrev_b32 v150, 1, v150\nQmaaa 96,60,36\nbuffer_store_dwordx4 v[180:183], v79, s[56:59], s88 offen offset:2048 sc1\nQmaaa 100,56,12\nv_mul_u32_u24 v57, 208, v148\n"
"v_add3_u32 v57, v57, v149, s66\nQmaaa 100,60,40\nQta 74,68,9472\nv_add_u32 v57, v57, v150\nQmaaa 104,56,16\ns_and_b32 s65, %19, 1\ns_and_b32 s68, %19, 4\nQmaaa 104,60,44\n"
"s_lshl_b32 s68, s68, 2\ns_lshl_b32 s69, s65, 3\nQmaaa 108,56,20\nQta 76,68,16640\ns_add_u32 s68, s68, s69\nQmaaa 108,60,48\ns_lshr_b32 s68, 0xc0004080, s68\ns_and_b32 s68, s68, 255\n"
"Qmaaa 112,56,24\ns_mul_i32 s69, s92, 286720\ns_add_u32 s68, s68, s69\nQmaaa 112,60,52\nQta 78,68,17664\nv_mul_u32_u24 v64, 2560, v148\ns_waitcnt vmcnt(24)\ns_waitcnt lgkmcnt(4)\n"
"Qmaav 140,64,84\ns_mov_b32 s31, 393216\nv_add3_u32 v64, v64, v149, s68\nQmaav 140,68,116\nbuffer_load_dwordx4 a[0:3], v79, s[48:51], s31 offen offset:0\nQmaav 136,64,240\ns_lshl_b32 s68, s93, 4\nv_lshrrev_b32 v148, 6, v60\n"
"Qmaav 136,68,248\nv_add_u32 v148, s68, v148\nv_and_b32 v149, 63, v60\nQmaav 132,64,208\nQta 56,68,8704\nv_and_b32 v150, 32, v60\nQmaav 132,68,216\nbuffer_load_dwordx4 a[4:7], v79, s[48:51], s31 offen offset:1024\n"
"Qmaav 128,64,176\nv_lshlrev_b32 v150, 1, v150\nv_mul_u32_u24 v71, 208, v148\nQmaav 128,68,184\nQta 58,68,9728\nv_add3_u32 v71, v71, v149, s66\nQmaav 124,64,144\nv_add_u32 v71, v71, v150\n"
"s_xor_b32 s65, s65, 1\nQmaav 124,68,152\nbuffer_load_dwordx4 a[8:11], v79, s[48:51], s31 offen offset:2048\nQmaav 120,64,112\nQta 60,68,16896\ns_lshl_b32 s65, s65, 8\nQmaav 120,68,120\ns_add_u32 s68, s65, s69\n"
"v_mul_u32_u24 v72, 2560, v148\nQmaav 116,64,80\nv_add3_u32 v72, v72, v149, s68\nQmaav 116,68,88\nQta 62,68,17920\ns_waitcnt lgkmcnt(4)\nQmaav 144,72,80\nbuffer_load_dwordx4 a[12:15], v79, s[48:51], s31 offen offset:3072\n"
"Qmaav 144,76,88\nQmaav 148,72,112\nQmaav 148,76,120\nQmaav 152,72,144\nQta 64,68,8960\ns_mov_b32 s31, 397312\nQmaav 152,76,152\nbuffer_load_dwordx4 a[16:19], v79, s[48:51], s31 offen offset:0\n"
"Qmaav 156,72,176\nQmaav 156,76,184\nQta 66,68,9984\nQmaav 160,72,208\nQmaav 160,76,216\nbuffer_load_dwordx4 a[20:23], v79, s[48:51], s31 offen offset:1024\nQmaav 164,72,240\nQta 68,68,17152\n"
"Qmaav 164,76,248\nQmaav 168,72,84\nQmaav 168,76,116\nQta 70,68,18176\ns_waitcnt lgkmcnt(4)\nQmvav 24,56,84\nbuffer_load_dwordx4 a[24:27], v79, s[48:51], s31 offen offset:2048\nQmvav 24,60,116\n"
"Qmvav 20,56,240\nQmvav 20,60,248\nQmvav 16,56,208\nQta 72,68,24576\ns_mov_b32 s31, 409600\nQmvav 16,60,216\nbuffer_load_dwordx4 a[28:31], v79, s[48:51], s31 offen offset:0\nQmvav 12,56,176\n"
"Qmvav 12,60,184\nQta 74,68,25088\nQmvav 8,56,144\nQmvav 8,60,152\nbuffer_load_dwordx4 a[32:35], v79, s[48:51], s31 offen offset:1024\nQmvav 4,56,112\nQta 76,68,25600\nv_add_u32 v75, 1, v75\n"
"Qmvav 4,60,120\ns_waitcnt vmcnt(19)\nds_write_b32 v73, v75\nQmvav 0,56,80\nQmvav 0,60,88\nQta 78,68,26112\ns_waitcnt lgkmcnt(5)\nQmvav 28,64,80\n"
"buffer_load_dwordx4 a[36:39], v79, s[48:51], s31 offen offset:2048\nQmvav 28,68,88\nQmvav 32,64,112\nQmvav 32,68,120\nQmvav 36,64,144\nQta 56,68,24832\nQmvav 36,68,152\nds_read_b32 v66, v74\n"
"Qmvav 40,64,176\nbuffer_load_dwordx4 a[40:43], v79, s[48:51], s31 offen offset:3072\nQmvav 40,68,184\nQta 58,68,25344\nQmvav 44,64,208\nQmvav 44,68,216\ns_waitcnt lgkmcnt(1)\nv_cmp_gt_u32 vcc, v75, v66\n"
"Qmvav 48,64,240\ns_cbranch_vccnz .Lso600\n.Lro600:\nQmvav 48,68,248\nQta 60,68,25856\ns_mov_b32 s31, 413696\nQmvav 52,64,84\nbuffer_load_dwordx4 a[44:47], v79, s[48:51], s31 offen offset:0\n"
"Qmvav 52,68,116\ns_waitcnt lgkmcnt(4)\nQmaav 196,72,84\nQta 62,68,26368\nQmaav 196,76,116\nQmaav 192,72,240\nbuffer_load_dwordx4 a[48:51], v79, s[48:51], s31 offen offset:1024\nQmaav 192,76,248\n"
"Qmaav 188,72,208\nQta 64,68,32768\nQmaav 188,76,216\nQmaav 184,72,176\nbuffer_load_dwordx4 a[52:55], v79, s[48:51], s31 offen offset:2048\nQmaav 184,76,184\nQta 66,68,33792\nQmaav 180,72,144\n"
"Qmaav 180,76,152\nQmaav 176,72,112\nQta 68,68,40960\ns_add_u32 m0, s96, 73728\ns_mov_b32 s95, 700416\nQmaav 176,76,120\nbuffer_load_dwordx4 v61, s[52:55], s95 offen lds\nQmaav 172,72,80\n"
"Qmaav 172,76,88\nQta 70,68,41984\ns_waitcnt lgkmcnt(4)\nQmvav 92,56,80\ns_mov_b32 s95, 719872\nQmvav 92,60,88\nbuffer_load_dwordx4 v61, s[52:55], s95 offen offset:1024 lds\nQmvav 124,56,112\n"
"Qmvav 124,60,120\nQmvav 156,56,144\nQta 72,68,33024\ns_add_u32 m0, s96, 81920\ns_mov_b32 s95, 659456\nQmvav 156,60,152\nbuffer_load_dwordx4 v62, s[52:55], s95 offen lds\nQmvav 188,56,176\n"
"Qmvav 188,60,184\nQta 74,68,34048\nQmvav 220,56,208\ns_mov_b32 s95, 699392\nQmvav 220,60,216\nbuffer_load_dwordx4 v62, s[52:55], s95 offen offset:1024 lds\nQmvav 252,56,240\nQta 76,68,41216\n"
"Qmvav 252,60,248\nQmvav 100,56,84\nQmvav 100,60,116\nQta 78,68,42240\ns_add_u32 m0, s96, 90112\ns_mov_b32 s95, 659456\ns_waitcnt lgkmcnt(4)\nQmaav 224,64,84\n"
"buffer_load_dwordx4 v63, s[52:55], s95 offen lds\nQmaav 224,68,116\nQmaav 220,64,240\nQmaav 220,68,248\nQmaav 216,64,208\nQta 56,68,33280\ns_mov_b32 s95, 678912\nQmaav 216,68,216\n"
"buffer_load_dwordx4 v63, s[52:55], s95 offen offset:1024 lds\nQmaav 212,64,176\nQmaav 212,68,184\nQta 58,68,34304\nQmaav 208,64,144\ns_add_u32 m0, s96, 98304\ns_mov_b32 s95, 700416\nQmaav 208,68,152\n"
"buffer_load_dwordx4 v63, s[52:55], s95 offen lds\nQmaav 204,64,112\nQta 60,68,41472\nQmaav 204,68,120\nQmaav 200,64,80\nQmaav 200,68,88\nQta 62,68,42496\ns_waitcnt lgkmcnt(4)\n"
"Qmaav 228,72,80\nQmaav 228,76,88\ns_add_u32 s88, s71, 147456\nQmaav 232,72,112\nbuffer_store_dwordx4 v[104:107], v79, s[56:59], s88 offen offset:0 sc1\nQmaav 232,76,120\nQmaav 236,72,144\nQta 64,68,33536\n"
"Qmaav 236,76,152\nQmaav 240,72,176\nQmaav 240,76,184\nQta 66,68,34560\nQmaav 244,72,208\nQmaav 244,76,216\nQmaav 248,72,240\nQta 68,68,41728\n"
"Qmaav 248,76,248\nQmaav 252,72,84\nQmaav 252,76,116\nQta 70,68,42752\ns_waitcnt lgkmcnt(4)\nQmvav 172,56,84\nbuffer_store_dwordx4 v[136:139], v79, s[56:59], s88 offen offset:1024 sc1\nQmvav 172,60,116\n"
"Qmvav 140,56,240\nQmvav 140,60,248\nQmvav 108,56,208\nQta 72,68,49152\nQmvav 108,60,216\nQmvav 228,56,176\nQmvav 228,60,184\nQta 74,68,50176\n"
"Qmvav 196,56,144\nQmvav 196,60,152\nQmvav 164,56,112\nQta 76,68,57344\ns_mov_b32 s95, 719872\nQmvav 164,60,120\nbuffer_load_dwordx4 v63, s[52:55], s95 offen offset:1024 lds\nQmvav 132,56,80\n"
"Qmvav 132,60,88\nQta 78,68,58368\ns_waitcnt lgkmcnt(4)\nQmvav 204,64,80\ns_mov_b32 s31, 524288\nQmvav 204,68,88\nbuffer_load_dwordx4 v[80:83], v79, s[48:51], s31 offen offset:0\nQmvav 236,64,112\n"
"Qmvav 236,68,120\nQmaav 96,64,144\nQta 56,68,49408\nQmaav 96,68,152\nbuffer_load_dwordx4 v[112:115], v79, s[48:51], s31 offen offset:1024\nQmaav 100,64,176\nQmaav 100,68,184\nQta 58,68,50432\n"
"Qmaav 104,64,208\nQmaav 104,68,216\nbuffer_load_dwordx4 v[144:147], v79, s[48:51], s31 offen offset:2048\nQmaav 108,64,240\nQta 60,68,57600\nQmaav 108,68,248\nQmaav 112,64,84\nQmaav 112,68,116\n"
"Qta 62,68,58624\ns_waitcnt vmcnt(13)\ns_waitcnt lgkmcnt(4)\nQmaaa 140,72,24\nbuffer_load_dwordx4 v[176:179], v79, s[48:51], s31 offen offset:3072\nQmaaa 140,76,52\nQmaaa 136,72,20\nQmaaa 136,76,48\n"
"Qmaaa 132,72,16\nQta 64,68,49664\ns_mov_b32 s31, 528384\nQmaaa 132,76,44\nbuffer_load_dwordx4 v[208:211], v79, s[48:51], s31 offen offset:0\nQmaaa 128,72,12\nQmaaa 128,76,40\nQta 66,68,50688\n"
"Qmaaa 124,72,8\nQmaaa 124,76,36\nbuffer_load_dwordx4 v[240:243], v79, s[48:51], s31 offen offset:1024\nQmaaa 120,72,4\nQta 68,68,57856\nQmaaa 120,76,32\nQmaaa 116,72,0\nQmaaa 116,76,28\n"
"Qta 70,68,58880\ns_waitcnt lgkmcnt(4)\nQmaaa 144,56,0\nbuffer_load_dwordx4 v[84:87], v79, s[48:51], s31 offen offset:2048\nQmaaa 144,60,28\nQmaaa 148,56,4\nQmaaa 148,60,32\nQmaaa 152,56,8\n"
"Qta 72,68,49920\ns_mov_b32 s31, 540672\nQmaaa 152,60,36\nbuffer_load_dwordx4 v[88:91], v79, s[48:51], s31 offen offset:0\nQmaaa 156,56,12\nQmaaa 156,60,40\nQta 74,68,50944\nQmaaa 160,56,16\n"
"Qmaaa 160,60,44\nbuffer_load_dwordx4 v[120:123], v79, s[48:51], s31 offen offset:1024\n.Lpg16:\ns_cbranch_execz .Lpg20\nQmaaa 164,56,20\nQta 76,68,58112\nQmaaa 164,60,48\nQmaaa 168,56,24\n"
"Qmaaa 168,60,52\nQta 78,68,59136\ns_waitcnt lgkmcnt(4)\nQmvaa 24,64,24\nbuffer_load_dwordx4 v[152:155], v79, s[48:51], s31 offen offset:2048\nQmvaa 24,68,52\nQmvaa 20,64,20\nQmvaa 20,68,48\n"
"Qmvaa 16,64,16\nQta 56,70,1024\nQmvaa 16,68,44\nbuffer_load_dwordx4 v[184:187], v79, s[48:51], s31 offen offset:3072\nQmvaa 12,64,12\nQmvaa 12,68,40\nQta 58,70,1536\nQmvaa 8,64,8\n"
"s_mov_b32 s31, 544768\nQmvaa 8,68,36\nbuffer_load_dwordx4 v[216:219], v79, s[48:51], s31 offen offset:0\nQmvaa 4,64,4\nQta 60,70,2048\nv_add_u32 v75, 1, v75\nQmvaa 4,68,32\ns_waitcnt vmcnt(18)\n"
"ds_write_b32 v73, v75\nQmvaa 0,64,0\nQmvaa 0,68,28\nQta 62,70,2560\ns_waitcnt lgkmcnt(5)\nQmvaa 28,72,0\nbuffer_load_dwordx4 v[248:251], v79, s[48:51], s31 offen offset:1024\nQmvaa 28,76,28\n"
"Qmvaa 32,72,4\nQmvaa 32,76,32\nQmvaa 36,72,8\nQta 64,70,1280\nQmvaa 36,76,36\nds_read_b32 v66, v74\nQmvaa 40,72,12\nbuffer_load_dwordx4 v[116:119], v79, s[48:51], s31 offen offset:2048\n"
"Qmvaa 40,76,40\nQta 66,70,1792\nQmvaa 44,72,16\nQmvaa 44,76,44\ns_waitcnt lgkmcnt(1)\nv_cmp_gt_u32 vcc, v75, v66\nQmvaa 48,72,20\ns_cbranch_vccnz .Lso740\n"
".Lro740:\nQmvaa 48,76,48\nQta 68,70,2304\ns_add_u32 m0, s96, 106496\ns_mov_b32 s95, 741376\nQmvaa 52,72,24\nbuffer_load_dwordx4 v61, s[52:55], s95 offen lds\nQmvaa 52,76,52\n"
"s_waitcnt lgkmcnt(4)\nQmaaa 196,56,24\nQta 70,70,2816\nQmaaa 196,60,52\ns_mov_b32 s95, 760832\nQmaaa 192,56,20\nbuffer_load_dwordx4 v61, s[52:55], s95 offen offset:1024 lds\nQmaaa 192,60,48\n"
"Qmaaa 188,56,16\nQta 72,70,9216\nQmaaa 188,60,44\ns_add_u32 m0, s96, 114688\ns_mov_b32 s95, 782336\nQmaaa 184,56,12\nbuffer_load_dwordx4 v61, s[52:55], s95 offen lds\nQmaaa 184,60,40\n"
"Qta 74,70,10240\nQmaaa 180,56,8\nQmaaa 180,60,36\nQmaaa 176,56,4\nQta 76,70,17408\ns_mov_b32 s95, 801792\nQmaaa 176,60,32\nbuffer_load_dwordx4 v61, s[52:55], s95 offen offset:1024 lds\n"
"Qmaaa 172,56,0\nQmaaa 172,60,28\nQta 78,70,18432\ns_waitcnt lgkmcnt(4)\nQmvaa 92,64,0\ns_add_u32 m0, s96, 122880\ns_mov_b32 s95, 741376\nQmvaa 92,68,28\n"
"buffer_load_dwordx4 v62, s[52:55], s95 offen lds\nQmvaa 124,64,4\nQmvaa 124,68,32\nQmvaa 156,64,8\nQta 56,70,9472\ns_mov_b32 s95, 781312\nQmvaa 156,68,36\nbuffer_load_dwordx4 v62, s[52:55], s95 offen offset:1024 lds\n"
"Qmvaa 188,64,12\nQmvaa 188,68,40\nQta 58,70,10496\nQmvaa 220,64,16\ns_add_u32 m0, s96, 0\ns_mov_b32 s95, 741376\nQmvaa 220,68,44\nbuffer_load_dwordx4 v63, s[52:55], s95 offen lds\n"
"Qmvaa 252,64,20\nQta 60,70,17664\nQmvaa 252,68,48\nQmvaa 100,64,24\nQmvaa 100,68,52\nQta 62,70,18688\ns_mov_b32 s95, 760832\ns_waitcnt lgkmcnt(4)\n"
"Qmaaa 224,72,24\nbuffer_load_dwordx4 v63, s[52:55], s95 offen offset:1024 lds\nQmaaa 224,76,52\nQmaaa 220,72,20\nQmaaa 220,76,48\nQmaaa 216,72,16\nQta 64,70,9728\ns_add_u32 m0, s96, 8192\n"
"s_mov_b32 s95, 782336\nQmaaa 216,76,44\nbuffer_load_dwordx4 v63, s[52:55], s95 offen lds\nQmaaa 212,72,12\nQmaaa 212,76,40\nQta 66,70,10752\nQmaaa 208,72,8\ns_mov_b32 s95, 801792\n"
"Qmaaa 208,76,36\nbuffer_load_dwordx4 v63, s[52:55], s95 offen offset:1024 lds\nQmaaa 204,72,4\nQta 68,70,17920\nQmaaa 204,76,32\nQmaaa 200,72,0\nQmaaa 200,76,28\nQta 70,70,18944\n"
"s_waitcnt lgkmcnt(4)\nQmaaa 228,56,0\nQmaaa 228,60,28\nQmaaa 232,56,4\nbuffer_store_dwordx4 v[168:171], v79, s[56:59], s88 offen offset:2048 sc1\nQmaaa 232,60,32\nQmaaa 236,56,8\nQta 72,70,9984\n"
"Qmaaa 236,60,36\nQmaaa 240,56,12\nQmaaa 240,60,40\nQta 74,70,11008\nQmaaa 244,56,16\nQmaaa 244,60,44\nQmaaa 248,56,20\nQta 76,70,18176\n"
"Qmaaa 248,60,48\nQmaaa 252,56,24\nQmaaa 252,60,52\nQta 78,70,19200\ns_waitcnt lgkmcnt(4)\nQmvaa 172,64,24\nbuffer_store_dwordx4 v[200:203], v79, s[56:59], s88 offen offset:3072 sc1\nQmvaa 172,68,52\n"
"Qmvaa 140,64,20\nQmvaa 140,68,48\nQmvaa 108,64,16\nQta 56,69,0\nQmvaa 108,68,44\nQmvaa 228,64,12\nQmvaa 228,68,40\nQta 58,69,1024\n"
"Qmvaa 196,64,8\nQmvaa 196,68,36\nQmvaa 164,64,4\nQta 60,69,8192\ns_add_u32 m0, s96, 16384\ns_mov_b32 s95, 987136\nQmvaa 164,68,32\nbuffer_load_dwordx4 v61, s[52:55], s95 offen lds\n"
"Qmvaa 132,64,0\nQmvaa 132,68,28\nQta 62,69,9216\ns_waitcnt lgkmcnt(4)\nQmvaa 204,72,0\ns_mov_b32 s95, 1006592\nQmvaa 204,76,28\nbuffer_load_dwordx4 v61, s[52:55], s95 offen offset:1024 lds\n"
"Qmvaa 236,72,4\nQmvaa 236,76,32\nQmaaa 96,72,8\nQta 64,69,256\ns_mov_b32 s31, 655360\nQmaaa 96,76,36\nbuffer_load_dwordx4 a[0:3], v79, s[48:51], s31 offen offset:0\nQmaaa 100,72,12\n"
"Qmaaa 100,76,40\nQta 66,69,1280\nQmaaa 104,72,16\nQmaaa 104,76,44\nbuffer_load_dwordx4 a[4:7], v79, s[48:51], s31 offen offset:1024\nQmaaa 108,72,20\nQta 68,69,8448\nQmaaa 108,76,48\n"
"Qmaaa 112,72,24\nQmaaa 112,76,52\nQta 70,69,9472\ns_waitcnt vmcnt(16)\ns_waitcnt lgkmcnt(4)\nQmaav 140,56,84\nbuffer_load_dwordx4 a[8:11], v79, s[48:51], s31 offen offset:2048\nQmaav 140,60,116\n"
"Qmaav 136,56,240\nQmaav 136,60,248\nQmaav 132,56,208\nQta 72,69,512\nQmaav 132,60,216\nbuffer_load_dwordx4 a[12:15], v79, s[48:51], s31 offen offset:3072\nQmaav 128,56,176\nQmaav 128,60,184\n"
"Qta 74,69,1536\nQmaav 124,56,144\ns_mov_b32 s31, 659456\nQmaav 124,60,152\nbuffer_load_dwordx4 a[16:19], v79, s[48:51], s31 offen offset:0\nQmaav 120,56,112\nQta 76,69,8704\nQmaav 120,60,120\n"
"Qmaav 116,56,80\nQmaav 116,60,88\nQta 78,69,9728\ns_waitcnt lgkmcnt(4)\nQmaav 144,64,80\nbuffer_load_dwordx4 a[20:23], v79, s[48:51], s31 offen offset:1024\nQmaav 144,68,88\nQmaav 148,64,112\n"
"Qmaav 148,68,120\nQmaav 152,64,144\nQta 56,69,768\nQmaav 152,68,152\nbuffer_load_dwordx4 a[24:27], v79, s[48:51], s31 offen offset:2048\nQmaav 156,64,176\nQmaav 156,68,184\nQta 58,69,1792\n"
"Qmaav 160,64,208\ns_mov_b32 s31, 671744\nQmaav 160,68,216\nbuffer_load_dwordx4 a[28:31], v79, s[48:51], s31 offen offset:0\nQmaav 164,64,240\nQta 60,69,8960\nQmaav 164,68,248\nQmaav 168,64,84\n"
"Qmaav 168,68,116\nQta 62,69,9984\ns_waitcnt lgkmcnt(4)\nQmvav 24,72,84\nbuffer_load_dwordx4 a[32:35], v79, s[48:51], s31 offen offset:1024\nQmvav 24,76,116\nQmvav 20,72,240\nQmvav 20,76,248\n"
"Qmvav 16,72,208\nQta 64,69,16384\nQmvav 16,76,216\nbuffer_load_dwordx4 a[36:39], v79, s[48:51], s31 offen offset:2048\nQmvav 12,72,176\nQmvav 12,76,184\nQta 66,69,16896\nQmvav 8,72,144\n"
"Qmvav 8,76,152\nbuffer_load_dwordx4 a[40:43], v79, s[48:51], s31 offen offset:3072\nQmvav 4,72,112\nQta 68,69,17408\nv_add_u32 v75, 1, v75\nQmvav 4,76,120\ns_waitcnt vmcnt(19)\nds_write_b32 v73, v75\n"
"Qmvav 0,72,80\nQmvav 0,76,88\nQta 70,69,17920\ns_mov_b32 s31, 675840\ns_waitcnt lgkmcnt(5)\nQmvav 28,56,80\nbuffer_load_dwordx4 a[44:47], v79, s[48:51], s31 offen offset:0\nQmvav 28,60,88\n"
"Qmvav 32,56,112\nQmvav 32,60,120\nQmvav 36,56,144\nQta 72,69,16640\nQmvav 36,60,152\nds_read_b32 v66, v74\nQmvav 40,56,176\nbuffer_load_dwordx4 a[48:51], v79, s[48:51], s31 offen offset:1024\n"
"Qmvav 40,60,184\nQta 74,69,17152\nQmvav 44,56,208\nQmvav 44,60,216\ns_waitcnt lgkmcnt(1)\nv_cmp_gt_u32 vcc, v75, v66\nQmvav 48,56,240\ns_cbranch_vccnz .Lso880\n"
".Lro880:\nQmvav 48,60,248\nQta 76,69,17664\nQmvav 52,56,84\nbuffer_load_dwordx4 a[52:55], v79, s[48:51], s31 offen offset:2048\nQmvav 52,60,116\ns_waitcnt lgkmcnt(4)\nQmaav 196,64,84\n"
"Qta 78,69,18176\nQmaav 196,68,116\ns_add_u32 m0, s96, 24576\ns_mov_b32 s95, 1028096\nQmaav 192,64,240\nbuffer_load_dwordx4 v61, s[52:55], s95 offen lds\nQmaav 192,68,248\nQmaav 188,64,208\n"
"Qta 56,69,24576\nQmaav 188,68,216\ns_mov_b32 s95, 1047552\nQmaav 184,64,176\nbuffer_load_dwordx4 v61, s[52:55], s95 offen offset:1024 lds\nQmaav 184,68,184\nQta 58,69,25600\nQmaav 180,64,144\n"
"Qmaav 180,68,152\nQmaav 176,64,112\nQta 60,69,32768\ns_add_u32 m0, s96, 32768\ns_mov_b32 s95, 987136\nQmaav 176,68,120\nbuffer_load_dwordx4 v62, s[52:55], s95 offen lds\nQmaav 172,64,80\n"
"Qmaav 172,68,88\nQta 62,69,33792\ns_waitcnt lgkmcnt(4)\nQmvav 92,72,80\ns_mov_b32 s95, 1027072\nQmvav 92,76,88\nbuffer_load_dwordx4 v62, s[52:55], s95 offen offset:1024 lds\nQmvav 124,72,112\n"
"Qmvav 124,76,120\nQmvav 156,72,144\nQta 64,69,24832\ns_add_u32 m0, s96, 40960\ns_mov_b32 s95, 987136\nQmvav 156,76,152\nbuffer_load_dwordx4 v63, s[52:55], s95 offen lds\nQmvav 188,72,176\n"
"Qmvav 188,76,184\nQta 66,69,25856\nQmvav 220,72,208\ns_mov_b32 s95, 1006592\nQmvav 220,76,216\nbuffer_load_dwordx4 v63, s[52:55], s95 offen offset:1024 lds\nQmvav 252,72,240\nQta 68,69,33024\n"
"Qmvav 252,76,248\nQmvav 100,72,84\nQmvav 100,76,116\nQta 70,69,34048\ns_add_u32 m0, s96, 49152\ns_mov_b32 s95, 1028096\ns_waitcnt lgkmcnt(4)\nQmaav 224,56,84\n"
"buffer_load_dwordx4 v63, s[52:55], s95 offen lds\nQmaav 224,60,116\nQmaav 220,56,240\nQmaav 220,60,248\nQmaav 216,56,208\nQta 72,69,25088\ns_mov_b32 s95, 1047552\nQmaav 216,60,216\n"
"buffer_load_dwordx4 v63, s[52:55], s95 offen offset:1024 lds\nQmaav 212,56,176\nQmaav 212,60,184\nQta 74,69,26112\nQmaav 208,56,144\ns_add_u32 m0, s96, 57344\ns_mov_b32 s95, 1069056\nQmaav 208,60,152\n"
"buffer_load_dwordx4 v61, s[52:55], s95 offen lds\nQmaav 204,56,112\nQta 76,69,33280\nQmaav 204,60,120\nQmaav 200,56,80\nQmaav 200,60,88\nQta 78,69,34304\ns_waitcnt lgkmcnt(4)\n"
"Qmaav 228,64,80\nQmaav 228,68,88\ns_add_u32 s88, s71, 151552\nQmaav 232,64,112\nbuffer_store_dwordx4 v[232:235], v79, s[56:59], s88 offen offset:0 sc1\nQmaav 232,68,120\nQmaav 236,64,144\nQta 56,69,25344\n"
"Qmaav 236,68,152\nQmaav 240,64,176\nQmaav 240,68,184\nQta 58,69,26368\nQmaav 244,64,208\nQmaav 244,68,216\nQmaav 248,64,240\nQta 60,69,33536\n"
"Qmaav 248,68,248\nQmaav 252,64,84\nQmaav 252,68,116\nQta 62,69,34560\ns_waitcnt lgkmcnt(4)\nQmvav 172,72,84\nbuffer_store_dwordx4 v[212:215], v79, s[56:59], s88 offen offset:1024 sc1\nQmvav 172,76,116\n"
"Qmvav 140,72,240\nQmvav 140,76,248\nQmvav 108,72,208\nQta 64,69,40960\nQmvav 108,76,216\nQmvav 228,72,176\nQmvav 228,76,184\nQta 66,69,41984\n"
"Qmvav 196,72,144\nQmvav 196,76,152\nQmvav 164,72,112\nQta 68,69,49152\ns_mov_b32 s95, 1088512\nQmvav 164,76,120\nbuffer_load_dwordx4 v61, s[52:55], s95 offen offset:1024 lds\nQmvav 132,72,80\n"
"Qmvav 132,76,88\nQta 70,69,50176\ns_waitcnt lgkmcnt(4)\nQmvav 204,56,80\ns_mov_b32 s31, 786432\nQmvav 204,60,88\nbuffer_load_dwordx4 v[80:83], v79, s[48:51], s31 offen offset:0\nQmvav 236,56,112\n"
"Qmvav 236,60,120\nQmaav 96,56,144\nQta 72,69,41216\nQmaav 96,60,152\nbuffer_load_dwordx4 v[112:115], v79, s[48:51], s31 offen offset:1024\nQmaav 100,56,176\nQmaav 100,60,184\nQta 74,69,42240\n"
"Qmaav 104,56,208\nQmaav 104,60,216\nbuffer_load_dwordx4 v[144:147], v79, s[48:51], s31 offen offset:2048\nQmaav 108,56,240\nQta 76,69,49408\nQmaav 108,60,248\nQmaav 112,56,84\nv_accvgpr_read_b32 v96, a96\n"
"v_accvgpr_read_b32 v97, a97\nQmaav 112,60,116\nQta 78,69,50432\nv_accvgpr_read_b32 v98, a98\ns_waitcnt vmcnt(15)\ns_waitcnt lgkmcnt(4)\nQmaaa 140,64,24\nbuffer_load_dwordx4 v[176:179], v79, s[48:51], s31 offen offset:3072\n"
"Qmaaa 140,68,52\nv_accvgpr_read_b32 v99, a99\nv_accvgpr_read_b32 v128, a100\nQmaaa 136,64,20\nv_accvgpr_read_b32 v129, a101\nv_accvgpr_read_b32 v130, a102\nQmaaa 136,68,48\nv_accvgpr_read_b32 v131, a103\n"
"v_accvgpr_read_b32 v160, a104\nQmaaa 132,64,16\nQta 56,69,41472\ns_mov_b32 s31, 790528\nQmaaa 132,68,44\nbuffer_load_dwordx4 v[208:211], v79, s[48:51], s31 offen offset:0\nQmaaa 128,64,12\nv_accvgpr_read_b32 v161, a105\n"
"v_accvgpr_read_b32 v162, a106\nQmaaa 128,68,40\nQta 58,69,42496\nv_accvgpr_read_b32 v163, a107\nQmaaa 124,64,8\nv_accvgpr_read_b32 v192, a108\nv_accvgpr_read_b32 v193, a109\nQmaaa 124,68,36\n"
"buffer_load_dwordx4 v[240:243], v79, s[48:51], s31 offen offset:1024\nQmaaa 120,64,4\nQta 60,69,49664\nv_accvgpr_read_b32 v194, a110\nQmaaa 120,68,32\nv_accvgpr_read_b32 v195, a111\nv_accvgpr_read_b32 v224, a112\nQmaaa 116,64,0\n"
"v_accvgpr_read_b32 v225, a113\nv_accvgpr_read_b32 v226, a114\nQmaaa 116,68,28\nQta 62,69,50688\nv_accvgpr_read_b32 v227, a115\ns_waitcnt lgkmcnt(4)\nQmaaa 144,72,0\nbuffer_load_dwordx4 v[84:87], v79, s[48:51], s31 offen offset:2048\n"
"Qmaaa 144,76,28\nQmaaa 148,72,4\nQmaaa 148,76,32\nQmaaa 152,72,8\nQta 64,69,41728\ns_mov_b32 s31, 802816\nQmaaa 152,76,36\nbuffer_load_dwordx4 v[88:91], v79, s[48:51], s31 offen offset:0\n"
"Qmaaa 156,72,12\nQmaaa 156,76,40\nQta 66,69,42752\nQmaaa 160,72,16\nQmaaa 160,76,44\nbuffer_load_dwordx4 v[120:123], v79, s[48:51], s31 offen offset:1024\nQmaaa 164,72,20\nQta 68,69,49920\n"
"Qmaaa 164,76,48\nQmaaa 168,72,24\nQmaaa 168,76,52\nQta 70,69,50944\ns_waitcnt lgkmcnt(4)\nQmvaa 24,56,24\nbuffer_load_dwordx4 v[152:155], v79, s[48:51], s31 offen offset:2048\nQmvaa 24,60,52\n"
"Qmvaa 20,56,20\nQmvaa 20,60,48\nQmvaa 16,56,16\nQta 72,69,57344\nQmvaa 16,60,44\nbuffer_load_dwordx4 v[184:187], v79, s[48:51], s31 offen offset:3072\n.Lpg17:\ns_cbranch_execz .Lpg21\n"
"Qmvaa 12,56,12\nQmvaa 12,60,40\nQta 74,69,57856\nQmvaa 8,56,8\ns_mov_b32 s31, 806912\nQmvaa 8,60,36\nbuffer_load_dwordx4 v[216:219], v79, s[48:51], s31 offen offset:0\nQmvaa 4,56,4\n"
"Qta 76,69,58368\nv_add_u32 v75, 1, v75\nQmvaa 4,60,32\ns_waitcnt vmcnt(20)\nds_write_b32 v73, v75\nQmvaa 0,56,0\nQmvaa 0,60,28\nQta 78,69,58880\n"
"s_waitcnt lgkmcnt(5)\nQmvaa 28,64,0\nbuffer_load_dwordx4 v[248:251], v79, s[48:51], s31 offen offset:1024\nQmvaa 28,68,28\nQmvaa 32,64,4\nQmvaa 32,68,32\nQmvaa 36,64,8\nQta 56,69,57600\n"
"Qmvaa 36,68,36\nds_read_b32 v66, v74\nQmvaa 40,64,12\nbuffer_load_dwordx4 v[116:119], v79, s[48:51], s31 offen offset:2048\nQmvaa 40,68,40\nQta 58,69,58112\nQmvaa 44,64,16\nQmvaa 44,68,44\n"
"s_waitcnt lgkmcnt(1)\nv_cmp_gt_u32 vcc, v75, v66\nQmvaa 48,64,20\ns_cbranch_vccnz .Lso1020\n.Lro1020:\nQmvaa 48,68,48\nQta 60,69,58624\ns_add_u32 m0, s96, 132096\n"
"s_mov_b32 s95, 1110016\nQmvaa 52,64,24\nbuffer_load_dwordx4 v61, s[52:55], s95 offen lds\nQmvaa 52,68,52\ns_waitcnt lgkmcnt(4)\nQmaaa 196,72,24\nQta 62,69,59136\nQmaaa 196,76,52\n"
"s_mov_b32 s95, 1129472\nQmaaa 192,72,20\nbuffer_load_dwordx4 v61, s[52:55], s95 offen offset:1024 lds\nQmaaa 192,76,48\nQmaaa 188,72,16\nQta 64,68,0\nQmaaa 188,76,44\ns_add_u32 m0, s96, 140288\n"
"s_mov_b32 s95, 1069056\nQmaaa 184,72,12\nbuffer_load_dwordx4 v62, s[52:55], s95 offen lds\nQmaaa 184,76,40\nQta 66,68,1024\nQmaaa 180,72,8\nQmaaa 180,76,36\nQmaaa 176,72,4\n"
"Qta 68,68,8192\ns_mov_b32 s95, 1108992\nQmaaa 176,76,32\nbuffer_load_dwordx4 v62, s[52:55], s95 offen offset:1024 lds\nQmaaa 172,72,0\nQmaaa 172,76,28\nQta 70,68,9216\ns_waitcnt lgkmcnt(4)\n"
"Qmvaa 92,56,0\ns_add_u32 m0, s96, 148480\ns_mov_b32 s95, 1069056\nQmvaa 92,60,28\nbuffer_load_dwordx4 v63, s[52:55], s95 offen lds\nQmvaa 124,56,4\nQmvaa 124,60,32\nQmvaa 156,56,8\n"
"Qta 72,68,256\ns_mov_b32 s95, 1088512\nQmvaa 156,60,36\nbuffer_load_dwordx4 v63, s[52:55], s95 offen offset:1024 lds\nQmvaa 188,56,12\nQmvaa 188,60,40\nQta 74,68,1280\nQmvaa 220,56,16\n"
"s_add_u32 m0, s96, 65536\ns_mov_b32 s95, 1110016\nQmvaa 220,60,44\nbuffer_load_dwordx4 v63, s[52:55], s95 offen lds\nQmvaa 252,56,20\nQta 76,68,8448\nQmvaa 252,60,48\nQmvaa 100,56,24\n"
"Qmvaa 100,60,52\nQta 78,68,9472\ns_mov_b32 s95, 1129472\ns_waitcnt lgkmcnt(4)\nQmaaa 224,64,24\nbuffer_load_dwordx4 v63, s[52:55], s95 offen offset:1024 lds\nQmaaa 224,68,52\nQmaaa 220,64,20\n"
"Qmaaa 220,68,48\nQmaaa 216,64,16\nQta 56,68,512\ns_add_u32 m0, s96, 73728\ns_add_u32 s95, s67, 4096\nQmaaa 216,68,44\nbuffer_load_dwordx4 v61, s[52:55], s95 offen lds\nQmaaa 212,64,12\n"
"Qmaaa 212,68,40\nQta 58,68,1536\nQmaaa 208,64,8\ns_add_u32 s95, s67, 23552\nQmaaa 208,68,36\nbuffer_load_dwordx4 v61, s[52:55], s95 offen offset:1024 lds\nQmaaa 204,64,4\nQta 60,68,8704\n"
"Qmaaa 204,68,32\nQmaaa 200,64,0\nQmaaa 200,68,28\nQta 62,68,9728\ns_waitcnt lgkmcnt(4)\nQmaaa 228,72,0\nQmaaa 228,76,28\nQmaaa 232,72,4\n"
"buffer_store_dwordx4 v[244:247], v79, s[56:59], s88 offen offset:2048 sc1\nQmaaa 232,76,32\nQmaaa 236,72,8\nQta 64,68,768\nQmaaa 236,76,36\nQmaaa 240,72,12\nQmaaa 240,76,40\nQta 66,68,1792\n"
"Qmaaa 244,72,16\nQmaaa 244,76,44\nQmaaa 248,72,20\nQta 68,68,8960\nQmaaa 248,76,48\ns_add_u32 m0, s96, 81920\ns_add_u32 s95, s67, 45056\nQmaaa 252,72,24\n"
"buffer_load_dwordx4 v61, s[52:55], s95 offen lds\nQmaaa 252,76,52\nQta 70,68,9984\ns_waitcnt lgkmcnt(4)\nQmvaa 172,56,24\nQmvaa 172,60,52\ns_add_u32 s95, s67, 64512\nQmvaa 140,56,20\n"
"buffer_load_dwordx4 v61, s[52:55], s95 offen offset:1024 lds\nQmvaa 140,60,48\nQmvaa 108,56,16\nQta 72,68,16384\nQmvaa 108,60,44\ns_add_u32 m0, s96, 90112\ns_add_u32 s95, s67, 4096\nQmvaa 228,56,12\n"
"buffer_load_dwordx4 v62, s[52:55], s95 offen lds\nQmvaa 228,60,40\nQta 74,68,17408\nQmvaa 196,56,8\nQmvaa 196,60,36\nQmvaa 164,56,4\nQta 76,68,24576\ns_add_u32 s95, s67, 44032\n"
"Qmvaa 164,60,32\nbuffer_load_dwordx4 v62, s[52:55], s95 offen offset:1024 lds\nQmvaa 132,56,0\nQmvaa 132,60,28\nQta 78,68,25600\ns_waitcnt lgkmcnt(4)\nQmvaa 204,64,0\nbuffer_load_dword v78, v76, s[24:27], 0 offen sc1\n"
"s_mov_b32 s31, 917504\nQmvaa 204,68,28\nbuffer_load_dwordx4 a[0:3], v79, s[48:51], s31 offen offset:0\nQmvaa 236,64,4\nQmvaa 236,68,32\nQmvaa 96,64,8\nQta 56,68,16640\nQmvaa 96,68,36\n"
"buffer_load_dwordx4 a[4:7], v79, s[48:51], s31 offen offset:1024\nQmvaa 128,64,12\nQmvaa 128,68,40\nQta 58,68,17664\nQmvaa 160,64,16\nQmvaa 160,68,44\nbuffer_load_dwordx4 a[8:11], v79, s[48:51], s31 offen offset:2048\nQmvaa 192,64,20\n"
"Qta 60,68,24832\nQmvaa 192,68,48\nQmvaa 224,64,24\nQmvaa 224,68,52\nQta 62,68,25856\ns_waitcnt vmcnt(19)\ns_waitcnt lgkmcnt(4)\nQmaav 140,72,84\n"
"buffer_load_dwordx4 a[12:15], v79, s[48:51], s31 offen offset:3072\nQmaav 140,76,116\nQmaav 136,72,240\nQmaav 136,76,248\nQmaav 132,72,208\nQta 64,68,16896\ns_mov_b32 s31, 921600\nQmaav 132,76,216\n"
"buffer_load_dwordx4 a[16:19], v79, s[48:51], s31 offen offset:0\nQmaav 128,72,176\nQmaav 128,76,184\nQta 66,68,17920\nQmaav 124,72,144\nQmaav 124,76,152\nbuffer_load_dwordx4 a[20:23], v79, s[48:51], s31 offen offset:1024\nQmaav 120,72,112\n"
"Qta 68,68,25088\nQmaav 120,76,120\nQmaav 116,72,80\nQmaav 116,76,88\nQta 70,68,26112\ns_waitcnt lgkmcnt(4)\nQmaav 144,56,80\nbuffer_load_dwordx4 a[24:27], v79, s[48:51], s31 offen offset:2048\n"
"Qmaav 144,60,88\nQmaav 148,56,112\nQmaav 148,60,120\nQmaav 152,56,144\nQta 72,68,17152\ns_mov_b32 s31, 933888\nQmaav 152,60,152\nbuffer_load_dwordx4 a[28:31], v79, s[48:51], s31 offen offset:0\n"
"Qmaav 156,56,176\nQmaav 156,60,184\nQta 74,68,18176\nQmaav 160,56,208\nQmaav 160,60,216\nbuffer_load_dwordx4 a[32:35], v79, s[48:51], s31 offen offset:1024\nQmaav 164,56,240\nQta 76,68,25344\n"
"Qmaav 164,60,248\nQmaav 168,56,84\nQmaav 168,60,116\nQta 78,68,26368\ns_waitcnt lgkmcnt(4)\nQmvav 24,64,84\nbuffer_load_dwordx4 a[36:39], v79, s[48:51], s31 offen offset:2048\nQmvav 24,68,116\n"
"Qmvav 20,64,240\nQmvav 20,68,248\nQmvav 16,64,208\nQta 56,68,32768\nQmvav 16,68,216\nbuffer_load_dwordx4 a[40:43], v79, s[48:51], s31 offen offset:3072\nQmvav 12,64,176\nQmvav 12,68,184\n"
"Qta 58,68,33280\nQmvav 8,64,144\ns_mov_b32 s31, 937984\nQmvav 8,68,152\nbuffer_load_dwordx4 a[44:47], v79, s[48:51], s31 offen offset:0\nQmvav 4,64,112\nQta 60,68,33792\nv_add_u32 v75, 1, v75\n"
"Qmvav 4,68,120\ns_waitcnt vmcnt(24)\nds_write_b32 v73, v75\nQmvav 0,64,80\nQmvav 0,68,88\nQta 62,68,34304\ns_waitcnt lgkmcnt(5)\nQmvav 28,72,80\n"
"buffer_load_dwordx4 a[48:51], v79, s[48:51], s31 offen offset:1024\nQmvav 28,76,88\nQmvav 32,72,112\nQmvav 32,76,120\ns_waitcnt vmcnt(13)\nv_sub_u32 v66, v78, v65\nv_cmp_gt_u32 vcc, 2, v66\ns_and_b64 vcc, vcc, s[28:29]\n"
"s_cbranch_scc1 .Lf2_w\n.Lf2_r:\nQmvav 36,72,144\nQta 64,68,33024\nQmvav 36,76,152\nds_read_b32 v66, v74\nQmvav 40,72,176\nbuffer_load_dwordx4 a[52:55], v79, s[48:51], s31 offen offset:2048\n"
"Qmvav 40,76,184\nQta 66,68,33536\nQmvav 44,72,208\nQmvav 44,76,216\ns_waitcnt lgkmcnt(1)\nv_cmp_gt_u32 vcc, v75, v66\nQmvav 48,72,240\ns_cbranch_vccnz .Lso1160\n"
".Lro1160:\nQmvav 48,76,248\nQta 68,68,34048\ns_add_u32 s31, s74, 0\nQmvav 52,72,84\nbuffer_load_dwordx4 a[80:83], v79, s[48:51], s31 offen offset:0 sc1\nQmvav 52,76,116\ns_waitcnt lgkmcnt(4)\n"
"Qmaav 196,56,84\nQta 70,68,34560\nQmaav 196,60,116\nQmaav 192,56,240\nbuffer_load_dwordx4 a[84:87], v79, s[48:51], s31 offen offset:1024 sc1\nQmaav 192,60,248\nQmaav 188,56,208\nQta 72,68,40960\n"
"Qmaav 188,60,216\nQmaav 184,56,176\nbuffer_load_dwordx4 a[88:91], v79, s[48:51], s31 offen offset:2048 sc1\nQmaav 184,60,184\nQta 74,68,41984\nQmaav 180,56,144\nQmaav 180,60,152\nQmaav 176,56,112\n"
"Qta 76,68,49152\nQmaav 176,60,120\nbuffer_load_dwordx4 a[92:95], v79, s[48:51], s31 offen offset:3072 sc1\nQmaav 172,56,80\nQmaav 172,60,88\nQta 78,68,50176\ns_waitcnt lgkmcnt(4)\nQmvav 92,64,80\n"
"s_add_u32 s31, s74, 4096\nQmvav 92,68,88\nbuffer_load_dwordx4 a[96:99], v79, s[48:51], s31 offen offset:0 sc1\nQmvav 124,64,112\nQmvav 124,68,120\nQmvav 156,64,144\nQta 56,68,41216\nQmvav 156,68,152\n"
"buffer_load_dwordx4 a[100:103], v79, s[48:51], s31 offen offset:1024 sc1\nQmvav 188,64,176\nQmvav 188,68,184\nQta 58,68,42240\nQmvav 220,64,208\nQmvav 220,68,216\nbuffer_load_dwordx4 a[104:107], v79, s[48:51], s31 offen offset:2048 sc1\nQmvav 252,64,240\n"
"Qta 60,68,49408\nQmvav 252,68,248\nQmvav 100,64,84\nQmvav 100,68,116\nQta 62,68,50432\ns_add_u32 s31, s74, 16384\ns_waitcnt lgkmcnt(4)\nQmaav 224,72,84\n"
"buffer_load_dwordx4 a[108:111], v79, s[48:51], s31 offen offset:0 sc1\nQmaav 224,76,116\nQmaav 220,72,240\nQmaav 220,76,248\nQmaav 216,72,208\nQta 64,68,41472\nQmaav 216,76,216\nbuffer_load_dwordx4 a[112:115], v79, s[48:51], s31 offen offset:1024 sc1\n"
"Qmaav 212,72,176\nQmaav 212,76,184\nQta 66,68,42496\nQmaav 208,72,144\nQmaav 208,76,152\nbuffer_load_dwordx4 v[168:171], v79, s[48:51], s31 offen offset:2048 sc1\nQmaav 204,72,112\nQta 68,68,49664\n"
"Qmaav 204,76,120\nQmaav 200,72,80\nQmaav 200,76,88\nQta 70,68,50688\ns_waitcnt lgkmcnt(4)\nQmaav 228,56,80\nbuffer_load_dwordx4 v[200:203], v79, s[48:51], s31 offen offset:3072 sc1\nQmaav 228,60,88\n"
"Qmaav 232,56,112\nQmaav 232,60,120\nQmaav 236,56,144\nQta 72,68,41728\ns_add_u32 s31, s74, 20480\nQmaav 236,60,152\nbuffer_load_dwordx4 v[232:235], v79, s[48:51], s31 offen offset:0 sc1\nQmaav 240,56,176\n"
"Qmaav 240,60,184\nQta 74,68,42752\nQmaav 244,56,208\nQmaav 244,60,216\nbuffer_load_dwordx4 v[212:215], v79, s[48:51], s31 offen offset:1024 sc1\nQmaav 248,56,240\nQta 76,68,49920\nQmaav 248,60,248\n"
"Qmaav 252,56,84\nQmaav 252,60,116\nQta 78,68,50944\ns_waitcnt lgkmcnt(4)\nQmvav 172,64,84\nbuffer_load_dwordx4 v[244:247], v79, s[48:51], s31 offen offset:2048 sc1\nQmvav 172,68,116\nQmvav 140,64,240\n"
"Qmvav 140,68,248\nQmvav 108,64,208\nQta 56,68,57344\ns_add_u32 m0, s96, 98304\ns_add_u32 s95, s67, 4096\nQmvav 108,68,216\nbuffer_load_dwordx4 v63, s[52:55], s95 offen lds\nQmvav 228,64,176\n"
"Qmvav 228,68,184\nQta 58,68,58368\nQmvav 196,64,144\ns_add_u32 s95, s67, 23552\nQmvav 196,68,152\nbuffer_load_dwordx4 v63, s[52:55], s95 offen offset:1024 lds\nQmvav 164,64,112\nQta 60,70,1024\n"
"Qmvav 164,68,120\nQmvav 132,64,80\nQmvav 132,68,88\nQta 62,70,2048\ns_add_u32 m0, s96, 106496\ns_add_u32 s95, s67, 45056\ns_waitcnt lgkmcnt(4)\nQmvav 204,72,80\n"
"buffer_load_dwordx4 v63, s[52:55], s95 offen lds\nQmvav 204,76,88\nQmvav 236,72,112\nQmvav 236,76,120\nQmvav 96,72,144\nQta 64,68,57600\ns_add_u32 s95, s67, 64512\nQmvav 96,76,152\n"
"buffer_load_dwordx4 v63, s[52:55], s95 offen offset:1024 lds\nQmvav 128,72,176\nQmvav 128,76,184\nQta 66,68,58624\nQmvav 160,72,208\ns_add_u32 m0, s96, 114688\ns_add_u32 s95, s72, 4096\nQmvav 160,76,216\n"
"buffer_load_dwordx4 v61, s[52:55], s95 offen lds\nQmvav 192,72,240\nQta 68,70,1280\nQmvav 192,76,248\nQmvav 224,72,84\nQmvav 224,76,116\nQta 70,70,2304\ns_add_u32 s95, s72, 23552\n"
"s_waitcnt vmcnt(19)\ns_waitcnt lgkmcnt(4)\nQmaaa 140,56,24\nbuffer_load_dwordx4 v61, s[52:55], s95 offen offset:1024 lds\nQmaaa 140,60,52\nQmaaa 136,56,20\nQmaaa 136,60,48\nQmaaa 132,56,16\n"
"Qta 72,68,57856\ns_add_u32 m0, s96, 122880\ns_add_u32 s95, s72, 45056\nQmaaa 132,60,44\nbuffer_load_dwordx4 v61, s[52:55], s95 offen lds\nQmaaa 128,56,12\nQmaaa 128,60,40\nQta 74,68,58880\n"
"Qmaaa 124,56,8\ns_add_u32 s95, s72, 64512\nQmaaa 124,60,36\nbuffer_load_dwordx4 v61, s[52:55], s95 offen offset:1024 lds\nQmaaa 120,56,4\nQta 76,70,1536\nQmaaa 120,60,32\nQmaaa 116,56,0\n"
"Qmaaa 116,60,28\nQta 78,70,2560\ns_add_u32 m0, s96, 0\ns_add_u32 s95, s72, 4096\ns_waitcnt lgkmcnt(4)\nQmaaa 144,64,0\nbuffer_load_dwordx4 v62, s[52:55], s95 offen lds\nQmaaa 144,68,28\n"
"Qmaaa 148,64,4\nQmaaa 148,68,32\nQmaaa 152,64,8\nQta 56,68,58112\ns_add_u32 s95, s72, 44032\nQmaaa 152,68,36\nbuffer_load_dwordx4 v62, s[52:55], s95 offen offset:1024 lds\nQmaaa 156,64,12\n"
"Qmaaa 156,68,40\nQta 58,68,59136\nQmaaa 160,64,16\ns_add_u32 m0, s96, 8192\ns_add_u32 s95, s72, 4096\nQmaaa 160,68,44\nbuffer_load_dwordx4 v63, s[52:55], s95 offen lds\nQmaaa 164,64,20\n"
"Qta 60,70,1792\nQmaaa 164,68,48\nQmaaa 168,64,24\nQmaaa 168,68,52\nQta 62,70,2816\ns_add_u32 s95, s72, 23552\ns_waitcnt lgkmcnt(4)\nQmvaa 24,72,24\n"
"buffer_load_dwordx4 v63, s[52:55], s95 offen offset:1024 lds\nQmvaa 24,76,52\nQmvaa 20,72,20\nQmvaa 20,76,48\nQmvaa 16,72,16\nQta 64,70,9216\ns_add_u32 s31, s75, 0\nQmvaa 16,76,44\n"
"buffer_load_dwordx4 v[80:83], v79, s[48:51], s31 offen offset:0 sc1\nQmvaa 12,72,12\nQmvaa 12,76,40\nQta 66,70,9728\nQmvaa 8,72,8\nQmvaa 8,76,36\nbuffer_load_dwordx4 v[112:115], v79, s[48:51], s31 offen offset:1024 sc1\nQmvaa 4,72,4\n"
"Qta 68,70,10240\nv_add_u32 v75, 1, v75\nQmvaa 4,76,32\ns_waitcnt vmcnt(43)\nds_write_b32 v73, v75\nQmvaa 0,72,0\nQmvaa 0,76,28\nQta 70,70,10752\n"
"s_waitcnt lgkmcnt(5)\nQmvaa 28,56,0\nbuffer_load_dwordx4 v[144:147], v79, s[48:51], s31 offen offset:2048 sc1\nQmvaa 28,60,28\nQmvaa 32,56,4\nQmvaa 32,60,32\nQmvaa 36,56,8\nQta 72,70,9472\n"
".Lpg18:\ns_cbranch_execz .Lpg22\nQmvaa 36,60,36\nds_read_b32 v66, v74\nQmvaa 40,56,12\nbuffer_load_dwordx4 v[176:179], v79, s[48:51], s31 offen offset:3072 sc1\nQmvaa 40,60,40\nQta 74,70,9984\n"
"Qmvaa 44,56,16\nv_add_u32 v67, 3, v65\nQmvaa 44,60,44\ns_waitcnt lgkmcnt(1)\nv_cmp_gt_u32 vcc, v75, v66\nQmvaa 48,56,20\ns_cbranch_vccnz .Lso1300\n.Lro1300:\n"
"Qmvaa 48,60,48\nQta 76,70,10496\ns_add_u32 s31, s75, 4096\nQmvaa 52,56,24\nbuffer_load_dwordx4 v[208:211], v79, s[48:51], s31 offen offset:0 sc1\nQmvaa 52,60,52\nbuffer_store_dword v67, v77, s[24:27], 0 offen sc1\ns_waitcnt lgkmcnt(4)\n"
"Qmaaa 196,64,24\nQta 78,70,11008\nQmaaa 196,68,52\nQmaaa 192,64,20\nbuffer_load_dwordx4 v[240:243], v79, s[48:51], s31 offen offset:1024 sc1\nQmaaa 192,68,48\nQmaaa 188,64,16\nQta 56,70,17408\n"
"Qmaaa 188,68,44\nQmaaa 184,64,12\nbuffer_load_dwordx4 v[84:87], v79, s[48:51], s31 offen offset:2048 sc1\nQmaaa 184,68,40\nQta 58,70,18432\nQmaaa 180,64,8\nQmaaa 180,68,36\nQmaaa 176,64,4\n"
"Qta 60,69,0\ns_add_u32 s31, s75, 16384\nQmaaa 176,68,32\nbuffer_load_dwordx4 v[88:91], v79, s[48:51], s31 offen offset:0 sc1\nQmaaa 172,64,0\nQmaaa 172,68,28\nQta 62,69,1024\ns_waitcnt lgkmcnt(4)\n"
"Qmvaa 92,72,0\nQmvaa 92,76,28\nbuffer_load_dwordx4 v[120:123], v79, s[48:51], s31 offen offset:1024 sc1\nQmvaa 124,72,4\nQmvaa 124,76,32\nQmvaa 156,72,8\nQta 64,70,17664\nQmvaa 156,76,36\n"
"buffer_load_dwordx4 v[152:155], v79, s[48:51], s31 offen offset:2048 sc1\nQmvaa 188,72,12\nQmvaa 188,76,40\nQta 66,70,18688\nQmvaa 220,72,16\nQmvaa 220,76,44\nbuffer_load_dwordx4 v[184:187], v79, s[48:51], s31 offen offset:3072 sc1\nQmvaa 252,72,20\n"
"Qta 68,69,256\nQmvaa 252,76,48\nQmvaa 100,72,24\nQmvaa 100,76,52\nQta 70,69,1280\ns_add_u32 s31, s75, 20480\ns_waitcnt lgkmcnt(4)\nQmaaa 224,56,24\n"
"buffer_load_dwordx4 v[216:219], v79, s[48:51], s31 offen offset:0 sc1\nQmaaa 224,60,52\nQmaaa 220,56,20\nQmaaa 220,60,48\nQmaaa 216,56,16\nQta 72,70,17920\nQmaaa 216,60,44\nbuffer_load_dwordx4 v[248:251], v79, s[48:51], s31 offen offset:1024 sc1\n"
"Qmaaa 212,56,12\nQmaaa 212,60,40\nQta 74,70,18944\nQmaaa 208,56,8\nQmaaa 208,60,36\nbuffer_load_dwordx4 v[116:119], v79, s[48:51], s31 offen offset:2048 sc1\nQmaaa 204,56,4\nQta 76,69,512\n"
"Qmaaa 204,60,32\nQmaaa 200,56,0\nQmaaa 200,60,28\nQta 78,69,1536\ns_add_u32 m0, s96, 16384\ns_add_u32 s95, s72, 45056\ns_waitcnt lgkmcnt(4)\nQmaaa 228,64,0\n"
"buffer_load_dwordx4 v63, s[52:55], s95 offen lds\nQmaaa 228,68,28\nQmaaa 232,64,4\nQmaaa 232,68,32\nQmaaa 236,64,8\nQta 56,70,18176\ns_add_u32 s95, s72, 64512\nQmaaa 236,68,36\n"
"buffer_load_dwordx4 v63, s[52:55], s95 offen offset:1024 lds\nQmaaa 240,64,12\nQmaaa 240,68,40\nQta 58,70,19200\nQmaaa 244,64,16\ns_add_u32 m0, s96, 24576\ns_add_u32 s95, s73, 4096\nQmaaa 244,68,44\n"
"buffer_load_dwordx4 v61, s[52:55], s95 offen lds\nQmaaa 248,64,20\nQta 60,69,768\nQmaaa 248,68,48\nQmaaa 252,64,24\nQmaaa 252,68,52\nQta 62,69,1792\ns_add_u32 s95, s73, 23552\n"
"s_waitcnt lgkmcnt(4)\nQmvaa 172,72,24\nbuffer_load_dwordx4 v61, s[52:55], s95 offen offset:1024 lds\nQmvaa 172,76,52\nQmvaa 140,72,20\nQmvaa 140,76,48\nQmvaa 108,72,16\nQta 64,69,8192\n"
"s_add_u32 m0, s96, 32768\ns_add_u32 s95, s73, 45056\nQmvaa 108,76,44\nbuffer_load_dwordx4 v61, s[52:55], s95 offen lds\nQmvaa 228,72,12\nQmvaa 228,76,40\nQta 66,69,9216\nQmvaa 196,72,8\n"
"s_add_u32 s95, s73, 64512\nQmvaa 196,76,36\nbuffer_load_dwordx4 v61, s[52:55], s95 offen offset:1024 lds\nQmvaa 164,72,4\nQta 68,69,16384\nQmvaa 164,76,32\nQmvaa 132,72,0\nQmvaa 132,76,28\n"
"Qta 70,69,17408\ns_add_u32 m0, s96, 40960\ns_add_u32 s95, s73, 4096\ns_waitcnt lgkmcnt(4)\nQmvaa 204,56,0\nbuffer_load_dwordx4 v62, s[52:55], s95 offen lds\nQmvaa 204,60,28\nQmvaa 236,56,4\n"
"Qmvaa 236,60,32\nQmvaa 96,56,8\nQta 72,69,8448\ns_add_u32 s95, s73, 44032\nQmvaa 96,60,36\nbuffer_load_dwordx4 v62, s[52:55], s95 offen offset:1024 lds\nQmvaa 128,56,12\nQmvaa 128,60,40\n"
"Qta 74,69,9472\nQmvaa 160,56,16\ns_add_u32 m0, s96, 49152\ns_add_u32 s95, s73, 4096\nQmvaa 160,60,44\nbuffer_load_dwordx4 v63, s[52:55], s95 offen lds\nQmvaa 192,56,20\nQta 76,69,16640\n"
"Qmvaa 192,60,48\nQmvaa 224,56,24\nQmvaa 224,60,52\nQta 78,69,17664\ns_add_u32 s95, s73, 23552\ns_waitcnt vmcnt(36)\ns_waitcnt lgkmcnt(4)\nQmaaa 140,64,104\n"
"buffer_load_dwordx4 v63, s[52:55], s95 offen offset:1024 lds\nQmaav 140,68,244\nQmaaa 136,64,100\nQmaav 136,68,212\nQmaaa 132,64,96\nQta 56,69,8704\ns_add_u32 s31, s77, 0\nQmaav 132,68,232\n"
"buffer_load_dwordx4 a[0:3], v79, s[48:51], s31 offen offset:0 sc1\nQmaaa 128,64,92\nQmaav 128,68,200\nQta 58,69,9728\nQmaaa 124,64,88\nQmaav 124,68,168\nbuffer_load_dwordx4 a[4:7], v79, s[48:51], s31 offen offset:1024 sc1\nQmaaa 120,64,84\n"
"Qta 60,69,16896\nQmaaa 120,68,112\nQmaaa 116,64,80\nQmaaa 116,68,108\nQta 62,69,17920\ns_waitcnt lgkmcnt(4)\nQmaaa 144,72,80\nbuffer_load_dwordx4 a[8:11], v79, s[48:51], s31 offen offset:2048 sc1\n"
"Qmaaa 144,76,108\nQmaaa 148,72,84\nQmaaa 148,76,112\nQmaaa 152,72,88\nQta 64,69,8960\nQmaav 152,76,168\nbuffer_load_dwordx4 a[12:15], v79, s[48:51], s31 offen offset:3072 sc1\nQmaaa 156,72,92\n"
"Qmaav 156,76,200\nQta 66,69,9984\nQmaaa 160,72,96\ns_add_u32 s31, s77, 4096\nQmaav 160,76,232\nbuffer_load_dwordx4 a[16:19], v79, s[48:51], s31 offen offset:0 sc1\nQmaaa 164,72,100\nQta 68,69,17152\n"
"Qmaav 164,76,212\nQmaaa 168,72,104\nQmaav 168,76,244\nQta 70,69,18176\ns_waitcnt lgkmcnt(4)\nQmvaa 24,56,104\nbuffer_load_dwordx4 a[20:23], v79, s[48:51], s31 offen offset:1024 sc1\nQmvav 24,60,244\n"
"Qmvaa 20,56,100\nQmvav 20,60,212\nQmvaa 16,56,96\nQta 72,69,24576\nQmvav 16,60,232\nbuffer_load_dwordx4 a[24:27], v79, s[48:51], s31 offen offset:2048 sc1\nQmvaa 12,56,92\nQmvav 12,60,200\n"
"Qta 74,69,25088\nQmvaa 8,56,88\ns_add_u32 s31, s77, 16384\nQmvav 8,60,168\nbuffer_load_dwordx4 a[28:31], v79, s[48:51], s31 offen offset:0 sc1\nQmvaa 4,56,84\nQta 76,69,25600\nv_add_u32 v75, 1, v75\n"
"Qmvaa 4,60,112\ns_waitcnt vmcnt(43)\nds_write_b32 v73, v75\nQmvaa 0,56,80\nQmvaa 0,60,108\nQta 78,69,26112\ns_waitcnt lgkmcnt(5)\nQmvaa 28,64,80\n"
"buffer_load_dwordx4 a[32:35], v79, s[48:51], s31 offen offset:1024 sc1\nQmvaa 28,68,108\nQmvaa 32,64,84\nQmvaa 32,68,112\nQmvaa 36,64,88\nQta 56,69,24832\nQmvav 36,68,168\nds_read_b32 v66, v74\n"
"Qmvaa 40,64,92\nbuffer_load_dwordx4 a[36:39], v79, s[48:51], s31 offen offset:2048 sc1\nQmvav 40,68,200\nQta 58,69,25344\nQmvaa 44,64,96\nQmvav 44,68,232\ns_waitcnt lgkmcnt(1)\nv_cmp_gt_u32 vcc, v75, v66\n"
"Qmvaa 48,64,100\ns_cbranch_vccnz .Lso1440\n.Lro1440:\nQmvav 48,68,212\nQta 60,69,25856\nv_add_u32 v75, 1, v75\nQmvaa 52,64,104\ns_waitcnt vmcnt(39)\n"
"ds_write_b32 v73, v75\nQmvav 52,68,244\nbuffer_load_dwordx4 a[40:43], v79, s[48:51], s31 offen offset:3072 sc1\ns_waitcnt lgkmcnt(5)\nQmaaa 196,72,104\nQta 62,69,26368\nQmaav 196,76,244\nQmaaa 192,72,100\n"
"s_add_u32 s31, s77, 20480\nQmaav 192,76,212\nbuffer_load_dwordx4 a[44:47], v79, s[48:51], s31 offen offset:0 sc1\nQmaaa 188,72,96\nQta 64,69,32768\nQmaav 188,76,232\nQmaaa 184,72,92\nds_read_b32 v66, v74\n"
"Qmaav 184,76,200\nQta 66,69,33792\nQmaaa 180,72,88\nbuffer_load_dwordx4 a[48:51], v79, s[48:51], s31 offen offset:1024 sc1\nQmaav 180,76,168\nbuffer_load_dword v78, v76, s[24:27], 0 offen sc1\nQmaaa 176,72,84\ns_waitcnt lgkmcnt(1)\n"
"v_cmp_gt_u32 vcc, v75, v66\nQmaaa 176,76,112\ns_cbranch_vccnz .Lso1455\n.Lro1455:\nQmaaa 172,72,80\nQta 68,69,40960\nQmaaa 172,76,108\nbuffer_load_dwordx4 a[52:55], v79, s[48:51], s31 offen offset:2048 sc1\n"
"s_waitcnt lgkmcnt(4)\nQmvaa 92,56,80\nQmvaa 92,60,108\nQta 70,69,41984\nQmvaa 124,56,84\ns_add_u32 m0, s96, 57344\ns_add_u32 s95, s73, 45056\nQmvaa 124,60,112\n"
"buffer_load_dwordx4 v63, s[52:55], s95 offen lds\nQmvaa 156,56,88\nQta 72,69,33024\nQmvav 156,60,168\nQmvaa 188,56,92\nQmvav 188,60,200\nQta 74,69,34048\ns_add_u32 s95, s73, 64512\n"
"Qmvaa 220,56,96\nbuffer_load_dwordx4 v63, s[52:55], s95 offen offset:1024 lds\nQmvav 220,60,232\nQmvaa 252,56,100\nQta 76,69,41216\nQmvav 252,60,212\nQmvaa 100,56,104\nQmvav 100,60,244\n"
"Qta 78,69,42240\ns_waitcnt lgkmcnt(4)\nQmaaa 224,64,104\nQmaav 224,68,244\nQmaaa 220,64,100\nQmaav 220,68,212\nQmaaa 216,64,96\nQta 56,69,33280\n"
"Qmaav 216,68,232\nQmaaa 212,64,92\nQmaav 212,68,200\nQta 58,69,34304\nQmaaa 208,64,88\nQmaav 208,68,168\nQmaaa 204,64,84\nQta 60,69,41472\n"
"Qmaaa 204,68,112\nQmaaa 200,64,80\nQmaaa 200,68,108\nQta 62,69,42496\ns_waitcnt lgkmcnt(4)\nQmaaa 228,72,80\nQmaaa 228,76,108\nQmaaa 232,72,84\n"
"Qmaaa 232,76,112\nQmaaa 236,72,88\nQta 64,69,33536\nQmaav 236,76,168\nQmaaa 240,72,92\nQmaav 240,76,200\nQta 66,69,34560\nQmaaa 244,72,96\n"
"Qmaav 244,76,232\nQmaaa 248,72,100\nQta 68,69,41728\nQmaav 248,76,212\nQmaaa 252,72,104\nQmaav 252,76,244\nQta 70,69,42752\ns_waitcnt lgkmcnt(4)\n"
"Qmvaa 172,56,104\nQmvav 172,60,244\nQmvaa 140,56,100\nQmvav 140,60,212\nQmvaa 108,56,96\nQta 72,69,49152\nQmvav 108,60,232\nQmvaa 228,56,92\n"
"Qmvav 228,60,200\nQta 74,69,50176\nQmvaa 196,56,88\nQmvav 196,60,168\nQmvaa 164,56,84\nQta 76,69,57344\nQmvaa 164,60,112\ns_waitcnt vmcnt(3)\n"
"v_sub_u32 v66, v78, v65\nv_cmp_gt_u32 vcc, 3, v66\ns_and_b64 vcc, vcc, s[28:29]\ns_cbranch_scc1 .Lf3_w\n.Lf3_r:\nv_add_u32 v75, 1, v75\nQmvaa 132,56,80\ns_waitcnt vmcnt(42)\n"
"ds_write_b32 v73, v75\nQmvaa 132,60,108\nQta 78,69,58368\ns_waitcnt lgkmcnt(5)\nQmvaa 204,64,80\ns_add_u32 s31, s74, 131072\nQmvaa 204,68,108\nbuffer_load_dwordx4 a[80:83], v79, s[48:51], s31 offen offset:0 sc1\n"
"Qmvaa 236,64,84\nQmvaa 236,68,112\ns_bitcmp1_b32 %19, 2\ns_cbranch_scc1 .Lrole1\nQmvaa 96,64,88\nQta 56,69,49408\ns_lshl_b32 s46, %19, 4\nQmvav 96,68,168\n"
"buffer_load_dwordx4 a[84:87], v79, s[48:51], s31 offen offset:1024 sc1\nQmvaa 128,64,92\nds_read_b32 v66, v74\ns_add_u32 s46, s46, 128\nQmvav 128,68,200\nQta 58,69,50432\nv_mov_b32 v77, 0x7ffff000\nQmvaa 160,64,96\n"
"Qmvav 160,68,232\nbuffer_load_dwordx4 a[88:91], v79, s[48:51], s31 offen offset:2048 sc1\nQmvaa 192,64,100\ns_waitcnt lgkmcnt(1)\nv_cmp_gt_u32 vcc, v75, v66\nQmvav 192,68,212\ns_cbranch_vccnz .Lso1525r0\n.Lro1525r0:\n"
"Qmvaa 224,64,104\nQta 60,69,57600\nQmvav 224,68,244\nbuffer_load_dwordx4 a[92:95], v79, s[48:51], s31 offen offset:3072 sc1\ns_waitcnt vmcnt(31)\ns_waitcnt lgkmcnt(4)\nQmaav 140,72,84\nQmaav 140,76,116\n"
"Qta 62,69,58624\nQmaav 136,72,240\ns_add_u32 m0, s96, 65536\ns_add_u32 s95, s67, 86016\nQmaav 136,76,248\nbuffer_load_dwordx4 v63, s[52:55], s95 offen lds\nQmaav 132,72,208\nQta 64,69,49664\n"
"Qmaav 132,76,216\nQmaav 128,72,176\nQmaav 128,76,184\nQta 66,69,50688\ns_add_u32 s95, s67, 105472\nQmaav 124,72,144\nbuffer_load_dwordx4 v63, s[52:55], s95 offen offset:1024 lds\nQmaav 124,76,152\n"
"Qmaav 120,72,112\nQta 68,69,57856\nQmaav 120,76,120\ns_add_u32 m0, s96, 73728\ns_add_u32 s95, s67, 126976\nQmaav 116,72,80\nbuffer_load_dwordx4 v63, s[52:55], s95 offen lds\nQmaav 116,76,88\n"
"Qta 70,69,58880\ns_waitcnt lgkmcnt(4)\nQmaav 144,56,80\nQmaav 144,60,88\ns_add_u32 s95, s67, 146432\nQmaav 148,56,112\nbuffer_load_dwordx4 v63, s[52:55], s95 offen offset:1024 lds\nQmaav 148,60,120\n"
"Qmaav 152,56,144\nQta 72,69,49920\nQmaav 152,60,152\ns_add_u32 m0, s96, 81920\ns_add_u32 s95, s72, 86016\nQmaav 156,56,176\nbuffer_load_dwordx4 v63, s[52:55], s95 offen lds\nQmaav 156,60,184\n"
"Qta 74,69,50944\nQmaav 160,56,208\nQmaav 160,60,216\nQmaav 164,56,240\nQta 76,69,58112\ns_add_u32 s95, s72, 105472\nQmaav 164,60,248\nbuffer_load_dwordx4 v63, s[52:55], s95 offen offset:1024 lds\n"
"Qmaav 168,56,84\nQmaav 168,60,116\nQta 78,69,59136\ns_waitcnt lgkmcnt(4)\nQmvav 24,64,84\ns_add_u32 m0, s96, 90112\ns_add_u32 s95, s72, 126976\nQmvav 24,68,116\n"
"buffer_load_dwordx4 v63, s[52:55], s95 offen lds\nQmvav 20,64,240\nQmvav 20,68,248\nQmvav 16,64,208\nQta 56,68,0\ns_add_u32 s95, s72, 146432\nQmvav 16,68,216\nbuffer_load_dwordx4 v63, s[52:55], s95 offen offset:1024 lds\n"
"Qmvav 12,64,176\nQmvav 12,68,184\nQta 58,68,512\nQmvav 8,64,144\ns_add_u32 s31, s74, 135168\nQmvav 8,68,152\nbuffer_load_dwordx4 a[96:99], v79, s[48:51], s31 offen offset:0 sc1\nQmvav 4,64,112\n"
"Qta 60,68,1024\nQmvav 4,68,120\nQmvav 0,64,80\nQmvav 0,68,88\nQta 62,68,1536\ns_waitcnt lgkmcnt(4)\nQmvav 28,72,80\nbuffer_load_dwordx4 a[100:103], v79, s[48:51], s31 offen offset:1024 sc1\n"
"Qmvav 28,76,88\nQmvav 32,72,112\nQmvav 32,76,120\nQmvav 36,72,144\nQta 64,68,256\nQmvav 36,76,152\nbuffer_load_dwordx4 a[104:107], v79, s[48:51], s31 offen offset:2048 sc1\nQmvav 40,72,176\n"
"Qmvav 40,76,184\nQta 66,68,768\nQmvav 44,72,208\ns_add_u32 s31, s74, 147456\nv_cmp_eq_u32 s[44:45], s46, v60\ns_nop 1\nv_cndmask_b32 v77, v77, v76, s[44:45]\nv_add_u32 v67, 1, v65\n"
"s_xor_b32 s46, %19, 12\ns_lshl_b32 s28, 1, s46\nQmvav 44,76,216\nbuffer_load_dwordx4 a[108:111], v79, s[48:51], s31 offen offset:0 sc1\nQmvav 48,72,240\nQta 68,68,1280\nQmvav 48,76,248\nv_add_u32 v75, 1, v75\n"
"Qmvav 52,72,84\ns_waitcnt vmcnt(37)\nds_write_b32 v73, v75\nQmvav 52,76,116\nQta 70,68,1792\ns_waitcnt lgkmcnt(5)\nQmaav 196,56,84\nbuffer_load_dwordx4 a[112:115], v79, s[48:51], s31 offen offset:1024 sc1\n"
"Qmaav 196,60,116\nQmaav 192,56,240\nQmaav 192,60,248\n.Lpg19:\ns_cbranch_execz .Lpg23\nQmaav 188,56,208\nQta 72,68,8192\nQmaav 188,60,216\n"
"buffer_load_dwordx4 v[168:171], v79, s[48:51], s31 offen offset:2048 sc1\nQmaav 184,56,176\nds_read_b32 v66, v74\nQmaav 184,60,184\nQta 74,68,9216\nQmaav 180,56,144\nQmaav 180,60,152\nbuffer_load_dwordx4 v[200:203], v79, s[48:51], s31 offen offset:3072 sc1\n"
"Qmaav 176,56,112\ns_waitcnt lgkmcnt(1)\nv_cmp_gt_u32 vcc, v75, v66\nQmaav 176,60,120\ns_cbranch_vccnz .Lso1595r0\n.Lro1595r0:\nQmaav 172,56,80\nQta 76,68,16384\n"
"s_add_u32 s31, s74, 151552\nQmaav 172,60,88\nbuffer_load_dwordx4 v[232:235], v79, s[48:51], s31 offen offset:0 sc1\ns_waitcnt lgkmcnt(4)\nQmvav 92,64,80\nQmvav 92,68,88\nQta 78,68,17408\nQmvav 124,64,112\n"
"Qmvav 124,68,120\nbuffer_load_dwordx4 v[212:215], v79, s[48:51], s31 offen offset:1024 sc1\nQmvav 156,64,144\nQta 56,68,8448\nQmvav 156,68,152\nQmvav 188,64,176\nQmvav 188,68,184\nQta 58,68,9472\n"
"Qmvav 220,64,208\nbuffer_load_dwordx4 v[244:247], v79, s[48:51], s31 offen offset:2048 sc1\nQmvav 220,68,216\nQmvav 252,64,240\nQta 60,68,16640\nQmvav 252,68,248\ns_add_u32 m0, s91, 0\ns_add_u32 s31, s77, 131072\n"
"Qmvav 100,64,84\nbuffer_load_dwordx4 v79, s[48:51], s31 offen sc1 lds\nQmvav 100,68,116\nQta 62,68,17664\ns_waitcnt lgkmcnt(4)\nQmaav 224,72,84\nQmaav 224,76,116\nQmaav 220,72,240\n"
"buffer_load_dwordx4 v79, s[48:51], s31 offen offset:1024 sc1 lds\nQmaav 220,76,248\nQmaav 216,72,208\nQta 64,68,8704\nQmaav 216,76,216\nQmaav 212,72,176\nbuffer_load_dwordx4 v79, s[48:51], s31 offen offset:2048 sc1 lds\nQmaav 212,76,184\n"
"Qta 66,68,9728\nQmaav 208,72,144\nQmaav 208,76,152\nQmaav 204,72,112\nQta 68,68,16896\nQmaav 204,76,120\nbuffer_load_dwordx4 v79, s[48:51], s31 offen offset:3072 sc1 lds\nQmaav 200,72,80\n"
"Qmaav 200,76,88\nQta 70,68,17920\ns_waitcnt lgkmcnt(4)\nQmaav 228,56,80\ns_add_u32 m0, s91, 4096\ns_add_u32 s31, s77, 135168\nQmaav 228,60,88\nbuffer_load_dwordx4 v79, s[48:51], s31 offen sc1 lds\n"
"Qmaav 232,56,112\nQmaav 232,60,120\nQmaav 236,56,144\nQta 72,68,8960\nQmaav 236,60,152\nbuffer_load_dwordx4 v79, s[48:51], s31 offen offset:1024 sc1 lds\nQmaav 240,56,176\nQmaav 240,60,184\n"
"Qta 74,68,9984\nQmaav 244,56,208\nQmaav 244,60,216\nbuffer_load_dwordx4 v79, s[48:51], s31 offen offset:2048 sc1 lds\nQmaav 248,56,240\nQta 76,68,17152\nQmaav 248,60,248\nQmaav 252,56,84\n"
"Qmaav 252,60,116\nQta 78,68,18176\ns_add_u32 m0, s91, 33792\ns_add_u32 s31, s77, 147456\ns_waitcnt lgkmcnt(4)\nQmvav 172,64,84\nbuffer_load_dwordx4 v79, s[48:51], s31 offen sc1 lds\nQmvav 172,68,116\n"
"Qmvav 140,64,240\nQmvav 140,68,248\nQmvav 108,64,208\nQta 56,68,24576\nQmvav 108,68,216\nbuffer_load_dwordx4 v79, s[48:51], s31 offen offset:1024 sc1 lds\nQmvav 228,64,176\nQmvav 228,68,184\n"
"Qta 58,68,25600\nQmvav 196,64,144\nQmvav 196,68,152\nbuffer_load_dwordx4 v79, s[48:51], s31 offen offset:2048 sc1 lds\nQmvav 164,64,112\nQta 60,68,32768\nQmvav 164,68,120\nv_add_u32 v75, 1, v75\n"
"Qmvav 132,64,80\ns_waitcnt vmcnt(49)\nds_write_b32 v73, v75\nQmvav 132,68,88\nQta 62,68,33792\ns_waitcnt lgkmcnt(5)\nQmvav 204,72,80\nbuffer_load_dwordx4 v79, s[48:51], s31 offen offset:3072 sc1 lds\n"
"Qmvav 204,76,88\nQmvav 236,72,112\nQmvav 236,76,120\nQmvav 96,72,144\nQta 64,68,24832\ns_add_u32 m0, s91, 37888\ns_add_u32 s31, s77, 151552\nQmvav 96,76,152\n"
"buffer_load_dwordx4 v79, s[48:51], s31 offen sc1 lds\nQmvav 128,72,176\nds_read_b32 v66, v74\nQmvav 128,76,184\nQta 66,68,25856\nQmvav 160,72,208\nQmvav 160,76,216\nbuffer_load_dwordx4 v79, s[48:51], s31 offen offset:1024 sc1 lds\n"
"Qmvav 192,72,240\ns_waitcnt lgkmcnt(1)\nv_cmp_gt_u32 vcc, v75, v66\nQmvav 192,76,248\ns_cbranch_vccnz .Lso1665r0\n.Lro1665r0:\nQmvav 224,72,84\nQta 68,68,33024\n"
"Qmvav 224,76,116\nbuffer_load_dwordx4 v79, s[48:51], s31 offen offset:2048 sc1 lds\ns_waitcnt vmcnt(38)\ns_waitcnt lgkmcnt(4)\nQmaaa 140,56,24\nQmaaa 140,60,52\nQta 70,68,34048\nQmaaa 136,56,20\n"
"s_add_u32 m0, s96, 0\ns_add_u32 s95, s73, 86016\nQmaaa 136,60,48\nbuffer_load_dwordx4 v63, s[52:55], s95 offen lds\nQmaaa 132,56,16\nQta 72,68,25088\nQmaaa 132,60,44\nQmaaa 128,56,12\n"
"Qmaaa 128,60,40\nQta 74,68,26112\ns_add_u32 s95, s73, 105472\nQmaaa 124,56,8\nbuffer_load_dwordx4 v63, s[52:55], s95 offen offset:1024 lds\nQmaaa 124,60,36\nQmaaa 120,56,4\nQta 76,68,33280\n"
"Qmaaa 120,60,32\ns_add_u32 m0, s96, 8192\ns_add_u32 s95, s73, 126976\nQmaaa 116,56,0\nbuffer_load_dwordx4 v63, s[52:55], s95 offen lds\nQmaaa 116,60,28\nQta 78,68,34304\ns_waitcnt lgkmcnt(4)\n"
"Qmaaa 144,64,0\nQmaaa 144,68,28\ns_add_u32 s95, s73, 146432\nQmaaa 148,64,4\nbuffer_load_dwordx4 v63, s[52:55], s95 offen offset:1024 lds\nQmaaa 148,68,32\nQmaaa 152,64,8\nQta 56,68,25344\n"
"Qmaaa 152,68,36\ns_add_u32 s31, s75, 131072\nQmaaa 156,64,12\nbuffer_load_dwordx4 v[80:83], v79, s[48:51], s31 offen offset:0 sc1\nQmaaa 156,68,40\nQta 58,68,26368\nQmaaa 160,64,16\nQmaaa 160,68,44\n"
"Qmaaa 164,64,20\nQta 60,68,33536\nQmaaa 164,68,48\nbuffer_load_dwordx4 v[112:115], v79, s[48:51], s31 offen offset:1024 sc1\nQmaaa 168,64,24\nQmaaa 168,68,52\nQta 62,68,34560\ns_waitcnt lgkmcnt(4)\n"
"Qmvaa 24,72,24\nQmvaa 24,76,52\nbuffer_load_dwordx4 v[144:147], v79, s[48:51], s31 offen offset:2048 sc1\nQmvaa 20,72,20\nQmvaa 20,76,48\nQmvaa 16,72,16\nQta 64,68,40960\nQmvaa 16,76,44\n"
"buffer_load_dwordx4 v[176:179], v79, s[48:51], s31 offen offset:3072 sc1\nQmvaa 12,72,12\nQmvaa 12,76,40\nQta 66,68,41472\nQmvaa 8,72,8\ns_add_u32 s31, s75, 135168\nQmvaa 8,76,36\nbuffer_load_dwordx4 v[208:211], v79, s[48:51], s31 offen offset:0 sc1\n"
"Qmvaa 4,72,4\nQta 68,68,41984\nQmvaa 4,76,32\nQmvaa 0,72,0\nQmvaa 0,76,28\nQta 70,68,42496\ns_waitcnt lgkmcnt(4)\nQmvaa 28,56,0\n"
"buffer_load_dwordx4 v[240:243], v79, s[48:51], s31 offen offset:1024 sc1\nQmvaa 28,60,28\nQmvaa 32,56,4\nQmvaa 32,60,32\nQmvaa 36,56,8\nQta 72,68,41216\nQmvaa 36,60,36\nbuffer_load_dwordx4 v[84:87], v79, s[48:51], s31 offen offset:2048 sc1\n"
"Qmvaa 40,56,12\nQmvaa 40,60,40\nQta 74,68,41728\nQmvaa 44,56,16\ns_add_u32 s31, s75, 147456\nQmvaa 44,60,44\nbuffer_load_dwordx4 v[88:91], v79, s[48:51], s31 offen offset:0 sc1\nQmvaa 48,56,20\n"
"Qta 76,68,42240\nQmvaa 48,60,48\nv_add_u32 v75, 1, v75\nQmvaa 52,56,24\ns_waitcnt vmcnt(38)\nds_write_b32 v73, v75\nQmvaa 52,60,52\nQta 78,68,42752\n"
"s_waitcnt lgkmcnt(5)\nQmaaa 196,64,24\nbuffer_load_dwordx4 v[120:123], v79, s[48:51], s31 offen offset:1024 sc1\nQmaaa 196,68,52\nQmaaa 192,64,20\nQmaaa 192,68,48\nQmaaa 188,64,16\nQta 56,68,49152\n"
"Qmaaa 188,68,44\nbuffer_load_dwordx4 v[152:155], v79, s[48:51], s31 offen offset:2048 sc1\nQmaaa 184,64,12\nds_read_b32 v66, v74\nQmaaa 184,68,40\nQta 58,68,50176\nQmaaa 180,64,8\nQmaaa 180,68,36\n"
"buffer_load_dwordx4 v[184:187], v79, s[48:51], s31 offen offset:3072 sc1\nQmaaa 176,64,4\ns_waitcnt lgkmcnt(1)\nv_cmp_gt_u32 vcc, v75, v66\nQmaaa 176,68,32\ns_cbranch_vccnz .Lso1735r0\n.Lro1735r0:\nQmaaa 172,64,0\n"
"Qta 60,68,57344\ns_add_u32 s31, s75, 151552\nQmaaa 172,68,28\nbuffer_load_dwordx4 v[216:219], v79, s[48:51], s31 offen offset:0 sc1\ns_waitcnt lgkmcnt(4)\nQmvaa 92,72,0\nQmvaa 92,76,28\nQta 62,68,58368\n"
"Qmvaa 124,72,4\nQmvaa 124,76,32\nbuffer_load_dwordx4 v[248:251], v79, s[48:51], s31 offen offset:1024 sc1\nQmvaa 156,72,8\nQta 64,68,49408\nQmvaa 156,76,36\nQmvaa 188,72,12\nQmvaa 188,76,40\n"
"Qta 66,68,50432\nQmvaa 220,72,16\nbuffer_load_dwordx4 v[116:119], v79, s[48:51], s31 offen offset:2048 sc1\nQmvaa 220,76,44\nQmvaa 252,72,20\nQta 68,68,57600\nQmvaa 252,76,48\ns_add_u32 m0, s96, 16384\n"
"s_add_u32 s95, s67, 86016\nQmvaa 100,72,24\nbuffer_load_dwordx4 v61, s[52:55], s95 offen lds\nQmvaa 100,76,52\nQta 70,68,58624\ns_waitcnt lgkmcnt(4)\nQmaaa 224,56,24\nQmaaa 224,60,52\n"
"s_add_u32 s95, s67, 105472\nQmaaa 220,56,20\nbuffer_load_dwordx4 v61, s[52:55], s95 offen offset:1024 lds\nQmaaa 220,60,48\nQmaaa 216,56,16\nQta 72,68,49664\nQmaaa 216,60,44\ns_add_u32 m0, s96, 24576\n"
"s_add_u32 s95, s67, 126976\nQmaaa 212,56,12\nbuffer_load_dwordx4 v61, s[52:55], s95 offen lds\nQmaaa 212,60,40\nQta 74,68,50688\nQmaaa 208,56,8\nQmaaa 208,60,36\nQmaaa 204,56,4\n"
"Qta 76,68,57856\ns_add_u32 s95, s67, 146432\nQmaaa 204,60,32\nbuffer_load_dwordx4 v61, s[52:55], s95 offen offset:1024 lds\nQmaaa 200,56,0\nQmaaa 200,60,28\nQta 78,68,58880\ns_waitcnt lgkmcnt(4)\n"
"Qmaaa 228,64,0\ns_add_u32 m0, s96, 32768\ns_add_u32 s95, s72, 86016\nQmaaa 228,68,28\nbuffer_load_dwordx4 v61, s[52:55], s95 offen lds\nQmaaa 232,64,4\nQmaaa 232,68,32\nQmaaa 236,64,8\n"
"Qta 56,68,49920\ns_add_u32 s95, s72, 105472\nQmaaa 236,68,36\nbuffer_load_dwordx4 v61, s[52:55], s95 offen offset:1024 lds\nQmaaa 240,64,12\nQmaaa 240,68,40\nQta 58,68,50944\nQmaaa 244,64,16\n"
"Qmaaa 244,68,44\nQmaaa 248,64,20\nQta 60,68,58112\nQmaaa 248,68,48\nQmaaa 252,64,24\nQmaaa 252,68,52\nQta 62,68,59136\ns_waitcnt lgkmcnt(4)\n"
"Qmvaa 172,72,24\nQmvaa 172,76,52\nQmvaa 140,72,20\nQmvaa 140,76,48\nQmvaa 108,72,16\nQta 64,69,0\nQmvaa 108,76,44\nQmvaa 228,72,12\n"
"Qmvaa 228,76,40\nQta 66,69,1024\nQmvaa 196,72,8\nQmvaa 196,76,36\nQmvaa 164,72,4\nQta 68,69,8192\nQmvaa 164,76,32\nv_add_u32 v75, 1, v75\n"
"Qmvaa 132,72,0\ns_waitcnt vmcnt(20)\nds_write_b32 v73, v75\nQmvaa 132,76,28\nQta 70,69,9216\ns_waitcnt lgkmcnt(5)\nQmvaa 204,56,0\ns_waitcnt vmcnt(24)\n"
"Qda 0,58,0\nQmvaa 204,60,28\nQmvaa 236,56,4\nQda 28,58,33792\nQmvaa 236,60,32\nQmvaa 96,56,8\nQta 72,69,16384\nQmvaa 96,60,36\n"
"Qda 4,58,1024\nQmvaa 128,56,12\nds_read_b32 v66, v74\nQmvaa 128,60,40\nQta 74,69,17408\nQmvaa 160,56,16\nQda 32,58,34816\nQmvaa 160,60,44\n"
"Qmvaa 192,56,20\ns_waitcnt lgkmcnt(2)\nv_cmp_gt_u32 vcc, v75, v66\nQmvaa 192,60,48\ns_cbranch_vccnz .Lso1805r0\n.Lro1805r0:\nQmvaa 224,56,24\nQta 76,69,24576\n"
"s_add_u32 m0, s96, 40960\ns_add_u32 s95, s72, 126976\nQmvaa 224,60,52\nbuffer_load_dwordx4 v61, s[52:55], s95 offen lds\ns_waitcnt vmcnt(39)\ns_waitcnt lgkmcnt(8)\nQmaaa 200,64,80\nQda 8,58,2048\n"
"Qmaaa 200,68,108\nQta 78,69,25600\nQmaaa 204,64,84\nQda 36,58,35840\ns_add_u32 s95, s72, 146432\nQmaaa 204,68,112\nbuffer_load_dwordx4 v61, s[52:55], s95 offen offset:1024 lds\nQmaaa 208,64,88\n"
"Qta 56,68,0\nQmaav 208,68,168\nQda 12,58,3072\nQmaaa 212,64,92\nQmaav 212,68,200\nQta 58,68,1024\ns_add_u32 m0, s96, 49152\ns_add_u32 s95, s73, 86016\n"
"Qmaaa 216,64,96\nbuffer_load_dwordx4 v61, s[52:55], s95 offen lds\nQmaav 216,68,232\nQda 40,58,36864\nQmaaa 220,64,100\nQta 60,68,8192\nQmaav 220,68,212\nQda 16,58,4096\n"
"s_add_u32 s95, s73, 105472\nQmaaa 224,64,104\nbuffer_load_dwordx4 v61, s[52:55], s95 offen offset:1024 lds\nQmaav 224,68,244\nQta 62,68,9216\ns_waitcnt vmcnt(10)\ns_waitcnt lgkmcnt(8)\nQmaav 200,72,80\n"
"Qda 44,58,37888\nQmaav 200,76,88\ns_add_u32 m0, s96, 57344\ns_add_u32 s95, s73, 126976\nQmaav 204,72,112\nbuffer_load_dwordx4 v61, s[52:55], s95 offen lds\nQmaav 204,76,120\nQda 20,58,5120\n"
"Qmaav 208,72,144\nQta 64,69,256\nQmaav 208,76,152\nQda 48,58,38912\ns_add_u32 s95, s73, 146432\nQmaav 212,72,176\nbuffer_load_dwordx4 v61, s[52:55], s95 offen offset:1024 lds\nQmaav 212,76,184\n"
"Qta 66,69,1280\nQmaav 216,72,208\nQda 24,58,6144\nQmaav 216,76,216\nQmaav 220,72,240\nQta 68,69,8448\nQmaav 220,76,248\nQda 52,58,39936\n"
"Qmaav 224,72,84\nQmaav 224,76,116\nQta 70,69,9472\ns_waitcnt lgkmcnt(9)\nQmaaa 200,56,0\nQmaaa 200,60,28\nQmaaa 204,56,4\nQmaaa 204,60,32\n"
"s_waitcnt lgkmcnt(9)\nQmaaa 208,56,8\nQta 72,69,16640\nQmaaa 208,60,36\ns_nop 1\nds_write_b128 v58, a[200:203] offset:0\nQmaaa 212,56,12\nQmaaa 212,60,40\n"
"Qta 74,69,17664\ns_waitcnt lgkmcnt(4)\nQmaaa 216,56,16\nds_write_b128 v58, a[204:207] offset:1024\nQmaaa 216,60,44\nQmaaa 220,56,20\nQta 76,69,24832\nQmaaa 220,60,48\n"
"ds_write_b128 v58, a[208:211] offset:2048\nQmaaa 224,56,24\nQmaaa 224,60,52\nQta 78,69,25856\ns_waitcnt lgkmcnt(7)\nQmaaa 228,64,80\nds_write_b128 v58, a[212:215] offset:3072\nQmaaa 228,68,108\n"
"Qmaaa 232,64,84\nQmaaa 232,68,112\nds_write_b128 v58, a[216:219] offset:4096\nQmaaa 236,64,88\nQta 56,68,256\nQmaav 236,68,168\nQmaaa 240,64,92\nds_write_b128 v58, a[220:223] offset:5120\n"
"Qmaav 240,68,200\nQta 58,68,1280\nQmaaa 244,64,96\nQmaav 244,68,232\nds_write_b128 v58, a[224:227] offset:6144\nQmaaa 248,64,100\nQta 60,68,8448\nQmaav 248,68,212\n"
"Qmaaa 252,64,104\nQmaav 252,68,244\nQta 62,68,9472\ns_waitcnt lgkmcnt(8)\nQmaav 228,72,80\nv_add_u32 v75, 1, v75\nQmaav 228,76,88\nds_write_b32 v73, v75\n"
"Qmaav 232,72,112\nQmaav 232,76,120\nQmaav 236,72,144\nQta 64,69,512\nQmaav 236,76,152\nQmaav 240,72,176\nQmaav 240,76,184\nQta 66,69,1536\n"
"Qmaav 244,72,208\nQmaav 244,76,216\nds_read_b32 v66, v74\nQmaav 248,72,240\nQta 68,69,8704\nQmaav 248,76,248\nQmaav 252,72,84\nQmaav 252,76,116\n"
"s_waitcnt lgkmcnt(1)\n.Lpg20:\ns_cbranch_execz .Lpg24\nv_cmp_gt_u32 vcc, v75, v66\ns_waitcnt lgkmcnt(5)\nQmaaa 228,56,0\ns_cbranch_vccnz .Lso1878r0\n.Lro1878r0:\n"
"Qmaaa 228,60,28\nQta 70,69,9728\nQmaaa 232,56,4\nQmaaa 232,60,32\nQdv 148,59,2048\nQmaaa 236,56,8\nQta 72,69,16896\nQmaaa 236,60,36\n"
"s_nop 0\nds_write_b128 v58, a[228:231] offset:33792\nQmaaa 240,56,12\nQdv 180,59,3072\nQmaaa 240,60,40\nQta 74,69,17920\nQmaaa 244,56,16\nds_write_b128 v58, a[232:235] offset:34816\n"
"Qmaaa 244,60,44\nQdv 104,59,5120\nQmaaa 248,56,20\nQta 76,69,25088\nQmaaa 248,60,48\nds_write_b128 v58, a[236:239] offset:35840\nQmaaa 252,56,24\nQdv 136,59,0\n"
"s_waitcnt lgkmcnt(9)\nQa 196,196,148\nQmaaa 252,60,52\nQta 78,69,26112\nQa 197,197,149\ns_waitcnt lgkmcnt(10)\nQmvaa 132,64,80\nds_write_b128 v58, a[240:243] offset:36864\n"
"Qmvaa 132,68,108\nQa 198,198,150\nQa 199,199,151\nQmvaa 164,64,84\ns_waitcnt lgkmcnt(8)\nQa 228,228,180\nQa 229,229,181\nQmvaa 164,68,112\n"
"ds_write_b128 v58, a[244:247] offset:37888\nQmvaa 196,64,88\nQta 56,68,512\nQa 230,230,182\nQa 231,231,183\nQmvav 196,68,168\nQdv 148,59,4096\ns_waitcnt lgkmcnt(8)\n"
"Qa 140,140,104\nQmvaa 228,64,92\nds_write_b128 v58, a[248:251] offset:38912\nQmvav 228,68,200\nQta 58,68,1536\nQa 141,141,105\nQmvaa 108,64,96\nQa 142,142,106\n"
"Qa 143,143,107\nQmvav 108,68,232\nQdv 180,59,6144\ns_waitcnt lgkmcnt(8)\nQa 132,132,136\nQmvaa 140,64,100\nQta 60,68,8704\nQa 133,133,137\n"
"Qmvav 140,68,212\nds_write_b128 v58, a[252:255] offset:39936\nQmvaa 172,64,104\nQa 134,134,138\nQa 135,135,139\nQmvav 172,68,244\nQta 62,68,9728\ns_waitcnt lgkmcnt(6)\n"
"Qa 108,108,148\ns_waitcnt lgkmcnt(6)\nQmvav 132,72,80\nQdv 104,59,1024\nv_add_u32 v75, 1, v75\nQmvav 132,76,88\ns_waitcnt vmcnt(10)\nds_write_b32 v73, v75\n"
"Qmvav 164,72,112\nQa 109,109,149\nQa 110,110,150\nQmvav 164,76,120\nQa 111,111,151\ns_waitcnt lgkmcnt(5)\nQa 172,172,180\nQmvav 196,72,144\n"
"Qta 64,69,768\nQa 173,173,181\nQmvav 196,76,152\nQa 174,174,182\nQa 175,175,183\nQmvav 228,72,176\nQmvav 228,76,184\nQta 66,69,1792\n"
"s_waitcnt lgkmcnt(3)\nQa 164,164,104\nQmvav 108,72,208\nQa 165,165,105\nQa 166,166,106\nQmvav 108,76,216\nds_read_b32 v66, v74\nQa 167,167,107\n"
"Qmvav 140,72,240\nQta 68,69,8960\nQmvav 140,76,248\nQmvav 172,72,84\nQmvav 172,76,116\ns_waitcnt lgkmcnt(1)\nv_cmp_gt_u32 vcc, v75, v66\ns_waitcnt lgkmcnt(6)\n"
"Qmvaa 132,56,0\ns_cbranch_vccnz .Lso1920r0\n.Lro1920r0:\ns_add_u32 s68, s64, 0\nQmvaa 132,60,28\nQta 70,69,9984\nQmvaa 164,56,4\nQmvaa 164,60,32\n"
"Qdv 136,59,35840\nQmvaa 196,56,8\nQta 72,69,17152\ns_nop 1\nQk 132,132,133\nQmvaa 196,60,36\nQk 133,134,135\nQmvaa 228,56,12\n"
"Qdv 148,59,36864\nQk 134,164,165\nQmvaa 228,60,40\nQta 74,69,18176\nQk 135,166,167\nQmvaa 108,56,16\nbuffer_store_dwordx4 v[132:135], v79, s[24:27], s68 offen offset:0 sc1\nQk 196,196,197\n"
"Qmvaa 108,60,44\nQdv 180,59,38912\nQk 197,198,199\nQmvaa 140,56,20\nQta 76,69,25344\nQk 198,228,229\nQmvaa 140,60,48\ns_waitcnt lgkmcnt(5)\n"
"Qa 96,96,136\nQa 97,97,137\nQmvaa 172,56,24\nQdv 104,59,33792\nQa 98,98,138\nQmvaa 172,60,52\nQta 78,69,26368\nQa 99,99,139\n"
"s_waitcnt lgkmcnt(11)\nQmvaa 204,64,80\ns_waitcnt lgkmcnt(5)\nQa 128,128,148\nQa 129,129,149\nQmvaa 204,68,108\nQa 130,130,150\nQa 131,131,151\n"
"Qmvaa 236,64,84\nQk 199,230,231\nQk 108,108,109\nQmvaa 236,68,112\ns_waitcnt lgkmcnt(3)\nQa 192,192,180\nQa 193,193,181\nQmvaa 96,64,88\n"
"Qta 56,68,768\nQa 194,194,182\nQmvav 96,68,168\nQdv 136,59,37888\nQa 195,195,183\nQmvaa 128,64,92\ns_waitcnt lgkmcnt(3)\nQa 204,204,104\n"
"Qa 205,205,105\nQmvav 128,68,200\nQta 58,68,1792\nQa 206,206,106\nQmvaa 160,64,96\nbuffer_store_dwordx4 v[196:199], v79, s[24:27], s68 offen offset:1024 sc1\nQa 207,207,107\nQmvav 160,68,232\n"
"Qdv 148,59,39936\nQk 109,110,111\nQmvaa 192,64,100\nQta 60,68,8960\nQk 110,140,141\nQmvav 192,68,212\nQk 111,142,143\nQk 172,172,173\n"
"Qmvaa 224,64,104\nbuffer_store_dwordx4 v[108:111], v79, s[24:27], s68 offen offset:2048 sc1\nv_add_u32 v75, 1, v75\nQmvav 224,68,244\ns_waitcnt vmcnt(3)\nds_write_b32 v73, v75\ns_waitcnt lgkmcnt(10)\nQmvav 204,72,80\n"
"Qta 62,68,9984\ns_waitcnt lgkmcnt(5)\nQa 160,160,136\nQmvav 204,76,88\nQdv 180,59,34816\nQa 161,161,137\nQmvav 236,72,112\nQa 162,162,138\n"
"Qa 163,163,139\nQmvav 236,76,120\ns_waitcnt lgkmcnt(4)\nQa 224,224,148\nQa 225,225,149\nQmvav 96,72,144\nQta 64,68,16384\nQa 226,226,150\n"
"Qmvav 96,76,152\nQa 227,227,151\nQk 173,174,175\nQmvav 128,72,176\nQmvav 128,76,184\nds_read_b32 v66, v74\ns_waitcnt lgkmcnt(2)\nQa 236,236,180\n"
"Qmvav 160,72,208\nQta 66,68,17408\nQa 237,237,181\nQmvav 160,76,216\nQa 238,238,182\nQa 239,239,183\nQmvav 192,72,240\nQmvav 192,76,248\n"
"s_waitcnt lgkmcnt(1)\nv_cmp_gt_u32 vcc, v75, v66\nQmvav 224,72,84\ns_cbranch_vccnz .Lso1960r0\n.Lro1960r0:\nQmvav 224,76,116\nQta 68,68,24576\ns_add_u32 m0, s96, 65536\n"
"s_add_u32 s95, s67, 86016\ns_waitcnt lgkmcnt(5)\nQmvaa 204,56,0\nbuffer_load_dwordx4 v62, s[52:55], s95 offen lds\nQmvaa 204,60,28\nQmvaa 236,56,4\nQta 70,68,25600\nQmvaa 236,60,32\n"
"s_add_u32 s95, s67, 125952\nQmvaa 96,56,8\nbuffer_load_dwordx4 v62, s[52:55], s95 offen offset:1024 lds\nQmvaa 96,60,36\nQta 72,68,32768\nQk 174,204,205\nQmvaa 128,56,12\nQk 175,206,207\n"
"Qk 236,236,237\nQmvaa 128,60,40\nbuffer_store_dwordx4 v[172:175], v79, s[24:27], s68 offen offset:3072 sc1\nQk 237,238,239\nQmvaa 160,56,16\nQta 74,68,33792\ns_add_u32 m0, s96, 73728\ns_add_u32 s95, s72, 86016\n"
"Qmvaa 160,60,44\nbuffer_load_dwordx4 v62, s[52:55], s95 offen lds\nQmvaa 192,56,20\nQk 238,96,97\nQk 239,98,99\nQmvaa 192,60,48\nQta 76,68,40960\ns_add_u32 s68, s64, 4096\n"
"Qmvaa 224,56,24\nbuffer_store_dwordx4 v[236:239], v79, s[24:27], s68 offen offset:0 sc1\ns_add_u32 s95, s72, 125952\nQmvaa 224,60,52\nbuffer_load_dwordx4 v62, s[52:55], s95 offen offset:1024 lds\ns_waitcnt lgkmcnt(3)\nQmaaa 116,64,80\nQta 78,68,41984\n"
"Qk 128,128,129\nQmaaa 116,68,108\nQk 129,130,131\nQk 130,160,161\nQmaaa 120,64,84\ns_add_u32 m0, s96, 81920\ns_add_u32 s95, s73, 86016\nQmaaa 120,68,112\n"
"buffer_load_dwordx4 v62, s[52:55], s95 offen lds\nQmaaa 124,64,88\nQta 56,68,49152\nQk 131,162,163\nQmaav 124,68,168\nbuffer_store_dwordx4 v[128:131], v79, s[24:27], s68 offen offset:1024 sc1\nQk 192,192,193\nQmaaa 128,64,92\n"
"Qk 193,194,195\nQk 194,224,225\nQmaav 128,68,200\nQta 58,68,50176\ns_add_u32 s95, s73, 125952\nQmaaa 132,64,96\nbuffer_load_dwordx4 v62, s[52:55], s95 offen offset:1024 lds\nQmaav 132,68,232\n"
"Qk 195,226,227\nQmaaa 136,64,100\nQta 60,68,57344\nv_bfe_u32 v61, v60, 6, 2\nQmaav 136,68,212\nv_lshrrev_b32 v62, 8, v60\nv_xor_b32 v61, v61, v62\nQmaaa 140,64,104\n"
"v_bfe_u32 v62, v60, 5, 1\nv_lshl_or_b32 v61, v61, 1, v62\nQmaav 140,68,244\nQta 62,68,58368\nv_and_b32 v62, 16, v60\ns_waitcnt lgkmcnt(4)\nQmaav 116,72,80\nv_mov_b32 v63, 0x3f80\n"
"v_lshlrev_b32 v63, v62, v63\nQmaav 116,76,88\nv_min_u32 v62, 1, v61\nv_add_u32 v62, -1, v62\nQmaav 120,72,112\nv_and_b32 v62, v62, v63\nv_xor_b32 v61, 1, v61\nQmaav 120,76,120\n"
"v_min_u32 v61, 1, v61\nv_add_u32 v61, -1, v61\nQmaav 124,72,144\nQta 64,68,16640\nv_and_b32 v61, v61, v63\nQmaav 124,76,152\nv_accvgpr_write_b32 a202, v62\nv_accvgpr_write_b32 a203, v61\n"
"Qmaav 128,72,176\nv_accvgpr_write_b32 a200, 0\nv_accvgpr_write_b32 a201, 0\nQmaav 128,76,184\nQta 66,68,17664\nv_accvgpr_write_b32 a204, 0\nQmaav 132,72,208\nbuffer_store_dwordx4 v[192:195], v79, s[24:27], s68 offen offset:2048 sc1\n"
"v_accvgpr_write_b32 a205, 0\nQmaav 132,76,216\nQmaav 136,72,240\nQta 68,68,24832\nQmaav 136,76,248\nQmaav 140,72,84\nQmaav 140,76,116\nQta 70,68,25856\n"
"s_waitcnt lgkmcnt(4)\nQmaaa 116,56,0\nQmaaa 116,60,28\nQmaaa 120,56,4\nQmaaa 120,60,32\nQmaaa 124,56,8\nQta 72,68,33024\nQmaaa 124,60,36\n"
"s_nop 2\nds_write_b128 v58, a[116:119] offset:0\nQmaaa 128,56,12\nQmaaa 128,60,40\nQta 74,68,34048\nQmaaa 132,56,16\nds_write_b128 v58, a[120:123] offset:1024\nQmaaa 132,60,44\n"
"Qmaaa 136,56,20\nQta 76,68,41216\nQmaaa 136,60,48\nds_write_b128 v58, a[124:127] offset:2048\nQmaaa 140,56,24\nQmaaa 140,60,52\nQta 78,68,42240\ns_waitcnt lgkmcnt(7)\n"
"Qmaaa 144,64,80\nds_write_b128 v58, a[128:131] offset:3072\nQmaaa 144,68,108\nQmaaa 148,64,84\nQmaaa 148,68,112\nds_write_b128 v58, a[132:135] offset:4096\nQmaaa 152,64,88\nQta 56,68,49408\n"
"Qmaav 152,68,168\nQmaaa 156,64,92\nds_write_b128 v58, a[136:139] offset:5120\nQmaav 156,68,200\nQta 58,68,50432\nQmaaa 160,64,96\nQmaav 160,68,232\nds_write_b128 v58, a[140:143] offset:6144\n"
"Qmaaa 164,64,100\nQta 60,68,57600\nQmaav 164,68,212\nQmaaa 168,64,104\nQmaav 168,68,244\nQta 62,68,58624\ns_waitcnt lgkmcnt(8)\nQmaav 144,72,80\n"
"v_add_u32 v75, 1, v75\nQmaav 144,76,88\ns_waitcnt vmcnt(8)\nds_write_b32 v73, v75\nQmaav 148,72,112\nQmaav 148,76,120\nQmaav 152,72,144\nQta 64,68,16896\n"
"Qmaav 152,76,152\nQmaav 156,72,176\nQmaav 156,76,184\nQta 66,68,17920\nQmaav 160,72,208\nQmaav 160,76,216\nds_read_b32 v66, v74\nQmaav 164,72,240\n"
"Qta 68,68,25088\nQmaav 164,76,248\nQmaav 168,72,84\nQmaav 168,76,116\ns_waitcnt lgkmcnt(1)\nv_cmp_gt_u32 vcc, v75, v66\ns_waitcnt lgkmcnt(5)\nQmaaa 144,56,0\n"
"s_cbranch_vccnz .Lso2046r0\n.Lro2046r0:\nQmaaa 144,60,28\nQta 70,68,26112\nQmaaa 148,56,4\nQmaaa 148,60,32\nQdv 104,59,2048\nQmaaa 152,56,8\n"
"Qta 72,68,33280\nQmaaa 152,60,36\ns_nop 0\nds_write_b128 v58, a[144:147] offset:33792\nQmaaa 156,56,12\nQdv 136,59,3072\nQmaaa 156,60,40\nQta 74,68,34304\n"
"Qmaaa 160,56,16\nds_write_b128 v58, a[148:151] offset:34816\nQmaaa 160,60,44\nQdv 148,59,5120\nQmaaa 164,56,20\nQta 76,68,41472\nQmaaa 164,60,48\nds_write_b128 v58, a[152:155] offset:35840\n"
"Qmaaa 168,56,24\nQdv 180,59,0\ns_waitcnt lgkmcnt(9)\nQa 8,8,104\nQmaaa 168,60,52\nQta 78,68,42496\nQa 9,9,105\ns_waitcnt lgkmcnt(10)\n"
"Qmvaa 0,64,80\nds_write_b128 v58, a[156:159] offset:36864\nQmvaa 0,68,108\nQa 10,10,106\nQa 11,11,107\nQmvaa 4,64,84\ns_waitcnt lgkmcnt(8)\nQa 12,12,136\n"
"Qa 13,13,137\nQmvaa 4,68,112\nds_write_b128 v58, a[160:163] offset:37888\nQmvaa 8,64,88\nQta 56,68,49664\nQa 14,14,138\nQa 15,15,139\nQmvav 8,68,168\n"
"Qdv 104,59,4096\ns_waitcnt lgkmcnt(8)\nQa 20,20,148\nQmvaa 12,64,92\nds_write_b128 v58, a[164:167] offset:38912\nQmvav 12,68,200\nQta 58,68,50688\nQa 21,21,149\n"
"Qmvaa 16,64,96\nQa 22,22,150\nQa 23,23,151\nQmvav 16,68,232\nQdv 136,59,6144\ns_waitcnt lgkmcnt(8)\nQa 0,0,180\nQmvaa 20,64,100\n"
"Qta 60,68,57856\nQa 1,1,181\nQmvav 20,68,212\nds_write_b128 v58, a[168:171] offset:39936\nQmvaa 24,64,104\nQa 2,2,182\nQa 3,3,183\nQmvav 24,68,244\n"
"Qta 62,68,58880\ns_waitcnt lgkmcnt(6)\nQa 16,16,104\ns_waitcnt lgkmcnt(6)\nQmvav 0,72,80\nQdv 148,59,1024\nv_add_u32 v75, 1, v75\nQmvav 0,76,88\n"
"s_waitcnt vmcnt(1)\nds_write_b32 v73, v75\nQmvav 4,72,112\nQa 17,17,105\nQa 18,18,106\nQmvav 4,76,120\nQa 19,19,107\ns_waitcnt lgkmcnt(5)\n"
"Qa 24,24,136\nQmvav 8,72,144\nQta 64,69,0\nQa 25,25,137\nQmvav 8,76,152\nQa 26,26,138\nQa 27,27,139\nQmvav 12,72,176\n"
"s_waitcnt vmcnt(0)\nbuffer_store_dword v67, v77, s[24:27], 0 offen sc1\nQmvav 12,76,184\nQta 66,69,512\ns_waitcnt lgkmcnt(3)\nQa 4,4,148\nQmvav 16,72,208\nQa 5,5,149\n"
"Qa 6,6,150\nQmvav 16,76,216\nds_read_b32 v66, v74\nQa 7,7,151\nQmvav 20,72,240\nQta 68,69,1024\nQmvav 20,76,248\nQmvav 24,72,84\n"
"Qmvav 24,76,116\ns_waitcnt lgkmcnt(1)\nv_cmp_gt_u32 vcc, v75, v66\ns_waitcnt lgkmcnt(6)\nQmvaa 0,56,0\ns_cbranch_vccnz .Lso2088r0\n.Lro2088r0:\nQmvaa 0,60,28\n"
"Qta 70,69,1536\nQmvaa 4,56,4\nQmvaa 4,60,32\nQdv 180,59,33792\nQmvaa 8,56,8\nQta 72,69,8192\nQmvaa 8,60,36\nQmvaa 12,56,12\n"
"Qdv 104,59,34816\nQmvaa 12,60,40\nQta 74,69,8704\nQmvaa 16,56,16\nQmvaa 16,60,44\nQdv 136,59,35840\nQmvaa 20,56,20\nQta 76,69,9216\n"
"Qmvaa 20,60,48\ns_waitcnt lgkmcnt(5)\nQa 28,28,180\nQa 29,29,181\nQmvaa 24,56,24\nQdv 148,59,36864\nQa 30,30,182\nQmvaa 24,60,52\n"
"Qta 78,69,9728\nQa 31,31,183\ns_waitcnt lgkmcnt(11)\nQmaaa 172,64,80\ns_waitcnt lgkmcnt(5)\nQa 32,32,104\nQa 33,33,105\nQmaaa 172,68,108\n"
"Qa 34,34,106\nQa 35,35,107\nQmaaa 176,64,84\nQmaaa 176,68,112\ns_waitcnt lgkmcnt(3)\nQa 36,36,136\nQa 37,37,137\nQmaaa 180,64,88\n"
"Qta 56,69,16384\nQa 38,38,138\nQmaav 180,68,168\nQdv 180,59,37888\nQa 39,39,139\nQmaaa 184,64,92\ns_waitcnt lgkmcnt(3)\nQa 40,40,148\n"
"Qa 41,41,149\nQmaav 184,68,200\nQta 58,69,16896\nQa 42,42,150\nQmaaa 188,64,96\nQdv 104,59,38912\nQa 43,43,151\nQmaav 188,68,232\n"
"Qmaaa 192,64,100\nQta 60,69,17408\n.Lpg21:\ns_cbranch_execz .Lpg25\nQmaav 192,68,212\nQdv 136,59,39936\nQmaaa 196,64,104\ns_waitcnt lgkmcnt(4)\n"
"Qa 44,44,180\nQa 45,45,181\nQmaav 196,68,244\nQta 62,69,17920\nQa 46,46,182\ns_waitcnt lgkmcnt(5)\nQmaav 172,72,80\nQa 47,47,183\n"
"Qmaav 172,76,88\nQmaav 176,72,112\ns_waitcnt lgkmcnt(3)\nQa 48,48,104\nQa 49,49,105\nQmaav 176,76,120\nQa 50,50,106\nQa 51,51,107\n"
"Qmaav 180,72,144\nQta 64,68,17152\nQmaav 180,76,152\ns_waitcnt lgkmcnt(2)\nQa 52,52,136\nQa 53,53,137\nQmaav 184,72,176\nQa 54,54,138\n"
"Qa 55,55,139\nQmaav 184,76,184\nQta 66,68,18176\nQmaav 188,72,208\nQmaav 188,76,216\nQmaav 192,72,240\nQta 68,68,25344\nQmaav 192,76,248\n"
"Qmaav 196,72,84\nQmaav 196,76,116\nQta 70,68,26368\ns_waitcnt lgkmcnt(4)\nQmaaa 172,56,0\nQmaaa 172,60,28\nQmaaa 176,56,4\nQmaaa 176,60,32\n"
"Qmaaa 180,56,8\nQta 72,68,33536\nQmaaa 180,60,36\ns_nop 2\nds_write_b128 v58, a[172:175] offset:0\nQmaaa 184,56,12\nQmaaa 184,60,40\nQta 74,68,34560\n"
"Qmaaa 188,56,16\nds_write_b128 v58, a[176:179] offset:1024\nQmaaa 188,60,44\nQmaaa 192,56,20\nQta 76,68,41728\nQmaaa 192,60,48\nds_write_b128 v58, a[180:183] offset:2048\nQmaaa 196,56,24\n"
"Qmaaa 196,60,52\nQta 78,68,42752\ns_waitcnt lgkmcnt(7)\nQmvaa 28,64,80\nds_write_b128 v58, a[184:187] offset:3072\nQmvaa 28,68,108\nQmvaa 32,64,84\nQmvaa 32,68,112\n"
"ds_write_b128 v58, a[188:191] offset:4096\nQmvaa 36,64,88\nQta 56,68,49920\nQmvav 36,68,168\nQmvaa 40,64,92\nds_write_b128 v58, a[192:195] offset:5120\nQmvav 40,68,200\nQta 58,68,50944\n"
"Qmvaa 44,64,96\nQmvav 44,68,232\nbuffer_load_dword v78, v76, s[24:27], 0 offen sc1\nQmvaa 48,64,100\nQta 60,68,58112\nQmvav 48,68,212\nds_write_b128 v58, a[196:199] offset:6144\nQmvaa 52,64,104\n"
"Qmvav 52,68,244\nQta 62,68,59136\ns_waitcnt lgkmcnt(8)\nQmvav 28,72,80\nv_add_u32 v75, 1, v75\nQmvav 28,76,88\nds_write_b32 v73, v75\nQmvav 32,72,112\n"
"Qmvav 32,76,120\nQmvav 36,72,144\nQta 64,69,256\nQmvav 36,76,152\nQmvav 40,72,176\nQmvav 40,76,184\nQta 66,69,768\nQmvav 44,72,208\n"
"Qmvav 44,76,216\nds_read_b32 v66, v74\nQmvav 48,72,240\nQta 68,69,1280\nQmvav 48,76,248\nQmvav 52,72,84\nQmvav 52,76,116\ns_waitcnt lgkmcnt(1)\n"
"v_cmp_gt_u32 vcc, v75, v66\ns_waitcnt lgkmcnt(5)\nQmvaa 28,56,0\ns_cbranch_vccnz .Lso2172r0\n.Lro2172r0:\nQmvaa 28,60,28\nQta 70,69,1792\nQmvaa 32,56,4\n"
"Qmvaa 32,60,32\nQdv 148,59,2048\nQmvaa 36,56,8\nQta 72,69,8448\nQmvaa 36,60,36\nQmvaa 40,56,12\nQdv 180,59,3072\nQmvaa 40,60,40\n"
"Qta 74,69,8960\nQmvaa 44,56,16\nQmvaa 44,60,44\nQdv 104,59,5120\nQmvaa 48,56,20\nQta 76,69,9472\nQmvaa 48,60,48\ns_waitcnt lgkmcnt(5)\n"
"Qa 156,156,148\nQa 157,157,149\nQmvaa 52,56,24\nQdv 136,59,0\nQa 158,158,150\nQmvaa 52,60,52\nQta 78,69,9984\nQa 159,159,151\n"
"s_waitcnt lgkmcnt(11)\nQmvaa 92,64,80\ns_waitcnt lgkmcnt(5)\nQa 188,188,180\nQa 189,189,181\nQmvaa 92,68,108\nQa 190,190,182\nQa 191,191,183\n"
"Qmvaa 124,64,84\ns_waitcnt vmcnt(0)\nv_sub_u32 v66, v78, v65\nv_cmp_gt_u32 vcc, 1, v66\ns_and_b64 vcc, vcc, s[28:29]\ns_cbranch_scc1 .Lx0_w\n.Lx0_r:\nQmvaa 124,68,112\n"
"s_add_u32 s69, s98, 0\nbuffer_load_dwordx4 a[208:211], v79, s[24:27], s69 offen offset:0 sc1\nQmvaa 156,64,88\nQta 56,69,16640\ns_waitcnt lgkmcnt(4)\nQa 252,252,104\nQmvav 156,68,168\nbuffer_load_dwordx4 a[212:215], v79, s[24:27], s69 offen offset:1024 sc1\n"
"Qmvaa 188,64,92\nQdv 148,59,4096\nQa 253,253,105\nQmvav 188,68,200\nbuffer_load_dwordx4 a[216:219], v79, s[24:27], s69 offen offset:2048 sc1\nQmvaa 220,64,96\nQta 58,69,17152\nQa 254,254,106\n"
"Qa 255,255,107\nQmvav 220,68,232\nbuffer_load_dwordx4 a[220:223], v79, s[24:27], s69 offen offset:3072 sc1\nQmvaa 252,64,100\nQdv 180,59,6144\ns_waitcnt lgkmcnt(5)\nQa 92,92,136\nQmvav 252,68,212\n"
"s_add_u32 s69, s98, 4096\nbuffer_load_dwordx4 a[224:227], v79, s[24:27], s69 offen offset:0 sc1\nQmvaa 100,64,104\nQta 60,69,17664\nQa 93,93,137\nQa 94,94,138\nQa 95,95,139\nQmvav 100,68,244\n"
"buffer_load_dwordx4 a[228:231], v79, s[24:27], s69 offen offset:1024 sc1\ns_waitcnt lgkmcnt(9)\nQmvav 92,72,80\nQdv 104,59,1024\ns_waitcnt lgkmcnt(4)\nQa 220,220,148\nQmvav 92,76,88\nbuffer_load_dwordx4 a[232:235], v79, s[24:27], s69 offen offset:2048 sc1\n"
"Qmvav 124,72,112\nQta 62,69,18176\nQa 221,221,149\nQmvav 124,76,120\nbuffer_load_dwordx4 a[236:239], v79, s[24:27], s69 offen offset:3072 sc1\nQmvav 156,72,144\nQa 222,222,150\nQa 223,223,151\n"
"Qmvav 156,76,152\ns_add_u32 s69, s98, 16384\nbuffer_load_dwordx4 a[240:243], v79, s[24:27], s69 offen offset:0 sc1\nQmvav 188,72,176\ns_waitcnt lgkmcnt(3)\nQa 100,100,180\nQa 101,101,181\nQmvav 188,76,184\n"
"buffer_load_dwordx4 a[244:247], v79, s[24:27], s69 offen offset:1024 sc1\nQmvav 220,72,208\nv_add_u32 v75, 1, v75\nQa 102,102,182\nQmvav 220,76,216\nds_write_b32 v73, v75\nQa 103,103,183\nQmvav 252,72,240\n"
"buffer_load_dwordx4 a[248:251], v79, s[24:27], s69 offen offset:2048 sc1\nQmvav 252,76,248\ns_waitcnt lgkmcnt(2)\nQa 124,124,104\nQa 125,125,105\nQmvav 100,72,84\nQa 126,126,106\nQa 127,127,107\n"
"Qmvav 100,76,116\ns_waitcnt lgkmcnt(1)\nQmvaa 92,56,0\nQmvaa 92,60,28\nQmvaa 124,56,4\nQmvaa 124,60,32\nds_read_b32 v66, v74\nQmvaa 156,56,8\n"
"Qmvaa 156,60,36\nQmvaa 188,56,12\nQmvaa 188,60,40\ns_waitcnt lgkmcnt(0)\nv_cmp_gt_u32 vcc, v75, v66\nQmvaa 220,56,16\ns_cbranch_vccnz .Lso2222r0\n.Lro2222r0:\n"
"Qmvaa 220,60,44\nQmvaa 252,56,20\nQmvaa 252,60,48\nQmvaa 100,56,24\nQmvaa 100,60,52\ns_waitcnt vmcnt(10)\nQmvaa 92,202,208\nQmvaa 124,200,208\n"
"s_waitcnt vmcnt(9)\nQmvaa 156,202,212\nQmvaa 188,200,212\ns_nop 3\nQk 92,92,93\nQk 93,94,95\nds_write_b64 v56, v[92:93] offset:64\ns_waitcnt vmcnt(8)\n"
"Qmvaa 220,202,216\nQk 124,124,125\nQk 125,126,127\nds_write_b64 v56, v[124:125] offset:3392\nQmvaa 252,200,216\nQk 156,156,157\nQk 157,158,159\nds_write_b64 v56, v[156:157] offset:6720\n"
"s_waitcnt vmcnt(7)\nQmvaa 100,202,220\nQk 188,188,189\nQk 189,190,191\nds_write_b64 v56, v[188:189] offset:10048\nQmvaa 0,200,220\nQk 220,220,221\nQk 221,222,223\n"
"ds_write_b64 v56, v[220:221] offset:13376\ns_waitcnt vmcnt(6)\nQmvaa 4,202,224\nQk 252,252,253\nQk 253,254,255\nds_write_b64 v56, v[252:253] offset:16704\nQmvaa 8,200,224\nQk 100,100,101\n"
"Qk 101,102,103\nds_write_b64 v56, v[100:101] offset:20032\ns_waitcnt vmcnt(5)\nQmvaa 12,202,228\nQk 0,0,1\nQk 1,2,3\nds_write_b64 v56, v[0:1] offset:0\nQmvaa 16,200,228\n"
"Qk 4,4,5\nQk 5,6,7\nds_write_b64 v56, v[4:5] offset:3328\ns_waitcnt vmcnt(4)\nQmvaa 20,202,232\nQk 8,8,9\nQk 9,10,11\nds_write_b64 v56, v[8:9] offset:6656\n"
"Qmvaa 24,200,232\nQk 12,12,13\nQk 13,14,15\nds_write_b64 v56, v[12:13] offset:9984\ns_waitcnt vmcnt(3)\nQmvaa 28,202,236\nQk 16,16,17\nQk 17,18,19\n"
"ds_write_b64 v56, v[16:17] offset:13312\nQmvaa 32,200,236\nQk 20,20,21\nQk 21,22,23\nds_write_b64 v56, v[20:21] offset:16640\ns_waitcnt vmcnt(2)\nQmvaa 36,202,240\nQk 24,24,25\n"
"Qk 25,26,27\nds_write_b64 v56, v[24:25] offset:19968\nQmvaa 40,200,240\nQk 28,28,29\nQk 29,30,31\nds_write_b64 v56, v[28:29] offset:32\ns_waitcnt vmcnt(1)\nQmvaa 44,202,244\n"
"Qk 32,32,33\nQk 33,34,35\nds_write_b64 v56, v[32:33] offset:3360\nQmvaa 48,200,244\nQk 36,36,37\nQk 37,38,39\nds_write_b64 v56, v[36:37] offset:6688\ns_waitcnt vmcnt(0)\n"
"Qmvaa 52,202,248\nQk 40,40,41\nQk 41,42,43\nds_write_b64 v56, v[40:41] offset:10016\nQk 44,44,45\nQk 45,46,47\nds_write_b64 v56, v[44:45] offset:13344\nQk 48,48,49\n"
"Qk 49,50,51\nds_write_b64 v56, v[48:49] offset:16672\nQk 52,52,53\nQk 53,54,55\nds_write_b64 v56, v[52:53] offset:20000\ns_waitcnt lgkmcnt(0)\ns_barrier\nQda 0,57,0\n"
"Qda 4,57,3328\nQda 8,57,6656\nQda 12,57,9984\nQda 16,57,13312\nQda 20,57,16640\nQda 24,57,19968\nQda 28,71,64\nQda 32,71,6720\n"
"Qda 36,71,13376\nQda 40,71,20032\ns_mov_b32 s68, 0\ns_waitcnt lgkmcnt(10)\nbuffer_store_dwordx4 a[0:3], v64, s[40:43], s68 offen offset:0 sc1\ns_mov_b32 s68, 40960\ns_waitcnt lgkmcnt(9)\nbuffer_store_dwordx4 a[4:7], v64, s[40:43], s68 offen offset:0 sc1\n"
"s_mov_b32 s68, 81920\ns_waitcnt lgkmcnt(8)\nbuffer_store_dwordx4 a[8:11], v64, s[40:43], s68 offen offset:0 sc1\ns_mov_b32 s68, 122880\ns_waitcnt lgkmcnt(7)\nbuffer_store_dwordx4 a[12:15], v64, s[40:43], s68 offen offset:0 sc1\ns_mov_b32 s68, 163840\ns_waitcnt lgkmcnt(6)\n"
"buffer_store_dwordx4 a[16:19], v64, s[40:43], s68 offen offset:0 sc1\ns_mov_b32 s68, 204800\ns_waitcnt lgkmcnt(5)\nbuffer_store_dwordx4 a[20:23], v64, s[40:43], s68 offen offset:0 sc1\ns_mov_b32 s68, 245760\ns_waitcnt lgkmcnt(4)\nbuffer_store_dwordx4 a[24:27], v64, s[40:43], s68 offen offset:0 sc1\ns_mov_b32 s68, 0\n"
"s_waitcnt lgkmcnt(3)\nbuffer_store_dwordx4 a[28:31], v72, s[40:43], s68 offen offset:0 sc1\ns_mov_b32 s68, 81920\ns_waitcnt lgkmcnt(2)\nbuffer_store_dwordx4 a[32:35], v72, s[40:43], s68 offen offset:0 sc1\ns_mov_b32 s68, 163840\ns_waitcnt lgkmcnt(1)\nbuffer_store_dwordx4 a[36:39], v72, s[40:43], s68 offen offset:0 sc1\n"
"s_mov_b32 s68, 245760\ns_cmp_eq_u32 s93, 1\ns_cselect_b32 s68, 0x7ffff000, s68\ns_waitcnt lgkmcnt(0)\nbuffer_store_dwordx4 a[40:43], v72, s[40:43], s68 offen offset:0 sc1\ns_branch .Lfin\n.Lrole1:\n.Lpg22:\n"
"s_cbranch_execz .Lpg26\nQmvaa 96,64,88\nQta 56,69,49408\ns_lshl_b32 s46, %19, 4\nQmvav 96,68,168\nbuffer_load_dwordx4 a[84:87], v79, s[48:51], s31 offen offset:1024 sc1\nQmvaa 128,64,92\nds_read_b32 v66, v74\n"
"s_add_u32 s46, s46, 128\nQmvav 128,68,200\nQta 58,69,50432\nv_mov_b32 v77, 0x7ffff000\nQmvaa 160,64,96\nQmvav 160,68,232\nbuffer_load_dwordx4 a[88:91], v79, s[48:51], s31 offen offset:2048 sc1\nQmvaa 192,64,100\n"
"s_waitcnt lgkmcnt(1)\nv_cmp_gt_u32 vcc, v75, v66\nQmvav 192,68,212\ns_cbranch_vccnz .Lso1525r1\n.Lro1525r1:\nQmvaa 224,64,104\nQta 60,69,57600\nQmvav 224,68,244\n"
"buffer_load_dwordx4 a[92:95], v79, s[48:51], s31 offen offset:3072 sc1\ns_waitcnt vmcnt(31)\ns_waitcnt lgkmcnt(4)\nQmaav 140,72,84\nQmaav 140,76,116\nQta 62,69,58624\nQmaav 136,72,240\ns_add_u32 m0, s96, 65536\n"
"s_add_u32 s95, s67, 86016\nQmaav 136,76,248\nbuffer_load_dwordx4 v62, s[52:55], s95 offen lds\nQmaav 132,72,208\nQta 64,69,49664\nQmaav 132,76,216\nQmaav 128,72,176\nQmaav 128,76,184\n"
"Qta 66,69,50688\ns_add_u32 s95, s67, 125952\nQmaav 124,72,144\nbuffer_load_dwordx4 v62, s[52:55], s95 offen offset:1024 lds\nQmaav 124,76,152\nQmaav 120,72,112\nQta 68,69,57856\nQmaav 120,76,120\n"
"s_add_u32 m0, s96, 73728\ns_add_u32 s95, s72, 86016\nQmaav 116,72,80\nbuffer_load_dwordx4 v62, s[52:55], s95 offen lds\nQmaav 116,76,88\nQta 70,69,58880\ns_waitcnt lgkmcnt(4)\nQmaav 144,56,80\n"
"Qmaav 144,60,88\ns_add_u32 s95, s72, 125952\nQmaav 148,56,112\nbuffer_load_dwordx4 v62, s[52:55], s95 offen offset:1024 lds\nQmaav 148,60,120\nQmaav 152,56,144\nQta 72,69,49920\nQmaav 152,60,152\n"
"s_add_u32 m0, s96, 81920\ns_add_u32 s95, s73, 86016\nQmaav 156,56,176\nbuffer_load_dwordx4 v62, s[52:55], s95 offen lds\nQmaav 156,60,184\nQta 74,69,50944\nQmaav 160,56,208\nQmaav 160,60,216\n"
"Qmaav 164,56,240\nQta 76,69,58112\ns_add_u32 s95, s73, 125952\nQmaav 164,60,248\nbuffer_load_dwordx4 v62, s[52:55], s95 offen offset:1024 lds\nQmaav 168,56,84\nQmaav 168,60,116\nQta 78,69,59136\n"
"s_waitcnt lgkmcnt(4)\nQmvav 24,64,84\ns_add_u32 m0, s96, 90112\ns_add_u32 s95, s67, 86016\nQmvav 24,68,116\nbuffer_load_dwordx4 v61, s[52:55], s95 offen lds\nQmvav 20,64,240\nQmvav 20,68,248\n"
"Qmvav 16,64,208\nQta 56,68,0\ns_add_u32 s95, s67, 105472\nQmvav 16,68,216\nbuffer_load_dwordx4 v61, s[52:55], s95 offen offset:1024 lds\nQmvav 12,64,176\nQmvav 12,68,184\nQta 58,68,512\n"
"Qmvav 8,64,144\ns_add_u32 s31, s74, 135168\nQmvav 8,68,152\nbuffer_load_dwordx4 a[96:99], v79, s[48:51], s31 offen offset:0 sc1\nQmvav 4,64,112\nQta 60,68,1024\nQmvav 4,68,120\nQmvav 0,64,80\n"
"Qmvav 0,68,88\nQta 62,68,1536\ns_waitcnt lgkmcnt(4)\nQmvav 28,72,80\nbuffer_load_dwordx4 a[100:103], v79, s[48:51], s31 offen offset:1024 sc1\nQmvav 28,76,88\nQmvav 32,72,112\nQmvav 32,76,120\n"
"Qmvav 36,72,144\nQta 64,68,256\nQmvav 36,76,152\nbuffer_load_dwordx4 a[104:107], v79, s[48:51], s31 offen offset:2048 sc1\nQmvav 40,72,176\nQmvav 40,76,184\nQta 66,68,768\nQmvav 44,72,208\n"
"s_add_u32 s31, s74, 147456\nv_cmp_eq_u32 s[44:45], s46, v60\ns_nop 1\nv_cndmask_b32 v77, v77, v76, s[44:45]\nv_add_u32 v67, 1, v65\ns_xor_b32 s46, %19, 12\ns_lshl_b32 s28, 1, s46\nQmvav 44,76,216\n"
"buffer_load_dwordx4 a[108:111], v79, s[48:51], s31 offen offset:0 sc1\nQmvav 48,72,240\nQta 68,68,1280\nQmvav 48,76,248\nv_add_u32 v75, 1, v75\nQmvav 52,72,84\ns_waitcnt vmcnt(37)\nds_write_b32 v73, v75\n"
"Qmvav 52,76,116\nQta 70,68,1792\ns_waitcnt lgkmcnt(5)\nQmaav 196,56,84\nbuffer_load_dwordx4 a[112:115], v79, s[48:51], s31 offen offset:1024 sc1\nQmaav 196,60,116\nQmaav 192,56,240\nQmaav 192,60,248\n"
"Qmaav 188,56,208\nQta 72,68,8192\nQmaav 188,60,216\nbuffer_load_dwordx4 v[168:171], v79, s[48:51], s31 offen offset:2048 sc1\nQmaav 184,56,176\nds_read_b32 v66, v74\nQmaav 184,60,184\nQta 74,68,9216\n"
"Qmaav 180,56,144\nQmaav 180,60,152\nbuffer_load_dwordx4 v[200:203], v79, s[48:51], s31 offen offset:3072 sc1\nQmaav 176,56,112\ns_waitcnt lgkmcnt(1)\nv_cmp_gt_u32 vcc, v75, v66\nQmaav 176,60,120\ns_cbranch_vccnz .Lso1595r1\n"
".Lro1595r1:\nQmaav 172,56,80\nQta 76,68,16384\ns_add_u32 s31, s74, 151552\nQmaav 172,60,88\nbuffer_load_dwordx4 v[232:235], v79, s[48:51], s31 offen offset:0 sc1\ns_waitcnt lgkmcnt(4)\nQmvav 92,64,80\n"
"Qmvav 92,68,88\nQta 78,68,17408\nQmvav 124,64,112\nQmvav 124,68,120\nbuffer_load_dwordx4 v[212:215], v79, s[48:51], s31 offen offset:1024 sc1\nQmvav 156,64,144\nQta 56,68,8448\nQmvav 156,68,152\n"
"Qmvav 188,64,176\nQmvav 188,68,184\nQta 58,68,9472\nQmvav 220,64,208\nbuffer_load_dwordx4 v[244:247], v79, s[48:51], s31 offen offset:2048 sc1\nQmvav 220,68,216\nQmvav 252,64,240\nQta 60,68,16640\n"
".Lpg23:\ns_cbranch_execz .Ltr8\nQmvav 252,68,248\ns_add_u32 m0, s91, 0\ns_add_u32 s31, s77, 131072\nQmvav 100,64,84\nbuffer_load_dwordx4 v79, s[48:51], s31 offen sc1 lds\nQmvav 100,68,116\n"
"Qta 62,68,17664\ns_waitcnt lgkmcnt(4)\nQmaav 224,72,84\nQmaav 224,76,116\nQmaav 220,72,240\nbuffer_load_dwordx4 v79, s[48:51], s31 offen offset:1024 sc1 lds\nQmaav 220,76,248\nQmaav 216,72,208\n"
"Qta 64,68,8704\nQmaav 216,76,216\nQmaav 212,72,176\nbuffer_load_dwordx4 v79, s[48:51], s31 offen offset:2048 sc1 lds\nQmaav 212,76,184\nQta 66,68,9728\nQmaav 208,72,144\nQmaav 208,76,152\n"
"Qmaav 204,72,112\nQta 68,68,16896\nQmaav 204,76,120\nbuffer_load_dwordx4 v79, s[48:51], s31 offen offset:3072 sc1 lds\nQmaav 200,72,80\nQmaav 200,76,88\nQta 70,68,17920\ns_waitcnt lgkmcnt(4)\n"
"Qmaav 228,56,80\ns_add_u32 m0, s91, 4096\ns_add_u32 s31, s77, 135168\nQmaav 228,60,88\nbuffer_load_dwordx4 v79, s[48:51], s31 offen sc1 lds\nQmaav 232,56,112\nQmaav 232,60,120\nQmaav 236,56,144\n"
"Qta 72,68,8960\nQmaav 236,60,152\nbuffer_load_dwordx4 v79, s[48:51], s31 offen offset:1024 sc1 lds\nQmaav 240,56,176\nQmaav 240,60,184\nQta 74,68,9984\nQmaav 244,56,208\nQmaav 244,60,216\n"
"buffer_load_dwordx4 v79, s[48:51], s31 offen offset:2048 sc1 lds\nQmaav 248,56,240\nQta 76,68,17152\nQmaav 248,60,248\nQmaav 252,56,84\nQmaav 252,60,116\nQta 78,68,18176\ns_add_u32 m0, s91, 33792\n"
"s_add_u32 s31, s77, 147456\ns_waitcnt lgkmcnt(4)\nQmvav 172,64,84\nbuffer_load_dwordx4 v79, s[48:51], s31 offen sc1 lds\nQmvav 172,68,116\nQmvav 140,64,240\nQmvav 140,68,248\nQmvav 108,64,208\n"
"Qta 56,68,24576\nQmvav 108,68,216\nbuffer_load_dwordx4 v79, s[48:51], s31 offen offset:1024 sc1 lds\nQmvav 228,64,176\nQmvav 228,68,184\nQta 58,68,25600\nQmvav 196,64,144\nQmvav 196,68,152\n"
"buffer_load_dwordx4 v79, s[48:51], s31 offen offset:2048 sc1 lds\nQmvav 164,64,112\nQta 60,68,32768\nQmvav 164,68,120\nv_add_u32 v75, 1, v75\nQmvav 132,64,80\ns_waitcnt vmcnt(49)\nds_write_b32 v73, v75\n"
"Qmvav 132,68,88\nQta 62,68,33792\ns_waitcnt lgkmcnt(5)\nQmvav 204,72,80\nbuffer_load_dwordx4 v79, s[48:51], s31 offen offset:3072 sc1 lds\nQmvav 204,76,88\nQmvav 236,72,112\nQmvav 236,76,120\n"
"Qmvav 96,72,144\nQta 64,68,24832\ns_add_u32 m0, s91, 37888\ns_add_u32 s31, s77, 151552\nQmvav 96,76,152\nbuffer_load_dwordx4 v79, s[48:51], s31 offen sc1 lds\nQmvav 128,72,176\nds_read_b32 v66, v74\n"
"Qmvav 128,76,184\nQta 66,68,25856\nQmvav 160,72,208\nQmvav 160,76,216\nbuffer_load_dwordx4 v79, s[48:51], s31 offen offset:1024 sc1 lds\nQmvav 192,72,240\ns_waitcnt lgkmcnt(1)\nv_cmp_gt_u32 vcc, v75, v66\n"
"Qmvav 192,76,248\ns_cbranch_vccnz .Lso1665r1\n.Lro1665r1:\nQmvav 224,72,84\nQta 68,68,33024\nQmvav 224,76,116\nbuffer_load_dwordx4 v79, s[48:51], s31 offen offset:2048 sc1 lds\ns_waitcnt vmcnt(38)\n"
"s_waitcnt lgkmcnt(4)\nQmaaa 140,56,24\nQmaaa 140,60,52\nQta 70,68,34048\nQmaaa 136,56,20\ns_add_u32 m0, s96, 0\ns_add_u32 s95, s67, 126976\nQmaaa 136,60,48\n"
"buffer_load_dwordx4 v61, s[52:55], s95 offen lds\nQmaaa 132,56,16\nQta 72,68,25088\nQmaaa 132,60,44\nQmaaa 128,56,12\nQmaaa 128,60,40\nQta 74,68,26112\ns_add_u32 s95, s67, 146432\n"
"Qmaaa 124,56,8\nbuffer_load_dwordx4 v61, s[52:55], s95 offen offset:1024 lds\nQmaaa 124,60,36\nQmaaa 120,56,4\nQta 76,68,33280\nQmaaa 120,60,32\ns_add_u32 s31, s75, 131072\nQmaaa 116,56,0\n"
"buffer_load_dwordx4 v[80:83], v79, s[48:51], s31 offen offset:0 sc1\nQmaaa 116,60,28\nQta 78,68,34304\ns_waitcnt lgkmcnt(4)\nQmaaa 144,64,0\nQmaaa 144,68,28\nQmaaa 148,64,4\nbuffer_load_dwordx4 v[112:115], v79, s[48:51], s31 offen offset:1024 sc1\n"
"Qmaaa 148,68,32\nQmaaa 152,64,8\nQta 56,68,25344\nQmaaa 152,68,36\nQmaaa 156,64,12\nbuffer_load_dwordx4 v[144:147], v79, s[48:51], s31 offen offset:2048 sc1\nQmaaa 156,68,40\nQta 58,68,26368\n"
"Qmaaa 160,64,16\nQmaaa 160,68,44\nQmaaa 164,64,20\nQta 60,68,33536\nQmaaa 164,68,48\nbuffer_load_dwordx4 v[176:179], v79, s[48:51], s31 offen offset:3072 sc1\nQmaaa 168,64,24\nQmaaa 168,68,52\n"
"Qta 62,68,34560\ns_waitcnt lgkmcnt(4)\nQmvaa 24,72,24\ns_add_u32 s31, s75, 135168\nQmvaa 24,76,52\nbuffer_load_dwordx4 v[208:211], v79, s[48:51], s31 offen offset:0 sc1\nQmvaa 20,72,20\nQmvaa 20,76,48\n"
"Qmvaa 16,72,16\nQta 64,68,40960\nQmvaa 16,76,44\nbuffer_load_dwordx4 v[240:243], v79, s[48:51], s31 offen offset:1024 sc1\nQmvaa 12,72,12\nQmvaa 12,76,40\nQta 66,68,41472\nQmvaa 8,72,8\n"
"Qmvaa 8,76,36\nbuffer_load_dwordx4 v[84:87], v79, s[48:51], s31 offen offset:2048 sc1\nQmvaa 4,72,4\nQta 68,68,41984\nQmvaa 4,76,32\nQmvaa 0,72,0\nQmvaa 0,76,28\nQta 70,68,42496\n"
"s_add_u32 s31, s75, 147456\ns_waitcnt lgkmcnt(4)\nQmvaa 28,56,0\nbuffer_load_dwordx4 v[88:91], v79, s[48:51], s31 offen offset:0 sc1\nQmvaa 28,60,28\nQmvaa 32,56,4\nQmvaa 32,60,32\nQmvaa 36,56,8\n"
"Qta 72,68,41216\nQmvaa 36,60,36\nbuffer_load_dwordx4 v[120:123], v79, s[48:51], s31 offen offset:1024 sc1\nQmvaa 40,56,12\nQmvaa 40,60,40\nQta 74,68,41728\nQmvaa 44,56,16\nQmvaa 44,60,44\n"
"buffer_load_dwordx4 v[152:155], v79, s[48:51], s31 offen offset:2048 sc1\nQmvaa 48,56,20\nQta 76,68,42240\nQmvaa 48,60,48\nv_add_u32 v75, 1, v75\nQmvaa 52,56,24\ns_waitcnt vmcnt(40)\nds_write_b32 v73, v75\n"
"Qmvaa 52,60,52\nQta 78,68,42752\ns_waitcnt lgkmcnt(5)\nQmaaa 196,64,24\nbuffer_load_dwordx4 v[184:187], v79, s[48:51], s31 offen offset:3072 sc1\nQmaaa 196,68,52\nQmaaa 192,64,20\nQmaaa 192,68,48\n"
"Qmaaa 188,64,16\nQta 56,68,49152\ns_add_u32 s31, s75, 151552\nQmaaa 188,68,44\nbuffer_load_dwordx4 v[216:219], v79, s[48:51], s31 offen offset:0 sc1\nQmaaa 184,64,12\nds_read_b32 v66, v74\nQmaaa 184,68,40\n"
"Qta 58,68,50176\nQmaaa 180,64,8\nQmaaa 180,68,36\nbuffer_load_dwordx4 v[248:251], v79, s[48:51], s31 offen offset:1024 sc1\nQmaaa 176,64,4\ns_waitcnt lgkmcnt(1)\nv_cmp_gt_u32 vcc, v75, v66\nQmaaa 176,68,32\n"
"s_cbranch_vccnz .Lso1735r1\n.Lro1735r1:\nQmaaa 172,64,0\nQta 60,68,57344\nQmaaa 172,68,28\nbuffer_load_dwordx4 v[116:119], v79, s[48:51], s31 offen offset:2048 sc1\ns_waitcnt lgkmcnt(4)\nQmvaa 92,72,0\n"
"Qmvaa 92,76,28\nQta 62,68,58368\nQmvaa 124,72,4\ns_add_u32 m0, s96, 8192\ns_add_u32 s95, s72, 86016\nQmvaa 124,76,32\nbuffer_load_dwordx4 v61, s[52:55], s95 offen lds\nQmvaa 156,72,8\n"
"Qta 64,68,49408\nQmvaa 156,76,36\nQmvaa 188,72,12\nQmvaa 188,76,40\nQta 66,68,50432\ns_add_u32 s95, s72, 105472\nQmvaa 220,72,16\nbuffer_load_dwordx4 v61, s[52:55], s95 offen offset:1024 lds\n"
"Qmvaa 220,76,44\nQmvaa 252,72,20\nQta 68,68,57600\nQmvaa 252,76,48\ns_add_u32 m0, s96, 16384\ns_add_u32 s95, s72, 126976\nQmvaa 100,72,24\nbuffer_load_dwordx4 v61, s[52:55], s95 offen lds\n"
"Qmvaa 100,76,52\nQta 70,68,58624\ns_waitcnt lgkmcnt(4)\nQmaaa 224,56,24\nQmaaa 224,60,52\ns_add_u32 s95, s72, 146432\nQmaaa 220,56,20\nbuffer_load_dwordx4 v61, s[52:55], s95 offen offset:1024 lds\n"
"Qmaaa 220,60,48\nQmaaa 216,56,16\nQta 72,68,49664\nQmaaa 216,60,44\ns_add_u32 m0, s96, 24576\ns_add_u32 s95, s73, 86016\nQmaaa 212,56,12\nbuffer_load_dwordx4 v61, s[52:55], s95 offen lds\n"
"Qmaaa 212,60,40\nQta 74,68,50688\nQmaaa 208,56,8\nQmaaa 208,60,36\nQmaaa 204,56,4\nQta 76,68,57856\ns_add_u32 s95, s73, 105472\nQmaaa 204,60,32\n"
"buffer_load_dwordx4 v61, s[52:55], s95 offen offset:1024 lds\nQmaaa 200,56,0\nQmaaa 200,60,28\nQta 78,68,58880\ns_waitcnt lgkmcnt(4)\nQmaaa 228,64,0\ns_add_u32 m0, s96, 32768\ns_add_u32 s95, s73, 126976\n"
"Qmaaa 228,68,28\nbuffer_load_dwordx4 v61, s[52:55], s95 offen lds\nQmaaa 232,64,4\nQmaaa 232,68,32\nQmaaa 236,64,8\nQta 56,68,49920\ns_add_u32 s95, s73, 146432\nQmaaa 236,68,36\n"
"buffer_load_dwordx4 v61, s[52:55], s95 offen offset:1024 lds\nQmaaa 240,64,12\nQmaaa 240,68,40\nQta 58,68,50944\nQmaaa 244,64,16\nQmaaa 244,68,44\nQmaaa 248,64,20\nQta 60,68,58112\n"
"Qmaaa 248,68,48\nQmaaa 252,64,24\nQmaaa 252,68,52\nQta 62,68,59136\ns_waitcnt lgkmcnt(4)\nQmvaa 172,72,24\nQmvaa 172,76,52\nQmvaa 140,72,20\n"
"Qmvaa 140,76,48\nQmvaa 108,72,16\nQta 64,69,0\nQmvaa 108,76,44\nQmvaa 228,72,12\nQmvaa 228,76,40\nQta 66,69,512\nQmvaa 196,72,8\n"
"Qmvaa 196,76,36\nQmvaa 164,72,4\nQta 68,69,1024\nQmvaa 164,76,32\nv_add_u32 v75, 1, v75\nQmvaa 132,72,0\ns_waitcnt vmcnt(22)\nds_write_b32 v73, v75\n"
"Qmvaa 132,76,28\nQta 70,69,1536\ns_waitcnt lgkmcnt(5)\nQmvaa 204,56,0\ns_waitcnt vmcnt(24)\nQda 0,58,0\nQmvaa 204,60,28\nQmvaa 236,56,4\n"
"Qda 28,58,33792\nQmvaa 236,60,32\nQmvaa 96,56,8\nQta 72,69,8192\nQmvaa 96,60,36\nQda 4,58,1024\nQmvaa 128,56,12\nds_read_b32 v66, v74\n"
"Qmvaa 128,60,40\nQta 74,69,8704\nQmvaa 160,56,16\nQda 32,58,34816\nQmvaa 160,60,44\nQmvaa 192,56,20\ns_waitcnt lgkmcnt(2)\nv_cmp_gt_u32 vcc, v75, v66\n"
"Qmvaa 192,60,48\ns_cbranch_vccnz .Lso1805r1\n.Lro1805r1:\nQmvaa 224,56,24\nQta 76,69,9216\ns_add_u32 m0, s96, 40960\ns_add_u32 s95, s67, 86016\nQmvaa 224,60,52\n"
"buffer_load_dwordx4 v63, s[52:55], s95 offen lds\ns_waitcnt vmcnt(39)\ns_waitcnt lgkmcnt(8)\nQmaaa 172,64,80\nQda 8,58,2048\nQmaaa 172,68,108\nQta 78,69,9728\nQmaaa 176,64,84\n"
"Qda 36,58,35840\ns_add_u32 s95, s67, 105472\nQmaaa 176,68,112\nbuffer_load_dwordx4 v63, s[52:55], s95 offen offset:1024 lds\nQmaaa 180,64,88\nQta 56,69,16384\nQmaav 180,68,168\nQda 12,58,3072\n"
"Qmaaa 184,64,92\nQmaav 184,68,200\nQta 58,69,16896\ns_add_u32 m0, s96, 49152\ns_add_u32 s95, s67, 126976\nQmaaa 188,64,96\nbuffer_load_dwordx4 v63, s[52:55], s95 offen lds\nQmaav 188,68,232\n"
"Qda 40,58,36864\nQmaaa 192,64,100\nQta 60,69,17408\nQmaav 192,68,212\nQda 16,58,4096\ns_add_u32 s95, s67, 146432\nQmaaa 196,64,104\nbuffer_load_dwordx4 v63, s[52:55], s95 offen offset:1024 lds\n"
"Qmaav 196,68,244\nQta 62,69,17920\ns_waitcnt vmcnt(12)\ns_waitcnt lgkmcnt(8)\nQmaav 172,72,80\nQda 44,58,37888\nQmaav 172,76,88\ns_add_u32 m0, s96, 57344\n"
"s_add_u32 s95, s72, 86016\nQmaav 176,72,112\nbuffer_load_dwordx4 v63, s[52:55], s95 offen lds\nQmaav 176,76,120\nQda 20,58,5120\nQmaav 180,72,144\nQta 64,69,24576\nv_add_u32 v75, 1, v75\n"
"Qmaav 180,76,152\ns_waitcnt vmcnt(5)\nds_write_b32 v73, v75\ns_add_u32 s95, s72, 105472\nQmaav 184,72,176\nbuffer_load_dwordx4 v63, s[52:55], s95 offen offset:1024 lds\nQmaav 184,76,184\nQta 66,69,25600\n"
"Qmaav 188,72,208\nQda 48,58,38912\nQmaav 188,76,216\nQmaav 192,72,240\nQta 68,68,0\nQmaav 192,76,248\nQda 24,58,6144\nQmaav 196,72,84\n"
"Qmaav 196,76,116\nds_read_b32 v66, v74\ns_waitcnt lgkmcnt(9)\nQmaaa 172,56,0\nQta 70,68,1024\nQmaaa 172,60,28\nQda 52,58,39936\nQmaaa 176,56,4\n"
"Qmaaa 176,60,32\ns_waitcnt lgkmcnt(2)\nv_cmp_gt_u32 vcc, v75, v66\ns_waitcnt lgkmcnt(11)\nQmaaa 180,56,8\ns_cbranch_vccnz .Lso1840r1\n.Lro1840r1:\nQmaaa 180,60,36\n"
"Qta 72,68,8192\nQmaaa 184,56,12\nds_write_b128 v58, a[172:175] offset:0\nQmaaa 184,60,40\ns_waitcnt lgkmcnt(2)\nQmaaa 188,56,16\nQta 74,68,9216\nQmaaa 188,60,44\n"
"ds_write_b128 v58, a[176:179] offset:1024\nQmaaa 192,56,20\nQmaaa 192,60,48\nQta 76,68,16384\nQmaaa 196,56,24\nds_write_b128 v58, a[180:183] offset:2048\nQmaaa 196,60,52\ns_waitcnt lgkmcnt(7)\n"
"Qmaaa 116,64,80\nQta 78,68,17408\nQmaaa 116,68,108\nds_write_b128 v58, a[184:187] offset:3072\nQmaaa 120,64,84\nQmaaa 120,68,112\nQmaaa 124,64,88\nQta 56,68,24576\n"
"Qmaav 124,68,168\nds_write_b128 v58, a[188:191] offset:4096\nQmaaa 128,64,92\nQmaav 128,68,200\nQta 58,68,25600\nQmaaa 132,64,96\nds_write_b128 v58, a[192:195] offset:5120\nQmaav 132,68,232\n"
"Qmaaa 136,64,100\nQta 60,68,32768\nQmaav 136,68,212\nds_write_b128 v58, a[196:199] offset:6144\nQmaaa 140,64,104\nQmaav 140,68,244\nQta 62,68,33792\ns_waitcnt lgkmcnt(8)\n"
"Qmaav 116,72,80\nv_add_u32 v75, 1, v75\nQmaav 116,76,88\nds_write_b32 v73, v75\nQmaav 120,72,112\nQmaav 120,76,120\nQmaav 124,72,144\nQta 64,69,256\n"
"Qmaav 124,76,152\nQmaav 128,72,176\nQmaav 128,76,184\nQta 66,69,768\nQmaav 132,72,208\nQmaav 132,76,216\nds_read_b32 v66, v74\nQmaav 136,72,240\n"
"Qta 68,69,1280\nQmaav 136,76,248\nQmaav 140,72,84\nQmaav 140,76,116\ns_waitcnt lgkmcnt(1)\nv_cmp_gt_u32 vcc, v75, v66\ns_waitcnt lgkmcnt(5)\nQmaaa 116,56,0\n"
"s_cbranch_vccnz .Lso1878r1\n.Lro1878r1:\nQmaaa 116,60,28\nQta 70,69,1792\nQmaaa 120,56,4\nQmaaa 120,60,32\nQdv 148,59,2048\nQmaaa 124,56,8\n"
"Qta 72,69,8448\nQmaaa 124,60,36\ns_nop 0\nds_write_b128 v58, a[116:119] offset:33792\nQmaaa 128,56,12\nQdv 180,59,3072\nQmaaa 128,60,40\nQta 74,69,8960\n"
"Qmaaa 132,56,16\nds_write_b128 v58, a[120:123] offset:34816\nQmaaa 132,60,44\nQdv 104,59,5120\nQmaaa 136,56,20\nQta 76,69,9472\nQmaaa 136,60,48\nds_write_b128 v58, a[124:127] offset:35840\n"
"Qmaaa 140,56,24\nQdv 136,59,0\ns_waitcnt lgkmcnt(9)\nQa 156,156,148\nQmaaa 140,60,52\nQta 78,69,9984\nQa 157,157,149\ns_waitcnt lgkmcnt(10)\n"
"Qmvaa 92,64,80\nds_write_b128 v58, a[128:131] offset:36864\nQmvaa 92,68,108\n.Lpg24:\ns_cbranch_execz .Ltr8\nQa 158,158,150\nQa 159,159,151\nQmvaa 124,64,84\n"
"s_waitcnt lgkmcnt(8)\nQa 188,188,180\nQa 189,189,181\nQmvaa 124,68,112\nds_write_b128 v58, a[132:135] offset:37888\nQmvaa 156,64,88\nQta 56,69,16640\nQa 190,190,182\n"
"Qa 191,191,183\nQmvav 156,68,168\nQdv 148,59,4096\ns_waitcnt lgkmcnt(8)\nQa 252,252,104\nQmvaa 188,64,92\nds_write_b128 v58, a[136:139] offset:38912\nQmvav 188,68,200\n"
"Qta 58,69,17152\nQa 253,253,105\nQmvaa 220,64,96\nQa 254,254,106\nQa 255,255,107\nQmvav 220,68,232\nQdv 180,59,6144\ns_waitcnt lgkmcnt(8)\n"
"Qa 92,92,136\nQmvaa 252,64,100\nQta 60,69,17664\nQa 93,93,137\nQmvav 252,68,212\nds_write_b128 v58, a[140:143] offset:39936\nQmvaa 100,64,104\nQa 94,94,138\n"
"Qa 95,95,139\nQmvav 100,68,244\nQta 62,69,18176\ns_waitcnt lgkmcnt(6)\nQa 220,220,148\ns_waitcnt lgkmcnt(6)\nQmvav 92,72,80\nQdv 104,59,1024\n"
"v_add_u32 v75, 1, v75\nQmvav 92,76,88\nds_write_b32 v73, v75\nQa 221,221,149\nQmvav 124,72,112\nQa 222,222,150\nQa 223,223,151\nQmvav 124,76,120\n"
"s_waitcnt lgkmcnt(5)\nQa 100,100,180\nQa 101,101,181\nQmvav 156,72,144\nQta 64,69,24832\nQa 102,102,182\nQmvav 156,76,152\nQa 103,103,183\n"
"Qmvav 188,72,176\nQmvav 188,76,184\nQta 66,69,25856\ns_waitcnt lgkmcnt(3)\nQa 124,124,104\nQmvav 220,72,208\nQa 125,125,105\nQa 126,126,106\n"
"Qmvav 220,76,216\nds_read_b32 v66, v74\nQa 127,127,107\nQmvav 252,72,240\nQta 68,68,256\nQmvav 252,76,248\nQmvav 100,72,84\nQmvav 100,76,116\n"
"s_waitcnt lgkmcnt(1)\nv_cmp_gt_u32 vcc, v75, v66\ns_waitcnt lgkmcnt(6)\nQmvaa 92,56,0\ns_cbranch_vccnz .Lso1920r1\n.Lro1920r1:\ns_add_u32 s68, s64, 0\nQmvaa 92,60,28\n"
"Qta 70,68,1280\ns_add_u32 m0, s96, 65536\ns_add_u32 s95, s72, 126976\nQmvaa 124,56,4\nbuffer_load_dwordx4 v63, s[52:55], s95 offen lds\nQmvaa 124,60,32\nQdv 136,59,33792\nQmvaa 156,56,8\n"
"Qta 72,68,8448\nQk 92,92,93\nQmvaa 156,60,36\ns_add_u32 s95, s72, 146432\nQk 93,94,95\nQmvaa 188,56,12\nbuffer_load_dwordx4 v63, s[52:55], s95 offen offset:1024 lds\nQmvaa 188,60,40\n"
"Qta 74,68,9472\nQk 94,124,125\nQmvaa 220,56,16\nQdv 148,59,34816\nQk 95,126,127\nQmvaa 220,60,44\nbuffer_store_dwordx4 v[92:95], v79, s[24:27], s68 offen offset:0 sc1\nQk 156,156,157\n"
"Qmvaa 252,56,20\nQta 76,68,16640\ns_add_u32 m0, s96, 73728\ns_add_u32 s95, s73, 86016\nQmvaa 252,60,48\nbuffer_load_dwordx4 v63, s[52:55], s95 offen lds\nv_add_u32 v75, 1, v75\nQmvaa 100,56,24\n"
"ds_write_b32 v73, v75\ns_waitcnt lgkmcnt(5)\nQa 0,0,136\nQmvaa 100,60,52\nQta 78,68,17664\nQa 1,1,137\ns_waitcnt lgkmcnt(6)\nQmaaa 144,64,80\n"
"Qdv 180,59,35840\ns_add_u32 s95, s73, 105472\nQmaaa 144,68,108\nbuffer_load_dwordx4 v63, s[52:55], s95 offen offset:1024 lds\nQmaaa 148,64,84\nQa 2,2,138\nQa 3,3,139\nQmaaa 148,68,112\n"
"Qdv 104,59,36864\ns_waitcnt lgkmcnt(5)\nQa 4,4,148\nQmaaa 152,64,88\nQta 56,68,24832\ns_add_u32 m0, s96, 81920\ns_add_u32 s95, s73, 126976\nQmaav 152,68,168\n"
"buffer_load_dwordx4 v63, s[52:55], s95 offen lds\nQmaaa 156,64,92\nds_read_b32 v66, v74\nQa 5,5,149\nQa 6,6,150\nQa 7,7,151\nQmaav 156,68,200\nQta 58,68,25856\n"
"s_waitcnt lgkmcnt(4)\nQa 8,8,180\nQmaaa 160,64,96\nQdv 136,59,37888\ns_add_u32 s95, s73, 146432\nQmaav 160,68,232\nbuffer_load_dwordx4 v63, s[52:55], s95 offen offset:1024 lds\nQmaaa 164,64,100\n"
"s_waitcnt lgkmcnt(2)\nv_cmp_gt_u32 vcc, v75, v66\nQmaav 164,68,212\ns_cbranch_vccnz .Lso1945r1\n.Lro1945r1:\nQa 9,9,181\nQa 10,10,182\nQa 11,11,183\n"
"Qmaaa 168,64,104\nQta 60,68,33024\ns_waitcnt lgkmcnt(5)\nQa 12,12,104\nQmaav 168,68,244\nQdv 148,59,38912\nQa 13,13,105\ns_waitcnt lgkmcnt(6)\n"
"Qmaav 144,72,80\nQa 14,14,106\nQa 15,15,107\nQmaav 144,76,88\nQta 62,68,34048\ns_waitcnt lgkmcnt(3)\nQa 16,16,136\nQmaav 148,72,112\n"
"Qdv 180,59,39936\nQa 17,17,137\nQmaav 148,76,120\nQa 18,18,138\nQa 19,19,139\nQmaav 152,72,144\nQta 64,69,25088\nv_bfe_u32 v61, v60, 6, 2\n"
"Qmaav 152,76,152\ns_waitcnt lgkmcnt(3)\nQa 20,20,148\nQa 21,21,149\nQmaav 156,72,176\nQa 22,22,150\nQa 23,23,151\nQmaav 156,76,184\n"
"Qta 66,69,26112\nv_lshrrev_b32 v62, 8, v60\nQmaav 160,72,208\nv_xor_b32 v61, v61, v62\nv_bfe_u32 v62, v60, 5, 1\nQmaav 160,76,216\nv_lshl_or_b32 v61, v61, 1, v62\nv_and_b32 v62, 16, v60\n"
"Qmaav 164,72,240\nQta 68,68,512\ns_waitcnt lgkmcnt(3)\nQa 24,24,180\nQmaav 164,76,248\nQa 25,25,181\nQa 26,26,182\nQmaav 168,72,84\n"
"Qa 27,27,183\nv_mov_b32 v63, 0x3f80\nQmaav 168,76,116\nQta 70,68,1536\nv_lshlrev_b32 v63, v62, v63\ns_waitcnt lgkmcnt(5)\nQmaaa 144,56,0\nv_min_u32 v62, 1, v61\n"
"v_add_u32 v62, -1, v62\nQmaaa 144,60,28\nv_and_b32 v62, v62, v63\nv_xor_b32 v61, 1, v61\nQmaaa 148,56,4\nv_min_u32 v61, 1, v61\nv_add_u32 v61, -1, v61\nQmaaa 148,60,32\n"
"v_and_b32 v61, v61, v63\nv_accvgpr_write_b32 a174, v62\nQmaaa 152,56,8\nQta 72,68,8704\nv_accvgpr_write_b32 a175, v61\nQmaaa 152,60,36\nds_write_b128 v58, a[144:147] offset:0\nQmaaa 156,56,12\n"
"v_accvgpr_write_b32 a172, 0\nv_accvgpr_write_b32 a173, 0\nQmaaa 156,60,40\nQta 74,68,9728\nv_accvgpr_write_b32 a176, 0\nQmaaa 160,56,16\nds_write_b128 v58, a[148:151] offset:1024\nQmaaa 160,60,44\n"
"v_accvgpr_write_b32 a177, 0\nQk 157,158,159\nQmaaa 164,56,20\nQta 76,68,16896\nQk 158,188,189\nQmaaa 164,60,48\nds_write_b128 v58, a[152:155] offset:2048\nQmaaa 168,56,24\n"
"Qk 159,190,191\nQk 220,220,221\nQmaaa 168,60,52\nQta 78,68,17920\nbuffer_store_dwordx4 v[156:159], v79, s[24:27], s68 offen offset:1024 sc1\ns_waitcnt lgkmcnt(7)\nQmvaa 0,64,80\nds_write_b128 v58, a[156:159] offset:3072\n"
"Qmvaa 0,68,108\nQk 221,222,223\nQk 222,252,253\nQmvaa 4,64,84\nQk 223,254,255\nQk 100,100,101\nQmvaa 4,68,112\nds_write_b128 v58, a[160:163] offset:4096\n"
"Qmvaa 8,64,88\nQta 56,68,25088\nbuffer_store_dwordx4 v[220:223], v79, s[24:27], s68 offen offset:2048 sc1\nQmvav 8,68,168\nQk 101,102,103\nQmvaa 12,64,92\nds_write_b128 v58, a[164:167] offset:5120\nQmvav 12,68,200\n"
"Qta 58,68,26112\nQmvaa 16,64,96\nQmvav 16,68,232\nds_write_b128 v58, a[168:171] offset:6144\nQmvaa 20,64,100\nQta 60,68,33280\nQmvav 20,68,212\nQmvaa 24,64,104\n"
"Qmvav 24,68,244\nQta 62,68,34304\ns_waitcnt lgkmcnt(8)\nQmvav 0,72,80\nv_add_u32 v75, 1, v75\nQmvav 0,76,88\ns_waitcnt vmcnt(11)\nds_write_b32 v73, v75\n"
"Qmvav 4,72,112\nQmvav 4,76,120\nQmvav 8,72,144\nQta 64,69,25344\nQmvav 8,76,152\nQmvav 12,72,176\nQmvav 12,76,184\nQta 66,69,26368\n"
"Qmvav 16,72,208\nQmvav 16,76,216\nds_read_b32 v66, v74\nQmvav 20,72,240\nQta 68,68,768\nQmvav 20,76,248\nQmvav 24,72,84\nQmvav 24,76,116\n"
"s_waitcnt lgkmcnt(1)\nv_cmp_gt_u32 vcc, v75, v66\ns_waitcnt lgkmcnt(5)\nQmvaa 0,56,0\ns_cbranch_vccnz .Lso2004r1\n.Lro2004r1:\nQmvaa 0,60,28\nQta 70,68,1792\n"
"Qmvaa 4,56,4\nQmvaa 4,60,32\nQdv 104,59,2048\nQmvaa 8,56,8\nQta 72,68,8960\ns_nop 1\nQk 102,0,1\nQmvaa 8,60,36\n"
"Qk 103,2,3\nQmvaa 12,56,12\nQdv 136,59,3072\nbuffer_store_dwordx4 v[100:103], v79, s[24:27], s68 offen offset:3072 sc1\nQmvaa 12,60,40\nQta 74,68,9984\nQk 4,4,5\nQmvaa 16,56,16\n"
"Qk 5,6,7\nQk 6,8,9\nQmvaa 16,60,44\nQdv 148,59,5120\nQk 7,10,11\nQmvaa 20,56,20\nQta 76,68,17152\ns_add_u32 s68, s64, 4096\n"
"Qmvaa 20,60,48\ns_waitcnt lgkmcnt(5)\nQa 36,36,104\nQa 37,37,105\nQmvaa 24,56,24\nQdv 180,59,0\nQa 38,38,106\nQmvaa 24,60,52\n"
"Qta 78,68,18176\nQa 39,39,107\ns_waitcnt lgkmcnt(11)\nQmvaa 28,64,80\ns_waitcnt lgkmcnt(5)\nQa 40,40,136\nQa 41,41,137\nQmvaa 28,68,108\n"
"Qa 42,42,138\nQa 43,43,139\nQmvaa 32,64,84\nbuffer_store_dwordx4 v[4:7], v79, s[24:27], s68 offen offset:0 sc1\nQk 12,12,13\nQmvaa 32,68,112\ns_waitcnt lgkmcnt(3)\nQa 48,48,148\n"
"Qa 49,49,149\nQmvaa 36,64,88\nQta 56,68,25344\nQa 50,50,150\nQmvav 36,68,168\nQdv 104,59,4096\nQa 51,51,151\nQmvaa 40,64,92\n"
"s_waitcnt lgkmcnt(3)\nQa 28,28,180\nQa 29,29,181\nQmvav 40,68,200\nQta 58,68,26368\nQa 30,30,182\nQmvaa 44,64,96\nQa 31,31,183\n"
"Qk 13,14,15\nQmvav 44,68,232\nQdv 136,59,6144\nQk 14,16,17\nQmvaa 48,64,100\nQta 60,68,33536\nQk 15,18,19\nQmvav 48,68,212\n"
"buffer_store_dwordx4 v[12:15], v79, s[24:27], s68 offen offset:1024 sc1\nQk 20,20,21\nQmvaa 52,64,104\nQk 21,22,23\nQk 22,24,25\nQmvav 52,68,244\nQta 62,68,34560\nv_add_u32 v75, 1, v75\n"
"s_waitcnt lgkmcnt(6)\nQmvav 28,72,80\ns_waitcnt vmcnt(5)\nds_write_b32 v73, v75\nQmvav 28,76,88\nQdv 148,59,1024\ns_waitcnt lgkmcnt(6)\nQa 44,44,104\n"
"Qmvav 32,72,112\nQa 45,45,105\nQa 46,46,106\nQmvav 32,76,120\nQa 47,47,107\ns_waitcnt lgkmcnt(4)\nQa 52,52,136\nQmvav 36,72,144\n"
"Qta 64,68,40960\nQa 53,53,137\nQmvav 36,76,152\nQa 54,54,138\nQa 55,55,139\nQmvav 40,72,176\nQk 23,26,27\nQmvav 40,76,184\n"
"Qta 66,68,41984\ns_waitcnt lgkmcnt(2)\nQa 32,32,148\nQmvav 44,72,208\nds_read_b32 v66, v74\nQa 33,33,149\nQmvav 44,76,216\nQa 34,34,150\n"
"Qa 35,35,151\nQmvav 48,72,240\nQta 68,68,49152\nbuffer_store_dwordx4 v[20:23], v79, s[24:27], s68 offen offset:2048 sc1\nQmvav 48,76,248\nQmvav 52,72,84\ns_waitcnt lgkmcnt(1)\nv_cmp_gt_u32 vcc, v75, v66\n"
"Qmvav 52,76,116\ns_cbranch_vccnz .Lso2045r1\n.Lro2045r1:\ns_waitcnt lgkmcnt(6)\nQmvaa 28,56,0\nQta 70,68,50176\nQmvaa 28,60,28\nQmvaa 32,56,4\n"
"Qmvaa 32,60,32\nQmvaa 36,56,8\nQta 72,68,57344\ns_nop 3\nQk 28,28,29\nQmvaa 36,60,36\nQk 29,30,31\nQmvaa 40,56,12\n"
"Qk 30,32,33\nQk 31,34,35\nQmvaa 40,60,40\nQta 74,68,58368\nbuffer_store_dwordx4 v[28:31], v79, s[24:27], s68 offen offset:3072 sc1\nQmvaa 44,56,16\nQk 36,36,37\nQk 37,38,39\n"
"Qmvaa 44,60,44\ns_add_u32 s68, s64, 16384\nQmvaa 48,56,20\nQta 76,69,0\nQk 38,40,41\nQmvaa 48,60,48\nQk 39,42,43\nQmvaa 52,56,24\n"
"buffer_store_dwordx4 v[36:39], v79, s[24:27], s68 offen offset:0 sc1\nQk 44,44,45\nQmvaa 52,60,52\nQta 78,69,1024\nQk 45,46,47\ns_waitcnt lgkmcnt(4)\nQmaaa 200,64,80\nQk 46,48,49\n"
"Qk 47,50,51\nQmaaa 200,68,108\nbuffer_store_dwordx4 v[44:47], v79, s[24:27], s68 offen offset:1024 sc1\nQmaaa 204,64,84\nQk 52,52,53\nQk 53,54,55\nQmaaa 204,68,112\nv_mov_b32 v54, 0\n"
"v_mov_b32 v55, 0\nQmaaa 208,64,88\nQta 56,69,8192\nbuffer_store_dwordx4 v[52:55], v79, s[24:27], s68 offen offset:2048 sc1\nQmaav 208,68,168\nQmaaa 212,64,92\nQmaav 212,68,200\nQta 58,69,9216\n"
"Qmaaa 216,64,96\nQmaav 216,68,232\nQmaaa 220,64,100\nQta 60,69,16384\nQmaav 220,68,212\nQmaaa 224,64,104\nQmaav 224,68,244\nQta 62,69,17408\n"
"s_waitcnt lgkmcnt(4)\nQmaav 200,72,80\nQmaav 200,76,88\nQmaav 204,72,112\nQmaav 204,76,120\nQmaav 208,72,144\nQta 64,68,41216\nQmaav 208,76,152\n"
"Qmaav 212,72,176\nQmaav 212,76,184\nQta 66,68,42240\nQmaav 216,72,208\nQmaav 216,76,216\nQmaav 220,72,240\nQta 68,68,49408\nQmaav 220,76,248\n"
"Qmaav 224,72,84\nQmaav 224,76,116\nQta 70,68,50432\ns_waitcnt lgkmcnt(4)\nQmaaa 200,56,0\nQmaaa 200,60,28\nQmaaa 204,56,4\nQmaaa 204,60,32\n"
"Qmaaa 208,56,8\nQta 72,68,57600\nQmaaa 208,60,36\ns_nop 2\nds_write_b128 v58, a[200:203] offset:33792\nQmaaa 212,56,12\nQmaaa 212,60,40\nQta 74,68,58624\n"
"Qmaaa 216,56,16\nds_write_b128 v58, a[204:207] offset:34816\nQmaaa 216,60,44\nQmaaa 220,56,20\nQta 76,69,256\nQmaaa 220,60,48\nds_write_b128 v58, a[208:211] offset:35840\nQmaaa 224,56,24\n"
"Qmaaa 224,60,52\nQta 78,69,1280\ns_waitcnt lgkmcnt(7)\nQmaaa 228,64,80\nds_write_b128 v58, a[212:215] offset:36864\nQmaaa 228,68,108\nQmaaa 232,64,84\ns_waitcnt vmcnt(0)\n"
"buffer_store_dword v67, v77, s[24:27], 0 offen sc1\nQmaaa 232,68,112\nds_write_b128 v58, a[216:219] offset:37888\nQmaaa 236,64,88\nQta 56,69,8448\nQmaav 236,68,168\nQmaaa 240,64,92\nds_write_b128 v58, a[220:223] offset:38912\n"
"Qmaav 240,68,200\nQta 58,69,9472\nQmaaa 244,64,96\nQmaav 244,68,232\nds_write_b128 v58, a[224:227] offset:39936\nQmaaa 248,64,100\nQta 60,69,16640\nQmaav 248,68,212\n"
"Qmaaa 252,64,104\nQmaav 252,68,244\nQta 62,69,17664\ns_waitcnt lgkmcnt(8)\nQmaav 228,72,80\nv_add_u32 v75, 1, v75\nQmaav 228,76,88\nds_write_b32 v73, v75\n"
"Qmaav 232,72,112\nQmaav 232,76,120\nQmaav 236,72,144\nQta 64,68,41472\nQmaav 236,76,152\nQmaav 240,72,176\nQmaav 240,76,184\nQta 66,68,42496\n"
"Qmaav 244,72,208\nQmaav 244,76,216\nds_read_b32 v66, v74\nQmaav 248,72,240\nQta 68,68,49664\nQmaav 248,76,248\nQmaav 252,72,84\nbuffer_load_dword v78, v76, s[24:27], 0 offen sc1\n"
"Qmaav 252,76,116\ns_waitcnt lgkmcnt(1)\nv_cmp_gt_u32 vcc, v75, v66\ns_waitcnt lgkmcnt(5)\nQmaaa 228,56,0\ns_cbranch_vccnz .Lso2130r1\n.Lro2130r1:\nQmaaa 228,60,28\n"
"Qta 70,68,50688\n.Lpg25:\ns_cbranch_execz .Ltr8\nQmaaa 232,56,4\nQmaaa 232,60,32\nQdv 180,59,35840\nQmaaa 236,56,8\nQta 72,68,57856\n"
"Qmaaa 236,60,36\ns_nop 0\nds_write_b128 v58, a[228:231] offset:0\nQmaaa 240,56,12\nQdv 104,59,36864\nQmaaa 240,60,40\nQta 74,68,58880\nQmaaa 244,56,16\n"
"ds_write_b128 v58, a[232:235] offset:1024\nQmaaa 244,60,44\nQdv 136,59,38912\nQmaaa 248,56,20\nQta 76,69,512\nQmaaa 248,60,48\nds_write_b128 v58, a[236:239] offset:2048\nQmaaa 252,56,24\n"
"Qdv 148,59,33792\ns_waitcnt lgkmcnt(9)\nQa 196,196,180\nQmaaa 252,60,52\nQta 78,69,1536\nQa 197,197,181\ns_waitcnt lgkmcnt(10)\nQmvaa 132,64,80\n"
"ds_write_b128 v58, a[240:243] offset:3072\nQmvaa 132,68,108\nQa 198,198,182\nQa 199,199,183\nQmvaa 164,64,84\ns_waitcnt lgkmcnt(8)\nQa 228,228,104\nQa 229,229,105\n"
"Qmvaa 164,68,112\nds_write_b128 v58, a[244:247] offset:4096\nQmvaa 196,64,88\nQta 56,69,8704\nQa 230,230,106\nQa 231,231,107\nQmvav 196,68,168\nQdv 180,59,37888\n"
"s_waitcnt lgkmcnt(8)\nQa 140,140,136\nQmvaa 228,64,92\nds_write_b128 v58, a[248:251] offset:5120\nQmvav 228,68,200\nQta 58,69,9728\nQa 141,141,137\nQmvaa 108,64,96\n"
"Qa 142,142,138\nQa 143,143,139\nQmvav 108,68,232\nQdv 104,59,39936\ns_waitcnt lgkmcnt(8)\nQa 132,132,148\nQmvaa 140,64,100\nQta 60,69,16896\n"
"Qa 133,133,149\nQmvav 140,68,212\nds_write_b128 v58, a[252:255] offset:6144\nQmvaa 172,64,104\nQa 134,134,150\nQa 135,135,151\nQmvav 172,68,244\nQta 62,69,17920\n"
"s_waitcnt lgkmcnt(6)\nQa 108,108,180\ns_waitcnt lgkmcnt(6)\nQmvav 132,72,80\nQdv 136,59,34816\nv_add_u32 v75, 1, v75\nQmvav 132,76,88\nds_write_b32 v73, v75\n"
"Qa 109,109,181\nQmvav 164,72,112\nQa 110,110,182\nQa 111,111,183\nQmvav 164,76,120\ns_waitcnt lgkmcnt(5)\nQa 172,172,104\nQa 173,173,105\n"
"Qmvav 196,72,144\nQta 64,68,41728\nQa 174,174,106\nQmvav 196,76,152\nQa 175,175,107\nQmvav 228,72,176\nQmvav 228,76,184\nQta 66,68,42752\n"
"s_waitcnt lgkmcnt(3)\nQa 164,164,136\nQmvav 108,72,208\nQa 165,165,137\nQa 166,166,138\nQmvav 108,76,216\nds_read_b32 v66, v74\nQa 167,167,139\n"
"Qmvav 140,72,240\nQta 68,68,49920\nQmvav 140,76,248\nQmvav 172,72,84\nQmvav 172,76,116\ns_waitcnt lgkmcnt(1)\nv_cmp_gt_u32 vcc, v75, v66\ns_waitcnt lgkmcnt(6)\n"
"Qmvaa 132,56,0\ns_cbranch_vccnz .Lso2172r1\n.Lro2172r1:\nQmvaa 132,60,28\ns_waitcnt vmcnt(0)\nv_sub_u32 v66, v78, v65\nv_cmp_gt_u32 vcc, 1, v66\ns_and_b64 vcc, vcc, s[28:29]\n"
"s_cbranch_scc1 .Lx1_w\n.Lx1_r:\nQmvaa 164,56,4\ns_add_u32 s69, s98, 0\nbuffer_load_dwordx4 a[180:183], v79, s[24:27], s69 offen offset:0 sc1\nQmvaa 164,60,32\nQta 70,68,50944\nQmvaa 196,56,8\n"
"buffer_load_dwordx4 a[184:187], v79, s[24:27], s69 offen offset:1024 sc1\nQmvaa 196,60,36\nQdv 148,59,2048\nQmvaa 228,56,12\nbuffer_load_dwordx4 a[188:191], v79, s[24:27], s69 offen offset:2048 sc1\nQmvaa 228,60,40\nQta 72,68,58112\nQmvaa 108,56,16\n"
"buffer_load_dwordx4 a[192:195], v79, s[24:27], s69 offen offset:3072 sc1\nQmvaa 108,60,44\nQdv 180,59,4096\nQmvaa 140,56,20\ns_add_u32 s69, s98, 4096\nbuffer_load_dwordx4 a[196:199], v79, s[24:27], s69 offen offset:0 sc1\nQmvaa 140,60,48\nQta 74,68,59136\n"
"s_waitcnt lgkmcnt(3)\nQa 96,96,148\nQmvaa 172,56,24\nbuffer_load_dwordx4 a[116:119], v79, s[24:27], s69 offen offset:1024 sc1\nQmvaa 172,60,52\nQdv 104,59,0\nQa 97,97,149\ns_waitcnt lgkmcnt(4)\n"
"Qmvaa 204,64,80\nbuffer_load_dwordx4 a[120:123], v79, s[24:27], s69 offen offset:2048 sc1\nQmvaa 204,68,108\nQta 76,69,768\nQa 98,98,150\nQmvaa 236,64,84\nQdv 136,59,1024\nQa 99,99,151\n"
"Qmvaa 236,68,112\ns_waitcnt lgkmcnt(4)\nQa 160,160,180\nQa 161,161,181\nQmvaa 96,64,88\nQta 78,69,1792\nQa 162,162,182\nQmvav 96,68,168\n"
"Qdv 148,59,3072\nQa 163,163,183\nQmvaa 128,64,92\ns_waitcnt lgkmcnt(4)\nQa 204,204,104\nQa 205,205,105\nQmvav 128,68,200\nQta 56,69,8960\n"
"Qa 206,206,106\nQmvaa 160,64,96\nQdv 180,59,5120\nQa 207,207,107\nQmvav 160,68,232\nQmvaa 192,64,100\nQta 58,69,9984\ns_waitcnt lgkmcnt(5)\n"
"Qa 236,236,136\nQmvav 192,68,212\nQa 237,237,137\nQa 238,238,138\nQmvaa 224,64,104\nQa 239,239,139\nQmvav 224,68,244\nQta 60,69,17152\n"
"s_waitcnt lgkmcnt(4)\nQa 128,128,148\ns_waitcnt lgkmcnt(4)\nQmvav 204,72,80\nQa 129,129,149\nQa 130,130,150\nQmvav 204,76,88\nQa 131,131,151\n"
"Qmvav 236,72,112\nQta 62,69,18176\ns_waitcnt lgkmcnt(3)\nQa 192,192,180\nQmvav 236,76,120\nQa 193,193,181\nQa 194,194,182\nQmvav 96,72,144\n"
"Qa 195,195,183\nQmvav 96,76,152\nQmvav 128,72,176\nQmvav 128,76,184\nQmvav 160,72,208\nQmvav 160,76,216\nQdv 104,59,6144\nQmvav 192,72,240\n"
"Qmvav 192,76,248\nv_add_u32 v75, 1, v75\nQmvav 224,72,84\nds_write_b32 v73, v75\nQmvav 224,76,116\ns_waitcnt lgkmcnt(2)\nQmvaa 204,56,0\nQmvaa 204,60,28\n"
"Qmvaa 236,56,4\nQmvaa 236,60,32\ns_waitcnt lgkmcnt(1)\ns_nop 1\nQa 224,224,104\nQa 225,225,105\nQmvaa 96,56,8\nQa 226,226,106\n"
"Qa 227,227,107\nQmvaa 96,60,36\nQmvaa 128,56,12\nds_read_b32 v66, v74\nQmvaa 128,60,40\nQmvaa 160,56,16\nQmvaa 160,60,44\nQmvaa 192,56,20\n"
"s_waitcnt lgkmcnt(0)\nv_cmp_gt_u32 vcc, v75, v66\nQmvaa 192,60,48\ns_cbranch_vccnz .Lso2225r1\n.Lro2225r1:\nQmvaa 224,56,24\nQmvaa 224,60,52\ns_waitcnt vmcnt(6)\n"
"Qmvaa 132,174,180\nQmvaa 164,172,180\ns_waitcnt vmcnt(5)\nQmvaa 196,174,184\nQmvaa 228,172,184\ns_nop 3\nQk 132,132,133\nQk 133,134,135\n"
"ds_write_b64 v56, v[132:133] offset:0\ns_waitcnt vmcnt(4)\nQmvaa 108,174,188\nQk 164,164,165\nQk 165,166,167\nds_write_b64 v56, v[164:165] offset:3328\nQmvaa 140,172,188\nQk 196,196,197\n"
"Qk 197,198,199\nds_write_b64 v56, v[196:197] offset:6656\ns_waitcnt vmcnt(3)\nQmvaa 172,174,192\nQk 228,228,229\nQk 229,230,231\nds_write_b64 v56, v[228:229] offset:9984\nQmvaa 204,172,192\n"
"Qk 108,108,109\nQk 109,110,111\nds_write_b64 v56, v[108:109] offset:13312\ns_waitcnt vmcnt(2)\nQmvaa 236,174,196\nQk 140,140,141\nQk 141,142,143\nds_write_b64 v56, v[140:141] offset:16640\n"
"Qmvaa 96,172,196\nQk 172,172,173\nQk 173,174,175\nds_write_b64 v56, v[172:173] offset:19968\ns_waitcnt vmcnt(1)\nQmvaa 128,174,116\nQk 204,204,205\nQk 205,206,207\n"
"ds_write_b64 v56, v[204:205] offset:32\nQmvaa 160,172,116\nQk 236,236,237\nQk 237,238,239\nds_write_b64 v56, v[236:237] offset:3360\ns_waitcnt vmcnt(0)\nQmvaa 192,174,120\nQk 96,96,97\n"
"Qk 97,98,99\nds_write_b64 v56, v[96:97] offset:6688\nQmvaa 224,172,120\nQk 128,128,129\nQk 129,130,131\nds_write_b64 v56, v[128:129] offset:10016\nQk 160,160,161\nQk 161,162,163\n"
"ds_write_b64 v56, v[160:161] offset:13344\nQk 192,192,193\nQk 193,194,195\nds_write_b64 v56, v[192:193] offset:16672\nQk 224,224,225\nQk 225,226,227\nds_write_b64 v56, v[224:225] offset:20000\ns_waitcnt lgkmcnt(0)\n"
"s_barrier\nQda 0,57,0\nQda 4,57,3328\nQda 8,57,6656\nQda 12,57,9984\nQda 16,57,13312\nQda 20,57,16640\nQda 24,57,19968\n"
"s_mov_b32 s68, 0\ns_waitcnt lgkmcnt(6)\nbuffer_store_dwordx4 a[0:3], v64, s[40:43], s68 offen offset:0 sc1\ns_mov_b32 s68, 40960\ns_waitcnt lgkmcnt(5)\nbuffer_store_dwordx4 a[4:7], v64, s[40:43], s68 offen offset:0 sc1\ns_mov_b32 s68, 81920\ns_waitcnt lgkmcnt(4)\n"
"buffer_store_dwordx4 a[8:11], v64, s[40:43], s68 offen offset:0 sc1\ns_mov_b32 s68, 122880\ns_waitcnt lgkmcnt(3)\nbuffer_store_dwordx4 a[12:15], v64, s[40:43], s68 offen offset:0 sc1\ns_mov_b32 s68, 163840\ns_waitcnt lgkmcnt(2)\nbuffer_store_dwordx4 a[16:19], v64, s[40:43], s68 offen offset:0 sc1\ns_mov_b32 s68, 204800\n"
"s_waitcnt lgkmcnt(1)\nbuffer_store_dwordx4 a[20:23], v64, s[40:43], s68 offen offset:0 sc1\ns_mov_b32 s68, 245760\ns_waitcnt lgkmcnt(0)\nbuffer_store_dwordx4 a[24:27], v64, s[40:43], s68 offen offset:0 sc1\n.Lfin:\n.Lpg26:\ns_cbranch_execz .Ltr8\n"
"s_waitcnt vmcnt(0) lgkmcnt(0)\ns_branch .Lexit\n.Lend:\n.Lfb:\ns_waitcnt vmcnt(0) lgkmcnt(0)\nv_lshrrev_b32 v52, 4, v60\nv_and_b32 v53, 15, v52\nv_lshrrev_b32 v52, 4, v52\n"
"v_lshlrev_b32 v66, 8, v53\nv_lshl_add_u32 v66, v52, 4, v66\nv_add_u32 v66, 1024, v66\nv_lshlrev_b32 v67, 10, v52\nv_lshl_add_u32 v67, v53, 1, v67\nv_add_u32 v67, 1024, v67\nv_mov_b32 v52, 0x3f803f80\nv_accvgpr_write_b32 a204, v52\n"
"v_accvgpr_write_b32 a205, v52\nv_accvgpr_write_b32 a206, v52\nv_accvgpr_write_b32 a207, v52\nv_mov_b32 v24, 0xff800000\nv_mov_b32 v28, 0xff800000\nv_mov_b32 v32, 0xff800000\nv_mov_b32 v36, 0xff800000\nv_mov_b32 v40, 0xff800000\n"
"v_mov_b32 v44, 0xff800000\nv_mov_b32 v48, 0xff800000\ns_lshl_b32 s89, s67, 21\ns_lshl_b32 s68, s93, 18\ns_add_u32 s89, s89, s68\ns_mov_b32 s66, 0\n.Lfb1:\ns_add_u32 s68, s89, 4096\n"
"buffer_load_dwordx4 a[112:115], v66, s[40:43], s89 offen offset:0\nbuffer_load_dwordx4 a[116:119], v66, s[40:43], s89 offen offset:64\nbuffer_load_dwordx4 a[120:123], v66, s[40:43], s89 offen offset:128\nbuffer_load_dwordx4 a[124:127], v66, s[40:43], s89 offen offset:192\nbuffer_load_dwordx4 a[128:131], v66, s[40:43], s68 offen offset:0\nbuffer_load_dwordx4 a[132:135], v66, s[40:43], s68 offen offset:64\nbuffer_load_dwordx4 a[136:139], v66, s[40:43], s68 offen offset:128\nbuffer_load_dwordx4 a[140:143], v66, s[40:43], s68 offen offset:192\n"
"s_waitcnt vmcnt(0)\nQzvaa 0,112,0\nQmvaa 0,116,4\nQmvaa 0,120,8\nQmvaa 0,124,12\nQzvaa 4,128,0\nQmvaa 4,132,4\nQmvaa 4,136,8\n"
"Qmvaa 4,140,12\ns_nop 7\nv_max3_f32 v24, v24, v0, v1\nv_max3_f32 v24, v24, v2, v3\nv_max3_f32 v24, v24, v4, v5\nv_max3_f32 v24, v24, v6, v7\nQzvaa 0,112,16\nQmvaa 0,116,20\n"
"Qmvaa 0,120,24\nQmvaa 0,124,28\nQzvaa 4,128,16\nQmvaa 4,132,20\nQmvaa 4,136,24\nQmvaa 4,140,28\ns_nop 7\nv_max3_f32 v28, v28, v0, v1\n"
"v_max3_f32 v28, v28, v2, v3\nv_max3_f32 v28, v28, v4, v5\nv_max3_f32 v28, v28, v6, v7\nQzvaa 0,112,32\nQmvaa 0,116,36\nQmvaa 0,120,40\nQmvaa 0,124,44\nQzvaa 4,128,32\n"
"Qmvaa 4,132,36\nQmvaa 4,136,40\nQmvaa 4,140,44\ns_nop 7\nv_max3_f32 v32, v32, v0, v1\nv_max3_f32 v32, v32, v2, v3\nv_max3_f32 v32, v32, v4, v5\nv_max3_f32 v32, v32, v6, v7\n"
"Qzvaa 0,112,48\nQmvaa 0,116,52\nQmvaa 0,120,56\nQmvaa 0,124,60\nQzvaa 4,128,48\nQmvaa 4,132,52\nQmvaa 4,136,56\nQmvaa 4,140,60\n"
"s_nop 7\nv_max3_f32 v36, v36, v0, v1\nv_max3_f32 v36, v36, v2, v3\nv_max3_f32 v36, v36, v4, v5\nv_max3_f32 v36, v36, v6, v7\nQzvaa 0,112,64\nQmvaa 0,116,68\nQmvaa 0,120,72\n"
"Qmvaa 0,124,76\nQzvaa 4,128,64\nQmvaa 4,132,68\nQmvaa 4,136,72\nQmvaa 4,140,76\ns_nop 7\nv_max3_f32 v40, v40, v0, v1\nv_max3_f32 v40, v40, v2, v3\n"
"v_max3_f32 v40, v40, v4, v5\nv_max3_f32 v40, v40, v6, v7\nQzvaa 0,112,80\nQmvaa 0,116,84\nQmvaa 0,120,88\nQmvaa 0,124,92\nQzvaa 4,128,80\nQmvaa 4,132,84\n"
"Qmvaa 4,136,88\nQmvaa 4,140,92\ns_nop 7\nv_max3_f32 v44, v44, v0, v1\nv_max3_f32 v44, v44, v2, v3\nv_max3_f32 v44, v44, v4, v5\nv_max3_f32 v44, v44, v6, v7\nQzvaa 0,112,96\n"
"Qmvaa 0,116,100\nQmvaa 0,120,104\nQmvaa 0,124,108\nQzvaa 4,128,96\nQmvaa 4,132,100\nQmvaa 4,136,104\nQmvaa 4,140,108\ns_nop 7\n"
"v_max3_f32 v48, v48, v0, v1\nv_max3_f32 v48, v48, v2, v3\nv_max3_f32 v48, v48, v4, v5\nv_max3_f32 v48, v48, v6, v7\ns_add_u32 s89, s89, 8192\ns_add_u32 s66, s66, 1\ns_cmp_lt_u32 s66, 32\ns_cbranch_scc1 .Lfb1\n"
"v_mov_b32 v25, v24\ns_nop 1\nv_permlane32_swap_b32 v24, v25\nv_max_f32 v24, v24, v25\nv_mov_b32 v25, v24\ns_nop 1\nv_permlane16_swap_b32 v24, v25\nv_max_f32 v24, v24, v25\n"
"v_sub_f32 v24, 0, v24\nv_mov_b32 v25, v24\nv_mov_b32 v26, v24\nv_mov_b32 v27, v24\nv_mov_b32 v29, v28\ns_nop 1\nv_permlane32_swap_b32 v28, v29\nv_max_f32 v28, v28, v29\n"
"v_mov_b32 v29, v28\ns_nop 1\nv_permlane16_swap_b32 v28, v29\nv_max_f32 v28, v28, v29\nv_sub_f32 v28, 0, v28\nv_mov_b32 v29, v28\nv_mov_b32 v30, v28\nv_mov_b32 v31, v28\n"
"v_mov_b32 v33, v32\ns_nop 1\nv_permlane32_swap_b32 v32, v33\nv_max_f32 v32, v32, v33\nv_mov_b32 v33, v32\ns_nop 1\nv_permlane16_swap_b32 v32, v33\nv_max_f32 v32, v32, v33\n"
"v_sub_f32 v32, 0, v32\nv_mov_b32 v33, v32\nv_mov_b32 v34, v32\nv_mov_b32 v35, v32\nv_mov_b32 v37, v36\ns_nop 1\nv_permlane32_swap_b32 v36, v37\nv_max_f32 v36, v36, v37\n"
"v_mov_b32 v37, v36\ns_nop 1\nv_permlane16_swap_b32 v36, v37\nv_max_f32 v36, v36, v37\nv_sub_f32 v36, 0, v36\nv_mov_b32 v37, v36\nv_mov_b32 v38, v36\nv_mov_b32 v39, v36\n"
"v_mov_b32 v41, v40\ns_nop 1\nv_permlane32_swap_b32 v40, v41\nv_max_f32 v40, v40, v41\nv_mov_b32 v41, v40\ns_nop 1\nv_permlane16_swap_b32 v40, v41\nv_max_f32 v40, v40, v41\n"
"v_sub_f32 v40, 0, v40\nv_mov_b32 v41, v40\nv_mov_b32 v42, v40\nv_mov_b32 v43, v40\nv_mov_b32 v45, v44\ns_nop 1\nv_permlane32_swap_b32 v44, v45\nv_max_f32 v44, v44, v45\n"
"v_mov_b32 v45, v44\ns_nop 1\nv_permlane16_swap_b32 v44, v45\nv_max_f32 v44, v44, v45\nv_sub_f32 v44, 0, v44\nv_mov_b32 v45, v44\nv_mov_b32 v46, v44\nv_mov_b32 v47, v44\n"
"v_mov_b32 v49, v48\ns_nop 1\nv_permlane32_swap_b32 v48, v49\nv_max_f32 v48, v48, v49\nv_mov_b32 v49, v48\ns_nop 1\nv_permlane16_swap_b32 v48, v49\nv_max_f32 v48, v48, v49\n"
"v_sub_f32 v48, 0, v48\nv_mov_b32 v49, v48\nv_mov_b32 v50, v48\nv_mov_b32 v51, v48\nv_mov_b32 v52, 0\nv_mov_b32 v80, 0\nv_mov_b32 v81, 0\nv_mov_b32 v82, 0\n"
"v_mov_b32 v83, 0\nv_mov_b32 v84, 0\nv_mov_b32 v85, 0\nv_mov_b32 v86, 0\nv_mov_b32 v87, 0\nv_mov_b32 v88, 0\nv_mov_b32 v89, 0\nv_mov_b32 v90, 0\n"
"v_mov_b32 v91, 0\nv_mov_b32 v92, 0\nv_mov_b32 v93, 0\nv_mov_b32 v94, 0\nv_mov_b32 v95, 0\nv_mov_b32 v96, 0\nv_mov_b32 v97, 0\nv_mov_b32 v98, 0\n"
"v_mov_b32 v99, 0\nv_mov_b32 v100, 0\nv_mov_b32 v101, 0\nv_mov_b32 v102, 0\nv_mov_b32 v103, 0\nv_mov_b32 v104, 0\nv_mov_b32 v105, 0\nv_mov_b32 v106, 0\n"
"v_mov_b32 v107, 0\nv_mov_b32 v108, 0\nv_mov_b32 v109, 0\nv_mov_b32 v110, 0\nv_mov_b32 v111, 0\nv_accvgpr_write_b32 a176, v52\nv_accvgpr_write_b32 a177, v52\nv_accvgpr_write_b32 a178, v52\n"
"v_accvgpr_write_b32 a179, v52\nv_mov_b32 v112, 0\nv_mov_b32 v113, 0\nv_mov_b32 v114, 0\nv_mov_b32 v115, 0\nv_mov_b32 v116, 0\nv_mov_b32 v117, 0\nv_mov_b32 v118, 0\n"
"v_mov_b32 v119, 0\nv_mov_b32 v120, 0\nv_mov_b32 v121, 0\nv_mov_b32 v122, 0\nv_mov_b32 v123, 0\nv_mov_b32 v124, 0\nv_mov_b32 v125, 0\nv_mov_b32 v126, 0\n"
"v_mov_b32 v127, 0\nv_mov_b32 v128, 0\nv_mov_b32 v129, 0\nv_mov_b32 v130, 0\nv_mov_b32 v131, 0\nv_mov_b32 v132, 0\nv_mov_b32 v133, 0\nv_mov_b32 v134, 0\n"
"v_mov_b32 v135, 0\nv_mov_b32 v136, 0\nv_mov_b32 v137, 0\nv_mov_b32 v138, 0\nv_mov_b32 v139, 0\nv_mov_b32 v140, 0\nv_mov_b32 v141, 0\nv_mov_b32 v142, 0\n"
"v_mov_b32 v143, 0\nv_accvgpr_write_b32 a180, v52\nv_accvgpr_write_b32 a181, v52\nv_accvgpr_write_b32 a182, v52\nv_accvgpr_write_b32 a183, v52\nv_mov_b32 v144, 0\nv_mov_b32 v145, 0\nv_mov_b32 v146, 0\n"
"v_mov_b32 v147, 0\nv_mov_b32 v148, 0\nv_mov_b32 v149, 0\nv_mov_b32 v150, 0\nv_mov_b32 v151, 0\nv_mov_b32 v152, 0\nv_mov_b32 v153, 0\nv_mov_b32 v154, 0\n"
"v_mov_b32 v155, 0\nv_mov_b32 v156, 0\nv_mov_b32 v157, 0\nv_mov_b32 v158, 0\nv_mov_b32 v159, 0\nv_mov_b32 v160, 0\nv_mov_b32 v161, 0\nv_mov_b32 v162, 0\n"
"v_mov_b32 v163, 0\nv_mov_b32 v164, 0\nv_mov_b32 v165, 0\nv_mov_b32 v166, 0\nv_mov_b32 v167, 0\nv_mov_b32 v168, 0\nv_mov_b32 v169, 0\nv_mov_b32 v170, 0\n"
"v_mov_b32 v171, 0\nv_mov_b32 v172, 0\nv_mov_b32 v173, 0\nv_mov_b32 v174, 0\nv_mov_b32 v175, 0\nv_accvgpr_write_b32 a184, v52\nv_accvgpr_write_b32 a185, v52\nv_accvgpr_write_b32 a186, v52\n"
"v_accvgpr_write_b32 a187, v52\nv_mov_b32 v176, 0\nv_mov_b32 v177, 0\nv_mov_b32 v178, 0\nv_mov_b32 v179, 0\nv_mov_b32 v180, 0\nv_mov_b32 v181, 0\nv_mov_b32 v182, 0\n"
"v_mov_b32 v183, 0\nv_mov_b32 v184, 0\nv_mov_b32 v185, 0\nv_mov_b32 v186, 0\nv_mov_b32 v187, 0\nv_mov_b32 v188, 0\nv_mov_b32 v189, 0\nv_mov_b32 v190, 0\n"
"v_mov_b32 v191, 0\nv_mov_b32 v192, 0\nv_mov_b32 v193, 0\nv_mov_b32 v194, 0\nv_mov_b32 v195, 0\nv_mov_b32 v196, 0\nv_mov_b32 v197, 0\nv_mov_b32 v198, 0\n"
"v_mov_b32 v199, 0\nv_mov_b32 v200, 0\nv_mov_b32 v201, 0\nv_mov_b32 v202, 0\nv_mov_b32 v203, 0\nv_mov_b32 v204, 0\nv_mov_b32 v205, 0\nv_mov_b32 v206, 0\n"
"v_mov_b32 v207, 0\nv_accvgpr_write_b32 a188, v52\nv_accvgpr_write_b32 a189, v52\nv_accvgpr_write_b32 a190, v52\nv_accvgpr_write_b32 a191, v52\nv_mov_b32 v208, 0\nv_mov_b32 v209, 0\nv_mov_b32 v210, 0\n"
"v_mov_b32 v211, 0\nv_mov_b32 v212, 0\nv_mov_b32 v213, 0\nv_mov_b32 v214, 0\nv_mov_b32 v215, 0\nv_mov_b32 v216, 0\nv_mov_b32 v217, 0\nv_mov_b32 v218, 0\n"
"v_mov_b32 v219, 0\nv_mov_b32 v220, 0\nv_mov_b32 v221, 0\nv_mov_b32 v222, 0\nv_mov_b32 v223, 0\nv_mov_b32 v224, 0\nv_mov_b32 v225, 0\nv_mov_b32 v226, 0\n"
"v_mov_b32 v227, 0\nv_mov_b32 v228, 0\nv_mov_b32 v229, 0\nv_mov_b32 v230, 0\nv_mov_b32 v231, 0\nv_mov_b32 v232, 0\nv_mov_b32 v233, 0\nv_mov_b32 v234, 0\n"
"v_mov_b32 v235, 0\nv_mov_b32 v236, 0\nv_mov_b32 v237, 0\nv_mov_b32 v238, 0\nv_mov_b32 v239, 0\nv_accvgpr_write_b32 a192, v52\nv_accvgpr_write_b32 a193, v52\nv_accvgpr_write_b32 a194, v52\n"
"v_accvgpr_write_b32 a195, v52\nv_mov_b32 v240, 0\nv_mov_b32 v241, 0\nv_mov_b32 v242, 0\nv_mov_b32 v243, 0\nv_mov_b32 v244, 0\nv_mov_b32 v245, 0\nv_mov_b32 v246, 0\n"
"v_mov_b32 v247, 0\nv_mov_b32 v248, 0\nv_mov_b32 v249, 0\nv_mov_b32 v250, 0\nv_mov_b32 v251, 0\nv_mov_b32 v252, 0\nv_mov_b32 v253, 0\nv_mov_b32 v254, 0\n"
"v_mov_b32 v255, 0\nv_accvgpr_write_b32 a208, v52\nv_accvgpr_write_b32 a209, v52\nv_accvgpr_write_b32 a210, v52\nv_accvgpr_write_b32 a211, v52\nv_accvgpr_write_b32 a212, v52\nv_accvgpr_write_b32 a213, v52\nv_accvgpr_write_b32 a214, v52\n"
"v_accvgpr_write_b32 a215, v52\nv_accvgpr_write_b32 a216, v52\nv_accvgpr_write_b32 a217, v52\nv_accvgpr_write_b32 a218, v52\nv_accvgpr_write_b32 a219, v52\nv_accvgpr_write_b32 a220, v52\nv_accvgpr_write_b32 a221, v52\nv_accvgpr_write_b32 a222, v52\n"
"v_accvgpr_write_b32 a223, v52\nv_accvgpr_write_b32 a196, v52\nv_accvgpr_write_b32 a197, v52\nv_accvgpr_write_b32 a198, v52\nv_accvgpr_write_b32 a199, v52\nv_accvgpr_write_b32 a224, v52\nv_accvgpr_write_b32 a225, v52\nv_accvgpr_write_b32 a226, v52\n"
"v_accvgpr_write_b32 a227, v52\nv_accvgpr_write_b32 a228, v52\nv_accvgpr_write_b32 a229, v52\nv_accvgpr_write_b32 a230, v52\nv_accvgpr_write_b32 a231, v52\nv_accvgpr_write_b32 a232, v52\nv_accvgpr_write_b32 a233, v52\nv_accvgpr_write_b32 a234, v52\n"
"v_accvgpr_write_b32 a235, v52\nv_accvgpr_write_b32 a236, v52\nv_accvgpr_write_b32 a237, v52\nv_accvgpr_write_b32 a238, v52\nv_accvgpr_write_b32 a239, v52\nv_accvgpr_write_b32 a240, v52\nv_accvgpr_write_b32 a241, v52\nv_accvgpr_write_b32 a242, v52\n"
"v_accvgpr_write_b32 a243, v52\nv_accvgpr_write_b32 a244, v52\nv_accvgpr_write_b32 a245, v52\nv_accvgpr_write_b32 a246, v52\nv_accvgpr_write_b32 a247, v52\nv_accvgpr_write_b32 a248, v52\nv_accvgpr_write_b32 a249, v52\nv_accvgpr_write_b32 a250, v52\n"
"v_accvgpr_write_b32 a251, v52\nv_accvgpr_write_b32 a252, v52\nv_accvgpr_write_b32 a253, v52\nv_accvgpr_write_b32 a254, v52\nv_accvgpr_write_b32 a255, v52\nv_accvgpr_write_b32 a200, v52\nv_accvgpr_write_b32 a201, v52\nv_accvgpr_write_b32 a202, v52\n"
"v_accvgpr_write_b32 a203, v52\ns_lshl_b32 s89, s67, 21\ns_lshl_b32 s68, s93, 18\ns_add_u32 s89, s89, s68\ns_mov_b32 s66, 0\n.Lfb2:\ns_add_u32 s68, s89, 4096\nbuffer_load_dwordx4 a[112:115], v66, s[40:43], s89 offen offset:0\n"
"buffer_load_dwordx4 a[116:119], v66, s[40:43], s89 offen offset:64\nbuffer_load_dwordx4 a[120:123], v66, s[40:43], s89 offen offset:128\nbuffer_load_dwordx4 a[124:127], v66, s[40:43], s89 offen offset:192\nbuffer_load_dwordx4 a[128:131], v66, s[40:43], s68 offen offset:0\nbuffer_load_dwordx4 a[132:135], v66, s[40:43], s68 offen offset:64\nbuffer_load_dwordx4 a[136:139], v66, s[40:43], s68 offen offset:128\nbuffer_load_dwordx4 a[140:143], v66, s[40:43], s68 offen offset:192\nbuffer_load_ushort v8, v67, s[44:47], s89 offen offset:0\n"
"buffer_load_ushort v9, v67, s[44:47], s89 offen offset:256\nbuffer_load_ushort v10, v67, s[44:47], s89 offen offset:512\nbuffer_load_ushort v11, v67, s[44:47], s89 offen offset:768\nbuffer_load_ushort v12, v67, s[44:47], s68 offen offset:0\nbuffer_load_ushort v13, v67, s[44:47], s68 offen offset:256\nbuffer_load_ushort v14, v67, s[44:47], s68 offen offset:512\nbuffer_load_ushort v15, v67, s[44:47], s68 offen offset:768\ns_waitcnt vmcnt(0)\n"
"v_lshl_or_b32 v8, v9, 16, v8\nv_accvgpr_write_b32 a144, v8\nv_lshl_or_b32 v10, v11, 16, v10\nv_accvgpr_write_b32 a145, v10\nv_lshl_or_b32 v12, v13, 16, v12\nv_accvgpr_write_b32 a146, v12\nv_lshl_or_b32 v14, v15, 16, v14\nv_accvgpr_write_b32 a147, v14\n"
"buffer_load_ushort v8, v67, s[44:47], s89 offen offset:32\nbuffer_load_ushort v9, v67, s[44:47], s89 offen offset:288\nbuffer_load_ushort v10, v67, s[44:47], s89 offen offset:544\nbuffer_load_ushort v11, v67, s[44:47], s89 offen offset:800\nbuffer_load_ushort v12, v67, s[44:47], s68 offen offset:32\nbuffer_load_ushort v13, v67, s[44:47], s68 offen offset:288\nbuffer_load_ushort v14, v67, s[44:47], s68 offen offset:544\nbuffer_load_ushort v15, v67, s[44:47], s68 offen offset:800\n"
"s_waitcnt vmcnt(0)\nv_lshl_or_b32 v8, v9, 16, v8\nv_accvgpr_write_b32 a148, v8\nv_lshl_or_b32 v10, v11, 16, v10\nv_accvgpr_write_b32 a149, v10\nv_lshl_or_b32 v12, v13, 16, v12\nv_accvgpr_write_b32 a150, v12\nv_lshl_or_b32 v14, v15, 16, v14\n"
"v_accvgpr_write_b32 a151, v14\nbuffer_load_ushort v8, v67, s[44:47], s89 offen offset:64\nbuffer_load_ushort v9, v67, s[44:47], s89 offen offset:320\nbuffer_load_ushort v10, v67, s[44:47], s89 offen offset:576\nbuffer_load_ushort v11, v67, s[44:47], s89 offen offset:832\nbuffer_load_ushort v12, v67, s[44:47], s68 offen offset:64\nbuffer_load_ushort v13, v67, s[44:47], s68 offen offset:320\nbuffer_load_ushort v14, v67, s[44:47], s68 offen offset:576\n"
"buffer_load_ushort v15, v67, s[44:47], s68 offen offset:832\ns_waitcnt vmcnt(0)\nv_lshl_or_b32 v8, v9, 16, v8\nv_accvgpr_write_b32 a152, v8\nv_lshl_or_b32 v10, v11, 16, v10\nv_accvgpr_write_b32 a153, v10\nv_lshl_or_b32 v12, v13, 16, v12\nv_accvgpr_write_b32 a154, v12\n"
"v_lshl_or_b32 v14, v15, 16, v14\nv_accvgpr_write_b32 a155, v14\nbuffer_load_ushort v8, v67, s[44:47], s89 offen offset:96\nbuffer_load_ushort v9, v67, s[44:47], s89 offen offset:352\nbuffer_load_ushort v10, v67, s[44:47], s89 offen offset:608\nbuffer_load_ushort v11, v67, s[44:47], s89 offen offset:864\nbuffer_load_ushort v12, v67, s[44:47], s68 offen offset:96\nbuffer_load_ushort v13, v67, s[44:47], s68 offen offset:352\n"
"buffer_load_ushort v14, v67, s[44:47], s68 offen offset:608\nbuffer_load_ushort v15, v67, s[44:47], s68 offen offset:864\ns_waitcnt vmcnt(0)\nv_lshl_or_b32 v8, v9, 16, v8\nv_accvgpr_write_b32 a156, v8\nv_lshl_or_b32 v10, v11, 16, v10\nv_accvgpr_write_b32 a157, v10\nv_lshl_or_b32 v12, v13, 16, v12\n"
"v_accvgpr_write_b32 a158, v12\nv_lshl_or_b32 v14, v15, 16, v14\nv_accvgpr_write_b32 a159, v14\nbuffer_load_ushort v8, v67, s[44:47], s89 offen offset:128\nbuffer_load_ushort v9, v67, s[44:47], s89 offen offset:384\nbuffer_load_ushort v10, v67, s[44:47], s89 offen offset:640\nbuffer_load_ushort v11, v67, s[44:47], s89 offen offset:896\nbuffer_load_ushort v12, v67, s[44:47], s68 offen offset:128\n"
"buffer_load_ushort v13, v67, s[44:47], s68 offen offset:384\nbuffer_load_ushort v14, v67, s[44:47], s68 offen offset:640\nbuffer_load_ushort v15, v67, s[44:47], s68 offen offset:896\ns_waitcnt vmcnt(0)\nv_lshl_or_b32 v8, v9, 16, v8\nv_accvgpr_write_b32 a160, v8\nv_lshl_or_b32 v10, v11, 16, v10\nv_accvgpr_write_b32 a161, v10\n"
"v_lshl_or_b32 v12, v13, 16, v12\nv_accvgpr_write_b32 a162, v12\nv_lshl_or_b32 v14, v15, 16, v14\nv_accvgpr_write_b32 a163, v14\nbuffer_load_ushort v8, v67, s[44:47], s89 offen offset:160\nbuffer_load_ushort v9, v67, s[44:47], s89 offen offset:416\nbuffer_load_ushort v10, v67, s[44:47], s89 offen offset:672\nbuffer_load_ushort v11, v67, s[44:47], s89 offen offset:928\n"
"buffer_load_ushort v12, v67, s[44:47], s68 offen offset:160\nbuffer_load_ushort v13, v67, s[44:47], s68 offen offset:416\nbuffer_load_ushort v14, v67, s[44:47], s68 offen offset:672\nbuffer_load_ushort v15, v67, s[44:47], s68 offen offset:928\ns_waitcnt vmcnt(0)\nv_lshl_or_b32 v8, v9, 16, v8\nv_accvgpr_write_b32 a164, v8\nv_lshl_or_b32 v10, v11, 16, v10\n"
"v_accvgpr_write_b32 a165, v10\nv_lshl_or_b32 v12, v13, 16, v12\nv_accvgpr_write_b32 a166, v12\nv_lshl_or_b32 v14, v15, 16, v14\nv_accvgpr_write_b32 a167, v14\nbuffer_load_ushort v8, v67, s[44:47], s89 offen offset:192\nbuffer_load_ushort v9, v67, s[44:47], s89 offen offset:448\nbuffer_load_ushort v10, v67, s[44:47], s89 offen offset:704\n"
"buffer_load_ushort v11, v67, s[44:47], s89 offen offset:960\nbuffer_load_ushort v12, v67, s[44:47], s68 offen offset:192\nbuffer_load_ushort v13, v67, s[44:47], s68 offen offset:448\nbuffer_load_ushort v14, v67, s[44:47], s68 offen offset:704\nbuffer_load_ushort v15, v67, s[44:47], s68 offen offset:960\ns_waitcnt vmcnt(0)\nv_lshl_or_b32 v8, v9, 16, v8\nv_accvgpr_write_b32 a168, v8\n"
"v_lshl_or_b32 v10, v11, 16, v10\nv_accvgpr_write_b32 a169, v10\nv_lshl_or_b32 v12, v13, 16, v12\nv_accvgpr_write_b32 a170, v12\nv_lshl_or_b32 v14, v15, 16, v14\nv_accvgpr_write_b32 a171, v14\nbuffer_load_ushort v8, v67, s[44:47], s89 offen offset:224\nbuffer_load_ushort v9, v67, s[44:47], s89 offen offset:480\n"
"buffer_load_ushort v10, v67, s[44:47], s89 offen offset:736\nbuffer_load_ushort v11, v67, s[44:47], s89 offen offset:992\nbuffer_load_ushort v12, v67, s[44:47], s68 offen offset:224\nbuffer_load_ushort v13, v67, s[44:47], s68 offen offset:480\nbuffer_load_ushort v14, v67, s[44:47], s68 offen offset:736\nbuffer_load_ushort v15, v67, s[44:47], s68 offen offset:992\ns_waitcnt vmcnt(0)\nv_lshl_or_b32 v8, v9, 16, v8\n"
"v_accvgpr_write_b32 a172, v8\nv_lshl_or_b32 v10, v11, 16, v10\nv_accvgpr_write_b32 a173, v10\nv_lshl_or_b32 v12, v13, 16, v12\nv_accvgpr_write_b32 a174, v12\nv_lshl_or_b32 v14, v15, 16, v14\nv_accvgpr_write_b32 a175, v14\nQcvaa 0,112,0,24\n"
"Qmvaa 0,116,4\nQmvaa 0,120,8\nQmvaa 0,124,12\nQcvaa 4,128,0,24\nQmvaa 4,132,4\nQmvaa 4,136,8\nQmvaa 4,140,12\ns_nop 7\n"
"Qe 0\nQe 1\nQe 2\nQe 3\nQe 4\nQe 5\nQe 6\nQe 7\n"
"s_nop 0\nQk 16,0,1\nQk 17,2,3\nQk 18,4,5\nQk 19,6,7\ns_nop 1\nQmvva 80,16,144\nQmvva 84,16,148\n"
"Qmvva 88,16,152\nQmvva 92,16,156\nQmvva 96,16,160\nQmvva 100,16,164\nQmvva 104,16,168\nQmvva 108,16,172\nQmaav 176,204,16\nQcvaa 0,112,16,28\n"
"Qmvaa 0,116,20\nQmvaa 0,120,24\nQmvaa 0,124,28\nQcvaa 4,128,16,28\nQmvaa 4,132,20\nQmvaa 4,136,24\nQmvaa 4,140,28\ns_nop 7\n"
"Qe 0\nQe 1\nQe 2\nQe 3\nQe 4\nQe 5\nQe 6\nQe 7\n"
"s_nop 0\nQk 16,0,1\nQk 17,2,3\nQk 18,4,5\nQk 19,6,7\ns_nop 1\nQmvva 112,16,144\nQmvva 116,16,148\n"
"Qmvva 120,16,152\nQmvva 124,16,156\nQmvva 128,16,160\nQmvva 132,16,164\nQmvva 136,16,168\nQmvva 140,16,172\nQmaav 180,204,16\nQcvaa 0,112,32,32\n"
"Qmvaa 0,116,36\nQmvaa 0,120,40\nQmvaa 0,124,44\nQcvaa 4,128,32,32\nQmvaa 4,132,36\nQmvaa 4,136,40\nQmvaa 4,140,44\ns_nop 7\n"
"Qe 0\nQe 1\nQe 2\nQe 3\nQe 4\nQe 5\nQe 6\nQe 7\n"
"s_nop 0\nQk 16,0,1\nQk 17,2,3\nQk 18,4,5\nQk 19,6,7\ns_nop 1\nQmvva 144,16,144\nQmvva 148,16,148\n"
"Qmvva 152,16,152\nQmvva 156,16,156\nQmvva 160,16,160\nQmvva 164,16,164\nQmvva 168,16,168\nQmvva 172,16,172\nQmaav 184,204,16\nQcvaa 0,112,48,36\n"
"Qmvaa 0,116,52\nQmvaa 0,120,56\nQmvaa 0,124,60\nQcvaa 4,128,48,36\nQmvaa 4,132,52\nQmvaa 4,136,56\nQmvaa 4,140,60\ns_nop 7\n"
"Qe 0\nQe 1\nQe 2\nQe 3\nQe 4\nQe 5\nQe 6\nQe 7\n"
"s_nop 0\nQk 16,0,1\nQk 17,2,3\nQk 18,4,5\nQk 19,6,7\ns_nop 1\nQmvva 176,16,144\nQmvva 180,16,148\n"
"Qmvva 184,16,152\nQmvva 188,16,156\nQmvva 192,16,160\nQmvva 196,16,164\nQmvva 200,16,168\nQmvva 204,16,172\nQmaav 188,204,16\nQcvaa 0,112,64,40\n"
"Qmvaa 0,116,68\nQmvaa 0,120,72\nQmvaa 0,124,76\nQcvaa 4,128,64,40\nQmvaa 4,132,68\nQmvaa 4,136,72\nQmvaa 4,140,76\ns_nop 7\n"
"Qe 0\nQe 1\nQe 2\nQe 3\nQe 4\nQe 5\nQe 6\nQe 7\n"
"s_nop 0\nQk 16,0,1\nQk 17,2,3\nQk 18,4,5\nQk 19,6,7\ns_nop 1\nQmvva 208,16,144\nQmvva 212,16,148\n"
"Qmvva 216,16,152\nQmvva 220,16,156\nQmvva 224,16,160\nQmvva 228,16,164\nQmvva 232,16,168\nQmvva 236,16,172\nQmaav 192,204,16\nQcvaa 0,112,80,44\n"
"Qmvaa 0,116,84\nQmvaa 0,120,88\nQmvaa 0,124,92\nQcvaa 4,128,80,44\nQmvaa 4,132,84\nQmvaa 4,136,88\nQmvaa 4,140,92\ns_nop 7\n"
"Qe 0\nQe 1\nQe 2\nQe 3\nQe 4\nQe 5\nQe 6\nQe 7\n"
"s_nop 0\nQk 16,0,1\nQk 17,2,3\nQk 18,4,5\nQk 19,6,7\ns_nop 1\nQmvva 240,16,144\nQmvva 244,16,148\n"
"Qmvva 248,16,152\nQmvva 252,16,156\nQmava 208,16,160\nQmava 212,16,164\nQmava 216,16,168\nQmava 220,16,172\nQmaav 196,204,16\nQcvaa 0,112,96,48\n"
"Qmvaa 0,116,100\nQmvaa 0,120,104\nQmvaa 0,124,108\nQcvaa 4,128,96,48\nQmvaa 4,132,100\nQmvaa 4,136,104\nQmvaa 4,140,108\ns_nop 7\n"
"Qe 0\nQe 1\nQe 2\nQe 3\nQe 4\nQe 5\nQe 6\nQe 7\n"
"s_nop 0\nQk 16,0,1\nQk 17,2,3\nQk 18,4,5\nQk 19,6,7\ns_nop 1\nQmava 224,16,144\nQmava 228,16,148\n"
"Qmava 232,16,152\nQmava 236,16,156\nQmava 240,16,160\nQmava 244,16,164\nQmava 248,16,168\nQmava 252,16,172\nQmaav 200,204,16\ns_add_u32 s89, s89, 8192\n"
"s_add_u32 s66, s66, 1\ns_cmp_lt_u32 s66, 32\ns_cbranch_scc1 .Lfb2\ns_nop 7\nv_accvgpr_read_b32 v0, a176\nv_accvgpr_read_b32 v4, a180\nv_accvgpr_read_b32 v8, a184\nv_accvgpr_read_b32 v12, a188\n"
"v_accvgpr_read_b32 v16, a192\nv_accvgpr_read_b32 v20, a196\nv_accvgpr_read_b32 v24, a200\ns_mov_b32 s73, 0\ns_cmp_eq_u32 s67, 0\ns_cbranch_scc1 .Lfbret0\ns_branch .Lfbret1\n.Lf2_w:\n"
"s_mov_b32 s97, 0\n.Lf2_l:\ns_sleep 1\nbuffer_load_dword v78, v76, s[24:27], 0 offen sc1\ns_waitcnt vmcnt(0)\ns_add_u32 s97, s97, 1\ns_cmp_gt_u32 s97, 1048576\ns_cbranch_scc1 .Lf2_r\n"
"v_sub_u32 v66, v78, v65\nv_cmp_gt_u32 vcc, 2, v66\ns_and_b64 vcc, vcc, s[28:29]\ns_cbranch_scc1 .Lf2_l\ns_branch .Lf2_r\n.Lf3_w:\ns_mov_b32 s97, 0\n.Lf3_l:\n"
"s_sleep 1\nbuffer_load_dword v78, v76, s[24:27], 0 offen sc1\ns_waitcnt vmcnt(0)\ns_add_u32 s97, s97, 1\ns_cmp_gt_u32 s97, 1048576\ns_cbranch_scc1 .Lf3_r\nv_sub_u32 v66, v78, v65\nv_cmp_gt_u32 vcc, 3, v66\n"
"s_and_b64 vcc, vcc, s[28:29]\ns_cbranch_scc1 .Lf3_l\ns_branch .Lf3_r\n.Lx0_w:\ns_mov_b32 s97, 0\n.Lx0_l:\ns_sleep 1\nbuffer_load_dword v78, v76, s[24:27], 0 offen sc1\n"
"s_waitcnt vmcnt(0)\ns_add_u32 s97, s97, 1\ns_cmp_gt_u32 s97, 1048576\ns_cbranch_scc1 .Lx0_r\nv_sub_u32 v66, v78, v65\nv_cmp_gt_u32 vcc, 1, v66\ns_and_b64 vcc, vcc, s[28:29]\ns_cbranch_scc1 .Lx0_l\n"
"s_branch .Lx0_r\n.Lx1_w:\ns_mov_b32 s97, 0\n.Lx1_l:\ns_sleep 1\nbuffer_load_dword v78, v76, s[24:27], 0 offen sc1\ns_waitcnt vmcnt(0)\ns_add_u32 s97, s97, 1\n"
"s_cmp_gt_u32 s97, 1048576\ns_cbranch_scc1 .Lx1_r\nv_sub_u32 v66, v78, v65\nv_cmp_gt_u32 vcc, 1, v66\ns_and_b64 vcc, vcc, s[28:29]\ns_cbranch_scc1 .Lx1_l\ns_branch .Lx1_r\n.Lexit:\n"
  :: "s"((unsigned)kp), "s"((unsigned)(kp >> 32)), "s"((unsigned)vp), "s"((unsigned)(vp >> 32)), "s"((unsigned)xp), "s"((unsigned)(xp >> 32)),
     "s"((unsigned)wp), "s"((unsigned)(wp >> 32)), "s"((unsigned)op), "s"((unsigned)(op >> 32)), "s"(ldsb), "s"(wid), "s"(head0),
     "s"((unsigned)dp), "s"((unsigned)(dp >> 32)), "s"((unsigned)wop), "s"((unsigned)(wop >> 32)), "s"((unsigned)yp), "s"((unsigned)(yp >> 32)),
     "s"(hg), "s"(b * 8u + hg), "s"(kx)
  : "v0", "v1", "v2", "v3", "v4", "v5", "v6", "v7", "v8", "v9", "v10", "v11", "v12", "v13", "v14", "v15", "v16", "v17", "v18", "v19", "v20", "v21", "v22", "v23", "v24", "v25", "v26", "v27", "v28", "v29", "v30", "v31", "v32", "v33", "v34", "v35", "v36", "v37", "v38", "v39", "v40", "v41", "v42", "v43", "v44", "v45", "v46", "v47", "v48", "v49", "v50", "v51", "v52", "v53", "v54", "v55", "v56", "v57", "v58", "v59", "v60", "v61", "v62", "v63", "v64", "v65", "v66", "v67", "v68", "v69", "v70", "v71", "v72", "v73", "v74", "v75", "v76", "v77", "v78", "v79", "v80", "v81", "v82", "v83", "v84", "v85", "v86", "v87", "v88", "v89", "v90", "v91", "v92", "v93", "v94", "v95", "v96", "v97", "v98", "v99", "v100", "v101", "v102", "v103", "v104", "v105", "v106", "v107", "v108", "v109", "v110", "v111", "v112", "v113", "v114", "v115", "v116", "v117", "v118", "v119", "v120", "v121", "v122", "v123", "v124", "v125", "v126", "v127", "v128", "v129", "v130", "v131", "v132", "v133", "v134", "v135", "v136", "v137", "v138", "v139", "v140", "v141", "v142", "v143", "v144", "v145", "v146", "v147", "v148", "v149", "v150", "v151", "v152", "v153", "v154", "v155", "v156", "v157", "v158", "v159", "v160", "v161", "v162", "v163", "v164", "v165", "v166", "v167", "v168", "v169", "v170", "v171", "v172", "v173", "v174", "v175", "v176", "v177", "v178", "v179", "v180", "v181", "v182", "v183", "v184", "v185", "v186", "v187", "v188", "v189", "v190", "v191", "v192", "v193", "v194", "v195", "v196", "v197", "v198", "v199", "v200", "v201", "v202", "v203", "v204", "v205", "v206", "v207", "v208", "v209", "v210", "v211", "v212", "v213", "v214", "v215", "v216", "v217", "v218", "v219", "v220", "v221", "v222", "v223", "v224", "v225", "v226", "v227", "v228", "v229", "v230", "v231", "v232", "v233", "v234", "v235", "v236", "v237", "v238", "v239", "v240", "v241", "v242", "v243", "v244", "v245", "v246", "v247", "v248", "v249", "v250", "v251", "v252", "v253", "v254", "v255", "a0", "a1", "a2", "a3", "a4", "a5", "a6", "a7", "a8", "a9", "a10", "a11", "a12", "a13", "a14", "a15", "a16", "a17", "a18", "a19", "a20", "a21", "a22", "a23", "a24", "a25", "a26", "a27", "a28", "a29", "a30", "a31", "a32", "a33", "a34", "a35", "a36", "a37", "a38", "a39", "a40", "a41", "a42", "a43", "a44", "a45", "a46", "a47", "a48", "a49", "a50", "a51", "a52", "a53", "a54", "a55", "a56", "a57", "a58", "a59", "a60", "a61", "a62", "a63", "a64", "a65", "a66", "a67", "a68", "a69", "a70", "a71", "a72", "a73", "a74", "a75", "a76", "a77", "a78", "a79", "a80", "a81", "a82", "a83", "a84", "a85", "a86", "a87", "a88", "a89", "a90", "a91", "a92", "a93", "a94", "a95", "a96", "a97", "a98", "a99", "a100", "a101", "a102", "a103", "a104", "a105", "a106", "a107", "a108", "a109", "a110", "a111", "a112", "a113", "a114", "a115", "a116", "a117", "a118", "a119", "a120", "a121", "a122", "a123", "a124", "a125", "a126", "a127", "a128", "a129", "a130", "a131", "a132", "a133", "a134", "a135", "a136", "a137", "a138", "a139", "a140", "a141", "a142", "a143", "a144", "a145", "a146", "a147", "a148", "a149", "a150", "a151", "a152", "a153", "a154", "a155", "a156", "a157", "a158", "a159", "a160", "a161", "a162", "a163", "a164", "a165", "a166", "a167", "a168", "a169", "a170", "a171", "a172", "a173", "a174", "a175", "a176", "a177", "a178", "a179", "a180", "a181", "a182", "a183", "a184", "a185", "a186", "a187", "a188", "a189", "a190", "a191", "a192", "a193", "a194", "a195", "a196", "a197", "a198", "a199", "a200", "a201", "a202", "a203", "a204", "a205", "a206", "a207", "a208", "a209", "a210", "a211", "a212", "a213", "a214", "a215", "a216", "a217", "a218", "a219", "a220", "a221", "a222", "a223", "a224", "a225", "a226", "a227", "a228", "a229", "a230", "a231", "a232", "a233", "a234", "a235", "a236", "a237", "a238", "a239", "a240", "a241", "a242", "a243", "a244", "a245", "a246", "a247", "a248", "a249", "a250", "a251", "a252", "a253", "a254", "a255", "s24", "s25", "s26", "s27", "s28", "s29", "s30", "s31", "s40", "s41", "s42", "s43", "s44", "s45", "s46", "s47", "s48", "s49", "s50", "s51", "s52", "s53", "s54", "s55", "s56", "s57", "s58", "s59", "s60", "s61", "s62", "s63", "s64", "s65", "s66", "s67", "s68", "s69", "s70", "s71", "s72", "s73", "s74", "s75", "s76", "s77", "s78", "s79", "s80", "s81", "s82", "s83", "s84", "s85", "s86", "s87", "s88", "s89", "s90", "s91", "s92", "s93", "s94", "s95", "s96", "s97", "s98", "s99", "vcc", "scc", "memory");
}

extern "C" void kernel_launch(void* const* d_in, const int* in_sizes, int n_in, void* d_out, int out_size, void* d_ws, size_t ws_size, hipStream_t stream) {
  typedef const unsigned short* P;
  (void)hipFuncSetAttribute((const void*)mha2_asm, hipFuncAttributeMaxDynamicSharedMemorySize, 163840);
  hipLaunchKernelGGL(mha2_asm, dim3(256), dim3(256), 163840, stream, (P)d_in[0], (P)d_in[5], (P)d_in[3], (P)d_in[8], (P)d_in[1], (P)d_in[6], (P)d_in[2], (P)d_in[7],
                     (P)d_in[4], (P)d_in[9], (char*)d_ws, (unsigned short*)d_out);
}
```

```cpp
#include <hip/hip_runtime.h>
#include <stdint.h>

__global__ __launch_bounds__(256, 1) __attribute__((aligned(4096)))
void mha2_asm(const unsigned short* X0, const unsigned short* W0, const unsigned short* K0, const unsigned short* V0,
              const unsigned short* X1, const unsigned short* W1, const unsigned short* K1, const unsigned short* V1,
              const unsigned short* WO0, const unsigned short* WO1, char* ws, unsigned short* out) {
  extern __shared__ __attribute__((aligned(16))) char lds[];
  unsigned t0, t1;
  asm volatile(
".macro Qta d,v,o\nds_read_b64_tr_b16 a[\\d:\\d+1], v\\v offset:\\o\n.endm\n.macro Qda d,v,o\nds_read_b128 a[\\d:\\d+3], v\\v offset:\\o\n.endm\n.macro Qzvaa d,a,b\nv_mfma_f32_16x16x32_bf16 v[\\d:\\d+3], a[\\a:\\a+3], a[\\b:\\b+3], 0\n"
".endm\n.macro Qcvaa d,a,b,c\nv_mfma_f32_16x16x32_bf16 v[\\d:\\d+3], a[\\a:\\a+3], a[\\b:\\b+3], v[\\c:\\c+3]\n.endm\n.macro Qk d,a,b\nv_cvt_pk_bf16_f32 v\\d, v\\a, v\\b\n.endm\n.macro Qmvaa d,a,b\n"
"v_mfma_f32_16x16x32_bf16 v[\\d:\\d+3], a[\\a:\\a+3], a[\\b:\\b+3], v[\\d:\\d+3]\n.endm\n.macro Qtv d,v,o\nds_read_b64_tr_b16 v[\\d:\\d+1], v\\v offset:\\o\n.endm\n.macro Qa d,a,b\nv_add_f32 v\\d, v\\a, v\\b\n.endm\n"
".macro Qe d\nv_exp_f32 v\\d, v\\d\n.endm\n.macro Qmvva d,a,b\nv_mfma_f32_16x16x32_bf16 v[\\d:\\d+3], v[\\a:\\a+3], a[\\b:\\b+3], v[\\d:\\d+3]\n.endm\n.macro Qmava d,a,b\nv_mfma_f32_16x16x32_bf16 a[\\d:\\d+3], v[\\a:\\a+3], a[\\b:\\b+3], a[\\d:\\d+3]\n"
".endm\n.macro Qzvvv d,a,b\nv_mfma_f32_16x16x32_bf16 v[\\d:\\d+3], v[\\a:\\a+3], v[\\b:\\b+3], 0\n.endm\n.macro Qzvav d,a,b\nv_mfma_f32_16x16x32_bf16 v[\\d:\\d+3], a[\\a:\\a+3], v[\\b:\\b+3], 0\n.endm\n.macro Qmvav d,a,b\n"
"v_mfma_f32_16x16x32_bf16 v[\\d:\\d+3], a[\\a:\\a+3], v[\\b:\\b+3], v[\\d:\\d+3]\n.endm\n.macro Qcaav d,a,b,c\nv_mfma_f32_16x16x32_bf16 a[\\d:\\d+3], a[\\a:\\a+3], v[\\b:\\b+3], a[\\c:\\c+3]\n.endm\n.macro Qmaav d,a,b\nv_mfma_f32_16x16x32_bf16 a[\\d:\\d+3], a[\\a:\\a+3], v[\\b:\\b+3], a[\\d:\\d+3]\n.endm\n"
".macro Qcvav d,a,b,c\nv_mfma_f32_16x16x32_bf16 v[\\d:\\d+3], a[\\a:\\a+3], v[\\b:\\b+3], v[\\c:\\c+3]\n.endm\n.macro Qmaaa d,a,b\nv_mfma_f32_16x16x32_bf16 a[\\d:\\d+3], a[\\a:\\a+3], a[\\b:\\b+3], a[\\d:\\d+3]\n.endm\n.macro Qdv d,v,o\nds_read_b128 v[\\d:\\d+3], v\\v offset:\\o\n"
".endm\n.p2align 3\ns_and_b32 s33, %3, 4\ns_lshl_b32 s30, s33, 3\ns_load_dwordx8 s[8:15], %2, s30\ns_lshl_b32 s33, s33, 1\ns_load_dwordx4 s[20:23], %2, 0x50\ns_load_dwordx2 s[16:17], %2, s33 offset:0x40\n"
"s_memtime s[80:81]\ns_memrealtime s[82:83]\ns_bfe_u32 s19, %3, 0x30003\ns_and_b32 s39, %3, 3\ns_lshr_b32 %1, %3, 6\ns_lshl2_add_u32 s30, s39, %1\ns_lshl3_add_u32 %0, s30, s19\ns_and_b32 %1, s19, 4\n"
"s_add_u32 s18, s19, %1\ns_lshr_b32 s39, %5, 1\ns_lshl1_add_u32 s18, s18, s39\ns_lshl_b32 %1, %1, 11\ns_mul_i32 s39, s30, 286720\nv_mbcnt_lo_u32_b32 v0, -1, 0\nv_mbcnt_hi_u32_b32 v0, -1, v0\nv_lshlrev_b32 v60, 4, v0\n"
"s_add_u32 m0, %4, 160768\ns_getpc_b64 s[56:57]\n.Lpc0:\ns_and_b32 s56, s56, 0xffffff80\ns_and_b32 s57, s57, 0xffff\ns_mov_b32 s58, .Lend-.Lpc0\ns_mov_b32 s59, 0x20000\ns_and_b32 s94, %0, 31\n"
"s_lshl_b32 s94, s94, 2\ns_add_u32 s94, s94, %5\ns_lshl_b32 s94, s94, 10\nbuffer_load_dwordx4 v60, s[56:59], s94 offen lds\ns_mov_b32 s54, 5242880\ns_mov_b32 s55, 0x20000\ns_mov_b32 s50, 286720\ns_mov_b32 s51, 0x20000\n"
"s_mov_b32 s70, %5\ns_and_b32 s92, s70, 1\ns_lshr_b32 s93, s70, 1\nv_lshrrev_b32 v2, 4, v0\ns_lshl_b32 s68, s92, 18\nv_and_b32 v4, 3, v0\nv_lshlrev_b32 v4, 4, v4\nv_and_b32 v1, 1, v2\n"
"v_lshl_add_u32 v4, v1, 6, v4\nv_lshrrev_b32 v1, 5, v0\nv_lshl_add_u32 v4, v1, 15, v4\nv_bfe_u32 v1, v0, 2, 2\nv_lshl_add_u32 v4, v1, 13, v4\nv_add_u32 v62, s68, v4\ns_lshl_b32 s87, s18, 8\ns_mul_i32 s62, s70, 4128\n"
"s_add_u32 s62, s62, %4\ns_add_u32 s62, s62, 33024\ns_lshl_b32 s69, s92, 1\nv_lshrrev_b32 v3, 5, v0\nv_and_b32 v4, 3, v0\nv_xor_b32 v4, s69, v4\nv_lshlrev_b32 v4, 4, v4\nv_lshl_add_u32 v4, v3, 6, v4\n"
"v_bfe_u32 v1, v0, 2, 3\nv_mul_u32_u24 v64, 1280, v1\nv_add_u32 v64, v64, v4\ns_mul_i32 s86, s93, 143360\ns_mul_i32 s68, s92, 10240\ns_add_u32 s86, s86, s68\ns_mul_i32 s63, s92, 14336\ns_mul_i32 s68, s93, 7168\n"
"s_add_u32 s63, s63, s68\ns_add_u32 s63, s63, %4\ns_add_u32 s63, s63, 99072\ns_lshl_b32 s34, s33, 1\ns_add_u32 s34, s34, s30\ns_mul_hi_u32 s35, s34, 6291456\ns_mul_i32 s34, s34, 6291456\ns_lshl_b32 s36, s30, 5\n"
"s_and_b32 s37, s18, 30\ns_add_u32 s36, s36, s37\ns_lshl_b32 s36, s36, 18\ns_sub_u32 s36, s36, 1024\ns_subb_u32 s37, 0, 0\ns_mul_i32 s38, s30, 573440\ns_mul_i32 s33, s33, 160\ns_add_u32 s38, s38, s33\n"
"s_and_b32 s33, s19, 3\ns_mul_i32 s33, s33, 320\ns_add_u32 s38, s38, s33\ns_mov_b32 s26, 6291456\ns_mov_b32 s27, 0x20000\nv_and_b32 v5, 7, v0\nv_lshlrev_b32 v5, 7, v5\nv_add_u32 v76, 5242880, v5\n"
"s_lshl_b32 s68, s70, 2\ns_add_u32 s68, s68, 4\nv_bfe_u32 v5, v0, 3, 1\nv_mad_u32_u24 v76, v5, s68, v76\ns_mov_b32 s42, 2622464\ns_mov_b32 s43, 0x20000\ns_mov_b32 s46, 2622464\ns_mov_b32 s47, 0x20000\n"
"s_mov_b32 s58, 0\ns_mov_b32 s59, 0x20000\ns_mov_b32 s65, 0\ns_mov_b32 s60, %1\ns_add_u32 s90, s60, 260096\ns_mov_b32 s67, 0\ns_mov_b32 s72, 0x3e0293ee\ns_lshl_b32 s68, s70, 8\n"
"s_lshl_b32 s69, s93, 5\ns_or_b32 s88, s68, s69\nv_bfe_u32 v1, v0, 2, 2\nv_mul_u32_u24 v4, 0x8100, v1\ns_waitcnt lgkmcnt(0)\ns_mov_b32 s52, s10\ns_and_b32 s53, s11, 0xffff\ns_add_u32 m0, s62, 0\n"
"s_add_u32 s66, s87, 0\nbuffer_load_dwordx4 v62, s[52:55], s66 offen lds\ns_add_u32 s66, s87, 64504\nbuffer_load_dwordx4 v62, s[52:55], s66 offen offset:1032 lds\ns_add_u32 s66, s87, 129008\nbuffer_load_dwordx4 v62, s[52:55], s66 offen offset:2064 lds\ns_add_u32 s66, s87, 193512\nbuffer_load_dwordx4 v62, s[52:55], s66 offen offset:3096 lds\n"
"s_add_u32 s48, s8, s39\ns_addc_u32 s49, s9, 0\ns_and_b32 s49, s49, 0xffff\ns_add_u32 m0, s63, 33024\ns_add_u32 s97, s86, 0\nbuffer_load_dwordx4 v64, s[48:51], s97 offen lds\ns_add_u32 s97, s86, 19456\nbuffer_load_dwordx4 v64, s[48:51], s97 offen offset:1024 lds\n"
"s_add_u32 s97, s86, 38912\nbuffer_load_dwordx4 v64, s[48:51], s97 offen offset:2048 lds\ns_add_u32 s97, s86, 58368\nbuffer_load_dwordx4 v64, s[48:51], s97 offen offset:3072 lds\ns_add_u32 m0, s63, 37120\ns_add_u32 s97, s86, 81920\nbuffer_load_dwordx4 v64, s[48:51], s97 offen lds\ns_add_u32 s97, s86, 101376\n"
"buffer_load_dwordx4 v64, s[48:51], s97 offen offset:1024 lds\ns_add_u32 s97, s86, 120832\nbuffer_load_dwordx4 v64, s[48:51], s97 offen offset:2048 lds\ns_add_u32 m0, s62, 16512\ns_add_u32 s66, s87, 128\nbuffer_load_dwordx4 v62, s[52:55], s66 offen lds\ns_add_u32 s20, s20, s34\ns_addc_u32 s21, s21, s35\n"
"s_mov_b32 s24, s20\ns_and_b32 s25, s21, 0xffff\nbuffer_load_dword v65, v76, s[24:27], 0 offen sc1\ns_add_u32 s40, s12, s36\ns_addc_u32 s41, s13, s37\ns_and_b32 s41, s41, 0xffff\ns_add_u32 s44, s14, s36\ns_addc_u32 s45, s15, s37\n"
"s_add_u32 s66, s87, 64632\nbuffer_load_dwordx4 v62, s[52:55], s66 offen offset:1032 lds\ns_and_b32 s45, s45, 0xffff\ns_mov_b32 s56, s20\ns_and_b32 s57, s21, 0xffff\ns_add_u32 s16, s16, -4096\ns_addc_u32 s17, s17, -1\ns_add_u32 s22, s22, s38\n"
"s_addc_u32 s23, s23, 0\nv_and_b32 v1, 3, v0\nv_lshl_or_b32 v4, v1, 4, v4\nv_and_b32 v1, 1, v2\nv_lshl_or_b32 v4, v1, 7, v4\ns_add_u32 s66, s87, 129136\nbuffer_load_dwordx4 v62, s[52:55], s66 offen offset:2064 lds\nv_lshl_or_b32 v4, v3, 6, v4\n"
"v_xor_b32 v4, s88, v4\nv_add_u32 v69, 1024, v4\nv_bfe_u32 v1, v0, 1, 2\nv_mul_u32_u24 v4, 0x8100, v1\nv_and_b32 v1, 9, v0\nv_lshl_or_b32 v4, v1, 4, v4\nv_lshl_or_b32 v4, v2, 5, v4\nv_xor_b32 v4, s68, v4\n"
"s_add_u32 s66, s87, 193640\nbuffer_load_dwordx4 v62, s[52:55], s66 offen offset:3096 lds\nv_add_u32 v72, 1024, v4\ns_lshl_b32 s68, s70, 12\ns_add_u32 s61, s68, %4\ns_mul_i32 s68, s92, 8320\ns_lshl_b32 s69, s93, 12\ns_add_u32 s68, s68, s69\n"
"s_add_u32 s68, s68, %4\ns_add_u32 s64, s68, 16384\ns_lshl_b32 s69, s93, 11\ns_mov_b64 exec, 0\ns_cmp_eq_u32 s70, 0\ns_cbranch_scc1 .Lpg0\ns_cmp_eq_u32 s70, 1\ns_cbranch_scc1 .Lpg1\n"
"s_cmp_eq_u32 s70, 2\ns_cbranch_scc1 .Lpg2\ns_branch .Lpg3\n.Ltr0:\ns_mov_b64 exec, -1\ns_add_u32 s69, s69, %4\nv_bfe_u32 v3, v0, 3, 1\nv_lshlrev_b32 v4, 1, v3\n"
"v_xor_b32 v4, v4, v2\nv_and_b32 v1, 7, v0\nv_lshl_add_u32 v4, v1, 2, v4\nv_lshlrev_b32 v4, 4, v4\nv_bfe_u32 v1, v0, 2, 2\nv_and_b32 v5, 255, v4\ns_add_u32 m0, s63, 0\ns_add_u32 s97, s86, 128\n"
"buffer_load_dwordx4 v64, s[48:51], s97 offen lds\nv_lshl_or_b32 v1, v1, 12, v5\nv_add_u32 v68, s69, v1\ns_mul_i32 s68, s92, 7168\ns_add_u32 s68, s68, %4\ns_add_u32 s68, s68, 99072\nv_mul_u32_u24 v1, 14336, v3\nv_add3_u32 v63, v1, v4, s68\n"
"v_lshrrev_b32 v3, 5, v0\nv_and_b32 v4, 31, v0\ns_add_u32 s97, s86, 19584\nbuffer_load_dwordx4 v64, s[48:51], s97 offen offset:1024 lds\nv_lshlrev_b32 v4, 3, v4\nv_and_b32 v1, 1, v2\nv_lshl_add_u32 v4, v1, 13, v4\nv_lshl_add_u32 v4, v3, 12, v4\n"
"v_add_u32 v4, 16384, v4\nv_add_u32 v70, s69, v4\nv_add_u32 v71, 66048, v70\ns_mul_i32 s68, s93, 8256\ns_add_u32 s68, s68, %4\ns_add_u32 s97, s86, 39040\nbuffer_load_dwordx4 v64, s[48:51], s97 offen offset:2048 lds\ns_add_u32 s68, s68, 33024\n"
"v_mul_u32_u24 v3, 1032, v2\nv_and_b32 v4, 15, v0\nv_lshlrev_b32 v4, 4, v4\nv_add3_u32 v61, v3, v4, s68\ns_lshl_b32 s68, s70, 2\ns_add_u32 s69, %4, 131968\ns_add_u32 s68, s68, s69\nv_mov_b32 v73, s68\n"
"s_add_u32 s97, s86, 58496\nbuffer_load_dwordx4 v64, s[48:51], s97 offen offset:3072 lds\nv_and_b32 v3, 3, v0\nv_lshl_add_u32 v74, v3, 2, s69\nv_mov_b32 v75, 0\ns_mov_b32 s79, -2031\nds_write_b32 v73, v75\ns_waitcnt lgkmcnt(0)\n"
"s_mul_i32 s91, s70, 7168\ns_add_u32 s91, s91, %4\ns_add_u32 s91, s91, 98304\ns_add_u32 m0, s63, 4096\ns_add_u32 s97, s86, 82048\nbuffer_load_dwordx4 v64, s[48:51], s97 offen lds\ns_lshl_b32 s96, s70, 11\ns_add_u32 s96, s96, %4\n"
"v_and_b32 v3, 1, v0\nv_mul_u32_u24 v3, 0xaaaa, v3\nv_or_b32 v77, 0x4444, v3\ns_and_b32 s68, s19, 4\ns_lshl_b32 s28, 15, s68\ns_mov_b32 s29, 0\ns_and_b32 s68, s19, 4\ns_add_u32 s97, s86, 101504\n"
"buffer_load_dwordx4 v64, s[48:51], s97 offen offset:1024 lds\ns_add_u32 s68, s68, s19\ns_lshl_b32 s71, s68, 18\ns_mul_i32 s69, s70, 0x80100\ns_and_b32 s69, s69, 0x100100\ns_add_u32 s68, s71, s69\ns_lshr_b32 s98, %0, 1\ns_xor_b32 s98, s98, %0\n"
"s_lshl_b32 s98, s98, 11\ns_and_b32 s98, s98, 8192\ns_add_u32 s97, s86, 120960\nbuffer_load_dwordx4 v64, s[48:51], s97 offen offset:2048 lds\ns_add_u32 s68, s68, s98\ns_add_u32 s68, s68, 7168\ns_add_u32 s30, s20, s68\ns_addc_u32 s31, s21, 0\n"
"s_mov_b32 s77, 0\nv_bfe_i32 v8, v0, 3, 1\nv_lshlrev_b32 v4, 10, v0\nv_and_b32 v4, 0x6000, v4\nv_lshrrev_b32 v3, 5, v0\nv_lshl_or_b32 v4, v3, 16, v4\nv_and_b32 v3, 0x70, v60\nv_or_b32 v4, v3, v4\n"
"v_or_b32 v8, 1, v8\nv_mov_b32 v9, v8\nv_mov_b32 v10, v8\nv_mov_b32 v11, v8\nbuffer_store_dwordx4 v[8:11], v4, s[24:27], s68 offen offset:0 sc1\ns_nop 1\ns_lshl_b32 s68, s70, 8\nv_lshl_or_b32 v3, v2, 15, v60\n"
"v_xor_b32 v79, s68, v3\ns_and_b32 s68, s19, 3\ns_lshl_b32 s99, 3072, s68\ns_or_b32 s99, s99, 0x262\ns_getpc_b64 s[100:101]\n.Lpc1:\ns_min_u32 s94, s94, .Lend-.Lpc1-1024\ns_add_u32 s100, s100, s94\n"
"s_addc_u32 s101, s101, 0\ns_mov_b32 s94, 0x7ffff000\ns_mul_i32 s68, %0, 40960\ns_mul_i32 s69, s93, 20480\ns_add_u32 s68, s68, s69\ns_lshl_b32 s69, s92, 10\ns_add_u32 s68, s68, s69\ns_add_u32 s68, s68, 2048\n"
"s_add_u32 s94, s10, s68\ns_addc_u32 s95, s11, 0\ns_and_b32 s95, s95, 0xffff\ns_mov_b32 s75, 17408\n.Lround:\ns_mov_b64 vcc, 0\ns_waitcnt vmcnt(13)\ns_barrier\n"
"Qta 112,61,0\nQta 114,61,512\nQda 208,63,33024\nQda 212,63,34048\nQda 216,63,35072\nQda 220,63,36096\nQda 224,63,37120\nQda 228,63,38144\n"
"Qda 232,63,39168\ns_mov_b64 exec, 0\ns_cmp_eq_u32 s70, 0\ns_cbranch_scc1 .Lpg4\ns_cmp_eq_u32 s70, 1\ns_cbranch_scc1 .Lpg5\ns_cmp_eq_u32 s70, 2\ns_cbranch_scc1 .Lpg6\n"
"s_branch .Lpg7\n.Ltr9:\ns_mov_b64 exec, -1\nQda 176,63,33536\nQta 116,61,4128\nQta 118,61,4640\ns_add_u32 m0, s62, 33024\ns_add_u32 s66, s87, 524288\n"
"buffer_load_dwordx4 v62, s[52:55], s66 offen lds\nQta 120,61,8\nQta 122,61,520\nQta 124,61,4136\ns_add_u32 s66, s87, 588792\nbuffer_load_dwordx4 v62, s[52:55], s66 offen offset:1032 lds\nQta 126,61,4648\nQta 128,61,256\n"
"Qta 130,61,768\ns_add_u32 s66, s87, 653296\nbuffer_load_dwordx4 v62, s[52:55], s66 offen offset:2064 lds\nQta 132,61,4384\nQta 134,61,4896\nQta 136,61,264\ns_add_u32 s66, s87, 717800\nbuffer_load_dwordx4 v62, s[52:55], s66 offen offset:3096 lds\n"
"Qta 138,61,776\nQta 140,61,4392\nQta 142,61,4904\ns_add_u32 m0, s62, 49536\ns_add_u32 s66, s87, 524416\nbuffer_load_dwordx4 v62, s[52:55], s66 offen lds\ns_waitcnt lgkmcnt(15)\nQzvaa 0,112,208\n"
"Qzvaa 4,112,212\ns_add_u32 s66, s87, 588920\nbuffer_load_dwordx4 v62, s[52:55], s66 offen offset:1032 lds\nQda 180,63,34560\nQzvaa 8,112,216\nQda 184,63,35584\ns_add_u32 s66, s87, 653424\nbuffer_load_dwordx4 v62, s[52:55], s66 offen offset:2064 lds\n"
"Qzvaa 12,112,220\nQda 188,63,36608\nQzvaa 16,112,224\ns_add_u32 s66, s87, 717928\nbuffer_load_dwordx4 v62, s[52:55], s66 offen offset:3096 lds\nQda 192,63,37632\nQzvaa 20,112,228\nQda 196,63,38656\n"
"s_mov_b64 exec, 0\ns_cmp_eq_u32 s70, 0\ns_cbranch_scc1 .Lpg8\ns_cmp_eq_u32 s70, 1\ns_cbranch_scc1 .Lpg9\ns_cmp_eq_u32 s70, 2\ns_cbranch_scc1 .Lpg10\ns_branch .Lpg11\n"
".Ltr10:\ns_mov_b64 exec, -1\nQzvaa 52,112,232\nQda 200,63,39680\nv_max3_f32 v0, |v0|, |v1|, |v2|\nv_max3_f32 v4, |v4|, |v5|, |v6|\nv_max3_f32 v8, |v8|, |v9|, |v10|\nv_max3_f32 v12, |v12|, |v13|, |v14|\n"
"v_max3_f32 v16, |v16|, |v17|, |v18|\nv_max3_f32 v20, |v20|, |v21|, |v22|\ns_nop 0\nv_max3_f32 v52, |v52|, |v53|, |v54|\nv_min3_u32 v56, v0, v4, v8\nv_min3_u32 v56, v56, v12, v16\nv_min3_u32 v56, v56, v20, v52\nv_mov_b32 v57, v56\n"
"s_nop 1\nv_permlane32_swap_b32 v56, v57\nv_max_f32 v56, v56, v57\nv_mov_b32 v57, v56\ns_nop 1\nv_permlane16_swap_b32 v56, v57\nv_max_f32 v56, v56, v57\ns_nop 1\n"
"v_min_f32_dpp v56, v56, v56 row_ror:8 row_mask:0xf bank_mask:0xf\ns_nop 1\nv_min_f32_dpp v56, v56, v56 row_ror:4 row_mask:0xf bank_mask:0xf\ns_nop 1\nv_min_f32_dpp v56, v56, v56 row_ror:2 row_mask:0xf bank_mask:0xf\ns_nop 1\nv_min_f32_dpp v56, v56, v56 row_ror:1 row_mask:0xf bank_mask:0xf\nv_and_b32 v56, 0x7f800000, v56\n"
"v_min_u32 v56, 0x78000000, v56\nv_add_u32 v56, 0x5400000, v56\nv_mov_b32 v57, v56\nv_mov_b64 v[58:59], v[56:57]\nv_mul_f32 v52, -v56, s72\n.Lround2:\ns_waitcnt lgkmcnt(6)\nQcvaa 80,112,208,56\n"
"Qk 96,96,97\nQk 97,98,99\nQmvaa 80,116,176\ns_add_u32 s68, s71, 0\nQk 98,100,101\nQcvaa 84,120,208,56\ns_cbranch_vccz .Lst27\nbuffer_store_dwordx4 a[0:3], v79, s[56:59], s68 offen offset:0 sc1\n"
".Lst27:\nQmvaa 84,124,176\nQk 99,102,103\nv_accvgpr_write_b32 a8, v96\nQcvaa 88,128,208,56\ns_add_u32 s68, s71, 16384\nv_accvgpr_write_b32 a9, v97\nQmvaa 88,132,176\n"
"s_cbranch_vccz .Lst26\nbuffer_store_dwordx4 a[4:7], v79, s[56:59], s68 offen offset:0 sc1\n.Lst26:\nQcvaa 92,136,208,56\nv_accvgpr_write_b32 a10, v98\nv_accvgpr_write_b32 a11, v99\nQmvaa 92,140,176\ns_waitcnt lgkmcnt(0)\n"
"Qk 104,104,105\nQcvaa 124,136,212,56\ns_waitcnt vmcnt(16)\ns_barrier\nQmvaa 124,140,180\nQta 144,61,16512\ns_add_u32 m0, s62, 0\ns_add_u32 s69, s87, 1048576\n"
"Qcvaa 120,128,212,56\nbuffer_load_dwordx4 v62, s[52:55], s69 offen lds\nQmvaa 120,132,180\nQta 146,61,17024\nQk 105,106,107\nQcvaa 116,120,212,56\nQk 106,108,109\nQk 107,110,111\n"
"Qmvaa 116,124,180\nQta 148,61,20640\ns_add_u32 s69, s87, 1113080\nQcvaa 112,112,212,56\nbuffer_load_dwordx4 v62, s[52:55], s69 offen offset:1032 lds\nQmvaa 112,116,180\nQta 150,61,21152\nv_accvgpr_write_b32 a12, v104\n"
"Qcvaa 144,112,216,56\nv_accvgpr_write_b32 a13, v105\nv_accvgpr_write_b32 a14, v106\nQmvaa 144,116,184\nQta 152,61,16520\ns_add_u32 s69, s87, 1177584\nQcvaa 148,120,216,56\nbuffer_load_dwordx4 v62, s[52:55], s69 offen offset:2064 lds\n"
"Qmvaa 148,124,184\nQta 154,61,17032\nv_accvgpr_write_b32 a15, v107\nQcvaa 152,128,216,56\nQk 136,136,137\nQk 137,138,139\nQmvaa 152,132,184\nQta 156,61,20648\n"
"s_add_u32 s69, s87, 1242088\nQcvaa 156,136,216,56\nbuffer_load_dwordx4 v62, s[52:55], s69 offen offset:3096 lds\nQmvaa 156,140,184\nQta 158,61,21160\nQk 138,140,141\nQcvaa 188,136,220,56\nQk 139,142,143\n"
"v_accvgpr_write_b32 a28, v136\nQmvaa 188,140,188\nQta 160,61,16768\ns_add_u32 m0, s63, 33024\ns_add_u32 s88, s86, 256\nQcvaa 184,128,220,56\nbuffer_load_dwordx4 v64, s[48:51], s88 offen lds\nQmvaa 184,132,188\n"
"Qta 162,61,17280\nv_accvgpr_write_b32 a29, v137\nQcvaa 180,120,220,56\nv_accvgpr_write_b32 a30, v138\nv_accvgpr_write_b32 a31, v139\nQmvaa 180,124,188\nQta 164,61,20896\ns_add_u32 s88, s86, 19712\n"
"Qcvaa 176,112,220,56\nbuffer_load_dwordx4 v64, s[48:51], s88 offen offset:1024 lds\nQmvaa 176,116,188\nQta 166,61,21408\nQk 128,128,129\nQcvaa 208,112,224,56\nQk 129,130,131\nQk 130,132,133\n"
"Qmvaa 208,116,192\nQta 168,61,16776\ns_add_u32 s88, s86, 39168\nQcvaa 212,120,224,56\nbuffer_load_dwordx4 v64, s[48:51], s88 offen offset:2048 lds\nQmvaa 212,124,192\nQta 170,61,17288\nQk 131,134,135\n"
"Qcvaa 216,128,224,56\nv_accvgpr_write_b32 a24, v128\nv_accvgpr_write_b32 a25, v129\nQmvaa 216,132,192\nQta 172,61,20904\ns_add_u32 s88, s86, 58624\nQcvaa 220,136,224,56\nbuffer_load_dwordx4 v64, s[48:51], s88 offen offset:3072 lds\n"
"Qmvaa 220,140,192\nQta 174,61,21416\nv_accvgpr_write_b32 a26, v130\nQcvaa 252,136,228,56\nv_accvgpr_write_b32 a27, v131\nQk 160,160,161\nQmvaa 252,140,196\ns_add_u32 m0, s63, 37120\n"
"s_add_u32 s88, s86, 82176\nQk 161,162,163\nQcvaa 248,128,228,56\nbuffer_load_dwordx4 v64, s[48:51], s88 offen lds\nQmvaa 248,132,196\nQk 162,164,165\nQk 163,166,167\nQcvaa 244,120,228,56\n"
"v_accvgpr_write_b32 a40, v160\nv_accvgpr_write_b32 a41, v161\nQmvaa 244,124,196\ns_add_u32 s88, s86, 101632\nv_accvgpr_write_b32 a42, v162\nQcvaa 240,112,228,56\nbuffer_load_dwordx4 v64, s[48:51], s88 offen offset:1024 lds\nQmvaa 240,116,196\n"
"v_accvgpr_write_b32 a43, v163\nQk 168,168,169\nQcvaa 16,112,232,56\nQk 169,170,171\nQk 170,172,173\nQmvaa 16,116,200\ns_add_u32 s88, s86, 121088\nQk 171,174,175\n"
"Qcvaa 20,120,232,56\nbuffer_load_dwordx4 v64, s[48:51], s88 offen offset:2048 lds\nQmvaa 20,124,200\nv_accvgpr_write_b32 a44, v168\nv_accvgpr_write_b32 a45, v169\nQcvaa 24,128,232,56\nv_accvgpr_write_b32 a46, v170\nv_accvgpr_write_b32 a47, v171\n"
"Qmvaa 24,132,200\nQk 200,200,201\nQk 201,202,203\nQcvaa 28,136,232,56\nQk 202,204,205\nQk 203,206,207\nQmvaa 28,140,200\nv_accvgpr_write_b32 a60, v200\n"
"v_accvgpr_write_b32 a61, v201\ns_waitcnt lgkmcnt(0)\nQcvaa 96,144,208,56\nv_accvgpr_write_b32 a62, v202\nv_accvgpr_write_b32 a63, v203\nQmvaa 96,148,176\ns_add_u32 s68, s71, 0\nQk 192,192,193\n"
"Qcvaa 100,152,208,56\ns_cbranch_vccz .Lst25\nbuffer_store_dwordx4 a[16:19], v79, s[56:59], s68 offen offset:1024 sc1\n.Lst25:\nQmvaa 100,156,176\nQk 193,194,195\nQk 194,196,197\nQcvaa 104,160,208,56\n"
"s_add_u32 s68, s71, 16384\nQk 195,198,199\nQmvaa 104,164,176\ns_cbranch_vccz .Lst24\nbuffer_store_dwordx4 a[20:23], v79, s[56:59], s68 offen offset:1024 sc1\n.Lst24:\nQcvaa 108,168,208,56\nv_accvgpr_write_b32 a56, v192\n"
"v_accvgpr_write_b32 a57, v193\nQmvaa 108,172,176\n.Lpg0:\ns_cbranch_execz .Ltr0\ns_waitcnt lgkmcnt(0)\nv_accvgpr_write_b32 a58, v194\nQcvaa 140,168,212,56\ns_waitcnt vmcnt(15)\n"
"s_barrier\nQmvaa 140,172,180\nQda 208,63,0\ns_add_u32 m0, s62, 16512\ns_add_u32 s69, s87, 1048704\nQcvaa 136,160,212,56\nbuffer_load_dwordx4 v62, s[52:55], s69 offen lds\nQmvaa 136,164,180\n"
"Qda 176,63,512\nv_accvgpr_write_b32 a59, v195\nQcvaa 132,152,212,56\nQta 112,61,33024\nQk 224,224,225\nQmvaa 132,156,180\ns_add_u32 s69, s87, 1113208\nQk 225,226,227\n"
"Qcvaa 128,144,212,56\nbuffer_load_dwordx4 v62, s[52:55], s69 offen offset:1032 lds\nQmvaa 128,148,180\nQda 212,63,1024\nQk 226,228,229\nQcvaa 160,144,216,56\nQda 180,63,1536\nQk 227,230,231\n"
"Qmvaa 160,148,184\nQta 114,61,33536\ns_add_u32 s69, s87, 1177712\nQcvaa 164,152,216,56\nbuffer_load_dwordx4 v62, s[52:55], s69 offen offset:2064 lds\nQmvaa 164,156,184\nQta 116,61,37152\nv_accvgpr_write_b32 a72, v224\n"
"Qcvaa 168,160,216,56\nv_accvgpr_write_b32 a73, v225\nv_accvgpr_write_b32 a74, v226\nQmvaa 168,164,184\nQta 118,61,37664\ns_add_u32 s69, s87, 1242216\nQcvaa 172,168,216,56\nbuffer_load_dwordx4 v62, s[52:55], s69 offen offset:3096 lds\n"
"Qmvaa 172,172,184\nQda 216,63,2048\nv_accvgpr_write_b32 a75, v227\nQcvaa 204,168,220,56\nQda 184,63,2560\nQk 232,232,233\nQmvaa 204,172,188\nQta 120,61,33032\n"
"s_add_u32 m0, s61, 0\nQcvaa 200,160,220,56\ns_cbranch_vccnz .Ld0q0\nbuffer_load_dwordx4 v69, s[40:43], s60 offen nt lds\n.Ld0q0:\nQmvaa 200,164,188\nQta 122,61,33544\nQk 233,234,235\n"
"Qcvaa 196,152,220,56\nQk 234,236,237\nQk 235,238,239\nQmvaa 196,156,188\nQta 124,61,37160\nv_accvgpr_write_b32 a76, v232\nQcvaa 192,144,220,56\nQda 220,63,3072\n"
"v_accvgpr_write_b32 a77, v233\nQmvaa 192,148,188\nQda 188,63,3584\nv_accvgpr_write_b32 a78, v234\nQcvaa 224,144,224,56\nQta 126,61,37672\nv_accvgpr_write_b32 a79, v235\nQmvaa 224,148,192\n"
"Qk 32,32,33\nQk 33,34,35\nQcvaa 228,152,224,56\nQta 128,61,33280\nQk 34,36,37\nQmvaa 228,156,192\nQk 35,38,39\nv_accvgpr_write_b32 a104, v32\n"
"Qcvaa 232,160,224,56\nQta 130,61,33792\nv_accvgpr_write_b32 a105, v33\nQmvaa 232,164,192\nv_accvgpr_write_b32 a106, v34\nv_accvgpr_write_b32 a107, v35\nQcvaa 236,168,224,56\nQda 224,63,4096\n"
"Qk 40,40,41\nQmvaa 236,172,192\nQda 192,63,4608\nQk 41,42,43\nQcvaa 12,168,228,56\nQta 132,61,37408\nQk 42,44,45\nQmvaa 12,172,196\n"
"Qk 43,46,47\nv_accvgpr_write_b32 a108, v40\nQcvaa 8,160,228,56\nQta 134,61,37920\nv_accvgpr_write_b32 a109, v41\nQmvaa 8,164,196\nv_accvgpr_write_b32 a110, v42\nv_accvgpr_write_b32 a111, v43\n"
"Qcvaa 4,152,228,56\nQta 136,61,33288\nQmvaa 4,156,196\nQcvaa 0,144,228,56\nQda 228,63,5120\nQmvaa 0,148,196\nQda 196,63,5632\nQcvaa 32,144,232,56\n"
"Qta 138,61,33800\nQmvaa 32,148,200\nQcvaa 36,152,232,56\nQta 140,61,37416\nQmvaa 36,156,200\nQcvaa 40,160,232,56\nQta 142,61,37928\nQmvaa 40,164,200\n"
"Qcvaa 44,168,232,56\nQda 232,63,6144\nQmvaa 44,172,200\nQda 200,63,6656\ns_waitcnt lgkmcnt(2)\nQmvaa 80,112,208\nQmvaa 80,116,176\ns_add_u32 s68, s71, 0\n"
"Qmvaa 84,120,208\ns_cbranch_vccz .Lst23\nbuffer_store_dwordx4 a[32:35], v79, s[56:59], s68 offen offset:2048 sc1\n.Lst23:\nQmvaa 84,124,176\nQmvaa 88,128,208\ns_add_u32 s68, s71, 16384\nQmvaa 88,132,176\n"
"s_cbranch_vccz .Lst22\nbuffer_store_dwordx4 a[36:39], v79, s[56:59], s68 offen offset:2048 sc1\n.Lst22:\nQmvaa 92,136,208\nQmvaa 92,140,176\ns_waitcnt lgkmcnt(0)\nQmvaa 124,136,212\ns_waitcnt vmcnt(16)\n"
"s_barrier\nQmvaa 124,140,180\nQta 144,61,49536\ns_add_u32 m0, s62, 33024\ns_add_u32 s69, s87, 1572864\nQmvaa 120,128,212\nbuffer_load_dwordx4 v62, s[52:55], s69 offen lds\nQmvaa 120,132,180\n"
"Qta 146,61,50048\nQmvaa 116,120,212\ns_add_u32 s69, s87, 1637368\nQmvaa 116,124,180\nbuffer_load_dwordx4 v62, s[52:55], s69 offen offset:1032 lds\nQmvaa 112,112,212\nQta 148,61,53664\nQmvaa 112,116,180\n"
"s_add_u32 s69, s87, 1701872\nQmvaa 144,112,216\nbuffer_load_dwordx4 v62, s[52:55], s69 offen offset:2064 lds\nQmvaa 144,116,184\nQta 150,61,54176\nQmvaa 148,120,216\ns_add_u32 s69, s87, 1766376\nQmvaa 148,124,184\n"
"buffer_load_dwordx4 v62, s[52:55], s69 offen offset:3096 lds\nQmvaa 152,128,216\nQta 152,61,49544\nQmvaa 152,132,184\ns_add_u32 m0, s63, 0\ns_add_u32 s88, s86, 384\nQmvaa 156,136,216\nbuffer_load_dwordx4 v64, s[48:51], s88 offen lds\n"
"Qmvaa 156,140,184\nQta 154,61,50056\nQmvaa 188,136,220\ns_add_u32 s88, s86, 19840\nQmvaa 188,140,188\nbuffer_load_dwordx4 v64, s[48:51], s88 offen offset:1024 lds\nQmvaa 184,128,220\nQta 156,61,53672\n"
"Qmvaa 184,132,188\ns_add_u32 s88, s86, 39296\nQmvaa 180,120,220\nbuffer_load_dwordx4 v64, s[48:51], s88 offen offset:2048 lds\nQmvaa 180,124,188\nQta 158,61,54184\nQmvaa 176,112,220\ns_add_u32 s88, s86, 58752\n"
"Qmvaa 176,116,188\nbuffer_load_dwordx4 v64, s[48:51], s88 offen offset:3072 lds\nQmvaa 208,112,224\nQta 160,61,49792\nQmvaa 208,116,192\ns_add_u32 m0, s63, 4096\ns_add_u32 s88, s86, 82304\nQmvaa 212,120,224\n"
"buffer_load_dwordx4 v64, s[48:51], s88 offen lds\nQmvaa 212,124,192\nQta 162,61,50304\nQmvaa 216,128,224\ns_add_u32 s88, s86, 101760\nQmvaa 216,132,192\nbuffer_load_dwordx4 v64, s[48:51], s88 offen offset:1024 lds\nQmvaa 220,136,224\n"
"Qta 164,61,53920\nQmvaa 220,140,192\ns_add_u32 s88, s86, 121216\nQmvaa 252,136,228\nbuffer_load_dwordx4 v64, s[48:51], s88 offen offset:2048 lds\nQmvaa 252,140,196\nQta 166,61,54432\nQmvaa 248,128,228\n"
"s_add_u32 m0, s61, 0\nQmvaa 248,132,196\ns_cbranch_vccnz .Ld0q1\nbuffer_load_dwordx4 v69, s[40:43], s60 offen offset:1024 nt lds\n.Ld0q1:\nQmvaa 244,120,228\nQta 168,61,49800\nQmvaa 244,124,196\n"
"Qmvaa 240,112,228\nQta 170,61,50312\nQmvaa 240,116,196\nQmvaa 16,112,232\nQta 172,61,53928\nQmvaa 16,116,200\nQmvaa 20,120,232\nQta 174,61,54440\n"
"Qmvaa 20,124,200\nQmvaa 24,128,232\nQmvaa 24,132,200\nQmvaa 28,136,232\nQmvaa 28,140,200\ns_waitcnt lgkmcnt(0)\nQmvaa 96,144,208\nQmvaa 96,148,176\n"
"s_add_u32 s68, s71, 0\nQmvaa 100,152,208\ns_cbranch_vccz .Lst21\nbuffer_store_dwordx4 a[48:51], v79, s[56:59], s68 offen offset:3072 sc1\n.Lst21:\nQmvaa 100,156,176\nQmvaa 104,160,208\ns_add_u32 s68, s71, 16384\n"
"Qmvaa 104,164,176\ns_cbranch_vccz .Lst20\nbuffer_store_dwordx4 a[52:55], v79, s[56:59], s68 offen offset:3072 sc1\n.Lst20:\nQmvaa 108,168,208\nQmvaa 108,172,176\ns_waitcnt lgkmcnt(0)\nQmvaa 140,168,212\n"
"s_waitcnt vmcnt(17)\ns_barrier\nQmvaa 140,172,180\nQda 208,63,33024\ns_add_u32 m0, s62, 49536\ns_add_u32 s69, s87, 1572992\nQmvaa 136,160,212\nbuffer_load_dwordx4 v62, s[52:55], s69 offen lds\n"
"Qmvaa 136,164,180\nQda 176,63,33536\nQmvaa 132,152,212\nQta 112,61,0\nQmvaa 132,156,180\ns_add_u32 s69, s87, 1637496\nQmvaa 128,144,212\nbuffer_load_dwordx4 v62, s[52:55], s69 offen offset:1032 lds\n"
"Qmvaa 128,148,180\nQda 212,63,34048\nQmvaa 160,144,216\nQda 180,63,34560\nQmvaa 160,148,184\nQta 114,61,512\ns_add_u32 s69, s87, 1702000\nQmvaa 164,152,216\n"
"buffer_load_dwordx4 v62, s[52:55], s69 offen offset:2064 lds\nQmvaa 164,156,184\nQta 116,61,4128\nQmvaa 168,160,216\nQmvaa 168,164,184\nQta 118,61,4640\ns_add_u32 s69, s87, 1766504\nQmvaa 172,168,216\n"
"buffer_load_dwordx4 v62, s[52:55], s69 offen offset:3096 lds\nQmvaa 172,172,184\nQda 216,63,35072\nQmvaa 204,168,220\nQda 184,63,35584\nQmvaa 204,172,188\nQta 120,61,8\nQmvaa 200,160,220\n"
"Qmvaa 200,164,188\nQta 122,61,520\nQmvaa 196,152,220\nQmvaa 196,156,188\nQta 124,61,4136\nQmvaa 192,144,220\nQda 220,63,36096\nQmvaa 192,148,188\n"
"Qda 188,63,36608\nQmvaa 224,144,224\nQta 126,61,4648\nQmvaa 224,148,192\nQmvaa 228,152,224\nQta 128,61,256\nQmvaa 228,156,192\nQmvaa 232,160,224\n"
"Qta 130,61,768\nQmvaa 232,164,192\nQmvaa 236,168,224\nQda 224,63,37120\nQmvaa 236,172,192\nQda 192,63,37632\nQmvaa 12,168,228\nQta 132,61,4384\n"
"Qmvaa 12,172,196\nQmvaa 8,160,228\nQta 134,61,4896\nQmvaa 8,164,196\nQmvaa 4,152,228\nQta 136,61,264\nQmvaa 4,156,196\nQmvaa 0,144,228\n"
"Qda 228,63,38144\nQmvaa 0,148,196\nQda 196,63,38656\nQmvaa 32,144,232\nQta 138,61,776\nQmvaa 32,148,200\nQmvaa 36,152,232\nQta 140,61,4392\n"
"Qmvaa 36,156,200\nQmvaa 40,160,232\nQta 142,61,4904\nQmvaa 40,164,200\nQmvaa 44,168,232\nQda 232,63,39168\nQmvaa 44,172,200\nQda 200,63,39680\n"
"s_waitcnt lgkmcnt(2)\nQmvaa 80,112,208\nQmvaa 80,116,176\ns_add_u32 s68, s71, 4096\nQmvaa 84,120,208\ns_cbranch_vccz .Lst19\nbuffer_store_dwordx4 a[64:67], v79, s[56:59], s68 offen offset:0 sc1\n.Lst19:\n"
"Qmvaa 84,124,176\nQmvaa 88,128,208\ns_add_u32 s68, s71, 20480\nQmvaa 88,132,176\ns_cbranch_vccz .Lst18\nbuffer_store_dwordx4 a[68:71], v79, s[56:59], s68 offen offset:0 sc1\n.Lst18:\nQmvaa 92,136,208\n"
"Qmvaa 92,140,176\ns_waitcnt lgkmcnt(0)\nQmvaa 124,136,212\ns_waitcnt vmcnt(17)\ns_barrier\nQmvaa 124,140,180\nQta 144,61,16512\ns_add_u32 m0, s62, 0\n"
"s_add_u32 s69, s87, 2097152\nQmvaa 120,128,212\nbuffer_load_dwordx4 v62, s[52:55], s69 offen lds\nQmvaa 120,132,180\nQta 146,61,17024\nQmvaa 116,120,212\ns_add_u32 s69, s87, 2161656\nQmvaa 116,124,180\n"
"buffer_load_dwordx4 v62, s[52:55], s69 offen offset:1032 lds\nQmvaa 112,112,212\nQta 148,61,20640\nQmvaa 112,116,180\ns_add_u32 s69, s87, 2226160\nQmvaa 144,112,216\nbuffer_load_dwordx4 v62, s[52:55], s69 offen offset:2064 lds\nQmvaa 144,116,184\n"
"Qta 150,61,21152\nQmvaa 148,120,216\ns_add_u32 s69, s87, 2290664\nQmvaa 148,124,184\nbuffer_load_dwordx4 v62, s[52:55], s69 offen offset:3096 lds\nQmvaa 152,128,216\nQta 152,61,16520\nQmvaa 152,132,184\n"
"s_add_u32 m0, s63, 33024\ns_add_u32 s88, s86, 512\nQmvaa 156,136,216\nbuffer_load_dwordx4 v64, s[48:51], s88 offen lds\nQmvaa 156,140,184\nQta 154,61,17032\nQmvaa 188,136,220\ns_add_u32 s88, s86, 19968\n"
"Qmvaa 188,140,188\nbuffer_load_dwordx4 v64, s[48:51], s88 offen offset:1024 lds\nQmvaa 184,128,220\nQta 156,61,20648\nQmvaa 184,132,188\ns_add_u32 s88, s86, 39424\nQmvaa 180,120,220\nbuffer_load_dwordx4 v64, s[48:51], s88 offen offset:2048 lds\n"
"Qmvaa 180,124,188\nQta 158,61,21160\nQmvaa 176,112,220\ns_add_u32 s88, s86, 58880\nQmvaa 176,116,188\nbuffer_load_dwordx4 v64, s[48:51], s88 offen offset:3072 lds\nQmvaa 208,112,224\nQta 160,61,16768\n"
"Qmvaa 208,116,192\ns_add_u32 m0, s63, 37120\ns_add_u32 s88, s86, 82432\nQmvaa 212,120,224\nbuffer_load_dwordx4 v64, s[48:51], s88 offen lds\nQmvaa 212,124,192\nQta 162,61,17280\nQmvaa 216,128,224\n"
"s_add_u32 s88, s86, 101888\nQmvaa 216,132,192\nbuffer_load_dwordx4 v64, s[48:51], s88 offen offset:1024 lds\nQmvaa 220,136,224\nQta 164,61,20896\nQmvaa 220,140,192\ns_add_u32 s88, s86, 121344\nQmvaa 252,136,228\n"
"buffer_load_dwordx4 v64, s[48:51], s88 offen offset:2048 lds\nQmvaa 252,140,196\nQta 166,61,21408\nQmvaa 248,128,228\ns_add_u32 m0, s61, 0\nQmvaa 248,132,196\ns_cbranch_vccnz .Ld0q2\nbuffer_load_dwordx4 v69, s[40:43], s90 offen offset:2048 nt lds\n"
".Ld0q2:\nQmvaa 244,120,228\nQta 168,61,16776\nQmvaa 244,124,196\nQmvaa 240,112,228\nQta 170,61,17288\nQmvaa 240,116,196\nQmvaa 16,112,232\n"
"Qta 172,61,20904\nQmvaa 16,116,200\nQmvaa 20,120,232\nQta 174,61,21416\nQmvaa 20,124,200\nQmvaa 24,128,232\nQmvaa 24,132,200\nQmvaa 28,136,232\n"
"Qmvaa 28,140,200\ns_waitcnt lgkmcnt(0)\nQmvaa 96,144,208\nQmvaa 96,148,176\ns_add_u32 s68, s71, 4096\nQmvaa 100,152,208\ns_cbranch_vccz .Lst17\nbuffer_store_dwordx4 a[80:83], v79, s[56:59], s68 offen offset:1024 sc1\n"
".Lst17:\nQmvaa 100,156,176\nQmvaa 104,160,208\ns_add_u32 s68, s71, 20480\nQmvaa 104,164,176\ns_cbranch_vccz .Lst16\nbuffer_store_dwordx4 a[84:87], v79, s[56:59], s68 offen offset:1024 sc1\n.Lst16:\n"
"Qmvaa 108,168,208\nQmvaa 108,172,176\ns_waitcnt lgkmcnt(0)\nQmvaa 140,168,212\ns_waitcnt vmcnt(17)\ns_barrier\nQmvaa 140,172,180\nQda 208,63,0\n"
"s_add_u32 m0, s62, 16512\ns_add_u32 s69, s87, 2097280\nQmvaa 136,160,212\nbuffer_load_dwordx4 v62, s[52:55], s69 offen lds\nQmvaa 136,164,180\nQda 176,63,512\nQmvaa 132,152,212\nQta 112,61,33024\n"
"Qmvaa 132,156,180\ns_add_u32 s69, s87, 2161784\nQmvaa 128,144,212\nbuffer_load_dwordx4 v62, s[52:55], s69 offen offset:1032 lds\nQmvaa 128,148,180\nQda 212,63,1024\nQmvaa 160,144,216\nQda 180,63,1536\n"
"Qmvaa 160,148,184\nQta 114,61,33536\ns_add_u32 s69, s87, 2226288\nQmvaa 164,152,216\nbuffer_load_dwordx4 v62, s[52:55], s69 offen offset:2064 lds\nQmvaa 164,156,184\nQta 116,61,37152\nQmvaa 168,160,216\n"
"Qmvaa 168,164,184\nQta 118,61,37664\ns_add_u32 s69, s87, 2290792\nQmvaa 172,168,216\nbuffer_load_dwordx4 v62, s[52:55], s69 offen offset:3096 lds\nQmvaa 172,172,184\nQda 216,63,2048\nQmvaa 204,168,220\n"
"Qda 184,63,2560\nQmvaa 204,172,188\nQta 120,61,33032\ns_add_u32 m0, s61, 0\nQmvaa 200,160,220\ns_cbranch_vccnz .Ld0q3\nbuffer_load_dwordx4 v69, s[40:43], s90 offen offset:3072 nt lds\n.Ld0q3:\n"
"Qmvaa 200,164,188\nQta 122,61,33544\nQmvaa 196,152,220\nQmvaa 196,156,188\nQta 124,61,37160\nQmvaa 192,144,220\nQda 220,63,3072\nQmvaa 192,148,188\n"
"Qda 188,63,3584\nQmvaa 224,144,224\nQta 126,61,37672\nQmvaa 224,148,192\nQmvaa 228,152,224\nQta 128,61,33280\nQmvaa 228,156,192\nQmvaa 232,160,224\n"
"Qta 130,61,33792\nQmvaa 232,164,192\nQmvaa 236,168,224\n.Lpg1:\ns_cbranch_execz .Ltr0\nQda 224,63,4096\nQmvaa 236,172,192\nQda 192,63,4608\n"
"Qmvaa 12,168,228\nQta 132,61,37408\nQmvaa 12,172,196\nQmvaa 8,160,228\nQta 134,61,37920\nQmvaa 8,164,196\nQmvaa 4,152,228\nQta 136,61,33288\n"
"Qmvaa 4,156,196\nQmvaa 0,144,228\nQda 228,63,5120\nQmvaa 0,148,196\nQda 196,63,5632\nQmvaa 32,144,232\nQta 138,61,33800\nQmvaa 32,148,200\n"
"Qmvaa 36,152,232\nQta 140,61,37416\nQmvaa 36,156,200\nQmvaa 40,160,232\nQta 142,61,37928\nQmvaa 40,164,200\nQmvaa 44,168,232\nQda 232,63,6144\n"
"Qmvaa 44,172,200\nQda 200,63,6656\ns_waitcnt lgkmcnt(2)\nQmvaa 80,112,208\nQmvaa 80,116,176\ns_add_u32 s68, s71, 135168\nQmvaa 84,120,208\ns_cbranch_vccz .Lst15\n"
"buffer_store_dwordx4 a[88:91], v79, s[56:59], s68 offen offset:1024 sc1\n.Lst15:\nQmvaa 84,124,176\nQmvaa 88,128,208\ns_add_u32 s68, s71, 151552\nQmvaa 88,132,176\ns_cbranch_vccz .Lst14\nbuffer_store_dwordx4 a[92:95], v79, s[56:59], s68 offen offset:1024 sc1\n"
".Lst14:\nQmvaa 92,136,208\nQmvaa 92,140,176\ns_waitcnt lgkmcnt(0)\nQmvaa 124,136,212\ns_waitcnt vmcnt(17)\ns_barrier\nQmvaa 124,140,180\n"
"Qta 144,61,49536\ns_add_u32 m0, s62, 33024\ns_add_u32 s69, s87, 2621440\nQmvaa 120,128,212\nbuffer_load_dwordx4 v62, s[52:55], s69 offen lds\nQmvaa 120,132,180\nQta 146,61,50048\ns_mov_b32 s89, 0\n"
"Qmvaa 116,120,212\ns_cbranch_vccnz .Lpb0\ns_load_dword s89, s[30:31], 0x0\n.Lpb0:\nQmvaa 116,124,180\nQta 148,61,53664\ns_add_u32 s69, s87, 2685944\nQmvaa 112,112,212\n"
"buffer_load_dwordx4 v62, s[52:55], s69 offen offset:1032 lds\nQmvaa 112,116,180\nQta 150,61,54176\nQmvaa 144,112,216\ns_add_u32 s77, s77, s89\nQmvaa 144,116,184\nQta 152,61,49544\ns_add_u32 s69, s87, 2750448\n"
"Qmvaa 148,120,216\nbuffer_load_dwordx4 v62, s[52:55], s69 offen offset:2064 lds\nQmvaa 148,124,184\nQta 154,61,50056\nQmvaa 152,128,216\ns_add_u32 s77, s77, s89\nQmvaa 152,132,184\nQta 156,61,53672\n"
"s_add_u32 s69, s87, 2814952\nQmvaa 156,136,216\nbuffer_load_dwordx4 v62, s[52:55], s69 offen offset:3096 lds\nQmvaa 156,140,184\nQta 158,61,54184\nQmvaa 188,136,220\ns_add_u32 s77, s77, s89\nQmvaa 188,140,188\n"
"Qta 160,61,49792\ns_add_u32 m0, s63, 0\ns_add_u32 s88, s86, 640\nQmvaa 184,128,220\nbuffer_load_dwordx4 v64, s[48:51], s88 offen lds\nQmvaa 184,132,188\nQta 162,61,50304\nQmvaa 180,120,220\n"
"s_add_u32 s77, s77, s89\nQmvaa 180,124,188\nQta 164,61,53920\ns_add_u32 s88, s86, 20096\nQmvaa 176,112,220\nbuffer_load_dwordx4 v64, s[48:51], s88 offen offset:1024 lds\nQmvaa 176,116,188\nQta 166,61,54432\n"
"Qmvaa 208,112,224\ns_add_u32 s77, s77, s89\nQmvaa 208,116,192\nQta 168,61,49800\ns_add_u32 s88, s86, 39552\nQmvaa 212,120,224\nbuffer_load_dwordx4 v64, s[48:51], s88 offen offset:2048 lds\nQmvaa 212,124,192\n"
"Qta 170,61,50312\nQmvaa 216,128,224\ns_add_u32 s77, s77, s89\nQmvaa 216,132,192\nQta 172,61,53928\ns_add_u32 s88, s86, 59008\nQmvaa 220,136,224\nbuffer_load_dwordx4 v64, s[48:51], s88 offen offset:3072 lds\n"
"Qmvaa 220,140,192\nQta 174,61,54440\nQmvaa 252,136,228\ns_add_u32 s77, s77, s89\nQmvaa 252,140,196\ns_add_u32 m0, s63, 4096\ns_add_u32 s88, s86, 82560\nQmvaa 248,128,228\n"
"buffer_load_dwordx4 v64, s[48:51], s88 offen lds\nQmvaa 248,132,196\nQmvaa 244,120,228\ns_add_u32 s77, s77, s89\nQmvaa 244,124,196\ns_add_u32 s88, s86, 102016\nQmvaa 240,112,228\nbuffer_load_dwordx4 v64, s[48:51], s88 offen offset:1024 lds\n"
"Qmvaa 240,116,196\nQmvaa 16,112,232\ns_add_u32 s77, s77, s89\nQmvaa 16,116,200\ns_add_u32 s88, s86, 121472\nQmvaa 20,120,232\nbuffer_load_dwordx4 v64, s[48:51], s88 offen offset:2048 lds\nQmvaa 20,124,200\n"
"Qmvaa 24,128,232\ns_add_u32 s77, s77, s89\nQmvaa 24,132,200\nQmvaa 28,136,232\nQmvaa 28,140,200\ns_waitcnt lgkmcnt(0)\nQmvaa 96,144,208\nQmvaa 96,148,176\n"
"s_add_u32 s68, s71, 4096\nQmvaa 100,152,208\ns_cbranch_vccz .Lst13\nbuffer_store_dwordx4 a[96:99], v79, s[56:59], s68 offen offset:2048 sc1\n.Lst13:\nQmvaa 100,156,176\nQmvaa 104,160,208\ns_add_u32 s68, s71, 20480\n"
"Qmvaa 104,164,176\ns_cbranch_vccz .Lst12\nbuffer_store_dwordx4 a[100:103], v79, s[56:59], s68 offen offset:2048 sc1\n.Lst12:\nQmvaa 108,168,208\nQmvaa 108,172,176\ns_waitcnt lgkmcnt(0)\nQmvaa 140,168,212\n"
"s_waitcnt vmcnt(17)\ns_barrier\nQmvaa 140,172,180\nQda 208,63,33024\ns_add_u32 m0, s62, 49536\ns_add_u32 s69, s87, 2621568\nQmvaa 136,160,212\nbuffer_load_dwordx4 v62, s[52:55], s69 offen lds\n"
"Qmvaa 136,164,180\nQda 176,63,33536\nQmvaa 132,152,212\nQta 112,61,0\nQmvaa 132,156,180\ns_add_u32 s69, s87, 2686072\nQmvaa 128,144,212\nbuffer_load_dwordx4 v62, s[52:55], s69 offen offset:1032 lds\n"
"Qmvaa 128,148,180\nQda 212,63,34048\nQmvaa 160,144,216\nQda 180,63,34560\nQmvaa 160,148,184\nQta 114,61,512\ns_add_u32 s69, s87, 2750576\nQmvaa 164,152,216\n"
"buffer_load_dwordx4 v62, s[52:55], s69 offen offset:2064 lds\nQmvaa 164,156,184\nQta 116,61,4128\nQmvaa 168,160,216\nQmvaa 168,164,184\nQta 118,61,4640\ns_add_u32 s69, s87, 2815080\nQmvaa 172,168,216\n"
"buffer_load_dwordx4 v62, s[52:55], s69 offen offset:3096 lds\nQmvaa 172,172,184\nQda 216,63,35072\nQmvaa 204,168,220\nQda 184,63,35584\nQmvaa 204,172,188\nQta 120,61,8\ns_add_u32 m0, s64, 0\n"
"Qmvaa 200,160,220\ns_cbranch_vccnz .Ld0q4\nbuffer_load_dwordx4 v72, s[44:47], s60 offen nt lds\n.Ld0q4:\nQmvaa 200,164,188\nQta 122,61,520\nQmvaa 196,152,220\nQmvaa 196,156,188\n"
"Qta 124,61,4136\nQmvaa 192,144,220\nQda 220,63,36096\nQmvaa 192,148,188\nQda 188,63,36608\nQmvaa 224,144,224\nQta 126,61,4648\nQmvaa 224,148,192\n"
"Qmvaa 228,152,224\nQta 128,61,256\nQmvaa 228,156,192\nQmvaa 232,160,224\nQta 130,61,768\nQmvaa 232,164,192\nQmvaa 236,168,224\nQda 224,63,37120\n"
"Qmvaa 236,172,192\nQda 192,63,37632\nQmvaa 12,168,228\nQta 132,61,4384\nQmvaa 12,172,196\nQmvaa 8,160,228\nQta 134,61,4896\nQmvaa 8,164,196\n"
"Qmvaa 4,152,228\nQta 136,61,264\nQmvaa 4,156,196\nQmvaa 0,144,228\nQda 228,63,38144\nQmvaa 0,148,196\nQda 196,63,38656\nQmvaa 32,144,232\n"
"Qta 138,61,776\nQmvaa 32,148,200\nQmvaa 36,152,232\nQta 140,61,4392\nQmvaa 36,156,200\nQmvaa 40,160,232\nQta 142,61,4904\nQmvaa 40,164,200\n"
"Qmvaa 44,168,232\nQda 232,63,39168\nQmvaa 44,172,200\nQda 200,63,39680\ns_waitcnt lgkmcnt(2)\nQmvaa 80,112,208\nQmvaa 80,116,176\ns_add_u32 s68, s71, 131072\n"
"Qmvaa 84,120,208\ns_cbranch_vccz .Lst11\nbuffer_store_dwordx4 a[8:11], v79, s[56:59], s68 offen offset:0 sc1\n.Lst11:\nQmvaa 84,124,176\nQmvaa 88,128,208\ns_add_u32 s68, s71, 147456\nQmvaa 88,132,176\n"
"s_cbranch_vccz .Lst10\nbuffer_store_dwordx4 a[12:15], v79, s[56:59], s68 offen offset:0 sc1\n.Lst10:\nQmvaa 92,136,208\nQmvaa 92,140,176\ns_waitcnt lgkmcnt(0)\nQmvaa 124,136,212\ns_waitcnt vmcnt(17)\n"
"s_barrier\nQmvaa 124,140,180\nQta 144,61,16512\ns_add_u32 m0, s62, 0\ns_add_u32 s69, s87, 3145728\nQmvaa 120,128,212\nbuffer_load_dwordx4 v62, s[52:55], s69 offen lds\nQmvaa 120,132,180\n"
"Qta 146,61,17024\ns_mov_b32 s89, 0\nQmvaa 116,120,212\ns_cbranch_vccnz .Lpb1\ns_load_dword s89, s[30:31], 0x2000\n.Lpb1:\nQmvaa 116,124,180\nQta 148,61,20640\n"
"s_add_u32 s69, s87, 3210232\nQmvaa 112,112,212\nbuffer_load_dwordx4 v62, s[52:55], s69 offen offset:1032 lds\nQmvaa 112,116,180\nQta 150,61,21152\nQmvaa 144,112,216\ns_add_u32 s77, s77, s89\nQmvaa 144,116,184\n"
"Qta 152,61,16520\ns_add_u32 s69, s87, 3274736\nQmvaa 148,120,216\nbuffer_load_dwordx4 v62, s[52:55], s69 offen offset:2064 lds\nQmvaa 148,124,184\nQta 154,61,17032\nQmvaa 152,128,216\ns_add_u32 s77, s77, s89\n"
"Qmvaa 152,132,184\nQta 156,61,20648\ns_add_u32 s69, s87, 3339240\nQmvaa 156,136,216\nbuffer_load_dwordx4 v62, s[52:55], s69 offen offset:3096 lds\nQmvaa 156,140,184\nQta 158,61,21160\nQmvaa 188,136,220\n"
"s_add_u32 s77, s77, s89\nQmvaa 188,140,188\nQta 160,61,16768\ns_add_u32 m0, s63, 33024\ns_add_u32 s88, s86, 768\nQmvaa 184,128,220\nbuffer_load_dwordx4 v64, s[48:51], s88 offen lds\nQmvaa 184,132,188\n"
"Qta 162,61,17280\nQmvaa 180,120,220\ns_add_u32 s77, s77, s89\nQmvaa 180,124,188\nQta 164,61,20896\ns_add_u32 s88, s86, 20224\nQmvaa 176,112,220\nbuffer_load_dwordx4 v64, s[48:51], s88 offen offset:1024 lds\n"
"Qmvaa 176,116,188\nQta 166,61,21408\nQmvaa 208,112,224\ns_add_u32 s77, s77, s89\nQmvaa 208,116,192\nQta 168,61,16776\ns_add_u32 s88, s86, 39680\nQmvaa 212,120,224\n"
"buffer_load_dwordx4 v64, s[48:51], s88 offen offset:2048 lds\nQmvaa 212,124,192\nQta 170,61,17288\nQmvaa 216,128,224\ns_add_u32 s77, s77, s89\nQmvaa 216,132,192\nQta 172,61,20904\ns_add_u32 s88, s86, 59136\n"
"Qmvaa 220,136,224\nbuffer_load_dwordx4 v64, s[48:51], s88 offen offset:3072 lds\nQmvaa 220,140,192\nQta 174,61,21416\nQmvaa 252,136,228\ns_add_u32 s77, s77, s89\nQmvaa 252,140,196\ns_add_u32 m0, s63, 37120\n"
"s_add_u32 s88, s86, 82688\nQmvaa 248,128,228\nbuffer_load_dwordx4 v64, s[48:51], s88 offen lds\nQmvaa 248,132,196\nQmvaa 244,120,228\ns_add_u32 s77, s77, s89\nQmvaa 244,124,196\ns_add_u32 s88, s86, 102144\n"
"Qmvaa 240,112,228\nbuffer_load_dwordx4 v64, s[48:51], s88 offen offset:1024 lds\nQmvaa 240,116,196\nQmvaa 16,112,232\ns_add_u32 s77, s77, s89\nQmvaa 16,116,200\ns_add_u32 s88, s86, 121600\nQmvaa 20,120,232\n"
"buffer_load_dwordx4 v64, s[48:51], s88 offen offset:2048 lds\nQmvaa 20,124,200\nQmvaa 24,128,232\ns_add_u32 s77, s77, s89\nQmvaa 24,132,200\nQmvaa 28,136,232\nQmvaa 28,140,200\ns_waitcnt lgkmcnt(0)\n"
"Qmvaa 96,144,208\nQmvaa 96,148,176\ns_add_u32 s68, s71, 131072\nQmvaa 100,152,208\ns_cbranch_vccz .Lst9\nbuffer_store_dwordx4 a[24:27], v79, s[56:59], s68 offen offset:1024 sc1\n.Lst9:\nQmvaa 100,156,176\n"
"Qmvaa 104,160,208\ns_add_u32 s68, s71, 147456\nQmvaa 104,164,176\ns_cbranch_vccz .Lst8\nbuffer_store_dwordx4 a[28:31], v79, s[56:59], s68 offen offset:1024 sc1\n.Lst8:\nQmvaa 108,168,208\nQmvaa 108,172,176\n"
"s_waitcnt lgkmcnt(0)\nQmvaa 140,168,212\ns_waitcnt vmcnt(16)\ns_barrier\nQmvaa 140,172,180\nQda 208,63,0\ns_add_u32 m0, s62, 16512\ns_add_u32 s69, s87, 3145856\n"
"Qmvaa 136,160,212\nbuffer_load_dwordx4 v62, s[52:55], s69 offen lds\nQmvaa 136,164,180\nQda 176,63,512\nQmvaa 132,152,212\nQta 112,61,33024\nQmvaa 132,156,180\ns_add_u32 s69, s87, 3210360\n"
"Qmvaa 128,144,212\nbuffer_load_dwordx4 v62, s[52:55], s69 offen offset:1032 lds\nQmvaa 128,148,180\nQda 212,63,1024\nQmvaa 160,144,216\nQda 180,63,1536\nQmvaa 160,148,184\nQta 114,61,33536\n"
"s_add_u32 s69, s87, 3274864\nQmvaa 164,152,216\nbuffer_load_dwordx4 v62, s[52:55], s69 offen offset:2064 lds\nQmvaa 164,156,184\nQta 116,61,37152\nQmvaa 168,160,216\nQmvaa 168,164,184\nQta 118,61,37664\n"
"s_add_u32 s69, s87, 3339368\nQmvaa 172,168,216\nbuffer_load_dwordx4 v62, s[52:55], s69 offen offset:3096 lds\nQmvaa 172,172,184\nQda 216,63,2048\nQmvaa 204,168,220\nQda 184,63,2560\nQmvaa 204,172,188\n"
"Qta 120,61,33032\ns_add_u32 m0, s64, 0\nQmvaa 200,160,220\ns_cbranch_vccnz .Ld0q5\nbuffer_load_dwordx4 v72, s[44:47], s60 offen offset:1024 nt lds\n.Ld0q5:\nQmvaa 200,164,188\nQta 122,61,33544\n"
"Qmvaa 196,152,220\nQmvaa 196,156,188\nQta 124,61,37160\nQmvaa 192,144,220\nQda 220,63,3072\nQmvaa 192,148,188\nQda 188,63,3584\nQmvaa 224,144,224\n"
"Qta 126,61,37672\nQmvaa 224,148,192\nQmvaa 228,152,224\nQta 128,61,33280\nQmvaa 228,156,192\nQmvaa 232,160,224\nQta 130,61,33792\nQmvaa 232,164,192\n"
"Qmvaa 236,168,224\nQda 224,63,4096\nQmvaa 236,172,192\nQda 192,63,4608\nQmvaa 12,168,228\nQta 132,61,37408\nQmvaa 12,172,196\nQmvaa 8,160,228\n"
"Qta 134,61,37920\nQmvaa 8,164,196\nQmvaa 4,152,228\nQta 136,61,33288\nQmvaa 4,156,196\nQmvaa 0,144,228\nQda 228,63,5120\nQmvaa 0,148,196\n"
"Qda 196,63,5632\nQmvaa 32,144,232\nQta 138,61,33800\nQmvaa 32,148,200\nQmvaa 36,152,232\nQta 140,61,37416\nQmvaa 36,156,200\nQmvaa 40,160,232\n"
"Qta 142,61,37928\nQmvaa 40,164,200\nQmvaa 44,168,232\nQda 232,63,6144\nQmvaa 44,172,200\nQda 200,63,6656\ns_waitcnt lgkmcnt(2)\nQmvaa 80,112,208\n"
"Qmvaa 80,116,176\ns_add_u32 s68, s71, 131072\nQmvaa 84,120,208\ns_cbranch_vccz .Lst7\nbuffer_store_dwordx4 a[40:43], v79, s[56:59], s68 offen offset:2048 sc1\n.Lst7:\nQmvaa 84,124,176\nQmvaa 88,128,208\n"
"s_add_u32 s68, s71, 147456\nQmvaa 88,132,176\ns_cbranch_vccz .Lst6\nbuffer_store_dwordx4 a[44:47], v79, s[56:59], s68 offen offset:2048 sc1\n.Lst6:\nQmvaa 92,136,208\nQmvaa 92,140,176\ns_waitcnt lgkmcnt(0)\n"
"Qmvaa 124,136,212\ns_waitcnt vmcnt(17)\ns_barrier\nQmvaa 124,140,180\nQta 144,61,49536\ns_add_u32 m0, s62, 33024\ns_add_u32 s69, s87, 3670016\nQmvaa 120,128,212\n"
"buffer_load_dwordx4 v62, s[52:55], s69 offen lds\nQmvaa 120,132,180\nQta 146,61,50048\ns_mov_b32 s89, 0\nQmvaa 116,120,212\ns_cbranch_vccnz .Lpb2\ns_load_dword s89, s[30:31], 0x6000\n.Lpb2:\n"
"Qmvaa 116,124,180\nQta 148,61,53664\ns_add_u32 s69, s87, 3734520\nQmvaa 112,112,212\nbuffer_load_dwordx4 v62, s[52:55], s69 offen offset:1032 lds\nQmvaa 112,116,180\nQta 150,61,54176\nQmvaa 144,112,216\n"
"s_add_u32 s77, s77, s89\nQmvaa 144,116,184\nQta 152,61,49544\ns_add_u32 s69, s87, 3799024\nQmvaa 148,120,216\nbuffer_load_dwordx4 v62, s[52:55], s69 offen offset:2064 lds\nQmvaa 148,124,184\nQta 154,61,50056\n"
"Qmvaa 152,128,216\ns_add_u32 s77, s77, s89\nQmvaa 152,132,184\nQta 156,61,53672\ns_add_u32 s69, s87, 3863528\nQmvaa 156,136,216\nbuffer_load_dwordx4 v62, s[52:55], s69 offen offset:3096 lds\nQmvaa 156,140,184\n"
"Qta 158,61,54184\nQmvaa 188,136,220\ns_add_u32 s77, s77, s89\nQmvaa 188,140,188\n.Lpg2:\ns_cbranch_execz .Ltr0\nQta 160,61,49792\ns_add_u32 m0, s63, 0\n"
"s_add_u32 s88, s86, 896\nQmvaa 184,128,220\nbuffer_load_dwordx4 v64, s[48:51], s88 offen lds\nQmvaa 184,132,188\nQta 162,61,50304\nQmvaa 180,120,220\ns_add_u32 s77, s77, s89\nQmvaa 180,124,188\n"
"Qta 164,61,53920\ns_add_u32 s88, s86, 20352\nQmvaa 176,112,220\nbuffer_load_dwordx4 v64, s[48:51], s88 offen offset:1024 lds\nQmvaa 176,116,188\nQta 166,61,54432\nQmvaa 208,112,224\ns_add_u32 s77, s77, s89\n"
"Qmvaa 208,116,192\nQta 168,61,49800\ns_add_u32 s88, s86, 39808\nQmvaa 212,120,224\nbuffer_load_dwordx4 v64, s[48:51], s88 offen offset:2048 lds\nQmvaa 212,124,192\nQta 170,61,50312\nQmvaa 216,128,224\n"
"s_add_u32 s77, s77, s89\nQmvaa 216,132,192\nQta 172,61,53928\ns_add_u32 s88, s86, 59264\nQmvaa 220,136,224\nbuffer_load_dwordx4 v64, s[48:51], s88 offen offset:3072 lds\nQmvaa 220,140,192\nQta 174,61,54440\n"
"Qmvaa 252,136,228\ns_add_u32 s77, s77, s89\nQmvaa 252,140,196\ns_add_u32 m0, s63, 4096\ns_add_u32 s88, s86, 82816\nQmvaa 248,128,228\nbuffer_load_dwordx4 v64, s[48:51], s88 offen lds\nQmvaa 248,132,196\n"
"Qmvaa 244,120,228\ns_add_u32 s77, s77, s89\nQmvaa 244,124,196\ns_add_u32 s88, s86, 102272\nQmvaa 240,112,228\nbuffer_load_dwordx4 v64, s[48:51], s88 offen offset:1024 lds\nQmvaa 240,116,196\nQmvaa 16,112,232\n"
"s_add_u32 s77, s77, s89\nQmvaa 16,116,200\ns_add_u32 s88, s86, 121728\nQmvaa 20,120,232\nbuffer_load_dwordx4 v64, s[48:51], s88 offen offset:2048 lds\nQmvaa 20,124,200\nQmvaa 24,128,232\ns_add_u32 s77, s77, s89\n"
"Qmvaa 24,132,200\nQmvaa 28,136,232\nQmvaa 28,140,200\ns_waitcnt lgkmcnt(0)\nQmvaa 96,144,208\nQmvaa 96,148,176\ns_add_u32 s68, s71, 131072\nQmvaa 100,152,208\n"
"s_cbranch_vccz .Lst5\nbuffer_store_dwordx4 a[56:59], v79, s[56:59], s68 offen offset:3072 sc1\n.Lst5:\nQmvaa 100,156,176\nQmvaa 104,160,208\ns_add_u32 s68, s71, 147456\nQmvaa 104,164,176\ns_cbranch_vccz .Lst4\n"
"buffer_store_dwordx4 a[60:63], v79, s[56:59], s68 offen offset:3072 sc1\n.Lst4:\nQmvaa 108,168,208\nQmvaa 108,172,176\ns_waitcnt lgkmcnt(0)\nQmvaa 140,168,212\ns_waitcnt vmcnt(16)\ns_barrier\n"
"Qmvaa 140,172,180\nQda 208,63,33024\ns_add_u32 m0, s62, 49536\ns_add_u32 s69, s87, 3670144\nQmvaa 136,160,212\nbuffer_load_dwordx4 v62, s[52:55], s69 offen lds\nQmvaa 136,164,180\nQda 176,63,33536\n"
"Qmvaa 132,152,212\nQta 112,61,0\nQmvaa 132,156,180\ns_add_u32 s69, s87, 3734648\nQmvaa 128,144,212\nbuffer_load_dwordx4 v62, s[52:55], s69 offen offset:1032 lds\nQmvaa 128,148,180\nQda 212,63,34048\n"
"Qmvaa 160,144,216\nQda 180,63,34560\nQmvaa 160,148,184\nQta 114,61,512\ns_add_u32 s69, s87, 3799152\nQmvaa 164,152,216\nbuffer_load_dwordx4 v62, s[52:55], s69 offen offset:2064 lds\nQmvaa 164,156,184\n"
"Qta 116,61,4128\nQmvaa 168,160,216\nQmvaa 168,164,184\nQta 118,61,4640\ns_add_u32 s69, s87, 3863656\nQmvaa 172,168,216\nbuffer_load_dwordx4 v62, s[52:55], s69 offen offset:3096 lds\nQmvaa 172,172,184\n"
"Qda 216,63,35072\nQmvaa 204,168,220\nQda 184,63,35584\nQmvaa 204,172,188\nQta 120,61,8\ns_add_u32 m0, s64, 0\nQmvaa 200,160,220\ns_cbranch_vccnz .Ld0q6\n"
"buffer_load_dwordx4 v72, s[44:47], s90 offen offset:2048 nt lds\n.Ld0q6:\nQmvaa 200,164,188\nQta 122,61,520\nQmvaa 196,152,220\nQmvaa 196,156,188\nQta 124,61,4136\nQmvaa 192,144,220\n"
"Qda 220,63,36096\nQmvaa 192,148,188\nQda 188,63,36608\nQmvaa 224,144,224\nQta 126,61,4648\nQmvaa 224,148,192\nQmvaa 228,152,224\nQta 128,61,256\n"
"Qmvaa 228,156,192\nQmvaa 232,160,224\nQta 130,61,768\nQmvaa 232,164,192\nQmvaa 236,168,224\nQda 224,63,37120\nQmvaa 236,172,192\nQda 192,63,37632\n"
"Qmvaa 12,168,228\nQta 132,61,4384\nQmvaa 12,172,196\nQmvaa 8,160,228\nQta 134,61,4896\nQmvaa 8,164,196\nQmvaa 4,152,228\nQta 136,61,264\n"
"Qmvaa 4,156,196\nQmvaa 0,144,228\nQda 228,63,38144\nQmvaa 0,148,196\nQda 196,63,38656\nQmvaa 32,144,232\nQta 138,61,776\nQmvaa 32,148,200\n"
"Qmvaa 36,152,232\nQta 140,61,4392\nQmvaa 36,156,200\nQmvaa 40,160,232\nQta 142,61,4904\nQmvaa 40,164,200\nQmvaa 44,168,232\nQda 232,63,39168\n"
"Qmvaa 44,172,200\nQda 200,63,39680\ns_waitcnt lgkmcnt(2)\nQmvaa 80,112,208\nQmvaa 80,116,176\ns_add_u32 s68, s71, 135168\nQmvaa 84,120,208\ns_cbranch_vccz .Lst3\n"
"buffer_store_dwordx4 a[72:75], v79, s[56:59], s68 offen offset:0 sc1\n.Lst3:\nQmvaa 84,124,176\nQmvaa 88,128,208\ns_add_u32 s68, s71, 151552\nQmvaa 88,132,176\ns_cbranch_vccz .Lst2\nbuffer_store_dwordx4 a[76:79], v79, s[56:59], s68 offen offset:0 sc1\n"
".Lst2:\nQmvaa 92,136,208\nQmvaa 92,140,176\ns_waitcnt lgkmcnt(0)\nQmvaa 124,136,212\ns_waitcnt vmcnt(17)\ns_barrier\nQmvaa 124,140,180\n"
"Qta 144,61,16512\ns_add_u32 m0, s62, 0\ns_add_u32 s69, s87, 4194304\nQmvaa 120,128,212\nbuffer_load_dwordx4 v62, s[52:55], s69 offen lds\nQmvaa 120,132,180\nQta 146,61,17024\ns_mov_b32 s89, 0\n"
"Qmvaa 116,120,212\ns_cbranch_vccnz .Lpb3\ns_load_dword s89, s[30:31], 0x4000\n.Lpb3:\nQmvaa 116,124,180\nQta 148,61,20640\ns_add_u32 s69, s87, 4258808\nQmvaa 112,112,212\n"
"buffer_load_dwordx4 v62, s[52:55], s69 offen offset:1032 lds\nQmvaa 112,116,180\nQta 150,61,21152\nQmvaa 144,112,216\ns_add_u32 s77, s77, s89\nQmvaa 144,116,184\nQta 152,61,16520\ns_add_u32 s69, s87, 4323312\n"
"Qmvaa 148,120,216\nbuffer_load_dwordx4 v62, s[52:55], s69 offen offset:2064 lds\nQmvaa 148,124,184\nQta 154,61,17032\nQmvaa 152,128,216\ns_add_u32 s77, s77, s89\nQmvaa 152,132,184\nQta 156,61,20648\n"
"s_add_u32 s69, s87, 4387816\nQmvaa 156,136,216\nbuffer_load_dwordx4 v62, s[52:55], s69 offen offset:3096 lds\nQmvaa 156,140,184\nQta 158,61,21160\nQmvaa 188,136,220\ns_add_u32 s77, s77, s89\nQmvaa 188,140,188\n"
"Qta 160,61,16768\ns_add_u32 m0, s63, 33024\ns_add_u32 s88, s86, 1024\nQmvaa 184,128,220\nbuffer_load_dwordx4 v64, s[48:51], s88 offen lds\nQmvaa 184,132,188\nQta 162,61,17280\nQmvaa 180,120,220\n"
"s_add_u32 s77, s77, s89\nQmvaa 180,124,188\nQta 164,61,20896\ns_add_u32 s88, s86, 20480\nQmvaa 176,112,220\nbuffer_load_dwordx4 v64, s[48:51], s88 offen offset:1024 lds\nQmvaa 176,116,188\nQta 166,61,21408\n"
"Qmvaa 208,112,224\ns_add_u32 s77, s77, s89\nQmvaa 208,116,192\nQta 168,61,16776\ns_add_u32 s88, s86, 39936\nQmvaa 212,120,224\nbuffer_load_dwordx4 v64, s[48:51], s88 offen offset:2048 lds\nQmvaa 212,124,192\n"
"Qta 170,61,17288\nQmvaa 216,128,224\ns_add_u32 s77, s77, s89\nQmvaa 216,132,192\nQta 172,61,20904\ns_add_u32 s88, s86, 59392\nQmvaa 220,136,224\nbuffer_load_dwordx4 v64, s[48:51], s88 offen offset:3072 lds\n"
"Qmvaa 220,140,192\nQta 174,61,21416\nQmvaa 252,136,228\ns_add_u32 s77, s77, s89\nQmvaa 252,140,196\ns_add_u32 m0, s63, 37120\ns_add_u32 s88, s86, 82944\nQmvaa 248,128,228\n"
"buffer_load_dwordx4 v64, s[48:51], s88 offen lds\nQmvaa 248,132,196\nQmvaa 244,120,228\ns_add_u32 s77, s77, s89\nQmvaa 244,124,196\ns_add_u32 s88, s86, 102400\nQmvaa 240,112,228\nbuffer_load_dwordx4 v64, s[48:51], s88 offen offset:1024 lds\n"
"Qmvaa 240,116,196\nQmvaa 16,112,232\ns_add_u32 s77, s77, s89\nQmvaa 16,116,200\ns_add_u32 s88, s86, 121856\nQmvaa 20,120,232\nbuffer_load_dwordx4 v64, s[48:51], s88 offen offset:2048 lds\nQmvaa 20,124,200\n"
"Qmvaa 24,128,232\ns_add_u32 s77, s77, s89\nQmvaa 24,132,200\nQmvaa 28,136,232\nQmvaa 28,140,200\ns_waitcnt lgkmcnt(0)\nQmvaa 96,144,208\nQmvaa 96,148,176\n"
"s_add_u32 s68, s71, 135168\nQmvaa 100,152,208\ns_cbranch_vccz .Lst1\nbuffer_store_dwordx4 a[104:107], v79, s[56:59], s68 offen offset:2048 sc1\n.Lst1:\nQmvaa 100,156,176\nQmvaa 104,160,208\ns_add_u32 s68, s71, 151552\n"
"Qmvaa 104,164,176\ns_cbranch_vccz .Lst0\nbuffer_store_dwordx4 a[108:111], v79, s[56:59], s68 offen offset:2048 sc1\n.Lst0:\nQmvaa 108,168,208\nQmvaa 108,172,176\ns_waitcnt lgkmcnt(0)\nQmvaa 140,168,212\n"
"s_waitcnt vmcnt(16)\ns_barrier\nQmvaa 140,172,180\nQda 208,63,0\ns_add_u32 m0, s62, 16512\ns_add_u32 s69, s87, 4194432\nQmvaa 136,160,212\nbuffer_load_dwordx4 v62, s[52:55], s69 offen lds\n"
"Qmvaa 136,164,180\nQda 176,63,512\nQmvaa 132,152,212\nQta 112,61,33024\nQmvaa 132,156,180\ns_add_u32 s69, s87, 4258936\nQmvaa 128,144,212\nbuffer_load_dwordx4 v62, s[52:55], s69 offen offset:1032 lds\n"
"Qmvaa 128,148,180\nQda 212,63,1024\nQmvaa 160,144,216\nQda 180,63,1536\nQmvaa 160,148,184\nQta 114,61,33536\ns_add_u32 s69, s87, 4323440\nQmvaa 164,152,216\n"
"buffer_load_dwordx4 v62, s[52:55], s69 offen offset:2064 lds\nQmvaa 164,156,184\nQta 116,61,37152\nQmvaa 168,160,216\nQmvaa 168,164,184\nQta 118,61,37664\ns_add_u32 s69, s87, 4387944\nQmvaa 172,168,216\n"
"buffer_load_dwordx4 v62, s[52:55], s69 offen offset:3096 lds\nQmvaa 172,172,184\nQda 216,63,2048\nQmvaa 204,168,220\nQda 184,63,2560\nQmvaa 204,172,188\nQta 120,61,33032\ns_add_u32 m0, s64, 0\n"
"Qmvaa 200,160,220\ns_cbranch_vccnz .Ld0q7\nbuffer_load_dwordx4 v72, s[44:47], s90 offen offset:3072 nt lds\ns_add_u32 s65, s65, 2048\ns_xor_b32 s60, s65, %1\ns_add_u32 s90, s60, 260096\n.Ld0q7:\nQmvaa 200,164,188\n"
"Qta 122,61,33544\nQmvaa 196,152,220\nQmvaa 196,156,188\nQta 124,61,37160\nQmvaa 192,144,220\nQda 220,63,3072\nQmvaa 192,148,188\nQda 188,63,3584\n"
"Qmvaa 224,144,224\nQta 126,61,37672\nQmvaa 224,148,192\nQmvaa 228,152,224\nQta 128,61,33280\nQmvaa 228,156,192\nQmvaa 232,160,224\nQta 130,61,33792\n"
"Qmvaa 232,164,192\nQmvaa 236,168,224\nQda 224,63,4096\nQmvaa 236,172,192\nQda 192,63,4608\nQmvaa 12,168,228\nQta 132,61,37408\nQmvaa 12,172,196\n"
"Qmvaa 8,160,228\nQta 134,61,37920\nQmvaa 8,164,196\nQmvaa 4,152,228\nQta 136,61,33288\nQmvaa 4,156,196\nQmvaa 0,144,228\nQda 228,63,5120\n"
"Qmvaa 0,148,196\nQda 196,63,5632\nQmvaa 32,144,232\nQta 138,61,33800\nQmvaa 32,148,200\nQmvaa 36,152,232\nQta 140,61,37416\nQmvaa 36,156,200\n"
"Qmvaa 40,160,232\nQta 142,61,37928\nQmvaa 40,164,200\nQmvaa 44,168,232\nQda 232,63,6144\nQmvaa 44,172,200\nQda 200,63,6656\ns_waitcnt lgkmcnt(2)\n"
"Qmvaa 80,112,208\nQmvaa 80,116,176\nQmvaa 84,120,208\nQmvaa 84,124,176\nQmvaa 88,128,208\nQmvaa 88,132,176\nQmvaa 92,136,208\nQmvaa 92,140,176\n"
"s_waitcnt lgkmcnt(0)\nQmvaa 124,136,212\ns_waitcnt vmcnt(17)\ns_barrier\nQmvaa 124,140,180\nQta 144,61,49536\ns_add_u32 m0, s62, 33024\ns_add_u32 s69, s87, 4718592\n"
"Qmvaa 120,128,212\nbuffer_load_dwordx4 v62, s[52:55], s69 offen lds\nQmvaa 120,132,180\nQta 146,61,50048\nQmvaa 116,120,212\nQmvaa 116,124,180\nQta 148,61,53664\ns_add_u32 s69, s87, 4783096\n"
"Qmvaa 112,112,212\nbuffer_load_dwordx4 v62, s[52:55], s69 offen offset:1032 lds\nQmvaa 112,116,180\nQta 150,61,54176\nQmvaa 144,112,216\nQmvaa 144,116,184\nQta 152,61,49544\ns_add_u32 s69, s87, 4847600\n"
"Qmvaa 148,120,216\nbuffer_load_dwordx4 v62, s[52:55], s69 offen offset:2064 lds\nQmvaa 148,124,184\nQta 154,61,50056\nQmvaa 152,128,216\nQmvaa 152,132,184\nQta 156,61,53672\ns_add_u32 s69, s87, 4912104\n"
"Qmvaa 156,136,216\nbuffer_load_dwordx4 v62, s[52:55], s69 offen offset:3096 lds\nQmvaa 156,140,184\nQta 158,61,54184\nQmvaa 188,136,220\nQmvaa 188,140,188\nQta 160,61,49792\ns_add_u32 m0, s63, 0\n"
"s_add_u32 s88, s86, 1152\nQmvaa 184,128,220\nbuffer_load_dwordx4 v64, s[48:51], s88 offen lds\nQmvaa 184,132,188\nQta 162,61,50304\nQmvaa 180,120,220\nQmvaa 180,124,188\nQta 164,61,53920\n"
"s_add_u32 s88, s86, 20608\nQmvaa 176,112,220\nbuffer_load_dwordx4 v64, s[48:51], s88 offen offset:1024 lds\nQmvaa 176,116,188\nQta 166,61,54432\nQmvaa 208,112,224\nQmvaa 208,116,192\nQta 168,61,49800\n"
"s_add_u32 s88, s86, 40064\nQmvaa 212,120,224\nbuffer_load_dwordx4 v64, s[48:51], s88 offen offset:2048 lds\nQmvaa 212,124,192\nQta 170,61,50312\nQmvaa 216,128,224\nQmvaa 216,132,192\nQta 172,61,53928\n"
"s_add_u32 s88, s86, 59520\nQmvaa 220,136,224\nbuffer_load_dwordx4 v64, s[48:51], s88 offen offset:3072 lds\nQmvaa 220,140,192\nQta 174,61,54440\nQmvaa 252,136,228\nQmvaa 252,140,196\ns_add_u32 m0, s63, 4096\n"
"s_add_u32 s88, s86, 83072\nQmvaa 248,128,228\nbuffer_load_dwordx4 v64, s[48:51], s88 offen lds\nQmvaa 248,132,196\nv_mov_b32 v66, s77\nQmvaa 244,120,228\nds_write_b32 v73, v66 offset:16\nQmvaa 244,124,196\n"
"s_add_u32 s88, s86, 102528\nQmvaa 240,112,228\nbuffer_load_dwordx4 v64, s[48:51], s88 offen offset:1024 lds\nQmvaa 240,116,196\nQmvaa 16,112,232\nQmvaa 16,116,200\ns_add_u32 s88, s86, 121984\nQmvaa 20,120,232\n"
"buffer_load_dwordx4 v64, s[48:51], s88 offen offset:2048 lds\nQmvaa 20,124,200\nQmvaa 24,128,232\nQmvaa 24,132,200\nQmvaa 28,136,232\nQmvaa 28,140,200\ns_waitcnt lgkmcnt(1)\nQmvaa 96,144,208\n"
"Qmvaa 96,148,176\nQmvaa 100,152,208\nQmvaa 100,156,176\nQmvaa 104,160,208\nQmvaa 104,164,176\nQmvaa 108,168,208\nQmvaa 108,172,176\ns_waitcnt lgkmcnt(0)\n"
"Qmvaa 140,168,212\ns_waitcnt vmcnt(16)\ns_barrier\nQmvaa 140,172,180\nQda 208,63,33024\ns_add_u32 m0, s62, 49536\ns_add_u32 s69, s87, 4718720\nQmvaa 136,160,212\n"
"buffer_load_dwordx4 v62, s[52:55], s69 offen lds\nQmvaa 136,164,180\nQda 176,63,33536\nQmvaa 132,152,212\nQta 112,61,0\nQmvaa 132,156,180\ns_add_u32 s69, s87, 4783224\nQmvaa 128,144,212\n"
"buffer_load_dwordx4 v62, s[52:55], s69 offen offset:1032 lds\nQmvaa 128,148,180\nQda 212,63,34048\nQmvaa 160,144,216\nQda 180,63,34560\nQmvaa 160,148,184\nQta 114,61,512\ns_add_u32 s69, s87, 4847728\n"
"Qmvaa 164,152,216\nbuffer_load_dwordx4 v62, s[52:55], s69 offen offset:2064 lds\n.Lpg3:\ns_cbranch_execz .Ltr0\nQmvaa 164,156,184\nQta 116,61,4128\nQmvaa 168,160,216\nQmvaa 168,164,184\n"
"Qta 118,61,4640\ns_add_u32 s69, s87, 4912232\nQmvaa 172,168,216\nbuffer_load_dwordx4 v62, s[52:55], s69 offen offset:3096 lds\nQmvaa 172,172,184\nQda 216,63,35072\nQmvaa 204,168,220\nQda 184,63,35584\n"
"Qmvaa 204,172,188\nQta 120,61,8\nQmvaa 200,160,220\nQmvaa 200,164,188\nQta 122,61,520\nQmvaa 196,152,220\nQmvaa 196,156,188\nQta 124,61,4136\n"
"Qmvaa 192,144,220\nQda 220,63,36096\nQmvaa 192,148,188\nQda 188,63,36608\nQmvaa 224,144,224\nQta 126,61,4648\nQmvaa 224,148,192\nQmvaa 228,152,224\n"
"Qta 128,61,256\nQmvaa 228,156,192\nQmvaa 232,160,224\nQta 130,61,768\nQmvaa 232,164,192\nQmvaa 236,168,224\nQda 224,63,37120\nQmvaa 236,172,192\n"
"Qda 192,63,37632\nQmvaa 12,168,228\nQta 132,61,4384\nQmvaa 12,172,196\nQmvaa 8,160,228\nQta 134,61,4896\nQmvaa 8,164,196\nQmvaa 4,152,228\n"
"Qta 136,61,264\nQmvaa 4,156,196\nQmvaa 0,144,228\nQda 228,63,38144\nQmvaa 0,148,196\nQda 196,63,38656\nQmvaa 32,144,232\nQta 138,61,776\n"
"Qmvaa 32,148,200\nQmvaa 36,152,232\nQta 140,61,4392\nQmvaa 36,156,200\nQmvaa 40,160,232\nQta 142,61,4904\nQmvaa 40,164,200\nQmvaa 44,168,232\n"
"Qda 232,63,39168\nQmvaa 44,172,200\nQda 200,63,39680\ns_waitcnt lgkmcnt(2)\nQmvaa 80,112,208\nQmvaa 80,116,176\nQmvaa 84,120,208\nQmvaa 84,124,176\n"
"Qmvaa 88,128,208\nQmvaa 88,132,176\nQmvaa 92,136,208\nQmvaa 92,140,176\ns_waitcnt lgkmcnt(0)\nQmvaa 124,136,212\ns_waitcnt vmcnt(15)\ns_barrier\n"
"Qmvaa 124,140,180\nQta 144,61,16512\ns_mov_b32 s56, s94\ns_mov_b32 s57, s95\ns_mov_b32 s58, s75\ns_mov_b32 s94, 0\ns_add_u32 m0, %4, 160768\nQmvaa 120,128,212\n"
"buffer_load_dwordx4 v60, s[56:59], s94 offen lds\nQmvaa 120,132,180\nQta 146,61,17024\nQmvaa 116,120,212\nQmvaa 116,124,180\nQta 148,61,20640\ns_add_u32 s94, s94, 4096\nQmvaa 112,112,212\n"
"buffer_load_dwordx4 v60, s[56:59], s94 offen lds\nQmvaa 112,116,180\nQta 150,61,21152\nQmvaa 144,112,216\nQmvaa 144,116,184\nQta 152,61,16520\ns_add_u32 s94, s94, 4096\nQmvaa 148,120,216\n"
"buffer_load_dwordx4 v60, s[56:59], s94 offen lds\nQmvaa 148,124,184\nQta 154,61,17032\nQmvaa 152,128,216\nQmvaa 152,132,184\nQta 156,61,20648\ns_add_u32 s94, s94, 4096\nQmvaa 156,136,216\n"
"buffer_load_dwordx4 v60, s[56:59], s94 offen lds\nQmvaa 156,140,184\nQta 158,61,21160\nQmvaa 188,136,220\nQmvaa 188,140,188\nQta 160,61,16768\ns_add_u32 s94, s94, 4096\nQmvaa 184,128,220\n"
"buffer_load_dwordx4 v60, s[56:59], s94 offen lds\nQmvaa 184,132,188\nQta 162,61,17280\nQmvaa 180,120,220\nQmvaa 180,124,188\nQta 164,61,20896\ns_add_u32 s94, s94, 4096\nQmvaa 176,112,220\n"
"buffer_load_dwordx4 v60, s[56:59], s94 offen lds\nQmvaa 176,116,188\nQta 166,61,21408\nQmvaa 208,112,224\nQmvaa 208,116,192\nQta 168,61,16776\ns_add_u32 s94, s94, 4096\nQmvaa 212,120,224\n"
"buffer_load_dwordx4 v60, s[56:59], s94 offen lds\nQmvaa 212,124,192\nQta 170,61,17288\nQmvaa 216,128,224\nQmvaa 216,132,192\nQta 172,61,20904\ns_add_u32 s94, s94, 4096\nQmvaa 220,136,224\n"
"buffer_load_dwordx4 v60, s[56:59], s94 offen lds\nQmvaa 220,140,192\nQta 174,61,21416\nQmvaa 252,136,228\nQmvaa 252,140,196\ns_add_u32 s94, s94, 4096\nQmvaa 248,128,228\nbuffer_load_dwordx4 v60, s[56:59], s94 offen lds\n"
"Qmvaa 248,132,196\nQmvaa 244,120,228\nQmvaa 244,124,196\ns_add_u32 s94, s94, 4096\nQmvaa 240,112,228\nbuffer_load_dwordx4 v60, s[56:59], s94 offen lds\nQmvaa 240,116,196\nv_mov_b32 v55, 0x3f803f80\n"
"s_mov_b32 vcc_lo, 0x33333333\nQmvaa 16,112,232\ns_mov_b32 vcc_hi, 0xcccccccc\nv_cndmask_b32 v55, 0, v55, vcc\nQmvaa 16,116,200\nv_accvgpr_write_b32 a204, v55\nv_accvgpr_write_b32 a205, v55\nQmvaa 20,120,232\n"
"v_accvgpr_write_b32 a206, v55\nv_accvgpr_write_b32 a207, v55\nQmvaa 20,124,200\ns_add_u32 s87, s87, 2048\nv_mov_b64 v[54:55], 0\nQmvaa 24,128,232\nglobal_load_dword v78, v60, s[100:101]\nQmvaa 24,132,200\n"
"Qmvaa 28,136,232\nQmvaa 28,140,200\ns_waitcnt lgkmcnt(0)\nQmvaa 96,144,208\nQmvaa 96,148,176\nQmvaa 100,152,208\nQmvaa 100,156,176\nQmvaa 104,160,208\n"
"Qmvaa 104,164,176\nQmvaa 108,168,208\nQmvaa 108,172,176\ns_waitcnt lgkmcnt(0)\nQmvaa 140,168,212\ns_waitcnt vmcnt(15)\ns_barrier\nQmvaa 140,172,180\n"
"Qda 208,63,0\ns_add_u32 m0, s61, 33024\nQmvaa 136,160,212\nbuffer_load_dwordx4 v69, s[40:43], s60 offen nt lds\nQmvaa 136,164,180\nQda 176,63,512\nQmvaa 132,152,212\nQta 112,61,33024\n"
"Qmvaa 132,156,180\nQtv 48,70,128\nQmvaa 128,144,212\nbuffer_load_dwordx4 v69, s[40:43], s60 offen offset:1024 nt lds\nQmvaa 128,148,180\nQda 212,63,1024\nQmvaa 160,144,216\nQda 180,63,1536\n"
"Qmvaa 160,148,184\nQta 114,61,33536\nQmvaa 164,152,216\nbuffer_load_dwordx4 v69, s[40:43], s90 offen offset:2048 nt lds\nQmvaa 164,156,184\nQta 116,61,37152\nQmvaa 168,160,216\nQtv 50,70,1152\n"
"Qmvaa 168,164,184\nQta 118,61,37664\nQmvaa 172,168,216\nbuffer_load_dwordx4 v69, s[40:43], s90 offen offset:3072 nt lds\nQmvaa 172,172,184\nQda 216,63,2048\nQmvaa 204,168,220\nQda 184,63,2560\n"
"Qmvaa 204,172,188\nQta 120,61,33032\ns_add_u32 m0, s64, 33024\nQmvaa 200,160,220\nbuffer_load_dwordx4 v72, s[44:47], s60 offen nt lds\nQmvaa 200,164,188\nQta 122,61,33544\ns_waitcnt lgkmcnt(5)\n"
"Qmvaa 196,152,220\nv_max_f32 v66, |v48|, |v50|\nQmvaa 196,156,188\nQta 124,61,37160\nQmvaa 192,144,220\nbuffer_load_dwordx4 v72, s[44:47], s60 offen offset:1024 nt lds\nQmvaa 192,148,188\nQda 220,63,3072\n"
"v_mov_b32 v67, v66\nQmvaa 224,144,224\nQda 188,63,3584\nv_permlane32_swap_b32 v66, v67\nQmvaa 224,148,192\nQta 126,61,37672\nv_max_f32 v66, v66, v67\nQmvaa 228,152,224\n"
"buffer_load_dwordx4 v72, s[44:47], s90 offen offset:2048 nt lds\nQmvaa 228,156,192\nQta 128,61,33280\nv_mov_b32 v67, v66\nQmvaa 232,160,224\ns_nop 0\nv_permlane16_swap_b32 v66, v67\nQmvaa 232,164,192\n"
"Qta 130,61,33792\nv_max_f32 v66, v66, v67\nQmvaa 236,168,224\nbuffer_load_dwordx4 v72, s[44:47], s90 offen offset:3072 nt lds\ns_add_u32 s65, s65, 2048\ns_xor_b32 s60, s65, %1\ns_add_u32 s90, s60, 260096\nQmvaa 236,172,192\n"
"Qda 224,63,4096\nv_min_f32_dpp v66, v66, v66 row_ror:8 row_mask:0xf bank_mask:0xf\nQmvaa 12,168,228\nQda 192,63,4608\nv_min_f32_dpp v66, v66, v66 row_ror:4 row_mask:0xf bank_mask:0xf\nQmvaa 12,172,196\nQta 132,61,37408\nQmvaa 8,160,228\n"
"v_min_f32_dpp v66, v66, v66 row_ror:2 row_mask:0xf bank_mask:0xf\nQmvaa 8,164,196\nQta 134,61,37920\nv_min_f32_dpp v66, v66, v66 row_ror:1 row_mask:0xf bank_mask:0xf\nQmvaa 4,152,228\nQmvaa 4,156,196\nQta 136,61,33288\nv_and_b32 v66, 0x7f000000, v66\n"
"Qmvaa 0,144,228\nQda 228,63,5120\nv_or_b32 v66, 0x400000, v66\nQmvaa 0,148,196\nQda 196,63,5632\nQmvaa 32,144,232\nQta 138,61,33800\nv_mov_b32 v67, 0\n"
"Qmvaa 32,148,200\nv_mov_b32 v58, 0x39800000\nQmvaa 36,152,232\nQta 140,61,37416\nQmvaa 36,156,200\nv_mov_b32 v59, 0\nQmvaa 40,160,232\nQta 142,61,37928\n"
"v_readfirstlane_b32 s73, v66\nQmvaa 40,164,200\nQmvaa 44,168,232\nQda 232,63,6144\nQmvaa 44,172,200\nQda 200,63,6656\ns_waitcnt lgkmcnt(2)\nQmvaa 80,112,208\n"
"Qmvaa 80,116,176\nQmvaa 84,120,208\nQmvaa 84,124,176\nQmvaa 88,128,208\nQmvaa 88,132,176\nQmvaa 92,136,208\nQmvaa 92,140,176\ns_waitcnt lgkmcnt(0)\n"
"Qmvaa 124,136,212\ns_waitcnt vmcnt(19)\ns_barrier\nQmvaa 124,140,180\nQta 144,61,49536\nv_fma_f32 v80, v80, s72, v52\nQmvaa 120,128,212\nv_fma_f32 v81, v81, s72, v52\n"
"v_fma_f32 v82, v82, s72, v52\nQmvaa 120,132,180\nQta 146,61,50048\nv_fma_f32 v83, v83, s72, v52\nQmvaa 116,120,212\nv_fma_f32 v84, v84, s72, v52\nv_fma_f32 v85, v85, s72, v52\nQmvaa 116,124,180\n"
"Qta 148,61,53664\nv_fma_f32 v86, v86, s72, v52\nQmvaa 112,112,212\nv_fma_f32 v87, v87, s72, v52\nQk 80,80,81\nQmvaa 112,116,180\nQta 150,61,54176\nQk 81,82,83\n"
"Qmvaa 144,112,216\nQk 82,84,85\nQk 83,86,87\nQmvaa 144,116,184\nQta 152,61,49544\nv_accvgpr_write_b32 a0, v80\nQmvaa 148,120,216\nv_accvgpr_write_b32 a1, v81\n"
"v_accvgpr_write_b32 a2, v82\nQmvaa 148,124,184\nQta 154,61,50056\nv_accvgpr_write_b32 a3, v83\nQmvaa 152,128,216\nv_fma_f32 v88, v88, s72, v52\nv_fma_f32 v89, v89, s72, v52\nQmvaa 152,132,184\n"
"Qta 156,61,53672\nv_fma_f32 v90, v90, s72, v52\nQmvaa 156,136,216\nv_fma_f32 v91, v91, s72, v52\nv_fma_f32 v92, v92, s72, v52\nQmvaa 156,140,184\nQta 158,61,54184\nv_fma_f32 v93, v93, s72, v52\n"
"Qmvaa 188,136,220\nv_fma_f32 v94, v94, s72, v52\nv_fma_f32 v95, v95, s72, v52\nQmvaa 188,140,188\nQta 160,61,49792\nQk 88,88,89\nQmvaa 184,128,220\nQk 89,90,91\n"
"Qk 90,92,93\nQmvaa 184,132,188\nQta 162,61,50304\nQk 91,94,95\nQmvaa 180,120,220\nv_accvgpr_write_b32 a4, v88\nv_accvgpr_write_b32 a5, v89\nQmvaa 180,124,188\n"
"Qta 164,61,53920\nv_accvgpr_write_b32 a6, v90\nQmvaa 176,112,220\nv_accvgpr_write_b32 a7, v91\nv_fma_f32 v120, v120, s72, v52\nQmvaa 176,116,188\nQta 166,61,54432\nv_fma_f32 v121, v121, s72, v52\n"
"Qmvaa 208,112,224\nv_fma_f32 v122, v122, s72, v52\nv_fma_f32 v123, v123, s72, v52\nQmvaa 208,116,192\nQta 168,61,49800\nv_fma_f32 v124, v124, s72, v52\nQmvaa 212,120,224\nv_fma_f32 v125, v125, s72, v52\n"
"v_fma_f32 v126, v126, s72, v52\nQmvaa 212,124,192\nQta 170,61,50312\nv_fma_f32 v127, v127, s72, v52\nQmvaa 216,128,224\nQk 120,120,121\nQk 121,122,123\nQmvaa 216,132,192\n"
"Qta 172,61,53928\nQk 122,124,125\nQmvaa 220,136,224\nQk 123,126,127\nv_accvgpr_write_b32 a20, v120\nQmvaa 220,140,192\nQta 174,61,54440\nv_accvgpr_write_b32 a21, v121\n"
"Qmvaa 252,136,228\nv_accvgpr_write_b32 a22, v122\nv_accvgpr_write_b32 a23, v123\nQmvaa 252,140,196\nv_fma_f32 v112, v112, s72, v52\nv_fma_f32 v113, v113, s72, v52\nQmvaa 248,128,228\nv_fma_f32 v114, v114, s72, v52\n"
"v_fma_f32 v115, v115, s72, v52\nQmvaa 248,132,196\nv_fma_f32 v116, v116, s72, v52\nv_fma_f32 v117, v117, s72, v52\nQmvaa 244,120,228\nv_fma_f32 v118, v118, s72, v52\nv_fma_f32 v119, v119, s72, v52\nQmvaa 244,124,196\n"
"Qk 112,112,113\nQk 113,114,115\nQmvaa 240,112,228\nQk 114,116,117\nQk 115,118,119\nQmvaa 240,116,196\nv_accvgpr_write_b32 a16, v112\nv_accvgpr_write_b32 a17, v113\n"
"Qmvaa 16,112,232\nv_accvgpr_write_b32 a18, v114\nv_accvgpr_write_b32 a19, v115\nQmvaa 16,116,200\nv_fma_f32 v144, v144, s72, v52\nv_fma_f32 v145, v145, s72, v52\nQmvaa 20,120,232\nv_fma_f32 v146, v146, s72, v52\n"
"v_fma_f32 v147, v147, s72, v52\nQmvaa 20,124,200\nv_fma_f32 v148, v148, s72, v52\nv_fma_f32 v149, v149, s72, v52\nQmvaa 24,128,232\nv_fma_f32 v150, v150, s72, v52\nv_fma_f32 v151, v151, s72, v52\nQmvaa 24,132,200\n"
"Qk 144,144,145\nQk 145,146,147\nQmvaa 28,136,232\nQk 146,148,149\nQk 147,150,151\nQmvaa 28,140,200\nv_accvgpr_write_b32 a32, v144\nv_accvgpr_write_b32 a33, v145\n"
"s_waitcnt lgkmcnt(0)\nQmvaa 96,144,208\nv_accvgpr_write_b32 a34, v146\nv_accvgpr_write_b32 a35, v147\nQmvaa 96,148,176\nv_fma_f32 v152, v152, s72, v52\nv_fma_f32 v153, v153, s72, v52\nQmvaa 100,152,208\n"
"v_fma_f32 v154, v154, s72, v52\nv_fma_f32 v155, v155, s72, v52\nQmvaa 100,156,176\nv_fma_f32 v156, v156, s72, v52\nv_fma_f32 v157, v157, s72, v52\nQmvaa 104,160,208\nv_fma_f32 v158, v158, s72, v52\nv_fma_f32 v159, v159, s72, v52\n"
"Qmvaa 104,164,176\nQk 152,152,153\nQk 153,154,155\nQmvaa 108,168,208\nQk 154,156,157\nQk 155,158,159\nQmvaa 108,172,176\ns_waitcnt lgkmcnt(0)\n"
"v_accvgpr_write_b32 a36, v152\nQmvaa 140,168,212\ns_waitcnt vmcnt(34)\ns_barrier\nQmvaa 140,172,180\nQda 112,68,0\ns_add_u32 m0, s61, 66048\nQmvaa 136,160,212\n"
"buffer_load_dwordx4 v69, s[40:43], s60 offen nt lds\nQmvaa 136,164,180\nQda 116,68,512\nv_accvgpr_write_b32 a37, v153\nQmvaa 132,152,212\nv_accvgpr_write_b32 a38, v154\nv_accvgpr_write_b32 a39, v155\nQmvaa 132,156,180\n"
"Qda 120,68,256\nv_fma_f32 v184, v184, s72, v52\nQmvaa 128,144,212\nbuffer_load_dwordx4 v69, s[40:43], s60 offen offset:1024 nt lds\nQmvaa 128,148,180\nQda 124,68,768\nv_fma_f32 v185, v185, s72, v52\nQmvaa 160,144,216\n"
"v_fma_f32 v186, v186, s72, v52\nv_fma_f32 v187, v187, s72, v52\nQmvaa 160,148,184\nQda 128,68,1024\nv_fma_f32 v188, v188, s72, v52\nQmvaa 164,152,216\nbuffer_load_dwordx4 v69, s[40:43], s90 offen offset:2048 nt lds\nQmvaa 164,156,184\n"
"Qda 132,68,1536\nv_fma_f32 v189, v189, s72, v52\nQmvaa 168,160,216\nv_fma_f32 v190, v190, s72, v52\nv_fma_f32 v191, v191, s72, v52\nQmvaa 168,164,184\nQda 136,68,1280\nQk 184,184,185\n"
"Qmvaa 172,168,216\nbuffer_load_dwordx4 v69, s[40:43], s90 offen offset:3072 nt lds\nQmvaa 172,172,184\nQda 140,68,1792\nQk 185,186,187\nQmvaa 204,168,220\nQk 186,188,189\nQk 187,190,191\n"
"Qmvaa 204,172,188\ns_add_u32 m0, s64, 66048\nv_accvgpr_write_b32 a52, v184\nQmvaa 200,160,220\n.Lpg4:\ns_cbranch_execz .Ltr9\nbuffer_load_dwordx4 v72, s[44:47], s60 offen nt lds\nQmvaa 200,164,188\n"
"v_accvgpr_write_b32 a53, v185\nv_accvgpr_write_b32 a54, v186\nQmvaa 196,152,220\nv_accvgpr_write_b32 a55, v187\nv_fma_f32 v176, v176, s72, v52\nQmvaa 196,156,188\nv_fma_f32 v177, v177, s72, v52\nv_fma_f32 v178, v178, s72, v52\n"
"Qmvaa 192,144,220\nbuffer_load_dwordx4 v72, s[44:47], s60 offen offset:1024 nt lds\nQmvaa 192,148,188\nv_fma_f32 v179, v179, s72, v52\nv_fma_f32 v180, v180, s72, v52\nQmvaa 224,144,224\nv_fma_f32 v181, v181, s72, v52\nv_fma_f32 v182, v182, s72, v52\n"
"Qmvaa 224,148,192\nv_fma_f32 v183, v183, s72, v52\nQk 176,176,177\nQmvaa 228,152,224\nbuffer_load_dwordx4 v72, s[44:47], s90 offen offset:2048 nt lds\nQmvaa 228,156,192\nQk 177,178,179\nQk 178,180,181\n"
"Qmvaa 232,160,224\nQk 179,182,183\nv_accvgpr_write_b32 a48, v176\nQmvaa 232,164,192\nv_accvgpr_write_b32 a49, v177\nv_accvgpr_write_b32 a50, v178\nQmvaa 236,168,224\nbuffer_load_dwordx4 v72, s[44:47], s90 offen offset:3072 nt lds\n"
"s_add_u32 s65, s65, 2048\ns_xor_b32 s60, s65, %1\ns_add_u32 s90, s60, 260096\nQmvaa 236,172,192\nv_accvgpr_write_b32 a51, v179\nv_fma_f32 v208, v208, s72, v52\nQmvaa 12,168,228\nv_fma_f32 v209, v209, s72, v52\n"
"v_fma_f32 v210, v210, s72, v52\nQmvaa 12,172,196\nv_fma_f32 v211, v211, s72, v52\nv_fma_f32 v212, v212, s72, v52\nQmvaa 8,160,228\nv_fma_f32 v213, v213, s72, v52\nv_fma_f32 v214, v214, s72, v52\nQmvaa 8,164,196\n"
"v_fma_f32 v215, v215, s72, v52\nQk 208,208,209\nQmvaa 4,152,228\nQk 209,210,211\nQk 210,212,213\nQmvaa 4,156,196\nQk 211,214,215\nv_accvgpr_write_b32 a64, v208\n"
"Qmvaa 0,144,228\nv_accvgpr_write_b32 a65, v209\nv_accvgpr_write_b32 a66, v210\nQmvaa 0,148,196\nv_accvgpr_write_b32 a67, v211\nv_fma_f32 v216, v216, s72, v52\nQmvaa 32,144,232\nv_fma_f32 v217, v217, s72, v52\n"
"v_fma_f32 v218, v218, s72, v52\nQmvaa 32,148,200\nv_fma_f32 v219, v219, s72, v52\nv_fma_f32 v220, v220, s72, v52\nQmvaa 36,152,232\nv_fma_f32 v221, v221, s72, v52\nv_fma_f32 v222, v222, s72, v52\nQmvaa 36,156,200\n"
"v_fma_f32 v223, v223, s72, v52\nQk 216,216,217\nQmvaa 40,160,232\nQk 217,218,219\nQk 218,220,221\nQmvaa 40,164,200\nQk 219,222,223\nv_accvgpr_write_b32 a68, v216\n"
"Qmvaa 44,168,232\nv_accvgpr_write_b32 a69, v217\nv_accvgpr_write_b32 a70, v218\nQmvaa 44,172,200\nv_accvgpr_write_b32 a71, v219\nQta 148,70,128\nQta 150,70,1152\nQta 156,70,384\n"
"Qta 158,70,1408\nQta 164,70,640\nQta 166,70,1664\nQta 172,70,896\nQta 174,70,1920\nv_pk_fma_f32 v[96:97], v[96:97], s[72:73], v[52:53] op_sel_hi:[1,0,0]\nv_pk_fma_f32 v[98:99], v[98:99], s[72:73], v[52:53] op_sel_hi:[1,0,0]\nv_pk_fma_f32 v[100:101], v[100:101], s[72:73], v[52:53] op_sel_hi:[1,0,0]\n"
"v_pk_fma_f32 v[102:103], v[102:103], s[72:73], v[52:53] op_sel_hi:[1,0,0]\nv_mfma_f32_16x16x4_4b_bf16 v[80:95], v[66:67], v[58:59], 0\nQk 96,96,97\nQk 97,98,99\nQk 98,100,101\nQk 99,102,103\nv_accvgpr_write_b32 a8, v96\nv_accvgpr_write_b32 a9, v97\n"
"v_accvgpr_write_b32 a10, v98\nv_accvgpr_write_b32 a11, v99\nv_pk_fma_f32 v[104:105], v[104:105], s[72:73], v[52:53] op_sel_hi:[1,0,0]\nv_pk_fma_f32 v[106:107], v[106:107], s[72:73], v[52:53] op_sel_hi:[1,0,0]\nv_pk_fma_f32 v[108:109], v[108:109], s[72:73], v[52:53] op_sel_hi:[1,0,0]\nv_pk_fma_f32 v[110:111], v[110:111], s[72:73], v[52:53] op_sel_hi:[1,0,0]\nv_mfma_f32_16x16x4_4b_bf16 v[112:127], v[66:67], v[58:59], 0\nQk 104,104,105\n"
"Qk 105,106,107\nQk 106,108,109\nQk 107,110,111\nv_accvgpr_write_b32 a12, v104\nv_accvgpr_write_b32 a13, v105\nv_accvgpr_write_b32 a14, v106\nv_accvgpr_write_b32 a15, v107\nv_pk_fma_f32 v[128:129], v[128:129], s[72:73], v[52:53] op_sel_hi:[1,0,0]\n"
"v_pk_fma_f32 v[130:131], v[130:131], s[72:73], v[52:53] op_sel_hi:[1,0,0]\nv_pk_fma_f32 v[132:133], v[132:133], s[72:73], v[52:53] op_sel_hi:[1,0,0]\nv_pk_fma_f32 v[134:135], v[134:135], s[72:73], v[52:53] op_sel_hi:[1,0,0]\nv_mfma_f32_16x16x4_4b_bf16 v[144:159], v[66:67], v[58:59], 0\nQk 128,128,129\nQk 129,130,131\nQk 130,132,133\nQk 131,134,135\n"
"v_accvgpr_write_b32 a24, v128\nv_accvgpr_write_b32 a25, v129\nv_accvgpr_write_b32 a26, v130\nv_accvgpr_write_b32 a27, v131\nv_pk_fma_f32 v[136:137], v[136:137], s[72:73], v[52:53] op_sel_hi:[1,0,0]\nv_pk_fma_f32 v[138:139], v[138:139], s[72:73], v[52:53] op_sel_hi:[1,0,0]\nv_pk_fma_f32 v[140:141], v[140:141], s[72:73], v[52:53] op_sel_hi:[1,0,0]\nv_pk_fma_f32 v[142:143], v[142:143], s[72:73], v[52:53] op_sel_hi:[1,0,0]\n"
"v_mfma_f32_16x16x4_4b_bf16 v[176:191], v[66:67], v[58:59], 0\nQk 136,136,137\nQk 137,138,139\nQk 138,140,141\nQk 139,142,143\nv_accvgpr_write_b32 a28, v136\nv_accvgpr_write_b32 a29, v137\nv_accvgpr_write_b32 a30, v138\n"
"v_accvgpr_write_b32 a31, v139\nv_pk_fma_f32 v[160:161], v[160:161], s[72:73], v[52:53] op_sel_hi:[1,0,0]\nv_pk_fma_f32 v[162:163], v[162:163], s[72:73], v[52:53] op_sel_hi:[1,0,0]\nv_pk_fma_f32 v[164:165], v[164:165], s[72:73], v[52:53] op_sel_hi:[1,0,0]\nv_pk_fma_f32 v[166:167], v[166:167], s[72:73], v[52:53] op_sel_hi:[1,0,0]\nv_mfma_f32_16x16x4_4b_bf16 v[208:223], v[66:67], v[58:59], 0\nQk 160,160,161\nQk 161,162,163\n"
"Qk 162,164,165\nQk 163,166,167\nv_accvgpr_write_b32 a40, v160\nv_accvgpr_write_b32 a41, v161\nv_accvgpr_write_b32 a42, v162\nv_accvgpr_write_b32 a43, v163\nv_pk_fma_f32 v[168:169], v[168:169], s[72:73], v[52:53] op_sel_hi:[1,0,0]\nv_pk_fma_f32 v[170:171], v[170:171], s[72:73], v[52:53] op_sel_hi:[1,0,0]\n"
"v_pk_fma_f32 v[172:173], v[172:173], s[72:73], v[52:53] op_sel_hi:[1,0,0]\nv_pk_fma_f32 v[174:175], v[174:175], s[72:73], v[52:53] op_sel_hi:[1,0,0]\nv_mfma_f32_16x16x4_4b_bf16 a[208:223], v[66:67], v[58:59], 0\nQk 168,168,169\nQk 169,170,171\nQk 170,172,173\nQk 171,174,175\nv_accvgpr_write_b32 a44, v168\n"
"v_accvgpr_write_b32 a45, v169\nv_accvgpr_write_b32 a46, v170\nv_accvgpr_write_b32 a47, v171\nv_pk_fma_f32 v[192:193], v[192:193], s[72:73], v[52:53] op_sel_hi:[1,0,0]\nv_pk_fma_f32 v[194:195], v[194:195], s[72:73], v[52:53] op_sel_hi:[1,0,0]\nv_pk_fma_f32 v[196:197], v[196:197], s[72:73], v[52:53] op_sel_hi:[1,0,0]\nv_pk_fma_f32 v[198:199], v[198:199], s[72:73], v[52:53] op_sel_hi:[1,0,0]\nv_mfma_f32_16x16x4_4b_bf16 a[224:239], v[66:67], v[58:59], 0\n"
"Qk 192,192,193\nQk 193,194,195\nQk 194,196,197\nQk 195,198,199\nv_accvgpr_write_b32 a56, v192\nv_accvgpr_write_b32 a57, v193\nv_accvgpr_write_b32 a58, v194\nv_accvgpr_write_b32 a59, v195\n"
"v_pk_fma_f32 v[200:201], v[200:201], s[72:73], v[52:53] op_sel_hi:[1,0,0]\nv_pk_fma_f32 v[202:203], v[202:203], s[72:73], v[52:53] op_sel_hi:[1,0,0]\nv_pk_fma_f32 v[204:205], v[204:205], s[72:73], v[52:53] op_sel_hi:[1,0,0]\nv_pk_fma_f32 v[206:207], v[206:207], s[72:73], v[52:53] op_sel_hi:[1,0,0]\nv_mfma_f32_16x16x4_4b_bf16 a[240:255], v[66:67], v[58:59], 0\nQk 200,200,201\nQk 201,202,203\nQk 202,204,205\n"
"Qk 203,206,207\nv_accvgpr_write_b32 a60, v200\nv_accvgpr_write_b32 a61, v201\nv_accvgpr_write_b32 a62, v202\nv_accvgpr_write_b32 a63, v203\nv_pk_fma_f32 v[224:225], v[224:225], s[72:73], v[52:53] op_sel_hi:[1,0,0]\nv_pk_fma_f32 v[226:227], v[226:227], s[72:73], v[52:53] op_sel_hi:[1,0,0]\nv_pk_fma_f32 v[228:229], v[228:229], s[72:73], v[52:53] op_sel_hi:[1,0,0]\n"
"v_pk_fma_f32 v[230:231], v[230:231], s[72:73], v[52:53] op_sel_hi:[1,0,0]\nv_mfma_f32_16x16x4_4b_bf16 v[96:111], v[66:67], v[58:59], 0\nQk 224,224,225\nQk 225,226,227\nQk 226,228,229\nQk 227,230,231\nv_accvgpr_write_b32 a72, v224\nv_accvgpr_write_b32 a73, v225\n"
"v_accvgpr_write_b32 a74, v226\nv_accvgpr_write_b32 a75, v227\nv_pk_fma_f32 v[232:233], v[232:233], s[72:73], v[52:53] op_sel_hi:[1,0,0]\nv_pk_fma_f32 v[234:235], v[234:235], s[72:73], v[52:53] op_sel_hi:[1,0,0]\nv_pk_fma_f32 v[236:237], v[236:237], s[72:73], v[52:53] op_sel_hi:[1,0,0]\nv_pk_fma_f32 v[238:239], v[238:239], s[72:73], v[52:53] op_sel_hi:[1,0,0]\nv_mfma_f32_16x16x4_4b_bf16 v[128:143], v[66:67], v[58:59], 0\nQk 232,232,233\n"
"Qk 233,234,235\nQk 234,236,237\nQk 235,238,239\nv_accvgpr_write_b32 a76, v232\nv_accvgpr_write_b32 a77, v233\nv_accvgpr_write_b32 a78, v234\nv_accvgpr_write_b32 a79, v235\nv_pk_fma_f32 v[240:241], v[240:241], s[72:73], v[52:53] op_sel_hi:[1,0,0]\n"
"v_pk_fma_f32 v[242:243], v[242:243], s[72:73], v[52:53] op_sel_hi:[1,0,0]\nv_pk_fma_f32 v[244:245], v[244:245], s[72:73], v[52:53] op_sel_hi:[1,0,0]\nv_pk_fma_f32 v[246:247], v[246:247], s[72:73], v[52:53] op_sel_hi:[1,0,0]\nv_mfma_f32_16x16x4_4b_bf16 v[160:175], v[66:67], v[58:59], 0\nQk 240,240,241\nQk 241,242,243\nQk 242,244,245\nQk 243,246,247\n"
"v_accvgpr_write_b32 a80, v240\nv_accvgpr_write_b32 a81, v241\nv_accvgpr_write_b32 a82, v242\nv_accvgpr_write_b32 a83, v243\nv_pk_fma_f32 v[248:249], v[248:249], s[72:73], v[52:53] op_sel_hi:[1,0,0]\nv_pk_fma_f32 v[250:251], v[250:251], s[72:73], v[52:53] op_sel_hi:[1,0,0]\nv_pk_fma_f32 v[252:253], v[252:253], s[72:73], v[52:53] op_sel_hi:[1,0,0]\nv_pk_fma_f32 v[254:255], v[254:255], s[72:73], v[52:53] op_sel_hi:[1,0,0]\n"
"v_mfma_f32_16x16x4_4b_bf16 v[192:207], v[66:67], v[58:59], 0\nQk 248,248,249\nQk 249,250,251\nQk 250,252,253\nQk 251,254,255\nv_accvgpr_write_b32 a84, v248\nv_accvgpr_write_b32 a85, v249\nv_accvgpr_write_b32 a86, v250\n"
"v_accvgpr_write_b32 a87, v251\nv_pk_fma_f32 v[0:1], v[0:1], s[72:73], v[52:53] op_sel_hi:[1,0,0]\nv_pk_fma_f32 v[2:3], v[2:3], s[72:73], v[52:53] op_sel_hi:[1,0,0]\nv_pk_fma_f32 v[4:5], v[4:5], s[72:73], v[52:53] op_sel_hi:[1,0,0]\nv_pk_fma_f32 v[6:7], v[6:7], s[72:73], v[52:53] op_sel_hi:[1,0,0]\nv_mfma_f32_16x16x4_4b_bf16 v[224:239], v[66:67], v[58:59], 0\nQk 0,0,1\nQk 1,2,3\n"
"Qk 2,4,5\nQk 3,6,7\nv_accvgpr_write_b32 a88, v0\nv_accvgpr_write_b32 a89, v1\nv_accvgpr_write_b32 a90, v2\nv_accvgpr_write_b32 a91, v3\nv_pk_fma_f32 v[8:9], v[8:9], s[72:73], v[52:53] op_sel_hi:[1,0,0]\nv_pk_fma_f32 v[10:11], v[10:11], s[72:73], v[52:53] op_sel_hi:[1,0,0]\n"
"v_pk_fma_f32 v[12:13], v[12:13], s[72:73], v[52:53] op_sel_hi:[1,0,0]\nv_pk_fma_f32 v[14:15], v[14:15], s[72:73], v[52:53] op_sel_hi:[1,0,0]\nv_mfma_f32_16x16x4_4b_bf16 v[240:255], v[66:67], v[58:59], 0\nQk 8,8,9\nQk 9,10,11\nQk 10,12,13\nQk 11,14,15\nv_accvgpr_write_b32 a92, v8\n"
"v_accvgpr_write_b32 a93, v9\nv_accvgpr_write_b32 a94, v10\nv_accvgpr_write_b32 a95, v11\nv_pk_fma_f32 v[16:17], v[16:17], s[72:73], v[52:53] op_sel_hi:[1,0,0]\nv_pk_fma_f32 v[18:19], v[18:19], s[72:73], v[52:53] op_sel_hi:[1,0,0]\nv_pk_fma_f32 v[20:21], v[20:21], s[72:73], v[52:53] op_sel_hi:[1,0,0]\nv_pk_fma_f32 v[22:23], v[22:23], s[72:73], v[52:53] op_sel_hi:[1,0,0]\nv_mfma_f32_16x16x4_4b_bf16 a[176:191], v[54:55], v[54:55], 0\n"
"Qk 16,16,17\nQk 17,18,19\nQk 18,20,21\nQk 19,22,23\nv_accvgpr_write_b32 a96, v16\nv_accvgpr_write_b32 a97, v17\nv_accvgpr_write_b32 a98, v18\nv_accvgpr_write_b32 a99, v19\n"
"v_pk_fma_f32 v[24:25], v[24:25], s[72:73], v[52:53] op_sel_hi:[1,0,0]\nv_pk_fma_f32 v[26:27], v[26:27], s[72:73], v[52:53] op_sel_hi:[1,0,0]\nv_pk_fma_f32 v[28:29], v[28:29], s[72:73], v[52:53] op_sel_hi:[1,0,0]\nv_pk_fma_f32 v[30:31], v[30:31], s[72:73], v[52:53] op_sel_hi:[1,0,0]\nv_mfma_f32_16x16x4_4b_bf16 a[188:203], v[54:55], v[54:55], 0\nQk 24,24,25\nQk 25,26,27\nQk 26,28,29\n"
"Qk 27,30,31\nv_accvgpr_write_b32 a100, v24\nv_accvgpr_write_b32 a101, v25\nv_accvgpr_write_b32 a102, v26\nv_accvgpr_write_b32 a103, v27\nv_pk_fma_f32 v[32:33], v[32:33], s[72:73], v[52:53] op_sel_hi:[1,0,0]\nv_pk_fma_f32 v[34:35], v[34:35], s[72:73], v[52:53] op_sel_hi:[1,0,0]\nv_pk_fma_f32 v[36:37], v[36:37], s[72:73], v[52:53] op_sel_hi:[1,0,0]\n"
"v_pk_fma_f32 v[38:39], v[38:39], s[72:73], v[52:53] op_sel_hi:[1,0,0]\nQk 32,32,33\nQk 33,34,35\nQk 34,36,37\nQk 35,38,39\nv_accvgpr_write_b32 a104, v32\nv_accvgpr_write_b32 a105, v33\nv_accvgpr_write_b32 a106, v34\n"
"v_accvgpr_write_b32 a107, v35\nv_pk_fma_f32 v[40:41], v[40:41], s[72:73], v[52:53] op_sel_hi:[1,0,0]\nv_pk_fma_f32 v[42:43], v[42:43], s[72:73], v[52:53] op_sel_hi:[1,0,0]\nv_pk_fma_f32 v[44:45], v[44:45], s[72:73], v[52:53] op_sel_hi:[1,0,0]\nv_pk_fma_f32 v[46:47], v[46:47], s[72:73], v[52:53] op_sel_hi:[1,0,0]\nQk 40,40,41\nQk 41,42,43\nQk 42,44,45\n"
"Qk 43,46,47\nv_accvgpr_write_b32 a108, v40\nv_accvgpr_write_b32 a109, v41\nv_accvgpr_write_b32 a110, v42\nv_accvgpr_write_b32 a111, v43\ns_waitcnt lgkmcnt(8)\nQzvaa 0,112,0\nQmvaa 0,116,4\n"
"s_memrealtime s[74:75]\ns_memtime s[68:69]\nQmvaa 0,120,8\nQmvaa 0,124,12\nQzvaa 4,140,12\nQmvaa 4,136,8\nQmvaa 4,132,4\nQmvaa 4,128,0\n"
"s_nop 3\nv_max3_f32 v66, v0, v1, v2\ns_nop 2\nv_max3_f32 v66, v66, v3, v4\nv_max3_f32 v66, v66, v5, v6\nv_max_f32 v66, v66, v7\nQzvaa 8,112,16\ns_waitcnt lgkmcnt(0)\n"
"v_mov_b32 v67, v66\nQmvaa 8,116,20\ns_sub_u32 s68, s68, s80\ns_sub_u32 s69, s74, s82\nQmvaa 8,120,24\ns_mul_i32 s69, s69, 73\ns_lshl_b32 s68, s68, 2\nQmvaa 8,124,28\n"
"s_sub_u32 s75, s67, 1\ns_cmp_gt_u32 s68, s69\ns_cselect_b32 s68, s75, 0\ns_cselect_b32 s31, 4, 0\ns_add_u32 s31, s31, 240\nQzvaa 12,140,28\ns_and_b32 s75, s68, 25\ns_add_u32 s77, s74, 60\n"
"Qmvaa 12,136,24\ns_add_u32 s77, s77, s75\nv_permlane32_swap_b32 v66, v67\nQmvaa 12,132,20\nv_max_f32 v66, v66, v67\nv_mov_b32 v67, v66\nQmvaa 12,128,16\ns_nop 0\n"
"v_permlane16_swap_b32 v66, v67\nv_max_f32 v66, v66, v67\ns_nop 1\nv_min_f32_dpp v66, v66, v66 row_ror:8 row_mask:0xf bank_mask:0xf\ns_nop 1\nv_min_f32_dpp v66, v66, v66 row_ror:4 row_mask:0xf bank_mask:0xf\ns_nop 1\nv_min_f32_dpp v66, v66, v66 row_ror:2 row_mask:0xf bank_mask:0xf\n"
"s_nop 1\nv_min_f32_dpp v66, v66, v66 row_ror:1 row_mask:0xf bank_mask:0xf\nv_sub_f32 v56, 0xc1a00000, v66\nv_mov_b32 v57, v56\nv_mov_b32 v58, v56\nv_mov_b32 v59, v56\nQa 0,0,56\nQa 1,1,56\n"
"Qa 2,2,56\nQa 3,3,56\nQe 0\nQe 1\nQe 2\nQe 3\nQk 16,0,1\nQk 17,2,3\n"
"Qcvaa 0,112,32,56\nQa 4,4,56\nQa 5,5,56\nQmvaa 0,116,36\nQa 6,6,56\nQa 7,7,56\nQmvaa 0,120,40\nQe 4\n"
"Qe 5\nQe 6\nQe 7\nQmvaa 0,124,44\nQk 18,4,5\nQk 19,6,7\nQcvaa 4,140,44,56\nQa 8,8,56\n"
"Qa 9,9,56\nQmvaa 4,136,40\nQa 10,10,56\nQa 11,11,56\nQmvaa 4,132,36\nQa 12,12,56\nQa 13,13,56\nQmvaa 4,128,32\n"
"s_waitcnt lgkmcnt(0)\nQa 14,14,56\nQmvva 96,16,148\nQa 15,15,56\nQmvva 100,16,156\nQe 8\nQmvva 104,16,164\nQe 9\n"
"Qe 10\nQe 11\nQe 12\nQe 13\nQe 14\nQe 15\nQmvva 108,16,172\nQk 20,8,9\n"
"Qk 21,10,11\nQk 22,12,13\nQk 23,14,15\ns_nop 1\nQmvva 140,20,172\nQe 0\nQmvva 136,20,164\nQe 1\n"
"Qmvva 132,20,156\nQe 2\nQmvva 128,20,148\nQe 3\nv_smfmac_f32_16x16x64_bf16 a[176:179], a[204:207], v[16:23], v77\nQk 24,0,1\nQcvaa 8,112,48,56\ns_add_u32 m0, s61, 99072\n"
"Qk 25,2,3\nQmvaa 8,116,52\nbuffer_load_dwordx4 v69, s[40:43], s60 offen nt lds\nQmvaa 8,120,56\nQe 4\nQmvaa 8,124,60\ns_or_b32 s65, s65, 0x1d8000\ns_add_u32 s65, s65, 2048\n"
"Qcvaa 12,140,60,56\ns_andn2_b32 s65, s65, 0x1d8000\nQmvaa 12,136,56\nQe 5\nQmvaa 12,132,52\nbuffer_load_dwordx4 v69, s[40:43], s60 offen offset:1024 nt lds\nQmvaa 12,128,48\nQe 6\n"
"Qcvaa 0,128,64,56\ns_memrealtime s[74:75]\nQk 26,4,5\nQmvaa 0,132,68\nQe 7\nQmvaa 0,136,72\nQk 27,6,7\nQmvaa 0,140,76\n"
"buffer_load_dwordx4 v69, s[40:43], s90 offen offset:2048 nt lds\nQcvaa 4,124,76,56\nQe 8\nQmvaa 4,120,72\nQe 9\nQmvaa 4,116,68\nQe 10\nQmvaa 4,112,64\n"
"Qe 11\nbuffer_load_dwordx4 v69, s[40:43], s90 offen offset:3072 nt lds\nQmvva 160,24,148\nQe 12\nQmvva 164,24,156\nQe 13\ns_add_u32 m0, s64, 99072\nQmvva 168,24,164\n"
"buffer_load_dwordx4 v72, s[44:47], s60 offen nt lds\nQe 14\nQe 15\nQmvva 172,24,172\nQk 28,8,9\nQk 29,10,11\nQk 30,12,13\nQk 31,14,15\n"
"s_nop 1\nQmvva 204,28,172\nQe 0\nbuffer_load_dwordx4 v72, s[44:47], s60 offen offset:1024 nt lds\nQmvva 200,28,164\nQe 1\nQmvva 196,28,156\nQe 2\n"
"Qmvva 192,28,148\nbuffer_load_dwordx4 v72, s[44:47], s90 offen offset:2048 nt lds\nv_smfmac_f32_16x16x64_bf16 a[180:183], a[204:207], v[24:31], v77\nQk 34,0,1\nQcvaa 8,112,80,56\nQe 3\nQmvaa 8,116,84\nQk 35,2,3\n"
"Qmvaa 8,120,88\ns_waitcnt vmcnt(15)\ns_barrier\nQmvaa 8,124,92\nbuffer_load_dwordx4 v72, s[44:47], s90 offen offset:3072 nt lds\nQcvaa 12,140,92,56\ns_waitcnt lgkmcnt(0)\nQmvaa 12,136,88\n"
"s_cmp_gt_u32 s77, s74\ns_cbranch_scc1 .Lsp0\n.Lrp0:\nQmvaa 12,132,84\ns_and_b32 s75, s68, 30\ns_add_u32 s77, s74, 90\nQmvaa 12,128,80\ns_add_u32 s77, s77, s75\n"
"s_xor_b32 s60, s65, %1\ns_add_u32 s90, s60, 260096\nQcvaa 0,128,96,56\nQda 128,68,34048\nQmvaa 0,132,100\nQda 132,68,34560\nQmvaa 0,136,104\nQda 136,68,34304\n"
"Qe 4\nQe 5\nQe 6\nQe 7\nQmvaa 0,140,108\nQda 140,68,34816\nQk 32,4,5\nQk 33,6,7\n"
"Qcvaa 4,124,108,56\nQda 124,68,33792\nQmvaa 4,120,104\nQda 120,68,33280\nQmvaa 4,116,100\nQda 116,68,33536\nQmvaa 4,112,96\nQda 112,68,33024\n"
"Qmvva 224,32,148\nQe 8\nQmvva 228,32,156\nQe 9\nQmvva 232,32,164\nQe 10\nQe 11\nQmvva 236,32,172\n"
"s_waitcnt lgkmcnt(0)\nQk 36,8,9\n.Lpg5:\ns_cbranch_execz .Ltr9\nQk 37,10,11\nQcvaa 8,112,0,56\nQe 12\nQmvaa 8,116,4\n"
"v_add_u32 v68, 66048, v68\nQmvaa 8,120,8\nQe 13\nQe 14\nQe 15\nQmvaa 8,124,12\nQk 38,12,13\nQk 39,14,15\n"
"Qcvaa 12,140,12,56\nQe 0\nQmvaa 12,136,8\nQe 1\nQmvaa 12,132,4\nQe 2\nQmvaa 12,128,0\nQe 3\n"
"v_smfmac_f32_16x16x64_bf16 a[184:187], a[204:207], v[32:39], v77\nQk 42,0,1\nQmava 208,36,148\nQe 4\nQmava 212,36,156\nQe 5\nQmava 216,36,164\nQe 6\n"
"Qe 8\nQe 7\nQmava 220,36,172\nQe 9\nQk 43,2,3\nQk 40,4,5\nQk 41,6,7\nQta 168,70,768\n"
"Qta 170,70,1792\nQmava 252,40,172\nQe 10\nQta 172,70,33792\nQta 174,70,34816\nQmava 248,40,164\nQe 11\nQta 160,70,512\n"
"Qta 162,70,1536\nQmava 244,40,156\nQk 44,8,9\nQk 45,10,11\nQta 164,70,33536\nQta 166,70,34560\nQmava 240,40,148\nQe 12\n"
"Qcvaa 0,112,16,56\nQta 152,70,256\nQta 154,70,1280\nQmvaa 0,116,20\nQe 13\nQmvaa 0,120,24\nQta 156,70,33280\nQta 158,70,34304\n"
"Qmvaa 0,124,28\nQe 14\nQcvaa 4,140,28,56\nQta 144,70,0\nQta 146,70,1024\nQmvaa 4,136,24\nQe 15\nQmvaa 4,132,20\n"
"Qta 148,70,33024\nQta 150,70,34048\nQmvaa 4,128,16\nQk 46,12,13\nQk 47,14,15\nQcvaa 8,128,32,56\nv_add_u32 v75, 1, v75\nQmvaa 8,132,36\n"
"s_waitcnt vmcnt(8)\nds_write_b32 v73, v75\nQmvaa 8,136,40\nQe 0\nQmvaa 8,140,44\nQe 1\nQcvaa 12,124,44,56\nQe 2\n"
"Qmvaa 12,120,40\nQe 3\nQmvaa 12,116,36\nQe 4\nQmvaa 12,112,32\ns_waitcnt lgkmcnt(1)\nQk 48,0,1\nv_smfmac_f32_16x16x64_bf16 a[188:191], a[204:207], v[40:47], v77\n"
"Qk 49,2,3\nQmvva 80,16,144\nQe 5\nQmvva 80,44,148\nQe 6\nQmvva 84,16,152\nQe 7\nQmvva 84,44,156\n"
"Qk 50,4,5\nQk 51,6,7\nQmvva 88,16,160\nQe 8\nQmvva 88,44,164\nQe 9\nQmvva 92,16,168\nQe 10\n"
"Qmvva 92,44,172\nQe 11\nQmvva 124,20,168\nQk 54,8,9\nQk 55,10,11\nQmvva 124,48,172\nds_read_b32 v66, v74\nQmvva 120,20,160\n"
"Qe 12\nQmvva 120,48,164\nQe 13\nQmvva 116,20,152\nQe 14\nQmvva 116,48,156\nQe 15\nQmvva 112,20,144\n"
"s_waitcnt lgkmcnt(0)\nv_cmp_gt_u32 vcc, v75, v66\nQmvva 112,48,148\ns_cbranch_vccnz .Lsa1\n.Lra1:\nQk 52,12,13\nQcvaa 0,112,48,56\ns_add_u32 m0, s61, 0\n"
"Qk 53,14,15\nQmvaa 0,116,52\nbuffer_load_dwordx4 v69, s[40:43], s60 offen nt lds\nQmvaa 0,120,56\nQmvaa 0,124,60\ns_add_u32 s65, s65, 2048\nQcvaa 4,140,60,56\nQmvaa 4,136,56\n"
"Qmvaa 4,132,52\nbuffer_load_dwordx4 v69, s[40:43], s60 offen offset:1024 nt lds\nQmvaa 4,128,48\nQcvaa 8,128,64,56\ns_memrealtime s[74:75]\nQmvaa 8,132,68\nQe 0\nQmvaa 8,136,72\n"
"Qe 1\nQmvaa 8,140,76\nbuffer_load_dwordx4 v69, s[40:43], s90 offen offset:2048 nt lds\nQcvaa 12,124,76,56\nQe 2\nQmvaa 12,120,72\nQe 3\nQmvaa 12,116,68\n"
"Qe 4\nQmvaa 12,112,64\nQe 5\nv_smfmac_f32_16x16x64_bf16 a[192:195], a[204:207], v[48:55], v77\nQk 20,0,1\nQmvva 144,24,144\nbuffer_load_dwordx4 v69, s[40:43], s90 offen offset:3072 nt lds\nQmvva 144,52,148\n"
"Qe 6\nQmvva 148,24,152\nQe 7\nQmvva 148,52,156\ns_add_u32 m0, s64, 0\nQk 21,2,3\nQmvva 152,24,160\nbuffer_load_dwordx4 v72, s[44:47], s60 offen nt lds\n"
"Qmvva 152,52,164\nQk 22,4,5\nQk 23,6,7\nQmvva 156,24,168\nQe 8\nQmvva 156,52,172\nQe 9\nQmvva 188,28,168\n"
"Qe 10\nQmvva 188,20,172\nbuffer_load_dwordx4 v72, s[44:47], s60 offen offset:1024 nt lds\nQmvva 184,28,160\nQe 11\nQmvva 184,20,164\nQk 26,8,9\nQk 27,10,11\n"
"Qmvva 180,28,152\nQe 12\nQmvva 180,20,156\nQe 13\nQmvva 176,28,144\nbuffer_load_dwordx4 v72, s[44:47], s90 offen offset:2048 nt lds\nQmvva 176,20,148\nQe 14\n"
"Qcvaa 0,112,80,56\nQe 15\nQmvaa 0,116,84\nQk 24,12,13\nQk 25,14,15\nQmvaa 0,120,88\nQmvaa 0,124,92\nbuffer_load_dwordx4 v72, s[44:47], s90 offen offset:3072 nt lds\n"
"Qcvaa 4,140,92,56\ns_waitcnt lgkmcnt(0)\nQmvaa 4,136,88\ns_cmp_gt_u32 s77, s74\ns_cbranch_scc1 .Lsp1\n.Lrp1:\nQmvaa 4,132,84\ns_and_b32 s75, s68, 45\n"
"s_add_u32 s77, s74, 110\nQmvaa 4,128,80\ns_add_u32 s77, s77, s75\ns_xor_b32 s60, s65, %1\ns_add_u32 s90, s60, 260096\nQcvaa 8,128,96,56\nQda 128,68,1024\ns_add_u32 s79, s79, 2030\n"
"Qmvaa 8,132,100\nQda 132,68,1536\ns_min_u32 s79, s79, s77\nQmvaa 8,136,104\nQda 136,68,1280\nQmvaa 8,140,108\nQda 140,68,1792\nQcvaa 12,124,108,56\n"
"Qda 124,68,768\nQmvaa 12,120,104\nQda 120,68,256\nQmvaa 12,116,100\nQda 116,68,512\nQmvaa 12,112,96\nQda 112,68,0\nv_smfmac_f32_16x16x64_bf16 a[196:199], a[204:207], v[20:27], v77\n"
"Qmvva 208,32,144\nQe 0\nQmvva 208,24,148\nQe 1\nQmvva 212,32,152\nQe 2\nQmvva 212,24,156\nQe 3\n"
"Qmvva 216,32,160\nQk 28,0,1\nQk 29,2,3\nQmvva 216,24,164\nQe 4\nQmvva 220,32,168\nQe 5\nQmvva 220,24,172\n"
"s_waitcnt lgkmcnt(0)\nQk 30,4,5\nQcvaa 0,112,0,56\nQe 6\nQmvaa 0,116,4\nQe 7\nQmvaa 0,120,8\nQk 31,6,7\n"
"Qmvaa 0,124,12\nQe 8\nQcvaa 4,140,12,56\nQe 9\nQmvaa 4,136,8\nQe 10\nQmvaa 4,132,4\nQe 11\n"
"Qmvaa 4,128,0\nQe 12\nQmvva 240,36,144\nQe 13\nQmvva 240,28,148\nQe 14\nQmvva 244,36,152\nQe 15\n"
"Qmvva 244,28,156\nQk 34,8,9\nQk 35,10,11\nQmvva 248,36,160\nQk 32,12,13\nQk 33,14,15\nQmvva 248,28,164\nQe 0\n"
"Qmvva 252,36,168\nQe 1\nQmvva 252,28,172\nQe 2\nQmava 236,40,168\nQta 168,70,33920\nQta 170,70,34944\nQmava 236,32,172\n"
"Qe 3\nQmava 232,40,160\nQta 172,71,896\nQta 174,71,1920\nQmava 232,32,164\nQk 36,0,1\nQk 37,2,3\nQmava 228,40,152\n"
"Qta 160,70,33664\nQta 162,70,34688\nQmava 228,32,156\nQe 4\nQmava 224,40,144\nQta 164,71,640\nQta 166,71,1664\nQmava 224,32,148\n"
"Qe 5\nv_smfmac_f32_16x16x64_bf16 a[200:203], a[204:207], v[28:35], v77\nQta 152,70,33408\nQta 154,70,34432\nQcvaa 8,112,16,56\nQe 6\nQmvaa 8,116,20\nQta 156,71,384\n"
"Qta 158,71,1408\nQmvaa 8,120,24\nQe 7\nQmvaa 8,124,28\nQta 144,70,33152\nQta 146,70,34176\nQcvaa 12,140,28,56\nQk 38,4,5\n"
"Qk 39,6,7\nQmvaa 12,136,24\nQta 148,71,128\nQta 150,71,1152\nQmvaa 12,132,20\nQmvaa 12,128,16\ns_lshl_b32 s66, s67, 3\n.Lloop:\n"
"s_bitcmp1_b32 s99, s66\ns_cbranch_scc1 .Lhook\n.Lhookret:\nQcvaa 0,128,32,56\nv_add_u32 v75, 1, v75\nQmvaa 0,132,36\ns_waitcnt vmcnt(8)\nds_write_b32 v73, v75\n"
"Qmvaa 0,136,40\nQe 8\nQmvaa 0,140,44\nQe 9\nQcvaa 4,124,44,56\nQe 10\nQmvaa 4,120,40\nQe 11\n"
"Qmvaa 4,116,36\nQe 12\nQmvaa 4,112,32\ns_waitcnt lgkmcnt(1)\nQk 40,8,9\nQmvva 96,44,144\nQe 13\nQmvva 96,36,148\n"
"Qe 14\nQmvva 100,44,152\nQe 15\nQmvva 100,36,156\nQk 41,10,11\nQk 42,12,13\nQmvva 104,44,160\nQk 43,14,15\n"
"Qmvva 104,36,164\nQe 0\nQmvva 108,44,168\nQe 1\nQmvva 108,36,172\nQe 2\nQmvva 140,48,168\nQe 3\n"
"Qmvva 140,40,172\nQk 46,0,1\nQk 47,2,3\nQmvva 136,48,160\nds_read_b32 v66, v74\nQmvva 136,40,164\nQe 4\nQmvva 132,48,152\n"
"Qe 5\nQmvva 132,40,156\nQe 6\nQmvva 128,48,144\nQe 7\nQmvva 128,40,148\ns_waitcnt lgkmcnt(0)\nv_cmp_gt_u32 vcc, v75, v66\n"
"v_smfmac_f32_16x16x64_bf16 a[176:179], a[204:207], v[36:43], v77\ns_cbranch_vccnz .Lsa2\n.Lra2:\nQcvaa 8,112,48,56\ns_add_u32 m0, s61, 33024\nQk 44,4,5\nQmvaa 8,116,52\nbuffer_load_dwordx4 v69, s[40:43], s60 offen nt lds\n"
"Qmvaa 8,120,56\nQk 45,6,7\nQmvaa 8,124,60\ns_add_u32 s65, s65, 2048\nQcvaa 12,140,60,56\nQmvaa 12,136,56\nQmvaa 12,132,52\nbuffer_load_dwordx4 v69, s[40:43], s60 offen offset:1024 nt lds\n"
"Qmvaa 12,128,48\nQcvaa 0,128,64,56\ns_memrealtime s[74:75]\nQmvaa 0,132,68\nQe 8\nQmvaa 0,136,72\nQe 9\nQmvaa 0,140,76\n"
"buffer_load_dwordx4 v69, s[40:43], s90 offen offset:2048 nt lds\nQcvaa 4,124,76,56\nQe 10\nQmvaa 4,120,72\nQe 11\nQmvaa 4,116,68\nQe 12\nQmvaa 4,112,64\n"
"Qe 13\nQmvva 160,52,144\nbuffer_load_dwordx4 v69, s[40:43], s90 offen offset:3072 nt lds\nQmvva 160,44,148\nQe 14\nQmvva 164,52,152\nQe 15\nQmvva 164,44,156\n"
"Qk 48,8,9\nQk 49,10,11\nQmvva 168,52,160\ns_add_u32 m0, s64, 33024\nQk 50,12,13\nQmvva 168,44,164\nbuffer_load_dwordx4 v72, s[44:47], s60 offen nt lds\nQmvva 172,52,168\n"
"Qk 51,14,15\nQmvva 172,44,172\nQe 0\nQmvva 204,20,168\nQe 1\nQmvva 204,48,172\nQe 2\nQmvva 200,20,160\n"
"buffer_load_dwordx4 v72, s[44:47], s60 offen offset:1024 nt lds\nQmvva 200,48,164\nQe 3\nQmvva 196,20,152\nQk 18,0,1\nQk 19,2,3\nQmvva 196,48,156\nQe 4\n"
"Qmvva 192,20,144\nQe 5\nQmvva 192,48,148\nbuffer_load_dwordx4 v72, s[44:47], s90 offen offset:2048 nt lds\nv_smfmac_f32_16x16x64_bf16 a[180:183], a[204:207], v[44:51], v77\nQk 16,4,5\nQcvaa 8,112,80,56\nQe 6\n"
"Qmvaa 8,116,84\nQe 7\nQmvaa 8,120,88\nQk 17,6,7\nQmvaa 8,124,92\nbuffer_load_dwordx4 v72, s[44:47], s90 offen offset:3072 nt lds\nQcvaa 12,140,92,56\ns_waitcnt lgkmcnt(0)\n"
"Qmvaa 12,136,88\ns_cmp_gt_u32 s77, s74\ns_cbranch_scc1 .Lsp2\n.Lrp2:\nQmvaa 12,132,84\ns_add_u32 s77, s74, 220\nQmvaa 12,128,80\ns_xor_b32 s60, s65, %1\n"
"s_add_u32 s90, s60, 260096\nQcvaa 0,128,96,56\nQda 128,68,34048\ns_add_u32 s79, s79, s31\nQmvaa 0,132,100\nQda 132,68,34560\ns_max_u32 s77, s77, s79\nQmvaa 0,136,104\n"
"Qda 136,68,34304\nQmvaa 0,140,108\nQda 140,68,34816\nQcvaa 4,124,108,56\nQda 124,68,33792\nQmvaa 4,120,104\nQda 120,68,33280\nQmvaa 4,116,100\n"
"Qda 116,68,33536\nQmvaa 4,112,96\nQda 112,68,33024\nQmvva 224,24,144\nQe 8\nQmvva 224,16,148\nQe 9\nQmvva 228,24,152\n"
"Qe 10\nQmvva 228,16,156\nQe 11\nQmvva 232,24,160\nQk 20,8,9\nQk 21,10,11\nQmvva 232,16,164\nQe 12\n"
"Qmvva 236,24,168\nQe 13\nQmvva 236,16,172\ns_waitcnt lgkmcnt(0)\nQk 22,12,13\nQcvaa 8,112,0,56\nQe 14\nQmvaa 8,116,4\n"
"v_subrev_u32 v68, 66048, v68\nQmvaa 8,120,8\nQe 15\nQmvaa 8,124,12\nQk 23,14,15\nQcvaa 12,140,12,56\nQe 0\nQmvaa 12,136,8\n"
"Qe 1\nQmvaa 12,132,4\nQe 2\nQmvaa 12,128,0\nQe 3\nv_smfmac_f32_16x16x64_bf16 a[184:187], a[204:207], v[16:23], v77\nQk 26,0,1\nQmava 208,28,144\n"
"Qe 4\nQmava 208,20,148\nQe 5\nQmava 212,28,152\nQe 6\nQmava 212,20,156\nQe 7\nQmava 216,28,160\n"
"Qk 27,2,3\nQk 24,4,5\nQmava 216,20,164\nQk 25,6,7\nQmava 220,28,168\nQe 8\nQmava 220,20,172\nQe 9\n"
"Qmava 252,32,168\nQta 168,71,768\nQta 170,71,1792\nQmava 252,24,172\nQe 10\nQmava 248,32,160\nQta 172,71,33792\nQta 174,71,34816\n"
"Qmava 248,24,164\nQe 11\nQmava 244,32,152\nQta 160,71,512\nQta 162,71,1536\nQmava 244,24,156\nQk 28,8,9\nQk 29,10,11\n"
"Qmava 240,32,144\nQta 164,71,33536\nQta 166,71,34560\nQmava 240,24,148\nQe 12\nQcvaa 0,112,16,56\nQta 152,71,256\nQta 154,71,1280\n"
"Qmvaa 0,116,20\nQe 13\nQmvaa 0,120,24\nQta 156,71,33280\nQta 158,71,34304\nQmvaa 0,124,28\nQe 14\nQcvaa 4,140,28,56\n"
"Qta 144,71,0\nQta 146,71,1024\nQmvaa 4,136,24\nQe 15\nQmvaa 4,132,20\nQta 148,71,33024\nQta 150,71,34048\nQmvaa 4,128,16\n"
"Qk 30,12,13\nQk 31,14,15\nQcvaa 8,128,32,56\nv_add_u32 v75, 1, v75\nQmvaa 8,132,36\ns_waitcnt vmcnt(8)\nds_write_b32 v73, v75\nQmvaa 8,136,40\n"
"Qe 0\nQmvaa 8,140,44\nQe 1\nQcvaa 12,124,44,56\nQe 2\nQmvaa 12,120,40\n.Lpg6:\ns_cbranch_execz .Ltr9\n"
"Qe 3\nQmvaa 12,116,36\nQe 4\nQmvaa 12,112,32\ns_waitcnt lgkmcnt(1)\nQk 32,0,1\nv_smfmac_f32_16x16x64_bf16 a[188:191], a[204:207], v[24:31], v77\nQk 33,2,3\n"
"Qmvva 80,36,144\nQe 5\nQmvva 80,28,148\nQe 6\nQmvva 84,36,152\nQe 7\nQmvva 84,28,156\nQk 34,4,5\n"
"Qk 35,6,7\nQmvva 88,36,160\nQe 8\nQmvva 88,28,164\nQe 9\nQmvva 92,36,168\nQe 10\nQmvva 92,28,172\n"
"Qe 11\nQmvva 124,40,168\nQk 38,8,9\nQk 39,10,11\nQmvva 124,32,172\nds_read_b32 v66, v74\nQmvva 120,40,160\nQe 12\n"
"Qmvva 120,32,164\nQe 13\nQmvva 116,40,152\nQe 14\nQmvva 116,32,156\nQe 15\nQmvva 112,40,144\ns_waitcnt lgkmcnt(0)\n"
"v_cmp_gt_u32 vcc, v75, v66\nQmvva 112,32,148\ns_cbranch_vccnz .Lsa3\n.Lra3:\nQk 36,12,13\nQcvaa 0,112,48,56\ns_add_u32 m0, s61, 66048\nQk 37,14,15\n"
"Qmvaa 0,116,52\nbuffer_load_dwordx4 v69, s[40:43], s60 offen nt lds\nQmvaa 0,120,56\nQmvaa 0,124,60\ns_add_u32 s65, s65, 2048\nQcvaa 4,140,60,56\nQmvaa 4,136,56\nQmvaa 4,132,52\n"
"buffer_load_dwordx4 v69, s[40:43], s60 offen offset:1024 nt lds\nQmvaa 4,128,48\nQcvaa 8,128,64,56\ns_nop 0\nQmvaa 8,132,68\nQe 0\nQmvaa 8,136,72\nQe 1\n"
"Qmvaa 8,140,76\nbuffer_load_dwordx4 v69, s[40:43], s90 offen offset:2048 nt lds\nQcvaa 12,124,76,56\nQe 2\nQmvaa 12,120,72\nQe 3\nQmvaa 12,116,68\nQe 4\n"
"Qmvaa 12,112,64\nQe 5\nv_smfmac_f32_16x16x64_bf16 a[192:195], a[204:207], v[32:39], v77\nQk 40,0,1\nQmvva 144,44,144\nbuffer_load_dwordx4 v69, s[40:43], s90 offen offset:3072 nt lds\nQmvva 144,36,148\nQe 6\n"
"Qmvva 148,44,152\nQe 7\nQmvva 148,36,156\ns_add_u32 m0, s64, 66048\nQk 41,2,3\nQmvva 152,44,160\nbuffer_load_dwordx4 v72, s[44:47], s60 offen nt lds\nQmvva 152,36,164\n"
"Qk 42,4,5\nQk 43,6,7\nQmvva 156,44,168\nQe 8\nQmvva 156,36,172\nQe 9\nQmvva 188,48,168\nQe 10\n"
"Qmvva 188,40,172\nbuffer_load_dwordx4 v72, s[44:47], s60 offen offset:1024 nt lds\nQmvva 184,48,160\nQe 11\nQmvva 184,40,164\nQk 46,8,9\nQk 47,10,11\nQmvva 180,48,152\n"
"Qe 12\nQmvva 180,40,156\nQe 13\nQmvva 176,48,144\nbuffer_load_dwordx4 v72, s[44:47], s90 offen offset:2048 nt lds\nQmvva 176,40,148\nQe 14\nQcvaa 0,112,80,56\n"
"Qe 15\nQmvaa 0,116,84\nQk 44,12,13\nQk 45,14,15\nQmvaa 0,120,88\nQmvaa 0,124,92\nbuffer_load_dwordx4 v72, s[44:47], s90 offen offset:3072 nt lds\nQcvaa 4,140,92,56\n"
"Qmvaa 4,136,88\nQmvaa 4,132,84\nQmvaa 4,128,80\ns_xor_b32 s60, s65, %1\ns_add_u32 s90, s60, 260096\nQcvaa 8,128,96,56\nQda 128,68,1024\nQmvaa 8,132,100\n"
"Qda 132,68,1536\nQmvaa 8,136,104\nQda 136,68,1280\nQmvaa 8,140,108\nQda 140,68,1792\nQcvaa 12,124,108,56\nQda 124,68,768\nQmvaa 12,120,104\n"
"Qda 120,68,256\nQmvaa 12,116,100\nQda 116,68,512\nQmvaa 12,112,96\nQda 112,68,0\nv_smfmac_f32_16x16x64_bf16 a[196:199], a[204:207], v[40:47], v77\nQmvva 208,16,144\nQe 0\n"
"Qmvva 208,44,148\nQe 1\nQmvva 212,16,152\nQe 2\nQmvva 212,44,156\nQe 3\nQmvva 216,16,160\nQk 48,0,1\n"
"Qk 49,2,3\nQmvva 216,44,164\nQe 4\nQmvva 220,16,168\nQe 5\nQmvva 220,44,172\ns_waitcnt lgkmcnt(0)\nQk 50,4,5\n"
"Qcvaa 0,112,0,56\nQe 6\nQmvaa 0,116,4\nQe 7\nQmvaa 0,120,8\nQk 51,6,7\nQmvaa 0,124,12\nQe 8\n"
"Qcvaa 4,140,12,56\nQe 9\nQmvaa 4,136,8\nQe 10\nQmvaa 4,132,4\nQe 11\nQmvaa 4,128,0\nQe 12\n"
"Qmvva 240,20,144\nQe 13\nQmvva 240,48,148\nQe 14\nQmvva 244,20,152\nQe 15\nQmvva 244,48,156\nQk 54,8,9\n"
"Qk 55,10,11\nQmvva 248,20,160\nQk 52,12,13\nQk 53,14,15\nQmvva 248,48,164\nQe 0\nQmvva 252,20,168\nQe 1\n"
"Qmvva 252,48,172\nQe 2\nQmava 236,24,168\nQta 168,71,33920\nQta 170,71,34944\nQmava 236,52,172\nQe 3\nQmava 232,24,160\n"
"Qta 172,70,896\nQta 174,70,1920\nQmava 232,52,164\nQk 16,0,1\nQk 17,2,3\nQmava 228,24,152\nQta 160,71,33664\nQta 162,71,34688\n"
"Qmava 228,52,156\nQe 4\nQmava 224,24,144\nQta 164,70,640\nQta 166,70,1664\nQmava 224,52,148\nQe 5\nv_smfmac_f32_16x16x64_bf16 a[200:203], a[204:207], v[48:55], v77\n"
"Qta 152,71,33408\nQta 154,71,34432\nQcvaa 8,112,16,56\nQe 6\nQmvaa 8,116,20\nQta 156,70,384\nQta 158,70,1408\nQmvaa 8,120,24\n"
"Qe 7\nQmvaa 8,124,28\nQta 144,71,33152\nQta 146,71,34176\nQcvaa 12,140,28,56\nQk 18,4,5\nQk 19,6,7\nQmvaa 12,136,24\n"
"Qta 148,70,128\nQta 150,70,1152\nQmvaa 12,132,20\nQmvaa 12,128,16\nQcvaa 0,128,32,56\nv_add_u32 v75, 1, v75\nQmvaa 0,132,36\ns_waitcnt vmcnt(8)\n"
"ds_write_b32 v73, v75\nQmvaa 0,136,40\nQe 8\nQmvaa 0,140,44\nQe 9\nQcvaa 4,124,44,56\nQe 10\nQmvaa 4,120,40\n"
"Qe 11\nQmvaa 4,116,36\nQe 12\nQmvaa 4,112,32\ns_waitcnt lgkmcnt(1)\nQk 20,8,9\nQmvva 96,28,144\nQe 13\n"
"Qmvva 96,16,148\nQe 14\nQmvva 100,28,152\nQe 15\nQmvva 100,16,156\nQk 21,10,11\nQk 22,12,13\nQmvva 104,28,160\n"
"Qk 23,14,15\nQmvva 104,16,164\nQe 0\nQmvva 108,28,168\nQe 1\nQmvva 108,16,172\nQe 2\nQmvva 140,32,168\n"
"Qe 3\nQmvva 140,20,172\nQk 26,0,1\nQk 27,2,3\nQmvva 136,32,160\nds_read_b32 v66, v74\nQmvva 136,20,164\nQe 4\n"
"Qmvva 132,32,152\nQe 5\nQmvva 132,20,156\nQe 6\nQmvva 128,32,144\nQe 7\nQmvva 128,20,148\ns_waitcnt lgkmcnt(0)\n"
"v_cmp_gt_u32 vcc, v75, v66\nv_smfmac_f32_16x16x64_bf16 a[176:179], a[204:207], v[16:23], v77\ns_cbranch_vccnz .Lsa4\n.Lra4:\nQcvaa 8,112,48,56\ns_add_u32 m0, s61, 99072\nQk 24,4,5\nQmvaa 8,116,52\n"
"buffer_load_dwordx4 v69, s[40:43], s60 offen nt lds\nQmvaa 8,120,56\nQk 25,6,7\nQmvaa 8,124,60\ns_or_b32 s65, s65, 0x1d8000\ns_add_u32 s65, s65, 2048\nQcvaa 12,140,60,56\ns_andn2_b32 s65, s65, 0x1d8000\n"
"Qmvaa 12,136,56\nQmvaa 12,132,52\nbuffer_load_dwordx4 v69, s[40:43], s60 offen offset:1024 nt lds\nQmvaa 12,128,48\nQcvaa 0,128,64,56\ns_memrealtime s[74:75]\nQmvaa 0,132,68\nQe 8\n"
"Qmvaa 0,136,72\nQe 9\nQmvaa 0,140,76\nbuffer_load_dwordx4 v69, s[40:43], s90 offen offset:2048 nt lds\nQcvaa 4,124,76,56\nQe 10\nQmvaa 4,120,72\nQe 11\n"
"Qmvaa 4,116,68\nQe 12\nQmvaa 4,112,64\nQe 13\nQmvva 160,36,144\nbuffer_load_dwordx4 v69, s[40:43], s90 offen offset:3072 nt lds\nQmvva 160,24,148\nQe 14\n"
"Qmvva 164,36,152\nQe 15\nQmvva 164,24,156\nQk 28,8,9\nQk 29,10,11\nQmvva 168,36,160\ns_add_u32 m0, s64, 99072\nQk 30,12,13\n"
"Qmvva 168,24,164\nbuffer_load_dwordx4 v72, s[44:47], s60 offen nt lds\nQmvva 172,36,168\nQk 31,14,15\nQmvva 172,24,172\nQe 0\nQmvva 204,40,168\nQe 1\n"
"Qmvva 204,28,172\nQe 2\nQmvva 200,40,160\nbuffer_load_dwordx4 v72, s[44:47], s60 offen offset:1024 nt lds\nQmvva 200,28,164\nQe 3\nQmvva 196,40,152\nQk 34,0,1\n"
"Qk 35,2,3\nQmvva 196,28,156\nQe 4\nQmvva 192,40,144\nQe 5\nQmvva 192,28,148\nbuffer_load_dwordx4 v72, s[44:47], s90 offen offset:2048 nt lds\nv_smfmac_f32_16x16x64_bf16 a[180:183], a[204:207], v[24:31], v77\n"
"Qk 32,4,5\nQcvaa 8,112,80,56\nQe 6\nQmvaa 8,116,84\nQe 7\nQmvaa 8,120,88\nQk 33,6,7\nQmvaa 8,124,92\n"
"buffer_load_dwordx4 v72, s[44:47], s90 offen offset:3072 nt lds\nQcvaa 12,140,92,56\ns_waitcnt lgkmcnt(0)\nQmvaa 12,136,88\ns_cmp_gt_u32 s77, s74\ns_cbranch_scc1 .Lsp4\n.Lrp4:\nQmvaa 12,132,84\n"
"s_add_u32 s77, s74, 220\nQmvaa 12,128,80\ns_xor_b32 s60, s65, %1\ns_add_u32 s90, s60, 260096\nQcvaa 0,128,96,56\nQda 128,68,34048\ns_add_u32 s79, s79, s31\nQmvaa 0,132,100\n"
"Qda 132,68,34560\ns_max_u32 s77, s77, s79\nQmvaa 0,136,104\nQda 136,68,34304\nQmvaa 0,140,108\nQda 140,68,34816\nQcvaa 4,124,108,56\nQda 124,68,33792\n"
"Qmvaa 4,120,104\nQda 120,68,33280\nQmvaa 4,116,100\nQda 116,68,33536\nQmvaa 4,112,96\nQda 112,68,33024\nQmvva 224,44,144\nQe 8\n"
"Qmvva 224,32,148\nQe 9\nQmvva 228,44,152\nQe 10\nQmvva 228,32,156\nQe 11\nQmvva 232,44,160\nQk 36,8,9\n"
"Qk 37,10,11\nQmvva 232,32,164\nQe 12\nQmvva 236,44,168\nQe 13\nQmvva 236,32,172\ns_waitcnt lgkmcnt(0)\nQk 38,12,13\n"
"Qcvaa 8,112,0,56\nQe 14\nQmvaa 8,116,4\nv_add_u32 v68, 66048, v68\nQmvaa 8,120,8\nQe 15\nQmvaa 8,124,12\nQk 39,14,15\n"
"Qcvaa 12,140,12,56\nQe 0\nQmvaa 12,136,8\nQe 1\nQmvaa 12,132,4\nQe 2\nQmvaa 12,128,0\nQe 3\n"
"v_smfmac_f32_16x16x64_bf16 a[184:187], a[204:207], v[32:39], v77\nQk 42,0,1\nQmava 208,48,144\nQe 4\nQmava 208,36,148\nQe 5\nQmava 212,48,152\nQe 6\n"
"Qmava 212,36,156\nQe 7\nQmava 216,48,160\nQk 43,2,3\nQk 40,4,5\nQmava 216,36,164\nQk 41,6,7\nQmava 220,48,168\n"
"Qe 8\nQmava 220,36,172\nQe 9\nQmava 252,52,168\nQta 168,70,768\nQta 170,70,1792\nQmava 252,40,172\nQe 10\n"
"Qmava 248,52,160\nQta 172,70,33792\nQta 174,70,34816\nQmava 248,40,164\nQe 11\nQmava 244,52,152\nQta 160,70,512\nQta 162,70,1536\n"
"Qmava 244,40,156\nQk 44,8,9\nQk 45,10,11\nQmava 240,52,144\nQta 164,70,33536\nQta 166,70,34560\nQmava 240,40,148\nQe 12\n"
"Qcvaa 0,112,16,56\nQta 152,70,256\nQta 154,70,1280\nQmvaa 0,116,20\nQe 13\nQmvaa 0,120,24\nQta 156,70,33280\nQta 158,70,34304\n"
"Qmvaa 0,124,28\nQe 14\nQcvaa 4,140,28,56\nQta 144,70,0\nQta 146,70,1024\nQmvaa 4,136,24\nQe 15\nQmvaa 4,132,20\n"
"Qta 148,70,33024\nQta 150,70,34048\nQmvaa 4,128,16\nQk 46,12,13\nQk 47,14,15\nQcvaa 8,128,32,56\nv_add_u32 v75, 1, v75\nQmvaa 8,132,36\n"
"s_waitcnt vmcnt(8)\nds_write_b32 v73, v75\nQmvaa 8,136,40\nQe 0\nQmvaa 8,140,44\nQe 1\nQcvaa 12,124,44,56\nQe 2\n"
"Qmvaa 12,120,40\nQe 3\nQmvaa 12,116,36\nQe 4\nQmvaa 12,112,32\ns_waitcnt lgkmcnt(1)\nQk 48,0,1\nv_smfmac_f32_16x16x64_bf16 a[188:191], a[204:207], v[40:47], v77\n"
"Qk 49,2,3\nQmvva 80,16,144\nQe 5\nQmvva 80,44,148\nQe 6\nQmvva 84,16,152\nQe 7\nQmvva 84,44,156\n"
"Qk 50,4,5\nQk 51,6,7\nQmvva 88,16,160\nQe 8\nQmvva 88,44,164\nQe 9\nQmvva 92,16,168\nQe 10\n"
"Qmvva 92,44,172\nQe 11\nQmvva 124,20,168\nQk 54,8,9\nQk 55,10,11\nQmvva 124,48,172\nds_read_b32 v66, v74\nQmvva 120,20,160\n"
"Qe 12\nQmvva 120,48,164\nQe 13\nQmvva 116,20,152\nQe 14\nQmvva 116,48,156\nQe 15\nQmvva 112,20,144\n"
"s_waitcnt lgkmcnt(0)\nv_cmp_gt_u32 vcc, v75, v66\nQmvva 112,48,148\ns_cbranch_vccnz .Lsa5\n.Lra5:\nQk 52,12,13\nQcvaa 0,112,48,56\ns_add_u32 m0, s61, 0\n"
"Qk 53,14,15\nQmvaa 0,116,52\nbuffer_load_dwordx4 v69, s[40:43], s60 offen nt lds\nQmvaa 0,120,56\nQmvaa 0,124,60\ns_add_u32 s65, s65, 2048\nQcvaa 4,140,60,56\nQmvaa 4,136,56\n"
"Qmvaa 4,132,52\nbuffer_load_dwordx4 v69, s[40:43], s60 offen offset:1024 nt lds\nQmvaa 4,128,48\nQcvaa 8,128,64,56\ns_nop 0\nQmvaa 8,132,68\nQe 0\nQmvaa 8,136,72\n"
"Qe 1\nQmvaa 8,140,76\nbuffer_load_dwordx4 v69, s[40:43], s90 offen offset:2048 nt lds\nQcvaa 12,124,76,56\nQe 2\nQmvaa 12,120,72\nQe 3\nQmvaa 12,116,68\n"
"Qe 4\nQmvaa 12,112,64\nQe 5\nv_smfmac_f32_16x16x64_bf16 a[192:195], a[204:207], v[48:55], v77\nQk 20,0,1\nQmvva 144,24,144\nbuffer_load_dwordx4 v69, s[40:43], s90 offen offset:3072 nt lds\nQmvva 144,52,148\n"
"Qe 6\nQmvva 148,24,152\nQe 7\nQmvva 148,52,156\ns_add_u32 m0, s64, 0\nQk 21,2,3\nQmvva 152,24,160\nbuffer_load_dwordx4 v72, s[44:47], s60 offen nt lds\n"
"Qmvva 152,52,164\nQk 22,4,5\nQk 23,6,7\nQmvva 156,24,168\n.Lpg7:\ns_cbranch_execz .Ltr9\nQe 8\nQmvva 156,52,172\n"
"Qe 9\nQmvva 188,28,168\nQe 10\nQmvva 188,20,172\nbuffer_load_dwordx4 v72, s[44:47], s60 offen offset:1024 nt lds\nQmvva 184,28,160\nQe 11\nQmvva 184,20,164\n"
"Qk 26,8,9\nQk 27,10,11\nQmvva 180,28,152\nQe 12\nQmvva 180,20,156\nQe 13\nQmvva 176,28,144\nbuffer_load_dwordx4 v72, s[44:47], s90 offen offset:2048 nt lds\n"
"Qmvva 176,20,148\nQe 14\nQcvaa 0,112,80,56\nQe 15\nQmvaa 0,116,84\nQk 24,12,13\nQk 25,14,15\nQmvaa 0,120,88\n"
"Qmvaa 0,124,92\nbuffer_load_dwordx4 v72, s[44:47], s90 offen offset:3072 nt lds\nQcvaa 4,140,92,56\nQmvaa 4,136,88\nQmvaa 4,132,84\nQmvaa 4,128,80\ns_xor_b32 s60, s65, %1\ns_add_u32 s90, s60, 260096\n"
"Qcvaa 8,128,96,56\nQda 128,68,1024\nQmvaa 8,132,100\nQda 132,68,1536\nQmvaa 8,136,104\nQda 136,68,1280\nQmvaa 8,140,108\nQda 140,68,1792\n"
"Qcvaa 12,124,108,56\nQda 124,68,768\nQmvaa 12,120,104\nQda 120,68,256\nQmvaa 12,116,100\nQda 116,68,512\nQmvaa 12,112,96\nQda 112,68,0\n"
"v_smfmac_f32_16x16x64_bf16 a[196:199], a[204:207], v[20:27], v77\nQmvva 208,32,144\nQe 0\nQmvva 208,24,148\nQe 1\nQmvva 212,32,152\nQe 2\nQmvva 212,24,156\n"
"Qe 3\nQmvva 216,32,160\nQk 28,0,1\nQk 29,2,3\nQmvva 216,24,164\nQe 4\nQmvva 220,32,168\nQe 5\n"
"Qmvva 220,24,172\ns_waitcnt lgkmcnt(0)\nQk 30,4,5\nQcvaa 0,112,0,56\nQe 6\nQmvaa 0,116,4\nQe 7\nQmvaa 0,120,8\n"
"Qk 31,6,7\nQmvaa 0,124,12\nQe 8\nQcvaa 4,140,12,56\nQe 9\nQmvaa 4,136,8\nQe 10\nQmvaa 4,132,4\n"
"Qe 11\nQmvaa 4,128,0\nQe 12\nQmvva 240,36,144\nQe 13\nQmvva 240,28,148\nQe 14\nQmvva 244,36,152\n"
"Qe 15\nQmvva 244,28,156\nQk 34,8,9\nQk 35,10,11\nQmvva 248,36,160\nQk 32,12,13\nQk 33,14,15\nQmvva 248,28,164\n"
"Qe 0\nQmvva 252,36,168\nQe 1\nQmvva 252,28,172\nQe 2\nQmava 236,40,168\nQta 168,70,33920\nQta 170,70,34944\n"
"Qmava 236,32,172\nQe 3\nQmava 232,40,160\nQta 172,71,896\nQta 174,71,1920\nQmava 232,32,164\nQk 36,0,1\nQk 37,2,3\n"
"Qmava 228,40,152\nQta 160,70,33664\nQta 162,70,34688\nQmava 228,32,156\nQe 4\nQmava 224,40,144\nQta 164,71,640\nQta 166,71,1664\n"
"Qmava 224,32,148\nQe 5\nv_smfmac_f32_16x16x64_bf16 a[200:203], a[204:207], v[28:35], v77\nQta 152,70,33408\nQta 154,70,34432\nQcvaa 8,112,16,56\nQe 6\nQmvaa 8,116,20\n"
"Qta 156,71,384\nQta 158,71,1408\nQmvaa 8,120,24\nQe 7\nQmvaa 8,124,28\nQta 144,70,33152\nQta 146,70,34176\nQcvaa 12,140,28,56\n"
"Qk 38,4,5\nQk 39,6,7\nQmvaa 12,136,24\nQta 148,71,128\nQta 150,71,1152\nQmvaa 12,132,20\nQmvaa 12,128,16\ns_add_u32 s66, s66, 1\n"
"s_bitcmp1_b32 0x7f7f, s66\ns_cbranch_scc1 .Lloop\ns_cmp_lg_u32 s67, 0\ns_cbranch_scc1 .Ltail1\nQcvaa 0,128,32,56\nv_add_u32 v75, 1, v75\nQmvaa 0,132,36\ns_waitcnt vmcnt(8)\n"
"ds_write_b32 v73, v75\nQmvaa 0,136,40\nQe 8\nQmvaa 0,140,44\nQe 9\nQcvaa 4,124,44,56\nQe 10\nQmvaa 4,120,40\n"
"Qe 11\nQmvaa 4,116,36\nQe 12\nQmvaa 4,112,32\ns_waitcnt lgkmcnt(1)\nQk 40,8,9\nQmvva 96,44,144\nQe 13\n"
"Qmvva 96,36,148\nQe 14\nQmvva 100,44,152\nQe 15\nQmvva 100,36,156\nQk 41,10,11\nQk 42,12,13\nQmvva 104,44,160\n"
"Qk 43,14,15\nQmvva 104,36,164\nQe 0\nQmvva 108,44,168\nQe 1\nQmvva 108,36,172\nQe 2\nQmvva 140,48,168\n"
"Qe 3\nQmvva 140,40,172\nQk 46,0,1\nQk 47,2,3\nQmvva 136,48,160\nds_read_b32 v66, v74\nQmvva 136,40,164\nQe 4\n"
"Qmvva 132,48,152\nQe 5\nQmvva 132,40,156\nQe 6\nQmvva 128,48,144\nQe 7\nQmvva 128,40,148\ns_waitcnt lgkmcnt(0)\n"
"v_cmp_gt_u32 vcc, v75, v66\nv_smfmac_f32_16x16x64_bf16 a[176:179], a[204:207], v[36:43], v77\ns_cbranch_vccnz .Lsa30\n.Lra30:\nQcvaa 8,112,48,56\ns_add_u32 m0, s62, 0\ns_add_u32 s69, s87, 0\nQk 44,4,5\n"
"Qmvaa 8,116,52\nbuffer_load_dwordx4 v62, s[52:55], s69 offen lds\nQmvaa 8,120,56\nQk 45,6,7\nQmvaa 8,124,60\nQcvaa 12,140,60,56\nQmvaa 12,136,56\ns_add_u32 s69, s87, 64504\n"
"Qmvaa 12,132,52\nbuffer_load_dwordx4 v62, s[52:55], s69 offen offset:1032 lds\nQmvaa 12,128,48\nQcvaa 0,128,64,56\nQmvaa 0,132,68\nQe 8\nQmvaa 0,136,72\ns_add_u32 s69, s87, 129008\n"
"Qe 9\nQmvaa 0,140,76\nbuffer_load_dwordx4 v62, s[52:55], s69 offen offset:2064 lds\nQcvaa 4,124,76,56\nQe 10\nQmvaa 4,120,72\nQe 11\nQmvaa 4,116,68\n"
"Qe 12\nQmvaa 4,112,64\ns_add_u32 s69, s87, 193512\nQe 13\nQmvva 160,52,144\nbuffer_load_dwordx4 v62, s[52:55], s69 offen offset:3096 lds\nQmvva 160,44,148\nQe 14\n"
"Qmvva 164,52,152\nQe 15\nQmvva 164,44,156\nQk 48,8,9\nQk 49,10,11\nQmvva 168,52,160\ns_add_u32 m0, s62, 16512\ns_add_u32 s69, s87, 128\n"
"Qk 50,12,13\nQmvva 168,44,164\nbuffer_load_dwordx4 v62, s[52:55], s69 offen lds\nQmvva 172,52,168\nQk 51,14,15\nQmvva 172,44,172\nQe 0\nQmvva 204,20,168\n"
"Qe 1\nQmvva 204,48,172\ns_add_u32 s69, s87, 64632\nQe 2\nQmvva 200,20,160\nbuffer_load_dwordx4 v62, s[52:55], s69 offen offset:1032 lds\nQmvva 200,48,164\nQe 3\n"
"Qmvva 196,20,152\nQk 18,0,1\nQk 19,2,3\nQmvva 196,48,156\nQe 4\nQmvva 192,20,144\ns_add_u32 s69, s87, 129136\nQe 5\n"
"Qmvva 192,48,148\nbuffer_load_dwordx4 v62, s[52:55], s69 offen offset:2064 lds\nv_smfmac_f32_16x16x64_bf16 a[180:183], a[204:207], v[44:51], v77\nQk 16,4,5\nQcvaa 8,112,80,56\nQe 6\nQmvaa 8,116,84\nQe 7\n"
"Qmvaa 8,120,88\ns_add_u32 s69, s87, 193640\nQk 17,6,7\nQmvaa 8,124,92\nbuffer_load_dwordx4 v62, s[52:55], s69 offen offset:3096 lds\nQcvaa 12,140,92,56\nQmvaa 12,136,88\nQmvaa 12,132,84\n"
"Qmvaa 12,128,80\nQcvaa 0,128,96,56\nQda 128,68,34048\nQmvaa 0,132,100\nQda 132,68,34560\nQmvaa 0,136,104\nQda 136,68,34304\nQmvaa 0,140,108\n"
"Qda 140,68,34816\nQcvaa 4,124,108,56\nQda 124,68,33792\nQmvaa 4,120,104\nQda 120,68,33280\nQmvaa 4,116,100\nQda 116,68,33536\nQmvaa 4,112,96\n"
"Qda 112,68,33024\nQmvva 224,24,144\nQe 8\nQmvva 224,16,148\ns_add_u32 m0, s63, 33024\ns_add_u32 s88, s86, 0\nQe 9\nQmvva 228,24,152\n"
"buffer_load_dwordx4 v64, s[48:51], s88 offen lds\nQe 10\nQmvva 228,16,156\nQe 11\nQmvva 232,24,160\nQk 20,8,9\nQk 21,10,11\nQmvva 232,16,164\n"
"s_add_u32 s88, s86, 19456\nQe 12\nQmvva 236,24,168\nbuffer_load_dwordx4 v64, s[48:51], s88 offen offset:1024 lds\nQe 13\nQmvva 236,16,172\ns_waitcnt lgkmcnt(0)\nQk 22,12,13\n"
"Qcvaa 8,112,0,56\nQe 14\nQmvaa 8,116,4\nv_subrev_u32 v68, 66048, v68\ns_add_u32 s88, s86, 38912\nQmvaa 8,120,8\nbuffer_load_dwordx4 v64, s[48:51], s88 offen offset:2048 lds\nQe 15\n"
"Qmvaa 8,124,12\nQk 23,14,15\nQcvaa 12,140,12,56\nQe 0\nQmvaa 12,136,8\ns_add_u32 s88, s86, 58368\nQe 1\nQmvaa 12,132,4\n"
"buffer_load_dwordx4 v64, s[48:51], s88 offen offset:3072 lds\nQe 2\nQmvaa 12,128,0\nQe 3\nv_smfmac_f32_16x16x64_bf16 a[184:187], a[204:207], v[16:23], v77\nQk 26,0,1\nQmava 208,28,144\ns_add_u32 m0, s63, 37120\n"
"s_add_u32 s88, s86, 81920\nQe 4\nQmava 208,20,148\nbuffer_load_dwordx4 v64, s[48:51], s88 offen lds\nQe 5\nQmava 212,28,152\nQe 6\nQmava 212,20,156\n"
"Qe 7\nQmava 216,28,160\ns_add_u32 s88, s86, 101376\nQk 27,2,3\nQk 24,4,5\nQmava 216,20,164\nbuffer_load_dwordx4 v64, s[48:51], s88 offen offset:1024 lds\nQk 25,6,7\n"
"Qmava 220,28,168\nQe 8\nQmava 220,20,172\nQe 9\nQmava 252,32,168\nQta 168,71,768\nQta 170,71,1792\ns_add_u32 s88, s86, 120832\n"
"Qmava 252,24,172\nbuffer_load_dwordx4 v64, s[48:51], s88 offen offset:2048 lds\nQe 10\nQmava 248,32,160\nQta 172,71,33792\nQta 174,71,34816\nQmava 248,24,164\nQe 11\n"
"Qmava 244,32,152\nQta 160,71,512\nQta 162,71,1536\nQmava 244,24,156\nQk 28,8,9\nQk 29,10,11\nQmava 240,32,144\nQta 164,71,33536\n"
"Qta 166,71,34560\nQmava 240,24,148\nQe 12\nQcvaa 0,112,16,56\nQta 152,71,256\nQta 154,71,1280\nQmvaa 0,116,20\nQe 13\n"
"Qmvaa 0,120,24\nQta 156,71,33280\nQta 158,71,34304\nQmvaa 0,124,28\nQe 14\nQcvaa 4,140,28,56\nQta 144,71,0\nQta 146,71,1024\n"
"Qmvaa 4,136,24\nQe 15\nQmvaa 4,132,20\nQta 148,71,33024\nQta 150,71,34048\nQmvaa 4,128,16\nQk 30,12,13\nQk 31,14,15\n"
"Qcvaa 8,128,32,56\nv_add_u32 v75, 1, v75\nQmvaa 8,132,36\nds_write_b32 v73, v75\nQmvaa 8,136,40\nQe 0\nQmvaa 8,140,44\nQe 1\n"
"Qcvaa 12,124,44,56\nQe 2\nQmvaa 12,120,40\nQe 3\nQmvaa 12,116,36\nQe 4\nQmvaa 12,112,32\ns_waitcnt lgkmcnt(1)\n"
"Qk 32,0,1\nv_smfmac_f32_16x16x64_bf16 a[188:191], a[204:207], v[24:31], v77\nQk 33,2,3\nQmvva 80,36,144\nQe 5\nQmvva 80,28,148\nQe 6\nQmvva 84,36,152\n"
"Qe 7\nQmvva 84,28,156\nQk 34,4,5\nQk 35,6,7\nQmvva 88,36,160\nQe 8\nQmvva 88,28,164\nQe 9\n"
"Qmvva 92,36,168\nQe 10\nQmvva 92,28,172\nQe 11\nQmvva 124,40,168\nQk 38,8,9\nQk 39,10,11\nQmvva 124,32,172\n"
"ds_read_b32 v66, v74\nQmvva 120,40,160\nQe 12\nQmvva 120,32,164\nQe 13\nQmvva 116,40,152\nQe 14\nQmvva 116,32,156\n"
"Qe 15\nQmvva 112,40,144\ns_waitcnt lgkmcnt(0)\nv_cmp_gt_u32 vcc, v75, v66\nQmvva 112,32,148\ns_cbranch_vccnz .Lsa31\n.Lra31:\nQk 36,12,13\n"
"Qcvaa 0,112,48,56\ns_add_u32 m0, s62, 33024\ns_add_u32 s69, s87, 524288\nQk 37,14,15\nQmvaa 0,116,52\nbuffer_load_dwordx4 v62, s[52:55], s69 offen lds\nQmvaa 0,120,56\nQmvaa 0,124,60\n"
"Qcvaa 4,140,60,56\nQmvaa 4,136,56\ns_add_u32 s69, s87, 588792\nQmvaa 4,132,52\nbuffer_load_dwordx4 v62, s[52:55], s69 offen offset:1032 lds\nQmvaa 4,128,48\nQcvaa 8,128,64,56\nQmvaa 8,132,68\n"
"Qe 0\nQmvaa 8,136,72\ns_add_u32 s69, s87, 653296\nQe 1\nQmvaa 8,140,76\nbuffer_load_dwordx4 v62, s[52:55], s69 offen offset:2064 lds\nQcvaa 12,124,76,56\nQe 2\n"
"Qmvaa 12,120,72\nQe 3\nQmvaa 12,116,68\nQe 4\nQmvaa 12,112,64\nQe 5\nv_smfmac_f32_16x16x64_bf16 a[192:195], a[204:207], v[32:39], v77\ns_add_u32 s69, s87, 717800\n"
"Qk 40,0,1\nQmvva 144,44,144\nbuffer_load_dwordx4 v62, s[52:55], s69 offen offset:3096 lds\nQmvva 144,36,148\nQe 6\nQmvva 148,44,152\nQe 7\nQmvva 148,36,156\n"
"s_add_u32 m0, s62, 49536\ns_add_u32 s69, s87, 524416\nQk 41,2,3\nQmvva 152,44,160\nbuffer_load_dwordx4 v62, s[52:55], s69 offen lds\nQmvva 152,36,164\nQk 42,4,5\nQk 43,6,7\n"
"Qmvva 156,44,168\nQe 8\nQmvva 156,36,172\nQe 9\nQmvva 188,48,168\ns_add_u32 s69, s87, 588920\nQe 10\nQmvva 188,40,172\n"
"buffer_load_dwordx4 v62, s[52:55], s69 offen offset:1032 lds\nQmvva 184,48,160\nQe 11\nQmvva 184,40,164\nQk 46,8,9\nQk 47,10,11\nQmvva 180,48,152\nQe 12\n"
"Qmvva 180,40,156\ns_add_u32 s69, s87, 653424\nQe 13\nQmvva 176,48,144\nbuffer_load_dwordx4 v62, s[52:55], s69 offen offset:2064 lds\nQmvva 176,40,148\nQe 14\nQcvaa 0,112,80,56\n"
"Qe 15\nQmvaa 0,116,84\nQk 44,12,13\nQk 45,14,15\nQmvaa 0,120,88\ns_add_u32 s69, s87, 717928\nQmvaa 0,124,92\nbuffer_load_dwordx4 v62, s[52:55], s69 offen offset:3096 lds\n"
"Qcvaa 4,140,92,56\nQmvaa 4,136,88\nQmvaa 4,132,84\nQmvaa 4,128,80\nQcvaa 8,128,96,56\nQmvaa 8,132,100\nQmvaa 8,136,104\nQmvaa 8,140,108\n"
"Qcvaa 12,124,108,56\nQmvaa 12,120,104\nQmvaa 12,116,100\nQmvaa 12,112,96\nv_smfmac_f32_16x16x64_bf16 a[196:199], a[204:207], v[40:47], v77\nQmvva 208,16,144\nQta 112,71,33152\nQe 0\n"
"Qmvva 208,44,148\nQta 114,71,34176\nQe 1\nQmvva 212,16,152\nQta 116,71,33408\nQe 2\nQmvva 212,44,156\nQta 118,71,34432\n"
"Qe 3\nQmvva 216,16,160\nQta 120,71,33664\nQk 48,0,1\nQk 49,2,3\nQmvva 216,44,164\nQta 122,71,34688\nQe 4\n"
"Qmvva 220,16,168\n.Lpg8:\ns_cbranch_execz .Ltr10\nQta 124,71,33920\nQe 5\nQmvva 220,44,172\nQta 126,71,34944\nQk 50,4,5\n"
"Qe 6\nQe 7\ns_nop 0\nQk 51,6,7\nQe 8\nQe 9\nQe 10\nQe 11\n"
"Qe 12\nQmvva 240,20,144\nv_add_u32 v75, 1, v75\nQe 13\nQmvva 240,48,148\nds_write_b32 v73, v75\nQe 14\nQmvva 244,20,152\n"
"Qe 15\nQmvva 244,48,156\nQk 54,8,9\nQk 55,10,11\nQmvva 248,20,160\nQk 52,12,13\nQk 53,14,15\nQmvva 248,48,164\n"
"Qmvva 252,20,168\nQmvva 252,48,172\nv_smfmac_f32_16x16x64_bf16 a[200:203], a[204:207], v[48:55], v77\nQmava 224,24,144\nds_read_b32 v66, v74\nv_accvgpr_read_b32 v0, a176\nQmava 224,52,148\nv_accvgpr_read_b32 v1, a177\n"
"v_accvgpr_read_b32 v6, a178\nQmava 228,24,152\nv_accvgpr_read_b32 v7, a179\nv_accvgpr_read_b32 v8, a180\nQmava 228,52,156\nv_accvgpr_read_b32 v9, a181\nv_accvgpr_read_b32 v14, a182\nQmava 232,24,160\n"
"v_accvgpr_read_b32 v15, a183\nv_accvgpr_read_b32 v16, a184\nQmava 232,52,164\ns_waitcnt lgkmcnt(0)\nv_cmp_gt_u32 vcc, v75, v66\nQmava 236,24,168\ns_cbranch_vccnz .Lsa32\n.Lra32:\n"
"v_accvgpr_read_b32 v17, a185\nQmava 236,52,172\ns_waitcnt lgkmcnt(0)\nv_accvgpr_read_b32 v22, a186\nQmvva 96,28,112\ns_add_u32 m0, s63, 0\ns_add_u32 s88, s86, 128\nv_accvgpr_read_b32 v23, a187\n"
"Qmvva 100,28,116\nbuffer_load_dwordx4 v64, s[48:51], s88 offen lds\nQmvva 104,28,120\nv_accvgpr_read_b32 v24, a188\nv_accvgpr_read_b32 v25, a189\nQmvva 108,28,124\nv_accvgpr_read_b32 v2, a190\nv_accvgpr_read_b32 v3, a191\n"
"Qmvva 128,32,112\ns_add_u32 s88, s86, 19584\nv_accvgpr_read_b32 v4, a192\nQmvva 132,32,116\nbuffer_load_dwordx4 v64, s[48:51], s88 offen offset:1024 lds\nQmvva 136,32,120\nv_accvgpr_read_b32 v5, a193\nv_accvgpr_read_b32 v10, a194\n"
"Qmvva 140,32,124\nv_accvgpr_read_b32 v11, a195\nv_accvgpr_read_b32 v12, a196\nQmvva 160,36,112\ns_add_u32 s88, s86, 39040\nv_accvgpr_read_b32 v13, a197\nQmvva 164,36,116\nbuffer_load_dwordx4 v64, s[48:51], s88 offen offset:2048 lds\n"
"Qmvva 168,36,120\nv_accvgpr_read_b32 v18, a198\nv_accvgpr_read_b32 v19, a199\nQmvva 172,36,124\nv_accvgpr_read_b32 v20, a200\nv_accvgpr_read_b32 v21, a201\nQmvva 192,40,112\ns_add_u32 s88, s86, 58496\n"
"v_accvgpr_read_b32 v26, a202\nQmvva 196,40,116\nbuffer_load_dwordx4 v64, s[48:51], s88 offen offset:3072 lds\nQmvva 200,40,120\nv_accvgpr_read_b32 v27, a203\nQa 8,8,9\nQmvva 204,40,124\nQa 10,10,11\n"
"Qa 8,8,10\nQmvva 224,44,112\ns_add_u32 m0, s63, 4096\ns_add_u32 s88, s86, 82048\nQa 16,16,17\nQmvva 228,44,116\nbuffer_load_dwordx4 v64, s[48:51], s88 offen lds\nQmvva 232,44,120\n"
"Qa 18,18,19\nQa 16,16,18\nQmvva 236,44,124\nQa 4,4,5\nQa 6,6,7\nQmava 208,48,112\ns_add_u32 s88, s86, 101504\nQa 4,4,6\n"
"Qmava 212,48,116\nbuffer_load_dwordx4 v64, s[48:51], s88 offen offset:1024 lds\nQmava 216,48,120\nQa 12,12,13\nQa 14,14,15\nQmava 220,48,124\nQa 12,12,14\nQa 0,0,1\n"
"Qmava 240,52,112\ns_add_u32 s88, s86, 120960\nQa 2,2,3\nQmava 244,52,116\nbuffer_load_dwordx4 v64, s[48:51], s88 offen offset:2048 lds\nQmava 248,52,120\nQa 0,0,2\nQa 24,24,25\n"
"Qmava 252,52,124\nQa 26,26,27\nQa 24,24,26\nQa 20,20,21\nQa 22,22,23\nQa 20,20,22\nv_max3_u32 v9, v8, v16, v4\nv_max3_u32 v9, v9, v12, v0\n"
"v_max3_u32 v9, v9, v24, v20\nv_min3_u32 v11, v8, v16, v4\nv_min3_u32 v11, v11, v12, v0\nv_min3_u32 v11, v11, v24, v20\nv_cmp_lt_u32 vcc, 0x4e800000, v9\ns_mov_b64 s[74:75], vcc\nv_cmp_gt_u32 vcc, 0x35800000, v11\ns_or_b64 s[74:75], s[74:75], vcc\n"
"v_mov_b32 v30, s73\nv_mov_b32 v31, 0\nv_mov_b32 v58, 0xb9800000\nv_mov_b32 v59, 0\nv_and_b32 v66, 16, v60\nv_mov_b32 v67, 0xffff\nv_lshlrev_b32 v66, v66, v67\ns_mov_b32 vcc_lo, 0x00c0000c\n"
"s_mov_b32 vcc_hi, 0xc0000c00\nv_cndmask_b32 v67, 0, v66, vcc\ns_lshr_b64 vcc, vcc, 2\nv_cndmask_b32 v66, 0, v66, vcc\ns_cmp_lg_u64 s[74:75], 0\ns_cbranch_scc1 .Lfb\n.Lfbret0:\nv_rcp_f32 v2, v0\n"
"v_rcp_f32_e64 v6, v4\nv_mfma_f32_16x16x4_4b_bf16 a[208:223], v[30:31], v[58:59], a[208:223]\nv_rcp_f32 v10, v8\nv_rcp_f32 v14, v12\nv_rcp_f32 v18, v16\nv_rcp_f32 v22, v20\nv_rcp_f32 v26, v24\nv_mov_b64 v[0:1], 0\n"
"v_mfma_f32_16x16x4_4b_bf16 a[224:239], v[30:31], v[58:59], a[224:239]\nQk 2,2,2\nv_and_b32 v3, v2, v67\nv_and_b32 v2, v2, v66\nv_mov_b64_e64 v[4:5], 0\nQk 6,6,6\nv_and_b32_e64 v7, v6, v67\nv_mfma_f32_16x16x4_4b_bf16 a[240:255], v[30:31], v[58:59], a[240:255]\n"
"v_and_b32 v6, v6, v66\nv_mov_b64 v[8:9], 0\nQk 10,10,10\nv_and_b32 v11, v10, v67\nv_and_b32 v10, v10, v66\nv_mov_b64_e64 v[12:13], 0\nv_mfma_f32_16x16x4_4b_bf16 v[80:95], v[30:31], v[58:59], v[80:95]\nQk 14,14,14\n"
"v_and_b32 v15, v14, v67\nv_and_b32 v14, v14, v66\nv_mov_b64_e64 v[16:17], 0\nQk 18,18,18\nv_and_b32_e64 v19, v18, v67\nv_mfma_f32_16x16x4_4b_bf16 v[112:127], v[30:31], v[58:59], v[112:127]\nv_and_b32 v18, v18, v66\nv_mov_b64 v[20:21], 0\n"
"Qk 22,22,22\nv_and_b32 v23, v22, v67\nv_and_b32 v22, v22, v66\nv_mov_b64_e64 v[24:25], 0\nv_mfma_f32_16x16x4_4b_bf16 v[144:159], v[30:31], v[58:59], v[144:159]\nQk 26,26,26\nv_and_b32 v27, v26, v67\nv_and_b32 v26, v26, v66\n"
"v_mov_b64_e64 v[28:29], 0\nv_accvgpr_read_b32 v48, a208\nv_accvgpr_read_b32 v49, a209\nv_accvgpr_read_b32 v50, a210\nv_accvgpr_read_b32 v51, a211\nv_accvgpr_read_b32 v52, a212\nv_accvgpr_read_b32 v53, a213\nv_accvgpr_read_b32 v54, a214\n"
"v_accvgpr_read_b32 v55, a215\nQk 48,48,49\nQk 49,50,51\nQk 50,52,53\nQk 51,54,55\nv_accvgpr_read_b32 v32, a216\nv_accvgpr_read_b32 v33, a217\nQzvvv 52,48,20\n"
"v_accvgpr_read_b32 v34, a218\nv_accvgpr_read_b32 v35, a219\nQzvvv 48,48,22\nv_accvgpr_read_b32 v36, a220\nv_accvgpr_read_b32 v37, a221\nv_accvgpr_read_b32 v38, a222\nv_accvgpr_read_b32 v39, a223\nQk 32,32,33\n"
"Qk 33,34,35\nQk 34,36,37\nQk 35,38,39\nv_mfma_f32_16x16x4_4b_bf16 v[240:255], v[30:31], v[58:59], v[240:255]\nv_accvgpr_read_b32 v40, a224\nv_accvgpr_read_b32 v41, a225\nv_accvgpr_read_b32 v42, a226\nv_accvgpr_read_b32 v43, a227\n"
"v_accvgpr_read_b32 v44, a228\nv_accvgpr_read_b32 v45, a229\nQzvvv 36,32,20\nv_accvgpr_read_b32 v46, a230\nv_accvgpr_read_b32 v47, a231\nQzvvv 32,32,22\nQk 40,40,41\nQk 41,42,43\n"
"Qk 42,44,45\nQk 43,46,47\nQk 48,48,49\nQk 49,50,51\nQzvvv 44,40,24\nQk 50,52,53\nQk 51,54,55\nQzvvv 40,40,26\n"
"v_accvgpr_write_b32 a88, v48\nv_accvgpr_write_b32 a89, v49\nv_accvgpr_write_b32 a90, v50\nv_accvgpr_write_b32 a91, v51\nv_mfma_f32_16x16x4_4b_bf16 v[96:111], v[30:31], v[58:59], v[96:111]\nv_accvgpr_read_b32 v48, a232\nv_accvgpr_read_b32 v49, a233\nv_accvgpr_read_b32 v50, a234\n"
"v_accvgpr_read_b32 v51, a235\nv_accvgpr_read_b32 v52, a236\nv_accvgpr_read_b32 v53, a237\nv_accvgpr_read_b32 v54, a238\nv_accvgpr_read_b32 v55, a239\nQk 48,48,49\nQk 49,50,51\nQk 50,52,53\n"
"Qk 51,54,55\nQk 32,32,33\nQk 33,34,35\nQzvvv 52,48,24\nQk 34,36,37\nQk 35,38,39\nQzvvv 48,48,26\nv_accvgpr_write_b32 a92, v32\n"
"v_accvgpr_write_b32 a93, v33\nv_accvgpr_write_b32 a94, v34\nv_accvgpr_write_b32 a95, v35\nv_mfma_f32_16x16x4_4b_bf16 v[176:191], v[30:31], v[58:59], v[176:191]\nv_accvgpr_read_b32 v32, a240\nv_accvgpr_read_b32 v33, a241\nv_accvgpr_read_b32 v34, a242\nv_accvgpr_read_b32 v35, a243\n"
"v_accvgpr_read_b32 v36, a244\nv_accvgpr_read_b32 v37, a245\nv_accvgpr_read_b32 v38, a246\nv_accvgpr_read_b32 v39, a247\nQk 32,32,33\nQk 33,34,35\nQk 34,36,37\nQk 35,38,39\n"
"Qk 40,40,41\nQk 41,42,43\nQzvvv 36,32,24\nQk 42,44,45\nQk 43,46,47\nQzvvv 32,32,26\nv_accvgpr_write_b32 a96, v40\nv_accvgpr_write_b32 a97, v41\n"
"v_accvgpr_write_b32 a98, v42\nv_accvgpr_write_b32 a99, v43\nv_mfma_f32_16x16x4_4b_bf16 v[128:143], v[30:31], v[58:59], v[128:143]\nv_accvgpr_read_b32 v40, a248\nv_accvgpr_read_b32 v41, a249\nv_accvgpr_read_b32 v42, a250\nv_accvgpr_read_b32 v43, a251\nv_accvgpr_read_b32 v44, a252\n"
"v_accvgpr_read_b32 v45, a253\nv_accvgpr_read_b32 v46, a254\nv_accvgpr_read_b32 v47, a255\nQk 40,40,41\nQk 41,42,43\ns_mov_b64 vcc, -1\ns_nop 0\nQk 42,44,45\n"
"Qk 43,46,47\nQk 48,48,49\ns_waitcnt vmcnt(15)\ns_nop 0\nQk 49,50,51\nQzvvv 44,40,24\nQk 50,52,53\ns_barrier\n"
"s_nop 0\nQk 51,54,55\nQzvvv 40,40,26\nv_accvgpr_write_b32 a100, v48\nQta 112,61,0\nQta 114,61,512\nQda 208,63,33024\nQda 212,63,34048\n"
"v_accvgpr_write_b32 a101, v49\nv_accvgpr_write_b32 a102, v50\nv_accvgpr_write_b32 a103, v51\nv_mfma_f32_16x16x4_4b_bf16 v[160:175], v[30:31], v[58:59], v[160:175]\nQk 80,80,81\nQk 81,82,83\nQk 82,84,85\nQk 83,86,87\n"
"Qk 88,88,89\nQda 216,63,35072\nQzvvv 84,80,0\nQda 220,63,36096\nQda 224,63,37120\nQzvvv 80,80,2\nQda 228,63,38144\nQk 89,90,91\n"
"v_mfma_f32_16x16x4_4b_bf16 v[208:223], v[30:31], v[58:59], v[208:223]\nQk 90,92,93\nQk 91,94,95\nQk 112,112,113\nQk 113,114,115\nQk 114,116,117\nQk 115,118,119\nQzvvv 92,88,0\n"
"Qk 80,80,81\nQk 81,82,83\nQzvvv 88,88,2\nQda 232,63,39168\nQda 176,63,33536\nv_mfma_f32_16x16x4_4b_bf16 v[192:207], v[30:31], v[58:59], v[192:207]\nQta 116,61,4128\nQta 118,61,4640\n"
"Qk 82,84,85\nQk 83,86,87\nv_accvgpr_write_b32 a0, v80\nv_accvgpr_write_b32 a1, v81\nQzvvv 116,112,4\nv_accvgpr_write_b32 a2, v82\nv_accvgpr_write_b32 a3, v83\nQzvvv 112,112,6\n"
"Qk 120,120,121\nQk 121,122,123\nv_mfma_f32_16x16x4_4b_bf16 v[224:239], v[30:31], v[58:59], v[224:239]\nQk 122,124,125\nQta 120,61,8\nQta 122,61,520\nQta 124,61,4136\nQta 126,61,4648\n"
"Qk 123,126,127\nQk 88,88,89\nQk 89,90,91\nQzvvv 124,120,4\nQk 90,92,93\nQk 91,94,95\nQzvvv 120,120,6\nv_accvgpr_write_b32 a4, v88\n"
"v_accvgpr_write_b32 a5, v89\nv_accvgpr_write_b32 a6, v90\nv_accvgpr_write_b32 a7, v91\nQk 144,144,145\nQta 128,61,256\nQta 130,61,768\nQta 132,61,4384\nQta 134,61,4896\n"
"Qk 145,146,147\nQk 146,148,149\nQk 147,150,151\nQk 112,112,113\nQk 113,114,115\nQzvvv 148,144,8\nQk 114,116,117\nQk 115,118,119\n"
"Qzvvv 144,144,10\nv_accvgpr_write_b32 a16, v112\nv_accvgpr_write_b32 a17, v113\nv_accvgpr_write_b32 a18, v114\nQta 136,61,264\nQta 138,61,776\nQta 140,61,4392\nQta 142,61,4904\n"
"v_accvgpr_write_b32 a19, v115\nQk 240,240,241\nQk 241,242,243\nQk 242,244,245\nQk 243,246,247\nQk 120,120,121\nQk 121,122,123\nQzvvv 244,240,20\n"
"Qk 122,124,125\nQk 123,126,127\nQzvvv 240,240,22\nv_accvgpr_write_b32 a20, v120\ns_waitcnt lgkmcnt(15)\ns_nop 0\nv_accvgpr_write_b32 a21, v121\nv_accvgpr_write_b32 a22, v122\n"
"v_accvgpr_write_b32 a23, v123\nQzvaa 80,112,208\nQk 248,248,249\nQk 249,250,251\nQk 250,252,253\nQzvaa 84,112,212\nQda 180,63,34560\nQk 251,254,255\n"
"Qk 144,144,145\nQk 145,146,147\nQzvvv 252,248,20\nQk 146,148,149\nQk 147,150,151\nQzvaa 88,112,216\nQda 184,63,35584\nv_accvgpr_write_b32 a32, v144\n"
"Qzvvv 248,248,22\nv_accvgpr_write_b32 a33, v145\nv_accvgpr_write_b32 a34, v146\nv_accvgpr_write_b32 a35, v147\nQk 152,152,153\nQzvaa 92,112,220\nQda 188,63,36608\nQk 153,154,155\n"
"Qk 154,156,157\nQk 155,158,159\nQk 240,240,241\nQk 241,242,243\nQzvvv 156,152,8\nQk 242,244,245\nQk 243,246,247\nQzvaa 112,112,224\n"
"Qda 192,63,37632\nv_accvgpr_write_b32 a80, v240\nQzvvv 152,152,10\nv_accvgpr_write_b32 a81, v241\nv_accvgpr_write_b32 a82, v242\nQzvaa 116,112,228\nQda 196,63,38656\nv_accvgpr_write_b32 a83, v243\n"
"Qk 96,96,97\nQk 97,98,99\nQk 98,100,101\nQk 99,102,103\nQk 248,248,249\nQzvaa 120,112,232\nQda 200,63,39680\nQk 249,250,251\n"
"Qzvvv 100,96,0\nQk 250,252,253\nQk 251,254,255\nQzvvv 96,96,2\nv_accvgpr_write_b32 a84, v248\nv_max3_f32 v80, |v80|, |v81|, |v82|\nv_accvgpr_write_b32 a85, v249\nv_accvgpr_write_b32 a86, v250\n"
"v_accvgpr_write_b32 a87, v251\nv_max3_f32 v84, |v84|, |v85|, |v86|\nQk 104,104,105\nQk 105,106,107\nQk 106,108,109\nv_max3_f32 v88, |v88|, |v89|, |v90|\nQk 107,110,111\nQk 152,152,153\n"
"Qk 153,154,155\nv_max3_f32 v92, |v92|, |v93|, |v94|\nQzvvv 108,104,0\nQk 154,156,157\nQk 155,158,159\nv_max3_f32 v112, |v112|, |v113|, |v114|\nQzvvv 104,104,2\nv_accvgpr_write_b32 a36, v152\n"
"v_accvgpr_write_b32 a37, v153\nv_max3_f32 v116, |v116|, |v117|, |v118|\nv_accvgpr_write_b32 a38, v154\nv_accvgpr_write_b32 a39, v155\nQk 176,176,177\nv_max3_f32 v120, |v120|, |v121|, |v122|\nQk 177,178,179\nQk 178,180,181\n"
"Qk 179,182,183\nv_min3_u32 v56, v80, v84, v88\nQk 128,128,129\nQzvvv 180,176,12\nv_min3_u32 v56, v56, v92, v112\nQk 129,130,131\nQzvvv 176,176,14\nQk 130,132,133\n"
"Qk 131,134,135\nv_min3_u32 v56, v56, v116, v120\nQk 136,136,137\nQzvvv 132,128,4\nv_mov_b32_e64 v57, v56\nQk 137,138,139\nQzvvv 128,128,6\nQk 138,140,141\n"
"Qk 139,142,143\nv_permlane32_swap_b32_e64 v56, v57\nQk 176,176,177\nQk 177,178,179\nQzvvv 140,136,4\nv_max_f32_e64 v56, v56, v57\nQk 178,180,181\nQk 179,182,183\n"
"Qzvvv 136,136,6\nv_mov_b32_e64 v57, v56\nv_accvgpr_write_b32 a48, v176\nv_accvgpr_write_b32 a49, v177\nv_accvgpr_write_b32 a50, v178\nv_permlane16_swap_b32_e64 v56, v57\nv_accvgpr_write_b32 a51, v179\nQk 184,184,185\n"
"Qk 185,186,187\nv_max_f32_e64 v56, v56, v57\nQk 186,188,189\nQk 187,190,191\nv_min_f32_dpp v56, v56, v56 row_ror:8 row_mask:0xf bank_mask:0xf\nQk 160,160,161\nQzvvv 188,184,12\nQk 161,162,163\n"
"v_min_f32_dpp v56, v56, v56 row_ror:4 row_mask:0xf bank_mask:0xf\nQzvvv 184,184,14\nQk 162,164,165\nQk 163,166,167\n.Lpg9:\ns_cbranch_execz .Ltr10\ns_nop 0\nv_min_f32_dpp v56, v56, v56 row_ror:2 row_mask:0xf bank_mask:0xf\n"
"Qk 168,168,169\nQzvvv 164,160,8\nQk 169,170,171\nv_min_f32_dpp v56, v56, v56 row_ror:1 row_mask:0xf bank_mask:0xf\nQzvvv 160,160,10\nQk 170,172,173\nQk 171,174,175\nQk 184,184,185\n"
"v_and_b32 v56, 0x7f800000, v56\nQk 185,186,187\nQzvvv 172,168,8\nQk 186,188,189\nv_min_u32 v56, 0x78000000, v56\nQk 187,190,191\nQzvvv 168,168,10\nv_accvgpr_write_b32 a52, v184\n"
"v_add_u32 v56, 0x5400000, v56\nv_accvgpr_write_b32 a53, v185\nv_accvgpr_write_b32 a54, v186\nv_accvgpr_write_b32 a55, v187\nv_mov_b32_e64 v57, v56\nQk 208,208,209\nQk 209,210,211\nQk 210,212,213\n"
"v_mov_b64_e64 v[58:59], v[56:57]\nQk 211,214,215\nv_mul_f32 v52, -v56, s72\nQk 192,192,193\nQzvvv 212,208,16\nQk 193,194,195\nQk 194,196,197\nQzvvv 208,208,18\n"
"Qk 195,198,199\nQk 200,200,201\nQk 201,202,203\nQzvvv 196,192,12\nQk 202,204,205\nQk 203,206,207\nQzvvv 192,192,14\nQk 216,216,217\n"
"Qk 208,208,209\nQk 209,210,211\nQzvvv 204,200,12\nQk 210,212,213\nQk 211,214,215\nQzvvv 200,200,14\nv_accvgpr_write_b32 a64, v208\nv_accvgpr_write_b32 a65, v209\n"
"v_accvgpr_write_b32 a66, v210\nv_accvgpr_write_b32 a67, v211\nQk 217,218,219\nQk 218,220,221\nQk 219,222,223\nQk 224,224,225\nQk 225,226,227\nQzvvv 220,216,16\n"
"Qk 226,228,229\nQk 227,230,231\nQzvvv 216,216,18\nQk 232,232,233\nQk 233,234,235\nQzvvv 228,224,16\nQk 234,236,237\nQk 235,238,239\n"
"Qzvvv 224,224,18\ns_nop 1\ns_nop 0\nQk 216,216,217\nQzvvv 236,232,16\nQk 217,218,219\nQk 218,220,221\nQk 219,222,223\n"
"Qzvvv 232,232,18\nv_accvgpr_write_b32 a68, v216\nv_accvgpr_write_b32 a69, v217\nv_accvgpr_write_b32 a70, v218\nv_accvgpr_write_b32 a71, v219\ns_mov_b32 s56, s20\ns_and_b32 s57, s21, 0xffff\ns_mov_b32 s94, 0x7ffff000\n"
"s_mul_i32 s68, %0, 40960\ns_lshl_b32 s69, s70, 10\ns_add_u32 s68, s68, s69\ns_add_u32 s68, s68, 4096\ns_add_u32 s94, s16, s68\ns_addc_u32 s95, s17, 0\ns_and_b32 s95, s95, 0xffff\ns_mov_b32 s75, 37888\n"
"s_mov_b32 s58, 4194304\ns_mov_b32 s67, 1\ns_branch .Lround2\n.Lhook:\n.Lpg10:\ns_cbranch_execz .Ltr10\ns_cmp_eq_u32 s66, 1\ns_cbranch_scc1 .Lh1\n"
"s_cmp_eq_u32 s66, 5\ns_cbranch_scc1 .Lh5\ns_cmp_eq_u32 s66, 6\ns_cbranch_scc1 .Lh6\ns_cmp_eq_u32 s66, 9\ns_cbranch_scc1 .Lh9\ns_lshr_b32 s68, s99, s66\ns_cmp_eq_u32 s68, 1\n"
"s_cbranch_scc1 .Lh12\ns_branch .Lh11\n.Lh1:\nds_read_b32 v78, v74 offset:16\ns_cmp_eq_u32 s70, 0\ns_cselect_b32 s68, 128, 0\nv_mov_b32 v66, 0x7ffff000\nv_cmp_gt_u32 vcc, s68, v60\n"
"v_not_b32 v67, v65\ns_lshl_b32 s68, s19, 2\nv_cndmask_b32 v66, v66, v76, vcc\nv_readlane_b32 s30, v67, s19\ns_waitcnt lgkmcnt(0)\ns_nop 1\nv_add_u32_dpp v78, v78, v78 quad_perm:[1,0,3,2] row_mask:0xf bank_mask:0xf\ns_nop 1\n"
"v_add_u32_dpp v78, v78, v78 quad_perm:[2,3,0,1] row_mask:0xf bank_mask:0xf\nv_ashrrev_i32 v78, 1, v78\nv_med3_i32 v78, v78, -7, 7\nv_add_lshl_u32 v78, v78, 8, 28\nv_xor_b32 v67, v67, v78\nbuffer_store_dword v67, v66, s[24:27], s68 offen offset:1024 sc1\ns_branch .Lhookret\n.Lh9:\n"
"s_lshl_b32 s68, s19, 4\ns_cmp_eq_u32 s70, 0\ns_cselect_b32 s68, s68, -1\nv_mov_b32 v67, 0x7ffff000\nv_cmp_eq_u32 vcc, s68, v60\ns_nop 1\nv_cndmask_b32 v67, v67, v76, vcc\nv_add_u32 v66, 1, v65\n"
"buffer_store_dword v66, v67, s[24:27], 0 offen sc1\ns_mov_b64 exec, 0\ns_cmp_eq_u32 s70, 0\ns_cbranch_scc1 .Lpg12\ns_cmp_eq_u32 s70, 1\ns_cbranch_scc1 .Lpg13\ns_cmp_eq_u32 s70, 2\ns_cbranch_scc1 .Lpg14\n"
"s_branch .Lpg15\n.Ltr6:\ns_mov_b64 exec, -1\ns_branch .Lhookret\n.Lh5:\nv_bfe_u32 v66, v60, 4, 3\ns_lshl_b32 s68, s19, 7\nv_lshlrev_b32 v66, 2, v66\n"
"s_add_u32 s68, s68, 5242880\nbuffer_load_dword v78, v66, s[24:27], s68 offen offset:1024 sc1\ns_branch .Lhookret\n.Lh6:\ns_waitcnt vmcnt(32)\ns_mov_b32 s97, 0\n.Lack_l:\nv_xor_b32 v78, s30, v78\n"
"v_and_b32 v66, 0x0fffffff, v78\nv_cmp_ne_u32 vcc, 0, v66\ns_cmp_eq_u64 vcc, 0\ns_cbranch_scc1 .Lack_ok\ns_sleep 1\nv_bfe_u32 v66, v60, 4, 3\ns_lshl_b32 s68, s19, 7\nv_lshlrev_b32 v66, 2, v66\n"
"s_add_u32 s68, s68, 5242880\nbuffer_load_dword v78, v66, s[24:27], s68 offen offset:1024 sc1\ns_waitcnt vmcnt(0)\ns_add_u32 s97, s97, 1\ns_cmp_lt_u32 s97, 1048576\ns_cbranch_scc1 .Lack_l\n.Lack_ok:\nv_lshrrev_b32 v78, 28, v78\n"
"s_nop 1\nv_add_u32_dpp v78, v78, v78 quad_perm:[1,0,3,2] row_mask:0xf bank_mask:0xf\ns_nop 1\nv_add_u32_dpp v78, v78, v78 quad_perm:[2,3,0,1] row_mask:0xf bank_mask:0xf\ns_nop 1\nv_readlane_b32 s68, v78, s19\nv_readlane_b32 s69, v78, 4\ns_cmp_lt_u32 s68, 32\n"
"s_cselect_b32 s68, 8192, 0\ns_xor_b32 s68, s68, s98\ns_add_u32 s71, s71, s68\ns_cmp_lt_u32 s69, 32\ns_cselect_b32 s69, 8, 0\ns_xor_b32 s69, s69, %0\ns_and_b32 s69, s69, 8\ns_lshl_b32 s98, s69, 10\n"
"s_branch .Lhookret\n.Lh11:\nbuffer_load_dword v78, v76, s[24:27], 0 offen sc1\ns_mov_b64 exec, 0\ns_cmp_eq_u32 s70, 0\ns_cbranch_scc1 .Lpg16\ns_cmp_eq_u32 s70, 1\ns_cbranch_scc1 .Lpg17\n"
"s_cmp_eq_u32 s70, 2\ns_cbranch_scc1 .Lpg18\ns_branch .Lpg19\n.Ltr8:\ns_mov_b64 exec, -1\ns_branch .Lhookret\n.Lh12:\ns_waitcnt vmcnt(32)\n"
"v_sub_u32 v66, v78, v65\nv_cmp_gt_u32 vcc, 1, v66\ns_and_b64 vcc, vcc, s[28:29]\ns_cbranch_scc1 .Lf1_w\n.Lf1_r:\ns_cmp_lg_u32 s70, 3\ns_cbranch_scc1 .Lf1_n\nbuffer_inv sc1\n"
".Lf1_n:\ns_lshr_b32 s88, s19, 2\ns_lshl_b32 s88, s88, 4\ns_add_u32 s88, s88, s93\ns_andn2_b32 s68, s71, 786432\ns_add_u32 s48, s20, s68\ns_addc_u32 s49, s21, 0\ns_and_b32 s49, s49, 0xffff\n"
"s_sub_u32 s50, 4194304, s68\ns_mul_i32 s68, s88, 163840\ns_and_b32 s69, s19, 3\ns_mul_i32 s69, s69, 320\ns_add_u32 s68, s68, s69\ns_add_u32 s52, s16, s68\ns_addc_u32 s53, s17, 0\ns_and_b32 s53, s53, 0xffff\n"
"s_sub_u32 s54, 5246976, s68\ns_add_u32 s71, s71, 1048576\ns_and_b32 s87, s19, 3\ns_mul_i32 s87, s87, 327680\ns_add_u32 s87, s87, 1310720\ns_mul_i32 s69, s92, 10240\nv_bfe_u32 v66, v60, 5, 3\nv_mul_u32_u24 v66, 1280, v66\n"
"v_and_b32 v67, 16, v60\nv_add3_u32 v67, v67, v66, s69\ns_and_b32 s69, s19, 1\nv_lshrrev_b32 v66, 8, v60\ns_lshl_b32 s68, s93, 1\nv_xor_b32 v66, s68, v66\nv_lshl_add_u32 v66, v66, 5, v67\ns_lshr_b32 s68, 128, s69\n"
"v_add_u32 v61, s68, v66\ns_mul_i32 s68, s69, 192\nv_add_u32 v63, s68, v66\nv_bfe_u32 v66, v60, 8, 1\nv_xor_b32 v66, s93, v66\nv_lshl_add_u32 v67, v66, 5, v67\nv_lshrrev_b32 v66, 9, v60\nv_mul_u32_u24 v66, 20480, v66\n"
"s_xor_b32 s69, s69, 1\ns_lshl_b32 s69, s69, 8\nv_add3_u32 v62, v66, v67, s69\ns_branch .Lhookret\n.Lf1_w:\ns_mov_b32 s97, 0\n.Lf1_l:\ns_sleep 1\n"
"buffer_load_dword v78, v76, s[24:27], 0 offen sc1\ns_waitcnt vmcnt(0)\ns_add_u32 s97, s97, 1\ns_cmp_gt_u32 s97, 1048576\ns_cbranch_scc1 .Lf1_r\nv_sub_u32 v66, v78, v65\nv_cmp_gt_u32 vcc, 1, v66\ns_and_b64 vcc, vcc, s[28:29]\n"
"s_cbranch_scc1 .Lf1_l\ns_branch .Lf1_r\n.Lsp0:\ns_sub_u32 s75, s77, s74\ns_cmp_lt_u32 s75, 1024\ns_cbranch_scc0 .Lrp0\ns_memrealtime s[74:75]\ns_waitcnt lgkmcnt(0)\n"
"s_branch .Lsp0\n.Lsa1:\nds_read_b32 v66, v74\ns_waitcnt lgkmcnt(0)\nv_cmp_gt_u32 vcc, v75, v66\ns_cbranch_vccnz .Lsa1\ns_branch .Lra1\n.Lsp1:\n"
"s_sub_u32 s75, s77, s74\ns_cmp_lt_u32 s75, 1024\ns_cbranch_scc0 .Lrp1\ns_memrealtime s[74:75]\ns_waitcnt lgkmcnt(0)\ns_branch .Lsp1\n.Lsa2:\nds_read_b32 v66, v74\n"
"s_waitcnt lgkmcnt(0)\nv_cmp_gt_u32 vcc, v75, v66\ns_cbranch_vccnz .Lsa2\ns_branch .Lra2\n.Lsp2:\ns_sub_u32 s75, s77, s74\ns_cmp_lt_u32 s75, 1024\ns_cbranch_scc0 .Lrp2\n"
"s_memrealtime s[74:75]\ns_waitcnt lgkmcnt(0)\ns_branch .Lsp2\n.Lsa3:\nds_read_b32 v66, v74\ns_waitcnt lgkmcnt(0)\nv_cmp_gt_u32 vcc, v75, v66\ns_cbranch_vccnz .Lsa3\n"
"s_branch .Lra3\n.Lsa4:\nds_read_b32 v66, v74\ns_waitcnt lgkmcnt(0)\nv_cmp_gt_u32 vcc, v75, v66\ns_cbranch_vccnz .Lsa4\ns_branch .Lra4\n.Lsp4:\n"
"s_sub_u32 s75, s77, s74\ns_cmp_lt_u32 s75, 1024\ns_cbranch_scc0 .Lrp4\ns_memrealtime s[74:75]\ns_waitcnt lgkmcnt(0)\ns_branch .Lsp4\n.Lsa5:\nds_read_b32 v66, v74\n"
"s_waitcnt lgkmcnt(0)\nv_cmp_gt_u32 vcc, v75, v66\ns_cbranch_vccnz .Lsa5\ns_branch .Lra5\n.Lsa30:\nds_read_b32 v66, v74\ns_waitcnt lgkmcnt(0)\nv_cmp_gt_u32 vcc, v75, v66\n"
"s_cbranch_vccnz .Lsa30\ns_branch .Lra30\n.Lsa31:\nds_read_b32 v66, v74\ns_waitcnt lgkmcnt(0)\nv_cmp_gt_u32 vcc, v75, v66\ns_cbranch_vccnz .Lsa31\ns_branch .Lra31\n"
".Lsa32:\nds_read_b32 v66, v74\ns_waitcnt lgkmcnt(0)\nv_cmp_gt_u32 vcc, v75, v66\ns_cbranch_vccnz .Lsa32\ns_branch .Lra32\n.Lsa30x:\nds_read_b32 v66, v74\n"
"s_waitcnt lgkmcnt(0)\nv_cmp_gt_u32 vcc, v75, v66\ns_cbranch_vccnz .Lsa30x\ns_branch .Lra30x\n.Lsa31x:\nds_read_b32 v66, v74\ns_waitcnt lgkmcnt(0)\nv_cmp_gt_u32 vcc, v75, v66\n"
"s_cbranch_vccnz .Lsa31x\ns_branch .Lra31x\n.Lso40:\nds_read_b32 v66, v74\ns_waitcnt lgkmcnt(0)\nv_cmp_gt_u32 vcc, v75, v66\ns_cbranch_vccnz .Lso40\ns_branch .Lro40\n"
".Lso180:\nds_read_b32 v66, v74\ns_waitcnt lgkmcnt(0)\nv_cmp_gt_u32 vcc, v75, v66\ns_cbranch_vccnz .Lso180\ns_branch .Lro180\n.Lso320:\nds_read_b32 v66, v74\n"
"s_waitcnt lgkmcnt(0)\nv_cmp_gt_u32 vcc, v75, v66\ns_cbranch_vccnz .Lso320\ns_branch .Lro320\n.Lso460:\nds_read_b32 v66, v74\ns_waitcnt lgkmcnt(0)\nv_cmp_gt_u32 vcc, v75, v66\n"
"s_cbranch_vccnz .Lso460\ns_branch .Lro460\n.Lso600:\nds_read_b32 v66, v74\ns_waitcnt lgkmcnt(0)\nv_cmp_gt_u32 vcc, v75, v66\ns_cbranch_vccnz .Lso600\ns_branch .Lro600\n"
".Lso740:\nds_read_b32 v66, v74\ns_waitcnt lgkmcnt(0)\nv_cmp_gt_u32 vcc, v75, v66\ns_cbranch_vccnz .Lso740\ns_branch .Lro740\n.Lso880:\nds_read_b32 v66, v74\n"
"s_waitcnt lgkmcnt(0)\nv_cmp_gt_u32 vcc, v75, v66\ns_cbranch_vccnz .Lso880\ns_branch .Lro880\n.Lso1020:\nds_read_b32 v66, v74\ns_waitcnt lgkmcnt(0)\nv_cmp_gt_u32 vcc, v75, v66\n"
"s_cbranch_vccnz .Lso1020\ns_branch .Lro1020\n.Lso1160:\nds_read_b32 v66, v74\ns_waitcnt lgkmcnt(0)\nv_cmp_gt_u32 vcc, v75, v66\ns_cbranch_vccnz .Lso1160\ns_branch .Lro1160\n"
".Lso1300:\nds_read_b32 v66, v74\ns_waitcnt lgkmcnt(0)\nv_cmp_gt_u32 vcc, v75, v66\ns_cbranch_vccnz .Lso1300\ns_branch .Lro1300\n.Lso1440:\nds_read_b32 v66, v74\n"
"s_waitcnt lgkmcnt(0)\nv_cmp_gt_u32 vcc, v75, v66\ns_cbranch_vccnz .Lso1440\ns_branch .Lro1440\n.Lso1455:\nds_read_b32 v66, v74\ns_waitcnt lgkmcnt(0)\nv_cmp_gt_u32 vcc, v75, v66\n"
"s_cbranch_vccnz .Lso1455\ns_branch .Lro1455\n.Lso1525r0:\nds_read_b32 v66, v74\ns_waitcnt lgkmcnt(0)\nv_cmp_gt_u32 vcc, v75, v66\ns_cbranch_vccnz .Lso1525r0\ns_branch .Lro1525r0\n"
".Lso1595r0:\nds_read_b32 v66, v74\ns_waitcnt lgkmcnt(0)\nv_cmp_gt_u32 vcc, v75, v66\ns_cbranch_vccnz .Lso1595r0\ns_branch .Lro1595r0\n.Lso1665r0:\nds_read_b32 v66, v74\n"
"s_waitcnt lgkmcnt(0)\nv_cmp_gt_u32 vcc, v75, v66\ns_cbranch_vccnz .Lso1665r0\ns_branch .Lro1665r0\n.Lso1735r0:\nds_read_b32 v66, v74\ns_waitcnt lgkmcnt(0)\nv_cmp_gt_u32 vcc, v75, v66\n"
"s_cbranch_vccnz .Lso1735r0\ns_branch .Lro1735r0\n.Lso1805r0:\nds_read_b32 v66, v74\ns_waitcnt lgkmcnt(0)\nv_cmp_gt_u32 vcc, v75, v66\ns_cbranch_vccnz .Lso1805r0\ns_branch .Lro1805r0\n"
".Lso1878r0:\nds_read_b32 v66, v74\ns_waitcnt lgkmcnt(0)\nv_cmp_gt_u32 vcc, v75, v66\ns_cbranch_vccnz .Lso1878r0\ns_branch .Lro1878r0\n.Lso1920r0:\nds_read_b32 v66, v74\n"
"s_waitcnt lgkmcnt(0)\nv_cmp_gt_u32 vcc, v75, v66\ns_cbranch_vccnz .Lso1920r0\ns_branch .Lro1920r0\n.Lso1960r0:\nds_read_b32 v66, v74\ns_waitcnt lgkmcnt(0)\nv_cmp_gt_u32 vcc, v75, v66\n"
"s_cbranch_vccnz .Lso1960r0\ns_branch .Lro1960r0\n.Lso2046r0:\nds_read_b32 v66, v74\ns_waitcnt lgkmcnt(0)\nv_cmp_gt_u32 vcc, v75, v66\ns_cbranch_vccnz .Lso2046r0\ns_branch .Lro2046r0\n"
".Lso2088r0:\nds_read_b32 v66, v74\ns_waitcnt lgkmcnt(0)\nv_cmp_gt_u32 vcc, v75, v66\ns_cbranch_vccnz .Lso2088r0\ns_branch .Lro2088r0\n.Lso2172r0:\nds_read_b32 v66, v74\n"
"s_waitcnt lgkmcnt(0)\nv_cmp_gt_u32 vcc, v75, v66\ns_cbranch_vccnz .Lso2172r0\ns_branch .Lro2172r0\n.Lso2222r0:\nds_read_b32 v66, v74\ns_waitcnt lgkmcnt(0)\nv_cmp_gt_u32 vcc, v75, v66\n"
"s_cbranch_vccnz .Lso2222r0\ns_branch .Lro2222r0\n.Lso1525r1:\nds_read_b32 v66, v74\ns_waitcnt lgkmcnt(0)\nv_cmp_gt_u32 vcc, v75, v66\ns_cbranch_vccnz .Lso1525r1\ns_branch .Lro1525r1\n"
".Lso1595r1:\nds_read_b32 v66, v74\ns_waitcnt lgkmcnt(0)\nv_cmp_gt_u32 vcc, v75, v66\ns_cbranch_vccnz .Lso1595r1\ns_branch .Lro1595r1\n.Lso1665r1:\nds_read_b32 v66, v74\n"
"s_waitcnt lgkmcnt(0)\nv_cmp_gt_u32 vcc, v75, v66\ns_cbranch_vccnz .Lso1665r1\ns_branch .Lro1665r1\n.Lso1735r1:\nds_read_b32 v66, v74\ns_waitcnt lgkmcnt(0)\nv_cmp_gt_u32 vcc, v75, v66\n"
"s_cbranch_vccnz .Lso1735r1\ns_branch .Lro1735r1\n.Lso1805r1:\nds_read_b32 v66, v74\ns_waitcnt lgkmcnt(0)\nv_cmp_gt_u32 vcc, v75, v66\ns_cbranch_vccnz .Lso1805r1\ns_branch .Lro1805r1\n"
".Lso1840r1:\nds_read_b32 v66, v74\ns_waitcnt lgkmcnt(0)\nv_cmp_gt_u32 vcc, v75, v66\ns_cbranch_vccnz .Lso1840r1\ns_branch .Lro1840r1\n.Lso1878r1:\nds_read_b32 v66, v74\n"
"s_waitcnt lgkmcnt(0)\nv_cmp_gt_u32 vcc, v75, v66\ns_cbranch_vccnz .Lso1878r1\ns_branch .Lro1878r1\n.Lso1920r1:\nds_read_b32 v66, v74\ns_waitcnt lgkmcnt(0)\nv_cmp_gt_u32 vcc, v75, v66\n"
"s_cbranch_vccnz .Lso1920r1\ns_branch .Lro1920r1\n.Lso1945r1:\nds_read_b32 v66, v74\ns_waitcnt lgkmcnt(0)\nv_cmp_gt_u32 vcc, v75, v66\ns_cbranch_vccnz .Lso1945r1\ns_branch .Lro1945r1\n"
".Lso2004r1:\nds_read_b32 v66, v74\ns_waitcnt lgkmcnt(0)\nv_cmp_gt_u32 vcc, v75, v66\ns_cbranch_vccnz .Lso2004r1\ns_branch .Lro2004r1\n.Lso2045r1:\nds_read_b32 v66, v74\n"
"s_waitcnt lgkmcnt(0)\nv_cmp_gt_u32 vcc, v75, v66\ns_cbranch_vccnz .Lso2045r1\ns_branch .Lro2045r1\n.Lso2130r1:\nds_read_b32 v66, v74\ns_waitcnt lgkmcnt(0)\nv_cmp_gt_u32 vcc, v75, v66\n"
"s_cbranch_vccnz .Lso2130r1\ns_branch .Lro2130r1\n.Lso2172r1:\nds_read_b32 v66, v74\ns_waitcnt lgkmcnt(0)\nv_cmp_gt_u32 vcc, v75, v66\ns_cbranch_vccnz .Lso2172r1\ns_branch .Lro2172r1\n"
".Lso2225r1:\nds_read_b32 v66, v74\ns_waitcnt lgkmcnt(0)\nv_cmp_gt_u32 vcc, v75, v66\ns_cbranch_vccnz .Lso2225r1\ns_branch .Lro2225r1\n.Ltail1:\n.Lpg11:\n"
"s_cbranch_execz .Ltr10\nQcvaa 0,128,32,56\nv_add_u32 v75, 1, v75\nQmvaa 0,132,36\ns_waitcnt vmcnt(0)\nds_write_b32 v73, v75\nQmvaa 0,136,40\nQe 8\n"
"Qmvaa 0,140,44\nQe 9\nQcvaa 4,124,44,56\nQe 10\nQmvaa 4,120,40\nQe 11\nQmvaa 4,116,36\nQe 12\n"
"Qmvaa 4,112,32\ns_waitcnt lgkmcnt(1)\nQk 40,8,9\nQmvva 96,44,144\nQe 13\nQmvva 96,36,148\nQe 14\nQmvva 100,44,152\n"
"Qe 15\nQmvva 100,36,156\nQk 41,10,11\nQk 42,12,13\nQmvva 104,44,160\nQk 43,14,15\nQmvva 104,36,164\nQe 0\n"
"Qmvva 108,44,168\nQe 1\nQmvva 108,36,172\nQe 2\nQmvva 140,48,168\nQe 3\nQmvva 140,40,172\nQk 46,0,1\n"
"Qk 47,2,3\nQmvva 136,48,160\nds_read_b32 v66, v74\nQmvva 136,40,164\nQe 4\nQmvva 132,48,152\nQe 5\nQmvva 132,40,156\n"
"Qe 6\nQmvva 128,48,144\nQe 7\nQmvva 128,40,148\ns_waitcnt lgkmcnt(0)\nv_cmp_gt_u32 vcc, v75, v66\nv_smfmac_f32_16x16x64_bf16 a[176:179], a[204:207], v[36:43], v77\ns_cbranch_vccnz .Lsa30x\n"
".Lra30x:\nQcvaa 8,112,48,56\ns_add_u32 m0, s96, 0\ns_add_u32 s95, s87, 4096\nQk 44,4,5\nQmvaa 8,116,52\nbuffer_load_dwordx4 v61, s[52:55], s95 offen lds\nQmvaa 8,120,56\n"
"Qk 45,6,7\nQmvaa 8,124,60\nQcvaa 12,140,60,56\nQmvaa 12,136,56\ns_add_u32 s95, s87, 23552\nQmvaa 12,132,52\nbuffer_load_dwordx4 v61, s[52:55], s95 offen offset:1024 lds\nQmvaa 12,128,48\n"
"Qcvaa 0,128,64,56\nQmvaa 0,132,68\nQe 8\nQmvaa 0,136,72\ns_add_u32 m0, s96, 8192\ns_add_u32 s95, s87, 45056\nQe 9\nQmvaa 0,140,76\n"
"buffer_load_dwordx4 v61, s[52:55], s95 offen lds\nQcvaa 4,124,76,56\nQe 10\nQmvaa 4,120,72\nQe 11\nQmvaa 4,116,68\nQe 12\nQmvaa 4,112,64\n"
"s_add_u32 s95, s87, 64512\nQe 13\nQmvva 160,52,144\nbuffer_load_dwordx4 v61, s[52:55], s95 offen offset:1024 lds\nQmvva 160,44,148\nQe 14\nQmvva 164,52,152\nQe 15\n"
"Qmvva 164,44,156\nQk 48,8,9\nQk 49,10,11\nQmvva 168,52,160\ns_add_u32 m0, s96, 16384\ns_add_u32 s95, s87, 4096\nQk 50,12,13\nQmvva 168,44,164\n"
"buffer_load_dwordx4 v62, s[52:55], s95 offen lds\nQmvva 172,52,168\nQk 51,14,15\nQmvva 172,44,172\nQe 0\nQmvva 204,20,168\nQe 1\nQmvva 204,48,172\n"
"s_add_u32 s95, s87, 44032\nQe 2\nQmvva 200,20,160\nbuffer_load_dwordx4 v62, s[52:55], s95 offen offset:1024 lds\nQmvva 200,48,164\nQe 3\nQmvva 196,20,152\nQk 18,0,1\n"
"Qk 19,2,3\nQmvva 196,48,156\nQe 4\nQmvva 192,20,144\ns_add_u32 m0, s96, 24576\ns_add_u32 s95, s87, 4096\nQe 5\nQmvva 192,48,148\n"
"buffer_load_dwordx4 v63, s[52:55], s95 offen lds\nv_smfmac_f32_16x16x64_bf16 a[180:183], a[204:207], v[44:51], v77\nQk 16,4,5\nQcvaa 8,112,80,56\nQe 6\nQmvaa 8,116,84\nQe 7\nQmvaa 8,120,88\n"
"s_add_u32 s95, s87, 23552\nQk 17,6,7\nQmvaa 8,124,92\nbuffer_load_dwordx4 v63, s[52:55], s95 offen offset:1024 lds\nQcvaa 12,140,92,56\nQmvaa 12,136,88\nQmvaa 12,132,84\nQmvaa 12,128,80\n"
"s_add_u32 m0, s96, 32768\ns_add_u32 s95, s87, 45056\nQcvaa 0,128,96,56\nQda 128,68,34048\nbuffer_load_dwordx4 v63, s[52:55], s95 offen lds\nQmvaa 0,132,100\nQda 132,68,34560\nQmvaa 0,136,104\n"
"Qda 136,68,34304\nQmvaa 0,140,108\nQda 140,68,34816\nQcvaa 4,124,108,56\nQda 124,68,33792\ns_add_u32 s95, s87, 64512\nQmvaa 4,120,104\nQda 120,68,33280\n"
"buffer_load_dwordx4 v63, s[52:55], s95 offen offset:1024 lds\nQmvaa 4,116,100\nQda 116,68,33536\nQmvaa 4,112,96\nQda 112,68,33024\nQmvva 224,24,144\nQe 8\nQmvva 224,16,148\n"
"s_add_u32 m0, s96, 40960\ns_mov_b32 s95, 4096\nQe 9\nQmvva 228,24,152\nbuffer_load_dwordx4 v61, s[52:55], s95 offen lds\nQe 10\nQmvva 228,16,156\nQe 11\n"
"Qmvva 232,24,160\nQk 20,8,9\nQk 21,10,11\n.Lpg12:\ns_cbranch_execz .Ltr6\nQmvva 232,16,164\nQe 12\nQmvva 236,24,168\n"
"s_mov_b32 s95, 23552\nQe 13\nQmvva 236,16,172\nbuffer_load_dwordx4 v61, s[52:55], s95 offen offset:1024 lds\ns_waitcnt lgkmcnt(0)\nQk 22,12,13\nQcvaa 8,112,0,56\nQe 14\n"
"Qmvaa 8,116,4\nv_subrev_u32 v68, 66048, v68\nQmvaa 8,120,8\nQe 15\nQmvaa 8,124,12\ns_add_u32 m0, s96, 49152\ns_mov_b32 s95, 45056\nQk 23,14,15\n"
"Qcvaa 12,140,12,56\nbuffer_load_dwordx4 v61, s[52:55], s95 offen lds\nQe 0\nQmvaa 12,136,8\nQe 1\nQmvaa 12,132,4\nQe 2\nQmvaa 12,128,0\n"
"Qe 3\nv_smfmac_f32_16x16x64_bf16 a[184:187], a[204:207], v[16:23], v77\ns_mov_b32 s95, 64512\nQk 26,0,1\nQmava 208,28,144\nbuffer_load_dwordx4 v61, s[52:55], s95 offen offset:1024 lds\nQe 4\nQmava 208,20,148\n"
"Qe 5\nQmava 212,28,152\nQe 6\nQmava 212,20,156\nQe 7\nQmava 216,28,160\ns_add_u32 m0, s96, 57344\ns_mov_b32 s95, 4096\n"
"Qk 27,2,3\nQk 24,4,5\nQmava 216,20,164\nbuffer_load_dwordx4 v62, s[52:55], s95 offen lds\nQk 25,6,7\nQmava 220,28,168\nQe 8\nQmava 220,20,172\n"
"Qe 9\nQmava 252,32,168\nQta 168,71,768\nQta 170,71,1792\nQmava 252,24,172\ns_mov_b32 s95, 44032\nQe 10\nQmava 248,32,160\n"
"Qta 172,71,33792\nQta 174,71,34816\nbuffer_load_dwordx4 v62, s[52:55], s95 offen offset:1024 lds\nQmava 248,24,164\nQe 11\nQmava 244,32,152\nQta 160,71,512\nQta 162,71,1536\n"
"Qmava 244,24,156\nQk 28,8,9\nQk 29,10,11\nQmava 240,32,144\nQta 164,71,33536\nQta 166,71,34560\ns_add_u32 m0, s96, 132096\ns_mov_b32 s95, 4096\n"
"Qmava 240,24,148\nbuffer_load_dwordx4 v63, s[52:55], s95 offen lds\nQe 12\nQcvaa 0,112,16,56\nQta 152,71,256\nQta 154,71,1280\nQmvaa 0,116,20\nQe 13\n"
"Qmvaa 0,120,24\nQta 156,71,33280\nQta 158,71,34304\nQmvaa 0,124,28\ns_mov_b32 s95, 23552\nQe 14\nQcvaa 4,140,28,56\nQta 144,71,0\n"
"Qta 146,71,1024\nbuffer_load_dwordx4 v63, s[52:55], s95 offen offset:1024 lds\nQmvaa 4,136,24\nQe 15\nQmvaa 4,132,20\nQta 148,71,33024\nQta 150,71,34048\nQmvaa 4,128,16\n"
"Qk 30,12,13\nQk 31,14,15\nQcvaa 8,128,32,56\nv_add_u32 v75, 1, v75\nQmvaa 8,132,36\nds_write_b32 v73, v75\nQmvaa 8,136,40\nQe 0\n"
"Qmvaa 8,140,44\nQe 1\nQcvaa 12,124,44,56\nQe 2\nQmvaa 12,120,40\nQe 3\nQmvaa 12,116,36\nQe 4\n"
"Qmvaa 12,112,32\ns_waitcnt lgkmcnt(1)\nQk 32,0,1\nv_smfmac_f32_16x16x64_bf16 a[188:191], a[204:207], v[24:31], v77\nQk 33,2,3\nQmvva 80,36,144\nQe 5\nQmvva 80,28,148\n"
"Qe 6\nQmvva 84,36,152\nQe 7\nQmvva 84,28,156\nQk 34,4,5\nQk 35,6,7\nQmvva 88,36,160\nQe 8\n"
"Qmvva 88,28,164\nQe 9\nQmvva 92,36,168\nQe 10\nQmvva 92,28,172\nQe 11\nQmvva 124,40,168\nQk 38,8,9\n"
"Qk 39,10,11\nQmvva 124,32,172\nds_read_b32 v66, v74\nQmvva 120,40,160\nQe 12\nQmvva 120,32,164\nQe 13\nQmvva 116,40,152\n"
"Qe 14\nQmvva 116,32,156\nQe 15\nQmvva 112,40,144\ns_waitcnt lgkmcnt(0)\nv_cmp_gt_u32 vcc, v75, v66\nQmvva 112,32,148\ns_cbranch_vccnz .Lsa31x\n"
".Lra31x:\nQk 36,12,13\nQcvaa 0,112,48,56\ns_add_u32 m0, s96, 140288\ns_mov_b32 s95, 45056\nQk 37,14,15\nQmvaa 0,116,52\nbuffer_load_dwordx4 v63, s[52:55], s95 offen lds\n"
"Qmvaa 0,120,56\nQmvaa 0,124,60\nQcvaa 4,140,60,56\nQmvaa 4,136,56\ns_mov_b32 s95, 64512\nQmvaa 4,132,52\nbuffer_load_dwordx4 v63, s[52:55], s95 offen offset:1024 lds\nQmvaa 4,128,48\n"
"Qcvaa 8,128,64,56\nQmvaa 8,132,68\nQe 0\nQmvaa 8,136,72\ns_add_u32 m0, s96, 148480\ns_add_u32 s95, s87, 86016\nQe 1\nQmvaa 8,140,76\n"
"buffer_load_dwordx4 v61, s[52:55], s95 offen lds\nQcvaa 12,124,76,56\nQe 2\nQmvaa 12,120,72\nQe 3\nQmvaa 12,116,68\nQe 4\nQmvaa 12,112,64\n"
"Qe 5\nv_smfmac_f32_16x16x64_bf16 a[192:195], a[204:207], v[32:39], v77\ns_add_u32 s95, s87, 105472\nQk 40,0,1\nQmvva 144,44,144\nbuffer_load_dwordx4 v61, s[52:55], s95 offen offset:1024 lds\nQmvva 144,36,148\nQe 6\n"
"Qmvva 148,44,152\nQe 7\nQmvva 148,36,156\ns_add_u32 m0, s96, 65536\ns_add_u32 s95, s87, 126976\nQk 41,2,3\nQmvva 152,44,160\nbuffer_load_dwordx4 v61, s[52:55], s95 offen lds\n"
"Qmvva 152,36,164\nQk 42,4,5\nQk 43,6,7\nQmvva 156,44,168\nQe 8\nQmvva 156,36,172\nQe 9\nQmvva 188,48,168\n"
"s_add_u32 s95, s87, 146432\nQe 10\nQmvva 188,40,172\nbuffer_load_dwordx4 v61, s[52:55], s95 offen offset:1024 lds\nQmvva 184,48,160\nQe 11\nQmvva 184,40,164\nQk 46,8,9\n"
"Qk 47,10,11\nQmvva 180,48,152\nQe 12\nQmvva 180,40,156\ns_add_u32 m0, s96, 73728\ns_add_u32 s95, s87, 86016\nQe 13\nQmvva 176,48,144\n"
"buffer_load_dwordx4 v62, s[52:55], s95 offen lds\nQmvva 176,40,148\nQe 14\nQcvaa 0,112,80,56\nQe 15\nQmvaa 0,116,84\nQk 44,12,13\nQk 45,14,15\n"
"Qmvaa 0,120,88\ns_add_u32 s95, s87, 125952\nQmvaa 0,124,92\nbuffer_load_dwordx4 v62, s[52:55], s95 offen offset:1024 lds\nQcvaa 4,140,92,56\nQmvaa 4,136,88\nQmvaa 4,132,84\nQmvaa 4,128,80\n"
"s_add_u32 m0, s96, 81920\ns_add_u32 s95, s87, 86016\nQcvaa 8,128,96,56\nbuffer_load_dwordx4 v63, s[52:55], s95 offen lds\nQmvaa 8,132,100\nQmvaa 8,136,104\nQmvaa 8,140,108\nQcvaa 12,124,108,56\n"
"s_add_u32 s95, s87, 105472\nQmvaa 12,120,104\nbuffer_load_dwordx4 v63, s[52:55], s95 offen offset:1024 lds\nQmvaa 12,116,100\nQmvaa 12,112,96\nv_smfmac_f32_16x16x64_bf16 a[196:199], a[204:207], v[40:47], v77\nQmvva 208,16,144\nQta 112,71,33152\n"
"s_add_u32 m0, s96, 90112\ns_add_u32 s95, s87, 126976\nQe 0\nQmvva 208,44,148\nQta 114,71,34176\nbuffer_load_dwordx4 v63, s[52:55], s95 offen lds\nQe 1\nQmvva 212,16,152\n"
"Qta 116,71,33408\nQe 2\nQmvva 212,44,156\nQta 118,71,34432\nQe 3\nQmvva 216,16,160\nQta 120,71,33664\nQk 48,0,1\n"
"Qk 49,2,3\nQmvva 216,44,164\nQta 122,71,34688\ns_add_u32 s95, s87, 146432\nQe 4\nQmvva 220,16,168\nQta 124,71,33920\nbuffer_load_dwordx4 v63, s[52:55], s95 offen offset:1024 lds\n"
"Qe 5\nQmvva 220,44,172\nQta 126,71,34944\nQk 50,4,5\nQe 6\nQe 7\ns_nop 0\nQk 51,6,7\n"
"Qe 8\nQe 9\nQe 10\nQe 11\nQe 12\nQmvva 240,20,144\nQe 13\nQmvva 240,48,148\n"
"Qe 14\nQmvva 244,20,152\nQe 15\nQmvva 244,48,156\nQk 54,8,9\nQk 55,10,11\nQmvva 248,20,160\nQk 52,12,13\n"
"Qk 53,14,15\nQmvva 248,48,164\nQmvva 252,20,168\nQmvva 252,48,172\nv_smfmac_f32_16x16x64_bf16 a[200:203], a[204:207], v[48:55], v77\nQmava 224,24,144\nv_accvgpr_read_b32 v0, a176\nv_accvgpr_read_b32 v1, a177\n"
"Qmava 224,52,148\nv_accvgpr_read_b32 v6, a178\nv_accvgpr_read_b32 v7, a179\nQmava 228,24,152\nv_accvgpr_read_b32 v8, a180\nv_accvgpr_read_b32 v9, a181\nQmava 228,52,156\nv_accvgpr_read_b32 v14, a182\n"
"v_accvgpr_read_b32 v15, a183\nQmava 232,24,160\nv_accvgpr_read_b32 v16, a184\nv_accvgpr_read_b32 v17, a185\nQmava 232,52,164\nv_accvgpr_read_b32 v22, a186\nv_accvgpr_read_b32 v23, a187\nQmava 236,24,168\n"
"v_accvgpr_read_b32 v2, a190\nv_accvgpr_read_b32 v3, a191\nQmava 236,52,172\ns_waitcnt lgkmcnt(0)\nv_accvgpr_read_b32 v24, a188\nQmvva 96,28,112\nv_accvgpr_read_b32 v25, a189\nv_accvgpr_read_b32 v4, a192\n"
"Qmvva 100,28,116\nv_accvgpr_read_b32 v5, a193\nv_accvgpr_read_b32 v10, a194\nQmvva 104,28,120\nv_accvgpr_read_b32 v11, a195\nv_accvgpr_read_b32 v12, a196\nQmvva 108,28,124\nv_accvgpr_read_b32 v13, a197\n"
"v_accvgpr_read_b32 v18, a198\nQmvva 128,32,112\nv_accvgpr_read_b32 v19, a199\nv_accvgpr_read_b32 v20, a200\nQmvva 132,32,116\nv_accvgpr_read_b32 v21, a201\nv_accvgpr_read_b32 v26, a202\nQmvva 136,32,120\n"
"v_accvgpr_read_b32 v27, a203\nQa 8,8,9\nQmvva 140,32,124\nQa 10,10,11\nQa 8,8,10\nQmvva 160,36,112\nQa 16,16,17\nQa 18,18,19\n"
"Qmvva 164,36,116\nQa 16,16,18\nQa 4,4,5\nQmvva 168,36,120\nQa 6,6,7\nQa 4,4,6\nQmvva 172,36,124\nQa 12,12,13\n"
"Qa 14,14,15\nQmvva 192,40,112\nQa 12,12,14\nQa 0,0,1\nQmvva 196,40,116\nQa 2,2,3\nQa 0,0,2\nQmvva 200,40,120\n"
"Qa 24,24,25\nQa 26,26,27\nQmvva 204,40,124\nQa 24,24,26\nQa 20,20,21\nQmvva 224,44,112\nQa 22,22,23\nQa 20,20,22\n"
"Qmvva 228,44,116\nv_max3_u32 v9, v8, v16, v4\nv_max3_u32 v9, v9, v12, v0\nQmvva 232,44,120\nv_max3_u32 v9, v9, v24, v20\nv_min3_u32 v11, v8, v16, v4\nQmvva 236,44,124\nv_min3_u32 v11, v11, v12, v0\n"
"v_min3_u32 v11, v11, v24, v20\nQmava 208,48,112\nv_cmp_lt_u32 vcc, 0x4e800000, v9\ns_mov_b64 s[74:75], vcc\nQmava 212,48,116\nv_cmp_gt_u32 vcc, 0x35800000, v11\ns_or_b64 s[74:75], s[74:75], vcc\nQmava 216,48,120\n"
"v_mov_b32 v30, s73\nv_mov_b32 v31, 0\nQmava 220,48,124\nv_mov_b32 v58, 0xb9800000\nv_mov_b32 v59, 0\nQmava 240,52,112\nv_and_b32 v66, 16, v60\nv_mov_b32 v67, 0xffff\n"
"Qmava 244,52,116\nv_lshlrev_b32 v66, v66, v67\ns_mov_b32 vcc_lo, 0x00c0000c\nQmava 248,52,120\ns_mov_b32 vcc_hi, 0xc0000c00\nv_cndmask_b32 v67, 0, v66, vcc\nQmava 252,52,124\ns_lshr_b64 vcc, vcc, 2\n"
"v_cndmask_b32 v66, 0, v66, vcc\ns_lshl_b32 s68, s93, 12\ns_add_u32 s68, s68, %4\nv_lshrrev_b32 v69, 9, v60\nv_lshlrev_b32 v69, 11, v69\nv_bfe_u32 v70, v60, 1, 8\nv_add3_u32 v68, v69, v70, s68\nv_add_u32 v69, 65536, v68\n"
"v_add_u32 v70, 131072, v68\ns_cmp_lg_u64 s[74:75], 0\ns_cbranch_scc1 .Lfb\n.Lfbret1:\nv_rcp_f32 v2, v0\nv_rcp_f32_e64 v6, v4\nv_mfma_f32_16x16x4_4b_bf16 v[80:95], v[30:31], v[58:59], v[80:95]\nv_rcp_f32 v10, v8\n"
"v_rcp_f32 v14, v12\nv_rcp_f32 v18, v16\nv_rcp_f32 v22, v20\nv_rcp_f32 v26, v24\nv_mov_b64 v[0:1], 0\nv_mfma_f32_16x16x4_4b_bf16 v[112:127], v[30:31], v[58:59], v[112:127]\nQk 2,2,2\nv_and_b32 v3, v2, v67\n"
"v_and_b32 v2, v2, v66\nv_mov_b64_e64 v[4:5], 0\nQk 6,6,6\nv_and_b32_e64 v7, v6, v67\nv_mfma_f32_16x16x4_4b_bf16 v[144:159], v[30:31], v[58:59], v[144:159]\nv_and_b32 v6, v6, v66\nv_mov_b64 v[8:9], 0\nQk 10,10,10\n"
"v_and_b32 v11, v10, v67\nv_and_b32 v10, v10, v66\nv_mov_b64_e64 v[12:13], 0\nv_mfma_f32_16x16x4_4b_bf16 v[176:191], v[30:31], v[58:59], v[176:191]\nQk 14,14,14\nv_and_b32 v15, v14, v67\nv_and_b32 v14, v14, v66\nv_mov_b64_e64 v[16:17], 0\n"
"Qk 18,18,18\nv_and_b32_e64 v19, v18, v67\nv_mfma_f32_16x16x4_4b_bf16 v[208:223], v[30:31], v[58:59], v[208:223]\nv_and_b32 v18, v18, v66\nv_mov_b64 v[20:21], 0\nQk 22,22,22\nv_and_b32 v23, v22, v67\nv_and_b32 v22, v22, v66\n"
"v_mov_b64_e64 v[24:25], 0\nv_mfma_f32_16x16x4_4b_bf16 v[240:255], v[30:31], v[58:59], v[240:255]\nQk 26,26,26\nv_and_b32 v27, v26, v67\nv_and_b32 v26, v26, v66\nv_mov_b64_e64 v[28:29], 0\nQk 80,80,81\nQk 81,82,83\n"
"Qk 82,84,85\nQk 83,86,87\nQk 112,112,113\nQk 113,114,115\nQzvvv 84,80,0\nQk 114,116,117\nQk 115,118,119\nQzvvv 80,80,2\n"
"Qk 144,144,145\nQk 145,146,147\nQzvvv 116,112,4\nQk 146,148,149\nQk 147,150,151\nQzvvv 112,112,6\nQk 176,176,177\nQk 177,178,179\n"
"v_mfma_f32_16x16x4_4b_bf16 a[224:239], v[30:31], v[58:59], a[224:239]\nQk 80,80,81\nQk 81,82,83\nQk 82,84,85\nQk 83,86,87\nQk 178,180,181\nQk 179,182,183\nQzvvv 148,144,8\n"
"Qk 112,112,113\nQk 113,114,115\nQzvvv 144,144,10\nQk 114,116,117\nQk 115,118,119\nv_mfma_f32_16x16x4_4b_bf16 v[96:111], v[30:31], v[58:59], v[96:111]\nQk 208,208,209\nQk 209,210,211\n"
"Qk 210,212,213\nQk 211,214,215\nQk 240,240,241\nQk 144,144,145\nQzvvv 180,176,12\nQk 145,146,147\nQk 146,148,149\nQzvvv 176,176,14\n"
"Qk 147,150,151\nQk 241,242,243\nv_mfma_f32_16x16x4_4b_bf16 v[128:143], v[30:31], v[58:59], v[128:143]\nQk 242,244,245\nQk 243,246,247\nv_accvgpr_read_b32 v84, a224\nv_accvgpr_read_b32 v85, a225\nv_accvgpr_read_b32 v86, a226\n"
"Qk 176,176,177\nQzvvv 212,208,16\nQk 177,178,179\nQk 178,180,181\nQzvvv 208,208,18\nQk 179,182,183\nv_accvgpr_read_b32 v87, a227\nv_mfma_f32_16x16x4_4b_bf16 v[160:175], v[30:31], v[58:59], v[160:175]\n"
"v_accvgpr_read_b32 v32, a228\nv_accvgpr_read_b32 v33, a229\nv_accvgpr_read_b32 v34, a230\nv_accvgpr_read_b32 v35, a231\nQk 84,84,85\nQk 85,86,87\nQzvvv 244,240,20\nQk 86,32,33\n"
"Qk 87,34,35\nQzvvv 240,240,22\nQk 208,208,209\nQk 209,210,211\nv_mfma_f32_16x16x4_4b_bf16 v[192:207], v[30:31], v[58:59], v[192:207]\nQk 210,212,213\nQk 211,214,215\nQk 88,88,89\n"
"Qk 89,90,91\nQk 90,92,93\nQk 91,94,95\nQzvvv 32,84,24\nQk 240,240,241\nQk 241,242,243\nQzvvv 84,84,26\nQk 242,244,245\n"
"Qk 243,246,247\nv_mfma_f32_16x16x4_4b_bf16 v[224:239], v[30:31], v[58:59], v[224:239]\nQk 120,120,121\nQk 121,122,123\nQk 122,124,125\nQk 123,126,127\nQk 152,152,153\nQk 84,84,85\n"
"Qzvvv 92,88,0\nQk 85,86,87\nQk 86,32,33\nQzvvv 88,88,2\nQk 87,34,35\nQk 153,154,155\nv_mfma_f32_16x16x4_4b_bf16 a[208:223], v[30:31], v[58:59], a[208:223]\nQk 154,156,157\n"
"Qk 155,158,159\ns_waitcnt vmcnt(20)\ns_barrier\nQk 184,184,185\nQk 88,88,89\nQzvvv 124,120,4\nQk 89,90,91\nQk 90,92,93\n"
"Qzvvv 120,120,6\nQk 91,94,95\n.Lpg13:\ns_cbranch_execz .Ltr6\ns_nop 0\nQk 185,186,187\nv_mfma_f32_16x16x4_4b_bf16 a[240:255], v[30:31], v[58:59], a[240:255]\nQk 186,188,189\n"
"Qk 187,190,191\nQta 56,68,0\nQk 216,216,217\nQk 217,218,219\nQk 120,120,121\nQzvvv 156,152,8\nQk 121,122,123\nQk 122,124,125\n"
"Qzvvv 152,152,10\nQk 123,126,127\nQk 218,220,221\nQzvvv 188,184,12\nQk 219,222,223\nQta 58,68,1024\nQzvvv 184,184,14\nQk 248,248,249\n"
"Qk 249,250,251\nQk 152,152,153\nQk 153,154,155\nQzvvv 220,216,16\nQk 154,156,157\nQk 155,158,159\nQzvvv 216,216,18\nQk 250,252,253\n"
"Qk 251,254,255\nQk 184,184,185\nQk 185,186,187\nQzvvv 252,248,20\nQk 186,188,189\nQk 187,190,191\nQzvvv 248,248,22\nQta 60,68,8192\n"
"v_accvgpr_read_b32 v116, a232\nv_accvgpr_read_b32 v117, a233\nv_accvgpr_read_b32 v118, a234\nv_accvgpr_read_b32 v119, a235\nv_accvgpr_read_b32 v36, a236\nv_accvgpr_read_b32 v37, a237\nv_accvgpr_read_b32 v38, a238\nv_accvgpr_read_b32 v39, a239\n"
"Qk 116,116,117\nQk 117,118,119\nQk 118,36,37\nQk 119,38,39\nQk 216,216,217\nQk 217,218,219\nQzvvv 36,116,24\nQk 218,220,221\n"
"Qk 219,222,223\nQzvvv 116,116,26\nQta 62,68,9216\ns_mov_b32 s31, 0\ns_nop 0\nQk 96,96,97\nQk 97,98,99\nQk 98,100,101\n"
"Qk 99,102,103\nQk 248,248,249\nQk 249,250,251\nQzvvv 100,96,0\nQk 250,252,253\nQk 251,254,255\nQzvvv 96,96,2\nbuffer_load_dwordx4 a[0:3], v79, s[48:51], s31 offen offset:0\n"
"Qta 64,68,256\nQk 128,128,129\nQk 129,130,131\nQk 130,132,133\nQk 131,134,135\nQk 116,116,117\nQk 117,118,119\nQzvvv 132,128,4\n"
"Qk 118,36,37\nQk 119,38,39\nQzvvv 128,128,6\nbuffer_load_dwordx4 a[4:7], v79, s[48:51], s31 offen offset:1024\nQta 66,68,1280\ns_waitcnt lgkmcnt(2)\ns_nop 0\nQzvav 56,56,80\n"
"Qk 160,160,161\nQk 161,162,163\nQmvav 56,60,88\nQk 162,164,165\nQk 163,166,167\nbuffer_load_dwordx4 a[8:11], v79, s[48:51], s31 offen offset:2048\nQta 68,68,8448\nQzvvv 164,160,8\n"
"Qk 192,192,193\nQk 193,194,195\nQzvvv 160,160,10\nQk 194,196,197\nQk 195,198,199\nbuffer_load_dwordx4 a[12:15], v79, s[48:51], s31 offen offset:3072\nQta 70,68,9472\nQzvvv 196,192,12\n"
"v_max3_f32 v66, |v56|, |v57|, |v58|\nv_max_f32 v66, v66, |v59|\nQzvvv 192,192,14\nv_mov_b32_e64 v67, v66\ns_mov_b32 s31, 4096\nQk 224,224,225\nQk 225,226,227\nQk 226,228,229\n"
"Qk 227,230,231\nbuffer_load_dwordx4 a[16:19], v79, s[48:51], s31 offen offset:0\nv_permlane32_swap_b32_e64 v66, v67\nQzvvv 228,224,16\nv_max_f32 v66, v66, v67\nv_mov_b32 v67, v66\nQzvvv 224,224,18\nv_accvgpr_read_b32 v148, a208\n"
"v_accvgpr_read_b32 v149, a209\nv_accvgpr_read_b32 v150, a210\nv_accvgpr_read_b32 v151, a211\nv_accvgpr_read_b32 v40, a212\nv_accvgpr_read_b32 v41, a213\nv_accvgpr_read_b32 v42, a214\nv_accvgpr_read_b32 v43, a215\nQk 148,148,149\n"
"Qk 149,150,151\nQk 150,40,41\nQk 151,42,43\nbuffer_load_dwordx4 a[20:23], v79, s[48:51], s31 offen offset:1024\nv_permlane16_swap_b32_e64 v66, v67\nQzvvv 40,148,20\nv_max_f32_e64 v66, v66, v67\nv_accvgpr_read_b32 v180, a240\n"
"Qzvvv 148,148,22\nv_accvgpr_read_b32 v181, a241\nv_accvgpr_read_b32 v182, a242\nv_accvgpr_read_b32 v183, a243\nv_accvgpr_read_b32 v44, a244\nv_accvgpr_read_b32 v45, a245\nv_accvgpr_read_b32 v46, a246\nv_accvgpr_read_b32 v47, a247\n"
"Qk 180,180,181\nQk 181,182,183\nQk 182,44,45\nQk 183,46,47\nbuffer_load_dwordx4 a[24:27], v79, s[48:51], s31 offen offset:2048\nv_max_f32_dpp v66, v66, v66 row_ror:8 row_mask:0xf bank_mask:0xf\nQzvvv 44,180,24\ns_mov_b32 s31, 16384\n"
"Qk 104,104,105\nQzvvv 180,180,26\nQk 105,106,107\nQk 106,108,109\nQk 107,110,111\nbuffer_load_dwordx4 a[28:31], v79, s[48:51], s31 offen offset:0\nv_max_f32_dpp v66, v66, v66 row_ror:4 row_mask:0xf bank_mask:0xf\nQzvvv 108,104,0\n"
"Qk 136,136,137\nQk 137,138,139\nQzvvv 104,104,2\nQk 138,140,141\nQk 139,142,143\nbuffer_load_dwordx4 a[32:35], v79, s[48:51], s31 offen offset:1024\nv_max_f32_dpp v66, v66, v66 row_ror:2 row_mask:0xf bank_mask:0xf\nQzvvv 140,136,4\n"
"Qk 168,168,169\nQk 169,170,171\nQzvvv 136,136,6\nQk 170,172,173\nQk 171,174,175\nbuffer_load_dwordx4 a[36:39], v79, s[48:51], s31 offen offset:2048\nv_max_f32_dpp v66, v66, v66 row_ror:1 row_mask:0xf bank_mask:0xf\nQzvvv 172,168,8\n"
"v_and_b32 v66, 0x7f800000, v66\nv_add_u32 v66, 0x3000000, v66\nQzvvv 168,168,10\nQk 200,200,201\nQk 201,202,203\nQk 202,204,205\nQk 203,206,207\nbuffer_load_dwordx4 a[40:43], v79, s[48:51], s31 offen offset:3072\n"
"v_min_u32 v66, 0x7e000000, v66\nQzvvv 204,200,12\nv_or_b32 v66, 0x400000, v66\nds_write_b32 v73, v66 offset:16\nQzvvv 200,200,14\nv_xor_b32 v67, 0x80000000, v66\ns_mov_b32 s31, 20480\nQk 232,232,233\n"
"Qk 233,234,235\nQk 234,236,237\nQk 235,238,239\nbuffer_load_dwordx4 a[44:47], v79, s[48:51], s31 offen offset:0\nv_accvgpr_write_b32 a112, v67\nQzvvv 236,232,16\nv_accvgpr_write_b32 a113, v67\nv_accvgpr_write_b32 a114, v67\n"
"Qzvvv 232,232,18\nv_accvgpr_write_b32 a115, v67\nv_accvgpr_read_b32 v212, a216\nv_accvgpr_read_b32 v213, a217\nv_accvgpr_read_b32 v214, a218\nv_accvgpr_read_b32 v215, a219\nv_accvgpr_read_b32 v48, a220\nv_accvgpr_read_b32 v49, a221\n"
"v_accvgpr_read_b32 v50, a222\nv_accvgpr_read_b32 v51, a223\nQk 212,212,213\nQk 213,214,215\nQk 214,48,49\nQk 215,50,51\nbuffer_load_dwordx4 a[48:51], v79, s[48:51], s31 offen offset:1024\nv_accvgpr_read_b32 v244, a248\n"
"Qzvvv 48,212,20\nv_accvgpr_read_b32 v245, a249\nv_accvgpr_read_b32 v246, a250\nQzvvv 212,212,22\nv_accvgpr_read_b32 v247, a251\nv_accvgpr_read_b32 v52, a252\nv_accvgpr_read_b32 v53, a253\nv_accvgpr_read_b32 v54, a254\n"
"v_accvgpr_read_b32 v55, a255\nQk 244,244,245\nQk 245,246,247\nQk 246,52,53\nQk 247,54,55\nbuffer_load_dwordx4 a[52:55], v79, s[48:51], s31 offen offset:2048\ns_nop 0\ns_nop 0\n"
"Qzvvv 52,244,24\nQzvvv 244,244,26\ns_waitcnt lgkmcnt(5)\nQcaav 140,56,84,112\ns_add_u32 m0, s96, 98304\ns_mov_b32 s95, 86016\nQmaav 140,60,116\nbuffer_load_dwordx4 v61, s[52:55], s95 offen lds\n"
"Qcaav 136,56,240,112\ns_mov_b32 s56, s20\ns_and_b32 s57, s21, 0xffff\nQmaav 136,60,248\ns_mov_b32 s58, 4194304\nQk 244,244,245\nQcaav 132,56,208,112\nQta 72,68,16384\n"
"Qk 245,246,247\nQmaav 132,60,216\ns_mov_b32 s95, 105472\nQk 246,52,53\nQcaav 128,56,176,112\nbuffer_load_dwordx4 v61, s[52:55], s95 offen offset:1024 lds\nQmaav 128,60,184\nQta 74,68,16896\n"
"Qk 247,54,55\nQcaav 124,56,144,112\nQk 212,212,213\nQk 213,214,215\nQmaav 124,60,152\nQk 214,48,49\nQk 215,50,51\nQcaav 120,56,112,112\n"
"Qta 76,68,17408\nQk 180,180,181\nQmaav 120,60,120\ns_add_u32 s88, s71, 0\nQk 181,182,183\nQcaav 116,56,80,112\nbuffer_store_dwordx4 v[80:83], v79, s[56:59], s88 offen offset:0 sc1\nQmaav 116,60,88\n"
"Qta 78,68,17920\nQk 182,44,45\ns_waitcnt lgkmcnt(5)\nQcaav 144,64,80,112\nQk 183,46,47\nQk 148,148,149\nQmaav 144,68,88\nQk 149,150,151\n"
"Qk 150,40,41\nQcaav 148,64,112,112\nQk 151,42,43\nQk 96,96,97\nQmaav 148,68,120\nQk 97,98,99\nQk 98,100,101\nQcaav 152,64,144,112\n"
"Qta 56,68,24576\nQk 99,102,103\nQmaav 152,68,152\ns_add_u32 m0, s96, 106496\ns_mov_b32 s95, 126976\nQcaav 156,64,176,112\nbuffer_load_dwordx4 v61, s[52:55], s95 offen lds\nQmaav 156,68,184\n"
"Qta 58,68,25600\nQk 168,168,169\nQcaav 160,64,208,112\nQk 169,170,171\nQk 170,172,173\nQmaav 160,68,216\nQk 171,174,175\nQk 136,136,137\n"
"Qcaav 164,64,240,112\nQta 60,68,32768\ns_mov_b32 s95, 146432\nQmaav 164,68,248\nbuffer_load_dwordx4 v61, s[52:55], s95 offen offset:1024 lds\nQcaav 168,64,84,112\nQk 137,138,139\nQk 138,140,141\n"
"Qmaav 168,68,116\nQta 62,68,33792\nQk 139,142,143\ns_waitcnt lgkmcnt(4)\nQcaav 196,72,84,112\nQk 104,104,105\nQk 105,106,107\nQmaav 196,76,116\n"
"s_add_u32 m0, s96, 114688\ns_mov_b32 s95, 86016\nQcaav 192,72,240,112\nbuffer_load_dwordx4 v62, s[52:55], s95 offen lds\nQmaav 192,76,248\nQk 106,108,109\nQk 107,110,111\nQcaav 188,72,208,112\n"
"Qta 64,68,24832\nQk 224,224,225\nQmaav 188,76,216\nbuffer_store_dwordx4 v[112:115], v79, s[56:59], s88 offen offset:1024 sc1\nQcaav 184,72,176,112\nQk 225,226,227\nQk 226,228,229\nQmaav 184,76,184\n"
"Qta 66,68,25856\nQk 227,230,231\nQcaav 180,72,144,112\nQk 192,192,193\nQk 193,194,195\nQmaav 180,76,152\nQk 194,196,197\nQk 195,198,199\n"
"Qcaav 176,72,112,112\nQta 68,68,33024\nv_add_u32 v75, 1, v75\nQmaav 176,76,120\ns_waitcnt vmcnt(35)\nds_write_b32 v73, v75\nQcaav 172,72,80,112\nQk 160,160,161\n"
"Qk 161,162,163\nQmaav 172,76,88\nQta 70,68,34048\ns_mov_b32 s95, 125952\ns_waitcnt lgkmcnt(5)\nQcaav 200,56,80,112\nbuffer_load_dwordx4 v62, s[52:55], s95 offen offset:1024 lds\nQmaav 200,60,88\n"
"Qk 162,164,165\nQk 163,166,167\nQcaav 204,56,112,112\nQk 128,128,129\nQk 129,130,131\nQmaav 204,60,120\nQk 130,132,133\nQk 131,134,135\n"
"Qcaav 208,56,144,112\nQta 72,68,512\nQk 200,200,201\nQmaav 208,60,152\nds_read_b32 v66, v74\ns_add_u32 m0, s96, 122880\ns_mov_b32 s95, 86016\nQcaav 212,56,176,112\n"
"buffer_load_dwordx4 v63, s[52:55], s95 offen lds\nQmaav 212,60,184\nQta 74,68,1536\nQk 201,202,203\nQcaav 216,56,208,112\nQk 202,204,205\nQk 203,206,207\nQmaav 216,60,216\n"
"s_waitcnt lgkmcnt(1)\nv_cmp_gt_u32 vcc, v75, v66\nQcaav 220,56,240,112\ns_cbranch_vccnz .Lso40\n.Lro40:\nQk 232,232,233\nQmaav 220,60,248\nQta 76,68,8704\n"
"Qk 233,234,235\nQcaav 224,56,84,112\nbuffer_store_dwordx4 v[144:147], v79, s[56:59], s88 offen offset:2048 sc1\nv_xor_b32 v56, 8, v73\nQmaav 224,60,116\nds_read_b32 v56, v56 offset:16\nQk 234,236,237\ns_waitcnt lgkmcnt(5)\n"
"Qcaav 252,64,84,112\nQta 78,68,9728\nQk 235,238,239\nQmaav 252,68,116\nQcaav 248,64,240,112\nQmaav 248,68,248\nQcaav 244,64,208,112\nQta 56,68,768\n"
"Qmaav 244,68,216\ns_mov_b32 s95, 105472\nQcaav 240,64,176,112\nbuffer_load_dwordx4 v63, s[52:55], s95 offen offset:1024 lds\nQmaav 240,68,184\nQta 58,68,1792\ns_waitcnt lgkmcnt(3)\nv_mov_b32 v57, v56\n"
"Qcaav 236,64,144,112\nv_mov_b32 v58, v56\nv_mov_b32 v59, v56\nQmaav 236,68,152\nv_accvgpr_write_b32 a80, v56\nv_accvgpr_write_b32 a81, v56\nQcaav 232,64,112,112\nQta 60,68,8960\n"
"v_accvgpr_write_b32 a82, v56\nQmaav 232,68,120\nv_accvgpr_write_b32 a83, v56\nQcaav 228,64,80,112\nQmaav 228,68,88\nQta 62,68,9984\ns_waitcnt lgkmcnt(4)\nQcvav 0,72,80,56\n"
"Qmvav 0,76,88\nQcvav 4,72,112,56\nbuffer_store_dwordx4 v[176:179], v79, s[56:59], s88 offen offset:3072 sc1\nQmvav 4,76,120\nQcvav 8,72,144,56\nQta 64,68,16640\nQmvav 8,76,152\nQcvav 12,72,176,56\n"
"Qmvav 12,76,184\nQta 66,68,17152\nQcvav 16,72,208,56\nQmvav 16,76,216\nQcvav 20,72,240,56\nQta 68,68,17664\nQmvav 20,76,248\nQcvav 24,72,84,56\n"
"Qmvav 24,76,116\nQta 70,68,18176\ns_waitcnt lgkmcnt(4)\nQcvav 52,56,84,56\nQmvav 52,60,116\nQcvav 48,56,240,56\nQmvav 48,60,248\nQcvav 44,56,208,56\n"
"Qta 72,68,25088\nQmvav 44,60,216\nQcvav 40,56,176,56\nQmvav 40,60,184\nQta 74,68,26112\ns_add_u32 s88, s71, 4096\nQcvav 36,56,144,56\nbuffer_store_dwordx4 v[208:211], v79, s[56:59], s88 offen offset:0 sc1\n"
"Qmvav 36,60,152\nQcvav 32,56,112,56\nQta 76,68,33280\nQmvav 32,60,120\nQcvav 28,56,80,56\nQmvav 28,60,88\nQta 78,68,34304\ns_waitcnt lgkmcnt(4)\n"
"Qcvav 92,64,80,56\nQmvav 92,68,88\nQcvav 124,64,112,56\ns_lshl_b32 s46, s19, 4\nQmvav 124,68,120\ns_cmp_eq_u32 s70, 0\ns_cselect_b32 s46, s46, -1\nQcvav 156,64,144,56\n"
"Qta 56,68,25344\nv_mov_b32 v77, 0x7ffff000\nQmvav 156,68,152\nQcvav 188,64,176,56\nQmvav 188,68,184\nQta 58,68,26368\nQcvav 220,64,208,56\nQmvav 220,68,216\n"
"Qcvav 252,64,240,56\nQta 60,68,33536\nQmvav 252,68,248\nQcvav 100,64,84,56\nQmvav 100,68,116\nQta 62,68,34560\ns_waitcnt lgkmcnt(4)\nQcvav 172,72,84,56\n"
"buffer_store_dwordx4 v[240:243], v79, s[56:59], s88 offen offset:1024 sc1\nQmvav 172,76,116\nQcvav 140,72,240,56\nQmvav 140,76,248\nQcvav 108,72,208,56\nQta 64,68,40960\nQmvav 108,76,216\nQcvav 228,72,176,56\n"
"Qmvav 228,76,184\nQta 66,68,41984\nQcvav 196,72,144,56\nQmvav 196,76,152\nQcvav 164,72,112,56\nQta 68,68,49152\nQmvav 164,76,120\nQcvav 132,72,80,56\n"
"Qmvav 132,76,88\nQta 70,68,50176\ns_waitcnt lgkmcnt(4)\nQcvav 204,56,80,56\nQmvav 204,60,88\nQcvav 236,56,112,56\nQmvav 236,60,120\nQcaav 96,56,144,80\n"
"Qta 72,68,41216\nQmaav 96,60,152\nQcaav 100,56,176,80\nbuffer_store_dwordx4 v[84:87], v79, s[56:59], s88 offen offset:2048 sc1\nQmaav 100,60,184\nQta 74,68,42240\nQcaav 104,56,208,80\nQmaav 104,60,216\n"
"Qcaav 108,56,240,80\nQta 76,68,49408\nQmaav 108,60,248\nQcaav 112,56,84,80\nQmaav 112,60,116\nQta 78,68,50432\n.Lpg14:\ns_cbranch_execz .Ltr6\n"
"s_waitcnt vmcnt(15)\ns_waitcnt lgkmcnt(4)\nQmaaa 140,64,24\nQmaaa 140,68,52\nQmaaa 136,64,20\nQmaaa 136,68,48\nQmaaa 132,64,16\nQta 56,68,41472\n"
"Qmaaa 132,68,44\nQmaaa 128,64,12\nQmaaa 128,68,40\nQta 58,68,42496\nQmaaa 124,64,8\nQmaaa 124,68,36\nQmaaa 120,64,4\nQta 60,68,49664\n"
"s_and_b32 s68, s19, 3\nQmaaa 120,68,32\ns_add_u32 s88, s71, 16384\nQmaaa 116,64,0\nbuffer_store_dwordx4 v[88:91], v79, s[56:59], s88 offen offset:0 sc1\nQmaaa 116,68,28\nQta 62,68,50688\ns_waitcnt lgkmcnt(4)\n"
"Qmaaa 144,72,0\ns_cmp_lt_u32 s68, 1\ns_cselect_b32 s68, 40, 32\nQmaaa 144,76,28\ns_mul_i32 s67, s68, 40960\ns_mul_i32 s74, s68, 32768\nQmaaa 148,72,4\ns_and_b32 s68, s19, 3\n"
"Qmaaa 148,76,32\ns_cmp_lt_u32 s68, 2\ns_cselect_b32 s68, 48, 40\nQmaaa 152,72,8\nQta 64,68,41728\ns_mul_i32 s72, s68, 40960\nQmaaa 152,76,36\ns_mul_i32 s75, s68, 32768\n"
"s_and_b32 s68, s19, 3\nQmaaa 156,72,12\ns_cmp_lt_u32 s68, 3\ns_cselect_b32 s68, 56, 48\nQmaaa 156,76,40\nQta 66,68,42752\ns_mul_i32 s73, s68, 40960\nQmaaa 160,72,16\n"
"s_mul_i32 s77, s68, 32768\nv_add_u32 v58, s91, v60\nQmaaa 160,76,44\ns_mul_i32 s68, s93, 28672\ns_sub_u32 s68, 14336, s68\nQmaaa 164,72,20\nQta 68,68,49920\nv_add_u32 v59, s68, v58\n"
"Qmaaa 164,76,48\nQmaaa 168,72,24\nQmaaa 168,76,52\nQta 70,68,50944\ns_waitcnt lgkmcnt(4)\nQmvaa 24,56,24\nQmvaa 24,60,52\nQmvaa 20,56,20\n"
"Qmvaa 20,60,48\nQmvaa 16,56,16\nQta 72,68,57344\nQmvaa 16,60,44\nbuffer_store_dwordx4 v[120:123], v79, s[56:59], s88 offen offset:1024 sc1\nQmvaa 12,56,12\nQmvaa 12,60,40\nQta 74,68,57856\n"
"Qmvaa 8,56,8\nQmvaa 8,60,36\nQmvaa 4,56,4\nQta 76,68,58368\nv_add_u32 v75, 1, v75\nQmvaa 4,60,32\ns_waitcnt vmcnt(35)\nds_write_b32 v73, v75\n"
"Qmvaa 0,56,0\nQmvaa 0,60,28\nQta 78,68,58880\ns_waitcnt lgkmcnt(5)\nQmvaa 28,64,0\nQmvaa 28,68,28\nQmvaa 32,64,4\nQmvaa 32,68,32\n"
"Qmvaa 36,64,8\nQta 56,68,57600\nQmvaa 36,68,36\nds_read_b32 v66, v74\nQmvaa 40,64,12\nQmvaa 40,68,40\nQta 58,68,58112\nQmvaa 44,64,16\n"
"Qmvaa 44,68,44\ns_waitcnt lgkmcnt(1)\nv_cmp_gt_u32 vcc, v75, v66\nQmvaa 48,64,20\ns_cbranch_vccnz .Lso180\n.Lro180:\nQmvaa 48,68,48\nQta 60,68,58624\n"
"Qmvaa 52,64,24\nbuffer_store_dwordx4 v[152:155], v79, s[56:59], s88 offen offset:2048 sc1\nQmvaa 52,68,52\ns_waitcnt lgkmcnt(4)\nQmaaa 196,72,24\nQta 62,68,59136\nQmaaa 196,76,52\nQmaaa 192,72,20\n"
"Qmaaa 192,76,48\nQmaaa 188,72,16\nQta 64,70,1024\nQmaaa 188,76,44\ns_add_u32 m0, s96, 0\ns_mov_b32 s95, 126976\nQmaaa 184,72,12\nbuffer_load_dwordx4 v63, s[52:55], s95 offen lds\n"
"Qmaaa 184,76,40\nQta 66,70,2048\nQmaaa 180,72,8\nQmaaa 180,76,36\nQmaaa 176,72,4\nQta 68,70,9216\ns_mov_b32 s95, 146432\nQmaaa 176,76,32\n"
"buffer_load_dwordx4 v63, s[52:55], s95 offen offset:1024 lds\nQmaaa 172,72,0\nQmaaa 172,76,28\nQta 70,70,10240\ns_waitcnt lgkmcnt(4)\nQmvaa 92,56,0\nQmvaa 92,60,28\nQmvaa 124,56,4\n"
"buffer_store_dwordx4 v[184:187], v79, s[56:59], s88 offen offset:3072 sc1\nQmvaa 124,60,32\nQmvaa 156,56,8\nQta 72,70,1280\nQmvaa 156,60,36\nQmvaa 188,56,12\nQmvaa 188,60,40\nQta 74,70,2304\n"
"Qmvaa 220,56,16\nQmvaa 220,60,44\nQmvaa 252,56,20\nQta 76,70,9472\ns_add_u32 m0, s96, 8192\ns_mov_b32 s95, 331776\nQmvaa 252,60,48\nbuffer_load_dwordx4 v61, s[52:55], s95 offen lds\n"
"Qmvaa 100,56,24\nQmvaa 100,60,52\nQta 78,70,10496\ns_waitcnt lgkmcnt(4)\nQmaaa 224,64,24\nQmaaa 224,68,52\ns_mov_b32 s95, 351232\nQmaaa 220,64,20\n"
"buffer_load_dwordx4 v61, s[52:55], s95 offen offset:1024 lds\nQmaaa 220,68,48\nQmaaa 216,64,16\nQta 56,70,1536\nQmaaa 216,68,44\nQmaaa 212,64,12\nQmaaa 212,68,40\nQta 58,70,2560\n"
"s_add_u32 s88, s71, 20480\nQmaaa 208,64,8\nbuffer_store_dwordx4 v[216:219], v79, s[56:59], s88 offen offset:0 sc1\nQmaaa 208,68,36\nQmaaa 204,64,4\nQta 60,70,9728\nQmaaa 204,68,32\nQmaaa 200,64,0\n"
"Qmaaa 200,68,28\nQta 62,70,10752\ns_waitcnt lgkmcnt(4)\nQmaaa 228,72,0\nQmaaa 228,76,28\ns_add_u32 m0, s96, 16384\ns_mov_b32 s95, 372736\nQmaaa 232,72,4\n"
"buffer_load_dwordx4 v61, s[52:55], s95 offen lds\nQmaaa 232,76,32\nQmaaa 236,72,8\nQta 64,70,1792\nQmaaa 236,76,36\nQmaaa 240,72,12\nQmaaa 240,76,40\nQta 66,70,2816\n"
"s_mov_b32 s95, 392192\nQmaaa 244,72,16\nbuffer_load_dwordx4 v61, s[52:55], s95 offen offset:1024 lds\nQmaaa 244,76,44\nQmaaa 248,72,20\nQta 68,70,9984\nQmaaa 248,76,48\nQmaaa 252,72,24\n"
"Qmaaa 252,76,52\nQta 70,70,11008\ns_waitcnt lgkmcnt(4)\nQmvaa 172,56,24\nbuffer_store_dwordx4 v[248:251], v79, s[56:59], s88 offen offset:1024 sc1\nQmvaa 172,60,52\nQmvaa 140,56,20\nQmvaa 140,60,48\n"
"Qmvaa 108,56,16\nQta 72,70,17408\nQmvaa 108,60,44\nQmvaa 228,56,12\nQmvaa 228,60,40\nQta 74,70,18432\ns_add_u32 m0, s96, 24576\ns_mov_b32 s95, 331776\n"
"Qmvaa 196,56,8\nbuffer_load_dwordx4 v62, s[52:55], s95 offen lds\nQmvaa 196,60,36\nQmvaa 164,56,4\nQta 76,69,0\nQmvaa 164,60,32\nQmvaa 132,56,0\nQmvaa 132,60,28\n"
"Qta 78,69,1024\ns_mov_b32 s95, 371712\ns_waitcnt lgkmcnt(4)\nQmvaa 204,64,0\nbuffer_load_dwordx4 v62, s[52:55], s95 offen offset:1024 lds\nQmvaa 204,68,28\nQmvaa 236,64,4\nQmvaa 236,68,32\n"
"Qmaaa 96,64,8\nQta 56,70,17664\nQmaaa 96,68,36\nQmaaa 100,64,12\nbuffer_store_dwordx4 v[116:119], v79, s[56:59], s88 offen offset:2048 sc1\nv_cmp_eq_u32 s[44:45], s46, v60\ns_nop 1\nv_cndmask_b32 v77, v77, v76, s[44:45]\n"
"Qmaaa 100,68,40\nQta 58,70,18688\nQmaaa 104,64,16\nQmaaa 104,68,44\nQmaaa 108,64,20\nQta 60,69,256\nQmaaa 108,68,48\nQmaaa 112,64,24\n"
"Qmaaa 112,68,52\nQta 62,69,1280\ns_mov_b32 s31, 131072\ns_waitcnt lgkmcnt(4)\nQmaav 140,72,180\nbuffer_load_dwordx4 a[0:3], v79, s[48:51], s31 offen offset:0\nQmaav 140,76,244\nQmaav 136,72,148\n"
"Qmaav 136,76,212\nQmaav 132,72,224\nQta 64,70,17920\nQmaav 132,76,232\nbuffer_load_dwordx4 a[4:7], v79, s[48:51], s31 offen offset:1024\nQmaav 128,72,192\nQmaav 128,76,200\nQta 66,70,18944\n"
"Qmaav 124,72,160\nQmaav 124,76,168\nbuffer_load_dwordx4 a[8:11], v79, s[48:51], s31 offen offset:2048\nQmaav 120,72,128\nQta 68,69,512\nQmaav 120,76,136\nQmaav 116,72,96\nQmaav 116,76,104\n"
"Qta 70,69,1536\ns_waitcnt lgkmcnt(4)\nQmaav 144,56,96\nbuffer_load_dwordx4 a[12:15], v79, s[48:51], s31 offen offset:3072\nQmaav 144,60,104\nQmaav 148,56,128\nQmaav 148,60,136\nQmaav 152,56,160\n"
"Qta 72,70,18176\ns_mov_b32 s31, 135168\nQmaav 152,60,168\nbuffer_load_dwordx4 a[16:19], v79, s[48:51], s31 offen offset:0\nQmaav 156,56,192\nQmaav 156,60,200\nQta 74,70,19200\nQmaav 160,56,224\n"
"Qmaav 160,60,232\nbuffer_load_dwordx4 a[20:23], v79, s[48:51], s31 offen offset:1024\nQmaav 164,56,148\nQta 76,69,768\nQmaav 164,60,212\nQmaav 168,56,180\nQmaav 168,60,244\nQta 78,69,1792\n"
"s_waitcnt lgkmcnt(4)\nQmvav 24,64,180\nbuffer_load_dwordx4 a[24:27], v79, s[48:51], s31 offen offset:2048\nQmvav 24,68,244\nQmvav 20,64,148\nQmvav 20,68,212\nQmvav 16,64,224\nQta 56,69,8192\n"
"s_mov_b32 s31, 147456\nQmvav 16,68,232\nbuffer_load_dwordx4 a[28:31], v79, s[48:51], s31 offen offset:0\nQmvav 12,64,192\nQmvav 12,68,200\nQta 58,69,8704\nQmvav 8,64,160\nQmvav 8,68,168\n"
"buffer_load_dwordx4 a[32:35], v79, s[48:51], s31 offen offset:1024\nQmvav 4,64,128\nQta 60,69,9216\nv_add_u32 v75, 1, v75\nQmvav 4,68,136\ns_waitcnt vmcnt(31)\nds_write_b32 v73, v75\nQmvav 0,64,96\n"
"Qmvav 0,68,104\nQta 62,69,9728\ns_waitcnt lgkmcnt(5)\nQmvav 28,72,96\nbuffer_load_dwordx4 a[36:39], v79, s[48:51], s31 offen offset:2048\nQmvav 28,76,104\nQmvav 32,72,128\nQmvav 32,76,136\n"
"Qmvav 36,72,160\nQta 64,69,8448\nQmvav 36,76,168\nds_read_b32 v66, v74\nQmvav 40,72,192\nbuffer_load_dwordx4 a[40:43], v79, s[48:51], s31 offen offset:3072\nQmvav 40,76,200\nQta 66,69,8960\n"
"Qmvav 44,72,224\nQmvav 44,76,232\ns_waitcnt lgkmcnt(1)\nv_cmp_gt_u32 vcc, v75, v66\nQmvav 48,72,148\ns_cbranch_vccnz .Lso320\n.Lro320:\nQmvav 48,76,212\n"
"Qta 68,69,9472\ns_mov_b32 s31, 151552\nQmvav 52,72,180\nbuffer_load_dwordx4 a[44:47], v79, s[48:51], s31 offen offset:0\nQmvav 52,76,244\ns_waitcnt lgkmcnt(4)\nQmaav 196,56,180\nQta 70,69,9984\n"
"Qmaav 196,60,244\nQmaav 192,56,148\nbuffer_load_dwordx4 a[48:51], v79, s[48:51], s31 offen offset:1024\nQmaav 192,60,212\nQmaav 188,56,224\nQta 72,69,16384\nQmaav 188,60,232\nQmaav 184,56,192\n"
"buffer_load_dwordx4 a[52:55], v79, s[48:51], s31 offen offset:2048\nQmaav 184,60,200\nQta 74,69,17408\nQmaav 180,56,160\nQmaav 180,60,168\nQmaav 176,56,128\nQta 76,69,24576\ns_mov_b32 s31, 262144\n"
"Qmaav 176,60,136\nbuffer_load_dwordx4 v[80:83], v79, s[48:51], s31 offen offset:0\nQmaav 172,56,96\nQmaav 172,60,104\nQta 78,69,25600\ns_waitcnt lgkmcnt(4)\nQmvav 92,64,96\nQmvav 92,68,104\n"
"buffer_load_dwordx4 v[112:115], v79, s[48:51], s31 offen offset:1024\nQmvav 124,64,128\nQmvav 124,68,136\nQmvav 156,64,160\nQta 56,69,16640\nQmvav 156,68,168\nbuffer_load_dwordx4 v[144:147], v79, s[48:51], s31 offen offset:2048\nQmvav 188,64,192\n"
"Qmvav 188,68,200\nQta 58,69,17664\nQmvav 220,64,224\nQmvav 220,68,232\nbuffer_load_dwordx4 v[176:179], v79, s[48:51], s31 offen offset:3072\nQmvav 252,64,148\nQta 60,69,24832\nQmvav 252,68,212\n"
"Qmvav 100,64,180\nQmvav 100,68,244\nQta 62,69,25856\ns_mov_b32 s31, 266240\ns_waitcnt lgkmcnt(4)\nQmaav 224,72,180\nbuffer_load_dwordx4 v[208:211], v79, s[48:51], s31 offen offset:0\nQmaav 224,76,244\n"
"Qmaav 220,72,148\nQmaav 220,76,212\nQmaav 216,72,224\nQta 64,69,16896\nQmaav 216,76,232\nbuffer_load_dwordx4 v[240:243], v79, s[48:51], s31 offen offset:1024\nQmaav 212,72,192\nQmaav 212,76,200\n"
"Qta 66,69,17920\nQmaav 208,72,160\nQmaav 208,76,168\nbuffer_load_dwordx4 v[84:87], v79, s[48:51], s31 offen offset:2048\nQmaav 204,72,128\nQta 68,69,25088\nQmaav 204,76,136\nQmaav 200,72,96\n"
"Qmaav 200,76,104\nQta 70,69,26112\ns_mov_b32 s31, 278528\ns_waitcnt lgkmcnt(4)\nQmaav 228,56,96\nbuffer_load_dwordx4 v[88:91], v79, s[48:51], s31 offen offset:0\nQmaav 228,60,104\nQmaav 232,56,128\n"
"Qmaav 232,60,136\nQmaav 236,56,160\nQta 72,69,17152\nQmaav 236,60,168\nbuffer_load_dwordx4 v[120:123], v79, s[48:51], s31 offen offset:1024\nQmaav 240,56,192\nQmaav 240,60,200\nQta 74,69,18176\n"
"Qmaav 244,56,224\nQmaav 244,60,232\nbuffer_load_dwordx4 v[152:155], v79, s[48:51], s31 offen offset:2048\nQmaav 248,56,148\nQta 76,69,25344\nQmaav 248,60,212\nQmaav 252,56,180\nQmaav 252,60,244\n"
"Qta 78,69,26368\ns_waitcnt lgkmcnt(4)\nQmvav 172,64,180\nbuffer_load_dwordx4 v[184:187], v79, s[48:51], s31 offen offset:3072\nQmvav 172,68,244\nQmvav 140,64,148\nQmvav 140,68,212\nQmvav 108,64,224\n"
"Qta 56,69,32768\ns_mov_b32 s31, 282624\nQmvav 108,68,232\nbuffer_load_dwordx4 v[216:219], v79, s[48:51], s31 offen offset:0\nQmvav 228,64,192\nQmvav 228,68,200\nQta 58,69,33792\nQmvav 196,64,160\n"
"Qmvav 196,68,168\nbuffer_load_dwordx4 v[248:251], v79, s[48:51], s31 offen offset:1024\nQmvav 164,64,128\nQta 60,69,40960\nQmvav 164,68,136\nQmvav 132,64,96\nQmvav 132,68,104\nQta 62,69,41984\n"
"s_waitcnt lgkmcnt(4)\nQmvav 204,72,96\nbuffer_load_dwordx4 v[116:119], v79, s[48:51], s31 offen offset:2048\nQmvav 204,76,104\nQmvav 236,72,128\nQmvav 236,76,136\nQmaav 96,72,160\nQta 64,69,33024\n"
"s_add_u32 m0, s96, 32768\ns_mov_b32 s95, 331776\nQmaav 96,76,168\nbuffer_load_dwordx4 v63, s[52:55], s95 offen lds\nQmaav 100,72,192\nQmaav 100,76,200\nQta 66,69,34048\nQmaav 104,72,224\n"
"Qmaav 104,76,232\nQmaav 108,72,148\nQta 68,69,41216\ns_mov_b32 s95, 351232\nQmaav 108,76,212\nbuffer_load_dwordx4 v63, s[52:55], s95 offen offset:1024 lds\nQmaav 112,72,180\nQmaav 112,76,244\n"
"Qta 70,69,42240\ns_waitcnt vmcnt(16)\ns_waitcnt lgkmcnt(4)\nQmaaa 140,56,24\nQmaaa 140,60,52\ns_add_u32 m0, s96, 40960\ns_mov_b32 s95, 372736\nQmaaa 136,56,20\n"
"buffer_load_dwordx4 v63, s[52:55], s95 offen lds\nQmaaa 136,60,48\nQmaaa 132,56,16\nQta 72,69,33280\nQmaaa 132,60,44\nQmaaa 128,56,12\nQmaaa 128,60,40\nQta 74,69,34304\n"
"s_mov_b32 s95, 392192\nQmaaa 124,56,8\nbuffer_load_dwordx4 v63, s[52:55], s95 offen offset:1024 lds\nQmaaa 124,60,36\nQmaaa 120,56,4\nQta 76,69,41472\ns_add_u32 s88, s71, 131072\nQmaaa 120,60,32\n"
"buffer_store_dwordx4 v[96:99], v79, s[56:59], s88 offen offset:0 sc1\nQmaaa 116,56,0\nQmaaa 116,60,28\nQta 78,69,42496\ns_waitcnt lgkmcnt(4)\nQmaaa 144,64,0\nQmaaa 144,68,28\nQmaaa 148,64,4\n"
"Qmaaa 148,68,32\nQmaaa 152,64,8\nQta 56,69,33536\ns_add_u32 m0, s96, 49152\ns_mov_b32 s95, 413696\nQmaaa 152,68,36\nbuffer_load_dwordx4 v61, s[52:55], s95 offen lds\nQmaaa 156,64,12\n"
"Qmaaa 156,68,40\nQta 58,69,34560\nQmaaa 160,64,16\nQmaaa 160,68,44\nQmaaa 164,64,20\nQta 60,69,41728\ns_mov_b32 s95, 433152\nQmaaa 164,68,48\n"
"buffer_load_dwordx4 v61, s[52:55], s95 offen offset:1024 lds\n.Lpg15:\ns_cbranch_execz .Ltr6\nQmaaa 168,64,24\nQmaaa 168,68,52\nQta 62,69,42752\ns_waitcnt lgkmcnt(4)\nQmvaa 24,72,24\n"
"Qmvaa 24,76,52\nQmvaa 20,72,20\nbuffer_store_dwordx4 v[128:131], v79, s[56:59], s88 offen offset:1024 sc1\nQmvaa 20,76,48\nQmvaa 16,72,16\nQta 64,69,49152\nQmvaa 16,76,44\nQmvaa 12,72,12\n"
"Qmvaa 12,76,40\nQta 66,69,49664\nQmvaa 8,72,8\nQmvaa 8,76,36\nQmvaa 4,72,4\nQta 68,69,50176\nv_add_u32 v75, 1, v75\nQmvaa 4,76,32\n"
"s_waitcnt vmcnt(36)\nds_write_b32 v73, v75\ns_add_u32 m0, s96, 57344\ns_mov_b32 s95, 454656\nQmvaa 0,72,0\nbuffer_load_dwordx4 v61, s[52:55], s95 offen lds\nQmvaa 0,76,28\nQta 70,69,50688\n"
"s_waitcnt lgkmcnt(5)\nQmvaa 28,56,0\nQmvaa 28,60,28\nQmvaa 32,56,4\ns_mov_b32 s95, 474112\nQmvaa 32,60,32\nbuffer_load_dwordx4 v61, s[52:55], s95 offen offset:1024 lds\nQmvaa 36,56,8\n"
"Qta 72,69,49408\nQmvaa 36,60,36\nds_read_b32 v66, v74\nQmvaa 40,56,12\nQmvaa 40,60,40\nQta 74,69,49920\nQmvaa 44,56,16\nbuffer_store_dwordx4 v[160:163], v79, s[56:59], s88 offen offset:2048 sc1\n"
"v_add_u32 v67, 2, v65\nQmvaa 44,60,44\ns_waitcnt lgkmcnt(1)\nv_cmp_gt_u32 vcc, v75, v66\nQmvaa 48,56,20\ns_cbranch_vccnz .Lso460\n.Lro460:\nQmvaa 48,60,48\n"
"Qta 76,69,50432\nQmvaa 52,56,24\nbuffer_store_dword v67, v77, s[24:27], 0 offen sc1\nQmvaa 52,60,52\ns_waitcnt lgkmcnt(4)\nQmaaa 196,64,24\nQta 78,69,50944\nQmaaa 196,68,52\n"
"s_add_u32 m0, s96, 132096\ns_mov_b32 s95, 413696\nQmaaa 192,64,20\nbuffer_load_dwordx4 v62, s[52:55], s95 offen lds\nQmaaa 192,68,48\nQmaaa 188,64,16\nQta 56,69,57344\nQmaaa 188,68,44\n"
"Qmaaa 184,64,12\nQmaaa 184,68,40\nQta 58,69,58368\ns_mov_b32 s95, 453632\nQmaaa 180,64,8\nbuffer_load_dwordx4 v62, s[52:55], s95 offen offset:1024 lds\nQmaaa 180,68,36\nQmaaa 176,64,4\n"
"Qta 60,68,0\nQmaaa 176,68,32\nQmaaa 172,64,0\nQmaaa 172,68,28\nQta 62,68,1024\ns_waitcnt lgkmcnt(4)\nQmvaa 92,72,0\nbuffer_store_dwordx4 v[192:195], v79, s[56:59], s88 offen offset:3072 sc1\n"
"Qmvaa 92,76,28\nQmvaa 124,72,4\nQmvaa 124,76,32\nQmvaa 156,72,8\nQta 64,69,57600\nQmvaa 156,76,36\nQmvaa 188,72,12\nQmvaa 188,76,40\n"
"Qta 66,69,58624\ns_add_u32 m0, s96, 140288\ns_mov_b32 s95, 413696\nQmvaa 220,72,16\nbuffer_load_dwordx4 v63, s[52:55], s95 offen lds\nQmvaa 220,76,44\nQmvaa 252,72,20\nQta 68,68,256\n"
"Qmvaa 252,76,48\nQmvaa 100,72,24\nQmvaa 100,76,52\nQta 70,68,1280\ns_mov_b32 s95, 433152\ns_waitcnt lgkmcnt(4)\nQmaaa 224,56,24\nbuffer_load_dwordx4 v63, s[52:55], s95 offen offset:1024 lds\n"
"Qmaaa 224,60,52\nQmaaa 220,56,20\nQmaaa 220,60,48\nQmaaa 216,56,16\nQta 72,69,57856\nQmaaa 216,60,44\ns_add_u32 s88, s71, 135168\nQmaaa 212,56,12\n"
"buffer_store_dwordx4 v[224:227], v79, s[56:59], s88 offen offset:0 sc1\nQmaaa 212,60,40\nQta 74,69,58880\nQmaaa 208,56,8\nQmaaa 208,60,36\nQmaaa 204,56,4\nQta 76,68,512\nQmaaa 204,60,32\n"
"Qmaaa 200,56,0\nQmaaa 200,60,28\nQta 78,68,1536\ns_add_u32 m0, s96, 148480\ns_mov_b32 s95, 454656\ns_waitcnt lgkmcnt(4)\nQmaaa 228,64,0\nbuffer_load_dwordx4 v63, s[52:55], s95 offen lds\n"
"Qmaaa 228,68,28\nQmaaa 232,64,4\nQmaaa 232,68,32\nQmaaa 236,64,8\nQta 56,69,58112\ns_mov_b32 s95, 474112\nQmaaa 236,68,36\nbuffer_load_dwordx4 v63, s[52:55], s95 offen offset:1024 lds\n"
"Qmaaa 240,64,12\nQmaaa 240,68,40\nQta 58,69,59136\nQmaaa 244,64,16\nQmaaa 244,68,44\nQmaaa 248,64,20\nQta 60,68,768\nQmaaa 248,68,48\n"
"Qmaaa 252,64,24\nbuffer_store_dwordx4 v[148:151], v79, s[56:59], s88 offen offset:1024 sc1\nQmaaa 252,68,52\nQta 62,68,1792\ns_waitcnt lgkmcnt(4)\nQmvaa 172,72,24\nQmvaa 172,76,52\ns_mov_b32 s40, s22\n"
"s_and_b32 s41, s23, 0xffff\nQmvaa 140,72,20\ns_mov_b32 s42, 571200\ns_lshl_b32 s66, s19, 17\nQmvaa 140,76,48\ns_add_u32 s69, s98, 4194304\ns_add_u32 s98, s66, s69\nQmvaa 108,72,16\n"
"Qta 64,68,8192\ns_xor_b32 s66, s66, 524288\nQmvaa 108,76,44\ns_add_u32 m0, s96, 65536\ns_mov_b32 s95, 659456\nQmvaa 228,72,12\nbuffer_load_dwordx4 v61, s[52:55], s95 offen lds\nQmvaa 228,76,40\n"
"Qta 66,68,9216\ns_add_u32 s64, s66, s69\nQmvaa 196,72,8\ns_mul_i32 s66, s92, 33792\ns_add_u32 s66, s66, %4\nQmvaa 196,76,36\ns_add_u32 s66, s66, 98304\ns_mul_i32 s68, s93, 96\n"
"Qmvaa 164,72,4\nQta 68,68,16384\ns_mov_b32 s95, 678912\nQmvaa 164,76,32\nbuffer_load_dwordx4 v61, s[52:55], s95 offen offset:1024 lds\nQmvaa 132,72,0\ns_sub_u32 s68, s66, s68\ns_add_u32 s68, s68, 96\n"
"Qmvaa 132,76,28\nQta 70,68,17408\nv_bfe_u32 v148, v60, 4, 4\ns_waitcnt lgkmcnt(4)\nQmvaa 204,56,0\ns_mov_b32 s31, 393216\nv_mul_u32_u24 v148, 208, v148\nQmvaa 204,60,28\n"
"buffer_load_dwordx4 a[0:3], v79, s[48:51], s31 offen offset:0\nQmvaa 236,56,4\nv_lshrrev_b32 v149, 8, v60\nv_lshl_add_u32 v148, v149, 3, v148\nQmvaa 236,60,32\nv_add_u32 v56, s68, v148\ns_lshl_b32 s68, s93, 3\nQmaaa 96,56,8\n"
"Qta 72,68,8448\nv_lshrrev_b32 v148, 7, v60\nQmaaa 96,60,36\nbuffer_store_dwordx4 v[180:183], v79, s[56:59], s88 offen offset:2048 sc1\nQmaaa 100,56,12\nv_add_u32 v148, s68, v148\nv_and_b32 v149, 127, v60\nQmaaa 100,60,40\n"
"Qta 74,68,9472\nv_and_b32 v150, 64, v60\nQmaaa 104,56,16\nv_lshrrev_b32 v150, 1, v150\nv_mul_u32_u24 v57, 208, v148\nQmaaa 104,60,44\nv_add3_u32 v57, v57, v149, s66\nv_add_u32 v57, v57, v150\n"
"Qmaaa 108,56,20\nQta 76,68,16640\ns_and_b32 s65, s19, 1\nQmaaa 108,60,48\ns_and_b32 s68, s19, 4\ns_lshl_b32 s68, s68, 2\nQmaaa 112,56,24\ns_lshl_b32 s69, s65, 3\n"
"s_add_u32 s68, s68, s69\nQmaaa 112,60,52\nQta 78,68,17664\ns_lshr_b32 s68, 0xc0004080, s68\ns_waitcnt vmcnt(25)\ns_waitcnt lgkmcnt(4)\nQmaav 140,64,84\nbuffer_load_dwordx4 a[4:7], v79, s[48:51], s31 offen offset:1024\n"
"Qmaav 140,68,116\ns_and_b32 s68, s68, 255\ns_mul_i32 s69, s92, 286720\nQmaav 136,64,240\ns_add_u32 s68, s68, s69\nv_mul_u32_u24 v64, 2560, v148\nQmaav 136,68,248\nv_add3_u32 v64, v64, v149, s68\n"
"s_lshl_b32 s68, s93, 4\nQmaav 132,64,208\nQta 56,68,8704\nv_lshrrev_b32 v148, 6, v60\nQmaav 132,68,216\nbuffer_load_dwordx4 a[8:11], v79, s[48:51], s31 offen offset:2048\nQmaav 128,64,176\nv_add_u32 v148, s68, v148\n"
"v_and_b32 v149, 63, v60\nQmaav 128,68,184\nQta 58,68,9728\nv_and_b32 v150, 32, v60\nQmaav 124,64,144\nv_lshlrev_b32 v150, 1, v150\nv_mul_u32_u24 v71, 208, v148\nQmaav 124,68,152\n"
"buffer_load_dwordx4 a[12:15], v79, s[48:51], s31 offen offset:3072\nQmaav 120,64,112\nQta 60,68,16896\nv_add3_u32 v71, v71, v149, s66\nQmaav 120,68,120\nv_add_u32 v71, v71, v150\ns_xor_b32 s65, s65, 1\nQmaav 116,64,80\n"
"s_lshl_b32 s65, s65, 8\ns_add_u32 s68, s65, s69\nQmaav 116,68,88\nQta 62,68,17920\ns_mov_b32 s31, 397312\ns_waitcnt lgkmcnt(4)\nQmaav 144,72,80\nbuffer_load_dwordx4 a[16:19], v79, s[48:51], s31 offen offset:0\n"
"Qmaav 144,76,88\nv_mul_u32_u24 v72, 2560, v148\nv_add3_u32 v72, v72, v149, s68\nQmaav 148,72,112\nQmaav 148,76,120\nQmaav 152,72,144\nQta 64,68,8960\nQmaav 152,76,152\n"
"buffer_load_dwordx4 a[20:23], v79, s[48:51], s31 offen offset:1024\nQmaav 156,72,176\nQmaav 156,76,184\nQta 66,68,9984\nQmaav 160,72,208\nQmaav 160,76,216\nbuffer_load_dwordx4 a[24:27], v79, s[48:51], s31 offen offset:2048\nQmaav 164,72,240\n"
"Qta 68,68,17152\nQmaav 164,76,248\nQmaav 168,72,84\nQmaav 168,76,116\nQta 70,68,18176\ns_mov_b32 s31, 409600\ns_waitcnt lgkmcnt(4)\nQmvav 24,56,84\n"
"buffer_load_dwordx4 a[28:31], v79, s[48:51], s31 offen offset:0\nQmvav 24,60,116\nQmvav 20,56,240\nQmvav 20,60,248\nQmvav 16,56,208\nQta 72,68,24576\nQmvav 16,60,216\nbuffer_load_dwordx4 a[32:35], v79, s[48:51], s31 offen offset:1024\n"
"Qmvav 12,56,176\nQmvav 12,60,184\nQta 74,68,25088\nQmvav 8,56,144\nQmvav 8,60,152\nbuffer_load_dwordx4 a[36:39], v79, s[48:51], s31 offen offset:2048\nQmvav 4,56,112\nQta 76,68,25600\n"
"v_add_u32 v75, 1, v75\nQmvav 4,60,120\ns_waitcnt vmcnt(20)\nds_write_b32 v73, v75\nQmvav 0,56,80\nQmvav 0,60,88\nQta 78,68,26112\ns_waitcnt lgkmcnt(5)\n"
"Qmvav 28,64,80\nbuffer_load_dwordx4 a[40:43], v79, s[48:51], s31 offen offset:3072\nQmvav 28,68,88\nQmvav 32,64,112\nQmvav 32,68,120\nQmvav 36,64,144\nQta 56,68,24832\nQmvav 36,68,152\n"
"ds_read_b32 v66, v74\ns_mov_b32 s31, 413696\nQmvav 40,64,176\nbuffer_load_dwordx4 a[44:47], v79, s[48:51], s31 offen offset:0\nQmvav 40,68,184\nQta 58,68,25344\nQmvav 44,64,208\nQmvav 44,68,216\n"
"s_waitcnt lgkmcnt(1)\nv_cmp_gt_u32 vcc, v75, v66\nQmvav 48,64,240\ns_cbranch_vccnz .Lso600\n.Lro600:\nQmvav 48,68,248\nQta 60,68,25856\nQmvav 52,64,84\n"
"buffer_load_dwordx4 a[48:51], v79, s[48:51], s31 offen offset:1024\nQmvav 52,68,116\ns_waitcnt lgkmcnt(4)\nQmaav 196,72,84\nQta 62,68,26368\nQmaav 196,76,116\nQmaav 192,72,240\nbuffer_load_dwordx4 a[52:55], v79, s[48:51], s31 offen offset:2048\n"
"Qmaav 192,76,248\nQmaav 188,72,208\nQta 64,68,32768\nQmaav 188,76,216\nQmaav 184,72,176\nQmaav 184,76,184\nQta 66,68,33792\ns_add_u32 m0, s96, 73728\n"
"s_mov_b32 s95, 700416\nQmaav 180,72,144\nbuffer_load_dwordx4 v61, s[52:55], s95 offen lds\nQmaav 180,76,152\nQmaav 176,72,112\nQta 68,68,40960\nQmaav 176,76,120\nQmaav 172,72,80\n"
"Qmaav 172,76,88\nQta 70,68,41984\ns_mov_b32 s95, 719872\ns_waitcnt lgkmcnt(4)\nQmvav 92,56,80\nbuffer_load_dwordx4 v61, s[52:55], s95 offen offset:1024 lds\nQmvav 92,60,88\nQmvav 124,56,112\n"
"Qmvav 124,60,120\nQmvav 156,56,144\nQta 72,68,33024\ns_add_u32 m0, s96, 81920\ns_mov_b32 s95, 659456\nQmvav 156,60,152\nbuffer_load_dwordx4 v62, s[52:55], s95 offen lds\nQmvav 188,56,176\n"
"Qmvav 188,60,184\nQta 74,68,34048\nQmvav 220,56,208\nQmvav 220,60,216\nQmvav 252,56,240\nQta 76,68,41216\ns_mov_b32 s95, 699392\nQmvav 252,60,248\n"
"buffer_load_dwordx4 v62, s[52:55], s95 offen offset:1024 lds\nQmvav 100,56,84\nQmvav 100,60,116\nQta 78,68,42240\ns_waitcnt lgkmcnt(4)\nQmaav 224,64,84\nQmaav 224,68,116\ns_add_u32 m0, s96, 90112\n"
"s_mov_b32 s95, 659456\nQmaav 220,64,240\nbuffer_load_dwordx4 v63, s[52:55], s95 offen lds\nQmaav 220,68,248\nQmaav 216,64,208\nQta 56,68,33280\nQmaav 216,68,216\nQmaav 212,64,176\n"
"Qmaav 212,68,184\nQta 58,68,34304\ns_mov_b32 s95, 678912\nQmaav 208,64,144\nbuffer_load_dwordx4 v63, s[52:55], s95 offen offset:1024 lds\nQmaav 208,68,152\nQmaav 204,64,112\nQta 60,68,41472\n"
"Qmaav 204,68,120\nQmaav 200,64,80\nQmaav 200,68,88\nQta 62,68,42496\ns_waitcnt lgkmcnt(4)\nQmaav 228,72,80\nQmaav 228,76,88\ns_add_u32 s88, s71, 147456\n"
"Qmaav 232,72,112\nbuffer_store_dwordx4 v[104:107], v79, s[56:59], s88 offen offset:0 sc1\nQmaav 232,76,120\nQmaav 236,72,144\nQta 64,68,33536\nQmaav 236,76,152\nQmaav 240,72,176\nQmaav 240,76,184\n"
"Qta 66,68,34560\nQmaav 244,72,208\nQmaav 244,76,216\nQmaav 248,72,240\nQta 68,68,41728\ns_add_u32 m0, s96, 98304\ns_mov_b32 s95, 700416\nQmaav 248,76,248\n"
"buffer_load_dwordx4 v63, s[52:55], s95 offen lds\nQmaav 252,72,84\nQmaav 252,76,116\nQta 70,68,42752\ns_waitcnt lgkmcnt(4)\nQmvav 172,56,84\nbuffer_store_dwordx4 v[136:139], v79, s[56:59], s88 offen offset:1024 sc1\nQmvav 172,60,116\n"
"Qmvav 140,56,240\nQmvav 140,60,248\nQmvav 108,56,208\nQta 72,68,49152\nQmvav 108,60,216\nQmvav 228,56,176\nQmvav 228,60,184\nQta 74,68,50176\n"
"s_mov_b32 s95, 719872\nQmvav 196,56,144\nbuffer_load_dwordx4 v63, s[52:55], s95 offen offset:1024 lds\nQmvav 196,60,152\nQmvav 164,56,112\nQta 76,68,57344\nQmvav 164,60,120\nQmvav 132,56,80\n"
"Qmvav 132,60,88\nQta 78,68,58368\ns_add_u32 m0, s96, 106496\ns_mov_b32 s95, 741376\ns_waitcnt lgkmcnt(4)\nQmvav 204,64,80\nbuffer_load_dwordx4 v61, s[52:55], s95 offen lds\nQmvav 204,68,88\n"
"Qmvav 236,64,112\nQmvav 236,68,120\nQmaav 96,64,144\nQta 56,68,49408\ns_mov_b32 s95, 760832\nQmaav 96,68,152\nbuffer_load_dwordx4 v61, s[52:55], s95 offen offset:1024 lds\nQmaav 100,64,176\n"
"Qmaav 100,68,184\nQta 58,68,50432\nQmaav 104,64,208\ns_mov_b32 s31, 524288\nQmaav 104,68,216\nbuffer_load_dwordx4 v[80:83], v79, s[48:51], s31 offen offset:0\nQmaav 108,64,240\nQta 60,68,57600\n"
"Qmaav 108,68,248\nQmaav 112,64,84\nQmaav 112,68,116\nQta 62,68,58624\ns_waitcnt vmcnt(13)\ns_waitcnt lgkmcnt(4)\nQmaaa 140,72,24\nbuffer_load_dwordx4 v[112:115], v79, s[48:51], s31 offen offset:1024\n"
"Qmaaa 140,76,52\nQmaaa 136,72,20\nQmaaa 136,76,48\nQmaaa 132,72,16\nQta 64,68,49664\nQmaaa 132,76,44\nbuffer_load_dwordx4 v[144:147], v79, s[48:51], s31 offen offset:2048\nQmaaa 128,72,12\n"
"Qmaaa 128,76,40\nQta 66,68,50688\nQmaaa 124,72,8\nQmaaa 124,76,36\nbuffer_load_dwordx4 v[176:179], v79, s[48:51], s31 offen offset:3072\nQmaaa 120,72,4\nQta 68,68,57856\nQmaaa 120,76,32\n"
"Qmaaa 116,72,0\nQmaaa 116,76,28\nQta 70,68,58880\ns_mov_b32 s31, 528384\ns_waitcnt lgkmcnt(4)\nQmaaa 144,56,0\nbuffer_load_dwordx4 v[208:211], v79, s[48:51], s31 offen offset:0\nQmaaa 144,60,28\n"
"Qmaaa 148,56,4\nQmaaa 148,60,32\nQmaaa 152,56,8\nQta 72,68,49920\nQmaaa 152,60,36\nbuffer_load_dwordx4 v[240:243], v79, s[48:51], s31 offen offset:1024\nQmaaa 156,56,12\nQmaaa 156,60,40\n"
"Qta 74,68,50944\nQmaaa 160,56,16\nQmaaa 160,60,44\nbuffer_load_dwordx4 v[84:87], v79, s[48:51], s31 offen offset:2048\nQmaaa 164,56,20\nQta 76,68,58112\nQmaaa 164,60,48\nQmaaa 168,56,24\n"
"Qmaaa 168,60,52\nQta 78,68,59136\ns_mov_b32 s31, 540672\ns_waitcnt lgkmcnt(4)\n.Lpg16:\ns_cbranch_execz .Lpg20\nQmvaa 24,64,24\nbuffer_load_dwordx4 v[88:91], v79, s[48:51], s31 offen offset:0\n"
"Qmvaa 24,68,52\nQmvaa 20,64,20\nQmvaa 20,68,48\nQmvaa 16,64,16\nQta 56,70,1024\nQmvaa 16,68,44\nbuffer_load_dwordx4 v[120:123], v79, s[48:51], s31 offen offset:1024\nQmvaa 12,64,12\n"
"Qmvaa 12,68,40\nQta 58,70,1536\nQmvaa 8,64,8\nQmvaa 8,68,36\nbuffer_load_dwordx4 v[152:155], v79, s[48:51], s31 offen offset:2048\nQmvaa 4,64,4\nQta 60,70,2048\nv_add_u32 v75, 1, v75\n"
"Qmvaa 4,68,32\ns_waitcnt vmcnt(18)\nds_write_b32 v73, v75\nQmvaa 0,64,0\nQmvaa 0,68,28\nQta 62,70,2560\ns_waitcnt lgkmcnt(5)\nQmvaa 28,72,0\n"
"buffer_load_dwordx4 v[184:187], v79, s[48:51], s31 offen offset:3072\nQmvaa 28,76,28\nQmvaa 32,72,4\nQmvaa 32,76,32\nQmvaa 36,72,8\nQta 64,70,1280\nQmvaa 36,76,36\nds_read_b32 v66, v74\n"
"s_mov_b32 s31, 544768\nQmvaa 40,72,12\nbuffer_load_dwordx4 v[216:219], v79, s[48:51], s31 offen offset:0\nQmvaa 40,76,40\nQta 66,70,1792\nQmvaa 44,72,16\nQmvaa 44,76,44\ns_waitcnt lgkmcnt(1)\n"
"v_cmp_gt_u32 vcc, v75, v66\nQmvaa 48,72,20\ns_cbranch_vccnz .Lso740\n.Lro740:\nQmvaa 48,76,48\nQta 68,70,2304\nQmvaa 52,72,24\nbuffer_load_dwordx4 v[248:251], v79, s[48:51], s31 offen offset:1024\n"
"Qmvaa 52,76,52\ns_waitcnt lgkmcnt(4)\nQmaaa 196,56,24\nQta 70,70,2816\nQmaaa 196,60,52\nQmaaa 192,56,20\nbuffer_load_dwordx4 v[116:119], v79, s[48:51], s31 offen offset:2048\nQmaaa 192,60,48\n"
"Qmaaa 188,56,16\nQta 72,70,9216\nQmaaa 188,60,44\nQmaaa 184,56,12\nQmaaa 184,60,40\nQta 74,70,10240\ns_add_u32 m0, s96, 114688\ns_mov_b32 s95, 782336\n"
"Qmaaa 180,56,8\nbuffer_load_dwordx4 v61, s[52:55], s95 offen lds\nQmaaa 180,60,36\nQmaaa 176,56,4\nQta 76,70,17408\nQmaaa 176,60,32\nQmaaa 172,56,0\nQmaaa 172,60,28\n"
"Qta 78,70,18432\ns_mov_b32 s95, 801792\ns_waitcnt lgkmcnt(4)\nQmvaa 92,64,0\nbuffer_load_dwordx4 v61, s[52:55], s95 offen offset:1024 lds\nQmvaa 92,68,28\nQmvaa 124,64,4\nQmvaa 124,68,32\n"
"Qmvaa 156,64,8\nQta 56,70,9472\ns_add_u32 m0, s96, 122880\ns_mov_b32 s95, 741376\nQmvaa 156,68,36\nbuffer_load_dwordx4 v62, s[52:55], s95 offen lds\nQmvaa 188,64,12\nQmvaa 188,68,40\n"
"Qta 58,70,10496\nQmvaa 220,64,16\nQmvaa 220,68,44\nQmvaa 252,64,20\nQta 60,70,17664\ns_mov_b32 s95, 781312\nQmvaa 252,68,48\nbuffer_load_dwordx4 v62, s[52:55], s95 offen offset:1024 lds\n"
"Qmvaa 100,64,24\nQmvaa 100,68,52\nQta 62,70,18688\ns_waitcnt lgkmcnt(4)\nQmaaa 224,72,24\nQmaaa 224,76,52\ns_add_u32 m0, s96, 0\ns_mov_b32 s95, 741376\n"
"Qmaaa 220,72,20\nbuffer_load_dwordx4 v63, s[52:55], s95 offen lds\nQmaaa 220,76,48\nQmaaa 216,72,16\nQta 64,70,9728\nQmaaa 216,76,44\nQmaaa 212,72,12\nQmaaa 212,76,40\n"
"Qta 66,70,10752\ns_mov_b32 s95, 760832\nQmaaa 208,72,8\nbuffer_load_dwordx4 v63, s[52:55], s95 offen offset:1024 lds\nQmaaa 208,76,36\nQmaaa 204,72,4\nQta 68,70,17920\nQmaaa 204,76,32\n"
"Qmaaa 200,72,0\nQmaaa 200,76,28\nQta 70,70,18944\ns_waitcnt lgkmcnt(4)\nQmaaa 228,56,0\nQmaaa 228,60,28\nQmaaa 232,56,4\nbuffer_store_dwordx4 v[168:171], v79, s[56:59], s88 offen offset:2048 sc1\n"
"Qmaaa 232,60,32\nQmaaa 236,56,8\nQta 72,70,9984\nQmaaa 236,60,36\nQmaaa 240,56,12\nQmaaa 240,60,40\nQta 74,70,11008\nQmaaa 244,56,16\n"
"Qmaaa 244,60,44\nQmaaa 248,56,20\nQta 76,70,18176\ns_add_u32 m0, s96, 8192\ns_mov_b32 s95, 782336\nQmaaa 248,60,48\nbuffer_load_dwordx4 v63, s[52:55], s95 offen lds\nQmaaa 252,56,24\n"
"Qmaaa 252,60,52\nQta 78,70,19200\ns_waitcnt lgkmcnt(4)\nQmvaa 172,64,24\nbuffer_store_dwordx4 v[200:203], v79, s[56:59], s88 offen offset:3072 sc1\nQmvaa 172,68,52\nQmvaa 140,64,20\nQmvaa 140,68,48\n"
"Qmvaa 108,64,16\nQta 56,69,0\nQmvaa 108,68,44\nQmvaa 228,64,12\nQmvaa 228,68,40\nQta 58,69,1024\ns_mov_b32 s95, 801792\nQmvaa 196,64,8\n"
"buffer_load_dwordx4 v63, s[52:55], s95 offen offset:1024 lds\nQmvaa 196,68,36\nQmvaa 164,64,4\nQta 60,69,8192\nQmvaa 164,68,32\nQmvaa 132,64,0\nQmvaa 132,68,28\nQta 62,69,9216\n"
"s_add_u32 m0, s96, 16384\ns_mov_b32 s95, 987136\ns_waitcnt lgkmcnt(4)\nQmvaa 204,72,0\nbuffer_load_dwordx4 v61, s[52:55], s95 offen lds\nQmvaa 204,76,28\nQmvaa 236,72,4\nQmvaa 236,76,32\n"
"Qmaaa 96,72,8\nQta 64,69,256\ns_mov_b32 s95, 1006592\nQmaaa 96,76,36\nbuffer_load_dwordx4 v61, s[52:55], s95 offen offset:1024 lds\nQmaaa 100,72,12\nQmaaa 100,76,40\nQta 66,69,1280\n"
"Qmaaa 104,72,16\ns_mov_b32 s31, 655360\nQmaaa 104,76,44\nbuffer_load_dwordx4 a[0:3], v79, s[48:51], s31 offen offset:0\nQmaaa 108,72,20\nQta 68,69,8448\nQmaaa 108,76,48\nQmaaa 112,72,24\n"
"Qmaaa 112,76,52\nQta 70,69,9472\ns_waitcnt vmcnt(13)\ns_waitcnt lgkmcnt(4)\nQmaav 140,56,84\nbuffer_load_dwordx4 a[4:7], v79, s[48:51], s31 offen offset:1024\nQmaav 140,60,116\nQmaav 136,56,240\n"
"Qmaav 136,60,248\nQmaav 132,56,208\nQta 72,69,512\nQmaav 132,60,216\nbuffer_load_dwordx4 a[8:11], v79, s[48:51], s31 offen offset:2048\nQmaav 128,56,176\nQmaav 128,60,184\nQta 74,69,1536\n"
"Qmaav 124,56,144\nQmaav 124,60,152\nbuffer_load_dwordx4 a[12:15], v79, s[48:51], s31 offen offset:3072\nQmaav 120,56,112\nQta 76,69,8704\nQmaav 120,60,120\nQmaav 116,56,80\nQmaav 116,60,88\n"
"Qta 78,69,9728\ns_mov_b32 s31, 659456\ns_waitcnt lgkmcnt(4)\nQmaav 144,64,80\nbuffer_load_dwordx4 a[16:19], v79, s[48:51], s31 offen offset:0\nQmaav 144,68,88\nQmaav 148,64,112\nQmaav 148,68,120\n"
"Qmaav 152,64,144\nQta 56,69,768\nQmaav 152,68,152\nbuffer_load_dwordx4 a[20:23], v79, s[48:51], s31 offen offset:1024\nQmaav 156,64,176\nQmaav 156,68,184\nQta 58,69,1792\nQmaav 160,64,208\n"
"Qmaav 160,68,216\nbuffer_load_dwordx4 a[24:27], v79, s[48:51], s31 offen offset:2048\nQmaav 164,64,240\nQta 60,69,8960\nQmaav 164,68,248\nQmaav 168,64,84\nQmaav 168,68,116\nQta 62,69,9984\n"
"s_mov_b32 s31, 671744\ns_waitcnt lgkmcnt(4)\nQmvav 24,72,84\nbuffer_load_dwordx4 a[28:31], v79, s[48:51], s31 offen offset:0\nQmvav 24,76,116\nQmvav 20,72,240\nQmvav 20,76,248\nQmvav 16,72,208\n"
"Qta 64,69,16384\nQmvav 16,76,216\nbuffer_load_dwordx4 a[32:35], v79, s[48:51], s31 offen offset:1024\nQmvav 12,72,176\nQmvav 12,76,184\nQta 66,69,16896\nQmvav 8,72,144\nQmvav 8,76,152\n"
"buffer_load_dwordx4 a[36:39], v79, s[48:51], s31 offen offset:2048\nQmvav 4,72,112\nQta 68,69,17408\nv_add_u32 v75, 1, v75\nQmvav 4,76,120\ns_waitcnt vmcnt(18)\nds_write_b32 v73, v75\nQmvav 0,72,80\n"
"Qmvav 0,76,88\nQta 70,69,17920\ns_waitcnt lgkmcnt(5)\nQmvav 28,56,80\nbuffer_load_dwordx4 a[40:43], v79, s[48:51], s31 offen offset:3072\nQmvav 28,60,88\nQmvav 32,56,112\nQmvav 32,60,120\n"
"Qmvav 36,56,144\nQta 72,69,16640\nQmvav 36,60,152\nds_read_b32 v66, v74\ns_mov_b32 s31, 675840\nQmvav 40,56,176\nbuffer_load_dwordx4 a[44:47], v79, s[48:51], s31 offen offset:0\nQmvav 40,60,184\n"
"Qta 74,69,17152\nQmvav 44,56,208\nQmvav 44,60,216\ns_waitcnt lgkmcnt(1)\nv_cmp_gt_u32 vcc, v75, v66\nQmvav 48,56,240\ns_cbranch_vccnz .Lso880\n.Lro880:\n"
"Qmvav 48,60,248\nQta 76,69,17664\nQmvav 52,56,84\nbuffer_load_dwordx4 a[48:51], v79, s[48:51], s31 offen offset:1024\nQmvav 52,60,116\ns_waitcnt lgkmcnt(4)\nQmaav 196,64,84\nQta 78,69,18176\n"
"Qmaav 196,68,116\nQmaav 192,64,240\nbuffer_load_dwordx4 a[52:55], v79, s[48:51], s31 offen offset:2048\nQmaav 192,68,248\nQmaav 188,64,208\nQta 56,69,24576\nQmaav 188,68,216\nQmaav 184,64,176\n"
"Qmaav 184,68,184\nQta 58,69,25600\ns_add_u32 m0, s96, 24576\ns_mov_b32 s95, 1028096\nQmaav 180,64,144\nbuffer_load_dwordx4 v61, s[52:55], s95 offen lds\nQmaav 180,68,152\nQmaav 176,64,112\n"
"Qta 60,69,32768\nQmaav 176,68,120\nQmaav 172,64,80\nQmaav 172,68,88\nQta 62,69,33792\ns_mov_b32 s95, 1047552\ns_waitcnt lgkmcnt(4)\nQmvav 92,72,80\n"
"buffer_load_dwordx4 v61, s[52:55], s95 offen offset:1024 lds\nQmvav 92,76,88\nQmvav 124,72,112\nQmvav 124,76,120\nQmvav 156,72,144\nQta 64,69,24832\ns_add_u32 m0, s96, 32768\ns_mov_b32 s95, 987136\n"
"Qmvav 156,76,152\nbuffer_load_dwordx4 v62, s[52:55], s95 offen lds\nQmvav 188,72,176\nQmvav 188,76,184\nQta 66,69,25856\nQmvav 220,72,208\nQmvav 220,76,216\nQmvav 252,72,240\n"
"Qta 68,69,33024\ns_mov_b32 s95, 1027072\nQmvav 252,76,248\nbuffer_load_dwordx4 v62, s[52:55], s95 offen offset:1024 lds\nQmvav 100,72,84\nQmvav 100,76,116\nQta 70,69,34048\ns_waitcnt lgkmcnt(4)\n"
"Qmaav 224,56,84\nQmaav 224,60,116\ns_add_u32 m0, s96, 40960\ns_mov_b32 s95, 987136\nQmaav 220,56,240\nbuffer_load_dwordx4 v63, s[52:55], s95 offen lds\nQmaav 220,60,248\nQmaav 216,56,208\n"
"Qta 72,69,25088\nQmaav 216,60,216\nQmaav 212,56,176\nQmaav 212,60,184\nQta 74,69,26112\ns_mov_b32 s95, 1006592\nQmaav 208,56,144\nbuffer_load_dwordx4 v63, s[52:55], s95 offen offset:1024 lds\n"
"Qmaav 208,60,152\nQmaav 204,56,112\nQta 76,69,33280\nQmaav 204,60,120\nQmaav 200,56,80\nQmaav 200,60,88\nQta 78,69,34304\ns_waitcnt lgkmcnt(4)\n"
"Qmaav 228,64,80\nQmaav 228,68,88\ns_add_u32 s88, s71, 151552\nQmaav 232,64,112\nbuffer_store_dwordx4 v[232:235], v79, s[56:59], s88 offen offset:0 sc1\nQmaav 232,68,120\nQmaav 236,64,144\nQta 56,69,25344\n"
"Qmaav 236,68,152\nQmaav 240,64,176\nQmaav 240,68,184\nQta 58,69,26368\nQmaav 244,64,208\nQmaav 244,68,216\nQmaav 248,64,240\nQta 60,69,33536\n"
"s_add_u32 m0, s96, 49152\ns_mov_b32 s95, 1028096\nQmaav 248,68,248\nbuffer_load_dwordx4 v63, s[52:55], s95 offen lds\nQmaav 252,64,84\nQmaav 252,68,116\nQta 62,69,34560\ns_waitcnt lgkmcnt(4)\n"
"Qmvav 172,72,84\nbuffer_store_dwordx4 v[212:215], v79, s[56:59], s88 offen offset:1024 sc1\nQmvav 172,76,116\nQmvav 140,72,240\nQmvav 140,76,248\nQmvav 108,72,208\nQta 64,69,40960\nQmvav 108,76,216\n"
"Qmvav 228,72,176\nQmvav 228,76,184\nQta 66,69,41984\ns_mov_b32 s95, 1047552\nQmvav 196,72,144\nbuffer_load_dwordx4 v63, s[52:55], s95 offen offset:1024 lds\nQmvav 196,76,152\nQmvav 164,72,112\n"
"Qta 68,69,49152\nQmvav 164,76,120\nQmvav 132,72,80\nQmvav 132,76,88\nQta 70,69,50176\ns_add_u32 m0, s96, 57344\ns_mov_b32 s95, 1069056\ns_waitcnt lgkmcnt(4)\n"
"Qmvav 204,56,80\nbuffer_load_dwordx4 v61, s[52:55], s95 offen lds\nQmvav 204,60,88\nQmvav 236,56,112\nQmvav 236,60,120\nQmaav 96,56,144\nQta 72,69,41216\ns_mov_b32 s95, 1088512\n"
"Qmaav 96,60,152\nbuffer_load_dwordx4 v61, s[52:55], s95 offen offset:1024 lds\nQmaav 100,56,176\nQmaav 100,60,184\nQta 74,69,42240\nQmaav 104,56,208\ns_mov_b32 s31, 786432\nQmaav 104,60,216\n"
"buffer_load_dwordx4 v[80:83], v79, s[48:51], s31 offen offset:0\nQmaav 108,56,240\nQta 76,69,49408\nQmaav 108,60,248\nQmaav 112,56,84\nv_accvgpr_read_b32 v96, a96\nv_accvgpr_read_b32 v97, a97\nQmaav 112,60,116\n"
"Qta 78,69,50432\nv_accvgpr_read_b32 v98, a98\ns_waitcnt vmcnt(13)\ns_waitcnt lgkmcnt(4)\nQmaaa 140,64,24\nbuffer_load_dwordx4 v[112:115], v79, s[48:51], s31 offen offset:1024\nQmaaa 140,68,52\nv_accvgpr_read_b32 v99, a99\n"
"v_accvgpr_read_b32 v128, a100\nQmaaa 136,64,20\nv_accvgpr_read_b32 v129, a101\nv_accvgpr_read_b32 v130, a102\nQmaaa 136,68,48\nv_accvgpr_read_b32 v131, a103\nv_accvgpr_read_b32 v160, a104\nQmaaa 132,64,16\n"
"Qta 56,69,41472\nv_accvgpr_read_b32 v161, a105\nQmaaa 132,68,44\nbuffer_load_dwordx4 v[144:147], v79, s[48:51], s31 offen offset:2048\nQmaaa 128,64,12\nv_accvgpr_read_b32 v162, a106\nv_accvgpr_read_b32 v163, a107\nQmaaa 128,68,40\n"
"Qta 58,69,42496\nv_accvgpr_read_b32 v192, a108\nQmaaa 124,64,8\nv_accvgpr_read_b32 v193, a109\nv_accvgpr_read_b32 v194, a110\nQmaaa 124,68,36\nbuffer_load_dwordx4 v[176:179], v79, s[48:51], s31 offen offset:3072\nQmaaa 120,64,4\n"
"Qta 60,69,49664\nv_accvgpr_read_b32 v195, a111\nQmaaa 120,68,32\nv_accvgpr_read_b32 v224, a112\nv_accvgpr_read_b32 v225, a113\nQmaaa 116,64,0\nv_accvgpr_read_b32 v226, a114\nv_accvgpr_read_b32 v227, a115\n"
"Qmaaa 116,68,28\nQta 62,69,50688\ns_mov_b32 s31, 790528\ns_waitcnt lgkmcnt(4)\nQmaaa 144,72,0\nbuffer_load_dwordx4 v[208:211], v79, s[48:51], s31 offen offset:0\nQmaaa 144,76,28\nQmaaa 148,72,4\n"
"Qmaaa 148,76,32\nQmaaa 152,72,8\nQta 64,69,41728\nQmaaa 152,76,36\nbuffer_load_dwordx4 v[240:243], v79, s[48:51], s31 offen offset:1024\nQmaaa 156,72,12\nQmaaa 156,76,40\nQta 66,69,42752\n"
"Qmaaa 160,72,16\nQmaaa 160,76,44\nbuffer_load_dwordx4 v[84:87], v79, s[48:51], s31 offen offset:2048\nQmaaa 164,72,20\nQta 68,69,49920\nQmaaa 164,76,48\nQmaaa 168,72,24\nQmaaa 168,76,52\n"
"Qta 70,69,50944\ns_mov_b32 s31, 802816\ns_waitcnt lgkmcnt(4)\nQmvaa 24,56,24\nbuffer_load_dwordx4 v[88:91], v79, s[48:51], s31 offen offset:0\nQmvaa 24,60,52\nQmvaa 20,56,20\nQmvaa 20,60,48\n"
"Qmvaa 16,56,16\nQta 72,69,57344\nQmvaa 16,60,44\nbuffer_load_dwordx4 v[120:123], v79, s[48:51], s31 offen offset:1024\nQmvaa 12,56,12\nQmvaa 12,60,40\nQta 74,69,57856\nQmvaa 8,56,8\n"
"Qmvaa 8,60,36\nbuffer_load_dwordx4 v[152:155], v79, s[48:51], s31 offen offset:2048\nQmvaa 4,56,4\nQta 76,69,58368\nv_add_u32 v75, 1, v75\nQmvaa 4,60,32\ns_waitcnt vmcnt(18)\nds_write_b32 v73, v75\n"
"Qmvaa 0,56,0\n.Lpg17:\ns_cbranch_execz .Lpg21\nQmvaa 0,60,28\nQta 78,69,58880\ns_waitcnt lgkmcnt(5)\nQmvaa 28,64,0\nbuffer_load_dwordx4 v[184:187], v79, s[48:51], s31 offen offset:3072\n"
"Qmvaa 28,68,28\nQmvaa 32,64,4\nQmvaa 32,68,32\nQmvaa 36,64,8\nQta 56,69,57600\nQmvaa 36,68,36\nds_read_b32 v66, v74\ns_mov_b32 s31, 806912\n"
"Qmvaa 40,64,12\nbuffer_load_dwordx4 v[216:219], v79, s[48:51], s31 offen offset:0\nQmvaa 40,68,40\nQta 58,69,58112\nQmvaa 44,64,16\nQmvaa 44,68,44\ns_waitcnt lgkmcnt(1)\nv_cmp_gt_u32 vcc, v75, v66\n"
"Qmvaa 48,64,20\ns_cbranch_vccnz .Lso1020\n.Lro1020:\nQmvaa 48,68,48\nQta 60,69,58624\nQmvaa 52,64,24\nbuffer_load_dwordx4 v[248:251], v79, s[48:51], s31 offen offset:1024\nQmvaa 52,68,52\n"
"s_waitcnt lgkmcnt(4)\nQmaaa 196,72,24\nQta 62,69,59136\nQmaaa 196,76,52\nQmaaa 192,72,20\nbuffer_load_dwordx4 v[116:119], v79, s[48:51], s31 offen offset:2048\nQmaaa 192,76,48\nQmaaa 188,72,16\n"
"Qta 64,68,0\nQmaaa 188,76,44\nQmaaa 184,72,12\nQmaaa 184,76,40\nQta 66,68,1024\ns_add_u32 m0, s96, 132096\ns_mov_b32 s95, 1110016\nQmaaa 180,72,8\n"
"buffer_load_dwordx4 v61, s[52:55], s95 offen lds\nQmaaa 180,76,36\nQmaaa 176,72,4\nQta 68,68,8192\nQmaaa 176,76,32\nQmaaa 172,72,0\nQmaaa 172,76,28\nQta 70,68,9216\n"
"s_mov_b32 s95, 1129472\ns_waitcnt lgkmcnt(4)\nQmvaa 92,56,0\nbuffer_load_dwordx4 v61, s[52:55], s95 offen offset:1024 lds\nQmvaa 92,60,28\nQmvaa 124,56,4\nQmvaa 124,60,32\nQmvaa 156,56,8\n"
"Qta 72,68,256\ns_add_u32 m0, s96, 140288\ns_mov_b32 s95, 1069056\nQmvaa 156,60,36\nbuffer_load_dwordx4 v62, s[52:55], s95 offen lds\nQmvaa 188,56,12\nQmvaa 188,60,40\nQta 74,68,1280\n"
"Qmvaa 220,56,16\nQmvaa 220,60,44\nQmvaa 252,56,20\nQta 76,68,8448\ns_mov_b32 s95, 1108992\nQmvaa 252,60,48\nbuffer_load_dwordx4 v62, s[52:55], s95 offen offset:1024 lds\nQmvaa 100,56,24\n"
"Qmvaa 100,60,52\nQta 78,68,9472\ns_waitcnt lgkmcnt(4)\nQmaaa 224,64,24\nQmaaa 224,68,52\ns_add_u32 m0, s96, 148480\ns_mov_b32 s95, 1069056\nQmaaa 220,64,20\n"
"buffer_load_dwordx4 v63, s[52:55], s95 offen lds\nQmaaa 220,68,48\nQmaaa 216,64,16\nQta 56,68,512\nQmaaa 216,68,44\nQmaaa 212,64,12\nQmaaa 212,68,40\nQta 58,68,1536\n"
"s_mov_b32 s95, 1088512\nQmaaa 208,64,8\nbuffer_load_dwordx4 v63, s[52:55], s95 offen offset:1024 lds\nQmaaa 208,68,36\nQmaaa 204,64,4\nQta 60,68,8704\nQmaaa 204,68,32\nQmaaa 200,64,0\n"
"Qmaaa 200,68,28\nQta 62,68,9728\ns_waitcnt lgkmcnt(4)\nQmaaa 228,72,0\nQmaaa 228,76,28\nQmaaa 232,72,4\nbuffer_store_dwordx4 v[244:247], v79, s[56:59], s88 offen offset:2048 sc1\nQmaaa 232,76,32\n"
"Qmaaa 236,72,8\nQta 64,68,768\nQmaaa 236,76,36\nQmaaa 240,72,12\nQmaaa 240,76,40\nQta 66,68,1792\nQmaaa 244,72,16\nQmaaa 244,76,44\n"
"Qmaaa 248,72,20\nQta 68,68,8960\ns_add_u32 m0, s96, 65536\ns_mov_b32 s95, 1110016\nQmaaa 248,76,48\nbuffer_load_dwordx4 v63, s[52:55], s95 offen lds\nQmaaa 252,72,24\nQmaaa 252,76,52\n"
"Qta 70,68,9984\ns_waitcnt lgkmcnt(4)\nQmvaa 172,56,24\nQmvaa 172,60,52\ns_mov_b32 s95, 1129472\nQmvaa 140,56,20\nbuffer_load_dwordx4 v63, s[52:55], s95 offen offset:1024 lds\nQmvaa 140,60,48\n"
"Qmvaa 108,56,16\nQta 72,68,16384\nQmvaa 108,60,44\nQmvaa 228,56,12\nQmvaa 228,60,40\nQta 74,68,17408\ns_add_u32 m0, s96, 73728\ns_add_u32 s95, s67, 4096\n"
"Qmvaa 196,56,8\nbuffer_load_dwordx4 v61, s[52:55], s95 offen lds\nQmvaa 196,60,36\nQmvaa 164,56,4\nQta 76,68,24576\nQmvaa 164,60,32\nQmvaa 132,56,0\nQmvaa 132,60,28\n"
"Qta 78,68,25600\ns_add_u32 s95, s67, 23552\ns_waitcnt lgkmcnt(4)\nQmvaa 204,64,0\nbuffer_load_dwordx4 v61, s[52:55], s95 offen offset:1024 lds\nQmvaa 204,68,28\nQmvaa 236,64,4\nQmvaa 236,68,32\n"
"Qmvaa 96,64,8\nQta 56,68,16640\ns_mov_b32 s31, 917504\nQmvaa 96,68,36\nbuffer_load_dwordx4 a[0:3], v79, s[48:51], s31 offen offset:0\nQmvaa 128,64,12\nQmvaa 128,68,40\nQta 58,68,17664\n"
"Qmvaa 160,64,16\nQmvaa 160,68,44\nbuffer_load_dwordx4 a[4:7], v79, s[48:51], s31 offen offset:1024\nQmvaa 192,64,20\nQta 60,68,24832\nQmvaa 192,68,48\nQmvaa 224,64,24\nQmvaa 224,68,52\n"
"Qta 62,68,25856\ns_waitcnt vmcnt(13)\ns_waitcnt lgkmcnt(4)\nQmaav 140,72,84\nbuffer_load_dwordx4 a[8:11], v79, s[48:51], s31 offen offset:2048\nQmaav 140,76,116\nQmaav 136,72,240\nQmaav 136,76,248\n"
"Qmaav 132,72,208\nQta 64,68,16896\nQmaav 132,76,216\nbuffer_load_dwordx4 a[12:15], v79, s[48:51], s31 offen offset:3072\nQmaav 128,72,176\nQmaav 128,76,184\nQta 66,68,17920\nQmaav 124,72,144\n"
"s_mov_b32 s31, 921600\nQmaav 124,76,152\nbuffer_load_dwordx4 a[16:19], v79, s[48:51], s31 offen offset:0\nQmaav 120,72,112\nQta 68,68,25088\nQmaav 120,76,120\nQmaav 116,72,80\nQmaav 116,76,88\n"
"Qta 70,68,26112\ns_waitcnt lgkmcnt(4)\nQmaav 144,56,80\nbuffer_load_dwordx4 a[20:23], v79, s[48:51], s31 offen offset:1024\nQmaav 144,60,88\nQmaav 148,56,112\nbuffer_load_dword v78, v76, s[24:27], 0 offen sc1\nQmaav 148,60,120\n"
"Qmaav 152,56,144\nQta 72,68,17152\nQmaav 152,60,152\nbuffer_load_dwordx4 a[24:27], v79, s[48:51], s31 offen offset:2048\nQmaav 156,56,176\nQmaav 156,60,184\nQta 74,68,18176\nQmaav 160,56,208\n"
"s_mov_b32 s31, 933888\nQmaav 160,60,216\nbuffer_load_dwordx4 a[28:31], v79, s[48:51], s31 offen offset:0\nQmaav 164,56,240\nQta 76,68,25344\nQmaav 164,60,248\nQmaav 168,56,84\nQmaav 168,60,116\n"
"Qta 78,68,26368\ns_waitcnt lgkmcnt(4)\nQmvav 24,64,84\nbuffer_load_dwordx4 a[32:35], v79, s[48:51], s31 offen offset:1024\nQmvav 24,68,116\nQmvav 20,64,240\nQmvav 20,68,248\nQmvav 16,64,208\n"
"Qta 56,68,32768\nQmvav 16,68,216\nbuffer_load_dwordx4 a[36:39], v79, s[48:51], s31 offen offset:2048\nQmvav 12,64,176\nQmvav 12,68,184\nQta 58,68,33280\nQmvav 8,64,144\nQmvav 8,68,152\n"
"buffer_load_dwordx4 a[40:43], v79, s[48:51], s31 offen offset:3072\nQmvav 4,64,112\nQta 60,68,33792\nv_add_u32 v75, 1, v75\nQmvav 4,68,120\ns_waitcnt vmcnt(19)\nds_write_b32 v73, v75\nQmvav 0,64,80\n"
"Qmvav 0,68,88\nQta 62,68,34304\ns_mov_b32 s31, 937984\ns_waitcnt lgkmcnt(5)\nQmvav 28,72,80\nbuffer_load_dwordx4 a[44:47], v79, s[48:51], s31 offen offset:0\nQmvav 28,76,88\nQmvav 32,72,112\n"
"Qmvav 32,76,120\nQmvav 36,72,144\nQta 64,68,33024\nQmvav 36,76,152\nds_read_b32 v66, v74\nQmvav 40,72,176\nbuffer_load_dwordx4 a[48:51], v79, s[48:51], s31 offen offset:1024\nQmvav 40,76,184\n"
"Qta 66,68,33536\nQmvav 44,72,208\nQmvav 44,76,216\ns_waitcnt lgkmcnt(1)\nv_cmp_gt_u32 vcc, v75, v66\nQmvav 48,72,240\ns_cbranch_vccnz .Lso1160\n.Lro1160:\n"
"Qmvav 48,76,248\nQta 68,68,34048\nQmvav 52,72,84\nbuffer_load_dwordx4 a[52:55], v79, s[48:51], s31 offen offset:2048\nQmvav 52,76,116\ns_waitcnt lgkmcnt(4)\nQmaav 196,56,84\nQta 70,68,34560\n"
"Qmaav 196,60,116\nQmaav 192,56,240\ns_add_u32 m0, s96, 81920\ns_add_u32 s95, s67, 45056\nQmaav 192,60,248\nbuffer_load_dwordx4 v61, s[52:55], s95 offen lds\nQmaav 188,56,208\nQta 72,68,40960\n"
"Qmaav 188,60,216\nQmaav 184,56,176\nQmaav 184,60,184\nQta 74,68,41984\ns_add_u32 s95, s67, 64512\nQmaav 180,56,144\nbuffer_load_dwordx4 v61, s[52:55], s95 offen offset:1024 lds\nQmaav 180,60,152\n"
"Qmaav 176,56,112\nQta 76,68,49152\nQmaav 176,60,120\nQmaav 172,56,80\nQmaav 172,60,88\nQta 78,68,50176\ns_add_u32 m0, s96, 90112\ns_add_u32 s95, s67, 4096\n"
"s_waitcnt lgkmcnt(4)\nQmvav 92,64,80\nbuffer_load_dwordx4 v62, s[52:55], s95 offen lds\nQmvav 92,68,88\nQmvav 124,64,112\nQmvav 124,68,120\nQmvav 156,64,144\nQta 56,68,41216\n"
"s_add_u32 s95, s67, 44032\nQmvav 156,68,152\nbuffer_load_dwordx4 v62, s[52:55], s95 offen offset:1024 lds\nQmvav 188,64,176\ns_waitcnt vmcnt(12)\nv_sub_u32 v66, v78, v65\nv_cmp_gt_u32 vcc, 2, v66\ns_and_b64 vcc, vcc, s[28:29]\n"
"s_cbranch_scc1 .Lf2_w\n.Lf2_r:\nQmvav 188,68,184\nQta 58,68,42240\nQmvav 220,64,208\ns_add_u32 s31, s74, 0\nQmvav 220,68,216\nbuffer_load_dwordx4 a[80:83], v79, s[48:51], s31 offen offset:0 sc1\n"
"Qmvav 252,64,240\nQta 60,68,49408\nQmvav 252,68,248\nQmvav 100,64,84\nQmvav 100,68,116\nQta 62,68,50432\ns_waitcnt lgkmcnt(4)\nQmaav 224,72,84\n"
"buffer_load_dwordx4 a[84:87], v79, s[48:51], s31 offen offset:1024 sc1\nQmaav 224,76,116\nQmaav 220,72,240\nQmaav 220,76,248\nQmaav 216,72,208\nQta 64,68,41472\nQmaav 216,76,216\nbuffer_load_dwordx4 a[88:91], v79, s[48:51], s31 offen offset:2048 sc1\n"
"Qmaav 212,72,176\nQmaav 212,76,184\nQta 66,68,42496\nQmaav 208,72,144\nQmaav 208,76,152\nbuffer_load_dwordx4 a[92:95], v79, s[48:51], s31 offen offset:3072 sc1\nQmaav 204,72,112\nQta 68,68,49664\n"
"Qmaav 204,76,120\nQmaav 200,72,80\nQmaav 200,76,88\nQta 70,68,50688\ns_add_u32 s31, s74, 4096\ns_waitcnt lgkmcnt(4)\nQmaav 228,56,80\nbuffer_load_dwordx4 a[96:99], v79, s[48:51], s31 offen offset:0 sc1\n"
"Qmaav 228,60,88\nQmaav 232,56,112\nQmaav 232,60,120\nQmaav 236,56,144\nQta 72,68,41728\nQmaav 236,60,152\nbuffer_load_dwordx4 a[100:103], v79, s[48:51], s31 offen offset:1024 sc1\nQmaav 240,56,176\n"
"Qmaav 240,60,184\nQta 74,68,42752\nQmaav 244,56,208\nQmaav 244,60,216\nbuffer_load_dwordx4 a[104:107], v79, s[48:51], s31 offen offset:2048 sc1\nQmaav 248,56,240\nQta 76,68,49920\nQmaav 248,60,248\n"
"Qmaav 252,56,84\nQmaav 252,60,116\nQta 78,68,50944\ns_add_u32 s31, s74, 16384\ns_waitcnt lgkmcnt(4)\nQmvav 172,64,84\nbuffer_load_dwordx4 a[108:111], v79, s[48:51], s31 offen offset:0 sc1\nQmvav 172,68,116\n"
"Qmvav 140,64,240\nQmvav 140,68,248\nQmvav 108,64,208\nQta 56,68,57344\nQmvav 108,68,216\nbuffer_load_dwordx4 a[112:115], v79, s[48:51], s31 offen offset:1024 sc1\nQmvav 228,64,176\nQmvav 228,68,184\n"
"Qta 58,68,58368\nQmvav 196,64,144\nQmvav 196,68,152\nbuffer_load_dwordx4 v[168:171], v79, s[48:51], s31 offen offset:2048 sc1\nQmvav 164,64,112\nQta 60,70,1024\nQmvav 164,68,120\nQmvav 132,64,80\n"
"Qmvav 132,68,88\nQta 62,70,2048\ns_waitcnt lgkmcnt(4)\nQmvav 204,72,80\nbuffer_load_dwordx4 v[200:203], v79, s[48:51], s31 offen offset:3072 sc1\nQmvav 204,76,88\nQmvav 236,72,112\nQmvav 236,76,120\n"
"Qmvav 96,72,144\nQta 64,68,57600\ns_add_u32 s31, s74, 20480\nQmvav 96,76,152\nbuffer_load_dwordx4 v[232:235], v79, s[48:51], s31 offen offset:0 sc1\nQmvav 128,72,176\nQmvav 128,76,184\nQta 66,68,58624\n"
"Qmvav 160,72,208\nQmvav 160,76,216\nbuffer_load_dwordx4 v[212:215], v79, s[48:51], s31 offen offset:1024 sc1\nQmvav 192,72,240\nQta 68,70,1280\nQmvav 192,76,248\nQmvav 224,72,84\nQmvav 224,76,116\n"
"Qta 70,70,2304\ns_waitcnt vmcnt(17)\ns_waitcnt lgkmcnt(4)\nQmaaa 140,56,24\nbuffer_load_dwordx4 v[244:247], v79, s[48:51], s31 offen offset:2048 sc1\nQmaaa 140,60,52\nQmaaa 136,56,20\nQmaaa 136,60,48\n"
"Qmaaa 132,56,16\nQta 72,68,57856\ns_add_u32 m0, s96, 98304\ns_add_u32 s95, s67, 4096\nQmaaa 132,60,44\nbuffer_load_dwordx4 v63, s[52:55], s95 offen lds\nQmaaa 128,56,12\nQmaaa 128,60,40\n"
"Qta 74,68,58880\nQmaaa 124,56,8\nQmaaa 124,60,36\nQmaaa 120,56,4\nQta 76,70,1536\ns_add_u32 s95, s67, 23552\nQmaaa 120,60,32\nbuffer_load_dwordx4 v63, s[52:55], s95 offen offset:1024 lds\n"
"Qmaaa 116,56,0\nQmaaa 116,60,28\nQta 78,70,2560\ns_waitcnt lgkmcnt(4)\nQmaaa 144,64,0\nQmaaa 144,68,28\ns_add_u32 m0, s96, 106496\ns_add_u32 s95, s67, 45056\n"
"Qmaaa 148,64,4\nbuffer_load_dwordx4 v63, s[52:55], s95 offen lds\nQmaaa 148,68,32\nQmaaa 152,64,8\nQta 56,68,58112\nQmaaa 152,68,36\nQmaaa 156,64,12\nQmaaa 156,68,40\n"
"Qta 58,68,59136\ns_add_u32 s95, s67, 64512\nQmaaa 160,64,16\nbuffer_load_dwordx4 v63, s[52:55], s95 offen offset:1024 lds\nQmaaa 160,68,44\nQmaaa 164,64,20\nQta 60,70,1792\nQmaaa 164,68,48\n"
"Qmaaa 168,64,24\nQmaaa 168,68,52\nQta 62,70,2816\ns_add_u32 m0, s96, 114688\ns_add_u32 s95, s72, 4096\ns_waitcnt lgkmcnt(4)\nQmvaa 24,72,24\nbuffer_load_dwordx4 v61, s[52:55], s95 offen lds\n"
"Qmvaa 24,76,52\nQmvaa 20,72,20\nQmvaa 20,76,48\nQmvaa 16,72,16\nQta 64,70,9216\ns_add_u32 s95, s72, 23552\nQmvaa 16,76,44\nbuffer_load_dwordx4 v61, s[52:55], s95 offen offset:1024 lds\n"
"Qmvaa 12,72,12\nQmvaa 12,76,40\nQta 66,70,9728\nQmvaa 8,72,8\nQmvaa 8,76,36\nQmvaa 4,72,4\nQta 68,70,10240\nv_add_u32 v75, 1, v75\n"
"Qmvaa 4,76,32\ns_waitcnt vmcnt(20)\nds_write_b32 v73, v75\ns_add_u32 m0, s96, 122880\ns_add_u32 s95, s72, 45056\nQmvaa 0,72,0\nbuffer_load_dwordx4 v61, s[52:55], s95 offen lds\nQmvaa 0,76,28\n"
"Qta 70,70,10752\ns_waitcnt lgkmcnt(5)\nQmvaa 28,56,0\nQmvaa 28,60,28\nQmvaa 32,56,4\ns_add_u32 s95, s72, 64512\nQmvaa 32,60,32\nbuffer_load_dwordx4 v61, s[52:55], s95 offen offset:1024 lds\n"
"Qmvaa 36,56,8\nQta 72,70,9472\nQmvaa 36,60,36\nds_read_b32 v66, v74\nQmvaa 40,56,12\nQmvaa 40,60,40\nQta 74,70,9984\ns_add_u32 m0, s96, 0\n"
"s_add_u32 s95, s72, 4096\nQmvaa 44,56,16\nbuffer_load_dwordx4 v62, s[52:55], s95 offen lds\nv_add_u32 v67, 3, v65\nQmvaa 44,60,44\ns_waitcnt lgkmcnt(1)\nv_cmp_gt_u32 vcc, v75, v66\nQmvaa 48,56,20\n"
"s_cbranch_vccnz .Lso1300\n.Lro1300:\nQmvaa 48,60,48\nQta 76,70,10496\nQmvaa 52,56,24\nbuffer_store_dword v67, v77, s[24:27], 0 offen sc1\ns_add_u32 s95, s72, 44032\nQmvaa 52,60,52\n"
"buffer_load_dwordx4 v62, s[52:55], s95 offen offset:1024 lds\ns_waitcnt lgkmcnt(4)\nQmaaa 196,64,24\nQta 78,70,11008\nQmaaa 196,68,52\n.Lpg18:\ns_cbranch_execz .Lpg22\nQmaaa 192,64,20\n"
"Qmaaa 192,68,48\nQmaaa 188,64,16\nQta 56,70,17408\ns_add_u32 m0, s96, 8192\ns_add_u32 s95, s72, 4096\nQmaaa 188,68,44\nbuffer_load_dwordx4 v63, s[52:55], s95 offen lds\nQmaaa 184,64,12\n"
"Qmaaa 184,68,40\nQta 58,70,18432\nQmaaa 180,64,8\nQmaaa 180,68,36\nQmaaa 176,64,4\nQta 60,69,0\ns_add_u32 s95, s72, 23552\nQmaaa 176,68,32\n"
"buffer_load_dwordx4 v63, s[52:55], s95 offen offset:1024 lds\nQmaaa 172,64,0\nQmaaa 172,68,28\nQta 62,69,1024\ns_waitcnt lgkmcnt(4)\nQmvaa 92,72,0\ns_add_u32 s31, s75, 0\nQmvaa 92,76,28\n"
"buffer_load_dwordx4 v[80:83], v79, s[48:51], s31 offen offset:0 sc1\nQmvaa 124,72,4\nQmvaa 124,76,32\nQmvaa 156,72,8\nQta 64,70,17664\nQmvaa 156,76,36\nbuffer_load_dwordx4 v[112:115], v79, s[48:51], s31 offen offset:1024 sc1\nQmvaa 188,72,12\n"
"Qmvaa 188,76,40\nQta 66,70,18688\nQmvaa 220,72,16\nQmvaa 220,76,44\nbuffer_load_dwordx4 v[144:147], v79, s[48:51], s31 offen offset:2048 sc1\nQmvaa 252,72,20\nQta 68,69,256\nQmvaa 252,76,48\n"
"Qmvaa 100,72,24\nQmvaa 100,76,52\nQta 70,69,1280\ns_waitcnt lgkmcnt(4)\nQmaaa 224,56,24\nbuffer_load_dwordx4 v[176:179], v79, s[48:51], s31 offen offset:3072 sc1\nQmaaa 224,60,52\nQmaaa 220,56,20\n"
"Qmaaa 220,60,48\nQmaaa 216,56,16\nQta 72,70,17920\ns_add_u32 s31, s75, 4096\nQmaaa 216,60,44\nbuffer_load_dwordx4 v[208:211], v79, s[48:51], s31 offen offset:0 sc1\nQmaaa 212,56,12\nQmaaa 212,60,40\n"
"Qta 74,70,18944\nQmaaa 208,56,8\nQmaaa 208,60,36\nbuffer_load_dwordx4 v[240:243], v79, s[48:51], s31 offen offset:1024 sc1\nQmaaa 204,56,4\nQta 76,69,512\nQmaaa 204,60,32\nQmaaa 200,56,0\n"
"Qmaaa 200,60,28\nQta 78,69,1536\ns_waitcnt lgkmcnt(4)\nQmaaa 228,64,0\nbuffer_load_dwordx4 v[84:87], v79, s[48:51], s31 offen offset:2048 sc1\nQmaaa 228,68,28\nQmaaa 232,64,4\nQmaaa 232,68,32\n"
"Qmaaa 236,64,8\nQta 56,70,18176\ns_add_u32 s31, s75, 16384\nQmaaa 236,68,36\nbuffer_load_dwordx4 v[88:91], v79, s[48:51], s31 offen offset:0 sc1\nQmaaa 240,64,12\nQmaaa 240,68,40\nQta 58,70,19200\n"
"Qmaaa 244,64,16\nQmaaa 244,68,44\nbuffer_load_dwordx4 v[120:123], v79, s[48:51], s31 offen offset:1024 sc1\nQmaaa 248,64,20\nQta 60,69,768\nQmaaa 248,68,48\nQmaaa 252,64,24\nQmaaa 252,68,52\n"
"Qta 62,69,1792\ns_waitcnt lgkmcnt(4)\nQmvaa 172,72,24\nbuffer_load_dwordx4 v[152:155], v79, s[48:51], s31 offen offset:2048 sc1\nQmvaa 172,76,52\nQmvaa 140,72,20\nQmvaa 140,76,48\nQmvaa 108,72,16\n"
"Qta 64,69,8192\nQmvaa 108,76,44\nbuffer_load_dwordx4 v[184:187], v79, s[48:51], s31 offen offset:3072 sc1\nQmvaa 228,72,12\nQmvaa 228,76,40\nQta 66,69,9216\nQmvaa 196,72,8\ns_add_u32 s31, s75, 20480\n"
"Qmvaa 196,76,36\nbuffer_load_dwordx4 v[216:219], v79, s[48:51], s31 offen offset:0 sc1\nQmvaa 164,72,4\nQta 68,69,16384\nQmvaa 164,76,32\nQmvaa 132,72,0\nQmvaa 132,76,28\nQta 70,69,17408\n"
"s_waitcnt lgkmcnt(4)\nQmvaa 204,56,0\nbuffer_load_dwordx4 v[248:251], v79, s[48:51], s31 offen offset:1024 sc1\nQmvaa 204,60,28\nQmvaa 236,56,4\nQmvaa 236,60,32\nQmvaa 96,56,8\nQta 72,69,8448\n"
"Qmvaa 96,60,36\nbuffer_load_dwordx4 v[116:119], v79, s[48:51], s31 offen offset:2048 sc1\nQmvaa 128,56,12\nQmvaa 128,60,40\nQta 74,69,9472\nQmvaa 160,56,16\nQmvaa 160,60,44\nQmvaa 192,56,20\n"
"Qta 76,69,16640\ns_add_u32 m0, s96, 16384\ns_add_u32 s95, s72, 45056\nQmvaa 192,60,48\nbuffer_load_dwordx4 v63, s[52:55], s95 offen lds\nQmvaa 224,56,24\nQmvaa 224,60,52\nQta 78,69,17664\n"
"s_waitcnt vmcnt(28)\ns_waitcnt lgkmcnt(4)\nQmaaa 140,64,104\nQmaav 140,68,244\ns_add_u32 s95, s72, 64512\nQmaaa 136,64,100\nbuffer_load_dwordx4 v63, s[52:55], s95 offen offset:1024 lds\nQmaav 136,68,212\n"
"Qmaaa 132,64,96\nQta 56,69,8704\nQmaav 132,68,232\nQmaaa 128,64,92\nQmaav 128,68,200\nQta 58,69,9728\ns_add_u32 m0, s96, 24576\ns_add_u32 s95, s73, 4096\n"
"Qmaaa 124,64,88\nbuffer_load_dwordx4 v61, s[52:55], s95 offen lds\nQmaav 124,68,168\nQmaaa 120,64,84\nQta 60,69,16896\nQmaaa 120,68,112\nQmaaa 116,64,80\nQmaaa 116,68,108\n"
"Qta 62,69,17920\ns_add_u32 s95, s73, 23552\ns_waitcnt lgkmcnt(4)\nQmaaa 144,72,80\nbuffer_load_dwordx4 v61, s[52:55], s95 offen offset:1024 lds\nQmaaa 144,76,108\nQmaaa 148,72,84\nQmaaa 148,76,112\n"
"Qmaaa 152,72,88\nQta 64,69,8960\ns_add_u32 m0, s96, 32768\ns_add_u32 s95, s73, 45056\nQmaav 152,76,168\nbuffer_load_dwordx4 v61, s[52:55], s95 offen lds\nQmaaa 156,72,92\nQmaav 156,76,200\n"
"Qta 66,69,9984\nQmaaa 160,72,96\nQmaav 160,76,232\nQmaaa 164,72,100\nQta 68,69,17152\ns_add_u32 s95, s73, 64512\nQmaav 164,76,212\nbuffer_load_dwordx4 v61, s[52:55], s95 offen offset:1024 lds\n"
"Qmaaa 168,72,104\nQmaav 168,76,244\nQta 70,69,18176\ns_waitcnt lgkmcnt(4)\nQmvaa 24,56,104\nQmvav 24,60,244\ns_add_u32 m0, s96, 40960\ns_add_u32 s95, s73, 4096\n"
"Qmvaa 20,56,100\nbuffer_load_dwordx4 v62, s[52:55], s95 offen lds\nQmvav 20,60,212\nQmvaa 16,56,96\nQta 72,69,24576\nQmvav 16,60,232\nQmvaa 12,56,92\nQmvav 12,60,200\n"
"Qta 74,69,25088\ns_add_u32 s95, s73, 44032\nQmvaa 8,56,88\nbuffer_load_dwordx4 v62, s[52:55], s95 offen offset:1024 lds\nQmvav 8,60,168\nQmvaa 4,56,84\nQta 76,69,25600\nv_add_u32 v75, 1, v75\n"
"Qmvaa 4,60,112\ns_waitcnt vmcnt(33)\nds_write_b32 v73, v75\nQmvaa 0,56,80\nQmvaa 0,60,108\nQta 78,69,26112\ns_add_u32 m0, s96, 49152\ns_add_u32 s95, s73, 4096\n"
"s_waitcnt lgkmcnt(5)\nQmvaa 28,64,80\nbuffer_load_dwordx4 v63, s[52:55], s95 offen lds\nQmvaa 28,68,108\nQmvaa 32,64,84\nQmvaa 32,68,112\nQmvaa 36,64,88\nQta 56,69,24832\n"
"Qmvav 36,68,168\nds_read_b32 v66, v74\ns_add_u32 s95, s73, 23552\nQmvaa 40,64,92\nbuffer_load_dwordx4 v63, s[52:55], s95 offen offset:1024 lds\nQmvav 40,68,200\nQta 58,69,25344\nQmvaa 44,64,96\n"
"Qmvav 44,68,232\ns_waitcnt lgkmcnt(1)\nv_cmp_gt_u32 vcc, v75, v66\nQmvaa 48,64,100\ns_cbranch_vccnz .Lso1440\n.Lro1440:\nQmvav 48,68,212\nQta 60,69,25856\n"
"v_add_u32 v75, 1, v75\nQmvaa 52,64,104\ns_waitcnt vmcnt(29)\nds_write_b32 v73, v75\ns_add_u32 s31, s77, 0\nQmvav 52,68,244\nbuffer_load_dwordx4 a[0:3], v79, s[48:51], s31 offen offset:0 sc1\ns_waitcnt lgkmcnt(5)\n"
"Qmaaa 196,72,104\nQta 62,69,26368\nQmaav 196,76,244\nQmaaa 192,72,100\nQmaav 192,76,212\nbuffer_load_dwordx4 a[4:7], v79, s[48:51], s31 offen offset:1024 sc1\nQmaaa 188,72,96\nQta 64,69,32768\n"
"Qmaav 188,76,232\nbuffer_load_dword v78, v76, s[24:27], 0 offen sc1\nQmaaa 184,72,92\nds_read_b32 v66, v74\nQmaav 184,76,200\nQta 66,69,33792\nQmaaa 180,72,88\nbuffer_load_dwordx4 a[8:11], v79, s[48:51], s31 offen offset:2048 sc1\n"
"Qmaav 180,76,168\nQmaaa 176,72,84\ns_waitcnt lgkmcnt(1)\nv_cmp_gt_u32 vcc, v75, v66\nQmaaa 176,76,112\ns_cbranch_vccnz .Lso1455\n.Lro1455:\nQmaaa 172,72,80\n"
"Qta 68,69,40960\nQmaaa 172,76,108\nbuffer_load_dwordx4 a[12:15], v79, s[48:51], s31 offen offset:3072 sc1\ns_waitcnt lgkmcnt(4)\nQmvaa 92,56,80\nQmvaa 92,60,108\nQta 70,69,41984\nQmvaa 124,56,84\n"
"s_add_u32 s31, s77, 4096\nQmvaa 124,60,112\nbuffer_load_dwordx4 a[16:19], v79, s[48:51], s31 offen offset:0 sc1\nQmvaa 156,56,88\nQta 72,69,33024\nQmvav 156,60,168\nQmvaa 188,56,92\nQmvav 188,60,200\n"
"Qta 74,69,34048\nQmvaa 220,56,96\nbuffer_load_dwordx4 a[20:23], v79, s[48:51], s31 offen offset:1024 sc1\nQmvav 220,60,232\nQmvaa 252,56,100\nQta 76,69,41216\nQmvav 252,60,212\nQmvaa 100,56,104\n"
"buffer_load_dwordx4 a[24:27], v79, s[48:51], s31 offen offset:2048 sc1\nQmvav 100,60,244\nQta 78,69,42240\ns_waitcnt lgkmcnt(4)\nQmaaa 224,64,104\nQmaav 224,68,244\ns_add_u32 s31, s77, 16384\nQmaaa 220,64,100\n"
"buffer_load_dwordx4 a[28:31], v79, s[48:51], s31 offen offset:0 sc1\nQmaav 220,68,212\nQmaaa 216,64,96\nQta 56,69,33280\nQmaav 216,68,232\nQmaaa 212,64,92\nbuffer_load_dwordx4 a[32:35], v79, s[48:51], s31 offen offset:1024 sc1\nQmaav 212,68,200\n"
"Qta 58,69,34304\nQmaaa 208,64,88\nQmaav 208,68,168\nQmaaa 204,64,84\nQta 60,69,41472\nQmaaa 204,68,112\nbuffer_load_dwordx4 a[36:39], v79, s[48:51], s31 offen offset:2048 sc1\nQmaaa 200,64,80\n"
"Qmaaa 200,68,108\nQta 62,69,42496\ns_waitcnt lgkmcnt(4)\nQmaaa 228,72,80\nQmaaa 228,76,108\nbuffer_load_dwordx4 a[40:43], v79, s[48:51], s31 offen offset:3072 sc1\nQmaaa 232,72,84\nQmaaa 232,76,112\n"
"Qmaaa 236,72,88\nQta 64,69,33536\ns_add_u32 s31, s77, 20480\nQmaav 236,76,168\nbuffer_load_dwordx4 a[44:47], v79, s[48:51], s31 offen offset:0 sc1\nQmaaa 240,72,92\nQmaav 240,76,200\nQta 66,69,34560\n"
"Qmaaa 244,72,96\nQmaav 244,76,232\nbuffer_load_dwordx4 a[48:51], v79, s[48:51], s31 offen offset:1024 sc1\nQmaaa 248,72,100\nQta 68,69,41728\nQmaav 248,76,212\nQmaaa 252,72,104\nQmaav 252,76,244\n"
"Qta 70,69,42752\ns_waitcnt lgkmcnt(4)\nQmvaa 172,56,104\nbuffer_load_dwordx4 a[52:55], v79, s[48:51], s31 offen offset:2048 sc1\nQmvav 172,60,244\nQmvaa 140,56,100\nQmvav 140,60,212\nQmvaa 108,56,96\n"
"Qta 72,69,49152\ns_add_u32 m0, s96, 57344\ns_add_u32 s95, s73, 45056\nQmvav 108,60,232\nbuffer_load_dwordx4 v63, s[52:55], s95 offen lds\nQmvaa 228,56,92\nQmvav 228,60,200\nQta 74,69,50176\n"
"Qmvaa 196,56,88\nQmvav 196,60,168\ns_waitcnt vmcnt(13)\nv_sub_u32 v66, v78, v65\nv_cmp_gt_u32 vcc, 3, v66\ns_and_b64 vcc, vcc, s[28:29]\ns_cbranch_scc1 .Lf3_w\n.Lf3_r:\n"
"Qmvaa 164,56,84\nQta 76,69,57344\ns_add_u32 s95, s73, 64512\nQmvaa 164,60,112\nbuffer_load_dwordx4 v63, s[52:55], s95 offen offset:1024 lds\nv_add_u32 v75, 1, v75\nQmvaa 132,56,80\ns_waitcnt vmcnt(41)\n"
"ds_write_b32 v73, v75\nQmvaa 132,60,108\nQta 78,69,58368\ns_waitcnt lgkmcnt(5)\nQmvaa 204,64,80\ns_add_u32 s31, s74, 131072\nQmvaa 204,68,108\nbuffer_load_dwordx4 a[80:83], v79, s[48:51], s31 offen offset:0 sc1\n"
"Qmvaa 236,64,84\nQmvaa 236,68,112\ns_bitcmp1_b32 s19, 2\ns_cbranch_scc1 .Lrole1\nQmvaa 96,64,88\nQta 56,69,49408\ns_lshl_b32 s46, s19, 4\nQmvav 96,68,168\n"
"buffer_load_dwordx4 a[84:87], v79, s[48:51], s31 offen offset:1024 sc1\nQmvaa 128,64,92\nds_read_b32 v66, v74\ns_add_u32 s46, s46, 128\nQmvav 128,68,200\nQta 58,69,50432\nv_mov_b32 v77, 0x7ffff000\nQmvaa 160,64,96\n"
"Qmvav 160,68,232\nbuffer_load_dwordx4 a[88:91], v79, s[48:51], s31 offen offset:2048 sc1\nQmvaa 192,64,100\ns_waitcnt lgkmcnt(1)\nv_cmp_gt_u32 vcc, v75, v66\nQmvav 192,68,212\ns_cbranch_vccnz .Lso1525r0\n.Lro1525r0:\n"
"Qmvaa 224,64,104\nQta 60,69,57600\nQmvav 224,68,244\nbuffer_load_dwordx4 a[92:95], v79, s[48:51], s31 offen offset:3072 sc1\ns_waitcnt vmcnt(31)\ns_waitcnt lgkmcnt(4)\nQmaav 140,72,84\nQmaav 140,76,116\n"
"Qta 62,69,58624\nQmaav 136,72,240\nQmaav 136,76,248\nQmaav 132,72,208\nQta 64,69,49664\ns_add_u32 m0, s96, 65536\ns_add_u32 s95, s67, 86016\nQmaav 132,76,216\n"
"buffer_load_dwordx4 v63, s[52:55], s95 offen lds\nQmaav 128,72,176\nQmaav 128,76,184\nQta 66,69,50688\nQmaav 124,72,144\nQmaav 124,76,152\nQmaav 120,72,112\nQta 68,69,57856\n"
"s_add_u32 s95, s67, 105472\nQmaav 120,76,120\nbuffer_load_dwordx4 v63, s[52:55], s95 offen offset:1024 lds\nQmaav 116,72,80\nQmaav 116,76,88\nQta 70,69,58880\ns_waitcnt lgkmcnt(4)\nQmaav 144,56,80\n"
"Qmaav 144,60,88\ns_add_u32 m0, s96, 73728\ns_add_u32 s95, s67, 126976\nQmaav 148,56,112\nbuffer_load_dwordx4 v63, s[52:55], s95 offen lds\nQmaav 148,60,120\nQmaav 152,56,144\nQta 72,69,49920\n"
"Qmaav 152,60,152\nQmaav 156,56,176\nQmaav 156,60,184\nQta 74,69,50944\ns_add_u32 s95, s67, 146432\nQmaav 160,56,208\nbuffer_load_dwordx4 v63, s[52:55], s95 offen offset:1024 lds\nQmaav 160,60,216\n"
"Qmaav 164,56,240\nQta 76,69,58112\nQmaav 164,60,248\nQmaav 168,56,84\nQmaav 168,60,116\nQta 78,69,59136\ns_add_u32 m0, s96, 81920\ns_add_u32 s95, s72, 86016\n"
"s_waitcnt lgkmcnt(4)\nQmvav 24,64,84\nbuffer_load_dwordx4 v63, s[52:55], s95 offen lds\nQmvav 24,68,116\nQmvav 20,64,240\nQmvav 20,68,248\nQmvav 16,64,208\nQta 56,68,0\n"
"s_add_u32 s95, s72, 105472\nQmvav 16,68,216\nbuffer_load_dwordx4 v63, s[52:55], s95 offen offset:1024 lds\nQmvav 12,64,176\nQmvav 12,68,184\nQta 58,68,512\nQmvav 8,64,144\nQmvav 8,68,152\n"
"Qmvav 4,64,112\nQta 60,68,1024\ns_add_u32 m0, s96, 90112\ns_add_u32 s95, s72, 126976\nQmvav 4,68,120\nbuffer_load_dwordx4 v63, s[52:55], s95 offen lds\nQmvav 0,64,80\nQmvav 0,68,88\n"
"Qta 62,68,1536\ns_waitcnt lgkmcnt(4)\nQmvav 28,72,80\nQmvav 28,76,88\ns_add_u32 s95, s72, 146432\nQmvav 32,72,112\nbuffer_load_dwordx4 v63, s[52:55], s95 offen offset:1024 lds\nQmvav 32,76,120\n"
"Qmvav 36,72,144\nQta 64,68,256\nQmvav 36,76,152\ns_add_u32 s31, s74, 135168\nQmvav 40,72,176\nbuffer_load_dwordx4 a[96:99], v79, s[48:51], s31 offen offset:0 sc1\nQmvav 40,76,184\nQta 66,68,768\n"
"Qmvav 44,72,208\nv_cmp_eq_u32 s[44:45], s46, v60\ns_nop 1\nv_cndmask_b32 v77, v77, v76, s[44:45]\nv_add_u32 v67, 1, v65\ns_xor_b32 s46, s19, 12\ns_lshl_b32 s28, 1, s46\nQmvav 44,76,216\n"
"Qmvav 48,72,240\nQta 68,68,1280\nQmvav 48,76,248\nbuffer_load_dwordx4 a[100:103], v79, s[48:51], s31 offen offset:1024 sc1\nv_add_u32 v75, 1, v75\nQmvav 52,72,84\ns_waitcnt vmcnt(35)\nds_write_b32 v73, v75\n"
"Qmvav 52,76,116\nQta 70,68,1792\ns_waitcnt lgkmcnt(5)\nQmaav 196,56,84\nQmaav 196,60,116\nbuffer_load_dwordx4 a[104:107], v79, s[48:51], s31 offen offset:2048 sc1\nQmaav 192,56,240\nQmaav 192,60,248\n"
"Qmaav 188,56,208\nQta 72,68,8192\ns_add_u32 s31, s74, 147456\nQmaav 188,60,216\nbuffer_load_dwordx4 a[108:111], v79, s[48:51], s31 offen offset:0 sc1\nQmaav 184,56,176\nds_read_b32 v66, v74\nQmaav 184,60,184\n"
"Qta 74,68,9216\nQmaav 180,56,144\nQmaav 180,60,152\nbuffer_load_dwordx4 a[112:115], v79, s[48:51], s31 offen offset:1024 sc1\nQmaav 176,56,112\n.Lpg19:\ns_cbranch_execz .Lpg23\ns_waitcnt lgkmcnt(1)\n"
"v_cmp_gt_u32 vcc, v75, v66\nQmaav 176,60,120\ns_cbranch_vccnz .Lso1595r0\n.Lro1595r0:\nQmaav 172,56,80\nQta 76,68,16384\nQmaav 172,60,88\nbuffer_load_dwordx4 v[168:171], v79, s[48:51], s31 offen offset:2048 sc1\n"
"s_waitcnt lgkmcnt(4)\nQmvav 92,64,80\nQmvav 92,68,88\nQta 78,68,17408\nQmvav 124,64,112\nQmvav 124,68,120\nbuffer_load_dwordx4 v[200:203], v79, s[48:51], s31 offen offset:3072 sc1\nQmvav 156,64,144\n"
"Qta 56,68,8448\nQmvav 156,68,152\nQmvav 188,64,176\nQmvav 188,68,184\nQta 58,68,9472\ns_add_u32 s31, s74, 151552\nQmvav 220,64,208\nbuffer_load_dwordx4 v[232:235], v79, s[48:51], s31 offen offset:0 sc1\n"
"Qmvav 220,68,216\nQmvav 252,64,240\nQta 60,68,16640\nQmvav 252,68,248\nQmvav 100,64,84\nbuffer_load_dwordx4 v[212:215], v79, s[48:51], s31 offen offset:1024 sc1\nQmvav 100,68,116\nQta 62,68,17664\n"
"s_waitcnt lgkmcnt(4)\nQmaav 224,72,84\nQmaav 224,76,116\nQmaav 220,72,240\nbuffer_load_dwordx4 v[244:247], v79, s[48:51], s31 offen offset:2048 sc1\nQmaav 220,76,248\nQmaav 216,72,208\nQta 64,68,8704\n"
"Qmaav 216,76,216\ns_add_u32 m0, s91, 0\ns_add_u32 s31, s77, 131072\nQmaav 212,72,176\nbuffer_load_dwordx4 v79, s[48:51], s31 offen sc1 lds\nQmaav 212,76,184\nQta 66,68,9728\nQmaav 208,72,144\n"
"Qmaav 208,76,152\nQmaav 204,72,112\nQta 68,68,16896\nQmaav 204,76,120\nbuffer_load_dwordx4 v79, s[48:51], s31 offen offset:1024 sc1 lds\nQmaav 200,72,80\nQmaav 200,76,88\nQta 70,68,17920\n"
"s_waitcnt lgkmcnt(4)\nQmaav 228,56,80\nQmaav 228,60,88\nbuffer_load_dwordx4 v79, s[48:51], s31 offen offset:2048 sc1 lds\nQmaav 232,56,112\nQmaav 232,60,120\nQmaav 236,56,144\nQta 72,68,8960\n"
"Qmaav 236,60,152\nbuffer_load_dwordx4 v79, s[48:51], s31 offen offset:3072 sc1 lds\nQmaav 240,56,176\nQmaav 240,60,184\nQta 74,68,9984\nQmaav 244,56,208\ns_add_u32 m0, s91, 4096\ns_add_u32 s31, s77, 135168\n"
"Qmaav 244,60,216\nbuffer_load_dwordx4 v79, s[48:51], s31 offen sc1 lds\nQmaav 248,56,240\nQta 76,68,17152\nQmaav 248,60,248\nQmaav 252,56,84\nQmaav 252,60,116\nQta 78,68,18176\n"
"s_waitcnt lgkmcnt(4)\nQmvav 172,64,84\nbuffer_load_dwordx4 v79, s[48:51], s31 offen offset:1024 sc1 lds\nQmvav 172,68,116\nQmvav 140,64,240\nQmvav 140,68,248\nQmvav 108,64,208\nQta 56,68,24576\n"
"Qmvav 108,68,216\nbuffer_load_dwordx4 v79, s[48:51], s31 offen offset:2048 sc1 lds\nQmvav 228,64,176\nQmvav 228,68,184\nQta 58,68,25600\nQmvav 196,64,144\ns_add_u32 m0, s91, 33792\ns_add_u32 s31, s77, 147456\n"
"Qmvav 196,68,152\nbuffer_load_dwordx4 v79, s[48:51], s31 offen sc1 lds\nQmvav 164,64,112\nQta 60,68,32768\nQmvav 164,68,120\nv_add_u32 v75, 1, v75\nQmvav 132,64,80\ns_waitcnt vmcnt(47)\n"
"ds_write_b32 v73, v75\nQmvav 132,68,88\nQta 62,68,33792\ns_waitcnt lgkmcnt(5)\nQmvav 204,72,80\nbuffer_load_dwordx4 v79, s[48:51], s31 offen offset:1024 sc1 lds\nQmvav 204,76,88\nQmvav 236,72,112\n"
"Qmvav 236,76,120\nQmvav 96,72,144\nQta 64,68,24832\nQmvav 96,76,152\nbuffer_load_dwordx4 v79, s[48:51], s31 offen offset:2048 sc1 lds\nQmvav 128,72,176\nds_read_b32 v66, v74\nQmvav 128,76,184\n"
"Qta 66,68,25856\nQmvav 160,72,208\nQmvav 160,76,216\nbuffer_load_dwordx4 v79, s[48:51], s31 offen offset:3072 sc1 lds\nQmvav 192,72,240\ns_waitcnt lgkmcnt(1)\nv_cmp_gt_u32 vcc, v75, v66\nQmvav 192,76,248\n"
"s_cbranch_vccnz .Lso1665r0\n.Lro1665r0:\nQmvav 224,72,84\nQta 68,68,33024\ns_add_u32 m0, s91, 37888\ns_add_u32 s31, s77, 151552\nQmvav 224,76,116\nbuffer_load_dwordx4 v79, s[48:51], s31 offen sc1 lds\n"
"s_waitcnt vmcnt(36)\ns_waitcnt lgkmcnt(4)\nQmaaa 140,56,24\nQmaaa 140,60,52\nQta 70,68,34048\nQmaaa 136,56,20\nQmaaa 136,60,48\nbuffer_load_dwordx4 v79, s[48:51], s31 offen offset:1024 sc1 lds\n"
"Qmaaa 132,56,16\nQta 72,68,25088\nQmaaa 132,60,44\nQmaaa 128,56,12\nQmaaa 128,60,40\nQta 74,68,26112\nQmaaa 124,56,8\nbuffer_load_dwordx4 v79, s[48:51], s31 offen offset:2048 sc1 lds\n"
"Qmaaa 124,60,36\nQmaaa 120,56,4\nQta 76,68,33280\nQmaaa 120,60,32\nQmaaa 116,56,0\nQmaaa 116,60,28\nQta 78,68,34304\ns_add_u32 m0, s96, 0\n"
"s_add_u32 s95, s73, 86016\ns_waitcnt lgkmcnt(4)\nQmaaa 144,64,0\nbuffer_load_dwordx4 v63, s[52:55], s95 offen lds\nQmaaa 144,68,28\nQmaaa 148,64,4\nQmaaa 148,68,32\nQmaaa 152,64,8\n"
"Qta 56,68,25344\ns_add_u32 s95, s73, 105472\nQmaaa 152,68,36\nbuffer_load_dwordx4 v63, s[52:55], s95 offen offset:1024 lds\nQmaaa 156,64,12\nQmaaa 156,68,40\nQta 58,68,26368\nQmaaa 160,64,16\n"
"Qmaaa 160,68,44\nQmaaa 164,64,20\nQta 60,68,33536\ns_add_u32 m0, s96, 8192\ns_add_u32 s95, s73, 126976\nQmaaa 164,68,48\nbuffer_load_dwordx4 v63, s[52:55], s95 offen lds\nQmaaa 168,64,24\n"
"Qmaaa 168,68,52\nQta 62,68,34560\ns_waitcnt lgkmcnt(4)\nQmvaa 24,72,24\nQmvaa 24,76,52\ns_add_u32 s95, s73, 146432\nQmvaa 20,72,20\nbuffer_load_dwordx4 v63, s[52:55], s95 offen offset:1024 lds\n"
"Qmvaa 20,76,48\nQmvaa 16,72,16\nQta 64,68,40960\nQmvaa 16,76,44\ns_add_u32 s31, s75, 131072\nQmvaa 12,72,12\nbuffer_load_dwordx4 v[80:83], v79, s[48:51], s31 offen offset:0 sc1\nQmvaa 12,76,40\n"
"Qta 66,68,41472\nQmvaa 8,72,8\nQmvaa 8,76,36\nQmvaa 4,72,4\nQta 68,68,41984\nQmvaa 4,76,32\nbuffer_load_dwordx4 v[112:115], v79, s[48:51], s31 offen offset:1024 sc1\nQmvaa 0,72,0\n"
"Qmvaa 0,76,28\nQta 70,68,42496\ns_waitcnt lgkmcnt(4)\nQmvaa 28,56,0\nQmvaa 28,60,28\nbuffer_load_dwordx4 v[144:147], v79, s[48:51], s31 offen offset:2048 sc1\nQmvaa 32,56,4\nQmvaa 32,60,32\n"
"Qmvaa 36,56,8\nQta 72,68,41216\nQmvaa 36,60,36\nbuffer_load_dwordx4 v[176:179], v79, s[48:51], s31 offen offset:3072 sc1\nQmvaa 40,56,12\nQmvaa 40,60,40\nQta 74,68,41728\nQmvaa 44,56,16\n"
"s_add_u32 s31, s75, 135168\nQmvaa 44,60,44\nbuffer_load_dwordx4 v[208:211], v79, s[48:51], s31 offen offset:0 sc1\nQmvaa 48,56,20\nQta 76,68,42240\nQmvaa 48,60,48\nv_add_u32 v75, 1, v75\nQmvaa 52,56,24\n"
"s_waitcnt vmcnt(35)\nds_write_b32 v73, v75\nQmvaa 52,60,52\nQta 78,68,42752\ns_waitcnt lgkmcnt(5)\nQmaaa 196,64,24\nbuffer_load_dwordx4 v[240:243], v79, s[48:51], s31 offen offset:1024 sc1\nQmaaa 196,68,52\n"
"Qmaaa 192,64,20\nQmaaa 192,68,48\nQmaaa 188,64,16\nQta 56,68,49152\nQmaaa 188,68,44\nbuffer_load_dwordx4 v[84:87], v79, s[48:51], s31 offen offset:2048 sc1\nQmaaa 184,64,12\nds_read_b32 v66, v74\n"
"Qmaaa 184,68,40\nQta 58,68,50176\nQmaaa 180,64,8\ns_add_u32 s31, s75, 147456\nQmaaa 180,68,36\nbuffer_load_dwordx4 v[88:91], v79, s[48:51], s31 offen offset:0 sc1\nQmaaa 176,64,4\ns_waitcnt lgkmcnt(1)\n"
"v_cmp_gt_u32 vcc, v75, v66\nQmaaa 176,68,32\ns_cbranch_vccnz .Lso1735r0\n.Lro1735r0:\nQmaaa 172,64,0\nQta 60,68,57344\nQmaaa 172,68,28\nbuffer_load_dwordx4 v[120:123], v79, s[48:51], s31 offen offset:1024 sc1\n"
"s_waitcnt lgkmcnt(4)\nQmvaa 92,72,0\nQmvaa 92,76,28\nQta 62,68,58368\nQmvaa 124,72,4\nQmvaa 124,76,32\nbuffer_load_dwordx4 v[152:155], v79, s[48:51], s31 offen offset:2048 sc1\nQmvaa 156,72,8\n"
"Qta 64,68,49408\nQmvaa 156,76,36\nQmvaa 188,72,12\nQmvaa 188,76,40\nQta 66,68,50432\nQmvaa 220,72,16\nbuffer_load_dwordx4 v[184:187], v79, s[48:51], s31 offen offset:3072 sc1\nQmvaa 220,76,44\n"
"Qmvaa 252,72,20\nQta 68,68,57600\nQmvaa 252,76,48\ns_add_u32 s31, s75, 151552\nQmvaa 100,72,24\nbuffer_load_dwordx4 v[216:219], v79, s[48:51], s31 offen offset:0 sc1\nQmvaa 100,76,52\nQta 70,68,58624\n"
"s_waitcnt lgkmcnt(4)\nQmaaa 224,56,24\nQmaaa 224,60,52\nQmaaa 220,56,20\nbuffer_load_dwordx4 v[248:251], v79, s[48:51], s31 offen offset:1024 sc1\nQmaaa 220,60,48\nQmaaa 216,56,16\nQta 72,68,49664\n"
"Qmaaa 216,60,44\nQmaaa 212,56,12\nbuffer_load_dwordx4 v[116:119], v79, s[48:51], s31 offen offset:2048 sc1\nQmaaa 212,60,40\nQta 74,68,50688\nQmaaa 208,56,8\nQmaaa 208,60,36\nQmaaa 204,56,4\n"
"Qta 76,68,57856\ns_add_u32 m0, s96, 16384\ns_add_u32 s95, s67, 86016\nQmaaa 204,60,32\nbuffer_load_dwordx4 v61, s[52:55], s95 offen lds\nQmaaa 200,56,0\nQmaaa 200,60,28\nQta 78,68,58880\n"
"s_waitcnt lgkmcnt(4)\nQmaaa 228,64,0\nQmaaa 228,68,28\ns_add_u32 s95, s67, 105472\nQmaaa 232,64,4\nbuffer_load_dwordx4 v61, s[52:55], s95 offen offset:1024 lds\nQmaaa 232,68,32\nQmaaa 236,64,8\n"
"Qta 56,68,49920\nQmaaa 236,68,36\nQmaaa 240,64,12\nQmaaa 240,68,40\nQta 58,68,50944\ns_add_u32 m0, s96, 24576\ns_add_u32 s95, s67, 126976\nQmaaa 244,64,16\n"
"buffer_load_dwordx4 v61, s[52:55], s95 offen lds\nQmaaa 244,68,44\nQmaaa 248,64,20\nQta 60,68,58112\nQmaaa 248,68,48\nQmaaa 252,64,24\nQmaaa 252,68,52\nQta 62,68,59136\n"
"s_add_u32 s95, s67, 146432\ns_waitcnt lgkmcnt(4)\nQmvaa 172,72,24\nbuffer_load_dwordx4 v61, s[52:55], s95 offen offset:1024 lds\nQmvaa 172,76,52\nQmvaa 140,72,20\nQmvaa 140,76,48\nQmvaa 108,72,16\n"
"Qta 64,69,0\ns_add_u32 m0, s96, 32768\ns_add_u32 s95, s72, 86016\nQmvaa 108,76,44\nbuffer_load_dwordx4 v61, s[52:55], s95 offen lds\nQmvaa 228,72,12\nQmvaa 228,76,40\nQta 66,69,1024\n"
"Qmvaa 196,72,8\nQmvaa 196,76,36\nQmvaa 164,72,4\nQta 68,69,8192\ns_add_u32 s95, s72, 105472\nQmvaa 164,76,32\nbuffer_load_dwordx4 v61, s[52:55], s95 offen offset:1024 lds\nv_add_u32 v75, 1, v75\n"
"Qmvaa 132,72,0\ns_waitcnt vmcnt(20)\nds_write_b32 v73, v75\nQmvaa 132,76,28\nQta 70,69,9216\ns_waitcnt lgkmcnt(5)\nQmvaa 204,56,0\ns_waitcnt vmcnt(24)\n"
"Qda 0,58,0\nQmvaa 204,60,28\nQmvaa 236,56,4\nQda 28,58,33792\nQmvaa 236,60,32\nQmvaa 96,56,8\nQta 72,69,16384\nQmvaa 96,60,36\n"
"Qda 4,58,1024\nQmvaa 128,56,12\nds_read_b32 v66, v74\nQmvaa 128,60,40\nQta 74,69,17408\nQmvaa 160,56,16\nQda 32,58,34816\nQmvaa 160,60,44\n"
"Qmvaa 192,56,20\ns_waitcnt lgkmcnt(2)\nv_cmp_gt_u32 vcc, v75, v66\nQmvaa 192,60,48\ns_cbranch_vccnz .Lso1805r0\n.Lro1805r0:\nQmvaa 224,56,24\nQta 76,69,24576\n"
"s_add_u32 m0, s96, 40960\ns_add_u32 s95, s72, 126976\nQmvaa 224,60,52\nbuffer_load_dwordx4 v61, s[52:55], s95 offen lds\ns_waitcnt vmcnt(39)\ns_waitcnt lgkmcnt(8)\nQmaaa 200,64,80\nQda 8,58,2048\n"
"Qmaaa 200,68,108\nQta 78,69,25600\nQmaaa 204,64,84\nQda 36,58,35840\nQmaaa 204,68,112\nQmaaa 208,64,88\nQta 56,68,0\ns_add_u32 s95, s72, 146432\n"
"Qmaav 208,68,168\nbuffer_load_dwordx4 v61, s[52:55], s95 offen offset:1024 lds\nQmaaa 212,64,92\nQda 12,58,3072\nQmaav 212,68,200\nQta 58,68,1024\nQmaaa 216,64,96\nQda 40,58,36864\n"
"Qmaav 216,68,232\nQmaaa 220,64,100\nQta 60,68,8192\ns_add_u32 m0, s96, 49152\ns_add_u32 s95, s73, 86016\nQmaav 220,68,212\nbuffer_load_dwordx4 v61, s[52:55], s95 offen lds\nQmaaa 224,64,104\n"
"Qda 16,58,4096\nQmaav 224,68,244\nQta 62,68,9216\ns_waitcnt vmcnt(9)\ns_waitcnt lgkmcnt(8)\nQmaav 200,72,80\nQda 44,58,37888\nQmaav 200,76,88\n"
"s_add_u32 s95, s73, 105472\nQmaav 204,72,112\nbuffer_load_dwordx4 v61, s[52:55], s95 offen offset:1024 lds\nQmaav 204,76,120\nQda 20,58,5120\nQmaav 208,72,144\nQta 64,69,256\nQmaav 208,76,152\n"
"Qda 48,58,38912\nQmaav 212,72,176\nQmaav 212,76,184\nQta 66,69,1280\ns_add_u32 m0, s96, 57344\ns_add_u32 s95, s73, 126976\nQmaav 216,72,208\nbuffer_load_dwordx4 v61, s[52:55], s95 offen lds\n"
"Qmaav 216,76,216\nQda 24,58,6144\nQmaav 220,72,240\nQta 68,69,8448\nQmaav 220,76,248\nQda 52,58,39936\nQmaav 224,72,84\nQmaav 224,76,116\n"
"Qta 70,69,9472\ns_add_u32 s95, s73, 146432\ns_waitcnt lgkmcnt(9)\nQmaaa 200,56,0\nbuffer_load_dwordx4 v61, s[52:55], s95 offen offset:1024 lds\nQmaaa 200,60,28\nQmaaa 204,56,4\nQmaaa 204,60,32\n"
"s_waitcnt lgkmcnt(9)\nQmaaa 208,56,8\nQta 72,69,16640\nQmaaa 208,60,36\ns_nop 1\nds_write_b128 v58, a[200:203] offset:0\nQmaaa 212,56,12\nQmaaa 212,60,40\n"
"Qta 74,69,17664\ns_waitcnt lgkmcnt(4)\nQmaaa 216,56,16\nds_write_b128 v58, a[204:207] offset:1024\nQmaaa 216,60,44\nQmaaa 220,56,20\nQta 76,69,24832\nQmaaa 220,60,48\n"
"ds_write_b128 v58, a[208:211] offset:2048\nQmaaa 224,56,24\nQmaaa 224,60,52\nQta 78,69,25856\ns_waitcnt lgkmcnt(7)\nQmaaa 228,64,80\nds_write_b128 v58, a[212:215] offset:3072\nQmaaa 228,68,108\n"
"Qmaaa 232,64,84\nQmaaa 232,68,112\nds_write_b128 v58, a[216:219] offset:4096\nQmaaa 236,64,88\nQta 56,68,256\nQmaav 236,68,168\nQmaaa 240,64,92\nds_write_b128 v58, a[220:223] offset:5120\n"
"Qmaav 240,68,200\nQta 58,68,1280\nQmaaa 244,64,96\nQmaav 244,68,232\nds_write_b128 v58, a[224:227] offset:6144\nQmaaa 248,64,100\nQta 60,68,8448\nQmaav 248,68,212\n"
"Qmaaa 252,64,104\nQmaav 252,68,244\nQta 62,68,9472\ns_waitcnt lgkmcnt(8)\nQmaav 228,72,80\nv_add_u32 v75, 1, v75\nQmaav 228,76,88\nds_write_b32 v73, v75\n"
"Qmaav 232,72,112\nQmaav 232,76,120\nQmaav 236,72,144\nQta 64,69,512\nQmaav 236,76,152\nQmaav 240,72,176\nQmaav 240,76,184\nQta 66,69,1536\n"
"Qmaav 244,72,208\nQmaav 244,76,216\nds_read_b32 v66, v74\nQmaav 248,72,240\nQta 68,69,8704\nQmaav 248,76,248\nQmaav 252,72,84\nQmaav 252,76,116\n"
"s_waitcnt lgkmcnt(1)\nv_cmp_gt_u32 vcc, v75, v66\ns_waitcnt lgkmcnt(5)\nQmaaa 228,56,0\ns_cbranch_vccnz .Lso1878r0\n.Lro1878r0:\nQmaaa 228,60,28\nQta 70,69,9728\n"
"Qmaaa 232,56,4\nQmaaa 232,60,32\nQdv 148,59,2048\nQmaaa 236,56,8\nQta 72,69,16896\nQmaaa 236,60,36\n.Lpg20:\ns_cbranch_execz .Lpg24\n"
"s_nop 0\nds_write_b128 v58, a[228:231] offset:33792\nQmaaa 240,56,12\nQdv 180,59,3072\nQmaaa 240,60,40\nQta 74,69,17920\nQmaaa 244,56,16\nds_write_b128 v58, a[232:235] offset:34816\n"
"Qmaaa 244,60,44\nQdv 104,59,5120\nQmaaa 248,56,20\nQta 76,69,25088\nQmaaa 248,60,48\nds_write_b128 v58, a[236:239] offset:35840\nQmaaa 252,56,24\nQdv 136,59,0\n"
"s_waitcnt lgkmcnt(9)\nQa 196,196,148\nQmaaa 252,60,52\nQta 78,69,26112\nQa 197,197,149\ns_waitcnt lgkmcnt(10)\nQmvaa 132,64,80\nds_write_b128 v58, a[240:243] offset:36864\n"
"Qmvaa 132,68,108\nQa 198,198,150\nQa 199,199,151\nQmvaa 164,64,84\ns_waitcnt lgkmcnt(8)\nQa 228,228,180\nQa 229,229,181\nQmvaa 164,68,112\n"
"ds_write_b128 v58, a[244:247] offset:37888\nQmvaa 196,64,88\nQta 56,68,512\nQa 230,230,182\nQa 231,231,183\nQmvav 196,68,168\nQdv 148,59,4096\ns_waitcnt lgkmcnt(8)\n"
"Qa 140,140,104\nQmvaa 228,64,92\nds_write_b128 v58, a[248:251] offset:38912\nQmvav 228,68,200\nQta 58,68,1536\nQa 141,141,105\nQmvaa 108,64,96\nQa 142,142,106\n"
"Qa 143,143,107\nQmvav 108,68,232\nQdv 180,59,6144\ns_waitcnt lgkmcnt(8)\nQa 132,132,136\nQmvaa 140,64,100\nQta 60,68,8704\nQa 133,133,137\n"
"Qmvav 140,68,212\nds_write_b128 v58, a[252:255] offset:39936\nQmvaa 172,64,104\nQa 134,134,138\nQa 135,135,139\nQmvav 172,68,244\nQta 62,68,9728\ns_waitcnt lgkmcnt(6)\n"
"Qa 108,108,148\ns_waitcnt lgkmcnt(6)\nQmvav 132,72,80\nQdv 104,59,1024\nv_add_u32 v75, 1, v75\nQmvav 132,76,88\ns_waitcnt vmcnt(10)\nds_write_b32 v73, v75\n"
"Qmvav 164,72,112\nQa 109,109,149\nQa 110,110,150\nQmvav 164,76,120\nQa 111,111,151\ns_waitcnt lgkmcnt(5)\nQa 172,172,180\nQmvav 196,72,144\n"
"Qta 64,69,768\nQa 173,173,181\nQmvav 196,76,152\nQa 174,174,182\nQa 175,175,183\nQmvav 228,72,176\nQmvav 228,76,184\nQta 66,69,1792\n"
"s_waitcnt lgkmcnt(3)\nQa 164,164,104\nQmvav 108,72,208\nQa 165,165,105\nQa 166,166,106\nQmvav 108,76,216\nds_read_b32 v66, v74\nQa 167,167,107\n"
"Qmvav 140,72,240\nQta 68,69,8960\nQmvav 140,76,248\nQmvav 172,72,84\nQmvav 172,76,116\ns_waitcnt lgkmcnt(1)\nv_cmp_gt_u32 vcc, v75, v66\ns_waitcnt lgkmcnt(6)\n"
"Qmvaa 132,56,0\ns_cbranch_vccnz .Lso1920r0\n.Lro1920r0:\ns_add_u32 s68, s64, 0\nQmvaa 132,60,28\nQta 70,69,9984\nQmvaa 164,56,4\nQmvaa 164,60,32\n"
"Qdv 136,59,35840\nQmvaa 196,56,8\nQta 72,69,17152\ns_nop 1\nQk 132,132,133\nQmvaa 196,60,36\nQk 133,134,135\nQmvaa 228,56,12\n"
"Qdv 148,59,36864\nQk 134,164,165\nQmvaa 228,60,40\nQta 74,69,18176\nQk 135,166,167\nQmvaa 108,56,16\nbuffer_store_dwordx4 v[132:135], v79, s[24:27], s68 offen offset:0 sc1\nQk 196,196,197\n"
"Qmvaa 108,60,44\nQdv 180,59,38912\nQk 197,198,199\nQmvaa 140,56,20\nQta 76,69,25344\nQk 198,228,229\nQmvaa 140,60,48\ns_waitcnt lgkmcnt(5)\n"
"Qa 96,96,136\nQa 97,97,137\nQmvaa 172,56,24\nQdv 104,59,33792\nQa 98,98,138\nQmvaa 172,60,52\nQta 78,69,26368\nQa 99,99,139\n"
"s_waitcnt lgkmcnt(11)\nQmvaa 204,64,80\ns_waitcnt lgkmcnt(5)\nQa 128,128,148\nQa 129,129,149\nQmvaa 204,68,108\nQa 130,130,150\nQa 131,131,151\n"
"Qmvaa 236,64,84\nQk 199,230,231\nQk 108,108,109\nQmvaa 236,68,112\ns_waitcnt lgkmcnt(3)\nQa 192,192,180\nQa 193,193,181\nQmvaa 96,64,88\n"
"Qta 56,68,768\nQa 194,194,182\nQmvav 96,68,168\nQdv 136,59,37888\nQa 195,195,183\nQmvaa 128,64,92\ns_waitcnt lgkmcnt(3)\nQa 204,204,104\n"
"Qa 205,205,105\nQmvav 128,68,200\nQta 58,68,1792\nQa 206,206,106\nQmvaa 160,64,96\nbuffer_store_dwordx4 v[196:199], v79, s[24:27], s68 offen offset:1024 sc1\nQa 207,207,107\nQmvav 160,68,232\n"
"Qdv 148,59,39936\nQk 109,110,111\nQmvaa 192,64,100\nQta 60,68,8960\nQk 110,140,141\nQmvav 192,68,212\nQk 111,142,143\nQk 172,172,173\n"
"Qmvaa 224,64,104\nbuffer_store_dwordx4 v[108:111], v79, s[24:27], s68 offen offset:2048 sc1\nv_add_u32 v75, 1, v75\nQmvav 224,68,244\ns_waitcnt vmcnt(3)\nds_write_b32 v73, v75\ns_waitcnt lgkmcnt(10)\nQmvav 204,72,80\n"
"Qta 62,68,9984\ns_waitcnt lgkmcnt(5)\nQa 160,160,136\nQmvav 204,76,88\nQdv 180,59,34816\nQa 161,161,137\nQmvav 236,72,112\nQa 162,162,138\n"
"Qa 163,163,139\nQmvav 236,76,120\ns_waitcnt lgkmcnt(4)\nQa 224,224,148\nQa 225,225,149\nQmvav 96,72,144\nQta 64,68,16384\nQa 226,226,150\n"
"Qmvav 96,76,152\nQa 227,227,151\nQk 173,174,175\nQmvav 128,72,176\nQmvav 128,76,184\nds_read_b32 v66, v74\ns_waitcnt lgkmcnt(2)\nQa 236,236,180\n"
"Qmvav 160,72,208\nQta 66,68,17408\nQa 237,237,181\nQmvav 160,76,216\nQa 238,238,182\nQa 239,239,183\nQmvav 192,72,240\nQmvav 192,76,248\n"
"s_waitcnt lgkmcnt(1)\nv_cmp_gt_u32 vcc, v75, v66\nQmvav 224,72,84\ns_cbranch_vccnz .Lso1960r0\n.Lro1960r0:\nQmvav 224,76,116\nQta 68,68,24576\ns_add_u32 m0, s96, 65536\n"
"s_add_u32 s95, s67, 86016\ns_waitcnt lgkmcnt(5)\nQmvaa 204,56,0\nbuffer_load_dwordx4 v62, s[52:55], s95 offen lds\nQmvaa 204,60,28\nQmvaa 236,56,4\nQta 70,68,25600\nQmvaa 236,60,32\n"
"Qmvaa 96,56,8\ns_nop 3\nQk 174,204,205\nQk 175,206,207\nQmvaa 96,60,36\nQta 72,68,32768\ns_add_u32 s95, s67, 125952\nQmvaa 128,56,12\n"
"buffer_load_dwordx4 v62, s[52:55], s95 offen offset:1024 lds\nQmvaa 128,60,40\nbuffer_store_dwordx4 v[172:175], v79, s[24:27], s68 offen offset:3072 sc1\nQk 236,236,237\nQmvaa 160,56,16\nQta 74,68,33792\nQk 237,238,239\nQmvaa 160,60,44\n"
"Qk 238,96,97\nQk 239,98,99\nQmvaa 192,56,20\ns_add_u32 s68, s64, 4096\nQk 128,128,129\nQmvaa 192,60,48\nQta 76,68,40960\ns_add_u32 m0, s96, 73728\n"
"s_add_u32 s95, s72, 86016\nQmvaa 224,56,24\nbuffer_load_dwordx4 v62, s[52:55], s95 offen lds\nQmvaa 224,60,52\nbuffer_store_dwordx4 v[236:239], v79, s[24:27], s68 offen offset:0 sc1\nQk 129,130,131\ns_waitcnt lgkmcnt(3)\nQmaaa 116,64,80\n"
"Qta 78,68,41984\nQk 130,160,161\nQmaaa 116,68,108\nQk 131,162,163\nQk 192,192,193\nQmaaa 120,64,84\nbuffer_store_dwordx4 v[128:131], v79, s[24:27], s68 offen offset:1024 sc1\ns_add_u32 s95, s72, 125952\n"
"Qmaaa 120,68,112\nbuffer_load_dwordx4 v62, s[52:55], s95 offen offset:1024 lds\nQmaaa 124,64,88\nQta 56,68,49152\nQk 193,194,195\nQmaav 124,68,168\nQk 194,224,225\nQk 195,226,227\n"
"Qmaaa 128,64,92\nbuffer_store_dwordx4 v[192:195], v79, s[24:27], s68 offen offset:2048 sc1\nQmaav 128,68,200\nQta 58,68,50176\ns_add_u32 m0, s96, 81920\ns_add_u32 s95, s73, 86016\nQmaaa 132,64,96\nbuffer_load_dwordx4 v62, s[52:55], s95 offen lds\n"
"Qmaav 132,68,232\nQmaaa 136,64,100\nQta 60,68,57344\nQmaav 136,68,212\nQmaaa 140,64,104\nQmaav 140,68,244\nQta 62,68,58368\ns_add_u32 s95, s73, 125952\n"
"s_waitcnt lgkmcnt(4)\nQmaav 116,72,80\nbuffer_load_dwordx4 v62, s[52:55], s95 offen offset:1024 lds\nQmaav 116,76,88\nQmaav 120,72,112\nv_bfe_u32 v61, v60, 6, 2\nv_lshrrev_b32 v62, 8, v60\nQmaav 120,76,120\n"
"v_xor_b32 v61, v61, v62\nv_bfe_u32 v62, v60, 5, 1\nQmaav 124,72,144\nQta 64,68,16640\nv_lshl_or_b32 v61, v61, 1, v62\nQmaav 124,76,152\nv_and_b32 v62, 16, v60\nv_mov_b32 v63, 0x3f80\n"
"Qmaav 128,72,176\nv_lshlrev_b32 v63, v62, v63\nv_min_u32 v62, 1, v61\nQmaav 128,76,184\nQta 66,68,17664\nv_add_u32 v62, -1, v62\nQmaav 132,72,208\nv_and_b32 v62, v62, v63\n"
"v_xor_b32 v61, 1, v61\nQmaav 132,76,216\nv_min_u32 v61, 1, v61\nv_add_u32 v61, -1, v61\nQmaav 136,72,240\nQta 68,68,24832\nv_and_b32 v61, v61, v63\nQmaav 136,76,248\n"
"v_accvgpr_write_b32 a202, v62\nv_accvgpr_write_b32 a203, v61\nQmaav 140,72,84\nv_accvgpr_write_b32 a200, 0\nv_accvgpr_write_b32 a201, 0\nQmaav 140,76,116\nQta 70,68,25856\nv_accvgpr_write_b32 a204, 0\n"
"s_waitcnt lgkmcnt(4)\nQmaaa 116,56,0\nv_accvgpr_write_b32 a205, 0\nQmaaa 116,60,28\nQmaaa 120,56,4\nQmaaa 120,60,32\nQmaaa 124,56,8\nQta 72,68,33024\n"
"Qmaaa 124,60,36\ns_nop 2\nds_write_b128 v58, a[116:119] offset:0\nQmaaa 128,56,12\nQmaaa 128,60,40\nQta 74,68,34048\nQmaaa 132,56,16\nds_write_b128 v58, a[120:123] offset:1024\n"
"Qmaaa 132,60,44\nQmaaa 136,56,20\nQta 76,68,41216\nQmaaa 136,60,48\nds_write_b128 v58, a[124:127] offset:2048\nQmaaa 140,56,24\nQmaaa 140,60,52\nQta 78,68,42240\n"
"s_waitcnt lgkmcnt(7)\nQmaaa 144,64,80\nds_write_b128 v58, a[128:131] offset:3072\nQmaaa 144,68,108\nQmaaa 148,64,84\nQmaaa 148,68,112\nds_write_b128 v58, a[132:135] offset:4096\nQmaaa 152,64,88\n"
"Qta 56,68,49408\nQmaav 152,68,168\nQmaaa 156,64,92\nds_write_b128 v58, a[136:139] offset:5120\nQmaav 156,68,200\nQta 58,68,50432\nQmaaa 160,64,96\nQmaav 160,68,232\n"
"ds_write_b128 v58, a[140:143] offset:6144\nQmaaa 164,64,100\nQta 60,68,57600\nQmaav 164,68,212\nQmaaa 168,64,104\nQmaav 168,68,244\nQta 62,68,58624\ns_waitcnt lgkmcnt(8)\n"
"Qmaav 144,72,80\nv_add_u32 v75, 1, v75\nQmaav 144,76,88\ns_waitcnt vmcnt(8)\nds_write_b32 v73, v75\nQmaav 148,72,112\nQmaav 148,76,120\nQmaav 152,72,144\n"
"Qta 64,68,16896\nQmaav 152,76,152\nQmaav 156,72,176\nQmaav 156,76,184\nQta 66,68,17920\nQmaav 160,72,208\nQmaav 160,76,216\nds_read_b32 v66, v74\n"
"Qmaav 164,72,240\nQta 68,68,25088\ns_waitcnt vmcnt(2)\nbuffer_store_dword v67, v77, s[24:27], 0 offen sc1\nQmaav 164,76,248\nQmaav 168,72,84\nQmaav 168,76,116\ns_waitcnt lgkmcnt(1)\n"
"v_cmp_gt_u32 vcc, v75, v66\ns_waitcnt lgkmcnt(5)\nQmaaa 144,56,0\ns_cbranch_vccnz .Lso2046r0\n.Lro2046r0:\nQmaaa 144,60,28\nQta 70,68,26112\nQmaaa 148,56,4\n"
"Qmaaa 148,60,32\nQdv 104,59,2048\nQmaaa 152,56,8\nQta 72,68,33280\nQmaaa 152,60,36\ns_nop 0\nds_write_b128 v58, a[144:147] offset:33792\nQmaaa 156,56,12\n"
"Qdv 136,59,3072\nQmaaa 156,60,40\nQta 74,68,34304\nQmaaa 160,56,16\nds_write_b128 v58, a[148:151] offset:34816\nQmaaa 160,60,44\nQdv 148,59,5120\nQmaaa 164,56,20\n"
"Qta 76,68,41472\nQmaaa 164,60,48\nds_write_b128 v58, a[152:155] offset:35840\nQmaaa 168,56,24\nQdv 180,59,0\ns_waitcnt lgkmcnt(9)\nQa 8,8,104\nQmaaa 168,60,52\n"
"Qta 78,68,42496\nQa 9,9,105\ns_waitcnt lgkmcnt(10)\nQmvaa 0,64,80\nds_write_b128 v58, a[156:159] offset:36864\nQmvaa 0,68,108\nQa 10,10,106\nQa 11,11,107\n"
"Qmvaa 4,64,84\ns_waitcnt lgkmcnt(8)\nQa 12,12,136\nQa 13,13,137\nQmvaa 4,68,112\nds_write_b128 v58, a[160:163] offset:37888\nQmvaa 8,64,88\nQta 56,68,49664\n"
"Qa 14,14,138\nQa 15,15,139\nQmvav 8,68,168\nQdv 104,59,4096\ns_waitcnt lgkmcnt(8)\nQa 20,20,148\nQmvaa 12,64,92\nds_write_b128 v58, a[164:167] offset:38912\n"
"Qmvav 12,68,200\nQta 58,68,50688\nQa 21,21,149\nQmvaa 16,64,96\nQa 22,22,150\nQa 23,23,151\nQmvav 16,68,232\nQdv 136,59,6144\n"
"s_waitcnt lgkmcnt(8)\nQa 0,0,180\nQmvaa 20,64,100\nQta 60,68,57856\nQa 1,1,181\nQmvav 20,68,212\nds_write_b128 v58, a[168:171] offset:39936\nQmvaa 24,64,104\n"
"Qa 2,2,182\nQa 3,3,183\nQmvav 24,68,244\nQta 62,68,58880\ns_waitcnt lgkmcnt(6)\nQa 16,16,104\ns_waitcnt lgkmcnt(6)\nQmvav 0,72,80\n"
"Qdv 148,59,1024\nv_add_u32 v75, 1, v75\nQmvav 0,76,88\ns_waitcnt vmcnt(1)\nds_write_b32 v73, v75\nQmvav 4,72,112\nQa 17,17,105\nQa 18,18,106\n"
"Qmvav 4,76,120\nQa 19,19,107\ns_waitcnt lgkmcnt(5)\nQa 24,24,136\nQmvav 8,72,144\nQta 64,69,0\nQa 25,25,137\nQmvav 8,76,152\n"
"Qa 26,26,138\nQa 27,27,139\nQmvav 12,72,176\nQmvav 12,76,184\nQta 66,69,512\ns_waitcnt lgkmcnt(3)\nQa 4,4,148\nQmvav 16,72,208\n"
"Qa 5,5,149\nQa 6,6,150\nQmvav 16,76,216\nds_read_b32 v66, v74\nQa 7,7,151\nQmvav 20,72,240\nQta 68,69,1024\nQmvav 20,76,248\n"
"Qmvav 24,72,84\nQmvav 24,76,116\ns_waitcnt lgkmcnt(1)\nv_cmp_gt_u32 vcc, v75, v66\ns_waitcnt lgkmcnt(6)\nQmvaa 0,56,0\ns_cbranch_vccnz .Lso2088r0\n.Lro2088r0:\n"
"Qmvaa 0,60,28\nQta 70,69,1536\nQmvaa 4,56,4\nQmvaa 4,60,32\nQdv 180,59,33792\nQmvaa 8,56,8\nQta 72,69,8192\nQmvaa 8,60,36\n"
"Qmvaa 12,56,12\nQdv 104,59,34816\nQmvaa 12,60,40\nQta 74,69,8704\nQmvaa 16,56,16\nQmvaa 16,60,44\nQdv 136,59,35840\nQmvaa 20,56,20\n"
"Qta 76,69,9216\nQmvaa 20,60,48\ns_waitcnt lgkmcnt(5)\nQa 28,28,180\nQa 29,29,181\nQmvaa 24,56,24\nQdv 148,59,36864\nQa 30,30,182\n"
"Qmvaa 24,60,52\nQta 78,69,9728\nQa 31,31,183\ns_waitcnt lgkmcnt(11)\nQmaaa 172,64,80\ns_waitcnt lgkmcnt(5)\nQa 32,32,104\nQa 33,33,105\n"
"Qmaaa 172,68,108\nQa 34,34,106\nQa 35,35,107\nQmaaa 176,64,84\nQmaaa 176,68,112\ns_waitcnt lgkmcnt(3)\nQa 36,36,136\nQa 37,37,137\n"
"Qmaaa 180,64,88\nQta 56,69,16384\nQa 38,38,138\nQmaav 180,68,168\nQdv 180,59,37888\nQa 39,39,139\nQmaaa 184,64,92\ns_waitcnt lgkmcnt(3)\n"
"Qa 40,40,148\nQa 41,41,149\nQmaav 184,68,200\nQta 58,69,16896\nQa 42,42,150\nQmaaa 188,64,96\nQdv 104,59,38912\nQa 43,43,151\n"
"Qmaav 188,68,232\nQmaaa 192,64,100\nQta 60,69,17408\nQmaav 192,68,212\nQdv 136,59,39936\nQmaaa 196,64,104\ns_waitcnt lgkmcnt(4)\nQa 44,44,180\n"
"Qa 45,45,181\nQmaav 196,68,244\nQta 62,69,17920\nQa 46,46,182\ns_waitcnt lgkmcnt(5)\nQmaav 172,72,80\nQa 47,47,183\nQmaav 172,76,88\n"
".Lpg21:\ns_cbranch_execz .Lpg25\nQmaav 176,72,112\ns_waitcnt lgkmcnt(3)\nQa 48,48,104\nQa 49,49,105\nQmaav 176,76,120\nQa 50,50,106\n"
"Qa 51,51,107\nQmaav 180,72,144\nQta 64,68,17152\nQmaav 180,76,152\ns_waitcnt lgkmcnt(2)\nQa 52,52,136\nQa 53,53,137\nQmaav 184,72,176\n"
"Qa 54,54,138\nQa 55,55,139\nQmaav 184,76,184\nQta 66,68,18176\nQmaav 188,72,208\nQmaav 188,76,216\nQmaav 192,72,240\nQta 68,68,25344\n"
"Qmaav 192,76,248\nQmaav 196,72,84\nQmaav 196,76,116\nQta 70,68,26368\ns_waitcnt lgkmcnt(4)\nQmaaa 172,56,0\nQmaaa 172,60,28\nQmaaa 176,56,4\n"
"Qmaaa 176,60,32\nQmaaa 180,56,8\nQta 72,68,33536\nQmaaa 180,60,36\ns_nop 2\nds_write_b128 v58, a[172:175] offset:0\nQmaaa 184,56,12\nQmaaa 184,60,40\n"
"Qta 74,68,34560\nQmaaa 188,56,16\nds_write_b128 v58, a[176:179] offset:1024\nQmaaa 188,60,44\nQmaaa 192,56,20\nQta 76,68,41728\nQmaaa 192,60,48\nds_write_b128 v58, a[180:183] offset:2048\n"
"Qmaaa 196,56,24\nQmaaa 196,60,52\nQta 78,68,42752\ns_waitcnt lgkmcnt(7)\nQmvaa 28,64,80\nds_write_b128 v58, a[184:187] offset:3072\nQmvaa 28,68,108\nQmvaa 32,64,84\n"
"Qmvaa 32,68,112\nds_write_b128 v58, a[188:191] offset:4096\nQmvaa 36,64,88\nQta 56,68,49920\nQmvav 36,68,168\nQmvaa 40,64,92\nds_write_b128 v58, a[192:195] offset:5120\nQmvav 40,68,200\n"
"Qta 58,68,50944\nQmvaa 44,64,96\nQmvav 44,68,232\nds_write_b128 v58, a[196:199] offset:6144\nQmvaa 48,64,100\nQta 60,68,58112\nQmvav 48,68,212\nQmvaa 52,64,104\n"
"Qmvav 52,68,244\nQta 62,68,59136\ns_waitcnt lgkmcnt(8)\nQmvav 28,72,80\nv_add_u32 v75, 1, v75\nQmvav 28,76,88\nds_write_b32 v73, v75\nQmvav 32,72,112\n"
"Qmvav 32,76,120\nQmvav 36,72,144\nQta 64,69,256\nQmvav 36,76,152\nQmvav 40,72,176\nQmvav 40,76,184\nQta 66,69,768\nQmvav 44,72,208\n"
"Qmvav 44,76,216\nds_read_b32 v66, v74\nQmvav 48,72,240\nQta 68,69,1280\nQmvav 48,76,248\nQmvav 52,72,84\nQmvav 52,76,116\ns_waitcnt lgkmcnt(1)\n"
"v_cmp_gt_u32 vcc, v75, v66\ns_waitcnt lgkmcnt(5)\nQmvaa 28,56,0\ns_cbranch_vccnz .Lso2172r0\n.Lro2172r0:\nQmvaa 28,60,28\nbuffer_load_dword v78, v76, s[24:27], 0 offen sc1\nQmvaa 32,56,4\n"
"Qta 70,69,1792\nQmvaa 32,60,32\nQdv 148,59,2048\nQmvaa 36,56,8\nQmvaa 36,60,36\nQta 72,69,8448\nQmvaa 40,56,12\nQdv 180,59,3072\n"
"Qmvaa 40,60,40\nQmvaa 44,56,16\nQta 74,69,8960\nQmvaa 44,60,44\nQdv 104,59,5120\nQmvaa 48,56,20\nQmvaa 48,60,48\nQta 76,69,9472\n"
"s_waitcnt lgkmcnt(5)\nQa 156,156,148\nQmvaa 52,56,24\nQdv 136,59,0\nQa 157,157,149\nQmvaa 52,60,52\nQa 158,158,150\nQa 159,159,151\n"
"s_waitcnt lgkmcnt(10)\nQmvaa 92,64,80\nQta 78,69,9984\ns_waitcnt lgkmcnt(5)\nQa 188,188,180\nQmvaa 92,68,108\nQa 189,189,181\nQa 190,190,182\n"
"Qmvaa 124,64,84\nQa 191,191,183\nQmvaa 124,68,112\ns_waitcnt lgkmcnt(3)\nQa 252,252,104\nQa 253,253,105\nQmvaa 156,64,88\nQta 56,69,16640\n"
"Qa 254,254,106\nQmvav 156,68,168\nQdv 148,59,4096\nQa 255,255,107\nQmvaa 188,64,92\ns_waitcnt lgkmcnt(3)\nQa 92,92,136\nQa 93,93,137\n"
"Qmvav 188,68,200\nQta 58,69,17152\nQa 94,94,138\nQmvaa 220,64,96\nQa 95,95,139\nQmvav 220,68,232\nQdv 180,59,6144\nQmvaa 252,64,100\n"
"Qta 60,69,17664\nQmvav 252,68,212\nQmvaa 100,64,104\nQmvav 100,68,244\nQta 62,69,18176\ns_waitcnt lgkmcnt(4)\nQa 220,220,148\ns_waitcnt lgkmcnt(4)\n"
"Qmvav 92,72,80\nQdv 104,59,1024\nQa 221,221,149\nQmvav 92,76,88\nQa 222,222,150\nQa 223,223,151\nQmvav 124,72,112\nQmvav 124,76,120\n"
"s_waitcnt vmcnt(0)\nv_sub_u32 v66, v78, v65\nv_cmp_gt_u32 vcc, 1, v66\ns_and_b64 vcc, vcc, s[28:29]\ns_cbranch_scc1 .Lx0_w\n.Lx0_r:\nQmvav 156,72,144\ns_add_u32 s69, s98, 0\n"
"buffer_load_dwordx4 a[208:211], v79, s[24:27], s69 offen offset:0 sc1\nQmvav 156,76,152\ns_waitcnt lgkmcnt(3)\nQa 100,100,180\nQa 101,101,181\nQmvav 188,72,176\nbuffer_load_dwordx4 a[212:215], v79, s[24:27], s69 offen offset:1024 sc1\nQmvav 188,76,184\n"
"Qa 102,102,182\nQa 103,103,183\nQmvav 220,72,208\nbuffer_load_dwordx4 a[216:219], v79, s[24:27], s69 offen offset:2048 sc1\nv_add_u32 v75, 1, v75\nQmvav 220,76,216\nds_write_b32 v73, v75\ns_waitcnt lgkmcnt(1)\n"
"Qa 124,124,104\nQmvav 252,72,240\nbuffer_load_dwordx4 a[220:223], v79, s[24:27], s69 offen offset:3072 sc1\nQmvav 252,76,248\nQa 125,125,105\nQa 126,126,106\nQmvav 100,72,84\ns_add_u32 s69, s98, 4096\n"
"buffer_load_dwordx4 a[224:227], v79, s[24:27], s69 offen offset:0 sc1\nQmvav 100,76,116\nQa 127,127,107\ns_waitcnt lgkmcnt(2)\nQmvaa 92,56,0\nbuffer_load_dwordx4 a[228:231], v79, s[24:27], s69 offen offset:1024 sc1\nQmvaa 92,60,28\nQmvaa 124,56,4\n"
"buffer_load_dwordx4 a[232:235], v79, s[24:27], s69 offen offset:2048 sc1\nQmvaa 124,60,32\nds_read_b32 v66, v74\nQmvaa 156,56,8\nbuffer_load_dwordx4 a[236:239], v79, s[24:27], s69 offen offset:3072 sc1\nQmvaa 156,60,36\nQmvaa 188,56,12\ns_add_u32 s69, s98, 16384\n"
"buffer_load_dwordx4 a[240:243], v79, s[24:27], s69 offen offset:0 sc1\nQmvaa 188,60,40\ns_waitcnt lgkmcnt(0)\nv_cmp_gt_u32 vcc, v75, v66\nQmvaa 220,56,16\ns_cbranch_vccnz .Lso2222r0\n.Lro2222r0:\nQmvaa 220,60,44\n"
"buffer_load_dwordx4 a[244:247], v79, s[24:27], s69 offen offset:1024 sc1\nQmvaa 252,56,20\nQmvaa 252,60,48\nbuffer_load_dwordx4 a[248:251], v79, s[24:27], s69 offen offset:2048 sc1\nQmvaa 100,56,24\nQmvaa 100,60,52\ns_waitcnt vmcnt(10)\nQmvaa 92,202,208\n"
"Qmvaa 124,200,208\ns_waitcnt vmcnt(9)\nQmvaa 156,202,212\nQmvaa 188,200,212\ns_nop 3\nQk 92,92,93\nQk 93,94,95\nds_write_b64 v56, v[92:93] offset:64\n"
"s_waitcnt vmcnt(8)\nQmvaa 220,202,216\nQk 124,124,125\nQk 125,126,127\nds_write_b64 v56, v[124:125] offset:3392\nQmvaa 252,200,216\nQk 156,156,157\nQk 157,158,159\n"
"ds_write_b64 v56, v[156:157] offset:6720\ns_waitcnt vmcnt(7)\nQmvaa 100,202,220\nQk 188,188,189\nQk 189,190,191\nds_write_b64 v56, v[188:189] offset:10048\nQmvaa 0,200,220\nQk 220,220,221\n"
"Qk 221,222,223\nds_write_b64 v56, v[220:221] offset:13376\ns_waitcnt vmcnt(6)\nQmvaa 4,202,224\nQk 252,252,253\nQk 253,254,255\nds_write_b64 v56, v[252:253] offset:16704\nQmvaa 8,200,224\n"
"Qk 100,100,101\nQk 101,102,103\nds_write_b64 v56, v[100:101] offset:20032\ns_waitcnt vmcnt(5)\nQmvaa 12,202,228\nQk 0,0,1\nQk 1,2,3\nds_write_b64 v56, v[0:1] offset:0\n"
"Qmvaa 16,200,228\nQk 4,4,5\nQk 5,6,7\nds_write_b64 v56, v[4:5] offset:3328\ns_waitcnt vmcnt(4)\nQmvaa 20,202,232\nQk 8,8,9\nQk 9,10,11\n"
"ds_write_b64 v56, v[8:9] offset:6656\nQmvaa 24,200,232\nQk 12,12,13\nQk 13,14,15\nds_write_b64 v56, v[12:13] offset:9984\ns_waitcnt vmcnt(3)\nQmvaa 28,202,236\nQk 16,16,17\n"
"Qk 17,18,19\nds_write_b64 v56, v[16:17] offset:13312\nQmvaa 32,200,236\nQk 20,20,21\nQk 21,22,23\nds_write_b64 v56, v[20:21] offset:16640\ns_waitcnt vmcnt(2)\nQmvaa 36,202,240\n"
"Qk 24,24,25\nQk 25,26,27\nds_write_b64 v56, v[24:25] offset:19968\nQmvaa 40,200,240\nQk 28,28,29\nQk 29,30,31\nds_write_b64 v56, v[28:29] offset:32\ns_waitcnt vmcnt(1)\n"
"Qmvaa 44,202,244\nQk 32,32,33\nQk 33,34,35\nds_write_b64 v56, v[32:33] offset:3360\nQmvaa 48,200,244\nQk 36,36,37\nQk 37,38,39\nds_write_b64 v56, v[36:37] offset:6688\n"
"s_waitcnt vmcnt(0)\nQmvaa 52,202,248\nQk 40,40,41\nQk 41,42,43\nds_write_b64 v56, v[40:41] offset:10016\nQk 44,44,45\nQk 45,46,47\nds_write_b64 v56, v[44:45] offset:13344\n"
"Qk 48,48,49\nQk 49,50,51\nds_write_b64 v56, v[48:49] offset:16672\nQk 52,52,53\nQk 53,54,55\nds_write_b64 v56, v[52:53] offset:20000\ns_waitcnt lgkmcnt(0)\ns_barrier\n"
"Qda 0,57,0\nQda 4,57,3328\nQda 8,57,6656\nQda 12,57,9984\nQda 16,57,13312\nQda 20,57,16640\nQda 24,57,19968\nQda 28,71,64\n"
"Qda 32,71,6720\nQda 36,71,13376\nQda 40,71,20032\ns_mov_b32 s68, 0\ns_waitcnt lgkmcnt(10)\nbuffer_store_dwordx4 a[0:3], v64, s[40:43], s68 offen offset:0 sc1\ns_mov_b32 s68, 40960\ns_waitcnt lgkmcnt(9)\n"
"buffer_store_dwordx4 a[4:7], v64, s[40:43], s68 offen offset:0 sc1\ns_mov_b32 s68, 81920\ns_waitcnt lgkmcnt(8)\nbuffer_store_dwordx4 a[8:11], v64, s[40:43], s68 offen offset:0 sc1\ns_mov_b32 s68, 122880\ns_waitcnt lgkmcnt(7)\nbuffer_store_dwordx4 a[12:15], v64, s[40:43], s68 offen offset:0 sc1\ns_mov_b32 s68, 163840\n"
"s_waitcnt lgkmcnt(6)\nbuffer_store_dwordx4 a[16:19], v64, s[40:43], s68 offen offset:0 sc1\ns_mov_b32 s68, 204800\ns_waitcnt lgkmcnt(5)\nbuffer_store_dwordx4 a[20:23], v64, s[40:43], s68 offen offset:0 sc1\ns_mov_b32 s68, 245760\ns_waitcnt lgkmcnt(4)\nbuffer_store_dwordx4 a[24:27], v64, s[40:43], s68 offen offset:0 sc1\n"
"s_mov_b32 s68, 0\ns_waitcnt lgkmcnt(3)\nbuffer_store_dwordx4 a[28:31], v72, s[40:43], s68 offen offset:0 sc1\ns_mov_b32 s68, 81920\ns_waitcnt lgkmcnt(2)\nbuffer_store_dwordx4 a[32:35], v72, s[40:43], s68 offen offset:0 sc1\ns_mov_b32 s68, 163840\ns_waitcnt lgkmcnt(1)\n"
"buffer_store_dwordx4 a[36:39], v72, s[40:43], s68 offen offset:0 sc1\ns_mov_b32 s68, 245760\ns_cmp_eq_u32 s93, 1\ns_cselect_b32 s68, 0x7ffff000, s68\ns_waitcnt lgkmcnt(0)\nbuffer_store_dwordx4 a[40:43], v72, s[40:43], s68 offen offset:0 sc1\ns_branch .Lfin\n.Lrole1:\n"
".Lpg22:\ns_cbranch_execz .Lpg26\nQmvaa 96,64,88\nQta 56,69,49408\ns_lshl_b32 s46, s19, 4\nQmvav 96,68,168\nbuffer_load_dwordx4 a[84:87], v79, s[48:51], s31 offen offset:1024 sc1\nQmvaa 128,64,92\n"
"ds_read_b32 v66, v74\ns_add_u32 s46, s46, 128\nQmvav 128,68,200\nQta 58,69,50432\nv_mov_b32 v77, 0x7ffff000\nQmvaa 160,64,96\nQmvav 160,68,232\nbuffer_load_dwordx4 a[88:91], v79, s[48:51], s31 offen offset:2048 sc1\n"
"Qmvaa 192,64,100\ns_waitcnt lgkmcnt(1)\nv_cmp_gt_u32 vcc, v75, v66\nQmvav 192,68,212\ns_cbranch_vccnz .Lso1525r1\n.Lro1525r1:\nQmvaa 224,64,104\nQta 60,69,57600\n"
"Qmvav 224,68,244\nbuffer_load_dwordx4 a[92:95], v79, s[48:51], s31 offen offset:3072 sc1\ns_waitcnt vmcnt(31)\ns_waitcnt lgkmcnt(4)\nQmaav 140,72,84\nQmaav 140,76,116\nQta 62,69,58624\nQmaav 136,72,240\n"
"Qmaav 136,76,248\nQmaav 132,72,208\nQta 64,69,49664\ns_add_u32 m0, s96, 65536\ns_add_u32 s95, s67, 86016\nQmaav 132,76,216\nbuffer_load_dwordx4 v62, s[52:55], s95 offen lds\nQmaav 128,72,176\n"
"Qmaav 128,76,184\nQta 66,69,50688\nQmaav 124,72,144\nQmaav 124,76,152\nQmaav 120,72,112\nQta 68,69,57856\ns_add_u32 s95, s67, 125952\nQmaav 120,76,120\n"
"buffer_load_dwordx4 v62, s[52:55], s95 offen offset:1024 lds\nQmaav 116,72,80\nQmaav 116,76,88\nQta 70,69,58880\ns_waitcnt lgkmcnt(4)\nQmaav 144,56,80\nQmaav 144,60,88\ns_add_u32 m0, s96, 73728\n"
"s_add_u32 s95, s72, 86016\nQmaav 148,56,112\nbuffer_load_dwordx4 v62, s[52:55], s95 offen lds\nQmaav 148,60,120\nQmaav 152,56,144\nQta 72,69,49920\nQmaav 152,60,152\nQmaav 156,56,176\n"
"Qmaav 156,60,184\nQta 74,69,50944\ns_add_u32 s95, s72, 125952\nQmaav 160,56,208\nbuffer_load_dwordx4 v62, s[52:55], s95 offen offset:1024 lds\nQmaav 160,60,216\nQmaav 164,56,240\nQta 76,69,58112\n"
"Qmaav 164,60,248\nQmaav 168,56,84\nQmaav 168,60,116\nQta 78,69,59136\ns_add_u32 m0, s96, 81920\ns_add_u32 s95, s73, 86016\ns_waitcnt lgkmcnt(4)\nQmvav 24,64,84\n"
"buffer_load_dwordx4 v62, s[52:55], s95 offen lds\nQmvav 24,68,116\nQmvav 20,64,240\nQmvav 20,68,248\nQmvav 16,64,208\nQta 56,68,0\ns_add_u32 s95, s73, 125952\nQmvav 16,68,216\n"
"buffer_load_dwordx4 v62, s[52:55], s95 offen offset:1024 lds\nQmvav 12,64,176\nQmvav 12,68,184\nQta 58,68,512\nQmvav 8,64,144\nQmvav 8,68,152\nQmvav 4,64,112\nQta 60,68,1024\n"
"s_add_u32 m0, s96, 90112\ns_add_u32 s95, s67, 86016\nQmvav 4,68,120\nbuffer_load_dwordx4 v61, s[52:55], s95 offen lds\nQmvav 0,64,80\nQmvav 0,68,88\nQta 62,68,1536\ns_waitcnt lgkmcnt(4)\n"
"Qmvav 28,72,80\nQmvav 28,76,88\ns_add_u32 s95, s67, 105472\nQmvav 32,72,112\nbuffer_load_dwordx4 v61, s[52:55], s95 offen offset:1024 lds\nQmvav 32,76,120\nQmvav 36,72,144\nQta 64,68,256\n"
"Qmvav 36,76,152\ns_add_u32 s31, s74, 135168\nQmvav 40,72,176\nbuffer_load_dwordx4 a[96:99], v79, s[48:51], s31 offen offset:0 sc1\nQmvav 40,76,184\nQta 66,68,768\nQmvav 44,72,208\nv_cmp_eq_u32 s[44:45], s46, v60\n"
"s_nop 1\nv_cndmask_b32 v77, v77, v76, s[44:45]\nv_add_u32 v67, 1, v65\ns_xor_b32 s46, s19, 12\ns_lshl_b32 s28, 1, s46\nQmvav 44,76,216\nQmvav 48,72,240\nQta 68,68,1280\n"
"Qmvav 48,76,248\nbuffer_load_dwordx4 a[100:103], v79, s[48:51], s31 offen offset:1024 sc1\nv_add_u32 v75, 1, v75\nQmvav 52,72,84\ns_waitcnt vmcnt(35)\nds_write_b32 v73, v75\nQmvav 52,76,116\nQta 70,68,1792\n"
"s_waitcnt lgkmcnt(5)\nQmaav 196,56,84\nQmaav 196,60,116\nbuffer_load_dwordx4 a[104:107], v79, s[48:51], s31 offen offset:2048 sc1\nQmaav 192,56,240\nQmaav 192,60,248\nQmaav 188,56,208\nQta 72,68,8192\n"
"s_add_u32 s31, s74, 147456\nQmaav 188,60,216\nbuffer_load_dwordx4 a[108:111], v79, s[48:51], s31 offen offset:0 sc1\nQmaav 184,56,176\nds_read_b32 v66, v74\nQmaav 184,60,184\nQta 74,68,9216\nQmaav 180,56,144\n"
"Qmaav 180,60,152\nbuffer_load_dwordx4 a[112:115], v79, s[48:51], s31 offen offset:1024 sc1\nQmaav 176,56,112\ns_waitcnt lgkmcnt(1)\nv_cmp_gt_u32 vcc, v75, v66\nQmaav 176,60,120\ns_cbranch_vccnz .Lso1595r1\n.Lro1595r1:\n"
"Qmaav 172,56,80\nQta 76,68,16384\nQmaav 172,60,88\nbuffer_load_dwordx4 v[168:171], v79, s[48:51], s31 offen offset:2048 sc1\ns_waitcnt lgkmcnt(4)\nQmvav 92,64,80\nQmvav 92,68,88\nQta 78,68,17408\n"
"Qmvav 124,64,112\nQmvav 124,68,120\nbuffer_load_dwordx4 v[200:203], v79, s[48:51], s31 offen offset:3072 sc1\nQmvav 156,64,144\nQta 56,68,8448\nQmvav 156,68,152\nQmvav 188,64,176\nQmvav 188,68,184\n"
"Qta 58,68,9472\ns_add_u32 s31, s74, 151552\nQmvav 220,64,208\nbuffer_load_dwordx4 v[232:235], v79, s[48:51], s31 offen offset:0 sc1\nQmvav 220,68,216\nQmvav 252,64,240\nQta 60,68,16640\nQmvav 252,68,248\n"
"Qmvav 100,64,84\nbuffer_load_dwordx4 v[212:215], v79, s[48:51], s31 offen offset:1024 sc1\nQmvav 100,68,116\nQta 62,68,17664\ns_waitcnt lgkmcnt(4)\nQmaav 224,72,84\nQmaav 224,76,116\nQmaav 220,72,240\n"
"buffer_load_dwordx4 v[244:247], v79, s[48:51], s31 offen offset:2048 sc1\nQmaav 220,76,248\nQmaav 216,72,208\n.Lpg23:\ns_cbranch_execz .Ltr8\nQta 64,68,8704\nQmaav 216,76,216\ns_add_u32 m0, s91, 0\n"
"s_add_u32 s31, s77, 131072\nQmaav 212,72,176\nbuffer_load_dwordx4 v79, s[48:51], s31 offen sc1 lds\nQmaav 212,76,184\nQta 66,68,9728\nQmaav 208,72,144\nQmaav 208,76,152\nQmaav 204,72,112\n"
"Qta 68,68,16896\nQmaav 204,76,120\nbuffer_load_dwordx4 v79, s[48:51], s31 offen offset:1024 sc1 lds\nQmaav 200,72,80\nQmaav 200,76,88\nQta 70,68,17920\ns_waitcnt lgkmcnt(4)\nQmaav 228,56,80\n"
"Qmaav 228,60,88\nbuffer_load_dwordx4 v79, s[48:51], s31 offen offset:2048 sc1 lds\nQmaav 232,56,112\nQmaav 232,60,120\nQmaav 236,56,144\nQta 72,68,8960\nQmaav 236,60,152\nbuffer_load_dwordx4 v79, s[48:51], s31 offen offset:3072 sc1 lds\n"
"Qmaav 240,56,176\nQmaav 240,60,184\nQta 74,68,9984\nQmaav 244,56,208\ns_add_u32 m0, s91, 4096\ns_add_u32 s31, s77, 135168\nQmaav 244,60,216\nbuffer_load_dwordx4 v79, s[48:51], s31 offen sc1 lds\n"
"Qmaav 248,56,240\nQta 76,68,17152\nQmaav 248,60,248\nQmaav 252,56,84\nQmaav 252,60,116\nQta 78,68,18176\ns_waitcnt lgkmcnt(4)\nQmvav 172,64,84\n"
"buffer_load_dwordx4 v79, s[48:51], s31 offen offset:1024 sc1 lds\nQmvav 172,68,116\nQmvav 140,64,240\nQmvav 140,68,248\nQmvav 108,64,208\nQta 56,68,24576\nQmvav 108,68,216\nbuffer_load_dwordx4 v79, s[48:51], s31 offen offset:2048 sc1 lds\n"
"Qmvav 228,64,176\nQmvav 228,68,184\nQta 58,68,25600\nQmvav 196,64,144\ns_add_u32 m0, s91, 33792\ns_add_u32 s31, s77, 147456\nQmvav 196,68,152\nbuffer_load_dwordx4 v79, s[48:51], s31 offen sc1 lds\n"
"Qmvav 164,64,112\nQta 60,68,32768\nQmvav 164,68,120\nv_add_u32 v75, 1, v75\nQmvav 132,64,80\ns_waitcnt vmcnt(47)\nds_write_b32 v73, v75\nQmvav 132,68,88\n"
"Qta 62,68,33792\ns_waitcnt lgkmcnt(5)\nQmvav 204,72,80\nbuffer_load_dwordx4 v79, s[48:51], s31 offen offset:1024 sc1 lds\nQmvav 204,76,88\nQmvav 236,72,112\nQmvav 236,76,120\nQmvav 96,72,144\n"
"Qta 64,68,24832\nQmvav 96,76,152\nbuffer_load_dwordx4 v79, s[48:51], s31 offen offset:2048 sc1 lds\nQmvav 128,72,176\nds_read_b32 v66, v74\nQmvav 128,76,184\nQta 66,68,25856\nQmvav 160,72,208\n"
"Qmvav 160,76,216\nbuffer_load_dwordx4 v79, s[48:51], s31 offen offset:3072 sc1 lds\nQmvav 192,72,240\ns_waitcnt lgkmcnt(1)\nv_cmp_gt_u32 vcc, v75, v66\nQmvav 192,76,248\ns_cbranch_vccnz .Lso1665r1\n.Lro1665r1:\n"
"Qmvav 224,72,84\nQta 68,68,33024\ns_add_u32 m0, s91, 37888\ns_add_u32 s31, s77, 151552\nQmvav 224,76,116\nbuffer_load_dwordx4 v79, s[48:51], s31 offen sc1 lds\ns_waitcnt vmcnt(36)\ns_waitcnt lgkmcnt(4)\n"
"Qmaaa 140,56,24\nQmaaa 140,60,52\nQta 70,68,34048\nQmaaa 136,56,20\nQmaaa 136,60,48\nbuffer_load_dwordx4 v79, s[48:51], s31 offen offset:1024 sc1 lds\nQmaaa 132,56,16\nQta 72,68,25088\n"
"Qmaaa 132,60,44\nQmaaa 128,56,12\nQmaaa 128,60,40\nQta 74,68,26112\nQmaaa 124,56,8\nbuffer_load_dwordx4 v79, s[48:51], s31 offen offset:2048 sc1 lds\nQmaaa 124,60,36\nQmaaa 120,56,4\n"
"Qta 76,68,33280\nQmaaa 120,60,32\nQmaaa 116,56,0\nQmaaa 116,60,28\nQta 78,68,34304\ns_add_u32 m0, s96, 0\ns_add_u32 s95, s67, 126976\ns_waitcnt lgkmcnt(4)\n"
"Qmaaa 144,64,0\nbuffer_load_dwordx4 v61, s[52:55], s95 offen lds\nQmaaa 144,68,28\nQmaaa 148,64,4\nQmaaa 148,68,32\nQmaaa 152,64,8\nQta 56,68,25344\ns_add_u32 s95, s67, 146432\n"
"Qmaaa 152,68,36\nbuffer_load_dwordx4 v61, s[52:55], s95 offen offset:1024 lds\nQmaaa 156,64,12\nQmaaa 156,68,40\nQta 58,68,26368\nQmaaa 160,64,16\ns_add_u32 s31, s75, 131072\nQmaaa 160,68,44\n"
"buffer_load_dwordx4 v[80:83], v79, s[48:51], s31 offen offset:0 sc1\nQmaaa 164,64,20\nQta 60,68,33536\nQmaaa 164,68,48\nQmaaa 168,64,24\nQmaaa 168,68,52\nQta 62,68,34560\ns_waitcnt lgkmcnt(4)\n"
"Qmvaa 24,72,24\nbuffer_load_dwordx4 v[112:115], v79, s[48:51], s31 offen offset:1024 sc1\nQmvaa 24,76,52\nQmvaa 20,72,20\nQmvaa 20,76,48\nQmvaa 16,72,16\nQta 64,68,40960\nQmvaa 16,76,44\n"
"buffer_load_dwordx4 v[144:147], v79, s[48:51], s31 offen offset:2048 sc1\nQmvaa 12,72,12\nQmvaa 12,76,40\nQta 66,68,41472\nQmvaa 8,72,8\nQmvaa 8,76,36\nbuffer_load_dwordx4 v[176:179], v79, s[48:51], s31 offen offset:3072 sc1\nQmvaa 4,72,4\n"
"Qta 68,68,41984\nQmvaa 4,76,32\nQmvaa 0,72,0\nQmvaa 0,76,28\nQta 70,68,42496\ns_add_u32 s31, s75, 135168\ns_waitcnt lgkmcnt(4)\nQmvaa 28,56,0\n"
"buffer_load_dwordx4 v[208:211], v79, s[48:51], s31 offen offset:0 sc1\nQmvaa 28,60,28\nQmvaa 32,56,4\nQmvaa 32,60,32\nQmvaa 36,56,8\nQta 72,68,41216\nQmvaa 36,60,36\nbuffer_load_dwordx4 v[240:243], v79, s[48:51], s31 offen offset:1024 sc1\n"
"Qmvaa 40,56,12\nQmvaa 40,60,40\nQta 74,68,41728\nQmvaa 44,56,16\nQmvaa 44,60,44\nbuffer_load_dwordx4 v[84:87], v79, s[48:51], s31 offen offset:2048 sc1\nQmvaa 48,56,20\nQta 76,68,42240\n"
"Qmvaa 48,60,48\nv_add_u32 v75, 1, v75\nQmvaa 52,56,24\ns_waitcnt vmcnt(37)\nds_write_b32 v73, v75\nQmvaa 52,60,52\nQta 78,68,42752\ns_add_u32 s31, s75, 147456\n"
"s_waitcnt lgkmcnt(5)\nQmaaa 196,64,24\nbuffer_load_dwordx4 v[88:91], v79, s[48:51], s31 offen offset:0 sc1\nQmaaa 196,68,52\nQmaaa 192,64,20\nQmaaa 192,68,48\nQmaaa 188,64,16\nQta 56,68,49152\n"
"Qmaaa 188,68,44\nbuffer_load_dwordx4 v[120:123], v79, s[48:51], s31 offen offset:1024 sc1\nQmaaa 184,64,12\nds_read_b32 v66, v74\nQmaaa 184,68,40\nQta 58,68,50176\nQmaaa 180,64,8\nQmaaa 180,68,36\n"
"buffer_load_dwordx4 v[152:155], v79, s[48:51], s31 offen offset:2048 sc1\nQmaaa 176,64,4\ns_waitcnt lgkmcnt(1)\nv_cmp_gt_u32 vcc, v75, v66\nQmaaa 176,68,32\ns_cbranch_vccnz .Lso1735r1\n.Lro1735r1:\nQmaaa 172,64,0\n"
"Qta 60,68,57344\nQmaaa 172,68,28\nbuffer_load_dwordx4 v[184:187], v79, s[48:51], s31 offen offset:3072 sc1\ns_waitcnt lgkmcnt(4)\nQmvaa 92,72,0\nQmvaa 92,76,28\nQta 62,68,58368\nQmvaa 124,72,4\n"
"s_add_u32 s31, s75, 151552\nQmvaa 124,76,32\nbuffer_load_dwordx4 v[216:219], v79, s[48:51], s31 offen offset:0 sc1\nQmvaa 156,72,8\nQta 64,68,49408\nQmvaa 156,76,36\nQmvaa 188,72,12\nQmvaa 188,76,40\n"
"Qta 66,68,50432\nQmvaa 220,72,16\nbuffer_load_dwordx4 v[248:251], v79, s[48:51], s31 offen offset:1024 sc1\nQmvaa 220,76,44\nQmvaa 252,72,20\nQta 68,68,57600\nQmvaa 252,76,48\nQmvaa 100,72,24\n"
"buffer_load_dwordx4 v[116:119], v79, s[48:51], s31 offen offset:2048 sc1\nQmvaa 100,76,52\nQta 70,68,58624\ns_waitcnt lgkmcnt(4)\nQmaaa 224,56,24\nQmaaa 224,60,52\nQmaaa 220,56,20\ns_add_u32 m0, s96, 8192\n"
"s_add_u32 s95, s72, 86016\nQmaaa 220,60,48\nbuffer_load_dwordx4 v61, s[52:55], s95 offen lds\nQmaaa 216,56,16\nQta 72,68,49664\nQmaaa 216,60,44\nQmaaa 212,56,12\nQmaaa 212,60,40\n"
"Qta 74,68,50688\ns_add_u32 s95, s72, 105472\nQmaaa 208,56,8\nbuffer_load_dwordx4 v61, s[52:55], s95 offen offset:1024 lds\nQmaaa 208,60,36\nQmaaa 204,56,4\nQta 76,68,57856\nQmaaa 204,60,32\n"
"Qmaaa 200,56,0\nQmaaa 200,60,28\nQta 78,68,58880\ns_add_u32 m0, s96, 16384\ns_add_u32 s95, s72, 126976\ns_waitcnt lgkmcnt(4)\nQmaaa 228,64,0\nbuffer_load_dwordx4 v61, s[52:55], s95 offen lds\n"
"Qmaaa 228,68,28\nQmaaa 232,64,4\nQmaaa 232,68,32\nQmaaa 236,64,8\nQta 56,68,49920\ns_add_u32 s95, s72, 146432\nQmaaa 236,68,36\nbuffer_load_dwordx4 v61, s[52:55], s95 offen offset:1024 lds\n"
"Qmaaa 240,64,12\nQmaaa 240,68,40\nQta 58,68,50944\nQmaaa 244,64,16\nQmaaa 244,68,44\nQmaaa 248,64,20\nQta 60,68,58112\ns_add_u32 m0, s96, 24576\n"
"s_add_u32 s95, s73, 86016\nQmaaa 248,68,48\nbuffer_load_dwordx4 v61, s[52:55], s95 offen lds\nQmaaa 252,64,24\nQmaaa 252,68,52\nQta 62,68,59136\ns_waitcnt lgkmcnt(4)\nQmvaa 172,72,24\n"
"Qmvaa 172,76,52\ns_add_u32 s95, s73, 105472\nQmvaa 140,72,20\nbuffer_load_dwordx4 v61, s[52:55], s95 offen offset:1024 lds\nQmvaa 140,76,48\nQmvaa 108,72,16\nQta 64,69,0\nQmvaa 108,76,44\n"
"Qmvaa 228,72,12\nQmvaa 228,76,40\nQta 66,69,512\ns_add_u32 m0, s96, 32768\ns_add_u32 s95, s73, 126976\nQmvaa 196,72,8\nbuffer_load_dwordx4 v61, s[52:55], s95 offen lds\nQmvaa 196,76,36\n"
"Qmvaa 164,72,4\nQta 68,69,1024\nQmvaa 164,76,32\nv_add_u32 v75, 1, v75\nQmvaa 132,72,0\ns_waitcnt vmcnt(21)\nds_write_b32 v73, v75\nQmvaa 132,76,28\n"
"Qta 70,69,1536\ns_add_u32 s95, s73, 146432\ns_waitcnt lgkmcnt(5)\nQmvaa 204,56,0\nbuffer_load_dwordx4 v61, s[52:55], s95 offen offset:1024 lds\nQmvaa 204,60,28\ns_waitcnt vmcnt(24)\nQda 0,58,0\n"
"Qmvaa 236,56,4\nQmvaa 236,60,32\nQda 28,58,33792\nQmvaa 96,56,8\nQta 72,69,8192\nQmvaa 96,60,36\nQda 4,58,1024\nQmvaa 128,56,12\n"
"ds_read_b32 v66, v74\nQmvaa 128,60,40\nQta 74,69,8704\nQmvaa 160,56,16\nQda 32,58,34816\nQmvaa 160,60,44\nQmvaa 192,56,20\ns_waitcnt lgkmcnt(2)\n"
"v_cmp_gt_u32 vcc, v75, v66\nQmvaa 192,60,48\ns_cbranch_vccnz .Lso1805r1\n.Lro1805r1:\nQmvaa 224,56,24\nQta 76,69,9216\ns_add_u32 m0, s96, 40960\ns_add_u32 s95, s67, 86016\n"
"Qmvaa 224,60,52\nbuffer_load_dwordx4 v63, s[52:55], s95 offen lds\ns_waitcnt vmcnt(39)\ns_waitcnt lgkmcnt(8)\nQmaaa 172,64,80\nQda 8,58,2048\nQmaaa 172,68,108\nQta 78,69,9728\n"
"Qmaaa 176,64,84\nQda 36,58,35840\nQmaaa 176,68,112\nQmaaa 180,64,88\nQta 56,69,16384\ns_add_u32 s95, s67, 105472\nQmaav 180,68,168\nbuffer_load_dwordx4 v63, s[52:55], s95 offen offset:1024 lds\n"
"Qmaaa 184,64,92\nQda 12,58,3072\nQmaav 184,68,200\nQta 58,69,16896\nQmaaa 188,64,96\nQda 40,58,36864\nQmaav 188,68,232\nQmaaa 192,64,100\n"
"Qta 60,69,17408\ns_add_u32 m0, s96, 49152\ns_add_u32 s95, s67, 126976\nQmaav 192,68,212\nbuffer_load_dwordx4 v63, s[52:55], s95 offen lds\nQmaaa 196,64,104\nQda 16,58,4096\nQmaav 196,68,244\n"
"Qta 62,69,17920\ns_waitcnt vmcnt(11)\ns_waitcnt lgkmcnt(8)\nQmaav 172,72,80\nQda 44,58,37888\nQmaav 172,76,88\ns_add_u32 s95, s67, 146432\nQmaav 176,72,112\n"
"buffer_load_dwordx4 v63, s[52:55], s95 offen offset:1024 lds\nQmaav 176,76,120\nQda 20,58,5120\nQmaav 180,72,144\nQta 64,69,24576\nv_add_u32 v75, 1, v75\nQmaav 180,76,152\ns_waitcnt vmcnt(4)\n"
"ds_write_b32 v73, v75\nQmaav 184,72,176\nQda 48,58,38912\nQmaav 184,76,184\nQta 66,69,25600\ns_add_u32 m0, s96, 57344\ns_add_u32 s95, s72, 86016\nQmaav 188,72,208\n"
"buffer_load_dwordx4 v63, s[52:55], s95 offen lds\nQmaav 188,76,216\nQda 24,58,6144\nQmaav 192,72,240\nQta 68,68,0\nQmaav 192,76,248\nQda 52,58,39936\nQmaav 196,72,84\n"
"Qmaav 196,76,116\nds_read_b32 v66, v74\ns_waitcnt lgkmcnt(10)\nQmaaa 172,56,0\nQta 70,68,1024\ns_add_u32 s95, s72, 105472\nQmaaa 172,60,28\nbuffer_load_dwordx4 v63, s[52:55], s95 offen offset:1024 lds\n"
"Qmaaa 176,56,4\nQmaaa 176,60,32\ns_waitcnt lgkmcnt(1)\nv_cmp_gt_u32 vcc, v75, v66\ns_waitcnt lgkmcnt(11)\nQmaaa 180,56,8\ns_cbranch_vccnz .Lso1840r1\n.Lro1840r1:\n"
"Qmaaa 180,60,36\nQta 72,68,8192\nQmaaa 184,56,12\nds_write_b128 v58, a[172:175] offset:0\nQmaaa 184,60,40\ns_waitcnt lgkmcnt(4)\nQmaaa 188,56,16\nQta 74,68,9216\n"
"Qmaaa 188,60,44\nds_write_b128 v58, a[176:179] offset:1024\nQmaaa 192,56,20\nQmaaa 192,60,48\nQta 76,68,16384\nQmaaa 196,56,24\nds_write_b128 v58, a[180:183] offset:2048\nQmaaa 196,60,52\n"
"s_waitcnt lgkmcnt(6)\nQmaaa 116,64,80\nQta 78,68,17408\nQmaaa 116,68,108\nds_write_b128 v58, a[184:187] offset:3072\nQmaaa 120,64,84\nQmaaa 120,68,112\nQmaaa 124,64,88\n"
"Qta 56,68,24576\nQmaav 124,68,168\nds_write_b128 v58, a[188:191] offset:4096\nQmaaa 128,64,92\nQmaav 128,68,200\nQta 58,68,25600\nQmaaa 132,64,96\nds_write_b128 v58, a[192:195] offset:5120\n"
"Qmaav 132,68,232\nQmaaa 136,64,100\nQta 60,68,32768\nQmaav 136,68,212\nds_write_b128 v58, a[196:199] offset:6144\nQmaaa 140,64,104\nQmaav 140,68,244\nQta 62,68,33792\n"
"s_waitcnt lgkmcnt(8)\nQmaav 116,72,80\nv_add_u32 v75, 1, v75\nQmaav 116,76,88\nds_write_b32 v73, v75\nQmaav 120,72,112\nQmaav 120,76,120\nQmaav 124,72,144\n"
"Qta 64,69,256\nQmaav 124,76,152\nQmaav 128,72,176\nQmaav 128,76,184\nQta 66,69,768\nQmaav 132,72,208\nQmaav 132,76,216\nds_read_b32 v66, v74\n"
"Qmaav 136,72,240\nQta 68,69,1280\nQmaav 136,76,248\nQmaav 140,72,84\nQmaav 140,76,116\ns_waitcnt lgkmcnt(1)\nv_cmp_gt_u32 vcc, v75, v66\ns_waitcnt lgkmcnt(5)\n"
"Qmaaa 116,56,0\ns_cbranch_vccnz .Lso1878r1\n.Lro1878r1:\nQmaaa 116,60,28\nQta 70,69,1792\nQmaaa 120,56,4\nQmaaa 120,60,32\nQdv 148,59,2048\n"
"Qmaaa 124,56,8\nQta 72,69,8448\nQmaaa 124,60,36\ns_nop 0\nds_write_b128 v58, a[116:119] offset:33792\nQmaaa 128,56,12\nQdv 180,59,3072\nQmaaa 128,60,40\n"
"Qta 74,69,8960\nQmaaa 132,56,16\nds_write_b128 v58, a[120:123] offset:34816\nQmaaa 132,60,44\nQdv 104,59,5120\nQmaaa 136,56,20\nQta 76,69,9472\nQmaaa 136,60,48\n"
"ds_write_b128 v58, a[124:127] offset:35840\nQmaaa 140,56,24\nQdv 136,59,0\ns_waitcnt lgkmcnt(9)\nQa 156,156,148\nQmaaa 140,60,52\nQta 78,69,9984\nQa 157,157,149\n"
"s_waitcnt lgkmcnt(10)\nQmvaa 92,64,80\nds_write_b128 v58, a[128:131] offset:36864\nQmvaa 92,68,108\nQa 158,158,150\nQa 159,159,151\nQmvaa 124,64,84\ns_waitcnt lgkmcnt(8)\n"
"Qa 188,188,180\nQa 189,189,181\nQmvaa 124,68,112\nds_write_b128 v58, a[132:135] offset:37888\nQmvaa 156,64,88\nQta 56,69,16640\nQa 190,190,182\nQa 191,191,183\n"
"Qmvav 156,68,168\n.Lpg24:\ns_cbranch_execz .Ltr8\nQdv 148,59,4096\ns_waitcnt lgkmcnt(8)\nQa 252,252,104\nQmvaa 188,64,92\nds_write_b128 v58, a[136:139] offset:38912\n"
"Qmvav 188,68,200\nQta 58,69,17152\nQa 253,253,105\nQmvaa 220,64,96\nQa 254,254,106\nQa 255,255,107\nQmvav 220,68,232\nQdv 180,59,6144\n"
"s_waitcnt lgkmcnt(8)\nQa 92,92,136\nQmvaa 252,64,100\nQta 60,69,17664\nQa 93,93,137\nQmvav 252,68,212\nds_write_b128 v58, a[140:143] offset:39936\nQmvaa 100,64,104\n"
"Qa 94,94,138\nQa 95,95,139\nQmvav 100,68,244\nQta 62,69,18176\ns_waitcnt lgkmcnt(6)\nQa 220,220,148\ns_waitcnt lgkmcnt(6)\nQmvav 92,72,80\n"
"Qdv 104,59,1024\nv_add_u32 v75, 1, v75\nQmvav 92,76,88\nds_write_b32 v73, v75\nQa 221,221,149\nQmvav 124,72,112\nQa 222,222,150\nQa 223,223,151\n"
"Qmvav 124,76,120\ns_waitcnt lgkmcnt(5)\nQa 100,100,180\nQa 101,101,181\nQmvav 156,72,144\nQta 64,69,24832\nQa 102,102,182\nQmvav 156,76,152\n"
"Qa 103,103,183\nQmvav 188,72,176\nQmvav 188,76,184\nQta 66,69,25856\ns_waitcnt lgkmcnt(3)\nQa 124,124,104\nQmvav 220,72,208\nQa 125,125,105\n"
"Qa 126,126,106\nQmvav 220,76,216\nds_read_b32 v66, v74\nQa 127,127,107\nQmvav 252,72,240\nQta 68,68,256\nQmvav 252,76,248\nQmvav 100,72,84\n"
"Qmvav 100,76,116\ns_waitcnt lgkmcnt(1)\nv_cmp_gt_u32 vcc, v75, v66\ns_waitcnt lgkmcnt(6)\nQmvaa 92,56,0\ns_cbranch_vccnz .Lso1920r1\n.Lro1920r1:\ns_add_u32 s68, s64, 0\n"
"Qmvaa 92,60,28\nQta 70,68,1280\ns_add_u32 m0, s96, 65536\ns_add_u32 s95, s72, 126976\nQmvaa 124,56,4\nbuffer_load_dwordx4 v63, s[52:55], s95 offen lds\nQmvaa 124,60,32\nQdv 136,59,33792\n"
"Qmvaa 156,56,8\nQta 72,68,8448\nQk 92,92,93\nQmvaa 156,60,36\nQk 93,94,95\nQmvaa 188,56,12\nQdv 148,59,34816\nQk 94,124,125\n"
"Qmvaa 188,60,40\nQta 74,68,9472\ns_add_u32 s95, s72, 146432\nQmvaa 220,56,16\nbuffer_load_dwordx4 v63, s[52:55], s95 offen offset:1024 lds\nQmvaa 220,60,44\nQdv 180,59,35840\nQk 95,126,127\n"
"Qmvaa 252,56,20\nQta 76,68,16640\nbuffer_store_dwordx4 v[92:95], v79, s[24:27], s68 offen offset:0 sc1\nQmvaa 252,60,48\nv_add_u32 v75, 1, v75\ns_waitcnt lgkmcnt(5)\nQa 0,0,136\nQmvaa 100,56,24\n"
"ds_write_b32 v73, v75\nQa 1,1,137\nQmvaa 100,60,52\nQta 78,68,17664\ns_add_u32 m0, s96, 73728\ns_add_u32 s95, s73, 86016\ns_waitcnt lgkmcnt(7)\nQmaaa 144,64,80\n"
"buffer_load_dwordx4 v63, s[52:55], s95 offen lds\nQmaaa 144,68,108\nQdv 104,59,36864\nQa 2,2,138\nQmaaa 148,64,84\nQa 3,3,139\ns_waitcnt lgkmcnt(6)\nQa 4,4,148\n"
"Qmaaa 148,68,112\nQa 5,5,149\nQa 6,6,150\nQmaaa 152,64,88\nQta 56,68,24832\ns_add_u32 s95, s73, 105472\nQmaav 152,68,168\nbuffer_load_dwordx4 v63, s[52:55], s95 offen offset:1024 lds\n"
"Qmaaa 156,64,92\nds_read_b32 v66, v74\nQa 7,7,151\nQmaav 156,68,200\nQta 58,68,25856\ns_waitcnt lgkmcnt(7)\nQa 8,8,180\nQmaaa 160,64,96\n"
"Qdv 136,59,37888\nQa 9,9,181\nQmaav 160,68,232\nQa 10,10,182\nQa 11,11,183\nQmaaa 164,64,100\ns_waitcnt lgkmcnt(2)\nv_cmp_gt_u32 vcc, v75, v66\n"
"Qmaav 164,68,212\ns_cbranch_vccnz .Lso1945r1\n.Lro1945r1:\ns_waitcnt lgkmcnt(4)\nQa 12,12,104\nQmaaa 168,64,104\nQta 60,68,33024\ns_add_u32 m0, s96, 81920\n"
"s_add_u32 s95, s73, 126976\nQa 13,13,105\nQa 14,14,106\nQa 15,15,107\nQmaav 168,68,244\nbuffer_load_dwordx4 v63, s[52:55], s95 offen lds\ns_waitcnt lgkmcnt(10)\nQmaav 144,72,80\n"
"Qdv 148,59,38912\ns_waitcnt lgkmcnt(2)\nQa 16,16,136\nQmaav 144,76,88\nQta 62,68,34048\nQa 17,17,137\nQmaav 148,72,112\nQa 18,18,138\n"
"Qa 19,19,139\nQmaav 148,76,120\nQdv 180,59,39936\nQk 156,156,157\nQmaav 152,72,144\nQta 64,69,25088\ns_add_u32 s95, s73, 146432\nQmaav 152,76,152\n"
"buffer_load_dwordx4 v63, s[52:55], s95 offen offset:1024 lds\nQmaav 156,72,176\ns_waitcnt lgkmcnt(3)\nQa 20,20,148\nQa 21,21,149\nQmaav 156,76,184\nQta 66,69,26112\nQa 22,22,150\n"
"Qmaav 160,72,208\nQa 23,23,151\nv_bfe_u32 v61, v60, 6, 2\nQmaav 160,76,216\nv_lshrrev_b32 v62, 8, v60\nv_xor_b32 v61, v61, v62\nQmaav 164,72,240\nQta 68,68,512\n"
"v_bfe_u32 v62, v60, 5, 1\nQmaav 164,76,248\ns_waitcnt lgkmcnt(3)\nQa 24,24,180\nQa 25,25,181\nQmaav 168,72,84\nQa 26,26,182\nQa 27,27,183\n"
"Qmaav 168,76,116\nQta 70,68,1536\nv_lshl_or_b32 v61, v61, 1, v62\ns_waitcnt lgkmcnt(5)\nQmaaa 144,56,0\nv_and_b32 v62, 16, v60\nv_mov_b32 v63, 0x3f80\nQmaaa 144,60,28\n"
"v_lshlrev_b32 v63, v62, v63\nv_min_u32 v62, 1, v61\nQmaaa 148,56,4\nv_add_u32 v62, -1, v62\nv_and_b32 v62, v62, v63\nQmaaa 148,60,32\nv_xor_b32 v61, 1, v61\nv_min_u32 v61, 1, v61\n"
"Qmaaa 152,56,8\nQta 72,68,8704\nv_add_u32 v61, -1, v61\nQmaaa 152,60,36\nds_write_b128 v58, a[144:147] offset:0\nQmaaa 156,56,12\nv_and_b32 v61, v61, v63\nv_accvgpr_write_b32 a174, v62\n"
"Qmaaa 156,60,40\nQta 74,68,9728\nv_accvgpr_write_b32 a175, v61\nQmaaa 160,56,16\nds_write_b128 v58, a[148:151] offset:1024\nQmaaa 160,60,44\nv_accvgpr_write_b32 a172, 0\nv_accvgpr_write_b32 a173, 0\n"
"Qmaaa 164,56,20\nQta 76,68,16896\nv_accvgpr_write_b32 a176, 0\nQmaaa 164,60,48\nds_write_b128 v58, a[152:155] offset:2048\nQmaaa 168,56,24\nv_accvgpr_write_b32 a177, 0\nQk 157,158,159\n"
"Qmaaa 168,60,52\nQta 78,68,17920\nQk 158,188,189\ns_waitcnt lgkmcnt(7)\nQmvaa 0,64,80\nds_write_b128 v58, a[156:159] offset:3072\nQmvaa 0,68,108\nQk 159,190,191\n"
"Qk 220,220,221\nQmvaa 4,64,84\nbuffer_store_dwordx4 v[156:159], v79, s[24:27], s68 offen offset:1024 sc1\nQk 221,222,223\nQmvaa 4,68,112\nds_write_b128 v58, a[160:163] offset:4096\nQmvaa 8,64,88\nQta 56,68,25088\n"
"Qk 222,252,253\nQmvav 8,68,168\nQk 223,254,255\nQk 100,100,101\nQmvaa 12,64,92\nds_write_b128 v58, a[164:167] offset:5120\nQmvav 12,68,200\nQta 58,68,26112\n"
"buffer_store_dwordx4 v[220:223], v79, s[24:27], s68 offen offset:2048 sc1\nQmvaa 16,64,96\nQk 101,102,103\nQmvav 16,68,232\nds_write_b128 v58, a[168:171] offset:6144\nQmvaa 20,64,100\nQta 60,68,33280\nQmvav 20,68,212\n"
"Qmvaa 24,64,104\nQmvav 24,68,244\nQta 62,68,34304\ns_waitcnt lgkmcnt(8)\nQmvav 0,72,80\nv_add_u32 v75, 1, v75\nQmvav 0,76,88\ns_waitcnt vmcnt(11)\n"
"ds_write_b32 v73, v75\nQmvav 4,72,112\nQmvav 4,76,120\nQmvav 8,72,144\nQta 64,69,25344\nQmvav 8,76,152\nQmvav 12,72,176\nQmvav 12,76,184\n"
"Qta 66,69,26368\nQmvav 16,72,208\nQmvav 16,76,216\nds_read_b32 v66, v74\nQmvav 20,72,240\nQta 68,68,768\nQmvav 20,76,248\nQmvav 24,72,84\n"
"Qmvav 24,76,116\ns_waitcnt lgkmcnt(1)\nv_cmp_gt_u32 vcc, v75, v66\ns_waitcnt lgkmcnt(5)\nQmvaa 0,56,0\ns_cbranch_vccnz .Lso2004r1\n.Lro2004r1:\nQmvaa 0,60,28\n"
"Qta 70,68,1792\nQmvaa 4,56,4\nQmvaa 4,60,32\nQdv 104,59,2048\nQmvaa 8,56,8\nQta 72,68,8960\ns_nop 1\nQk 102,0,1\n"
"Qmvaa 8,60,36\nQk 103,2,3\nQmvaa 12,56,12\nQdv 136,59,3072\nbuffer_store_dwordx4 v[100:103], v79, s[24:27], s68 offen offset:3072 sc1\nQmvaa 12,60,40\nQta 74,68,9984\nQk 4,4,5\n"
"Qmvaa 16,56,16\nQk 5,6,7\nQk 6,8,9\nQmvaa 16,60,44\nQdv 148,59,5120\nQk 7,10,11\nQmvaa 20,56,20\nQta 76,68,17152\n"
"s_add_u32 s68, s64, 4096\nQmvaa 20,60,48\ns_waitcnt lgkmcnt(5)\nQa 36,36,104\nQa 37,37,105\nQmvaa 24,56,24\nQdv 180,59,0\nQa 38,38,106\n"
"Qmvaa 24,60,52\nQta 78,68,18176\nQa 39,39,107\ns_waitcnt lgkmcnt(11)\nQmvaa 28,64,80\ns_waitcnt lgkmcnt(5)\nQa 40,40,136\nQa 41,41,137\n"
"Qmvaa 28,68,108\nQa 42,42,138\nQa 43,43,139\nQmvaa 32,64,84\nbuffer_store_dwordx4 v[4:7], v79, s[24:27], s68 offen offset:0 sc1\nQk 12,12,13\nQmvaa 32,68,112\ns_waitcnt lgkmcnt(3)\n"
"Qa 48,48,148\nQa 49,49,149\nQmvaa 36,64,88\nQta 56,68,25344\nQa 50,50,150\nQmvav 36,68,168\nQdv 104,59,4096\nQa 51,51,151\n"
"Qmvaa 40,64,92\ns_waitcnt lgkmcnt(3)\nQa 28,28,180\nQa 29,29,181\nQmvav 40,68,200\nQta 58,68,26368\nQa 30,30,182\nQmvaa 44,64,96\n"
"Qa 31,31,183\nQk 13,14,15\nQmvav 44,68,232\nQdv 136,59,6144\nQk 14,16,17\nQmvaa 48,64,100\nQta 60,68,33536\nQk 15,18,19\n"
"Qmvav 48,68,212\nbuffer_store_dwordx4 v[12:15], v79, s[24:27], s68 offen offset:1024 sc1\nQk 20,20,21\nQmvaa 52,64,104\nQk 21,22,23\nQk 22,24,25\nQmvav 52,68,244\nQta 62,68,34560\n"
"v_add_u32 v75, 1, v75\ns_waitcnt lgkmcnt(6)\nQmvav 28,72,80\ns_waitcnt vmcnt(5)\nds_write_b32 v73, v75\nQmvav 28,76,88\nQdv 148,59,1024\ns_waitcnt lgkmcnt(6)\n"
"Qa 44,44,104\nQmvav 32,72,112\nQa 45,45,105\nQa 46,46,106\nQmvav 32,76,120\nQa 47,47,107\ns_waitcnt lgkmcnt(4)\nQa 52,52,136\n"
"Qmvav 36,72,144\nQta 64,68,40960\nQa 53,53,137\nQmvav 36,76,152\nQa 54,54,138\nQa 55,55,139\nQmvav 40,72,176\nQk 23,26,27\n"
"Qmvav 40,76,184\nQta 66,68,41984\ns_waitcnt lgkmcnt(2)\nQa 32,32,148\nQmvav 44,72,208\nds_read_b32 v66, v74\nQa 33,33,149\nQmvav 44,76,216\n"
"Qa 34,34,150\nQa 35,35,151\nQmvav 48,72,240\nQta 68,68,49152\nbuffer_store_dwordx4 v[20:23], v79, s[24:27], s68 offen offset:2048 sc1\nQmvav 48,76,248\nQmvav 52,72,84\ns_waitcnt lgkmcnt(1)\n"
"v_cmp_gt_u32 vcc, v75, v66\nQmvav 52,76,116\ns_cbranch_vccnz .Lso2045r1\n.Lro2045r1:\ns_waitcnt lgkmcnt(6)\nQmvaa 28,56,0\nQta 70,68,50176\nQmvaa 28,60,28\n"
"Qmvaa 32,56,4\nQmvaa 32,60,32\nQmvaa 36,56,8\nQta 72,68,57344\ns_nop 3\nQk 28,28,29\nQmvaa 36,60,36\nQk 29,30,31\n"
"Qmvaa 40,56,12\nQk 30,32,33\nQk 31,34,35\nQmvaa 40,60,40\nQta 74,68,58368\nbuffer_store_dwordx4 v[28:31], v79, s[24:27], s68 offen offset:3072 sc1\nQmvaa 44,56,16\nQk 36,36,37\n"
"Qk 37,38,39\nQmvaa 44,60,44\ns_add_u32 s68, s64, 16384\nQmvaa 48,56,20\nQta 76,69,0\nQk 38,40,41\nQmvaa 48,60,48\nQk 39,42,43\n"
"Qmvaa 52,56,24\nbuffer_store_dwordx4 v[36:39], v79, s[24:27], s68 offen offset:0 sc1\nQk 44,44,45\nQmvaa 52,60,52\nQta 78,69,1024\nQk 45,46,47\ns_waitcnt lgkmcnt(4)\nQmaaa 200,64,80\n"
"Qk 46,48,49\nQk 47,50,51\nQmaaa 200,68,108\nbuffer_store_dwordx4 v[44:47], v79, s[24:27], s68 offen offset:1024 sc1\nQmaaa 204,64,84\nQk 52,52,53\nQk 53,54,55\nQmaaa 204,68,112\n"
"v_mov_b32 v54, 0\nv_mov_b32 v55, 0\nQmaaa 208,64,88\nQta 56,69,8192\nbuffer_store_dwordx4 v[52:55], v79, s[24:27], s68 offen offset:2048 sc1\nQmaav 208,68,168\nQmaaa 212,64,92\nQmaav 212,68,200\n"
"Qta 58,69,9216\nQmaaa 216,64,96\nQmaav 216,68,232\nQmaaa 220,64,100\nQta 60,69,16384\nQmaav 220,68,212\nQmaaa 224,64,104\nQmaav 224,68,244\n"
"Qta 62,69,17408\ns_waitcnt lgkmcnt(4)\nQmaav 200,72,80\nQmaav 200,76,88\nQmaav 204,72,112\nQmaav 204,76,120\nQmaav 208,72,144\nQta 64,68,41216\n"
"Qmaav 208,76,152\nQmaav 212,72,176\nQmaav 212,76,184\nQta 66,68,42240\nQmaav 216,72,208\nQmaav 216,76,216\nQmaav 220,72,240\nQta 68,68,49408\n"
"Qmaav 220,76,248\nQmaav 224,72,84\nQmaav 224,76,116\nQta 70,68,50432\ns_waitcnt lgkmcnt(4)\nQmaaa 200,56,0\nQmaaa 200,60,28\ns_waitcnt vmcnt(0)\n"
"buffer_store_dword v67, v77, s[24:27], 0 offen sc1\nQmaaa 204,56,4\nQmaaa 204,60,32\nQmaaa 208,56,8\nQta 72,68,57600\nQmaaa 208,60,36\ns_nop 0\nds_write_b128 v58, a[200:203] offset:33792\n"
"Qmaaa 212,56,12\nQmaaa 212,60,40\nQta 74,68,58624\nQmaaa 216,56,16\nds_write_b128 v58, a[204:207] offset:34816\nQmaaa 216,60,44\nQmaaa 220,56,20\nQta 76,69,256\n"
"Qmaaa 220,60,48\nds_write_b128 v58, a[208:211] offset:35840\nQmaaa 224,56,24\nQmaaa 224,60,52\nQta 78,69,1280\ns_waitcnt lgkmcnt(7)\nQmaaa 228,64,80\nds_write_b128 v58, a[212:215] offset:36864\n"
"Qmaaa 228,68,108\nQmaaa 232,64,84\nQmaaa 232,68,112\nds_write_b128 v58, a[216:219] offset:37888\nQmaaa 236,64,88\nQta 56,69,8448\nQmaav 236,68,168\nQmaaa 240,64,92\n"
"ds_write_b128 v58, a[220:223] offset:38912\nQmaav 240,68,200\nQta 58,69,9472\nQmaaa 244,64,96\nQmaav 244,68,232\nds_write_b128 v58, a[224:227] offset:39936\nQmaaa 248,64,100\nQta 60,69,16640\n"
"Qmaav 248,68,212\nQmaaa 252,64,104\nQmaav 252,68,244\nQta 62,69,17664\ns_waitcnt lgkmcnt(8)\nQmaav 228,72,80\nv_add_u32 v75, 1, v75\nQmaav 228,76,88\n"
"ds_write_b32 v73, v75\nQmaav 232,72,112\nQmaav 232,76,120\nQmaav 236,72,144\nQta 64,68,41472\nQmaav 236,76,152\nQmaav 240,72,176\nQmaav 240,76,184\n"
"Qta 66,68,42496\nQmaav 244,72,208\nQmaav 244,76,216\nds_read_b32 v66, v74\nQmaav 248,72,240\nQta 68,68,49664\nQmaav 248,76,248\nQmaav 252,72,84\n"
"buffer_load_dword v78, v76, s[24:27], 0 offen sc1\nQmaav 252,76,116\ns_waitcnt lgkmcnt(1)\nv_cmp_gt_u32 vcc, v75, v66\ns_waitcnt lgkmcnt(5)\nQmaaa 228,56,0\ns_cbranch_vccnz .Lso2130r1\n.Lro2130r1:\n"
"Qmaaa 228,60,28\nQta 70,68,50688\nQmaaa 232,56,4\nQmaaa 232,60,32\nQdv 180,59,35840\nQmaaa 236,56,8\nQta 72,68,57856\nQmaaa 236,60,36\n"
"s_nop 0\nds_write_b128 v58, a[228:231] offset:0\nQmaaa 240,56,12\nQdv 104,59,36864\n.Lpg25:\ns_cbranch_execz .Ltr8\nQmaaa 240,60,40\nQta 74,68,58880\n"
"Qmaaa 244,56,16\nds_write_b128 v58, a[232:235] offset:1024\nQmaaa 244,60,44\nQdv 136,59,38912\nQmaaa 248,56,20\nQta 76,69,512\nQmaaa 248,60,48\nds_write_b128 v58, a[236:239] offset:2048\n"
"Qmaaa 252,56,24\nQdv 148,59,33792\ns_waitcnt lgkmcnt(9)\nQa 196,196,180\nQmaaa 252,60,52\nQta 78,69,1536\nQa 197,197,181\ns_waitcnt lgkmcnt(10)\n"
"Qmvaa 132,64,80\nds_write_b128 v58, a[240:243] offset:3072\nQmvaa 132,68,108\nQa 198,198,182\nQa 199,199,183\nQmvaa 164,64,84\ns_waitcnt lgkmcnt(8)\nQa 228,228,104\n"
"Qa 229,229,105\nQmvaa 164,68,112\nds_write_b128 v58, a[244:247] offset:4096\nQmvaa 196,64,88\nQta 56,69,8704\nQa 230,230,106\nQa 231,231,107\nQmvav 196,68,168\n"
"Qdv 180,59,37888\ns_waitcnt lgkmcnt(8)\nQa 140,140,136\nQmvaa 228,64,92\nds_write_b128 v58, a[248:251] offset:5120\nQmvav 228,68,200\nQta 58,69,9728\nQa 141,141,137\n"
"Qmvaa 108,64,96\nQa 142,142,138\nQa 143,143,139\nQmvav 108,68,232\nQdv 104,59,39936\ns_waitcnt lgkmcnt(8)\nQa 132,132,148\nQmvaa 140,64,100\n"
"Qta 60,69,16896\nQa 133,133,149\nQmvav 140,68,212\nds_write_b128 v58, a[252:255] offset:6144\nQmvaa 172,64,104\nQa 134,134,150\nQa 135,135,151\nQmvav 172,68,244\n"
"Qta 62,69,17920\ns_waitcnt lgkmcnt(6)\nQa 108,108,180\ns_waitcnt lgkmcnt(6)\nQmvav 132,72,80\nQdv 136,59,34816\nv_add_u32 v75, 1, v75\nQmvav 132,76,88\n"
"ds_write_b32 v73, v75\nQa 109,109,181\nQmvav 164,72,112\nQa 110,110,182\nQa 111,111,183\nQmvav 164,76,120\ns_waitcnt lgkmcnt(5)\nQa 172,172,104\n"
"Qa 173,173,105\nQmvav 196,72,144\nQta 64,68,41728\nQa 174,174,106\nQmvav 196,76,152\nQa 175,175,107\nQmvav 228,72,176\nQmvav 228,76,184\n"
"Qta 66,68,42752\ns_waitcnt lgkmcnt(3)\nQa 164,164,136\nQmvav 108,72,208\nQa 165,165,137\nQa 166,166,138\nQmvav 108,76,216\nds_read_b32 v66, v74\n"
"Qa 167,167,139\nQmvav 140,72,240\nQta 68,68,49920\nQmvav 140,76,248\nQmvav 172,72,84\nQmvav 172,76,116\ns_waitcnt lgkmcnt(1)\nv_cmp_gt_u32 vcc, v75, v66\n"
"s_waitcnt lgkmcnt(6)\nQmvaa 132,56,0\ns_cbranch_vccnz .Lso2172r1\n.Lro2172r1:\nQmvaa 132,60,28\ns_waitcnt vmcnt(0)\nv_sub_u32 v66, v78, v65\nv_cmp_gt_u32 vcc, 1, v66\n"
"s_and_b64 vcc, vcc, s[28:29]\ns_cbranch_scc1 .Lx1_w\n.Lx1_r:\nQmvaa 164,56,4\ns_add_u32 s69, s98, 0\nbuffer_load_dwordx4 a[180:183], v79, s[24:27], s69 offen offset:0 sc1\nQmvaa 164,60,32\nQta 70,68,50944\n"
"Qmvaa 196,56,8\nbuffer_load_dwordx4 a[184:187], v79, s[24:27], s69 offen offset:1024 sc1\nQmvaa 196,60,36\nQdv 148,59,2048\nQmvaa 228,56,12\nbuffer_load_dwordx4 a[188:191], v79, s[24:27], s69 offen offset:2048 sc1\nQmvaa 228,60,40\nQta 72,68,58112\n"
"Qmvaa 108,56,16\nbuffer_load_dwordx4 a[192:195], v79, s[24:27], s69 offen offset:3072 sc1\nQmvaa 108,60,44\nQdv 180,59,4096\nQmvaa 140,56,20\ns_add_u32 s69, s98, 4096\nbuffer_load_dwordx4 a[196:199], v79, s[24:27], s69 offen offset:0 sc1\nQmvaa 140,60,48\n"
"Qta 74,68,59136\ns_waitcnt lgkmcnt(3)\nQa 96,96,148\nQmvaa 172,56,24\nbuffer_load_dwordx4 a[116:119], v79, s[24:27], s69 offen offset:1024 sc1\nQmvaa 172,60,52\nQdv 104,59,0\nQa 97,97,149\n"
"s_waitcnt lgkmcnt(4)\nQmvaa 204,64,80\nbuffer_load_dwordx4 a[120:123], v79, s[24:27], s69 offen offset:2048 sc1\nQmvaa 204,68,108\nQta 76,69,768\nQa 98,98,150\nQmvaa 236,64,84\nQdv 136,59,1024\n"
"Qa 99,99,151\nQmvaa 236,68,112\ns_waitcnt lgkmcnt(4)\nQa 160,160,180\nQa 161,161,181\nQmvaa 96,64,88\nQta 78,69,1792\nQa 162,162,182\n"
"Qmvav 96,68,168\nQdv 148,59,3072\nQa 163,163,183\nQmvaa 128,64,92\ns_waitcnt lgkmcnt(4)\nQa 204,204,104\nQa 205,205,105\nQmvav 128,68,200\n"
"Qta 56,69,8960\nQa 206,206,106\nQmvaa 160,64,96\nQdv 180,59,5120\nQa 207,207,107\nQmvav 160,68,232\nQmvaa 192,64,100\nQta 58,69,9984\n"
"s_waitcnt lgkmcnt(5)\nQa 236,236,136\nQmvav 192,68,212\nQa 237,237,137\nQa 238,238,138\nQmvaa 224,64,104\nQa 239,239,139\nQmvav 224,68,244\n"
"Qta 60,69,17152\ns_waitcnt lgkmcnt(4)\nQa 128,128,148\ns_waitcnt lgkmcnt(4)\nQmvav 204,72,80\nQa 129,129,149\nQa 130,130,150\nQmvav 204,76,88\n"
"Qa 131,131,151\nQmvav 236,72,112\nQta 62,69,18176\ns_waitcnt lgkmcnt(3)\nQa 192,192,180\nQmvav 236,76,120\nQa 193,193,181\nQa 194,194,182\n"
"Qmvav 96,72,144\nQa 195,195,183\nQmvav 96,76,152\nQmvav 128,72,176\nQmvav 128,76,184\nQmvav 160,72,208\nQmvav 160,76,216\nQdv 104,59,6144\n"
"Qmvav 192,72,240\nQmvav 192,76,248\nv_add_u32 v75, 1, v75\nQmvav 224,72,84\nds_write_b32 v73, v75\nQmvav 224,76,116\ns_waitcnt lgkmcnt(2)\nQmvaa 204,56,0\n"
"Qmvaa 204,60,28\nQmvaa 236,56,4\nQmvaa 236,60,32\ns_waitcnt lgkmcnt(1)\ns_nop 1\nQa 224,224,104\nQa 225,225,105\nQmvaa 96,56,8\n"
"Qa 226,226,106\nQa 227,227,107\nQmvaa 96,60,36\nQmvaa 128,56,12\nds_read_b32 v66, v74\nQmvaa 128,60,40\nQmvaa 160,56,16\nQmvaa 160,60,44\n"
"Qmvaa 192,56,20\ns_waitcnt lgkmcnt(0)\nv_cmp_gt_u32 vcc, v75, v66\nQmvaa 192,60,48\ns_cbranch_vccnz .Lso2225r1\n.Lro2225r1:\nQmvaa 224,56,24\nQmvaa 224,60,52\n"
"s_waitcnt vmcnt(6)\nQmvaa 132,174,180\nQmvaa 164,172,180\ns_waitcnt vmcnt(5)\nQmvaa 196,174,184\nQmvaa 228,172,184\ns_nop 3\nQk 132,132,133\n"
"Qk 133,134,135\nds_write_b64 v56, v[132:133] offset:0\ns_waitcnt vmcnt(4)\nQmvaa 108,174,188\nQk 164,164,165\nQk 165,166,167\nds_write_b64 v56, v[164:165] offset:3328\nQmvaa 140,172,188\n"
"Qk 196,196,197\nQk 197,198,199\nds_write_b64 v56, v[196:197] offset:6656\ns_waitcnt vmcnt(3)\nQmvaa 172,174,192\nQk 228,228,229\nQk 229,230,231\nds_write_b64 v56, v[228:229] offset:9984\n"
"Qmvaa 204,172,192\nQk 108,108,109\nQk 109,110,111\nds_write_b64 v56, v[108:109] offset:13312\ns_waitcnt vmcnt(2)\nQmvaa 236,174,196\nQk 140,140,141\nQk 141,142,143\n"
"ds_write_b64 v56, v[140:141] offset:16640\nQmvaa 96,172,196\nQk 172,172,173\nQk 173,174,175\nds_write_b64 v56, v[172:173] offset:19968\ns_waitcnt vmcnt(1)\nQmvaa 128,174,116\nQk 204,204,205\n"
"Qk 205,206,207\nds_write_b64 v56, v[204:205] offset:32\nQmvaa 160,172,116\nQk 236,236,237\nQk 237,238,239\nds_write_b64 v56, v[236:237] offset:3360\ns_waitcnt vmcnt(0)\nQmvaa 192,174,120\n"
"Qk 96,96,97\nQk 97,98,99\nds_write_b64 v56, v[96:97] offset:6688\nQmvaa 224,172,120\nQk 128,128,129\nQk 129,130,131\nds_write_b64 v56, v[128:129] offset:10016\nQk 160,160,161\n"
"Qk 161,162,163\nds_write_b64 v56, v[160:161] offset:13344\nQk 192,192,193\nQk 193,194,195\nds_write_b64 v56, v[192:193] offset:16672\nQk 224,224,225\nQk 225,226,227\nds_write_b64 v56, v[224:225] offset:20000\n"
"s_waitcnt lgkmcnt(0)\ns_barrier\nQda 0,57,0\nQda 4,57,3328\nQda 8,57,6656\nQda 12,57,9984\nQda 16,57,13312\nQda 20,57,16640\n"
"Qda 24,57,19968\ns_mov_b32 s68, 0\ns_waitcnt lgkmcnt(6)\nbuffer_store_dwordx4 a[0:3], v64, s[40:43], s68 offen offset:0 sc1\ns_mov_b32 s68, 40960\ns_waitcnt lgkmcnt(5)\nbuffer_store_dwordx4 a[4:7], v64, s[40:43], s68 offen offset:0 sc1\ns_mov_b32 s68, 81920\n"
"s_waitcnt lgkmcnt(4)\nbuffer_store_dwordx4 a[8:11], v64, s[40:43], s68 offen offset:0 sc1\ns_mov_b32 s68, 122880\ns_waitcnt lgkmcnt(3)\nbuffer_store_dwordx4 a[12:15], v64, s[40:43], s68 offen offset:0 sc1\ns_mov_b32 s68, 163840\ns_waitcnt lgkmcnt(2)\nbuffer_store_dwordx4 a[16:19], v64, s[40:43], s68 offen offset:0 sc1\n"
"s_mov_b32 s68, 204800\ns_waitcnt lgkmcnt(1)\nbuffer_store_dwordx4 a[20:23], v64, s[40:43], s68 offen offset:0 sc1\ns_mov_b32 s68, 245760\ns_waitcnt lgkmcnt(0)\nbuffer_store_dwordx4 a[24:27], v64, s[40:43], s68 offen offset:0 sc1\n.Lfin:\n.Lpg26:\n"
"s_cbranch_execz .Ltr8\ns_waitcnt vmcnt(0) lgkmcnt(0)\ns_branch .Lexit\n.Lend:\n.Lfb:\ns_waitcnt vmcnt(0) lgkmcnt(0)\nv_lshrrev_b32 v52, 4, v60\nv_and_b32 v53, 15, v52\n"
"v_lshrrev_b32 v52, 4, v52\nv_lshlrev_b32 v66, 8, v53\nv_lshl_add_u32 v66, v52, 4, v66\nv_add_u32 v66, 1024, v66\nv_lshlrev_b32 v67, 10, v52\nv_lshl_add_u32 v67, v53, 1, v67\nv_add_u32 v67, 1024, v67\nv_mov_b32 v52, 0x3f803f80\n"
"v_accvgpr_write_b32 a204, v52\nv_accvgpr_write_b32 a205, v52\nv_accvgpr_write_b32 a206, v52\nv_accvgpr_write_b32 a207, v52\nv_mov_b32 v24, 0xff800000\nv_mov_b32 v28, 0xff800000\nv_mov_b32 v32, 0xff800000\nv_mov_b32 v36, 0xff800000\n"
"v_mov_b32 v40, 0xff800000\nv_mov_b32 v44, 0xff800000\nv_mov_b32 v48, 0xff800000\ns_lshl_b32 s89, s67, 21\ns_lshl_b32 s68, s93, 18\ns_add_u32 s89, s89, s68\ns_mov_b32 s66, 0\n.Lfb1:\n"
"s_add_u32 s68, s89, 4096\nbuffer_load_dwordx4 a[112:115], v66, s[40:43], s89 offen offset:0\nbuffer_load_dwordx4 a[116:119], v66, s[40:43], s89 offen offset:64\nbuffer_load_dwordx4 a[120:123], v66, s[40:43], s89 offen offset:128\nbuffer_load_dwordx4 a[124:127], v66, s[40:43], s89 offen offset:192\nbuffer_load_dwordx4 a[128:131], v66, s[40:43], s68 offen offset:0\nbuffer_load_dwordx4 a[132:135], v66, s[40:43], s68 offen offset:64\nbuffer_load_dwordx4 a[136:139], v66, s[40:43], s68 offen offset:128\n"
"buffer_load_dwordx4 a[140:143], v66, s[40:43], s68 offen offset:192\ns_waitcnt vmcnt(0)\nQzvaa 0,112,0\nQmvaa 0,116,4\nQmvaa 0,120,8\nQmvaa 0,124,12\nQzvaa 4,128,0\nQmvaa 4,132,4\n"
"Qmvaa 4,136,8\nQmvaa 4,140,12\ns_nop 7\nv_max3_f32 v24, v24, v0, v1\nv_max3_f32 v24, v24, v2, v3\nv_max3_f32 v24, v24, v4, v5\nv_max3_f32 v24, v24, v6, v7\nQzvaa 0,112,16\n"
"Qmvaa 0,116,20\nQmvaa 0,120,24\nQmvaa 0,124,28\nQzvaa 4,128,16\nQmvaa 4,132,20\nQmvaa 4,136,24\nQmvaa 4,140,28\ns_nop 7\n"
"v_max3_f32 v28, v28, v0, v1\nv_max3_f32 v28, v28, v2, v3\nv_max3_f32 v28, v28, v4, v5\nv_max3_f32 v28, v28, v6, v7\nQzvaa 0,112,32\nQmvaa 0,116,36\nQmvaa 0,120,40\nQmvaa 0,124,44\n"
"Qzvaa 4,128,32\nQmvaa 4,132,36\nQmvaa 4,136,40\nQmvaa 4,140,44\ns_nop 7\nv_max3_f32 v32, v32, v0, v1\nv_max3_f32 v32, v32, v2, v3\nv_max3_f32 v32, v32, v4, v5\n"
"v_max3_f32 v32, v32, v6, v7\nQzvaa 0,112,48\nQmvaa 0,116,52\nQmvaa 0,120,56\nQmvaa 0,124,60\nQzvaa 4,128,48\nQmvaa 4,132,52\nQmvaa 4,136,56\n"
"Qmvaa 4,140,60\ns_nop 7\nv_max3_f32 v36, v36, v0, v1\nv_max3_f32 v36, v36, v2, v3\nv_max3_f32 v36, v36, v4, v5\nv_max3_f32 v36, v36, v6, v7\nQzvaa 0,112,64\nQmvaa 0,116,68\n"
"Qmvaa 0,120,72\nQmvaa 0,124,76\nQzvaa 4,128,64\nQmvaa 4,132,68\nQmvaa 4,136,72\nQmvaa 4,140,76\ns_nop 7\nv_max3_f32 v40, v40, v0, v1\n"
"v_max3_f32 v40, v40, v2, v3\nv_max3_f32 v40, v40, v4, v5\nv_max3_f32 v40, v40, v6, v7\nQzvaa 0,112,80\nQmvaa 0,116,84\nQmvaa 0,120,88\nQmvaa 0,124,92\nQzvaa 4,128,80\n"
"Qmvaa 4,132,84\nQmvaa 4,136,88\nQmvaa 4,140,92\ns_nop 7\nv_max3_f32 v44, v44, v0, v1\nv_max3_f32 v44, v44, v2, v3\nv_max3_f32 v44, v44, v4, v5\nv_max3_f32 v44, v44, v6, v7\n"
"Qzvaa 0,112,96\nQmvaa 0,116,100\nQmvaa 0,120,104\nQmvaa 0,124,108\nQzvaa 4,128,96\nQmvaa 4,132,100\nQmvaa 4,136,104\nQmvaa 4,140,108\n"
"s_nop 7\nv_max3_f32 v48, v48, v0, v1\nv_max3_f32 v48, v48, v2, v3\nv_max3_f32 v48, v48, v4, v5\nv_max3_f32 v48, v48, v6, v7\ns_add_u32 s89, s89, 8192\ns_add_u32 s66, s66, 1\ns_cmp_lt_u32 s66, 32\n"
"s_cbranch_scc1 .Lfb1\nv_mov_b32 v25, v24\ns_nop 1\nv_permlane32_swap_b32 v24, v25\nv_max_f32 v24, v24, v25\nv_mov_b32 v25, v24\ns_nop 1\nv_permlane16_swap_b32 v24, v25\n"
"v_max_f32 v24, v24, v25\nv_sub_f32 v24, 0, v24\nv_mov_b32 v25, v24\nv_mov_b32 v26, v24\nv_mov_b32 v27, v24\nv_mov_b32 v29, v28\ns_nop 1\nv_permlane32_swap_b32 v28, v29\n"
"v_max_f32 v28, v28, v29\nv_mov_b32 v29, v28\ns_nop 1\nv_permlane16_swap_b32 v28, v29\nv_max_f32 v28, v28, v29\nv_sub_f32 v28, 0, v28\nv_mov_b32 v29, v28\nv_mov_b32 v30, v28\n"
"v_mov_b32 v31, v28\nv_mov_b32 v33, v32\ns_nop 1\nv_permlane32_swap_b32 v32, v33\nv_max_f32 v32, v32, v33\nv_mov_b32 v33, v32\ns_nop 1\nv_permlane16_swap_b32 v32, v33\n"
"v_max_f32 v32, v32, v33\nv_sub_f32 v32, 0, v32\nv_mov_b32 v33, v32\nv_mov_b32 v34, v32\nv_mov_b32 v35, v32\nv_mov_b32 v37, v36\ns_nop 1\nv_permlane32_swap_b32 v36, v37\n"
"v_max_f32 v36, v36, v37\nv_mov_b32 v37, v36\ns_nop 1\nv_permlane16_swap_b32 v36, v37\nv_max_f32 v36, v36, v37\nv_sub_f32 v36, 0, v36\nv_mov_b32 v37, v36\nv_mov_b32 v38, v36\n"
"v_mov_b32 v39, v36\nv_mov_b32 v41, v40\ns_nop 1\nv_permlane32_swap_b32 v40, v41\nv_max_f32 v40, v40, v41\nv_mov_b32 v41, v40\ns_nop 1\nv_permlane16_swap_b32 v40, v41\n"
"v_max_f32 v40, v40, v41\nv_sub_f32 v40, 0, v40\nv_mov_b32 v41, v40\nv_mov_b32 v42, v40\nv_mov_b32 v43, v40\nv_mov_b32 v45, v44\ns_nop 1\nv_permlane32_swap_b32 v44, v45\n"
"v_max_f32 v44, v44, v45\nv_mov_b32 v45, v44\ns_nop 1\nv_permlane16_swap_b32 v44, v45\nv_max_f32 v44, v44, v45\nv_sub_f32 v44, 0, v44\nv_mov_b32 v45, v44\nv_mov_b32 v46, v44\n"
"v_mov_b32 v47, v44\nv_mov_b32 v49, v48\ns_nop 1\nv_permlane32_swap_b32 v48, v49\nv_max_f32 v48, v48, v49\nv_mov_b32 v49, v48\ns_nop 1\nv_permlane16_swap_b32 v48, v49\n"
"v_max_f32 v48, v48, v49\nv_sub_f32 v48, 0, v48\nv_mov_b32 v49, v48\nv_mov_b32 v50, v48\nv_mov_b32 v51, v48\nv_mov_b32 v52, 0\nv_mov_b32 v80, 0\nv_mov_b32 v81, 0\n"
"v_mov_b32 v82, 0\nv_mov_b32 v83, 0\nv_mov_b32 v84, 0\nv_mov_b32 v85, 0\nv_mov_b32 v86, 0\nv_mov_b32 v87, 0\nv_mov_b32 v88, 0\nv_mov_b32 v89, 0\n"
"v_mov_b32 v90, 0\nv_mov_b32 v91, 0\nv_mov_b32 v92, 0\nv_mov_b32 v93, 0\nv_mov_b32 v94, 0\nv_mov_b32 v95, 0\nv_mov_b32 v96, 0\nv_mov_b32 v97, 0\n"
"v_mov_b32 v98, 0\nv_mov_b32 v99, 0\nv_mov_b32 v100, 0\nv_mov_b32 v101, 0\nv_mov_b32 v102, 0\nv_mov_b32 v103, 0\nv_mov_b32 v104, 0\nv_mov_b32 v105, 0\n"
"v_mov_b32 v106, 0\nv_mov_b32 v107, 0\nv_mov_b32 v108, 0\nv_mov_b32 v109, 0\nv_mov_b32 v110, 0\nv_mov_b32 v111, 0\nv_accvgpr_write_b32 a176, v52\nv_accvgpr_write_b32 a177, v52\n"
"v_accvgpr_write_b32 a178, v52\nv_accvgpr_write_b32 a179, v52\nv_mov_b32 v112, 0\nv_mov_b32 v113, 0\nv_mov_b32 v114, 0\nv_mov_b32 v115, 0\nv_mov_b32 v116, 0\nv_mov_b32 v117, 0\n"
"v_mov_b32 v118, 0\nv_mov_b32 v119, 0\nv_mov_b32 v120, 0\nv_mov_b32 v121, 0\nv_mov_b32 v122, 0\nv_mov_b32 v123, 0\nv_mov_b32 v124, 0\nv_mov_b32 v125, 0\n"
"v_mov_b32 v126, 0\nv_mov_b32 v127, 0\nv_mov_b32 v128, 0\nv_mov_b32 v129, 0\nv_mov_b32 v130, 0\nv_mov_b32 v131, 0\nv_mov_b32 v132, 0\nv_mov_b32 v133, 0\n"
"v_mov_b32 v134, 0\nv_mov_b32 v135, 0\nv_mov_b32 v136, 0\nv_mov_b32 v137, 0\nv_mov_b32 v138, 0\nv_mov_b32 v139, 0\nv_mov_b32 v140, 0\nv_mov_b32 v141, 0\n"
"v_mov_b32 v142, 0\nv_mov_b32 v143, 0\nv_accvgpr_write_b32 a180, v52\nv_accvgpr_write_b32 a181, v52\nv_accvgpr_write_b32 a182, v52\nv_accvgpr_write_b32 a183, v52\nv_mov_b32 v144, 0\nv_mov_b32 v145, 0\n"
"v_mov_b32 v146, 0\nv_mov_b32 v147, 0\nv_mov_b32 v148, 0\nv_mov_b32 v149, 0\nv_mov_b32 v150, 0\nv_mov_b32 v151, 0\nv_mov_b32 v152, 0\nv_mov_b32 v153, 0\n"
"v_mov_b32 v154, 0\nv_mov_b32 v155, 0\nv_mov_b32 v156, 0\nv_mov_b32 v157, 0\nv_mov_b32 v158, 0\nv_mov_b32 v159, 0\nv_mov_b32 v160, 0\nv_mov_b32 v161, 0\n"
"v_mov_b32 v162, 0\nv_mov_b32 v163, 0\nv_mov_b32 v164, 0\nv_mov_b32 v165, 0\nv_mov_b32 v166, 0\nv_mov_b32 v167, 0\nv_mov_b32 v168, 0\nv_mov_b32 v169, 0\n"
"v_mov_b32 v170, 0\nv_mov_b32 v171, 0\nv_mov_b32 v172, 0\nv_mov_b32 v173, 0\nv_mov_b32 v174, 0\nv_mov_b32 v175, 0\nv_accvgpr_write_b32 a184, v52\nv_accvgpr_write_b32 a185, v52\n"
"v_accvgpr_write_b32 a186, v52\nv_accvgpr_write_b32 a187, v52\nv_mov_b32 v176, 0\nv_mov_b32 v177, 0\nv_mov_b32 v178, 0\nv_mov_b32 v179, 0\nv_mov_b32 v180, 0\nv_mov_b32 v181, 0\n"
"v_mov_b32 v182, 0\nv_mov_b32 v183, 0\nv_mov_b32 v184, 0\nv_mov_b32 v185, 0\nv_mov_b32 v186, 0\nv_mov_b32 v187, 0\nv_mov_b32 v188, 0\nv_mov_b32 v189, 0\n"
"v_mov_b32 v190, 0\nv_mov_b32 v191, 0\nv_mov_b32 v192, 0\nv_mov_b32 v193, 0\nv_mov_b32 v194, 0\nv_mov_b32 v195, 0\nv_mov_b32 v196, 0\nv_mov_b32 v197, 0\n"
"v_mov_b32 v198, 0\nv_mov_b32 v199, 0\nv_mov_b32 v200, 0\nv_mov_b32 v201, 0\nv_mov_b32 v202, 0\nv_mov_b32 v203, 0\nv_mov_b32 v204, 0\nv_mov_b32 v205, 0\n"
"v_mov_b32 v206, 0\nv_mov_b32 v207, 0\nv_accvgpr_write_b32 a188, v52\nv_accvgpr_write_b32 a189, v52\nv_accvgpr_write_b32 a190, v52\nv_accvgpr_write_b32 a191, v52\nv_mov_b32 v208, 0\nv_mov_b32 v209, 0\n"
"v_mov_b32 v210, 0\nv_mov_b32 v211, 0\nv_mov_b32 v212, 0\nv_mov_b32 v213, 0\nv_mov_b32 v214, 0\nv_mov_b32 v215, 0\nv_mov_b32 v216, 0\nv_mov_b32 v217, 0\n"
"v_mov_b32 v218, 0\nv_mov_b32 v219, 0\nv_mov_b32 v220, 0\nv_mov_b32 v221, 0\nv_mov_b32 v222, 0\nv_mov_b32 v223, 0\nv_mov_b32 v224, 0\nv_mov_b32 v225, 0\n"
"v_mov_b32 v226, 0\nv_mov_b32 v227, 0\nv_mov_b32 v228, 0\nv_mov_b32 v229, 0\nv_mov_b32 v230, 0\nv_mov_b32 v231, 0\nv_mov_b32 v232, 0\nv_mov_b32 v233, 0\n"
"v_mov_b32 v234, 0\nv_mov_b32 v235, 0\nv_mov_b32 v236, 0\nv_mov_b32 v237, 0\nv_mov_b32 v238, 0\nv_mov_b32 v239, 0\nv_accvgpr_write_b32 a192, v52\nv_accvgpr_write_b32 a193, v52\n"
"v_accvgpr_write_b32 a194, v52\nv_accvgpr_write_b32 a195, v52\nv_mov_b32 v240, 0\nv_mov_b32 v241, 0\nv_mov_b32 v242, 0\nv_mov_b32 v243, 0\nv_mov_b32 v244, 0\nv_mov_b32 v245, 0\n"
"v_mov_b32 v246, 0\nv_mov_b32 v247, 0\nv_mov_b32 v248, 0\nv_mov_b32 v249, 0\nv_mov_b32 v250, 0\nv_mov_b32 v251, 0\nv_mov_b32 v252, 0\nv_mov_b32 v253, 0\n"
"v_mov_b32 v254, 0\nv_mov_b32 v255, 0\nv_accvgpr_write_b32 a208, v52\nv_accvgpr_write_b32 a209, v52\nv_accvgpr_write_b32 a210, v52\nv_accvgpr_write_b32 a211, v52\nv_accvgpr_write_b32 a212, v52\nv_accvgpr_write_b32 a213, v52\n"
"v_accvgpr_write_b32 a214, v52\nv_accvgpr_write_b32 a215, v52\nv_accvgpr_write_b32 a216, v52\nv_accvgpr_write_b32 a217, v52\nv_accvgpr_write_b32 a218, v52\nv_accvgpr_write_b32 a219, v52\nv_accvgpr_write_b32 a220, v52\nv_accvgpr_write_b32 a221, v52\n"
"v_accvgpr_write_b32 a222, v52\nv_accvgpr_write_b32 a223, v52\nv_accvgpr_write_b32 a196, v52\nv_accvgpr_write_b32 a197, v52\nv_accvgpr_write_b32 a198, v52\nv_accvgpr_write_b32 a199, v52\nv_accvgpr_write_b32 a224, v52\nv_accvgpr_write_b32 a225, v52\n"
"v_accvgpr_write_b32 a226, v52\nv_accvgpr_write_b32 a227, v52\nv_accvgpr_write_b32 a228, v52\nv_accvgpr_write_b32 a229, v52\nv_accvgpr_write_b32 a230, v52\nv_accvgpr_write_b32 a231, v52\nv_accvgpr_write_b32 a232, v52\nv_accvgpr_write_b32 a233, v52\n"
"v_accvgpr_write_b32 a234, v52\nv_accvgpr_write_b32 a235, v52\nv_accvgpr_write_b32 a236, v52\nv_accvgpr_write_b32 a237, v52\nv_accvgpr_write_b32 a238, v52\nv_accvgpr_write_b32 a239, v52\nv_accvgpr_write_b32 a240, v52\nv_accvgpr_write_b32 a241, v52\n"
"v_accvgpr_write_b32 a242, v52\nv_accvgpr_write_b32 a243, v52\nv_accvgpr_write_b32 a244, v52\nv_accvgpr_write_b32 a245, v52\nv_accvgpr_write_b32 a246, v52\nv_accvgpr_write_b32 a247, v52\nv_accvgpr_write_b32 a248, v52\nv_accvgpr_write_b32 a249, v52\n"
"v_accvgpr_write_b32 a250, v52\nv_accvgpr_write_b32 a251, v52\nv_accvgpr_write_b32 a252, v52\nv_accvgpr_write_b32 a253, v52\nv_accvgpr_write_b32 a254, v52\nv_accvgpr_write_b32 a255, v52\nv_accvgpr_write_b32 a200, v52\nv_accvgpr_write_b32 a201, v52\n"
"v_accvgpr_write_b32 a202, v52\nv_accvgpr_write_b32 a203, v52\ns_lshl_b32 s89, s67, 21\ns_lshl_b32 s68, s93, 18\ns_add_u32 s89, s89, s68\ns_mov_b32 s66, 0\n.Lfb2:\ns_add_u32 s68, s89, 4096\n"
"buffer_load_dwordx4 a[112:115], v66, s[40:43], s89 offen offset:0\nbuffer_load_dwordx4 a[116:119], v66, s[40:43], s89 offen offset:64\nbuffer_load_dwordx4 a[120:123], v66, s[40:43], s89 offen offset:128\nbuffer_load_dwordx4 a[124:127], v66, s[40:43], s89 offen offset:192\nbuffer_load_dwordx4 a[128:131], v66, s[40:43], s68 offen offset:0\nbuffer_load_dwordx4 a[132:135], v66, s[40:43], s68 offen offset:64\nbuffer_load_dwordx4 a[136:139], v66, s[40:43], s68 offen offset:128\nbuffer_load_dwordx4 a[140:143], v66, s[40:43], s68 offen offset:192\n"
"buffer_load_ushort v8, v67, s[44:47], s89 offen offset:0\nbuffer_load_ushort v9, v67, s[44:47], s89 offen offset:256\nbuffer_load_ushort v10, v67, s[44:47], s89 offen offset:512\nbuffer_load_ushort v11, v67, s[44:47], s89 offen offset:768\nbuffer_load_ushort v12, v67, s[44:47], s68 offen offset:0\nbuffer_load_ushort v13, v67, s[44:47], s68 offen offset:256\nbuffer_load_ushort v14, v67, s[44:47], s68 offen offset:512\nbuffer_load_ushort v15, v67, s[44:47], s68 offen offset:768\n"
"s_waitcnt vmcnt(0)\nv_lshl_or_b32 v8, v9, 16, v8\nv_accvgpr_write_b32 a144, v8\nv_lshl_or_b32 v10, v11, 16, v10\nv_accvgpr_write_b32 a145, v10\nv_lshl_or_b32 v12, v13, 16, v12\nv_accvgpr_write_b32 a146, v12\nv_lshl_or_b32 v14, v15, 16, v14\n"
"v_accvgpr_write_b32 a147, v14\nbuffer_load_ushort v8, v67, s[44:47], s89 offen offset:32\nbuffer_load_ushort v9, v67, s[44:47], s89 offen offset:288\nbuffer_load_ushort v10, v67, s[44:47], s89 offen offset:544\nbuffer_load_ushort v11, v67, s[44:47], s89 offen offset:800\nbuffer_load_ushort v12, v67, s[44:47], s68 offen offset:32\nbuffer_load_ushort v13, v67, s[44:47], s68 offen offset:288\nbuffer_load_ushort v14, v67, s[44:47], s68 offen offset:544\n"
"buffer_load_ushort v15, v67, s[44:47], s68 offen offset:800\ns_waitcnt vmcnt(0)\nv_lshl_or_b32 v8, v9, 16, v8\nv_accvgpr_write_b32 a148, v8\nv_lshl_or_b32 v10, v11, 16, v10\nv_accvgpr_write_b32 a149, v10\nv_lshl_or_b32 v12, v13, 16, v12\nv_accvgpr_write_b32 a150, v12\n"
"v_lshl_or_b32 v14, v15, 16, v14\nv_accvgpr_write_b32 a151, v14\nbuffer_load_ushort v8, v67, s[44:47], s89 offen offset:64\nbuffer_load_ushort v9, v67, s[44:47], s89 offen offset:320\nbuffer_load_ushort v10, v67, s[44:47], s89 offen offset:576\nbuffer_load_ushort v11, v67, s[44:47], s89 offen offset:832\nbuffer_load_ushort v12, v67, s[44:47], s68 offen offset:64\nbuffer_load_ushort v13, v67, s[44:47], s68 offen offset:320\n"
"buffer_load_ushort v14, v67, s[44:47], s68 offen offset:576\nbuffer_load_ushort v15, v67, s[44:47], s68 offen offset:832\ns_waitcnt vmcnt(0)\nv_lshl_or_b32 v8, v9, 16, v8\nv_accvgpr_write_b32 a152, v8\nv_lshl_or_b32 v10, v11, 16, v10\nv_accvgpr_write_b32 a153, v10\nv_lshl_or_b32 v12, v13, 16, v12\n"
"v_accvgpr_write_b32 a154, v12\nv_lshl_or_b32 v14, v15, 16, v14\nv_accvgpr_write_b32 a155, v14\nbuffer_load_ushort v8, v67, s[44:47], s89 offen offset:96\nbuffer_load_ushort v9, v67, s[44:47], s89 offen offset:352\nbuffer_load_ushort v10, v67, s[44:47], s89 offen offset:608\nbuffer_load_ushort v11, v67, s[44:47], s89 offen offset:864\nbuffer_load_ushort v12, v67, s[44:47], s68 offen offset:96\n"
"buffer_load_ushort v13, v67, s[44:47], s68 offen offset:352\nbuffer_load_ushort v14, v67, s[44:47], s68 offen offset:608\nbuffer_load_ushort v15, v67, s[44:47], s68 offen offset:864\ns_waitcnt vmcnt(0)\nv_lshl_or_b32 v8, v9, 16, v8\nv_accvgpr_write_b32 a156, v8\nv_lshl_or_b32 v10, v11, 16, v10\nv_accvgpr_write_b32 a157, v10\n"
"v_lshl_or_b32 v12, v13, 16, v12\nv_accvgpr_write_b32 a158, v12\nv_lshl_or_b32 v14, v15, 16, v14\nv_accvgpr_write_b32 a159, v14\nbuffer_load_ushort v8, v67, s[44:47], s89 offen offset:128\nbuffer_load_ushort v9, v67, s[44:47], s89 offen offset:384\nbuffer_load_ushort v10, v67, s[44:47], s89 offen offset:640\nbuffer_load_ushort v11, v67, s[44:47], s89 offen offset:896\n"
"buffer_load_ushort v12, v67, s[44:47], s68 offen offset:128\nbuffer_load_ushort v13, v67, s[44:47], s68 offen offset:384\nbuffer_load_ushort v14, v67, s[44:47], s68 offen offset:640\nbuffer_load_ushort v15, v67, s[44:47], s68 offen offset:896\ns_waitcnt vmcnt(0)\nv_lshl_or_b32 v8, v9, 16, v8\nv_accvgpr_write_b32 a160, v8\nv_lshl_or_b32 v10, v11, 16, v10\n"
"v_accvgpr_write_b32 a161, v10\nv_lshl_or_b32 v12, v13, 16, v12\nv_accvgpr_write_b32 a162, v12\nv_lshl_or_b32 v14, v15, 16, v14\nv_accvgpr_write_b32 a163, v14\nbuffer_load_ushort v8, v67, s[44:47], s89 offen offset:160\nbuffer_load_ushort v9, v67, s[44:47], s89 offen offset:416\nbuffer_load_ushort v10, v67, s[44:47], s89 offen offset:672\n"
"buffer_load_ushort v11, v67, s[44:47], s89 offen offset:928\nbuffer_load_ushort v12, v67, s[44:47], s68 offen offset:160\nbuffer_load_ushort v13, v67, s[44:47], s68 offen offset:416\nbuffer_load_ushort v14, v67, s[44:47], s68 offen offset:672\nbuffer_load_ushort v15, v67, s[44:47], s68 offen offset:928\ns_waitcnt vmcnt(0)\nv_lshl_or_b32 v8, v9, 16, v8\nv_accvgpr_write_b32 a164, v8\n"
"v_lshl_or_b32 v10, v11, 16, v10\nv_accvgpr_write_b32 a165, v10\nv_lshl_or_b32 v12, v13, 16, v12\nv_accvgpr_write_b32 a166, v12\nv_lshl_or_b32 v14, v15, 16, v14\nv_accvgpr_write_b32 a167, v14\nbuffer_load_ushort v8, v67, s[44:47], s89 offen offset:192\nbuffer_load_ushort v9, v67, s[44:47], s89 offen offset:448\n"
"buffer_load_ushort v10, v67, s[44:47], s89 offen offset:704\nbuffer_load_ushort v11, v67, s[44:47], s89 offen offset:960\nbuffer_load_ushort v12, v67, s[44:47], s68 offen offset:192\nbuffer_load_ushort v13, v67, s[44:47], s68 offen offset:448\nbuffer_load_ushort v14, v67, s[44:47], s68 offen offset:704\nbuffer_load_ushort v15, v67, s[44:47], s68 offen offset:960\ns_waitcnt vmcnt(0)\nv_lshl_or_b32 v8, v9, 16, v8\n"
"v_accvgpr_write_b32 a168, v8\nv_lshl_or_b32 v10, v11, 16, v10\nv_accvgpr_write_b32 a169, v10\nv_lshl_or_b32 v12, v13, 16, v12\nv_accvgpr_write_b32 a170, v12\nv_lshl_or_b32 v14, v15, 16, v14\nv_accvgpr_write_b32 a171, v14\nbuffer_load_ushort v8, v67, s[44:47], s89 offen offset:224\n"
"buffer_load_ushort v9, v67, s[44:47], s89 offen offset:480\nbuffer_load_ushort v10, v67, s[44:47], s89 offen offset:736\nbuffer_load_ushort v11, v67, s[44:47], s89 offen offset:992\nbuffer_load_ushort v12, v67, s[44:47], s68 offen offset:224\nbuffer_load_ushort v13, v67, s[44:47], s68 offen offset:480\nbuffer_load_ushort v14, v67, s[44:47], s68 offen offset:736\nbuffer_load_ushort v15, v67, s[44:47], s68 offen offset:992\ns_waitcnt vmcnt(0)\n"
"v_lshl_or_b32 v8, v9, 16, v8\nv_accvgpr_write_b32 a172, v8\nv_lshl_or_b32 v10, v11, 16, v10\nv_accvgpr_write_b32 a173, v10\nv_lshl_or_b32 v12, v13, 16, v12\nv_accvgpr_write_b32 a174, v12\nv_lshl_or_b32 v14, v15, 16, v14\nv_accvgpr_write_b32 a175, v14\n"
"Qcvaa 0,112,0,24\nQmvaa 0,116,4\nQmvaa 0,120,8\nQmvaa 0,124,12\nQcvaa 4,128,0,24\nQmvaa 4,132,4\nQmvaa 4,136,8\nQmvaa 4,140,12\n"
"s_nop 7\nQe 0\nQe 1\nQe 2\nQe 3\nQe 4\nQe 5\nQe 6\n"
"Qe 7\ns_nop 0\nQk 16,0,1\nQk 17,2,3\nQk 18,4,5\nQk 19,6,7\ns_nop 1\nQmvva 80,16,144\n"
"Qmvva 84,16,148\nQmvva 88,16,152\nQmvva 92,16,156\nQmvva 96,16,160\nQmvva 100,16,164\nQmvva 104,16,168\nQmvva 108,16,172\nQmaav 176,204,16\n"
"Qcvaa 0,112,16,28\nQmvaa 0,116,20\nQmvaa 0,120,24\nQmvaa 0,124,28\nQcvaa 4,128,16,28\nQmvaa 4,132,20\nQmvaa 4,136,24\nQmvaa 4,140,28\n"
"s_nop 7\nQe 0\nQe 1\nQe 2\nQe 3\nQe 4\nQe 5\nQe 6\n"
"Qe 7\ns_nop 0\nQk 16,0,1\nQk 17,2,3\nQk 18,4,5\nQk 19,6,7\ns_nop 1\nQmvva 112,16,144\n"
"Qmvva 116,16,148\nQmvva 120,16,152\nQmvva 124,16,156\nQmvva 128,16,160\nQmvva 132,16,164\nQmvva 136,16,168\nQmvva 140,16,172\nQmaav 180,204,16\n"
"Qcvaa 0,112,32,32\nQmvaa 0,116,36\nQmvaa 0,120,40\nQmvaa 0,124,44\nQcvaa 4,128,32,32\nQmvaa 4,132,36\nQmvaa 4,136,40\nQmvaa 4,140,44\n"
"s_nop 7\nQe 0\nQe 1\nQe 2\nQe 3\nQe 4\nQe 5\nQe 6\n"
"Qe 7\ns_nop 0\nQk 16,0,1\nQk 17,2,3\nQk 18,4,5\nQk 19,6,7\ns_nop 1\nQmvva 144,16,144\n"
"Qmvva 148,16,148\nQmvva 152,16,152\nQmvva 156,16,156\nQmvva 160,16,160\nQmvva 164,16,164\nQmvva 168,16,168\nQmvva 172,16,172\nQmaav 184,204,16\n"
"Qcvaa 0,112,48,36\nQmvaa 0,116,52\nQmvaa 0,120,56\nQmvaa 0,124,60\nQcvaa 4,128,48,36\nQmvaa 4,132,52\nQmvaa 4,136,56\nQmvaa 4,140,60\n"
"s_nop 7\nQe 0\nQe 1\nQe 2\nQe 3\nQe 4\nQe 5\nQe 6\n"
"Qe 7\ns_nop 0\nQk 16,0,1\nQk 17,2,3\nQk 18,4,5\nQk 19,6,7\ns_nop 1\nQmvva 176,16,144\n"
"Qmvva 180,16,148\nQmvva 184,16,152\nQmvva 188,16,156\nQmvva 192,16,160\nQmvva 196,16,164\nQmvva 200,16,168\nQmvva 204,16,172\nQmaav 188,204,16\n"
"Qcvaa 0,112,64,40\nQmvaa 0,116,68\nQmvaa 0,120,72\nQmvaa 0,124,76\nQcvaa 4,128,64,40\nQmvaa 4,132,68\nQmvaa 4,136,72\nQmvaa 4,140,76\n"
"s_nop 7\nQe 0\nQe 1\nQe 2\nQe 3\nQe 4\nQe 5\nQe 6\n"
"Qe 7\ns_nop 0\nQk 16,0,1\nQk 17,2,3\nQk 18,4,5\nQk 19,6,7\ns_nop 1\nQmvva 208,16,144\n"
"Qmvva 212,16,148\nQmvva 216,16,152\nQmvva 220,16,156\nQmvva 224,16,160\nQmvva 228,16,164\nQmvva 232,16,168\nQmvva 236,16,172\nQmaav 192,204,16\n"
"Qcvaa 0,112,80,44\nQmvaa 0,116,84\nQmvaa 0,120,88\nQmvaa 0,124,92\nQcvaa 4,128,80,44\nQmvaa 4,132,84\nQmvaa 4,136,88\nQmvaa 4,140,92\n"
"s_nop 7\nQe 0\nQe 1\nQe 2\nQe 3\nQe 4\nQe 5\nQe 6\n"
"Qe 7\ns_nop 0\nQk 16,0,1\nQk 17,2,3\nQk 18,4,5\nQk 19,6,7\ns_nop 1\nQmvva 240,16,144\n"
"Qmvva 244,16,148\nQmvva 248,16,152\nQmvva 252,16,156\nQmava 208,16,160\nQmava 212,16,164\nQmava 216,16,168\nQmava 220,16,172\nQmaav 196,204,16\n"
"Qcvaa 0,112,96,48\nQmvaa 0,116,100\nQmvaa 0,120,104\nQmvaa 0,124,108\nQcvaa 4,128,96,48\nQmvaa 4,132,100\nQmvaa 4,136,104\nQmvaa 4,140,108\n"
"s_nop 7\nQe 0\nQe 1\nQe 2\nQe 3\nQe 4\nQe 5\nQe 6\n"
"Qe 7\ns_nop 0\nQk 16,0,1\nQk 17,2,3\nQk 18,4,5\nQk 19,6,7\ns_nop 1\nQmava 224,16,144\n"
"Qmava 228,16,148\nQmava 232,16,152\nQmava 236,16,156\nQmava 240,16,160\nQmava 244,16,164\nQmava 248,16,168\nQmava 252,16,172\nQmaav 200,204,16\n"
"s_add_u32 s89, s89, 8192\ns_add_u32 s66, s66, 1\ns_cmp_lt_u32 s66, 32\ns_cbranch_scc1 .Lfb2\ns_nop 7\nv_accvgpr_read_b32 v0, a176\nv_accvgpr_read_b32 v4, a180\nv_accvgpr_read_b32 v8, a184\n"
"v_accvgpr_read_b32 v12, a188\nv_accvgpr_read_b32 v16, a192\nv_accvgpr_read_b32 v20, a196\nv_accvgpr_read_b32 v24, a200\ns_mov_b32 s73, 0\nv_mov_b32 v30, s73\nv_mov_b32 v31, 0\nv_mov_b32 v58, 0xb9800000\n"
"v_mov_b32 v59, 0\nv_and_b32 v66, 16, v60\nv_mov_b32 v67, 0xffff\nv_lshlrev_b32 v66, v66, v67\ns_mov_b32 vcc_lo, 0x00c0000c\ns_mov_b32 vcc_hi, 0xc0000c00\nv_cndmask_b32 v67, 0, v66, vcc\ns_lshr_b64 vcc, vcc, 2\n"
"v_cndmask_b32 v66, 0, v66, vcc\ns_cmp_eq_u32 s67, 0\ns_cbranch_scc1 .Lfbret0\ns_branch .Lfbret1\n.Lf2_w:\ns_mov_b32 s97, 0\n.Lf2_l:\ns_sleep 1\n"
"buffer_load_dword v78, v76, s[24:27], 0 offen sc1\ns_waitcnt vmcnt(0)\ns_add_u32 s97, s97, 1\ns_cmp_gt_u32 s97, 1048576\ns_cbranch_scc1 .Lf2_r\nv_sub_u32 v66, v78, v65\nv_cmp_gt_u32 vcc, 2, v66\ns_and_b64 vcc, vcc, s[28:29]\n"
"s_cbranch_scc1 .Lf2_l\ns_branch .Lf2_r\n.Lf3_w:\ns_mov_b32 s97, 0\n.Lf3_l:\ns_sleep 1\nbuffer_load_dword v78, v76, s[24:27], 0 offen sc1\ns_waitcnt vmcnt(0)\n"
"s_add_u32 s97, s97, 1\ns_cmp_gt_u32 s97, 1048576\ns_cbranch_scc1 .Lf3_r\nv_sub_u32 v66, v78, v65\nv_cmp_gt_u32 vcc, 3, v66\ns_and_b64 vcc, vcc, s[28:29]\ns_cbranch_scc1 .Lf3_l\ns_branch .Lf3_r\n"
".Lx0_w:\ns_mov_b32 s97, 0\n.Lx0_l:\ns_sleep 1\nbuffer_load_dword v78, v76, s[24:27], 0 offen sc1\ns_waitcnt vmcnt(0)\ns_add_u32 s97, s97, 1\ns_cmp_gt_u32 s97, 1048576\n"
"s_cbranch_scc1 .Lx0_r\nv_sub_u32 v66, v78, v65\nv_cmp_gt_u32 vcc, 1, v66\ns_and_b64 vcc, vcc, s[28:29]\ns_cbranch_scc1 .Lx0_l\ns_branch .Lx0_r\n.Lx1_w:\ns_mov_b32 s97, 0\n"
".Lx1_l:\ns_sleep 1\nbuffer_load_dword v78, v76, s[24:27], 0 offen sc1\ns_waitcnt vmcnt(0)\ns_add_u32 s97, s97, 1\ns_cmp_gt_u32 s97, 1048576\ns_cbranch_scc1 .Lx1_r\nv_sub_u32 v66, v78, v65\n"
"v_cmp_gt_u32 vcc, 1, v66\ns_and_b64 vcc, vcc, s[28:29]\ns_cbranch_scc1 .Lx1_l\ns_branch .Lx1_r\n.Lexit:\n"
  : "=&s"(t0), "=&s"(t1)
  : "s"(__builtin_amdgcn_kernarg_segment_ptr()), "s"(blockIdx.x), "s"((unsigned)(size_t)lds), "s"(__builtin_amdgcn_readfirstlane(threadIdx.x >> 6))
  : "v0", "v1", "v2", "v3", "v4", "v5", "v6", "v7", "v8", "v9", "v10", "v11", "v12", "v13", "v14", "v15", "v16", "v17", "v18", "v19", "v20", "v21", "v22", "v23", "v24", "v25", "v26", "v27", "v28", "v29", "v30", "v31", "v32", "v33", "v34", "v35", "v36", "v37", "v38", "v39", "v40", "v41", "v42", "v43", "v44", "v45", "v46", "v47", "v48", "v49", "v50", "v51", "v52", "v53", "v54", "v55", "v56", "v57", "v58", "v59", "v60", "v61", "v62", "v63", "v64", "v65", "v66", "v67", "v68", "v69", "v70", "v71", "v72", "v73", "v74", "v75", "v76", "v77", "v78", "v79", "v80", "v81", "v82", "v83", "v84", "v85", "v86", "v87", "v88", "v89", "v90", "v91", "v92", "v93", "v94", "v95", "v96", "v97", "v98", "v99", "v100", "v101", "v102", "v103", "v104", "v105", "v106", "v107", "v108", "v109", "v110", "v111", "v112", "v113", "v114", "v115", "v116", "v117", "v118", "v119", "v120", "v121", "v122", "v123", "v124", "v125", "v126", "v127", "v128", "v129", "v130", "v131", "v132", "v133", "v134", "v135", "v136", "v137", "v138", "v139", "v140", "v141", "v142", "v143", "v144", "v145", "v146", "v147", "v148", "v149", "v150", "v151", "v152", "v153", "v154", "v155", "v156", "v157", "v158", "v159", "v160", "v161", "v162", "v163", "v164", "v165", "v166", "v167", "v168", "v169", "v170", "v171", "v172", "v173", "v174", "v175", "v176", "v177", "v178", "v179", "v180", "v181", "v182", "v183", "v184", "v185", "v186", "v187", "v188", "v189", "v190", "v191", "v192", "v193", "v194", "v195", "v196", "v197", "v198", "v199", "v200", "v201", "v202", "v203", "v204", "v205", "v206", "v207", "v208", "v209", "v210", "v211", "v212", "v213", "v214", "v215", "v216", "v217", "v218", "v219", "v220", "v221", "v222", "v223", "v224", "v225", "v226", "v227", "v228", "v229", "v230", "v231", "v232", "v233", "v234", "v235", "v236", "v237", "v238", "v239", "v240", "v241", "v242", "v243", "v244", "v245", "v246", "v247", "v248", "v249", "v250", "v251", "v252", "v253", "v254", "v255", "a0", "a1", "a2", "a3", "a4", "a5", "a6", "a7", "a8", "a9", "a10", "a11", "a12", "a13", "a14", "a15", "a16", "a17", "a18", "a19", "a20", "a21", "a22", "a23", "a24", "a25", "a26", "a27", "a28", "a29", "a30", "a31", "a32", "a33", "a34", "a35", "a36", "a37", "a38", "a39", "a40", "a41", "a42", "a43", "a44", "a45", "a46", "a47", "a48", "a49", "a50", "a51", "a52", "a53", "a54", "a55", "a56", "a57", "a58", "a59", "a60", "a61", "a62", "a63", "a64", "a65", "a66", "a67", "a68", "a69", "a70", "a71", "a72", "a73", "a74", "a75", "a76", "a77", "a78", "a79", "a80", "a81", "a82", "a83", "a84", "a85", "a86", "a87", "a88", "a89", "a90", "a91", "a92", "a93", "a94", "a95", "a96", "a97", "a98", "a99", "a100", "a101", "a102", "a103", "a104", "a105", "a106", "a107", "a108", "a109", "a110", "a111", "a112", "a113", "a114", "a115", "a116", "a117", "a118", "a119", "a120", "a121", "a122", "a123", "a124", "a125", "a126", "a127", "a128", "a129", "a130", "a131", "a132", "a133", "a134", "a135", "a136", "a137", "a138", "a139", "a140", "a141", "a142", "a143", "a144", "a145", "a146", "a147", "a148", "a149", "a150", "a151", "a152", "a153", "a154", "a155", "a156", "a157", "a158", "a159", "a160", "a161", "a162", "a163", "a164", "a165", "a166", "a167", "a168", "a169", "a170", "a171", "a172", "a173", "a174", "a175", "a176", "a177", "a178", "a179", "a180", "a181", "a182", "a183", "a184", "a185", "a186", "a187", "a188", "a189", "a190", "a191", "a192", "a193", "a194", "a195", "a196", "a197", "a198", "a199", "a200", "a201", "a202", "a203", "a204", "a205", "a206", "a207", "a208", "a209", "a210", "a211", "a212", "a213", "a214", "a215", "a216", "a217", "a218", "a219", "a220", "a221", "a222", "a223", "a224", "a225", "a226", "a227", "a228", "a229", "a230", "a231", "a232", "a233", "a234", "a235", "a236", "a237", "a238", "a239", "a240", "a241", "a242", "a243", "a244", "a245", "a246", "a247", "a248", "a249", "a250", "a251", "a252", "a253", "a254", "a255", "s8", "s9", "s10", "s11", "s12", "s13", "s14", "s15", "s16", "s17", "s18", "s19", "s20", "s21", "s22", "s23", "s24", "s25", "s26", "s27", "s28", "s29", "s30", "s31", "s33", "s34", "s35", "s36", "s37", "s38", "s39", "s40", "s41", "s42", "s43", "s44", "s45", "s46", "s47", "s48", "s49", "s50", "s51", "s52", "s53", "s54", "s55", "s56", "s57", "s58", "s59", "s60", "s61", "s62", "s63", "s64", "s65", "s66", "s67", "s68", "s69", "s70", "s71", "s72", "s73", "s74", "s75", "s76", "s77", "s78", "s79", "s80", "s81", "s82", "s83", "s84", "s85", "s86", "s87", "s88", "s89", "s90", "s91", "s92", "s93", "s94", "s95", "s96", "s97", "s98", "s99", "s100", "s101", "vcc", "scc", "memory");
}

extern "C" void kernel_launch(void* const* d_in, const int* in_sizes, int n_in, void* d_out, int out_size, void* d_ws, size_t ws_size, hipStream_t stream) {
  typedef const unsigned short* P;
  (void)hipFuncSetAttribute((const void*)mha2_asm, hipFuncAttributeMaxDynamicSharedMemorySize, 163840);
  hipLaunchKernelGGL(mha2_asm, dim3(256), dim3(256), 163840, stream, (P)d_in[0], (P)d_in[3], (P)d_in[1], (P)d_in[2], (P)d_in[5], (P)d_in[8], (P)d_in[6], (P)d_in[7],
                     (P)d_in[4], (P)d_in[9], (char*)d_ws, (unsigned short*)d_out);
}
```

```cpp
#include <hip/hip_runtime.h>
#include <stdint.h>

__global__ __launch_bounds__(256, 1) __attribute__((aligned(4096)))
void mha2_asm(const unsigned short* X0, const unsigned short* W0, const unsigned short* K0, const unsigned short* V0,
              const unsigned short* X1, const unsigned short* W1, const unsigned short* K1, const unsigned short* V1,
              const unsigned short* WO0, const unsigned short* WO1, char* ws, unsigned short* out) {
  extern __shared__ __attribute__((aligned(16))) char lds[];
  unsigned t0, t1;
  asm volatile(
".macro Qta d,v,o\nds_read_b64_tr_b16 a[\\d:\\d+1], v\\v offset:\\o\n.endm\n.macro Qda d,v,o\nds_read_b128 a[\\d:\\d+3], v\\v offset:\\o\n.endm\n.macro Qzvaa d,a,b\nv_mfma_f32_16x16x32_bf16 v[\\d:\\d+3], a[\\a:\\a+3], a[\\b:\\b+3], 0\n"
".endm\n.macro Qcvaa d,a,b,c\nv_mfma_f32_16x16x32_bf16 v[\\d:\\d+3], a[\\a:\\a+3], a[\\b:\\b+3], v[\\c:\\c+3]\n.endm\n.macro Qk d,a,b\nv_cvt_pk_bf16_f32 v\\d, v\\a, v\\b\n.endm\n.macro Qmvaa d,a,b\n"
"v_mfma_f32_16x16x32_bf16 v[\\d:\\d+3], a[\\a:\\a+3], a[\\b:\\b+3], v[\\d:\\d+3]\n.endm\n.macro Qtv d,v,o\nds_read_b64_tr_b16 v[\\d:\\d+1], v\\v offset:\\o\n.endm\n.macro Qa d,a,b\nv_add_f32 v\\d, v\\a, v\\b\n.endm\n"
".macro Qe d\nv_exp_f32 v\\d, v\\d\n.endm\n.macro Qmvva d,a,b\nv_mfma_f32_16x16x32_bf16 v[\\d:\\d+3], v[\\a:\\a+3], a[\\b:\\b+3], v[\\d:\\d+3]\n.endm\n.macro Qmava d,a,b\nv_mfma_f32_16x16x32_bf16 a[\\d:\\d+3], v[\\a:\\a+3], a[\\b:\\b+3], a[\\d:\\d+3]\n"
".endm\n.macro Qzvvv d,a,b\nv_mfma_f32_16x16x32_bf16 v[\\d:\\d+3], v[\\a:\\a+3], v[\\b:\\b+3], 0\n.endm\n.macro Qzvav d,a,b\nv_mfma_f32_16x16x32_bf16 v[\\d:\\d+3], a[\\a:\\a+3], v[\\b:\\b+3], 0\n.endm\n.macro Qmvav d,a,b\n"
"v_mfma_f32_16x16x32_bf16 v[\\d:\\d+3], a[\\a:\\a+3], v[\\b:\\b+3], v[\\d:\\d+3]\n.endm\n.macro Qcaav d,a,b,c\nv_mfma_f32_16x16x32_bf16 a[\\d:\\d+3], a[\\a:\\a+3], v[\\b:\\b+3], a[\\c:\\c+3]\n.endm\n.macro Qmaav d,a,b\nv_mfma_f32_16x16x32_bf16 a[\\d:\\d+3], a[\\a:\\a+3], v[\\b:\\b+3], a[\\d:\\d+3]\n.endm\n"
".macro Qcvav d,a,b,c\nv_mfma_f32_16x16x32_bf16 v[\\d:\\d+3], a[\\a:\\a+3], v[\\b:\\b+3], v[\\c:\\c+3]\n.endm\n.macro Qmaaa d,a,b\nv_mfma_f32_16x16x32_bf16 a[\\d:\\d+3], a[\\a:\\a+3], a[\\b:\\b+3], a[\\d:\\d+3]\n.endm\n.macro Qdv d,v,o\nds_read_b128 v[\\d:\\d+3], v\\v offset:\\o\n"
".endm\n.p2align 3\ns_and_b32 s33, %3, 4\ns_lshl_b32 s30, s33, 3\ns_load_dwordx8 s[8:15], %2, s30\ns_lshl_b32 s33, s33, 1\ns_load_dwordx4 s[20:23], %2, 0x50\ns_load_dwordx2 s[16:17], %2, s33 offset:0x40\n"
"s_memtime s[80:81]\ns_memrealtime s[82:83]\ns_bfe_u32 s19, %3, 0x30003\ns_and_b32 s39, %3, 3\ns_lshr_b32 %1, %3, 6\ns_lshl2_add_u32 s30, s39, %1\ns_lshl3_add_u32 %0, s30, s19\ns_and_b32 %1, s19, 4\n"
"s_add_u32 s18, s19, %1\ns_lshr_b32 s39, %5, 1\ns_lshl1_add_u32 s18, s18, s39\ns_lshl_b32 %1, %1, 11\ns_mul_i32 s39, s30, 286720\nv_mbcnt_lo_u32_b32 v0, -1, 0\nv_mbcnt_hi_u32_b32 v0, -1, v0\nv_lshlrev_b32 v60, 4, v0\n"
"s_add_u32 m0, %4, 160768\ns_getpc_b64 s[56:57]\n.Lpc0:\ns_and_b32 s56, s56, 0xffffff80\ns_and_b32 s57, s57, 0xffff\ns_mov_b32 s58, .Lend-.Lpc0\ns_mov_b32 s59, 0x20000\ns_and_b32 s94, %0, 31\n"
"s_lshl_b32 s94, s94, 2\ns_add_u32 s94, s94, %5\ns_lshl_b32 s94, s94, 10\nbuffer_load_dwordx4 v60, s[56:59], s94 offen lds\ns_mov_b32 s54, 5242880\ns_mov_b32 s55, 0x20000\ns_mov_b32 s50, 286720\ns_mov_b32 s51, 0x20000\n"
"s_mov_b32 s70, %5\ns_and_b32 s92, s70, 1\ns_lshr_b32 s93, s70, 1\nv_lshrrev_b32 v2, 4, v0\ns_lshl_b32 s68, s92, 18\nv_and_b32 v4, 3, v0\nv_lshlrev_b32 v4, 4, v4\nv_and_b32 v1, 1, v2\n"
"v_lshl_add_u32 v4, v1, 6, v4\nv_lshrrev_b32 v1, 5, v0\nv_lshl_add_u32 v4, v1, 15, v4\nv_bfe_u32 v1, v0, 2, 2\nv_lshl_add_u32 v4, v1, 13, v4\nv_add_u32 v62, s68, v4\ns_lshl_b32 s87, s18, 8\ns_mul_i32 s62, s70, 4128\n"
"s_add_u32 s62, s62, %4\ns_add_u32 s62, s62, 33024\ns_lshl_b32 s69, s92, 1\nv_lshrrev_b32 v3, 5, v0\nv_and_b32 v4, 3, v0\nv_xor_b32 v4, s69, v4\nv_lshlrev_b32 v4, 4, v4\nv_lshl_add_u32 v4, v3, 6, v4\n"
"v_bfe_u32 v1, v0, 2, 3\nv_mul_u32_u24 v64, 1280, v1\nv_add_u32 v64, v64, v4\ns_mul_i32 s86, s93, 143360\ns_mul_i32 s68, s92, 10240\ns_add_u32 s86, s86, s68\ns_mul_i32 s63, s92, 14336\ns_mul_i32 s68, s93, 7168\n"
"s_add_u32 s63, s63, s68\ns_add_u32 s63, s63, %4\ns_add_u32 s63, s63, 99072\ns_lshl_b32 s34, s33, 1\ns_add_u32 s34, s34, s30\ns_mul_hi_u32 s35, s34, 6291456\ns_mul_i32 s34, s34, 6291456\ns_lshl_b32 s36, s30, 5\n"
"s_and_b32 s37, s18, 30\ns_add_u32 s36, s36, s37\ns_lshl_b32 s36, s36, 18\ns_sub_u32 s36, s36, 1024\ns_subb_u32 s37, 0, 0\ns_mul_i32 s38, s30, 573440\ns_mul_i32 s33, s33, 160\ns_add_u32 s38, s38, s33\n"
"s_and_b32 s33, s19, 3\ns_mul_i32 s33, s33, 320\ns_add_u32 s38, s38, s33\ns_mov_b32 s26, 6291456\ns_mov_b32 s27, 0x20000\nv_and_b32 v5, 7, v0\nv_lshlrev_b32 v5, 7, v5\nv_add_u32 v76, 5242880, v5\n"
"s_lshl_b32 s68, s70, 2\ns_add_u32 s68, s68, 4\nv_bfe_u32 v5, v0, 3, 1\nv_mad_u32_u24 v76, v5, s68, v76\ns_mov_b32 s42, 2622464\ns_mov_b32 s43, 0x20000\ns_mov_b32 s46, 2622464\ns_mov_b32 s47, 0x20000\n"
"s_mov_b32 s58, 0\ns_mov_b32 s59, 0x20000\ns_mov_b32 s65, 0\ns_mov_b32 s60, %1\ns_add_u32 s90, s60, 260096\ns_mov_b32 s67, 0\ns_mov_b32 s72, 0x3e0293ee\ns_lshl_b32 s68, s70, 8\n"
"s_lshl_b32 s69, s93, 5\ns_or_b32 s88, s68, s69\nv_bfe_u32 v1, v0, 2, 2\nv_mul_u32_u24 v4, 0x8100, v1\ns_waitcnt lgkmcnt(0)\ns_mov_b32 s52, s10\ns_and_b32 s53, s11, 0xffff\ns_add_u32 m0, s62, 0\n"
"s_add_u32 s66, s87, 0\nbuffer_load_dwordx4 v62, s[52:55], s66 offen lds\ns_add_u32 s66, s87, 64504\nbuffer_load_dwordx4 v62, s[52:55], s66 offen offset:1032 lds\ns_add_u32 s66, s87, 129008\nbuffer_load_dwordx4 v62, s[52:55], s66 offen offset:2064 lds\ns_add_u32 s66, s87, 193512\nbuffer_load_dwordx4 v62, s[52:55], s66 offen offset:3096 lds\n"
"s_add_u32 s48, s8, s39\ns_addc_u32 s49, s9, 0\ns_and_b32 s49, s49, 0xffff\ns_add_u32 m0, s63, 33024\ns_add_u32 s97, s86, 0\nbuffer_load_dwordx4 v64, s[48:51], s97 offen lds\ns_add_u32 s97, s86, 19456\nbuffer_load_dwordx4 v64, s[48:51], s97 offen offset:1024 lds\n"
"s_add_u32 s97, s86, 38912\nbuffer_load_dwordx4 v64, s[48:51], s97 offen offset:2048 lds\ns_add_u32 s97, s86, 58368\nbuffer_load_dwordx4 v64, s[48:51], s97 offen offset:3072 lds\ns_add_u32 m0, s63, 37120\ns_add_u32 s97, s86, 81920\nbuffer_load_dwordx4 v64, s[48:51], s97 offen lds\ns_add_u32 s97, s86, 101376\n"
"buffer_load_dwordx4 v64, s[48:51], s97 offen offset:1024 lds\ns_add_u32 s97, s86, 120832\nbuffer_load_dwordx4 v64, s[48:51], s97 offen offset:2048 lds\ns_add_u32 m0, s62, 16512\ns_add_u32 s66, s87, 128\nbuffer_load_dwordx4 v62, s[52:55], s66 offen lds\ns_add_u32 s20, s20, s34\ns_addc_u32 s21, s21, s35\n"
"s_mov_b32 s24, s20\ns_and_b32 s25, s21, 0xffff\nbuffer_load_dword v65, v76, s[24:27], 0 offen sc1\ns_add_u32 s40, s12, s36\ns_addc_u32 s41, s13, s37\ns_and_b32 s41, s41, 0xffff\ns_add_u32 s44, s14, s36\ns_addc_u32 s45, s15, s37\n"
"s_add_u32 s66, s87, 64632\nbuffer_load_dwordx4 v62, s[52:55], s66 offen offset:1032 lds\ns_and_b32 s45, s45, 0xffff\ns_mov_b32 s56, s20\ns_and_b32 s57, s21, 0xffff\ns_add_u32 s16, s16, -4096\ns_addc_u32 s17, s17, -1\ns_add_u32 s22, s22, s38\n"
"s_addc_u32 s23, s23, 0\nv_and_b32 v1, 3, v0\nv_lshl_or_b32 v4, v1, 4, v4\nv_and_b32 v1, 1, v2\nv_lshl_or_b32 v4, v1, 7, v4\ns_add_u32 s66, s87, 129136\nbuffer_load_dwordx4 v62, s[52:55], s66 offen offset:2064 lds\nv_lshl_or_b32 v4, v3, 6, v4\n"
"v_xor_b32 v4, s88, v4\nv_add_u32 v69, 1024, v4\nv_bfe_u32 v1, v0, 1, 2\nv_mul_u32_u24 v4, 0x8100, v1\nv_and_b32 v1, 9, v0\nv_lshl_or_b32 v4, v1, 4, v4\nv_lshl_or_b32 v4, v2, 5, v4\nv_xor_b32 v4, s68, v4\n"
"s_add_u32 s66, s87, 193640\nbuffer_load_dwordx4 v62, s[52:55], s66 offen offset:3096 lds\nv_add_u32 v72, 1024, v4\ns_lshl_b32 s68, s70, 12\ns_add_u32 s61, s68, %4\ns_mul_i32 s68, s92, 8320\ns_lshl_b32 s69, s93, 12\ns_add_u32 s68, s68, s69\n"
"s_add_u32 s68, s68, %4\ns_add_u32 s64, s68, 16384\ns_lshl_b32 s69, s93, 11\ns_mov_b64 exec, 0\ns_cmp_eq_u32 s70, 0\ns_cbranch_scc1 .Lpg0\ns_cmp_eq_u32 s70, 1\ns_cbranch_scc1 .Lpg1\n"
"s_cmp_eq_u32 s70, 2\ns_cbranch_scc1 .Lpg2\ns_branch .Lpg3\n.Ltr0:\ns_mov_b64 exec, -1\ns_add_u32 s69, s69, %4\nv_bfe_u32 v3, v0, 3, 1\nv_lshlrev_b32 v4, 1, v3\n"
"v_xor_b32 v4, v4, v2\nv_and_b32 v1, 7, v0\nv_lshl_add_u32 v4, v1, 2, v4\nv_lshlrev_b32 v4, 4, v4\nv_bfe_u32 v1, v0, 2, 2\nv_and_b32 v5, 255, v4\ns_add_u32 m0, s63, 0\ns_add_u32 s97, s86, 128\n"
"buffer_load_dwordx4 v64, s[48:51], s97 offen lds\nv_lshl_or_b32 v1, v1, 12, v5\nv_add_u32 v68, s69, v1\ns_mul_i32 s68, s92, 7168\ns_add_u32 s68, s68, %4\ns_add_u32 s68, s68, 99072\nv_mul_u32_u24 v1, 14336, v3\nv_add3_u32 v63, v1, v4, s68\n"
"v_lshrrev_b32 v3, 5, v0\nv_and_b32 v4, 31, v0\ns_add_u32 s97, s86, 19584\nbuffer_load_dwordx4 v64, s[48:51], s97 offen offset:1024 lds\nv_lshlrev_b32 v4, 3, v4\nv_and_b32 v1, 1, v2\nv_lshl_add_u32 v4, v1, 13, v4\nv_lshl_add_u32 v4, v3, 12, v4\n"
"v_add_u32 v4, 16384, v4\nv_add_u32 v70, s69, v4\nv_add_u32 v71, 66048, v70\ns_mul_i32 s68, s93, 8256\ns_add_u32 s68, s68, %4\ns_add_u32 s97, s86, 39040\nbuffer_load_dwordx4 v64, s[48:51], s97 offen offset:2048 lds\ns_add_u32 s68, s68, 33024\n"
"v_mul_u32_u24 v3, 1032, v2\nv_and_b32 v4, 15, v0\nv_lshlrev_b32 v4, 4, v4\nv_add3_u32 v61, v3, v4, s68\ns_lshl_b32 s68, s70, 2\ns_add_u32 s69, %4, 131968\ns_add_u32 s68, s68, s69\nv_mov_b32 v73, s68\n"
"s_add_u32 s97, s86, 58496\nbuffer_load_dwordx4 v64, s[48:51], s97 offen offset:3072 lds\nv_and_b32 v3, 3, v0\nv_lshl_add_u32 v74, v3, 2, s69\nv_mov_b32 v75, 0\ns_mov_b32 s79, -2031\nds_write_b32 v73, v75\ns_waitcnt lgkmcnt(0)\n"
"s_mul_i32 s91, s70, 7168\ns_add_u32 s91, s91, %4\ns_add_u32 s91, s91, 98304\ns_add_u32 m0, s63, 4096\ns_add_u32 s97, s86, 82048\nbuffer_load_dwordx4 v64, s[48:51], s97 offen lds\ns_lshl_b32 s96, s70, 11\ns_add_u32 s96, s96, %4\n"
"v_and_b32 v3, 1, v0\nv_mul_u32_u24 v3, 0xaaaa, v3\nv_or_b32 v77, 0x4444, v3\ns_and_b32 s68, s19, 4\ns_lshl_b32 s28, 15, s68\ns_mov_b32 s29, 0\ns_and_b32 s68, s19, 4\ns_add_u32 s97, s86, 101504\n"
"buffer_load_dwordx4 v64, s[48:51], s97 offen offset:1024 lds\ns_add_u32 s68, s68, s19\ns_lshl_b32 s71, s68, 18\ns_mul_i32 s69, s70, 0x80100\ns_and_b32 s69, s69, 0x100100\ns_add_u32 s68, s71, s69\ns_lshr_b32 s98, %0, 1\ns_xor_b32 s98, s98, %0\n"
"s_lshl_b32 s98, s98, 11\ns_and_b32 s98, s98, 8192\ns_add_u32 s97, s86, 120960\nbuffer_load_dwordx4 v64, s[48:51], s97 offen offset:2048 lds\ns_add_u32 s68, s68, s98\ns_add_u32 s68, s68, 7168\ns_add_u32 s30, s20, s68\ns_addc_u32 s31, s21, 0\n"
"s_mov_b32 s77, 0\nv_bfe_i32 v8, v0, 3, 1\nv_lshlrev_b32 v4, 10, v0\nv_and_b32 v4, 0x6000, v4\nv_lshrrev_b32 v3, 5, v0\nv_lshl_or_b32 v4, v3, 16, v4\nv_and_b32 v3, 0x70, v60\nv_or_b32 v4, v3, v4\n"
"v_or_b32 v8, 1, v8\nv_mov_b32 v9, v8\nv_mov_b32 v10, v8\nv_mov_b32 v11, v8\nbuffer_store_dwordx4 v[8:11], v4, s[24:27], s68 offen offset:0 sc1\ns_nop 1\ns_lshl_b32 s68, s70, 8\nv_lshl_or_b32 v3, v2, 15, v60\n"
"v_xor_b32 v79, s68, v3\ns_and_b32 s68, s19, 3\ns_lshl_b32 s99, 3072, s68\ns_or_b32 s99, s99, 0x262\ns_getpc_b64 s[100:101]\n.Lpc1:\ns_min_u32 s94, s94, .Lend-.Lpc1-1024\ns_add_u32 s100, s100, s94\n"
"s_addc_u32 s101, s101, 0\ns_mov_b32 s94, 0x7ffff000\ns_mul_i32 s68, %0, 40960\ns_mul_i32 s69, s93, 20480\ns_add_u32 s68, s68, s69\ns_lshl_b32 s69, s92, 10\ns_add_u32 s68, s68, s69\ns_add_u32 s68, s68, 2048\n"
"s_add_u32 s94, s10, s68\ns_addc_u32 s95, s11, 0\ns_and_b32 s95, s95, 0xffff\ns_mov_b32 s75, 17408\n.Lround:\ns_mov_b64 vcc, 0\ns_waitcnt vmcnt(13)\ns_barrier\n"
"Qta 112,61,0\nQta 114,61,512\nQda 208,63,33024\nQda 212,63,34048\nQda 216,63,35072\nQda 220,63,36096\nQda 224,63,37120\nQda 228,63,38144\n"
"Qda 232,63,39168\ns_mov_b64 exec, 0\ns_cmp_eq_u32 s70, 0\ns_cbranch_scc1 .Lpg4\ns_cmp_eq_u32 s70, 1\ns_cbranch_scc1 .Lpg5\ns_cmp_eq_u32 s70, 2\ns_cbranch_scc1 .Lpg6\n"
"s_branch .Lpg7\n.Ltr9:\ns_mov_b64 exec, -1\nQda 176,63,33536\nQta 116,61,4128\nQta 118,61,4640\ns_add_u32 m0, s62, 33024\ns_add_u32 s66, s87, 524288\n"
"buffer_load_dwordx4 v62, s[52:55], s66 offen lds\nQta 120,61,8\nQta 122,61,520\nQta 124,61,4136\ns_add_u32 s66, s87, 588792\nbuffer_load_dwordx4 v62, s[52:55], s66 offen offset:1032 lds\nQta 126,61,4648\nQta 128,61,256\n"
"Qta 130,61,768\ns_add_u32 s66, s87, 653296\nbuffer_load_dwordx4 v62, s[52:55], s66 offen offset:2064 lds\nQta 132,61,4384\nQta 134,61,4896\nQta 136,61,264\ns_add_u32 s66, s87, 717800\nbuffer_load_dwordx4 v62, s[52:55], s66 offen offset:3096 lds\n"
"Qta 138,61,776\nQta 140,61,4392\nQta 142,61,4904\ns_add_u32 m0, s62, 49536\ns_add_u32 s66, s87, 524416\nbuffer_load_dwordx4 v62, s[52:55], s66 offen lds\ns_waitcnt lgkmcnt(15)\nQzvaa 0,112,208\n"
"Qzvaa 4,112,212\ns_add_u32 s66, s87, 588920\nbuffer_load_dwordx4 v62, s[52:55], s66 offen offset:1032 lds\nQda 180,63,34560\nQzvaa 8,112,216\nQda 184,63,35584\ns_add_u32 s66, s87, 653424\nbuffer_load_dwordx4 v62, s[52:55], s66 offen offset:2064 lds\n"
"Qzvaa 12,112,220\nQda 188,63,36608\nQzvaa 16,112,224\ns_add_u32 s66, s87, 717928\nbuffer_load_dwordx4 v62, s[52:55], s66 offen offset:3096 lds\nQda 192,63,37632\nQzvaa 20,112,228\nQda 196,63,38656\n"
"s_mov_b64 exec, 0\ns_cmp_eq_u32 s70, 0\ns_cbranch_scc1 .Lpg8\ns_cmp_eq_u32 s70, 1\ns_cbranch_scc1 .Lpg9\ns_cmp_eq_u32 s70, 2\ns_cbranch_scc1 .Lpg10\ns_branch .Lpg11\n"
".Ltr10:\ns_mov_b64 exec, -1\nQzvaa 52,112,232\nQda 200,63,39680\nv_max3_f32 v0, |v0|, |v1|, |v2|\nv_max3_f32 v4, |v4|, |v5|, |v6|\nv_max3_f32 v8, |v8|, |v9|, |v10|\nv_max3_f32 v12, |v12|, |v13|, |v14|\n"
"v_max3_f32 v16, |v16|, |v17|, |v18|\nv_max3_f32 v20, |v20|, |v21|, |v22|\ns_nop 0\nv_max3_f32 v52, |v52|, |v53|, |v54|\nv_min3_u32 v56, v0, v4, v8\nv_min3_u32 v56, v56, v12, v16\nv_min3_u32 v56, v56, v20, v52\nv_mov_b32 v57, v56\n"
"s_nop 1\nv_permlane32_swap_b32 v56, v57\nv_max_f32 v56, v56, v57\nv_mov_b32 v57, v56\ns_nop 1\nv_permlane16_swap_b32 v56, v57\nv_max_f32 v56, v56, v57\ns_nop 1\n"
"v_min_f32_dpp v56, v56, v56 row_ror:8 row_mask:0xf bank_mask:0xf\ns_nop 1\nv_min_f32_dpp v56, v56, v56 row_ror:4 row_mask:0xf bank_mask:0xf\ns_nop 1\nv_min_f32_dpp v56, v56, v56 row_ror:2 row_mask:0xf bank_mask:0xf\ns_nop 1\nv_min_f32_dpp v56, v56, v56 row_ror:1 row_mask:0xf bank_mask:0xf\nv_and_b32 v56, 0x7f800000, v56\n"
"v_min_u32 v56, 0x78000000, v56\nv_add_u32 v56, 0x5400000, v56\nv_mov_b32 v57, v56\nv_mov_b64 v[58:59], v[56:57]\nv_mul_f32 v52, -v56, s72\n.Lround2:\ns_waitcnt lgkmcnt(6)\nQcvaa 80,112,208,56\n"
"Qk 96,96,97\nQk 97,98,99\nQmvaa 80,116,176\ns_add_u32 s68, s71, 0\nQk 98,100,101\nQcvaa 84,120,208,56\ns_cbranch_vccz .Lst27\nbuffer_store_dwordx4 a[0:3], v79, s[56:59], s68 offen offset:0 sc1\n"
".Lst27:\nQmvaa 84,124,176\nQk 99,102,103\nv_accvgpr_write_b32 a8, v96\nQcvaa 88,128,208,56\ns_add_u32 s68, s71, 16384\nv_accvgpr_write_b32 a9, v97\nQmvaa 88,132,176\n"
"s_cbranch_vccz .Lst26\nbuffer_store_dwordx4 a[4:7], v79, s[56:59], s68 offen offset:0 sc1\n.Lst26:\nQcvaa 92,136,208,56\nv_accvgpr_write_b32 a10, v98\nv_accvgpr_write_b32 a11, v99\nQmvaa 92,140,176\ns_waitcnt lgkmcnt(0)\n"
"Qk 104,104,105\nQcvaa 124,136,212,56\ns_waitcnt vmcnt(16)\ns_barrier\nQmvaa 124,140,180\nQta 144,61,16512\ns_add_u32 m0, s62, 0\ns_add_u32 s69, s87, 1048576\n"
"Qcvaa 120,128,212,56\nbuffer_load_dwordx4 v62, s[52:55], s69 offen lds\nQmvaa 120,132,180\nQta 146,61,17024\nQk 105,106,107\nQcvaa 116,120,212,56\nQk 106,108,109\nQk 107,110,111\n"
"Qmvaa 116,124,180\nQta 148,61,20640\ns_add_u32 s69, s87, 1113080\nQcvaa 112,112,212,56\nbuffer_load_dwordx4 v62, s[52:55], s69 offen offset:1032 lds\nQmvaa 112,116,180\nQta 150,61,21152\nv_accvgpr_write_b32 a12, v104\n"
"Qcvaa 144,112,216,56\nv_accvgpr_write_b32 a13, v105\nv_accvgpr_write_b32 a14, v106\nQmvaa 144,116,184\nQta 152,61,16520\ns_add_u32 s69, s87, 1177584\nQcvaa 148,120,216,56\nbuffer_load_dwordx4 v62, s[52:55], s69 offen offset:2064 lds\n"
"Qmvaa 148,124,184\nQta 154,61,17032\nv_accvgpr_write_b32 a15, v107\nQcvaa 152,128,216,56\nQk 136,136,137\nQk 137,138,139\nQmvaa 152,132,184\nQta 156,61,20648\n"
"s_add_u32 s69, s87, 1242088\nQcvaa 156,136,216,56\nbuffer_load_dwordx4 v62, s[52:55], s69 offen offset:3096 lds\nQmvaa 156,140,184\nQta 158,61,21160\nQk 138,140,141\nQcvaa 188,136,220,56\nQk 139,142,143\n"
"v_accvgpr_write_b32 a28, v136\nQmvaa 188,140,188\nQta 160,61,16768\ns_add_u32 m0, s63, 33024\ns_add_u32 s88, s86, 256\nQcvaa 184,128,220,56\nbuffer_load_dwordx4 v64, s[48:51], s88 offen lds\nQmvaa 184,132,188\n"
"Qta 162,61,17280\nv_accvgpr_write_b32 a29, v137\nQcvaa 180,120,220,56\nv_accvgpr_write_b32 a30, v138\nv_accvgpr_write_b32 a31, v139\nQmvaa 180,124,188\nQta 164,61,20896\ns_add_u32 s88, s86, 19712\n"
"Qcvaa 176,112,220,56\nbuffer_load_dwordx4 v64, s[48:51], s88 offen offset:1024 lds\nQmvaa 176,116,188\nQta 166,61,21408\nQk 128,128,129\nQcvaa 208,112,224,56\nQk 129,130,131\nQk 130,132,133\n"
"Qmvaa 208,116,192\nQta 168,61,16776\ns_add_u32 s88, s86, 39168\nQcvaa 212,120,224,56\nbuffer_load_dwordx4 v64, s[48:51], s88 offen offset:2048 lds\nQmvaa 212,124,192\nQta 170,61,17288\nQk 131,134,135\n"
"Qcvaa 216,128,224,56\nv_accvgpr_write_b32 a24, v128\nv_accvgpr_write_b32 a25, v129\nQmvaa 216,132,192\nQta 172,61,20904\ns_add_u32 s88, s86, 58624\nQcvaa 220,136,224,56\nbuffer_load_dwordx4 v64, s[48:51], s88 offen offset:3072 lds\n"
"Qmvaa 220,140,192\nQta 174,61,21416\nv_accvgpr_write_b32 a26, v130\nQcvaa 252,136,228,56\nv_accvgpr_write_b32 a27, v131\nQk 160,160,161\nQmvaa 252,140,196\ns_add_u32 m0, s63, 37120\n"
"s_add_u32 s88, s86, 82176\nQk 161,162,163\nQcvaa 248,128,228,56\nbuffer_load_dwordx4 v64, s[48:51], s88 offen lds\nQmvaa 248,132,196\nQk 162,164,165\nQk 163,166,167\nQcvaa 244,120,228,56\n"
"v_accvgpr_write_b32 a40, v160\nv_accvgpr_write_b32 a41, v161\nQmvaa 244,124,196\ns_add_u32 s88, s86, 101632\nv_accvgpr_write_b32 a42, v162\nQcvaa 240,112,228,56\nbuffer_load_dwordx4 v64, s[48:51], s88 offen offset:1024 lds\nQmvaa 240,116,196\n"
"v_accvgpr_write_b32 a43, v163\nQk 168,168,169\nQcvaa 16,112,232,56\nQk 169,170,171\nQk 170,172,173\nQmvaa 16,116,200\ns_add_u32 s88, s86, 121088\nQk 171,174,175\n"
"Qcvaa 20,120,232,56\nbuffer_load_dwordx4 v64, s[48:51], s88 offen offset:2048 lds\nQmvaa 20,124,200\nv_accvgpr_write_b32 a44, v168\nv_accvgpr_write_b32 a45, v169\nQcvaa 24,128,232,56\nv_accvgpr_write_b32 a46, v170\nv_accvgpr_write_b32 a47, v171\n"
"Qmvaa 24,132,200\nQk 200,200,201\nQk 201,202,203\nQcvaa 28,136,232,56\nQk 202,204,205\nQk 203,206,207\nQmvaa 28,140,200\nv_accvgpr_write_b32 a60, v200\n"
"v_accvgpr_write_b32 a61, v201\ns_waitcnt lgkmcnt(0)\nQcvaa 96,144,208,56\nv_accvgpr_write_b32 a62, v202\nv_accvgpr_write_b32 a63, v203\nQmvaa 96,148,176\ns_add_u32 s68, s71, 0\nQk 192,192,193\n"
"Qcvaa 100,152,208,56\ns_cbranch_vccz .Lst25\nbuffer_store_dwordx4 a[16:19], v79, s[56:59], s68 offen offset:1024 sc1\n.Lst25:\nQmvaa 100,156,176\nQk 193,194,195\nQk 194,196,197\nQcvaa 104,160,208,56\n"
"s_add_u32 s68, s71, 16384\nQk 195,198,199\nQmvaa 104,164,176\ns_cbranch_vccz .Lst24\nbuffer_store_dwordx4 a[20:23], v79, s[56:59], s68 offen offset:1024 sc1\n.Lst24:\nQcvaa 108,168,208,56\nv_accvgpr_write_b32 a56, v192\n"
"v_accvgpr_write_b32 a57, v193\nQmvaa 108,172,176\n.Lpg0:\ns_cbranch_execz .Ltr0\ns_waitcnt lgkmcnt(0)\nv_accvgpr_write_b32 a58, v194\nQcvaa 140,168,212,56\ns_waitcnt vmcnt(15)\n"
"s_barrier\nQmvaa 140,172,180\nQda 208,63,0\ns_add_u32 m0, s62, 16512\ns_add_u32 s69, s87, 1048704\nQcvaa 136,160,212,56\nbuffer_load_dwordx4 v62, s[52:55], s69 offen lds\nQmvaa 136,164,180\n"
"Qda 176,63,512\nv_accvgpr_write_b32 a59, v195\nQcvaa 132,152,212,56\nQta 112,61,33024\nQk 224,224,225\nQmvaa 132,156,180\ns_add_u32 s69, s87, 1113208\nQk 225,226,227\n"
"Qcvaa 128,144,212,56\nbuffer_load_dwordx4 v62, s[52:55], s69 offen offset:1032 lds\nQmvaa 128,148,180\nQda 212,63,1024\nQk 226,228,229\nQcvaa 160,144,216,56\nQda 180,63,1536\nQk 227,230,231\n"
"Qmvaa 160,148,184\nQta 114,61,33536\ns_add_u32 s69, s87, 1177712\nQcvaa 164,152,216,56\nbuffer_load_dwordx4 v62, s[52:55], s69 offen offset:2064 lds\nQmvaa 164,156,184\nQta 116,61,37152\nv_accvgpr_write_b32 a72, v224\n"
"Qcvaa 168,160,216,56\nv_accvgpr_write_b32 a73, v225\nv_accvgpr_write_b32 a74, v226\nQmvaa 168,164,184\nQta 118,61,37664\ns_add_u32 s69, s87, 1242216\nQcvaa 172,168,216,56\nbuffer_load_dwordx4 v62, s[52:55], s69 offen offset:3096 lds\n"
"Qmvaa 172,172,184\nQda 216,63,2048\nv_accvgpr_write_b32 a75, v227\nQcvaa 204,168,220,56\nQda 184,63,2560\nQk 232,232,233\nQmvaa 204,172,188\nQta 120,61,33032\n"
"s_add_u32 m0, s61, 0\nQcvaa 200,160,220,56\ns_cbranch_vccnz .Ld0q0\nbuffer_load_dwordx4 v69, s[40:43], s60 offen nt lds\n.Ld0q0:\nQmvaa 200,164,188\nQta 122,61,33544\nQk 233,234,235\n"
"Qcvaa 196,152,220,56\nQk 234,236,237\nQk 235,238,239\nQmvaa 196,156,188\nQta 124,61,37160\nv_accvgpr_write_b32 a76, v232\nQcvaa 192,144,220,56\nQda 220,63,3072\n"
"v_accvgpr_write_b32 a77, v233\nQmvaa 192,148,188\nQda 188,63,3584\nv_accvgpr_write_b32 a78, v234\nQcvaa 224,144,224,56\nQta 126,61,37672\nv_accvgpr_write_b32 a79, v235\nQmvaa 224,148,192\n"
"Qk 32,32,33\nQk 33,34,35\nQcvaa 228,152,224,56\nQta 128,61,33280\nQk 34,36,37\nQmvaa 228,156,192\nQk 35,38,39\nv_accvgpr_write_b32 a104, v32\n"
"Qcvaa 232,160,224,56\nQta 130,61,33792\nv_accvgpr_write_b32 a105, v33\nQmvaa 232,164,192\nv_accvgpr_write_b32 a106, v34\nv_accvgpr_write_b32 a107, v35\nQcvaa 236,168,224,56\nQda 224,63,4096\n"
"Qk 40,40,41\nQmvaa 236,172,192\nQda 192,63,4608\nQk 41,42,43\nQcvaa 12,168,228,56\nQta 132,61,37408\nQk 42,44,45\nQmvaa 12,172,196\n"
"Qk 43,46,47\nv_accvgpr_write_b32 a108, v40\nQcvaa 8,160,228,56\nQta 134,61,37920\nv_accvgpr_write_b32 a109, v41\nQmvaa 8,164,196\nv_accvgpr_write_b32 a110, v42\nv_accvgpr_write_b32 a111, v43\n"
"Qcvaa 4,152,228,56\nQta 136,61,33288\nQmvaa 4,156,196\nQcvaa 0,144,228,56\nQda 228,63,5120\nQmvaa 0,148,196\nQda 196,63,5632\nQcvaa 32,144,232,56\n"
"Qta 138,61,33800\nQmvaa 32,148,200\nQcvaa 36,152,232,56\nQta 140,61,37416\nQmvaa 36,156,200\nQcvaa 40,160,232,56\nQta 142,61,37928\nQmvaa 40,164,200\n"
"Qcvaa 44,168,232,56\nQda 232,63,6144\nQmvaa 44,172,200\nQda 200,63,6656\ns_waitcnt lgkmcnt(2)\nQmvaa 80,112,208\nQmvaa 80,116,176\ns_add_u32 s68, s71, 0\n"
"Qmvaa 84,120,208\ns_cbranch_vccz .Lst23\nbuffer_store_dwordx4 a[32:35], v79, s[56:59], s68 offen offset:2048 sc1\n.Lst23:\nQmvaa 84,124,176\nQmvaa 88,128,208\ns_add_u32 s68, s71, 16384\nQmvaa 88,132,176\n"
"s_cbranch_vccz .Lst22\nbuffer_store_dwordx4 a[36:39], v79, s[56:59], s68 offen offset:2048 sc1\n.Lst22:\nQmvaa 92,136,208\nQmvaa 92,140,176\ns_waitcnt lgkmcnt(0)\nQmvaa 124,136,212\ns_waitcnt vmcnt(16)\n"
"s_barrier\nQmvaa 124,140,180\nQta 144,61,49536\ns_add_u32 m0, s62, 33024\ns_add_u32 s69, s87, 1572864\nQmvaa 120,128,212\nbuffer_load_dwordx4 v62, s[52:55], s69 offen lds\nQmvaa 120,132,180\n"
"Qta 146,61,50048\nQmvaa 116,120,212\ns_add_u32 s69, s87, 1637368\nQmvaa 116,124,180\nbuffer_load_dwordx4 v62, s[52:55], s69 offen offset:1032 lds\nQmvaa 112,112,212\nQta 148,61,53664\nQmvaa 112,116,180\n"
"s_add_u32 s69, s87, 1701872\nQmvaa 144,112,216\nbuffer_load_dwordx4 v62, s[52:55], s69 offen offset:2064 lds\nQmvaa 144,116,184\nQta 150,61,54176\nQmvaa 148,120,216\ns_add_u32 s69, s87, 1766376\nQmvaa 148,124,184\n"
"buffer_load_dwordx4 v62, s[52:55], s69 offen offset:3096 lds\nQmvaa 152,128,216\nQta 152,61,49544\nQmvaa 152,132,184\ns_add_u32 m0, s63, 0\ns_add_u32 s88, s86, 384\nQmvaa 156,136,216\nbuffer_load_dwordx4 v64, s[48:51], s88 offen lds\n"
"Qmvaa 156,140,184\nQta 154,61,50056\nQmvaa 188,136,220\ns_add_u32 s88, s86, 19840\nQmvaa 188,140,188\nbuffer_load_dwordx4 v64, s[48:51], s88 offen offset:1024 lds\nQmvaa 184,128,220\nQta 156,61,53672\n"
"Qmvaa 184,132,188\ns_add_u32 s88, s86, 39296\nQmvaa 180,120,220\nbuffer_load_dwordx4 v64, s[48:51], s88 offen offset:2048 lds\nQmvaa 180,124,188\nQta 158,61,54184\nQmvaa 176,112,220\ns_add_u32 s88, s86, 58752\n"
"Qmvaa 176,116,188\nbuffer_load_dwordx4 v64, s[48:51], s88 offen offset:3072 lds\nQmvaa 208,112,224\nQta 160,61,49792\nQmvaa 208,116,192\ns_add_u32 m0, s63, 4096\ns_add_u32 s88, s86, 82304\nQmvaa 212,120,224\n"
"buffer_load_dwordx4 v64, s[48:51], s88 offen lds\nQmvaa 212,124,192\nQta 162,61,50304\nQmvaa 216,128,224\ns_add_u32 s88, s86, 101760\nQmvaa 216,132,192\nbuffer_load_dwordx4 v64, s[48:51], s88 offen offset:1024 lds\nQmvaa 220,136,224\n"
"Qta 164,61,53920\nQmvaa 220,140,192\ns_add_u32 s88, s86, 121216\nQmvaa 252,136,228\nbuffer_load_dwordx4 v64, s[48:51], s88 offen offset:2048 lds\nQmvaa 252,140,196\nQta 166,61,54432\nQmvaa 248,128,228\n"
"s_add_u32 m0, s61, 0\nQmvaa 248,132,196\ns_cbranch_vccnz .Ld0q1\nbuffer_load_dwordx4 v69, s[40:43], s60 offen offset:1024 nt lds\n.Ld0q1:\nQmvaa 244,120,228\nQta 168,61,49800\nQmvaa 244,124,196\n"
"Qmvaa 240,112,228\nQta 170,61,50312\nQmvaa 240,116,196\nQmvaa 16,112,232\nQta 172,61,53928\nQmvaa 16,116,200\nQmvaa 20,120,232\nQta 174,61,54440\n"
"Qmvaa 20,124,200\nQmvaa 24,128,232\nQmvaa 24,132,200\nQmvaa 28,136,232\nQmvaa 28,140,200\ns_waitcnt lgkmcnt(0)\nQmvaa 96,144,208\nQmvaa 96,148,176\n"
"s_add_u32 s68, s71, 0\nQmvaa 100,152,208\ns_cbranch_vccz .Lst21\nbuffer_store_dwordx4 a[48:51], v79, s[56:59], s68 offen offset:3072 sc1\n.Lst21:\nQmvaa 100,156,176\nQmvaa 104,160,208\ns_add_u32 s68, s71, 16384\n"
"Qmvaa 104,164,176\ns_cbranch_vccz .Lst20\nbuffer_store_dwordx4 a[52:55], v79, s[56:59], s68 offen offset:3072 sc1\n.Lst20:\nQmvaa 108,168,208\nQmvaa 108,172,176\ns_waitcnt lgkmcnt(0)\nQmvaa 140,168,212\n"
"s_waitcnt vmcnt(17)\ns_barrier\nQmvaa 140,172,180\nQda 208,63,33024\ns_add_u32 m0, s62, 49536\ns_add_u32 s69, s87, 1572992\nQmvaa 136,160,212\nbuffer_load_dwordx4 v62, s[52:55], s69 offen lds\n"
"Qmvaa 136,164,180\nQda 176,63,33536\nQmvaa 132,152,212\nQta 112,61,0\nQmvaa 132,156,180\ns_add_u32 s69, s87, 1637496\nQmvaa 128,144,212\nbuffer_load_dwordx4 v62, s[52:55], s69 offen offset:1032 lds\n"
"Qmvaa 128,148,180\nQda 212,63,34048\nQmvaa 160,144,216\nQda 180,63,34560\nQmvaa 160,148,184\nQta 114,61,512\ns_add_u32 s69, s87, 1702000\nQmvaa 164,152,216\n"
"buffer_load_dwordx4 v62, s[52:55], s69 offen offset:2064 lds\nQmvaa 164,156,184\nQta 116,61,4128\nQmvaa 168,160,216\nQmvaa 168,164,184\nQta 118,61,4640\ns_add_u32 s69, s87, 1766504\nQmvaa 172,168,216\n"
"buffer_load_dwordx4 v62, s[52:55], s69 offen offset:3096 lds\nQmvaa 172,172,184\nQda 216,63,35072\nQmvaa 204,168,220\nQda 184,63,35584\nQmvaa 204,172,188\nQta 120,61,8\nQmvaa 200,160,220\n"
"Qmvaa 200,164,188\nQta 122,61,520\nQmvaa 196,152,220\nQmvaa 196,156,188\nQta 124,61,4136\nQmvaa 192,144,220\nQda 220,63,36096\nQmvaa 192,148,188\n"
"Qda 188,63,36608\nQmvaa 224,144,224\nQta 126,61,4648\nQmvaa 224,148,192\nQmvaa 228,152,224\nQta 128,61,256\nQmvaa 228,156,192\nQmvaa 232,160,224\n"
"Qta 130,61,768\nQmvaa 232,164,192\nQmvaa 236,168,224\nQda 224,63,37120\nQmvaa 236,172,192\nQda 192,63,37632\nQmvaa 12,168,228\nQta 132,61,4384\n"
"Qmvaa 12,172,196\nQmvaa 8,160,228\nQta 134,61,4896\nQmvaa 8,164,196\nQmvaa 4,152,228\nQta 136,61,264\nQmvaa 4,156,196\nQmvaa 0,144,228\n"
"Qda 228,63,38144\nQmvaa 0,148,196\nQda 196,63,38656\nQmvaa 32,144,232\nQta 138,61,776\nQmvaa 32,148,200\nQmvaa 36,152,232\nQta 140,61,4392\n"
"Qmvaa 36,156,200\nQmvaa 40,160,232\nQta 142,61,4904\nQmvaa 40,164,200\nQmvaa 44,168,232\nQda 232,63,39168\nQmvaa 44,172,200\nQda 200,63,39680\n"
"s_waitcnt lgkmcnt(2)\nQmvaa 80,112,208\nQmvaa 80,116,176\ns_add_u32 s68, s71, 4096\nQmvaa 84,120,208\ns_cbranch_vccz .Lst19\nbuffer_store_dwordx4 a[64:67], v79, s[56:59], s68 offen offset:0 sc1\n.Lst19:\n"
"Qmvaa 84,124,176\nQmvaa 88,128,208\ns_add_u32 s68, s71, 20480\nQmvaa 88,132,176\ns_cbranch_vccz .Lst18\nbuffer_store_dwordx4 a[68:71], v79, s[56:59], s68 offen offset:0 sc1\n.Lst18:\nQmvaa 92,136,208\n"
"Qmvaa 92,140,176\ns_waitcnt lgkmcnt(0)\nQmvaa 124,136,212\ns_waitcnt vmcnt(17)\ns_barrier\nQmvaa 124,140,180\nQta 144,61,16512\ns_add_u32 m0, s62, 0\n"
"s_add_u32 s69, s87, 2097152\nQmvaa 120,128,212\nbuffer_load_dwordx4 v62, s[52:55], s69 offen lds\nQmvaa 120,132,180\nQta 146,61,17024\nQmvaa 116,120,212\ns_add_u32 s69, s87, 2161656\nQmvaa 116,124,180\n"
"buffer_load_dwordx4 v62, s[52:55], s69 offen offset:1032 lds\nQmvaa 112,112,212\nQta 148,61,20640\nQmvaa 112,116,180\ns_add_u32 s69, s87, 2226160\nQmvaa 144,112,216\nbuffer_load_dwordx4 v62, s[52:55], s69 offen offset:2064 lds\nQmvaa 144,116,184\n"
"Qta 150,61,21152\nQmvaa 148,120,216\ns_add_u32 s69, s87, 2290664\nQmvaa 148,124,184\nbuffer_load_dwordx4 v62, s[52:55], s69 offen offset:3096 lds\nQmvaa 152,128,216\nQta 152,61,16520\nQmvaa 152,132,184\n"
"s_add_u32 m0, s63, 33024\ns_add_u32 s88, s86, 512\nQmvaa 156,136,216\nbuffer_load_dwordx4 v64, s[48:51], s88 offen lds\nQmvaa 156,140,184\nQta 154,61,17032\nQmvaa 188,136,220\ns_add_u32 s88, s86, 19968\n"
"Qmvaa 188,140,188\nbuffer_load_dwordx4 v64, s[48:51], s88 offen offset:1024 lds\nQmvaa 184,128,220\nQta 156,61,20648\nQmvaa 184,132,188\ns_add_u32 s88, s86, 39424\nQmvaa 180,120,220\nbuffer_load_dwordx4 v64, s[48:51], s88 offen offset:2048 lds\n"
"Qmvaa 180,124,188\nQta 158,61,21160\nQmvaa 176,112,220\ns_add_u32 s88, s86, 58880\nQmvaa 176,116,188\nbuffer_load_dwordx4 v64, s[48:51], s88 offen offset:3072 lds\nQmvaa 208,112,224\nQta 160,61,16768\n"
"Qmvaa 208,116,192\ns_add_u32 m0, s63, 37120\ns_add_u32 s88, s86, 82432\nQmvaa 212,120,224\nbuffer_load_dwordx4 v64, s[48:51], s88 offen lds\nQmvaa 212,124,192\nQta 162,61,17280\nQmvaa 216,128,224\n"
"s_add_u32 s88, s86, 101888\nQmvaa 216,132,192\nbuffer_load_dwordx4 v64, s[48:51], s88 offen offset:1024 lds\nQmvaa 220,136,224\nQta 164,61,20896\nQmvaa 220,140,192\ns_add_u32 s88, s86, 121344\nQmvaa 252,136,228\n"
"buffer_load_dwordx4 v64, s[48:51], s88 offen offset:2048 lds\nQmvaa 252,140,196\nQta 166,61,21408\nQmvaa 248,128,228\ns_add_u32 m0, s61, 0\nQmvaa 248,132,196\ns_cbranch_vccnz .Ld0q2\nbuffer_load_dwordx4 v69, s[40:43], s90 offen offset:2048 nt lds\n"
".Ld0q2:\nQmvaa 244,120,228\nQta 168,61,16776\nQmvaa 244,124,196\nQmvaa 240,112,228\nQta 170,61,17288\nQmvaa 240,116,196\nQmvaa 16,112,232\n"
"Qta 172,61,20904\nQmvaa 16,116,200\nQmvaa 20,120,232\nQta 174,61,21416\nQmvaa 20,124,200\nQmvaa 24,128,232\nQmvaa 24,132,200\nQmvaa 28,136,232\n"
"Qmvaa 28,140,200\ns_waitcnt lgkmcnt(0)\nQmvaa 96,144,208\nQmvaa 96,148,176\ns_add_u32 s68, s71, 4096\nQmvaa 100,152,208\ns_cbranch_vccz .Lst17\nbuffer_store_dwordx4 a[80:83], v79, s[56:59], s68 offen offset:1024 sc1\n"
".Lst17:\nQmvaa 100,156,176\nQmvaa 104,160,208\ns_add_u32 s68, s71, 20480\nQmvaa 104,164,176\ns_cbranch_vccz .Lst16\nbuffer_store_dwordx4 a[84:87], v79, s[56:59], s68 offen offset:1024 sc1\n.Lst16:\n"
"Qmvaa 108,168,208\nQmvaa 108,172,176\ns_waitcnt lgkmcnt(0)\nQmvaa 140,168,212\ns_waitcnt vmcnt(17)\ns_barrier\nQmvaa 140,172,180\nQda 208,63,0\n"
"s_add_u32 m0, s62, 16512\ns_add_u32 s69, s87, 2097280\nQmvaa 136,160,212\nbuffer_load_dwordx4 v62, s[52:55], s69 offen lds\nQmvaa 136,164,180\nQda 176,63,512\nQmvaa 132,152,212\nQta 112,61,33024\n"
"Qmvaa 132,156,180\ns_add_u32 s69, s87, 2161784\nQmvaa 128,144,212\nbuffer_load_dwordx4 v62, s[52:55], s69 offen offset:1032 lds\nQmvaa 128,148,180\nQda 212,63,1024\nQmvaa 160,144,216\nQda 180,63,1536\n"
"Qmvaa 160,148,184\nQta 114,61,33536\ns_add_u32 s69, s87, 2226288\nQmvaa 164,152,216\nbuffer_load_dwordx4 v62, s[52:55], s69 offen offset:2064 lds\nQmvaa 164,156,184\nQta 116,61,37152\nQmvaa 168,160,216\n"
"Qmvaa 168,164,184\nQta 118,61,37664\ns_add_u32 s69, s87, 2290792\nQmvaa 172,168,216\nbuffer_load_dwordx4 v62, s[52:55], s69 offen offset:3096 lds\nQmvaa 172,172,184\nQda 216,63,2048\nQmvaa 204,168,220\n"
"Qda 184,63,2560\nQmvaa 204,172,188\nQta 120,61,33032\ns_add_u32 m0, s61, 0\nQmvaa 200,160,220\ns_cbranch_vccnz .Ld0q3\nbuffer_load_dwordx4 v69, s[40:43], s90 offen offset:3072 nt lds\n.Ld0q3:\n"
"Qmvaa 200,164,188\nQta 122,61,33544\nQmvaa 196,152,220\nQmvaa 196,156,188\nQta 124,61,37160\nQmvaa 192,144,220\nQda 220,63,3072\nQmvaa 192,148,188\n"
"Qda 188,63,3584\nQmvaa 224,144,224\nQta 126,61,37672\nQmvaa 224,148,192\nQmvaa 228,152,224\nQta 128,61,33280\nQmvaa 228,156,192\nQmvaa 232,160,224\n"
"Qta 130,61,33792\nQmvaa 232,164,192\nQmvaa 236,168,224\n.Lpg1:\ns_cbranch_execz .Ltr0\nQda 224,63,4096\nQmvaa 236,172,192\nQda 192,63,4608\n"
"Qmvaa 12,168,228\nQta 132,61,37408\nQmvaa 12,172,196\nQmvaa 8,160,228\nQta 134,61,37920\nQmvaa 8,164,196\nQmvaa 4,152,228\nQta 136,61,33288\n"
"Qmvaa 4,156,196\nQmvaa 0,144,228\nQda 228,63,5120\nQmvaa 0,148,196\nQda 196,63,5632\nQmvaa 32,144,232\nQta 138,61,33800\nQmvaa 32,148,200\n"
"Qmvaa 36,152,232\nQta 140,61,37416\nQmvaa 36,156,200\nQmvaa 40,160,232\nQta 142,61,37928\nQmvaa 40,164,200\nQmvaa 44,168,232\nQda 232,63,6144\n"
"Qmvaa 44,172,200\nQda 200,63,6656\ns_waitcnt lgkmcnt(2)\nQmvaa 80,112,208\nQmvaa 80,116,176\ns_add_u32 s68, s71, 135168\nQmvaa 84,120,208\ns_cbranch_vccz .Lst15\n"
"buffer_store_dwordx4 a[88:91], v79, s[56:59], s68 offen offset:1024 sc1\n.Lst15:\nQmvaa 84,124,176\nQmvaa 88,128,208\ns_add_u32 s68, s71, 151552\nQmvaa 88,132,176\ns_cbranch_vccz .Lst14\nbuffer_store_dwordx4 a[92:95], v79, s[56:59], s68 offen offset:1024 sc1\n"
".Lst14:\nQmvaa 92,136,208\nQmvaa 92,140,176\ns_waitcnt lgkmcnt(0)\nQmvaa 124,136,212\ns_waitcnt vmcnt(17)\ns_barrier\nQmvaa 124,140,180\n"
"Qta 144,61,49536\ns_add_u32 m0, s62, 33024\ns_add_u32 s69, s87, 2621440\nQmvaa 120,128,212\nbuffer_load_dwordx4 v62, s[52:55], s69 offen lds\nQmvaa 120,132,180\nQta 146,61,50048\ns_mov_b32 s89, 0\n"
"Qmvaa 116,120,212\ns_cbranch_vccnz .Lpb0\ns_load_dword s89, s[30:31], 0x0\n.Lpb0:\nQmvaa 116,124,180\nQta 148,61,53664\ns_add_u32 s69, s87, 2685944\nQmvaa 112,112,212\n"
"buffer_load_dwordx4 v62, s[52:55], s69 offen offset:1032 lds\nQmvaa 112,116,180\nQta 150,61,54176\nQmvaa 144,112,216\ns_add_u32 s77, s77, s89\nQmvaa 144,116,184\nQta 152,61,49544\ns_add_u32 s69, s87, 2750448\n"
"Qmvaa 148,120,216\nbuffer_load_dwordx4 v62, s[52:55], s69 offen offset:2064 lds\nQmvaa 148,124,184\nQta 154,61,50056\nQmvaa 152,128,216\ns_add_u32 s77, s77, s89\nQmvaa 152,132,184\nQta 156,61,53672\n"
"s_add_u32 s69, s87, 2814952\nQmvaa 156,136,216\nbuffer_load_dwordx4 v62, s[52:55], s69 offen offset:3096 lds\nQmvaa 156,140,184\nQta 158,61,54184\nQmvaa 188,136,220\ns_add_u32 s77, s77, s89\nQmvaa 188,140,188\n"
"Qta 160,61,49792\ns_add_u32 m0, s63, 0\ns_add_u32 s88, s86, 640\nQmvaa 184,128,220\nbuffer_load_dwordx4 v64, s[48:51], s88 offen lds\nQmvaa 184,132,188\nQta 162,61,50304\nQmvaa 180,120,220\n"
"s_add_u32 s77, s77, s89\nQmvaa 180,124,188\nQta 164,61,53920\ns_add_u32 s88, s86, 20096\nQmvaa 176,112,220\nbuffer_load_dwordx4 v64, s[48:51], s88 offen offset:1024 lds\nQmvaa 176,116,188\nQta 166,61,54432\n"
"Qmvaa 208,112,224\ns_add_u32 s77, s77, s89\nQmvaa 208,116,192\nQta 168,61,49800\ns_add_u32 s88, s86, 39552\nQmvaa 212,120,224\nbuffer_load_dwordx4 v64, s[48:51], s88 offen offset:2048 lds\nQmvaa 212,124,192\n"
"Qta 170,61,50312\nQmvaa 216,128,224\ns_add_u32 s77, s77, s89\nQmvaa 216,132,192\nQta 172,61,53928\ns_add_u32 s88, s86, 59008\nQmvaa 220,136,224\nbuffer_load_dwordx4 v64, s[48:51], s88 offen offset:3072 lds\n"
"Qmvaa 220,140,192\nQta 174,61,54440\nQmvaa 252,136,228\ns_add_u32 s77, s77, s89\nQmvaa 252,140,196\ns_add_u32 m0, s63, 4096\ns_add_u32 s88, s86, 82560\nQmvaa 248,128,228\n"
"buffer_load_dwordx4 v64, s[48:51], s88 offen lds\nQmvaa 248,132,196\nQmvaa 244,120,228\ns_add_u32 s77, s77, s89\nQmvaa 244,124,196\ns_add_u32 s88, s86, 102016\nQmvaa 240,112,228\nbuffer_load_dwordx4 v64, s[48:51], s88 offen offset:1024 lds\n"
"Qmvaa 240,116,196\nQmvaa 16,112,232\ns_add_u32 s77, s77, s89\nQmvaa 16,116,200\ns_add_u32 s88, s86, 121472\nQmvaa 20,120,232\nbuffer_load_dwordx4 v64, s[48:51], s88 offen offset:2048 lds\nQmvaa 20,124,200\n"
"Qmvaa 24,128,232\ns_add_u32 s77, s77, s89\nQmvaa 24,132,200\nQmvaa 28,136,232\nQmvaa 28,140,200\ns_waitcnt lgkmcnt(0)\nQmvaa 96,144,208\nQmvaa 96,148,176\n"
"s_add_u32 s68, s71, 4096\nQmvaa 100,152,208\ns_cbranch_vccz .Lst13\nbuffer_store_dwordx4 a[96:99], v79, s[56:59], s68 offen offset:2048 sc1\n.Lst13:\nQmvaa 100,156,176\nQmvaa 104,160,208\ns_add_u32 s68, s71, 20480\n"
"Qmvaa 104,164,176\ns_cbranch_vccz .Lst12\nbuffer_store_dwordx4 a[100:103], v79, s[56:59], s68 offen offset:2048 sc1\n.Lst12:\nQmvaa 108,168,208\nQmvaa 108,172,176\ns_waitcnt lgkmcnt(0)\nQmvaa 140,168,212\n"
"s_waitcnt vmcnt(17)\ns_barrier\nQmvaa 140,172,180\nQda 208,63,33024\ns_add_u32 m0, s62, 49536\ns_add_u32 s69, s87, 2621568\nQmvaa 136,160,212\nbuffer_load_dwordx4 v62, s[52:55], s69 offen lds\n"
"Qmvaa 136,164,180\nQda 176,63,33536\nQmvaa 132,152,212\nQta 112,61,0\nQmvaa 132,156,180\ns_add_u32 s69, s87, 2686072\nQmvaa 128,144,212\nbuffer_load_dwordx4 v62, s[52:55], s69 offen offset:1032 lds\n"
"Qmvaa 128,148,180\nQda 212,63,34048\nQmvaa 160,144,216\nQda 180,63,34560\nQmvaa 160,148,184\nQta 114,61,512\ns_add_u32 s69, s87, 2750576\nQmvaa 164,152,216\n"
"buffer_load_dwordx4 v62, s[52:55], s69 offen offset:2064 lds\nQmvaa 164,156,184\nQta 116,61,4128\nQmvaa 168,160,216\nQmvaa 168,164,184\nQta 118,61,4640\ns_add_u32 s69, s87, 2815080\nQmvaa 172,168,216\n"
"buffer_load_dwordx4 v62, s[52:55], s69 offen offset:3096 lds\nQmvaa 172,172,184\nQda 216,63,35072\nQmvaa 204,168,220\nQda 184,63,35584\nQmvaa 204,172,188\nQta 120,61,8\ns_add_u32 m0, s64, 0\n"
"Qmvaa 200,160,220\ns_cbranch_vccnz .Ld0q4\nbuffer_load_dwordx4 v72, s[44:47], s60 offen nt lds\n.Ld0q4:\nQmvaa 200,164,188\nQta 122,61,520\nQmvaa 196,152,220\nQmvaa 196,156,188\n"
"Qta 124,61,4136\nQmvaa 192,144,220\nQda 220,63,36096\nQmvaa 192,148,188\nQda 188,63,36608\nQmvaa 224,144,224\nQta 126,61,4648\nQmvaa 224,148,192\n"
"Qmvaa 228,152,224\nQta 128,61,256\nQmvaa 228,156,192\nQmvaa 232,160,224\nQta 130,61,768\nQmvaa 232,164,192\nQmvaa 236,168,224\nQda 224,63,37120\n"
"Qmvaa 236,172,192\nQda 192,63,37632\nQmvaa 12,168,228\nQta 132,61,4384\nQmvaa 12,172,196\nQmvaa 8,160,228\nQta 134,61,4896\nQmvaa 8,164,196\n"
"Qmvaa 4,152,228\nQta 136,61,264\nQmvaa 4,156,196\nQmvaa 0,144,228\nQda 228,63,38144\nQmvaa 0,148,196\nQda 196,63,38656\nQmvaa 32,144,232\n"
"Qta 138,61,776\nQmvaa 32,148,200\nQmvaa 36,152,232\nQta 140,61,4392\nQmvaa 36,156,200\nQmvaa 40,160,232\nQta 142,61,4904\nQmvaa 40,164,200\n"
"Qmvaa 44,168,232\nQda 232,63,39168\nQmvaa 44,172,200\nQda 200,63,39680\ns_waitcnt lgkmcnt(2)\nQmvaa 80,112,208\nQmvaa 80,116,176\ns_add_u32 s68, s71, 131072\n"
"Qmvaa 84,120,208\ns_cbranch_vccz .Lst11\nbuffer_store_dwordx4 a[8:11], v79, s[56:59], s68 offen offset:0 sc1\n.Lst11:\nQmvaa 84,124,176\nQmvaa 88,128,208\ns_add_u32 s68, s71, 147456\nQmvaa 88,132,176\n"
"s_cbranch_vccz .Lst10\nbuffer_store_dwordx4 a[12:15], v79, s[56:59], s68 offen offset:0 sc1\n.Lst10:\nQmvaa 92,136,208\nQmvaa 92,140,176\ns_waitcnt lgkmcnt(0)\nQmvaa 124,136,212\ns_waitcnt vmcnt(17)\n"
"s_barrier\nQmvaa 124,140,180\nQta 144,61,16512\ns_add_u32 m0, s62, 0\ns_add_u32 s69, s87, 3145728\nQmvaa 120,128,212\nbuffer_load_dwordx4 v62, s[52:55], s69 offen lds\nQmvaa 120,132,180\n"
"Qta 146,61,17024\ns_mov_b32 s89, 0\nQmvaa 116,120,212\ns_cbranch_vccnz .Lpb1\ns_load_dword s89, s[30:31], 0x2000\n.Lpb1:\nQmvaa 116,124,180\nQta 148,61,20640\n"
"s_add_u32 s69, s87, 3210232\nQmvaa 112,112,212\nbuffer_load_dwordx4 v62, s[52:55], s69 offen offset:1032 lds\nQmvaa 112,116,180\nQta 150,61,21152\nQmvaa 144,112,216\ns_add_u32 s77, s77, s89\nQmvaa 144,116,184\n"
"Qta 152,61,16520\ns_add_u32 s69, s87, 3274736\nQmvaa 148,120,216\nbuffer_load_dwordx4 v62, s[52:55], s69 offen offset:2064 lds\nQmvaa 148,124,184\nQta 154,61,17032\nQmvaa 152,128,216\ns_add_u32 s77, s77, s89\n"
"Qmvaa 152,132,184\nQta 156,61,20648\ns_add_u32 s69, s87, 3339240\nQmvaa 156,136,216\nbuffer_load_dwordx4 v62, s[52:55], s69 offen offset:3096 lds\nQmvaa 156,140,184\nQta 158,61,21160\nQmvaa 188,136,220\n"
"s_add_u32 s77, s77, s89\nQmvaa 188,140,188\nQta 160,61,16768\ns_add_u32 m0, s63, 33024\ns_add_u32 s88, s86, 768\nQmvaa 184,128,220\nbuffer_load_dwordx4 v64, s[48:51], s88 offen lds\nQmvaa 184,132,188\n"
"Qta 162,61,17280\nQmvaa 180,120,220\ns_add_u32 s77, s77, s89\nQmvaa 180,124,188\nQta 164,61,20896\ns_add_u32 s88, s86, 20224\nQmvaa 176,112,220\nbuffer_load_dwordx4 v64, s[48:51], s88 offen offset:1024 lds\n"
"Qmvaa 176,116,188\nQta 166,61,21408\nQmvaa 208,112,224\ns_add_u32 s77, s77, s89\nQmvaa 208,116,192\nQta 168,61,16776\ns_add_u32 s88, s86, 39680\nQmvaa 212,120,224\n"
"buffer_load_dwordx4 v64, s[48:51], s88 offen offset:2048 lds\nQmvaa 212,124,192\nQta 170,61,17288\nQmvaa 216,128,224\ns_add_u32 s77, s77, s89\nQmvaa 216,132,192\nQta 172,61,20904\ns_add_u32 s88, s86, 59136\n"
"Qmvaa 220,136,224\nbuffer_load_dwordx4 v64, s[48:51], s88 offen offset:3072 lds\nQmvaa 220,140,192\nQta 174,61,21416\nQmvaa 252,136,228\ns_add_u32 s77, s77, s89\nQmvaa 252,140,196\ns_add_u32 m0, s63, 37120\n"
"s_add_u32 s88, s86, 82688\nQmvaa 248,128,228\nbuffer_load_dwordx4 v64, s[48:51], s88 offen lds\nQmvaa 248,132,196\nQmvaa 244,120,228\ns_add_u32 s77, s77, s89\nQmvaa 244,124,196\ns_add_u32 s88, s86, 102144\n"
"Qmvaa 240,112,228\nbuffer_load_dwordx4 v64, s[48:51], s88 offen offset:1024 lds\nQmvaa 240,116,196\nQmvaa 16,112,232\ns_add_u32 s77, s77, s89\nQmvaa 16,116,200\ns_add_u32 s88, s86, 121600\nQmvaa 20,120,232\n"
"buffer_load_dwordx4 v64, s[48:51], s88 offen offset:2048 lds\nQmvaa 20,124,200\nQmvaa 24,128,232\ns_add_u32 s77, s77, s89\nQmvaa 24,132,200\nQmvaa 28,136,232\nQmvaa 28,140,200\ns_waitcnt lgkmcnt(0)\n"
"Qmvaa 96,144,208\nQmvaa 96,148,176\ns_add_u32 s68, s71, 131072\nQmvaa 100,152,208\ns_cbranch_vccz .Lst9\nbuffer_store_dwordx4 a[24:27], v79, s[56:59], s68 offen offset:1024 sc1\n.Lst9:\nQmvaa 100,156,176\n"
"Qmvaa 104,160,208\ns_add_u32 s68, s71, 147456\nQmvaa 104,164,176\ns_cbranch_vccz .Lst8\nbuffer_store_dwordx4 a[28:31], v79, s[56:59], s68 offen offset:1024 sc1\n.Lst8:\nQmvaa 108,168,208\nQmvaa 108,172,176\n"
"s_waitcnt lgkmcnt(0)\nQmvaa 140,168,212\ns_waitcnt vmcnt(16)\ns_barrier\nQmvaa 140,172,180\nQda 208,63,0\ns_add_u32 m0, s62, 16512\ns_add_u32 s69, s87, 3145856\n"
"Qmvaa 136,160,212\nbuffer_load_dwordx4 v62, s[52:55], s69 offen lds\nQmvaa 136,164,180\nQda 176,63,512\nQmvaa 132,152,212\nQta 112,61,33024\nQmvaa 132,156,180\ns_add_u32 s69, s87, 3210360\n"
"Qmvaa 128,144,212\nbuffer_load_dwordx4 v62, s[52:55], s69 offen offset:1032 lds\nQmvaa 128,148,180\nQda 212,63,1024\nQmvaa 160,144,216\nQda 180,63,1536\nQmvaa 160,148,184\nQta 114,61,33536\n"
"s_add_u32 s69, s87, 3274864\nQmvaa 164,152,216\nbuffer_load_dwordx4 v62, s[52:55], s69 offen offset:2064 lds\nQmvaa 164,156,184\nQta 116,61,37152\nQmvaa 168,160,216\nQmvaa 168,164,184\nQta 118,61,37664\n"
"s_add_u32 s69, s87, 3339368\nQmvaa 172,168,216\nbuffer_load_dwordx4 v62, s[52:55], s69 offen offset:3096 lds\nQmvaa 172,172,184\nQda 216,63,2048\nQmvaa 204,168,220\nQda 184,63,2560\nQmvaa 204,172,188\n"
"Qta 120,61,33032\ns_add_u32 m0, s64, 0\nQmvaa 200,160,220\ns_cbranch_vccnz .Ld0q5\nbuffer_load_dwordx4 v72, s[44:47], s60 offen offset:1024 nt lds\n.Ld0q5:\nQmvaa 200,164,188\nQta 122,61,33544\n"
"Qmvaa 196,152,220\nQmvaa 196,156,188\nQta 124,61,37160\nQmvaa 192,144,220\nQda 220,63,3072\nQmvaa 192,148,188\nQda 188,63,3584\nQmvaa 224,144,224\n"
"Qta 126,61,37672\nQmvaa 224,148,192\nQmvaa 228,152,224\nQta 128,61,33280\nQmvaa 228,156,192\nQmvaa 232,160,224\nQta 130,61,33792\nQmvaa 232,164,192\n"
"Qmvaa 236,168,224\nQda 224,63,4096\nQmvaa 236,172,192\nQda 192,63,4608\nQmvaa 12,168,228\nQta 132,61,37408\nQmvaa 12,172,196\nQmvaa 8,160,228\n"
"Qta 134,61,37920\nQmvaa 8,164,196\nQmvaa 4,152,228\nQta 136,61,33288\nQmvaa 4,156,196\nQmvaa 0,144,228\nQda 228,63,5120\nQmvaa 0,148,196\n"
"Qda 196,63,5632\nQmvaa 32,144,232\nQta 138,61,33800\nQmvaa 32,148,200\nQmvaa 36,152,232\nQta 140,61,37416\nQmvaa 36,156,200\nQmvaa 40,160,232\n"
"Qta 142,61,37928\nQmvaa 40,164,200\nQmvaa 44,168,232\nQda 232,63,6144\nQmvaa 44,172,200\nQda 200,63,6656\ns_waitcnt lgkmcnt(2)\nQmvaa 80,112,208\n"
"Qmvaa 80,116,176\ns_add_u32 s68, s71, 131072\nQmvaa 84,120,208\ns_cbranch_vccz .Lst7\nbuffer_store_dwordx4 a[40:43], v79, s[56:59], s68 offen offset:2048 sc1\n.Lst7:\nQmvaa 84,124,176\nQmvaa 88,128,208\n"
"s_add_u32 s68, s71, 147456\nQmvaa 88,132,176\ns_cbranch_vccz .Lst6\nbuffer_store_dwordx4 a[44:47], v79, s[56:59], s68 offen offset:2048 sc1\n.Lst6:\nQmvaa 92,136,208\nQmvaa 92,140,176\ns_waitcnt lgkmcnt(0)\n"
"Qmvaa 124,136,212\ns_waitcnt vmcnt(17)\ns_barrier\nQmvaa 124,140,180\nQta 144,61,49536\ns_add_u32 m0, s62, 33024\ns_add_u32 s69, s87, 3670016\nQmvaa 120,128,212\n"
"buffer_load_dwordx4 v62, s[52:55], s69 offen lds\nQmvaa 120,132,180\nQta 146,61,50048\ns_mov_b32 s89, 0\nQmvaa 116,120,212\ns_cbranch_vccnz .Lpb2\ns_load_dword s89, s[30:31], 0x6000\n.Lpb2:\n"
"Qmvaa 116,124,180\nQta 148,61,53664\ns_add_u32 s69, s87, 3734520\nQmvaa 112,112,212\nbuffer_load_dwordx4 v62, s[52:55], s69 offen offset:1032 lds\nQmvaa 112,116,180\nQta 150,61,54176\nQmvaa 144,112,216\n"
"s_add_u32 s77, s77, s89\nQmvaa 144,116,184\nQta 152,61,49544\ns_add_u32 s69, s87, 3799024\nQmvaa 148,120,216\nbuffer_load_dwordx4 v62, s[52:55], s69 offen offset:2064 lds\nQmvaa 148,124,184\nQta 154,61,50056\n"
"Qmvaa 152,128,216\ns_add_u32 s77, s77, s89\nQmvaa 152,132,184\nQta 156,61,53672\ns_add_u32 s69, s87, 3863528\nQmvaa 156,136,216\nbuffer_load_dwordx4 v62, s[52:55], s69 offen offset:3096 lds\nQmvaa 156,140,184\n"
"Qta 158,61,54184\nQmvaa 188,136,220\ns_add_u32 s77, s77, s89\nQmvaa 188,140,188\n.Lpg2:\ns_cbranch_execz .Ltr0\nQta 160,61,49792\ns_add_u32 m0, s63, 0\n"
"s_add_u32 s88, s86, 896\nQmvaa 184,128,220\nbuffer_load_dwordx4 v64, s[48:51], s88 offen lds\nQmvaa 184,132,188\nQta 162,61,50304\nQmvaa 180,120,220\ns_add_u32 s77, s77, s89\nQmvaa 180,124,188\n"
"Qta 164,61,53920\ns_add_u32 s88, s86, 20352\nQmvaa 176,112,220\nbuffer_load_dwordx4 v64, s[48:51], s88 offen offset:1024 lds\nQmvaa 176,116,188\nQta 166,61,54432\nQmvaa 208,112,224\ns_add_u32 s77, s77, s89\n"
"Qmvaa 208,116,192\nQta 168,61,49800\ns_add_u32 s88, s86, 39808\nQmvaa 212,120,224\nbuffer_load_dwordx4 v64, s[48:51], s88 offen offset:2048 lds\nQmvaa 212,124,192\nQta 170,61,50312\nQmvaa 216,128,224\n"
"s_add_u32 s77, s77, s89\nQmvaa 216,132,192\nQta 172,61,53928\ns_add_u32 s88, s86, 59264\nQmvaa 220,136,224\nbuffer_load_dwordx4 v64, s[48:51], s88 offen offset:3072 lds\nQmvaa 220,140,192\nQta 174,61,54440\n"
"Qmvaa 252,136,228\ns_add_u32 s77, s77, s89\nQmvaa 252,140,196\ns_add_u32 m0, s63, 4096\ns_add_u32 s88, s86, 82816\nQmvaa 248,128,228\nbuffer_load_dwordx4 v64, s[48:51], s88 offen lds\nQmvaa 248,132,196\n"
"Qmvaa 244,120,228\ns_add_u32 s77, s77, s89\nQmvaa 244,124,196\ns_add_u32 s88, s86, 102272\nQmvaa 240,112,228\nbuffer_load_dwordx4 v64, s[48:51], s88 offen offset:1024 lds\nQmvaa 240,116,196\nQmvaa 16,112,232\n"
"s_add_u32 s77, s77, s89\nQmvaa 16,116,200\ns_add_u32 s88, s86, 121728\nQmvaa 20,120,232\nbuffer_load_dwordx4 v64, s[48:51], s88 offen offset:2048 lds\nQmvaa 20,124,200\nQmvaa 24,128,232\ns_add_u32 s77, s77, s89\n"
"Qmvaa 24,132,200\nQmvaa 28,136,232\nQmvaa 28,140,200\ns_waitcnt lgkmcnt(0)\nQmvaa 96,144,208\nQmvaa 96,148,176\ns_add_u32 s68, s71, 131072\nQmvaa 100,152,208\n"
"s_cbranch_vccz .Lst5\nbuffer_store_dwordx4 a[56:59], v79, s[56:59], s68 offen offset:3072 sc1\n.Lst5:\nQmvaa 100,156,176\nQmvaa 104,160,208\ns_add_u32 s68, s71, 147456\nQmvaa 104,164,176\ns_cbranch_vccz .Lst4\n"
"buffer_store_dwordx4 a[60:63], v79, s[56:59], s68 offen offset:3072 sc1\n.Lst4:\nQmvaa 108,168,208\nQmvaa 108,172,176\ns_waitcnt lgkmcnt(0)\nQmvaa 140,168,212\ns_waitcnt vmcnt(16)\ns_barrier\n"
"Qmvaa 140,172,180\nQda 208,63,33024\ns_add_u32 m0, s62, 49536\ns_add_u32 s69, s87, 3670144\nQmvaa 136,160,212\nbuffer_load_dwordx4 v62, s[52:55], s69 offen lds\nQmvaa 136,164,180\nQda 176,63,33536\n"
"Qmvaa 132,152,212\nQta 112,61,0\nQmvaa 132,156,180\ns_add_u32 s69, s87, 3734648\nQmvaa 128,144,212\nbuffer_load_dwordx4 v62, s[52:55], s69 offen offset:1032 lds\nQmvaa 128,148,180\nQda 212,63,34048\n"
"Qmvaa 160,144,216\nQda 180,63,34560\nQmvaa 160,148,184\nQta 114,61,512\ns_add_u32 s69, s87, 3799152\nQmvaa 164,152,216\nbuffer_load_dwordx4 v62, s[52:55], s69 offen offset:2064 lds\nQmvaa 164,156,184\n"
"Qta 116,61,4128\nQmvaa 168,160,216\nQmvaa 168,164,184\nQta 118,61,4640\ns_add_u32 s69, s87, 3863656\nQmvaa 172,168,216\nbuffer_load_dwordx4 v62, s[52:55], s69 offen offset:3096 lds\nQmvaa 172,172,184\n"
"Qda 216,63,35072\nQmvaa 204,168,220\nQda 184,63,35584\nQmvaa 204,172,188\nQta 120,61,8\ns_add_u32 m0, s64, 0\nQmvaa 200,160,220\ns_cbranch_vccnz .Ld0q6\n"
"buffer_load_dwordx4 v72, s[44:47], s90 offen offset:2048 nt lds\n.Ld0q6:\nQmvaa 200,164,188\nQta 122,61,520\nQmvaa 196,152,220\nQmvaa 196,156,188\nQta 124,61,4136\nQmvaa 192,144,220\n"
"Qda 220,63,36096\nQmvaa 192,148,188\nQda 188,63,36608\nQmvaa 224,144,224\nQta 126,61,4648\nQmvaa 224,148,192\nQmvaa 228,152,224\nQta 128,61,256\n"
"Qmvaa 228,156,192\nQmvaa 232,160,224\nQta 130,61,768\nQmvaa 232,164,192\nQmvaa 236,168,224\nQda 224,63,37120\nQmvaa 236,172,192\nQda 192,63,37632\n"
"Qmvaa 12,168,228\nQta 132,61,4384\nQmvaa 12,172,196\nQmvaa 8,160,228\nQta 134,61,4896\nQmvaa 8,164,196\nQmvaa 4,152,228\nQta 136,61,264\n"
"Qmvaa 4,156,196\nQmvaa 0,144,228\nQda 228,63,38144\nQmvaa 0,148,196\nQda 196,63,38656\nQmvaa 32,144,232\nQta 138,61,776\nQmvaa 32,148,200\n"
"Qmvaa 36,152,232\nQta 140,61,4392\nQmvaa 36,156,200\nQmvaa 40,160,232\nQta 142,61,4904\nQmvaa 40,164,200\nQmvaa 44,168,232\nQda 232,63,39168\n"
"Qmvaa 44,172,200\nQda 200,63,39680\ns_waitcnt lgkmcnt(2)\nQmvaa 80,112,208\nQmvaa 80,116,176\ns_add_u32 s68, s71, 135168\nQmvaa 84,120,208\ns_cbranch_vccz .Lst3\n"
"buffer_store_dwordx4 a[72:75], v79, s[56:59], s68 offen offset:0 sc1\n.Lst3:\nQmvaa 84,124,176\nQmvaa 88,128,208\ns_add_u32 s68, s71, 151552\nQmvaa 88,132,176\ns_cbranch_vccz .Lst2\nbuffer_store_dwordx4 a[76:79], v79, s[56:59], s68 offen offset:0 sc1\n"
".Lst2:\nQmvaa 92,136,208\nQmvaa 92,140,176\ns_waitcnt lgkmcnt(0)\nQmvaa 124,136,212\ns_waitcnt vmcnt(17)\ns_barrier\nQmvaa 124,140,180\n"
"Qta 144,61,16512\ns_add_u32 m0, s62, 0\ns_add_u32 s69, s87, 4194304\nQmvaa 120,128,212\nbuffer_load_dwordx4 v62, s[52:55], s69 offen lds\nQmvaa 120,132,180\nQta 146,61,17024\ns_mov_b32 s89, 0\n"
"Qmvaa 116,120,212\ns_cbranch_vccnz .Lpb3\ns_load_dword s89, s[30:31], 0x4000\n.Lpb3:\nQmvaa 116,124,180\nQta 148,61,20640\ns_add_u32 s69, s87, 4258808\nQmvaa 112,112,212\n"
"buffer_load_dwordx4 v62, s[52:55], s69 offen offset:1032 lds\nQmvaa 112,116,180\nQta 150,61,21152\nQmvaa 144,112,216\ns_add_u32 s77, s77, s89\nQmvaa 144,116,184\nQta 152,61,16520\ns_add_u32 s69, s87, 4323312\n"
"Qmvaa 148,120,216\nbuffer_load_dwordx4 v62, s[52:55], s69 offen offset:2064 lds\nQmvaa 148,124,184\nQta 154,61,17032\nQmvaa 152,128,216\ns_add_u32 s77, s77, s89\nQmvaa 152,132,184\nQta 156,61,20648\n"
"s_add_u32 s69, s87, 4387816\nQmvaa 156,136,216\nbuffer_load_dwordx4 v62, s[52:55], s69 offen offset:3096 lds\nQmvaa 156,140,184\nQta 158,61,21160\nQmvaa 188,136,220\ns_add_u32 s77, s77, s89\nQmvaa 188,140,188\n"
"Qta 160,61,16768\ns_add_u32 m0, s63, 33024\ns_add_u32 s88, s86, 1024\nQmvaa 184,128,220\nbuffer_load_dwordx4 v64, s[48:51], s88 offen lds\nQmvaa 184,132,188\nQta 162,61,17280\nQmvaa 180,120,220\n"
"s_add_u32 s77, s77, s89\nQmvaa 180,124,188\nQta 164,61,20896\ns_add_u32 s88, s86, 20480\nQmvaa 176,112,220\nbuffer_load_dwordx4 v64, s[48:51], s88 offen offset:1024 lds\nQmvaa 176,116,188\nQta 166,61,21408\n"
"Qmvaa 208,112,224\ns_add_u32 s77, s77, s89\nQmvaa 208,116,192\nQta 168,61,16776\ns_add_u32 s88, s86, 39936\nQmvaa 212,120,224\nbuffer_load_dwordx4 v64, s[48:51], s88 offen offset:2048 lds\nQmvaa 212,124,192\n"
"Qta 170,61,17288\nQmvaa 216,128,224\ns_add_u32 s77, s77, s89\nQmvaa 216,132,192\nQta 172,61,20904\ns_add_u32 s88, s86, 59392\nQmvaa 220,136,224\nbuffer_load_dwordx4 v64, s[48:51], s88 offen offset:3072 lds\n"
"Qmvaa 220,140,192\nQta 174,61,21416\nQmvaa 252,136,228\ns_add_u32 s77, s77, s89\nQmvaa 252,140,196\ns_add_u32 m0, s63, 37120\ns_add_u32 s88, s86, 82944\nQmvaa 248,128,228\n"
"buffer_load_dwordx4 v64, s[48:51], s88 offen lds\nQmvaa 248,132,196\nQmvaa 244,120,228\ns_add_u32 s77, s77, s89\nQmvaa 244,124,196\ns_add_u32 s88, s86, 102400\nQmvaa 240,112,228\nbuffer_load_dwordx4 v64, s[48:51], s88 offen offset:1024 lds\n"
"Qmvaa 240,116,196\nQmvaa 16,112,232\ns_add_u32 s77, s77, s89\nQmvaa 16,116,200\ns_add_u32 s88, s86, 121856\nQmvaa 20,120,232\nbuffer_load_dwordx4 v64, s[48:51], s88 offen offset:2048 lds\nQmvaa 20,124,200\n"
"Qmvaa 24,128,232\ns_add_u32 s77, s77, s89\nQmvaa 24,132,200\nQmvaa 28,136,232\nQmvaa 28,140,200\ns_waitcnt lgkmcnt(0)\nQmvaa 96,144,208\nQmvaa 96,148,176\n"
"s_add_u32 s68, s71, 135168\nQmvaa 100,152,208\ns_cbranch_vccz .Lst1\nbuffer_store_dwordx4 a[104:107], v79, s[56:59], s68 offen offset:2048 sc1\n.Lst1:\nQmvaa 100,156,176\nQmvaa 104,160,208\ns_add_u32 s68, s71, 151552\n"
"Qmvaa 104,164,176\ns_cbranch_vccz .Lst0\nbuffer_store_dwordx4 a[108:111], v79, s[56:59], s68 offen offset:2048 sc1\n.Lst0:\nQmvaa 108,168,208\nQmvaa 108,172,176\ns_waitcnt lgkmcnt(0)\nQmvaa 140,168,212\n"
"s_waitcnt vmcnt(16)\ns_barrier\nQmvaa 140,172,180\nQda 208,63,0\ns_add_u32 m0, s62, 16512\ns_add_u32 s69, s87, 4194432\nQmvaa 136,160,212\nbuffer_load_dwordx4 v62, s[52:55], s69 offen lds\n"
"Qmvaa 136,164,180\nQda 176,63,512\nQmvaa 132,152,212\nQta 112,61,33024\nQmvaa 132,156,180\ns_add_u32 s69, s87, 4258936\nQmvaa 128,144,212\nbuffer_load_dwordx4 v62, s[52:55], s69 offen offset:1032 lds\n"
"Qmvaa 128,148,180\nQda 212,63,1024\nQmvaa 160,144,216\nQda 180,63,1536\nQmvaa 160,148,184\nQta 114,61,33536\ns_add_u32 s69, s87, 4323440\nQmvaa 164,152,216\n"
"buffer_load_dwordx4 v62, s[52:55], s69 offen offset:2064 lds\nQmvaa 164,156,184\nQta 116,61,37152\nQmvaa 168,160,216\nQmvaa 168,164,184\nQta 118,61,37664\ns_add_u32 s69, s87, 4387944\nQmvaa 172,168,216\n"
"buffer_load_dwordx4 v62, s[52:55], s69 offen offset:3096 lds\nQmvaa 172,172,184\nQda 216,63,2048\nQmvaa 204,168,220\nQda 184,63,2560\nQmvaa 204,172,188\nQta 120,61,33032\ns_add_u32 m0, s64, 0\n"
"Qmvaa 200,160,220\ns_cbranch_vccnz .Ld0q7\nbuffer_load_dwordx4 v72, s[44:47], s90 offen offset:3072 nt lds\ns_add_u32 s65, s65, 2048\ns_xor_b32 s60, s65, %1\ns_add_u32 s90, s60, 260096\n.Ld0q7:\nQmvaa 200,164,188\n"
"Qta 122,61,33544\nQmvaa 196,152,220\nQmvaa 196,156,188\nQta 124,61,37160\nQmvaa 192,144,220\nQda 220,63,3072\nQmvaa 192,148,188\nQda 188,63,3584\n"
"Qmvaa 224,144,224\nQta 126,61,37672\nQmvaa 224,148,192\nQmvaa 228,152,224\nQta 128,61,33280\nQmvaa 228,156,192\nQmvaa 232,160,224\nQta 130,61,33792\n"
"Qmvaa 232,164,192\nQmvaa 236,168,224\nQda 224,63,4096\nQmvaa 236,172,192\nQda 192,63,4608\nQmvaa 12,168,228\nQta 132,61,37408\nQmvaa 12,172,196\n"
"Qmvaa 8,160,228\nQta 134,61,37920\nQmvaa 8,164,196\nQmvaa 4,152,228\nQta 136,61,33288\nQmvaa 4,156,196\nQmvaa 0,144,228\nQda 228,63,5120\n"
"Qmvaa 0,148,196\nQda 196,63,5632\nQmvaa 32,144,232\nQta 138,61,33800\nQmvaa 32,148,200\nQmvaa 36,152,232\nQta 140,61,37416\nQmvaa 36,156,200\n"
"Qmvaa 40,160,232\nQta 142,61,37928\nQmvaa 40,164,200\nQmvaa 44,168,232\nQda 232,63,6144\nQmvaa 44,172,200\nQda 200,63,6656\ns_waitcnt lgkmcnt(2)\n"
"Qmvaa 80,112,208\nQmvaa 80,116,176\nQmvaa 84,120,208\nQmvaa 84,124,176\nQmvaa 88,128,208\nQmvaa 88,132,176\nQmvaa 92,136,208\nQmvaa 92,140,176\n"
"s_waitcnt lgkmcnt(0)\nQmvaa 124,136,212\ns_waitcnt vmcnt(17)\ns_barrier\nQmvaa 124,140,180\nQta 144,61,49536\ns_add_u32 m0, s62, 33024\ns_add_u32 s69, s87, 4718592\n"
"Qmvaa 120,128,212\nbuffer_load_dwordx4 v62, s[52:55], s69 offen lds\nQmvaa 120,132,180\nQta 146,61,50048\nQmvaa 116,120,212\nQmvaa 116,124,180\nQta 148,61,53664\ns_add_u32 s69, s87, 4783096\n"
"Qmvaa 112,112,212\nbuffer_load_dwordx4 v62, s[52:55], s69 offen offset:1032 lds\nQmvaa 112,116,180\nQta 150,61,54176\nQmvaa 144,112,216\nQmvaa 144,116,184\nQta 152,61,49544\ns_add_u32 s69, s87, 4847600\n"
"Qmvaa 148,120,216\nbuffer_load_dwordx4 v62, s[52:55], s69 offen offset:2064 lds\nQmvaa 148,124,184\nQta 154,61,50056\nQmvaa 152,128,216\nQmvaa 152,132,184\nQta 156,61,53672\ns_add_u32 s69, s87, 4912104\n"
"Qmvaa 156,136,216\nbuffer_load_dwordx4 v62, s[52:55], s69 offen offset:3096 lds\nQmvaa 156,140,184\nQta 158,61,54184\nQmvaa 188,136,220\nQmvaa 188,140,188\nQta 160,61,49792\ns_add_u32 m0, s63, 0\n"
"s_add_u32 s88, s86, 1152\nQmvaa 184,128,220\nbuffer_load_dwordx4 v64, s[48:51], s88 offen lds\nQmvaa 184,132,188\nQta 162,61,50304\nQmvaa 180,120,220\nQmvaa 180,124,188\nQta 164,61,53920\n"
"s_add_u32 s88, s86, 20608\nQmvaa 176,112,220\nbuffer_load_dwordx4 v64, s[48:51], s88 offen offset:1024 lds\nQmvaa 176,116,188\nQta 166,61,54432\nQmvaa 208,112,224\nQmvaa 208,116,192\nQta 168,61,49800\n"
"s_add_u32 s88, s86, 40064\nQmvaa 212,120,224\nbuffer_load_dwordx4 v64, s[48:51], s88 offen offset:2048 lds\nQmvaa 212,124,192\nQta 170,61,50312\nQmvaa 216,128,224\nQmvaa 216,132,192\nQta 172,61,53928\n"
"s_add_u32 s88, s86, 59520\nQmvaa 220,136,224\nbuffer_load_dwordx4 v64, s[48:51], s88 offen offset:3072 lds\nQmvaa 220,140,192\nQta 174,61,54440\nQmvaa 252,136,228\nQmvaa 252,140,196\ns_add_u32 m0, s63, 4096\n"
"s_add_u32 s88, s86, 83072\nQmvaa 248,128,228\nbuffer_load_dwordx4 v64, s[48:51], s88 offen lds\nQmvaa 248,132,196\nv_mov_b32 v66, s77\nQmvaa 244,120,228\nds_write_b32 v73, v66 offset:16\nQmvaa 244,124,196\n"
"s_add_u32 s88, s86, 102528\nQmvaa 240,112,228\nbuffer_load_dwordx4 v64, s[48:51], s88 offen offset:1024 lds\nQmvaa 240,116,196\nQmvaa 16,112,232\nQmvaa 16,116,200\ns_add_u32 s88, s86, 121984\nQmvaa 20,120,232\n"
"buffer_load_dwordx4 v64, s[48:51], s88 offen offset:2048 lds\nQmvaa 20,124,200\nQmvaa 24,128,232\nQmvaa 24,132,200\nQmvaa 28,136,232\nQmvaa 28,140,200\ns_waitcnt lgkmcnt(1)\nQmvaa 96,144,208\n"
"Qmvaa 96,148,176\nQmvaa 100,152,208\nQmvaa 100,156,176\nQmvaa 104,160,208\nQmvaa 104,164,176\nQmvaa 108,168,208\nQmvaa 108,172,176\ns_waitcnt lgkmcnt(0)\n"
"Qmvaa 140,168,212\ns_waitcnt vmcnt(16)\ns_barrier\nQmvaa 140,172,180\nQda 208,63,33024\ns_add_u32 m0, s62, 49536\ns_add_u32 s69, s87, 4718720\nQmvaa 136,160,212\n"
"buffer_load_dwordx4 v62, s[52:55], s69 offen lds\nQmvaa 136,164,180\nQda 176,63,33536\nQmvaa 132,152,212\nQta 112,61,0\nQmvaa 132,156,180\ns_add_u32 s69, s87, 4783224\nQmvaa 128,144,212\n"
"buffer_load_dwordx4 v62, s[52:55], s69 offen offset:1032 lds\nQmvaa 128,148,180\nQda 212,63,34048\nQmvaa 160,144,216\nQda 180,63,34560\nQmvaa 160,148,184\nQta 114,61,512\ns_add_u32 s69, s87, 4847728\n"
"Qmvaa 164,152,216\nbuffer_load_dwordx4 v62, s[52:55], s69 offen offset:2064 lds\n.Lpg3:\ns_cbranch_execz .Ltr0\nQmvaa 164,156,184\nQta 116,61,4128\nQmvaa 168,160,216\nQmvaa 168,164,184\n"
"Qta 118,61,4640\ns_add_u32 s69, s87, 4912232\nQmvaa 172,168,216\nbuffer_load_dwordx4 v62, s[52:55], s69 offen offset:3096 lds\nQmvaa 172,172,184\nQda 216,63,35072\nQmvaa 204,168,220\nQda 184,63,35584\n"
"Qmvaa 204,172,188\nQta 120,61,8\nQmvaa 200,160,220\nQmvaa 200,164,188\nQta 122,61,520\nQmvaa 196,152,220\nQmvaa 196,156,188\nQta 124,61,4136\n"
"Qmvaa 192,144,220\nQda 220,63,36096\nQmvaa 192,148,188\nQda 188,63,36608\nQmvaa 224,144,224\nQta 126,61,4648\nQmvaa 224,148,192\nQmvaa 228,152,224\n"
"Qta 128,61,256\nQmvaa 228,156,192\nQmvaa 232,160,224\nQta 130,61,768\nQmvaa 232,164,192\nQmvaa 236,168,224\nQda 224,63,37120\nQmvaa 236,172,192\n"
"Qda 192,63,37632\nQmvaa 12,168,228\nQta 132,61,4384\nQmvaa 12,172,196\nQmvaa 8,160,228\nQta 134,61,4896\nQmvaa 8,164,196\nQmvaa 4,152,228\n"
"Qta 136,61,264\nQmvaa 4,156,196\nQmvaa 0,144,228\nQda 228,63,38144\nQmvaa 0,148,196\nQda 196,63,38656\nQmvaa 32,144,232\nQta 138,61,776\n"
"Qmvaa 32,148,200\nQmvaa 36,152,232\nQta 140,61,4392\nQmvaa 36,156,200\nQmvaa 40,160,232\nQta 142,61,4904\nQmvaa 40,164,200\nQmvaa 44,168,232\n"
"Qda 232,63,39168\nQmvaa 44,172,200\nQda 200,63,39680\ns_waitcnt lgkmcnt(2)\nQmvaa 80,112,208\nQmvaa 80,116,176\nQmvaa 84,120,208\nQmvaa 84,124,176\n"
"Qmvaa 88,128,208\nQmvaa 88,132,176\nQmvaa 92,136,208\nQmvaa 92,140,176\ns_waitcnt lgkmcnt(0)\nQmvaa 124,136,212\ns_waitcnt vmcnt(15)\ns_barrier\n"
"Qmvaa 124,140,180\nQta 144,61,16512\ns_mov_b32 s56, s94\ns_mov_b32 s57, s95\ns_mov_b32 s58, s75\ns_mov_b32 s94, 0\ns_add_u32 m0, %4, 160768\nQmvaa 120,128,212\n"
"buffer_load_dwordx4 v60, s[56:59], s94 offen lds\nQmvaa 120,132,180\nQta 146,61,17024\nQmvaa 116,120,212\nQmvaa 116,124,180\nQta 148,61,20640\ns_add_u32 s94, s94, 4096\nQmvaa 112,112,212\n"
"buffer_load_dwordx4 v60, s[56:59], s94 offen lds\nQmvaa 112,116,180\nQta 150,61,21152\nQmvaa 144,112,216\nQmvaa 144,116,184\nQta 152,61,16520\ns_add_u32 s94, s94, 4096\nQmvaa 148,120,216\n"
"buffer_load_dwordx4 v60, s[56:59], s94 offen lds\nQmvaa 148,124,184\nQta 154,61,17032\nQmvaa 152,128,216\nQmvaa 152,132,184\nQta 156,61,20648\ns_add_u32 s94, s94, 4096\nQmvaa 156,136,216\n"
"buffer_load_dwordx4 v60, s[56:59], s94 offen lds\nQmvaa 156,140,184\nQta 158,61,21160\nQmvaa 188,136,220\nQmvaa 188,140,188\nQta 160,61,16768\ns_add_u32 s94, s94, 4096\nQmvaa 184,128,220\n"
"buffer_load_dwordx4 v60, s[56:59], s94 offen lds\nQmvaa 184,132,188\nQta 162,61,17280\nQmvaa 180,120,220\nQmvaa 180,124,188\nQta 164,61,20896\ns_add_u32 s94, s94, 4096\nQmvaa 176,112,220\n"
"buffer_load_dwordx4 v60, s[56:59], s94 offen lds\nQmvaa 176,116,188\nQta 166,61,21408\nQmvaa 208,112,224\nQmvaa 208,116,192\nQta 168,61,16776\ns_add_u32 s94, s94, 4096\nQmvaa 212,120,224\n"
"buffer_load_dwordx4 v60, s[56:59], s94 offen lds\nQmvaa 212,124,192\nQta 170,61,17288\nQmvaa 216,128,224\nQmvaa 216,132,192\nQta 172,61,20904\ns_add_u32 s94, s94, 4096\nQmvaa 220,136,224\n"
"buffer_load_dwordx4 v60, s[56:59], s94 offen lds\nQmvaa 220,140,192\nQta 174,61,21416\nQmvaa 252,136,228\nQmvaa 252,140,196\ns_add_u32 s94, s94, 4096\nQmvaa 248,128,228\nbuffer_load_dwordx4 v60, s[56:59], s94 offen lds\n"
"Qmvaa 248,132,196\nQmvaa 244,120,228\nQmvaa 244,124,196\ns_add_u32 s94, s94, 4096\nQmvaa 240,112,228\nbuffer_load_dwordx4 v60, s[56:59], s94 offen lds\nQmvaa 240,116,196\nv_mov_b32 v55, 0x3f803f80\n"
"s_mov_b32 vcc_lo, 0x33333333\nQmvaa 16,112,232\ns_mov_b32 vcc_hi, 0xcccccccc\nv_cndmask_b32 v55, 0, v55, vcc\nQmvaa 16,116,200\nv_accvgpr_write_b32 a204, v55\nv_accvgpr_write_b32 a205, v55\nQmvaa 20,120,232\n"
"v_accvgpr_write_b32 a206, v55\nv_accvgpr_write_b32 a207, v55\nQmvaa 20,124,200\ns_add_u32 s87, s87, 2048\nv_mov_b64 v[54:55], 0\nQmvaa 24,128,232\nglobal_load_dword v78, v60, s[100:101]\nQmvaa 24,132,200\n"
"Qmvaa 28,136,232\nQmvaa 28,140,200\ns_waitcnt lgkmcnt(0)\nQmvaa 96,144,208\nQmvaa 96,148,176\nQmvaa 100,152,208\nQmvaa 100,156,176\nQmvaa 104,160,208\n"
"Qmvaa 104,164,176\nQmvaa 108,168,208\nQmvaa 108,172,176\ns_waitcnt lgkmcnt(0)\nQmvaa 140,168,212\ns_waitcnt vmcnt(15)\ns_barrier\nQmvaa 140,172,180\n"
"Qda 208,63,0\ns_add_u32 m0, s61, 33024\nQmvaa 136,160,212\nbuffer_load_dwordx4 v69, s[40:43], s60 offen nt lds\nQmvaa 136,164,180\nQda 176,63,512\nQmvaa 132,152,212\nQta 112,61,33024\n"
"Qmvaa 132,156,180\nQtv 48,70,128\nQmvaa 128,144,212\nbuffer_load_dwordx4 v69, s[40:43], s60 offen offset:1024 nt lds\nQmvaa 128,148,180\nQda 212,63,1024\nQmvaa 160,144,216\nQda 180,63,1536\n"
"Qmvaa 160,148,184\nQta 114,61,33536\nQmvaa 164,152,216\nbuffer_load_dwordx4 v69, s[40:43], s90 offen offset:2048 nt lds\nQmvaa 164,156,184\nQta 116,61,37152\nQmvaa 168,160,216\nQtv 50,70,1152\n"
"Qmvaa 168,164,184\nQta 118,61,37664\nQmvaa 172,168,216\nbuffer_load_dwordx4 v69, s[40:43], s90 offen offset:3072 nt lds\nQmvaa 172,172,184\nQda 216,63,2048\nQmvaa 204,168,220\nQda 184,63,2560\n"
"Qmvaa 204,172,188\nQta 120,61,33032\ns_add_u32 m0, s64, 33024\nQmvaa 200,160,220\nbuffer_load_dwordx4 v72, s[44:47], s60 offen nt lds\nQmvaa 200,164,188\nQta 122,61,33544\ns_waitcnt lgkmcnt(5)\n"
"Qmvaa 196,152,220\nv_max_f32 v66, |v48|, |v50|\nQmvaa 196,156,188\nQta 124,61,37160\nQmvaa 192,144,220\nbuffer_load_dwordx4 v72, s[44:47], s60 offen offset:1024 nt lds\nQmvaa 192,148,188\nQda 220,63,3072\n"
"v_mov_b32 v67, v66\nQmvaa 224,144,224\nQda 188,63,3584\nv_permlane32_swap_b32 v66, v67\nQmvaa 224,148,192\nQta 126,61,37672\nv_max_f32 v66, v66, v67\nQmvaa 228,152,224\n"
"buffer_load_dwordx4 v72, s[44:47], s90 offen offset:2048 nt lds\nQmvaa 228,156,192\nQta 128,61,33280\nv_mov_b32 v67, v66\nQmvaa 232,160,224\ns_nop 0\nv_permlane16_swap_b32 v66, v67\nQmvaa 232,164,192\n"
"Qta 130,61,33792\nv_max_f32 v66, v66, v67\nQmvaa 236,168,224\nbuffer_load_dwordx4 v72, s[44:47], s90 offen offset:3072 nt lds\ns_add_u32 s65, s65, 2048\ns_xor_b32 s60, s65, %1\ns_add_u32 s90, s60, 260096\nQmvaa 236,172,192\n"
"Qda 224,63,4096\nv_min_f32_dpp v66, v66, v66 row_ror:8 row_mask:0xf bank_mask:0xf\nQmvaa 12,168,228\nQda 192,63,4608\nv_min_f32_dpp v66, v66, v66 row_ror:4 row_mask:0xf bank_mask:0xf\nQmvaa 12,172,196\nQta 132,61,37408\nQmvaa 8,160,228\n"
"v_min_f32_dpp v66, v66, v66 row_ror:2 row_mask:0xf bank_mask:0xf\nQmvaa 8,164,196\nQta 134,61,37920\nv_min_f32_dpp v66, v66, v66 row_ror:1 row_mask:0xf bank_mask:0xf\nQmvaa 4,152,228\nQmvaa 4,156,196\nQta 136,61,33288\nv_and_b32 v66, 0x7f000000, v66\n"
"Qmvaa 0,144,228\nQda 228,63,5120\nv_or_b32 v66, 0x400000, v66\nQmvaa 0,148,196\nQda 196,63,5632\nQmvaa 32,144,232\nQta 138,61,33800\nv_mov_b32 v67, 0\n"
"Qmvaa 32,148,200\nv_mov_b32 v58, 0x39800000\nQmvaa 36,152,232\nQta 140,61,37416\nQmvaa 36,156,200\nv_mov_b32 v59, 0\nQmvaa 40,160,232\nQta 142,61,37928\n"
"v_readfirstlane_b32 s73, v66\nQmvaa 40,164,200\nQmvaa 44,168,232\nQda 232,63,6144\nQmvaa 44,172,200\nQda 200,63,6656\ns_waitcnt lgkmcnt(2)\nQmvaa 80,112,208\n"
"Qmvaa 80,116,176\nQmvaa 84,120,208\nQmvaa 84,124,176\nQmvaa 88,128,208\nQmvaa 88,132,176\nQmvaa 92,136,208\nQmvaa 92,140,176\ns_waitcnt lgkmcnt(0)\n"
"Qmvaa 124,136,212\ns_waitcnt vmcnt(19)\ns_barrier\nQmvaa 124,140,180\nQta 144,61,49536\nv_fma_f32 v80, v80, s72, v52\nQmvaa 120,128,212\nv_fma_f32 v81, v81, s72, v52\n"
"v_fma_f32 v82, v82, s72, v52\nQmvaa 120,132,180\nQta 146,61,50048\nv_fma_f32 v83, v83, s72, v52\nQmvaa 116,120,212\nv_fma_f32 v84, v84, s72, v52\nv_fma_f32 v85, v85, s72, v52\nQmvaa 116,124,180\n"
"Qta 148,61,53664\nv_fma_f32 v86, v86, s72, v52\nQmvaa 112,112,212\nv_fma_f32 v87, v87, s72, v52\nQk 80,80,81\nQmvaa 112,116,180\nQta 150,61,54176\nQk 81,82,83\n"
"Qmvaa 144,112,216\nQk 82,84,85\nQk 83,86,87\nQmvaa 144,116,184\nQta 152,61,49544\nv_accvgpr_write_b32 a0, v80\nQmvaa 148,120,216\nv_accvgpr_write_b32 a1, v81\n"
"v_accvgpr_write_b32 a2, v82\nQmvaa 148,124,184\nQta 154,61,50056\nv_accvgpr_write_b32 a3, v83\nQmvaa 152,128,216\nv_fma_f32 v88, v88, s72, v52\nv_fma_f32 v89, v89, s72, v52\nQmvaa 152,132,184\n"
"Qta 156,61,53672\nv_fma_f32 v90, v90, s72, v52\nQmvaa 156,136,216\nv_fma_f32 v91, v91, s72, v52\nv_fma_f32 v92, v92, s72, v52\nQmvaa 156,140,184\nQta 158,61,54184\nv_fma_f32 v93, v93, s72, v52\n"
"Qmvaa 188,136,220\nv_fma_f32 v94, v94, s72, v52\nv_fma_f32 v95, v95, s72, v52\nQmvaa 188,140,188\nQta 160,61,49792\nQk 88,88,89\nQmvaa 184,128,220\nQk 89,90,91\n"
"Qk 90,92,93\nQmvaa 184,132,188\nQta 162,61,50304\nQk 91,94,95\nQmvaa 180,120,220\nv_accvgpr_write_b32 a4, v88\nv_accvgpr_write_b32 a5, v89\nQmvaa 180,124,188\n"
"Qta 164,61,53920\nv_accvgpr_write_b32 a6, v90\nQmvaa 176,112,220\nv_accvgpr_write_b32 a7, v91\nv_fma_f32 v120, v120, s72, v52\nQmvaa 176,116,188\nQta 166,61,54432\nv_fma_f32 v121, v121, s72, v52\n"
"Qmvaa 208,112,224\nv_fma_f32 v122, v122, s72, v52\nv_fma_f32 v123, v123, s72, v52\nQmvaa 208,116,192\nQta 168,61,49800\nv_fma_f32 v124, v124, s72, v52\nQmvaa 212,120,224\nv_fma_f32 v125, v125, s72, v52\n"
"v_fma_f32 v126, v126, s72, v52\nQmvaa 212,124,192\nQta 170,61,50312\nv_fma_f32 v127, v127, s72, v52\nQmvaa 216,128,224\nQk 120,120,121\nQk 121,122,123\nQmvaa 216,132,192\n"
"Qta 172,61,53928\nQk 122,124,125\nQmvaa 220,136,224\nQk 123,126,127\nv_accvgpr_write_b32 a20, v120\nQmvaa 220,140,192\nQta 174,61,54440\nv_accvgpr_write_b32 a21, v121\n"
"Qmvaa 252,136,228\nv_accvgpr_write_b32 a22, v122\nv_accvgpr_write_b32 a23, v123\nQmvaa 252,140,196\nv_fma_f32 v112, v112, s72, v52\nv_fma_f32 v113, v113, s72, v52\nQmvaa 248,128,228\nv_fma_f32 v114, v114, s72, v52\n"
"v_fma_f32 v115, v115, s72, v52\nQmvaa 248,132,196\nv_fma_f32 v116, v116, s72, v52\nv_fma_f32 v117, v117, s72, v52\nQmvaa 244,120,228\nv_fma_f32 v118, v118, s72, v52\nv_fma_f32 v119, v119, s72, v52\nQmvaa 244,124,196\n"
"Qk 112,112,113\nQk 113,114,115\nQmvaa 240,112,228\nQk 114,116,117\nQk 115,118,119\nQmvaa 240,116,196\nv_accvgpr_write_b32 a16, v112\nv_accvgpr_write_b32 a17, v113\n"
"Qmvaa 16,112,232\nv_accvgpr_write_b32 a18, v114\nv_accvgpr_write_b32 a19, v115\nQmvaa 16,116,200\nv_fma_f32 v144, v144, s72, v52\nv_fma_f32 v145, v145, s72, v52\nQmvaa 20,120,232\nv_fma_f32 v146, v146, s72, v52\n"
"v_fma_f32 v147, v147, s72, v52\nQmvaa 20,124,200\nv_fma_f32 v148, v148, s72, v52\nv_fma_f32 v149, v149, s72, v52\nQmvaa 24,128,232\nv_fma_f32 v150, v150, s72, v52\nv_fma_f32 v151, v151, s72, v52\nQmvaa 24,132,200\n"
"Qk 144,144,145\nQk 145,146,147\nQmvaa 28,136,232\nQk 146,148,149\nQk 147,150,151\nQmvaa 28,140,200\nv_accvgpr_write_b32 a32, v144\nv_accvgpr_write_b32 a33, v145\n"
"s_waitcnt lgkmcnt(0)\nQmvaa 96,144,208\nv_accvgpr_write_b32 a34, v146\nv_accvgpr_write_b32 a35, v147\nQmvaa 96,148,176\nv_fma_f32 v152, v152, s72, v52\nv_fma_f32 v153, v153, s72, v52\nQmvaa 100,152,208\n"
"v_fma_f32 v154, v154, s72, v52\nv_fma_f32 v155, v155, s72, v52\nQmvaa 100,156,176\nv_fma_f32 v156, v156, s72, v52\nv_fma_f32 v157, v157, s72, v52\nQmvaa 104,160,208\nv_fma_f32 v158, v158, s72, v52\nv_fma_f32 v159, v159, s72, v52\n"
"Qmvaa 104,164,176\nQk 152,152,153\nQk 153,154,155\nQmvaa 108,168,208\nQk 154,156,157\nQk 155,158,159\nQmvaa 108,172,176\ns_waitcnt lgkmcnt(0)\n"
"v_accvgpr_write_b32 a36, v152\nQmvaa 140,168,212\ns_waitcnt vmcnt(34)\ns_barrier\nQmvaa 140,172,180\nQda 112,68,0\ns_add_u32 m0, s61, 66048\nQmvaa 136,160,212\n"
"buffer_load_dwordx4 v69, s[40:43], s60 offen nt lds\nQmvaa 136,164,180\nQda 116,68,512\nv_accvgpr_write_b32 a37, v153\nQmvaa 132,152,212\nv_accvgpr_write_b32 a38, v154\nv_accvgpr_write_b32 a39, v155\nQmvaa 132,156,180\n"
"Qda 120,68,256\nv_fma_f32 v184, v184, s72, v52\nQmvaa 128,144,212\nbuffer_load_dwordx4 v69, s[40:43], s60 offen offset:1024 nt lds\nQmvaa 128,148,180\nQda 124,68,768\nv_fma_f32 v185, v185, s72, v52\nQmvaa 160,144,216\n"
"v_fma_f32 v186, v186, s72, v52\nv_fma_f32 v187, v187, s72, v52\nQmvaa 160,148,184\nQda 128,68,1024\nv_fma_f32 v188, v188, s72, v52\nQmvaa 164,152,216\nbuffer_load_dwordx4 v69, s[40:43], s90 offen offset:2048 nt lds\nQmvaa 164,156,184\n"
"Qda 132,68,1536\nv_fma_f32 v189, v189, s72, v52\nQmvaa 168,160,216\nv_fma_f32 v190, v190, s72, v52\nv_fma_f32 v191, v191, s72, v52\nQmvaa 168,164,184\nQda 136,68,1280\nQk 184,184,185\n"
"Qmvaa 172,168,216\nbuffer_load_dwordx4 v69, s[40:43], s90 offen offset:3072 nt lds\nQmvaa 172,172,184\nQda 140,68,1792\nQk 185,186,187\nQmvaa 204,168,220\nQk 186,188,189\nQk 187,190,191\n"
"Qmvaa 204,172,188\ns_add_u32 m0, s64, 66048\nv_accvgpr_write_b32 a52, v184\nQmvaa 200,160,220\n.Lpg4:\ns_cbranch_execz .Ltr9\nbuffer_load_dwordx4 v72, s[44:47], s60 offen nt lds\nQmvaa 200,164,188\n"
"v_accvgpr_write_b32 a53, v185\nv_accvgpr_write_b32 a54, v186\nQmvaa 196,152,220\nv_accvgpr_write_b32 a55, v187\nv_fma_f32 v176, v176, s72, v52\nQmvaa 196,156,188\nv_fma_f32 v177, v177, s72, v52\nv_fma_f32 v178, v178, s72, v52\n"
"Qmvaa 192,144,220\nbuffer_load_dwordx4 v72, s[44:47], s60 offen offset:1024 nt lds\nQmvaa 192,148,188\nv_fma_f32 v179, v179, s72, v52\nv_fma_f32 v180, v180, s72, v52\nQmvaa 224,144,224\nv_fma_f32 v181, v181, s72, v52\nv_fma_f32 v182, v182, s72, v52\n"
"Qmvaa 224,148,192\nv_fma_f32 v183, v183, s72, v52\nQk 176,176,177\nQmvaa 228,152,224\nbuffer_load_dwordx4 v72, s[44:47], s90 offen offset:2048 nt lds\nQmvaa 228,156,192\nQk 177,178,179\nQk 178,180,181\n"
"Qmvaa 232,160,224\nQk 179,182,183\nv_accvgpr_write_b32 a48, v176\nQmvaa 232,164,192\nv_accvgpr_write_b32 a49, v177\nv_accvgpr_write_b32 a50, v178\nQmvaa 236,168,224\nbuffer_load_dwordx4 v72, s[44:47], s90 offen offset:3072 nt lds\n"
"s_add_u32 s65, s65, 2048\ns_xor_b32 s60, s65, %1\ns_add_u32 s90, s60, 260096\nQmvaa 236,172,192\nv_accvgpr_write_b32 a51, v179\nv_fma_f32 v208, v208, s72, v52\nQmvaa 12,168,228\nv_fma_f32 v209, v209, s72, v52\n"
"v_fma_f32 v210, v210, s72, v52\nQmvaa 12,172,196\nv_fma_f32 v211, v211, s72, v52\nv_fma_f32 v212, v212, s72, v52\nQmvaa 8,160,228\nv_fma_f32 v213, v213, s72, v52\nv_fma_f32 v214, v214, s72, v52\nQmvaa 8,164,196\n"
"v_fma_f32 v215, v215, s72, v52\nQk 208,208,209\nQmvaa 4,152,228\nQk 209,210,211\nQk 210,212,213\nQmvaa 4,156,196\nQk 211,214,215\nv_accvgpr_write_b32 a64, v208\n"
"Qmvaa 0,144,228\nv_accvgpr_write_b32 a65, v209\nv_accvgpr_write_b32 a66, v210\nQmvaa 0,148,196\nv_accvgpr_write_b32 a67, v211\nv_fma_f32 v216, v216, s72, v52\nQmvaa 32,144,232\nv_fma_f32 v217, v217, s72, v52\n"
"v_fma_f32 v218, v218, s72, v52\nQmvaa 32,148,200\nv_fma_f32 v219, v219, s72, v52\nv_fma_f32 v220, v220, s72, v52\nQmvaa 36,152,232\nv_fma_f32 v221, v221, s72, v52\nv_fma_f32 v222, v222, s72, v52\nQmvaa 36,156,200\n"
"v_fma_f32 v223, v223, s72, v52\nQk 216,216,217\nQmvaa 40,160,232\nQk 217,218,219\nQk 218,220,221\nQmvaa 40,164,200\nQk 219,222,223\nv_accvgpr_write_b32 a68, v216\n"
"Qmvaa 44,168,232\nv_accvgpr_write_b32 a69, v217\nv_accvgpr_write_b32 a70, v218\nQmvaa 44,172,200\nv_accvgpr_write_b32 a71, v219\nQta 148,70,128\nQta 150,70,1152\nQta 156,70,384\n"
"Qta 158,70,1408\nQta 164,70,640\nQta 166,70,1664\nQta 172,70,896\nQta 174,70,1920\nv_pk_fma_f32 v[96:97], v[96:97], s[72:73], v[52:53] op_sel_hi:[1,0,0]\nv_pk_fma_f32 v[98:99], v[98:99], s[72:73], v[52:53] op_sel_hi:[1,0,0]\nv_pk_fma_f32 v[100:101], v[100:101], s[72:73], v[52:53] op_sel_hi:[1,0,0]\n"
"v_pk_fma_f32 v[102:103], v[102:103], s[72:73], v[52:53] op_sel_hi:[1,0,0]\nv_mfma_f32_16x16x4_4b_bf16 v[80:95], v[66:67], v[58:59], 0\nQk 96,96,97\nQk 97,98,99\nQk 98,100,101\nQk 99,102,103\nv_accvgpr_write_b32 a8, v96\nv_accvgpr_write_b32 a9, v97\n"
"v_accvgpr_write_b32 a10, v98\nv_accvgpr_write_b32 a11, v99\nv_pk_fma_f32 v[104:105], v[104:105], s[72:73], v[52:53] op_sel_hi:[1,0,0]\nv_pk_fma_f32 v[106:107], v[106:107], s[72:73], v[52:53] op_sel_hi:[1,0,0]\nv_pk_fma_f32 v[108:109], v[108:109], s[72:73], v[52:53] op_sel_hi:[1,0,0]\nv_pk_fma_f32 v[110:111], v[110:111], s[72:73], v[52:53] op_sel_hi:[1,0,0]\nv_mfma_f32_16x16x4_4b_bf16 v[112:127], v[66:67], v[58:59], 0\nQk 104,104,105\n"
"Qk 105,106,107\nQk 106,108,109\nQk 107,110,111\nv_accvgpr_write_b32 a12, v104\nv_accvgpr_write_b32 a13, v105\nv_accvgpr_write_b32 a14, v106\nv_accvgpr_write_b32 a15, v107\nv_pk_fma_f32 v[128:129], v[128:129], s[72:73], v[52:53] op_sel_hi:[1,0,0]\n"
"v_pk_fma_f32 v[130:131], v[130:131], s[72:73], v[52:53] op_sel_hi:[1,0,0]\nv_pk_fma_f32 v[132:133], v[132:133], s[72:73], v[52:53] op_sel_hi:[1,0,0]\nv_pk_fma_f32 v[134:135], v[134:135], s[72:73], v[52:53] op_sel_hi:[1,0,0]\nv_mfma_f32_16x16x4_4b_bf16 v[144:159], v[66:67], v[58:59], 0\nQk 128,128,129\nQk 129,130,131\nQk 130,132,133\nQk 131,134,135\n"
"v_accvgpr_write_b32 a24, v128\nv_accvgpr_write_b32 a25, v129\nv_accvgpr_write_b32 a26, v130\nv_accvgpr_write_b32 a27, v131\nv_pk_fma_f32 v[136:137], v[136:137], s[72:73], v[52:53] op_sel_hi:[1,0,0]\nv_pk_fma_f32 v[138:139], v[138:139], s[72:73], v[52:53] op_sel_hi:[1,0,0]\nv_pk_fma_f32 v[140:141], v[140:141], s[72:73], v[52:53] op_sel_hi:[1,0,0]\nv_pk_fma_f32 v[142:143], v[142:143], s[72:73], v[52:53] op_sel_hi:[1,0,0]\n"
"v_mfma_f32_16x16x4_4b_bf16 v[176:191], v[66:67], v[58:59], 0\nQk 136,136,137\nQk 137,138,139\nQk 138,140,141\nQk 139,142,143\nv_accvgpr_write_b32 a28, v136\nv_accvgpr_write_b32 a29, v137\nv_accvgpr_write_b32 a30, v138\n"
"v_accvgpr_write_b32 a31, v139\nv_pk_fma_f32 v[160:161], v[160:161], s[72:73], v[52:53] op_sel_hi:[1,0,0]\nv_pk_fma_f32 v[162:163], v[162:163], s[72:73], v[52:53] op_sel_hi:[1,0,0]\nv_pk_fma_f32 v[164:165], v[164:165], s[72:73], v[52:53] op_sel_hi:[1,0,0]\nv_pk_fma_f32 v[166:167], v[166:167], s[72:73], v[52:53] op_sel_hi:[1,0,0]\nv_mfma_f32_16x16x4_4b_bf16 v[208:223], v[66:67], v[58:59], 0\nQk 160,160,161\nQk 161,162,163\n"
"Qk 162,164,165\nQk 163,166,167\nv_accvgpr_write_b32 a40, v160\nv_accvgpr_write_b32 a41, v161\nv_accvgpr_write_b32 a42, v162\nv_accvgpr_write_b32 a43, v163\nv_pk_fma_f32 v[168:169], v[168:169], s[72:73], v[52:53] op_sel_hi:[1,0,0]\nv_pk_fma_f32 v[170:171], v[170:171], s[72:73], v[52:53] op_sel_hi:[1,0,0]\n"
"v_pk_fma_f32 v[172:173], v[172:173], s[72:73], v[52:53] op_sel_hi:[1,0,0]\nv_pk_fma_f32 v[174:175], v[174:175], s[72:73], v[52:53] op_sel_hi:[1,0,0]\nv_mfma_f32_16x16x4_4b_bf16 a[208:223], v[66:67], v[58:59], 0\nQk 168,168,169\nQk 169,170,171\nQk 170,172,173\nQk 171,174,175\nv_accvgpr_write_b32 a44, v168\n"
"v_accvgpr_write_b32 a45, v169\nv_accvgpr_write_b32 a46, v170\nv_accvgpr_write_b32 a47, v171\nv_pk_fma_f32 v[192:193], v[192:193], s[72:73], v[52:53] op_sel_hi:[1,0,0]\nv_pk_fma_f32 v[194:195], v[194:195], s[72:73], v[52:53] op_sel_hi:[1,0,0]\nv_pk_fma_f32 v[196:197], v[196:197], s[72:73], v[52:53] op_sel_hi:[1,0,0]\nv_pk_fma_f32 v[198:199], v[198:199], s[72:73], v[52:53] op_sel_hi:[1,0,0]\nv_mfma_f32_16x16x4_4b_bf16 a[224:239], v[66:67], v[58:59], 0\n"
"Qk 192,192,193\nQk 193,194,195\nQk 194,196,197\nQk 195,198,199\nv_accvgpr_write_b32 a56, v192\nv_accvgpr_write_b32 a57, v193\nv_accvgpr_write_b32 a58, v194\nv_accvgpr_write_b32 a59, v195\n"
"v_pk_fma_f32 v[200:201], v[200:201], s[72:73], v[52:53] op_sel_hi:[1,0,0]\nv_pk_fma_f32 v[202:203], v[202:203], s[72:73], v[52:53] op_sel_hi:[1,0,0]\nv_pk_fma_f32 v[204:205], v[204:205], s[72:73], v[52:53] op_sel_hi:[1,0,0]\nv_pk_fma_f32 v[206:207], v[206:207], s[72:73], v[52:53] op_sel_hi:[1,0,0]\nv_mfma_f32_16x16x4_4b_bf16 a[240:255], v[66:67], v[58:59], 0\nQk 200,200,201\nQk 201,202,203\nQk 202,204,205\n"
"Qk 203,206,207\nv_accvgpr_write_b32 a60, v200\nv_accvgpr_write_b32 a61, v201\nv_accvgpr_write_b32 a62, v202\nv_accvgpr_write_b32 a63, v203\nv_pk_fma_f32 v[224:225], v[224:225], s[72:73], v[52:53] op_sel_hi:[1,0,0]\nv_pk_fma_f32 v[226:227], v[226:227], s[72:73], v[52:53] op_sel_hi:[1,0,0]\nv_pk_fma_f32 v[228:229], v[228:229], s[72:73], v[52:53] op_sel_hi:[1,0,0]\n"
"v_pk_fma_f32 v[230:231], v[230:231], s[72:73], v[52:53] op_sel_hi:[1,0,0]\nv_mfma_f32_16x16x4_4b_bf16 v[96:111], v[66:67], v[58:59], 0\nQk 224,224,225\nQk 225,226,227\nQk 226,228,229\nQk 227,230,231\nv_accvgpr_write_b32 a72, v224\nv_accvgpr_write_b32 a73, v225\n"
"v_accvgpr_write_b32 a74, v226\nv_accvgpr_write_b32 a75, v227\nv_pk_fma_f32 v[232:233], v[232:233], s[72:73], v[52:53] op_sel_hi:[1,0,0]\nv_pk_fma_f32 v[234:235], v[234:235], s[72:73], v[52:53] op_sel_hi:[1,0,0]\nv_pk_fma_f32 v[236:237], v[236:237], s[72:73], v[52:53] op_sel_hi:[1,0,0]\nv_pk_fma_f32 v[238:239], v[238:239], s[72:73], v[52:53] op_sel_hi:[1,0,0]\nv_mfma_f32_16x16x4_4b_bf16 v[128:143], v[66:67], v[58:59], 0\nQk 232,232,233\n"
"Qk 233,234,235\nQk 234,236,237\nQk 235,238,239\nv_accvgpr_write_b32 a76, v232\nv_accvgpr_write_b32 a77, v233\nv_accvgpr_write_b32 a78, v234\nv_accvgpr_write_b32 a79, v235\nv_pk_fma_f32 v[240:241], v[240:241], s[72:73], v[52:53] op_sel_hi:[1,0,0]\n"
"v_pk_fma_f32 v[242:243], v[242:243], s[72:73], v[52:53] op_sel_hi:[1,0,0]\nv_pk_fma_f32 v[244:245], v[244:245], s[72:73], v[52:53] op_sel_hi:[1,0,0]\nv_pk_fma_f32 v[246:247], v[246:247], s[72:73], v[52:53] op_sel_hi:[1,0,0]\nv_mfma_f32_16x16x4_4b_bf16 v[160:175], v[66:67], v[58:59], 0\nQk 240,240,241\nQk 241,242,243\nQk 242,244,245\nQk 243,246,247\n"
"v_accvgpr_write_b32 a80, v240\nv_accvgpr_write_b32 a81, v241\nv_accvgpr_write_b32 a82, v242\nv_accvgpr_write_b32 a83, v243\nv_pk_fma_f32 v[248:249], v[248:249], s[72:73], v[52:53] op_sel_hi:[1,0,0]\nv_pk_fma_f32 v[250:251], v[250:251], s[72:73], v[52:53] op_sel_hi:[1,0,0]\nv_pk_fma_f32 v[252:253], v[252:253], s[72:73], v[52:53] op_sel_hi:[1,0,0]\nv_pk_fma_f32 v[254:255], v[254:255], s[72:73], v[52:53] op_sel_hi:[1,0,0]\n"
"v_mfma_f32_16x16x4_4b_bf16 v[192:207], v[66:67], v[58:59], 0\nQk 248,248,249\nQk 249,250,251\nQk 250,252,253\nQk 251,254,255\nv_accvgpr_write_b32 a84, v248\nv_accvgpr_write_b32 a85, v249\nv_accvgpr_write_b32 a86, v250\n"
"v_accvgpr_write_b32 a87, v251\nv_pk_fma_f32 v[0:1], v[0:1], s[72:73], v[52:53] op_sel_hi:[1,0,0]\nv_pk_fma_f32 v[2:3], v[2:3], s[72:73], v[52:53] op_sel_hi:[1,0,0]\nv_pk_fma_f32 v[4:5], v[4:5], s[72:73], v[52:53] op_sel_hi:[1,0,0]\nv_pk_fma_f32 v[6:7], v[6:7], s[72:73], v[52:53] op_sel_hi:[1,0,0]\nv_mfma_f32_16x16x4_4b_bf16 v[224:239], v[66:67], v[58:59], 0\nQk 0,0,1\nQk 1,2,3\n"
"Qk 2,4,5\nQk 3,6,7\nv_accvgpr_write_b32 a88, v0\nv_accvgpr_write_b32 a89, v1\nv_accvgpr_write_b32 a90, v2\nv_accvgpr_write_b32 a91, v3\nv_pk_fma_f32 v[8:9], v[8:9], s[72:73], v[52:53] op_sel_hi:[1,0,0]\nv_pk_fma_f32 v[10:11], v[10:11], s[72:73], v[52:53] op_sel_hi:[1,0,0]\n"
"v_pk_fma_f32 v[12:13], v[12:13], s[72:73], v[52:53] op_sel_hi:[1,0,0]\nv_pk_fma_f32 v[14:15], v[14:15], s[72:73], v[52:53] op_sel_hi:[1,0,0]\nv_mfma_f32_16x16x4_4b_bf16 v[240:255], v[66:67], v[58:59], 0\nQk 8,8,9\nQk 9,10,11\nQk 10,12,13\nQk 11,14,15\nv_accvgpr_write_b32 a92, v8\n"
"v_accvgpr_write_b32 a93, v9\nv_accvgpr_write_b32 a94, v10\nv_accvgpr_write_b32 a95, v11\nv_pk_fma_f32 v[16:17], v[16:17], s[72:73], v[52:53] op_sel_hi:[1,0,0]\nv_pk_fma_f32 v[18:19], v[18:19], s[72:73], v[52:53] op_sel_hi:[1,0,0]\nv_pk_fma_f32 v[20:21], v[20:21], s[72:73], v[52:53] op_sel_hi:[1,0,0]\nv_pk_fma_f32 v[22:23], v[22:23], s[72:73], v[52:53] op_sel_hi:[1,0,0]\nv_mfma_f32_16x16x4_4b_bf16 a[176:191], v[54:55], v[54:55], 0\n"
"Qk 16,16,17\nQk 17,18,19\nQk 18,20,21\nQk 19,22,23\nv_accvgpr_write_b32 a96, v16\nv_accvgpr_write_b32 a97, v17\nv_accvgpr_write_b32 a98, v18\nv_accvgpr_write_b32 a99, v19\n"
"v_pk_fma_f32 v[24:25], v[24:25], s[72:73], v[52:53] op_sel_hi:[1,0,0]\nv_pk_fma_f32 v[26:27], v[26:27], s[72:73], v[52:53] op_sel_hi:[1,0,0]\nv_pk_fma_f32 v[28:29], v[28:29], s[72:73], v[52:53] op_sel_hi:[1,0,0]\nv_pk_fma_f32 v[30:31], v[30:31], s[72:73], v[52:53] op_sel_hi:[1,0,0]\nv_mfma_f32_16x16x4_4b_bf16 a[188:203], v[54:55], v[54:55], 0\nQk 24,24,25\nQk 25,26,27\nQk 26,28,29\n"
"Qk 27,30,31\nv_accvgpr_write_b32 a100, v24\nv_accvgpr_write_b32 a101, v25\nv_accvgpr_write_b32 a102, v26\nv_accvgpr_write_b32 a103, v27\nv_pk_fma_f32 v[32:33], v[32:33], s[72:73], v[52:53] op_sel_hi:[1,0,0]\nv_pk_fma_f32 v[34:35], v[34:35], s[72:73], v[52:53] op_sel_hi:[1,0,0]\nv_pk_fma_f32 v[36:37], v[36:37], s[72:73], v[52:53] op_sel_hi:[1,0,0]\n"
"v_pk_fma_f32 v[38:39], v[38:39], s[72:73], v[52:53] op_sel_hi:[1,0,0]\nQk 32,32,33\nQk 33,34,35\nQk 34,36,37\nQk 35,38,39\nv_accvgpr_write_b32 a104, v32\nv_accvgpr_write_b32 a105, v33\nv_accvgpr_write_b32 a106, v34\n"
"v_accvgpr_write_b32 a107, v35\nv_pk_fma_f32 v[40:41], v[40:41], s[72:73], v[52:53] op_sel_hi:[1,0,0]\nv_pk_fma_f32 v[42:43], v[42:43], s[72:73], v[52:53] op_sel_hi:[1,0,0]\nv_pk_fma_f32 v[44:45], v[44:45], s[72:73], v[52:53] op_sel_hi:[1,0,0]\nv_pk_fma_f32 v[46:47], v[46:47], s[72:73], v[52:53] op_sel_hi:[1,0,0]\nQk 40,40,41\nQk 41,42,43\nQk 42,44,45\n"
"Qk 43,46,47\nv_accvgpr_write_b32 a108, v40\nv_accvgpr_write_b32 a109, v41\nv_accvgpr_write_b32 a110, v42\nv_accvgpr_write_b32 a111, v43\ns_waitcnt lgkmcnt(8)\nQzvaa 0,112,0\nQmvaa 0,116,4\n"
"s_memrealtime s[74:75]\ns_memtime s[68:69]\nQmvaa 0,120,8\nQmvaa 0,124,12\nQzvaa 4,140,12\nQmvaa 4,136,8\nQmvaa 4,132,4\nQmvaa 4,128,0\n"
"s_nop 3\nv_max3_f32 v66, v0, v1, v2\ns_nop 2\nv_max3_f32 v66, v66, v3, v4\nv_max3_f32 v66, v66, v5, v6\nv_max_f32 v66, v66, v7\nQzvaa 8,112,16\ns_waitcnt lgkmcnt(0)\n"
"v_mov_b32 v67, v66\nQmvaa 8,116,20\ns_sub_u32 s68, s68, s80\ns_sub_u32 s69, s74, s82\nQmvaa 8,120,24\ns_mul_i32 s69, s69, 73\ns_lshl_b32 s68, s68, 2\nQmvaa 8,124,28\n"
"s_sub_u32 s75, s67, 1\ns_cmp_gt_u32 s68, s69\ns_cselect_b32 s68, s75, 0\ns_cselect_b32 s31, 4, 0\ns_add_u32 s31, s31, 240\nQzvaa 12,140,28\ns_and_b32 s75, s68, 25\ns_add_u32 s77, s74, 60\n"
"Qmvaa 12,136,24\ns_add_u32 s77, s77, s75\nv_permlane32_swap_b32 v66, v67\nQmvaa 12,132,20\nv_max_f32 v66, v66, v67\nv_mov_b32 v67, v66\nQmvaa 12,128,16\ns_nop 0\n"
"v_permlane16_swap_b32 v66, v67\nv_max_f32 v66, v66, v67\ns_nop 1\nv_min_f32_dpp v66, v66, v66 row_ror:8 row_mask:0xf bank_mask:0xf\ns_nop 1\nv_min_f32_dpp v66, v66, v66 row_ror:4 row_mask:0xf bank_mask:0xf\ns_nop 1\nv_min_f32_dpp v66, v66, v66 row_ror:2 row_mask:0xf bank_mask:0xf\n"
"s_nop 1\nv_min_f32_dpp v66, v66, v66 row_ror:1 row_mask:0xf bank_mask:0xf\nv_sub_f32 v56, 0xc1a00000, v66\nv_mov_b32 v57, v56\nv_mov_b32 v58, v56\nv_mov_b32 v59, v56\nQa 0,0,56\nQa 1,1,56\n"
"Qa 2,2,56\nQa 3,3,56\nQe 0\nQe 1\nQe 2\nQe 3\nQk 16,0,1\nQk 17,2,3\n"
"Qcvaa 0,112,32,56\nQa 4,4,56\nQa 5,5,56\nQmvaa 0,116,36\nQa 6,6,56\nQa 7,7,56\nQmvaa 0,120,40\nQe 4\n"
"Qe 5\nQe 6\nQe 7\nQmvaa 0,124,44\nQk 18,4,5\nQk 19,6,7\nQcvaa 4,140,44,56\nQa 8,8,56\n"
"Qa 9,9,56\nQmvaa 4,136,40\nQa 10,10,56\nQa 11,11,56\nQmvaa 4,132,36\nQa 12,12,56\nQa 13,13,56\nQmvaa 4,128,32\n"
"s_waitcnt lgkmcnt(0)\nQa 14,14,56\nQmvva 96,16,148\nQa 15,15,56\nQmvva 100,16,156\nQe 8\nQmvva 104,16,164\nQe 9\n"
"Qe 10\nQe 11\nQe 12\nQe 13\nQe 14\nQe 15\nQmvva 108,16,172\nQk 20,8,9\n"
"Qk 21,10,11\nQk 22,12,13\nQk 23,14,15\ns_nop 1\nQmvva 140,20,172\nQe 0\nQmvva 136,20,164\nQe 1\n"
"Qmvva 132,20,156\nQe 2\nQmvva 128,20,148\nQe 3\nv_smfmac_f32_16x16x64_bf16 a[176:179], a[204:207], v[16:23], v77\nQk 24,0,1\nQcvaa 8,112,48,56\ns_add_u32 m0, s61, 99072\n"
"Qk 25,2,3\nQmvaa 8,116,52\nbuffer_load_dwordx4 v69, s[40:43], s60 offen nt lds\nQmvaa 8,120,56\nQe 4\nQmvaa 8,124,60\ns_or_b32 s65, s65, 0x1d8000\ns_add_u32 s65, s65, 2048\n"
"Qcvaa 12,140,60,56\ns_andn2_b32 s65, s65, 0x1d8000\nQmvaa 12,136,56\nQe 5\nQmvaa 12,132,52\nbuffer_load_dwordx4 v69, s[40:43], s60 offen offset:1024 nt lds\nQmvaa 12,128,48\nQe 6\n"
"Qcvaa 0,128,64,56\ns_memrealtime s[74:75]\nQk 26,4,5\nQmvaa 0,132,68\nQe 7\nQmvaa 0,136,72\nQk 27,6,7\nQmvaa 0,140,76\n"
"buffer_load_dwordx4 v69, s[40:43], s90 offen offset:2048 nt lds\nQcvaa 4,124,76,56\nQe 8\nQmvaa 4,120,72\nQe 9\nQmvaa 4,116,68\nQe 10\nQmvaa 4,112,64\n"
"Qe 11\nbuffer_load_dwordx4 v69, s[40:43], s90 offen offset:3072 nt lds\nQmvva 160,24,148\nQe 12\nQmvva 164,24,156\nQe 13\ns_add_u32 m0, s64, 99072\nQmvva 168,24,164\n"
"buffer_load_dwordx4 v72, s[44:47], s60 offen nt lds\nQe 14\nQe 15\nQmvva 172,24,172\nQk 28,8,9\nQk 29,10,11\nQk 30,12,13\nQk 31,14,15\n"
"s_nop 1\nQmvva 204,28,172\nQe 0\nbuffer_load_dwordx4 v72, s[44:47], s60 offen offset:1024 nt lds\nQmvva 200,28,164\nQe 1\nQmvva 196,28,156\nQe 2\n"
"Qmvva 192,28,148\nbuffer_load_dwordx4 v72, s[44:47], s90 offen offset:2048 nt lds\nv_smfmac_f32_16x16x64_bf16 a[180:183], a[204:207], v[24:31], v77\nQk 34,0,1\nQcvaa 8,112,80,56\nQe 3\nQmvaa 8,116,84\nQk 35,2,3\n"
"Qmvaa 8,120,88\ns_waitcnt vmcnt(15)\ns_barrier\nQmvaa 8,124,92\nbuffer_load_dwordx4 v72, s[44:47], s90 offen offset:3072 nt lds\nQcvaa 12,140,92,56\ns_waitcnt lgkmcnt(0)\nQmvaa 12,136,88\n"
"s_cmp_gt_u32 s77, s74\ns_cbranch_scc1 .Lsp0\n.Lrp0:\nQmvaa 12,132,84\ns_and_b32 s75, s68, 30\ns_add_u32 s77, s74, 90\nQmvaa 12,128,80\ns_add_u32 s77, s77, s75\n"
"s_xor_b32 s60, s65, %1\ns_add_u32 s90, s60, 260096\nQcvaa 0,128,96,56\nQda 128,68,34048\nQmvaa 0,132,100\nQda 132,68,34560\nQmvaa 0,136,104\nQda 136,68,34304\n"
"Qe 4\nQe 5\nQe 6\nQe 7\nQmvaa 0,140,108\nQda 140,68,34816\nQk 32,4,5\nQk 33,6,7\n"
"Qcvaa 4,124,108,56\nQda 124,68,33792\nQmvaa 4,120,104\nQda 120,68,33280\nQmvaa 4,116,100\nQda 116,68,33536\nQmvaa 4,112,96\nQda 112,68,33024\n"
"Qmvva 224,32,148\nQe 8\nQmvva 228,32,156\nQe 9\nQmvva 232,32,164\nQe 10\nQe 11\nQmvva 236,32,172\n"
"s_waitcnt lgkmcnt(0)\nQk 36,8,9\n.Lpg5:\ns_cbranch_execz .Ltr9\nQk 37,10,11\nQcvaa 8,112,0,56\nQe 12\nQmvaa 8,116,4\n"
"v_add_u32 v68, 66048, v68\nQmvaa 8,120,8\nQe 13\nQe 14\nQe 15\nQmvaa 8,124,12\nQk 38,12,13\nQk 39,14,15\n"
"Qcvaa 12,140,12,56\nQe 0\nQmvaa 12,136,8\nQe 1\nQmvaa 12,132,4\nQe 2\nQmvaa 12,128,0\nQe 3\n"
"v_smfmac_f32_16x16x64_bf16 a[184:187], a[204:207], v[32:39], v77\nQk 42,0,1\nQmava 208,36,148\nQe 4\nQmava 212,36,156\nQe 5\nQmava 216,36,164\nQe 6\n"
"Qe 8\nQe 7\nQmava 220,36,172\nQe 9\nQk 43,2,3\nQk 40,4,5\nQk 41,6,7\nQta 168,70,768\n"
"Qta 170,70,1792\nQmava 252,40,172\nQe 10\nQta 172,70,33792\nQta 174,70,34816\nQmava 248,40,164\nQe 11\nQta 160,70,512\n"
"Qta 162,70,1536\nQmava 244,40,156\nQk 44,8,9\nQk 45,10,11\nQta 164,70,33536\nQta 166,70,34560\nQmava 240,40,148\nQe 12\n"
"Qcvaa 0,112,16,56\nQta 152,70,256\nQta 154,70,1280\nQmvaa 0,116,20\nQe 13\nQmvaa 0,120,24\nQta 156,70,33280\nQta 158,70,34304\n"
"Qmvaa 0,124,28\nQe 14\nQcvaa 4,140,28,56\nQta 144,70,0\nQta 146,70,1024\nQmvaa 4,136,24\nQe 15\nQmvaa 4,132,20\n"
"Qta 148,70,33024\nQta 150,70,34048\nQmvaa 4,128,16\nQk 46,12,13\nQk 47,14,15\nQcvaa 8,128,32,56\nv_add_u32 v75, 1, v75\nQmvaa 8,132,36\n"
"s_waitcnt vmcnt(8)\nds_write_b32 v73, v75\nQmvaa 8,136,40\nQe 0\nQmvaa 8,140,44\nQe 1\nQcvaa 12,124,44,56\nQe 2\n"
"Qmvaa 12,120,40\nQe 3\nQmvaa 12,116,36\nQe 4\nQmvaa 12,112,32\ns_waitcnt lgkmcnt(1)\nQk 48,0,1\nv_smfmac_f32_16x16x64_bf16 a[188:191], a[204:207], v[40:47], v77\n"
"Qk 49,2,3\nQmvva 80,16,144\nQe 5\nQmvva 80,44,148\nQe 6\nQmvva 84,16,152\nQe 7\nQmvva 84,44,156\n"
"Qk 50,4,5\nQk 51,6,7\nQmvva 88,16,160\nQe 8\nQmvva 88,44,164\nQe 9\nQmvva 92,16,168\nQe 10\n"
"Qmvva 92,44,172\nQe 11\nQmvva 124,20,168\nQk 54,8,9\nQk 55,10,11\nQmvva 124,48,172\nds_read_b32 v66, v74\nQmvva 120,20,160\n"
"Qe 12\nQmvva 120,48,164\nQe 13\nQmvva 116,20,152\nQe 14\nQmvva 116,48,156\nQe 15\nQmvva 112,20,144\n"
"s_waitcnt lgkmcnt(0)\nv_cmp_gt_u32 vcc, v75, v66\nQmvva 112,48,148\ns_cbranch_vccnz .Lsa1\n.Lra1:\nQk 52,12,13\nQcvaa 0,112,48,56\ns_add_u32 m0, s61, 0\n"
"Qk 53,14,15\nQmvaa 0,116,52\nbuffer_load_dwordx4 v69, s[40:43], s60 offen nt lds\nQmvaa 0,120,56\nQmvaa 0,124,60\ns_add_u32 s65, s65, 2048\nQcvaa 4,140,60,56\nQmvaa 4,136,56\n"
"Qmvaa 4,132,52\nbuffer_load_dwordx4 v69, s[40:43], s60 offen offset:1024 nt lds\nQmvaa 4,128,48\nQcvaa 8,128,64,56\ns_memrealtime s[74:75]\nQmvaa 8,132,68\nQe 0\nQmvaa 8,136,72\n"
"Qe 1\nQmvaa 8,140,76\nbuffer_load_dwordx4 v69, s[40:43], s90 offen offset:2048 nt lds\nQcvaa 12,124,76,56\nQe 2\nQmvaa 12,120,72\nQe 3\nQmvaa 12,116,68\n"
"Qe 4\nQmvaa 12,112,64\nQe 5\nv_smfmac_f32_16x16x64_bf16 a[192:195], a[204:207], v[48:55], v77\nQk 20,0,1\nQmvva 144,24,144\nbuffer_load_dwordx4 v69, s[40:43], s90 offen offset:3072 nt lds\nQmvva 144,52,148\n"
"Qe 6\nQmvva 148,24,152\nQe 7\nQmvva 148,52,156\ns_add_u32 m0, s64, 0\nQk 21,2,3\nQmvva 152,24,160\nbuffer_load_dwordx4 v72, s[44:47], s60 offen nt lds\n"
"Qmvva 152,52,164\nQk 22,4,5\nQk 23,6,7\nQmvva 156,24,168\nQe 8\nQmvva 156,52,172\nQe 9\nQmvva 188,28,168\n"
"Qe 10\nQmvva 188,20,172\nbuffer_load_dwordx4 v72, s[44:47], s60 offen offset:1024 nt lds\nQmvva 184,28,160\nQe 11\nQmvva 184,20,164\nQk 26,8,9\nQk 27,10,11\n"
"Qmvva 180,28,152\nQe 12\nQmvva 180,20,156\nQe 13\nQmvva 176,28,144\nbuffer_load_dwordx4 v72, s[44:47], s90 offen offset:2048 nt lds\nQmvva 176,20,148\nQe 14\n"
"Qcvaa 0,112,80,56\nQe 15\nQmvaa 0,116,84\nQk 24,12,13\nQk 25,14,15\nQmvaa 0,120,88\nQmvaa 0,124,92\nbuffer_load_dwordx4 v72, s[44:47], s90 offen offset:3072 nt lds\n"
"Qcvaa 4,140,92,56\ns_waitcnt lgkmcnt(0)\nQmvaa 4,136,88\ns_cmp_gt_u32 s77, s74\ns_cbranch_scc1 .Lsp1\n.Lrp1:\nQmvaa 4,132,84\ns_and_b32 s75, s68, 45\n"
"s_add_u32 s77, s74, 110\nQmvaa 4,128,80\ns_add_u32 s77, s77, s75\ns_xor_b32 s60, s65, %1\ns_add_u32 s90, s60, 260096\nQcvaa 8,128,96,56\nQda 128,68,1024\ns_add_u32 s79, s79, 2030\n"
"Qmvaa 8,132,100\nQda 132,68,1536\ns_min_u32 s79, s79, s77\nQmvaa 8,136,104\nQda 136,68,1280\nQmvaa 8,140,108\nQda 140,68,1792\nQcvaa 12,124,108,56\n"
"Qda 124,68,768\nQmvaa 12,120,104\nQda 120,68,256\nQmvaa 12,116,100\nQda 116,68,512\nQmvaa 12,112,96\nQda 112,68,0\nv_smfmac_f32_16x16x64_bf16 a[196:199], a[204:207], v[20:27], v77\n"
"Qmvva 208,32,144\nQe 0\nQmvva 208,24,148\nQe 1\nQmvva 212,32,152\nQe 2\nQmvva 212,24,156\nQe 3\n"
"Qmvva 216,32,160\nQk 28,0,1\nQk 29,2,3\nQmvva 216,24,164\nQe 4\nQmvva 220,32,168\nQe 5\nQmvva 220,24,172\n"
"s_waitcnt lgkmcnt(0)\nQk 30,4,5\nQcvaa 0,112,0,56\nQe 6\nQmvaa 0,116,4\nQe 7\nQmvaa 0,120,8\nQk 31,6,7\n"
"Qmvaa 0,124,12\nQe 8\nQcvaa 4,140,12,56\nQe 9\nQmvaa 4,136,8\nQe 10\nQmvaa 4,132,4\nQe 11\n"
"Qmvaa 4,128,0\nQe 12\nQmvva 240,36,144\nQe 13\nQmvva 240,28,148\nQe 14\nQmvva 244,36,152\nQe 15\n"
"Qmvva 244,28,156\nQk 34,8,9\nQk 35,10,11\nQmvva 248,36,160\nQk 32,12,13\nQk 33,14,15\nQmvva 248,28,164\nQe 0\n"
"Qmvva 252,36,168\nQe 1\nQmvva 252,28,172\nQe 2\nQmava 236,40,168\nQta 168,70,33920\nQta 170,70,34944\nQmava 236,32,172\n"
"Qe 3\nQmava 232,40,160\nQta 172,71,896\nQta 174,71,1920\nQmava 232,32,164\nQk 36,0,1\nQk 37,2,3\nQmava 228,40,152\n"
"Qta 160,70,33664\nQta 162,70,34688\nQmava 228,32,156\nQe 4\nQmava 224,40,144\nQta 164,71,640\nQta 166,71,1664\nQmava 224,32,148\n"
"Qe 5\nv_smfmac_f32_16x16x64_bf16 a[200:203], a[204:207], v[28:35], v77\nQta 152,70,33408\nQta 154,70,34432\nQcvaa 8,112,16,56\nQe 6\nQmvaa 8,116,20\nQta 156,71,384\n"
"Qta 158,71,1408\nQmvaa 8,120,24\nQe 7\nQmvaa 8,124,28\nQta 144,70,33152\nQta 146,70,34176\nQcvaa 12,140,28,56\nQk 38,4,5\n"
"Qk 39,6,7\nQmvaa 12,136,24\nQta 148,71,128\nQta 150,71,1152\nQmvaa 12,132,20\nQmvaa 12,128,16\ns_lshl_b32 s66, s67, 3\n.Lloop:\n"
"s_bitcmp1_b32 s99, s66\ns_cbranch_scc1 .Lhook\n.Lhookret:\nQcvaa 0,128,32,56\nv_add_u32 v75, 1, v75\nQmvaa 0,132,36\ns_waitcnt vmcnt(8)\nds_write_b32 v73, v75\n"
"Qmvaa 0,136,40\nQe 8\nQmvaa 0,140,44\nQe 9\nQcvaa 4,124,44,56\nQe 10\nQmvaa 4,120,40\nQe 11\n"
"Qmvaa 4,116,36\nQe 12\nQmvaa 4,112,32\ns_waitcnt lgkmcnt(1)\nQk 40,8,9\nQmvva 96,44,144\nQe 13\nQmvva 96,36,148\n"
"Qe 14\nQmvva 100,44,152\nQe 15\nQmvva 100,36,156\nQk 41,10,11\nQk 42,12,13\nQmvva 104,44,160\nQk 43,14,15\n"
"Qmvva 104,36,164\nQe 0\nQmvva 108,44,168\nQe 1\nQmvva 108,36,172\nQe 2\nQmvva 140,48,168\nQe 3\n"
"Qmvva 140,40,172\nQk 46,0,1\nQk 47,2,3\nQmvva 136,48,160\nds_read_b32 v66, v74\nQmvva 136,40,164\nQe 4\nQmvva 132,48,152\n"
"Qe 5\nQmvva 132,40,156\nQe 6\nQmvva 128,48,144\nQe 7\nQmvva 128,40,148\ns_waitcnt lgkmcnt(0)\nv_cmp_gt_u32 vcc, v75, v66\n"
"v_smfmac_f32_16x16x64_bf16 a[176:179], a[204:207], v[36:43], v77\ns_cbranch_vccnz .Lsa2\n.Lra2:\nQcvaa 8,112,48,56\ns_add_u32 m0, s61, 33024\nQk 44,4,5\nQmvaa 8,116,52\nbuffer_load_dwordx4 v69, s[40:43], s60 offen nt lds\n"
"Qmvaa 8,120,56\nQk 45,6,7\nQmvaa 8,124,60\ns_add_u32 s65, s65, 2048\nQcvaa 12,140,60,56\nQmvaa 12,136,56\nQmvaa 12,132,52\nbuffer_load_dwordx4 v69, s[40:43], s60 offen offset:1024 nt lds\n"
"Qmvaa 12,128,48\nQcvaa 0,128,64,56\ns_memrealtime s[74:75]\nQmvaa 0,132,68\nQe 8\nQmvaa 0,136,72\nQe 9\nQmvaa 0,140,76\n"
"buffer_load_dwordx4 v69, s[40:43], s90 offen offset:2048 nt lds\nQcvaa 4,124,76,56\nQe 10\nQmvaa 4,120,72\nQe 11\nQmvaa 4,116,68\nQe 12\nQmvaa 4,112,64\n"
"Qe 13\nQmvva 160,52,144\nbuffer_load_dwordx4 v69, s[40:43], s90 offen offset:3072 nt lds\nQmvva 160,44,148\nQe 14\nQmvva 164,52,152\nQe 15\nQmvva 164,44,156\n"
"Qk 48,8,9\nQk 49,10,11\nQmvva 168,52,160\ns_add_u32 m0, s64, 33024\nQk 50,12,13\nQmvva 168,44,164\nbuffer_load_dwordx4 v72, s[44:47], s60 offen nt lds\nQmvva 172,52,168\n"
"Qk 51,14,15\nQmvva 172,44,172\nQe 0\nQmvva 204,20,168\nQe 1\nQmvva 204,48,172\nQe 2\nQmvva 200,20,160\n"
"buffer_load_dwordx4 v72, s[44:47], s60 offen offset:1024 nt lds\nQmvva 200,48,164\nQe 3\nQmvva 196,20,152\nQk 18,0,1\nQk 19,2,3\nQmvva 196,48,156\nQe 4\n"
"Qmvva 192,20,144\nQe 5\nQmvva 192,48,148\nbuffer_load_dwordx4 v72, s[44:47], s90 offen offset:2048 nt lds\nv_smfmac_f32_16x16x64_bf16 a[180:183], a[204:207], v[44:51], v77\nQk 16,4,5\nQcvaa 8,112,80,56\nQe 6\n"
"Qmvaa 8,116,84\nQe 7\nQmvaa 8,120,88\nQk 17,6,7\nQmvaa 8,124,92\nbuffer_load_dwordx4 v72, s[44:47], s90 offen offset:3072 nt lds\nQcvaa 12,140,92,56\ns_waitcnt lgkmcnt(0)\n"
"Qmvaa 12,136,88\ns_cmp_gt_u32 s77, s74\ns_cbranch_scc1 .Lsp2\n.Lrp2:\nQmvaa 12,132,84\ns_add_u32 s77, s74, 220\nQmvaa 12,128,80\ns_xor_b32 s60, s65, %1\n"
"s_add_u32 s90, s60, 260096\nQcvaa 0,128,96,56\nQda 128,68,34048\ns_add_u32 s79, s79, s31\nQmvaa 0,132,100\nQda 132,68,34560\ns_max_u32 s77, s77, s79\nQmvaa 0,136,104\n"
"Qda 136,68,34304\nQmvaa 0,140,108\nQda 140,68,34816\nQcvaa 4,124,108,56\nQda 124,68,33792\nQmvaa 4,120,104\nQda 120,68,33280\nQmvaa 4,116,100\n"
"Qda 116,68,33536\nQmvaa 4,112,96\nQda 112,68,33024\nQmvva 224,24,144\nQe 8\nQmvva 224,16,148\nQe 9\nQmvva 228,24,152\n"
"Qe 10\nQmvva 228,16,156\nQe 11\nQmvva 232,24,160\nQk 20,8,9\nQk 21,10,11\nQmvva 232,16,164\nQe 12\n"
"Qmvva 236,24,168\nQe 13\nQmvva 236,16,172\ns_waitcnt lgkmcnt(0)\nQk 22,12,13\nQcvaa 8,112,0,56\nQe 14\nQmvaa 8,116,4\n"
"v_subrev_u32 v68, 66048, v68\nQmvaa 8,120,8\nQe 15\nQmvaa 8,124,12\nQk 23,14,15\nQcvaa 12,140,12,56\nQe 0\nQmvaa 12,136,8\n"
"Qe 1\nQmvaa 12,132,4\nQe 2\nQmvaa 12,128,0\nQe 3\nv_smfmac_f32_16x16x64_bf16 a[184:187], a[204:207], v[16:23], v77\nQk 26,0,1\nQmava 208,28,144\n"
"Qe 4\nQmava 208,20,148\nQe 5\nQmava 212,28,152\nQe 6\nQmava 212,20,156\nQe 7\nQmava 216,28,160\n"
"Qk 27,2,3\nQk 24,4,5\nQmava 216,20,164\nQk 25,6,7\nQmava 220,28,168\nQe 8\nQmava 220,20,172\nQe 9\n"
"Qmava 252,32,168\nQta 168,71,768\nQta 170,71,1792\nQmava 252,24,172\nQe 10\nQmava 248,32,160\nQta 172,71,33792\nQta 174,71,34816\n"
"Qmava 248,24,164\nQe 11\nQmava 244,32,152\nQta 160,71,512\nQta 162,71,1536\nQmava 244,24,156\nQk 28,8,9\nQk 29,10,11\n"
"Qmava 240,32,144\nQta 164,71,33536\nQta 166,71,34560\nQmava 240,24,148\nQe 12\nQcvaa 0,112,16,56\nQta 152,71,256\nQta 154,71,1280\n"
"Qmvaa 0,116,20\nQe 13\nQmvaa 0,120,24\nQta 156,71,33280\nQta 158,71,34304\nQmvaa 0,124,28\nQe 14\nQcvaa 4,140,28,56\n"
"Qta 144,71,0\nQta 146,71,1024\nQmvaa 4,136,24\nQe 15\nQmvaa 4,132,20\nQta 148,71,33024\nQta 150,71,34048\nQmvaa 4,128,16\n"
"Qk 30,12,13\nQk 31,14,15\nQcvaa 8,128,32,56\nv_add_u32 v75, 1, v75\nQmvaa 8,132,36\ns_waitcnt vmcnt(8)\nds_write_b32 v73, v75\nQmvaa 8,136,40\n"
"Qe 0\nQmvaa 8,140,44\nQe 1\nQcvaa 12,124,44,56\nQe 2\nQmvaa 12,120,40\n.Lpg6:\ns_cbranch_execz .Ltr9\n"
"Qe 3\nQmvaa 12,116,36\nQe 4\nQmvaa 12,112,32\ns_waitcnt lgkmcnt(1)\nQk 32,0,1\nv_smfmac_f32_16x16x64_bf16 a[188:191], a[204:207], v[24:31], v77\nQk 33,2,3\n"
"Qmvva 80,36,144\nQe 5\nQmvva 80,28,148\nQe 6\nQmvva 84,36,152\nQe 7\nQmvva 84,28,156\nQk 34,4,5\n"
"Qk 35,6,7\nQmvva 88,36,160\nQe 8\nQmvva 88,28,164\nQe 9\nQmvva 92,36,168\nQe 10\nQmvva 92,28,172\n"
"Qe 11\nQmvva 124,40,168\nQk 38,8,9\nQk 39,10,11\nQmvva 124,32,172\nds_read_b32 v66, v74\nQmvva 120,40,160\nQe 12\n"
"Qmvva 120,32,164\nQe 13\nQmvva 116,40,152\nQe 14\nQmvva 116,32,156\nQe 15\nQmvva 112,40,144\ns_waitcnt lgkmcnt(0)\n"
"v_cmp_gt_u32 vcc, v75, v66\nQmvva 112,32,148\ns_cbranch_vccnz .Lsa3\n.Lra3:\nQk 36,12,13\nQcvaa 0,112,48,56\ns_add_u32 m0, s61, 66048\nQk 37,14,15\n"
"Qmvaa 0,116,52\nbuffer_load_dwordx4 v69, s[40:43], s60 offen nt lds\nQmvaa 0,120,56\nQmvaa 0,124,60\ns_add_u32 s65, s65, 2048\nQcvaa 4,140,60,56\nQmvaa 4,136,56\nQmvaa 4,132,52\n"
"buffer_load_dwordx4 v69, s[40:43], s60 offen offset:1024 nt lds\nQmvaa 4,128,48\nQcvaa 8,128,64,56\ns_nop 0\nQmvaa 8,132,68\nQe 0\nQmvaa 8,136,72\nQe 1\n"
"Qmvaa 8,140,76\nbuffer_load_dwordx4 v69, s[40:43], s90 offen offset:2048 nt lds\nQcvaa 12,124,76,56\nQe 2\nQmvaa 12,120,72\nQe 3\nQmvaa 12,116,68\nQe 4\n"
"Qmvaa 12,112,64\nQe 5\nv_smfmac_f32_16x16x64_bf16 a[192:195], a[204:207], v[32:39], v77\nQk 40,0,1\nQmvva 144,44,144\nbuffer_load_dwordx4 v69, s[40:43], s90 offen offset:3072 nt lds\nQmvva 144,36,148\nQe 6\n"
"Qmvva 148,44,152\nQe 7\nQmvva 148,36,156\ns_add_u32 m0, s64, 66048\nQk 41,2,3\nQmvva 152,44,160\nbuffer_load_dwordx4 v72, s[44:47], s60 offen nt lds\nQmvva 152,36,164\n"
"Qk 42,4,5\nQk 43,6,7\nQmvva 156,44,168\nQe 8\nQmvva 156,36,172\nQe 9\nQmvva 188,48,168\nQe 10\n"
"Qmvva 188,40,172\nbuffer_load_dwordx4 v72, s[44:47], s60 offen offset:1024 nt lds\nQmvva 184,48,160\nQe 11\nQmvva 184,40,164\nQk 46,8,9\nQk 47,10,11\nQmvva 180,48,152\n"
"Qe 12\nQmvva 180,40,156\nQe 13\nQmvva 176,48,144\nbuffer_load_dwordx4 v72, s[44:47], s90 offen offset:2048 nt lds\nQmvva 176,40,148\nQe 14\nQcvaa 0,112,80,56\n"
"Qe 15\nQmvaa 0,116,84\nQk 44,12,13\nQk 45,14,15\nQmvaa 0,120,88\nQmvaa 0,124,92\nbuffer_load_dwordx4 v72, s[44:47], s90 offen offset:3072 nt lds\nQcvaa 4,140,92,56\n"
"Qmvaa 4,136,88\nQmvaa 4,132,84\nQmvaa 4,128,80\ns_xor_b32 s60, s65, %1\ns_add_u32 s90, s60, 260096\nQcvaa 8,128,96,56\nQda 128,68,1024\nQmvaa 8,132,100\n"
"Qda 132,68,1536\nQmvaa 8,136,104\nQda 136,68,1280\nQmvaa 8,140,108\nQda 140,68,1792\nQcvaa 12,124,108,56\nQda 124,68,768\nQmvaa 12,120,104\n"
"Qda 120,68,256\nQmvaa 12,116,100\nQda 116,68,512\nQmvaa 12,112,96\nQda 112,68,0\nv_smfmac_f32_16x16x64_bf16 a[196:199], a[204:207], v[40:47], v77\nQmvva 208,16,144\nQe 0\n"
"Qmvva 208,44,148\nQe 1\nQmvva 212,16,152\nQe 2\nQmvva 212,44,156\nQe 3\nQmvva 216,16,160\nQk 48,0,1\n"
"Qk 49,2,3\nQmvva 216,44,164\nQe 4\nQmvva 220,16,168\nQe 5\nQmvva 220,44,172\ns_waitcnt lgkmcnt(0)\nQk 50,4,5\n"
"Qcvaa 0,112,0,56\nQe 6\nQmvaa 0,116,4\nQe 7\nQmvaa 0,120,8\nQk 51,6,7\nQmvaa 0,124,12\nQe 8\n"
"Qcvaa 4,140,12,56\nQe 9\nQmvaa 4,136,8\nQe 10\nQmvaa 4,132,4\nQe 11\nQmvaa 4,128,0\nQe 12\n"
"Qmvva 240,20,144\nQe 13\nQmvva 240,48,148\nQe 14\nQmvva 244,20,152\nQe 15\nQmvva 244,48,156\nQk 54,8,9\n"
"Qk 55,10,11\nQmvva 248,20,160\nQk 52,12,13\nQk 53,14,15\nQmvva 248,48,164\nQe 0\nQmvva 252,20,168\nQe 1\n"
"Qmvva 252,48,172\nQe 2\nQmava 236,24,168\nQta 168,71,33920\nQta 170,71,34944\nQmava 236,52,172\nQe 3\nQmava 232,24,160\n"
"Qta 172,70,896\nQta 174,70,1920\nQmava 232,52,164\nQk 16,0,1\nQk 17,2,3\nQmava 228,24,152\nQta 160,71,33664\nQta 162,71,34688\n"
"Qmava 228,52,156\nQe 4\nQmava 224,24,144\nQta 164,70,640\nQta 166,70,1664\nQmava 224,52,148\nQe 5\nv_smfmac_f32_16x16x64_bf16 a[200:203], a[204:207], v[48:55], v77\n"
"Qta 152,71,33408\nQta 154,71,34432\nQcvaa 8,112,16,56\nQe 6\nQmvaa 8,116,20\nQta 156,70,384\nQta 158,70,1408\nQmvaa 8,120,24\n"
"Qe 7\nQmvaa 8,124,28\nQta 144,71,33152\nQta 146,71,34176\nQcvaa 12,140,28,56\nQk 18,4,5\nQk 19,6,7\nQmvaa 12,136,24\n"
"Qta 148,70,128\nQta 150,70,1152\nQmvaa 12,132,20\nQmvaa 12,128,16\nQcvaa 0,128,32,56\nv_add_u32 v75, 1, v75\nQmvaa 0,132,36\ns_waitcnt vmcnt(8)\n"
"ds_write_b32 v73, v75\nQmvaa 0,136,40\nQe 8\nQmvaa 0,140,44\nQe 9\nQcvaa 4,124,44,56\nQe 10\nQmvaa 4,120,40\n"
"Qe 11\nQmvaa 4,116,36\nQe 12\nQmvaa 4,112,32\ns_waitcnt lgkmcnt(1)\nQk 20,8,9\nQmvva 96,28,144\nQe 13\n"
"Qmvva 96,16,148\nQe 14\nQmvva 100,28,152\nQe 15\nQmvva 100,16,156\nQk 21,10,11\nQk 22,12,13\nQmvva 104,28,160\n"
"Qk 23,14,15\nQmvva 104,16,164\nQe 0\nQmvva 108,28,168\nQe 1\nQmvva 108,16,172\nQe 2\nQmvva 140,32,168\n"
"Qe 3\nQmvva 140,20,172\nQk 26,0,1\nQk 27,2,3\nQmvva 136,32,160\nds_read_b32 v66, v74\nQmvva 136,20,164\nQe 4\n"
"Qmvva 132,32,152\nQe 5\nQmvva 132,20,156\nQe 6\nQmvva 128,32,144\nQe 7\nQmvva 128,20,148\ns_waitcnt lgkmcnt(0)\n"
"v_cmp_gt_u32 vcc, v75, v66\nv_smfmac_f32_16x16x64_bf16 a[176:179], a[204:207], v[16:23], v77\ns_cbranch_vccnz .Lsa4\n.Lra4:\nQcvaa 8,112,48,56\ns_add_u32 m0, s61, 99072\nQk 24,4,5\nQmvaa 8,116,52\n"
"buffer_load_dwordx4 v69, s[40:43], s60 offen nt lds\nQmvaa 8,120,56\nQk 25,6,7\nQmvaa 8,124,60\ns_or_b32 s65, s65, 0x1d8000\ns_add_u32 s65, s65, 2048\nQcvaa 12,140,60,56\ns_andn2_b32 s65, s65, 0x1d8000\n"
"Qmvaa 12,136,56\nQmvaa 12,132,52\nbuffer_load_dwordx4 v69, s[40:43], s60 offen offset:1024 nt lds\nQmvaa 12,128,48\nQcvaa 0,128,64,56\ns_memrealtime s[74:75]\nQmvaa 0,132,68\nQe 8\n"
"Qmvaa 0,136,72\nQe 9\nQmvaa 0,140,76\nbuffer_load_dwordx4 v69, s[40:43], s90 offen offset:2048 nt lds\nQcvaa 4,124,76,56\nQe 10\nQmvaa 4,120,72\nQe 11\n"
"Qmvaa 4,116,68\nQe 12\nQmvaa 4,112,64\nQe 13\nQmvva 160,36,144\nbuffer_load_dwordx4 v69, s[40:43], s90 offen offset:3072 nt lds\nQmvva 160,24,148\nQe 14\n"
"Qmvva 164,36,152\nQe 15\nQmvva 164,24,156\nQk 28,8,9\nQk 29,10,11\nQmvva 168,36,160\ns_add_u32 m0, s64, 99072\nQk 30,12,13\n"
"Qmvva 168,24,164\nbuffer_load_dwordx4 v72, s[44:47], s60 offen nt lds\nQmvva 172,36,168\nQk 31,14,15\nQmvva 172,24,172\nQe 0\nQmvva 204,40,168\nQe 1\n"
"Qmvva 204,28,172\nQe 2\nQmvva 200,40,160\nbuffer_load_dwordx4 v72, s[44:47], s60 offen offset:1024 nt lds\nQmvva 200,28,164\nQe 3\nQmvva 196,40,152\nQk 34,0,1\n"
"Qk 35,2,3\nQmvva 196,28,156\nQe 4\nQmvva 192,40,144\nQe 5\nQmvva 192,28,148\nbuffer_load_dwordx4 v72, s[44:47], s90 offen offset:2048 nt lds\nv_smfmac_f32_16x16x64_bf16 a[180:183], a[204:207], v[24:31], v77\n"
"Qk 32,4,5\nQcvaa 8,112,80,56\nQe 6\nQmvaa 8,116,84\nQe 7\nQmvaa 8,120,88\nQk 33,6,7\nQmvaa 8,124,92\n"
"buffer_load_dwordx4 v72, s[44:47], s90 offen offset:3072 nt lds\nQcvaa 12,140,92,56\ns_waitcnt lgkmcnt(0)\nQmvaa 12,136,88\ns_cmp_gt_u32 s77, s74\ns_cbranch_scc1 .Lsp4\n.Lrp4:\nQmvaa 12,132,84\n"
"s_add_u32 s77, s74, 220\nQmvaa 12,128,80\ns_xor_b32 s60, s65, %1\ns_add_u32 s90, s60, 260096\nQcvaa 0,128,96,56\nQda 128,68,34048\ns_add_u32 s79, s79, s31\nQmvaa 0,132,100\n"
"Qda 132,68,34560\ns_max_u32 s77, s77, s79\nQmvaa 0,136,104\nQda 136,68,34304\nQmvaa 0,140,108\nQda 140,68,34816\nQcvaa 4,124,108,56\nQda 124,68,33792\n"
"Qmvaa 4,120,104\nQda 120,68,33280\nQmvaa 4,116,100\nQda 116,68,33536\nQmvaa 4,112,96\nQda 112,68,33024\nQmvva 224,44,144\nQe 8\n"
"Qmvva 224,32,148\nQe 9\nQmvva 228,44,152\nQe 10\nQmvva 228,32,156\nQe 11\nQmvva 232,44,160\nQk 36,8,9\n"
"Qk 37,10,11\nQmvva 232,32,164\nQe 12\nQmvva 236,44,168\nQe 13\nQmvva 236,32,172\ns_waitcnt lgkmcnt(0)\nQk 38,12,13\n"
"Qcvaa 8,112,0,56\nQe 14\nQmvaa 8,116,4\nv_add_u32 v68, 66048, v68\nQmvaa 8,120,8\nQe 15\nQmvaa 8,124,12\nQk 39,14,15\n"
"Qcvaa 12,140,12,56\nQe 0\nQmvaa 12,136,8\nQe 1\nQmvaa 12,132,4\nQe 2\nQmvaa 12,128,0\nQe 3\n"
"v_smfmac_f32_16x16x64_bf16 a[184:187], a[204:207], v[32:39], v77\nQk 42,0,1\nQmava 208,48,144\nQe 4\nQmava 208,36,148\nQe 5\nQmava 212,48,152\nQe 6\n"
"Qmava 212,36,156\nQe 7\nQmava 216,48,160\nQk 43,2,3\nQk 40,4,5\nQmava 216,36,164\nQk 41,6,7\nQmava 220,48,168\n"
"Qe 8\nQmava 220,36,172\nQe 9\nQmava 252,52,168\nQta 168,70,768\nQta 170,70,1792\nQmava 252,40,172\nQe 10\n"
"Qmava 248,52,160\nQta 172,70,33792\nQta 174,70,34816\nQmava 248,40,164\nQe 11\nQmava 244,52,152\nQta 160,70,512\nQta 162,70,1536\n"
"Qmava 244,40,156\nQk 44,8,9\nQk 45,10,11\nQmava 240,52,144\nQta 164,70,33536\nQta 166,70,34560\nQmava 240,40,148\nQe 12\n"
"Qcvaa 0,112,16,56\nQta 152,70,256\nQta 154,70,1280\nQmvaa 0,116,20\nQe 13\nQmvaa 0,120,24\nQta 156,70,33280\nQta 158,70,34304\n"
"Qmvaa 0,124,28\nQe 14\nQcvaa 4,140,28,56\nQta 144,70,0\nQta 146,70,1024\nQmvaa 4,136,24\nQe 15\nQmvaa 4,132,20\n"
"Qta 148,70,33024\nQta 150,70,34048\nQmvaa 4,128,16\nQk 46,12,13\nQk 47,14,15\nQcvaa 8,128,32,56\nv_add_u32 v75, 1, v75\nQmvaa 8,132,36\n"
"s_waitcnt vmcnt(8)\nds_write_b32 v73, v75\nQmvaa 8,136,40\nQe 0\nQmvaa 8,140,44\nQe 1\nQcvaa 12,124,44,56\nQe 2\n"
"Qmvaa 12,120,40\nQe 3\nQmvaa 12,116,36\nQe 4\nQmvaa 12,112,32\ns_waitcnt lgkmcnt(1)\nQk 48,0,1\nv_smfmac_f32_16x16x64_bf16 a[188:191], a[204:207], v[40:47], v77\n"
"Qk 49,2,3\nQmvva 80,16,144\nQe 5\nQmvva 80,44,148\nQe 6\nQmvva 84,16,152\nQe 7\nQmvva 84,44,156\n"
"Qk 50,4,5\nQk 51,6,7\nQmvva 88,16,160\nQe 8\nQmvva 88,44,164\nQe 9\nQmvva 92,16,168\nQe 10\n"
"Qmvva 92,44,172\nQe 11\nQmvva 124,20,168\nQk 54,8,9\nQk 55,10,11\nQmvva 124,48,172\nds_read_b32 v66, v74\nQmvva 120,20,160\n"
"Qe 12\nQmvva 120,48,164\nQe 13\nQmvva 116,20,152\nQe 14\nQmvva 116,48,156\nQe 15\nQmvva 112,20,144\n"
"s_waitcnt lgkmcnt(0)\nv_cmp_gt_u32 vcc, v75, v66\nQmvva 112,48,148\ns_cbranch_vccnz .Lsa5\n.Lra5:\nQk 52,12,13\nQcvaa 0,112,48,56\ns_add_u32 m0, s61, 0\n"
"Qk 53,14,15\nQmvaa 0,116,52\nbuffer_load_dwordx4 v69, s[40:43], s60 offen nt lds\nQmvaa 0,120,56\nQmvaa 0,124,60\ns_add_u32 s65, s65, 2048\nQcvaa 4,140,60,56\nQmvaa 4,136,56\n"
"Qmvaa 4,132,52\nbuffer_load_dwordx4 v69, s[40:43], s60 offen offset:1024 nt lds\nQmvaa 4,128,48\nQcvaa 8,128,64,56\ns_nop 0\nQmvaa 8,132,68\nQe 0\nQmvaa 8,136,72\n"
"Qe 1\nQmvaa 8,140,76\nbuffer_load_dwordx4 v69, s[40:43], s90 offen offset:2048 nt lds\nQcvaa 12,124,76,56\nQe 2\nQmvaa 12,120,72\nQe 3\nQmvaa 12,116,68\n"
"Qe 4\nQmvaa 12,112,64\nQe 5\nv_smfmac_f32_16x16x64_bf16 a[192:195], a[204:207], v[48:55], v77\nQk 20,0,1\nQmvva 144,24,144\nbuffer_load_dwordx4 v69, s[40:43], s90 offen offset:3072 nt lds\nQmvva 144,52,148\n"
"Qe 6\nQmvva 148,24,152\nQe 7\nQmvva 148,52,156\ns_add_u32 m0, s64, 0\nQk 21,2,3\nQmvva 152,24,160\nbuffer_load_dwordx4 v72, s[44:47], s60 offen nt lds\n"
"Qmvva 152,52,164\nQk 22,4,5\nQk 23,6,7\nQmvva 156,24,168\n.Lpg7:\ns_cbranch_execz .Ltr9\nQe 8\nQmvva 156,52,172\n"
"Qe 9\nQmvva 188,28,168\nQe 10\nQmvva 188,20,172\nbuffer_load_dwordx4 v72, s[44:47], s60 offen offset:1024 nt lds\nQmvva 184,28,160\nQe 11\nQmvva 184,20,164\n"
"Qk 26,8,9\nQk 27,10,11\nQmvva 180,28,152\nQe 12\nQmvva 180,20,156\nQe 13\nQmvva 176,28,144\nbuffer_load_dwordx4 v72, s[44:47], s90 offen offset:2048 nt lds\n"
"Qmvva 176,20,148\nQe 14\nQcvaa 0,112,80,56\nQe 15\nQmvaa 0,116,84\nQk 24,12,13\nQk 25,14,15\nQmvaa 0,120,88\n"
"Qmvaa 0,124,92\nbuffer_load_dwordx4 v72, s[44:47], s90 offen offset:3072 nt lds\nQcvaa 4,140,92,56\nQmvaa 4,136,88\nQmvaa 4,132,84\nQmvaa 4,128,80\ns_xor_b32 s60, s65, %1\ns_add_u32 s90, s60, 260096\n"
"Qcvaa 8,128,96,56\nQda 128,68,1024\nQmvaa 8,132,100\nQda 132,68,1536\nQmvaa 8,136,104\nQda 136,68,1280\nQmvaa 8,140,108\nQda 140,68,1792\n"
"Qcvaa 12,124,108,56\nQda 124,68,768\nQmvaa 12,120,104\nQda 120,68,256\nQmvaa 12,116,100\nQda 116,68,512\nQmvaa 12,112,96\nQda 112,68,0\n"
"v_smfmac_f32_16x16x64_bf16 a[196:199], a[204:207], v[20:27], v77\nQmvva 208,32,144\nQe 0\nQmvva 208,24,148\nQe 1\nQmvva 212,32,152\nQe 2\nQmvva 212,24,156\n"
"Qe 3\nQmvva 216,32,160\nQk 28,0,1\nQk 29,2,3\nQmvva 216,24,164\nQe 4\nQmvva 220,32,168\nQe 5\n"
"Qmvva 220,24,172\ns_waitcnt lgkmcnt(0)\nQk 30,4,5\nQcvaa 0,112,0,56\nQe 6\nQmvaa 0,116,4\nQe 7\nQmvaa 0,120,8\n"
"Qk 31,6,7\nQmvaa 0,124,12\nQe 8\nQcvaa 4,140,12,56\nQe 9\nQmvaa 4,136,8\nQe 10\nQmvaa 4,132,4\n"
"Qe 11\nQmvaa 4,128,0\nQe 12\nQmvva 240,36,144\nQe 13\nQmvva 240,28,148\nQe 14\nQmvva 244,36,152\n"
"Qe 15\nQmvva 244,28,156\nQk 34,8,9\nQk 35,10,11\nQmvva 248,36,160\nQk 32,12,13\nQk 33,14,15\nQmvva 248,28,164\n"
"Qe 0\nQmvva 252,36,168\nQe 1\nQmvva 252,28,172\nQe 2\nQmava 236,40,168\nQta 168,70,33920\nQta 170,70,34944\n"
"Qmava 236,32,172\nQe 3\nQmava 232,40,160\nQta 172,71,896\nQta 174,71,1920\nQmava 232,32,164\nQk 36,0,1\nQk 37,2,3\n"
"Qmava 228,40,152\nQta 160,70,33664\nQta 162,70,34688\nQmava 228,32,156\nQe 4\nQmava 224,40,144\nQta 164,71,640\nQta 166,71,1664\n"
"Qmava 224,32,148\nQe 5\nv_smfmac_f32_16x16x64_bf16 a[200:203], a[204:207], v[28:35], v77\nQta 152,70,33408\nQta 154,70,34432\nQcvaa 8,112,16,56\nQe 6\nQmvaa 8,116,20\n"
"Qta 156,71,384\nQta 158,71,1408\nQmvaa 8,120,24\nQe 7\nQmvaa 8,124,28\nQta 144,70,33152\nQta 146,70,34176\nQcvaa 12,140,28,56\n"
"Qk 38,4,5\nQk 39,6,7\nQmvaa 12,136,24\nQta 148,71,128\nQta 150,71,1152\nQmvaa 12,132,20\nQmvaa 12,128,16\ns_add_u32 s66, s66, 1\n"
"s_bitcmp1_b32 0x7f7f, s66\ns_cbranch_scc1 .Lloop\ns_cmp_lg_u32 s67, 0\ns_cbranch_scc1 .Ltail1\nQcvaa 0,128,32,56\nv_add_u32 v75, 1, v75\nQmvaa 0,132,36\ns_waitcnt vmcnt(8)\n"
"ds_write_b32 v73, v75\nQmvaa 0,136,40\nQe 8\nQmvaa 0,140,44\nQe 9\nQcvaa 4,124,44,56\nQe 10\nQmvaa 4,120,40\n"
"Qe 11\nQmvaa 4,116,36\nQe 12\nQmvaa 4,112,32\ns_waitcnt lgkmcnt(1)\nQk 40,8,9\nQmvva 96,44,144\nQe 13\n"
"Qmvva 96,36,148\nQe 14\nQmvva 100,44,152\nQe 15\nQmvva 100,36,156\nQk 41,10,11\nQk 42,12,13\nQmvva 104,44,160\n"
"Qk 43,14,15\nQmvva 104,36,164\nQe 0\nQmvva 108,44,168\nQe 1\nQmvva 108,36,172\nQe 2\nQmvva 140,48,168\n"
"Qe 3\nQmvva 140,40,172\nQk 46,0,1\nQk 47,2,3\nQmvva 136,48,160\nds_read_b32 v66, v74\nQmvva 136,40,164\nQe 4\n"
"Qmvva 132,48,152\nQe 5\nQmvva 132,40,156\nQe 6\nQmvva 128,48,144\nQe 7\nQmvva 128,40,148\ns_waitcnt lgkmcnt(0)\n"
"v_cmp_gt_u32 vcc, v75, v66\nv_smfmac_f32_16x16x64_bf16 a[176:179], a[204:207], v[36:43], v77\ns_cbranch_vccnz .Lsa30\n.Lra30:\nQcvaa 8,112,48,56\ns_add_u32 m0, s62, 0\ns_add_u32 s69, s87, 0\nQk 44,4,5\n"
"Qmvaa 8,116,52\nbuffer_load_dwordx4 v62, s[52:55], s69 offen lds\nQmvaa 8,120,56\nQk 45,6,7\nQmvaa 8,124,60\nQcvaa 12,140,60,56\nQmvaa 12,136,56\ns_add_u32 s69, s87, 64504\n"
"Qmvaa 12,132,52\nbuffer_load_dwordx4 v62, s[52:55], s69 offen offset:1032 lds\nQmvaa 12,128,48\nQcvaa 0,128,64,56\nQmvaa 0,132,68\nQe 8\nQmvaa 0,136,72\ns_add_u32 s69, s87, 129008\n"
"Qe 9\nQmvaa 0,140,76\nbuffer_load_dwordx4 v62, s[52:55], s69 offen offset:2064 lds\nQcvaa 4,124,76,56\nQe 10\nQmvaa 4,120,72\nQe 11\nQmvaa 4,116,68\n"
"Qe 12\nQmvaa 4,112,64\ns_add_u32 s69, s87, 193512\nQe 13\nQmvva 160,52,144\nbuffer_load_dwordx4 v62, s[52:55], s69 offen offset:3096 lds\nQmvva 160,44,148\nQe 14\n"
"Qmvva 164,52,152\nQe 15\nQmvva 164,44,156\nQk 48,8,9\nQk 49,10,11\nQmvva 168,52,160\ns_add_u32 m0, s62, 16512\ns_add_u32 s69, s87, 128\n"
"Qk 50,12,13\nQmvva 168,44,164\nbuffer_load_dwordx4 v62, s[52:55], s69 offen lds\nQmvva 172,52,168\nQk 51,14,15\nQmvva 172,44,172\nQe 0\nQmvva 204,20,168\n"
"Qe 1\nQmvva 204,48,172\ns_add_u32 s69, s87, 64632\nQe 2\nQmvva 200,20,160\nbuffer_load_dwordx4 v62, s[52:55], s69 offen offset:1032 lds\nQmvva 200,48,164\nQe 3\n"
"Qmvva 196,20,152\nQk 18,0,1\nQk 19,2,3\nQmvva 196,48,156\nQe 4\nQmvva 192,20,144\ns_add_u32 s69, s87, 129136\nQe 5\n"
"Qmvva 192,48,148\nbuffer_load_dwordx4 v62, s[52:55], s69 offen offset:2064 lds\nv_smfmac_f32_16x16x64_bf16 a[180:183], a[204:207], v[44:51], v77\nQk 16,4,5\nQcvaa 8,112,80,56\nQe 6\nQmvaa 8,116,84\nQe 7\n"
"Qmvaa 8,120,88\ns_add_u32 s69, s87, 193640\nQk 17,6,7\nQmvaa 8,124,92\nbuffer_load_dwordx4 v62, s[52:55], s69 offen offset:3096 lds\nQcvaa 12,140,92,56\nQmvaa 12,136,88\nQmvaa 12,132,84\n"
"Qmvaa 12,128,80\nQcvaa 0,128,96,56\nQda 128,68,34048\nQmvaa 0,132,100\nQda 132,68,34560\nQmvaa 0,136,104\nQda 136,68,34304\nQmvaa 0,140,108\n"
"Qda 140,68,34816\nQcvaa 4,124,108,56\nQda 124,68,33792\nQmvaa 4,120,104\nQda 120,68,33280\nQmvaa 4,116,100\nQda 116,68,33536\nQmvaa 4,112,96\n"
"Qda 112,68,33024\nQmvva 224,24,144\nQe 8\nQmvva 224,16,148\ns_add_u32 m0, s63, 33024\ns_add_u32 s88, s86, 0\nQe 9\nQmvva 228,24,152\n"
"buffer_load_dwordx4 v64, s[48:51], s88 offen lds\nQe 10\nQmvva 228,16,156\nQe 11\nQmvva 232,24,160\nQk 20,8,9\nQk 21,10,11\nQmvva 232,16,164\n"
"s_add_u32 s88, s86, 19456\nQe 12\nQmvva 236,24,168\nbuffer_load_dwordx4 v64, s[48:51], s88 offen offset:1024 lds\nQe 13\nQmvva 236,16,172\ns_waitcnt lgkmcnt(0)\nQk 22,12,13\n"
"Qcvaa 8,112,0,56\nQe 14\nQmvaa 8,116,4\nv_subrev_u32 v68, 66048, v68\ns_add_u32 s88, s86, 38912\nQmvaa 8,120,8\nbuffer_load_dwordx4 v64, s[48:51], s88 offen offset:2048 lds\nQe 15\n"
"Qmvaa 8,124,12\nQk 23,14,15\nQcvaa 12,140,12,56\nQe 0\nQmvaa 12,136,8\ns_add_u32 s88, s86, 58368\nQe 1\nQmvaa 12,132,4\n"
"buffer_load_dwordx4 v64, s[48:51], s88 offen offset:3072 lds\nQe 2\nQmvaa 12,128,0\nQe 3\nv_smfmac_f32_16x16x64_bf16 a[184:187], a[204:207], v[16:23], v77\nQk 26,0,1\nQmava 208,28,144\ns_add_u32 m0, s63, 37120\n"
"s_add_u32 s88, s86, 81920\nQe 4\nQmava 208,20,148\nbuffer_load_dwordx4 v64, s[48:51], s88 offen lds\nQe 5\nQmava 212,28,152\nQe 6\nQmava 212,20,156\n"
"Qe 7\nQmava 216,28,160\ns_add_u32 s88, s86, 101376\nQk 27,2,3\nQk 24,4,5\nQmava 216,20,164\nbuffer_load_dwordx4 v64, s[48:51], s88 offen offset:1024 lds\nQk 25,6,7\n"
"Qmava 220,28,168\nQe 8\nQmava 220,20,172\nQe 9\nQmava 252,32,168\nQta 168,71,768\nQta 170,71,1792\ns_add_u32 s88, s86, 120832\n"
"Qmava 252,24,172\nbuffer_load_dwordx4 v64, s[48:51], s88 offen offset:2048 lds\nQe 10\nQmava 248,32,160\nQta 172,71,33792\nQta 174,71,34816\nQmava 248,24,164\nQe 11\n"
"Qmava 244,32,152\nQta 160,71,512\nQta 162,71,1536\nQmava 244,24,156\nQk 28,8,9\nQk 29,10,11\nQmava 240,32,144\nQta 164,71,33536\n"
"Qta 166,71,34560\nQmava 240,24,148\nQe 12\nQcvaa 0,112,16,56\nQta 152,71,256\nQta 154,71,1280\nQmvaa 0,116,20\nQe 13\n"
"Qmvaa 0,120,24\nQta 156,71,33280\nQta 158,71,34304\nQmvaa 0,124,28\nQe 14\nQcvaa 4,140,28,56\nQta 144,71,0\nQta 146,71,1024\n"
"Qmvaa 4,136,24\nQe 15\nQmvaa 4,132,20\nQta 148,71,33024\nQta 150,71,34048\nQmvaa 4,128,16\nQk 30,12,13\nQk 31,14,15\n"
"Qcvaa 8,128,32,56\nv_add_u32 v75, 1, v75\nQmvaa 8,132,36\nds_write_b32 v73, v75\nQmvaa 8,136,40\nQe 0\nQmvaa 8,140,44\nQe 1\n"
"Qcvaa 12,124,44,56\nQe 2\nQmvaa 12,120,40\nQe 3\nQmvaa 12,116,36\nQe 4\nQmvaa 12,112,32\ns_waitcnt lgkmcnt(1)\n"
"Qk 32,0,1\nv_smfmac_f32_16x16x64_bf16 a[188:191], a[204:207], v[24:31], v77\nQk 33,2,3\nQmvva 80,36,144\nQe 5\nQmvva 80,28,148\nQe 6\nQmvva 84,36,152\n"
"Qe 7\nQmvva 84,28,156\nQk 34,4,5\nQk 35,6,7\nQmvva 88,36,160\nQe 8\nQmvva 88,28,164\nQe 9\n"
"Qmvva 92,36,168\nQe 10\nQmvva 92,28,172\nQe 11\nQmvva 124,40,168\nQk 38,8,9\nQk 39,10,11\nQmvva 124,32,172\n"
"ds_read_b32 v66, v74\nQmvva 120,40,160\nQe 12\nQmvva 120,32,164\nQe 13\nQmvva 116,40,152\nQe 14\nQmvva 116,32,156\n"
"Qe 15\nQmvva 112,40,144\ns_waitcnt lgkmcnt(0)\nv_cmp_gt_u32 vcc, v75, v66\nQmvva 112,32,148\ns_cbranch_vccnz .Lsa31\n.Lra31:\nQk 36,12,13\n"
"Qcvaa 0,112,48,56\ns_add_u32 m0, s62, 33024\ns_add_u32 s69, s87, 524288\nQk 37,14,15\nQmvaa 0,116,52\nbuffer_load_dwordx4 v62, s[52:55], s69 offen lds\nQmvaa 0,120,56\nQmvaa 0,124,60\n"
"Qcvaa 4,140,60,56\nQmvaa 4,136,56\ns_add_u32 s69, s87, 588792\nQmvaa 4,132,52\nbuffer_load_dwordx4 v62, s[52:55], s69 offen offset:1032 lds\nQmvaa 4,128,48\nQcvaa 8,128,64,56\nQmvaa 8,132,68\n"
"Qe 0\nQmvaa 8,136,72\ns_add_u32 s69, s87, 653296\nQe 1\nQmvaa 8,140,76\nbuffer_load_dwordx4 v62, s[52:55], s69 offen offset:2064 lds\nQcvaa 12,124,76,56\nQe 2\n"
"Qmvaa 12,120,72\nQe 3\nQmvaa 12,116,68\nQe 4\nQmvaa 12,112,64\nQe 5\nv_smfmac_f32_16x16x64_bf16 a[192:195], a[204:207], v[32:39], v77\ns_add_u32 s69, s87, 717800\n"
"Qk 40,0,1\nQmvva 144,44,144\nbuffer_load_dwordx4 v62, s[52:55], s69 offen offset:3096 lds\nQmvva 144,36,148\nQe 6\nQmvva 148,44,152\nQe 7\nQmvva 148,36,156\n"
"s_add_u32 m0, s62, 49536\ns_add_u32 s69, s87, 524416\nQk 41,2,3\nQmvva 152,44,160\nbuffer_load_dwordx4 v62, s[52:55], s69 offen lds\nQmvva 152,36,164\nQk 42,4,5\nQk 43,6,7\n"
"Qmvva 156,44,168\nQe 8\nQmvva 156,36,172\nQe 9\nQmvva 188,48,168\ns_add_u32 s69, s87, 588920\nQe 10\nQmvva 188,40,172\n"
"buffer_load_dwordx4 v62, s[52:55], s69 offen offset:1032 lds\nQmvva 184,48,160\nQe 11\nQmvva 184,40,164\nQk 46,8,9\nQk 47,10,11\nQmvva 180,48,152\nQe 12\n"
"Qmvva 180,40,156\ns_add_u32 s69, s87, 653424\nQe 13\nQmvva 176,48,144\nbuffer_load_dwordx4 v62, s[52:55], s69 offen offset:2064 lds\nQmvva 176,40,148\nQe 14\nQcvaa 0,112,80,56\n"
"Qe 15\nQmvaa 0,116,84\nQk 44,12,13\nQk 45,14,15\nQmvaa 0,120,88\ns_add_u32 s69, s87, 717928\nQmvaa 0,124,92\nbuffer_load_dwordx4 v62, s[52:55], s69 offen offset:3096 lds\n"
"Qcvaa 4,140,92,56\nQmvaa 4,136,88\nQmvaa 4,132,84\nQmvaa 4,128,80\nQcvaa 8,128,96,56\nQmvaa 8,132,100\nQmvaa 8,136,104\nQmvaa 8,140,108\n"
"Qcvaa 12,124,108,56\nQmvaa 12,120,104\nQmvaa 12,116,100\nQmvaa 12,112,96\nv_smfmac_f32_16x16x64_bf16 a[196:199], a[204:207], v[40:47], v77\nQmvva 208,16,144\nQta 112,71,33152\nQe 0\n"
"Qmvva 208,44,148\nQta 114,71,34176\nQe 1\nQmvva 212,16,152\nQta 116,71,33408\nQe 2\nQmvva 212,44,156\nQta 118,71,34432\n"
"Qe 3\nQmvva 216,16,160\nQta 120,71,33664\nQk 48,0,1\nQk 49,2,3\nQmvva 216,44,164\nQta 122,71,34688\nQe 4\n"
"Qmvva 220,16,168\n.Lpg8:\ns_cbranch_execz .Ltr10\nQta 124,71,33920\nQe 5\nQmvva 220,44,172\nQta 126,71,34944\nQk 50,4,5\n"
"Qe 6\nQe 7\ns_nop 0\nQk 51,6,7\nQe 8\nQe 9\nQe 10\nQe 11\n"
"Qe 12\nQmvva 240,20,144\nv_add_u32 v75, 1, v75\nQe 13\nQmvva 240,48,148\nds_write_b32 v73, v75\nQe 14\nQmvva 244,20,152\n"
"Qe 15\nQmvva 244,48,156\nQk 54,8,9\nQk 55,10,11\nQmvva 248,20,160\nQk 52,12,13\nQk 53,14,15\nQmvva 248,48,164\n"
"Qmvva 252,20,168\nQmvva 252,48,172\nv_smfmac_f32_16x16x64_bf16 a[200:203], a[204:207], v[48:55], v77\nQmava 224,24,144\nds_read_b32 v66, v74\nv_accvgpr_read_b32 v0, a176\nQmava 224,52,148\nv_accvgpr_read_b32 v1, a177\n"
"v_accvgpr_read_b32 v6, a178\nQmava 228,24,152\nv_accvgpr_read_b32 v7, a179\nv_accvgpr_read_b32 v8, a180\nQmava 228,52,156\nv_accvgpr_read_b32 v9, a181\nv_accvgpr_read_b32 v14, a182\nQmava 232,24,160\n"
"v_accvgpr_read_b32 v15, a183\nv_accvgpr_read_b32 v16, a184\nQmava 232,52,164\ns_waitcnt lgkmcnt(0)\nv_cmp_gt_u32 vcc, v75, v66\nQmava 236,24,168\ns_cbranch_vccnz .Lsa32\n.Lra32:\n"
"v_accvgpr_read_b32 v17, a185\nQmava 236,52,172\ns_waitcnt lgkmcnt(0)\nv_accvgpr_read_b32 v22, a186\nQmvva 96,28,112\ns_add_u32 m0, s63, 0\ns_add_u32 s88, s86, 128\nv_accvgpr_read_b32 v23, a187\n"
"Qmvva 100,28,116\nbuffer_load_dwordx4 v64, s[48:51], s88 offen lds\nQmvva 104,28,120\nv_accvgpr_read_b32 v24, a188\nv_accvgpr_read_b32 v25, a189\nQmvva 108,28,124\nv_accvgpr_read_b32 v2, a190\nv_accvgpr_read_b32 v3, a191\n"
"Qmvva 128,32,112\ns_add_u32 s88, s86, 19584\nv_accvgpr_read_b32 v4, a192\nQmvva 132,32,116\nbuffer_load_dwordx4 v64, s[48:51], s88 offen offset:1024 lds\nQmvva 136,32,120\nv_accvgpr_read_b32 v5, a193\nv_accvgpr_read_b32 v10, a194\n"
"Qmvva 140,32,124\nv_accvgpr_read_b32 v11, a195\nv_accvgpr_read_b32 v12, a196\nQmvva 160,36,112\ns_add_u32 s88, s86, 39040\nv_accvgpr_read_b32 v13, a197\nQmvva 164,36,116\nbuffer_load_dwordx4 v64, s[48:51], s88 offen offset:2048 lds\n"
"Qmvva 168,36,120\nv_accvgpr_read_b32 v18, a198\nv_accvgpr_read_b32 v19, a199\nQmvva 172,36,124\nv_accvgpr_read_b32 v20, a200\nv_accvgpr_read_b32 v21, a201\nQmvva 192,40,112\ns_add_u32 s88, s86, 58496\n"
"v_accvgpr_read_b32 v26, a202\nQmvva 196,40,116\nbuffer_load_dwordx4 v64, s[48:51], s88 offen offset:3072 lds\nQmvva 200,40,120\nv_accvgpr_read_b32 v27, a203\nQa 8,8,9\nQmvva 204,40,124\nQa 10,10,11\n"
"Qa 8,8,10\nQmvva 224,44,112\ns_add_u32 m0, s63, 4096\ns_add_u32 s88, s86, 82048\nQa 16,16,17\nQmvva 228,44,116\nbuffer_load_dwordx4 v64, s[48:51], s88 offen lds\nQmvva 232,44,120\n"
"Qa 18,18,19\nQa 16,16,18\nQmvva 236,44,124\nQa 4,4,5\nQa 6,6,7\nQmava 208,48,112\ns_add_u32 s88, s86, 101504\nQa 4,4,6\n"
"Qmava 212,48,116\nbuffer_load_dwordx4 v64, s[48:51], s88 offen offset:1024 lds\nQmava 216,48,120\nQa 12,12,13\nQa 14,14,15\nQmava 220,48,124\nQa 12,12,14\nQa 0,0,1\n"
"Qmava 240,52,112\ns_add_u32 s88, s86, 120960\nQa 2,2,3\nQmava 244,52,116\nbuffer_load_dwordx4 v64, s[48:51], s88 offen offset:2048 lds\nQmava 248,52,120\nQa 0,0,2\nQa 24,24,25\n"
"Qmava 252,52,124\nQa 26,26,27\nQa 24,24,26\nQa 20,20,21\nQa 22,22,23\nQa 20,20,22\nv_max3_u32 v9, v8, v16, v4\nv_max3_u32 v9, v9, v12, v0\n"
"v_max3_u32 v9, v9, v24, v20\nv_min3_u32 v11, v8, v16, v4\nv_min3_u32 v11, v11, v12, v0\nv_min3_u32 v11, v11, v24, v20\nv_cmp_lt_u32 vcc, 0x4e800000, v9\ns_mov_b64 s[74:75], vcc\nv_cmp_gt_u32 vcc, 0x35800000, v11\ns_or_b64 s[74:75], s[74:75], vcc\n"
"v_mov_b32 v30, s73\nv_mov_b32 v31, 0\nv_mov_b32 v58, 0xb9800000\nv_mov_b32 v59, 0\nv_and_b32 v66, 16, v60\nv_mov_b32 v67, 0xffff\nv_lshlrev_b32 v66, v66, v67\ns_mov_b32 vcc_lo, 0x00c0000c\n"
"s_mov_b32 vcc_hi, 0xc0000c00\nv_cndmask_b32 v67, 0, v66, vcc\ns_lshr_b64 vcc, vcc, 2\nv_cndmask_b32 v66, 0, v66, vcc\ns_cmp_lg_u64 s[74:75], 0\ns_cbranch_scc1 .Lfb\n.Lfbret0:\nv_rcp_f32 v2, v0\n"
"v_rcp_f32_e64 v6, v4\nv_mfma_f32_16x16x4_4b_bf16 a[208:223], v[30:31], v[58:59], a[208:223]\nv_rcp_f32 v10, v8\nv_rcp_f32 v14, v12\nv_rcp_f32 v18, v16\nv_rcp_f32 v22, v20\nv_rcp_f32 v26, v24\nv_mov_b64 v[0:1], 0\n"
"v_mfma_f32_16x16x4_4b_bf16 a[224:239], v[30:31], v[58:59], a[224:239]\nQk 2,2,2\nv_and_b32 v3, v2, v67\nv_and_b32 v2, v2, v66\nv_mov_b64_e64 v[4:5], 0\nQk 6,6,6\nv_and_b32_e64 v7, v6, v67\nv_mfma_f32_16x16x4_4b_bf16 a[240:255], v[30:31], v[58:59], a[240:255]\n"
"v_and_b32 v6, v6, v66\nv_mov_b64 v[8:9], 0\nQk 10,10,10\nv_and_b32 v11, v10, v67\nv_and_b32 v10, v10, v66\nv_mov_b64_e64 v[12:13], 0\nv_mfma_f32_16x16x4_4b_bf16 v[80:95], v[30:31], v[58:59], v[80:95]\nQk 14,14,14\n"
"v_and_b32 v15, v14, v67\nv_and_b32 v14, v14, v66\nv_mov_b64_e64 v[16:17], 0\nQk 18,18,18\nv_and_b32_e64 v19, v18, v67\nv_mfma_f32_16x16x4_4b_bf16 v[112:127], v[30:31], v[58:59], v[112:127]\nv_and_b32 v18, v18, v66\nv_mov_b64 v[20:21], 0\n"
"Qk 22,22,22\nv_and_b32 v23, v22, v67\nv_and_b32 v22, v22, v66\nv_mov_b64_e64 v[24:25], 0\nv_mfma_f32_16x16x4_4b_bf16 v[144:159], v[30:31], v[58:59], v[144:159]\nQk 26,26,26\nv_and_b32 v27, v26, v67\nv_and_b32 v26, v26, v66\n"
"v_mov_b64_e64 v[28:29], 0\nv_accvgpr_read_b32 v48, a208\nv_accvgpr_read_b32 v49, a209\nv_accvgpr_read_b32 v50, a210\nv_accvgpr_read_b32 v51, a211\nv_accvgpr_read_b32 v52, a212\nv_accvgpr_read_b32 v53, a213\nv_accvgpr_read_b32 v54, a214\n"
"v_accvgpr_read_b32 v55, a215\nQk 48,48,49\nQk 49,50,51\nQk 50,52,53\nQk 51,54,55\nv_accvgpr_read_b32 v32, a216\nv_accvgpr_read_b32 v33, a217\nQzvvv 52,48,20\n"
"v_accvgpr_read_b32 v34, a218\nv_accvgpr_read_b32 v35, a219\nQzvvv 48,48,22\nv_accvgpr_read_b32 v36, a220\nv_accvgpr_read_b32 v37, a221\nv_accvgpr_read_b32 v38, a222\nv_accvgpr_read_b32 v39, a223\nQk 32,32,33\n"
"Qk 33,34,35\nQk 34,36,37\nQk 35,38,39\nv_mfma_f32_16x16x4_4b_bf16 v[240:255], v[30:31], v[58:59], v[240:255]\nv_accvgpr_read_b32 v40, a224\nv_accvgpr_read_b32 v41, a225\nv_accvgpr_read_b32 v42, a226\nv_accvgpr_read_b32 v43, a227\n"
"v_accvgpr_read_b32 v44, a228\nv_accvgpr_read_b32 v45, a229\nQzvvv 36,32,20\nv_accvgpr_read_b32 v46, a230\nv_accvgpr_read_b32 v47, a231\nQzvvv 32,32,22\nQk 40,40,41\nQk 41,42,43\n"
"Qk 42,44,45\nQk 43,46,47\nQk 48,48,49\nQk 49,50,51\nQzvvv 44,40,24\nQk 50,52,53\nQk 51,54,55\nQzvvv 40,40,26\n"
"v_accvgpr_write_b32 a88, v48\nv_accvgpr_write_b32 a89, v49\nv_accvgpr_write_b32 a90, v50\nv_accvgpr_write_b32 a91, v51\nv_mfma_f32_16x16x4_4b_bf16 v[96:111], v[30:31], v[58:59], v[96:111]\nv_accvgpr_read_b32 v48, a232\nv_accvgpr_read_b32 v49, a233\nv_accvgpr_read_b32 v50, a234\n"
"v_accvgpr_read_b32 v51, a235\nv_accvgpr_read_b32 v52, a236\nv_accvgpr_read_b32 v53, a237\nv_accvgpr_read_b32 v54, a238\nv_accvgpr_read_b32 v55, a239\nQk 48,48,49\nQk 49,50,51\nQk 50,52,53\n"
"Qk 51,54,55\nQk 32,32,33\nQk 33,34,35\nQzvvv 52,48,24\nQk 34,36,37\nQk 35,38,39\nQzvvv 48,48,26\nv_accvgpr_write_b32 a92, v32\n"
"v_accvgpr_write_b32 a93, v33\nv_accvgpr_write_b32 a94, v34\nv_accvgpr_write_b32 a95, v35\nv_mfma_f32_16x16x4_4b_bf16 v[176:191], v[30:31], v[58:59], v[176:191]\nv_accvgpr_read_b32 v32, a240\nv_accvgpr_read_b32 v33, a241\nv_accvgpr_read_b32 v34, a242\nv_accvgpr_read_b32 v35, a243\n"
"v_accvgpr_read_b32 v36, a244\nv_accvgpr_read_b32 v37, a245\nv_accvgpr_read_b32 v38, a246\nv_accvgpr_read_b32 v39, a247\nQk 32,32,33\nQk 33,34,35\nQk 34,36,37\nQk 35,38,39\n"
"Qk 40,40,41\nQk 41,42,43\nQzvvv 36,32,24\nQk 42,44,45\nQk 43,46,47\nQzvvv 32,32,26\nv_accvgpr_write_b32 a96, v40\nv_accvgpr_write_b32 a97, v41\n"
"v_accvgpr_write_b32 a98, v42\nv_accvgpr_write_b32 a99, v43\nv_mfma_f32_16x16x4_4b_bf16 v[128:143], v[30:31], v[58:59], v[128:143]\nv_accvgpr_read_b32 v40, a248\nv_accvgpr_read_b32 v41, a249\nv_accvgpr_read_b32 v42, a250\nv_accvgpr_read_b32 v43, a251\nv_accvgpr_read_b32 v44, a252\n"
"v_accvgpr_read_b32 v45, a253\nv_accvgpr_read_b32 v46, a254\nv_accvgpr_read_b32 v47, a255\nQk 40,40,41\nQk 41,42,43\ns_mov_b64 vcc, -1\ns_nop 0\nQk 42,44,45\n"
"Qk 43,46,47\nQk 48,48,49\ns_waitcnt vmcnt(15)\ns_nop 0\nQk 49,50,51\nQzvvv 44,40,24\nQk 50,52,53\ns_barrier\n"
"s_nop 0\nQk 51,54,55\nQzvvv 40,40,26\nv_accvgpr_write_b32 a100, v48\nQta 112,61,0\nQta 114,61,512\nQda 208,63,33024\nQda 212,63,34048\n"
"v_accvgpr_write_b32 a101, v49\nv_accvgpr_write_b32 a102, v50\nv_accvgpr_write_b32 a103, v51\nv_mfma_f32_16x16x4_4b_bf16 v[160:175], v[30:31], v[58:59], v[160:175]\nQk 80,80,81\nQk 81,82,83\nQk 82,84,85\nQk 83,86,87\n"
"Qk 88,88,89\nQda 216,63,35072\nQzvvv 84,80,0\nQda 220,63,36096\nQda 224,63,37120\nQzvvv 80,80,2\nQda 228,63,38144\nQk 89,90,91\n"
"v_mfma_f32_16x16x4_4b_bf16 v[208:223], v[30:31], v[58:59], v[208:223]\nQk 90,92,93\nQk 91,94,95\nQk 112,112,113\nQk 113,114,115\nQk 114,116,117\nQk 115,118,119\nQzvvv 92,88,0\n"
"Qk 80,80,81\nQk 81,82,83\nQzvvv 88,88,2\nQda 232,63,39168\nQda 176,63,33536\nv_mfma_f32_16x16x4_4b_bf16 v[192:207], v[30:31], v[58:59], v[192:207]\nQta 116,61,4128\nQta 118,61,4640\n"
"Qk 82,84,85\nQk 83,86,87\nv_accvgpr_write_b32 a0, v80\nv_accvgpr_write_b32 a1, v81\nQzvvv 116,112,4\nv_accvgpr_write_b32 a2, v82\nv_accvgpr_write_b32 a3, v83\nQzvvv 112,112,6\n"
"Qk 120,120,121\nQk 121,122,123\nv_mfma_f32_16x16x4_4b_bf16 v[224:239], v[30:31], v[58:59], v[224:239]\nQk 122,124,125\nQta 120,61,8\nQta 122,61,520\nQta 124,61,4136\nQta 126,61,4648\n"
"Qk 123,126,127\nQk 88,88,89\nQk 89,90,91\nQzvvv 124,120,4\nQk 90,92,93\nQk 91,94,95\nQzvvv 120,120,6\nv_accvgpr_write_b32 a4, v88\n"
"v_accvgpr_write_b32 a5, v89\nv_accvgpr_write_b32 a6, v90\nv_accvgpr_write_b32 a7, v91\nQk 144,144,145\nQta 128,61,256\nQta 130,61,768\nQta 132,61,4384\nQta 134,61,4896\n"
"Qk 145,146,147\nQk 146,148,149\nQk 147,150,151\nQk 112,112,113\nQk 113,114,115\nQzvvv 148,144,8\nQk 114,116,117\nQk 115,118,119\n"
"Qzvvv 144,144,10\nv_accvgpr_write_b32 a16, v112\nv_accvgpr_write_b32 a17, v113\nv_accvgpr_write_b32 a18, v114\nQta 136,61,264\nQta 138,61,776\nQta 140,61,4392\nQta 142,61,4904\n"
"v_accvgpr_write_b32 a19, v115\nQk 240,240,241\nQk 241,242,243\nQk 242,244,245\nQk 243,246,247\nQk 120,120,121\nQk 121,122,123\nQzvvv 244,240,20\n"
"Qk 122,124,125\nQk 123,126,127\nQzvvv 240,240,22\nv_accvgpr_write_b32 a20, v120\ns_waitcnt lgkmcnt(15)\ns_nop 0\nv_accvgpr_write_b32 a21, v121\nv_accvgpr_write_b32 a22, v122\n"
"v_accvgpr_write_b32 a23, v123\nQzvaa 80,112,208\nQk 248,248,249\nQk 249,250,251\nQk 250,252,253\nQzvaa 84,112,212\nQda 180,63,34560\nQk 251,254,255\n"
"Qk 144,144,145\nQk 145,146,147\nQzvvv 252,248,20\nQk 146,148,149\nQk 147,150,151\nQzvaa 88,112,216\nQda 184,63,35584\nv_accvgpr_write_b32 a32, v144\n"
"Qzvvv 248,248,22\nv_accvgpr_write_b32 a33, v145\nv_accvgpr_write_b32 a34, v146\nv_accvgpr_write_b32 a35, v147\nQk 152,152,153\nQzvaa 92,112,220\nQda 188,63,36608\nQk 153,154,155\n"
"Qk 154,156,157\nQk 155,158,159\nQk 240,240,241\nQk 241,242,243\nQzvvv 156,152,8\nQk 242,244,245\nQk 243,246,247\nQzvaa 112,112,224\n"
"Qda 192,63,37632\nv_accvgpr_write_b32 a80, v240\nQzvvv 152,152,10\nv_accvgpr_write_b32 a81, v241\nv_accvgpr_write_b32 a82, v242\nQzvaa 116,112,228\nQda 196,63,38656\nv_accvgpr_write_b32 a83, v243\n"
"Qk 96,96,97\nQk 97,98,99\nQk 98,100,101\nQk 99,102,103\nQk 248,248,249\nQzvaa 120,112,232\nQda 200,63,39680\nQk 249,250,251\n"
"Qzvvv 100,96,0\nQk 250,252,253\nQk 251,254,255\nQzvvv 96,96,2\nv_accvgpr_write_b32 a84, v248\nv_max3_f32 v80, |v80|, |v81|, |v82|\nv_accvgpr_write_b32 a85, v249\nv_accvgpr_write_b32 a86, v250\n"
"v_accvgpr_write_b32 a87, v251\nv_max3_f32 v84, |v84|, |v85|, |v86|\nQk 104,104,105\nQk 105,106,107\nQk 106,108,109\nv_max3_f32 v88, |v88|, |v89|, |v90|\nQk 107,110,111\nQk 152,152,153\n"
"Qk 153,154,155\nv_max3_f32 v92, |v92|, |v93|, |v94|\nQzvvv 108,104,0\nQk 154,156,157\nQk 155,158,159\nv_max3_f32 v112, |v112|, |v113|, |v114|\nQzvvv 104,104,2\nv_accvgpr_write_b32 a36, v152\n"
"v_accvgpr_write_b32 a37, v153\nv_max3_f32 v116, |v116|, |v117|, |v118|\nv_accvgpr_write_b32 a38, v154\nv_accvgpr_write_b32 a39, v155\nQk 176,176,177\nv_max3_f32 v120, |v120|, |v121|, |v122|\nQk 177,178,179\nQk 178,180,181\n"
"Qk 179,182,183\nv_min3_u32 v56, v80, v84, v88\nQk 128,128,129\nQzvvv 180,176,12\nv_min3_u32 v56, v56, v92, v112\nQk 129,130,131\nQzvvv 176,176,14\nQk 130,132,133\n"
"Qk 131,134,135\nv_min3_u32 v56, v56, v116, v120\nQk 136,136,137\nQzvvv 132,128,4\nv_mov_b32_e64 v57, v56\nQk 137,138,139\nQzvvv 128,128,6\nQk 138,140,141\n"
"Qk 139,142,143\nv_permlane32_swap_b32_e64 v56, v57\nQk 176,176,177\nQk 177,178,179\nQzvvv 140,136,4\nv_max_f32_e64 v56, v56, v57\nQk 178,180,181\nQk 179,182,183\n"
"Qzvvv 136,136,6\nv_mov_b32_e64 v57, v56\nv_accvgpr_write_b32 a48, v176\nv_accvgpr_write_b32 a49, v177\nv_accvgpr_write_b32 a50, v178\nv_permlane16_swap_b32_e64 v56, v57\nv_accvgpr_write_b32 a51, v179\nQk 184,184,185\n"
"Qk 185,186,187\nv_max_f32_e64 v56, v56, v57\nQk 186,188,189\nQk 187,190,191\nv_min_f32_dpp v56, v56, v56 row_ror:8 row_mask:0xf bank_mask:0xf\nQk 160,160,161\nQzvvv 188,184,12\nQk 161,162,163\n"
"v_min_f32_dpp v56, v56, v56 row_ror:4 row_mask:0xf bank_mask:0xf\nQzvvv 184,184,14\nQk 162,164,165\nQk 163,166,167\n.Lpg9:\ns_cbranch_execz .Ltr10\ns_nop 0\nv_min_f32_dpp v56, v56, v56 row_ror:2 row_mask:0xf bank_mask:0xf\n"
"Qk 168,168,169\nQzvvv 164,160,8\nQk 169,170,171\nv_min_f32_dpp v56, v56, v56 row_ror:1 row_mask:0xf bank_mask:0xf\nQzvvv 160,160,10\nQk 170,172,173\nQk 171,174,175\nQk 184,184,185\n"
"v_and_b32 v56, 0x7f800000, v56\nQk 185,186,187\nQzvvv 172,168,8\nQk 186,188,189\nv_min_u32 v56, 0x78000000, v56\nQk 187,190,191\nQzvvv 168,168,10\nv_accvgpr_write_b32 a52, v184\n"
"v_add_u32 v56, 0x5400000, v56\nv_accvgpr_write_b32 a53, v185\nv_accvgpr_write_b32 a54, v186\nv_accvgpr_write_b32 a55, v187\nv_mov_b32_e64 v57, v56\nQk 208,208,209\nQk 209,210,211\nQk 210,212,213\n"
"v_mov_b64_e64 v[58:59], v[56:57]\nQk 211,214,215\nv_mul_f32 v52, -v56, s72\nQk 192,192,193\nQzvvv 212,208,16\nQk 193,194,195\nQk 194,196,197\nQzvvv 208,208,18\n"
"Qk 195,198,199\nQk 200,200,201\nQk 201,202,203\nQzvvv 196,192,12\nQk 202,204,205\nQk 203,206,207\nQzvvv 192,192,14\nQk 216,216,217\n"
"Qk 208,208,209\nQk 209,210,211\nQzvvv 204,200,12\nQk 210,212,213\nQk 211,214,215\nQzvvv 200,200,14\nv_accvgpr_write_b32 a64, v208\nv_accvgpr_write_b32 a65, v209\n"
"v_accvgpr_write_b32 a66, v210\nv_accvgpr_write_b32 a67, v211\nQk 217,218,219\nQk 218,220,221\nQk 219,222,223\nQk 224,224,225\nQk 225,226,227\nQzvvv 220,216,16\n"
"Qk 226,228,229\nQk 227,230,231\nQzvvv 216,216,18\nQk 232,232,233\nQk 233,234,235\nQzvvv 228,224,16\nQk 234,236,237\nQk 235,238,239\n"
"Qzvvv 224,224,18\ns_nop 1\ns_nop 0\nQk 216,216,217\nQzvvv 236,232,16\nQk 217,218,219\nQk 218,220,221\nQk 219,222,223\n"
"Qzvvv 232,232,18\nv_accvgpr_write_b32 a68, v216\nv_accvgpr_write_b32 a69, v217\nv_accvgpr_write_b32 a70, v218\nv_accvgpr_write_b32 a71, v219\ns_mov_b32 s56, s20\ns_and_b32 s57, s21, 0xffff\ns_mov_b32 s94, 0x7ffff000\n"
"s_mul_i32 s68, %0, 40960\ns_lshl_b32 s69, s70, 10\ns_add_u32 s68, s68, s69\ns_add_u32 s68, s68, 4096\ns_add_u32 s94, s16, s68\ns_addc_u32 s95, s17, 0\ns_and_b32 s95, s95, 0xffff\ns_mov_b32 s75, 37888\n"
"s_mov_b32 s58, 4194304\ns_mov_b32 s67, 1\ns_branch .Lround2\n.Lhook:\n.Lpg10:\ns_cbranch_execz .Ltr10\ns_cmp_eq_u32 s66, 1\ns_cbranch_scc1 .Lh1\n"
"s_cmp_eq_u32 s66, 5\ns_cbranch_scc1 .Lh5\ns_cmp_eq_u32 s66, 6\ns_cbranch_scc1 .Lh6\ns_cmp_eq_u32 s66, 9\ns_cbranch_scc1 .Lh9\ns_lshr_b32 s68, s99, s66\ns_cmp_eq_u32 s68, 1\n"
"s_cbranch_scc1 .Lh12\ns_branch .Lh11\n.Lh1:\nds_read_b32 v78, v74 offset:16\ns_cmp_eq_u32 s70, 0\ns_cselect_b32 s68, 128, 0\nv_mov_b32 v66, 0x7ffff000\nv_cmp_gt_u32 vcc, s68, v60\n"
"v_not_b32 v67, v65\ns_lshl_b32 s68, s19, 2\nv_cndmask_b32 v66, v66, v76, vcc\nv_readlane_b32 s30, v67, s19\ns_waitcnt lgkmcnt(0)\ns_nop 1\nv_add_u32_dpp v78, v78, v78 quad_perm:[1,0,3,2] row_mask:0xf bank_mask:0xf\ns_nop 1\n"
"v_add_u32_dpp v78, v78, v78 quad_perm:[2,3,0,1] row_mask:0xf bank_mask:0xf\nv_ashrrev_i32 v78, 1, v78\nv_med3_i32 v78, v78, -7, 7\nv_add_lshl_u32 v78, v78, 8, 28\nv_xor_b32 v67, v67, v78\nbuffer_store_dword v67, v66, s[24:27], s68 offen offset:1024 sc1\ns_branch .Lhookret\n.Lh9:\n"
"s_lshl_b32 s68, s19, 4\ns_cmp_eq_u32 s70, 0\ns_cselect_b32 s68, s68, -1\nv_mov_b32 v67, 0x7ffff000\nv_cmp_eq_u32 vcc, s68, v60\ns_nop 1\nv_cndmask_b32 v67, v67, v76, vcc\nv_add_u32 v66, 1, v65\n"
"buffer_store_dword v66, v67, s[24:27], 0 offen sc1\ns_mov_b64 exec, 0\ns_cmp_eq_u32 s70, 0\ns_cbranch_scc1 .Lpg12\ns_cmp_eq_u32 s70, 1\ns_cbranch_scc1 .Lpg13\ns_cmp_eq_u32 s70, 2\ns_cbranch_scc1 .Lpg14\n"
"s_branch .Lpg15\n.Ltr6:\ns_mov_b64 exec, -1\ns_branch .Lhookret\n.Lh5:\nv_bfe_u32 v66, v60, 4, 3\ns_lshl_b32 s68, s19, 7\nv_lshlrev_b32 v66, 2, v66\n"
"s_add_u32 s68, s68, 5242880\nbuffer_load_dword v78, v66, s[24:27], s68 offen offset:1024 sc1\ns_branch .Lhookret\n.Lh6:\ns_waitcnt vmcnt(32)\ns_mov_b32 s97, 0\n.Lack_l:\nv_xor_b32 v78, s30, v78\n"
"v_and_b32 v66, 0x0fffffff, v78\nv_cmp_ne_u32 vcc, 0, v66\ns_cmp_eq_u64 vcc, 0\ns_cbranch_scc1 .Lack_ok\ns_sleep 1\nv_bfe_u32 v66, v60, 4, 3\ns_lshl_b32 s68, s19, 7\nv_lshlrev_b32 v66, 2, v66\n"
"s_add_u32 s68, s68, 5242880\nbuffer_load_dword v78, v66, s[24:27], s68 offen offset:1024 sc1\ns_waitcnt vmcnt(0)\ns_add_u32 s97, s97, 1\ns_cmp_lt_u32 s97, 1048576\ns_cbranch_scc1 .Lack_l\n.Lack_ok:\nv_lshrrev_b32 v78, 28, v78\n"
"s_nop 1\nv_add_u32_dpp v78, v78, v78 quad_perm:[1,0,3,2] row_mask:0xf bank_mask:0xf\ns_nop 1\nv_add_u32_dpp v78, v78, v78 quad_perm:[2,3,0,1] row_mask:0xf bank_mask:0xf\ns_nop 1\nv_readlane_b32 s68, v78, s19\nv_readlane_b32 s69, v78, 4\ns_cmp_lt_u32 s68, 32\n"
"s_cselect_b32 s68, 8192, 0\ns_xor_b32 s68, s68, s98\ns_add_u32 s71, s71, s68\ns_cmp_lt_u32 s69, 32\ns_cselect_b32 s69, 8, 0\ns_xor_b32 s69, s69, %0\ns_and_b32 s69, s69, 8\ns_lshl_b32 s98, s69, 10\n"
"s_branch .Lhookret\n.Lh11:\nbuffer_load_dword v78, v76, s[24:27], 0 offen sc1\ns_mov_b64 exec, 0\ns_cmp_eq_u32 s70, 0\ns_cbranch_scc1 .Lpg16\ns_cmp_eq_u32 s70, 1\ns_cbranch_scc1 .Lpg17\n"
"s_cmp_eq_u32 s70, 2\ns_cbranch_scc1 .Lpg18\ns_branch .Lpg19\n.Ltr8:\ns_mov_b64 exec, -1\ns_branch .Lhookret\n.Lh12:\ns_waitcnt vmcnt(32)\n"
"v_sub_u32 v66, v78, v65\nv_cmp_gt_u32 vcc, 1, v66\ns_and_b64 vcc, vcc, s[28:29]\ns_cbranch_scc1 .Lf1_w\n.Lf1_r:\ns_cmp_lg_u32 s70, 3\ns_cbranch_scc1 .Lf1_n\nbuffer_inv sc1\n"
".Lf1_n:\ns_lshr_b32 s88, s19, 2\ns_lshl_b32 s88, s88, 4\ns_add_u32 s88, s88, s93\ns_andn2_b32 s68, s71, 786432\ns_add_u32 s48, s20, s68\ns_addc_u32 s49, s21, 0\ns_and_b32 s49, s49, 0xffff\n"
"s_sub_u32 s50, 4194304, s68\ns_mul_i32 s68, s88, 163840\ns_and_b32 s69, s19, 3\ns_mul_i32 s69, s69, 320\ns_add_u32 s68, s68, s69\ns_add_u32 s52, s16, s68\ns_addc_u32 s53, s17, 0\ns_and_b32 s53, s53, 0xffff\n"
"s_sub_u32 s54, 5246976, s68\ns_add_u32 s71, s71, 1048576\ns_and_b32 s87, s19, 3\ns_mul_i32 s87, s87, 327680\ns_add_u32 s87, s87, 1310720\ns_mul_i32 s69, s92, 10240\nv_bfe_u32 v66, v60, 5, 3\nv_mul_u32_u24 v66, 1280, v66\n"
"v_and_b32 v67, 16, v60\nv_add3_u32 v67, v67, v66, s69\ns_and_b32 s69, s19, 1\nv_lshrrev_b32 v66, 8, v60\ns_lshl_b32 s68, s93, 1\nv_xor_b32 v66, s68, v66\nv_lshl_add_u32 v66, v66, 5, v67\ns_lshr_b32 s68, 128, s69\n"
"v_add_u32 v61, s68, v66\ns_mul_i32 s68, s69, 192\nv_add_u32 v63, s68, v66\nv_bfe_u32 v66, v60, 8, 1\nv_xor_b32 v66, s93, v66\nv_lshl_add_u32 v67, v66, 5, v67\nv_lshrrev_b32 v66, 9, v60\nv_mul_u32_u24 v66, 20480, v66\n"
"s_xor_b32 s69, s69, 1\ns_lshl_b32 s69, s69, 8\nv_add3_u32 v62, v66, v67, s69\ns_branch .Lhookret\n.Lf1_w:\ns_mov_b32 s97, 0\n.Lf1_l:\ns_sleep 1\n"
"buffer_load_dword v78, v76, s[24:27], 0 offen sc1\ns_waitcnt vmcnt(0)\ns_add_u32 s97, s97, 1\ns_cmp_gt_u32 s97, 1048576\ns_cbranch_scc1 .Lf1_r\nv_sub_u32 v66, v78, v65\nv_cmp_gt_u32 vcc, 1, v66\ns_and_b64 vcc, vcc, s[28:29]\n"
"s_cbranch_scc1 .Lf1_l\ns_branch .Lf1_r\n.Lsp0:\ns_sub_u32 s75, s77, s74\ns_cmp_lt_u32 s75, 1024\ns_cbranch_scc0 .Lrp0\ns_memrealtime s[74:75]\ns_waitcnt lgkmcnt(0)\n"
"s_branch .Lsp0\n.Lsa1:\nds_read_b32 v66, v74\ns_waitcnt lgkmcnt(0)\nv_cmp_gt_u32 vcc, v75, v66\ns_cbranch_vccnz .Lsa1\ns_branch .Lra1\n.Lsp1:\n"
"s_sub_u32 s75, s77, s74\ns_cmp_lt_u32 s75, 1024\ns_cbranch_scc0 .Lrp1\ns_memrealtime s[74:75]\ns_waitcnt lgkmcnt(0)\ns_branch .Lsp1\n.Lsa2:\nds_read_b32 v66, v74\n"
"s_waitcnt lgkmcnt(0)\nv_cmp_gt_u32 vcc, v75, v66\ns_cbranch_vccnz .Lsa2\ns_branch .Lra2\n.Lsp2:\ns_sub_u32 s75, s77, s74\ns_cmp_lt_u32 s75, 1024\ns_cbranch_scc0 .Lrp2\n"
"s_memrealtime s[74:75]\ns_waitcnt lgkmcnt(0)\ns_branch .Lsp2\n.Lsa3:\nds_read_b32 v66, v74\ns_waitcnt lgkmcnt(0)\nv_cmp_gt_u32 vcc, v75, v66\ns_cbranch_vccnz .Lsa3\n"
"s_branch .Lra3\n.Lsa4:\nds_read_b32 v66, v74\ns_waitcnt lgkmcnt(0)\nv_cmp_gt_u32 vcc, v75, v66\ns_cbranch_vccnz .Lsa4\ns_branch .Lra4\n.Lsp4:\n"
"s_sub_u32 s75, s77, s74\ns_cmp_lt_u32 s75, 1024\ns_cbranch_scc0 .Lrp4\ns_memrealtime s[74:75]\ns_waitcnt lgkmcnt(0)\ns_branch .Lsp4\n.Lsa5:\nds_read_b32 v66, v74\n"
"s_waitcnt lgkmcnt(0)\nv_cmp_gt_u32 vcc, v75, v66\ns_cbranch_vccnz .Lsa5\ns_branch .Lra5\n.Lsa30:\nds_read_b32 v66, v74\ns_waitcnt lgkmcnt(0)\nv_cmp_gt_u32 vcc, v75, v66\n"
"s_cbranch_vccnz .Lsa30\ns_branch .Lra30\n.Lsa31:\nds_read_b32 v66, v74\ns_waitcnt lgkmcnt(0)\nv_cmp_gt_u32 vcc, v75, v66\ns_cbranch_vccnz .Lsa31\ns_branch .Lra31\n"
".Lsa32:\nds_read_b32 v66, v74\ns_waitcnt lgkmcnt(0)\nv_cmp_gt_u32 vcc, v75, v66\ns_cbranch_vccnz .Lsa32\ns_branch .Lra32\n.Lsa30x:\nds_read_b32 v66, v74\n"
"s_waitcnt lgkmcnt(0)\nv_cmp_gt_u32 vcc, v75, v66\ns_cbranch_vccnz .Lsa30x\ns_branch .Lra30x\n.Lsa31x:\nds_read_b32 v66, v74\ns_waitcnt lgkmcnt(0)\nv_cmp_gt_u32 vcc, v75, v66\n"
"s_cbranch_vccnz .Lsa31x\ns_branch .Lra31x\n.Lso40:\nds_read_b32 v66, v74\ns_waitcnt lgkmcnt(0)\nv_cmp_gt_u32 vcc, v75, v66\ns_cbranch_vccnz .Lso40\ns_branch .Lro40\n"
".Lso180:\nds_read_b32 v66, v74\ns_waitcnt lgkmcnt(0)\nv_cmp_gt_u32 vcc, v75, v66\ns_cbranch_vccnz .Lso180\ns_branch .Lro180\n.Lso320:\nds_read_b32 v66, v74\n"
"s_waitcnt lgkmcnt(0)\nv_cmp_gt_u32 vcc, v75, v66\ns_cbranch_vccnz .Lso320\ns_branch .Lro320\n.Lso460:\nds_read_b32 v66, v74\ns_waitcnt lgkmcnt(0)\nv_cmp_gt_u32 vcc, v75, v66\n"
"s_cbranch_vccnz .Lso460\ns_branch .Lro460\n.Lso600:\nds_read_b32 v66, v74\ns_waitcnt lgkmcnt(0)\nv_cmp_gt_u32 vcc, v75, v66\ns_cbranch_vccnz .Lso600\ns_branch .Lro600\n"
".Lso740:\nds_read_b32 v66, v74\ns_waitcnt lgkmcnt(0)\nv_cmp_gt_u32 vcc, v75, v66\ns_cbranch_vccnz .Lso740\ns_branch .Lro740\n.Lso880:\nds_read_b32 v66, v74\n"
"s_waitcnt lgkmcnt(0)\nv_cmp_gt_u32 vcc, v75, v66\ns_cbranch_vccnz .Lso880\ns_branch .Lro880\n.Lso1020:\nds_read_b32 v66, v74\ns_waitcnt lgkmcnt(0)\nv_cmp_gt_u32 vcc, v75, v66\n"
"s_cbranch_vccnz .Lso1020\ns_branch .Lro1020\n.Lso1160:\nds_read_b32 v66, v74\ns_waitcnt lgkmcnt(0)\nv_cmp_gt_u32 vcc, v75, v66\ns_cbranch_vccnz .Lso1160\ns_branch .Lro1160\n"
".Lso1300:\nds_read_b32 v66, v74\ns_waitcnt lgkmcnt(0)\nv_cmp_gt_u32 vcc, v75, v66\ns_cbranch_vccnz .Lso1300\ns_branch .Lro1300\n.Lso1440:\nds_read_b32 v66, v74\n"
"s_waitcnt lgkmcnt(0)\nv_cmp_gt_u32 vcc, v75, v66\ns_cbranch_vccnz .Lso1440\ns_branch .Lro1440\n.Lso1455:\nds_read_b32 v66, v74\ns_waitcnt lgkmcnt(0)\nv_cmp_gt_u32 vcc, v75, v66\n"
"s_cbranch_vccnz .Lso1455\ns_branch .Lro1455\n.Lso1525r0:\nds_read_b32 v66, v74\ns_waitcnt lgkmcnt(0)\nv_cmp_gt_u32 vcc, v75, v66\ns_cbranch_vccnz .Lso1525r0\ns_branch .Lro1525r0\n"
".Lso1595r0:\nds_read_b32 v66, v74\ns_waitcnt lgkmcnt(0)\nv_cmp_gt_u32 vcc, v75, v66\ns_cbranch_vccnz .Lso1595r0\ns_branch .Lro1595r0\n.Lso1665r0:\nds_read_b32 v66, v74\n"
"s_waitcnt lgkmcnt(0)\nv_cmp_gt_u32 vcc, v75, v66\ns_cbranch_vccnz .Lso1665r0\ns_branch .Lro1665r0\n.Lso1735r0:\nds_read_b32 v66, v74\ns_waitcnt lgkmcnt(0)\nv_cmp_gt_u32 vcc, v75, v66\n"
"s_cbranch_vccnz .Lso1735r0\ns_branch .Lro1735r0\n.Lso1805r0:\nds_read_b32 v66, v74\ns_waitcnt lgkmcnt(0)\nv_cmp_gt_u32 vcc, v75, v66\ns_cbranch_vccnz .Lso1805r0\ns_branch .Lro1805r0\n"
".Lso1878r0:\nds_read_b32 v66, v74\ns_waitcnt lgkmcnt(0)\nv_cmp_gt_u32 vcc, v75, v66\ns_cbranch_vccnz .Lso1878r0\ns_branch .Lro1878r0\n.Lso1920r0:\nds_read_b32 v66, v74\n"
"s_waitcnt lgkmcnt(0)\nv_cmp_gt_u32 vcc, v75, v66\ns_cbranch_vccnz .Lso1920r0\ns_branch .Lro1920r0\n.Lso1960r0:\nds_read_b32 v66, v74\ns_waitcnt lgkmcnt(0)\nv_cmp_gt_u32 vcc, v75, v66\n"
"s_cbranch_vccnz .Lso1960r0\ns_branch .Lro1960r0\n.Lso2046r0:\nds_read_b32 v66, v74\ns_waitcnt lgkmcnt(0)\nv_cmp_gt_u32 vcc, v75, v66\ns_cbranch_vccnz .Lso2046r0\ns_branch .Lro2046r0\n"
".Lso2088r0:\nds_read_b32 v66, v74\ns_waitcnt lgkmcnt(0)\nv_cmp_gt_u32 vcc, v75, v66\ns_cbranch_vccnz .Lso2088r0\ns_branch .Lro2088r0\n.Lso2172r0:\nds_read_b32 v66, v74\n"
"s_waitcnt lgkmcnt(0)\nv_cmp_gt_u32 vcc, v75, v66\ns_cbranch_vccnz .Lso2172r0\ns_branch .Lro2172r0\n.Lso2222r0:\nds_read_b32 v66, v74\ns_waitcnt lgkmcnt(0)\nv_cmp_gt_u32 vcc, v75, v66\n"
"s_cbranch_vccnz .Lso2222r0\ns_branch .Lro2222r0\n.Lso1525r1:\nds_read_b32 v66, v74\ns_waitcnt lgkmcnt(0)\nv_cmp_gt_u32 vcc, v75, v66\ns_cbranch_vccnz .Lso1525r1\ns_branch .Lro1525r1\n"
".Lso1595r1:\nds_read_b32 v66, v74\ns_waitcnt lgkmcnt(0)\nv_cmp_gt_u32 vcc, v75, v66\ns_cbranch_vccnz .Lso1595r1\ns_branch .Lro1595r1\n.Lso1665r1:\nds_read_b32 v66, v74\n"
"s_waitcnt lgkmcnt(0)\nv_cmp_gt_u32 vcc, v75, v66\ns_cbranch_vccnz .Lso1665r1\ns_branch .Lro1665r1\n.Lso1735r1:\nds_read_b32 v66, v74\ns_waitcnt lgkmcnt(0)\nv_cmp_gt_u32 vcc, v75, v66\n"
"s_cbranch_vccnz .Lso1735r1\ns_branch .Lro1735r1\n.Lso1805r1:\nds_read_b32 v66, v74\ns_waitcnt lgkmcnt(0)\nv_cmp_gt_u32 vcc, v75, v66\ns_cbranch_vccnz .Lso1805r1\ns_branch .Lro1805r1\n"
".Lso1840r1:\nds_read_b32 v66, v74\ns_waitcnt lgkmcnt(0)\nv_cmp_gt_u32 vcc, v75, v66\ns_cbranch_vccnz .Lso1840r1\ns_branch .Lro1840r1\n.Lso1878r1:\nds_read_b32 v66, v74\n"
"s_waitcnt lgkmcnt(0)\nv_cmp_gt_u32 vcc, v75, v66\ns_cbranch_vccnz .Lso1878r1\ns_branch .Lro1878r1\n.Lso1920r1:\nds_read_b32 v66, v74\ns_waitcnt lgkmcnt(0)\nv_cmp_gt_u32 vcc, v75, v66\n"
"s_cbranch_vccnz .Lso1920r1\ns_branch .Lro1920r1\n.Lso1945r1:\nds_read_b32 v66, v74\ns_waitcnt lgkmcnt(0)\nv_cmp_gt_u32 vcc, v75, v66\ns_cbranch_vccnz .Lso1945r1\ns_branch .Lro1945r1\n"
".Lso2004r1:\nds_read_b32 v66, v74\ns_waitcnt lgkmcnt(0)\nv_cmp_gt_u32 vcc, v75, v66\ns_cbranch_vccnz .Lso2004r1\ns_branch .Lro2004r1\n.Lso2045r1:\nds_read_b32 v66, v74\n"
"s_waitcnt lgkmcnt(0)\nv_cmp_gt_u32 vcc, v75, v66\ns_cbranch_vccnz .Lso2045r1\ns_branch .Lro2045r1\n.Lso2130r1:\nds_read_b32 v66, v74\ns_waitcnt lgkmcnt(0)\nv_cmp_gt_u32 vcc, v75, v66\n"
"s_cbranch_vccnz .Lso2130r1\ns_branch .Lro2130r1\n.Lso2172r1:\nds_read_b32 v66, v74\ns_waitcnt lgkmcnt(0)\nv_cmp_gt_u32 vcc, v75, v66\ns_cbranch_vccnz .Lso2172r1\ns_branch .Lro2172r1\n"
".Lso2225r1:\nds_read_b32 v66, v74\ns_waitcnt lgkmcnt(0)\nv_cmp_gt_u32 vcc, v75, v66\ns_cbranch_vccnz .Lso2225r1\ns_branch .Lro2225r1\n.Ltail1:\n.Lpg11:\n"
"s_cbranch_execz .Ltr10\nQcvaa 0,128,32,56\nv_add_u32 v75, 1, v75\nQmvaa 0,132,36\ns_waitcnt vmcnt(0)\nds_write_b32 v73, v75\nQmvaa 0,136,40\nQe 8\n"
"Qmvaa 0,140,44\nQe 9\nQcvaa 4,124,44,56\nQe 10\nQmvaa 4,120,40\nQe 11\nQmvaa 4,116,36\nQe 12\n"
"Qmvaa 4,112,32\ns_waitcnt lgkmcnt(1)\nQk 40,8,9\nQmvva 96,44,144\nQe 13\nQmvva 96,36,148\nQe 14\nQmvva 100,44,152\n"
"Qe 15\nQmvva 100,36,156\nQk 41,10,11\nQk 42,12,13\nQmvva 104,44,160\nQk 43,14,15\nQmvva 104,36,164\nQe 0\n"
"Qmvva 108,44,168\nQe 1\nQmvva 108,36,172\nQe 2\nQmvva 140,48,168\nQe 3\nQmvva 140,40,172\nQk 46,0,1\n"
"Qk 47,2,3\nQmvva 136,48,160\nds_read_b32 v66, v74\nQmvva 136,40,164\nQe 4\nQmvva 132,48,152\nQe 5\nQmvva 132,40,156\n"
"Qe 6\nQmvva 128,48,144\nQe 7\nQmvva 128,40,148\ns_waitcnt lgkmcnt(0)\nv_cmp_gt_u32 vcc, v75, v66\nv_smfmac_f32_16x16x64_bf16 a[176:179], a[204:207], v[36:43], v77\ns_cbranch_vccnz .Lsa30x\n"
".Lra30x:\nQcvaa 8,112,48,56\ns_add_u32 m0, s96, 0\ns_add_u32 s95, s87, 4096\nQk 44,4,5\nQmvaa 8,116,52\nbuffer_load_dwordx4 v61, s[52:55], s95 offen lds\nQmvaa 8,120,56\n"
"Qk 45,6,7\nQmvaa 8,124,60\nQcvaa 12,140,60,56\nQmvaa 12,136,56\ns_add_u32 s95, s87, 23552\nQmvaa 12,132,52\nbuffer_load_dwordx4 v61, s[52:55], s95 offen offset:1024 lds\nQmvaa 12,128,48\n"
"Qcvaa 0,128,64,56\nQmvaa 0,132,68\nQe 8\nQmvaa 0,136,72\ns_add_u32 m0, s96, 8192\ns_add_u32 s95, s87, 45056\nQe 9\nQmvaa 0,140,76\n"
"buffer_load_dwordx4 v61, s[52:55], s95 offen lds\nQcvaa 4,124,76,56\nQe 10\nQmvaa 4,120,72\nQe 11\nQmvaa 4,116,68\nQe 12\nQmvaa 4,112,64\n"
"s_add_u32 s95, s87, 64512\nQe 13\nQmvva 160,52,144\nbuffer_load_dwordx4 v61, s[52:55], s95 offen offset:1024 lds\nQmvva 160,44,148\nQe 14\nQmvva 164,52,152\nQe 15\n"
"Qmvva 164,44,156\nQk 48,8,9\nQk 49,10,11\nQmvva 168,52,160\ns_add_u32 m0, s96, 16384\ns_add_u32 s95, s87, 4096\nQk 50,12,13\nQmvva 168,44,164\n"
"buffer_load_dwordx4 v62, s[52:55], s95 offen lds\nQmvva 172,52,168\nQk 51,14,15\nQmvva 172,44,172\nQe 0\nQmvva 204,20,168\nQe 1\nQmvva 204,48,172\n"
"s_add_u32 s95, s87, 44032\nQe 2\nQmvva 200,20,160\nbuffer_load_dwordx4 v62, s[52:55], s95 offen offset:1024 lds\nQmvva 200,48,164\nQe 3\nQmvva 196,20,152\nQk 18,0,1\n"
"Qk 19,2,3\nQmvva 196,48,156\nQe 4\nQmvva 192,20,144\ns_add_u32 m0, s96, 24576\ns_add_u32 s95, s87, 4096\nQe 5\nQmvva 192,48,148\n"
"buffer_load_dwordx4 v63, s[52:55], s95 offen lds\nv_smfmac_f32_16x16x64_bf16 a[180:183], a[204:207], v[44:51], v77\nQk 16,4,5\nQcvaa 8,112,80,56\nQe 6\nQmvaa 8,116,84\nQe 7\nQmvaa 8,120,88\n"
"s_add_u32 s95, s87, 23552\nQk 17,6,7\nQmvaa 8,124,92\nbuffer_load_dwordx4 v63, s[52:55], s95 offen offset:1024 lds\nQcvaa 12,140,92,56\nQmvaa 12,136,88\nQmvaa 12,132,84\nQmvaa 12,128,80\n"
"s_add_u32 m0, s96, 32768\ns_add_u32 s95, s87, 45056\nQcvaa 0,128,96,56\nQda 128,68,34048\nbuffer_load_dwordx4 v63, s[52:55], s95 offen lds\nQmvaa 0,132,100\nQda 132,68,34560\nQmvaa 0,136,104\n"
"Qda 136,68,34304\nQmvaa 0,140,108\nQda 140,68,34816\nQcvaa 4,124,108,56\nQda 124,68,33792\ns_add_u32 s95, s87, 64512\nQmvaa 4,120,104\nQda 120,68,33280\n"
"buffer_load_dwordx4 v63, s[52:55], s95 offen offset:1024 lds\nQmvaa 4,116,100\nQda 116,68,33536\nQmvaa 4,112,96\nQda 112,68,33024\nQmvva 224,24,144\nQe 8\nQmvva 224,16,148\n"
"s_add_u32 m0, s96, 40960\ns_mov_b32 s95, 4096\nQe 9\nQmvva 228,24,152\nbuffer_load_dwordx4 v61, s[52:55], s95 offen lds\nQe 10\nQmvva 228,16,156\nQe 11\n"
"Qmvva 232,24,160\nQk 20,8,9\nQk 21,10,11\n.Lpg12:\ns_cbranch_execz .Ltr6\nQmvva 232,16,164\nQe 12\nQmvva 236,24,168\n"
"s_mov_b32 s95, 23552\nQe 13\nQmvva 236,16,172\nbuffer_load_dwordx4 v61, s[52:55], s95 offen offset:1024 lds\ns_waitcnt lgkmcnt(0)\nQk 22,12,13\nQcvaa 8,112,0,56\nQe 14\n"
"Qmvaa 8,116,4\nv_subrev_u32 v68, 66048, v68\nQmvaa 8,120,8\nQe 15\nQmvaa 8,124,12\ns_add_u32 m0, s96, 49152\ns_mov_b32 s95, 45056\nQk 23,14,15\n"
"Qcvaa 12,140,12,56\nbuffer_load_dwordx4 v61, s[52:55], s95 offen lds\nQe 0\nQmvaa 12,136,8\nQe 1\nQmvaa 12,132,4\nQe 2\nQmvaa 12,128,0\n"
"Qe 3\nv_smfmac_f32_16x16x64_bf16 a[184:187], a[204:207], v[16:23], v77\ns_mov_b32 s95, 64512\nQk 26,0,1\nQmava 208,28,144\nbuffer_load_dwordx4 v61, s[52:55], s95 offen offset:1024 lds\nQe 4\nQmava 208,20,148\n"
"Qe 5\nQmava 212,28,152\nQe 6\nQmava 212,20,156\nQe 7\nQmava 216,28,160\ns_add_u32 m0, s96, 57344\ns_mov_b32 s95, 4096\n"
"Qk 27,2,3\nQk 24,4,5\nQmava 216,20,164\nbuffer_load_dwordx4 v62, s[52:55], s95 offen lds\nQk 25,6,7\nQmava 220,28,168\nQe 8\nQmava 220,20,172\n"
"Qe 9\nQmava 252,32,168\nQta 168,71,768\nQta 170,71,1792\nQmava 252,24,172\ns_mov_b32 s95, 44032\nQe 10\nQmava 248,32,160\n"
"Qta 172,71,33792\nQta 174,71,34816\nbuffer_load_dwordx4 v62, s[52:55], s95 offen offset:1024 lds\nQmava 248,24,164\nQe 11\nQmava 244,32,152\nQta 160,71,512\nQta 162,71,1536\n"
"Qmava 244,24,156\nQk 28,8,9\nQk 29,10,11\nQmava 240,32,144\nQta 164,71,33536\nQta 166,71,34560\ns_add_u32 m0, s96, 132096\ns_mov_b32 s95, 4096\n"
"Qmava 240,24,148\nbuffer_load_dwordx4 v63, s[52:55], s95 offen lds\nQe 12\nQcvaa 0,112,16,56\nQta 152,71,256\nQta 154,71,1280\nQmvaa 0,116,20\nQe 13\n"
"Qmvaa 0,120,24\nQta 156,71,33280\nQta 158,71,34304\nQmvaa 0,124,28\ns_mov_b32 s95, 23552\nQe 14\nQcvaa 4,140,28,56\nQta 144,71,0\n"
"Qta 146,71,1024\nbuffer_load_dwordx4 v63, s[52:55], s95 offen offset:1024 lds\nQmvaa 4,136,24\nQe 15\nQmvaa 4,132,20\nQta 148,71,33024\nQta 150,71,34048\nQmvaa 4,128,16\n"
"Qk 30,12,13\nQk 31,14,15\nQcvaa 8,128,32,56\nv_add_u32 v75, 1, v75\nQmvaa 8,132,36\nds_write_b32 v73, v75\nQmvaa 8,136,40\nQe 0\n"
"Qmvaa 8,140,44\nQe 1\nQcvaa 12,124,44,56\nQe 2\nQmvaa 12,120,40\nQe 3\nQmvaa 12,116,36\nQe 4\n"
"Qmvaa 12,112,32\ns_waitcnt lgkmcnt(1)\nQk 32,0,1\nv_smfmac_f32_16x16x64_bf16 a[188:191], a[204:207], v[24:31], v77\nQk 33,2,3\nQmvva 80,36,144\nQe 5\nQmvva 80,28,148\n"
"Qe 6\nQmvva 84,36,152\nQe 7\nQmvva 84,28,156\nQk 34,4,5\nQk 35,6,7\nQmvva 88,36,160\nQe 8\n"
"Qmvva 88,28,164\nQe 9\nQmvva 92,36,168\nQe 10\nQmvva 92,28,172\nQe 11\nQmvva 124,40,168\nQk 38,8,9\n"
"Qk 39,10,11\nQmvva 124,32,172\nds_read_b32 v66, v74\nQmvva 120,40,160\nQe 12\nQmvva 120,32,164\nQe 13\nQmvva 116,40,152\n"
"Qe 14\nQmvva 116,32,156\nQe 15\nQmvva 112,40,144\ns_waitcnt lgkmcnt(0)\nv_cmp_gt_u32 vcc, v75, v66\nQmvva 112,32,148\ns_cbranch_vccnz .Lsa31x\n"
".Lra31x:\nQk 36,12,13\nQcvaa 0,112,48,56\ns_add_u32 m0, s96, 140288\ns_mov_b32 s95, 45056\nQk 37,14,15\nQmvaa 0,116,52\nbuffer_load_dwordx4 v63, s[52:55], s95 offen lds\n"
"Qmvaa 0,120,56\nQmvaa 0,124,60\nQcvaa 4,140,60,56\nQmvaa 4,136,56\ns_mov_b32 s95, 64512\nQmvaa 4,132,52\nbuffer_load_dwordx4 v63, s[52:55], s95 offen offset:1024 lds\nQmvaa 4,128,48\n"
"Qcvaa 8,128,64,56\nQmvaa 8,132,68\nQe 0\nQmvaa 8,136,72\ns_add_u32 m0, s96, 148480\ns_add_u32 s95, s87, 86016\nQe 1\nQmvaa 8,140,76\n"
"buffer_load_dwordx4 v61, s[52:55], s95 offen lds\nQcvaa 12,124,76,56\nQe 2\nQmvaa 12,120,72\nQe 3\nQmvaa 12,116,68\nQe 4\nQmvaa 12,112,64\n"
"Qe 5\nv_smfmac_f32_16x16x64_bf16 a[192:195], a[204:207], v[32:39], v77\ns_add_u32 s95, s87, 105472\nQk 40,0,1\nQmvva 144,44,144\nbuffer_load_dwordx4 v61, s[52:55], s95 offen offset:1024 lds\nQmvva 144,36,148\nQe 6\n"
"Qmvva 148,44,152\nQe 7\nQmvva 148,36,156\ns_add_u32 m0, s96, 65536\ns_add_u32 s95, s87, 126976\nQk 41,2,3\nQmvva 152,44,160\nbuffer_load_dwordx4 v61, s[52:55], s95 offen lds\n"
"Qmvva 152,36,164\nQk 42,4,5\nQk 43,6,7\nQmvva 156,44,168\nQe 8\nQmvva 156,36,172\nQe 9\nQmvva 188,48,168\n"
"s_add_u32 s95, s87, 146432\nQe 10\nQmvva 188,40,172\nbuffer_load_dwordx4 v61, s[52:55], s95 offen offset:1024 lds\nQmvva 184,48,160\nQe 11\nQmvva 184,40,164\nQk 46,8,9\n"
"Qk 47,10,11\nQmvva 180,48,152\nQe 12\nQmvva 180,40,156\ns_add_u32 m0, s96, 73728\ns_add_u32 s95, s87, 86016\nQe 13\nQmvva 176,48,144\n"
"buffer_load_dwordx4 v62, s[52:55], s95 offen lds\nQmvva 176,40,148\nQe 14\nQcvaa 0,112,80,56\nQe 15\nQmvaa 0,116,84\nQk 44,12,13\nQk 45,14,15\n"
"Qmvaa 0,120,88\ns_add_u32 s95, s87, 125952\nQmvaa 0,124,92\nbuffer_load_dwordx4 v62, s[52:55], s95 offen offset:1024 lds\nQcvaa 4,140,92,56\nQmvaa 4,136,88\nQmvaa 4,132,84\nQmvaa 4,128,80\n"
"s_add_u32 m0, s96, 81920\ns_add_u32 s95, s87, 86016\nQcvaa 8,128,96,56\nbuffer_load_dwordx4 v63, s[52:55], s95 offen lds\nQmvaa 8,132,100\nQmvaa 8,136,104\nQmvaa 8,140,108\nQcvaa 12,124,108,56\n"
"s_add_u32 s95, s87, 105472\nQmvaa 12,120,104\nbuffer_load_dwordx4 v63, s[52:55], s95 offen offset:1024 lds\nQmvaa 12,116,100\nQmvaa 12,112,96\nv_smfmac_f32_16x16x64_bf16 a[196:199], a[204:207], v[40:47], v77\nQmvva 208,16,144\nQta 112,71,33152\n"
"s_add_u32 m0, s96, 90112\ns_add_u32 s95, s87, 126976\nQe 0\nQmvva 208,44,148\nQta 114,71,34176\nbuffer_load_dwordx4 v63, s[52:55], s95 offen lds\nQe 1\nQmvva 212,16,152\n"
"Qta 116,71,33408\nQe 2\nQmvva 212,44,156\nQta 118,71,34432\nQe 3\nQmvva 216,16,160\nQta 120,71,33664\nQk 48,0,1\n"
"Qk 49,2,3\nQmvva 216,44,164\nQta 122,71,34688\ns_add_u32 s95, s87, 146432\nQe 4\nQmvva 220,16,168\nQta 124,71,33920\nbuffer_load_dwordx4 v63, s[52:55], s95 offen offset:1024 lds\n"
"Qe 5\nQmvva 220,44,172\nQta 126,71,34944\nQk 50,4,5\nQe 6\nQe 7\ns_nop 0\nQk 51,6,7\n"
"Qe 8\nQe 9\nQe 10\nQe 11\nQe 12\nQmvva 240,20,144\nQe 13\nQmvva 240,48,148\n"
"Qe 14\nQmvva 244,20,152\nQe 15\nQmvva 244,48,156\nQk 54,8,9\nQk 55,10,11\nQmvva 248,20,160\nQk 52,12,13\n"
"Qk 53,14,15\nQmvva 248,48,164\nQmvva 252,20,168\nQmvva 252,48,172\nv_smfmac_f32_16x16x64_bf16 a[200:203], a[204:207], v[48:55], v77\nQmava 224,24,144\nv_accvgpr_read_b32 v0, a176\nv_accvgpr_read_b32 v1, a177\n"
"Qmava 224,52,148\nv_accvgpr_read_b32 v6, a178\nv_accvgpr_read_b32 v7, a179\nQmava 228,24,152\nv_accvgpr_read_b32 v8, a180\nv_accvgpr_read_b32 v9, a181\nQmava 228,52,156\nv_accvgpr_read_b32 v14, a182\n"
"v_accvgpr_read_b32 v15, a183\nQmava 232,24,160\nv_accvgpr_read_b32 v16, a184\nv_accvgpr_read_b32 v17, a185\nQmava 232,52,164\nv_accvgpr_read_b32 v22, a186\nv_accvgpr_read_b32 v23, a187\nQmava 236,24,168\n"
"v_accvgpr_read_b32 v2, a190\nv_accvgpr_read_b32 v3, a191\nQmava 236,52,172\ns_waitcnt lgkmcnt(0)\nv_accvgpr_read_b32 v24, a188\nQmvva 96,28,112\nv_accvgpr_read_b32 v25, a189\nv_accvgpr_read_b32 v4, a192\n"
"Qmvva 100,28,116\nv_accvgpr_read_b32 v5, a193\nv_accvgpr_read_b32 v10, a194\nQmvva 104,28,120\nv_accvgpr_read_b32 v11, a195\nv_accvgpr_read_b32 v12, a196\nQmvva 108,28,124\nv_accvgpr_read_b32 v13, a197\n"
"v_accvgpr_read_b32 v18, a198\nQmvva 128,32,112\nv_accvgpr_read_b32 v19, a199\nv_accvgpr_read_b32 v20, a200\nQmvva 132,32,116\nv_accvgpr_read_b32 v21, a201\nv_accvgpr_read_b32 v26, a202\nQmvva 136,32,120\n"
"v_accvgpr_read_b32 v27, a203\nQa 8,8,9\nQmvva 140,32,124\nQa 10,10,11\nQa 8,8,10\nQmvva 160,36,112\nQa 16,16,17\nQa 18,18,19\n"
"Qmvva 164,36,116\nQa 16,16,18\nQa 4,4,5\nQmvva 168,36,120\nQa 6,6,7\nQa 4,4,6\nQmvva 172,36,124\nQa 12,12,13\n"
"Qa 14,14,15\nQmvva 192,40,112\nQa 12,12,14\nQa 0,0,1\nQmvva 196,40,116\nQa 2,2,3\nQa 0,0,2\nQmvva 200,40,120\n"
"Qa 24,24,25\nQa 26,26,27\nQmvva 204,40,124\nQa 24,24,26\nQa 20,20,21\nQmvva 224,44,112\nQa 22,22,23\nQa 20,20,22\n"
"Qmvva 228,44,116\nv_max3_u32 v9, v8, v16, v4\nv_max3_u32 v9, v9, v12, v0\nQmvva 232,44,120\nv_max3_u32 v9, v9, v24, v20\nv_min3_u32 v11, v8, v16, v4\nQmvva 236,44,124\nv_min3_u32 v11, v11, v12, v0\n"
"v_min3_u32 v11, v11, v24, v20\nQmava 208,48,112\nv_cmp_lt_u32 vcc, 0x4e800000, v9\ns_mov_b64 s[74:75], vcc\nQmava 212,48,116\nv_cmp_gt_u32 vcc, 0x35800000, v11\ns_or_b64 s[74:75], s[74:75], vcc\nQmava 216,48,120\n"
"v_mov_b32 v30, s73\nv_mov_b32 v31, 0\nQmava 220,48,124\nv_mov_b32 v58, 0xb9800000\nv_mov_b32 v59, 0\nQmava 240,52,112\nv_and_b32 v66, 16, v60\nv_mov_b32 v67, 0xffff\n"
"Qmava 244,52,116\nv_lshlrev_b32 v66, v66, v67\ns_mov_b32 vcc_lo, 0x00c0000c\nQmava 248,52,120\ns_mov_b32 vcc_hi, 0xc0000c00\nv_cndmask_b32 v67, 0, v66, vcc\nQmava 252,52,124\ns_lshr_b64 vcc, vcc, 2\n"
"v_cndmask_b32 v66, 0, v66, vcc\ns_lshl_b32 s68, s93, 12\ns_add_u32 s68, s68, %4\nv_lshrrev_b32 v69, 9, v60\nv_lshlrev_b32 v69, 11, v69\nv_bfe_u32 v70, v60, 1, 8\nv_add3_u32 v68, v69, v70, s68\nv_add_u32 v69, 65536, v68\n"
"v_add_u32 v70, 131072, v68\ns_cmp_lg_u64 s[74:75], 0\ns_cbranch_scc1 .Lfb\n.Lfbret1:\nv_rcp_f32 v2, v0\nv_rcp_f32_e64 v6, v4\nv_mfma_f32_16x16x4_4b_bf16 v[80:95], v[30:31], v[58:59], v[80:95]\nv_rcp_f32 v10, v8\n"
"v_rcp_f32 v14, v12\nv_rcp_f32 v18, v16\nv_rcp_f32 v22, v20\nv_rcp_f32 v26, v24\nv_mov_b64 v[0:1], 0\nv_mfma_f32_16x16x4_4b_bf16 v[112:127], v[30:31], v[58:59], v[112:127]\nQk 2,2,2\nv_and_b32 v3, v2, v67\n"
"v_and_b32 v2, v2, v66\nv_mov_b64_e64 v[4:5], 0\nQk 6,6,6\nv_and_b32_e64 v7, v6, v67\nv_mfma_f32_16x16x4_4b_bf16 v[144:159], v[30:31], v[58:59], v[144:159]\nv_and_b32 v6, v6, v66\nv_mov_b64 v[8:9], 0\nQk 10,10,10\n"
"v_and_b32 v11, v10, v67\nv_and_b32 v10, v10, v66\nv_mov_b64_e64 v[12:13], 0\nv_mfma_f32_16x16x4_4b_bf16 v[176:191], v[30:31], v[58:59], v[176:191]\nQk 14,14,14\nv_and_b32 v15, v14, v67\nv_and_b32 v14, v14, v66\nv_mov_b64_e64 v[16:17], 0\n"
"Qk 18,18,18\nv_and_b32_e64 v19, v18, v67\nv_mfma_f32_16x16x4_4b_bf16 v[208:223], v[30:31], v[58:59], v[208:223]\nv_and_b32 v18, v18, v66\nv_mov_b64 v[20:21], 0\nQk 22,22,22\nv_and_b32 v23, v22, v67\nv_and_b32 v22, v22, v66\n"
"v_mov_b64_e64 v[24:25], 0\nv_mfma_f32_16x16x4_4b_bf16 v[240:255], v[30:31], v[58:59], v[240:255]\nQk 26,26,26\nv_and_b32 v27, v26, v67\nv_and_b32 v26, v26, v66\nv_mov_b64_e64 v[28:29], 0\nQk 80,80,81\nQk 81,82,83\n"
"Qk 82,84,85\nQk 83,86,87\nQk 112,112,113\nQk 113,114,115\nQzvvv 84,80,0\nQk 114,116,117\nQk 115,118,119\nQzvvv 80,80,2\n"
"Qk 144,144,145\nQk 145,146,147\nQzvvv 116,112,4\nQk 146,148,149\nQk 147,150,151\nQzvvv 112,112,6\nQk 176,176,177\nQk 177,178,179\n"
"v_mfma_f32_16x16x4_4b_bf16 a[224:239], v[30:31], v[58:59], a[224:239]\nQk 80,80,81\nQk 81,82,83\nQk 82,84,85\nQk 83,86,87\nQk 178,180,181\nQk 179,182,183\nQzvvv 148,144,8\n"
"Qk 112,112,113\nQk 113,114,115\nQzvvv 144,144,10\nQk 114,116,117\nQk 115,118,119\nv_mfma_f32_16x16x4_4b_bf16 v[96:111], v[30:31], v[58:59], v[96:111]\nQk 208,208,209\nQk 209,210,211\n"
"Qk 210,212,213\nQk 211,214,215\nQk 240,240,241\nQk 144,144,145\nQzvvv 180,176,12\nQk 145,146,147\nQk 146,148,149\nQzvvv 176,176,14\n"
"Qk 147,150,151\nQk 241,242,243\nv_mfma_f32_16x16x4_4b_bf16 v[128:143], v[30:31], v[58:59], v[128:143]\nQk 242,244,245\nQk 243,246,247\nv_accvgpr_read_b32 v84, a224\nv_accvgpr_read_b32 v85, a225\nv_accvgpr_read_b32 v86, a226\n"
"Qk 176,176,177\nQzvvv 212,208,16\nQk 177,178,179\nQk 178,180,181\nQzvvv 208,208,18\nQk 179,182,183\nv_accvgpr_read_b32 v87, a227\nv_mfma_f32_16x16x4_4b_bf16 v[160:175], v[30:31], v[58:59], v[160:175]\n"
"v_accvgpr_read_b32 v32, a228\nv_accvgpr_read_b32 v33, a229\nv_accvgpr_read_b32 v34, a230\nv_accvgpr_read_b32 v35, a231\nQk 84,84,85\nQk 85,86,87\nQzvvv 244,240,20\nQk 86,32,33\n"
"Qk 87,34,35\nQzvvv 240,240,22\nQk 208,208,209\nQk 209,210,211\nv_mfma_f32_16x16x4_4b_bf16 v[192:207], v[30:31], v[58:59], v[192:207]\nQk 210,212,213\nQk 211,214,215\nQk 88,88,89\n"
"Qk 89,90,91\nQk 90,92,93\nQk 91,94,95\nQzvvv 32,84,24\nQk 240,240,241\nQk 241,242,243\nQzvvv 84,84,26\nQk 242,244,245\n"
"Qk 243,246,247\nv_mfma_f32_16x16x4_4b_bf16 v[224:239], v[30:31], v[58:59], v[224:239]\nQk 120,120,121\nQk 121,122,123\nQk 122,124,125\nQk 123,126,127\nQk 152,152,153\nQk 84,84,85\n"
"Qzvvv 92,88,0\nQk 85,86,87\nQk 86,32,33\nQzvvv 88,88,2\nQk 87,34,35\nQk 153,154,155\nv_mfma_f32_16x16x4_4b_bf16 a[208:223], v[30:31], v[58:59], a[208:223]\nQk 154,156,157\n"
"Qk 155,158,159\ns_waitcnt vmcnt(20)\ns_barrier\nQk 184,184,185\nQk 88,88,89\nQzvvv 124,120,4\nQk 89,90,91\nQk 90,92,93\n"
"Qzvvv 120,120,6\nQk 91,94,95\n.Lpg13:\ns_cbranch_execz .Ltr6\ns_nop 0\nQk 185,186,187\nv_mfma_f32_16x16x4_4b_bf16 a[240:255], v[30:31], v[58:59], a[240:255]\nQk 186,188,189\n"
"Qk 187,190,191\nQta 56,68,0\nQk 216,216,217\nQk 217,218,219\nQk 120,120,121\nQzvvv 156,152,8\nQk 121,122,123\nQk 122,124,125\n"
"Qzvvv 152,152,10\nQk 123,126,127\nQk 218,220,221\nQzvvv 188,184,12\nQk 219,222,223\nQta 58,68,1024\nQzvvv 184,184,14\nQk 248,248,249\n"
"Qk 249,250,251\nQk 152,152,153\nQk 153,154,155\nQzvvv 220,216,16\nQk 154,156,157\nQk 155,158,159\nQzvvv 216,216,18\nQk 250,252,253\n"
"Qk 251,254,255\nQk 184,184,185\nQk 185,186,187\nQzvvv 252,248,20\nQk 186,188,189\nQk 187,190,191\nQzvvv 248,248,22\nQta 60,68,8192\n"
"v_accvgpr_read_b32 v116, a232\nv_accvgpr_read_b32 v117, a233\nv_accvgpr_read_b32 v118, a234\nv_accvgpr_read_b32 v119, a235\nv_accvgpr_read_b32 v36, a236\nv_accvgpr_read_b32 v37, a237\nv_accvgpr_read_b32 v38, a238\nv_accvgpr_read_b32 v39, a239\n"
"Qk 116,116,117\nQk 117,118,119\nQk 118,36,37\nQk 119,38,39\nQk 216,216,217\nQk 217,218,219\nQzvvv 36,116,24\nQk 218,220,221\n"
"Qk 219,222,223\nQzvvv 116,116,26\nQta 62,68,9216\ns_mov_b32 s31, 0\ns_nop 0\nQk 96,96,97\nQk 97,98,99\nQk 98,100,101\n"
"Qk 99,102,103\nQk 248,248,249\nQk 249,250,251\nQzvvv 100,96,0\nQk 250,252,253\nQk 251,254,255\nQzvvv 96,96,2\nbuffer_load_dwordx4 a[0:3], v79, s[48:51], s31 offen offset:0\n"
"Qta 64,68,256\nQk 128,128,129\nQk 129,130,131\nQk 130,132,133\nQk 131,134,135\nQk 116,116,117\nQk 117,118,119\nQzvvv 132,128,4\n"
"Qk 118,36,37\nQk 119,38,39\nQzvvv 128,128,6\nbuffer_load_dwordx4 a[4:7], v79, s[48:51], s31 offen offset:1024\nQta 66,68,1280\ns_waitcnt lgkmcnt(2)\ns_nop 0\nQzvav 56,56,80\n"
"Qk 160,160,161\nQk 161,162,163\nQmvav 56,60,88\nQk 162,164,165\nQk 163,166,167\nbuffer_load_dwordx4 a[8:11], v79, s[48:51], s31 offen offset:2048\nQta 68,68,8448\nQzvvv 164,160,8\n"
"Qk 192,192,193\nQk 193,194,195\nQzvvv 160,160,10\nQk 194,196,197\nQk 195,198,199\nbuffer_load_dwordx4 a[12:15], v79, s[48:51], s31 offen offset:3072\nQta 70,68,9472\nQzvvv 196,192,12\n"
"v_max3_f32 v66, |v56|, |v57|, |v58|\nv_max_f32 v66, v66, |v59|\nQzvvv 192,192,14\nv_mov_b32_e64 v67, v66\ns_mov_b32 s31, 4096\nQk 224,224,225\nQk 225,226,227\nQk 226,228,229\n"
"Qk 227,230,231\nbuffer_load_dwordx4 a[16:19], v79, s[48:51], s31 offen offset:0\nv_permlane32_swap_b32_e64 v66, v67\nQzvvv 228,224,16\nv_max_f32 v66, v66, v67\nv_mov_b32 v67, v66\nQzvvv 224,224,18\nv_accvgpr_read_b32 v148, a208\n"
"v_accvgpr_read_b32 v149, a209\nv_accvgpr_read_b32 v150, a210\nv_accvgpr_read_b32 v151, a211\nv_accvgpr_read_b32 v40, a212\nv_accvgpr_read_b32 v41, a213\nv_accvgpr_read_b32 v42, a214\nv_accvgpr_read_b32 v43, a215\nQk 148,148,149\n"
"Qk 149,150,151\nQk 150,40,41\nQk 151,42,43\nbuffer_load_dwordx4 a[20:23], v79, s[48:51], s31 offen offset:1024\nv_permlane16_swap_b32_e64 v66, v67\nQzvvv 40,148,20\nv_max_f32_e64 v66, v66, v67\nv_accvgpr_read_b32 v180, a240\n"
"Qzvvv 148,148,22\nv_accvgpr_read_b32 v181, a241\nv_accvgpr_read_b32 v182, a242\nv_accvgpr_read_b32 v183, a243\nv_accvgpr_read_b32 v44, a244\nv_accvgpr_read_b32 v45, a245\nv_accvgpr_read_b32 v46, a246\nv_accvgpr_read_b32 v47, a247\n"
"Qk 180,180,181\nQk 181,182,183\nQk 182,44,45\nQk 183,46,47\nbuffer_load_dwordx4 a[24:27], v79, s[48:51], s31 offen offset:2048\nv_max_f32_dpp v66, v66, v66 row_ror:8 row_mask:0xf bank_mask:0xf\nQzvvv 44,180,24\ns_mov_b32 s31, 16384\n"
"Qk 104,104,105\nQzvvv 180,180,26\nQk 105,106,107\nQk 106,108,109\nQk 107,110,111\nbuffer_load_dwordx4 a[28:31], v79, s[48:51], s31 offen offset:0\nv_max_f32_dpp v66, v66, v66 row_ror:4 row_mask:0xf bank_mask:0xf\nQzvvv 108,104,0\n"
"Qk 136,136,137\nQk 137,138,139\nQzvvv 104,104,2\nQk 138,140,141\nQk 139,142,143\nbuffer_load_dwordx4 a[32:35], v79, s[48:51], s31 offen offset:1024\nv_max_f32_dpp v66, v66, v66 row_ror:2 row_mask:0xf bank_mask:0xf\nQzvvv 140,136,4\n"
"Qk 168,168,169\nQk 169,170,171\nQzvvv 136,136,6\nQk 170,172,173\nQk 171,174,175\nbuffer_load_dwordx4 a[36:39], v79, s[48:51], s31 offen offset:2048\nv_max_f32_dpp v66, v66, v66 row_ror:1 row_mask:0xf bank_mask:0xf\nQzvvv 172,168,8\n"
"v_and_b32 v66, 0x7f800000, v66\nv_add_u32 v66, 0x3000000, v66\nQzvvv 168,168,10\nQk 200,200,201\nQk 201,202,203\nQk 202,204,205\nQk 203,206,207\nbuffer_load_dwordx4 a[40:43], v79, s[48:51], s31 offen offset:3072\n"
"v_min_u32 v66, 0x7e000000, v66\nQzvvv 204,200,12\nv_or_b32 v66, 0x400000, v66\nds_write_b32 v73, v66 offset:16\nQzvvv 200,200,14\nv_xor_b32 v67, 0x80000000, v66\ns_mov_b32 s31, 20480\nQk 232,232,233\n"
"Qk 233,234,235\nQk 234,236,237\nQk 235,238,239\nbuffer_load_dwordx4 a[44:47], v79, s[48:51], s31 offen offset:0\nv_accvgpr_write_b32 a112, v67\nQzvvv 236,232,16\nv_accvgpr_write_b32 a113, v67\nv_accvgpr_write_b32 a114, v67\n"
"Qzvvv 232,232,18\nv_accvgpr_write_b32 a115, v67\nv_accvgpr_read_b32 v212, a216\nv_accvgpr_read_b32 v213, a217\nv_accvgpr_read_b32 v214, a218\nv_accvgpr_read_b32 v215, a219\nv_accvgpr_read_b32 v48, a220\nv_accvgpr_read_b32 v49, a221\n"
"v_accvgpr_read_b32 v50, a222\nv_accvgpr_read_b32 v51, a223\nQk 212,212,213\nQk 213,214,215\nQk 214,48,49\nQk 215,50,51\nbuffer_load_dwordx4 a[48:51], v79, s[48:51], s31 offen offset:1024\nv_accvgpr_read_b32 v244, a248\n"
"Qzvvv 48,212,20\nv_accvgpr_read_b32 v245, a249\nv_accvgpr_read_b32 v246, a250\nQzvvv 212,212,22\nv_accvgpr_read_b32 v247, a251\nv_accvgpr_read_b32 v52, a252\nv_accvgpr_read_b32 v53, a253\nv_accvgpr_read_b32 v54, a254\n"
"v_accvgpr_read_b32 v55, a255\nQk 244,244,245\nQk 245,246,247\nQk 246,52,53\nQk 247,54,55\nbuffer_load_dwordx4 a[52:55], v79, s[48:51], s31 offen offset:2048\ns_nop 0\ns_nop 0\n"
"Qzvvv 52,244,24\nQzvvv 244,244,26\ns_waitcnt lgkmcnt(5)\nQcaav 140,56,84,112\ns_add_u32 m0, s96, 98304\ns_mov_b32 s95, 86016\nQmaav 140,60,116\nbuffer_load_dwordx4 v61, s[52:55], s95 offen lds\n"
"Qcaav 136,56,240,112\ns_mov_b32 s56, s20\ns_and_b32 s57, s21, 0xffff\nQmaav 136,60,248\ns_mov_b32 s58, 4194304\nQk 244,244,245\nQcaav 132,56,208,112\nQta 72,68,16384\n"
"Qk 245,246,247\nQmaav 132,60,216\ns_mov_b32 s95, 105472\nQk 246,52,53\nQcaav 128,56,176,112\nbuffer_load_dwordx4 v61, s[52:55], s95 offen offset:1024 lds\nQmaav 128,60,184\nQta 74,68,16896\n"
"Qk 247,54,55\nQcaav 124,56,144,112\nQk 212,212,213\nQk 213,214,215\nQmaav 124,60,152\nQk 214,48,49\nQk 215,50,51\nQcaav 120,56,112,112\n"
"Qta 76,68,17408\nQk 180,180,181\nQmaav 120,60,120\ns_add_u32 s88, s71, 0\nQk 181,182,183\nQcaav 116,56,80,112\nbuffer_store_dwordx4 v[80:83], v79, s[56:59], s88 offen offset:0 sc1\nQmaav 116,60,88\n"
"Qta 78,68,17920\nQk 182,44,45\ns_waitcnt lgkmcnt(5)\nQcaav 144,64,80,112\nQk 183,46,47\nQk 148,148,149\nQmaav 144,68,88\nQk 149,150,151\n"
"Qk 150,40,41\nQcaav 148,64,112,112\nQk 151,42,43\nQk 96,96,97\nQmaav 148,68,120\nQk 97,98,99\nQk 98,100,101\nQcaav 152,64,144,112\n"
"Qta 56,68,24576\nQk 99,102,103\nQmaav 152,68,152\ns_add_u32 m0, s96, 106496\ns_mov_b32 s95, 126976\nQcaav 156,64,176,112\nbuffer_load_dwordx4 v61, s[52:55], s95 offen lds\nQmaav 156,68,184\n"
"Qta 58,68,25600\nQk 168,168,169\nQcaav 160,64,208,112\nQk 169,170,171\nQk 170,172,173\nQmaav 160,68,216\nQk 171,174,175\nQk 136,136,137\n"
"Qcaav 164,64,240,112\nQta 60,68,32768\ns_mov_b32 s95, 146432\nQmaav 164,68,248\nbuffer_load_dwordx4 v61, s[52:55], s95 offen offset:1024 lds\nQcaav 168,64,84,112\nQk 137,138,139\nQk 138,140,141\n"
"Qmaav 168,68,116\nQta 62,68,33792\nQk 139,142,143\ns_waitcnt lgkmcnt(4)\nQcaav 196,72,84,112\nQk 104,104,105\nQk 105,106,107\nQmaav 196,76,116\n"
"s_add_u32 m0, s96, 114688\ns_mov_b32 s95, 86016\nQcaav 192,72,240,112\nbuffer_load_dwordx4 v62, s[52:55], s95 offen lds\nQmaav 192,76,248\nQk 106,108,109\nQk 107,110,111\nQcaav 188,72,208,112\n"
"Qta 64,68,24832\nQk 224,224,225\nQmaav 188,76,216\nbuffer_store_dwordx4 v[112:115], v79, s[56:59], s88 offen offset:1024 sc1\nQcaav 184,72,176,112\nQk 225,226,227\nQk 226,228,229\nQmaav 184,76,184\n"
"Qta 66,68,25856\nQk 227,230,231\nQcaav 180,72,144,112\nQk 192,192,193\nQk 193,194,195\nQmaav 180,76,152\nQk 194,196,197\nQk 195,198,199\n"
"Qcaav 176,72,112,112\nQta 68,68,33024\nv_add_u32 v75, 1, v75\nQmaav 176,76,120\ns_waitcnt vmcnt(35)\nds_write_b32 v73, v75\nQcaav 172,72,80,112\nQk 160,160,161\n"
"Qk 161,162,163\nQmaav 172,76,88\nQta 70,68,34048\ns_mov_b32 s95, 125952\ns_waitcnt lgkmcnt(5)\nQcaav 200,56,80,112\nbuffer_load_dwordx4 v62, s[52:55], s95 offen offset:1024 lds\nQmaav 200,60,88\n"
"Qk 162,164,165\nQk 163,166,167\nQcaav 204,56,112,112\nQk 128,128,129\nQk 129,130,131\nQmaav 204,60,120\nQk 130,132,133\nQk 131,134,135\n"
"Qcaav 208,56,144,112\nQta 72,68,512\nQk 200,200,201\nQmaav 208,60,152\nds_read_b32 v66, v74\ns_add_u32 m0, s96, 122880\ns_mov_b32 s95, 86016\nQcaav 212,56,176,112\n"
"buffer_load_dwordx4 v63, s[52:55], s95 offen lds\nQmaav 212,60,184\nQta 74,68,1536\nQk 201,202,203\nQcaav 216,56,208,112\nQk 202,204,205\nQk 203,206,207\nQmaav 216,60,216\n"
"s_waitcnt lgkmcnt(1)\nv_cmp_gt_u32 vcc, v75, v66\nQcaav 220,56,240,112\ns_cbranch_vccnz .Lso40\n.Lro40:\nQk 232,232,233\nQmaav 220,60,248\nQta 76,68,8704\n"
"Qk 233,234,235\nQcaav 224,56,84,112\nbuffer_store_dwordx4 v[144:147], v79, s[56:59], s88 offen offset:2048 sc1\nv_xor_b32 v56, 8, v73\nQmaav 224,60,116\nds_read_b32 v56, v56 offset:16\nQk 234,236,237\ns_waitcnt lgkmcnt(5)\n"
"Qcaav 252,64,84,112\nQta 78,68,9728\nQk 235,238,239\nQmaav 252,68,116\nQcaav 248,64,240,112\nQmaav 248,68,248\nQcaav 244,64,208,112\nQta 56,68,768\n"
"Qmaav 244,68,216\ns_mov_b32 s95, 105472\nQcaav 240,64,176,112\nbuffer_load_dwordx4 v63, s[52:55], s95 offen offset:1024 lds\nQmaav 240,68,184\nQta 58,68,1792\ns_waitcnt lgkmcnt(3)\nv_mov_b32 v57, v56\n"
"Qcaav 236,64,144,112\nv_mov_b32 v58, v56\nv_mov_b32 v59, v56\nQmaav 236,68,152\nv_accvgpr_write_b32 a80, v56\nv_accvgpr_write_b32 a81, v56\nQcaav 232,64,112,112\nQta 60,68,8960\n"
"v_accvgpr_write_b32 a82, v56\nQmaav 232,68,120\nv_accvgpr_write_b32 a83, v56\nQcaav 228,64,80,112\nQmaav 228,68,88\nQta 62,68,9984\ns_waitcnt lgkmcnt(4)\nQcvav 0,72,80,56\n"
"Qmvav 0,76,88\nQcvav 4,72,112,56\nbuffer_store_dwordx4 v[176:179], v79, s[56:59], s88 offen offset:3072 sc1\nQmvav 4,76,120\nQcvav 8,72,144,56\nQta 64,68,16640\nQmvav 8,76,152\nQcvav 12,72,176,56\n"
"Qmvav 12,76,184\nQta 66,68,17152\nQcvav 16,72,208,56\nQmvav 16,76,216\nQcvav 20,72,240,56\nQta 68,68,17664\nQmvav 20,76,248\nQcvav 24,72,84,56\n"
"Qmvav 24,76,116\nQta 70,68,18176\ns_waitcnt lgkmcnt(4)\nQcvav 52,56,84,56\nQmvav 52,60,116\nQcvav 48,56,240,56\nQmvav 48,60,248\nQcvav 44,56,208,56\n"
"Qta 72,68,25088\nQmvav 44,60,216\nQcvav 40,56,176,56\nQmvav 40,60,184\nQta 74,68,26112\ns_add_u32 s88, s71, 4096\nQcvav 36,56,144,56\nbuffer_store_dwordx4 v[208:211], v79, s[56:59], s88 offen offset:0 sc1\n"
"Qmvav 36,60,152\nQcvav 32,56,112,56\nQta 76,68,33280\nQmvav 32,60,120\nQcvav 28,56,80,56\nQmvav 28,60,88\nQta 78,68,34304\ns_waitcnt lgkmcnt(4)\n"
"Qcvav 92,64,80,56\nQmvav 92,68,88\nQcvav 124,64,112,56\ns_lshl_b32 s46, s19, 4\nQmvav 124,68,120\ns_cmp_eq_u32 s70, 0\ns_cselect_b32 s46, s46, -1\nQcvav 156,64,144,56\n"
"Qta 56,68,25344\nv_mov_b32 v77, 0x7ffff000\nQmvav 156,68,152\nQcvav 188,64,176,56\nQmvav 188,68,184\nQta 58,68,26368\nQcvav 220,64,208,56\nQmvav 220,68,216\n"
"Qcvav 252,64,240,56\nQta 60,68,33536\nQmvav 252,68,248\nQcvav 100,64,84,56\nQmvav 100,68,116\nQta 62,68,34560\ns_waitcnt lgkmcnt(4)\nQcvav 172,72,84,56\n"
"buffer_store_dwordx4 v[240:243], v79, s[56:59], s88 offen offset:1024 sc1\nQmvav 172,76,116\nQcvav 140,72,240,56\nQmvav 140,76,248\nQcvav 108,72,208,56\nQta 64,68,40960\nQmvav 108,76,216\nQcvav 228,72,176,56\n"
"Qmvav 228,76,184\nQta 66,68,41984\nQcvav 196,72,144,56\nQmvav 196,76,152\nQcvav 164,72,112,56\nQta 68,68,49152\nQmvav 164,76,120\nQcvav 132,72,80,56\n"
"Qmvav 132,76,88\nQta 70,68,50176\ns_waitcnt lgkmcnt(4)\nQcvav 204,56,80,56\nQmvav 204,60,88\nQcvav 236,56,112,56\nQmvav 236,60,120\nQcaav 96,56,144,80\n"
"Qta 72,68,41216\nQmaav 96,60,152\nQcaav 100,56,176,80\nbuffer_store_dwordx4 v[84:87], v79, s[56:59], s88 offen offset:2048 sc1\nQmaav 100,60,184\nQta 74,68,42240\nQcaav 104,56,208,80\nQmaav 104,60,216\n"
"Qcaav 108,56,240,80\nQta 76,68,49408\nQmaav 108,60,248\nQcaav 112,56,84,80\nQmaav 112,60,116\nQta 78,68,50432\n.Lpg14:\ns_cbranch_execz .Ltr6\n"
"s_waitcnt vmcnt(15)\ns_waitcnt lgkmcnt(4)\nQmaaa 140,64,24\nQmaaa 140,68,52\nQmaaa 136,64,20\nQmaaa 136,68,48\nQmaaa 132,64,16\nQta 56,68,41472\n"
"Qmaaa 132,68,44\nQmaaa 128,64,12\nQmaaa 128,68,40\nQta 58,68,42496\nQmaaa 124,64,8\nQmaaa 124,68,36\nQmaaa 120,64,4\nQta 60,68,49664\n"
"s_and_b32 s68, s19, 3\nQmaaa 120,68,32\ns_add_u32 s88, s71, 16384\nQmaaa 116,64,0\nbuffer_store_dwordx4 v[88:91], v79, s[56:59], s88 offen offset:0 sc1\nQmaaa 116,68,28\nQta 62,68,50688\ns_waitcnt lgkmcnt(4)\n"
"Qmaaa 144,72,0\ns_cmp_lt_u32 s68, 1\ns_cselect_b32 s68, 40, 32\nQmaaa 144,76,28\ns_mul_i32 s67, s68, 40960\ns_mul_i32 s74, s68, 32768\nQmaaa 148,72,4\ns_and_b32 s68, s19, 3\n"
"Qmaaa 148,76,32\ns_cmp_lt_u32 s68, 2\ns_cselect_b32 s68, 48, 40\nQmaaa 152,72,8\nQta 64,68,41728\ns_mul_i32 s72, s68, 40960\nQmaaa 152,76,36\ns_mul_i32 s75, s68, 32768\n"
"s_and_b32 s68, s19, 3\nQmaaa 156,72,12\ns_cmp_lt_u32 s68, 3\ns_cselect_b32 s68, 56, 48\nQmaaa 156,76,40\nQta 66,68,42752\ns_mul_i32 s73, s68, 40960\nQmaaa 160,72,16\n"
"s_mul_i32 s77, s68, 32768\nv_add_u32 v58, s91, v60\nQmaaa 160,76,44\ns_mul_i32 s68, s93, 28672\ns_sub_u32 s68, 14336, s68\nQmaaa 164,72,20\nQta 68,68,49920\nv_add_u32 v59, s68, v58\n"
"Qmaaa 164,76,48\nQmaaa 168,72,24\nQmaaa 168,76,52\nQta 70,68,50944\ns_waitcnt lgkmcnt(4)\nQmvaa 24,56,24\nQmvaa 24,60,52\nQmvaa 20,56,20\n"
"Qmvaa 20,60,48\nQmvaa 16,56,16\nQta 72,68,57344\nQmvaa 16,60,44\nbuffer_store_dwordx4 v[120:123], v79, s[56:59], s88 offen offset:1024 sc1\nQmvaa 12,56,12\nQmvaa 12,60,40\nQta 74,68,57856\n"
"Qmvaa 8,56,8\nQmvaa 8,60,36\nQmvaa 4,56,4\nQta 76,68,58368\nv_add_u32 v75, 1, v75\nQmvaa 4,60,32\ns_waitcnt vmcnt(35)\nds_write_b32 v73, v75\n"
"Qmvaa 0,56,0\nQmvaa 0,60,28\nQta 78,68,58880\ns_waitcnt lgkmcnt(5)\nQmvaa 28,64,0\nQmvaa 28,68,28\nQmvaa 32,64,4\nQmvaa 32,68,32\n"
"Qmvaa 36,64,8\nQta 56,68,57600\nQmvaa 36,68,36\nds_read_b32 v66, v74\nQmvaa 40,64,12\nQmvaa 40,68,40\nQta 58,68,58112\nQmvaa 44,64,16\n"
"Qmvaa 44,68,44\ns_waitcnt lgkmcnt(1)\nv_cmp_gt_u32 vcc, v75, v66\nQmvaa 48,64,20\ns_cbranch_vccnz .Lso180\n.Lro180:\nQmvaa 48,68,48\nQta 60,68,58624\n"
"Qmvaa 52,64,24\nbuffer_store_dwordx4 v[152:155], v79, s[56:59], s88 offen offset:2048 sc1\nQmvaa 52,68,52\ns_waitcnt lgkmcnt(4)\nQmaaa 196,72,24\nQta 62,68,59136\nQmaaa 196,76,52\nQmaaa 192,72,20\n"
"Qmaaa 192,76,48\nQmaaa 188,72,16\nQta 64,70,1024\nQmaaa 188,76,44\ns_add_u32 m0, s96, 0\ns_mov_b32 s95, 126976\nQmaaa 184,72,12\nbuffer_load_dwordx4 v63, s[52:55], s95 offen lds\n"
"Qmaaa 184,76,40\nQta 66,70,2048\nQmaaa 180,72,8\nQmaaa 180,76,36\nQmaaa 176,72,4\nQta 68,70,9216\ns_mov_b32 s95, 146432\nQmaaa 176,76,32\n"
"buffer_load_dwordx4 v63, s[52:55], s95 offen offset:1024 lds\nQmaaa 172,72,0\nQmaaa 172,76,28\nQta 70,70,10240\ns_waitcnt lgkmcnt(4)\nQmvaa 92,56,0\nQmvaa 92,60,28\nQmvaa 124,56,4\n"
"buffer_store_dwordx4 v[184:187], v79, s[56:59], s88 offen offset:3072 sc1\nQmvaa 124,60,32\nQmvaa 156,56,8\nQta 72,70,1280\nQmvaa 156,60,36\nQmvaa 188,56,12\nQmvaa 188,60,40\nQta 74,70,2304\n"
"Qmvaa 220,56,16\nQmvaa 220,60,44\nQmvaa 252,56,20\nQta 76,70,9472\ns_add_u32 m0, s96, 8192\ns_mov_b32 s95, 331776\nQmvaa 252,60,48\nbuffer_load_dwordx4 v61, s[52:55], s95 offen lds\n"
"Qmvaa 100,56,24\nQmvaa 100,60,52\nQta 78,70,10496\ns_waitcnt lgkmcnt(4)\nQmaaa 224,64,24\nQmaaa 224,68,52\ns_mov_b32 s95, 351232\nQmaaa 220,64,20\n"
"buffer_load_dwordx4 v61, s[52:55], s95 offen offset:1024 lds\nQmaaa 220,68,48\nQmaaa 216,64,16\nQta 56,70,1536\nQmaaa 216,68,44\nQmaaa 212,64,12\nQmaaa 212,68,40\nQta 58,70,2560\n"
"s_add_u32 s88, s71, 20480\nQmaaa 208,64,8\nbuffer_store_dwordx4 v[216:219], v79, s[56:59], s88 offen offset:0 sc1\nQmaaa 208,68,36\nQmaaa 204,64,4\nQta 60,70,9728\nQmaaa 204,68,32\nQmaaa 200,64,0\n"
"Qmaaa 200,68,28\nQta 62,70,10752\ns_waitcnt lgkmcnt(4)\nQmaaa 228,72,0\nQmaaa 228,76,28\ns_add_u32 m0, s96, 16384\ns_mov_b32 s95, 372736\nQmaaa 232,72,4\n"
"buffer_load_dwordx4 v61, s[52:55], s95 offen lds\nQmaaa 232,76,32\nQmaaa 236,72,8\nQta 64,70,1792\nQmaaa 236,76,36\nQmaaa 240,72,12\nQmaaa 240,76,40\nQta 66,70,2816\n"
"s_mov_b32 s95, 392192\nQmaaa 244,72,16\nbuffer_load_dwordx4 v61, s[52:55], s95 offen offset:1024 lds\nQmaaa 244,76,44\nQmaaa 248,72,20\nQta 68,70,9984\nQmaaa 248,76,48\nQmaaa 252,72,24\n"
"Qmaaa 252,76,52\nQta 70,70,11008\ns_waitcnt lgkmcnt(4)\nQmvaa 172,56,24\nbuffer_store_dwordx4 v[248:251], v79, s[56:59], s88 offen offset:1024 sc1\nQmvaa 172,60,52\nQmvaa 140,56,20\nQmvaa 140,60,48\n"
"Qmvaa 108,56,16\nQta 72,70,17408\nQmvaa 108,60,44\nQmvaa 228,56,12\nQmvaa 228,60,40\nQta 74,70,18432\ns_add_u32 m0, s96, 24576\ns_mov_b32 s95, 331776\n"
"Qmvaa 196,56,8\nbuffer_load_dwordx4 v62, s[52:55], s95 offen lds\nQmvaa 196,60,36\nQmvaa 164,56,4\nQta 76,69,0\nQmvaa 164,60,32\nQmvaa 132,56,0\nQmvaa 132,60,28\n"
"Qta 78,69,1024\ns_mov_b32 s95, 371712\ns_waitcnt lgkmcnt(4)\nQmvaa 204,64,0\nbuffer_load_dwordx4 v62, s[52:55], s95 offen offset:1024 lds\nQmvaa 204,68,28\nQmvaa 236,64,4\nQmvaa 236,68,32\n"
"Qmaaa 96,64,8\nQta 56,70,17664\nQmaaa 96,68,36\nQmaaa 100,64,12\nbuffer_store_dwordx4 v[116:119], v79, s[56:59], s88 offen offset:2048 sc1\nv_cmp_eq_u32 s[44:45], s46, v60\ns_nop 1\nv_cndmask_b32 v77, v77, v76, s[44:45]\n"
"Qmaaa 100,68,40\nQta 58,70,18688\nQmaaa 104,64,16\nQmaaa 104,68,44\nQmaaa 108,64,20\nQta 60,69,256\nQmaaa 108,68,48\nQmaaa 112,64,24\n"
"Qmaaa 112,68,52\nQta 62,69,1280\ns_mov_b32 s31, 131072\ns_waitcnt lgkmcnt(4)\nQmaav 140,72,180\nbuffer_load_dwordx4 a[0:3], v79, s[48:51], s31 offen offset:0\nQmaav 140,76,244\nQmaav 136,72,148\n"
"Qmaav 136,76,212\nQmaav 132,72,224\nQta 64,70,17920\nQmaav 132,76,232\nbuffer_load_dwordx4 a[4:7], v79, s[48:51], s31 offen offset:1024\nQmaav 128,72,192\nQmaav 128,76,200\nQta 66,70,18944\n"
"Qmaav 124,72,160\nQmaav 124,76,168\nbuffer_load_dwordx4 a[8:11], v79, s[48:51], s31 offen offset:2048\nQmaav 120,72,128\nQta 68,69,512\nQmaav 120,76,136\nQmaav 116,72,96\nQmaav 116,76,104\n"
"Qta 70,69,1536\ns_waitcnt lgkmcnt(4)\nQmaav 144,56,96\nbuffer_load_dwordx4 a[12:15], v79, s[48:51], s31 offen offset:3072\nQmaav 144,60,104\nQmaav 148,56,128\nQmaav 148,60,136\nQmaav 152,56,160\n"
"Qta 72,70,18176\ns_mov_b32 s31, 135168\nQmaav 152,60,168\nbuffer_load_dwordx4 a[16:19], v79, s[48:51], s31 offen offset:0\nQmaav 156,56,192\nQmaav 156,60,200\nQta 74,70,19200\nQmaav 160,56,224\n"
"Qmaav 160,60,232\nbuffer_load_dwordx4 a[20:23], v79, s[48:51], s31 offen offset:1024\nQmaav 164,56,148\nQta 76,69,768\nQmaav 164,60,212\nQmaav 168,56,180\nQmaav 168,60,244\nQta 78,69,1792\n"
"s_waitcnt lgkmcnt(4)\nQmvav 24,64,180\nbuffer_load_dwordx4 a[24:27], v79, s[48:51], s31 offen offset:2048\nQmvav 24,68,244\nQmvav 20,64,148\nQmvav 20,68,212\nQmvav 16,64,224\nQta 56,69,8192\n"
"s_mov_b32 s31, 147456\nQmvav 16,68,232\nbuffer_load_dwordx4 a[28:31], v79, s[48:51], s31 offen offset:0\nQmvav 12,64,192\nQmvav 12,68,200\nQta 58,69,8704\nQmvav 8,64,160\nQmvav 8,68,168\n"
"buffer_load_dwordx4 a[32:35], v79, s[48:51], s31 offen offset:1024\nQmvav 4,64,128\nQta 60,69,9216\nv_add_u32 v75, 1, v75\nQmvav 4,68,136\ns_waitcnt vmcnt(31)\nds_write_b32 v73, v75\nQmvav 0,64,96\n"
"Qmvav 0,68,104\nQta 62,69,9728\ns_waitcnt lgkmcnt(5)\nQmvav 28,72,96\nbuffer_load_dwordx4 a[36:39], v79, s[48:51], s31 offen offset:2048\nQmvav 28,76,104\nQmvav 32,72,128\nQmvav 32,76,136\n"
"Qmvav 36,72,160\nQta 64,69,8448\nQmvav 36,76,168\nds_read_b32 v66, v74\nQmvav 40,72,192\nbuffer_load_dwordx4 a[40:43], v79, s[48:51], s31 offen offset:3072\nQmvav 40,76,200\nQta 66,69,8960\n"
"Qmvav 44,72,224\nQmvav 44,76,232\ns_waitcnt lgkmcnt(1)\nv_cmp_gt_u32 vcc, v75, v66\nQmvav 48,72,148\ns_cbranch_vccnz .Lso320\n.Lro320:\nQmvav 48,76,212\n"
"Qta 68,69,9472\ns_mov_b32 s31, 151552\nQmvav 52,72,180\nbuffer_load_dwordx4 a[44:47], v79, s[48:51], s31 offen offset:0\nQmvav 52,76,244\ns_waitcnt lgkmcnt(4)\nQmaav 196,56,180\nQta 70,69,9984\n"
"Qmaav 196,60,244\nQmaav 192,56,148\nbuffer_load_dwordx4 a[48:51], v79, s[48:51], s31 offen offset:1024\nQmaav 192,60,212\nQmaav 188,56,224\nQta 72,69,16384\nQmaav 188,60,232\nQmaav 184,56,192\n"
"buffer_load_dwordx4 a[52:55], v79, s[48:51], s31 offen offset:2048\nQmaav 184,60,200\nQta 74,69,17408\nQmaav 180,56,160\nQmaav 180,60,168\nQmaav 176,56,128\nQta 76,69,24576\ns_mov_b32 s31, 262144\n"
"Qmaav 176,60,136\nbuffer_load_dwordx4 v[80:83], v79, s[48:51], s31 offen offset:0\nQmaav 172,56,96\nQmaav 172,60,104\nQta 78,69,25600\ns_waitcnt lgkmcnt(4)\nQmvav 92,64,96\nQmvav 92,68,104\n"
"buffer_load_dwordx4 v[112:115], v79, s[48:51], s31 offen offset:1024\nQmvav 124,64,128\nQmvav 124,68,136\nQmvav 156,64,160\nQta 56,69,16640\nQmvav 156,68,168\nbuffer_load_dwordx4 v[144:147], v79, s[48:51], s31 offen offset:2048\nQmvav 188,64,192\n"
"Qmvav 188,68,200\nQta 58,69,17664\nQmvav 220,64,224\nQmvav 220,68,232\nbuffer_load_dwordx4 v[176:179], v79, s[48:51], s31 offen offset:3072\nQmvav 252,64,148\nQta 60,69,24832\nQmvav 252,68,212\n"
"Qmvav 100,64,180\nQmvav 100,68,244\nQta 62,69,25856\ns_mov_b32 s31, 266240\ns_waitcnt lgkmcnt(4)\nQmaav 224,72,180\nbuffer_load_dwordx4 v[208:211], v79, s[48:51], s31 offen offset:0\nQmaav 224,76,244\n"
"Qmaav 220,72,148\nQmaav 220,76,212\nQmaav 216,72,224\nQta 64,69,16896\nQmaav 216,76,232\nbuffer_load_dwordx4 v[240:243], v79, s[48:51], s31 offen offset:1024\nQmaav 212,72,192\nQmaav 212,76,200\n"
"Qta 66,69,17920\nQmaav 208,72,160\nQmaav 208,76,168\nbuffer_load_dwordx4 v[84:87], v79, s[48:51], s31 offen offset:2048\nQmaav 204,72,128\nQta 68,69,25088\nQmaav 204,76,136\nQmaav 200,72,96\n"
"Qmaav 200,76,104\nQta 70,69,26112\ns_mov_b32 s31, 278528\ns_waitcnt lgkmcnt(4)\nQmaav 228,56,96\nbuffer_load_dwordx4 v[88:91], v79, s[48:51], s31 offen offset:0\nQmaav 228,60,104\nQmaav 232,56,128\n"
"Qmaav 232,60,136\nQmaav 236,56,160\nQta 72,69,17152\nQmaav 236,60,168\nbuffer_load_dwordx4 v[120:123], v79, s[48:51], s31 offen offset:1024\nQmaav 240,56,192\nQmaav 240,60,200\nQta 74,69,18176\n"
"Qmaav 244,56,224\nQmaav 244,60,232\nbuffer_load_dwordx4 v[152:155], v79, s[48:51], s31 offen offset:2048\nQmaav 248,56,148\nQta 76,69,25344\nQmaav 248,60,212\nQmaav 252,56,180\nQmaav 252,60,244\n"
"Qta 78,69,26368\ns_waitcnt lgkmcnt(4)\nQmvav 172,64,180\nbuffer_load_dwordx4 v[184:187], v79, s[48:51], s31 offen offset:3072\nQmvav 172,68,244\nQmvav 140,64,148\nQmvav 140,68,212\nQmvav 108,64,224\n"
"Qta 56,69,32768\ns_mov_b32 s31, 282624\nQmvav 108,68,232\nbuffer_load_dwordx4 v[216:219], v79, s[48:51], s31 offen offset:0\nQmvav 228,64,192\nQmvav 228,68,200\nQta 58,69,33792\nQmvav 196,64,160\n"
"Qmvav 196,68,168\nbuffer_load_dwordx4 v[248:251], v79, s[48:51], s31 offen offset:1024\nQmvav 164,64,128\nQta 60,69,40960\nQmvav 164,68,136\nQmvav 132,64,96\nQmvav 132,68,104\nQta 62,69,41984\n"
"s_waitcnt lgkmcnt(4)\nQmvav 204,72,96\nbuffer_load_dwordx4 v[116:119], v79, s[48:51], s31 offen offset:2048\nQmvav 204,76,104\nQmvav 236,72,128\nQmvav 236,76,136\nQmaav 96,72,160\nQta 64,69,33024\n"
"s_add_u32 m0, s96, 32768\ns_mov_b32 s95, 331776\nQmaav 96,76,168\nbuffer_load_dwordx4 v63, s[52:55], s95 offen lds\nQmaav 100,72,192\nQmaav 100,76,200\nQta 66,69,34048\nQmaav 104,72,224\n"
"Qmaav 104,76,232\nQmaav 108,72,148\nQta 68,69,41216\ns_mov_b32 s95, 351232\nQmaav 108,76,212\nbuffer_load_dwordx4 v63, s[52:55], s95 offen offset:1024 lds\nQmaav 112,72,180\nQmaav 112,76,244\n"
"Qta 70,69,42240\ns_waitcnt vmcnt(16)\ns_waitcnt lgkmcnt(4)\nQmaaa 140,56,24\nQmaaa 140,60,52\ns_add_u32 m0, s96, 40960\ns_mov_b32 s95, 372736\nQmaaa 136,56,20\n"
"buffer_load_dwordx4 v63, s[52:55], s95 offen lds\nQmaaa 136,60,48\nQmaaa 132,56,16\nQta 72,69,33280\nQmaaa 132,60,44\nQmaaa 128,56,12\nQmaaa 128,60,40\nQta 74,69,34304\n"
"s_mov_b32 s95, 392192\nQmaaa 124,56,8\nbuffer_load_dwordx4 v63, s[52:55], s95 offen offset:1024 lds\nQmaaa 124,60,36\nQmaaa 120,56,4\nQta 76,69,41472\ns_add_u32 s88, s71, 131072\nQmaaa 120,60,32\n"
"buffer_store_dwordx4 v[96:99], v79, s[56:59], s88 offen offset:0 sc1\nQmaaa 116,56,0\nQmaaa 116,60,28\nQta 78,69,42496\ns_waitcnt lgkmcnt(4)\nQmaaa 144,64,0\nQmaaa 144,68,28\nQmaaa 148,64,4\n"
"Qmaaa 148,68,32\nQmaaa 152,64,8\nQta 56,69,33536\ns_add_u32 m0, s96, 49152\ns_mov_b32 s95, 413696\nQmaaa 152,68,36\nbuffer_load_dwordx4 v61, s[52:55], s95 offen lds\nQmaaa 156,64,12\n"
"Qmaaa 156,68,40\nQta 58,69,34560\nQmaaa 160,64,16\nQmaaa 160,68,44\nQmaaa 164,64,20\nQta 60,69,41728\ns_mov_b32 s95, 433152\nQmaaa 164,68,48\n"
"buffer_load_dwordx4 v61, s[52:55], s95 offen offset:1024 lds\n.Lpg15:\ns_cbranch_execz .Ltr6\nQmaaa 168,64,24\nQmaaa 168,68,52\nQta 62,69,42752\ns_waitcnt lgkmcnt(4)\nQmvaa 24,72,24\n"
"Qmvaa 24,76,52\nQmvaa 20,72,20\nbuffer_store_dwordx4 v[128:131], v79, s[56:59], s88 offen offset:1024 sc1\nQmvaa 20,76,48\nQmvaa 16,72,16\nQta 64,69,49152\nQmvaa 16,76,44\nQmvaa 12,72,12\n"
"Qmvaa 12,76,40\nQta 66,69,49664\nQmvaa 8,72,8\nQmvaa 8,76,36\nQmvaa 4,72,4\nQta 68,69,50176\nv_add_u32 v75, 1, v75\nQmvaa 4,76,32\n"
"s_waitcnt vmcnt(36)\nds_write_b32 v73, v75\ns_add_u32 m0, s96, 57344\ns_mov_b32 s95, 454656\nQmvaa 0,72,0\nbuffer_load_dwordx4 v61, s[52:55], s95 offen lds\nQmvaa 0,76,28\nQta 70,69,50688\n"
"s_waitcnt lgkmcnt(5)\nQmvaa 28,56,0\nQmvaa 28,60,28\nQmvaa 32,56,4\ns_mov_b32 s95, 474112\nQmvaa 32,60,32\nbuffer_load_dwordx4 v61, s[52:55], s95 offen offset:1024 lds\nQmvaa 36,56,8\n"
"Qta 72,69,49408\nQmvaa 36,60,36\nds_read_b32 v66, v74\nQmvaa 40,56,12\nQmvaa 40,60,40\nQta 74,69,49920\nQmvaa 44,56,16\nbuffer_store_dwordx4 v[160:163], v79, s[56:59], s88 offen offset:2048 sc1\n"
"v_add_u32 v67, 2, v65\nQmvaa 44,60,44\ns_waitcnt lgkmcnt(1)\nv_cmp_gt_u32 vcc, v75, v66\nQmvaa 48,56,20\ns_cbranch_vccnz .Lso460\n.Lro460:\nQmvaa 48,60,48\n"
"Qta 76,69,50432\nQmvaa 52,56,24\nbuffer_store_dword v67, v77, s[24:27], 0 offen sc1\nQmvaa 52,60,52\ns_waitcnt lgkmcnt(4)\nQmaaa 196,64,24\nQta 78,69,50944\nQmaaa 196,68,52\n"
"s_add_u32 m0, s96, 132096\ns_mov_b32 s95, 413696\nQmaaa 192,64,20\nbuffer_load_dwordx4 v62, s[52:55], s95 offen lds\nQmaaa 192,68,48\nQmaaa 188,64,16\nQta 56,69,57344\nQmaaa 188,68,44\n"
"Qmaaa 184,64,12\nQmaaa 184,68,40\nQta 58,69,58368\ns_mov_b32 s95, 453632\nQmaaa 180,64,8\nbuffer_load_dwordx4 v62, s[52:55], s95 offen offset:1024 lds\nQmaaa 180,68,36\nQmaaa 176,64,4\n"
"Qta 60,68,0\nQmaaa 176,68,32\nQmaaa 172,64,0\nQmaaa 172,68,28\nQta 62,68,1024\ns_waitcnt lgkmcnt(4)\nQmvaa 92,72,0\nbuffer_store_dwordx4 v[192:195], v79, s[56:59], s88 offen offset:3072 sc1\n"
"Qmvaa 92,76,28\nQmvaa 124,72,4\nQmvaa 124,76,32\nQmvaa 156,72,8\nQta 64,69,57600\nQmvaa 156,76,36\nQmvaa 188,72,12\nQmvaa 188,76,40\n"
"Qta 66,69,58624\ns_add_u32 m0, s96, 140288\ns_mov_b32 s95, 413696\nQmvaa 220,72,16\nbuffer_load_dwordx4 v63, s[52:55], s95 offen lds\nQmvaa 220,76,44\nQmvaa 252,72,20\nQta 68,68,256\n"
"Qmvaa 252,76,48\nQmvaa 100,72,24\nQmvaa 100,76,52\nQta 70,68,1280\ns_mov_b32 s95, 433152\ns_waitcnt lgkmcnt(4)\nQmaaa 224,56,24\nbuffer_load_dwordx4 v63, s[52:55], s95 offen offset:1024 lds\n"
"Qmaaa 224,60,52\nQmaaa 220,56,20\nQmaaa 220,60,48\nQmaaa 216,56,16\nQta 72,69,57856\nQmaaa 216,60,44\ns_add_u32 s88, s71, 135168\nQmaaa 212,56,12\n"
"buffer_store_dwordx4 v[224:227], v79, s[56:59], s88 offen offset:0 sc1\nQmaaa 212,60,40\nQta 74,69,58880\nQmaaa 208,56,8\nQmaaa 208,60,36\nQmaaa 204,56,4\nQta 76,68,512\nQmaaa 204,60,32\n"
"Qmaaa 200,56,0\nQmaaa 200,60,28\nQta 78,68,1536\ns_add_u32 m0, s96, 148480\ns_mov_b32 s95, 454656\ns_waitcnt lgkmcnt(4)\nQmaaa 228,64,0\nbuffer_load_dwordx4 v63, s[52:55], s95 offen lds\n"
"Qmaaa 228,68,28\nQmaaa 232,64,4\nQmaaa 232,68,32\nQmaaa 236,64,8\nQta 56,69,58112\ns_mov_b32 s95, 474112\nQmaaa 236,68,36\nbuffer_load_dwordx4 v63, s[52:55], s95 offen offset:1024 lds\n"
"Qmaaa 240,64,12\nQmaaa 240,68,40\nQta 58,69,59136\nQmaaa 244,64,16\nQmaaa 244,68,44\nQmaaa 248,64,20\nQta 60,68,768\nQmaaa 248,68,48\n"
"Qmaaa 252,64,24\nbuffer_store_dwordx4 v[148:151], v79, s[56:59], s88 offen offset:1024 sc1\nQmaaa 252,68,52\nQta 62,68,1792\ns_waitcnt lgkmcnt(4)\nQmvaa 172,72,24\nQmvaa 172,76,52\ns_mov_b32 s40, s22\n"
"s_and_b32 s41, s23, 0xffff\nQmvaa 140,72,20\ns_mov_b32 s42, 571200\ns_lshl_b32 s66, s19, 17\nQmvaa 140,76,48\ns_add_u32 s69, s98, 4194304\ns_add_u32 s98, s66, s69\nQmvaa 108,72,16\n"
"Qta 64,68,8192\ns_xor_b32 s66, s66, 524288\nQmvaa 108,76,44\ns_add_u32 m0, s96, 65536\ns_mov_b32 s95, 659456\nQmvaa 228,72,12\nbuffer_load_dwordx4 v61, s[52:55], s95 offen lds\nQmvaa 228,76,40\n"
"Qta 66,68,9216\ns_add_u32 s64, s66, s69\nQmvaa 196,72,8\ns_mul_i32 s66, s92, 33792\ns_add_u32 s66, s66, %4\nQmvaa 196,76,36\ns_add_u32 s66, s66, 98304\ns_mul_i32 s68, s93, 96\n"
"Qmvaa 164,72,4\nQta 68,68,16384\ns_mov_b32 s95, 678912\nQmvaa 164,76,32\nbuffer_load_dwordx4 v61, s[52:55], s95 offen offset:1024 lds\nQmvaa 132,72,0\ns_sub_u32 s68, s66, s68\ns_add_u32 s68, s68, 96\n"
"Qmvaa 132,76,28\nQta 70,68,17408\nv_bfe_u32 v148, v60, 4, 4\ns_waitcnt lgkmcnt(4)\nQmvaa 204,56,0\ns_mov_b32 s31, 393216\nv_mul_u32_u24 v148, 208, v148\nQmvaa 204,60,28\n"
"buffer_load_dwordx4 a[0:3], v79, s[48:51], s31 offen offset:0\nQmvaa 236,56,4\nv_lshrrev_b32 v149, 8, v60\nv_lshl_add_u32 v148, v149, 3, v148\nQmvaa 236,60,32\nv_add_u32 v56, s68, v148\ns_lshl_b32 s68, s93, 3\nQmaaa 96,56,8\n"
"Qta 72,68,8448\nv_lshrrev_b32 v148, 7, v60\nQmaaa 96,60,36\nbuffer_store_dwordx4 v[180:183], v79, s[56:59], s88 offen offset:2048 sc1\nQmaaa 100,56,12\nv_add_u32 v148, s68, v148\nv_and_b32 v149, 127, v60\nQmaaa 100,60,40\n"
"Qta 74,68,9472\nv_and_b32 v150, 64, v60\nQmaaa 104,56,16\nv_lshrrev_b32 v150, 1, v150\nv_mul_u32_u24 v57, 208, v148\nQmaaa 104,60,44\nv_add3_u32 v57, v57, v149, s66\nv_add_u32 v57, v57, v150\n"
"Qmaaa 108,56,20\nQta 76,68,16640\ns_and_b32 s65, s19, 1\nQmaaa 108,60,48\ns_and_b32 s68, s19, 4\ns_lshl_b32 s68, s68, 2\nQmaaa 112,56,24\ns_lshl_b32 s69, s65, 3\n"
"s_add_u32 s68, s68, s69\nQmaaa 112,60,52\nQta 78,68,17664\ns_lshr_b32 s68, 0xc0004080, s68\ns_waitcnt vmcnt(25)\ns_waitcnt lgkmcnt(4)\nQmaav 140,64,84\nbuffer_load_dwordx4 a[4:7], v79, s[48:51], s31 offen offset:1024\n"
"Qmaav 140,68,116\ns_and_b32 s68, s68, 255\ns_mul_i32 s69, s92, 286720\nQmaav 136,64,240\ns_add_u32 s68, s68, s69\nv_mul_u32_u24 v64, 2560, v148\nQmaav 136,68,248\nv_add3_u32 v64, v64, v149, s68\n"
"s_lshl_b32 s68, s93, 4\nQmaav 132,64,208\nQta 56,68,8704\nv_lshrrev_b32 v148, 6, v60\nQmaav 132,68,216\nbuffer_load_dwordx4 a[8:11], v79, s[48:51], s31 offen offset:2048\nQmaav 128,64,176\nv_add_u32 v148, s68, v148\n"
"v_and_b32 v149, 63, v60\nQmaav 128,68,184\nQta 58,68,9728\nv_and_b32 v150, 32, v60\nQmaav 124,64,144\nv_lshlrev_b32 v150, 1, v150\nv_mul_u32_u24 v71, 208, v148\nQmaav 124,68,152\n"
"buffer_load_dwordx4 a[12:15], v79, s[48:51], s31 offen offset:3072\nQmaav 120,64,112\nQta 60,68,16896\nv_add3_u32 v71, v71, v149, s66\nQmaav 120,68,120\nv_add_u32 v71, v71, v150\ns_xor_b32 s65, s65, 1\nQmaav 116,64,80\n"
"s_lshl_b32 s65, s65, 8\ns_add_u32 s68, s65, s69\nQmaav 116,68,88\nQta 62,68,17920\ns_mov_b32 s31, 397312\ns_waitcnt lgkmcnt(4)\nQmaav 144,72,80\nbuffer_load_dwordx4 a[16:19], v79, s[48:51], s31 offen offset:0\n"
"Qmaav 144,76,88\nv_mul_u32_u24 v72, 2560, v148\nv_add3_u32 v72, v72, v149, s68\nQmaav 148,72,112\nQmaav 148,76,120\nQmaav 152,72,144\nQta 64,68,8960\nQmaav 152,76,152\n"
"buffer_load_dwordx4 a[20:23], v79, s[48:51], s31 offen offset:1024\nQmaav 156,72,176\nQmaav 156,76,184\nQta 66,68,9984\nQmaav 160,72,208\nQmaav 160,76,216\nbuffer_load_dwordx4 a[24:27], v79, s[48:51], s31 offen offset:2048\nQmaav 164,72,240\n"
"Qta 68,68,17152\nQmaav 164,76,248\nQmaav 168,72,84\nQmaav 168,76,116\nQta 70,68,18176\ns_mov_b32 s31, 409600\ns_waitcnt lgkmcnt(4)\nQmvav 24,56,84\n"
"buffer_load_dwordx4 a[28:31], v79, s[48:51], s31 offen offset:0\nQmvav 24,60,116\nQmvav 20,56,240\nQmvav 20,60,248\nQmvav 16,56,208\nQta 72,68,24576\nQmvav 16,60,216\nbuffer_load_dwordx4 a[32:35], v79, s[48:51], s31 offen offset:1024\n"
"Qmvav 12,56,176\nQmvav 12,60,184\nQta 74,68,25088\nQmvav 8,56,144\nQmvav 8,60,152\nbuffer_load_dwordx4 a[36:39], v79, s[48:51], s31 offen offset:2048\nQmvav 4,56,112\nQta 76,68,25600\n"
"v_add_u32 v75, 1, v75\nQmvav 4,60,120\ns_waitcnt vmcnt(20)\nds_write_b32 v73, v75\nQmvav 0,56,80\nQmvav 0,60,88\nQta 78,68,26112\ns_waitcnt lgkmcnt(5)\n"
"Qmvav 28,64,80\nbuffer_load_dwordx4 a[40:43], v79, s[48:51], s31 offen offset:3072\nQmvav 28,68,88\nQmvav 32,64,112\nQmvav 32,68,120\nQmvav 36,64,144\nQta 56,68,24832\nQmvav 36,68,152\n"
"ds_read_b32 v66, v74\ns_mov_b32 s31, 413696\nQmvav 40,64,176\nbuffer_load_dwordx4 a[44:47], v79, s[48:51], s31 offen offset:0\nQmvav 40,68,184\nQta 58,68,25344\nQmvav 44,64,208\nQmvav 44,68,216\n"
"s_waitcnt lgkmcnt(1)\nv_cmp_gt_u32 vcc, v75, v66\nQmvav 48,64,240\ns_cbranch_vccnz .Lso600\n.Lro600:\nQmvav 48,68,248\nQta 60,68,25856\nQmvav 52,64,84\n"
"buffer_load_dwordx4 a[48:51], v79, s[48:51], s31 offen offset:1024\nQmvav 52,68,116\ns_waitcnt lgkmcnt(4)\nQmaav 196,72,84\nQta 62,68,26368\nQmaav 196,76,116\nQmaav 192,72,240\nbuffer_load_dwordx4 a[52:55], v79, s[48:51], s31 offen offset:2048\n"
"Qmaav 192,76,248\nQmaav 188,72,208\nQta 64,68,32768\nQmaav 188,76,216\nQmaav 184,72,176\nQmaav 184,76,184\nQta 66,68,33792\ns_add_u32 m0, s96, 73728\n"
"s_mov_b32 s95, 700416\nQmaav 180,72,144\nbuffer_load_dwordx4 v61, s[52:55], s95 offen lds\nQmaav 180,76,152\nQmaav 176,72,112\nQta 68,68,40960\nQmaav 176,76,120\nQmaav 172,72,80\n"
"Qmaav 172,76,88\nQta 70,68,41984\ns_mov_b32 s95, 719872\ns_waitcnt lgkmcnt(4)\nQmvav 92,56,80\nbuffer_load_dwordx4 v61, s[52:55], s95 offen offset:1024 lds\nQmvav 92,60,88\nQmvav 124,56,112\n"
"Qmvav 124,60,120\nQmvav 156,56,144\nQta 72,68,33024\ns_add_u32 m0, s96, 81920\ns_mov_b32 s95, 659456\nQmvav 156,60,152\nbuffer_load_dwordx4 v62, s[52:55], s95 offen lds\nQmvav 188,56,176\n"
"Qmvav 188,60,184\nQta 74,68,34048\nQmvav 220,56,208\nQmvav 220,60,216\nQmvav 252,56,240\nQta 76,68,41216\ns_mov_b32 s95, 699392\nQmvav 252,60,248\n"
"buffer_load_dwordx4 v62, s[52:55], s95 offen offset:1024 lds\nQmvav 100,56,84\nQmvav 100,60,116\nQta 78,68,42240\ns_waitcnt lgkmcnt(4)\nQmaav 224,64,84\nQmaav 224,68,116\ns_add_u32 m0, s96, 90112\n"
"s_mov_b32 s95, 659456\nQmaav 220,64,240\nbuffer_load_dwordx4 v63, s[52:55], s95 offen lds\nQmaav 220,68,248\nQmaav 216,64,208\nQta 56,68,33280\nQmaav 216,68,216\nQmaav 212,64,176\n"
"Qmaav 212,68,184\nQta 58,68,34304\ns_mov_b32 s95, 678912\nQmaav 208,64,144\nbuffer_load_dwordx4 v63, s[52:55], s95 offen offset:1024 lds\nQmaav 208,68,152\nQmaav 204,64,112\nQta 60,68,41472\n"
"Qmaav 204,68,120\nQmaav 200,64,80\nQmaav 200,68,88\nQta 62,68,42496\ns_waitcnt lgkmcnt(4)\nQmaav 228,72,80\nQmaav 228,76,88\ns_add_u32 s88, s71, 147456\n"
"Qmaav 232,72,112\nbuffer_store_dwordx4 v[104:107], v79, s[56:59], s88 offen offset:0 sc1\nQmaav 232,76,120\nQmaav 236,72,144\nQta 64,68,33536\nQmaav 236,76,152\nQmaav 240,72,176\nQmaav 240,76,184\n"
"Qta 66,68,34560\nQmaav 244,72,208\nQmaav 244,76,216\nQmaav 248,72,240\nQta 68,68,41728\ns_add_u32 m0, s96, 98304\ns_mov_b32 s95, 700416\nQmaav 248,76,248\n"
"buffer_load_dwordx4 v63, s[52:55], s95 offen lds\nQmaav 252,72,84\nQmaav 252,76,116\nQta 70,68,42752\ns_waitcnt lgkmcnt(4)\nQmvav 172,56,84\nbuffer_store_dwordx4 v[136:139], v79, s[56:59], s88 offen offset:1024 sc1\nQmvav 172,60,116\n"
"Qmvav 140,56,240\nQmvav 140,60,248\nQmvav 108,56,208\nQta 72,68,49152\nQmvav 108,60,216\nQmvav 228,56,176\nQmvav 228,60,184\nQta 74,68,50176\n"
"s_mov_b32 s95, 719872\nQmvav 196,56,144\nbuffer_load_dwordx4 v63, s[52:55], s95 offen offset:1024 lds\nQmvav 196,60,152\nQmvav 164,56,112\nQta 76,68,57344\nQmvav 164,60,120\nQmvav 132,56,80\n"
"Qmvav 132,60,88\nQta 78,68,58368\ns_add_u32 m0, s96, 106496\ns_mov_b32 s95, 741376\ns_waitcnt lgkmcnt(4)\nQmvav 204,64,80\nbuffer_load_dwordx4 v61, s[52:55], s95 offen lds\nQmvav 204,68,88\n"
"Qmvav 236,64,112\nQmvav 236,68,120\nQmaav 96,64,144\nQta 56,68,49408\ns_mov_b32 s95, 760832\nQmaav 96,68,152\nbuffer_load_dwordx4 v61, s[52:55], s95 offen offset:1024 lds\nQmaav 100,64,176\n"
"Qmaav 100,68,184\nQta 58,68,50432\nQmaav 104,64,208\ns_mov_b32 s31, 524288\nQmaav 104,68,216\nbuffer_load_dwordx4 v[80:83], v79, s[48:51], s31 offen offset:0\nQmaav 108,64,240\nQta 60,68,57600\n"
"Qmaav 108,68,248\nQmaav 112,64,84\nQmaav 112,68,116\nQta 62,68,58624\ns_waitcnt vmcnt(13)\ns_waitcnt lgkmcnt(4)\nQmaaa 140,72,24\nbuffer_load_dwordx4 v[112:115], v79, s[48:51], s31 offen offset:1024\n"
"Qmaaa 140,76,52\nQmaaa 136,72,20\nQmaaa 136,76,48\nQmaaa 132,72,16\nQta 64,68,49664\nQmaaa 132,76,44\nbuffer_load_dwordx4 v[144:147], v79, s[48:51], s31 offen offset:2048\nQmaaa 128,72,12\n"
"Qmaaa 128,76,40\nQta 66,68,50688\nQmaaa 124,72,8\nQmaaa 124,76,36\nbuffer_load_dwordx4 v[176:179], v79, s[48:51], s31 offen offset:3072\nQmaaa 120,72,4\nQta 68,68,57856\nQmaaa 120,76,32\n"
"Qmaaa 116,72,0\nQmaaa 116,76,28\nQta 70,68,58880\ns_mov_b32 s31, 528384\ns_waitcnt lgkmcnt(4)\nQmaaa 144,56,0\nbuffer_load_dwordx4 v[208:211], v79, s[48:51], s31 offen offset:0\nQmaaa 144,60,28\n"
"Qmaaa 148,56,4\nQmaaa 148,60,32\nQmaaa 152,56,8\nQta 72,68,49920\nQmaaa 152,60,36\nbuffer_load_dwordx4 v[240:243], v79, s[48:51], s31 offen offset:1024\nQmaaa 156,56,12\nQmaaa 156,60,40\n"
"Qta 74,68,50944\nQmaaa 160,56,16\nQmaaa 160,60,44\nbuffer_load_dwordx4 v[84:87], v79, s[48:51], s31 offen offset:2048\nQmaaa 164,56,20\nQta 76,68,58112\nQmaaa 164,60,48\nQmaaa 168,56,24\n"
"Qmaaa 168,60,52\nQta 78,68,59136\ns_mov_b32 s31, 540672\ns_waitcnt lgkmcnt(4)\n.Lpg16:\ns_cbranch_execz .Lpg20\nQmvaa 24,64,24\nbuffer_load_dwordx4 v[88:91], v79, s[48:51], s31 offen offset:0\n"
"Qmvaa 24,68,52\nQmvaa 20,64,20\nQmvaa 20,68,48\nQmvaa 16,64,16\nQta 56,70,1024\nQmvaa 16,68,44\nbuffer_load_dwordx4 v[120:123], v79, s[48:51], s31 offen offset:1024\nQmvaa 12,64,12\n"
"Qmvaa 12,68,40\nQta 58,70,1536\nQmvaa 8,64,8\nQmvaa 8,68,36\nbuffer_load_dwordx4 v[152:155], v79, s[48:51], s31 offen offset:2048\nQmvaa 4,64,4\nQta 60,70,2048\nv_add_u32 v75, 1, v75\n"
"Qmvaa 4,68,32\ns_waitcnt vmcnt(18)\nds_write_b32 v73, v75\nQmvaa 0,64,0\nQmvaa 0,68,28\nQta 62,70,2560\ns_waitcnt lgkmcnt(5)\nQmvaa 28,72,0\n"
"buffer_load_dwordx4 v[184:187], v79, s[48:51], s31 offen offset:3072\nQmvaa 28,76,28\nQmvaa 32,72,4\nQmvaa 32,76,32\nQmvaa 36,72,8\nQta 64,70,1280\nQmvaa 36,76,36\nds_read_b32 v66, v74\n"
"s_mov_b32 s31, 544768\nQmvaa 40,72,12\nbuffer_load_dwordx4 v[216:219], v79, s[48:51], s31 offen offset:0\nQmvaa 40,76,40\nQta 66,70,1792\nQmvaa 44,72,16\nQmvaa 44,76,44\ns_waitcnt lgkmcnt(1)\n"
"v_cmp_gt_u32 vcc, v75, v66\nQmvaa 48,72,20\ns_cbranch_vccnz .Lso740\n.Lro740:\nQmvaa 48,76,48\nQta 68,70,2304\nQmvaa 52,72,24\nbuffer_load_dwordx4 v[248:251], v79, s[48:51], s31 offen offset:1024\n"
"Qmvaa 52,76,52\ns_waitcnt lgkmcnt(4)\nQmaaa 196,56,24\nQta 70,70,2816\nQmaaa 196,60,52\nQmaaa 192,56,20\nbuffer_load_dwordx4 v[116:119], v79, s[48:51], s31 offen offset:2048\nQmaaa 192,60,48\n"
"Qmaaa 188,56,16\nQta 72,70,9216\nQmaaa 188,60,44\nQmaaa 184,56,12\nQmaaa 184,60,40\nQta 74,70,10240\ns_add_u32 m0, s96, 114688\ns_mov_b32 s95, 782336\n"
"Qmaaa 180,56,8\nbuffer_load_dwordx4 v61, s[52:55], s95 offen lds\nQmaaa 180,60,36\nQmaaa 176,56,4\nQta 76,70,17408\nQmaaa 176,60,32\nQmaaa 172,56,0\nQmaaa 172,60,28\n"
"Qta 78,70,18432\ns_mov_b32 s95, 801792\ns_waitcnt lgkmcnt(4)\nQmvaa 92,64,0\nbuffer_load_dwordx4 v61, s[52:55], s95 offen offset:1024 lds\nQmvaa 92,68,28\nQmvaa 124,64,4\nQmvaa 124,68,32\n"
"Qmvaa 156,64,8\nQta 56,70,9472\ns_add_u32 m0, s96, 122880\ns_mov_b32 s95, 741376\nQmvaa 156,68,36\nbuffer_load_dwordx4 v62, s[52:55], s95 offen lds\nQmvaa 188,64,12\nQmvaa 188,68,40\n"
"Qta 58,70,10496\nQmvaa 220,64,16\nQmvaa 220,68,44\nQmvaa 252,64,20\nQta 60,70,17664\ns_mov_b32 s95, 781312\nQmvaa 252,68,48\nbuffer_load_dwordx4 v62, s[52:55], s95 offen offset:1024 lds\n"
"Qmvaa 100,64,24\nQmvaa 100,68,52\nQta 62,70,18688\ns_waitcnt lgkmcnt(4)\nQmaaa 224,72,24\nQmaaa 224,76,52\ns_add_u32 m0, s96, 0\ns_mov_b32 s95, 741376\n"
"Qmaaa 220,72,20\nbuffer_load_dwordx4 v63, s[52:55], s95 offen lds\nQmaaa 220,76,48\nQmaaa 216,72,16\nQta 64,70,9728\nQmaaa 216,76,44\nQmaaa 212,72,12\nQmaaa 212,76,40\n"
"Qta 66,70,10752\ns_mov_b32 s95, 760832\nQmaaa 208,72,8\nbuffer_load_dwordx4 v63, s[52:55], s95 offen offset:1024 lds\nQmaaa 208,76,36\nQmaaa 204,72,4\nQta 68,70,17920\nQmaaa 204,76,32\n"
"Qmaaa 200,72,0\nQmaaa 200,76,28\nQta 70,70,18944\ns_waitcnt lgkmcnt(4)\nQmaaa 228,56,0\nQmaaa 228,60,28\nQmaaa 232,56,4\nbuffer_store_dwordx4 v[168:171], v79, s[56:59], s88 offen offset:2048 sc1\n"
"Qmaaa 232,60,32\nQmaaa 236,56,8\nQta 72,70,9984\nQmaaa 236,60,36\nQmaaa 240,56,12\nQmaaa 240,60,40\nQta 74,70,11008\nQmaaa 244,56,16\n"
"Qmaaa 244,60,44\nQmaaa 248,56,20\nQta 76,70,18176\ns_add_u32 m0, s96, 8192\ns_mov_b32 s95, 782336\nQmaaa 248,60,48\nbuffer_load_dwordx4 v63, s[52:55], s95 offen lds\nQmaaa 252,56,24\n"
"Qmaaa 252,60,52\nQta 78,70,19200\ns_waitcnt lgkmcnt(4)\nQmvaa 172,64,24\nbuffer_store_dwordx4 v[200:203], v79, s[56:59], s88 offen offset:3072 sc1\nQmvaa 172,68,52\nQmvaa 140,64,20\nQmvaa 140,68,48\n"
"Qmvaa 108,64,16\nQta 56,69,0\nQmvaa 108,68,44\nQmvaa 228,64,12\nQmvaa 228,68,40\nQta 58,69,1024\ns_mov_b32 s95, 801792\nQmvaa 196,64,8\n"
"buffer_load_dwordx4 v63, s[52:55], s95 offen offset:1024 lds\nQmvaa 196,68,36\nQmvaa 164,64,4\nQta 60,69,8192\nQmvaa 164,68,32\nQmvaa 132,64,0\nQmvaa 132,68,28\nQta 62,69,9216\n"
"s_add_u32 m0, s96, 16384\ns_mov_b32 s95, 987136\ns_waitcnt lgkmcnt(4)\nQmvaa 204,72,0\nbuffer_load_dwordx4 v61, s[52:55], s95 offen lds\nQmvaa 204,76,28\nQmvaa 236,72,4\nQmvaa 236,76,32\n"
"Qmaaa 96,72,8\nQta 64,69,256\ns_mov_b32 s95, 1006592\nQmaaa 96,76,36\nbuffer_load_dwordx4 v61, s[52:55], s95 offen offset:1024 lds\nQmaaa 100,72,12\nQmaaa 100,76,40\nQta 66,69,1280\n"
"Qmaaa 104,72,16\ns_mov_b32 s31, 655360\nQmaaa 104,76,44\nbuffer_load_dwordx4 a[0:3], v79, s[48:51], s31 offen offset:0\nQmaaa 108,72,20\nQta 68,69,8448\nQmaaa 108,76,48\nQmaaa 112,72,24\n"
"Qmaaa 112,76,52\nQta 70,69,9472\ns_waitcnt vmcnt(13)\ns_waitcnt lgkmcnt(4)\nQmaav 140,56,84\nbuffer_load_dwordx4 a[4:7], v79, s[48:51], s31 offen offset:1024\nQmaav 140,60,116\nQmaav 136,56,240\n"
"Qmaav 136,60,248\nQmaav 132,56,208\nQta 72,69,512\nQmaav 132,60,216\nbuffer_load_dwordx4 a[8:11], v79, s[48:51], s31 offen offset:2048\nQmaav 128,56,176\nQmaav 128,60,184\nQta 74,69,1536\n"
"Qmaav 124,56,144\nQmaav 124,60,152\nbuffer_load_dwordx4 a[12:15], v79, s[48:51], s31 offen offset:3072\nQmaav 120,56,112\nQta 76,69,8704\nQmaav 120,60,120\nQmaav 116,56,80\nQmaav 116,60,88\n"
"Qta 78,69,9728\ns_mov_b32 s31, 659456\ns_waitcnt lgkmcnt(4)\nQmaav 144,64,80\nbuffer_load_dwordx4 a[16:19], v79, s[48:51], s31 offen offset:0\nQmaav 144,68,88\nQmaav 148,64,112\nQmaav 148,68,120\n"
"Qmaav 152,64,144\nQta 56,69,768\nQmaav 152,68,152\nbuffer_load_dwordx4 a[20:23], v79, s[48:51], s31 offen offset:1024\nQmaav 156,64,176\nQmaav 156,68,184\nQta 58,69,1792\nQmaav 160,64,208\n"
"Qmaav 160,68,216\nbuffer_load_dwordx4 a[24:27], v79, s[48:51], s31 offen offset:2048\nQmaav 164,64,240\nQta 60,69,8960\nQmaav 164,68,248\nQmaav 168,64,84\nQmaav 168,68,116\nQta 62,69,9984\n"
"s_mov_b32 s31, 671744\ns_waitcnt lgkmcnt(4)\nQmvav 24,72,84\nbuffer_load_dwordx4 a[28:31], v79, s[48:51], s31 offen offset:0\nQmvav 24,76,116\nQmvav 20,72,240\nQmvav 20,76,248\nQmvav 16,72,208\n"
"Qta 64,69,16384\nQmvav 16,76,216\nbuffer_load_dwordx4 a[32:35], v79, s[48:51], s31 offen offset:1024\nQmvav 12,72,176\nQmvav 12,76,184\nQta 66,69,16896\nQmvav 8,72,144\nQmvav 8,76,152\n"
"buffer_load_dwordx4 a[36:39], v79, s[48:51], s31 offen offset:2048\nQmvav 4,72,112\nQta 68,69,17408\nv_add_u32 v75, 1, v75\nQmvav 4,76,120\ns_waitcnt vmcnt(18)\nds_write_b32 v73, v75\nQmvav 0,72,80\n"
"Qmvav 0,76,88\nQta 70,69,17920\ns_waitcnt lgkmcnt(5)\nQmvav 28,56,80\nbuffer_load_dwordx4 a[40:43], v79, s[48:51], s31 offen offset:3072\nQmvav 28,60,88\nQmvav 32,56,112\nQmvav 32,60,120\n"
"Qmvav 36,56,144\nQta 72,69,16640\nQmvav 36,60,152\nds_read_b32 v66, v74\ns_mov_b32 s31, 675840\nQmvav 40,56,176\nbuffer_load_dwordx4 a[44:47], v79, s[48:51], s31 offen offset:0\nQmvav 40,60,184\n"
"Qta 74,69,17152\nQmvav 44,56,208\nQmvav 44,60,216\ns_waitcnt lgkmcnt(1)\nv_cmp_gt_u32 vcc, v75, v66\nQmvav 48,56,240\ns_cbranch_vccnz .Lso880\n.Lro880:\n"
"Qmvav 48,60,248\nQta 76,69,17664\nQmvav 52,56,84\nbuffer_load_dwordx4 a[48:51], v79, s[48:51], s31 offen offset:1024\nQmvav 52,60,116\ns_waitcnt lgkmcnt(4)\nQmaav 196,64,84\nQta 78,69,18176\n"
"Qmaav 196,68,116\nQmaav 192,64,240\nbuffer_load_dwordx4 a[52:55], v79, s[48:51], s31 offen offset:2048\nQmaav 192,68,248\nQmaav 188,64,208\nQta 56,69,24576\nQmaav 188,68,216\nQmaav 184,64,176\n"
"Qmaav 184,68,184\nQta 58,69,25600\ns_add_u32 m0, s96, 24576\ns_mov_b32 s95, 1028096\nQmaav 180,64,144\nbuffer_load_dwordx4 v61, s[52:55], s95 offen lds\nQmaav 180,68,152\nQmaav 176,64,112\n"
"Qta 60,69,32768\nQmaav 176,68,120\nQmaav 172,64,80\nQmaav 172,68,88\nQta 62,69,33792\ns_mov_b32 s95, 1047552\ns_waitcnt lgkmcnt(4)\nQmvav 92,72,80\n"
"buffer_load_dwordx4 v61, s[52:55], s95 offen offset:1024 lds\nQmvav 92,76,88\nQmvav 124,72,112\nQmvav 124,76,120\nQmvav 156,72,144\nQta 64,69,24832\ns_add_u32 m0, s96, 32768\ns_mov_b32 s95, 987136\n"
"Qmvav 156,76,152\nbuffer_load_dwordx4 v62, s[52:55], s95 offen lds\nQmvav 188,72,176\nQmvav 188,76,184\nQta 66,69,25856\nQmvav 220,72,208\nQmvav 220,76,216\nQmvav 252,72,240\n"
"Qta 68,69,33024\ns_mov_b32 s95, 1027072\nQmvav 252,76,248\nbuffer_load_dwordx4 v62, s[52:55], s95 offen offset:1024 lds\nQmvav 100,72,84\nQmvav 100,76,116\nQta 70,69,34048\ns_waitcnt lgkmcnt(4)\n"
"Qmaav 224,56,84\nQmaav 224,60,116\ns_add_u32 m0, s96, 40960\ns_mov_b32 s95, 987136\nQmaav 220,56,240\nbuffer_load_dwordx4 v63, s[52:55], s95 offen lds\nQmaav 220,60,248\nQmaav 216,56,208\n"
"Qta 72,69,25088\nQmaav 216,60,216\nQmaav 212,56,176\nQmaav 212,60,184\nQta 74,69,26112\ns_mov_b32 s95, 1006592\nQmaav 208,56,144\nbuffer_load_dwordx4 v63, s[52:55], s95 offen offset:1024 lds\n"
"Qmaav 208,60,152\nQmaav 204,56,112\nQta 76,69,33280\nQmaav 204,60,120\nQmaav 200,56,80\nQmaav 200,60,88\nQta 78,69,34304\ns_waitcnt lgkmcnt(4)\n"
"Qmaav 228,64,80\nQmaav 228,68,88\ns_add_u32 s88, s71, 151552\nQmaav 232,64,112\nbuffer_store_dwordx4 v[232:235], v79, s[56:59], s88 offen offset:0 sc1\nQmaav 232,68,120\nQmaav 236,64,144\nQta 56,69,25344\n"
"Qmaav 236,68,152\nQmaav 240,64,176\nQmaav 240,68,184\nQta 58,69,26368\nQmaav 244,64,208\nQmaav 244,68,216\nQmaav 248,64,240\nQta 60,69,33536\n"
"s_add_u32 m0, s96, 49152\ns_mov_b32 s95, 1028096\nQmaav 248,68,248\nbuffer_load_dwordx4 v63, s[52:55], s95 offen lds\nQmaav 252,64,84\nQmaav 252,68,116\nQta 62,69,34560\ns_waitcnt lgkmcnt(4)\n"
"Qmvav 172,72,84\nbuffer_store_dwordx4 v[212:215], v79, s[56:59], s88 offen offset:1024 sc1\nQmvav 172,76,116\nQmvav 140,72,240\nQmvav 140,76,248\nQmvav 108,72,208\nQta 64,69,40960\nQmvav 108,76,216\n"
"Qmvav 228,72,176\nQmvav 228,76,184\nQta 66,69,41984\ns_mov_b32 s95, 1047552\nQmvav 196,72,144\nbuffer_load_dwordx4 v63, s[52:55], s95 offen offset:1024 lds\nQmvav 196,76,152\nQmvav 164,72,112\n"
"Qta 68,69,49152\nQmvav 164,76,120\nQmvav 132,72,80\nQmvav 132,76,88\nQta 70,69,50176\ns_add_u32 m0, s96, 57344\ns_mov_b32 s95, 1069056\ns_waitcnt lgkmcnt(4)\n"
"Qmvav 204,56,80\nbuffer_load_dwordx4 v61, s[52:55], s95 offen lds\nQmvav 204,60,88\nQmvav 236,56,112\nQmvav 236,60,120\nQmaav 96,56,144\nQta 72,69,41216\ns_mov_b32 s95, 1088512\n"
"Qmaav 96,60,152\nbuffer_load_dwordx4 v61, s[52:55], s95 offen offset:1024 lds\nQmaav 100,56,176\nQmaav 100,60,184\nQta 74,69,42240\nQmaav 104,56,208\ns_mov_b32 s31, 786432\nQmaav 104,60,216\n"
"buffer_load_dwordx4 v[80:83], v79, s[48:51], s31 offen offset:0\nQmaav 108,56,240\nQta 76,69,49408\nQmaav 108,60,248\nQmaav 112,56,84\nv_accvgpr_read_b32 v96, a96\nv_accvgpr_read_b32 v97, a97\nQmaav 112,60,116\n"
"Qta 78,69,50432\nv_accvgpr_read_b32 v98, a98\ns_waitcnt vmcnt(13)\ns_waitcnt lgkmcnt(4)\nQmaaa 140,64,24\nbuffer_load_dwordx4 v[112:115], v79, s[48:51], s31 offen offset:1024\nQmaaa 140,68,52\nv_accvgpr_read_b32 v99, a99\n"
"v_accvgpr_read_b32 v128, a100\nQmaaa 136,64,20\nv_accvgpr_read_b32 v129, a101\nv_accvgpr_read_b32 v130, a102\nQmaaa 136,68,48\nv_accvgpr_read_b32 v131, a103\nv_accvgpr_read_b32 v160, a104\nQmaaa 132,64,16\n"
"Qta 56,69,41472\nv_accvgpr_read_b32 v161, a105\nQmaaa 132,68,44\nbuffer_load_dwordx4 v[144:147], v79, s[48:51], s31 offen offset:2048\nQmaaa 128,64,12\nv_accvgpr_read_b32 v162, a106\nv_accvgpr_read_b32 v163, a107\nQmaaa 128,68,40\n"
"Qta 58,69,42496\nv_accvgpr_read_b32 v192, a108\nQmaaa 124,64,8\nv_accvgpr_read_b32 v193, a109\nv_accvgpr_read_b32 v194, a110\nQmaaa 124,68,36\nbuffer_load_dwordx4 v[176:179], v79, s[48:51], s31 offen offset:3072\nQmaaa 120,64,4\n"
"Qta 60,69,49664\nv_accvgpr_read_b32 v195, a111\nQmaaa 120,68,32\nv_accvgpr_read_b32 v224, a112\nv_accvgpr_read_b32 v225, a113\nQmaaa 116,64,0\nv_accvgpr_read_b32 v226, a114\nv_accvgpr_read_b32 v227, a115\n"
"Qmaaa 116,68,28\nQta 62,69,50688\ns_mov_b32 s31, 790528\ns_waitcnt lgkmcnt(4)\nQmaaa 144,72,0\nbuffer_load_dwordx4 v[208:211], v79, s[48:51], s31 offen offset:0\nQmaaa 144,76,28\nQmaaa 148,72,4\n"
"Qmaaa 148,76,32\nQmaaa 152,72,8\nQta 64,69,41728\nQmaaa 152,76,36\nbuffer_load_dwordx4 v[240:243], v79, s[48:51], s31 offen offset:1024\nQmaaa 156,72,12\nQmaaa 156,76,40\nQta 66,69,42752\n"
"Qmaaa 160,72,16\nQmaaa 160,76,44\nbuffer_load_dwordx4 v[84:87], v79, s[48:51], s31 offen offset:2048\nQmaaa 164,72,20\nQta 68,69,49920\nQmaaa 164,76,48\nQmaaa 168,72,24\nQmaaa 168,76,52\n"
"Qta 70,69,50944\ns_mov_b32 s31, 802816\ns_waitcnt lgkmcnt(4)\nQmvaa 24,56,24\nbuffer_load_dwordx4 v[88:91], v79, s[48:51], s31 offen offset:0\nQmvaa 24,60,52\nQmvaa 20,56,20\nQmvaa 20,60,48\n"
"Qmvaa 16,56,16\nQta 72,69,57344\nQmvaa 16,60,44\nbuffer_load_dwordx4 v[120:123], v79, s[48:51], s31 offen offset:1024\nQmvaa 12,56,12\nQmvaa 12,60,40\nQta 74,69,57856\nQmvaa 8,56,8\n"
"Qmvaa 8,60,36\nbuffer_load_dwordx4 v[152:155], v79, s[48:51], s31 offen offset:2048\nQmvaa 4,56,4\nQta 76,69,58368\nv_add_u32 v75, 1, v75\nQmvaa 4,60,32\ns_waitcnt vmcnt(18)\nds_write_b32 v73, v75\n"
"Qmvaa 0,56,0\n.Lpg17:\ns_cbranch_execz .Lpg21\nQmvaa 0,60,28\nQta 78,69,58880\ns_waitcnt lgkmcnt(5)\nQmvaa 28,64,0\nbuffer_load_dwordx4 v[184:187], v79, s[48:51], s31 offen offset:3072\n"
"Qmvaa 28,68,28\nQmvaa 32,64,4\nQmvaa 32,68,32\nQmvaa 36,64,8\nQta 56,69,57600\nQmvaa 36,68,36\nds_read_b32 v66, v74\ns_mov_b32 s31, 806912\n"
"Qmvaa 40,64,12\nbuffer_load_dwordx4 v[216:219], v79, s[48:51], s31 offen offset:0\nQmvaa 40,68,40\nQta 58,69,58112\nQmvaa 44,64,16\nQmvaa 44,68,44\ns_waitcnt lgkmcnt(1)\nv_cmp_gt_u32 vcc, v75, v66\n"
"Qmvaa 48,64,20\ns_cbranch_vccnz .Lso1020\n.Lro1020:\nQmvaa 48,68,48\nQta 60,69,58624\nQmvaa 52,64,24\nbuffer_load_dwordx4 v[248:251], v79, s[48:51], s31 offen offset:1024\nQmvaa 52,68,52\n"
"s_waitcnt lgkmcnt(4)\nQmaaa 196,72,24\nQta 62,69,59136\nQmaaa 196,76,52\nQmaaa 192,72,20\nbuffer_load_dwordx4 v[116:119], v79, s[48:51], s31 offen offset:2048\nQmaaa 192,76,48\nQmaaa 188,72,16\n"
"Qta 64,68,0\nQmaaa 188,76,44\nQmaaa 184,72,12\nQmaaa 184,76,40\nQta 66,68,1024\ns_add_u32 m0, s96, 132096\ns_mov_b32 s95, 1110016\nQmaaa 180,72,8\n"
"buffer_load_dwordx4 v61, s[52:55], s95 offen lds\nQmaaa 180,76,36\nQmaaa 176,72,4\nQta 68,68,8192\nQmaaa 176,76,32\nQmaaa 172,72,0\nQmaaa 172,76,28\nQta 70,68,9216\n"
"s_mov_b32 s95, 1129472\ns_waitcnt lgkmcnt(4)\nQmvaa 92,56,0\nbuffer_load_dwordx4 v61, s[52:55], s95 offen offset:1024 lds\nQmvaa 92,60,28\nQmvaa 124,56,4\nQmvaa 124,60,32\nQmvaa 156,56,8\n"
"Qta 72,68,256\ns_add_u32 m0, s96, 140288\ns_mov_b32 s95, 1069056\nQmvaa 156,60,36\nbuffer_load_dwordx4 v62, s[52:55], s95 offen lds\nQmvaa 188,56,12\nQmvaa 188,60,40\nQta 74,68,1280\n"
"Qmvaa 220,56,16\nQmvaa 220,60,44\nQmvaa 252,56,20\nQta 76,68,8448\ns_mov_b32 s95, 1108992\nQmvaa 252,60,48\nbuffer_load_dwordx4 v62, s[52:55], s95 offen offset:1024 lds\nQmvaa 100,56,24\n"
"Qmvaa 100,60,52\nQta 78,68,9472\ns_waitcnt lgkmcnt(4)\nQmaaa 224,64,24\nQmaaa 224,68,52\ns_add_u32 m0, s96, 148480\ns_mov_b32 s95, 1069056\nQmaaa 220,64,20\n"
"buffer_load_dwordx4 v63, s[52:55], s95 offen lds\nQmaaa 220,68,48\nQmaaa 216,64,16\nQta 56,68,512\nQmaaa 216,68,44\nQmaaa 212,64,12\nQmaaa 212,68,40\nQta 58,68,1536\n"
"s_mov_b32 s95, 1088512\nQmaaa 208,64,8\nbuffer_load_dwordx4 v63, s[52:55], s95 offen offset:1024 lds\nQmaaa 208,68,36\nQmaaa 204,64,4\nQta 60,68,8704\nQmaaa 204,68,32\nQmaaa 200,64,0\n"
"Qmaaa 200,68,28\nQta 62,68,9728\ns_waitcnt lgkmcnt(4)\nQmaaa 228,72,0\nQmaaa 228,76,28\nQmaaa 232,72,4\nbuffer_store_dwordx4 v[244:247], v79, s[56:59], s88 offen offset:2048 sc1\nQmaaa 232,76,32\n"
"Qmaaa 236,72,8\nQta 64,68,768\nQmaaa 236,76,36\nQmaaa 240,72,12\nQmaaa 240,76,40\nQta 66,68,1792\nQmaaa 244,72,16\nQmaaa 244,76,44\n"
"Qmaaa 248,72,20\nQta 68,68,8960\ns_add_u32 m0, s96, 65536\ns_mov_b32 s95, 1110016\nQmaaa 248,76,48\nbuffer_load_dwordx4 v63, s[52:55], s95 offen lds\nQmaaa 252,72,24\nQmaaa 252,76,52\n"
"Qta 70,68,9984\ns_waitcnt lgkmcnt(4)\nQmvaa 172,56,24\nQmvaa 172,60,52\ns_mov_b32 s95, 1129472\nQmvaa 140,56,20\nbuffer_load_dwordx4 v63, s[52:55], s95 offen offset:1024 lds\nQmvaa 140,60,48\n"
"Qmvaa 108,56,16\nQta 72,68,16384\nQmvaa 108,60,44\nQmvaa 228,56,12\nQmvaa 228,60,40\nQta 74,68,17408\ns_add_u32 m0, s96, 73728\ns_add_u32 s95, s67, 4096\n"
"Qmvaa 196,56,8\nbuffer_load_dwordx4 v61, s[52:55], s95 offen lds\nQmvaa 196,60,36\nQmvaa 164,56,4\nQta 76,68,24576\nQmvaa 164,60,32\nQmvaa 132,56,0\nQmvaa 132,60,28\n"
"Qta 78,68,25600\ns_add_u32 s95, s67, 23552\ns_waitcnt lgkmcnt(4)\nQmvaa 204,64,0\nbuffer_load_dwordx4 v61, s[52:55], s95 offen offset:1024 lds\nQmvaa 204,68,28\nQmvaa 236,64,4\nQmvaa 236,68,32\n"
"Qmvaa 96,64,8\nQta 56,68,16640\ns_mov_b32 s31, 917504\nQmvaa 96,68,36\nbuffer_load_dwordx4 a[0:3], v79, s[48:51], s31 offen offset:0\nQmvaa 128,64,12\nQmvaa 128,68,40\nQta 58,68,17664\n"
"Qmvaa 160,64,16\nQmvaa 160,68,44\nbuffer_load_dwordx4 a[4:7], v79, s[48:51], s31 offen offset:1024\nQmvaa 192,64,20\nQta 60,68,24832\nQmvaa 192,68,48\nQmvaa 224,64,24\nQmvaa 224,68,52\n"
"Qta 62,68,25856\ns_waitcnt vmcnt(13)\ns_waitcnt lgkmcnt(4)\nQmaav 140,72,84\nbuffer_load_dwordx4 a[8:11], v79, s[48:51], s31 offen offset:2048\nQmaav 140,76,116\nQmaav 136,72,240\nQmaav 136,76,248\n"
"Qmaav 132,72,208\nQta 64,68,16896\nQmaav 132,76,216\nbuffer_load_dwordx4 a[12:15], v79, s[48:51], s31 offen offset:3072\nQmaav 128,72,176\nQmaav 128,76,184\nQta 66,68,17920\nQmaav 124,72,144\n"
"s_mov_b32 s31, 921600\nQmaav 124,76,152\nbuffer_load_dwordx4 a[16:19], v79, s[48:51], s31 offen offset:0\nQmaav 120,72,112\nQta 68,68,25088\nQmaav 120,76,120\nQmaav 116,72,80\nQmaav 116,76,88\n"
"Qta 70,68,26112\ns_waitcnt lgkmcnt(4)\nQmaav 144,56,80\nbuffer_load_dwordx4 a[20:23], v79, s[48:51], s31 offen offset:1024\nQmaav 144,60,88\nQmaav 148,56,112\nbuffer_load_dword v78, v76, s[24:27], 0 offen sc1\nQmaav 148,60,120\n"
"Qmaav 152,56,144\nQta 72,68,17152\nQmaav 152,60,152\nbuffer_load_dwordx4 a[24:27], v79, s[48:51], s31 offen offset:2048\nQmaav 156,56,176\nQmaav 156,60,184\nQta 74,68,18176\nQmaav 160,56,208\n"
"s_mov_b32 s31, 933888\nQmaav 160,60,216\nbuffer_load_dwordx4 a[28:31], v79, s[48:51], s31 offen offset:0\nQmaav 164,56,240\nQta 76,68,25344\nQmaav 164,60,248\nQmaav 168,56,84\nQmaav 168,60,116\n"
"Qta 78,68,26368\ns_waitcnt lgkmcnt(4)\nQmvav 24,64,84\nbuffer_load_dwordx4 a[32:35], v79, s[48:51], s31 offen offset:1024\nQmvav 24,68,116\nQmvav 20,64,240\nQmvav 20,68,248\nQmvav 16,64,208\n"
"Qta 56,68,32768\nQmvav 16,68,216\nbuffer_load_dwordx4 a[36:39], v79, s[48:51], s31 offen offset:2048\nQmvav 12,64,176\nQmvav 12,68,184\nQta 58,68,33280\nQmvav 8,64,144\nQmvav 8,68,152\n"
"buffer_load_dwordx4 a[40:43], v79, s[48:51], s31 offen offset:3072\nQmvav 4,64,112\nQta 60,68,33792\nv_add_u32 v75, 1, v75\nQmvav 4,68,120\ns_waitcnt vmcnt(19)\nds_write_b32 v73, v75\nQmvav 0,64,80\n"
"Qmvav 0,68,88\nQta 62,68,34304\ns_mov_b32 s31, 937984\ns_waitcnt lgkmcnt(5)\nQmvav 28,72,80\nbuffer_load_dwordx4 a[44:47], v79, s[48:51], s31 offen offset:0\nQmvav 28,76,88\nQmvav 32,72,112\n"
"Qmvav 32,76,120\nQmvav 36,72,144\nQta 64,68,33024\nQmvav 36,76,152\nds_read_b32 v66, v74\nQmvav 40,72,176\nbuffer_load_dwordx4 a[48:51], v79, s[48:51], s31 offen offset:1024\nQmvav 40,76,184\n"
"Qta 66,68,33536\nQmvav 44,72,208\nQmvav 44,76,216\ns_waitcnt lgkmcnt(1)\nv_cmp_gt_u32 vcc, v75, v66\nQmvav 48,72,240\ns_cbranch_vccnz .Lso1160\n.Lro1160:\n"
"Qmvav 48,76,248\nQta 68,68,34048\nQmvav 52,72,84\nbuffer_load_dwordx4 a[52:55], v79, s[48:51], s31 offen offset:2048\nQmvav 52,76,116\ns_waitcnt lgkmcnt(4)\nQmaav 196,56,84\nQta 70,68,34560\n"
"Qmaav 196,60,116\nQmaav 192,56,240\ns_add_u32 m0, s96, 81920\ns_add_u32 s95, s67, 45056\nQmaav 192,60,248\nbuffer_load_dwordx4 v61, s[52:55], s95 offen lds\nQmaav 188,56,208\nQta 72,68,40960\n"
"Qmaav 188,60,216\nQmaav 184,56,176\nQmaav 184,60,184\nQta 74,68,41984\ns_add_u32 s95, s67, 64512\nQmaav 180,56,144\nbuffer_load_dwordx4 v61, s[52:55], s95 offen offset:1024 lds\nQmaav 180,60,152\n"
"Qmaav 176,56,112\nQta 76,68,49152\nQmaav 176,60,120\nQmaav 172,56,80\nQmaav 172,60,88\nQta 78,68,50176\ns_add_u32 m0, s96, 90112\ns_add_u32 s95, s67, 4096\n"
"s_waitcnt lgkmcnt(4)\nQmvav 92,64,80\nbuffer_load_dwordx4 v62, s[52:55], s95 offen lds\nQmvav 92,68,88\nQmvav 124,64,112\nQmvav 124,68,120\nQmvav 156,64,144\nQta 56,68,41216\n"
"s_add_u32 s95, s67, 44032\nQmvav 156,68,152\nbuffer_load_dwordx4 v62, s[52:55], s95 offen offset:1024 lds\nQmvav 188,64,176\ns_waitcnt vmcnt(12)\nv_sub_u32 v66, v78, v65\nv_cmp_gt_u32 vcc, 2, v66\ns_and_b64 vcc, vcc, s[28:29]\n"
"s_cbranch_scc1 .Lf2_w\n.Lf2_r:\nQmvav 188,68,184\nQta 58,68,42240\nQmvav 220,64,208\ns_add_u32 s31, s74, 0\nQmvav 220,68,216\nbuffer_load_dwordx4 a[80:83], v79, s[48:51], s31 offen offset:0 sc1\n"
"Qmvav 252,64,240\nQta 60,68,49408\nQmvav 252,68,248\nQmvav 100,64,84\nQmvav 100,68,116\nQta 62,68,50432\ns_waitcnt lgkmcnt(4)\nQmaav 224,72,84\n"
"buffer_load_dwordx4 a[84:87], v79, s[48:51], s31 offen offset:1024 sc1\nQmaav 224,76,116\nQmaav 220,72,240\nQmaav 220,76,248\nQmaav 216,72,208\nQta 64,68,41472\nQmaav 216,76,216\nbuffer_load_dwordx4 a[88:91], v79, s[48:51], s31 offen offset:2048 sc1\n"
"Qmaav 212,72,176\nQmaav 212,76,184\nQta 66,68,42496\nQmaav 208,72,144\nQmaav 208,76,152\nbuffer_load_dwordx4 a[92:95], v79, s[48:51], s31 offen offset:3072 sc1\nQmaav 204,72,112\nQta 68,68,49664\n"
"Qmaav 204,76,120\nQmaav 200,72,80\nQmaav 200,76,88\nQta 70,68,50688\ns_add_u32 s31, s74, 4096\ns_waitcnt lgkmcnt(4)\nQmaav 228,56,80\nbuffer_load_dwordx4 a[96:99], v79, s[48:51], s31 offen offset:0 sc1\n"
"Qmaav 228,60,88\nQmaav 232,56,112\nQmaav 232,60,120\nQmaav 236,56,144\nQta 72,68,41728\nQmaav 236,60,152\nbuffer_load_dwordx4 a[100:103], v79, s[48:51], s31 offen offset:1024 sc1\nQmaav 240,56,176\n"
"Qmaav 240,60,184\nQta 74,68,42752\nQmaav 244,56,208\nQmaav 244,60,216\nbuffer_load_dwordx4 a[104:107], v79, s[48:51], s31 offen offset:2048 sc1\nQmaav 248,56,240\nQta 76,68,49920\nQmaav 248,60,248\n"
"Qmaav 252,56,84\nQmaav 252,60,116\nQta 78,68,50944\ns_add_u32 s31, s74, 16384\ns_waitcnt lgkmcnt(4)\nQmvav 172,64,84\nbuffer_load_dwordx4 a[108:111], v79, s[48:51], s31 offen offset:0 sc1\nQmvav 172,68,116\n"
"Qmvav 140,64,240\nQmvav 140,68,248\nQmvav 108,64,208\nQta 56,68,57344\nQmvav 108,68,216\nbuffer_load_dwordx4 a[112:115], v79, s[48:51], s31 offen offset:1024 sc1\nQmvav 228,64,176\nQmvav 228,68,184\n"
"Qta 58,68,58368\nQmvav 196,64,144\nQmvav 196,68,152\nbuffer_load_dwordx4 v[168:171], v79, s[48:51], s31 offen offset:2048 sc1\nQmvav 164,64,112\nQta 60,70,1024\nQmvav 164,68,120\nQmvav 132,64,80\n"
"Qmvav 132,68,88\nQta 62,70,2048\ns_waitcnt lgkmcnt(4)\nQmvav 204,72,80\nbuffer_load_dwordx4 v[200:203], v79, s[48:51], s31 offen offset:3072 sc1\nQmvav 204,76,88\nQmvav 236,72,112\nQmvav 236,76,120\n"
"Qmvav 96,72,144\nQta 64,68,57600\ns_add_u32 s31, s74, 20480\nQmvav 96,76,152\nbuffer_load_dwordx4 v[232:235], v79, s[48:51], s31 offen offset:0 sc1\nQmvav 128,72,176\nQmvav 128,76,184\nQta 66,68,58624\n"
"Qmvav 160,72,208\nQmvav 160,76,216\nbuffer_load_dwordx4 v[212:215], v79, s[48:51], s31 offen offset:1024 sc1\nQmvav 192,72,240\nQta 68,70,1280\nQmvav 192,76,248\nQmvav 224,72,84\nQmvav 224,76,116\n"
"Qta 70,70,2304\ns_waitcnt vmcnt(17)\ns_waitcnt lgkmcnt(4)\nQmaaa 140,56,24\nbuffer_load_dwordx4 v[244:247], v79, s[48:51], s31 offen offset:2048 sc1\nQmaaa 140,60,52\nQmaaa 136,56,20\nQmaaa 136,60,48\n"
"Qmaaa 132,56,16\nQta 72,68,57856\ns_add_u32 m0, s96, 98304\ns_add_u32 s95, s67, 4096\nQmaaa 132,60,44\nbuffer_load_dwordx4 v63, s[52:55], s95 offen lds\nQmaaa 128,56,12\nQmaaa 128,60,40\n"
"Qta 74,68,58880\nQmaaa 124,56,8\nQmaaa 124,60,36\nQmaaa 120,56,4\nQta 76,70,1536\ns_add_u32 s95, s67, 23552\nQmaaa 120,60,32\nbuffer_load_dwordx4 v63, s[52:55], s95 offen offset:1024 lds\n"
"Qmaaa 116,56,0\nQmaaa 116,60,28\nQta 78,70,2560\ns_waitcnt lgkmcnt(4)\nQmaaa 144,64,0\nQmaaa 144,68,28\ns_add_u32 m0, s96, 106496\ns_add_u32 s95, s67, 45056\n"
"Qmaaa 148,64,4\nbuffer_load_dwordx4 v63, s[52:55], s95 offen lds\nQmaaa 148,68,32\nQmaaa 152,64,8\nQta 56,68,58112\nQmaaa 152,68,36\nQmaaa 156,64,12\nQmaaa 156,68,40\n"
"Qta 58,68,59136\ns_add_u32 s95, s67, 64512\nQmaaa 160,64,16\nbuffer_load_dwordx4 v63, s[52:55], s95 offen offset:1024 lds\nQmaaa 160,68,44\nQmaaa 164,64,20\nQta 60,70,1792\nQmaaa 164,68,48\n"
"Qmaaa 168,64,24\nQmaaa 168,68,52\nQta 62,70,2816\ns_add_u32 m0, s96, 114688\ns_add_u32 s95, s72, 4096\ns_waitcnt lgkmcnt(4)\nQmvaa 24,72,24\nbuffer_load_dwordx4 v61, s[52:55], s95 offen lds\n"
"Qmvaa 24,76,52\nQmvaa 20,72,20\nQmvaa 20,76,48\nQmvaa 16,72,16\nQta 64,70,9216\ns_add_u32 s95, s72, 23552\nQmvaa 16,76,44\nbuffer_load_dwordx4 v61, s[52:55], s95 offen offset:1024 lds\n"
"Qmvaa 12,72,12\nQmvaa 12,76,40\nQta 66,70,9728\nQmvaa 8,72,8\nQmvaa 8,76,36\nQmvaa 4,72,4\nQta 68,70,10240\nv_add_u32 v75, 1, v75\n"
"Qmvaa 4,76,32\ns_waitcnt vmcnt(20)\nds_write_b32 v73, v75\ns_add_u32 m0, s96, 122880\ns_add_u32 s95, s72, 45056\nQmvaa 0,72,0\nbuffer_load_dwordx4 v61, s[52:55], s95 offen lds\nQmvaa 0,76,28\n"
"Qta 70,70,10752\ns_waitcnt lgkmcnt(5)\nQmvaa 28,56,0\nQmvaa 28,60,28\nQmvaa 32,56,4\ns_add_u32 s95, s72, 64512\nQmvaa 32,60,32\nbuffer_load_dwordx4 v61, s[52:55], s95 offen offset:1024 lds\n"
"Qmvaa 36,56,8\nQta 72,70,9472\nQmvaa 36,60,36\nds_read_b32 v66, v74\nQmvaa 40,56,12\nQmvaa 40,60,40\nQta 74,70,9984\ns_add_u32 m0, s96, 0\n"
"s_add_u32 s95, s72, 4096\nQmvaa 44,56,16\nbuffer_load_dwordx4 v62, s[52:55], s95 offen lds\nv_add_u32 v67, 3, v65\nQmvaa 44,60,44\ns_waitcnt lgkmcnt(1)\nv_cmp_gt_u32 vcc, v75, v66\nQmvaa 48,56,20\n"
"s_cbranch_vccnz .Lso1300\n.Lro1300:\nQmvaa 48,60,48\nQta 76,70,10496\nQmvaa 52,56,24\nbuffer_store_dword v67, v77, s[24:27], 0 offen sc1\ns_add_u32 s95, s72, 44032\nQmvaa 52,60,52\n"
"buffer_load_dwordx4 v62, s[52:55], s95 offen offset:1024 lds\ns_waitcnt lgkmcnt(4)\nQmaaa 196,64,24\nQta 78,70,11008\nQmaaa 196,68,52\n.Lpg18:\ns_cbranch_execz .Lpg22\nQmaaa 192,64,20\n"
"Qmaaa 192,68,48\nQmaaa 188,64,16\nQta 56,70,17408\ns_add_u32 m0, s96, 8192\ns_add_u32 s95, s72, 4096\nQmaaa 188,68,44\nbuffer_load_dwordx4 v63, s[52:55], s95 offen lds\nQmaaa 184,64,12\n"
"Qmaaa 184,68,40\nQta 58,70,18432\nQmaaa 180,64,8\nQmaaa 180,68,36\nQmaaa 176,64,4\nQta 60,69,0\ns_add_u32 s95, s72, 23552\nQmaaa 176,68,32\n"
"buffer_load_dwordx4 v63, s[52:55], s95 offen offset:1024 lds\nQmaaa 172,64,0\nQmaaa 172,68,28\nQta 62,69,1024\ns_waitcnt lgkmcnt(4)\nQmvaa 92,72,0\ns_add_u32 s31, s75, 0\nQmvaa 92,76,28\n"
"buffer_load_dwordx4 v[80:83], v79, s[48:51], s31 offen offset:0 sc1\nQmvaa 124,72,4\nQmvaa 124,76,32\nQmvaa 156,72,8\nQta 64,70,17664\nQmvaa 156,76,36\nbuffer_load_dwordx4 v[112:115], v79, s[48:51], s31 offen offset:1024 sc1\nQmvaa 188,72,12\n"
"Qmvaa 188,76,40\nQta 66,70,18688\nQmvaa 220,72,16\nQmvaa 220,76,44\nbuffer_load_dwordx4 v[144:147], v79, s[48:51], s31 offen offset:2048 sc1\nQmvaa 252,72,20\nQta 68,69,256\nQmvaa 252,76,48\n"
"Qmvaa 100,72,24\nQmvaa 100,76,52\nQta 70,69,1280\ns_waitcnt lgkmcnt(4)\nQmaaa 224,56,24\nbuffer_load_dwordx4 v[176:179], v79, s[48:51], s31 offen offset:3072 sc1\nQmaaa 224,60,52\nQmaaa 220,56,20\n"
"Qmaaa 220,60,48\nQmaaa 216,56,16\nQta 72,70,17920\ns_add_u32 s31, s75, 4096\nQmaaa 216,60,44\nbuffer_load_dwordx4 v[208:211], v79, s[48:51], s31 offen offset:0 sc1\nQmaaa 212,56,12\nQmaaa 212,60,40\n"
"Qta 74,70,18944\nQmaaa 208,56,8\nQmaaa 208,60,36\nbuffer_load_dwordx4 v[240:243], v79, s[48:51], s31 offen offset:1024 sc1\nQmaaa 204,56,4\nQta 76,69,512\nQmaaa 204,60,32\nQmaaa 200,56,0\n"
"Qmaaa 200,60,28\nQta 78,69,1536\ns_waitcnt lgkmcnt(4)\nQmaaa 228,64,0\nbuffer_load_dwordx4 v[84:87], v79, s[48:51], s31 offen offset:2048 sc1\nQmaaa 228,68,28\nQmaaa 232,64,4\nQmaaa 232,68,32\n"
"Qmaaa 236,64,8\nQta 56,70,18176\ns_add_u32 s31, s75, 16384\nQmaaa 236,68,36\nbuffer_load_dwordx4 v[88:91], v79, s[48:51], s31 offen offset:0 sc1\nQmaaa 240,64,12\nQmaaa 240,68,40\nQta 58,70,19200\n"
"Qmaaa 244,64,16\nQmaaa 244,68,44\nbuffer_load_dwordx4 v[120:123], v79, s[48:51], s31 offen offset:1024 sc1\nQmaaa 248,64,20\nQta 60,69,768\nQmaaa 248,68,48\nQmaaa 252,64,24\nQmaaa 252,68,52\n"
"Qta 62,69,1792\ns_waitcnt lgkmcnt(4)\nQmvaa 172,72,24\nbuffer_load_dwordx4 v[152:155], v79, s[48:51], s31 offen offset:2048 sc1\nQmvaa 172,76,52\nQmvaa 140,72,20\nQmvaa 140,76,48\nQmvaa 108,72,16\n"
"Qta 64,69,8192\nQmvaa 108,76,44\nbuffer_load_dwordx4 v[184:187], v79, s[48:51], s31 offen offset:3072 sc1\nQmvaa 228,72,12\nQmvaa 228,76,40\nQta 66,69,9216\nQmvaa 196,72,8\ns_add_u32 s31, s75, 20480\n"
"Qmvaa 196,76,36\nbuffer_load_dwordx4 v[216:219], v79, s[48:51], s31 offen offset:0 sc1\nQmvaa 164,72,4\nQta 68,69,16384\nQmvaa 164,76,32\nQmvaa 132,72,0\nQmvaa 132,76,28\nQta 70,69,17408\n"
"s_waitcnt lgkmcnt(4)\nQmvaa 204,56,0\nbuffer_load_dwordx4 v[248:251], v79, s[48:51], s31 offen offset:1024 sc1\nQmvaa 204,60,28\nQmvaa 236,56,4\nQmvaa 236,60,32\nQmvaa 96,56,8\nQta 72,69,8448\n"
"Qmvaa 96,60,36\nbuffer_load_dwordx4 v[116:119], v79, s[48:51], s31 offen offset:2048 sc1\nQmvaa 128,56,12\nQmvaa 128,60,40\nQta 74,69,9472\nQmvaa 160,56,16\nQmvaa 160,60,44\nQmvaa 192,56,20\n"
"Qta 76,69,16640\ns_add_u32 m0, s96, 16384\ns_add_u32 s95, s72, 45056\nQmvaa 192,60,48\nbuffer_load_dwordx4 v63, s[52:55], s95 offen lds\nQmvaa 224,56,24\nQmvaa 224,60,52\nQta 78,69,17664\n"
"s_waitcnt vmcnt(28)\ns_waitcnt lgkmcnt(4)\nQmaaa 140,64,104\nQmaav 140,68,244\ns_add_u32 s95, s72, 64512\nQmaaa 136,64,100\nbuffer_load_dwordx4 v63, s[52:55], s95 offen offset:1024 lds\nQmaav 136,68,212\n"
"Qmaaa 132,64,96\nQta 56,69,8704\nQmaav 132,68,232\nQmaaa 128,64,92\nQmaav 128,68,200\nQta 58,69,9728\ns_add_u32 m0, s96, 24576\ns_add_u32 s95, s73, 4096\n"
"Qmaaa 124,64,88\nbuffer_load_dwordx4 v61, s[52:55], s95 offen lds\nQmaav 124,68,168\nQmaaa 120,64,84\nQta 60,69,16896\nQmaaa 120,68,112\nQmaaa 116,64,80\nQmaaa 116,68,108\n"
"Qta 62,69,17920\ns_add_u32 s95, s73, 23552\ns_waitcnt lgkmcnt(4)\nQmaaa 144,72,80\nbuffer_load_dwordx4 v61, s[52:55], s95 offen offset:1024 lds\nQmaaa 144,76,108\nQmaaa 148,72,84\nQmaaa 148,76,112\n"
"Qmaaa 152,72,88\nQta 64,69,8960\ns_add_u32 m0, s96, 32768\ns_add_u32 s95, s73, 45056\nQmaav 152,76,168\nbuffer_load_dwordx4 v61, s[52:55], s95 offen lds\nQmaaa 156,72,92\nQmaav 156,76,200\n"
"Qta 66,69,9984\nQmaaa 160,72,96\nQmaav 160,76,232\nQmaaa 164,72,100\nQta 68,69,17152\ns_add_u32 s95, s73, 64512\nQmaav 164,76,212\nbuffer_load_dwordx4 v61, s[52:55], s95 offen offset:1024 lds\n"
"Qmaaa 168,72,104\nQmaav 168,76,244\nQta 70,69,18176\ns_waitcnt lgkmcnt(4)\nQmvaa 24,56,104\nQmvav 24,60,244\ns_add_u32 m0, s96, 40960\ns_add_u32 s95, s73, 4096\n"
"Qmvaa 20,56,100\nbuffer_load_dwordx4 v62, s[52:55], s95 offen lds\nQmvav 20,60,212\nQmvaa 16,56,96\nQta 72,69,24576\nQmvav 16,60,232\nQmvaa 12,56,92\nQmvav 12,60,200\n"
"Qta 74,69,25088\ns_add_u32 s95, s73, 44032\nQmvaa 8,56,88\nbuffer_load_dwordx4 v62, s[52:55], s95 offen offset:1024 lds\nQmvav 8,60,168\nQmvaa 4,56,84\nQta 76,69,25600\nv_add_u32 v75, 1, v75\n"
"Qmvaa 4,60,112\ns_waitcnt vmcnt(33)\nds_write_b32 v73, v75\nQmvaa 0,56,80\nQmvaa 0,60,108\nQta 78,69,26112\ns_add_u32 m0, s96, 49152\ns_add_u32 s95, s73, 4096\n"
"s_waitcnt lgkmcnt(5)\nQmvaa 28,64,80\nbuffer_load_dwordx4 v63, s[52:55], s95 offen lds\nQmvaa 28,68,108\nQmvaa 32,64,84\nQmvaa 32,68,112\nQmvaa 36,64,88\nQta 56,69,24832\n"
"Qmvav 36,68,168\nds_read_b32 v66, v74\ns_add_u32 s95, s73, 23552\nQmvaa 40,64,92\nbuffer_load_dwordx4 v63, s[52:55], s95 offen offset:1024 lds\nQmvav 40,68,200\nQta 58,69,25344\nQmvaa 44,64,96\n"
"Qmvav 44,68,232\ns_waitcnt lgkmcnt(1)\nv_cmp_gt_u32 vcc, v75, v66\nQmvaa 48,64,100\ns_cbranch_vccnz .Lso1440\n.Lro1440:\nQmvav 48,68,212\nQta 60,69,25856\n"
"v_add_u32 v75, 1, v75\nQmvaa 52,64,104\ns_waitcnt vmcnt(29)\nds_write_b32 v73, v75\ns_add_u32 s31, s77, 0\nQmvav 52,68,244\nbuffer_load_dwordx4 a[0:3], v79, s[48:51], s31 offen offset:0 sc1\ns_waitcnt lgkmcnt(5)\n"
"Qmaaa 196,72,104\nQta 62,69,26368\nQmaav 196,76,244\nQmaaa 192,72,100\nQmaav 192,76,212\nbuffer_load_dwordx4 a[4:7], v79, s[48:51], s31 offen offset:1024 sc1\nQmaaa 188,72,96\nQta 64,69,32768\n"
"Qmaav 188,76,232\nbuffer_load_dword v78, v76, s[24:27], 0 offen sc1\nQmaaa 184,72,92\nds_read_b32 v66, v74\nQmaav 184,76,200\nQta 66,69,33792\nQmaaa 180,72,88\nbuffer_load_dwordx4 a[8:11], v79, s[48:51], s31 offen offset:2048 sc1\n"
"Qmaav 180,76,168\nQmaaa 176,72,84\ns_waitcnt lgkmcnt(1)\nv_cmp_gt_u32 vcc, v75, v66\nQmaaa 176,76,112\ns_cbranch_vccnz .Lso1455\n.Lro1455:\nQmaaa 172,72,80\n"
"Qta 68,69,40960\nQmaaa 172,76,108\nbuffer_load_dwordx4 a[12:15], v79, s[48:51], s31 offen offset:3072 sc1\ns_waitcnt lgkmcnt(4)\nQmvaa 92,56,80\nQmvaa 92,60,108\nQta 70,69,41984\nQmvaa 124,56,84\n"
"s_add_u32 s31, s77, 4096\nQmvaa 124,60,112\nbuffer_load_dwordx4 a[16:19], v79, s[48:51], s31 offen offset:0 sc1\nQmvaa 156,56,88\nQta 72,69,33024\nQmvav 156,60,168\nQmvaa 188,56,92\nQmvav 188,60,200\n"
"Qta 74,69,34048\nQmvaa 220,56,96\nbuffer_load_dwordx4 a[20:23], v79, s[48:51], s31 offen offset:1024 sc1\nQmvav 220,60,232\nQmvaa 252,56,100\nQta 76,69,41216\nQmvav 252,60,212\nQmvaa 100,56,104\n"
"buffer_load_dwordx4 a[24:27], v79, s[48:51], s31 offen offset:2048 sc1\nQmvav 100,60,244\nQta 78,69,42240\ns_waitcnt lgkmcnt(4)\nQmaaa 224,64,104\nQmaav 224,68,244\ns_add_u32 s31, s77, 16384\nQmaaa 220,64,100\n"
"buffer_load_dwordx4 a[28:31], v79, s[48:51], s31 offen offset:0 sc1\nQmaav 220,68,212\nQmaaa 216,64,96\nQta 56,69,33280\nQmaav 216,68,232\nQmaaa 212,64,92\nbuffer_load_dwordx4 a[32:35], v79, s[48:51], s31 offen offset:1024 sc1\nQmaav 212,68,200\n"
"Qta 58,69,34304\nQmaaa 208,64,88\nQmaav 208,68,168\nQmaaa 204,64,84\nQta 60,69,41472\nQmaaa 204,68,112\nbuffer_load_dwordx4 a[36:39], v79, s[48:51], s31 offen offset:2048 sc1\nQmaaa 200,64,80\n"
"Qmaaa 200,68,108\nQta 62,69,42496\ns_waitcnt lgkmcnt(4)\nQmaaa 228,72,80\nQmaaa 228,76,108\nbuffer_load_dwordx4 a[40:43], v79, s[48:51], s31 offen offset:3072 sc1\nQmaaa 232,72,84\nQmaaa 232,76,112\n"
"Qmaaa 236,72,88\nQta 64,69,33536\ns_add_u32 s31, s77, 20480\nQmaav 236,76,168\nbuffer_load_dwordx4 a[44:47], v79, s[48:51], s31 offen offset:0 sc1\nQmaaa 240,72,92\nQmaav 240,76,200\nQta 66,69,34560\n"
"Qmaaa 244,72,96\nQmaav 244,76,232\nbuffer_load_dwordx4 a[48:51], v79, s[48:51], s31 offen offset:1024 sc1\nQmaaa 248,72,100\nQta 68,69,41728\nQmaav 248,76,212\nQmaaa 252,72,104\nQmaav 252,76,244\n"
"Qta 70,69,42752\ns_waitcnt lgkmcnt(4)\nQmvaa 172,56,104\nbuffer_load_dwordx4 a[52:55], v79, s[48:51], s31 offen offset:2048 sc1\nQmvav 172,60,244\nQmvaa 140,56,100\nQmvav 140,60,212\nQmvaa 108,56,96\n"
"Qta 72,69,49152\ns_add_u32 m0, s96, 57344\ns_add_u32 s95, s73, 45056\nQmvav 108,60,232\nbuffer_load_dwordx4 v63, s[52:55], s95 offen lds\nQmvaa 228,56,92\nQmvav 228,60,200\nQta 74,69,50176\n"
"Qmvaa 196,56,88\nQmvav 196,60,168\ns_waitcnt vmcnt(13)\nv_sub_u32 v66, v78, v65\nv_cmp_gt_u32 vcc, 3, v66\ns_and_b64 vcc, vcc, s[28:29]\ns_cbranch_scc1 .Lf3_w\n.Lf3_r:\n"
"Qmvaa 164,56,84\nQta 76,69,57344\ns_add_u32 s95, s73, 64512\nQmvaa 164,60,112\nbuffer_load_dwordx4 v63, s[52:55], s95 offen offset:1024 lds\nv_add_u32 v75, 1, v75\nQmvaa 132,56,80\ns_waitcnt vmcnt(41)\n"
"ds_write_b32 v73, v75\nQmvaa 132,60,108\nQta 78,69,58368\ns_waitcnt lgkmcnt(5)\nQmvaa 204,64,80\ns_add_u32 s31, s74, 131072\nQmvaa 204,68,108\nbuffer_load_dwordx4 a[80:83], v79, s[48:51], s31 offen offset:0 sc1\n"
"Qmvaa 236,64,84\nQmvaa 236,68,112\ns_bitcmp1_b32 s19, 2\ns_cbranch_scc1 .Lrole1\nQmvaa 96,64,88\nQta 56,69,49408\ns_lshl_b32 s46, s19, 4\nQmvav 96,68,168\n"
"buffer_load_dwordx4 a[84:87], v79, s[48:51], s31 offen offset:1024 sc1\nQmvaa 128,64,92\nds_read_b32 v66, v74\ns_add_u32 s46, s46, 128\nQmvav 128,68,200\nQta 58,69,50432\nv_mov_b32 v77, 0x7ffff000\nQmvaa 160,64,96\n"
"Qmvav 160,68,232\nbuffer_load_dwordx4 a[88:91], v79, s[48:51], s31 offen offset:2048 sc1\nQmvaa 192,64,100\ns_waitcnt lgkmcnt(1)\nv_cmp_gt_u32 vcc, v75, v66\nQmvav 192,68,212\ns_cbranch_vccnz .Lso1525r0\n.Lro1525r0:\n"
"Qmvaa 224,64,104\nQta 60,69,57600\nQmvav 224,68,244\nbuffer_load_dwordx4 a[92:95], v79, s[48:51], s31 offen offset:3072 sc1\ns_waitcnt vmcnt(31)\ns_waitcnt lgkmcnt(4)\nQmaav 140,72,84\nQmaav 140,76,116\n"
"Qta 62,69,58624\nQmaav 136,72,240\nQmaav 136,76,248\nQmaav 132,72,208\nQta 64,69,49664\ns_add_u32 m0, s96, 65536\ns_add_u32 s95, s67, 86016\nQmaav 132,76,216\n"
"buffer_load_dwordx4 v63, s[52:55], s95 offen lds\nQmaav 128,72,176\nQmaav 128,76,184\nQta 66,69,50688\nQmaav 124,72,144\nQmaav 124,76,152\nQmaav 120,72,112\nQta 68,69,57856\n"
"s_add_u32 s95, s67, 105472\nQmaav 120,76,120\nbuffer_load_dwordx4 v63, s[52:55], s95 offen offset:1024 lds\nQmaav 116,72,80\nQmaav 116,76,88\nQta 70,69,58880\ns_waitcnt lgkmcnt(4)\nQmaav 144,56,80\n"
"Qmaav 144,60,88\ns_add_u32 m0, s96, 73728\ns_add_u32 s95, s67, 126976\nQmaav 148,56,112\nbuffer_load_dwordx4 v63, s[52:55], s95 offen lds\nQmaav 148,60,120\nQmaav 152,56,144\nQta 72,69,49920\n"
"Qmaav 152,60,152\nQmaav 156,56,176\nQmaav 156,60,184\nQta 74,69,50944\ns_add_u32 s95, s67, 146432\nQmaav 160,56,208\nbuffer_load_dwordx4 v63, s[52:55], s95 offen offset:1024 lds\nQmaav 160,60,216\n"
"Qmaav 164,56,240\nQta 76,69,58112\nQmaav 164,60,248\nQmaav 168,56,84\nQmaav 168,60,116\nQta 78,69,59136\ns_add_u32 m0, s96, 81920\ns_add_u32 s95, s72, 86016\n"
"s_waitcnt lgkmcnt(4)\nQmvav 24,64,84\nbuffer_load_dwordx4 v63, s[52:55], s95 offen lds\nQmvav 24,68,116\nQmvav 20,64,240\nQmvav 20,68,248\nQmvav 16,64,208\nQta 56,68,0\n"
"s_add_u32 s95, s72, 105472\nQmvav 16,68,216\nbuffer_load_dwordx4 v63, s[52:55], s95 offen offset:1024 lds\nQmvav 12,64,176\nQmvav 12,68,184\nQta 58,68,512\nQmvav 8,64,144\nQmvav 8,68,152\n"
"Qmvav 4,64,112\nQta 60,68,1024\ns_add_u32 m0, s96, 90112\ns_add_u32 s95, s72, 126976\nQmvav 4,68,120\nbuffer_load_dwordx4 v63, s[52:55], s95 offen lds\nQmvav 0,64,80\nQmvav 0,68,88\n"
"Qta 62,68,1536\ns_waitcnt lgkmcnt(4)\nQmvav 28,72,80\nQmvav 28,76,88\ns_add_u32 s95, s72, 146432\nQmvav 32,72,112\nbuffer_load_dwordx4 v63, s[52:55], s95 offen offset:1024 lds\nQmvav 32,76,120\n"
"Qmvav 36,72,144\nQta 64,68,256\nQmvav 36,76,152\ns_add_u32 s31, s74, 135168\nQmvav 40,72,176\nbuffer_load_dwordx4 a[96:99], v79, s[48:51], s31 offen offset:0 sc1\nQmvav 40,76,184\nQta 66,68,768\n"
"Qmvav 44,72,208\nv_cmp_eq_u32 s[44:45], s46, v60\ns_nop 1\nv_cndmask_b32 v77, v77, v76, s[44:45]\nv_add_u32 v67, 1, v65\ns_xor_b32 s46, s19, 12\ns_lshl_b32 s28, 1, s46\nQmvav 44,76,216\n"
"Qmvav 48,72,240\nQta 68,68,1280\nQmvav 48,76,248\nbuffer_load_dwordx4 a[100:103], v79, s[48:51], s31 offen offset:1024 sc1\nv_add_u32 v75, 1, v75\nQmvav 52,72,84\ns_waitcnt vmcnt(35)\nds_write_b32 v73, v75\n"
"Qmvav 52,76,116\nQta 70,68,1792\ns_waitcnt lgkmcnt(5)\nQmaav 196,56,84\nQmaav 196,60,116\nbuffer_load_dwordx4 a[104:107], v79, s[48:51], s31 offen offset:2048 sc1\nQmaav 192,56,240\nQmaav 192,60,248\n"
"Qmaav 188,56,208\nQta 72,68,8192\ns_add_u32 s31, s74, 147456\nQmaav 188,60,216\nbuffer_load_dwordx4 a[108:111], v79, s[48:51], s31 offen offset:0 sc1\nQmaav 184,56,176\nds_read_b32 v66, v74\nQmaav 184,60,184\n"
"Qta 74,68,9216\nQmaav 180,56,144\nQmaav 180,60,152\nbuffer_load_dwordx4 a[112:115], v79, s[48:51], s31 offen offset:1024 sc1\nQmaav 176,56,112\n.Lpg19:\ns_cbranch_execz .Lpg23\ns_waitcnt lgkmcnt(1)\n"
"v_cmp_gt_u32 vcc, v75, v66\nQmaav 176,60,120\ns_cbranch_vccnz .Lso1595r0\n.Lro1595r0:\nQmaav 172,56,80\nQta 76,68,16384\nQmaav 172,60,88\nbuffer_load_dwordx4 v[168:171], v79, s[48:51], s31 offen offset:2048 sc1\n"
"s_waitcnt lgkmcnt(4)\nQmvav 92,64,80\nQmvav 92,68,88\nQta 78,68,17408\nQmvav 124,64,112\nQmvav 124,68,120\nbuffer_load_dwordx4 v[200:203], v79, s[48:51], s31 offen offset:3072 sc1\nQmvav 156,64,144\n"
"Qta 56,68,8448\nQmvav 156,68,152\nQmvav 188,64,176\nQmvav 188,68,184\nQta 58,68,9472\ns_add_u32 s31, s74, 151552\nQmvav 220,64,208\nbuffer_load_dwordx4 v[232:235], v79, s[48:51], s31 offen offset:0 sc1\n"
"Qmvav 220,68,216\nQmvav 252,64,240\nQta 60,68,16640\nQmvav 252,68,248\nQmvav 100,64,84\nbuffer_load_dwordx4 v[212:215], v79, s[48:51], s31 offen offset:1024 sc1\nQmvav 100,68,116\nQta 62,68,17664\n"
"s_waitcnt lgkmcnt(4)\nQmaav 224,72,84\nQmaav 224,76,116\nQmaav 220,72,240\nbuffer_load_dwordx4 v[244:247], v79, s[48:51], s31 offen offset:2048 sc1\nQmaav 220,76,248\nQmaav 216,72,208\nQta 64,68,8704\n"
"Qmaav 216,76,216\ns_add_u32 m0, s91, 0\ns_add_u32 s31, s77, 131072\nQmaav 212,72,176\nbuffer_load_dwordx4 v79, s[48:51], s31 offen sc1 lds\nQmaav 212,76,184\nQta 66,68,9728\nQmaav 208,72,144\n"
"Qmaav 208,76,152\nQmaav 204,72,112\nQta 68,68,16896\nQmaav 204,76,120\nbuffer_load_dwordx4 v79, s[48:51], s31 offen offset:1024 sc1 lds\nQmaav 200,72,80\nQmaav 200,76,88\nQta 70,68,17920\n"
"s_waitcnt lgkmcnt(4)\nQmaav 228,56,80\nQmaav 228,60,88\nbuffer_load_dwordx4 v79, s[48:51], s31 offen offset:2048 sc1 lds\nQmaav 232,56,112\nQmaav 232,60,120\nQmaav 236,56,144\nQta 72,68,8960\n"
"Qmaav 236,60,152\nbuffer_load_dwordx4 v79, s[48:51], s31 offen offset:3072 sc1 lds\nQmaav 240,56,176\nQmaav 240,60,184\nQta 74,68,9984\nQmaav 244,56,208\ns_add_u32 m0, s91, 4096\ns_add_u32 s31, s77, 135168\n"
"Qmaav 244,60,216\nbuffer_load_dwordx4 v79, s[48:51], s31 offen sc1 lds\nQmaav 248,56,240\nQta 76,68,17152\nQmaav 248,60,248\nQmaav 252,56,84\nQmaav 252,60,116\nQta 78,68,18176\n"
"s_waitcnt lgkmcnt(4)\nQmvav 172,64,84\nbuffer_load_dwordx4 v79, s[48:51], s31 offen offset:1024 sc1 lds\nQmvav 172,68,116\nQmvav 140,64,240\nQmvav 140,68,248\nQmvav 108,64,208\nQta 56,68,24576\n"
"Qmvav 108,68,216\nbuffer_load_dwordx4 v79, s[48:51], s31 offen offset:2048 sc1 lds\nQmvav 228,64,176\nQmvav 228,68,184\nQta 58,68,25600\nQmvav 196,64,144\ns_add_u32 m0, s91, 33792\ns_add_u32 s31, s77, 147456\n"
"Qmvav 196,68,152\nbuffer_load_dwordx4 v79, s[48:51], s31 offen sc1 lds\nQmvav 164,64,112\nQta 60,68,32768\nQmvav 164,68,120\nv_add_u32 v75, 1, v75\nQmvav 132,64,80\ns_waitcnt vmcnt(47)\n"
"ds_write_b32 v73, v75\nQmvav 132,68,88\nQta 62,68,33792\ns_waitcnt lgkmcnt(5)\nQmvav 204,72,80\nbuffer_load_dwordx4 v79, s[48:51], s31 offen offset:1024 sc1 lds\nQmvav 204,76,88\nQmvav 236,72,112\n"
"Qmvav 236,76,120\nQmvav 96,72,144\nQta 64,68,24832\nQmvav 96,76,152\nbuffer_load_dwordx4 v79, s[48:51], s31 offen offset:2048 sc1 lds\nQmvav 128,72,176\nds_read_b32 v66, v74\nQmvav 128,76,184\n"
"Qta 66,68,25856\nQmvav 160,72,208\nQmvav 160,76,216\nbuffer_load_dwordx4 v79, s[48:51], s31 offen offset:3072 sc1 lds\nQmvav 192,72,240\ns_waitcnt lgkmcnt(1)\nv_cmp_gt_u32 vcc, v75, v66\nQmvav 192,76,248\n"
"s_cbranch_vccnz .Lso1665r0\n.Lro1665r0:\nQmvav 224,72,84\nQta 68,68,33024\ns_add_u32 m0, s91, 37888\ns_add_u32 s31, s77, 151552\nQmvav 224,76,116\nbuffer_load_dwordx4 v79, s[48:51], s31 offen sc1 lds\n"
"s_waitcnt vmcnt(36)\ns_waitcnt lgkmcnt(4)\nQmaaa 140,56,24\nQmaaa 140,60,52\nQta 70,68,34048\nQmaaa 136,56,20\nQmaaa 136,60,48\nbuffer_load_dwordx4 v79, s[48:51], s31 offen offset:1024 sc1 lds\n"
"Qmaaa 132,56,16\nQta 72,68,25088\nQmaaa 132,60,44\nQmaaa 128,56,12\nQmaaa 128,60,40\nQta 74,68,26112\nQmaaa 124,56,8\nbuffer_load_dwordx4 v79, s[48:51], s31 offen offset:2048 sc1 lds\n"
"Qmaaa 124,60,36\nQmaaa 120,56,4\nQta 76,68,33280\nQmaaa 120,60,32\nQmaaa 116,56,0\nQmaaa 116,60,28\nQta 78,68,34304\ns_add_u32 m0, s96, 0\n"
"s_add_u32 s95, s73, 86016\ns_waitcnt lgkmcnt(4)\nQmaaa 144,64,0\nbuffer_load_dwordx4 v63, s[52:55], s95 offen lds\nQmaaa 144,68,28\nQmaaa 148,64,4\nQmaaa 148,68,32\nQmaaa 152,64,8\n"
"Qta 56,68,25344\ns_add_u32 s95, s73, 105472\nQmaaa 152,68,36\nbuffer_load_dwordx4 v63, s[52:55], s95 offen offset:1024 lds\nQmaaa 156,64,12\nQmaaa 156,68,40\nQta 58,68,26368\nQmaaa 160,64,16\n"
"Qmaaa 160,68,44\nQmaaa 164,64,20\nQta 60,68,33536\ns_add_u32 m0, s96, 8192\ns_add_u32 s95, s73, 126976\nQmaaa 164,68,48\nbuffer_load_dwordx4 v63, s[52:55], s95 offen lds\nQmaaa 168,64,24\n"
"Qmaaa 168,68,52\nQta 62,68,34560\ns_waitcnt lgkmcnt(4)\nQmvaa 24,72,24\nQmvaa 24,76,52\ns_add_u32 s95, s73, 146432\nQmvaa 20,72,20\nbuffer_load_dwordx4 v63, s[52:55], s95 offen offset:1024 lds\n"
"Qmvaa 20,76,48\nQmvaa 16,72,16\nQta 64,68,40960\nQmvaa 16,76,44\ns_add_u32 s31, s75, 131072\nQmvaa 12,72,12\nbuffer_load_dwordx4 v[80:83], v79, s[48:51], s31 offen offset:0 sc1\nQmvaa 12,76,40\n"
"Qta 66,68,41472\nQmvaa 8,72,8\nQmvaa 8,76,36\nQmvaa 4,72,4\nQta 68,68,41984\nQmvaa 4,76,32\nbuffer_load_dwordx4 v[112:115], v79, s[48:51], s31 offen offset:1024 sc1\nQmvaa 0,72,0\n"
"Qmvaa 0,76,28\nQta 70,68,42496\ns_waitcnt lgkmcnt(4)\nQmvaa 28,56,0\nQmvaa 28,60,28\nbuffer_load_dwordx4 v[144:147], v79, s[48:51], s31 offen offset:2048 sc1\nQmvaa 32,56,4\nQmvaa 32,60,32\n"
"Qmvaa 36,56,8\nQta 72,68,41216\nQmvaa 36,60,36\nbuffer_load_dwordx4 v[176:179], v79, s[48:51], s31 offen offset:3072 sc1\nQmvaa 40,56,12\nQmvaa 40,60,40\nQta 74,68,41728\nQmvaa 44,56,16\n"
"s_add_u32 s31, s75, 135168\nQmvaa 44,60,44\nbuffer_load_dwordx4 v[208:211], v79, s[48:51], s31 offen offset:0 sc1\nQmvaa 48,56,20\nQta 76,68,42240\nQmvaa 48,60,48\nv_add_u32 v75, 1, v75\nQmvaa 52,56,24\n"
"s_waitcnt vmcnt(35)\nds_write_b32 v73, v75\nQmvaa 52,60,52\nQta 78,68,42752\ns_waitcnt lgkmcnt(5)\nQmaaa 196,64,24\nbuffer_load_dwordx4 v[240:243], v79, s[48:51], s31 offen offset:1024 sc1\nQmaaa 196,68,52\n"
"Qmaaa 192,64,20\nQmaaa 192,68,48\nQmaaa 188,64,16\nQta 56,68,49152\nQmaaa 188,68,44\nbuffer_load_dwordx4 v[84:87], v79, s[48:51], s31 offen offset:2048 sc1\nQmaaa 184,64,12\nds_read_b32 v66, v74\n"
"Qmaaa 184,68,40\nQta 58,68,50176\nQmaaa 180,64,8\ns_add_u32 s31, s75, 147456\nQmaaa 180,68,36\nbuffer_load_dwordx4 v[88:91], v79, s[48:51], s31 offen offset:0 sc1\nQmaaa 176,64,4\ns_waitcnt lgkmcnt(1)\n"
"v_cmp_gt_u32 vcc, v75, v66\nQmaaa 176,68,32\ns_cbranch_vccnz .Lso1735r0\n.Lro1735r0:\nQmaaa 172,64,0\nQta 60,68,57344\nQmaaa 172,68,28\nbuffer_load_dwordx4 v[120:123], v79, s[48:51], s31 offen offset:1024 sc1\n"
"s_waitcnt lgkmcnt(4)\nQmvaa 92,72,0\nQmvaa 92,76,28\nQta 62,68,58368\nQmvaa 124,72,4\nQmvaa 124,76,32\nbuffer_load_dwordx4 v[152:155], v79, s[48:51], s31 offen offset:2048 sc1\nQmvaa 156,72,8\n"
"Qta 64,68,49408\nQmvaa 156,76,36\nQmvaa 188,72,12\nQmvaa 188,76,40\nQta 66,68,50432\nQmvaa 220,72,16\nbuffer_load_dwordx4 v[184:187], v79, s[48:51], s31 offen offset:3072 sc1\nQmvaa 220,76,44\n"
"Qmvaa 252,72,20\nQta 68,68,57600\nQmvaa 252,76,48\ns_add_u32 s31, s75, 151552\nQmvaa 100,72,24\nbuffer_load_dwordx4 v[216:219], v79, s[48:51], s31 offen offset:0 sc1\nQmvaa 100,76,52\nQta 70,68,58624\n"
"s_waitcnt lgkmcnt(4)\nQmaaa 224,56,24\nQmaaa 224,60,52\nQmaaa 220,56,20\nbuffer_load_dwordx4 v[248:251], v79, s[48:51], s31 offen offset:1024 sc1\nQmaaa 220,60,48\nQmaaa 216,56,16\nQta 72,68,49664\n"
"Qmaaa 216,60,44\nQmaaa 212,56,12\nbuffer_load_dwordx4 v[116:119], v79, s[48:51], s31 offen offset:2048 sc1\nQmaaa 212,60,40\nQta 74,68,50688\nQmaaa 208,56,8\nQmaaa 208,60,36\nQmaaa 204,56,4\n"
"Qta 76,68,57856\ns_add_u32 m0, s96, 16384\ns_add_u32 s95, s67, 86016\nQmaaa 204,60,32\nbuffer_load_dwordx4 v61, s[52:55], s95 offen lds\nQmaaa 200,56,0\nQmaaa 200,60,28\nQta 78,68,58880\n"
"s_waitcnt lgkmcnt(4)\nQmaaa 228,64,0\nQmaaa 228,68,28\ns_add_u32 s95, s67, 105472\nQmaaa 232,64,4\nbuffer_load_dwordx4 v61, s[52:55], s95 offen offset:1024 lds\nQmaaa 232,68,32\nQmaaa 236,64,8\n"
"Qta 56,68,49920\nQmaaa 236,68,36\nQmaaa 240,64,12\nQmaaa 240,68,40\nQta 58,68,50944\ns_add_u32 m0, s96, 24576\ns_add_u32 s95, s67, 126976\nQmaaa 244,64,16\n"
"buffer_load_dwordx4 v61, s[52:55], s95 offen lds\nQmaaa 244,68,44\nQmaaa 248,64,20\nQta 60,68,58112\nQmaaa 248,68,48\nQmaaa 252,64,24\nQmaaa 252,68,52\nQta 62,68,59136\n"
"s_add_u32 s95, s67, 146432\ns_waitcnt lgkmcnt(4)\nQmvaa 172,72,24\nbuffer_load_dwordx4 v61, s[52:55], s95 offen offset:1024 lds\nQmvaa 172,76,52\nQmvaa 140,72,20\nQmvaa 140,76,48\nQmvaa 108,72,16\n"
"Qta 64,69,0\ns_add_u32 m0, s96, 32768\ns_add_u32 s95, s72, 86016\nQmvaa 108,76,44\nbuffer_load_dwordx4 v61, s[52:55], s95 offen lds\nQmvaa 228,72,12\nQmvaa 228,76,40\nQta 66,69,1024\n"
"Qmvaa 196,72,8\nQmvaa 196,76,36\nQmvaa 164,72,4\nQta 68,69,8192\ns_add_u32 s95, s72, 105472\nQmvaa 164,76,32\nbuffer_load_dwordx4 v61, s[52:55], s95 offen offset:1024 lds\nv_add_u32 v75, 1, v75\n"
"Qmvaa 132,72,0\ns_waitcnt vmcnt(20)\nds_write_b32 v73, v75\nQmvaa 132,76,28\nQta 70,69,9216\ns_waitcnt lgkmcnt(5)\nQmvaa 204,56,0\ns_waitcnt vmcnt(24)\n"
"Qda 0,58,0\nQmvaa 204,60,28\nQmvaa 236,56,4\nQda 28,58,33792\nQmvaa 236,60,32\nQmvaa 96,56,8\nQta 72,69,16384\nQmvaa 96,60,36\n"
"Qda 4,58,1024\nQmvaa 128,56,12\nds_read_b32 v66, v74\nQmvaa 128,60,40\nQta 74,69,17408\nQmvaa 160,56,16\nQda 32,58,34816\nQmvaa 160,60,44\n"
"Qmvaa 192,56,20\ns_waitcnt lgkmcnt(2)\nv_cmp_gt_u32 vcc, v75, v66\nQmvaa 192,60,48\ns_cbranch_vccnz .Lso1805r0\n.Lro1805r0:\nQmvaa 224,56,24\nQta 76,69,24576\n"
"s_add_u32 m0, s96, 40960\ns_add_u32 s95, s72, 126976\nQmvaa 224,60,52\nbuffer_load_dwordx4 v61, s[52:55], s95 offen lds\ns_waitcnt vmcnt(39)\ns_waitcnt lgkmcnt(8)\nQmaaa 200,64,80\nQda 8,58,2048\n"
"Qmaaa 200,68,108\nQta 78,69,25600\nQmaaa 204,64,84\nQda 36,58,35840\nQmaaa 204,68,112\nQmaaa 208,64,88\nQta 56,68,0\ns_add_u32 s95, s72, 146432\n"
"Qmaav 208,68,168\nbuffer_load_dwordx4 v61, s[52:55], s95 offen offset:1024 lds\nQmaaa 212,64,92\nQda 12,58,3072\nQmaav 212,68,200\nQta 58,68,1024\nQmaaa 216,64,96\nQda 40,58,36864\n"
"Qmaav 216,68,232\nQmaaa 220,64,100\nQta 60,68,8192\ns_add_u32 m0, s96, 49152\ns_add_u32 s95, s73, 86016\nQmaav 220,68,212\nbuffer_load_dwordx4 v61, s[52:55], s95 offen lds\nQmaaa 224,64,104\n"
"Qda 16,58,4096\nQmaav 224,68,244\nQta 62,68,9216\ns_waitcnt vmcnt(9)\ns_waitcnt lgkmcnt(8)\nQmaav 200,72,80\nQda 44,58,37888\nQmaav 200,76,88\n"
"s_add_u32 s95, s73, 105472\nQmaav 204,72,112\nbuffer_load_dwordx4 v61, s[52:55], s95 offen offset:1024 lds\nQmaav 204,76,120\nQda 20,58,5120\nQmaav 208,72,144\nQta 64,69,256\nQmaav 208,76,152\n"
"Qda 48,58,38912\nQmaav 212,72,176\nQmaav 212,76,184\nQta 66,69,1280\ns_add_u32 m0, s96, 57344\ns_add_u32 s95, s73, 126976\nQmaav 216,72,208\nbuffer_load_dwordx4 v61, s[52:55], s95 offen lds\n"
"Qmaav 216,76,216\nQda 24,58,6144\nQmaav 220,72,240\nQta 68,69,8448\nQmaav 220,76,248\nQda 52,58,39936\nQmaav 224,72,84\nQmaav 224,76,116\n"
"Qta 70,69,9472\ns_add_u32 s95, s73, 146432\ns_waitcnt lgkmcnt(9)\nQmaaa 200,56,0\nbuffer_load_dwordx4 v61, s[52:55], s95 offen offset:1024 lds\nQmaaa 200,60,28\nQmaaa 204,56,4\nQmaaa 204,60,32\n"
"s_waitcnt lgkmcnt(9)\nQmaaa 208,56,8\nQta 72,69,16640\nQmaaa 208,60,36\ns_nop 1\nds_write_b128 v58, a[200:203] offset:0\nQmaaa 212,56,12\nQmaaa 212,60,40\n"
"Qta 74,69,17664\ns_waitcnt lgkmcnt(4)\nQmaaa 216,56,16\nds_write_b128 v58, a[204:207] offset:1024\nQmaaa 216,60,44\nQmaaa 220,56,20\nQta 76,69,24832\nQmaaa 220,60,48\n"
"ds_write_b128 v58, a[208:211] offset:2048\nQmaaa 224,56,24\nQmaaa 224,60,52\nQta 78,69,25856\ns_waitcnt lgkmcnt(7)\nQmaaa 228,64,80\nds_write_b128 v58, a[212:215] offset:3072\nQmaaa 228,68,108\n"
"Qmaaa 232,64,84\nQmaaa 232,68,112\nds_write_b128 v58, a[216:219] offset:4096\nQmaaa 236,64,88\nQta 56,68,256\nQmaav 236,68,168\nQmaaa 240,64,92\nds_write_b128 v58, a[220:223] offset:5120\n"
"Qmaav 240,68,200\nQta 58,68,1280\nQmaaa 244,64,96\nQmaav 244,68,232\nds_write_b128 v58, a[224:227] offset:6144\nQmaaa 248,64,100\nQta 60,68,8448\nQmaav 248,68,212\n"
"Qmaaa 252,64,104\nQmaav 252,68,244\nQta 62,68,9472\ns_waitcnt lgkmcnt(8)\nQmaav 228,72,80\nv_add_u32 v75, 1, v75\nQmaav 228,76,88\nds_write_b32 v73, v75\n"
"Qmaav 232,72,112\nQmaav 232,76,120\nQmaav 236,72,144\nQta 64,69,512\nQmaav 236,76,152\nQmaav 240,72,176\nQmaav 240,76,184\nQta 66,69,1536\n"
"Qmaav 244,72,208\nQmaav 244,76,216\nds_read_b32 v66, v74\nQmaav 248,72,240\nQta 68,69,8704\nQmaav 248,76,248\nQmaav 252,72,84\nQmaav 252,76,116\n"
"s_waitcnt lgkmcnt(1)\nv_cmp_gt_u32 vcc, v75, v66\ns_waitcnt lgkmcnt(5)\nQmaaa 228,56,0\ns_cbranch_vccnz .Lso1878r0\n.Lro1878r0:\nQmaaa 228,60,28\nQta 70,69,9728\n"
"Qmaaa 232,56,4\nQmaaa 232,60,32\nQdv 148,59,2048\nQmaaa 236,56,8\nQta 72,69,16896\nQmaaa 236,60,36\n.Lpg20:\ns_cbranch_execz .Lpg24\n"
"s_nop 0\nds_write_b128 v58, a[228:231] offset:33792\nQmaaa 240,56,12\nQdv 180,59,3072\nQmaaa 240,60,40\nQta 74,69,17920\nQmaaa 244,56,16\nds_write_b128 v58, a[232:235] offset:34816\n"
"Qmaaa 244,60,44\nQdv 104,59,5120\nQmaaa 248,56,20\nQta 76,69,25088\nQmaaa 248,60,48\nds_write_b128 v58, a[236:239] offset:35840\nQmaaa 252,56,24\nQdv 136,59,0\n"
"s_waitcnt lgkmcnt(9)\nQa 196,196,148\nQmaaa 252,60,52\nQta 78,69,26112\nQa 197,197,149\ns_waitcnt lgkmcnt(10)\nQmvaa 132,64,80\nds_write_b128 v58, a[240:243] offset:36864\n"
"Qmvaa 132,68,108\nQa 198,198,150\nQa 199,199,151\nQmvaa 164,64,84\ns_waitcnt lgkmcnt(8)\nQa 228,228,180\nQa 229,229,181\nQmvaa 164,68,112\n"
"ds_write_b128 v58, a[244:247] offset:37888\nQmvaa 196,64,88\nQta 56,68,512\nQa 230,230,182\nQa 231,231,183\nQmvav 196,68,168\nQdv 148,59,4096\ns_waitcnt lgkmcnt(8)\n"
"Qa 140,140,104\nQmvaa 228,64,92\nds_write_b128 v58, a[248:251] offset:38912\nQmvav 228,68,200\nQta 58,68,1536\nQa 141,141,105\nQmvaa 108,64,96\nQa 142,142,106\n"
"Qa 143,143,107\nQmvav 108,68,232\nQdv 180,59,6144\ns_waitcnt lgkmcnt(8)\nQa 132,132,136\nQmvaa 140,64,100\nQta 60,68,8704\nQa 133,133,137\n"
"Qmvav 140,68,212\nds_write_b128 v58, a[252:255] offset:39936\nQmvaa 172,64,104\nQa 134,134,138\nQa 135,135,139\nQmvav 172,68,244\nQta 62,68,9728\ns_waitcnt lgkmcnt(6)\n"
"Qa 108,108,148\ns_waitcnt lgkmcnt(6)\nQmvav 132,72,80\nQdv 104,59,1024\nv_add_u32 v75, 1, v75\nQmvav 132,76,88\ns_waitcnt vmcnt(10)\nds_write_b32 v73, v75\n"
"Qmvav 164,72,112\nQa 109,109,149\nQa 110,110,150\nQmvav 164,76,120\nQa 111,111,151\ns_waitcnt lgkmcnt(5)\nQa 172,172,180\nQmvav 196,72,144\n"
"Qta 64,69,768\nQa 173,173,181\nQmvav 196,76,152\nQa 174,174,182\nQa 175,175,183\nQmvav 228,72,176\nQmvav 228,76,184\nQta 66,69,1792\n"
"s_waitcnt lgkmcnt(3)\nQa 164,164,104\nQmvav 108,72,208\nQa 165,165,105\nQa 166,166,106\nQmvav 108,76,216\nds_read_b32 v66, v74\nQa 167,167,107\n"
"Qmvav 140,72,240\nQta 68,69,8960\nQmvav 140,76,248\nQmvav 172,72,84\nQmvav 172,76,116\ns_waitcnt lgkmcnt(1)\nv_cmp_gt_u32 vcc, v75, v66\ns_waitcnt lgkmcnt(6)\n"
"Qmvaa 132,56,0\ns_cbranch_vccnz .Lso1920r0\n.Lro1920r0:\ns_add_u32 s68, s64, 0\nQmvaa 132,60,28\nQta 70,69,9984\nQmvaa 164,56,4\nQmvaa 164,60,32\n"
"Qdv 136,59,35840\nQmvaa 196,56,8\nQta 72,69,17152\ns_nop 1\nQk 132,132,133\nQmvaa 196,60,36\nQk 133,134,135\nQmvaa 228,56,12\n"
"Qdv 148,59,36864\nQk 134,164,165\nQmvaa 228,60,40\nQta 74,69,18176\nQk 135,166,167\nQmvaa 108,56,16\nbuffer_store_dwordx4 v[132:135], v79, s[24:27], s68 offen offset:0 sc1\nQk 196,196,197\n"
"Qmvaa 108,60,44\nQdv 180,59,38912\nQk 197,198,199\nQmvaa 140,56,20\nQta 76,69,25344\nQk 198,228,229\nQmvaa 140,60,48\ns_waitcnt lgkmcnt(5)\n"
"Qa 96,96,136\nQa 97,97,137\nQmvaa 172,56,24\nQdv 104,59,33792\nQa 98,98,138\nQmvaa 172,60,52\nQta 78,69,26368\nQa 99,99,139\n"
"s_waitcnt lgkmcnt(11)\nQmvaa 204,64,80\ns_waitcnt lgkmcnt(5)\nQa 128,128,148\nQa 129,129,149\nQmvaa 204,68,108\nQa 130,130,150\nQa 131,131,151\n"
"Qmvaa 236,64,84\nQk 199,230,231\nQk 108,108,109\nQmvaa 236,68,112\ns_waitcnt lgkmcnt(3)\nQa 192,192,180\nQa 193,193,181\nQmvaa 96,64,88\n"
"Qta 56,68,768\nQa 194,194,182\nQmvav 96,68,168\nQdv 136,59,37888\nQa 195,195,183\nQmvaa 128,64,92\ns_waitcnt lgkmcnt(3)\nQa 204,204,104\n"
"Qa 205,205,105\nQmvav 128,68,200\nQta 58,68,1792\nQa 206,206,106\nQmvaa 160,64,96\nbuffer_store_dwordx4 v[196:199], v79, s[24:27], s68 offen offset:1024 sc1\nQa 207,207,107\nQmvav 160,68,232\n"
"Qdv 148,59,39936\nQk 109,110,111\nQmvaa 192,64,100\nQta 60,68,8960\nQk 110,140,141\nQmvav 192,68,212\nQk 111,142,143\nQk 172,172,173\n"
"Qmvaa 224,64,104\nbuffer_store_dwordx4 v[108:111], v79, s[24:27], s68 offen offset:2048 sc1\nv_add_u32 v75, 1, v75\nQmvav 224,68,244\ns_waitcnt vmcnt(3)\nds_write_b32 v73, v75\ns_waitcnt lgkmcnt(10)\nQmvav 204,72,80\n"
"Qta 62,68,9984\ns_waitcnt lgkmcnt(5)\nQa 160,160,136\nQmvav 204,76,88\nQdv 180,59,34816\nQa 161,161,137\nQmvav 236,72,112\nQa 162,162,138\n"
"Qa 163,163,139\nQmvav 236,76,120\ns_waitcnt lgkmcnt(4)\nQa 224,224,148\nQa 225,225,149\nQmvav 96,72,144\nQta 64,68,16384\nQa 226,226,150\n"
"Qmvav 96,76,152\nQa 227,227,151\nQk 173,174,175\nQmvav 128,72,176\nQmvav 128,76,184\nds_read_b32 v66, v74\ns_waitcnt lgkmcnt(2)\nQa 236,236,180\n"
"Qmvav 160,72,208\nQta 66,68,17408\nQa 237,237,181\nQmvav 160,76,216\nQa 238,238,182\nQa 239,239,183\nQmvav 192,72,240\nQmvav 192,76,248\n"
"s_waitcnt lgkmcnt(1)\nv_cmp_gt_u32 vcc, v75, v66\nQmvav 224,72,84\ns_cbranch_vccnz .Lso1960r0\n.Lro1960r0:\nQmvav 224,76,116\nQta 68,68,24576\ns_add_u32 m0, s96, 65536\n"
"s_add_u32 s95, s67, 86016\ns_waitcnt lgkmcnt(5)\nQmvaa 204,56,0\nbuffer_load_dwordx4 v62, s[52:55], s95 offen lds\nQmvaa 204,60,28\nQmvaa 236,56,4\nQta 70,68,25600\nQmvaa 236,60,32\n"
"Qmvaa 96,56,8\ns_nop 3\nQk 174,204,205\nQk 175,206,207\nQmvaa 96,60,36\nQta 72,68,32768\ns_add_u32 s95, s67, 125952\nQmvaa 128,56,12\n"
"buffer_load_dwordx4 v62, s[52:55], s95 offen offset:1024 lds\nQmvaa 128,60,40\nbuffer_store_dwordx4 v[172:175], v79, s[24:27], s68 offen offset:3072 sc1\nQk 236,236,237\nQmvaa 160,56,16\nQta 74,68,33792\nQk 237,238,239\nQmvaa 160,60,44\n"
"Qk 238,96,97\nQk 239,98,99\nQmvaa 192,56,20\ns_add_u32 s68, s64, 4096\nQk 128,128,129\nQmvaa 192,60,48\nQta 76,68,40960\ns_add_u32 m0, s96, 73728\n"
"s_add_u32 s95, s72, 86016\nQmvaa 224,56,24\nbuffer_load_dwordx4 v62, s[52:55], s95 offen lds\nQmvaa 224,60,52\nbuffer_store_dwordx4 v[236:239], v79, s[24:27], s68 offen offset:0 sc1\nQk 129,130,131\ns_waitcnt lgkmcnt(3)\nQmaaa 116,64,80\n"
"Qta 78,68,41984\nQk 130,160,161\nQmaaa 116,68,108\nQk 131,162,163\nQk 192,192,193\nQmaaa 120,64,84\nbuffer_store_dwordx4 v[128:131], v79, s[24:27], s68 offen offset:1024 sc1\ns_add_u32 s95, s72, 125952\n"
"Qmaaa 120,68,112\nbuffer_load_dwordx4 v62, s[52:55], s95 offen offset:1024 lds\nQmaaa 124,64,88\nQta 56,68,49152\nQk 193,194,195\nQmaav 124,68,168\nQk 194,224,225\nQk 195,226,227\n"
"Qmaaa 128,64,92\nbuffer_store_dwordx4 v[192:195], v79, s[24:27], s68 offen offset:2048 sc1\nQmaav 128,68,200\nQta 58,68,50176\ns_add_u32 m0, s96, 81920\ns_add_u32 s95, s73, 86016\nQmaaa 132,64,96\nbuffer_load_dwordx4 v62, s[52:55], s95 offen lds\n"
"Qmaav 132,68,232\nQmaaa 136,64,100\nQta 60,68,57344\nQmaav 136,68,212\nQmaaa 140,64,104\nQmaav 140,68,244\nQta 62,68,58368\ns_add_u32 s95, s73, 125952\n"
"s_waitcnt lgkmcnt(4)\nQmaav 116,72,80\nbuffer_load_dwordx4 v62, s[52:55], s95 offen offset:1024 lds\nQmaav 116,76,88\nQmaav 120,72,112\nv_bfe_u32 v61, v60, 6, 2\nv_lshrrev_b32 v62, 8, v60\nQmaav 120,76,120\n"
"v_xor_b32 v61, v61, v62\nv_bfe_u32 v62, v60, 5, 1\nQmaav 124,72,144\nQta 64,68,16640\nv_lshl_or_b32 v61, v61, 1, v62\nQmaav 124,76,152\nv_and_b32 v62, 16, v60\nv_mov_b32 v63, 0x3f80\n"
"Qmaav 128,72,176\nv_lshlrev_b32 v63, v62, v63\nv_min_u32 v62, 1, v61\nQmaav 128,76,184\nQta 66,68,17664\nv_add_u32 v62, -1, v62\nQmaav 132,72,208\nv_and_b32 v62, v62, v63\n"
"v_xor_b32 v61, 1, v61\nQmaav 132,76,216\nv_min_u32 v61, 1, v61\nv_add_u32 v61, -1, v61\nQmaav 136,72,240\nQta 68,68,24832\nv_and_b32 v61, v61, v63\nQmaav 136,76,248\n"
"v_accvgpr_write_b32 a202, v62\nv_accvgpr_write_b32 a203, v61\nQmaav 140,72,84\nv_accvgpr_write_b32 a200, 0\nv_accvgpr_write_b32 a201, 0\nQmaav 140,76,116\nQta 70,68,25856\nv_accvgpr_write_b32 a204, 0\n"
"s_waitcnt lgkmcnt(4)\nQmaaa 116,56,0\nv_accvgpr_write_b32 a205, 0\nQmaaa 116,60,28\nQmaaa 120,56,4\nQmaaa 120,60,32\nQmaaa 124,56,8\nQta 72,68,33024\n"
"Qmaaa 124,60,36\ns_nop 2\nds_write_b128 v58, a[116:119] offset:0\nQmaaa 128,56,12\nQmaaa 128,60,40\nQta 74,68,34048\nQmaaa 132,56,16\nds_write_b128 v58, a[120:123] offset:1024\n"
"Qmaaa 132,60,44\nQmaaa 136,56,20\nQta 76,68,41216\nQmaaa 136,60,48\nds_write_b128 v58, a[124:127] offset:2048\nQmaaa 140,56,24\nQmaaa 140,60,52\nQta 78,68,42240\n"
"s_waitcnt lgkmcnt(7)\nQmaaa 144,64,80\nds_write_b128 v58, a[128:131] offset:3072\nQmaaa 144,68,108\nQmaaa 148,64,84\nQmaaa 148,68,112\nds_write_b128 v58, a[132:135] offset:4096\nQmaaa 152,64,88\n"
"Qta 56,68,49408\nQmaav 152,68,168\nQmaaa 156,64,92\nds_write_b128 v58, a[136:139] offset:5120\nQmaav 156,68,200\nQta 58,68,50432\nQmaaa 160,64,96\nQmaav 160,68,232\n"
"ds_write_b128 v58, a[140:143] offset:6144\nQmaaa 164,64,100\nQta 60,68,57600\nQmaav 164,68,212\nQmaaa 168,64,104\nQmaav 168,68,244\nQta 62,68,58624\ns_waitcnt lgkmcnt(8)\n"
"Qmaav 144,72,80\nv_add_u32 v75, 1, v75\nQmaav 144,76,88\ns_waitcnt vmcnt(8)\nds_write_b32 v73, v75\nQmaav 148,72,112\nQmaav 148,76,120\nQmaav 152,72,144\n"
"Qta 64,68,16896\nQmaav 152,76,152\nQmaav 156,72,176\nQmaav 156,76,184\nQta 66,68,17920\nQmaav 160,72,208\nQmaav 160,76,216\nds_read_b32 v66, v74\n"
"Qmaav 164,72,240\nQta 68,68,25088\ns_waitcnt vmcnt(2)\nbuffer_store_dword v67, v77, s[24:27], 0 offen sc1\nQmaav 164,76,248\nQmaav 168,72,84\nQmaav 168,76,116\ns_waitcnt lgkmcnt(1)\n"
"v_cmp_gt_u32 vcc, v75, v66\ns_waitcnt lgkmcnt(5)\nQmaaa 144,56,0\ns_cbranch_vccnz .Lso2046r0\n.Lro2046r0:\nQmaaa 144,60,28\nQta 70,68,26112\nQmaaa 148,56,4\n"
"Qmaaa 148,60,32\nQdv 104,59,2048\nQmaaa 152,56,8\nQta 72,68,33280\nQmaaa 152,60,36\ns_nop 0\nds_write_b128 v58, a[144:147] offset:33792\nQmaaa 156,56,12\n"
"Qdv 136,59,3072\nQmaaa 156,60,40\nQta 74,68,34304\nQmaaa 160,56,16\nds_write_b128 v58, a[148:151] offset:34816\nQmaaa 160,60,44\nQdv 148,59,5120\nQmaaa 164,56,20\n"
"Qta 76,68,41472\nQmaaa 164,60,48\nds_write_b128 v58, a[152:155] offset:35840\nQmaaa 168,56,24\nQdv 180,59,0\ns_waitcnt lgkmcnt(9)\nQa 8,8,104\nQmaaa 168,60,52\n"
"Qta 78,68,42496\nQa 9,9,105\ns_waitcnt lgkmcnt(10)\nQmvaa 0,64,80\nds_write_b128 v58, a[156:159] offset:36864\nQmvaa 0,68,108\nQa 10,10,106\nQa 11,11,107\n"
"Qmvaa 4,64,84\ns_waitcnt lgkmcnt(8)\nQa 12,12,136\nQa 13,13,137\nQmvaa 4,68,112\nds_write_b128 v58, a[160:163] offset:37888\nQmvaa 8,64,88\nQta 56,68,49664\n"
"Qa 14,14,138\nQa 15,15,139\nQmvav 8,68,168\nQdv 104,59,4096\ns_waitcnt lgkmcnt(8)\nQa 20,20,148\nQmvaa 12,64,92\nds_write_b128 v58, a[164:167] offset:38912\n"
"Qmvav 12,68,200\nQta 58,68,50688\nQa 21,21,149\nQmvaa 16,64,96\nQa 22,22,150\nQa 23,23,151\nQmvav 16,68,232\nQdv 136,59,6144\n"
"s_waitcnt lgkmcnt(8)\nQa 0,0,180\nQmvaa 20,64,100\nQta 60,68,57856\nQa 1,1,181\nQmvav 20,68,212\nds_write_b128 v58, a[168:171] offset:39936\nQmvaa 24,64,104\n"
"Qa 2,2,182\nQa 3,3,183\nQmvav 24,68,244\nQta 62,68,58880\ns_waitcnt lgkmcnt(6)\nQa 16,16,104\ns_waitcnt lgkmcnt(6)\nQmvav 0,72,80\n"
"Qdv 148,59,1024\nv_add_u32 v75, 1, v75\nQmvav 0,76,88\ns_waitcnt vmcnt(1)\nds_write_b32 v73, v75\nQmvav 4,72,112\nQa 17,17,105\nQa 18,18,106\n"
"Qmvav 4,76,120\nQa 19,19,107\ns_waitcnt lgkmcnt(5)\nQa 24,24,136\nQmvav 8,72,144\nQta 64,69,0\nQa 25,25,137\nQmvav 8,76,152\n"
"Qa 26,26,138\nQa 27,27,139\nQmvav 12,72,176\nQmvav 12,76,184\nQta 66,69,512\ns_waitcnt lgkmcnt(3)\nQa 4,4,148\nQmvav 16,72,208\n"
"Qa 5,5,149\nQa 6,6,150\nQmvav 16,76,216\nds_read_b32 v66, v74\nQa 7,7,151\nQmvav 20,72,240\nQta 68,69,1024\nQmvav 20,76,248\n"
"Qmvav 24,72,84\nQmvav 24,76,116\ns_waitcnt lgkmcnt(1)\nv_cmp_gt_u32 vcc, v75, v66\ns_waitcnt lgkmcnt(6)\nQmvaa 0,56,0\ns_cbranch_vccnz .Lso2088r0\n.Lro2088r0:\n"
"Qmvaa 0,60,28\nQta 70,69,1536\nQmvaa 4,56,4\nQmvaa 4,60,32\nQdv 180,59,33792\nQmvaa 8,56,8\nQta 72,69,8192\nQmvaa 8,60,36\n"
"Qmvaa 12,56,12\nQdv 104,59,34816\nQmvaa 12,60,40\nQta 74,69,8704\nQmvaa 16,56,16\nQmvaa 16,60,44\nQdv 136,59,35840\nQmvaa 20,56,20\n"
"Qta 76,69,9216\nQmvaa 20,60,48\ns_waitcnt lgkmcnt(5)\nQa 28,28,180\nQa 29,29,181\nQmvaa 24,56,24\nQdv 148,59,36864\nQa 30,30,182\n"
"Qmvaa 24,60,52\nQta 78,69,9728\nQa 31,31,183\ns_waitcnt lgkmcnt(11)\nQmaaa 172,64,80\ns_waitcnt lgkmcnt(5)\nQa 32,32,104\nQa 33,33,105\n"
"Qmaaa 172,68,108\nQa 34,34,106\nQa 35,35,107\nQmaaa 176,64,84\nQmaaa 176,68,112\ns_waitcnt lgkmcnt(3)\nQa 36,36,136\nQa 37,37,137\n"
"Qmaaa 180,64,88\nQta 56,69,16384\nQa 38,38,138\nQmaav 180,68,168\nQdv 180,59,37888\nQa 39,39,139\nQmaaa 184,64,92\ns_waitcnt lgkmcnt(3)\n"
"Qa 40,40,148\nQa 41,41,149\nQmaav 184,68,200\nQta 58,69,16896\nQa 42,42,150\nQmaaa 188,64,96\nQdv 104,59,38912\nQa 43,43,151\n"
"Qmaav 188,68,232\nQmaaa 192,64,100\nQta 60,69,17408\nQmaav 192,68,212\nQdv 136,59,39936\nQmaaa 196,64,104\ns_waitcnt lgkmcnt(4)\nQa 44,44,180\n"
"Qa 45,45,181\nQmaav 196,68,244\nQta 62,69,17920\nQa 46,46,182\ns_waitcnt lgkmcnt(5)\nQmaav 172,72,80\nQa 47,47,183\nQmaav 172,76,88\n"
".Lpg21:\ns_cbranch_execz .Lpg25\nQmaav 176,72,112\ns_waitcnt lgkmcnt(3)\nQa 48,48,104\nQa 49,49,105\nQmaav 176,76,120\nQa 50,50,106\n"
"Qa 51,51,107\nQmaav 180,72,144\nQta 64,68,17152\nQmaav 180,76,152\ns_waitcnt lgkmcnt(2)\nQa 52,52,136\nQa 53,53,137\nQmaav 184,72,176\n"
"Qa 54,54,138\nQa 55,55,139\nQmaav 184,76,184\nQta 66,68,18176\nQmaav 188,72,208\nQmaav 188,76,216\nQmaav 192,72,240\nQta 68,68,25344\n"
"Qmaav 192,76,248\nQmaav 196,72,84\nQmaav 196,76,116\nQta 70,68,26368\ns_waitcnt lgkmcnt(4)\nQmaaa 172,56,0\nQmaaa 172,60,28\nQmaaa 176,56,4\n"
"Qmaaa 176,60,32\nQmaaa 180,56,8\nQta 72,68,33536\nQmaaa 180,60,36\ns_nop 2\nds_write_b128 v58, a[172:175] offset:0\nQmaaa 184,56,12\nQmaaa 184,60,40\n"
"Qta 74,68,34560\nQmaaa 188,56,16\nds_write_b128 v58, a[176:179] offset:1024\nQmaaa 188,60,44\nQmaaa 192,56,20\nQta 76,68,41728\nQmaaa 192,60,48\nds_write_b128 v58, a[180:183] offset:2048\n"
"Qmaaa 196,56,24\nQmaaa 196,60,52\nQta 78,68,42752\ns_waitcnt lgkmcnt(7)\nQmvaa 28,64,80\nds_write_b128 v58, a[184:187] offset:3072\nQmvaa 28,68,108\nQmvaa 32,64,84\n"
"Qmvaa 32,68,112\nds_write_b128 v58, a[188:191] offset:4096\nQmvaa 36,64,88\nQta 56,68,49920\nQmvav 36,68,168\nQmvaa 40,64,92\nds_write_b128 v58, a[192:195] offset:5120\nQmvav 40,68,200\n"
"Qta 58,68,50944\nQmvaa 44,64,96\nQmvav 44,68,232\nds_write_b128 v58, a[196:199] offset:6144\nQmvaa 48,64,100\nQta 60,68,58112\nQmvav 48,68,212\nQmvaa 52,64,104\n"
"Qmvav 52,68,244\nQta 62,68,59136\ns_waitcnt lgkmcnt(8)\nQmvav 28,72,80\nv_add_u32 v75, 1, v75\nQmvav 28,76,88\nds_write_b32 v73, v75\nQmvav 32,72,112\n"
"Qmvav 32,76,120\nQmvav 36,72,144\nQta 64,69,256\nQmvav 36,76,152\nQmvav 40,72,176\nQmvav 40,76,184\nQta 66,69,768\nQmvav 44,72,208\n"
"Qmvav 44,76,216\nds_read_b32 v66, v74\nQmvav 48,72,240\nQta 68,69,1280\nQmvav 48,76,248\nQmvav 52,72,84\nQmvav 52,76,116\ns_waitcnt lgkmcnt(1)\n"
"v_cmp_gt_u32 vcc, v75, v66\ns_waitcnt lgkmcnt(5)\nQmvaa 28,56,0\ns_cbranch_vccnz .Lso2172r0\n.Lro2172r0:\nQmvaa 28,60,28\nbuffer_load_dword v78, v76, s[24:27], 0 offen sc1\nQmvaa 32,56,4\n"
"Qta 70,69,1792\nQmvaa 32,60,32\nQdv 148,59,2048\nQmvaa 36,56,8\nQmvaa 36,60,36\nQta 72,69,8448\nQmvaa 40,56,12\nQdv 180,59,3072\n"
"Qmvaa 40,60,40\nQmvaa 44,56,16\nQta 74,69,8960\nQmvaa 44,60,44\nQdv 104,59,5120\nQmvaa 48,56,20\nQmvaa 48,60,48\nQta 76,69,9472\n"
"s_waitcnt lgkmcnt(5)\nQa 156,156,148\nQmvaa 52,56,24\nQdv 136,59,0\nQa 157,157,149\nQmvaa 52,60,52\nQa 158,158,150\nQa 159,159,151\n"
"s_waitcnt lgkmcnt(10)\nQmvaa 92,64,80\nQta 78,69,9984\ns_waitcnt lgkmcnt(5)\nQa 188,188,180\nQmvaa 92,68,108\nQa 189,189,181\nQa 190,190,182\n"
"Qmvaa 124,64,84\nQa 191,191,183\nQmvaa 124,68,112\ns_waitcnt lgkmcnt(3)\nQa 252,252,104\nQa 253,253,105\nQmvaa 156,64,88\nQta 56,69,16640\n"
"Qa 254,254,106\nQmvav 156,68,168\nQdv 148,59,4096\nQa 255,255,107\nQmvaa 188,64,92\ns_waitcnt lgkmcnt(3)\nQa 92,92,136\nQa 93,93,137\n"
"Qmvav 188,68,200\nQta 58,69,17152\nQa 94,94,138\nQmvaa 220,64,96\nQa 95,95,139\nQmvav 220,68,232\nQdv 180,59,6144\nQmvaa 252,64,100\n"
"Qta 60,69,17664\nQmvav 252,68,212\nQmvaa 100,64,104\nQmvav 100,68,244\nQta 62,69,18176\ns_waitcnt lgkmcnt(4)\nQa 220,220,148\ns_waitcnt lgkmcnt(4)\n"
"Qmvav 92,72,80\nQdv 104,59,1024\nQa 221,221,149\nQmvav 92,76,88\nQa 222,222,150\nQa 223,223,151\nQmvav 124,72,112\nQmvav 124,76,120\n"
"s_waitcnt vmcnt(0)\nv_sub_u32 v66, v78, v65\nv_cmp_gt_u32 vcc, 1, v66\ns_and_b64 vcc, vcc, s[28:29]\ns_cbranch_scc1 .Lx0_w\n.Lx0_r:\nQmvav 156,72,144\ns_add_u32 s69, s98, 0\n"
"buffer_load_dwordx4 a[220:223], v79, s[24:27], s69 offen offset:3072 sc1\nQmvav 156,76,152\ns_waitcnt lgkmcnt(3)\nQa 100,100,180\nQa 101,101,181\nQmvav 188,72,176\ns_add_u32 s69, s98, 4096\nbuffer_load_dwordx4 a[236:239], v79, s[24:27], s69 offen offset:3072 sc1\n"
"Qmvav 188,76,184\nQa 102,102,182\nQa 103,103,183\nQmvav 220,72,208\nbuffer_load_dwordx4 a[224:227], v79, s[24:27], s69 offen offset:0 sc1\nv_add_u32 v75, 1, v75\nQmvav 220,76,216\nds_write_b32 v73, v75\n"
"s_waitcnt lgkmcnt(1)\nQa 124,124,104\nQmvav 252,72,240\ns_add_u32 s69, s98, 16384\nbuffer_load_dwordx4 a[240:243], v79, s[24:27], s69 offen offset:0 sc1\nQmvav 252,76,248\nQa 125,125,105\nQa 126,126,106\n"
"Qmvav 100,72,84\ns_add_u32 s69, s98, 4096\nbuffer_load_dwordx4 a[228:231], v79, s[24:27], s69 offen offset:1024 sc1\nQmvav 100,76,116\nQa 127,127,107\ns_waitcnt lgkmcnt(2)\nQmvaa 92,56,0\ns_add_u32 s69, s98, 16384\n"
"buffer_load_dwordx4 a[244:247], v79, s[24:27], s69 offen offset:1024 sc1\nQmvaa 92,60,28\nQmvaa 124,56,4\ns_add_u32 s69, s98, 4096\nbuffer_load_dwordx4 a[232:235], v79, s[24:27], s69 offen offset:2048 sc1\nQmvaa 124,60,32\nds_read_b32 v66, v74\nQmvaa 156,56,8\n"
"s_add_u32 s69, s98, 16384\nbuffer_load_dwordx4 a[248:251], v79, s[24:27], s69 offen offset:2048 sc1\nQmvaa 156,60,36\nQmvaa 188,56,12\ns_add_u32 s69, s98, 0\nbuffer_load_dwordx4 a[208:211], v79, s[24:27], s69 offen offset:0 sc1\nQmvaa 188,60,40\ns_waitcnt lgkmcnt(0)\n"
"v_cmp_gt_u32 vcc, v75, v66\nQmvaa 220,56,16\ns_cbranch_vccnz .Lso2222r0\n.Lro2222r0:\nQmvaa 220,60,44\nbuffer_load_dwordx4 a[212:215], v79, s[24:27], s69 offen offset:1024 sc1\nQmvaa 252,56,20\nQmvaa 252,60,48\n"
"buffer_load_dwordx4 a[216:219], v79, s[24:27], s69 offen offset:2048 sc1\nQmvaa 100,56,24\nQmvaa 100,60,52\ns_waitcnt vmcnt(10)\nQmvaa 0,200,220\ns_waitcnt vmcnt(9)\nQmvaa 28,202,236\ns_waitcnt vmcnt(8)\n"
"Qmvaa 4,202,224\nQmvaa 32,200,236\ns_nop 2\nQk 0,0,1\nQk 1,2,3\nds_write_b64 v56, v[0:1] offset:0\nQmvaa 8,200,224\nQk 28,28,29\n"
"Qk 29,30,31\nds_write_b64 v56, v[28:29] offset:32\ns_waitcnt vmcnt(7)\nQmvaa 36,202,240\nQk 4,4,5\nQk 5,6,7\nds_write_b64 v56, v[4:5] offset:3328\ns_waitcnt vmcnt(6)\n"
"Qmvaa 12,202,228\nQk 32,32,33\nQk 33,34,35\nds_write_b64 v56, v[32:33] offset:3360\nQmvaa 40,200,240\nQk 8,8,9\nQk 9,10,11\nds_write_b64 v56, v[8:9] offset:6656\n"
"Qmvaa 16,200,228\nQk 36,36,37\nQk 37,38,39\nds_write_b64 v56, v[36:37] offset:6688\ns_waitcnt lgkmcnt(0)\ns_barrier\nQda 0,57,0\nQda 4,57,3328\n"
"Qda 8,57,6656\ns_waitcnt vmcnt(5)\nQmvaa 44,202,244\nQk 12,12,13\nQk 13,14,15\nds_write_b64 v56, v[12:13] offset:9984\ns_mov_b32 s68, 0\ns_waitcnt lgkmcnt(3)\n"
"buffer_store_dwordx4 a[0:3], v64, s[40:43], s68 offen offset:0 sc1\ns_waitcnt vmcnt(5)\nQmvaa 20,202,232\nQk 40,40,41\nQk 41,42,43\nds_write_b64 v56, v[40:41] offset:10016\ns_mov_b32 s68, 40960\ns_waitcnt lgkmcnt(3)\n"
"buffer_store_dwordx4 a[4:7], v64, s[40:43], s68 offen offset:0 sc1\nQmvaa 48,200,244\nQk 16,16,17\nQk 17,18,19\nds_write_b64 v56, v[16:17] offset:13312\ns_mov_b32 s68, 81920\ns_waitcnt lgkmcnt(3)\nbuffer_store_dwordx4 a[8:11], v64, s[40:43], s68 offen offset:0 sc1\n"
"Qmvaa 24,200,232\nQk 44,44,45\nQk 45,46,47\nds_write_b64 v56, v[44:45] offset:13344\ns_waitcnt vmcnt(6)\nQmvaa 52,202,248\nQk 20,20,21\nQk 21,22,23\n"
"ds_write_b64 v56, v[20:21] offset:16640\ns_waitcnt vmcnt(5)\nQmvaa 92,202,208\nQk 48,48,49\nQk 49,50,51\nds_write_b64 v56, v[48:49] offset:16672\nQmvaa 124,200,208\nQk 24,24,25\n"
"Qk 25,26,27\nds_write_b64 v56, v[24:25] offset:19968\ns_waitcnt vmcnt(4)\nQmvaa 156,202,212\nQk 52,52,53\nQk 53,54,55\nds_write_b64 v56, v[52:53] offset:20000\ns_waitcnt lgkmcnt(0)\n"
"s_barrier\nQda 12,57,9984\nQda 16,57,13312\nQda 20,57,16640\nQda 24,57,19968\nQmvaa 188,200,212\nQk 92,92,93\nQk 93,94,95\n"
"ds_write_b64 v56, v[92:93] offset:64\ns_mov_b32 s68, 122880\ns_waitcnt lgkmcnt(4)\nbuffer_store_dwordx4 a[12:15], v64, s[40:43], s68 offen offset:0 sc1\ns_waitcnt vmcnt(4)\nQmvaa 220,202,216\nQk 124,124,125\nQk 125,126,127\n"
"ds_write_b64 v56, v[124:125] offset:3392\ns_mov_b32 s68, 163840\ns_waitcnt lgkmcnt(4)\nbuffer_store_dwordx4 a[16:19], v64, s[40:43], s68 offen offset:0 sc1\nQmvaa 252,200,216\nQk 156,156,157\nQk 157,158,159\nds_write_b64 v56, v[156:157] offset:6720\n"
"s_mov_b32 s68, 204800\ns_waitcnt lgkmcnt(4)\nbuffer_store_dwordx4 a[20:23], v64, s[40:43], s68 offen offset:0 sc1\nQmvaa 100,202,220\nQk 188,188,189\nQk 189,190,191\nds_write_b64 v56, v[188:189] offset:10048\ns_mov_b32 s68, 245760\n"
"s_waitcnt lgkmcnt(4)\nbuffer_store_dwordx4 a[24:27], v64, s[40:43], s68 offen offset:0 sc1\nQk 220,220,221\nQk 221,222,223\nds_write_b64 v56, v[220:221] offset:13376\nQk 252,252,253\nQk 253,254,255\nds_write_b64 v56, v[252:253] offset:16704\n"
"Qk 100,100,101\nQk 101,102,103\nds_write_b64 v56, v[100:101] offset:20032\ns_waitcnt lgkmcnt(0)\ns_barrier\nQda 28,71,64\nQda 32,71,6720\nQda 36,71,13376\n"
"Qda 40,71,20032\ns_mov_b32 s68, 0\ns_waitcnt lgkmcnt(3)\nbuffer_store_dwordx4 a[28:31], v72, s[40:43], s68 offen offset:0 sc1\ns_mov_b32 s68, 81920\ns_waitcnt lgkmcnt(2)\nbuffer_store_dwordx4 a[32:35], v72, s[40:43], s68 offen offset:0 sc1\ns_mov_b32 s68, 163840\n"
"s_waitcnt lgkmcnt(1)\nbuffer_store_dwordx4 a[36:39], v72, s[40:43], s68 offen offset:0 sc1\ns_mov_b32 s68, 245760\ns_cmp_eq_u32 s93, 1\ns_cselect_b32 s68, 0x7ffff000, s68\ns_waitcnt lgkmcnt(0)\nbuffer_store_dwordx4 a[40:43], v72, s[40:43], s68 offen offset:0 sc1\ns_branch .Lfin\n"
".Lrole1:\n.Lpg22:\ns_cbranch_execz .Lpg26\nQmvaa 96,64,88\nQta 56,69,49408\ns_lshl_b32 s46, s19, 4\nQmvav 96,68,168\nbuffer_load_dwordx4 a[84:87], v79, s[48:51], s31 offen offset:1024 sc1\n"
"Qmvaa 128,64,92\nds_read_b32 v66, v74\ns_add_u32 s46, s46, 128\nQmvav 128,68,200\nQta 58,69,50432\nv_mov_b32 v77, 0x7ffff000\nQmvaa 160,64,96\nQmvav 160,68,232\n"
"buffer_load_dwordx4 a[88:91], v79, s[48:51], s31 offen offset:2048 sc1\nQmvaa 192,64,100\ns_waitcnt lgkmcnt(1)\nv_cmp_gt_u32 vcc, v75, v66\nQmvav 192,68,212\ns_cbranch_vccnz .Lso1525r1\n.Lro1525r1:\nQmvaa 224,64,104\n"
"Qta 60,69,57600\nQmvav 224,68,244\nbuffer_load_dwordx4 a[92:95], v79, s[48:51], s31 offen offset:3072 sc1\ns_waitcnt vmcnt(31)\ns_waitcnt lgkmcnt(4)\nQmaav 140,72,84\nQmaav 140,76,116\nQta 62,69,58624\n"
"Qmaav 136,72,240\nQmaav 136,76,248\nQmaav 132,72,208\nQta 64,69,49664\ns_add_u32 m0, s96, 65536\ns_add_u32 s95, s67, 86016\nQmaav 132,76,216\nbuffer_load_dwordx4 v62, s[52:55], s95 offen lds\n"
"Qmaav 128,72,176\nQmaav 128,76,184\nQta 66,69,50688\nQmaav 124,72,144\nQmaav 124,76,152\nQmaav 120,72,112\nQta 68,69,57856\ns_add_u32 s95, s67, 125952\n"
"Qmaav 120,76,120\nbuffer_load_dwordx4 v62, s[52:55], s95 offen offset:1024 lds\nQmaav 116,72,80\nQmaav 116,76,88\nQta 70,69,58880\ns_waitcnt lgkmcnt(4)\nQmaav 144,56,80\nQmaav 144,60,88\n"
"s_add_u32 m0, s96, 73728\ns_add_u32 s95, s72, 86016\nQmaav 148,56,112\nbuffer_load_dwordx4 v62, s[52:55], s95 offen lds\nQmaav 148,60,120\nQmaav 152,56,144\nQta 72,69,49920\nQmaav 152,60,152\n"
"Qmaav 156,56,176\nQmaav 156,60,184\nQta 74,69,50944\ns_add_u32 s95, s72, 125952\nQmaav 160,56,208\nbuffer_load_dwordx4 v62, s[52:55], s95 offen offset:1024 lds\nQmaav 160,60,216\nQmaav 164,56,240\n"
"Qta 76,69,58112\nQmaav 164,60,248\nQmaav 168,56,84\nQmaav 168,60,116\nQta 78,69,59136\ns_add_u32 m0, s96, 81920\ns_add_u32 s95, s73, 86016\ns_waitcnt lgkmcnt(4)\n"
"Qmvav 24,64,84\nbuffer_load_dwordx4 v62, s[52:55], s95 offen lds\nQmvav 24,68,116\nQmvav 20,64,240\nQmvav 20,68,248\nQmvav 16,64,208\nQta 56,68,0\ns_add_u32 s95, s73, 125952\n"
"Qmvav 16,68,216\nbuffer_load_dwordx4 v62, s[52:55], s95 offen offset:1024 lds\nQmvav 12,64,176\nQmvav 12,68,184\nQta 58,68,512\nQmvav 8,64,144\nQmvav 8,68,152\nQmvav 4,64,112\n"
"Qta 60,68,1024\ns_add_u32 m0, s96, 90112\ns_add_u32 s95, s67, 86016\nQmvav 4,68,120\nbuffer_load_dwordx4 v61, s[52:55], s95 offen lds\nQmvav 0,64,80\nQmvav 0,68,88\nQta 62,68,1536\n"
"s_waitcnt lgkmcnt(4)\nQmvav 28,72,80\nQmvav 28,76,88\ns_add_u32 s95, s67, 105472\nQmvav 32,72,112\nbuffer_load_dwordx4 v61, s[52:55], s95 offen offset:1024 lds\nQmvav 32,76,120\nQmvav 36,72,144\n"
"Qta 64,68,256\nQmvav 36,76,152\ns_add_u32 s31, s74, 135168\nQmvav 40,72,176\nbuffer_load_dwordx4 a[96:99], v79, s[48:51], s31 offen offset:0 sc1\nQmvav 40,76,184\nQta 66,68,768\nQmvav 44,72,208\n"
"v_cmp_eq_u32 s[44:45], s46, v60\ns_nop 1\nv_cndmask_b32 v77, v77, v76, s[44:45]\nv_add_u32 v67, 1, v65\ns_xor_b32 s46, s19, 12\ns_lshl_b32 s28, 1, s46\nQmvav 44,76,216\nQmvav 48,72,240\n"
"Qta 68,68,1280\nQmvav 48,76,248\nbuffer_load_dwordx4 a[100:103], v79, s[48:51], s31 offen offset:1024 sc1\nv_add_u32 v75, 1, v75\nQmvav 52,72,84\ns_waitcnt vmcnt(35)\nds_write_b32 v73, v75\nQmvav 52,76,116\n"
"Qta 70,68,1792\ns_waitcnt lgkmcnt(5)\nQmaav 196,56,84\nQmaav 196,60,116\nbuffer_load_dwordx4 a[104:107], v79, s[48:51], s31 offen offset:2048 sc1\nQmaav 192,56,240\nQmaav 192,60,248\nQmaav 188,56,208\n"
"Qta 72,68,8192\ns_add_u32 s31, s74, 147456\nQmaav 188,60,216\nbuffer_load_dwordx4 a[108:111], v79, s[48:51], s31 offen offset:0 sc1\nQmaav 184,56,176\nds_read_b32 v66, v74\nQmaav 184,60,184\nQta 74,68,9216\n"
"Qmaav 180,56,144\nQmaav 180,60,152\nbuffer_load_dwordx4 a[112:115], v79, s[48:51], s31 offen offset:1024 sc1\nQmaav 176,56,112\ns_waitcnt lgkmcnt(1)\nv_cmp_gt_u32 vcc, v75, v66\nQmaav 176,60,120\ns_cbranch_vccnz .Lso1595r1\n"
".Lro1595r1:\nQmaav 172,56,80\nQta 76,68,16384\nQmaav 172,60,88\nbuffer_load_dwordx4 v[168:171], v79, s[48:51], s31 offen offset:2048 sc1\ns_waitcnt lgkmcnt(4)\nQmvav 92,64,80\nQmvav 92,68,88\n"
"Qta 78,68,17408\nQmvav 124,64,112\nQmvav 124,68,120\nbuffer_load_dwordx4 v[200:203], v79, s[48:51], s31 offen offset:3072 sc1\nQmvav 156,64,144\nQta 56,68,8448\nQmvav 156,68,152\nQmvav 188,64,176\n"
"Qmvav 188,68,184\nQta 58,68,9472\ns_add_u32 s31, s74, 151552\nQmvav 220,64,208\nbuffer_load_dwordx4 v[232:235], v79, s[48:51], s31 offen offset:0 sc1\nQmvav 220,68,216\nQmvav 252,64,240\nQta 60,68,16640\n"
"Qmvav 252,68,248\nQmvav 100,64,84\nbuffer_load_dwordx4 v[212:215], v79, s[48:51], s31 offen offset:1024 sc1\nQmvav 100,68,116\nQta 62,68,17664\n.Lpg23:\ns_cbranch_execz .Ltr8\ns_waitcnt lgkmcnt(4)\n"
"Qmaav 224,72,84\nQmaav 224,76,116\nQmaav 220,72,240\nbuffer_load_dwordx4 v[244:247], v79, s[48:51], s31 offen offset:2048 sc1\nQmaav 220,76,248\nQmaav 216,72,208\nQta 64,68,8704\nQmaav 216,76,216\n"
"s_add_u32 m0, s91, 0\ns_add_u32 s31, s77, 131072\nQmaav 212,72,176\nbuffer_load_dwordx4 v79, s[48:51], s31 offen sc1 lds\nQmaav 212,76,184\nQta 66,68,9728\nQmaav 208,72,144\nQmaav 208,76,152\n"
"Qmaav 204,72,112\nQta 68,68,16896\nQmaav 204,76,120\nbuffer_load_dwordx4 v79, s[48:51], s31 offen offset:1024 sc1 lds\nQmaav 200,72,80\nQmaav 200,76,88\nQta 70,68,17920\ns_waitcnt lgkmcnt(4)\n"
"Qmaav 228,56,80\nQmaav 228,60,88\nbuffer_load_dwordx4 v79, s[48:51], s31 offen offset:2048 sc1 lds\nQmaav 232,56,112\nQmaav 232,60,120\nQmaav 236,56,144\nQta 72,68,8960\nQmaav 236,60,152\n"
"buffer_load_dwordx4 v79, s[48:51], s31 offen offset:3072 sc1 lds\nQmaav 240,56,176\nQmaav 240,60,184\nQta 74,68,9984\nQmaav 244,56,208\ns_add_u32 m0, s91, 4096\ns_add_u32 s31, s77, 135168\nQmaav 244,60,216\n"
"buffer_load_dwordx4 v79, s[48:51], s31 offen sc1 lds\nQmaav 248,56,240\nQta 76,68,17152\nQmaav 248,60,248\nQmaav 252,56,84\nQmaav 252,60,116\nQta 78,68,18176\ns_waitcnt lgkmcnt(4)\n"
"Qmvav 172,64,84\nbuffer_load_dwordx4 v79, s[48:51], s31 offen offset:1024 sc1 lds\nQmvav 172,68,116\nQmvav 140,64,240\nQmvav 140,68,248\nQmvav 108,64,208\nQta 56,68,24576\nQmvav 108,68,216\n"
"buffer_load_dwordx4 v79, s[48:51], s31 offen offset:2048 sc1 lds\nQmvav 228,64,176\nQmvav 228,68,184\nQta 58,68,25600\nQmvav 196,64,144\ns_add_u32 m0, s91, 33792\ns_add_u32 s31, s77, 147456\nQmvav 196,68,152\n"
"buffer_load_dwordx4 v79, s[48:51], s31 offen sc1 lds\nQmvav 164,64,112\nQta 60,68,32768\nQmvav 164,68,120\nv_add_u32 v75, 1, v75\nQmvav 132,64,80\ns_waitcnt vmcnt(47)\nds_write_b32 v73, v75\n"
"Qmvav 132,68,88\nQta 62,68,33792\ns_waitcnt lgkmcnt(5)\nQmvav 204,72,80\nbuffer_load_dwordx4 v79, s[48:51], s31 offen offset:1024 sc1 lds\nQmvav 204,76,88\nQmvav 236,72,112\nQmvav 236,76,120\n"
"Qmvav 96,72,144\nQta 64,68,24832\nQmvav 96,76,152\nbuffer_load_dwordx4 v79, s[48:51], s31 offen offset:2048 sc1 lds\nQmvav 128,72,176\nds_read_b32 v66, v74\nQmvav 128,76,184\nQta 66,68,25856\n"
"Qmvav 160,72,208\nQmvav 160,76,216\nbuffer_load_dwordx4 v79, s[48:51], s31 offen offset:3072 sc1 lds\nQmvav 192,72,240\ns_waitcnt lgkmcnt(1)\nv_cmp_gt_u32 vcc, v75, v66\nQmvav 192,76,248\ns_cbranch_vccnz .Lso1665r1\n"
".Lro1665r1:\nQmvav 224,72,84\nQta 68,68,33024\ns_add_u32 m0, s91, 37888\ns_add_u32 s31, s77, 151552\nQmvav 224,76,116\nbuffer_load_dwordx4 v79, s[48:51], s31 offen sc1 lds\ns_waitcnt vmcnt(36)\n"
"s_waitcnt lgkmcnt(4)\nQmaaa 140,56,24\nQmaaa 140,60,52\nQta 70,68,34048\nQmaaa 136,56,20\nQmaaa 136,60,48\nbuffer_load_dwordx4 v79, s[48:51], s31 offen offset:1024 sc1 lds\nQmaaa 132,56,16\n"
"Qta 72,68,25088\nQmaaa 132,60,44\nQmaaa 128,56,12\nQmaaa 128,60,40\nQta 74,68,26112\nQmaaa 124,56,8\nbuffer_load_dwordx4 v79, s[48:51], s31 offen offset:2048 sc1 lds\nQmaaa 124,60,36\n"
"Qmaaa 120,56,4\nQta 76,68,33280\nQmaaa 120,60,32\nQmaaa 116,56,0\nQmaaa 116,60,28\nQta 78,68,34304\ns_add_u32 m0, s96, 0\ns_add_u32 s95, s67, 126976\n"
"s_waitcnt lgkmcnt(4)\nQmaaa 144,64,0\nbuffer_load_dwordx4 v61, s[52:55], s95 offen lds\nQmaaa 144,68,28\nQmaaa 148,64,4\nQmaaa 148,68,32\nQmaaa 152,64,8\nQta 56,68,25344\n"
"s_add_u32 s95, s67, 146432\nQmaaa 152,68,36\nbuffer_load_dwordx4 v61, s[52:55], s95 offen offset:1024 lds\nQmaaa 156,64,12\nQmaaa 156,68,40\nQta 58,68,26368\nQmaaa 160,64,16\ns_add_u32 s31, s75, 131072\n"
"Qmaaa 160,68,44\nbuffer_load_dwordx4 v[80:83], v79, s[48:51], s31 offen offset:0 sc1\nQmaaa 164,64,20\nQta 60,68,33536\nQmaaa 164,68,48\nQmaaa 168,64,24\nQmaaa 168,68,52\nQta 62,68,34560\n"
"s_waitcnt lgkmcnt(4)\nQmvaa 24,72,24\nbuffer_load_dwordx4 v[112:115], v79, s[48:51], s31 offen offset:1024 sc1\nQmvaa 24,76,52\nQmvaa 20,72,20\nQmvaa 20,76,48\nQmvaa 16,72,16\nQta 64,68,40960\n"
"Qmvaa 16,76,44\nbuffer_load_dwordx4 v[144:147], v79, s[48:51], s31 offen offset:2048 sc1\nQmvaa 12,72,12\nQmvaa 12,76,40\nQta 66,68,41472\nQmvaa 8,72,8\nQmvaa 8,76,36\nbuffer_load_dwordx4 v[176:179], v79, s[48:51], s31 offen offset:3072 sc1\n"
"Qmvaa 4,72,4\nQta 68,68,41984\nQmvaa 4,76,32\nQmvaa 0,72,0\nQmvaa 0,76,28\nQta 70,68,42496\ns_add_u32 s31, s75, 135168\ns_waitcnt lgkmcnt(4)\n"
"Qmvaa 28,56,0\nbuffer_load_dwordx4 v[208:211], v79, s[48:51], s31 offen offset:0 sc1\nQmvaa 28,60,28\nQmvaa 32,56,4\nQmvaa 32,60,32\nQmvaa 36,56,8\nQta 72,68,41216\nQmvaa 36,60,36\n"
"buffer_load_dwordx4 v[240:243], v79, s[48:51], s31 offen offset:1024 sc1\nQmvaa 40,56,12\nQmvaa 40,60,40\nQta 74,68,41728\nQmvaa 44,56,16\nQmvaa 44,60,44\nbuffer_load_dwordx4 v[84:87], v79, s[48:51], s31 offen offset:2048 sc1\nQmvaa 48,56,20\n"
"Qta 76,68,42240\nQmvaa 48,60,48\nv_add_u32 v75, 1, v75\nQmvaa 52,56,24\ns_waitcnt vmcnt(37)\nds_write_b32 v73, v75\nQmvaa 52,60,52\nQta 78,68,42752\n"
"s_add_u32 s31, s75, 147456\ns_waitcnt lgkmcnt(5)\nQmaaa 196,64,24\nbuffer_load_dwordx4 v[88:91], v79, s[48:51], s31 offen offset:0 sc1\nQmaaa 196,68,52\nQmaaa 192,64,20\nQmaaa 192,68,48\nQmaaa 188,64,16\n"
"Qta 56,68,49152\nQmaaa 188,68,44\nbuffer_load_dwordx4 v[120:123], v79, s[48:51], s31 offen offset:1024 sc1\nQmaaa 184,64,12\nds_read_b32 v66, v74\nQmaaa 184,68,40\nQta 58,68,50176\nQmaaa 180,64,8\n"
"Qmaaa 180,68,36\nbuffer_load_dwordx4 v[152:155], v79, s[48:51], s31 offen offset:2048 sc1\nQmaaa 176,64,4\ns_waitcnt lgkmcnt(1)\nv_cmp_gt_u32 vcc, v75, v66\nQmaaa 176,68,32\ns_cbranch_vccnz .Lso1735r1\n.Lro1735r1:\n"
"Qmaaa 172,64,0\nQta 60,68,57344\nQmaaa 172,68,28\nbuffer_load_dwordx4 v[184:187], v79, s[48:51], s31 offen offset:3072 sc1\ns_waitcnt lgkmcnt(4)\nQmvaa 92,72,0\nQmvaa 92,76,28\nQta 62,68,58368\n"
"Qmvaa 124,72,4\ns_add_u32 s31, s75, 151552\nQmvaa 124,76,32\nbuffer_load_dwordx4 v[216:219], v79, s[48:51], s31 offen offset:0 sc1\nQmvaa 156,72,8\nQta 64,68,49408\nQmvaa 156,76,36\nQmvaa 188,72,12\n"
"Qmvaa 188,76,40\nQta 66,68,50432\nQmvaa 220,72,16\nbuffer_load_dwordx4 v[248:251], v79, s[48:51], s31 offen offset:1024 sc1\nQmvaa 220,76,44\nQmvaa 252,72,20\nQta 68,68,57600\nQmvaa 252,76,48\n"
"Qmvaa 100,72,24\nbuffer_load_dwordx4 v[116:119], v79, s[48:51], s31 offen offset:2048 sc1\nQmvaa 100,76,52\nQta 70,68,58624\ns_waitcnt lgkmcnt(4)\nQmaaa 224,56,24\nQmaaa 224,60,52\nQmaaa 220,56,20\n"
"s_add_u32 m0, s96, 8192\ns_add_u32 s95, s72, 86016\nQmaaa 220,60,48\nbuffer_load_dwordx4 v61, s[52:55], s95 offen lds\nQmaaa 216,56,16\nQta 72,68,49664\nQmaaa 216,60,44\nQmaaa 212,56,12\n"
"Qmaaa 212,60,40\nQta 74,68,50688\ns_add_u32 s95, s72, 105472\nQmaaa 208,56,8\nbuffer_load_dwordx4 v61, s[52:55], s95 offen offset:1024 lds\nQmaaa 208,60,36\nQmaaa 204,56,4\nQta 76,68,57856\n"
"Qmaaa 204,60,32\nQmaaa 200,56,0\nQmaaa 200,60,28\nQta 78,68,58880\ns_add_u32 m0, s96, 16384\ns_add_u32 s95, s72, 126976\ns_waitcnt lgkmcnt(4)\nQmaaa 228,64,0\n"
"buffer_load_dwordx4 v61, s[52:55], s95 offen lds\nQmaaa 228,68,28\nQmaaa 232,64,4\nQmaaa 232,68,32\nQmaaa 236,64,8\nQta 56,68,49920\ns_add_u32 s95, s72, 146432\nQmaaa 236,68,36\n"
"buffer_load_dwordx4 v61, s[52:55], s95 offen offset:1024 lds\nQmaaa 240,64,12\nQmaaa 240,68,40\nQta 58,68,50944\nQmaaa 244,64,16\nQmaaa 244,68,44\nQmaaa 248,64,20\nQta 60,68,58112\n"
"s_add_u32 m0, s96, 24576\ns_add_u32 s95, s73, 86016\nQmaaa 248,68,48\nbuffer_load_dwordx4 v61, s[52:55], s95 offen lds\nQmaaa 252,64,24\nQmaaa 252,68,52\nQta 62,68,59136\ns_waitcnt lgkmcnt(4)\n"
"Qmvaa 172,72,24\nQmvaa 172,76,52\ns_add_u32 s95, s73, 105472\nQmvaa 140,72,20\nbuffer_load_dwordx4 v61, s[52:55], s95 offen offset:1024 lds\nQmvaa 140,76,48\nQmvaa 108,72,16\nQta 64,69,0\n"
"Qmvaa 108,76,44\nQmvaa 228,72,12\nQmvaa 228,76,40\nQta 66,69,512\ns_add_u32 m0, s96, 32768\ns_add_u32 s95, s73, 126976\nQmvaa 196,72,8\nbuffer_load_dwordx4 v61, s[52:55], s95 offen lds\n"
"Qmvaa 196,76,36\nQmvaa 164,72,4\nQta 68,69,1024\nQmvaa 164,76,32\nv_add_u32 v75, 1, v75\nQmvaa 132,72,0\ns_waitcnt vmcnt(21)\nds_write_b32 v73, v75\n"
"Qmvaa 132,76,28\nQta 70,69,1536\ns_add_u32 s95, s73, 146432\ns_waitcnt lgkmcnt(5)\nQmvaa 204,56,0\nbuffer_load_dwordx4 v61, s[52:55], s95 offen offset:1024 lds\nQmvaa 204,60,28\ns_waitcnt vmcnt(24)\n"
"Qda 0,58,0\nQmvaa 236,56,4\nQmvaa 236,60,32\nQda 28,58,33792\nQmvaa 96,56,8\nQta 72,69,8192\nQmvaa 96,60,36\nQda 4,58,1024\n"
"Qmvaa 128,56,12\nds_read_b32 v66, v74\nQmvaa 128,60,40\nQta 74,69,8704\nQmvaa 160,56,16\nQda 32,58,34816\nQmvaa 160,60,44\nQmvaa 192,56,20\n"
"s_waitcnt lgkmcnt(2)\nv_cmp_gt_u32 vcc, v75, v66\nQmvaa 192,60,48\ns_cbranch_vccnz .Lso1805r1\n.Lro1805r1:\nQmvaa 224,56,24\nQta 76,69,9216\ns_add_u32 m0, s96, 40960\n"
"s_add_u32 s95, s67, 86016\nQmvaa 224,60,52\nbuffer_load_dwordx4 v63, s[52:55], s95 offen lds\ns_waitcnt vmcnt(39)\ns_waitcnt lgkmcnt(8)\nQmaaa 172,64,80\nQda 8,58,2048\nQmaaa 172,68,108\n"
"Qta 78,69,9728\nQmaaa 176,64,84\nQda 36,58,35840\nQmaaa 176,68,112\nQmaaa 180,64,88\nQta 56,69,16384\ns_add_u32 s95, s67, 105472\nQmaav 180,68,168\n"
"buffer_load_dwordx4 v63, s[52:55], s95 offen offset:1024 lds\nQmaaa 184,64,92\nQda 12,58,3072\nQmaav 184,68,200\nQta 58,69,16896\nQmaaa 188,64,96\nQda 40,58,36864\nQmaav 188,68,232\n"
"Qmaaa 192,64,100\nQta 60,69,17408\ns_add_u32 m0, s96, 49152\ns_add_u32 s95, s67, 126976\nQmaav 192,68,212\nbuffer_load_dwordx4 v63, s[52:55], s95 offen lds\nQmaaa 196,64,104\nQda 16,58,4096\n"
"Qmaav 196,68,244\nQta 62,69,17920\ns_waitcnt vmcnt(11)\ns_waitcnt lgkmcnt(8)\nQmaav 172,72,80\nQda 44,58,37888\nQmaav 172,76,88\ns_add_u32 s95, s67, 146432\n"
"Qmaav 176,72,112\nbuffer_load_dwordx4 v63, s[52:55], s95 offen offset:1024 lds\nQmaav 176,76,120\nQda 20,58,5120\nQmaav 180,72,144\nQta 64,69,24576\nv_add_u32 v75, 1, v75\nQmaav 180,76,152\n"
"s_waitcnt vmcnt(4)\nds_write_b32 v73, v75\nQmaav 184,72,176\nQda 48,58,38912\nQmaav 184,76,184\nQta 66,69,25600\ns_add_u32 m0, s96, 57344\ns_add_u32 s95, s72, 86016\n"
"Qmaav 188,72,208\nbuffer_load_dwordx4 v63, s[52:55], s95 offen lds\nQmaav 188,76,216\nQda 24,58,6144\nQmaav 192,72,240\nQta 68,68,0\nQmaav 192,76,248\nQda 52,58,39936\n"
"Qmaav 196,72,84\nQmaav 196,76,116\nds_read_b32 v66, v74\ns_waitcnt lgkmcnt(10)\nQmaaa 172,56,0\nQta 70,68,1024\ns_add_u32 s95, s72, 105472\nQmaaa 172,60,28\n"
"buffer_load_dwordx4 v63, s[52:55], s95 offen offset:1024 lds\nQmaaa 176,56,4\nQmaaa 176,60,32\ns_waitcnt lgkmcnt(1)\nv_cmp_gt_u32 vcc, v75, v66\ns_waitcnt lgkmcnt(11)\nQmaaa 180,56,8\ns_cbranch_vccnz .Lso1840r1\n"
".Lro1840r1:\nQmaaa 180,60,36\nQta 72,68,8192\nQmaaa 184,56,12\nds_write_b128 v58, a[172:175] offset:0\nQmaaa 184,60,40\ns_waitcnt lgkmcnt(4)\nQmaaa 188,56,16\n"
"Qta 74,68,9216\nQmaaa 188,60,44\nds_write_b128 v58, a[176:179] offset:1024\nQmaaa 192,56,20\nQmaaa 192,60,48\nQta 76,68,16384\nQmaaa 196,56,24\nds_write_b128 v58, a[180:183] offset:2048\n"
"Qmaaa 196,60,52\ns_waitcnt lgkmcnt(6)\nQmaaa 116,64,80\nQta 78,68,17408\nQmaaa 116,68,108\nds_write_b128 v58, a[184:187] offset:3072\nQmaaa 120,64,84\nQmaaa 120,68,112\n"
"Qmaaa 124,64,88\nQta 56,68,24576\nQmaav 124,68,168\nds_write_b128 v58, a[188:191] offset:4096\nQmaaa 128,64,92\nQmaav 128,68,200\nQta 58,68,25600\nQmaaa 132,64,96\n"
"ds_write_b128 v58, a[192:195] offset:5120\nQmaav 132,68,232\nQmaaa 136,64,100\nQta 60,68,32768\nQmaav 136,68,212\nds_write_b128 v58, a[196:199] offset:6144\nQmaaa 140,64,104\nQmaav 140,68,244\n"
"Qta 62,68,33792\ns_waitcnt lgkmcnt(8)\nQmaav 116,72,80\nv_add_u32 v75, 1, v75\nQmaav 116,76,88\nds_write_b32 v73, v75\nQmaav 120,72,112\nQmaav 120,76,120\n"
"Qmaav 124,72,144\nQta 64,69,256\nQmaav 124,76,152\nQmaav 128,72,176\nQmaav 128,76,184\nQta 66,69,768\nQmaav 132,72,208\nQmaav 132,76,216\n"
"ds_read_b32 v66, v74\nQmaav 136,72,240\nQta 68,69,1280\nQmaav 136,76,248\nQmaav 140,72,84\nQmaav 140,76,116\ns_waitcnt lgkmcnt(1)\nv_cmp_gt_u32 vcc, v75, v66\n"
"s_waitcnt lgkmcnt(5)\nQmaaa 116,56,0\ns_cbranch_vccnz .Lso1878r1\n.Lro1878r1:\nQmaaa 116,60,28\nQta 70,69,1792\nQmaaa 120,56,4\nQmaaa 120,60,32\n"
"Qdv 148,59,2048\nQmaaa 124,56,8\nQta 72,69,8448\nQmaaa 124,60,36\ns_nop 0\nds_write_b128 v58, a[116:119] offset:33792\nQmaaa 128,56,12\nQdv 180,59,3072\n"
"Qmaaa 128,60,40\nQta 74,69,8960\nQmaaa 132,56,16\nds_write_b128 v58, a[120:123] offset:34816\nQmaaa 132,60,44\nQdv 104,59,5120\nQmaaa 136,56,20\nQta 76,69,9472\n"
"Qmaaa 136,60,48\nds_write_b128 v58, a[124:127] offset:35840\nQmaaa 140,56,24\nQdv 136,59,0\ns_waitcnt lgkmcnt(9)\nQa 156,156,148\nQmaaa 140,60,52\nQta 78,69,9984\n"
"Qa 157,157,149\ns_waitcnt lgkmcnt(10)\nQmvaa 92,64,80\nds_write_b128 v58, a[128:131] offset:36864\nQmvaa 92,68,108\nQa 158,158,150\nQa 159,159,151\nQmvaa 124,64,84\n"
"s_waitcnt lgkmcnt(8)\nQa 188,188,180\n.Lpg24:\ns_cbranch_execz .Ltr8\nQa 189,189,181\nQmvaa 124,68,112\nds_write_b128 v58, a[132:135] offset:37888\nQmvaa 156,64,88\n"
"Qta 56,69,16640\nQa 190,190,182\nQa 191,191,183\nQmvav 156,68,168\nQdv 148,59,4096\ns_waitcnt lgkmcnt(8)\nQa 252,252,104\nQmvaa 188,64,92\n"
"ds_write_b128 v58, a[136:139] offset:38912\nQmvav 188,68,200\nQta 58,69,17152\nQa 253,253,105\nQmvaa 220,64,96\nQa 254,254,106\nQa 255,255,107\nQmvav 220,68,232\n"
"Qdv 180,59,6144\ns_waitcnt lgkmcnt(8)\nQa 92,92,136\nQmvaa 252,64,100\nQta 60,69,17664\nQa 93,93,137\nQmvav 252,68,212\nds_write_b128 v58, a[140:143] offset:39936\n"
"Qmvaa 100,64,104\nQa 94,94,138\nQa 95,95,139\nQmvav 100,68,244\nQta 62,69,18176\ns_waitcnt lgkmcnt(6)\nQa 220,220,148\ns_waitcnt lgkmcnt(6)\n"
"Qmvav 92,72,80\nQdv 104,59,1024\nv_add_u32 v75, 1, v75\nQmvav 92,76,88\nds_write_b32 v73, v75\nQa 221,221,149\nQmvav 124,72,112\nQa 222,222,150\n"
"Qa 223,223,151\nQmvav 124,76,120\ns_waitcnt lgkmcnt(5)\nQa 100,100,180\nQa 101,101,181\nQmvav 156,72,144\nQta 64,69,24832\nQa 102,102,182\n"
"Qmvav 156,76,152\nQa 103,103,183\nQmvav 188,72,176\nQmvav 188,76,184\nQta 66,69,25856\ns_waitcnt lgkmcnt(3)\nQa 124,124,104\nQmvav 220,72,208\n"
"Qa 125,125,105\nQa 126,126,106\nQmvav 220,76,216\nds_read_b32 v66, v74\nQa 127,127,107\nQmvav 252,72,240\nQta 68,68,256\nQmvav 252,76,248\n"
"Qmvav 100,72,84\nQmvav 100,76,116\ns_waitcnt lgkmcnt(1)\nv_cmp_gt_u32 vcc, v75, v66\ns_waitcnt lgkmcnt(6)\nQmvaa 92,56,0\ns_cbranch_vccnz .Lso1920r1\n.Lro1920r1:\n"
"s_add_u32 s68, s64, 0\nQmvaa 92,60,28\nQta 70,68,1280\ns_add_u32 m0, s96, 65536\ns_add_u32 s95, s72, 126976\nQmvaa 124,56,4\nbuffer_load_dwordx4 v63, s[52:55], s95 offen lds\nQmvaa 124,60,32\n"
"Qdv 136,59,33792\nQmvaa 156,56,8\nQta 72,68,8448\nQk 92,92,93\nQmvaa 156,60,36\nQk 93,94,95\nQmvaa 188,56,12\nQdv 148,59,34816\n"
"Qk 94,124,125\nQmvaa 188,60,40\nQta 74,68,9472\ns_add_u32 s95, s72, 146432\nQmvaa 220,56,16\nbuffer_load_dwordx4 v63, s[52:55], s95 offen offset:1024 lds\nQmvaa 220,60,44\nQdv 180,59,35840\n"
"Qk 95,126,127\nQmvaa 252,56,20\nQta 76,68,16640\nbuffer_store_dwordx4 v[92:95], v79, s[24:27], s68 offen offset:0 sc1\nQmvaa 252,60,48\nv_add_u32 v75, 1, v75\ns_waitcnt lgkmcnt(5)\nQa 0,0,136\n"
"Qmvaa 100,56,24\nds_write_b32 v73, v75\nQa 1,1,137\nQmvaa 100,60,52\nQta 78,68,17664\ns_add_u32 m0, s96, 73728\ns_add_u32 s95, s73, 86016\ns_waitcnt lgkmcnt(7)\n"
"Qmaaa 144,64,80\nbuffer_load_dwordx4 v63, s[52:55], s95 offen lds\nQmaaa 144,68,108\nQdv 104,59,36864\nQa 2,2,138\nQmaaa 148,64,84\nQa 3,3,139\ns_waitcnt lgkmcnt(6)\n"
"Qa 4,4,148\nQmaaa 148,68,112\nQa 5,5,149\nQa 6,6,150\nQmaaa 152,64,88\nQta 56,68,24832\ns_add_u32 s95, s73, 105472\nQmaav 152,68,168\n"
"buffer_load_dwordx4 v63, s[52:55], s95 offen offset:1024 lds\nQmaaa 156,64,92\nds_read_b32 v66, v74\nQa 7,7,151\nQmaav 156,68,200\nQta 58,68,25856\ns_waitcnt lgkmcnt(7)\nQa 8,8,180\n"
"Qmaaa 160,64,96\nQdv 136,59,37888\nQa 9,9,181\nQmaav 160,68,232\nQa 10,10,182\nQa 11,11,183\nQmaaa 164,64,100\ns_waitcnt lgkmcnt(2)\n"
"v_cmp_gt_u32 vcc, v75, v66\nQmaav 164,68,212\ns_cbranch_vccnz .Lso1945r1\n.Lro1945r1:\ns_waitcnt lgkmcnt(4)\nQa 12,12,104\nQmaaa 168,64,104\nQta 60,68,33024\n"
"s_add_u32 m0, s96, 81920\ns_add_u32 s95, s73, 126976\nQa 13,13,105\nQa 14,14,106\nQa 15,15,107\nQmaav 168,68,244\nbuffer_load_dwordx4 v63, s[52:55], s95 offen lds\ns_waitcnt lgkmcnt(10)\n"
"Qmaav 144,72,80\nQdv 148,59,38912\ns_waitcnt lgkmcnt(2)\nQa 16,16,136\nQmaav 144,76,88\nQta 62,68,34048\nQa 17,17,137\nQmaav 148,72,112\n"
"Qa 18,18,138\nQa 19,19,139\nQmaav 148,76,120\nQdv 180,59,39936\nQk 156,156,157\nQmaav 152,72,144\nQta 64,69,25088\ns_add_u32 s95, s73, 146432\n"
"Qmaav 152,76,152\nbuffer_load_dwordx4 v63, s[52:55], s95 offen offset:1024 lds\nQmaav 156,72,176\ns_waitcnt lgkmcnt(3)\nQa 20,20,148\nQa 21,21,149\nQmaav 156,76,184\nQta 66,69,26112\n"
"Qa 22,22,150\nQmaav 160,72,208\nQa 23,23,151\nv_bfe_u32 v61, v60, 6, 2\nQmaav 160,76,216\nv_lshrrev_b32 v62, 8, v60\nv_xor_b32 v61, v61, v62\nQmaav 164,72,240\n"
"Qta 68,68,512\nv_bfe_u32 v62, v60, 5, 1\nQmaav 164,76,248\ns_waitcnt lgkmcnt(3)\nQa 24,24,180\nQa 25,25,181\nQmaav 168,72,84\nQa 26,26,182\n"
"Qa 27,27,183\nQmaav 168,76,116\nQta 70,68,1536\nv_lshl_or_b32 v61, v61, 1, v62\ns_waitcnt lgkmcnt(5)\nQmaaa 144,56,0\nv_and_b32 v62, 16, v60\nv_mov_b32 v63, 0x3f80\n"
"Qmaaa 144,60,28\nv_lshlrev_b32 v63, v62, v63\nv_min_u32 v62, 1, v61\nQmaaa 148,56,4\nv_add_u32 v62, -1, v62\nv_and_b32 v62, v62, v63\nQmaaa 148,60,32\nv_xor_b32 v61, 1, v61\n"
"v_min_u32 v61, 1, v61\nQmaaa 152,56,8\nQta 72,68,8704\nv_add_u32 v61, -1, v61\nQmaaa 152,60,36\nds_write_b128 v58, a[144:147] offset:0\nQmaaa 156,56,12\nv_and_b32 v61, v61, v63\n"
"v_accvgpr_write_b32 a174, v62\nQmaaa 156,60,40\nQta 74,68,9728\nv_accvgpr_write_b32 a175, v61\nQmaaa 160,56,16\nds_write_b128 v58, a[148:151] offset:1024\nQmaaa 160,60,44\nv_accvgpr_write_b32 a172, 0\n"
"v_accvgpr_write_b32 a173, 0\nQmaaa 164,56,20\nQta 76,68,16896\nv_accvgpr_write_b32 a176, 0\nQmaaa 164,60,48\nds_write_b128 v58, a[152:155] offset:2048\nQmaaa 168,56,24\nv_accvgpr_write_b32 a177, 0\n"
"Qk 157,158,159\nQmaaa 168,60,52\nQta 78,68,17920\nQk 158,188,189\ns_waitcnt lgkmcnt(7)\nQmvaa 0,64,80\nds_write_b128 v58, a[156:159] offset:3072\nQmvaa 0,68,108\n"
"Qk 159,190,191\nQk 220,220,221\nQmvaa 4,64,84\nbuffer_store_dwordx4 v[156:159], v79, s[24:27], s68 offen offset:1024 sc1\nQk 221,222,223\nQmvaa 4,68,112\nds_write_b128 v58, a[160:163] offset:4096\nQmvaa 8,64,88\n"
"Qta 56,68,25088\nQk 222,252,253\nQmvav 8,68,168\nQk 223,254,255\nQk 100,100,101\nQmvaa 12,64,92\nds_write_b128 v58, a[164:167] offset:5120\nQmvav 12,68,200\n"
"Qta 58,68,26112\nbuffer_store_dwordx4 v[220:223], v79, s[24:27], s68 offen offset:2048 sc1\nQmvaa 16,64,96\nQk 101,102,103\nQmvav 16,68,232\nds_write_b128 v58, a[168:171] offset:6144\nQmvaa 20,64,100\nQta 60,68,33280\n"
"Qmvav 20,68,212\nQmvaa 24,64,104\nQmvav 24,68,244\nQta 62,68,34304\ns_waitcnt lgkmcnt(8)\nQmvav 0,72,80\nv_add_u32 v75, 1, v75\nQmvav 0,76,88\n"
"s_waitcnt vmcnt(11)\nds_write_b32 v73, v75\nQmvav 4,72,112\nQmvav 4,76,120\nQmvav 8,72,144\nQta 64,69,25344\nQmvav 8,76,152\nQmvav 12,72,176\n"
"Qmvav 12,76,184\nQta 66,69,26368\nQmvav 16,72,208\nQmvav 16,76,216\nds_read_b32 v66, v74\nQmvav 20,72,240\nQta 68,68,768\nQmvav 20,76,248\n"
"Qmvav 24,72,84\nQmvav 24,76,116\ns_waitcnt lgkmcnt(1)\nv_cmp_gt_u32 vcc, v75, v66\ns_waitcnt lgkmcnt(5)\nQmvaa 0,56,0\ns_cbranch_vccnz .Lso2004r1\n.Lro2004r1:\n"
"Qmvaa 0,60,28\nQta 70,68,1792\nQmvaa 4,56,4\nQmvaa 4,60,32\nQdv 104,59,2048\nQmvaa 8,56,8\nQta 72,68,8960\ns_nop 1\n"
"Qk 102,0,1\nQmvaa 8,60,36\nQk 103,2,3\nQmvaa 12,56,12\nQdv 136,59,3072\nbuffer_store_dwordx4 v[100:103], v79, s[24:27], s68 offen offset:3072 sc1\nQmvaa 12,60,40\nQta 74,68,9984\n"
"Qk 4,4,5\nQmvaa 16,56,16\nQk 5,6,7\nQk 6,8,9\nQmvaa 16,60,44\nQdv 148,59,5120\nQk 7,10,11\nQmvaa 20,56,20\n"
"Qta 76,68,17152\ns_add_u32 s68, s64, 4096\nQmvaa 20,60,48\ns_waitcnt lgkmcnt(5)\nQa 36,36,104\nQa 37,37,105\nQmvaa 24,56,24\nQdv 180,59,0\n"
"Qa 38,38,106\nQmvaa 24,60,52\nQta 78,68,18176\nQa 39,39,107\ns_waitcnt lgkmcnt(11)\nQmvaa 28,64,80\ns_waitcnt lgkmcnt(5)\nQa 40,40,136\n"
"Qa 41,41,137\nQmvaa 28,68,108\nQa 42,42,138\nQa 43,43,139\nQmvaa 32,64,84\nbuffer_store_dwordx4 v[4:7], v79, s[24:27], s68 offen offset:0 sc1\nQk 12,12,13\nQmvaa 32,68,112\n"
"s_waitcnt lgkmcnt(3)\nQa 48,48,148\nQa 49,49,149\nQmvaa 36,64,88\nQta 56,68,25344\nQa 50,50,150\nQmvav 36,68,168\nQdv 104,59,4096\n"
"Qa 51,51,151\nQmvaa 40,64,92\ns_waitcnt lgkmcnt(3)\nQa 28,28,180\nQa 29,29,181\nQmvav 40,68,200\nQta 58,68,26368\nQa 30,30,182\n"
"Qmvaa 44,64,96\nQa 31,31,183\nQk 13,14,15\nQmvav 44,68,232\nQdv 136,59,6144\nQk 14,16,17\nQmvaa 48,64,100\nQta 60,68,33536\n"
"Qk 15,18,19\nQmvav 48,68,212\nbuffer_store_dwordx4 v[12:15], v79, s[24:27], s68 offen offset:1024 sc1\nQk 20,20,21\nQmvaa 52,64,104\nQk 21,22,23\nQk 22,24,25\nQmvav 52,68,244\n"
"Qta 62,68,34560\nv_add_u32 v75, 1, v75\ns_waitcnt lgkmcnt(6)\nQmvav 28,72,80\ns_waitcnt vmcnt(5)\nds_write_b32 v73, v75\nQmvav 28,76,88\nQdv 148,59,1024\n"
"s_waitcnt lgkmcnt(6)\nQa 44,44,104\nQmvav 32,72,112\nQa 45,45,105\nQa 46,46,106\nQmvav 32,76,120\nQa 47,47,107\ns_waitcnt lgkmcnt(4)\n"
"Qa 52,52,136\nQmvav 36,72,144\nQta 64,68,40960\nQa 53,53,137\nQmvav 36,76,152\nQa 54,54,138\nQa 55,55,139\nQmvav 40,72,176\n"
"Qk 23,26,27\nQmvav 40,76,184\nQta 66,68,41984\ns_waitcnt lgkmcnt(2)\nQa 32,32,148\nQmvav 44,72,208\nds_read_b32 v66, v74\nQa 33,33,149\n"
"Qmvav 44,76,216\nQa 34,34,150\nQa 35,35,151\nQmvav 48,72,240\nQta 68,68,49152\nbuffer_store_dwordx4 v[20:23], v79, s[24:27], s68 offen offset:2048 sc1\nQmvav 48,76,248\nQmvav 52,72,84\n"
"s_waitcnt lgkmcnt(1)\nv_cmp_gt_u32 vcc, v75, v66\nQmvav 52,76,116\ns_cbranch_vccnz .Lso2045r1\n.Lro2045r1:\ns_waitcnt lgkmcnt(6)\nQmvaa 28,56,0\nQta 70,68,50176\n"
"Qmvaa 28,60,28\nQmvaa 32,56,4\nQmvaa 32,60,32\nQmvaa 36,56,8\nQta 72,68,57344\ns_nop 3\nQk 28,28,29\nQmvaa 36,60,36\n"
"Qk 29,30,31\nQmvaa 40,56,12\nQk 30,32,33\nQk 31,34,35\nQmvaa 40,60,40\nQta 74,68,58368\nbuffer_store_dwordx4 v[28:31], v79, s[24:27], s68 offen offset:3072 sc1\nQmvaa 44,56,16\n"
"Qk 36,36,37\nQk 37,38,39\nQmvaa 44,60,44\ns_add_u32 s68, s64, 16384\nQmvaa 48,56,20\nQta 76,69,0\nQk 38,40,41\nQmvaa 48,60,48\n"
"Qk 39,42,43\nQmvaa 52,56,24\nbuffer_store_dwordx4 v[36:39], v79, s[24:27], s68 offen offset:0 sc1\nQk 44,44,45\nQmvaa 52,60,52\nQta 78,69,1024\nQk 45,46,47\ns_waitcnt lgkmcnt(4)\n"
"Qmaaa 200,64,80\nQk 46,48,49\nQk 47,50,51\nQmaaa 200,68,108\nbuffer_store_dwordx4 v[44:47], v79, s[24:27], s68 offen offset:1024 sc1\nQmaaa 204,64,84\nQk 52,52,53\nQk 53,54,55\n"
"Qmaaa 204,68,112\nv_mov_b32 v54, 0\nv_mov_b32 v55, 0\nQmaaa 208,64,88\nQta 56,69,8192\nbuffer_store_dwordx4 v[52:55], v79, s[24:27], s68 offen offset:2048 sc1\nQmaav 208,68,168\nQmaaa 212,64,92\n"
"Qmaav 212,68,200\nQta 58,69,9216\nQmaaa 216,64,96\nQmaav 216,68,232\nQmaaa 220,64,100\nQta 60,69,16384\nQmaav 220,68,212\nQmaaa 224,64,104\n"
"Qmaav 224,68,244\nQta 62,69,17408\ns_waitcnt lgkmcnt(4)\nQmaav 200,72,80\nQmaav 200,76,88\nQmaav 204,72,112\nQmaav 204,76,120\nQmaav 208,72,144\n"
"Qta 64,68,41216\nQmaav 208,76,152\nQmaav 212,72,176\nQmaav 212,76,184\nQta 66,68,42240\nQmaav 216,72,208\nQmaav 216,76,216\nQmaav 220,72,240\n"
"Qta 68,68,49408\nQmaav 220,76,248\nQmaav 224,72,84\nQmaav 224,76,116\nQta 70,68,50432\ns_waitcnt lgkmcnt(4)\nQmaaa 200,56,0\nQmaaa 200,60,28\n"
"s_waitcnt vmcnt(0)\nbuffer_store_dword v67, v77, s[24:27], 0 offen sc1\nQmaaa 204,56,4\nQmaaa 204,60,32\nQmaaa 208,56,8\nQta 72,68,57600\nQmaaa 208,60,36\ns_nop 0\n"
"ds_write_b128 v58, a[200:203] offset:33792\nQmaaa 212,56,12\nQmaaa 212,60,40\nQta 74,68,58624\nQmaaa 216,56,16\nds_write_b128 v58, a[204:207] offset:34816\nQmaaa 216,60,44\nQmaaa 220,56,20\n"
"Qta 76,69,256\nQmaaa 220,60,48\nds_write_b128 v58, a[208:211] offset:35840\nQmaaa 224,56,24\nQmaaa 224,60,52\nQta 78,69,1280\ns_waitcnt lgkmcnt(7)\nQmaaa 228,64,80\n"
"ds_write_b128 v58, a[212:215] offset:36864\nQmaaa 228,68,108\nQmaaa 232,64,84\nQmaaa 232,68,112\nds_write_b128 v58, a[216:219] offset:37888\nQmaaa 236,64,88\nQta 56,69,8448\nQmaav 236,68,168\n"
"Qmaaa 240,64,92\nds_write_b128 v58, a[220:223] offset:38912\nQmaav 240,68,200\nQta 58,69,9472\nQmaaa 244,64,96\nQmaav 244,68,232\nds_write_b128 v58, a[224:227] offset:39936\nQmaaa 248,64,100\n"
"Qta 60,69,16640\nQmaav 248,68,212\nQmaaa 252,64,104\nQmaav 252,68,244\nQta 62,69,17664\ns_waitcnt lgkmcnt(8)\nQmaav 228,72,80\nv_add_u32 v75, 1, v75\n"
"Qmaav 228,76,88\nds_write_b32 v73, v75\nQmaav 232,72,112\nQmaav 232,76,120\nQmaav 236,72,144\nQta 64,68,41472\nQmaav 236,76,152\nQmaav 240,72,176\n"
"Qmaav 240,76,184\nQta 66,68,42496\nQmaav 244,72,208\nQmaav 244,76,216\nds_read_b32 v66, v74\nQmaav 248,72,240\nQta 68,68,49664\nQmaav 248,76,248\n"
"Qmaav 252,72,84\nbuffer_load_dword v78, v76, s[24:27], 0 offen sc1\nQmaav 252,76,116\ns_waitcnt lgkmcnt(1)\nv_cmp_gt_u32 vcc, v75, v66\ns_waitcnt lgkmcnt(5)\nQmaaa 228,56,0\ns_cbranch_vccnz .Lso2130r1\n"
".Lro2130r1:\nQmaaa 228,60,28\nQta 70,68,50688\nQmaaa 232,56,4\nQmaaa 232,60,32\nQdv 180,59,35840\n.Lpg25:\ns_cbranch_execz .Ltr8\n"
"Qmaaa 236,56,8\nQta 72,68,57856\nQmaaa 236,60,36\ns_nop 0\nds_write_b128 v58, a[228:231] offset:0\nQmaaa 240,56,12\nQdv 104,59,36864\nQmaaa 240,60,40\n"
"Qta 74,68,58880\nQmaaa 244,56,16\nds_write_b128 v58, a[232:235] offset:1024\nQmaaa 244,60,44\nQdv 136,59,38912\nQmaaa 248,56,20\nQta 76,69,512\nQmaaa 248,60,48\n"
"ds_write_b128 v58, a[236:239] offset:2048\nQmaaa 252,56,24\nQdv 148,59,33792\ns_waitcnt lgkmcnt(9)\nQa 196,196,180\nQmaaa 252,60,52\nQta 78,69,1536\nQa 197,197,181\n"
"s_waitcnt lgkmcnt(10)\nQmvaa 132,64,80\nds_write_b128 v58, a[240:243] offset:3072\nQmvaa 132,68,108\nQa 198,198,182\nQa 199,199,183\nQmvaa 164,64,84\ns_waitcnt lgkmcnt(8)\n"
"Qa 228,228,104\nQa 229,229,105\nQmvaa 164,68,112\nds_write_b128 v58, a[244:247] offset:4096\nQmvaa 196,64,88\nQta 56,69,8704\nQa 230,230,106\nQa 231,231,107\n"
"Qmvav 196,68,168\nQdv 180,59,37888\ns_waitcnt lgkmcnt(8)\nQa 140,140,136\nQmvaa 228,64,92\nds_write_b128 v58, a[248:251] offset:5120\nQmvav 228,68,200\nQta 58,69,9728\n"
"Qa 141,141,137\nQmvaa 108,64,96\nQa 142,142,138\nQa 143,143,139\nQmvav 108,68,232\nQdv 104,59,39936\ns_waitcnt lgkmcnt(8)\nQa 132,132,148\n"
"Qmvaa 140,64,100\nQta 60,69,16896\nQa 133,133,149\nQmvav 140,68,212\nds_write_b128 v58, a[252:255] offset:6144\nQmvaa 172,64,104\nQa 134,134,150\nQa 135,135,151\n"
"Qmvav 172,68,244\nQta 62,69,17920\ns_waitcnt lgkmcnt(6)\nQa 108,108,180\ns_waitcnt lgkmcnt(6)\nQmvav 132,72,80\nQdv 136,59,34816\nv_add_u32 v75, 1, v75\n"
"Qmvav 132,76,88\nds_write_b32 v73, v75\nQa 109,109,181\nQmvav 164,72,112\nQa 110,110,182\nQa 111,111,183\nQmvav 164,76,120\ns_waitcnt lgkmcnt(5)\n"
"Qa 172,172,104\nQa 173,173,105\nQmvav 196,72,144\nQta 64,68,41728\nQa 174,174,106\nQmvav 196,76,152\nQa 175,175,107\nQmvav 228,72,176\n"
"Qmvav 228,76,184\nQta 66,68,42752\ns_waitcnt lgkmcnt(3)\nQa 164,164,136\nQmvav 108,72,208\nQa 165,165,137\nQa 166,166,138\nQmvav 108,76,216\n"
"ds_read_b32 v66, v74\nQa 167,167,139\nQmvav 140,72,240\nQta 68,68,49920\nQmvav 140,76,248\nQmvav 172,72,84\nQmvav 172,76,116\ns_waitcnt lgkmcnt(1)\n"
"v_cmp_gt_u32 vcc, v75, v66\ns_waitcnt lgkmcnt(6)\nQmvaa 132,56,0\ns_cbranch_vccnz .Lso2172r1\n.Lro2172r1:\nQmvaa 132,60,28\ns_waitcnt vmcnt(0)\nv_sub_u32 v66, v78, v65\n"
"v_cmp_gt_u32 vcc, 1, v66\ns_and_b64 vcc, vcc, s[28:29]\ns_cbranch_scc1 .Lx1_w\n.Lx1_r:\nQmvaa 164,56,4\ns_add_u32 s69, s98, 0\nbuffer_load_dwordx4 a[180:183], v79, s[24:27], s69 offen offset:0 sc1\nQmvaa 164,60,32\n"
"Qta 70,68,50944\nQmvaa 196,56,8\nbuffer_load_dwordx4 a[192:195], v79, s[24:27], s69 offen offset:3072 sc1\nQmvaa 196,60,36\nQdv 148,59,2048\nQmvaa 228,56,12\ns_add_u32 s69, s98, 4096\nbuffer_load_dwordx4 a[196:199], v79, s[24:27], s69 offen offset:0 sc1\n"
"Qmvaa 228,60,40\nQta 72,68,58112\nQmvaa 108,56,16\ns_add_u32 s69, s98, 0\nbuffer_load_dwordx4 a[184:187], v79, s[24:27], s69 offen offset:1024 sc1\nQmvaa 108,60,44\nQdv 180,59,4096\nQmvaa 140,56,20\n"
"s_add_u32 s69, s98, 4096\nbuffer_load_dwordx4 a[116:119], v79, s[24:27], s69 offen offset:1024 sc1\nQmvaa 140,60,48\nQta 74,68,59136\ns_waitcnt lgkmcnt(3)\nQa 96,96,148\nQmvaa 172,56,24\ns_add_u32 s69, s98, 0\n"
"buffer_load_dwordx4 a[188:191], v79, s[24:27], s69 offen offset:2048 sc1\nQmvaa 172,60,52\nQdv 104,59,0\nQa 97,97,149\ns_waitcnt lgkmcnt(4)\nQmvaa 204,64,80\ns_add_u32 s69, s98, 4096\nbuffer_load_dwordx4 a[120:123], v79, s[24:27], s69 offen offset:2048 sc1\n"
"Qmvaa 204,68,108\nQta 76,69,768\nQa 98,98,150\nQmvaa 236,64,84\nQdv 136,59,1024\nQa 99,99,151\nQmvaa 236,68,112\ns_waitcnt lgkmcnt(4)\n"
"Qa 160,160,180\nQa 161,161,181\nQmvaa 96,64,88\nQta 78,69,1792\nQa 162,162,182\nQmvav 96,68,168\nQdv 148,59,3072\nQa 163,163,183\n"
"Qmvaa 128,64,92\ns_waitcnt lgkmcnt(4)\nQa 204,204,104\nQa 205,205,105\nQmvav 128,68,200\nQta 56,69,8960\nQa 206,206,106\nQmvaa 160,64,96\n"
"Qdv 180,59,5120\nQa 207,207,107\nQmvav 160,68,232\nQmvaa 192,64,100\nQta 58,69,9984\ns_waitcnt lgkmcnt(5)\nQa 236,236,136\nQmvav 192,68,212\n"
"Qa 237,237,137\nQa 238,238,138\nQmvaa 224,64,104\nQa 239,239,139\nQmvav 224,68,244\nQta 60,69,17152\ns_waitcnt lgkmcnt(4)\nQa 128,128,148\n"
"s_waitcnt lgkmcnt(4)\nQmvav 204,72,80\nQa 129,129,149\nQa 130,130,150\nQmvav 204,76,88\nQa 131,131,151\nQmvav 236,72,112\nQta 62,69,18176\n"
"s_waitcnt lgkmcnt(3)\nQa 192,192,180\nQmvav 236,76,120\nQa 193,193,181\nQa 194,194,182\nQmvav 96,72,144\nQa 195,195,183\nQmvav 96,76,152\n"
"Qmvav 128,72,176\nQmvav 128,76,184\nQmvav 160,72,208\nQmvav 160,76,216\nQdv 104,59,6144\nQmvav 192,72,240\nQmvav 192,76,248\nv_add_u32 v75, 1, v75\n"
"Qmvav 224,72,84\nds_write_b32 v73, v75\nQmvav 224,76,116\ns_waitcnt lgkmcnt(2)\nQmvaa 204,56,0\nQmvaa 204,60,28\nQmvaa 236,56,4\nQmvaa 236,60,32\n"
"s_waitcnt lgkmcnt(1)\ns_nop 1\nQa 224,224,104\nQa 225,225,105\nQmvaa 96,56,8\nQa 226,226,106\nQa 227,227,107\nQmvaa 96,60,36\n"
"Qmvaa 128,56,12\nds_read_b32 v66, v74\nQmvaa 128,60,40\nQmvaa 160,56,16\nQmvaa 160,60,44\nQmvaa 192,56,20\ns_waitcnt lgkmcnt(0)\nv_cmp_gt_u32 vcc, v75, v66\n"
"Qmvaa 192,60,48\ns_cbranch_vccnz .Lso2225r1\n.Lro2225r1:\nQmvaa 224,56,24\nQmvaa 224,60,52\ns_waitcnt vmcnt(6)\nQmvaa 132,174,180\ns_waitcnt vmcnt(5)\n"
"Qmvaa 204,172,192\nQmvaa 164,172,180\ns_waitcnt vmcnt(4)\nQmvaa 236,174,196\ns_nop 2\nQk 132,132,133\nQk 133,134,135\nds_write_b64 v56, v[132:133] offset:0\n"
"s_waitcnt vmcnt(3)\nQmvaa 196,174,184\nQk 204,204,205\nQk 205,206,207\nds_write_b64 v56, v[204:205] offset:32\nQmvaa 96,172,196\nQk 164,164,165\nQk 165,166,167\n"
"ds_write_b64 v56, v[164:165] offset:3328\nQmvaa 228,172,184\nQk 236,236,237\nQk 237,238,239\nds_write_b64 v56, v[236:237] offset:3360\ns_waitcnt lgkmcnt(0)\ns_barrier\nQda 0,57,0\n"
"Qda 4,57,3328\ns_waitcnt vmcnt(2)\nQmvaa 128,174,116\nQk 196,196,197\nQk 197,198,199\nds_write_b64 v56, v[196:197] offset:6656\ns_mov_b32 s68, 0\ns_waitcnt lgkmcnt(2)\n"
"buffer_store_dwordx4 a[0:3], v64, s[40:43], s68 offen offset:0 sc1\ns_waitcnt vmcnt(2)\nQmvaa 108,174,188\nQk 96,96,97\nQk 97,98,99\nds_write_b64 v56, v[96:97] offset:6688\ns_mov_b32 s68, 40960\ns_waitcnt lgkmcnt(2)\n"
"buffer_store_dwordx4 a[4:7], v64, s[40:43], s68 offen offset:0 sc1\nQmvaa 160,172,116\nQk 228,228,229\nQk 229,230,231\nds_write_b64 v56, v[228:229] offset:9984\nQmvaa 140,172,188\nQk 128,128,129\nQk 129,130,131\n"
"ds_write_b64 v56, v[128:129] offset:10016\ns_waitcnt vmcnt(2)\nQmvaa 192,174,120\nQk 108,108,109\nQk 109,110,111\nds_write_b64 v56, v[108:109] offset:13312\nQmvaa 172,174,192\nQk 160,160,161\n"
"Qk 161,162,163\nds_write_b64 v56, v[160:161] offset:13344\ns_waitcnt lgkmcnt(0)\ns_barrier\nQda 8,57,6656\nQda 12,57,9984\nQda 16,57,13312\nQmvaa 224,172,120\n"
"Qk 140,140,141\nQk 141,142,143\nds_write_b64 v56, v[140:141] offset:16640\ns_mov_b32 s68, 81920\ns_waitcnt lgkmcnt(3)\nbuffer_store_dwordx4 a[8:11], v64, s[40:43], s68 offen offset:0 sc1\nQk 192,192,193\nQk 193,194,195\n"
"ds_write_b64 v56, v[192:193] offset:16672\ns_mov_b32 s68, 122880\ns_waitcnt lgkmcnt(3)\nbuffer_store_dwordx4 a[12:15], v64, s[40:43], s68 offen offset:0 sc1\nQk 172,172,173\nQk 173,174,175\nds_write_b64 v56, v[172:173] offset:19968\ns_mov_b32 s68, 163840\n"
"s_waitcnt lgkmcnt(3)\nbuffer_store_dwordx4 a[16:19], v64, s[40:43], s68 offen offset:0 sc1\nQk 224,224,225\nQk 225,226,227\nds_write_b64 v56, v[224:225] offset:20000\ns_waitcnt lgkmcnt(0)\ns_barrier\nQda 20,57,16640\n"
"Qda 24,57,19968\ns_mov_b32 s68, 204800\ns_waitcnt lgkmcnt(1)\nbuffer_store_dwordx4 a[20:23], v64, s[40:43], s68 offen offset:0 sc1\ns_mov_b32 s68, 245760\ns_waitcnt lgkmcnt(0)\nbuffer_store_dwordx4 a[24:27], v64, s[40:43], s68 offen offset:0 sc1\n.Lfin:\n"
".Lpg26:\ns_cbranch_execz .Ltr8\ns_waitcnt vmcnt(0) lgkmcnt(0)\ns_branch .Lexit\n.Lend:\n.Lfb:\ns_waitcnt vmcnt(0) lgkmcnt(0)\nv_lshrrev_b32 v52, 4, v60\n"
"v_and_b32 v53, 15, v52\nv_lshrrev_b32 v52, 4, v52\nv_lshlrev_b32 v66, 8, v53\nv_lshl_add_u32 v66, v52, 4, v66\nv_add_u32 v66, 1024, v66\nv_lshlrev_b32 v67, 10, v52\nv_lshl_add_u32 v67, v53, 1, v67\nv_add_u32 v67, 1024, v67\n"
"v_mov_b32 v52, 0x3f803f80\nv_accvgpr_write_b32 a204, v52\nv_accvgpr_write_b32 a205, v52\nv_accvgpr_write_b32 a206, v52\nv_accvgpr_write_b32 a207, v52\nv_mov_b32 v24, 0xff800000\nv_mov_b32 v28, 0xff800000\nv_mov_b32 v32, 0xff800000\n"
"v_mov_b32 v36, 0xff800000\nv_mov_b32 v40, 0xff800000\nv_mov_b32 v44, 0xff800000\nv_mov_b32 v48, 0xff800000\ns_lshl_b32 s89, s67, 21\ns_lshl_b32 s68, s93, 18\ns_add_u32 s89, s89, s68\ns_mov_b32 s66, 0\n"
".Lfb1:\ns_add_u32 s68, s89, 4096\nbuffer_load_dwordx4 a[112:115], v66, s[40:43], s89 offen offset:0\nbuffer_load_dwordx4 a[116:119], v66, s[40:43], s89 offen offset:64\nbuffer_load_dwordx4 a[120:123], v66, s[40:43], s89 offen offset:128\nbuffer_load_dwordx4 a[124:127], v66, s[40:43], s89 offen offset:192\nbuffer_load_dwordx4 a[128:131], v66, s[40:43], s68 offen offset:0\nbuffer_load_dwordx4 a[132:135], v66, s[40:43], s68 offen offset:64\n"
"buffer_load_dwordx4 a[136:139], v66, s[40:43], s68 offen offset:128\nbuffer_load_dwordx4 a[140:143], v66, s[40:43], s68 offen offset:192\ns_waitcnt vmcnt(0)\nQzvaa 0,112,0\nQmvaa 0,116,4\nQmvaa 0,120,8\nQmvaa 0,124,12\nQzvaa 4,128,0\n"
"Qmvaa 4,132,4\nQmvaa 4,136,8\nQmvaa 4,140,12\ns_nop 7\nv_max3_f32 v24, v24, v0, v1\nv_max3_f32 v24, v24, v2, v3\nv_max3_f32 v24, v24, v4, v5\nv_max3_f32 v24, v24, v6, v7\n"
"Qzvaa 0,112,16\nQmvaa 0,116,20\nQmvaa 0,120,24\nQmvaa 0,124,28\nQzvaa 4,128,16\nQmvaa 4,132,20\nQmvaa 4,136,24\nQmvaa 4,140,28\n"
"s_nop 7\nv_max3_f32 v28, v28, v0, v1\nv_max3_f32 v28, v28, v2, v3\nv_max3_f32 v28, v28, v4, v5\nv_max3_f32 v28, v28, v6, v7\nQzvaa 0,112,32\nQmvaa 0,116,36\nQmvaa 0,120,40\n"
"Qmvaa 0,124,44\nQzvaa 4,128,32\nQmvaa 4,132,36\nQmvaa 4,136,40\nQmvaa 4,140,44\ns_nop 7\nv_max3_f32 v32, v32, v0, v1\nv_max3_f32 v32, v32, v2, v3\n"
"v_max3_f32 v32, v32, v4, v5\nv_max3_f32 v32, v32, v6, v7\nQzvaa 0,112,48\nQmvaa 0,116,52\nQmvaa 0,120,56\nQmvaa 0,124,60\nQzvaa 4,128,48\nQmvaa 4,132,52\n"
"Qmvaa 4,136,56\nQmvaa 4,140,60\ns_nop 7\nv_max3_f32 v36, v36, v0, v1\nv_max3_f32 v36, v36, v2, v3\nv_max3_f32 v36, v36, v4, v5\nv_max3_f32 v36, v36, v6, v7\nQzvaa 0,112,64\n"
"Qmvaa 0,116,68\nQmvaa 0,120,72\nQmvaa 0,124,76\nQzvaa 4,128,64\nQmvaa 4,132,68\nQmvaa 4,136,72\nQmvaa 4,140,76\ns_nop 7\n"
"v_max3_f32 v40, v40, v0, v1\nv_max3_f32 v40, v40, v2, v3\nv_max3_f32 v40, v40, v4, v5\nv_max3_f32 v40, v40, v6, v7\nQzvaa 0,112,80\nQmvaa 0,116,84\nQmvaa 0,120,88\nQmvaa 0,124,92\n"
"Qzvaa 4,128,80\nQmvaa 4,132,84\nQmvaa 4,136,88\nQmvaa 4,140,92\ns_nop 7\nv_max3_f32 v44, v44, v0, v1\nv_max3_f32 v44, v44, v2, v3\nv_max3_f32 v44, v44, v4, v5\n"
"v_max3_f32 v44, v44, v6, v7\nQzvaa 0,112,96\nQmvaa 0,116,100\nQmvaa 0,120,104\nQmvaa 0,124,108\nQzvaa 4,128,96\nQmvaa 4,132,100\nQmvaa 4,136,104\n"
"Qmvaa 4,140,108\ns_nop 7\nv_max3_f32 v48, v48, v0, v1\nv_max3_f32 v48, v48, v2, v3\nv_max3_f32 v48, v48, v4, v5\nv_max3_f32 v48, v48, v6, v7\ns_add_u32 s89, s89, 8192\ns_add_u32 s66, s66, 1\n"
"s_cmp_lt_u32 s66, 32\ns_cbranch_scc1 .Lfb1\nv_mov_b32 v25, v24\ns_nop 1\nv_permlane32_swap_b32 v24, v25\nv_max_f32 v24, v24, v25\nv_mov_b32 v25, v24\ns_nop 1\n"
"v_permlane16_swap_b32 v24, v25\nv_max_f32 v24, v24, v25\nv_sub_f32 v24, 0, v24\nv_mov_b32 v25, v24\nv_mov_b32 v26, v24\nv_mov_b32 v27, v24\nv_mov_b32 v29, v28\ns_nop 1\n"
"v_permlane32_swap_b32 v28, v29\nv_max_f32 v28, v28, v29\nv_mov_b32 v29, v28\ns_nop 1\nv_permlane16_swap_b32 v28, v29\nv_max_f32 v28, v28, v29\nv_sub_f32 v28, 0, v28\nv_mov_b32 v29, v28\n"
"v_mov_b32 v30, v28\nv_mov_b32 v31, v28\nv_mov_b32 v33, v32\ns_nop 1\nv_permlane32_swap_b32 v32, v33\nv_max_f32 v32, v32, v33\nv_mov_b32 v33, v32\ns_nop 1\n"
"v_permlane16_swap_b32 v32, v33\nv_max_f32 v32, v32, v33\nv_sub_f32 v32, 0, v32\nv_mov_b32 v33, v32\nv_mov_b32 v34, v32\nv_mov_b32 v35, v32\nv_mov_b32 v37, v36\ns_nop 1\n"
"v_permlane32_swap_b32 v36, v37\nv_max_f32 v36, v36, v37\nv_mov_b32 v37, v36\ns_nop 1\nv_permlane16_swap_b32 v36, v37\nv_max_f32 v36, v36, v37\nv_sub_f32 v36, 0, v36\nv_mov_b32 v37, v36\n"
"v_mov_b32 v38, v36\nv_mov_b32 v39, v36\nv_mov_b32 v41, v40\ns_nop 1\nv_permlane32_swap_b32 v40, v41\nv_max_f32 v40, v40, v41\nv_mov_b32 v41, v40\ns_nop 1\n"
"v_permlane16_swap_b32 v40, v41\nv_max_f32 v40, v40, v41\nv_sub_f32 v40, 0, v40\nv_mov_b32 v41, v40\nv_mov_b32 v42, v40\nv_mov_b32 v43, v40\nv_mov_b32 v45, v44\ns_nop 1\n"
"v_permlane32_swap_b32 v44, v45\nv_max_f32 v44, v44, v45\nv_mov_b32 v45, v44\ns_nop 1\nv_permlane16_swap_b32 v44, v45\nv_max_f32 v44, v44, v45\nv_sub_f32 v44, 0, v44\nv_mov_b32 v45, v44\n"
"v_mov_b32 v46, v44\nv_mov_b32 v47, v44\nv_mov_b32 v49, v48\ns_nop 1\nv_permlane32_swap_b32 v48, v49\nv_max_f32 v48, v48, v49\nv_mov_b32 v49, v48\ns_nop 1\n"
"v_permlane16_swap_b32 v48, v49\nv_max_f32 v48, v48, v49\nv_sub_f32 v48, 0, v48\nv_mov_b32 v49, v48\nv_mov_b32 v50, v48\nv_mov_b32 v51, v48\nv_mov_b32 v52, 0\nv_mov_b32 v80, 0\n"
"v_mov_b32 v81, 0\nv_mov_b32 v82, 0\nv_mov_b32 v83, 0\nv_mov_b32 v84, 0\nv_mov_b32 v85, 0\nv_mov_b32 v86, 0\nv_mov_b32 v87, 0\nv_mov_b32 v88, 0\n"
"v_mov_b32 v89, 0\nv_mov_b32 v90, 0\nv_mov_b32 v91, 0\nv_mov_b32 v92, 0\nv_mov_b32 v93, 0\nv_mov_b32 v94, 0\nv_mov_b32 v95, 0\nv_mov_b32 v96, 0\n"
"v_mov_b32 v97, 0\nv_mov_b32 v98, 0\nv_mov_b32 v99, 0\nv_mov_b32 v100, 0\nv_mov_b32 v101, 0\nv_mov_b32 v102, 0\nv_mov_b32 v103, 0\nv_mov_b32 v104, 0\n"
"v_mov_b32 v105, 0\nv_mov_b32 v106, 0\nv_mov_b32 v107, 0\nv_mov_b32 v108, 0\nv_mov_b32 v109, 0\nv_mov_b32 v110, 0\nv_mov_b32 v111, 0\nv_accvgpr_write_b32 a176, v52\n"
"v_accvgpr_write_b32 a177, v52\nv_accvgpr_write_b32 a178, v52\nv_accvgpr_write_b32 a179, v52\nv_mov_b32 v112, 0\nv_mov_b32 v113, 0\nv_mov_b32 v114, 0\nv_mov_b32 v115, 0\nv_mov_b32 v116, 0\n"
"v_mov_b32 v117, 0\nv_mov_b32 v118, 0\nv_mov_b32 v119, 0\nv_mov_b32 v120, 0\nv_mov_b32 v121, 0\nv_mov_b32 v122, 0\nv_mov_b32 v123, 0\nv_mov_b32 v124, 0\n"
"v_mov_b32 v125, 0\nv_mov_b32 v126, 0\nv_mov_b32 v127, 0\nv_mov_b32 v128, 0\nv_mov_b32 v129, 0\nv_mov_b32 v130, 0\nv_mov_b32 v131, 0\nv_mov_b32 v132, 0\n"
"v_mov_b32 v133, 0\nv_mov_b32 v134, 0\nv_mov_b32 v135, 0\nv_mov_b32 v136, 0\nv_mov_b32 v137, 0\nv_mov_b32 v138, 0\nv_mov_b32 v139, 0\nv_mov_b32 v140, 0\n"
"v_mov_b32 v141, 0\nv_mov_b32 v142, 0\nv_mov_b32 v143, 0\nv_accvgpr_write_b32 a180, v52\nv_accvgpr_write_b32 a181, v52\nv_accvgpr_write_b32 a182, v52\nv_accvgpr_write_b32 a183, v52\nv_mov_b32 v144, 0\n"
"v_mov_b32 v145, 0\nv_mov_b32 v146, 0\nv_mov_b32 v147, 0\nv_mov_b32 v148, 0\nv_mov_b32 v149, 0\nv_mov_b32 v150, 0\nv_mov_b32 v151, 0\nv_mov_b32 v152, 0\n"
"v_mov_b32 v153, 0\nv_mov_b32 v154, 0\nv_mov_b32 v155, 0\nv_mov_b32 v156, 0\nv_mov_b32 v157, 0\nv_mov_b32 v158, 0\nv_mov_b32 v159, 0\nv_mov_b32 v160, 0\n"
"v_mov_b32 v161, 0\nv_mov_b32 v162, 0\nv_mov_b32 v163, 0\nv_mov_b32 v164, 0\nv_mov_b32 v165, 0\nv_mov_b32 v166, 0\nv_mov_b32 v167, 0\nv_mov_b32 v168, 0\n"
"v_mov_b32 v169, 0\nv_mov_b32 v170, 0\nv_mov_b32 v171, 0\nv_mov_b32 v172, 0\nv_mov_b32 v173, 0\nv_mov_b32 v174, 0\nv_mov_b32 v175, 0\nv_accvgpr_write_b32 a184, v52\n"
"v_accvgpr_write_b32 a185, v52\nv_accvgpr_write_b32 a186, v52\nv_accvgpr_write_b32 a187, v52\nv_mov_b32 v176, 0\nv_mov_b32 v177, 0\nv_mov_b32 v178, 0\nv_mov_b32 v179, 0\nv_mov_b32 v180, 0\n"
"v_mov_b32 v181, 0\nv_mov_b32 v182, 0\nv_mov_b32 v183, 0\nv_mov_b32 v184, 0\nv_mov_b32 v185, 0\nv_mov_b32 v186, 0\nv_mov_b32 v187, 0\nv_mov_b32 v188, 0\n"
"v_mov_b32 v189, 0\nv_mov_b32 v190, 0\nv_mov_b32 v191, 0\nv_mov_b32 v192, 0\nv_mov_b32 v193, 0\nv_mov_b32 v194, 0\nv_mov_b32 v195, 0\nv_mov_b32 v196, 0\n"
"v_mov_b32 v197, 0\nv_mov_b32 v198, 0\nv_mov_b32 v199, 0\nv_mov_b32 v200, 0\nv_mov_b32 v201, 0\nv_mov_b32 v202, 0\nv_mov_b32 v203, 0\nv_mov_b32 v204, 0\n"
"v_mov_b32 v205, 0\nv_mov_b32 v206, 0\nv_mov_b32 v207, 0\nv_accvgpr_write_b32 a188, v52\nv_accvgpr_write_b32 a189, v52\nv_accvgpr_write_b32 a190, v52\nv_accvgpr_write_b32 a191, v52\nv_mov_b32 v208, 0\n"
"v_mov_b32 v209, 0\nv_mov_b32 v210, 0\nv_mov_b32 v211, 0\nv_mov_b32 v212, 0\nv_mov_b32 v213, 0\nv_mov_b32 v214, 0\nv_mov_b32 v215, 0\nv_mov_b32 v216, 0\n"
"v_mov_b32 v217, 0\nv_mov_b32 v218, 0\nv_mov_b32 v219, 0\nv_mov_b32 v220, 0\nv_mov_b32 v221, 0\nv_mov_b32 v222, 0\nv_mov_b32 v223, 0\nv_mov_b32 v224, 0\n"
"v_mov_b32 v225, 0\nv_mov_b32 v226, 0\nv_mov_b32 v227, 0\nv_mov_b32 v228, 0\nv_mov_b32 v229, 0\nv_mov_b32 v230, 0\nv_mov_b32 v231, 0\nv_mov_b32 v232, 0\n"
"v_mov_b32 v233, 0\nv_mov_b32 v234, 0\nv_mov_b32 v235, 0\nv_mov_b32 v236, 0\nv_mov_b32 v237, 0\nv_mov_b32 v238, 0\nv_mov_b32 v239, 0\nv_accvgpr_write_b32 a192, v52\n"
"v_accvgpr_write_b32 a193, v52\nv_accvgpr_write_b32 a194, v52\nv_accvgpr_write_b32 a195, v52\nv_mov_b32 v240, 0\nv_mov_b32 v241, 0\nv_mov_b32 v242, 0\nv_mov_b32 v243, 0\nv_mov_b32 v244, 0\n"
"v_mov_b32 v245, 0\nv_mov_b32 v246, 0\nv_mov_b32 v247, 0\nv_mov_b32 v248, 0\nv_mov_b32 v249, 0\nv_mov_b32 v250, 0\nv_mov_b32 v251, 0\nv_mov_b32 v252, 0\n"
"v_mov_b32 v253, 0\nv_mov_b32 v254, 0\nv_mov_b32 v255, 0\nv_accvgpr_write_b32 a208, v52\nv_accvgpr_write_b32 a209, v52\nv_accvgpr_write_b32 a210, v52\nv_accvgpr_write_b32 a211, v52\nv_accvgpr_write_b32 a212, v52\n"
"v_accvgpr_write_b32 a213, v52\nv_accvgpr_write_b32 a214, v52\nv_accvgpr_write_b32 a215, v52\nv_accvgpr_write_b32 a216, v52\nv_accvgpr_write_b32 a217, v52\nv_accvgpr_write_b32 a218, v52\nv_accvgpr_write_b32 a219, v52\nv_accvgpr_write_b32 a220, v52\n"
"v_accvgpr_write_b32 a221, v52\nv_accvgpr_write_b32 a222, v52\nv_accvgpr_write_b32 a223, v52\nv_accvgpr_write_b32 a196, v52\nv_accvgpr_write_b32 a197, v52\nv_accvgpr_write_b32 a198, v52\nv_accvgpr_write_b32 a199, v52\nv_accvgpr_write_b32 a224, v52\n"
"v_accvgpr_write_b32 a225, v52\nv_accvgpr_write_b32 a226, v52\nv_accvgpr_write_b32 a227, v52\nv_accvgpr_write_b32 a228, v52\nv_accvgpr_write_b32 a229, v52\nv_accvgpr_write_b32 a230, v52\nv_accvgpr_write_b32 a231, v52\nv_accvgpr_write_b32 a232, v52\n"
"v_accvgpr_write_b32 a233, v52\nv_accvgpr_write_b32 a234, v52\nv_accvgpr_write_b32 a235, v52\nv_accvgpr_write_b32 a236, v52\nv_accvgpr_write_b32 a237, v52\nv_accvgpr_write_b32 a238, v52\nv_accvgpr_write_b32 a239, v52\nv_accvgpr_write_b32 a240, v52\n"
"v_accvgpr_write_b32 a241, v52\nv_accvgpr_write_b32 a242, v52\nv_accvgpr_write_b32 a243, v52\nv_accvgpr_write_b32 a244, v52\nv_accvgpr_write_b32 a245, v52\nv_accvgpr_write_b32 a246, v52\nv_accvgpr_write_b32 a247, v52\nv_accvgpr_write_b32 a248, v52\n"
"v_accvgpr_write_b32 a249, v52\nv_accvgpr_write_b32 a250, v52\nv_accvgpr_write_b32 a251, v52\nv_accvgpr_write_b32 a252, v52\nv_accvgpr_write_b32 a253, v52\nv_accvgpr_write_b32 a254, v52\nv_accvgpr_write_b32 a255, v52\nv_accvgpr_write_b32 a200, v52\n"
"v_accvgpr_write_b32 a201, v52\nv_accvgpr_write_b32 a202, v52\nv_accvgpr_write_b32 a203, v52\ns_lshl_b32 s89, s67, 21\ns_lshl_b32 s68, s93, 18\ns_add_u32 s89, s89, s68\ns_mov_b32 s66, 0\n.Lfb2:\n"
"s_add_u32 s68, s89, 4096\nbuffer_load_dwordx4 a[112:115], v66, s[40:43], s89 offen offset:0\nbuffer_load_dwordx4 a[116:119], v66, s[40:43], s89 offen offset:64\nbuffer_load_dwordx4 a[120:123], v66, s[40:43], s89 offen offset:128\nbuffer_load_dwordx4 a[124:127], v66, s[40:43], s89 offen offset:192\nbuffer_load_dwordx4 a[128:131], v66, s[40:43], s68 offen offset:0\nbuffer_load_dwordx4 a[132:135], v66, s[40:43], s68 offen offset:64\nbuffer_load_dwordx4 a[136:139], v66, s[40:43], s68 offen offset:128\n"
"buffer_load_dwordx4 a[140:143], v66, s[40:43], s68 offen offset:192\nbuffer_load_ushort v8, v67, s[44:47], s89 offen offset:0\nbuffer_load_ushort v9, v67, s[44:47], s89 offen offset:256\nbuffer_load_ushort v10, v67, s[44:47], s89 offen offset:512\nbuffer_load_ushort v11, v67, s[44:47], s89 offen offset:768\nbuffer_load_ushort v12, v67, s[44:47], s68 offen offset:0\nbuffer_load_ushort v13, v67, s[44:47], s68 offen offset:256\nbuffer_load_ushort v14, v67, s[44:47], s68 offen offset:512\n"
"buffer_load_ushort v15, v67, s[44:47], s68 offen offset:768\ns_waitcnt vmcnt(0)\nv_lshl_or_b32 v8, v9, 16, v8\nv_accvgpr_write_b32 a144, v8\nv_lshl_or_b32 v10, v11, 16, v10\nv_accvgpr_write_b32 a145, v10\nv_lshl_or_b32 v12, v13, 16, v12\nv_accvgpr_write_b32 a146, v12\n"
"v_lshl_or_b32 v14, v15, 16, v14\nv_accvgpr_write_b32 a147, v14\nbuffer_load_ushort v8, v67, s[44:47], s89 offen offset:32\nbuffer_load_ushort v9, v67, s[44:47], s89 offen offset:288\nbuffer_load_ushort v10, v67, s[44:47], s89 offen offset:544\nbuffer_load_ushort v11, v67, s[44:47], s89 offen offset:800\nbuffer_load_ushort v12, v67, s[44:47], s68 offen offset:32\nbuffer_load_ushort v13, v67, s[44:47], s68 offen offset:288\n"
"buffer_load_ushort v14, v67, s[44:47], s68 offen offset:544\nbuffer_load_ushort v15, v67, s[44:47], s68 offen offset:800\ns_waitcnt vmcnt(0)\nv_lshl_or_b32 v8, v9, 16, v8\nv_accvgpr_write_b32 a148, v8\nv_lshl_or_b32 v10, v11, 16, v10\nv_accvgpr_write_b32 a149, v10\nv_lshl_or_b32 v12, v13, 16, v12\n"
"v_accvgpr_write_b32 a150, v12\nv_lshl_or_b32 v14, v15, 16, v14\nv_accvgpr_write_b32 a151, v14\nbuffer_load_ushort v8, v67, s[44:47], s89 offen offset:64\nbuffer_load_ushort v9, v67, s[44:47], s89 offen offset:320\nbuffer_load_ushort v10, v67, s[44:47], s89 offen offset:576\nbuffer_load_ushort v11, v67, s[44:47], s89 offen offset:832\nbuffer_load_ushort v12, v67, s[44:47], s68 offen offset:64\n"
"buffer_load_ushort v13, v67, s[44:47], s68 offen offset:320\nbuffer_load_ushort v14, v67, s[44:47], s68 offen offset:576\nbuffer_load_ushort v15, v67, s[44:47], s68 offen offset:832\ns_waitcnt vmcnt(0)\nv_lshl_or_b32 v8, v9, 16, v8\nv_accvgpr_write_b32 a152, v8\nv_lshl_or_b32 v10, v11, 16, v10\nv_accvgpr_write_b32 a153, v10\n"
"v_lshl_or_b32 v12, v13, 16, v12\nv_accvgpr_write_b32 a154, v12\nv_lshl_or_b32 v14, v15, 16, v14\nv_accvgpr_write_b32 a155, v14\nbuffer_load_ushort v8, v67, s[44:47], s89 offen offset:96\nbuffer_load_ushort v9, v67, s[44:47], s89 offen offset:352\nbuffer_load_ushort v10, v67, s[44:47], s89 offen offset:608\nbuffer_load_ushort v11, v67, s[44:47], s89 offen offset:864\n"
"buffer_load_ushort v12, v67, s[44:47], s68 offen offset:96\nbuffer_load_ushort v13, v67, s[44:47], s68 offen offset:352\nbuffer_load_ushort v14, v67, s[44:47], s68 offen offset:608\nbuffer_load_ushort v15, v67, s[44:47], s68 offen offset:864\ns_waitcnt vmcnt(0)\nv_lshl_or_b32 v8, v9, 16, v8\nv_accvgpr_write_b32 a156, v8\nv_lshl_or_b32 v10, v11, 16, v10\n"
"v_accvgpr_write_b32 a157, v10\nv_lshl_or_b32 v12, v13, 16, v12\nv_accvgpr_write_b32 a158, v12\nv_lshl_or_b32 v14, v15, 16, v14\nv_accvgpr_write_b32 a159, v14\nbuffer_load_ushort v8, v67, s[44:47], s89 offen offset:128\nbuffer_load_ushort v9, v67, s[44:47], s89 offen offset:384\nbuffer_load_ushort v10, v67, s[44:47], s89 offen offset:640\n"
"buffer_load_ushort v11, v67, s[44:47], s89 offen offset:896\nbuffer_load_ushort v12, v67, s[44:47], s68 offen offset:128\nbuffer_load_ushort v13, v67, s[44:47], s68 offen offset:384\nbuffer_load_ushort v14, v67, s[44:47], s68 offen offset:640\nbuffer_load_ushort v15, v67, s[44:47], s68 offen offset:896\ns_waitcnt vmcnt(0)\nv_lshl_or_b32 v8, v9, 16, v8\nv_accvgpr_write_b32 a160, v8\n"
"v_lshl_or_b32 v10, v11, 16, v10\nv_accvgpr_write_b32 a161, v10\nv_lshl_or_b32 v12, v13, 16, v12\nv_accvgpr_write_b32 a162, v12\nv_lshl_or_b32 v14, v15, 16, v14\nv_accvgpr_write_b32 a163, v14\nbuffer_load_ushort v8, v67, s[44:47], s89 offen offset:160\nbuffer_load_ushort v9, v67, s[44:47], s89 offen offset:416\n"
"buffer_load_ushort v10, v67, s[44:47], s89 offen offset:672\nbuffer_load_ushort v11, v67, s[44:47], s89 offen offset:928\nbuffer_load_ushort v12, v67, s[44:47], s68 offen offset:160\nbuffer_load_ushort v13, v67, s[44:47], s68 offen offset:416\nbuffer_load_ushort v14, v67, s[44:47], s68 offen offset:672\nbuffer_load_ushort v15, v67, s[44:47], s68 offen offset:928\ns_waitcnt vmcnt(0)\nv_lshl_or_b32 v8, v9, 16, v8\n"
"v_accvgpr_write_b32 a164, v8\nv_lshl_or_b32 v10, v11, 16, v10\nv_accvgpr_write_b32 a165, v10\nv_lshl_or_b32 v12, v13, 16, v12\nv_accvgpr_write_b32 a166, v12\nv_lshl_or_b32 v14, v15, 16, v14\nv_accvgpr_write_b32 a167, v14\nbuffer_load_ushort v8, v67, s[44:47], s89 offen offset:192\n"
"buffer_load_ushort v9, v67, s[44:47], s89 offen offset:448\nbuffer_load_ushort v10, v67, s[44:47], s89 offen offset:704\nbuffer_load_ushort v11, v67, s[44:47], s89 offen offset:960\nbuffer_load_ushort v12, v67, s[44:47], s68 offen offset:192\nbuffer_load_ushort v13, v67, s[44:47], s68 offen offset:448\nbuffer_load_ushort v14, v67, s[44:47], s68 offen offset:704\nbuffer_load_ushort v15, v67, s[44:47], s68 offen offset:960\ns_waitcnt vmcnt(0)\n"
"v_lshl_or_b32 v8, v9, 16, v8\nv_accvgpr_write_b32 a168, v8\nv_lshl_or_b32 v10, v11, 16, v10\nv_accvgpr_write_b32 a169, v10\nv_lshl_or_b32 v12, v13, 16, v12\nv_accvgpr_write_b32 a170, v12\nv_lshl_or_b32 v14, v15, 16, v14\nv_accvgpr_write_b32 a171, v14\n"
"buffer_load_ushort v8, v67, s[44:47], s89 offen offset:224\nbuffer_load_ushort v9, v67, s[44:47], s89 offen offset:480\nbuffer_load_ushort v10, v67, s[44:47], s89 offen offset:736\nbuffer_load_ushort v11, v67, s[44:47], s89 offen offset:992\nbuffer_load_ushort v12, v67, s[44:47], s68 offen offset:224\nbuffer_load_ushort v13, v67, s[44:47], s68 offen offset:480\nbuffer_load_ushort v14, v67, s[44:47], s68 offen offset:736\nbuffer_load_ushort v15, v67, s[44:47], s68 offen offset:992\n"
"s_waitcnt vmcnt(0)\nv_lshl_or_b32 v8, v9, 16, v8\nv_accvgpr_write_b32 a172, v8\nv_lshl_or_b32 v10, v11, 16, v10\nv_accvgpr_write_b32 a173, v10\nv_lshl_or_b32 v12, v13, 16, v12\nv_accvgpr_write_b32 a174, v12\nv_lshl_or_b32 v14, v15, 16, v14\n"
"v_accvgpr_write_b32 a175, v14\nQcvaa 0,112,0,24\nQmvaa 0,116,4\nQmvaa 0,120,8\nQmvaa 0,124,12\nQcvaa 4,128,0,24\nQmvaa 4,132,4\nQmvaa 4,136,8\n"
"Qmvaa 4,140,12\ns_nop 7\nQe 0\nQe 1\nQe 2\nQe 3\nQe 4\nQe 5\n"
"Qe 6\nQe 7\ns_nop 0\nQk 16,0,1\nQk 17,2,3\nQk 18,4,5\nQk 19,6,7\ns_nop 1\n"
"Qmvva 80,16,144\nQmvva 84,16,148\nQmvva 88,16,152\nQmvva 92,16,156\nQmvva 96,16,160\nQmvva 100,16,164\nQmvva 104,16,168\nQmvva 108,16,172\n"
"Qmaav 176,204,16\nQcvaa 0,112,16,28\nQmvaa 0,116,20\nQmvaa 0,120,24\nQmvaa 0,124,28\nQcvaa 4,128,16,28\nQmvaa 4,132,20\nQmvaa 4,136,24\n"
"Qmvaa 4,140,28\ns_nop 7\nQe 0\nQe 1\nQe 2\nQe 3\nQe 4\nQe 5\n"
"Qe 6\nQe 7\ns_nop 0\nQk 16,0,1\nQk 17,2,3\nQk 18,4,5\nQk 19,6,7\ns_nop 1\n"
"Qmvva 112,16,144\nQmvva 116,16,148\nQmvva 120,16,152\nQmvva 124,16,156\nQmvva 128,16,160\nQmvva 132,16,164\nQmvva 136,16,168\nQmvva 140,16,172\n"
"Qmaav 180,204,16\nQcvaa 0,112,32,32\nQmvaa 0,116,36\nQmvaa 0,120,40\nQmvaa 0,124,44\nQcvaa 4,128,32,32\nQmvaa 4,132,36\nQmvaa 4,136,40\n"
"Qmvaa 4,140,44\ns_nop 7\nQe 0\nQe 1\nQe 2\nQe 3\nQe 4\nQe 5\n"
"Qe 6\nQe 7\ns_nop 0\nQk 16,0,1\nQk 17,2,3\nQk 18,4,5\nQk 19,6,7\ns_nop 1\n"
"Qmvva 144,16,144\nQmvva 148,16,148\nQmvva 152,16,152\nQmvva 156,16,156\nQmvva 160,16,160\nQmvva 164,16,164\nQmvva 168,16,168\nQmvva 172,16,172\n"
"Qmaav 184,204,16\nQcvaa 0,112,48,36\nQmvaa 0,116,52\nQmvaa 0,120,56\nQmvaa 0,124,60\nQcvaa 4,128,48,36\nQmvaa 4,132,52\nQmvaa 4,136,56\n"
"Qmvaa 4,140,60\ns_nop 7\nQe 0\nQe 1\nQe 2\nQe 3\nQe 4\nQe 5\n"
"Qe 6\nQe 7\ns_nop 0\nQk 16,0,1\nQk 17,2,3\nQk 18,4,5\nQk 19,6,7\ns_nop 1\n"
"Qmvva 176,16,144\nQmvva 180,16,148\nQmvva 184,16,152\nQmvva 188,16,156\nQmvva 192,16,160\nQmvva 196,16,164\nQmvva 200,16,168\nQmvva 204,16,172\n"
"Qmaav 188,204,16\nQcvaa 0,112,64,40\nQmvaa 0,116,68\nQmvaa 0,120,72\nQmvaa 0,124,76\nQcvaa 4,128,64,40\nQmvaa 4,132,68\nQmvaa 4,136,72\n"
"Qmvaa 4,140,76\ns_nop 7\nQe 0\nQe 1\nQe 2\nQe 3\nQe 4\nQe 5\n"
"Qe 6\nQe 7\ns_nop 0\nQk 16,0,1\nQk 17,2,3\nQk 18,4,5\nQk 19,6,7\ns_nop 1\n"
"Qmvva 208,16,144\nQmvva 212,16,148\nQmvva 216,16,152\nQmvva 220,16,156\nQmvva 224,16,160\nQmvva 228,16,164\nQmvva 232,16,168\nQmvva 236,16,172\n"
"Qmaav 192,204,16\nQcvaa 0,112,80,44\nQmvaa 0,116,84\nQmvaa 0,120,88\nQmvaa 0,124,92\nQcvaa 4,128,80,44\nQmvaa 4,132,84\nQmvaa 4,136,88\n"
"Qmvaa 4,140,92\ns_nop 7\nQe 0\nQe 1\nQe 2\nQe 3\nQe 4\nQe 5\n"
"Qe 6\nQe 7\ns_nop 0\nQk 16,0,1\nQk 17,2,3\nQk 18,4,5\nQk 19,6,7\ns_nop 1\n"
"Qmvva 240,16,144\nQmvva 244,16,148\nQmvva 248,16,152\nQmvva 252,16,156\nQmava 208,16,160\nQmava 212,16,164\nQmava 216,16,168\nQmava 220,16,172\n"
"Qmaav 196,204,16\nQcvaa 0,112,96,48\nQmvaa 0,116,100\nQmvaa 0,120,104\nQmvaa 0,124,108\nQcvaa 4,128,96,48\nQmvaa 4,132,100\nQmvaa 4,136,104\n"
"Qmvaa 4,140,108\ns_nop 7\nQe 0\nQe 1\nQe 2\nQe 3\nQe 4\nQe 5\n"
"Qe 6\nQe 7\ns_nop 0\nQk 16,0,1\nQk 17,2,3\nQk 18,4,5\nQk 19,6,7\ns_nop 1\n"
"Qmava 224,16,144\nQmava 228,16,148\nQmava 232,16,152\nQmava 236,16,156\nQmava 240,16,160\nQmava 244,16,164\nQmava 248,16,168\nQmava 252,16,172\n"
"Qmaav 200,204,16\ns_add_u32 s89, s89, 8192\ns_add_u32 s66, s66, 1\ns_cmp_lt_u32 s66, 32\ns_cbranch_scc1 .Lfb2\ns_nop 7\nv_accvgpr_read_b32 v0, a176\nv_accvgpr_read_b32 v4, a180\n"
"v_accvgpr_read_b32 v8, a184\nv_accvgpr_read_b32 v12, a188\nv_accvgpr_read_b32 v16, a192\nv_accvgpr_read_b32 v20, a196\nv_accvgpr_read_b32 v24, a200\ns_mov_b32 s73, 0\nv_mov_b32 v30, s73\nv_mov_b32 v31, 0\n"
"v_mov_b32 v58, 0xb9800000\nv_mov_b32 v59, 0\nv_and_b32 v66, 16, v60\nv_mov_b32 v67, 0xffff\nv_lshlrev_b32 v66, v66, v67\ns_mov_b32 vcc_lo, 0x00c0000c\ns_mov_b32 vcc_hi, 0xc0000c00\nv_cndmask_b32 v67, 0, v66, vcc\n"
"s_lshr_b64 vcc, vcc, 2\nv_cndmask_b32 v66, 0, v66, vcc\ns_cmp_eq_u32 s67, 0\ns_cbranch_scc1 .Lfbret0\ns_branch .Lfbret1\n.Lf2_w:\ns_mov_b32 s97, 0\n.Lf2_l:\n"
"s_sleep 1\nbuffer_load_dword v78, v76, s[24:27], 0 offen sc1\ns_waitcnt vmcnt(0)\ns_add_u32 s97, s97, 1\ns_cmp_gt_u32 s97, 1048576\ns_cbranch_scc1 .Lf2_r\nv_sub_u32 v66, v78, v65\nv_cmp_gt_u32 vcc, 2, v66\n"
"s_and_b64 vcc, vcc, s[28:29]\ns_cbranch_scc1 .Lf2_l\ns_branch .Lf2_r\n.Lf3_w:\ns_mov_b32 s97, 0\n.Lf3_l:\ns_sleep 1\nbuffer_load_dword v78, v76, s[24:27], 0 offen sc1\n"
"s_waitcnt vmcnt(0)\ns_add_u32 s97, s97, 1\ns_cmp_gt_u32 s97, 1048576\ns_cbranch_scc1 .Lf3_r\nv_sub_u32 v66, v78, v65\nv_cmp_gt_u32 vcc, 3, v66\ns_and_b64 vcc, vcc, s[28:29]\ns_cbranch_scc1 .Lf3_l\n"
"s_branch .Lf3_r\n.Lx0_w:\ns_mov_b32 s97, 0\n.Lx0_l:\ns_sleep 1\nbuffer_load_dword v78, v76, s[24:27], 0 offen sc1\ns_waitcnt vmcnt(0)\ns_add_u32 s97, s97, 1\n"
"s_cmp_gt_u32 s97, 1048576\ns_cbranch_scc1 .Lx0_r\nv_sub_u32 v66, v78, v65\nv_cmp_gt_u32 vcc, 1, v66\ns_and_b64 vcc, vcc, s[28:29]\ns_cbranch_scc1 .Lx0_l\ns_branch .Lx0_r\n.Lx1_w:\n"
"s_mov_b32 s97, 0\n.Lx1_l:\ns_sleep 1\nbuffer_load_dword v78, v76, s[24:27], 0 offen sc1\ns_waitcnt vmcnt(0)\ns_add_u32 s97, s97, 1\ns_cmp_gt_u32 s97, 1048576\ns_cbranch_scc1 .Lx1_r\n"
"v_sub_u32 v66, v78, v65\nv_cmp_gt_u32 vcc, 1, v66\ns_and_b64 vcc, vcc, s[28:29]\ns_cbranch_scc1 .Lx1_l\ns_branch .Lx1_r\n.Lexit:\n"
  : "=&s"(t0), "=&s"(t1)
  : "s"(__builtin_amdgcn_kernarg_segment_ptr()), "s"(blockIdx.x), "s"((unsigned)(size_t)lds), "s"(__builtin_amdgcn_readfirstlane(threadIdx.x >> 6))
  : "v0", "v1", "v2", "v3", "v4", "v5", "v6", "v7", "v8", "v9", "v10", "v11", "v12", "v13", "v14", "v15", "v16", "v17", "v18", "v19", "v20", "v21", "v22", "v23", "v24", "v25", "v26", "v27", "v28", "v29", "v30", "v31", "v32", "v33", "v34", "v35", "v36", "v37", "v38", "v39", "v40", "v41", "v42", "v43", "v44", "v45", "v46", "v47", "v48", "v49", "v50", "v51", "v52", "v53", "v54", "v55", "v56", "v57", "v58", "v59", "v60", "v61", "v62", "v63", "v64", "v65", "v66", "v67", "v68", "v69", "v70", "v71", "v72", "v73", "v74", "v75", "v76", "v77", "v78", "v79", "v80", "v81", "v82", "v83", "v84", "v85", "v86", "v87", "v88", "v89", "v90", "v91", "v92", "v93", "v94", "v95", "v96", "v97", "v98", "v99", "v100", "v101", "v102", "v103", "v104", "v105", "v106", "v107", "v108", "v109", "v110", "v111", "v112", "v113", "v114", "v115", "v116", "v117", "v118", "v119", "v120", "v121", "v122", "v123", "v124", "v125", "v126", "v127", "v128", "v129", "v130", "v131", "v132", "v133", "v134", "v135", "v136", "v137", "v138", "v139", "v140", "v141", "v142", "v143", "v144", "v145", "v146", "v147", "v148", "v149", "v150", "v151", "v152", "v153", "v154", "v155", "v156", "v157", "v158", "v159", "v160", "v161", "v162", "v163", "v164", "v165", "v166", "v167", "v168", "v169", "v170", "v171", "v172", "v173", "v174", "v175", "v176", "v177", "v178", "v179", "v180", "v181", "v182", "v183", "v184", "v185", "v186", "v187", "v188", "v189", "v190", "v191", "v192", "v193", "v194", "v195", "v196", "v197", "v198", "v199", "v200", "v201", "v202", "v203", "v204", "v205", "v206", "v207", "v208", "v209", "v210", "v211", "v212", "v213", "v214", "v215", "v216", "v217", "v218", "v219", "v220", "v221", "v222", "v223", "v224", "v225", "v226", "v227", "v228", "v229", "v230", "v231", "v232", "v233", "v234", "v235", "v236", "v237", "v238", "v239", "v240", "v241", "v242", "v243", "v244", "v245", "v246", "v247", "v248", "v249", "v250", "v251", "v252", "v253", "v254", "v255", "a0", "a1", "a2", "a3", "a4", "a5", "a6", "a7", "a8", "a9", "a10", "a11", "a12", "a13", "a14", "a15", "a16", "a17", "a18", "a19", "a20", "a21", "a22", "a23", "a24", "a25", "a26", "a27", "a28", "a29", "a30", "a31", "a32", "a33", "a34", "a35", "a36", "a37", "a38", "a39", "a40", "a41", "a42", "a43", "a44", "a45", "a46", "a47", "a48", "a49", "a50", "a51", "a52", "a53", "a54", "a55", "a56", "a57", "a58", "a59", "a60", "a61", "a62", "a63", "a64", "a65", "a66", "a67", "a68", "a69", "a70", "a71", "a72", "a73", "a74", "a75", "a76", "a77", "a78", "a79", "a80", "a81", "a82", "a83", "a84", "a85", "a86", "a87", "a88", "a89", "a90", "a91", "a92", "a93", "a94", "a95", "a96", "a97", "a98", "a99", "a100", "a101", "a102", "a103", "a104", "a105", "a106", "a107", "a108", "a109", "a110", "a111", "a112", "a113", "a114", "a115", "a116", "a117", "a118", "a119", "a120", "a121", "a122", "a123", "a124", "a125", "a126", "a127", "a128", "a129", "a130", "a131", "a132", "a133", "a134", "a135", "a136", "a137", "a138", "a139", "a140", "a141", "a142", "a143", "a144", "a145", "a146", "a147", "a148", "a149", "a150", "a151", "a152", "a153", "a154", "a155", "a156", "a157", "a158", "a159", "a160", "a161", "a162", "a163", "a164", "a165", "a166", "a167", "a168", "a169", "a170", "a171", "a172", "a173", "a174", "a175", "a176", "a177", "a178", "a179", "a180", "a181", "a182", "a183", "a184", "a185", "a186", "a187", "a188", "a189", "a190", "a191", "a192", "a193", "a194", "a195", "a196", "a197", "a198", "a199", "a200", "a201", "a202", "a203", "a204", "a205", "a206", "a207", "a208", "a209", "a210", "a211", "a212", "a213", "a214", "a215", "a216", "a217", "a218", "a219", "a220", "a221", "a222", "a223", "a224", "a225", "a226", "a227", "a228", "a229", "a230", "a231", "a232", "a233", "a234", "a235", "a236", "a237", "a238", "a239", "a240", "a241", "a242", "a243", "a244", "a245", "a246", "a247", "a248", "a249", "a250", "a251", "a252", "a253", "a254", "a255", "s8", "s9", "s10", "s11", "s12", "s13", "s14", "s15", "s16", "s17", "s18", "s19", "s20", "s21", "s22", "s23", "s24", "s25", "s26", "s27", "s28", "s29", "s30", "s31", "s33", "s34", "s35", "s36", "s37", "s38", "s39", "s40", "s41", "s42", "s43", "s44", "s45", "s46", "s47", "s48", "s49", "s50", "s51", "s52", "s53", "s54", "s55", "s56", "s57", "s58", "s59", "s60", "s61", "s62", "s63", "s64", "s65", "s66", "s67", "s68", "s69", "s70", "s71", "s72", "s73", "s74", "s75", "s76", "s77", "s78", "s79", "s80", "s81", "s82", "s83", "s84", "s85", "s86", "s87", "s88", "s89", "s90", "s91", "s92", "s93", "s94", "s95", "s96", "s97", "s98", "s99", "s100", "s101", "vcc", "scc", "memory");
}

extern "C" void kernel_launch(void* const* d_in, const int* in_sizes, int n_in, void* d_out, int out_size, void* d_ws, size_t ws_size, hipStream_t stream) {
  typedef const unsigned short* P;
  (void)hipFuncSetAttribute((const void*)mha2_asm, hipFuncAttributeMaxDynamicSharedMemorySize, 163840);
  hipLaunchKernelGGL(mha2_asm, dim3(256), dim3(256), 163840, stream, (P)d_in[0], (P)d_in[3], (P)d_in[1], (P)d_in[2], (P)d_in[5], (P)d_in[8], (P)d_in[6], (P)d_in[7],
                     (P)d_in[4], (P)d_in[9], (char*)d_ws, (unsigned short*)d_out);
}
```

```cpp
#include <hip/hip_runtime.h>
#include <stdint.h>

__global__ __launch_bounds__(256, 1) __attribute__((aligned(4096)))
void mha2_asm(const unsigned short* X0, const unsigned short* W0, const unsigned short* K0, const unsigned short* V0,
              const unsigned short* X1, const unsigned short* W1, const unsigned short* K1, const unsigned short* V1,
              const unsigned short* WO0, const unsigned short* WO1, char* ws, unsigned short* out) {
  extern __shared__ __attribute__((aligned(16))) char lds[];
  unsigned t0, t1;
  asm volatile(
".macro Qta d,v,o\nds_read_b64_tr_b16 a[\\d:\\d+1], v\\v offset:\\o\n.endm\n.macro Qda d,v,o\nds_read_b128 a[\\d:\\d+3], v\\v offset:\\o\n.endm\n.macro Qzvaa d,a,b\nv_mfma_f32_16x16x32_bf16 v[\\d:\\d+3], a[\\a:\\a+3], a[\\b:\\b+3], 0\n"
".endm\n.macro Qcvaa d,a,b,c\nv_mfma_f32_16x16x32_bf16 v[\\d:\\d+3], a[\\a:\\a+3], a[\\b:\\b+3], v[\\c:\\c+3]\n.endm\n.macro Qk d,a,b\nv_cvt_pk_bf16_f32 v\\d, v\\a, v\\b\n.endm\n.macro Qmvaa d,a,b\n"
"v_mfma_f32_16x16x32_bf16 v[\\d:\\d+3], a[\\a:\\a+3], a[\\b:\\b+3], v[\\d:\\d+3]\n.endm\n.macro Qtv d,v,o\nds_read_b64_tr_b16 v[\\d:\\d+1], v\\v offset:\\o\n.endm\n.macro Qa d,a,b\nv_add_f32 v\\d, v\\a, v\\b\n.endm\n"
".macro Qe d\nv_exp_f32 v\\d, v\\d\n.endm\n.macro Qmvva d,a,b\nv_mfma_f32_16x16x32_bf16 v[\\d:\\d+3], v[\\a:\\a+3], a[\\b:\\b+3], v[\\d:\\d+3]\n.endm\n.macro Qmava d,a,b\nv_mfma_f32_16x16x32_bf16 a[\\d:\\d+3], v[\\a:\\a+3], a[\\b:\\b+3], a[\\d:\\d+3]\n"
".endm\n.macro Qzvvv d,a,b\nv_mfma_f32_16x16x32_bf16 v[\\d:\\d+3], v[\\a:\\a+3], v[\\b:\\b+3], 0\n.endm\n.macro Qzvav d,a,b\nv_mfma_f32_16x16x32_bf16 v[\\d:\\d+3], a[\\a:\\a+3], v[\\b:\\b+3], 0\n.endm\n.macro Qmvav d,a,b\n"
"v_mfma_f32_16x16x32_bf16 v[\\d:\\d+3], a[\\a:\\a+3], v[\\b:\\b+3], v[\\d:\\d+3]\n.endm\n.macro Qcaav d,a,b,c\nv_mfma_f32_16x16x32_bf16 a[\\d:\\d+3], a[\\a:\\a+3], v[\\b:\\b+3], a[\\c:\\c+3]\n.endm\n.macro Qmaav d,a,b\nv_mfma_f32_16x16x32_bf16 a[\\d:\\d+3], a[\\a:\\a+3], v[\\b:\\b+3], a[\\d:\\d+3]\n.endm\n"
".macro Qcvav d,a,b,c\nv_mfma_f32_16x16x32_bf16 v[\\d:\\d+3], a[\\a:\\a+3], v[\\b:\\b+3], v[\\c:\\c+3]\n.endm\n.macro Qmaaa d,a,b\nv_mfma_f32_16x16x32_bf16 a[\\d:\\d+3], a[\\a:\\a+3], a[\\b:\\b+3], a[\\d:\\d+3]\n.endm\n.macro Qdv d,v,o\nds_read_b128 v[\\d:\\d+3], v\\v offset:\\o\n"
".endm\n.p2align 3\ns_and_b32 s33, %3, 4\ns_lshl_b32 s30, s33, 3\ns_load_dwordx8 s[8:15], %2, s30\ns_lshl_b32 s33, s33, 1\ns_load_dwordx4 s[20:23], %2, 0x50\ns_load_dwordx2 s[16:17], %2, s33 offset:0x40\n"
"s_memtime s[80:81]\ns_memrealtime s[82:83]\ns_bfe_u32 s19, %3, 0x30003\ns_and_b32 s39, %3, 3\ns_lshr_b32 %1, %3, 6\ns_lshl2_add_u32 s30, s39, %1\ns_lshl3_add_u32 %0, s30, s19\ns_and_b32 %1, s19, 4\n"
"s_add_u32 s18, s19, %1\ns_lshr_b32 s39, %5, 1\ns_lshl1_add_u32 s18, s18, s39\ns_lshl_b32 %1, %1, 11\ns_mul_i32 s39, s30, 286720\nv_mbcnt_lo_u32_b32 v0, -1, 0\nv_mbcnt_hi_u32_b32 v0, -1, v0\nv_lshlrev_b32 v60, 4, v0\n"
"s_add_u32 m0, %4, 160768\ns_getpc_b64 s[56:57]\n.Lpc0:\ns_and_b32 s56, s56, 0xffffff80\ns_and_b32 s57, s57, 0xffff\ns_mov_b32 s58, .Lend-.Lpc0\ns_mov_b32 s59, 0x20000\ns_and_b32 s94, %0, 31\n"
"s_lshl_b32 s94, s94, 2\ns_add_u32 s94, s94, %5\ns_lshl_b32 s94, s94, 10\nbuffer_load_dwordx4 v60, s[56:59], s94 offen lds\ns_mov_b32 s54, 5242880\ns_mov_b32 s55, 0x20000\ns_mov_b32 s50, 286720\ns_mov_b32 s51, 0x20000\n"
"s_mov_b32 s70, %5\ns_and_b32 s92, s70, 1\ns_lshr_b32 s93, s70, 1\nv_lshrrev_b32 v2, 4, v0\ns_lshl_b32 s68, s92, 18\nv_and_b32 v4, 3, v0\nv_lshlrev_b32 v4, 4, v4\nv_and_b32 v1, 1, v2\n"
"v_lshl_add_u32 v4, v1, 6, v4\nv_lshrrev_b32 v1, 5, v0\nv_lshl_add_u32 v4, v1, 15, v4\nv_bfe_u32 v1, v0, 2, 2\nv_lshl_add_u32 v4, v1, 13, v4\nv_add_u32 v62, s68, v4\ns_lshl_b32 s87, s18, 8\ns_mul_i32 s62, s70, 4128\n"
"s_add_u32 s62, s62, %4\ns_add_u32 s62, s62, 33024\ns_lshl_b32 s69, s92, 1\nv_lshrrev_b32 v3, 5, v0\nv_and_b32 v4, 3, v0\nv_xor_b32 v4, s69, v4\nv_lshlrev_b32 v4, 4, v4\nv_lshl_add_u32 v4, v3, 6, v4\n"
"v_bfe_u32 v1, v0, 2, 3\nv_mul_u32_u24 v64, 1280, v1\nv_add_u32 v64, v64, v4\ns_mul_i32 s86, s93, 143360\ns_mul_i32 s68, s92, 10240\ns_add_u32 s86, s86, s68\ns_mul_i32 s63, s92, 14336\ns_mul_i32 s68, s93, 7168\n"
"s_add_u32 s63, s63, s68\ns_add_u32 s63, s63, %4\ns_add_u32 s63, s63, 99072\ns_lshl_b32 s34, s33, 1\ns_add_u32 s34, s34, s30\ns_mul_hi_u32 s35, s34, 6291456\ns_mul_i32 s34, s34, 6291456\ns_lshl_b32 s36, s30, 5\n"
"s_and_b32 s37, s18, 30\ns_add_u32 s36, s36, s37\ns_lshl_b32 s36, s36, 18\ns_sub_u32 s36, s36, 1024\ns_subb_u32 s37, 0, 0\ns_mul_i32 s38, s30, 573440\ns_mul_i32 s33, s33, 160\ns_add_u32 s38, s38, s33\n"
"s_and_b32 s33, s19, 3\ns_mul_i32 s33, s33, 320\ns_add_u32 s38, s38, s33\ns_mov_b32 s26, 6291456\ns_mov_b32 s27, 0x20000\nv_and_b32 v5, 7, v0\nv_lshlrev_b32 v5, 7, v5\nv_add_u32 v76, 5242880, v5\n"
"s_lshl_b32 s68, s70, 2\ns_add_u32 s68, s68, 4\nv_bfe_u32 v5, v0, 3, 1\nv_mad_u32_u24 v76, v5, s68, v76\ns_mov_b32 s42, 2622464\ns_mov_b32 s43, 0x20000\ns_mov_b32 s46, 2622464\ns_mov_b32 s47, 0x20000\n"
"s_mov_b32 s58, 0\ns_mov_b32 s59, 0x20000\ns_mov_b32 s65, 0\ns_mov_b32 s60, %1\ns_add_u32 s90, s60, 260096\ns_mov_b32 s67, 0\ns_mov_b32 s72, 0x3e0293ee\ns_lshl_b32 s68, s70, 8\n"
"s_lshl_b32 s69, s93, 5\ns_or_b32 s88, s68, s69\nv_bfe_u32 v1, v0, 2, 2\nv_mul_u32_u24 v4, 0x8100, v1\ns_waitcnt lgkmcnt(0)\ns_mov_b32 s52, s10\ns_and_b32 s53, s11, 0xffff\ns_add_u32 m0, s62, 0\n"
"s_add_u32 s66, s87, 0\nbuffer_load_dwordx4 v62, s[52:55], s66 offen lds\ns_add_u32 s66, s87, 64504\nbuffer_load_dwordx4 v62, s[52:55], s66 offen offset:1032 lds\ns_add_u32 s66, s87, 129008\nbuffer_load_dwordx4 v62, s[52:55], s66 offen offset:2064 lds\ns_add_u32 s66, s87, 193512\nbuffer_load_dwordx4 v62, s[52:55], s66 offen offset:3096 lds\n"
"s_add_u32 s48, s8, s39\ns_addc_u32 s49, s9, 0\ns_and_b32 s49, s49, 0xffff\ns_add_u32 m0, s63, 33024\ns_add_u32 s97, s86, 0\nbuffer_load_dwordx4 v64, s[48:51], s97 offen lds\ns_add_u32 s97, s86, 19456\nbuffer_load_dwordx4 v64, s[48:51], s97 offen offset:1024 lds\n"
"s_add_u32 s97, s86, 38912\nbuffer_load_dwordx4 v64, s[48:51], s97 offen offset:2048 lds\ns_add_u32 s97, s86, 58368\nbuffer_load_dwordx4 v64, s[48:51], s97 offen offset:3072 lds\ns_add_u32 m0, s63, 37120\ns_add_u32 s97, s86, 81920\nbuffer_load_dwordx4 v64, s[48:51], s97 offen lds\ns_add_u32 s97, s86, 101376\n"
"buffer_load_dwordx4 v64, s[48:51], s97 offen offset:1024 lds\ns_add_u32 s97, s86, 120832\nbuffer_load_dwordx4 v64, s[48:51], s97 offen offset:2048 lds\ns_add_u32 m0, s62, 16512\ns_add_u32 s66, s87, 128\nbuffer_load_dwordx4 v62, s[52:55], s66 offen lds\ns_add_u32 s20, s20, s34\ns_addc_u32 s21, s21, s35\n"
"s_mov_b32 s24, s20\ns_and_b32 s25, s21, 0xffff\nbuffer_load_dword v65, v76, s[24:27], 0 offen sc1\ns_add_u32 s40, s12, s36\ns_addc_u32 s41, s13, s37\ns_and_b32 s41, s41, 0xffff\ns_add_u32 s44, s14, s36\ns_addc_u32 s45, s15, s37\n"
"s_add_u32 s66, s87, 64632\nbuffer_load_dwordx4 v62, s[52:55], s66 offen offset:1032 lds\ns_and_b32 s45, s45, 0xffff\ns_mov_b32 s56, s20\ns_and_b32 s57, s21, 0xffff\ns_add_u32 s16, s16, -4096\ns_addc_u32 s17, s17, -1\ns_add_u32 s22, s22, s38\n"
"s_addc_u32 s23, s23, 0\nv_and_b32 v1, 3, v0\nv_lshl_or_b32 v4, v1, 4, v4\nv_and_b32 v1, 1, v2\nv_lshl_or_b32 v4, v1, 7, v4\ns_add_u32 s66, s87, 129136\nbuffer_load_dwordx4 v62, s[52:55], s66 offen offset:2064 lds\nv_lshl_or_b32 v4, v3, 6, v4\n"
"v_xor_b32 v4, s88, v4\nv_add_u32 v69, 1024, v4\nv_bfe_u32 v1, v0, 1, 2\nv_mul_u32_u24 v4, 0x8100, v1\nv_and_b32 v1, 9, v0\nv_lshl_or_b32 v4, v1, 4, v4\nv_lshl_or_b32 v4, v2, 5, v4\nv_xor_b32 v4, s68, v4\n"
"s_add_u32 s66, s87, 193640\nbuffer_load_dwordx4 v62, s[52:55], s66 offen offset:3096 lds\nv_add_u32 v72, 1024, v4\ns_lshl_b32 s68, s70, 12\ns_add_u32 s61, s68, %4\ns_mul_i32 s68, s92, 8320\ns_lshl_b32 s69, s93, 12\ns_add_u32 s68, s68, s69\n"
"s_add_u32 s68, s68, %4\ns_add_u32 s64, s68, 16384\ns_lshl_b32 s69, s93, 11\ns_mov_b64 exec, 0\ns_cmp_eq_u32 s70, 0\ns_cbranch_scc1 .Lpg0\ns_cmp_eq_u32 s70, 1\ns_cbranch_scc1 .Lpg1\n"
"s_cmp_eq_u32 s70, 2\ns_cbranch_scc1 .Lpg2\ns_branch .Lpg3\n.Ltr0:\ns_mov_b64 exec, -1\ns_add_u32 s69, s69, %4\nv_bfe_u32 v3, v0, 3, 1\nv_lshlrev_b32 v4, 1, v3\n"
"v_xor_b32 v4, v4, v2\nv_and_b32 v1, 7, v0\nv_lshl_add_u32 v4, v1, 2, v4\nv_lshlrev_b32 v4, 4, v4\nv_bfe_u32 v1, v0, 2, 2\nv_and_b32 v5, 255, v4\ns_add_u32 m0, s63, 0\ns_add_u32 s97, s86, 128\n"
"buffer_load_dwordx4 v64, s[48:51], s97 offen lds\nv_lshl_or_b32 v1, v1, 12, v5\nv_add_u32 v68, s69, v1\ns_mul_i32 s68, s92, 7168\ns_add_u32 s68, s68, %4\ns_add_u32 s68, s68, 99072\nv_mul_u32_u24 v1, 14336, v3\nv_add3_u32 v63, v1, v4, s68\n"
"v_lshrrev_b32 v3, 5, v0\nv_and_b32 v4, 31, v0\ns_add_u32 s97, s86, 19584\nbuffer_load_dwordx4 v64, s[48:51], s97 offen offset:1024 lds\nv_lshlrev_b32 v4, 3, v4\nv_and_b32 v1, 1, v2\nv_lshl_add_u32 v4, v1, 13, v4\nv_lshl_add_u32 v4, v3, 12, v4\n"
"v_add_u32 v4, 16384, v4\nv_add_u32 v70, s69, v4\nv_add_u32 v71, 66048, v70\ns_mul_i32 s68, s93, 8256\ns_add_u32 s68, s68, %4\ns_add_u32 s97, s86, 39040\nbuffer_load_dwordx4 v64, s[48:51], s97 offen offset:2048 lds\ns_add_u32 s68, s68, 33024\n"
"v_mul_u32_u24 v3, 1032, v2\nv_and_b32 v4, 15, v0\nv_lshlrev_b32 v4, 4, v4\nv_add3_u32 v61, v3, v4, s68\ns_lshl_b32 s68, s70, 2\ns_add_u32 s69, %4, 131968\ns_add_u32 s68, s68, s69\nv_mov_b32 v73, s68\n"
"s_add_u32 s97, s86, 58496\nbuffer_load_dwordx4 v64, s[48:51], s97 offen offset:3072 lds\nv_and_b32 v3, 3, v0\nv_lshl_add_u32 v74, v3, 2, s69\nv_mov_b32 v75, 0\ns_mov_b32 s79, -2031\nds_write_b32 v73, v75\ns_waitcnt lgkmcnt(0)\n"
"s_mul_i32 s91, s70, 7168\ns_add_u32 s91, s91, %4\ns_add_u32 s91, s91, 98304\ns_add_u32 m0, s63, 4096\ns_add_u32 s97, s86, 82048\nbuffer_load_dwordx4 v64, s[48:51], s97 offen lds\ns_lshl_b32 s96, s70, 11\ns_add_u32 s96, s96, %4\n"
"v_and_b32 v3, 1, v0\nv_mul_u32_u24 v3, 0xaaaa, v3\nv_or_b32 v77, 0x4444, v3\ns_and_b32 s68, s19, 4\ns_lshl_b32 s28, 15, s68\ns_mov_b32 s29, 0\ns_and_b32 s68, s19, 4\ns_add_u32 s97, s86, 101504\n"
"buffer_load_dwordx4 v64, s[48:51], s97 offen offset:1024 lds\ns_add_u32 s68, s68, s19\ns_lshl_b32 s71, s68, 18\ns_mul_i32 s69, s70, 0x80100\ns_and_b32 s69, s69, 0x100100\ns_add_u32 s68, s71, s69\ns_lshr_b32 s98, %0, 1\ns_xor_b32 s98, s98, %0\n"
"s_lshl_b32 s98, s98, 11\ns_and_b32 s98, s98, 8192\ns_add_u32 s97, s86, 120960\nbuffer_load_dwordx4 v64, s[48:51], s97 offen offset:2048 lds\ns_add_u32 s68, s68, s98\ns_add_u32 s68, s68, 7168\ns_add_u32 s30, s20, s68\ns_addc_u32 s31, s21, 0\n"
"s_mov_b32 s77, 0\nv_bfe_i32 v8, v0, 3, 1\nv_lshlrev_b32 v4, 10, v0\nv_and_b32 v4, 0x6000, v4\nv_lshrrev_b32 v3, 5, v0\nv_lshl_or_b32 v4, v3, 16, v4\nv_and_b32 v3, 0x70, v60\nv_or_b32 v4, v3, v4\n"
"v_or_b32 v8, 1, v8\nv_mov_b32 v9, v8\nv_mov_b32 v10, v8\nv_mov_b32 v11, v8\nbuffer_store_dwordx4 v[8:11], v4, s[24:27], s68 offen offset:0 sc1\ns_nop 1\ns_lshl_b32 s68, s70, 8\nv_lshl_or_b32 v3, v2, 15, v60\n"
"v_xor_b32 v79, s68, v3\ns_mov_b32 s99, 512\ns_getpc_b64 s[100:101]\n.Lpc1:\ns_min_u32 s94, s94, .Lend-.Lpc1-1024\ns_add_u32 s100, s100, s94\ns_addc_u32 s101, s101, 0\ns_mov_b32 s94, 0x7ffff000\n"
"s_mul_i32 s68, %0, 40960\ns_mul_i32 s69, s93, 20480\ns_add_u32 s68, s68, s69\ns_lshl_b32 s69, s92, 10\ns_add_u32 s68, s68, s69\ns_add_u32 s68, s68, 2048\ns_add_u32 s94, s10, s68\ns_addc_u32 s95, s11, 0\n"
"s_and_b32 s95, s95, 0xffff\ns_mov_b32 s75, 17408\n.Lround:\ns_mov_b64 vcc, 0\ns_waitcnt vmcnt(13)\ns_barrier\nQta 112,61,0\nQta 114,61,512\n"
"Qda 208,63,33024\nQda 212,63,34048\nQda 216,63,35072\nQda 220,63,36096\nQda 224,63,37120\nQda 228,63,38144\nQda 232,63,39168\ns_mov_b64 exec, 0\n"
"s_cmp_eq_u32 s70, 0\ns_cbranch_scc1 .Lpg4\ns_cmp_eq_u32 s70, 1\ns_cbranch_scc1 .Lpg5\ns_cmp_eq_u32 s70, 2\ns_cbranch_scc1 .Lpg6\ns_branch .Lpg7\n.Ltr9:\n"
"s_mov_b64 exec, -1\nQda 176,63,33536\nQta 116,61,4128\nQta 118,61,4640\ns_add_u32 m0, s62, 33024\ns_add_u32 s66, s87, 524288\nbuffer_load_dwordx4 v62, s[52:55], s66 offen lds\nQta 120,61,8\n"
"Qta 122,61,520\nQta 124,61,4136\ns_add_u32 s66, s87, 588792\nbuffer_load_dwordx4 v62, s[52:55], s66 offen offset:1032 lds\nQta 126,61,4648\nQta 128,61,256\nQta 130,61,768\ns_add_u32 s66, s87, 653296\n"
"buffer_load_dwordx4 v62, s[52:55], s66 offen offset:2064 lds\nQta 132,61,4384\nQta 134,61,4896\nQta 136,61,264\ns_add_u32 s66, s87, 717800\nbuffer_load_dwordx4 v62, s[52:55], s66 offen offset:3096 lds\nQta 138,61,776\nQta 140,61,4392\n"
"Qta 142,61,4904\ns_add_u32 m0, s62, 49536\ns_add_u32 s66, s87, 524416\nbuffer_load_dwordx4 v62, s[52:55], s66 offen lds\ns_waitcnt lgkmcnt(15)\nQzvaa 0,112,208\nQzvaa 4,112,212\ns_add_u32 s66, s87, 588920\n"
"buffer_load_dwordx4 v62, s[52:55], s66 offen offset:1032 lds\nQda 180,63,34560\nQzvaa 8,112,216\nQda 184,63,35584\ns_add_u32 s66, s87, 653424\nbuffer_load_dwordx4 v62, s[52:55], s66 offen offset:2064 lds\nQzvaa 12,112,220\nQda 188,63,36608\n"
"Qzvaa 16,112,224\ns_add_u32 s66, s87, 717928\nbuffer_load_dwordx4 v62, s[52:55], s66 offen offset:3096 lds\nQda 192,63,37632\nQzvaa 20,112,228\nQda 196,63,38656\ns_mov_b64 exec, 0\ns_cmp_eq_u32 s70, 0\n"
"s_cbranch_scc1 .Lpg8\ns_cmp_eq_u32 s70, 1\ns_cbranch_scc1 .Lpg9\ns_cmp_eq_u32 s70, 2\ns_cbranch_scc1 .Lpg10\ns_branch .Lpg11\n.Ltr10:\ns_mov_b64 exec, -1\n"
"Qzvaa 52,112,232\nQda 200,63,39680\nv_max3_f32 v0, |v0|, |v1|, |v2|\nv_max3_f32 v4, |v4|, |v5|, |v6|\nv_max3_f32 v8, |v8|, |v9|, |v10|\nv_max3_f32 v12, |v12|, |v13|, |v14|\nv_max3_f32 v16, |v16|, |v17|, |v18|\nv_max3_f32 v20, |v20|, |v21|, |v22|\n"
"s_nop 0\nv_max3_f32 v52, |v52|, |v53|, |v54|\nv_min3_u32 v56, v0, v4, v8\nv_min3_u32 v56, v56, v12, v16\nv_min3_u32 v56, v56, v20, v52\nv_mov_b32 v57, v56\ns_nop 1\nv_permlane32_swap_b32 v56, v57\n"
"v_max_f32 v56, v56, v57\nv_mov_b32 v57, v56\ns_nop 1\nv_permlane16_swap_b32 v56, v57\nv_max_f32 v56, v56, v57\ns_nop 1\nv_min_f32_dpp v56, v56, v56 row_ror:8 row_mask:0xf bank_mask:0xf\ns_nop 1\n"
"v_min_f32_dpp v56, v56, v56 row_ror:4 row_mask:0xf bank_mask:0xf\ns_nop 1\nv_min_f32_dpp v56, v56, v56 row_ror:2 row_mask:0xf bank_mask:0xf\ns_nop 1\nv_min_f32_dpp v56, v56, v56 row_ror:1 row_mask:0xf bank_mask:0xf\nv_and_b32 v56, 0x7f800000, v56\nv_min_u32 v56, 0x78000000, v56\nv_add_u32 v56, 0x5400000, v56\n"
"v_mov_b32 v57, v56\nv_mov_b64 v[58:59], v[56:57]\nv_mul_f32 v52, -v56, s72\n.Lround2:\ns_waitcnt lgkmcnt(6)\nQcvaa 80,112,208,56\nQk 96,96,97\nQk 97,98,99\n"
"Qmvaa 80,116,176\ns_add_u32 s68, s71, 0\nQk 98,100,101\nQcvaa 84,120,208,56\ns_cbranch_vccz .Lst27\nbuffer_store_dwordx4 a[0:3], v79, s[56:59], s68 offen offset:0 sc1\n.Lst27:\nQmvaa 84,124,176\n"
"Qk 99,102,103\nv_accvgpr_write_b32 a8, v96\nQcvaa 88,128,208,56\ns_add_u32 s68, s71, 16384\nv_accvgpr_write_b32 a9, v97\nQmvaa 88,132,176\ns_cbranch_vccz .Lst26\nbuffer_store_dwordx4 a[4:7], v79, s[56:59], s68 offen offset:0 sc1\n"
".Lst26:\nQcvaa 92,136,208,56\nv_accvgpr_write_b32 a10, v98\nv_accvgpr_write_b32 a11, v99\nQmvaa 92,140,176\ns_waitcnt lgkmcnt(0)\nQk 104,104,105\nQcvaa 124,136,212,56\n"
"s_waitcnt vmcnt(16)\ns_barrier\nQmvaa 124,140,180\nQta 144,61,16512\ns_add_u32 m0, s62, 0\ns_add_u32 s69, s87, 1048576\nQcvaa 120,128,212,56\nbuffer_load_dwordx4 v62, s[52:55], s69 offen lds\n"
"Qmvaa 120,132,180\nQta 146,61,17024\nQk 105,106,107\nQcvaa 116,120,212,56\nQk 106,108,109\nQk 107,110,111\nQmvaa 116,124,180\nQta 148,61,20640\n"
"s_add_u32 s69, s87, 1113080\nQcvaa 112,112,212,56\nbuffer_load_dwordx4 v62, s[52:55], s69 offen offset:1032 lds\nQmvaa 112,116,180\nQta 150,61,21152\nv_accvgpr_write_b32 a12, v104\nQcvaa 144,112,216,56\nv_accvgpr_write_b32 a13, v105\n"
"v_accvgpr_write_b32 a14, v106\nQmvaa 144,116,184\nQta 152,61,16520\ns_add_u32 s69, s87, 1177584\nQcvaa 148,120,216,56\nbuffer_load_dwordx4 v62, s[52:55], s69 offen offset:2064 lds\nQmvaa 148,124,184\nQta 154,61,17032\n"
"v_accvgpr_write_b32 a15, v107\nQcvaa 152,128,216,56\nQk 136,136,137\nQk 137,138,139\nQmvaa 152,132,184\nQta 156,61,20648\ns_add_u32 s69, s87, 1242088\nQcvaa 156,136,216,56\n"
"buffer_load_dwordx4 v62, s[52:55], s69 offen offset:3096 lds\nQmvaa 156,140,184\nQta 158,61,21160\nQk 138,140,141\nQcvaa 188,136,220,56\nQk 139,142,143\nv_accvgpr_write_b32 a28, v136\nQmvaa 188,140,188\n"
"Qta 160,61,16768\ns_add_u32 m0, s63, 33024\ns_add_u32 s88, s86, 256\nQcvaa 184,128,220,56\nbuffer_load_dwordx4 v64, s[48:51], s88 offen lds\nQmvaa 184,132,188\nQta 162,61,17280\nv_accvgpr_write_b32 a29, v137\n"
"Qcvaa 180,120,220,56\nv_accvgpr_write_b32 a30, v138\nv_accvgpr_write_b32 a31, v139\nQmvaa 180,124,188\nQta 164,61,20896\ns_add_u32 s88, s86, 19712\nQcvaa 176,112,220,56\nbuffer_load_dwordx4 v64, s[48:51], s88 offen offset:1024 lds\n"
"Qmvaa 176,116,188\nQta 166,61,21408\nQk 128,128,129\nQcvaa 208,112,224,56\nQk 129,130,131\nQk 130,132,133\nQmvaa 208,116,192\nQta 168,61,16776\n"
"s_add_u32 s88, s86, 39168\nQcvaa 212,120,224,56\nbuffer_load_dwordx4 v64, s[48:51], s88 offen offset:2048 lds\nQmvaa 212,124,192\nQta 170,61,17288\nQk 131,134,135\nQcvaa 216,128,224,56\nv_accvgpr_write_b32 a24, v128\n"
"v_accvgpr_write_b32 a25, v129\nQmvaa 216,132,192\nQta 172,61,20904\ns_add_u32 s88, s86, 58624\nQcvaa 220,136,224,56\nbuffer_load_dwordx4 v64, s[48:51], s88 offen offset:3072 lds\nQmvaa 220,140,192\nQta 174,61,21416\n"
"v_accvgpr_write_b32 a26, v130\nQcvaa 252,136,228,56\nv_accvgpr_write_b32 a27, v131\nQk 160,160,161\nQmvaa 252,140,196\ns_add_u32 m0, s63, 37120\ns_add_u32 s88, s86, 82176\nQk 161,162,163\n"
"Qcvaa 248,128,228,56\nbuffer_load_dwordx4 v64, s[48:51], s88 offen lds\nQmvaa 248,132,196\nQk 162,164,165\nQk 163,166,167\nQcvaa 244,120,228,56\nv_accvgpr_write_b32 a40, v160\nv_accvgpr_write_b32 a41, v161\n"
"Qmvaa 244,124,196\ns_add_u32 s88, s86, 101632\nv_accvgpr_write_b32 a42, v162\nQcvaa 240,112,228,56\nbuffer_load_dwordx4 v64, s[48:51], s88 offen offset:1024 lds\nQmvaa 240,116,196\nv_accvgpr_write_b32 a43, v163\nQk 168,168,169\n"
"Qcvaa 16,112,232,56\nQk 169,170,171\nQk 170,172,173\nQmvaa 16,116,200\ns_add_u32 s88, s86, 121088\nQk 171,174,175\nQcvaa 20,120,232,56\nbuffer_load_dwordx4 v64, s[48:51], s88 offen offset:2048 lds\n"
"Qmvaa 20,124,200\nv_accvgpr_write_b32 a44, v168\nv_accvgpr_write_b32 a45, v169\nQcvaa 24,128,232,56\nv_accvgpr_write_b32 a46, v170\nv_accvgpr_write_b32 a47, v171\nQmvaa 24,132,200\nQk 200,200,201\n"
"Qk 201,202,203\nQcvaa 28,136,232,56\nQk 202,204,205\nQk 203,206,207\nQmvaa 28,140,200\nv_accvgpr_write_b32 a60, v200\nv_accvgpr_write_b32 a61, v201\ns_waitcnt lgkmcnt(0)\n"
"Qcvaa 96,144,208,56\nv_accvgpr_write_b32 a62, v202\nv_accvgpr_write_b32 a63, v203\nQmvaa 96,148,176\ns_add_u32 s68, s71, 0\nQk 192,192,193\nQcvaa 100,152,208,56\ns_cbranch_vccz .Lst25\n"
"buffer_store_dwordx4 a[16:19], v79, s[56:59], s68 offen offset:1024 sc1\n.Lst25:\nQmvaa 100,156,176\nQk 193,194,195\nQk 194,196,197\nQcvaa 104,160,208,56\ns_add_u32 s68, s71, 16384\nQk 195,198,199\n"
"Qmvaa 104,164,176\ns_cbranch_vccz .Lst24\nbuffer_store_dwordx4 a[20:23], v79, s[56:59], s68 offen offset:1024 sc1\n.Lst24:\nQcvaa 108,168,208,56\nv_accvgpr_write_b32 a56, v192\nv_accvgpr_write_b32 a57, v193\nQmvaa 108,172,176\n"
"s_waitcnt lgkmcnt(0)\nv_accvgpr_write_b32 a58, v194\n.Lpg0:\ns_cbranch_execz .Ltr0\nQcvaa 140,168,212,56\ns_waitcnt vmcnt(15)\ns_barrier\nQmvaa 140,172,180\n"
"Qda 208,63,0\ns_add_u32 m0, s62, 16512\ns_add_u32 s69, s87, 1048704\nQcvaa 136,160,212,56\nbuffer_load_dwordx4 v62, s[52:55], s69 offen lds\nQmvaa 136,164,180\nQda 176,63,512\nv_accvgpr_write_b32 a59, v195\n"
"Qcvaa 132,152,212,56\nQta 112,61,33024\nQk 224,224,225\nQmvaa 132,156,180\ns_add_u32 s69, s87, 1113208\nQk 225,226,227\nQcvaa 128,144,212,56\nbuffer_load_dwordx4 v62, s[52:55], s69 offen offset:1032 lds\n"
"Qmvaa 128,148,180\nQda 212,63,1024\nQk 226,228,229\nQcvaa 160,144,216,56\nQda 180,63,1536\nQk 227,230,231\nQmvaa 160,148,184\nQta 114,61,33536\n"
"s_add_u32 s69, s87, 1177712\nQcvaa 164,152,216,56\nbuffer_load_dwordx4 v62, s[52:55], s69 offen offset:2064 lds\nQmvaa 164,156,184\nQta 116,61,37152\nv_accvgpr_write_b32 a72, v224\nQcvaa 168,160,216,56\nv_accvgpr_write_b32 a73, v225\n"
"v_accvgpr_write_b32 a74, v226\nQmvaa 168,164,184\nQta 118,61,37664\ns_add_u32 s69, s87, 1242216\nQcvaa 172,168,216,56\nbuffer_load_dwordx4 v62, s[52:55], s69 offen offset:3096 lds\nQmvaa 172,172,184\nQda 216,63,2048\n"
"v_accvgpr_write_b32 a75, v227\nQcvaa 204,168,220,56\nQda 184,63,2560\nQk 232,232,233\nQmvaa 204,172,188\nQta 120,61,33032\ns_add_u32 m0, s61, 0\nQcvaa 200,160,220,56\n"
"s_cbranch_vccnz .Ld0q0\nbuffer_load_dwordx4 v69, s[40:43], s60 offen nt lds\n.Ld0q0:\nQmvaa 200,164,188\nQta 122,61,33544\nQk 233,234,235\nQcvaa 196,152,220,56\nQk 234,236,237\n"
"Qk 235,238,239\nQmvaa 196,156,188\nQta 124,61,37160\nv_accvgpr_write_b32 a76, v232\nQcvaa 192,144,220,56\nQda 220,63,3072\nv_accvgpr_write_b32 a77, v233\nQmvaa 192,148,188\n"
"Qda 188,63,3584\nv_accvgpr_write_b32 a78, v234\nQcvaa 224,144,224,56\nQta 126,61,37672\nv_accvgpr_write_b32 a79, v235\nQmvaa 224,148,192\nQk 32,32,33\nQk 33,34,35\n"
"Qcvaa 228,152,224,56\nQta 128,61,33280\nQk 34,36,37\nQmvaa 228,156,192\nQk 35,38,39\nv_accvgpr_write_b32 a104, v32\nQcvaa 232,160,224,56\nQta 130,61,33792\n"
"v_accvgpr_write_b32 a105, v33\nQmvaa 232,164,192\nv_accvgpr_write_b32 a106, v34\nv_accvgpr_write_b32 a107, v35\nQcvaa 236,168,224,56\nQda 224,63,4096\nQk 40,40,41\nQmvaa 236,172,192\n"
"Qda 192,63,4608\nQk 41,42,43\nQcvaa 12,168,228,56\nQta 132,61,37408\nQk 42,44,45\nQmvaa 12,172,196\nQk 43,46,47\nv_accvgpr_write_b32 a108, v40\n"
"Qcvaa 8,160,228,56\nQta 134,61,37920\nv_accvgpr_write_b32 a109, v41\nQmvaa 8,164,196\nv_accvgpr_write_b32 a110, v42\nv_accvgpr_write_b32 a111, v43\nQcvaa 4,152,228,56\nQta 136,61,33288\n"
"Qmvaa 4,156,196\nQcvaa 0,144,228,56\nQda 228,63,5120\nQmvaa 0,148,196\nQda 196,63,5632\nQcvaa 32,144,232,56\nQta 138,61,33800\nQmvaa 32,148,200\n"
"Qcvaa 36,152,232,56\nQta 140,61,37416\nQmvaa 36,156,200\nQcvaa 40,160,232,56\nQta 142,61,37928\nQmvaa 40,164,200\nQcvaa 44,168,232,56\nQda 232,63,6144\n"
"Qmvaa 44,172,200\nQda 200,63,6656\ns_waitcnt lgkmcnt(2)\nQmvaa 80,112,208\nQmvaa 80,116,176\ns_add_u32 s68, s71, 0\nQmvaa 84,120,208\ns_cbranch_vccz .Lst23\n"
"buffer_store_dwordx4 a[32:35], v79, s[56:59], s68 offen offset:2048 sc1\n.Lst23:\nQmvaa 84,124,176\nQmvaa 88,128,208\ns_add_u32 s68, s71, 16384\nQmvaa 88,132,176\ns_cbranch_vccz .Lst22\nbuffer_store_dwordx4 a[36:39], v79, s[56:59], s68 offen offset:2048 sc1\n"
".Lst22:\nQmvaa 92,136,208\nQmvaa 92,140,176\ns_waitcnt lgkmcnt(0)\nQmvaa 124,136,212\ns_waitcnt vmcnt(16)\ns_barrier\nQmvaa 124,140,180\n"
"Qta 144,61,49536\ns_add_u32 m0, s62, 33024\ns_add_u32 s69, s87, 1572864\nQmvaa 120,128,212\nbuffer_load_dwordx4 v62, s[52:55], s69 offen lds\nQmvaa 120,132,180\nQta 146,61,50048\nQmvaa 116,120,212\n"
"s_add_u32 s69, s87, 1637368\nQmvaa 116,124,180\nbuffer_load_dwordx4 v62, s[52:55], s69 offen offset:1032 lds\nQmvaa 112,112,212\nQta 148,61,53664\nQmvaa 112,116,180\ns_add_u32 s69, s87, 1701872\nQmvaa 144,112,216\n"
"buffer_load_dwordx4 v62, s[52:55], s69 offen offset:2064 lds\nQmvaa 144,116,184\nQta 150,61,54176\nQmvaa 148,120,216\ns_add_u32 s69, s87, 1766376\nQmvaa 148,124,184\nbuffer_load_dwordx4 v62, s[52:55], s69 offen offset:3096 lds\nQmvaa 152,128,216\n"
"Qta 152,61,49544\nQmvaa 152,132,184\ns_add_u32 m0, s63, 0\ns_add_u32 s88, s86, 384\nQmvaa 156,136,216\nbuffer_load_dwordx4 v64, s[48:51], s88 offen lds\nQmvaa 156,140,184\nQta 154,61,50056\n"
"Qmvaa 188,136,220\ns_add_u32 s88, s86, 19840\nQmvaa 188,140,188\nbuffer_load_dwordx4 v64, s[48:51], s88 offen offset:1024 lds\nQmvaa 184,128,220\nQta 156,61,53672\nQmvaa 184,132,188\ns_add_u32 s88, s86, 39296\n"
"Qmvaa 180,120,220\nbuffer_load_dwordx4 v64, s[48:51], s88 offen offset:2048 lds\nQmvaa 180,124,188\nQta 158,61,54184\nQmvaa 176,112,220\ns_add_u32 s88, s86, 58752\nQmvaa 176,116,188\nbuffer_load_dwordx4 v64, s[48:51], s88 offen offset:3072 lds\n"
"Qmvaa 208,112,224\nQta 160,61,49792\nQmvaa 208,116,192\ns_add_u32 m0, s63, 4096\ns_add_u32 s88, s86, 82304\nQmvaa 212,120,224\nbuffer_load_dwordx4 v64, s[48:51], s88 offen lds\nQmvaa 212,124,192\n"
"Qta 162,61,50304\nQmvaa 216,128,224\ns_add_u32 s88, s86, 101760\nQmvaa 216,132,192\nbuffer_load_dwordx4 v64, s[48:51], s88 offen offset:1024 lds\nQmvaa 220,136,224\nQta 164,61,53920\nQmvaa 220,140,192\n"
"s_add_u32 s88, s86, 121216\nQmvaa 252,136,228\nbuffer_load_dwordx4 v64, s[48:51], s88 offen offset:2048 lds\nQmvaa 252,140,196\nQta 166,61,54432\nQmvaa 248,128,228\ns_add_u32 m0, s61, 0\nQmvaa 248,132,196\n"
"s_cbranch_vccnz .Ld0q1\nbuffer_load_dwordx4 v69, s[40:43], s60 offen offset:1024 nt lds\n.Ld0q1:\nQmvaa 244,120,228\nQta 168,61,49800\nQmvaa 244,124,196\nQmvaa 240,112,228\nQta 170,61,50312\n"
"Qmvaa 240,116,196\nQmvaa 16,112,232\nQta 172,61,53928\nQmvaa 16,116,200\nQmvaa 20,120,232\nQta 174,61,54440\nQmvaa 20,124,200\nQmvaa 24,128,232\n"
"Qmvaa 24,132,200\nQmvaa 28,136,232\nQmvaa 28,140,200\ns_waitcnt lgkmcnt(0)\nQmvaa 96,144,208\nQmvaa 96,148,176\ns_add_u32 s68, s71, 0\nQmvaa 100,152,208\n"
"s_cbranch_vccz .Lst21\nbuffer_store_dwordx4 a[48:51], v79, s[56:59], s68 offen offset:3072 sc1\n.Lst21:\nQmvaa 100,156,176\nQmvaa 104,160,208\ns_add_u32 s68, s71, 16384\nQmvaa 104,164,176\ns_cbranch_vccz .Lst20\n"
"buffer_store_dwordx4 a[52:55], v79, s[56:59], s68 offen offset:3072 sc1\n.Lst20:\nQmvaa 108,168,208\nQmvaa 108,172,176\ns_waitcnt lgkmcnt(0)\nQmvaa 140,168,212\ns_waitcnt vmcnt(17)\ns_barrier\n"
"Qmvaa 140,172,180\nQda 208,63,33024\ns_add_u32 m0, s62, 49536\ns_add_u32 s69, s87, 1572992\nQmvaa 136,160,212\nbuffer_load_dwordx4 v62, s[52:55], s69 offen lds\nQmvaa 136,164,180\nQda 176,63,33536\n"
"Qmvaa 132,152,212\nQta 112,61,0\nQmvaa 132,156,180\ns_add_u32 s69, s87, 1637496\nQmvaa 128,144,212\nbuffer_load_dwordx4 v62, s[52:55], s69 offen offset:1032 lds\nQmvaa 128,148,180\nQda 212,63,34048\n"
"Qmvaa 160,144,216\nQda 180,63,34560\nQmvaa 160,148,184\nQta 114,61,512\ns_add_u32 s69, s87, 1702000\nQmvaa 164,152,216\nbuffer_load_dwordx4 v62, s[52:55], s69 offen offset:2064 lds\nQmvaa 164,156,184\n"
"Qta 116,61,4128\nQmvaa 168,160,216\nQmvaa 168,164,184\nQta 118,61,4640\ns_add_u32 s69, s87, 1766504\nQmvaa 172,168,216\nbuffer_load_dwordx4 v62, s[52:55], s69 offen offset:3096 lds\nQmvaa 172,172,184\n"
"Qda 216,63,35072\nQmvaa 204,168,220\nQda 184,63,35584\nQmvaa 204,172,188\nQta 120,61,8\nQmvaa 200,160,220\nQmvaa 200,164,188\nQta 122,61,520\n"
"Qmvaa 196,152,220\nQmvaa 196,156,188\nQta 124,61,4136\nQmvaa 192,144,220\nQda 220,63,36096\nQmvaa 192,148,188\nQda 188,63,36608\nQmvaa 224,144,224\n"
"Qta 126,61,4648\nQmvaa 224,148,192\nQmvaa 228,152,224\nQta 128,61,256\nQmvaa 228,156,192\nQmvaa 232,160,224\nQta 130,61,768\nQmvaa 232,164,192\n"
"Qmvaa 236,168,224\nQda 224,63,37120\nQmvaa 236,172,192\nQda 192,63,37632\nQmvaa 12,168,228\nQta 132,61,4384\nQmvaa 12,172,196\nQmvaa 8,160,228\n"
"Qta 134,61,4896\nQmvaa 8,164,196\nQmvaa 4,152,228\nQta 136,61,264\nQmvaa 4,156,196\nQmvaa 0,144,228\nQda 228,63,38144\nQmvaa 0,148,196\n"
"Qda 196,63,38656\nQmvaa 32,144,232\nQta 138,61,776\nQmvaa 32,148,200\nQmvaa 36,152,232\nQta 140,61,4392\nQmvaa 36,156,200\nQmvaa 40,160,232\n"
"Qta 142,61,4904\nQmvaa 40,164,200\nQmvaa 44,168,232\nQda 232,63,39168\nQmvaa 44,172,200\nQda 200,63,39680\ns_waitcnt lgkmcnt(2)\nQmvaa 80,112,208\n"
"Qmvaa 80,116,176\ns_add_u32 s68, s71, 4096\nQmvaa 84,120,208\ns_cbranch_vccz .Lst19\nbuffer_store_dwordx4 a[64:67], v79, s[56:59], s68 offen offset:0 sc1\n.Lst19:\nQmvaa 84,124,176\nQmvaa 88,128,208\n"
"s_add_u32 s68, s71, 20480\nQmvaa 88,132,176\ns_cbranch_vccz .Lst18\nbuffer_store_dwordx4 a[68:71], v79, s[56:59], s68 offen offset:0 sc1\n.Lst18:\nQmvaa 92,136,208\nQmvaa 92,140,176\ns_waitcnt lgkmcnt(0)\n"
"Qmvaa 124,136,212\ns_waitcnt vmcnt(17)\ns_barrier\nQmvaa 124,140,180\nQta 144,61,16512\ns_add_u32 m0, s62, 0\ns_add_u32 s69, s87, 2097152\nQmvaa 120,128,212\n"
"buffer_load_dwordx4 v62, s[52:55], s69 offen lds\nQmvaa 120,132,180\nQta 146,61,17024\nQmvaa 116,120,212\ns_add_u32 s69, s87, 2161656\nQmvaa 116,124,180\nbuffer_load_dwordx4 v62, s[52:55], s69 offen offset:1032 lds\nQmvaa 112,112,212\n"
"Qta 148,61,20640\nQmvaa 112,116,180\ns_add_u32 s69, s87, 2226160\nQmvaa 144,112,216\nbuffer_load_dwordx4 v62, s[52:55], s69 offen offset:2064 lds\nQmvaa 144,116,184\nQta 150,61,21152\nQmvaa 148,120,216\n"
"s_add_u32 s69, s87, 2290664\nQmvaa 148,124,184\nbuffer_load_dwordx4 v62, s[52:55], s69 offen offset:3096 lds\nQmvaa 152,128,216\nQta 152,61,16520\nQmvaa 152,132,184\ns_add_u32 m0, s63, 33024\ns_add_u32 s88, s86, 512\n"
"Qmvaa 156,136,216\nbuffer_load_dwordx4 v64, s[48:51], s88 offen lds\nQmvaa 156,140,184\nQta 154,61,17032\nQmvaa 188,136,220\ns_add_u32 s88, s86, 19968\nQmvaa 188,140,188\nbuffer_load_dwordx4 v64, s[48:51], s88 offen offset:1024 lds\n"
"Qmvaa 184,128,220\nQta 156,61,20648\nQmvaa 184,132,188\ns_add_u32 s88, s86, 39424\nQmvaa 180,120,220\nbuffer_load_dwordx4 v64, s[48:51], s88 offen offset:2048 lds\nQmvaa 180,124,188\nQta 158,61,21160\n"
"Qmvaa 176,112,220\ns_add_u32 s88, s86, 58880\nQmvaa 176,116,188\nbuffer_load_dwordx4 v64, s[48:51], s88 offen offset:3072 lds\nQmvaa 208,112,224\nQta 160,61,16768\nQmvaa 208,116,192\ns_add_u32 m0, s63, 37120\n"
"s_add_u32 s88, s86, 82432\nQmvaa 212,120,224\nbuffer_load_dwordx4 v64, s[48:51], s88 offen lds\nQmvaa 212,124,192\nQta 162,61,17280\nQmvaa 216,128,224\ns_add_u32 s88, s86, 101888\nQmvaa 216,132,192\n"
"buffer_load_dwordx4 v64, s[48:51], s88 offen offset:1024 lds\nQmvaa 220,136,224\nQta 164,61,20896\nQmvaa 220,140,192\ns_add_u32 s88, s86, 121344\nQmvaa 252,136,228\nbuffer_load_dwordx4 v64, s[48:51], s88 offen offset:2048 lds\nQmvaa 252,140,196\n"
"Qta 166,61,21408\nQmvaa 248,128,228\ns_add_u32 m0, s61, 0\nQmvaa 248,132,196\ns_cbranch_vccnz .Ld0q2\nbuffer_load_dwordx4 v69, s[40:43], s90 offen offset:2048 nt lds\n.Ld0q2:\nQmvaa 244,120,228\n"
"Qta 168,61,16776\nQmvaa 244,124,196\nQmvaa 240,112,228\nQta 170,61,17288\nQmvaa 240,116,196\nQmvaa 16,112,232\nQta 172,61,20904\nQmvaa 16,116,200\n"
"Qmvaa 20,120,232\nQta 174,61,21416\nQmvaa 20,124,200\nQmvaa 24,128,232\nQmvaa 24,132,200\nQmvaa 28,136,232\nQmvaa 28,140,200\ns_waitcnt lgkmcnt(0)\n"
"Qmvaa 96,144,208\nQmvaa 96,148,176\ns_add_u32 s68, s71, 4096\nQmvaa 100,152,208\ns_cbranch_vccz .Lst17\nbuffer_store_dwordx4 a[80:83], v79, s[56:59], s68 offen offset:1024 sc1\n.Lst17:\nQmvaa 100,156,176\n"
"Qmvaa 104,160,208\ns_add_u32 s68, s71, 20480\nQmvaa 104,164,176\ns_cbranch_vccz .Lst16\nbuffer_store_dwordx4 a[84:87], v79, s[56:59], s68 offen offset:1024 sc1\n.Lst16:\nQmvaa 108,168,208\nQmvaa 108,172,176\n"
"s_waitcnt lgkmcnt(0)\nQmvaa 140,168,212\ns_waitcnt vmcnt(17)\ns_barrier\nQmvaa 140,172,180\nQda 208,63,0\ns_add_u32 m0, s62, 16512\ns_add_u32 s69, s87, 2097280\n"
"Qmvaa 136,160,212\nbuffer_load_dwordx4 v62, s[52:55], s69 offen lds\nQmvaa 136,164,180\nQda 176,63,512\nQmvaa 132,152,212\nQta 112,61,33024\nQmvaa 132,156,180\ns_add_u32 s69, s87, 2161784\n"
"Qmvaa 128,144,212\nbuffer_load_dwordx4 v62, s[52:55], s69 offen offset:1032 lds\nQmvaa 128,148,180\nQda 212,63,1024\nQmvaa 160,144,216\nQda 180,63,1536\nQmvaa 160,148,184\nQta 114,61,33536\n"
"s_add_u32 s69, s87, 2226288\nQmvaa 164,152,216\nbuffer_load_dwordx4 v62, s[52:55], s69 offen offset:2064 lds\nQmvaa 164,156,184\nQta 116,61,37152\nQmvaa 168,160,216\nQmvaa 168,164,184\nQta 118,61,37664\n"
"s_add_u32 s69, s87, 2290792\nQmvaa 172,168,216\nbuffer_load_dwordx4 v62, s[52:55], s69 offen offset:3096 lds\nQmvaa 172,172,184\nQda 216,63,2048\nQmvaa 204,168,220\nQda 184,63,2560\nQmvaa 204,172,188\n"
"Qta 120,61,33032\ns_add_u32 m0, s61, 0\nQmvaa 200,160,220\ns_cbranch_vccnz .Ld0q3\nbuffer_load_dwordx4 v69, s[40:43], s90 offen offset:3072 nt lds\n.Ld0q3:\nQmvaa 200,164,188\nQta 122,61,33544\n"
"Qmvaa 196,152,220\nQmvaa 196,156,188\nQta 124,61,37160\nQmvaa 192,144,220\nQda 220,63,3072\nQmvaa 192,148,188\nQda 188,63,3584\nQmvaa 224,144,224\n"
"Qta 126,61,37672\nQmvaa 224,148,192\nQmvaa 228,152,224\nQta 128,61,33280\nQmvaa 228,156,192\nQmvaa 232,160,224\nQta 130,61,33792\nQmvaa 232,164,192\n"
"Qmvaa 236,168,224\nQda 224,63,4096\nQmvaa 236,172,192\n.Lpg1:\ns_cbranch_execz .Ltr0\nQda 192,63,4608\nQmvaa 12,168,228\nQta 132,61,37408\n"
"Qmvaa 12,172,196\nQmvaa 8,160,228\nQta 134,61,37920\nQmvaa 8,164,196\nQmvaa 4,152,228\nQta 136,61,33288\nQmvaa 4,156,196\nQmvaa 0,144,228\n"
"Qda 228,63,5120\nQmvaa 0,148,196\nQda 196,63,5632\nQmvaa 32,144,232\nQta 138,61,33800\nQmvaa 32,148,200\nQmvaa 36,152,232\nQta 140,61,37416\n"
"Qmvaa 36,156,200\nQmvaa 40,160,232\nQta 142,61,37928\nQmvaa 40,164,200\nQmvaa 44,168,232\nQda 232,63,6144\nQmvaa 44,172,200\nQda 200,63,6656\n"
"s_waitcnt lgkmcnt(2)\nQmvaa 80,112,208\nQmvaa 80,116,176\ns_add_u32 s68, s71, 135168\nQmvaa 84,120,208\ns_cbranch_vccz .Lst15\nbuffer_store_dwordx4 a[88:91], v79, s[56:59], s68 offen offset:1024 sc1\n.Lst15:\n"
"Qmvaa 84,124,176\nQmvaa 88,128,208\ns_add_u32 s68, s71, 151552\nQmvaa 88,132,176\ns_cbranch_vccz .Lst14\nbuffer_store_dwordx4 a[92:95], v79, s[56:59], s68 offen offset:1024 sc1\n.Lst14:\nQmvaa 92,136,208\n"
"Qmvaa 92,140,176\ns_waitcnt lgkmcnt(0)\nQmvaa 124,136,212\ns_waitcnt vmcnt(17)\ns_barrier\nQmvaa 124,140,180\nQta 144,61,49536\ns_add_u32 m0, s62, 33024\n"
"s_add_u32 s69, s87, 2621440\nQmvaa 120,128,212\nbuffer_load_dwordx4 v62, s[52:55], s69 offen lds\nQmvaa 120,132,180\nQta 146,61,50048\ns_mov_b32 s89, 0\nQmvaa 116,120,212\ns_cbranch_vccnz .Lpb0\n"
"s_load_dword s89, s[30:31], 0x0\n.Lpb0:\nQmvaa 116,124,180\nQta 148,61,53664\ns_add_u32 s69, s87, 2685944\nQmvaa 112,112,212\nbuffer_load_dwordx4 v62, s[52:55], s69 offen offset:1032 lds\nQmvaa 112,116,180\n"
"Qta 150,61,54176\nQmvaa 144,112,216\ns_add_u32 s77, s77, s89\nQmvaa 144,116,184\nQta 152,61,49544\ns_add_u32 s69, s87, 2750448\nQmvaa 148,120,216\nbuffer_load_dwordx4 v62, s[52:55], s69 offen offset:2064 lds\n"
"Qmvaa 148,124,184\nQta 154,61,50056\nQmvaa 152,128,216\ns_add_u32 s77, s77, s89\nQmvaa 152,132,184\nQta 156,61,53672\ns_add_u32 s69, s87, 2814952\nQmvaa 156,136,216\n"
"buffer_load_dwordx4 v62, s[52:55], s69 offen offset:3096 lds\nQmvaa 156,140,184\nQta 158,61,54184\nQmvaa 188,136,220\ns_add_u32 s77, s77, s89\nQmvaa 188,140,188\nQta 160,61,49792\ns_add_u32 m0, s63, 0\n"
"s_add_u32 s88, s86, 640\nQmvaa 184,128,220\nbuffer_load_dwordx4 v64, s[48:51], s88 offen lds\nQmvaa 184,132,188\nQta 162,61,50304\nQmvaa 180,120,220\ns_add_u32 s77, s77, s89\nQmvaa 180,124,188\n"
"Qta 164,61,53920\ns_add_u32 s88, s86, 20096\nQmvaa 176,112,220\nbuffer_load_dwordx4 v64, s[48:51], s88 offen offset:1024 lds\nQmvaa 176,116,188\nQta 166,61,54432\nQmvaa 208,112,224\ns_add_u32 s77, s77, s89\n"
"Qmvaa 208,116,192\nQta 168,61,49800\ns_add_u32 s88, s86, 39552\nQmvaa 212,120,224\nbuffer_load_dwordx4 v64, s[48:51], s88 offen offset:2048 lds\nQmvaa 212,124,192\nQta 170,61,50312\nQmvaa 216,128,224\n"
"s_add_u32 s77, s77, s89\nQmvaa 216,132,192\nQta 172,61,53928\ns_add_u32 s88, s86, 59008\nQmvaa 220,136,224\nbuffer_load_dwordx4 v64, s[48:51], s88 offen offset:3072 lds\nQmvaa 220,140,192\nQta 174,61,54440\n"
"Qmvaa 252,136,228\ns_add_u32 s77, s77, s89\nQmvaa 252,140,196\ns_add_u32 m0, s63, 4096\ns_add_u32 s88, s86, 82560\nQmvaa 248,128,228\nbuffer_load_dwordx4 v64, s[48:51], s88 offen lds\nQmvaa 248,132,196\n"
"Qmvaa 244,120,228\ns_add_u32 s77, s77, s89\nQmvaa 244,124,196\ns_add_u32 s88, s86, 102016\nQmvaa 240,112,228\nbuffer_load_dwordx4 v64, s[48:51], s88 offen offset:1024 lds\nQmvaa 240,116,196\nQmvaa 16,112,232\n"
"s_add_u32 s77, s77, s89\nQmvaa 16,116,200\ns_add_u32 s88, s86, 121472\nQmvaa 20,120,232\nbuffer_load_dwordx4 v64, s[48:51], s88 offen offset:2048 lds\nQmvaa 20,124,200\nQmvaa 24,128,232\ns_add_u32 s77, s77, s89\n"
"Qmvaa 24,132,200\nQmvaa 28,136,232\nQmvaa 28,140,200\ns_waitcnt lgkmcnt(0)\nQmvaa 96,144,208\nQmvaa 96,148,176\ns_add_u32 s68, s71, 4096\nQmvaa 100,152,208\n"
"s_cbranch_vccz .Lst13\nbuffer_store_dwordx4 a[96:99], v79, s[56:59], s68 offen offset:2048 sc1\n.Lst13:\nQmvaa 100,156,176\nQmvaa 104,160,208\ns_add_u32 s68, s71, 20480\nQmvaa 104,164,176\ns_cbranch_vccz .Lst12\n"
"buffer_store_dwordx4 a[100:103], v79, s[56:59], s68 offen offset:2048 sc1\n.Lst12:\nQmvaa 108,168,208\nQmvaa 108,172,176\ns_waitcnt lgkmcnt(0)\nQmvaa 140,168,212\ns_waitcnt vmcnt(17)\ns_barrier\n"
"Qmvaa 140,172,180\nQda 208,63,33024\ns_add_u32 m0, s62, 49536\ns_add_u32 s69, s87, 2621568\nQmvaa 136,160,212\nbuffer_load_dwordx4 v62, s[52:55], s69 offen lds\nQmvaa 136,164,180\nQda 176,63,33536\n"
"Qmvaa 132,152,212\nQta 112,61,0\nQmvaa 132,156,180\ns_add_u32 s69, s87, 2686072\nQmvaa 128,144,212\nbuffer_load_dwordx4 v62, s[52:55], s69 offen offset:1032 lds\nQmvaa 128,148,180\nQda 212,63,34048\n"
"Qmvaa 160,144,216\nQda 180,63,34560\nQmvaa 160,148,184\nQta 114,61,512\ns_add_u32 s69, s87, 2750576\nQmvaa 164,152,216\nbuffer_load_dwordx4 v62, s[52:55], s69 offen offset:2064 lds\nQmvaa 164,156,184\n"
"Qta 116,61,4128\nQmvaa 168,160,216\nQmvaa 168,164,184\nQta 118,61,4640\ns_add_u32 s69, s87, 2815080\nQmvaa 172,168,216\nbuffer_load_dwordx4 v62, s[52:55], s69 offen offset:3096 lds\nQmvaa 172,172,184\n"
"Qda 216,63,35072\nQmvaa 204,168,220\nQda 184,63,35584\nQmvaa 204,172,188\nQta 120,61,8\ns_add_u32 m0, s64, 0\nQmvaa 200,160,220\ns_cbranch_vccnz .Ld0q4\n"
"buffer_load_dwordx4 v72, s[44:47], s60 offen nt lds\n.Ld0q4:\nQmvaa 200,164,188\nQta 122,61,520\nQmvaa 196,152,220\nQmvaa 196,156,188\nQta 124,61,4136\nQmvaa 192,144,220\n"
"Qda 220,63,36096\nQmvaa 192,148,188\nQda 188,63,36608\nQmvaa 224,144,224\nQta 126,61,4648\nQmvaa 224,148,192\nQmvaa 228,152,224\nQta 128,61,256\n"
"Qmvaa 228,156,192\nQmvaa 232,160,224\nQta 130,61,768\nQmvaa 232,164,192\nQmvaa 236,168,224\nQda 224,63,37120\nQmvaa 236,172,192\nQda 192,63,37632\n"
"Qmvaa 12,168,228\nQta 132,61,4384\nQmvaa 12,172,196\nQmvaa 8,160,228\nQta 134,61,4896\nQmvaa 8,164,196\nQmvaa 4,152,228\nQta 136,61,264\n"
"Qmvaa 4,156,196\nQmvaa 0,144,228\nQda 228,63,38144\nQmvaa 0,148,196\nQda 196,63,38656\nQmvaa 32,144,232\nQta 138,61,776\nQmvaa 32,148,200\n"
"Qmvaa 36,152,232\nQta 140,61,4392\nQmvaa 36,156,200\nQmvaa 40,160,232\nQta 142,61,4904\nQmvaa 40,164,200\nQmvaa 44,168,232\nQda 232,63,39168\n"
"Qmvaa 44,172,200\nQda 200,63,39680\ns_waitcnt lgkmcnt(2)\nQmvaa 80,112,208\nQmvaa 80,116,176\ns_add_u32 s68, s71, 131072\nQmvaa 84,120,208\ns_cbranch_vccz .Lst11\n"
"buffer_store_dwordx4 a[8:11], v79, s[56:59], s68 offen offset:0 sc1\n.Lst11:\nQmvaa 84,124,176\nQmvaa 88,128,208\ns_add_u32 s68, s71, 147456\nQmvaa 88,132,176\ns_cbranch_vccz .Lst10\nbuffer_store_dwordx4 a[12:15], v79, s[56:59], s68 offen offset:0 sc1\n"
".Lst10:\nQmvaa 92,136,208\nQmvaa 92,140,176\ns_waitcnt lgkmcnt(0)\nQmvaa 124,136,212\ns_waitcnt vmcnt(17)\ns_barrier\nQmvaa 124,140,180\n"
"Qta 144,61,16512\ns_add_u32 m0, s62, 0\ns_add_u32 s69, s87, 3145728\nQmvaa 120,128,212\nbuffer_load_dwordx4 v62, s[52:55], s69 offen lds\nQmvaa 120,132,180\nQta 146,61,17024\ns_mov_b32 s89, 0\n"
"Qmvaa 116,120,212\ns_cbranch_vccnz .Lpb1\ns_load_dword s89, s[30:31], 0x2000\n.Lpb1:\nQmvaa 116,124,180\nQta 148,61,20640\ns_add_u32 s69, s87, 3210232\nQmvaa 112,112,212\n"
"buffer_load_dwordx4 v62, s[52:55], s69 offen offset:1032 lds\nQmvaa 112,116,180\nQta 150,61,21152\nQmvaa 144,112,216\ns_add_u32 s77, s77, s89\nQmvaa 144,116,184\nQta 152,61,16520\ns_add_u32 s69, s87, 3274736\n"
"Qmvaa 148,120,216\nbuffer_load_dwordx4 v62, s[52:55], s69 offen offset:2064 lds\nQmvaa 148,124,184\nQta 154,61,17032\nQmvaa 152,128,216\ns_add_u32 s77, s77, s89\nQmvaa 152,132,184\nQta 156,61,20648\n"
"s_add_u32 s69, s87, 3339240\nQmvaa 156,136,216\nbuffer_load_dwordx4 v62, s[52:55], s69 offen offset:3096 lds\nQmvaa 156,140,184\nQta 158,61,21160\nQmvaa 188,136,220\ns_add_u32 s77, s77, s89\nQmvaa 188,140,188\n"
"Qta 160,61,16768\ns_add_u32 m0, s63, 33024\ns_add_u32 s88, s86, 768\nQmvaa 184,128,220\nbuffer_load_dwordx4 v64, s[48:51], s88 offen lds\nQmvaa 184,132,188\nQta 162,61,17280\nQmvaa 180,120,220\n"
"s_add_u32 s77, s77, s89\nQmvaa 180,124,188\nQta 164,61,20896\ns_add_u32 s88, s86, 20224\nQmvaa 176,112,220\nbuffer_load_dwordx4 v64, s[48:51], s88 offen offset:1024 lds\nQmvaa 176,116,188\nQta 166,61,21408\n"
"Qmvaa 208,112,224\ns_add_u32 s77, s77, s89\nQmvaa 208,116,192\nQta 168,61,16776\ns_add_u32 s88, s86, 39680\nQmvaa 212,120,224\nbuffer_load_dwordx4 v64, s[48:51], s88 offen offset:2048 lds\nQmvaa 212,124,192\n"
"Qta 170,61,17288\nQmvaa 216,128,224\ns_add_u32 s77, s77, s89\nQmvaa 216,132,192\nQta 172,61,20904\ns_add_u32 s88, s86, 59136\nQmvaa 220,136,224\nbuffer_load_dwordx4 v64, s[48:51], s88 offen offset:3072 lds\n"
"Qmvaa 220,140,192\nQta 174,61,21416\nQmvaa 252,136,228\ns_add_u32 s77, s77, s89\nQmvaa 252,140,196\ns_add_u32 m0, s63, 37120\ns_add_u32 s88, s86, 82688\nQmvaa 248,128,228\n"
"buffer_load_dwordx4 v64, s[48:51], s88 offen lds\nQmvaa 248,132,196\nQmvaa 244,120,228\ns_add_u32 s77, s77, s89\nQmvaa 244,124,196\ns_add_u32 s88, s86, 102144\nQmvaa 240,112,228\nbuffer_load_dwordx4 v64, s[48:51], s88 offen offset:1024 lds\n"
"Qmvaa 240,116,196\nQmvaa 16,112,232\ns_add_u32 s77, s77, s89\nQmvaa 16,116,200\ns_add_u32 s88, s86, 121600\nQmvaa 20,120,232\nbuffer_load_dwordx4 v64, s[48:51], s88 offen offset:2048 lds\nQmvaa 20,124,200\n"
"Qmvaa 24,128,232\ns_add_u32 s77, s77, s89\nQmvaa 24,132,200\nQmvaa 28,136,232\nQmvaa 28,140,200\ns_waitcnt lgkmcnt(0)\nQmvaa 96,144,208\nQmvaa 96,148,176\n"
"s_add_u32 s68, s71, 131072\nQmvaa 100,152,208\ns_cbranch_vccz .Lst9\nbuffer_store_dwordx4 a[24:27], v79, s[56:59], s68 offen offset:1024 sc1\n.Lst9:\nQmvaa 100,156,176\nQmvaa 104,160,208\ns_add_u32 s68, s71, 147456\n"
"Qmvaa 104,164,176\ns_cbranch_vccz .Lst8\nbuffer_store_dwordx4 a[28:31], v79, s[56:59], s68 offen offset:1024 sc1\n.Lst8:\nQmvaa 108,168,208\nQmvaa 108,172,176\ns_waitcnt lgkmcnt(0)\nQmvaa 140,168,212\n"
"s_waitcnt vmcnt(16)\ns_barrier\nQmvaa 140,172,180\nQda 208,63,0\ns_add_u32 m0, s62, 16512\ns_add_u32 s69, s87, 3145856\nQmvaa 136,160,212\nbuffer_load_dwordx4 v62, s[52:55], s69 offen lds\n"
"Qmvaa 136,164,180\nQda 176,63,512\nQmvaa 132,152,212\nQta 112,61,33024\nQmvaa 132,156,180\ns_add_u32 s69, s87, 3210360\nQmvaa 128,144,212\nbuffer_load_dwordx4 v62, s[52:55], s69 offen offset:1032 lds\n"
"Qmvaa 128,148,180\nQda 212,63,1024\nQmvaa 160,144,216\nQda 180,63,1536\nQmvaa 160,148,184\nQta 114,61,33536\ns_add_u32 s69, s87, 3274864\nQmvaa 164,152,216\n"
"buffer_load_dwordx4 v62, s[52:55], s69 offen offset:2064 lds\nQmvaa 164,156,184\nQta 116,61,37152\nQmvaa 168,160,216\nQmvaa 168,164,184\nQta 118,61,37664\ns_add_u32 s69, s87, 3339368\nQmvaa 172,168,216\n"
"buffer_load_dwordx4 v62, s[52:55], s69 offen offset:3096 lds\nQmvaa 172,172,184\nQda 216,63,2048\nQmvaa 204,168,220\nQda 184,63,2560\nQmvaa 204,172,188\nQta 120,61,33032\ns_add_u32 m0, s64, 0\n"
"Qmvaa 200,160,220\ns_cbranch_vccnz .Ld0q5\nbuffer_load_dwordx4 v72, s[44:47], s60 offen offset:1024 nt lds\n.Ld0q5:\nQmvaa 200,164,188\nQta 122,61,33544\nQmvaa 196,152,220\nQmvaa 196,156,188\n"
"Qta 124,61,37160\nQmvaa 192,144,220\nQda 220,63,3072\nQmvaa 192,148,188\nQda 188,63,3584\nQmvaa 224,144,224\nQta 126,61,37672\nQmvaa 224,148,192\n"
"Qmvaa 228,152,224\nQta 128,61,33280\nQmvaa 228,156,192\nQmvaa 232,160,224\nQta 130,61,33792\nQmvaa 232,164,192\nQmvaa 236,168,224\nQda 224,63,4096\n"
"Qmvaa 236,172,192\nQda 192,63,4608\nQmvaa 12,168,228\nQta 132,61,37408\nQmvaa 12,172,196\nQmvaa 8,160,228\nQta 134,61,37920\nQmvaa 8,164,196\n"
"Qmvaa 4,152,228\nQta 136,61,33288\nQmvaa 4,156,196\nQmvaa 0,144,228\nQda 228,63,5120\nQmvaa 0,148,196\nQda 196,63,5632\nQmvaa 32,144,232\n"
"Qta 138,61,33800\nQmvaa 32,148,200\nQmvaa 36,152,232\nQta 140,61,37416\nQmvaa 36,156,200\nQmvaa 40,160,232\nQta 142,61,37928\nQmvaa 40,164,200\n"
"Qmvaa 44,168,232\nQda 232,63,6144\nQmvaa 44,172,200\nQda 200,63,6656\ns_waitcnt lgkmcnt(2)\nQmvaa 80,112,208\nQmvaa 80,116,176\ns_add_u32 s68, s71, 131072\n"
"Qmvaa 84,120,208\ns_cbranch_vccz .Lst7\nbuffer_store_dwordx4 a[40:43], v79, s[56:59], s68 offen offset:2048 sc1\n.Lst7:\nQmvaa 84,124,176\nQmvaa 88,128,208\ns_add_u32 s68, s71, 147456\nQmvaa 88,132,176\n"
"s_cbranch_vccz .Lst6\nbuffer_store_dwordx4 a[44:47], v79, s[56:59], s68 offen offset:2048 sc1\n.Lst6:\nQmvaa 92,136,208\nQmvaa 92,140,176\ns_waitcnt lgkmcnt(0)\nQmvaa 124,136,212\ns_waitcnt vmcnt(17)\n"
"s_barrier\nQmvaa 124,140,180\nQta 144,61,49536\ns_add_u32 m0, s62, 33024\ns_add_u32 s69, s87, 3670016\nQmvaa 120,128,212\nbuffer_load_dwordx4 v62, s[52:55], s69 offen lds\nQmvaa 120,132,180\n"
"Qta 146,61,50048\ns_mov_b32 s89, 0\nQmvaa 116,120,212\ns_cbranch_vccnz .Lpb2\ns_load_dword s89, s[30:31], 0x6000\n.Lpb2:\nQmvaa 116,124,180\nQta 148,61,53664\n"
"s_add_u32 s69, s87, 3734520\nQmvaa 112,112,212\nbuffer_load_dwordx4 v62, s[52:55], s69 offen offset:1032 lds\nQmvaa 112,116,180\nQta 150,61,54176\nQmvaa 144,112,216\ns_add_u32 s77, s77, s89\nQmvaa 144,116,184\n"
"Qta 152,61,49544\ns_add_u32 s69, s87, 3799024\nQmvaa 148,120,216\nbuffer_load_dwordx4 v62, s[52:55], s69 offen offset:2064 lds\nQmvaa 148,124,184\nQta 154,61,50056\nQmvaa 152,128,216\ns_add_u32 s77, s77, s89\n"
"Qmvaa 152,132,184\nQta 156,61,53672\ns_add_u32 s69, s87, 3863528\nQmvaa 156,136,216\nbuffer_load_dwordx4 v62, s[52:55], s69 offen offset:3096 lds\nQmvaa 156,140,184\nQta 158,61,54184\nQmvaa 188,136,220\n"
"s_add_u32 s77, s77, s89\nQmvaa 188,140,188\nQta 160,61,49792\ns_add_u32 m0, s63, 0\n.Lpg2:\ns_cbranch_execz .Ltr0\ns_add_u32 s88, s86, 896\nQmvaa 184,128,220\n"
"buffer_load_dwordx4 v64, s[48:51], s88 offen lds\nQmvaa 184,132,188\nQta 162,61,50304\nQmvaa 180,120,220\ns_add_u32 s77, s77, s89\nQmvaa 180,124,188\nQta 164,61,53920\ns_add_u32 s88, s86, 20352\n"
"Qmvaa 176,112,220\nbuffer_load_dwordx4 v64, s[48:51], s88 offen offset:1024 lds\nQmvaa 176,116,188\nQta 166,61,54432\nQmvaa 208,112,224\ns_add_u32 s77, s77, s89\nQmvaa 208,116,192\nQta 168,61,49800\n"
"s_add_u32 s88, s86, 39808\nQmvaa 212,120,224\nbuffer_load_dwordx4 v64, s[48:51], s88 offen offset:2048 lds\nQmvaa 212,124,192\nQta 170,61,50312\nQmvaa 216,128,224\ns_add_u32 s77, s77, s89\nQmvaa 216,132,192\n"
"Qta 172,61,53928\ns_add_u32 s88, s86, 59264\nQmvaa 220,136,224\nbuffer_load_dwordx4 v64, s[48:51], s88 offen offset:3072 lds\nQmvaa 220,140,192\nQta 174,61,54440\nQmvaa 252,136,228\ns_add_u32 s77, s77, s89\n"
"Qmvaa 252,140,196\ns_add_u32 m0, s63, 4096\ns_add_u32 s88, s86, 82816\nQmvaa 248,128,228\nbuffer_load_dwordx4 v64, s[48:51], s88 offen lds\nQmvaa 248,132,196\nQmvaa 244,120,228\ns_add_u32 s77, s77, s89\n"
"Qmvaa 244,124,196\ns_add_u32 s88, s86, 102272\nQmvaa 240,112,228\nbuffer_load_dwordx4 v64, s[48:51], s88 offen offset:1024 lds\nQmvaa 240,116,196\nQmvaa 16,112,232\ns_add_u32 s77, s77, s89\nQmvaa 16,116,200\n"
"s_add_u32 s88, s86, 121728\nQmvaa 20,120,232\nbuffer_load_dwordx4 v64, s[48:51], s88 offen offset:2048 lds\nQmvaa 20,124,200\nQmvaa 24,128,232\ns_add_u32 s77, s77, s89\nQmvaa 24,132,200\nQmvaa 28,136,232\n"
"Qmvaa 28,140,200\ns_waitcnt lgkmcnt(0)\nQmvaa 96,144,208\nQmvaa 96,148,176\ns_add_u32 s68, s71, 131072\nQmvaa 100,152,208\ns_cbranch_vccz .Lst5\nbuffer_store_dwordx4 a[56:59], v79, s[56:59], s68 offen offset:3072 sc1\n"
".Lst5:\nQmvaa 100,156,176\nQmvaa 104,160,208\ns_add_u32 s68, s71, 147456\nQmvaa 104,164,176\ns_cbranch_vccz .Lst4\nbuffer_store_dwordx4 a[60:63], v79, s[56:59], s68 offen offset:3072 sc1\n.Lst4:\n"
"Qmvaa 108,168,208\nQmvaa 108,172,176\ns_waitcnt lgkmcnt(0)\nQmvaa 140,168,212\ns_waitcnt vmcnt(16)\ns_barrier\nQmvaa 140,172,180\nQda 208,63,33024\n"
"s_add_u32 m0, s62, 49536\ns_add_u32 s69, s87, 3670144\nQmvaa 136,160,212\nbuffer_load_dwordx4 v62, s[52:55], s69 offen lds\nQmvaa 136,164,180\nQda 176,63,33536\nQmvaa 132,152,212\nQta 112,61,0\n"
"Qmvaa 132,156,180\ns_add_u32 s69, s87, 3734648\nQmvaa 128,144,212\nbuffer_load_dwordx4 v62, s[52:55], s69 offen offset:1032 lds\nQmvaa 128,148,180\nQda 212,63,34048\nQmvaa 160,144,216\nQda 180,63,34560\n"
"Qmvaa 160,148,184\nQta 114,61,512\ns_add_u32 s69, s87, 3799152\nQmvaa 164,152,216\nbuffer_load_dwordx4 v62, s[52:55], s69 offen offset:2064 lds\nQmvaa 164,156,184\nQta 116,61,4128\nQmvaa 168,160,216\n"
"Qmvaa 168,164,184\nQta 118,61,4640\ns_add_u32 s69, s87, 3863656\nQmvaa 172,168,216\nbuffer_load_dwordx4 v62, s[52:55], s69 offen offset:3096 lds\nQmvaa 172,172,184\nQda 216,63,35072\nQmvaa 204,168,220\n"
"Qda 184,63,35584\nQmvaa 204,172,188\nQta 120,61,8\ns_add_u32 m0, s64, 0\nQmvaa 200,160,220\ns_cbranch_vccnz .Ld0q6\nbuffer_load_dwordx4 v72, s[44:47], s90 offen offset:2048 nt lds\n.Ld0q6:\n"
"Qmvaa 200,164,188\nQta 122,61,520\nQmvaa 196,152,220\nQmvaa 196,156,188\nQta 124,61,4136\nQmvaa 192,144,220\nQda 220,63,36096\nQmvaa 192,148,188\n"
"Qda 188,63,36608\nQmvaa 224,144,224\nQta 126,61,4648\nQmvaa 224,148,192\nQmvaa 228,152,224\nQta 128,61,256\nQmvaa 228,156,192\nQmvaa 232,160,224\n"
"Qta 130,61,768\nQmvaa 232,164,192\nQmvaa 236,168,224\nQda 224,63,37120\nQmvaa 236,172,192\nQda 192,63,37632\nQmvaa 12,168,228\nQta 132,61,4384\n"
"Qmvaa 12,172,196\nQmvaa 8,160,228\nQta 134,61,4896\nQmvaa 8,164,196\nQmvaa 4,152,228\nQta 136,61,264\nQmvaa 4,156,196\nQmvaa 0,144,228\n"
"Qda 228,63,38144\nQmvaa 0,148,196\nQda 196,63,38656\nQmvaa 32,144,232\nQta 138,61,776\nQmvaa 32,148,200\nQmvaa 36,152,232\nQta 140,61,4392\n"
"Qmvaa 36,156,200\nQmvaa 40,160,232\nQta 142,61,4904\nQmvaa 40,164,200\nQmvaa 44,168,232\nQda 232,63,39168\nQmvaa 44,172,200\nQda 200,63,39680\n"
"s_waitcnt lgkmcnt(2)\nQmvaa 80,112,208\nQmvaa 80,116,176\ns_add_u32 s68, s71, 135168\nQmvaa 84,120,208\ns_cbranch_vccz .Lst3\nbuffer_store_dwordx4 a[72:75], v79, s[56:59], s68 offen offset:0 sc1\n.Lst3:\n"
"Qmvaa 84,124,176\nQmvaa 88,128,208\ns_add_u32 s68, s71, 151552\nQmvaa 88,132,176\ns_cbranch_vccz .Lst2\nbuffer_store_dwordx4 a[76:79], v79, s[56:59], s68 offen offset:0 sc1\n.Lst2:\nQmvaa 92,136,208\n"
"Qmvaa 92,140,176\ns_waitcnt lgkmcnt(0)\nQmvaa 124,136,212\ns_waitcnt vmcnt(17)\ns_barrier\nQmvaa 124,140,180\nQta 144,61,16512\ns_add_u32 m0, s62, 0\n"
"s_add_u32 s69, s87, 4194304\nQmvaa 120,128,212\nbuffer_load_dwordx4 v62, s[52:55], s69 offen lds\nQmvaa 120,132,180\nQta 146,61,17024\ns_mov_b32 s89, 0\nQmvaa 116,120,212\ns_cbranch_vccnz .Lpb3\n"
"s_load_dword s89, s[30:31], 0x4000\n.Lpb3:\nQmvaa 116,124,180\nQta 148,61,20640\ns_add_u32 s69, s87, 4258808\nQmvaa 112,112,212\nbuffer_load_dwordx4 v62, s[52:55], s69 offen offset:1032 lds\nQmvaa 112,116,180\n"
"Qta 150,61,21152\nQmvaa 144,112,216\ns_add_u32 s77, s77, s89\nQmvaa 144,116,184\nQta 152,61,16520\ns_add_u32 s69, s87, 4323312\nQmvaa 148,120,216\nbuffer_load_dwordx4 v62, s[52:55], s69 offen offset:2064 lds\n"
"Qmvaa 148,124,184\nQta 154,61,17032\nQmvaa 152,128,216\ns_add_u32 s77, s77, s89\nQmvaa 152,132,184\nQta 156,61,20648\ns_add_u32 s69, s87, 4387816\nQmvaa 156,136,216\n"
"buffer_load_dwordx4 v62, s[52:55], s69 offen offset:3096 lds\nQmvaa 156,140,184\nQta 158,61,21160\nQmvaa 188,136,220\ns_add_u32 s77, s77, s89\nQmvaa 188,140,188\nQta 160,61,16768\ns_add_u32 m0, s63, 33024\n"
"s_add_u32 s88, s86, 1024\nQmvaa 184,128,220\nbuffer_load_dwordx4 v64, s[48:51], s88 offen lds\nQmvaa 184,132,188\nQta 162,61,17280\nQmvaa 180,120,220\ns_add_u32 s77, s77, s89\nQmvaa 180,124,188\n"
"Qta 164,61,20896\ns_add_u32 s88, s86, 20480\nQmvaa 176,112,220\nbuffer_load_dwordx4 v64, s[48:51], s88 offen offset:1024 lds\nQmvaa 176,116,188\nQta 166,61,21408\nQmvaa 208,112,224\ns_add_u32 s77, s77, s89\n"
"Qmvaa 208,116,192\nQta 168,61,16776\ns_add_u32 s88, s86, 39936\nQmvaa 212,120,224\nbuffer_load_dwordx4 v64, s[48:51], s88 offen offset:2048 lds\nQmvaa 212,124,192\nQta 170,61,17288\nQmvaa 216,128,224\n"
"s_add_u32 s77, s77, s89\nQmvaa 216,132,192\nQta 172,61,20904\ns_add_u32 s88, s86, 59392\nQmvaa 220,136,224\nbuffer_load_dwordx4 v64, s[48:51], s88 offen offset:3072 lds\nQmvaa 220,140,192\nQta 174,61,21416\n"
"Qmvaa 252,136,228\ns_add_u32 s77, s77, s89\nQmvaa 252,140,196\ns_add_u32 m0, s63, 37120\ns_add_u32 s88, s86, 82944\nQmvaa 248,128,228\nbuffer_load_dwordx4 v64, s[48:51], s88 offen lds\nQmvaa 248,132,196\n"
"Qmvaa 244,120,228\ns_add_u32 s77, s77, s89\nQmvaa 244,124,196\ns_add_u32 s88, s86, 102400\nQmvaa 240,112,228\nbuffer_load_dwordx4 v64, s[48:51], s88 offen offset:1024 lds\nQmvaa 240,116,196\nQmvaa 16,112,232\n"
"s_add_u32 s77, s77, s89\nQmvaa 16,116,200\ns_add_u32 s88, s86, 121856\nQmvaa 20,120,232\nbuffer_load_dwordx4 v64, s[48:51], s88 offen offset:2048 lds\nQmvaa 20,124,200\nQmvaa 24,128,232\ns_add_u32 s77, s77, s89\n"
"Qmvaa 24,132,200\nQmvaa 28,136,232\nQmvaa 28,140,200\ns_waitcnt lgkmcnt(0)\nQmvaa 96,144,208\nQmvaa 96,148,176\ns_add_u32 s68, s71, 135168\nQmvaa 100,152,208\n"
"s_cbranch_vccz .Lst1\nbuffer_store_dwordx4 a[104:107], v79, s[56:59], s68 offen offset:2048 sc1\n.Lst1:\nQmvaa 100,156,176\nQmvaa 104,160,208\ns_add_u32 s68, s71, 151552\nQmvaa 104,164,176\ns_cbranch_vccz .Lst0\n"
"buffer_store_dwordx4 a[108:111], v79, s[56:59], s68 offen offset:2048 sc1\n.Lst0:\nQmvaa 108,168,208\nQmvaa 108,172,176\ns_waitcnt lgkmcnt(0)\nQmvaa 140,168,212\ns_waitcnt vmcnt(16)\ns_barrier\n"
"Qmvaa 140,172,180\nQda 208,63,0\ns_add_u32 m0, s62, 16512\ns_add_u32 s69, s87, 4194432\nQmvaa 136,160,212\nbuffer_load_dwordx4 v62, s[52:55], s69 offen lds\nQmvaa 136,164,180\nQda 176,63,512\n"
"Qmvaa 132,152,212\nQta 112,61,33024\nQmvaa 132,156,180\ns_add_u32 s69, s87, 4258936\nQmvaa 128,144,212\nbuffer_load_dwordx4 v62, s[52:55], s69 offen offset:1032 lds\nQmvaa 128,148,180\nQda 212,63,1024\n"
"Qmvaa 160,144,216\nQda 180,63,1536\nQmvaa 160,148,184\nQta 114,61,33536\ns_add_u32 s69, s87, 4323440\nQmvaa 164,152,216\nbuffer_load_dwordx4 v62, s[52:55], s69 offen offset:2064 lds\nQmvaa 164,156,184\n"
"Qta 116,61,37152\nQmvaa 168,160,216\nQmvaa 168,164,184\nQta 118,61,37664\ns_add_u32 s69, s87, 4387944\nQmvaa 172,168,216\nbuffer_load_dwordx4 v62, s[52:55], s69 offen offset:3096 lds\nQmvaa 172,172,184\n"
"Qda 216,63,2048\nQmvaa 204,168,220\nQda 184,63,2560\nQmvaa 204,172,188\nQta 120,61,33032\ns_add_u32 m0, s64, 0\nQmvaa 200,160,220\ns_cbranch_vccnz .Ld0q7\n"
"buffer_load_dwordx4 v72, s[44:47], s90 offen offset:3072 nt lds\ns_add_u32 s65, s65, 2048\ns_xor_b32 s60, s65, %1\ns_add_u32 s90, s60, 260096\n.Ld0q7:\nQmvaa 200,164,188\nQta 122,61,33544\nQmvaa 196,152,220\n"
"Qmvaa 196,156,188\nQta 124,61,37160\nQmvaa 192,144,220\nQda 220,63,3072\nQmvaa 192,148,188\nQda 188,63,3584\nQmvaa 224,144,224\nQta 126,61,37672\n"
"Qmvaa 224,148,192\nQmvaa 228,152,224\nQta 128,61,33280\nQmvaa 228,156,192\nQmvaa 232,160,224\nQta 130,61,33792\nQmvaa 232,164,192\nQmvaa 236,168,224\n"
"Qda 224,63,4096\nQmvaa 236,172,192\nQda 192,63,4608\nQmvaa 12,168,228\nQta 132,61,37408\nQmvaa 12,172,196\nQmvaa 8,160,228\nQta 134,61,37920\n"
"Qmvaa 8,164,196\nQmvaa 4,152,228\nQta 136,61,33288\nQmvaa 4,156,196\nQmvaa 0,144,228\nQda 228,63,5120\nQmvaa 0,148,196\nQda 196,63,5632\n"
"Qmvaa 32,144,232\nQta 138,61,33800\nQmvaa 32,148,200\nQmvaa 36,152,232\nQta 140,61,37416\nQmvaa 36,156,200\nQmvaa 40,160,232\nQta 142,61,37928\n"
"Qmvaa 40,164,200\nQmvaa 44,168,232\nQda 232,63,6144\nQmvaa 44,172,200\nQda 200,63,6656\ns_waitcnt lgkmcnt(2)\nQmvaa 80,112,208\nQmvaa 80,116,176\n"
"Qmvaa 84,120,208\nQmvaa 84,124,176\nQmvaa 88,128,208\nQmvaa 88,132,176\nQmvaa 92,136,208\nQmvaa 92,140,176\ns_waitcnt lgkmcnt(0)\nQmvaa 124,136,212\n"
"s_waitcnt vmcnt(17)\ns_barrier\nQmvaa 124,140,180\nQta 144,61,49536\ns_add_u32 m0, s62, 33024\ns_add_u32 s69, s87, 4718592\nQmvaa 120,128,212\nbuffer_load_dwordx4 v62, s[52:55], s69 offen lds\n"
"Qmvaa 120,132,180\nQta 146,61,50048\nQmvaa 116,120,212\nQmvaa 116,124,180\nQta 148,61,53664\ns_add_u32 s69, s87, 4783096\nQmvaa 112,112,212\nbuffer_load_dwordx4 v62, s[52:55], s69 offen offset:1032 lds\n"
"Qmvaa 112,116,180\nQta 150,61,54176\nQmvaa 144,112,216\nQmvaa 144,116,184\nQta 152,61,49544\ns_add_u32 s69, s87, 4847600\nQmvaa 148,120,216\nbuffer_load_dwordx4 v62, s[52:55], s69 offen offset:2064 lds\n"
"Qmvaa 148,124,184\nQta 154,61,50056\nQmvaa 152,128,216\nQmvaa 152,132,184\nQta 156,61,53672\ns_add_u32 s69, s87, 4912104\nQmvaa 156,136,216\nbuffer_load_dwordx4 v62, s[52:55], s69 offen offset:3096 lds\n"
"Qmvaa 156,140,184\nQta 158,61,54184\nQmvaa 188,136,220\nQmvaa 188,140,188\nQta 160,61,49792\ns_add_u32 m0, s63, 0\ns_add_u32 s88, s86, 1152\nQmvaa 184,128,220\n"
"buffer_load_dwordx4 v64, s[48:51], s88 offen lds\nQmvaa 184,132,188\nQta 162,61,50304\nQmvaa 180,120,220\nQmvaa 180,124,188\nQta 164,61,53920\ns_add_u32 s88, s86, 20608\nQmvaa 176,112,220\n"
"buffer_load_dwordx4 v64, s[48:51], s88 offen offset:1024 lds\nQmvaa 176,116,188\nQta 166,61,54432\nQmvaa 208,112,224\nQmvaa 208,116,192\nQta 168,61,49800\ns_add_u32 s88, s86, 40064\nQmvaa 212,120,224\n"
"buffer_load_dwordx4 v64, s[48:51], s88 offen offset:2048 lds\nQmvaa 212,124,192\nQta 170,61,50312\nQmvaa 216,128,224\nQmvaa 216,132,192\nQta 172,61,53928\ns_add_u32 s88, s86, 59520\nQmvaa 220,136,224\n"
"buffer_load_dwordx4 v64, s[48:51], s88 offen offset:3072 lds\nQmvaa 220,140,192\nQta 174,61,54440\nQmvaa 252,136,228\nQmvaa 252,140,196\ns_add_u32 m0, s63, 4096\ns_add_u32 s88, s86, 83072\nQmvaa 248,128,228\n"
"buffer_load_dwordx4 v64, s[48:51], s88 offen lds\nQmvaa 248,132,196\nv_mov_b32 v66, s77\nQmvaa 244,120,228\nds_write_b32 v73, v66 offset:16\nQmvaa 244,124,196\ns_add_u32 s88, s86, 102528\nQmvaa 240,112,228\n"
"buffer_load_dwordx4 v64, s[48:51], s88 offen offset:1024 lds\nQmvaa 240,116,196\nQmvaa 16,112,232\nQmvaa 16,116,200\ns_add_u32 s88, s86, 121984\nQmvaa 20,120,232\nbuffer_load_dwordx4 v64, s[48:51], s88 offen offset:2048 lds\nQmvaa 20,124,200\n"
"Qmvaa 24,128,232\nQmvaa 24,132,200\nQmvaa 28,136,232\nQmvaa 28,140,200\ns_waitcnt lgkmcnt(1)\nQmvaa 96,144,208\ns_or_b32 s68, s70, s67\ns_cselect_b32 s68, 0, 128\n"
"Qmvaa 96,148,176\nv_mov_b32 v66, 0x7ffff000\nv_cmp_gt_u32 vcc, s68, v60\nQmvaa 100,152,208\nv_not_b32 v67, v65\ns_lshl_b32 s68, s19, 2\nQmvaa 100,156,176\nQmvaa 104,160,208\n"
"v_cndmask_b32 v66, v66, v76, vcc\nv_readlane_b32 s30, v67, s19\nQmvaa 104,164,176\nQmvaa 108,168,208\nQmvaa 108,172,176\ns_waitcnt lgkmcnt(0)\nQmvaa 140,168,212\ns_waitcnt vmcnt(16)\n"
"s_barrier\nQmvaa 140,172,180\nQda 208,63,33024\ns_add_u32 m0, s62, 49536\ns_add_u32 s69, s87, 4718720\nQmvaa 136,160,212\nbuffer_load_dwordx4 v62, s[52:55], s69 offen lds\nQmvaa 136,164,180\n"
"Qda 176,63,33536\nQmvaa 132,152,212\nQta 112,61,0\nQmvaa 132,156,180\ns_add_u32 s69, s87, 4783224\nQmvaa 128,144,212\nbuffer_load_dwordx4 v62, s[52:55], s69 offen offset:1032 lds\nQmvaa 128,148,180\n"
"Qda 212,63,34048\nQmvaa 160,144,216\nQda 180,63,34560\nQmvaa 160,148,184\n.Lpg3:\ns_cbranch_execz .Ltr0\nQta 114,61,512\ns_add_u32 s69, s87, 4847728\n"
"Qmvaa 164,152,216\nbuffer_load_dwordx4 v62, s[52:55], s69 offen offset:2064 lds\nQmvaa 164,156,184\nQta 116,61,4128\nQmvaa 168,160,216\nQmvaa 168,164,184\nQta 118,61,4640\ns_add_u32 s69, s87, 4912232\n"
"Qmvaa 172,168,216\nbuffer_load_dwordx4 v62, s[52:55], s69 offen offset:3096 lds\nQmvaa 172,172,184\nQda 216,63,35072\nQmvaa 204,168,220\nQda 184,63,35584\nQmvaa 204,172,188\nQta 120,61,8\n"
"Qmvaa 200,160,220\nds_read_b32 v78, v74 offset:16\nQmvaa 200,164,188\nQta 122,61,520\nQmvaa 196,152,220\nQmvaa 196,156,188\nQta 124,61,4136\nQmvaa 192,144,220\n"
"Qda 220,63,36096\nQmvaa 192,148,188\nQda 188,63,36608\nQmvaa 224,144,224\nQta 126,61,4648\nQmvaa 224,148,192\nQmvaa 228,152,224\nQta 128,61,256\n"
"Qmvaa 228,156,192\nQmvaa 232,160,224\nQta 130,61,768\nQmvaa 232,164,192\nQmvaa 236,168,224\nQda 224,63,37120\nQmvaa 236,172,192\nQda 192,63,37632\n"
"Qmvaa 12,168,228\nQta 132,61,4384\nQmvaa 12,172,196\nQmvaa 8,160,228\nQta 134,61,4896\nQmvaa 8,164,196\nQmvaa 4,152,228\nQta 136,61,264\n"
"Qmvaa 4,156,196\nQmvaa 0,144,228\nQda 228,63,38144\nQmvaa 0,148,196\nQda 196,63,38656\nQmvaa 32,144,232\nQta 138,61,776\nQmvaa 32,148,200\n"
"Qmvaa 36,152,232\nQta 140,61,4392\nQmvaa 36,156,200\nQmvaa 40,160,232\nQta 142,61,4904\nQmvaa 40,164,200\nQmvaa 44,168,232\nQda 232,63,39168\n"
"Qmvaa 44,172,200\nQda 200,63,39680\ns_waitcnt lgkmcnt(2)\nQmvaa 80,112,208\nQmvaa 80,116,176\nQmvaa 84,120,208\nQmvaa 84,124,176\nQmvaa 88,128,208\n"
"Qmvaa 88,132,176\nQmvaa 92,136,208\nQmvaa 92,140,176\ns_waitcnt lgkmcnt(0)\nQmvaa 124,136,212\ns_waitcnt vmcnt(15)\ns_barrier\nQmvaa 124,140,180\n"
"Qta 144,61,16512\ns_mov_b32 s56, s94\ns_mov_b32 s57, s95\ns_mov_b32 s58, s75\ns_mov_b32 s94, 0\ns_add_u32 m0, %4, 160768\nQmvaa 120,128,212\nbuffer_load_dwordx4 v60, s[56:59], s94 offen lds\n"
"Qmvaa 120,132,180\nQta 146,61,17024\nQmvaa 116,120,212\nv_add_u32_dpp v78, v78, v78 quad_perm:[1,0,3,2] row_mask:0xf bank_mask:0xf\nQmvaa 116,124,180\nQta 148,61,20640\ns_add_u32 s94, s94, 4096\nQmvaa 112,112,212\n"
"buffer_load_dwordx4 v60, s[56:59], s94 offen lds\nQmvaa 112,116,180\nQta 150,61,21152\nQmvaa 144,112,216\nv_add_u32_dpp v78, v78, v78 quad_perm:[2,3,0,1] row_mask:0xf bank_mask:0xf\nQmvaa 144,116,184\nQta 152,61,16520\ns_add_u32 s94, s94, 4096\n"
"Qmvaa 148,120,216\nbuffer_load_dwordx4 v60, s[56:59], s94 offen lds\nQmvaa 148,124,184\nQta 154,61,17032\nQmvaa 152,128,216\nv_ashrrev_i32 v78, 1, v78\nv_med3_i32 v78, v78, -7, 7\nQmvaa 152,132,184\n"
"Qta 156,61,20648\ns_add_u32 s94, s94, 4096\nQmvaa 156,136,216\nbuffer_load_dwordx4 v60, s[56:59], s94 offen lds\nQmvaa 156,140,184\nQta 158,61,21160\nQmvaa 188,136,220\nv_add_lshl_u32 v78, v78, 8, 28\n"
"v_xor_b32 v67, v67, v78\nQmvaa 188,140,188\nQta 160,61,16768\ns_add_u32 s94, s94, 4096\nQmvaa 184,128,220\nbuffer_load_dwordx4 v60, s[56:59], s94 offen lds\nQmvaa 184,132,188\nQta 162,61,17280\n"
"Qmvaa 180,120,220\nbuffer_store_dword v67, v66, s[24:27], s68 offen offset:1024 sc1\nQmvaa 180,124,188\nQta 164,61,20896\ns_add_u32 s94, s94, 4096\nQmvaa 176,112,220\nbuffer_load_dwordx4 v60, s[56:59], s94 offen lds\nQmvaa 176,116,188\n"
"Qta 166,61,21408\nQmvaa 208,112,224\ns_lshl_b32 s68, s19, 4\ns_lshl1_add_u32 s33, s70, s67\nQmvaa 208,116,192\nQta 168,61,16776\ns_add_u32 s94, s94, 4096\nQmvaa 212,120,224\n"
"buffer_load_dwordx4 v60, s[56:59], s94 offen lds\nQmvaa 212,124,192\nQta 170,61,17288\nQmvaa 216,128,224\ns_cmp_eq_u32 s33, 1\ns_cselect_b32 s68, s68, -1\nQmvaa 216,132,192\nQta 172,61,20904\n"
"s_add_u32 s94, s94, 4096\nQmvaa 220,136,224\nbuffer_load_dwordx4 v60, s[56:59], s94 offen lds\nQmvaa 220,140,192\nQta 174,61,21416\nQmvaa 252,136,228\nv_mov_b32 v67, 0x7ffff000\nv_cmp_eq_u32 vcc, s68, v60\n"
"Qmvaa 252,140,196\ns_add_u32 s94, s94, 4096\nQmvaa 248,128,228\nbuffer_load_dwordx4 v60, s[56:59], s94 offen lds\nQmvaa 248,132,196\nv_cndmask_b32 v67, v67, v76, vcc\nv_add_u32 v66, 1, v65\nQmvaa 244,120,228\n"
"buffer_store_dword v66, v67, s[24:27], 0 offen sc1\nQmvaa 244,124,196\ns_add_u32 s94, s94, 4096\nQmvaa 240,112,228\nbuffer_load_dwordx4 v60, s[56:59], s94 offen lds\nQmvaa 240,116,196\nv_mov_b32 v55, 0x3f803f80\ns_mov_b32 vcc_lo, 0x33333333\n"
"Qmvaa 16,112,232\ns_mov_b32 vcc_hi, 0xcccccccc\nv_cndmask_b32 v55, 0, v55, vcc\nQmvaa 16,116,200\nv_accvgpr_write_b32 a204, v55\nv_accvgpr_write_b32 a205, v55\nQmvaa 20,120,232\nv_accvgpr_write_b32 a206, v55\n"
"v_accvgpr_write_b32 a207, v55\nQmvaa 20,124,200\ns_add_u32 s87, s87, 2048\nv_mov_b64 v[54:55], 0\nQmvaa 24,128,232\nglobal_load_dword v78, v60, s[100:101]\nQmvaa 24,132,200\nQmvaa 28,136,232\n"
"Qmvaa 28,140,200\ns_waitcnt lgkmcnt(0)\nQmvaa 96,144,208\nQmvaa 96,148,176\nQmvaa 100,152,208\nQmvaa 100,156,176\nQmvaa 104,160,208\nQmvaa 104,164,176\n"
"Qmvaa 108,168,208\nQmvaa 108,172,176\ns_waitcnt lgkmcnt(0)\nQmvaa 140,168,212\ns_waitcnt vmcnt(17)\ns_barrier\nQmvaa 140,172,180\nQda 208,63,0\n"
"s_add_u32 m0, s61, 33024\nQmvaa 136,160,212\nbuffer_load_dwordx4 v69, s[40:43], s60 offen nt lds\nQmvaa 136,164,180\nQda 176,63,512\nQmvaa 132,152,212\nQta 112,61,33024\nQmvaa 132,156,180\n"
"Qtv 48,70,128\nQmvaa 128,144,212\nbuffer_load_dwordx4 v69, s[40:43], s60 offen offset:1024 nt lds\nQmvaa 128,148,180\nQda 212,63,1024\nQmvaa 160,144,216\nQda 180,63,1536\nQmvaa 160,148,184\n"
"Qta 114,61,33536\nQmvaa 164,152,216\nbuffer_load_dwordx4 v69, s[40:43], s90 offen offset:2048 nt lds\nQmvaa 164,156,184\nQta 116,61,37152\nQmvaa 168,160,216\nQtv 50,70,1152\nQmvaa 168,164,184\n"
"Qta 118,61,37664\nQmvaa 172,168,216\nbuffer_load_dwordx4 v69, s[40:43], s90 offen offset:3072 nt lds\nQmvaa 172,172,184\nQda 216,63,2048\nQmvaa 204,168,220\nQda 184,63,2560\nQmvaa 204,172,188\n"
"Qta 120,61,33032\ns_add_u32 m0, s64, 33024\nQmvaa 200,160,220\nbuffer_load_dwordx4 v72, s[44:47], s60 offen nt lds\nQmvaa 200,164,188\nQta 122,61,33544\ns_waitcnt lgkmcnt(5)\nQmvaa 196,152,220\n"
"v_max_f32 v66, |v48|, |v50|\nQmvaa 196,156,188\nQta 124,61,37160\nQmvaa 192,144,220\nbuffer_load_dwordx4 v72, s[44:47], s60 offen offset:1024 nt lds\nQmvaa 192,148,188\nQda 220,63,3072\nv_mov_b32 v67, v66\n"
"Qmvaa 224,144,224\nQda 188,63,3584\nv_permlane32_swap_b32 v66, v67\nQmvaa 224,148,192\nQta 126,61,37672\nv_max_f32 v66, v66, v67\nQmvaa 228,152,224\nbuffer_load_dwordx4 v72, s[44:47], s90 offen offset:2048 nt lds\n"
"Qmvaa 228,156,192\nQta 128,61,33280\nv_mov_b32 v67, v66\nQmvaa 232,160,224\ns_nop 0\nv_permlane16_swap_b32 v66, v67\nQmvaa 232,164,192\nQta 130,61,33792\n"
"v_max_f32 v66, v66, v67\nQmvaa 236,168,224\nbuffer_load_dwordx4 v72, s[44:47], s90 offen offset:3072 nt lds\ns_add_u32 s65, s65, 2048\ns_xor_b32 s60, s65, %1\ns_add_u32 s90, s60, 260096\nQmvaa 236,172,192\nQda 224,63,4096\n"
"v_min_f32_dpp v66, v66, v66 row_ror:8 row_mask:0xf bank_mask:0xf\nQmvaa 12,168,228\nQda 192,63,4608\nv_min_f32_dpp v66, v66, v66 row_ror:4 row_mask:0xf bank_mask:0xf\nQmvaa 12,172,196\nQta 132,61,37408\nQmvaa 8,160,228\nv_min_f32_dpp v66, v66, v66 row_ror:2 row_mask:0xf bank_mask:0xf\n"
"Qmvaa 8,164,196\nQta 134,61,37920\nv_min_f32_dpp v66, v66, v66 row_ror:1 row_mask:0xf bank_mask:0xf\nQmvaa 4,152,228\nQmvaa 4,156,196\nQta 136,61,33288\nv_and_b32 v66, 0x7f000000, v66\nQmvaa 0,144,228\n"
"Qda 228,63,5120\nv_or_b32 v66, 0x400000, v66\nQmvaa 0,148,196\nQda 196,63,5632\nQmvaa 32,144,232\nQta 138,61,33800\nv_mov_b32 v67, 0\nQmvaa 32,148,200\n"
"v_mov_b32 v58, 0x39800000\nQmvaa 36,152,232\nQta 140,61,37416\nQmvaa 36,156,200\nv_mov_b32 v59, 0\nQmvaa 40,160,232\nQta 142,61,37928\nv_readfirstlane_b32 s73, v66\n"
"Qmvaa 40,164,200\nQmvaa 44,168,232\nQda 232,63,6144\nQmvaa 44,172,200\nQda 200,63,6656\ns_waitcnt lgkmcnt(2)\nQmvaa 80,112,208\nQmvaa 80,116,176\n"
"Qmvaa 84,120,208\nQmvaa 84,124,176\nQmvaa 88,128,208\nQmvaa 88,132,176\nQmvaa 92,136,208\nQmvaa 92,140,176\ns_waitcnt lgkmcnt(0)\nQmvaa 124,136,212\n"
"s_waitcnt vmcnt(21)\ns_barrier\nQmvaa 124,140,180\nQta 144,61,49536\nv_fma_f32 v80, v80, s72, v52\nQmvaa 120,128,212\nv_fma_f32 v81, v81, s72, v52\nv_fma_f32 v82, v82, s72, v52\n"
"Qmvaa 120,132,180\nQta 146,61,50048\nv_fma_f32 v83, v83, s72, v52\nQmvaa 116,120,212\nv_fma_f32 v84, v84, s72, v52\nv_fma_f32 v85, v85, s72, v52\nQmvaa 116,124,180\nQta 148,61,53664\n"
"v_fma_f32 v86, v86, s72, v52\nQmvaa 112,112,212\nv_fma_f32 v87, v87, s72, v52\nQk 80,80,81\nQmvaa 112,116,180\nQta 150,61,54176\nQk 81,82,83\nQmvaa 144,112,216\n"
"Qk 82,84,85\nQk 83,86,87\nQmvaa 144,116,184\nQta 152,61,49544\nv_accvgpr_write_b32 a0, v80\nQmvaa 148,120,216\nv_accvgpr_write_b32 a1, v81\nv_accvgpr_write_b32 a2, v82\n"
"Qmvaa 148,124,184\nQta 154,61,50056\nv_accvgpr_write_b32 a3, v83\nQmvaa 152,128,216\nv_fma_f32 v88, v88, s72, v52\nv_fma_f32 v89, v89, s72, v52\nQmvaa 152,132,184\nQta 156,61,53672\n"
"v_fma_f32 v90, v90, s72, v52\nQmvaa 156,136,216\nv_fma_f32 v91, v91, s72, v52\nv_fma_f32 v92, v92, s72, v52\nQmvaa 156,140,184\nQta 158,61,54184\nv_fma_f32 v93, v93, s72, v52\nQmvaa 188,136,220\n"
"v_fma_f32 v94, v94, s72, v52\nv_fma_f32 v95, v95, s72, v52\nQmvaa 188,140,188\nQta 160,61,49792\nQk 88,88,89\nQmvaa 184,128,220\nQk 89,90,91\nQk 90,92,93\n"
"Qmvaa 184,132,188\nQta 162,61,50304\nQk 91,94,95\nQmvaa 180,120,220\nv_accvgpr_write_b32 a4, v88\nv_accvgpr_write_b32 a5, v89\nQmvaa 180,124,188\nQta 164,61,53920\n"
"v_accvgpr_write_b32 a6, v90\nQmvaa 176,112,220\nv_accvgpr_write_b32 a7, v91\nv_fma_f32 v120, v120, s72, v52\nQmvaa 176,116,188\nQta 166,61,54432\nv_fma_f32 v121, v121, s72, v52\nQmvaa 208,112,224\n"
"v_fma_f32 v122, v122, s72, v52\nv_fma_f32 v123, v123, s72, v52\nQmvaa 208,116,192\nQta 168,61,49800\nv_fma_f32 v124, v124, s72, v52\nQmvaa 212,120,224\nv_fma_f32 v125, v125, s72, v52\nv_fma_f32 v126, v126, s72, v52\n"
"Qmvaa 212,124,192\nQta 170,61,50312\nv_fma_f32 v127, v127, s72, v52\nQmvaa 216,128,224\nQk 120,120,121\nQk 121,122,123\nQmvaa 216,132,192\nQta 172,61,53928\n"
"Qk 122,124,125\nQmvaa 220,136,224\nQk 123,126,127\nv_accvgpr_write_b32 a20, v120\nQmvaa 220,140,192\nQta 174,61,54440\nv_accvgpr_write_b32 a21, v121\nQmvaa 252,136,228\n"
"v_accvgpr_write_b32 a22, v122\nv_accvgpr_write_b32 a23, v123\nQmvaa 252,140,196\nv_fma_f32 v112, v112, s72, v52\nv_fma_f32 v113, v113, s72, v52\nQmvaa 248,128,228\nv_fma_f32 v114, v114, s72, v52\nv_fma_f32 v115, v115, s72, v52\n"
"Qmvaa 248,132,196\nv_fma_f32 v116, v116, s72, v52\nv_fma_f32 v117, v117, s72, v52\nQmvaa 244,120,228\nv_fma_f32 v118, v118, s72, v52\nv_fma_f32 v119, v119, s72, v52\nQmvaa 244,124,196\nQk 112,112,113\n"
"Qk 113,114,115\nQmvaa 240,112,228\nQk 114,116,117\nQk 115,118,119\nQmvaa 240,116,196\nv_accvgpr_write_b32 a16, v112\nv_accvgpr_write_b32 a17, v113\nQmvaa 16,112,232\n"
"v_accvgpr_write_b32 a18, v114\nv_accvgpr_write_b32 a19, v115\nQmvaa 16,116,200\nv_fma_f32 v144, v144, s72, v52\nv_fma_f32 v145, v145, s72, v52\nQmvaa 20,120,232\nv_fma_f32 v146, v146, s72, v52\nv_fma_f32 v147, v147, s72, v52\n"
"Qmvaa 20,124,200\nv_fma_f32 v148, v148, s72, v52\nv_fma_f32 v149, v149, s72, v52\nQmvaa 24,128,232\nv_fma_f32 v150, v150, s72, v52\nv_fma_f32 v151, v151, s72, v52\nQmvaa 24,132,200\nQk 144,144,145\n"
"Qk 145,146,147\nQmvaa 28,136,232\nQk 146,148,149\nQk 147,150,151\nQmvaa 28,140,200\nv_accvgpr_write_b32 a32, v144\nv_accvgpr_write_b32 a33, v145\ns_waitcnt lgkmcnt(0)\n"
"Qmvaa 96,144,208\nv_accvgpr_write_b32 a34, v146\nv_accvgpr_write_b32 a35, v147\nQmvaa 96,148,176\nv_fma_f32 v152, v152, s72, v52\nv_fma_f32 v153, v153, s72, v52\nQmvaa 100,152,208\nv_fma_f32 v154, v154, s72, v52\n"
"v_fma_f32 v155, v155, s72, v52\nQmvaa 100,156,176\nv_fma_f32 v156, v156, s72, v52\nv_fma_f32 v157, v157, s72, v52\nQmvaa 104,160,208\nv_fma_f32 v158, v158, s72, v52\nv_fma_f32 v159, v159, s72, v52\nQmvaa 104,164,176\n"
"Qk 152,152,153\nQk 153,154,155\nQmvaa 108,168,208\nQk 154,156,157\nQk 155,158,159\nQmvaa 108,172,176\ns_waitcnt lgkmcnt(0)\nv_accvgpr_write_b32 a36, v152\n"
"Qmvaa 140,168,212\ns_waitcnt vmcnt(36)\ns_barrier\nQmvaa 140,172,180\nQda 112,68,0\ns_add_u32 m0, s61, 66048\nQmvaa 136,160,212\nbuffer_load_dwordx4 v69, s[40:43], s60 offen nt lds\n"
"Qmvaa 136,164,180\nQda 116,68,512\nv_accvgpr_write_b32 a37, v153\nQmvaa 132,152,212\nv_accvgpr_write_b32 a38, v154\nv_accvgpr_write_b32 a39, v155\nQmvaa 132,156,180\nQda 120,68,256\n"
"v_fma_f32 v184, v184, s72, v52\nQmvaa 128,144,212\nbuffer_load_dwordx4 v69, s[40:43], s60 offen offset:1024 nt lds\nQmvaa 128,148,180\nQda 124,68,768\nv_fma_f32 v185, v185, s72, v52\nQmvaa 160,144,216\nv_fma_f32 v186, v186, s72, v52\n"
"v_fma_f32 v187, v187, s72, v52\nQmvaa 160,148,184\nQda 128,68,1024\nv_fma_f32 v188, v188, s72, v52\nQmvaa 164,152,216\nbuffer_load_dwordx4 v69, s[40:43], s90 offen offset:2048 nt lds\nQmvaa 164,156,184\nQda 132,68,1536\n"
"v_fma_f32 v189, v189, s72, v52\nQmvaa 168,160,216\nv_fma_f32 v190, v190, s72, v52\n.Lpg4:\ns_cbranch_execz .Ltr9\nv_fma_f32 v191, v191, s72, v52\nQmvaa 168,164,184\nQda 136,68,1280\n"
"Qk 184,184,185\nQmvaa 172,168,216\nbuffer_load_dwordx4 v69, s[40:43], s90 offen offset:3072 nt lds\nQmvaa 172,172,184\nQda 140,68,1792\nQk 185,186,187\nQmvaa 204,168,220\nQk 186,188,189\n"
"Qk 187,190,191\nQmvaa 204,172,188\ns_add_u32 m0, s64, 66048\nv_accvgpr_write_b32 a52, v184\nQmvaa 200,160,220\nbuffer_load_dwordx4 v72, s[44:47], s60 offen nt lds\nQmvaa 200,164,188\nv_accvgpr_write_b32 a53, v185\n"
"v_accvgpr_write_b32 a54, v186\nQmvaa 196,152,220\nv_accvgpr_write_b32 a55, v187\nv_fma_f32 v176, v176, s72, v52\nQmvaa 196,156,188\nv_fma_f32 v177, v177, s72, v52\nv_fma_f32 v178, v178, s72, v52\nQmvaa 192,144,220\n"
"buffer_load_dwordx4 v72, s[44:47], s60 offen offset:1024 nt lds\nQmvaa 192,148,188\nv_fma_f32 v179, v179, s72, v52\nv_fma_f32 v180, v180, s72, v52\nQmvaa 224,144,224\nv_fma_f32 v181, v181, s72, v52\nv_fma_f32 v182, v182, s72, v52\nQmvaa 224,148,192\n"
"v_fma_f32 v183, v183, s72, v52\nQk 176,176,177\nQmvaa 228,152,224\nbuffer_load_dwordx4 v72, s[44:47], s90 offen offset:2048 nt lds\nQmvaa 228,156,192\nQk 177,178,179\nQk 178,180,181\nQmvaa 232,160,224\n"
"Qk 179,182,183\nv_accvgpr_write_b32 a48, v176\nQmvaa 232,164,192\nv_accvgpr_write_b32 a49, v177\nv_accvgpr_write_b32 a50, v178\nQmvaa 236,168,224\nbuffer_load_dwordx4 v72, s[44:47], s90 offen offset:3072 nt lds\ns_add_u32 s65, s65, 2048\n"
"s_xor_b32 s60, s65, %1\ns_add_u32 s90, s60, 260096\nQmvaa 236,172,192\nv_accvgpr_write_b32 a51, v179\nv_fma_f32 v208, v208, s72, v52\nQmvaa 12,168,228\nv_fma_f32 v209, v209, s72, v52\nv_fma_f32 v210, v210, s72, v52\n"
"Qmvaa 12,172,196\nv_fma_f32 v211, v211, s72, v52\nv_fma_f32 v212, v212, s72, v52\nQmvaa 8,160,228\nv_fma_f32 v213, v213, s72, v52\nv_fma_f32 v214, v214, s72, v52\nQmvaa 8,164,196\nv_fma_f32 v215, v215, s72, v52\n"
"Qk 208,208,209\nQmvaa 4,152,228\nQk 209,210,211\nQk 210,212,213\nQmvaa 4,156,196\nQk 211,214,215\nv_accvgpr_write_b32 a64, v208\nQmvaa 0,144,228\n"
"v_accvgpr_write_b32 a65, v209\nv_accvgpr_write_b32 a66, v210\nQmvaa 0,148,196\nv_accvgpr_write_b32 a67, v211\nv_fma_f32 v216, v216, s72, v52\nQmvaa 32,144,232\nv_fma_f32 v217, v217, s72, v52\nv_fma_f32 v218, v218, s72, v52\n"
"Qmvaa 32,148,200\nv_fma_f32 v219, v219, s72, v52\nv_fma_f32 v220, v220, s72, v52\nQmvaa 36,152,232\nv_fma_f32 v221, v221, s72, v52\nv_fma_f32 v222, v222, s72, v52\nQmvaa 36,156,200\nv_fma_f32 v223, v223, s72, v52\n"
"Qk 216,216,217\nQmvaa 40,160,232\nQk 217,218,219\nQk 218,220,221\nQmvaa 40,164,200\nQk 219,222,223\nv_accvgpr_write_b32 a68, v216\nQmvaa 44,168,232\n"
"v_accvgpr_write_b32 a69, v217\nv_accvgpr_write_b32 a70, v218\nQmvaa 44,172,200\nv_accvgpr_write_b32 a71, v219\ns_nop 0\nQta 148,70,128\nQta 150,70,1152\nQta 156,70,384\n"
"Qta 158,70,1408\nQta 164,70,640\nQta 166,70,1664\nQta 172,70,896\nQta 174,70,1920\nv_pk_fma_f32 v[96:97], v[96:97], s[72:73], v[52:53] op_sel_hi:[1,0,0]\nv_pk_fma_f32 v[98:99], v[98:99], s[72:73], v[52:53] op_sel_hi:[1,0,0]\nv_pk_fma_f32 v[100:101], v[100:101], s[72:73], v[52:53] op_sel_hi:[1,0,0]\n"
"v_pk_fma_f32 v[102:103], v[102:103], s[72:73], v[52:53] op_sel_hi:[1,0,0]\nv_mfma_f32_16x16x4_4b_bf16 v[80:95], v[66:67], v[58:59], 0\nQk 96,96,97\nQk 97,98,99\nQk 98,100,101\nQk 99,102,103\nv_accvgpr_write_b32 a8, v96\nv_accvgpr_write_b32 a9, v97\n"
"v_accvgpr_write_b32 a10, v98\nv_accvgpr_write_b32 a11, v99\nv_pk_fma_f32 v[104:105], v[104:105], s[72:73], v[52:53] op_sel_hi:[1,0,0]\nv_pk_fma_f32 v[106:107], v[106:107], s[72:73], v[52:53] op_sel_hi:[1,0,0]\nv_pk_fma_f32 v[108:109], v[108:109], s[72:73], v[52:53] op_sel_hi:[1,0,0]\nv_pk_fma_f32 v[110:111], v[110:111], s[72:73], v[52:53] op_sel_hi:[1,0,0]\nv_mfma_f32_16x16x4_4b_bf16 v[112:127], v[66:67], v[58:59], 0\nQk 104,104,105\n"
"Qk 105,106,107\nQk 106,108,109\nQk 107,110,111\nv_accvgpr_write_b32 a12, v104\nv_accvgpr_write_b32 a13, v105\nv_accvgpr_write_b32 a14, v106\nv_accvgpr_write_b32 a15, v107\nv_pk_fma_f32 v[128:129], v[128:129], s[72:73], v[52:53] op_sel_hi:[1,0,0]\n"
"v_pk_fma_f32 v[130:131], v[130:131], s[72:73], v[52:53] op_sel_hi:[1,0,0]\nv_pk_fma_f32 v[132:133], v[132:133], s[72:73], v[52:53] op_sel_hi:[1,0,0]\nv_pk_fma_f32 v[134:135], v[134:135], s[72:73], v[52:53] op_sel_hi:[1,0,0]\nv_mfma_f32_16x16x4_4b_bf16 v[144:159], v[66:67], v[58:59], 0\nQk 128,128,129\nQk 129,130,131\nQk 130,132,133\nQk 131,134,135\n"
"v_accvgpr_write_b32 a24, v128\nv_accvgpr_write_b32 a25, v129\nv_accvgpr_write_b32 a26, v130\nv_accvgpr_write_b32 a27, v131\nv_pk_fma_f32 v[136:137], v[136:137], s[72:73], v[52:53] op_sel_hi:[1,0,0]\nv_pk_fma_f32 v[138:139], v[138:139], s[72:73], v[52:53] op_sel_hi:[1,0,0]\nv_pk_fma_f32 v[140:141], v[140:141], s[72:73], v[52:53] op_sel_hi:[1,0,0]\nv_pk_fma_f32 v[142:143], v[142:143], s[72:73], v[52:53] op_sel_hi:[1,0,0]\n"
"v_mfma_f32_16x16x4_4b_bf16 v[176:191], v[66:67], v[58:59], 0\nQk 136,136,137\nQk 137,138,139\nQk 138,140,141\nQk 139,142,143\nv_accvgpr_write_b32 a28, v136\nv_accvgpr_write_b32 a29, v137\nv_accvgpr_write_b32 a30, v138\n"
"v_accvgpr_write_b32 a31, v139\nv_pk_fma_f32 v[160:161], v[160:161], s[72:73], v[52:53] op_sel_hi:[1,0,0]\nv_pk_fma_f32 v[162:163], v[162:163], s[72:73], v[52:53] op_sel_hi:[1,0,0]\nv_pk_fma_f32 v[164:165], v[164:165], s[72:73], v[52:53] op_sel_hi:[1,0,0]\nv_pk_fma_f32 v[166:167], v[166:167], s[72:73], v[52:53] op_sel_hi:[1,0,0]\nv_mfma_f32_16x16x4_4b_bf16 v[208:223], v[66:67], v[58:59], 0\nQk 160,160,161\nQk 161,162,163\n"
"Qk 162,164,165\nQk 163,166,167\nv_accvgpr_write_b32 a40, v160\nv_accvgpr_write_b32 a41, v161\nv_accvgpr_write_b32 a42, v162\nv_accvgpr_write_b32 a43, v163\nv_pk_fma_f32 v[168:169], v[168:169], s[72:73], v[52:53] op_sel_hi:[1,0,0]\nv_pk_fma_f32 v[170:171], v[170:171], s[72:73], v[52:53] op_sel_hi:[1,0,0]\n"
"v_pk_fma_f32 v[172:173], v[172:173], s[72:73], v[52:53] op_sel_hi:[1,0,0]\nv_pk_fma_f32 v[174:175], v[174:175], s[72:73], v[52:53] op_sel_hi:[1,0,0]\nv_mfma_f32_16x16x4_4b_bf16 a[208:223], v[66:67], v[58:59], 0\nQk 168,168,169\nQk 169,170,171\nQk 170,172,173\nQk 171,174,175\nv_accvgpr_write_b32 a44, v168\n"
"v_accvgpr_write_b32 a45, v169\nv_accvgpr_write_b32 a46, v170\nv_accvgpr_write_b32 a47, v171\nv_pk_fma_f32 v[192:193], v[192:193], s[72:73], v[52:53] op_sel_hi:[1,0,0]\nv_pk_fma_f32 v[194:195], v[194:195], s[72:73], v[52:53] op_sel_hi:[1,0,0]\nv_pk_fma_f32 v[196:197], v[196:197], s[72:73], v[52:53] op_sel_hi:[1,0,0]\nv_pk_fma_f32 v[198:199], v[198:199], s[72:73], v[52:53] op_sel_hi:[1,0,0]\nv_mfma_f32_16x16x4_4b_bf16 a[224:239], v[66:67], v[58:59], 0\n"
"Qk 192,192,193\nQk 193,194,195\nQk 194,196,197\nQk 195,198,199\nv_accvgpr_write_b32 a56, v192\nv_accvgpr_write_b32 a57, v193\nv_accvgpr_write_b32 a58, v194\nv_accvgpr_write_b32 a59, v195\n"
"v_pk_fma_f32 v[200:201], v[200:201], s[72:73], v[52:53] op_sel_hi:[1,0,0]\nv_pk_fma_f32 v[202:203], v[202:203], s[72:73], v[52:53] op_sel_hi:[1,0,0]\nv_pk_fma_f32 v[204:205], v[204:205], s[72:73], v[52:53] op_sel_hi:[1,0,0]\nv_pk_fma_f32 v[206:207], v[206:207], s[72:73], v[52:53] op_sel_hi:[1,0,0]\nv_mfma_f32_16x16x4_4b_bf16 a[240:255], v[66:67], v[58:59], 0\nQk 200,200,201\nQk 201,202,203\nQk 202,204,205\n"
"Qk 203,206,207\nv_accvgpr_write_b32 a60, v200\nv_accvgpr_write_b32 a61, v201\nv_accvgpr_write_b32 a62, v202\nv_accvgpr_write_b32 a63, v203\nv_pk_fma_f32 v[224:225], v[224:225], s[72:73], v[52:53] op_sel_hi:[1,0,0]\nv_pk_fma_f32 v[226:227], v[226:227], s[72:73], v[52:53] op_sel_hi:[1,0,0]\nv_pk_fma_f32 v[228:229], v[228:229], s[72:73], v[52:53] op_sel_hi:[1,0,0]\n"
"v_pk_fma_f32 v[230:231], v[230:231], s[72:73], v[52:53] op_sel_hi:[1,0,0]\nv_mfma_f32_16x16x4_4b_bf16 v[96:111], v[66:67], v[58:59], 0\nQk 224,224,225\nQk 225,226,227\nQk 226,228,229\nQk 227,230,231\nv_accvgpr_write_b32 a72, v224\nv_accvgpr_write_b32 a73, v225\n"
"v_accvgpr_write_b32 a74, v226\nv_accvgpr_write_b32 a75, v227\nv_pk_fma_f32 v[232:233], v[232:233], s[72:73], v[52:53] op_sel_hi:[1,0,0]\nv_pk_fma_f32 v[234:235], v[234:235], s[72:73], v[52:53] op_sel_hi:[1,0,0]\nv_pk_fma_f32 v[236:237], v[236:237], s[72:73], v[52:53] op_sel_hi:[1,0,0]\nv_pk_fma_f32 v[238:239], v[238:239], s[72:73], v[52:53] op_sel_hi:[1,0,0]\nv_mfma_f32_16x16x4_4b_bf16 v[128:143], v[66:67], v[58:59], 0\nQk 232,232,233\n"
"Qk 233,234,235\nQk 234,236,237\nQk 235,238,239\nv_accvgpr_write_b32 a76, v232\nv_accvgpr_write_b32 a77, v233\nv_accvgpr_write_b32 a78, v234\nv_accvgpr_write_b32 a79, v235\nv_pk_fma_f32 v[240:241], v[240:241], s[72:73], v[52:53] op_sel_hi:[1,0,0]\n"
"v_pk_fma_f32 v[242:243], v[242:243], s[72:73], v[52:53] op_sel_hi:[1,0,0]\nv_pk_fma_f32 v[244:245], v[244:245], s[72:73], v[52:53] op_sel_hi:[1,0,0]\nv_pk_fma_f32 v[246:247], v[246:247], s[72:73], v[52:53] op_sel_hi:[1,0,0]\nv_mfma_f32_16x16x4_4b_bf16 v[160:175], v[66:67], v[58:59], 0\nQk 240,240,241\nQk 241,242,243\nQk 242,244,245\nQk 243,246,247\n"
"v_accvgpr_write_b32 a80, v240\nv_accvgpr_write_b32 a81, v241\nv_accvgpr_write_b32 a82, v242\nv_accvgpr_write_b32 a83, v243\nv_pk_fma_f32 v[248:249], v[248:249], s[72:73], v[52:53] op_sel_hi:[1,0,0]\nv_pk_fma_f32 v[250:251], v[250:251], s[72:73], v[52:53] op_sel_hi:[1,0,0]\nv_pk_fma_f32 v[252:253], v[252:253], s[72:73], v[52:53] op_sel_hi:[1,0,0]\nv_pk_fma_f32 v[254:255], v[254:255], s[72:73], v[52:53] op_sel_hi:[1,0,0]\n"
"v_mfma_f32_16x16x4_4b_bf16 v[192:207], v[66:67], v[58:59], 0\nQk 248,248,249\nQk 249,250,251\nQk 250,252,253\nQk 251,254,255\nv_accvgpr_write_b32 a84, v248\nv_accvgpr_write_b32 a85, v249\nv_accvgpr_write_b32 a86, v250\n"
"v_accvgpr_write_b32 a87, v251\nv_pk_fma_f32 v[0:1], v[0:1], s[72:73], v[52:53] op_sel_hi:[1,0,0]\nv_pk_fma_f32 v[2:3], v[2:3], s[72:73], v[52:53] op_sel_hi:[1,0,0]\nv_pk_fma_f32 v[4:5], v[4:5], s[72:73], v[52:53] op_sel_hi:[1,0,0]\nv_pk_fma_f32 v[6:7], v[6:7], s[72:73], v[52:53] op_sel_hi:[1,0,0]\nv_mfma_f32_16x16x4_4b_bf16 v[224:239], v[66:67], v[58:59], 0\nQk 0,0,1\nQk 1,2,3\n"
"Qk 2,4,5\nQk 3,6,7\nv_accvgpr_write_b32 a88, v0\nv_accvgpr_write_b32 a89, v1\nv_accvgpr_write_b32 a90, v2\nv_accvgpr_write_b32 a91, v3\nv_pk_fma_f32 v[8:9], v[8:9], s[72:73], v[52:53] op_sel_hi:[1,0,0]\nv_pk_fma_f32 v[10:11], v[10:11], s[72:73], v[52:53] op_sel_hi:[1,0,0]\n"
"v_pk_fma_f32 v[12:13], v[12:13], s[72:73], v[52:53] op_sel_hi:[1,0,0]\nv_pk_fma_f32 v[14:15], v[14:15], s[72:73], v[52:53] op_sel_hi:[1,0,0]\nv_mfma_f32_16x16x4_4b_bf16 v[240:255], v[66:67], v[58:59], 0\nQk 8,8,9\nQk 9,10,11\nQk 10,12,13\nQk 11,14,15\nv_accvgpr_write_b32 a92, v8\n"
"v_accvgpr_write_b32 a93, v9\nv_accvgpr_write_b32 a94, v10\nv_accvgpr_write_b32 a95, v11\nv_pk_fma_f32 v[16:17], v[16:17], s[72:73], v[52:53] op_sel_hi:[1,0,0]\nv_pk_fma_f32 v[18:19], v[18:19], s[72:73], v[52:53] op_sel_hi:[1,0,0]\nv_pk_fma_f32 v[20:21], v[20:21], s[72:73], v[52:53] op_sel_hi:[1,0,0]\nv_pk_fma_f32 v[22:23], v[22:23], s[72:73], v[52:53] op_sel_hi:[1,0,0]\nv_mfma_f32_16x16x4_4b_bf16 a[176:191], v[54:55], v[54:55], 0\n"
"Qk 16,16,17\nQk 17,18,19\nQk 18,20,21\nQk 19,22,23\nv_accvgpr_write_b32 a96, v16\nv_accvgpr_write_b32 a97, v17\nv_accvgpr_write_b32 a98, v18\nv_accvgpr_write_b32 a99, v19\n"
"v_pk_fma_f32 v[24:25], v[24:25], s[72:73], v[52:53] op_sel_hi:[1,0,0]\nv_pk_fma_f32 v[26:27], v[26:27], s[72:73], v[52:53] op_sel_hi:[1,0,0]\nv_pk_fma_f32 v[28:29], v[28:29], s[72:73], v[52:53] op_sel_hi:[1,0,0]\nv_pk_fma_f32 v[30:31], v[30:31], s[72:73], v[52:53] op_sel_hi:[1,0,0]\nv_mfma_f32_16x16x4_4b_bf16 a[188:203], v[54:55], v[54:55], 0\nQk 24,24,25\nQk 25,26,27\nQk 26,28,29\n"
"Qk 27,30,31\nv_accvgpr_write_b32 a100, v24\nv_accvgpr_write_b32 a101, v25\nv_accvgpr_write_b32 a102, v26\nv_accvgpr_write_b32 a103, v27\nv_pk_fma_f32 v[32:33], v[32:33], s[72:73], v[52:53] op_sel_hi:[1,0,0]\nv_pk_fma_f32 v[34:35], v[34:35], s[72:73], v[52:53] op_sel_hi:[1,0,0]\nv_pk_fma_f32 v[36:37], v[36:37], s[72:73], v[52:53] op_sel_hi:[1,0,0]\n"
"v_pk_fma_f32 v[38:39], v[38:39], s[72:73], v[52:53] op_sel_hi:[1,0,0]\nQk 32,32,33\nQk 33,34,35\nQk 34,36,37\nQk 35,38,39\nv_accvgpr_write_b32 a104, v32\nv_accvgpr_write_b32 a105, v33\nv_accvgpr_write_b32 a106, v34\n"
"v_accvgpr_write_b32 a107, v35\nv_pk_fma_f32 v[40:41], v[40:41], s[72:73], v[52:53] op_sel_hi:[1,0,0]\nv_pk_fma_f32 v[42:43], v[42:43], s[72:73], v[52:53] op_sel_hi:[1,0,0]\nv_pk_fma_f32 v[44:45], v[44:45], s[72:73], v[52:53] op_sel_hi:[1,0,0]\nv_pk_fma_f32 v[46:47], v[46:47], s[72:73], v[52:53] op_sel_hi:[1,0,0]\nQk 40,40,41\nQk 41,42,43\nQk 42,44,45\n"
"Qk 43,46,47\nv_accvgpr_write_b32 a108, v40\nv_accvgpr_write_b32 a109, v41\nv_accvgpr_write_b32 a110, v42\nv_accvgpr_write_b32 a111, v43\ns_waitcnt lgkmcnt(8)\nQzvaa 0,112,0\nQmvaa 0,116,4\n"
"s_memrealtime s[74:75]\ns_memtime s[68:69]\nQmvaa 0,120,8\nQmvaa 0,124,12\nQzvaa 4,140,12\nQmvaa 4,136,8\nQmvaa 4,132,4\nQmvaa 4,128,0\n"
"s_nop 3\nv_max3_f32 v66, v0, v1, v2\ns_nop 2\nv_max3_f32 v66, v66, v3, v4\nv_max3_f32 v66, v66, v5, v6\nv_max_f32 v66, v66, v7\nQzvaa 8,112,16\ns_waitcnt lgkmcnt(0)\n"
"v_mov_b32 v67, v66\nQmvaa 8,116,20\ns_sub_u32 s68, s68, s80\ns_sub_u32 s69, s74, s82\nQmvaa 8,120,24\ns_mul_i32 s69, s69, 73\ns_lshl_b32 s68, s68, 2\nQmvaa 8,124,28\n"
"s_sub_u32 s75, s67, 1\ns_cmp_gt_u32 s68, s69\ns_cselect_b32 s68, s75, 0\ns_cselect_b32 s31, 4, 0\ns_add_u32 s31, s31, 240\nQzvaa 12,140,28\ns_and_b32 s75, s68, 25\ns_add_u32 s77, s74, 60\n"
"Qmvaa 12,136,24\ns_add_u32 s77, s77, s75\nv_permlane32_swap_b32 v66, v67\nQmvaa 12,132,20\nv_max_f32 v66, v66, v67\nv_mov_b32 v67, v66\nQmvaa 12,128,16\ns_nop 0\n"
"v_permlane16_swap_b32 v66, v67\nv_max_f32 v66, v66, v67\ns_nop 1\nv_min_f32_dpp v66, v66, v66 row_ror:8 row_mask:0xf bank_mask:0xf\ns_nop 1\nv_min_f32_dpp v66, v66, v66 row_ror:4 row_mask:0xf bank_mask:0xf\ns_nop 1\nv_min_f32_dpp v66, v66, v66 row_ror:2 row_mask:0xf bank_mask:0xf\n"
"s_nop 1\nv_min_f32_dpp v66, v66, v66 row_ror:1 row_mask:0xf bank_mask:0xf\nv_sub_f32 v56, 0xc1a00000, v66\nv_mov_b32 v57, v56\nv_mov_b32 v58, v56\nv_mov_b32 v59, v56\nQa 0,0,56\nQa 1,1,56\n"
"Qa 2,2,56\nQa 3,3,56\nQe 0\nQe 1\nQe 2\nQe 3\nQk 16,0,1\nQk 17,2,3\n"
"Qcvaa 0,112,32,56\nQa 4,4,56\nQa 5,5,56\nQmvaa 0,116,36\nQa 6,6,56\nQa 7,7,56\nQmvaa 0,120,40\nQe 4\n"
"Qe 5\nQe 6\nQe 7\nQmvaa 0,124,44\nQk 18,4,5\nQk 19,6,7\nQcvaa 4,140,44,56\nQa 8,8,56\n"
"Qa 9,9,56\nQmvaa 4,136,40\nQa 10,10,56\nQa 11,11,56\nQmvaa 4,132,36\nQa 12,12,56\nQa 13,13,56\nQmvaa 4,128,32\n"
"s_waitcnt lgkmcnt(0)\nQa 14,14,56\nQmvva 96,16,148\nQa 15,15,56\nQmvva 100,16,156\nQe 8\nQmvva 104,16,164\nQe 9\n"
"Qe 10\nQe 11\nQe 12\nQe 13\nQe 14\nQe 15\nQmvva 108,16,172\nQk 20,8,9\n"
"Qk 21,10,11\nQk 22,12,13\nQk 23,14,15\ns_nop 1\nQmvva 140,20,172\nQe 0\nQmvva 136,20,164\nQe 1\n"
"Qmvva 132,20,156\nQe 2\nQmvva 128,20,148\nQe 3\nv_smfmac_f32_16x16x64_bf16 a[176:179], a[204:207], v[16:23], v77\nQk 24,0,1\nQcvaa 8,112,48,56\ns_add_u32 m0, s61, 99072\n"
"Qk 25,2,3\nQmvaa 8,116,52\nbuffer_load_dwordx4 v69, s[40:43], s60 offen nt lds\nQmvaa 8,120,56\nQe 4\nQmvaa 8,124,60\ns_or_b32 s65, s65, 0x1d8000\ns_add_u32 s65, s65, 2048\n"
"Qcvaa 12,140,60,56\ns_andn2_b32 s65, s65, 0x1d8000\nQmvaa 12,136,56\nQe 5\nQmvaa 12,132,52\nbuffer_load_dwordx4 v69, s[40:43], s60 offen offset:1024 nt lds\nQmvaa 12,128,48\nQe 6\n"
"Qcvaa 0,128,64,56\ns_memrealtime s[74:75]\nQk 26,4,5\nQmvaa 0,132,68\nQe 7\nQmvaa 0,136,72\nQk 27,6,7\nQmvaa 0,140,76\n"
"buffer_load_dwordx4 v69, s[40:43], s90 offen offset:2048 nt lds\nQcvaa 4,124,76,56\nQe 8\nQmvaa 4,120,72\nQe 9\nQmvaa 4,116,68\nQe 10\nQmvaa 4,112,64\n"
"Qe 11\nbuffer_load_dwordx4 v69, s[40:43], s90 offen offset:3072 nt lds\nQmvva 160,24,148\nQe 12\nQmvva 164,24,156\nQe 13\ns_add_u32 m0, s64, 99072\nQmvva 168,24,164\n"
"buffer_load_dwordx4 v72, s[44:47], s60 offen nt lds\nQe 14\nQe 15\nQmvva 172,24,172\nQk 28,8,9\nQk 29,10,11\nQk 30,12,13\nQk 31,14,15\n"
"s_nop 1\nQmvva 204,28,172\nQe 0\nbuffer_load_dwordx4 v72, s[44:47], s60 offen offset:1024 nt lds\nQmvva 200,28,164\nQe 1\nQmvva 196,28,156\nQe 2\n"
"Qmvva 192,28,148\nbuffer_load_dwordx4 v72, s[44:47], s90 offen offset:2048 nt lds\nv_smfmac_f32_16x16x64_bf16 a[180:183], a[204:207], v[24:31], v77\nQk 34,0,1\nQcvaa 8,112,80,56\nQe 3\nQmvaa 8,116,84\nQk 35,2,3\n"
"Qmvaa 8,120,88\ns_waitcnt vmcnt(15)\ns_barrier\nQmvaa 8,124,92\nbuffer_load_dwordx4 v72, s[44:47], s90 offen offset:3072 nt lds\nQcvaa 12,140,92,56\ns_waitcnt lgkmcnt(0)\nQmvaa 12,136,88\n"
"s_cmp_gt_u32 s77, s74\ns_cbranch_scc1 .Lsp0\n.Lrp0:\nQmvaa 12,132,84\ns_and_b32 s75, s68, 30\ns_add_u32 s77, s74, 90\nQmvaa 12,128,80\ns_add_u32 s77, s77, s75\n"
"s_xor_b32 s60, s65, %1\ns_add_u32 s90, s60, 260096\nQcvaa 0,128,96,56\nQda 128,68,34048\nQmvaa 0,132,100\nQda 132,68,34560\nQmvaa 0,136,104\nQda 136,68,34304\n"
"Qe 4\nQe 5\nQe 6\nQe 7\nQmvaa 0,140,108\nQda 140,68,34816\n.Lpg5:\ns_cbranch_execz .Ltr9\n"
"Qk 32,4,5\nQk 33,6,7\nQcvaa 4,124,108,56\nQda 124,68,33792\nQmvaa 4,120,104\nQda 120,68,33280\nQmvaa 4,116,100\nQda 116,68,33536\n"
"Qmvaa 4,112,96\nQda 112,68,33024\nQmvva 224,32,148\nQe 8\nQmvva 228,32,156\nQe 9\nQmvva 232,32,164\nQe 10\n"
"Qe 11\nQmvva 236,32,172\ns_waitcnt lgkmcnt(0)\nQk 36,8,9\nQk 37,10,11\nQcvaa 8,112,0,56\nQe 12\nQmvaa 8,116,4\n"
"v_add_u32 v68, 66048, v68\nQmvaa 8,120,8\nQe 13\nQe 14\nQe 15\nQmvaa 8,124,12\nQk 38,12,13\nQk 39,14,15\n"
"Qcvaa 12,140,12,56\nQe 0\nQmvaa 12,136,8\nQe 1\nQmvaa 12,132,4\nQe 2\nQmvaa 12,128,0\nQe 3\n"
"v_smfmac_f32_16x16x64_bf16 a[184:187], a[204:207], v[32:39], v77\nQk 42,0,1\nQmava 208,36,148\nQe 4\nQmava 212,36,156\nQe 5\nQmava 216,36,164\nQe 6\n"
"Qe 8\nQe 7\nQmava 220,36,172\nQe 9\nQk 43,2,3\nQk 40,4,5\nQk 41,6,7\nQta 168,70,768\n"
"Qta 170,70,1792\nQmava 252,40,172\nQe 10\nQta 172,70,33792\nQta 174,70,34816\nQmava 248,40,164\nQe 11\nQta 160,70,512\n"
"Qta 162,70,1536\nQmava 244,40,156\nQk 44,8,9\nQk 45,10,11\nQta 164,70,33536\nQta 166,70,34560\nQmava 240,40,148\nQe 12\n"
"Qcvaa 0,112,16,56\nQta 152,70,256\nQta 154,70,1280\nQmvaa 0,116,20\nQe 13\nQmvaa 0,120,24\nQta 156,70,33280\nQta 158,70,34304\n"
"Qmvaa 0,124,28\nQe 14\nQcvaa 4,140,28,56\nQta 144,70,0\nQta 146,70,1024\nQmvaa 4,136,24\nQe 15\nQmvaa 4,132,20\n"
"Qta 148,70,33024\nQta 150,70,34048\nQmvaa 4,128,16\nQk 46,12,13\nQk 47,14,15\nQcvaa 8,128,32,56\nv_add_u32 v75, 1, v75\nQmvaa 8,132,36\n"
"s_waitcnt vmcnt(8)\nds_write_b32 v73, v75\nQmvaa 8,136,40\nQe 0\nQmvaa 8,140,44\nQe 1\nQcvaa 12,124,44,56\nQe 2\n"
"Qmvaa 12,120,40\nQe 3\nQmvaa 12,116,36\nQe 4\nQmvaa 12,112,32\ns_waitcnt lgkmcnt(1)\nQk 48,0,1\nv_smfmac_f32_16x16x64_bf16 a[188:191], a[204:207], v[40:47], v77\n"
"Qk 49,2,3\nQmvva 80,16,144\nQe 5\nQmvva 80,44,148\nQe 6\nQmvva 84,16,152\nQe 7\nQmvva 84,44,156\n"
"Qk 50,4,5\nQk 51,6,7\nQmvva 88,16,160\nQe 8\nQmvva 88,44,164\nQe 9\nQmvva 92,16,168\nQe 10\n"
"Qmvva 92,44,172\nQe 11\nQmvva 124,20,168\nQk 54,8,9\nQk 55,10,11\nQmvva 124,48,172\nds_read_b32 v66, v74\nQmvva 120,20,160\n"
"Qe 12\nQmvva 120,48,164\nQe 13\nQmvva 116,20,152\nQe 14\nQmvva 116,48,156\nQe 15\nQmvva 112,20,144\n"
"s_waitcnt lgkmcnt(0)\nv_cmp_gt_u32 vcc, v75, v66\nQmvva 112,48,148\ns_cbranch_vccnz .Lsa1\n.Lra1:\nQk 52,12,13\nQcvaa 0,112,48,56\ns_add_u32 m0, s61, 0\n"
"Qk 53,14,15\nQmvaa 0,116,52\nbuffer_load_dwordx4 v69, s[40:43], s60 offen nt lds\nQmvaa 0,120,56\nQmvaa 0,124,60\ns_add_u32 s65, s65, 2048\nQcvaa 4,140,60,56\nQmvaa 4,136,56\n"
"Qmvaa 4,132,52\nbuffer_load_dwordx4 v69, s[40:43], s60 offen offset:1024 nt lds\nQmvaa 4,128,48\nQcvaa 8,128,64,56\ns_memrealtime s[74:75]\nQmvaa 8,132,68\nQe 0\nQmvaa 8,136,72\n"
"Qe 1\nQmvaa 8,140,76\nbuffer_load_dwordx4 v69, s[40:43], s90 offen offset:2048 nt lds\nQcvaa 12,124,76,56\nQe 2\nQmvaa 12,120,72\nQe 3\nQmvaa 12,116,68\n"
"Qe 4\nQmvaa 12,112,64\nQe 5\nv_smfmac_f32_16x16x64_bf16 a[192:195], a[204:207], v[48:55], v77\nQk 20,0,1\nQmvva 144,24,144\nbuffer_load_dwordx4 v69, s[40:43], s90 offen offset:3072 nt lds\nQmvva 144,52,148\n"
"Qe 6\nQmvva 148,24,152\nQe 7\nQmvva 148,52,156\ns_add_u32 m0, s64, 0\nQk 21,2,3\nQmvva 152,24,160\nbuffer_load_dwordx4 v72, s[44:47], s60 offen nt lds\n"
"Qmvva 152,52,164\nQk 22,4,5\nQk 23,6,7\nQmvva 156,24,168\nQe 8\nQmvva 156,52,172\nQe 9\nQmvva 188,28,168\n"
"Qe 10\nQmvva 188,20,172\nbuffer_load_dwordx4 v72, s[44:47], s60 offen offset:1024 nt lds\nQmvva 184,28,160\nQe 11\nQmvva 184,20,164\nQk 26,8,9\nQk 27,10,11\n"
"Qmvva 180,28,152\nQe 12\nQmvva 180,20,156\nQe 13\nQmvva 176,28,144\nbuffer_load_dwordx4 v72, s[44:47], s90 offen offset:2048 nt lds\nQmvva 176,20,148\nQe 14\n"
"Qcvaa 0,112,80,56\nQe 15\nQmvaa 0,116,84\nQk 24,12,13\nQk 25,14,15\nQmvaa 0,120,88\nQmvaa 0,124,92\nbuffer_load_dwordx4 v72, s[44:47], s90 offen offset:3072 nt lds\n"
"Qcvaa 4,140,92,56\ns_waitcnt lgkmcnt(0)\nQmvaa 4,136,88\ns_cmp_gt_u32 s77, s74\ns_cbranch_scc1 .Lsp1\n.Lrp1:\nQmvaa 4,132,84\ns_and_b32 s75, s68, 45\n"
"s_add_u32 s77, s74, 110\nQmvaa 4,128,80\ns_add_u32 s77, s77, s75\ns_xor_b32 s60, s65, %1\ns_add_u32 s90, s60, 260096\nQcvaa 8,128,96,56\nQda 128,68,1024\ns_add_u32 s79, s79, 2030\n"
"Qmvaa 8,132,100\nQda 132,68,1536\ns_min_u32 s79, s79, s77\nQmvaa 8,136,104\nQda 136,68,1280\nQmvaa 8,140,108\nQda 140,68,1792\nQcvaa 12,124,108,56\n"
"Qda 124,68,768\nQmvaa 12,120,104\nQda 120,68,256\nQmvaa 12,116,100\nQda 116,68,512\nQmvaa 12,112,96\nQda 112,68,0\nv_smfmac_f32_16x16x64_bf16 a[196:199], a[204:207], v[20:27], v77\n"
"Qmvva 208,32,144\nQe 0\nQmvva 208,24,148\nQe 1\nQmvva 212,32,152\nQe 2\nQmvva 212,24,156\nQe 3\n"
"Qmvva 216,32,160\nQk 28,0,1\nQk 29,2,3\nQmvva 216,24,164\nQe 4\nQmvva 220,32,168\nQe 5\nQmvva 220,24,172\n"
"s_waitcnt lgkmcnt(0)\nQk 30,4,5\nQcvaa 0,112,0,56\nQe 6\nQmvaa 0,116,4\nQe 7\nQmvaa 0,120,8\nQk 31,6,7\n"
"Qmvaa 0,124,12\nQe 8\nQcvaa 4,140,12,56\nQe 9\nQmvaa 4,136,8\nQe 10\nQmvaa 4,132,4\nQe 11\n"
"Qmvaa 4,128,0\nQe 12\nQmvva 240,36,144\nQe 13\nQmvva 240,28,148\nQe 14\nQmvva 244,36,152\nQe 15\n"
"Qmvva 244,28,156\nQk 34,8,9\nQk 35,10,11\nQmvva 248,36,160\nQk 32,12,13\nQk 33,14,15\nQmvva 248,28,164\nQe 0\n"
"Qmvva 252,36,168\nQe 1\nQmvva 252,28,172\nQe 2\nQmava 236,40,168\nQta 168,70,33920\nQta 170,70,34944\nQmava 236,32,172\n"
"Qe 3\nQmava 232,40,160\nQta 172,71,896\nQta 174,71,1920\nQmava 232,32,164\nQk 36,0,1\nQk 37,2,3\nQmava 228,40,152\n"
"Qta 160,70,33664\nQta 162,70,34688\nQmava 228,32,156\nQe 4\nQmava 224,40,144\nQta 164,71,640\nQta 166,71,1664\nQmava 224,32,148\n"
"Qe 5\nv_smfmac_f32_16x16x64_bf16 a[200:203], a[204:207], v[28:35], v77\nQta 152,70,33408\nQta 154,70,34432\nQcvaa 8,112,16,56\nQe 6\nQmvaa 8,116,20\nQta 156,71,384\n"
"Qta 158,71,1408\nQmvaa 8,120,24\nQe 7\nQmvaa 8,124,28\nQta 144,70,33152\nQta 146,70,34176\nQcvaa 12,140,28,56\nQk 38,4,5\n"
"Qk 39,6,7\nQmvaa 12,136,24\nQta 148,71,128\nQta 150,71,1152\nQmvaa 12,132,20\nQmvaa 12,128,16\ns_lshl_b32 s66, s67, 3\n.Lloop:\n"
"s_bitcmp1_b32 s99, s66\ns_cbranch_scc1 .Lhook\n.Lhookret:\nQcvaa 0,128,32,56\nv_add_u32 v75, 1, v75\nQmvaa 0,132,36\ns_waitcnt vmcnt(8)\nds_write_b32 v73, v75\n"
"Qmvaa 0,136,40\nQe 8\nQmvaa 0,140,44\nQe 9\nQcvaa 4,124,44,56\nQe 10\nQmvaa 4,120,40\nQe 11\n"
"Qmvaa 4,116,36\nQe 12\nQmvaa 4,112,32\ns_waitcnt lgkmcnt(1)\nQk 40,8,9\nQmvva 96,44,144\nQe 13\nQmvva 96,36,148\n"
"Qe 14\nQmvva 100,44,152\nQe 15\nQmvva 100,36,156\nQk 41,10,11\nQk 42,12,13\nQmvva 104,44,160\nQk 43,14,15\n"
"Qmvva 104,36,164\nQe 0\nQmvva 108,44,168\nQe 1\nQmvva 108,36,172\nQe 2\nQmvva 140,48,168\nQe 3\n"
"Qmvva 140,40,172\nQk 46,0,1\nQk 47,2,3\nQmvva 136,48,160\nds_read_b32 v66, v74\nQmvva 136,40,164\nQe 4\nQmvva 132,48,152\n"
"Qe 5\nQmvva 132,40,156\nQe 6\nQmvva 128,48,144\nQe 7\nQmvva 128,40,148\ns_waitcnt lgkmcnt(0)\nv_cmp_gt_u32 vcc, v75, v66\n"
"v_smfmac_f32_16x16x64_bf16 a[176:179], a[204:207], v[36:43], v77\ns_cbranch_vccnz .Lsa2\n.Lra2:\nQcvaa 8,112,48,56\ns_add_u32 m0, s61, 33024\nQk 44,4,5\nQmvaa 8,116,52\nbuffer_load_dwordx4 v69, s[40:43], s60 offen nt lds\n"
"Qmvaa 8,120,56\nQk 45,6,7\nQmvaa 8,124,60\ns_add_u32 s65, s65, 2048\nQcvaa 12,140,60,56\nQmvaa 12,136,56\nQmvaa 12,132,52\nbuffer_load_dwordx4 v69, s[40:43], s60 offen offset:1024 nt lds\n"
"Qmvaa 12,128,48\nQcvaa 0,128,64,56\ns_memrealtime s[74:75]\nQmvaa 0,132,68\nQe 8\nQmvaa 0,136,72\nQe 9\nQmvaa 0,140,76\n"
"buffer_load_dwordx4 v69, s[40:43], s90 offen offset:2048 nt lds\nQcvaa 4,124,76,56\nQe 10\nQmvaa 4,120,72\nQe 11\nQmvaa 4,116,68\nQe 12\nQmvaa 4,112,64\n"
"Qe 13\nQmvva 160,52,144\nbuffer_load_dwordx4 v69, s[40:43], s90 offen offset:3072 nt lds\nQmvva 160,44,148\nQe 14\nQmvva 164,52,152\nQe 15\nQmvva 164,44,156\n"
"Qk 48,8,9\nQk 49,10,11\nQmvva 168,52,160\ns_add_u32 m0, s64, 33024\nQk 50,12,13\nQmvva 168,44,164\nbuffer_load_dwordx4 v72, s[44:47], s60 offen nt lds\nQmvva 172,52,168\n"
"Qk 51,14,15\nQmvva 172,44,172\nQe 0\nQmvva 204,20,168\nQe 1\nQmvva 204,48,172\nQe 2\nQmvva 200,20,160\n"
"buffer_load_dwordx4 v72, s[44:47], s60 offen offset:1024 nt lds\nQmvva 200,48,164\nQe 3\nQmvva 196,20,152\nQk 18,0,1\nQk 19,2,3\nQmvva 196,48,156\nQe 4\n"
"Qmvva 192,20,144\nQe 5\nQmvva 192,48,148\nbuffer_load_dwordx4 v72, s[44:47], s90 offen offset:2048 nt lds\nv_smfmac_f32_16x16x64_bf16 a[180:183], a[204:207], v[44:51], v77\nQk 16,4,5\nQcvaa 8,112,80,56\nQe 6\n"
"Qmvaa 8,116,84\nQe 7\nQmvaa 8,120,88\nQk 17,6,7\nQmvaa 8,124,92\nbuffer_load_dwordx4 v72, s[44:47], s90 offen offset:3072 nt lds\nQcvaa 12,140,92,56\ns_waitcnt lgkmcnt(0)\n"
"Qmvaa 12,136,88\ns_cmp_gt_u32 s77, s74\ns_cbranch_scc1 .Lsp2\n.Lrp2:\nQmvaa 12,132,84\ns_add_u32 s77, s74, 220\nQmvaa 12,128,80\ns_xor_b32 s60, s65, %1\n"
"s_add_u32 s90, s60, 260096\nQcvaa 0,128,96,56\nQda 128,68,34048\ns_add_u32 s79, s79, s31\nQmvaa 0,132,100\nQda 132,68,34560\ns_max_u32 s77, s77, s79\nQmvaa 0,136,104\n"
"Qda 136,68,34304\nQmvaa 0,140,108\nQda 140,68,34816\nQcvaa 4,124,108,56\nQda 124,68,33792\nQmvaa 4,120,104\nQda 120,68,33280\nQmvaa 4,116,100\n"
"Qda 116,68,33536\nQmvaa 4,112,96\nQda 112,68,33024\nQmvva 224,24,144\nQe 8\nQmvva 224,16,148\nQe 9\nQmvva 228,24,152\n"
"Qe 10\nQmvva 228,16,156\nQe 11\nQmvva 232,24,160\nQk 20,8,9\nQk 21,10,11\nQmvva 232,16,164\nQe 12\n"
"Qmvva 236,24,168\nQe 13\nQmvva 236,16,172\ns_waitcnt lgkmcnt(0)\nQk 22,12,13\nQcvaa 8,112,0,56\nQe 14\nQmvaa 8,116,4\n"
"v_subrev_u32 v68, 66048, v68\nQmvaa 8,120,8\nQe 15\nQmvaa 8,124,12\nQk 23,14,15\nQcvaa 12,140,12,56\nQe 0\nQmvaa 12,136,8\n"
"Qe 1\nQmvaa 12,132,4\nQe 2\nQmvaa 12,128,0\nQe 3\nv_smfmac_f32_16x16x64_bf16 a[184:187], a[204:207], v[16:23], v77\nQk 26,0,1\nQmava 208,28,144\n"
"Qe 4\nQmava 208,20,148\nQe 5\nQmava 212,28,152\nQe 6\nQmava 212,20,156\nQe 7\nQmava 216,28,160\n"
"Qk 27,2,3\nQk 24,4,5\nQmava 216,20,164\nQk 25,6,7\nQmava 220,28,168\nQe 8\nQmava 220,20,172\nQe 9\n"
"Qmava 252,32,168\nQta 168,71,768\nQta 170,71,1792\nQmava 252,24,172\nQe 10\nQmava 248,32,160\nQta 172,71,33792\nQta 174,71,34816\n"
"Qmava 248,24,164\nQe 11\nQmava 244,32,152\nQta 160,71,512\nQta 162,71,1536\nQmava 244,24,156\nQk 28,8,9\nQk 29,10,11\n"
"Qmava 240,32,144\nQta 164,71,33536\nQta 166,71,34560\nQmava 240,24,148\nQe 12\nQcvaa 0,112,16,56\nQta 152,71,256\nQta 154,71,1280\n"
"Qmvaa 0,116,20\nQe 13\nQmvaa 0,120,24\nQta 156,71,33280\nQta 158,71,34304\nQmvaa 0,124,28\nQe 14\nQcvaa 4,140,28,56\n"
"Qta 144,71,0\nQta 146,71,1024\n.Lpg6:\ns_cbranch_execz .Ltr9\nQmvaa 4,136,24\nQe 15\nQmvaa 4,132,20\nQta 148,71,33024\n"
"Qta 150,71,34048\nQmvaa 4,128,16\nQk 30,12,13\nQk 31,14,15\nQcvaa 8,128,32,56\nv_add_u32 v75, 1, v75\nQmvaa 8,132,36\ns_waitcnt vmcnt(8)\n"
"ds_write_b32 v73, v75\nQmvaa 8,136,40\nQe 0\nQmvaa 8,140,44\nQe 1\nQcvaa 12,124,44,56\nQe 2\nQmvaa 12,120,40\n"
"Qe 3\nQmvaa 12,116,36\nQe 4\nQmvaa 12,112,32\ns_waitcnt lgkmcnt(1)\nQk 32,0,1\nv_smfmac_f32_16x16x64_bf16 a[188:191], a[204:207], v[24:31], v77\nQk 33,2,3\n"
"Qmvva 80,36,144\nQe 5\nQmvva 80,28,148\nQe 6\nQmvva 84,36,152\nQe 7\nQmvva 84,28,156\nQk 34,4,5\n"
"Qk 35,6,7\nQmvva 88,36,160\nQe 8\nQmvva 88,28,164\nQe 9\nQmvva 92,36,168\nQe 10\nQmvva 92,28,172\n"
"Qe 11\nQmvva 124,40,168\nQk 38,8,9\nQk 39,10,11\nQmvva 124,32,172\nds_read_b32 v66, v74\nQmvva 120,40,160\nQe 12\n"
"Qmvva 120,32,164\nQe 13\nQmvva 116,40,152\nQe 14\nQmvva 116,32,156\nQe 15\nQmvva 112,40,144\ns_waitcnt lgkmcnt(0)\n"
"v_cmp_gt_u32 vcc, v75, v66\nQmvva 112,32,148\ns_cbranch_vccnz .Lsa3\n.Lra3:\nQk 36,12,13\nQcvaa 0,112,48,56\ns_add_u32 m0, s61, 66048\nQk 37,14,15\n"
"Qmvaa 0,116,52\nbuffer_load_dwordx4 v69, s[40:43], s60 offen nt lds\nQmvaa 0,120,56\nQmvaa 0,124,60\ns_add_u32 s65, s65, 2048\nQcvaa 4,140,60,56\nQmvaa 4,136,56\nQmvaa 4,132,52\n"
"buffer_load_dwordx4 v69, s[40:43], s60 offen offset:1024 nt lds\nQmvaa 4,128,48\nQcvaa 8,128,64,56\ns_nop 0\nQmvaa 8,132,68\nQe 0\nQmvaa 8,136,72\nQe 1\n"
"Qmvaa 8,140,76\nbuffer_load_dwordx4 v69, s[40:43], s90 offen offset:2048 nt lds\nQcvaa 12,124,76,56\nQe 2\nQmvaa 12,120,72\nQe 3\nQmvaa 12,116,68\nQe 4\n"
"Qmvaa 12,112,64\nQe 5\nv_smfmac_f32_16x16x64_bf16 a[192:195], a[204:207], v[32:39], v77\nQk 40,0,1\nQmvva 144,44,144\nbuffer_load_dwordx4 v69, s[40:43], s90 offen offset:3072 nt lds\nQmvva 144,36,148\nQe 6\n"
"Qmvva 148,44,152\nQe 7\nQmvva 148,36,156\ns_add_u32 m0, s64, 66048\nQk 41,2,3\nQmvva 152,44,160\nbuffer_load_dwordx4 v72, s[44:47], s60 offen nt lds\nQmvva 152,36,164\n"
"Qk 42,4,5\nQk 43,6,7\nQmvva 156,44,168\nQe 8\nQmvva 156,36,172\nQe 9\nQmvva 188,48,168\nQe 10\n"
"Qmvva 188,40,172\nbuffer_load_dwordx4 v72, s[44:47], s60 offen offset:1024 nt lds\nQmvva 184,48,160\nQe 11\nQmvva 184,40,164\nQk 46,8,9\nQk 47,10,11\nQmvva 180,48,152\n"
"Qe 12\nQmvva 180,40,156\nQe 13\nQmvva 176,48,144\nbuffer_load_dwordx4 v72, s[44:47], s90 offen offset:2048 nt lds\nQmvva 176,40,148\nQe 14\nQcvaa 0,112,80,56\n"
"Qe 15\nQmvaa 0,116,84\nQk 44,12,13\nQk 45,14,15\nQmvaa 0,120,88\nQmvaa 0,124,92\nbuffer_load_dwordx4 v72, s[44:47], s90 offen offset:3072 nt lds\nQcvaa 4,140,92,56\n"
"Qmvaa 4,136,88\nQmvaa 4,132,84\nQmvaa 4,128,80\ns_xor_b32 s60, s65, %1\ns_add_u32 s90, s60, 260096\nQcvaa 8,128,96,56\nQda 128,68,1024\nQmvaa 8,132,100\n"
"Qda 132,68,1536\nQmvaa 8,136,104\nQda 136,68,1280\nQmvaa 8,140,108\nQda 140,68,1792\nQcvaa 12,124,108,56\nQda 124,68,768\nQmvaa 12,120,104\n"
"Qda 120,68,256\nQmvaa 12,116,100\nQda 116,68,512\nQmvaa 12,112,96\nQda 112,68,0\nv_smfmac_f32_16x16x64_bf16 a[196:199], a[204:207], v[40:47], v77\nQmvva 208,16,144\nQe 0\n"
"Qmvva 208,44,148\nQe 1\nQmvva 212,16,152\nQe 2\nQmvva 212,44,156\nQe 3\nQmvva 216,16,160\nQk 48,0,1\n"
"Qk 49,2,3\nQmvva 216,44,164\nQe 4\nQmvva 220,16,168\nQe 5\nQmvva 220,44,172\ns_waitcnt lgkmcnt(0)\nQk 50,4,5\n"
"Qcvaa 0,112,0,56\nQe 6\nQmvaa 0,116,4\nQe 7\nQmvaa 0,120,8\nQk 51,6,7\nQmvaa 0,124,12\nQe 8\n"
"Qcvaa 4,140,12,56\nQe 9\nQmvaa 4,136,8\nQe 10\nQmvaa 4,132,4\nQe 11\nQmvaa 4,128,0\nQe 12\n"
"Qmvva 240,20,144\nQe 13\nQmvva 240,48,148\nQe 14\nQmvva 244,20,152\nQe 15\nQmvva 244,48,156\nQk 54,8,9\n"
"Qk 55,10,11\nQmvva 248,20,160\nQk 52,12,13\nQk 53,14,15\nQmvva 248,48,164\nQe 0\nQmvva 252,20,168\nQe 1\n"
"Qmvva 252,48,172\nQe 2\nQmava 236,24,168\nQta 168,71,33920\nQta 170,71,34944\nQmava 236,52,172\nQe 3\nQmava 232,24,160\n"
"Qta 172,70,896\nQta 174,70,1920\nQmava 232,52,164\nQk 16,0,1\nQk 17,2,3\nQmava 228,24,152\nQta 160,71,33664\nQta 162,71,34688\n"
"Qmava 228,52,156\nQe 4\nQmava 224,24,144\nQta 164,70,640\nQta 166,70,1664\nQmava 224,52,148\nQe 5\nv_smfmac_f32_16x16x64_bf16 a[200:203], a[204:207], v[48:55], v77\n"
"Qta 152,71,33408\nQta 154,71,34432\nQcvaa 8,112,16,56\nQe 6\nQmvaa 8,116,20\nQta 156,70,384\nQta 158,70,1408\nQmvaa 8,120,24\n"
"Qe 7\nQmvaa 8,124,28\nQta 144,71,33152\nQta 146,71,34176\nQcvaa 12,140,28,56\nQk 18,4,5\nQk 19,6,7\nQmvaa 12,136,24\n"
"Qta 148,70,128\nQta 150,70,1152\nQmvaa 12,132,20\nQmvaa 12,128,16\nQcvaa 0,128,32,56\nv_add_u32 v75, 1, v75\nQmvaa 0,132,36\ns_waitcnt vmcnt(8)\n"
"ds_write_b32 v73, v75\nQmvaa 0,136,40\nQe 8\nQmvaa 0,140,44\nQe 9\nQcvaa 4,124,44,56\nQe 10\nQmvaa 4,120,40\n"
"Qe 11\nQmvaa 4,116,36\nQe 12\nQmvaa 4,112,32\ns_waitcnt lgkmcnt(1)\nQk 20,8,9\nQmvva 96,28,144\nQe 13\n"
"Qmvva 96,16,148\nQe 14\nQmvva 100,28,152\nQe 15\nQmvva 100,16,156\nQk 21,10,11\nQk 22,12,13\nQmvva 104,28,160\n"
"Qk 23,14,15\nQmvva 104,16,164\nQe 0\nQmvva 108,28,168\nQe 1\nQmvva 108,16,172\nQe 2\nQmvva 140,32,168\n"
"Qe 3\nQmvva 140,20,172\nQk 26,0,1\nQk 27,2,3\nQmvva 136,32,160\nds_read_b32 v66, v74\nQmvva 136,20,164\nQe 4\n"
"Qmvva 132,32,152\nQe 5\nQmvva 132,20,156\nQe 6\nQmvva 128,32,144\nQe 7\nQmvva 128,20,148\ns_waitcnt lgkmcnt(0)\n"
"v_cmp_gt_u32 vcc, v75, v66\nv_smfmac_f32_16x16x64_bf16 a[176:179], a[204:207], v[16:23], v77\ns_cbranch_vccnz .Lsa4\n.Lra4:\nQcvaa 8,112,48,56\ns_add_u32 m0, s61, 99072\nQk 24,4,5\nQmvaa 8,116,52\n"
"buffer_load_dwordx4 v69, s[40:43], s60 offen nt lds\nQmvaa 8,120,56\nQk 25,6,7\nQmvaa 8,124,60\ns_or_b32 s65, s65, 0x1d8000\ns_add_u32 s65, s65, 2048\nQcvaa 12,140,60,56\ns_andn2_b32 s65, s65, 0x1d8000\n"
"Qmvaa 12,136,56\nQmvaa 12,132,52\nbuffer_load_dwordx4 v69, s[40:43], s60 offen offset:1024 nt lds\nQmvaa 12,128,48\nQcvaa 0,128,64,56\ns_memrealtime s[74:75]\nQmvaa 0,132,68\nQe 8\n"
"Qmvaa 0,136,72\nQe 9\nQmvaa 0,140,76\nbuffer_load_dwordx4 v69, s[40:43], s90 offen offset:2048 nt lds\nQcvaa 4,124,76,56\nQe 10\nQmvaa 4,120,72\nQe 11\n"
"Qmvaa 4,116,68\nQe 12\nQmvaa 4,112,64\nQe 13\nQmvva 160,36,144\nbuffer_load_dwordx4 v69, s[40:43], s90 offen offset:3072 nt lds\nQmvva 160,24,148\nQe 14\n"
"Qmvva 164,36,152\nQe 15\nQmvva 164,24,156\nQk 28,8,9\nQk 29,10,11\nQmvva 168,36,160\ns_add_u32 m0, s64, 99072\nQk 30,12,13\n"
"Qmvva 168,24,164\nbuffer_load_dwordx4 v72, s[44:47], s60 offen nt lds\nQmvva 172,36,168\nQk 31,14,15\nQmvva 172,24,172\nQe 0\nQmvva 204,40,168\nQe 1\n"
"Qmvva 204,28,172\nQe 2\nQmvva 200,40,160\nbuffer_load_dwordx4 v72, s[44:47], s60 offen offset:1024 nt lds\nQmvva 200,28,164\nQe 3\nQmvva 196,40,152\nQk 34,0,1\n"
"Qk 35,2,3\nQmvva 196,28,156\nQe 4\nQmvva 192,40,144\nQe 5\nQmvva 192,28,148\nbuffer_load_dwordx4 v72, s[44:47], s90 offen offset:2048 nt lds\nv_smfmac_f32_16x16x64_bf16 a[180:183], a[204:207], v[24:31], v77\n"
"Qk 32,4,5\nQcvaa 8,112,80,56\nQe 6\nQmvaa 8,116,84\nQe 7\nQmvaa 8,120,88\nQk 33,6,7\nQmvaa 8,124,92\n"
"buffer_load_dwordx4 v72, s[44:47], s90 offen offset:3072 nt lds\nQcvaa 12,140,92,56\ns_waitcnt lgkmcnt(0)\nQmvaa 12,136,88\ns_cmp_gt_u32 s77, s74\ns_cbranch_scc1 .Lsp4\n.Lrp4:\nQmvaa 12,132,84\n"
"s_add_u32 s77, s74, 220\nQmvaa 12,128,80\ns_xor_b32 s60, s65, %1\ns_add_u32 s90, s60, 260096\nQcvaa 0,128,96,56\nQda 128,68,34048\ns_add_u32 s79, s79, s31\nQmvaa 0,132,100\n"
"Qda 132,68,34560\ns_max_u32 s77, s77, s79\nQmvaa 0,136,104\nQda 136,68,34304\nQmvaa 0,140,108\nQda 140,68,34816\nQcvaa 4,124,108,56\nQda 124,68,33792\n"
"Qmvaa 4,120,104\nQda 120,68,33280\nQmvaa 4,116,100\nQda 116,68,33536\nQmvaa 4,112,96\nQda 112,68,33024\nQmvva 224,44,144\nQe 8\n"
"Qmvva 224,32,148\nQe 9\nQmvva 228,44,152\nQe 10\nQmvva 228,32,156\nQe 11\nQmvva 232,44,160\nQk 36,8,9\n"
"Qk 37,10,11\nQmvva 232,32,164\nQe 12\nQmvva 236,44,168\nQe 13\nQmvva 236,32,172\ns_waitcnt lgkmcnt(0)\nQk 38,12,13\n"
"Qcvaa 8,112,0,56\nQe 14\nQmvaa 8,116,4\nv_add_u32 v68, 66048, v68\nQmvaa 8,120,8\nQe 15\nQmvaa 8,124,12\nQk 39,14,15\n"
"Qcvaa 12,140,12,56\nQe 0\nQmvaa 12,136,8\nQe 1\nQmvaa 12,132,4\nQe 2\nQmvaa 12,128,0\nQe 3\n"
"v_smfmac_f32_16x16x64_bf16 a[184:187], a[204:207], v[32:39], v77\nQk 42,0,1\nQmava 208,48,144\nQe 4\nQmava 208,36,148\nQe 5\nQmava 212,48,152\nQe 6\n"
"Qmava 212,36,156\nQe 7\nQmava 216,48,160\nQk 43,2,3\nQk 40,4,5\nQmava 216,36,164\nQk 41,6,7\nQmava 220,48,168\n"
"Qe 8\nQmava 220,36,172\nQe 9\nQmava 252,52,168\nQta 168,70,768\nQta 170,70,1792\nQmava 252,40,172\nQe 10\n"
"Qmava 248,52,160\nQta 172,70,33792\nQta 174,70,34816\nQmava 248,40,164\nQe 11\nQmava 244,52,152\nQta 160,70,512\nQta 162,70,1536\n"
"Qmava 244,40,156\nQk 44,8,9\nQk 45,10,11\nQmava 240,52,144\nQta 164,70,33536\nQta 166,70,34560\nQmava 240,40,148\nQe 12\n"
"Qcvaa 0,112,16,56\nQta 152,70,256\nQta 154,70,1280\nQmvaa 0,116,20\nQe 13\nQmvaa 0,120,24\nQta 156,70,33280\nQta 158,70,34304\n"
"Qmvaa 0,124,28\nQe 14\nQcvaa 4,140,28,56\nQta 144,70,0\nQta 146,70,1024\nQmvaa 4,136,24\nQe 15\nQmvaa 4,132,20\n"
"Qta 148,70,33024\nQta 150,70,34048\nQmvaa 4,128,16\nQk 46,12,13\nQk 47,14,15\nQcvaa 8,128,32,56\nv_add_u32 v75, 1, v75\nQmvaa 8,132,36\n"
"s_waitcnt vmcnt(8)\nds_write_b32 v73, v75\nQmvaa 8,136,40\nQe 0\nQmvaa 8,140,44\nQe 1\nQcvaa 12,124,44,56\nQe 2\n"
"Qmvaa 12,120,40\nQe 3\nQmvaa 12,116,36\nQe 4\nQmvaa 12,112,32\ns_waitcnt lgkmcnt(1)\nQk 48,0,1\nv_smfmac_f32_16x16x64_bf16 a[188:191], a[204:207], v[40:47], v77\n"
"Qk 49,2,3\nQmvva 80,16,144\nQe 5\nQmvva 80,44,148\nQe 6\nQmvva 84,16,152\nQe 7\nQmvva 84,44,156\n"
"Qk 50,4,5\nQk 51,6,7\nQmvva 88,16,160\nQe 8\nQmvva 88,44,164\nQe 9\nQmvva 92,16,168\nQe 10\n"
"Qmvva 92,44,172\nQe 11\nQmvva 124,20,168\nQk 54,8,9\nQk 55,10,11\nQmvva 124,48,172\nds_read_b32 v66, v74\nQmvva 120,20,160\n"
"Qe 12\nQmvva 120,48,164\nQe 13\nQmvva 116,20,152\nQe 14\nQmvva 116,48,156\nQe 15\nQmvva 112,20,144\n"
"s_waitcnt lgkmcnt(0)\nv_cmp_gt_u32 vcc, v75, v66\nQmvva 112,48,148\ns_cbranch_vccnz .Lsa5\n.Lra5:\nQk 52,12,13\nQcvaa 0,112,48,56\ns_add_u32 m0, s61, 0\n"
"Qk 53,14,15\nQmvaa 0,116,52\nbuffer_load_dwordx4 v69, s[40:43], s60 offen nt lds\nQmvaa 0,120,56\nQmvaa 0,124,60\ns_add_u32 s65, s65, 2048\nQcvaa 4,140,60,56\nQmvaa 4,136,56\n"
"Qmvaa 4,132,52\nbuffer_load_dwordx4 v69, s[40:43], s60 offen offset:1024 nt lds\nQmvaa 4,128,48\nQcvaa 8,128,64,56\ns_nop 0\nQmvaa 8,132,68\nQe 0\nQmvaa 8,136,72\n"
"Qe 1\nQmvaa 8,140,76\nbuffer_load_dwordx4 v69, s[40:43], s90 offen offset:2048 nt lds\nQcvaa 12,124,76,56\nQe 2\nQmvaa 12,120,72\nQe 3\nQmvaa 12,116,68\n"
"Qe 4\n.Lpg7:\ns_cbranch_execz .Ltr9\nQmvaa 12,112,64\nQe 5\nv_smfmac_f32_16x16x64_bf16 a[192:195], a[204:207], v[48:55], v77\nQk 20,0,1\nQmvva 144,24,144\n"
"buffer_load_dwordx4 v69, s[40:43], s90 offen offset:3072 nt lds\nQmvva 144,52,148\nQe 6\nQmvva 148,24,152\nQe 7\nQmvva 148,52,156\ns_add_u32 m0, s64, 0\nQk 21,2,3\n"
"Qmvva 152,24,160\nbuffer_load_dwordx4 v72, s[44:47], s60 offen nt lds\nQmvva 152,52,164\nQk 22,4,5\nQk 23,6,7\nQmvva 156,24,168\nQe 8\nQmvva 156,52,172\n"
"Qe 9\nQmvva 188,28,168\nQe 10\nQmvva 188,20,172\nbuffer_load_dwordx4 v72, s[44:47], s60 offen offset:1024 nt lds\nQmvva 184,28,160\nQe 11\nQmvva 184,20,164\n"
"Qk 26,8,9\nQk 27,10,11\nQmvva 180,28,152\nQe 12\nQmvva 180,20,156\nQe 13\nQmvva 176,28,144\nbuffer_load_dwordx4 v72, s[44:47], s90 offen offset:2048 nt lds\n"
"Qmvva 176,20,148\nQe 14\nQcvaa 0,112,80,56\nQe 15\nQmvaa 0,116,84\nQk 24,12,13\nQk 25,14,15\nQmvaa 0,120,88\n"
"Qmvaa 0,124,92\nbuffer_load_dwordx4 v72, s[44:47], s90 offen offset:3072 nt lds\nQcvaa 4,140,92,56\nQmvaa 4,136,88\nQmvaa 4,132,84\nQmvaa 4,128,80\ns_xor_b32 s60, s65, %1\ns_add_u32 s90, s60, 260096\n"
"Qcvaa 8,128,96,56\nQda 128,68,1024\nQmvaa 8,132,100\nQda 132,68,1536\nQmvaa 8,136,104\nQda 136,68,1280\nQmvaa 8,140,108\nQda 140,68,1792\n"
"Qcvaa 12,124,108,56\nQda 124,68,768\nQmvaa 12,120,104\nQda 120,68,256\nQmvaa 12,116,100\nQda 116,68,512\nQmvaa 12,112,96\nQda 112,68,0\n"
"v_smfmac_f32_16x16x64_bf16 a[196:199], a[204:207], v[20:27], v77\nQmvva 208,32,144\nQe 0\nQmvva 208,24,148\nQe 1\nQmvva 212,32,152\nQe 2\nQmvva 212,24,156\n"
"Qe 3\nQmvva 216,32,160\nQk 28,0,1\nQk 29,2,3\nQmvva 216,24,164\nQe 4\nQmvva 220,32,168\nQe 5\n"
"Qmvva 220,24,172\ns_waitcnt lgkmcnt(0)\nQk 30,4,5\nQcvaa 0,112,0,56\nQe 6\nQmvaa 0,116,4\nQe 7\nQmvaa 0,120,8\n"
"Qk 31,6,7\nQmvaa 0,124,12\nQe 8\nQcvaa 4,140,12,56\nQe 9\nQmvaa 4,136,8\nQe 10\nQmvaa 4,132,4\n"
"Qe 11\nQmvaa 4,128,0\nQe 12\nQmvva 240,36,144\nQe 13\nQmvva 240,28,148\nQe 14\nQmvva 244,36,152\n"
"Qe 15\nQmvva 244,28,156\nQk 34,8,9\nQk 35,10,11\nQmvva 248,36,160\nQk 32,12,13\nQk 33,14,15\nQmvva 248,28,164\n"
"Qe 0\nQmvva 252,36,168\nQe 1\nQmvva 252,28,172\nQe 2\nQmava 236,40,168\nQta 168,70,33920\nQta 170,70,34944\n"
"Qmava 236,32,172\nQe 3\nQmava 232,40,160\nQta 172,71,896\nQta 174,71,1920\nQmava 232,32,164\nQk 36,0,1\nQk 37,2,3\n"
"Qmava 228,40,152\nQta 160,70,33664\nQta 162,70,34688\nQmava 228,32,156\nQe 4\nQmava 224,40,144\nQta 164,71,640\nQta 166,71,1664\n"
"Qmava 224,32,148\nQe 5\nv_smfmac_f32_16x16x64_bf16 a[200:203], a[204:207], v[28:35], v77\nQta 152,70,33408\nQta 154,70,34432\nQcvaa 8,112,16,56\nQe 6\nQmvaa 8,116,20\n"
"Qta 156,71,384\nQta 158,71,1408\nQmvaa 8,120,24\nQe 7\nQmvaa 8,124,28\nQta 144,70,33152\nQta 146,70,34176\nQcvaa 12,140,28,56\n"
"Qk 38,4,5\nQk 39,6,7\nQmvaa 12,136,24\nQta 148,71,128\nQta 150,71,1152\nQmvaa 12,132,20\nQmvaa 12,128,16\ns_add_u32 s66, s66, 1\n"
"s_bitcmp1_b32 0x7f7f, s66\ns_cbranch_scc1 .Lloop\ns_cmp_lg_u32 s67, 0\ns_cbranch_scc1 .Ltail1\nQcvaa 0,128,32,56\nv_add_u32 v75, 1, v75\nv_bfe_u32 v67, v60, 4, 3\nQmvaa 0,132,36\n"
"s_waitcnt vmcnt(8)\nds_write_b32 v73, v75\nQmvaa 0,136,40\nQe 8\nQmvaa 0,140,44\nQe 9\nQcvaa 4,124,44,56\nQe 10\n"
"Qmvaa 4,120,40\nQe 11\nQmvaa 4,116,36\nQe 12\nQmvaa 4,112,32\ns_waitcnt lgkmcnt(1)\nQk 40,8,9\nQmvva 96,44,144\n"
"Qe 13\nQmvva 96,36,148\nQe 14\nQmvva 100,44,152\nQe 15\nQmvva 100,36,156\nQk 41,10,11\nQk 42,12,13\n"
"Qmvva 104,44,160\nQk 43,14,15\ns_lshl_b32 s68, s19, 7\nQmvva 104,36,164\nv_lshlrev_b32 v67, 2, v67\ns_add_u32 s68, s68, 5242880\nQmvva 108,44,168\nQe 0\n"
"Qmvva 108,36,172\nQe 1\nQmvva 140,48,168\nQe 2\nQmvva 140,40,172\nQe 3\nQmvva 136,48,160\nds_read_b32 v66, v74\n"
"Qk 46,0,1\nQmvva 136,40,164\nQk 47,2,3\nQmvva 132,48,152\nQe 4\nQmvva 132,40,156\nQe 5\nQmvva 128,48,144\n"
"Qe 6\nQmvva 128,40,148\ns_waitcnt lgkmcnt(0)\nv_cmp_gt_u32 vcc, v75, v66\nv_smfmac_f32_16x16x64_bf16 a[176:179], a[204:207], v[36:43], v77\ns_cbranch_vccnz .Lsa30\n.Lra30:\nQcvaa 8,112,48,56\n"
"Qe 7\nQmvaa 8,116,52\ns_add_u32 m0, s62, 0\ns_add_u32 s69, s87, 0\nQk 44,4,5\nQmvaa 8,120,56\nbuffer_load_dwordx4 v62, s[52:55], s69 offen lds\nQmvaa 8,124,60\n"
"Qk 45,6,7\nQcvaa 12,140,60,56\nQmvaa 12,136,56\nbuffer_load_dword v78, v67, s[24:27], s68 offen offset:1024 sc1\nQmvaa 12,132,52\nQmvaa 12,128,48\ns_add_u32 s69, s87, 64504\nQcvaa 0,128,64,56\n"
"buffer_load_dwordx4 v62, s[52:55], s69 offen offset:1032 lds\nQmvaa 0,132,68\nQe 8\nQmvaa 0,136,72\nQe 9\nQmvaa 0,140,76\ns_add_u32 s69, s87, 129008\nQk 48,8,9\n"
"Qcvaa 4,124,76,56\nbuffer_load_dwordx4 v62, s[52:55], s69 offen offset:2064 lds\nQmvaa 4,120,72\nQe 10\nQmvaa 4,116,68\nQe 11\nQmvaa 4,112,64\ns_add_u32 s69, s87, 193512\n"
"Qk 49,10,11\nQmvva 160,52,144\nbuffer_load_dwordx4 v62, s[52:55], s69 offen offset:3096 lds\nQmvva 160,44,148\nQe 12\nQmvva 164,52,152\nQe 13\nQmvva 164,44,156\n"
"s_add_u32 m0, s62, 16512\ns_add_u32 s69, s87, 128\nQk 50,12,13\nQmvva 168,52,160\nbuffer_load_dwordx4 v62, s[52:55], s69 offen lds\nQmvva 168,44,164\nQe 14\nQmvva 172,52,168\n"
"Qe 15\nQmvva 172,44,172\ns_add_u32 s69, s87, 64632\nQk 51,14,15\nQmvva 204,20,168\nbuffer_load_dwordx4 v62, s[52:55], s69 offen offset:1032 lds\nQmvva 204,48,172\nQe 0\n"
"Qmvva 200,20,160\nQe 1\nQmvva 200,48,164\ns_add_u32 s69, s87, 129136\nQk 18,0,1\nQmvva 196,20,152\nbuffer_load_dwordx4 v62, s[52:55], s69 offen offset:2064 lds\nQmvva 196,48,156\n"
"Qe 2\nQmvva 192,20,144\nQe 3\nQmvva 192,48,148\nQk 19,2,3\nv_smfmac_f32_16x16x64_bf16 a[180:183], a[204:207], v[44:51], v77\ns_add_u32 s69, s87, 193640\nQcvaa 8,112,80,56\n"
"buffer_load_dwordx4 v62, s[52:55], s69 offen offset:3096 lds\nQmvaa 8,116,84\nQe 4\nQmvaa 8,120,88\ns_add_u32 m0, s63, 33024\ns_add_u32 s88, s86, 0\nQmvaa 8,124,92\nbuffer_load_dwordx4 v64, s[48:51], s88 offen lds\n"
"Qcvaa 12,140,92,56\nQe 5\nQmvaa 12,136,88\nQe 6\nQmvaa 12,132,84\ns_add_u32 s88, s86, 19456\nQk 16,4,5\nQmvaa 12,128,80\n"
"buffer_load_dwordx4 v64, s[48:51], s88 offen offset:1024 lds\nQcvaa 0,128,96,56\nQda 128,68,34048\nQmvaa 0,132,100\nQda 132,68,34560\nQmvaa 0,136,104\nQda 136,68,34304\nQe 7\n"
"Qmvaa 0,140,108\nQda 140,68,34816\nQk 17,6,7\nQcvaa 4,124,108,56\nQda 124,68,33792\nQmvaa 4,120,104\nQda 120,68,33280\nQmvaa 4,116,100\n"
"Qda 116,68,33536\nQmvaa 4,112,96\nQda 112,68,33024\ns_add_u32 s88, s86, 38912\nQmvva 224,24,144\nbuffer_load_dwordx4 v64, s[48:51], s88 offen offset:2048 lds\nQmvva 224,16,148\nQe 8\n"
"Qmvva 228,24,152\nQe 9\nQmvva 228,16,156\nQe 10\nQmvva 232,24,160\ns_add_u32 s88, s86, 58368\nQk 20,8,9\nQmvva 232,16,164\n"
"buffer_load_dwordx4 v64, s[48:51], s88 offen offset:3072 lds\nQmvva 236,24,168\nQe 11\nQmvva 236,16,172\ns_waitcnt lgkmcnt(0)\nQk 21,10,11\nQcvaa 8,112,0,56\nQe 12\n"
"Qmvaa 8,116,4\nv_subrev_u32 v68, 66048, v68\ns_add_u32 m0, s63, 37120\ns_add_u32 s88, s86, 81920\nQmvaa 8,120,8\nbuffer_load_dwordx4 v64, s[48:51], s88 offen lds\nQe 13\nQe 14\n"
"Qe 15\nQmvaa 8,124,12\nQk 22,12,13\nQk 23,14,15\nQcvaa 12,140,12,56\nQe 0\nQmvaa 12,136,8\nQe 1\n"
"Qmvaa 12,132,4\nQe 2\nQmvaa 12,128,0\nQe 3\nv_smfmac_f32_16x16x64_bf16 a[184:187], a[204:207], v[16:23], v77\nQk 26,0,1\nQmava 208,28,144\ns_add_u32 s88, s86, 101376\n"
"Qk 27,2,3\nQmava 208,20,148\nbuffer_load_dwordx4 v64, s[48:51], s88 offen offset:1024 lds\nQmava 212,28,152\nQe 4\nQmava 212,20,156\nQe 5\nQmava 216,28,160\n"
"Qe 6\nQmava 216,20,164\nQe 7\nQmava 220,28,168\nQk 24,4,5\nQk 25,6,7\nQmava 220,20,172\nQe 8\n"
"Qmava 252,32,168\nQta 168,71,768\nQta 170,71,1792\nQmava 252,24,172\nQe 9\nQmava 248,32,160\nQta 172,71,33792\nQta 174,71,34816\n"
"Qmava 248,24,164\nQe 10\nQmava 244,32,152\nQta 160,71,512\nQta 162,71,1536\nQmava 244,24,156\nQe 11\nQmava 240,32,144\n"
"Qta 164,71,33536\nQta 166,71,34560\nQmava 240,24,148\nQk 28,8,9\nQk 29,10,11\nQcvaa 0,112,16,56\nQta 152,71,256\nQta 154,71,1280\n"
"s_add_u32 s88, s86, 120832\nQmvaa 0,116,20\nbuffer_load_dwordx4 v64, s[48:51], s88 offen offset:2048 lds\nQmvaa 0,120,24\nQta 156,71,33280\nQta 158,71,34304\nQmvaa 0,124,28\nQe 12\n"
"Qcvaa 4,140,28,56\nQta 144,71,0\nQta 146,71,1024\nQmvaa 4,136,24\nQe 13\nQmvaa 4,132,20\nQta 148,71,33024\nQta 150,71,34048\n"
"Qmvaa 4,128,16\nQe 14\nQcvaa 8,128,32,56\nv_add_u32 v75, 1, v75\nQk 30,12,13\nQmvaa 8,132,36\nds_write_b32 v73, v75\nQmvaa 8,136,40\n"
"Qe 15\nQmvaa 8,140,44\nQk 31,14,15\nQcvaa 12,124,44,56\nQe 0\nQmvaa 12,120,40\nQe 1\nQmvaa 12,116,36\n"
"Qe 2\nQmvaa 12,112,32\ns_waitcnt lgkmcnt(1)\nQk 32,0,1\nv_smfmac_f32_16x16x64_bf16 a[188:191], a[204:207], v[24:31], v77\nQmvva 80,36,144\nQe 3\nQmvva 80,28,148\n"
"Qe 4\nQmvva 84,36,152\nQe 5\nQmvva 84,28,156\nQe 6\nQmvva 88,36,160\nQe 7\nQmvva 88,28,164\n"
"Qk 33,2,3\nQk 34,4,5\nQmvva 92,36,168\nQk 35,6,7\nQmvva 92,28,172\nQe 8\nQmvva 124,40,168\nQe 9\n"
"Qmvva 124,32,172\nds_read_b32 v66, v74\nQk 38,8,9\nQmvva 120,40,160\nQe 10\nQmvva 120,32,164\nQe 11\nQmvva 116,40,152\n"
"Qk 39,10,11\nQmvva 116,32,156\nQe 12\nQmvva 112,40,144\ns_waitcnt lgkmcnt(0)\nv_cmp_gt_u32 vcc, v75, v66\nQmvva 112,32,148\ns_cbranch_vccnz .Lsa31\n"
".Lra31:\nQcvaa 0,112,48,56\nQe 13\nQmvaa 0,116,52\nQe 14\nQmvaa 0,120,56\ns_add_u32 m0, s62, 33024\ns_add_u32 s69, s87, 524288\n"
"Qk 36,12,13\nQmvaa 0,124,60\nbuffer_load_dwordx4 v62, s[52:55], s69 offen lds\nQcvaa 4,140,60,56\nQe 15\nQmvaa 4,136,56\nQk 37,14,15\nQmvaa 4,132,52\n"
"s_waitcnt vmcnt(15)\nv_xor_b32 v78, s30, v78\nv_and_b32 v66, 0x0fffffff, v78\nv_cmp_ne_u32 vcc, 0, v66\ns_cmp_eq_u64 vcc, 0\ns_cbranch_scc0 .Lack_w\n.Lack_ok:\nQmvaa 4,128,48\n"
"s_add_u32 s69, s87, 588792\nv_lshrrev_b32 v78, 28, v78\nQcvaa 8,128,64,56\nbuffer_load_dwordx4 v62, s[52:55], s69 offen offset:1032 lds\nQmvaa 8,132,68\nQe 0\nQmvaa 8,136,72\nQe 1\n"
"Qmvaa 8,140,76\ns_add_u32 s69, s87, 653296\nQk 40,0,1\nQcvaa 12,124,76,56\nbuffer_load_dwordx4 v62, s[52:55], s69 offen offset:2064 lds\nQmvaa 12,120,72\nQe 2\nQmvaa 12,116,68\n"
"Qe 3\nQmvaa 12,112,64\nQe 4\nv_smfmac_f32_16x16x64_bf16 a[192:195], a[204:207], v[32:39], v77\ns_add_u32 s69, s87, 717800\nQmvva 144,44,144\nbuffer_load_dwordx4 v62, s[52:55], s69 offen offset:3096 lds\nQmvva 144,36,148\n"
"Qe 5\nQmvva 148,44,152\nQe 6\nQmvva 148,36,156\nQe 7\nQmvva 152,44,160\nQk 41,2,3\nQk 42,4,5\n"
"Qmvva 152,36,164\ns_add_u32 m0, s62, 49536\ns_add_u32 s69, s87, 524416\nQk 43,6,7\nQmvva 156,44,168\nbuffer_load_dwordx4 v62, s[52:55], s69 offen lds\nQmvva 156,36,172\nQe 8\n"
"Qmvva 188,48,168\nQe 9\nQmvva 188,40,172\nQe 10\nQmvva 184,48,160\ns_add_u32 s69, s87, 588920\nQk 46,8,9\nQmvva 184,40,164\n"
"buffer_load_dwordx4 v62, s[52:55], s69 offen offset:1032 lds\nQmvva 180,48,152\nQe 11\nQmvva 180,40,156\nQk 47,10,11\nv_add_u32_dpp v78, v78, v78 quad_perm:[1,0,3,2] row_mask:0xf bank_mask:0xf\nQmvva 176,48,144\ns_add_u32 s69, s87, 653424\n"
"Qmvva 176,40,148\nbuffer_load_dwordx4 v62, s[52:55], s69 offen offset:2064 lds\nQcvaa 0,112,80,56\nQe 12\nQmvaa 0,116,84\nQe 13\nQmvaa 0,120,88\nQe 14\n"
"Qmvaa 0,124,92\nQe 15\nQcvaa 4,140,92,56\nQk 44,12,13\nQk 45,14,15\nQmvaa 4,136,88\ns_add_u32 s69, s87, 717928\nv_add_u32_dpp v78, v78, v78 quad_perm:[2,3,0,1] row_mask:0xf bank_mask:0xf\n"
"Qmvaa 4,132,84\nbuffer_load_dwordx4 v62, s[52:55], s69 offen offset:3096 lds\n.Lpg8:\ns_cbranch_execz .Ltr10\nQmvaa 4,128,80\nv_readlane_b32 s68, v78, s19\nv_readlane_b32 s33, v78, 4\nQcvaa 8,128,96,56\n"
"s_cmp_lt_u32 s68, 32\ns_cselect_b32 s68, 8192, 0\nQmvaa 8,132,100\nQe 0\nQmvaa 8,136,104\nQe 1\nQmvaa 8,140,108\nQe 2\n"
"Qcvaa 12,124,108,56\nQe 3\nQmvaa 12,120,104\nQk 48,0,1\nQk 49,2,3\nQmvaa 12,116,100\ns_xor_b32 s68, s68, s98\ns_add_u32 s71, s71, s68\n"
"Qmvaa 12,112,96\ns_cmp_lt_u32 s33, 32\ns_cselect_b32 s33, 8, 0\nv_smfmac_f32_16x16x64_bf16 a[196:199], a[204:207], v[40:47], v77\ns_xor_b32 s33, s33, %0\nQmvva 208,16,144\nQta 112,71,33152\ns_and_b32 s33, s33, 8\n"
"Qmvva 208,44,148\nQta 114,71,34176\ns_lshl_b32 s98, s33, 10\nQmvva 212,16,152\nQta 116,71,33408\nQmvva 212,44,156\nQta 118,71,34432\nQmvva 216,16,160\n"
"Qta 120,71,33664\nQmvva 216,44,164\nQta 122,71,34688\nQmvva 220,16,168\nQta 124,71,33920\nQmvva 220,44,172\nQta 126,71,34944\nQe 4\n"
"Qe 5\nQe 6\nQe 7\nQk 50,4,5\nQk 51,6,7\nQmvva 240,20,144\nv_add_u32 v75, 1, v75\nQmvva 240,48,148\n"
"ds_write_b32 v73, v75\nQmvva 244,20,152\nQe 8\nQmvva 244,48,156\nQe 9\nQmvva 248,20,160\nQe 10\nQmvva 248,48,164\n"
"Qe 11\nQmvva 252,20,168\nQk 54,8,9\nQk 55,10,11\nQmvva 252,48,172\nQe 12\nQe 13\nQe 14\n"
"Qe 15\nQk 52,12,13\nQk 53,14,15\ns_nop 1\nv_smfmac_f32_16x16x64_bf16 a[200:203], a[204:207], v[48:55], v77\nQmava 224,24,144\nds_read_b32 v66, v74\nv_accvgpr_read_b32 v0, a176\n"
"Qmava 224,52,148\nv_accvgpr_read_b32 v1, a177\nv_accvgpr_read_b32 v6, a178\nQmava 228,24,152\nv_accvgpr_read_b32 v7, a179\nv_accvgpr_read_b32 v8, a180\nQmava 228,52,156\nv_accvgpr_read_b32 v9, a181\n"
"v_accvgpr_read_b32 v14, a182\nQmava 232,24,160\nv_accvgpr_read_b32 v15, a183\nv_accvgpr_read_b32 v16, a184\nQmava 232,52,164\ns_waitcnt lgkmcnt(0)\nv_cmp_gt_u32 vcc, v75, v66\nQmava 236,24,168\n"
"s_cbranch_vccnz .Lsa32\n.Lra32:\nv_accvgpr_read_b32 v17, a185\nQmava 236,52,172\ns_waitcnt lgkmcnt(0)\nv_accvgpr_read_b32 v22, a186\nQmvva 96,28,112\ns_add_u32 m0, s63, 0\n"
"s_add_u32 s88, s86, 128\nv_accvgpr_read_b32 v23, a187\nQmvva 100,28,116\nbuffer_load_dwordx4 v64, s[48:51], s88 offen lds\nQmvva 104,28,120\nv_accvgpr_read_b32 v24, a188\nv_accvgpr_read_b32 v25, a189\nQmvva 108,28,124\n"
"v_accvgpr_read_b32 v2, a190\nv_accvgpr_read_b32 v3, a191\nQmvva 128,32,112\ns_add_u32 s88, s86, 19584\nv_accvgpr_read_b32 v4, a192\nQmvva 132,32,116\nbuffer_load_dwordx4 v64, s[48:51], s88 offen offset:1024 lds\nQmvva 136,32,120\n"
"v_accvgpr_read_b32 v5, a193\nv_accvgpr_read_b32 v10, a194\nQmvva 140,32,124\nv_accvgpr_read_b32 v11, a195\nv_accvgpr_read_b32 v12, a196\nQmvva 160,36,112\ns_add_u32 s88, s86, 39040\nv_accvgpr_read_b32 v13, a197\n"
"Qmvva 164,36,116\nbuffer_load_dwordx4 v64, s[48:51], s88 offen offset:2048 lds\nQmvva 168,36,120\nv_accvgpr_read_b32 v18, a198\nv_accvgpr_read_b32 v19, a199\nQmvva 172,36,124\nv_accvgpr_read_b32 v20, a200\nv_accvgpr_read_b32 v21, a201\n"
"Qmvva 192,40,112\ns_add_u32 s88, s86, 58496\nv_accvgpr_read_b32 v26, a202\nQmvva 196,40,116\nbuffer_load_dwordx4 v64, s[48:51], s88 offen offset:3072 lds\nQmvva 200,40,120\nv_accvgpr_read_b32 v27, a203\nQa 8,8,9\n"
"Qmvva 204,40,124\nQa 10,10,11\nQa 8,8,10\nQmvva 224,44,112\ns_add_u32 m0, s63, 4096\ns_add_u32 s88, s86, 82048\nQa 16,16,17\nQmvva 228,44,116\n"
"buffer_load_dwordx4 v64, s[48:51], s88 offen lds\nQmvva 232,44,120\nQa 18,18,19\nQa 16,16,18\nQmvva 236,44,124\nQa 4,4,5\nQa 6,6,7\nQmava 208,48,112\n"
"s_add_u32 s88, s86, 101504\nQa 4,4,6\nQmava 212,48,116\nbuffer_load_dwordx4 v64, s[48:51], s88 offen offset:1024 lds\nQmava 216,48,120\nQa 12,12,13\nQa 14,14,15\nQmava 220,48,124\n"
"Qa 12,12,14\nQa 0,0,1\nQmava 240,52,112\ns_add_u32 s88, s86, 120960\nQa 2,2,3\nQmava 244,52,116\nbuffer_load_dwordx4 v64, s[48:51], s88 offen offset:2048 lds\nQmava 248,52,120\n"
"Qa 0,0,2\nQa 24,24,25\nQmava 252,52,124\nQa 26,26,27\nQa 24,24,26\nQa 20,20,21\nQa 22,22,23\nQa 20,20,22\n"
"v_max3_u32 v9, v8, v16, v4\nv_max3_u32 v9, v9, v12, v0\nv_max3_u32 v9, v9, v24, v20\nv_min3_u32 v11, v8, v16, v4\nv_min3_u32 v11, v11, v12, v0\nv_min3_u32 v11, v11, v24, v20\nv_cmp_lt_u32 vcc, 0x4e800000, v9\ns_mov_b64 s[74:75], vcc\n"
"v_cmp_gt_u32 vcc, 0x35800000, v11\ns_or_b64 s[74:75], s[74:75], vcc\nv_mov_b32 v30, s73\nv_mov_b32 v31, 0\nv_mov_b32 v58, 0xb9800000\nv_mov_b32 v59, 0\nv_and_b32 v66, 16, v60\nv_mov_b32 v67, 0xffff\n"
"v_lshlrev_b32 v66, v66, v67\ns_mov_b32 vcc_lo, 0x00c0000c\ns_mov_b32 vcc_hi, 0xc0000c00\nv_cndmask_b32 v67, 0, v66, vcc\ns_lshr_b64 vcc, vcc, 2\nv_cndmask_b32 v66, 0, v66, vcc\ns_cmp_lg_u64 s[74:75], 0\ns_cbranch_scc1 .Lfb\n"
".Lfbret0:\nv_rcp_f32 v2, v0\nv_rcp_f32_e64 v6, v4\nv_mfma_f32_16x16x4_4b_bf16 a[208:223], v[30:31], v[58:59], a[208:223]\nv_rcp_f32 v10, v8\nv_rcp_f32 v14, v12\nv_rcp_f32 v18, v16\nv_rcp_f32 v22, v20\n"
"v_rcp_f32 v26, v24\nv_mov_b64 v[0:1], 0\nv_mfma_f32_16x16x4_4b_bf16 a[224:239], v[30:31], v[58:59], a[224:239]\nQk 2,2,2\nv_and_b32 v3, v2, v67\nv_and_b32 v2, v2, v66\nv_mov_b64_e64 v[4:5], 0\nQk 6,6,6\n"
"v_and_b32_e64 v7, v6, v67\nv_mfma_f32_16x16x4_4b_bf16 a[240:255], v[30:31], v[58:59], a[240:255]\nv_and_b32 v6, v6, v66\nv_mov_b64 v[8:9], 0\nQk 10,10,10\nv_and_b32 v11, v10, v67\nv_and_b32 v10, v10, v66\nv_mov_b64_e64 v[12:13], 0\n"
"v_mfma_f32_16x16x4_4b_bf16 v[80:95], v[30:31], v[58:59], v[80:95]\nQk 14,14,14\nv_and_b32 v15, v14, v67\nv_and_b32 v14, v14, v66\nv_mov_b64_e64 v[16:17], 0\nQk 18,18,18\nv_and_b32_e64 v19, v18, v67\nv_mfma_f32_16x16x4_4b_bf16 v[112:127], v[30:31], v[58:59], v[112:127]\n"
"v_and_b32 v18, v18, v66\nv_mov_b64 v[20:21], 0\nQk 22,22,22\nv_and_b32 v23, v22, v67\nv_and_b32 v22, v22, v66\nv_mov_b64_e64 v[24:25], 0\nv_mfma_f32_16x16x4_4b_bf16 v[144:159], v[30:31], v[58:59], v[144:159]\nQk 26,26,26\n"
"v_and_b32 v27, v26, v67\nv_and_b32 v26, v26, v66\nv_mov_b64_e64 v[28:29], 0\nv_accvgpr_read_b32 v48, a208\nv_accvgpr_read_b32 v49, a209\nv_accvgpr_read_b32 v50, a210\nv_accvgpr_read_b32 v51, a211\nv_accvgpr_read_b32 v52, a212\n"
"v_accvgpr_read_b32 v53, a213\nv_accvgpr_read_b32 v54, a214\nv_accvgpr_read_b32 v55, a215\nQk 48,48,49\nQk 49,50,51\nQk 50,52,53\nQk 51,54,55\nv_accvgpr_read_b32 v32, a216\n"
"v_accvgpr_read_b32 v33, a217\nQzvvv 52,48,20\nv_accvgpr_read_b32 v34, a218\nv_accvgpr_read_b32 v35, a219\nQzvvv 48,48,22\nv_accvgpr_read_b32 v36, a220\nv_accvgpr_read_b32 v37, a221\nv_accvgpr_read_b32 v38, a222\n"
"v_accvgpr_read_b32 v39, a223\nQk 32,32,33\nQk 33,34,35\nQk 34,36,37\nQk 35,38,39\nv_mfma_f32_16x16x4_4b_bf16 v[240:255], v[30:31], v[58:59], v[240:255]\nv_accvgpr_read_b32 v40, a224\nv_accvgpr_read_b32 v41, a225\n"
"v_accvgpr_read_b32 v42, a226\nv_accvgpr_read_b32 v43, a227\nv_accvgpr_read_b32 v44, a228\nv_accvgpr_read_b32 v45, a229\nQzvvv 36,32,20\nv_accvgpr_read_b32 v46, a230\nv_accvgpr_read_b32 v47, a231\nQzvvv 32,32,22\n"
"Qk 40,40,41\nQk 41,42,43\nQk 42,44,45\nQk 43,46,47\nQk 48,48,49\nQk 49,50,51\nQzvvv 44,40,24\nQk 50,52,53\n"
"Qk 51,54,55\nQzvvv 40,40,26\nv_accvgpr_write_b32 a88, v48\nv_accvgpr_write_b32 a89, v49\nv_accvgpr_write_b32 a90, v50\nv_accvgpr_write_b32 a91, v51\nv_mfma_f32_16x16x4_4b_bf16 v[96:111], v[30:31], v[58:59], v[96:111]\nv_accvgpr_read_b32 v48, a232\n"
"v_accvgpr_read_b32 v49, a233\nv_accvgpr_read_b32 v50, a234\nv_accvgpr_read_b32 v51, a235\nv_accvgpr_read_b32 v52, a236\nv_accvgpr_read_b32 v53, a237\nv_accvgpr_read_b32 v54, a238\nv_accvgpr_read_b32 v55, a239\nQk 48,48,49\n"
"Qk 49,50,51\nQk 50,52,53\nQk 51,54,55\nQk 32,32,33\nQk 33,34,35\nQzvvv 52,48,24\nQk 34,36,37\nQk 35,38,39\n"
"Qzvvv 48,48,26\nv_accvgpr_write_b32 a92, v32\nv_accvgpr_write_b32 a93, v33\nv_accvgpr_write_b32 a94, v34\nv_accvgpr_write_b32 a95, v35\nv_mfma_f32_16x16x4_4b_bf16 v[176:191], v[30:31], v[58:59], v[176:191]\nv_accvgpr_read_b32 v32, a240\nv_accvgpr_read_b32 v33, a241\n"
"v_accvgpr_read_b32 v34, a242\nv_accvgpr_read_b32 v35, a243\nv_accvgpr_read_b32 v36, a244\nv_accvgpr_read_b32 v37, a245\nv_accvgpr_read_b32 v38, a246\nv_accvgpr_read_b32 v39, a247\nQk 32,32,33\nQk 33,34,35\n"
"Qk 34,36,37\nQk 35,38,39\nQk 40,40,41\nQk 41,42,43\nQzvvv 36,32,24\nQk 42,44,45\nQk 43,46,47\nQzvvv 32,32,26\n"
"v_accvgpr_write_b32 a96, v40\nv_accvgpr_write_b32 a97, v41\nv_accvgpr_write_b32 a98, v42\nv_accvgpr_write_b32 a99, v43\nv_mfma_f32_16x16x4_4b_bf16 v[128:143], v[30:31], v[58:59], v[128:143]\nv_accvgpr_read_b32 v40, a248\nv_accvgpr_read_b32 v41, a249\nv_accvgpr_read_b32 v42, a250\n"
"v_accvgpr_read_b32 v43, a251\nv_accvgpr_read_b32 v44, a252\nv_accvgpr_read_b32 v45, a253\nv_accvgpr_read_b32 v46, a254\nv_accvgpr_read_b32 v47, a255\nQk 40,40,41\nQk 41,42,43\ns_mov_b64 vcc, -1\n"
"s_nop 0\nQk 42,44,45\nQk 43,46,47\nQk 48,48,49\ns_waitcnt vmcnt(15)\ns_nop 0\nQk 49,50,51\nQzvvv 44,40,24\n"
"Qk 50,52,53\ns_barrier\ns_nop 0\nQk 51,54,55\nQzvvv 40,40,26\nv_accvgpr_write_b32 a100, v48\nQta 112,61,0\nQta 114,61,512\n"
"Qda 208,63,33024\nQda 212,63,34048\nv_accvgpr_write_b32 a101, v49\nv_accvgpr_write_b32 a102, v50\nv_accvgpr_write_b32 a103, v51\nv_mfma_f32_16x16x4_4b_bf16 v[160:175], v[30:31], v[58:59], v[160:175]\nQk 80,80,81\nQk 81,82,83\n"
"Qk 82,84,85\nQk 83,86,87\nQk 88,88,89\nQda 216,63,35072\nQzvvv 84,80,0\nQda 220,63,36096\nQda 224,63,37120\nQzvvv 80,80,2\n"
"Qda 228,63,38144\nQk 89,90,91\nv_mfma_f32_16x16x4_4b_bf16 v[208:223], v[30:31], v[58:59], v[208:223]\nQk 90,92,93\nQk 91,94,95\nQk 112,112,113\nQk 113,114,115\nQk 114,116,117\n"
"Qk 115,118,119\nQzvvv 92,88,0\nQk 80,80,81\nQk 81,82,83\nQzvvv 88,88,2\nQda 232,63,39168\nQda 176,63,33536\nv_mfma_f32_16x16x4_4b_bf16 v[192:207], v[30:31], v[58:59], v[192:207]\n"
"Qta 116,61,4128\nQta 118,61,4640\nQk 82,84,85\nQk 83,86,87\nv_accvgpr_write_b32 a0, v80\nv_accvgpr_write_b32 a1, v81\nQzvvv 116,112,4\nv_accvgpr_write_b32 a2, v82\n"
"v_accvgpr_write_b32 a3, v83\nQzvvv 112,112,6\nQk 120,120,121\nQk 121,122,123\nv_mfma_f32_16x16x4_4b_bf16 v[224:239], v[30:31], v[58:59], v[224:239]\nQk 122,124,125\nQta 120,61,8\nQta 122,61,520\n"
"Qta 124,61,4136\nQta 126,61,4648\nQk 123,126,127\nQk 88,88,89\nQk 89,90,91\nQzvvv 124,120,4\nQk 90,92,93\nQk 91,94,95\n"
"Qzvvv 120,120,6\nv_accvgpr_write_b32 a4, v88\nv_accvgpr_write_b32 a5, v89\nv_accvgpr_write_b32 a6, v90\nv_accvgpr_write_b32 a7, v91\nQk 144,144,145\nQta 128,61,256\nQta 130,61,768\n"
"Qta 132,61,4384\nQta 134,61,4896\nQk 145,146,147\nQk 146,148,149\nQk 147,150,151\nQk 112,112,113\nQk 113,114,115\nQzvvv 148,144,8\n"
"Qk 114,116,117\nQk 115,118,119\nQzvvv 144,144,10\nv_accvgpr_write_b32 a16, v112\nv_accvgpr_write_b32 a17, v113\nv_accvgpr_write_b32 a18, v114\nQta 136,61,264\nQta 138,61,776\n"
"Qta 140,61,4392\nQta 142,61,4904\nv_accvgpr_write_b32 a19, v115\nQk 240,240,241\nQk 241,242,243\nQk 242,244,245\nQk 243,246,247\nQk 120,120,121\n"
"Qk 121,122,123\nQzvvv 244,240,20\nQk 122,124,125\nQk 123,126,127\nQzvvv 240,240,22\nv_accvgpr_write_b32 a20, v120\ns_waitcnt lgkmcnt(15)\ns_nop 0\n"
"v_accvgpr_write_b32 a21, v121\nv_accvgpr_write_b32 a22, v122\nv_accvgpr_write_b32 a23, v123\nQzvaa 80,112,208\nQk 248,248,249\nQk 249,250,251\nQk 250,252,253\nQzvaa 84,112,212\n"
"Qda 180,63,34560\nQk 251,254,255\nQk 144,144,145\nQk 145,146,147\nQzvvv 252,248,20\nQk 146,148,149\nQk 147,150,151\nQzvaa 88,112,216\n"
"Qda 184,63,35584\nv_accvgpr_write_b32 a32, v144\nQzvvv 248,248,22\nv_accvgpr_write_b32 a33, v145\nv_accvgpr_write_b32 a34, v146\nv_accvgpr_write_b32 a35, v147\nQk 152,152,153\nQzvaa 92,112,220\n"
"Qda 188,63,36608\nQk 153,154,155\nQk 154,156,157\nQk 155,158,159\nQk 240,240,241\nQk 241,242,243\nQzvvv 156,152,8\nQk 242,244,245\n"
"Qk 243,246,247\nQzvaa 112,112,224\nQda 192,63,37632\nv_accvgpr_write_b32 a80, v240\nQzvvv 152,152,10\nv_accvgpr_write_b32 a81, v241\nv_accvgpr_write_b32 a82, v242\nQzvaa 116,112,228\n"
"Qda 196,63,38656\nv_accvgpr_write_b32 a83, v243\nQk 96,96,97\nQk 97,98,99\nQk 98,100,101\nQk 99,102,103\nQk 248,248,249\nQzvaa 120,112,232\n"
"Qda 200,63,39680\nQk 249,250,251\nQzvvv 100,96,0\nQk 250,252,253\nQk 251,254,255\nQzvvv 96,96,2\nv_accvgpr_write_b32 a84, v248\nv_max3_f32 v80, |v80|, |v81|, |v82|\n"
"v_accvgpr_write_b32 a85, v249\nv_accvgpr_write_b32 a86, v250\nv_accvgpr_write_b32 a87, v251\nv_max3_f32 v84, |v84|, |v85|, |v86|\nQk 104,104,105\nQk 105,106,107\nQk 106,108,109\nv_max3_f32 v88, |v88|, |v89|, |v90|\n"
"Qk 107,110,111\nQk 152,152,153\nQk 153,154,155\nv_max3_f32 v92, |v92|, |v93|, |v94|\nQzvvv 108,104,0\nQk 154,156,157\nQk 155,158,159\nv_max3_f32 v112, |v112|, |v113|, |v114|\n"
"Qzvvv 104,104,2\nv_accvgpr_write_b32 a36, v152\nv_accvgpr_write_b32 a37, v153\nv_max3_f32 v116, |v116|, |v117|, |v118|\nv_accvgpr_write_b32 a38, v154\nv_accvgpr_write_b32 a39, v155\nQk 176,176,177\nv_max3_f32 v120, |v120|, |v121|, |v122|\n"
"Qk 177,178,179\nQk 178,180,181\nQk 179,182,183\nv_min3_u32 v56, v80, v84, v88\nQk 128,128,129\nQzvvv 180,176,12\nv_min3_u32 v56, v56, v92, v112\nQk 129,130,131\n"
"Qzvvv 176,176,14\nQk 130,132,133\nQk 131,134,135\nv_min3_u32 v56, v56, v116, v120\n.Lpg9:\ns_cbranch_execz .Ltr10\ns_nop 0\nQk 136,136,137\n"
"Qzvvv 132,128,4\nv_mov_b32_e64 v57, v56\nQk 137,138,139\nQzvvv 128,128,6\nQk 138,140,141\nQk 139,142,143\nv_permlane32_swap_b32_e64 v56, v57\nQk 176,176,177\n"
"Qk 177,178,179\nQzvvv 140,136,4\nv_max_f32_e64 v56, v56, v57\nQk 178,180,181\nQk 179,182,183\nQzvvv 136,136,6\nv_mov_b32_e64 v57, v56\nv_accvgpr_write_b32 a48, v176\n"
"v_accvgpr_write_b32 a49, v177\nv_accvgpr_write_b32 a50, v178\nv_permlane16_swap_b32_e64 v56, v57\nv_accvgpr_write_b32 a51, v179\nQk 184,184,185\nQk 185,186,187\nv_max_f32_e64 v56, v56, v57\nQk 186,188,189\n"
"Qk 187,190,191\nv_min_f32_dpp v56, v56, v56 row_ror:8 row_mask:0xf bank_mask:0xf\nQk 160,160,161\nQzvvv 188,184,12\nQk 161,162,163\nv_min_f32_dpp v56, v56, v56 row_ror:4 row_mask:0xf bank_mask:0xf\nQzvvv 184,184,14\nQk 162,164,165\n"
"Qk 163,166,167\nv_min_f32_dpp v56, v56, v56 row_ror:2 row_mask:0xf bank_mask:0xf\nQk 168,168,169\nQzvvv 164,160,8\nQk 169,170,171\nv_min_f32_dpp v56, v56, v56 row_ror:1 row_mask:0xf bank_mask:0xf\nQzvvv 160,160,10\nQk 170,172,173\n"
"Qk 171,174,175\nQk 184,184,185\nv_and_b32 v56, 0x7f800000, v56\nQk 185,186,187\nQzvvv 172,168,8\nQk 186,188,189\nv_min_u32 v56, 0x78000000, v56\nQk 187,190,191\n"
"Qzvvv 168,168,10\nv_accvgpr_write_b32 a52, v184\nv_add_u32 v56, 0x5400000, v56\nv_accvgpr_write_b32 a53, v185\nv_accvgpr_write_b32 a54, v186\nv_accvgpr_write_b32 a55, v187\nv_mov_b32_e64 v57, v56\nQk 208,208,209\n"
"Qk 209,210,211\nQk 210,212,213\nv_mov_b64_e64 v[58:59], v[56:57]\nQk 211,214,215\nv_mul_f32 v52, -v56, s72\nQk 192,192,193\nQzvvv 212,208,16\nQk 193,194,195\n"
"Qk 194,196,197\nQzvvv 208,208,18\nQk 195,198,199\nQk 200,200,201\nQk 201,202,203\nQzvvv 196,192,12\nQk 202,204,205\nQk 203,206,207\n"
"Qzvvv 192,192,14\nQk 216,216,217\nQk 208,208,209\nQk 209,210,211\nQzvvv 204,200,12\nQk 210,212,213\nQk 211,214,215\nQzvvv 200,200,14\n"
"v_accvgpr_write_b32 a64, v208\nv_accvgpr_write_b32 a65, v209\nv_accvgpr_write_b32 a66, v210\nv_accvgpr_write_b32 a67, v211\nQk 217,218,219\nQk 218,220,221\nQk 219,222,223\nQk 224,224,225\n"
"Qk 225,226,227\nQzvvv 220,216,16\nQk 226,228,229\nQk 227,230,231\nQzvvv 216,216,18\nQk 232,232,233\nQk 233,234,235\nQzvvv 228,224,16\n"
"Qk 234,236,237\nQk 235,238,239\nQzvvv 224,224,18\ns_nop 1\ns_nop 0\nQk 216,216,217\nQzvvv 236,232,16\nQk 217,218,219\n"
"Qk 218,220,221\nQk 219,222,223\nQzvvv 232,232,18\nv_accvgpr_write_b32 a68, v216\nv_accvgpr_write_b32 a69, v217\nv_accvgpr_write_b32 a70, v218\nv_accvgpr_write_b32 a71, v219\ns_mov_b32 s56, s20\n"
"s_and_b32 s57, s21, 0xffff\ns_mov_b32 s94, 0x7ffff000\ns_mul_i32 s68, %0, 40960\ns_lshl_b32 s69, s70, 10\ns_add_u32 s68, s68, s69\ns_add_u32 s68, s68, 4096\ns_add_u32 s94, s16, s68\ns_addc_u32 s95, s17, 0\n"
"s_and_b32 s95, s95, 0xffff\ns_mov_b32 s75, 37888\ns_mov_b32 s58, 4194304\ns_mov_b32 s67, 1\ns_branch .Lround2\n.Lhook:\n.Lpg10:\ns_cbranch_execz .Ltr10\n"
"s_mov_b64 exec, 0\ns_cmp_eq_u32 s70, 0\ns_cbranch_scc1 .Lpg12\ns_cmp_eq_u32 s70, 1\ns_cbranch_scc1 .Lpg13\ns_cmp_eq_u32 s70, 2\ns_cbranch_scc1 .Lpg14\ns_branch .Lpg15\n"
".Ltr6:\ns_mov_b64 exec, -1\ns_mov_b64 exec, 0\ns_cmp_eq_u32 s70, 0\ns_cbranch_scc1 .Lpg16\ns_cmp_eq_u32 s70, 1\ns_cbranch_scc1 .Lpg17\ns_cmp_eq_u32 s70, 2\n"
"s_cbranch_scc1 .Lpg18\ns_branch .Lpg19\n.Ltr8:\ns_mov_b64 exec, -1\ns_branch .Lhookret\n.Lf1_w:\ns_mov_b32 s97, 0\n.Lf1_l:\n"
"s_sleep 1\nbuffer_load_dword v78, v76, s[24:27], 0 offen sc1\ns_waitcnt vmcnt(0)\ns_add_u32 s97, s97, 1\ns_cmp_gt_u32 s97, 1048576\ns_cbranch_scc1 .Lf1_r\nv_sub_u32 v66, v78, v65\nv_cmp_gt_u32 vcc, 1, v66\n"
"s_and_b64 vcc, vcc, s[28:29]\ns_cbranch_scc1 .Lf1_l\ns_branch .Lf1_r\n.Lack_w:\ns_mov_b32 s97, 0\n.Lack_l:\ns_sleep 1\nv_bfe_u32 v66, v60, 4, 3\n"
"s_lshl_b32 s68, s19, 7\nv_lshlrev_b32 v66, 2, v66\ns_add_u32 s68, s68, 5242880\nbuffer_load_dword v78, v66, s[24:27], s68 offen offset:1024 sc1\ns_waitcnt vmcnt(0)\ns_add_u32 s97, s97, 1\ns_cmp_gt_u32 s97, 1048576\ns_cbranch_scc1 .Lack_ok\n"
"v_xor_b32 v78, s30, v78\nv_and_b32 v66, 0x0fffffff, v78\nv_cmp_ne_u32 vcc, 0, v66\ns_cmp_eq_u64 vcc, 0\ns_cbranch_scc0 .Lack_l\ns_branch .Lack_ok\n.Lsp0:\ns_sub_u32 s75, s77, s74\n"
"s_cmp_lt_u32 s75, 1024\ns_cbranch_scc0 .Lrp0\ns_memrealtime s[74:75]\ns_waitcnt lgkmcnt(0)\ns_branch .Lsp0\n.Lsa1:\nds_read_b32 v66, v74\ns_waitcnt lgkmcnt(0)\n"
"v_cmp_gt_u32 vcc, v75, v66\ns_cbranch_vccnz .Lsa1\ns_branch .Lra1\n.Lsp1:\ns_sub_u32 s75, s77, s74\ns_cmp_lt_u32 s75, 1024\ns_cbranch_scc0 .Lrp1\ns_memrealtime s[74:75]\n"
"s_waitcnt lgkmcnt(0)\ns_branch .Lsp1\n.Lsa2:\nds_read_b32 v66, v74\ns_waitcnt lgkmcnt(0)\nv_cmp_gt_u32 vcc, v75, v66\ns_cbranch_vccnz .Lsa2\ns_branch .Lra2\n"
".Lsp2:\ns_sub_u32 s75, s77, s74\ns_cmp_lt_u32 s75, 1024\ns_cbranch_scc0 .Lrp2\ns_memrealtime s[74:75]\ns_waitcnt lgkmcnt(0)\ns_branch .Lsp2\n.Lsa3:\n"
"ds_read_b32 v66, v74\ns_waitcnt lgkmcnt(0)\nv_cmp_gt_u32 vcc, v75, v66\ns_cbranch_vccnz .Lsa3\ns_branch .Lra3\n.Lsa4:\nds_read_b32 v66, v74\ns_waitcnt lgkmcnt(0)\n"
"v_cmp_gt_u32 vcc, v75, v66\ns_cbranch_vccnz .Lsa4\ns_branch .Lra4\n.Lsp4:\ns_sub_u32 s75, s77, s74\ns_cmp_lt_u32 s75, 1024\ns_cbranch_scc0 .Lrp4\ns_memrealtime s[74:75]\n"
"s_waitcnt lgkmcnt(0)\ns_branch .Lsp4\n.Lsa5:\nds_read_b32 v66, v74\ns_waitcnt lgkmcnt(0)\nv_cmp_gt_u32 vcc, v75, v66\ns_cbranch_vccnz .Lsa5\ns_branch .Lra5\n"
".Lsa30:\nds_read_b32 v66, v74\ns_waitcnt lgkmcnt(0)\nv_cmp_gt_u32 vcc, v75, v66\ns_cbranch_vccnz .Lsa30\ns_branch .Lra30\n.Lsa31:\nds_read_b32 v66, v74\n"
"s_waitcnt lgkmcnt(0)\nv_cmp_gt_u32 vcc, v75, v66\ns_cbranch_vccnz .Lsa31\ns_branch .Lra31\n.Lsa32:\nds_read_b32 v66, v74\ns_waitcnt lgkmcnt(0)\nv_cmp_gt_u32 vcc, v75, v66\n"
"s_cbranch_vccnz .Lsa32\ns_branch .Lra32\n.Lsa30x:\nds_read_b32 v66, v74\ns_waitcnt lgkmcnt(0)\nv_cmp_gt_u32 vcc, v75, v66\ns_cbranch_vccnz .Lsa30x\ns_branch .Lra30x\n"
".Lsa31x:\nds_read_b32 v66, v74\ns_waitcnt lgkmcnt(0)\nv_cmp_gt_u32 vcc, v75, v66\ns_cbranch_vccnz .Lsa31x\ns_branch .Lra31x\n.Lso40:\nds_read_b32 v66, v74\n"
"s_waitcnt lgkmcnt(0)\nv_cmp_gt_u32 vcc, v75, v66\ns_cbranch_vccnz .Lso40\ns_branch .Lro40\n.Lso180:\nds_read_b32 v66, v74\ns_waitcnt lgkmcnt(0)\nv_cmp_gt_u32 vcc, v75, v66\n"
"s_cbranch_vccnz .Lso180\ns_branch .Lro180\n.Lso320:\nds_read_b32 v66, v74\ns_waitcnt lgkmcnt(0)\nv_cmp_gt_u32 vcc, v75, v66\ns_cbranch_vccnz .Lso320\ns_branch .Lro320\n"
".Lso460:\nds_read_b32 v66, v74\ns_waitcnt lgkmcnt(0)\nv_cmp_gt_u32 vcc, v75, v66\ns_cbranch_vccnz .Lso460\ns_branch .Lro460\n.Lso600:\nds_read_b32 v66, v74\n"
"s_waitcnt lgkmcnt(0)\nv_cmp_gt_u32 vcc, v75, v66\ns_cbranch_vccnz .Lso600\ns_branch .Lro600\n.Lso740:\nds_read_b32 v66, v74\ns_waitcnt lgkmcnt(0)\nv_cmp_gt_u32 vcc, v75, v66\n"
"s_cbranch_vccnz .Lso740\ns_branch .Lro740\n.Lso880:\nds_read_b32 v66, v74\ns_waitcnt lgkmcnt(0)\nv_cmp_gt_u32 vcc, v75, v66\ns_cbranch_vccnz .Lso880\ns_branch .Lro880\n"
".Lso1020:\nds_read_b32 v66, v74\ns_waitcnt lgkmcnt(0)\nv_cmp_gt_u32 vcc, v75, v66\ns_cbranch_vccnz .Lso1020\ns_branch .Lro1020\n.Lso1160:\nds_read_b32 v66, v74\n"
"s_waitcnt lgkmcnt(0)\nv_cmp_gt_u32 vcc, v75, v66\ns_cbranch_vccnz .Lso1160\ns_branch .Lro1160\n.Lso1300:\nds_read_b32 v66, v74\ns_waitcnt lgkmcnt(0)\nv_cmp_gt_u32 vcc, v75, v66\n"
"s_cbranch_vccnz .Lso1300\ns_branch .Lro1300\n.Lso1440:\nds_read_b32 v66, v74\ns_waitcnt lgkmcnt(0)\nv_cmp_gt_u32 vcc, v75, v66\ns_cbranch_vccnz .Lso1440\ns_branch .Lro1440\n"
".Lso1455:\nds_read_b32 v66, v74\ns_waitcnt lgkmcnt(0)\nv_cmp_gt_u32 vcc, v75, v66\ns_cbranch_vccnz .Lso1455\ns_branch .Lro1455\n.Lso1525r0:\nds_read_b32 v66, v74\n"
"s_waitcnt lgkmcnt(0)\nv_cmp_gt_u32 vcc, v75, v66\ns_cbranch_vccnz .Lso1525r0\ns_branch .Lro1525r0\n.Lso1595r0:\nds_read_b32 v66, v74\ns_waitcnt lgkmcnt(0)\nv_cmp_gt_u32 vcc, v75, v66\n"
"s_cbranch_vccnz .Lso1595r0\ns_branch .Lro1595r0\n.Lso1665r0:\nds_read_b32 v66, v74\ns_waitcnt lgkmcnt(0)\nv_cmp_gt_u32 vcc, v75, v66\ns_cbranch_vccnz .Lso1665r0\ns_branch .Lro1665r0\n"
".Lso1735r0:\nds_read_b32 v66, v74\ns_waitcnt lgkmcnt(0)\nv_cmp_gt_u32 vcc, v75, v66\ns_cbranch_vccnz .Lso1735r0\ns_branch .Lro1735r0\n.Lso1805r0:\nds_read_b32 v66, v74\n"
"s_waitcnt lgkmcnt(0)\nv_cmp_gt_u32 vcc, v75, v66\ns_cbranch_vccnz .Lso1805r0\ns_branch .Lro1805r0\n.Lso1878r0:\nds_read_b32 v66, v74\ns_waitcnt lgkmcnt(0)\nv_cmp_gt_u32 vcc, v75, v66\n"
"s_cbranch_vccnz .Lso1878r0\ns_branch .Lro1878r0\n.Lso1920r0:\nds_read_b32 v66, v74\ns_waitcnt lgkmcnt(0)\nv_cmp_gt_u32 vcc, v75, v66\ns_cbranch_vccnz .Lso1920r0\ns_branch .Lro1920r0\n"
".Lso1960r0:\nds_read_b32 v66, v74\ns_waitcnt lgkmcnt(0)\nv_cmp_gt_u32 vcc, v75, v66\ns_cbranch_vccnz .Lso1960r0\ns_branch .Lro1960r0\n.Lso2046r0:\nds_read_b32 v66, v74\n"
"s_waitcnt lgkmcnt(0)\nv_cmp_gt_u32 vcc, v75, v66\ns_cbranch_vccnz .Lso2046r0\ns_branch .Lro2046r0\n.Lso2088r0:\nds_read_b32 v66, v74\ns_waitcnt lgkmcnt(0)\nv_cmp_gt_u32 vcc, v75, v66\n"
"s_cbranch_vccnz .Lso2088r0\ns_branch .Lro2088r0\n.Lso2172r0:\nds_read_b32 v66, v74\ns_waitcnt lgkmcnt(0)\nv_cmp_gt_u32 vcc, v75, v66\ns_cbranch_vccnz .Lso2172r0\ns_branch .Lro2172r0\n"
".Lso2222r0:\nds_read_b32 v66, v74\ns_waitcnt lgkmcnt(0)\nv_cmp_gt_u32 vcc, v75, v66\ns_cbranch_vccnz .Lso2222r0\ns_branch .Lro2222r0\n.Lso1525r1:\nds_read_b32 v66, v74\n"
"s_waitcnt lgkmcnt(0)\nv_cmp_gt_u32 vcc, v75, v66\ns_cbranch_vccnz .Lso1525r1\ns_branch .Lro1525r1\n.Lso1595r1:\nds_read_b32 v66, v74\ns_waitcnt lgkmcnt(0)\nv_cmp_gt_u32 vcc, v75, v66\n"
"s_cbranch_vccnz .Lso1595r1\ns_branch .Lro1595r1\n.Lso1665r1:\nds_read_b32 v66, v74\ns_waitcnt lgkmcnt(0)\nv_cmp_gt_u32 vcc, v75, v66\ns_cbranch_vccnz .Lso1665r1\ns_branch .Lro1665r1\n"
".Lso1735r1:\nds_read_b32 v66, v74\ns_waitcnt lgkmcnt(0)\nv_cmp_gt_u32 vcc, v75, v66\ns_cbranch_vccnz .Lso1735r1\ns_branch .Lro1735r1\n.Lso1805r1:\nds_read_b32 v66, v74\n"
"s_waitcnt lgkmcnt(0)\nv_cmp_gt_u32 vcc, v75, v66\ns_cbranch_vccnz .Lso1805r1\ns_branch .Lro1805r1\n.Lso1840r1:\nds_read_b32 v66, v74\ns_waitcnt lgkmcnt(0)\nv_cmp_gt_u32 vcc, v75, v66\n"
"s_cbranch_vccnz .Lso1840r1\ns_branch .Lro1840r1\n.Lso1878r1:\nds_read_b32 v66, v74\ns_waitcnt lgkmcnt(0)\nv_cmp_gt_u32 vcc, v75, v66\ns_cbranch_vccnz .Lso1878r1\ns_branch .Lro1878r1\n"
".Lso1920r1:\nds_read_b32 v66, v74\ns_waitcnt lgkmcnt(0)\nv_cmp_gt_u32 vcc, v75, v66\ns_cbranch_vccnz .Lso1920r1\ns_branch .Lro1920r1\n.Lso1945r1:\nds_read_b32 v66, v74\n"
"s_waitcnt lgkmcnt(0)\nv_cmp_gt_u32 vcc, v75, v66\ns_cbranch_vccnz .Lso1945r1\ns_branch .Lro1945r1\n.Lso2004r1:\nds_read_b32 v66, v74\ns_waitcnt lgkmcnt(0)\nv_cmp_gt_u32 vcc, v75, v66\n"
"s_cbranch_vccnz .Lso2004r1\ns_branch .Lro2004r1\n.Lso2045r1:\nds_read_b32 v66, v74\ns_waitcnt lgkmcnt(0)\nv_cmp_gt_u32 vcc, v75, v66\ns_cbranch_vccnz .Lso2045r1\ns_branch .Lro2045r1\n"
".Lso2130r1:\nds_read_b32 v66, v74\ns_waitcnt lgkmcnt(0)\nv_cmp_gt_u32 vcc, v75, v66\ns_cbranch_vccnz .Lso2130r1\ns_branch .Lro2130r1\n.Lso2172r1:\nds_read_b32 v66, v74\n"
"s_waitcnt lgkmcnt(0)\nv_cmp_gt_u32 vcc, v75, v66\ns_cbranch_vccnz .Lso2172r1\ns_branch .Lro2172r1\n.Lso2225r1:\nds_read_b32 v66, v74\ns_waitcnt lgkmcnt(0)\nv_cmp_gt_u32 vcc, v75, v66\n"
"s_cbranch_vccnz .Lso2225r1\ns_branch .Lro2225r1\n.Ltail1:\n.Lpg11:\ns_cbranch_execz .Ltr10\nQcvaa 0,128,32,56\nv_add_u32 v75, 1, v75\ns_lshr_b32 s88, s19, 2\n"
"Qmvaa 0,132,36\ns_waitcnt vmcnt(0)\nds_write_b32 v73, v75\nQmvaa 0,136,40\nQe 8\nQmvaa 0,140,44\nQe 9\nQcvaa 4,124,44,56\n"
"Qe 10\nQmvaa 4,120,40\nQe 11\nQmvaa 4,116,36\nQe 12\nQmvaa 4,112,32\ns_waitcnt lgkmcnt(1)\nQk 40,8,9\n"
"Qmvva 96,44,144\nQe 13\nQmvva 96,36,148\nQe 14\nQmvva 100,44,152\nQe 15\nQmvva 100,36,156\nQk 41,10,11\n"
"Qk 42,12,13\nQmvva 104,44,160\nQk 43,14,15\ns_lshl_b32 s88, s88, 4\nQmvva 104,36,164\nQe 0\nQmvva 108,44,168\nQe 1\n"
"Qmvva 108,36,172\nQe 2\nQmvva 140,48,168\nQe 3\nQmvva 140,40,172\ns_add_u32 s88, s88, s93\ns_mul_i32 s68, s88, 163840\nQmvva 136,48,160\n"
"ds_read_b32 v66, v74\ns_and_b32 s69, s19, 3\nQmvva 136,40,164\ns_mul_i32 s69, s69, 320\ns_add_u32 s68, s68, s69\nQmvva 132,48,152\ns_add_u32 s52, s16, s68\ns_addc_u32 s53, s17, 0\n"
"Qmvva 132,40,156\ns_and_b32 s53, s53, 0xffff\ns_sub_u32 s54, 5246976, s68\nQmvva 128,48,144\ns_and_b32 s87, s19, 3\ns_mul_i32 s87, s87, 327680\nQmvva 128,40,148\ns_waitcnt lgkmcnt(0)\n"
"v_cmp_gt_u32 vcc, v75, v66\nv_smfmac_f32_16x16x64_bf16 a[176:179], a[204:207], v[36:43], v77\ns_cbranch_vccnz .Lsa30x\n.Lra30x:\nQcvaa 8,112,48,56\ns_add_u32 s87, s87, 1310720\ns_mul_i32 s69, s92, 10240\nQmvaa 8,116,52\n"
"v_bfe_u32 v78, v60, 5, 3\nv_mul_u32_u24 v78, 1280, v78\nQmvaa 8,120,56\nv_and_b32 v67, 16, v60\nv_add3_u32 v67, v67, v78, s69\nQmvaa 8,124,60\ns_and_b32 s69, s19, 1\nv_lshrrev_b32 v78, 8, v60\n"
"Qcvaa 12,140,60,56\ns_lshl_b32 s68, s93, 1\nv_xor_b32 v78, s68, v78\nQmvaa 12,136,56\nv_lshl_add_u32 v78, v78, 5, v67\ns_lshr_b32 s68, 128, s69\nQmvaa 12,132,52\nv_add_u32 v61, s68, v78\n"
"Qk 46,0,1\nQmvaa 12,128,48\ns_add_u32 m0, s96, 0\ns_add_u32 s95, s87, 4096\nQk 47,2,3\nQcvaa 0,128,64,56\nbuffer_load_dwordx4 v61, s[52:55], s95 offen lds\nQmvaa 0,132,68\n"
"Qe 4\nQmvaa 0,136,72\nQe 5\nQe 6\nQe 7\nQmvaa 0,140,76\ns_add_u32 s95, s87, 23552\nQk 44,4,5\n"
"Qk 45,6,7\nQcvaa 4,124,76,56\nbuffer_load_dwordx4 v61, s[52:55], s95 offen offset:1024 lds\nQmvaa 4,120,72\nQe 8\nQmvaa 4,116,68\nQe 9\nQmvaa 4,112,64\n"
"s_add_u32 m0, s96, 8192\ns_add_u32 s95, s87, 45056\ns_mul_i32 s68, s69, 192\nQmvva 160,52,144\nbuffer_load_dwordx4 v61, s[52:55], s95 offen lds\nQmvva 160,44,148\nQe 10\nQmvva 164,52,152\n"
"Qe 11\nQmvva 164,44,156\ns_add_u32 s95, s87, 64512\nv_add_u32 v63, s68, v78\nQmvva 168,52,160\nbuffer_load_dwordx4 v61, s[52:55], s95 offen offset:1024 lds\nQmvva 168,44,164\nQe 12\n"
"Qmvva 172,52,168\nQe 13\nQe 14\nQe 15\nv_bfe_u32 v78, v60, 8, 1\nv_xor_b32 v78, s93, v78\nv_lshl_add_u32 v67, v78, 5, v67\nv_lshrrev_b32 v78, 9, v60\n"
"v_mul_u32_u24 v78, 20480, v78\ns_xor_b32 s69, s69, 1\ns_lshl_b32 s69, s69, 8\nv_add3_u32 v62, v78, v67, s69\nQmvva 172,44,172\ns_add_u32 m0, s96, 16384\ns_add_u32 s95, s87, 4096\nQk 48,8,9\n"
"Qk 49,10,11\nQk 50,12,13\nQk 51,14,15\nQmvva 204,20,168\nbuffer_load_dwordx4 v62, s[52:55], s95 offen lds\nQmvva 204,48,172\nQe 0\nQmvva 200,20,160\n"
"Qe 1\nQmvva 200,48,164\ns_add_u32 s95, s87, 44032\nQk 18,0,1\nQmvva 196,20,152\nbuffer_load_dwordx4 v62, s[52:55], s95 offen offset:1024 lds\nQmvva 196,48,156\nQe 2\n"
"Qmvva 192,20,144\nQe 3\nQmvva 192,48,148\nQk 19,2,3\ns_andn2_b32 s68, s71, 786432\nv_smfmac_f32_16x16x64_bf16 a[180:183], a[204:207], v[44:51], v77\ns_add_u32 m0, s96, 24576\ns_add_u32 s95, s87, 4096\n"
"Qcvaa 8,112,80,56\nbuffer_load_dwordx4 v63, s[52:55], s95 offen lds\nQmvaa 8,116,84\nQe 4\nQmvaa 8,120,88\ns_add_u32 s95, s87, 23552\nQmvaa 8,124,92\nbuffer_load_dwordx4 v63, s[52:55], s95 offen offset:1024 lds\n"
"Qcvaa 12,140,92,56\nQe 5\nQmvaa 12,136,88\nQe 6\nQmvaa 12,132,84\ns_add_u32 m0, s96, 32768\ns_add_u32 s95, s87, 45056\nQk 16,4,5\n"
"Qmvaa 12,128,80\nbuffer_load_dwordx4 v63, s[52:55], s95 offen lds\nQcvaa 0,128,96,56\nQda 128,68,34048\nQmvaa 0,132,100\nQda 132,68,34560\nQmvaa 0,136,104\nQda 136,68,34304\n"
"Qe 7\nQmvaa 0,140,108\nQda 140,68,34816\ns_add_u32 s95, s87, 64512\nQk 17,6,7\nQcvaa 4,124,108,56\nQda 124,68,33792\nbuffer_load_dwordx4 v63, s[52:55], s95 offen offset:1024 lds\n"
"Qmvaa 4,120,104\nQda 120,68,33280\nQmvaa 4,116,100\nQda 116,68,33536\nbuffer_load_dword v78, v76, s[24:27], 0 offen sc1\nQmvaa 4,112,96\nQda 112,68,33024\ns_add_u32 m0, s96, 40960\n"
"s_mov_b32 s95, 4096\nQmvva 224,24,144\nbuffer_load_dwordx4 v61, s[52:55], s95 offen lds\nQmvva 224,16,148\nQe 8\nQmvva 228,24,152\nQe 9\nQmvva 228,16,156\n"
"Qe 10\nQmvva 232,24,160\ns_mov_b32 s95, 23552\nQk 20,8,9\nQmvva 232,16,164\nbuffer_load_dwordx4 v61, s[52:55], s95 offen offset:1024 lds\nQmvva 236,24,168\nQe 11\n"
"Qmvva 236,16,172\ns_waitcnt lgkmcnt(0)\nQk 21,10,11\nQcvaa 8,112,0,56\nQe 12\nQmvaa 8,116,4\nv_subrev_u32 v68, 66048, v68\ns_add_u32 m0, s96, 49152\n"
"s_mov_b32 s95, 45056\nQmvaa 8,120,8\nbuffer_load_dwordx4 v61, s[52:55], s95 offen lds\nQe 13\nQe 14\nQe 15\nQmvaa 8,124,12\nQk 22,12,13\n"
"Qk 23,14,15\n.Lpg12:\ns_cbranch_execz .Ltr6\nQcvaa 12,140,12,56\nQe 0\nQmvaa 12,136,8\nQe 1\nQmvaa 12,132,4\n"
"Qe 2\nQmvaa 12,128,0\nQe 3\nv_smfmac_f32_16x16x64_bf16 a[184:187], a[204:207], v[16:23], v77\ns_mov_b32 s95, 64512\nQmava 208,28,144\nbuffer_load_dwordx4 v61, s[52:55], s95 offen offset:1024 lds\nQmava 208,20,148\n"
"Qe 4\nQmava 212,28,152\nQe 5\nQmava 212,20,156\nQe 6\nQmava 216,28,160\ns_add_u32 m0, s96, 57344\ns_mov_b32 s95, 4096\n"
"Qk 26,0,1\nQmava 216,20,164\nbuffer_load_dwordx4 v62, s[52:55], s95 offen lds\nQmava 220,28,168\nQe 7\nQmava 220,20,172\nQk 27,2,3\nQk 24,4,5\n"
"Qk 25,6,7\nQmava 252,32,168\nQta 168,71,768\nQta 170,71,1792\nQmava 252,24,172\nQe 8\nQmava 248,32,160\nQta 172,71,33792\n"
"Qta 174,71,34816\ns_mov_b32 s95, 44032\nQmava 248,24,164\nbuffer_load_dwordx4 v62, s[52:55], s95 offen offset:1024 lds\nQmava 244,32,152\nQta 160,71,512\nQta 162,71,1536\nQmava 244,24,156\n"
"Qe 9\nQmava 240,32,144\nQta 164,71,33536\nQta 166,71,34560\ns_add_u32 m0, s96, 132096\ns_mov_b32 s95, 4096\nQmava 240,24,148\nbuffer_load_dwordx4 v63, s[52:55], s95 offen lds\n"
"Qcvaa 0,112,16,56\nQta 152,71,256\nQta 154,71,1280\nQmvaa 0,116,20\nQe 10\nQmvaa 0,120,24\nQta 156,71,33280\nQta 158,71,34304\n"
"Qmvaa 0,124,28\nQe 11\nQcvaa 4,140,28,56\nQta 144,71,0\nQta 146,71,1024\ns_mov_b32 s95, 23552\nQmvaa 4,136,24\nbuffer_load_dwordx4 v63, s[52:55], s95 offen offset:1024 lds\n"
"Qmvaa 4,132,20\nQta 148,71,33024\nQta 150,71,34048\nQmvaa 4,128,16\nQk 28,8,9\nQk 29,10,11\nQcvaa 8,128,32,56\nv_add_u32 v75, 1, v75\n"
"Qmvaa 8,132,36\nds_write_b32 v73, v75\nQmvaa 8,136,40\nQe 12\nQe 13\nQe 14\nQe 15\nQmvaa 8,140,44\n"
"Qk 30,12,13\nQk 31,14,15\nQcvaa 12,124,44,56\nQe 0\nQmvaa 12,120,40\nQe 1\nQmvaa 12,116,36\nQe 2\n"
"Qmvaa 12,112,32\ns_waitcnt lgkmcnt(1)\nQk 32,0,1\nv_smfmac_f32_16x16x64_bf16 a[188:191], a[204:207], v[24:31], v77\nQmvva 80,36,144\nQe 3\nQmvva 80,28,148\nQe 4\n"
"Qmvva 84,36,152\nQe 5\nQmvva 84,28,156\nQe 6\nQmvva 88,36,160\nQe 7\nQmvva 88,28,164\nQk 33,2,3\n"
"Qk 34,4,5\nQmvva 92,36,168\nQk 35,6,7\nQmvva 92,28,172\nQe 8\nQmvva 124,40,168\nQe 9\nQmvva 124,32,172\n"
"ds_read_b32 v66, v74\nQk 38,8,9\nQmvva 120,40,160\nQe 10\nQmvva 120,32,164\nQe 11\nQmvva 116,40,152\nQk 39,10,11\n"
"Qmvva 116,32,156\nQe 12\nQmvva 112,40,144\ns_waitcnt lgkmcnt(0)\nv_cmp_gt_u32 vcc, v75, v66\nQmvva 112,32,148\ns_cbranch_vccnz .Lsa31x\n.Lra31x:\n"
"Qcvaa 0,112,48,56\nQe 13\nQmvaa 0,116,52\nQe 14\nQmvaa 0,120,56\nQe 15\nQmvaa 0,124,60\ns_add_u32 m0, s96, 140288\n"
"s_mov_b32 s95, 45056\nQk 36,12,13\nQcvaa 4,140,60,56\nbuffer_load_dwordx4 v63, s[52:55], s95 offen lds\nQmvaa 4,136,56\nQk 37,14,15\nQmvaa 4,132,52\ns_add_u32 s48, s20, s68\n"
"s_addc_u32 s49, s21, 0\nQmvaa 4,128,48\ns_mov_b32 s95, 64512\ns_and_b32 s49, s49, 0xffff\nQcvaa 8,128,64,56\nbuffer_load_dwordx4 v63, s[52:55], s95 offen offset:1024 lds\nQmvaa 8,132,68\nQe 0\n"
"Qmvaa 8,136,72\nQe 1\nQmvaa 8,140,76\ns_add_u32 m0, s96, 148480\ns_add_u32 s95, s87, 86016\nQk 40,0,1\nQcvaa 12,124,76,56\nbuffer_load_dwordx4 v61, s[52:55], s95 offen lds\n"
"Qmvaa 12,120,72\nQe 2\nQmvaa 12,116,68\nQe 3\nQmvaa 12,112,64\nQe 4\nv_smfmac_f32_16x16x64_bf16 a[192:195], a[204:207], v[32:39], v77\ns_add_u32 s95, s87, 105472\n"
"Qmvva 144,44,144\nbuffer_load_dwordx4 v61, s[52:55], s95 offen offset:1024 lds\nQmvva 144,36,148\nQe 5\nQmvva 148,44,152\ns_add_u32 m0, s96, 65536\ns_add_u32 s95, s87, 126976\nQk 41,2,3\n"
"Qmvva 148,36,156\nbuffer_load_dwordx4 v61, s[52:55], s95 offen lds\nQmvva 152,44,160\nQe 6\nQmvva 152,36,164\nQe 7\nQmvva 156,44,168\nQk 42,4,5\n"
"Qk 43,6,7\nQmvva 156,36,172\nQe 8\nQmvva 188,48,168\ns_add_u32 s95, s87, 146432\ns_sub_u32 s50, 4194304, s68\nQmvva 188,40,172\nbuffer_load_dwordx4 v61, s[52:55], s95 offen offset:1024 lds\n"
"Qmvva 184,48,160\nQe 9\nQmvva 184,40,164\nQe 10\nQmvva 180,48,152\nQe 11\nQmvva 180,40,156\nQk 46,8,9\n"
"Qk 47,10,11\nQmvva 176,48,144\ns_add_u32 m0, s96, 73728\ns_add_u32 s95, s87, 86016\ns_add_u32 s71, s71, 1048576\nQmvva 176,40,148\nbuffer_load_dwordx4 v62, s[52:55], s95 offen lds\nQcvaa 0,112,80,56\n"
"Qe 12\nQmvaa 0,116,84\nQe 13\nQmvaa 0,120,88\ns_add_u32 s95, s87, 125952\nQk 44,12,13\nQmvaa 0,124,92\nbuffer_load_dwordx4 v62, s[52:55], s95 offen offset:1024 lds\n"
"Qcvaa 4,140,92,56\nQe 14\nQmvaa 4,136,88\nQe 15\nQmvaa 4,132,84\ns_waitcnt vmcnt(16)\nv_sub_u32 v66, v78, v65\nv_cmp_gt_u32 vcc, 1, v66\n"
"s_and_b64 vcc, vcc, s[28:29]\ns_cbranch_scc1 .Lf1_w\n.Lf1_r:\nQmvaa 4,128,80\ns_add_u32 m0, s96, 81920\ns_add_u32 s95, s87, 86016\nQk 45,14,15\nQcvaa 8,128,96,56\n"
"buffer_load_dwordx4 v63, s[52:55], s95 offen lds\nQmvaa 8,132,100\nQe 0\nQmvaa 8,136,104\nQe 1\nQmvaa 8,140,108\nQe 2\nQcvaa 12,124,108,56\n"
"Qe 3\nQmvaa 12,120,104\nQk 48,0,1\nQk 49,2,3\nQmvaa 12,116,100\nQe 4\nQmvaa 12,112,96\nQe 5\n"
"v_smfmac_f32_16x16x64_bf16 a[196:199], a[204:207], v[40:47], v77\ns_add_u32 s95, s87, 105472\nQmvva 208,16,144\nQta 112,71,33152\nbuffer_load_dwordx4 v63, s[52:55], s95 offen offset:1024 lds\nQmvva 208,44,148\nQta 114,71,34176\nQk 50,4,5\n"
"Qmvva 212,16,152\nQta 116,71,33408\ns_add_u32 m0, s96, 90112\ns_add_u32 s95, s87, 126976\nQmvva 212,44,156\nQta 118,71,34432\nbuffer_load_dwordx4 v63, s[52:55], s95 offen lds\nQmvva 216,16,160\n"
"Qta 120,71,33664\nQmvva 216,44,164\nQta 122,71,34688\nQmvva 220,16,168\nQta 124,71,33920\ns_add_u32 s95, s87, 146432\nQmvva 220,44,172\nQta 126,71,34944\n"
"buffer_load_dwordx4 v63, s[52:55], s95 offen offset:1024 lds\nQe 6\nQe 7\ns_nop 0\nQk 51,6,7\nQmvva 240,20,144\nQe 8\nQmvva 240,48,148\n"
"Qe 9\nQmvva 244,20,152\nQe 10\nQmvva 244,48,156\nQe 11\nQmvva 248,20,160\nQk 54,8,9\nQk 55,10,11\n"
"Qmvva 248,48,164\nQe 12\nQmvva 252,20,168\nQe 13\nQmvva 252,48,172\nQe 14\nQk 52,12,13\nQe 15\n"
"s_nop 0\nQk 53,14,15\ns_nop 1\nv_smfmac_f32_16x16x64_bf16 a[200:203], a[204:207], v[48:55], v77\nQmava 224,24,144\nv_accvgpr_read_b32 v0, a176\nv_accvgpr_read_b32 v1, a177\nQmava 224,52,148\n"
"v_accvgpr_read_b32 v6, a178\nv_accvgpr_read_b32 v7, a179\nQmava 228,24,152\nv_accvgpr_read_b32 v8, a180\nv_accvgpr_read_b32 v9, a181\nQmava 228,52,156\nv_accvgpr_read_b32 v14, a182\nv_accvgpr_read_b32 v15, a183\n"
"Qmava 232,24,160\nv_accvgpr_read_b32 v16, a184\nv_accvgpr_read_b32 v17, a185\nQmava 232,52,164\nv_accvgpr_read_b32 v22, a186\nv_accvgpr_read_b32 v23, a187\nQmava 236,24,168\nv_accvgpr_read_b32 v2, a190\n"
"v_accvgpr_read_b32 v3, a191\nQmava 236,52,172\ns_waitcnt lgkmcnt(0)\nv_accvgpr_read_b32 v24, a188\nQmvva 96,28,112\nv_accvgpr_read_b32 v25, a189\nv_accvgpr_read_b32 v4, a192\nQmvva 100,28,116\n"
"v_accvgpr_read_b32 v5, a193\nv_accvgpr_read_b32 v10, a194\nQmvva 104,28,120\nv_accvgpr_read_b32 v11, a195\nv_accvgpr_read_b32 v12, a196\nQmvva 108,28,124\nv_accvgpr_read_b32 v13, a197\nv_accvgpr_read_b32 v18, a198\n"
"Qmvva 128,32,112\nv_accvgpr_read_b32 v19, a199\nv_accvgpr_read_b32 v20, a200\nQmvva 132,32,116\nv_accvgpr_read_b32 v21, a201\nv_accvgpr_read_b32 v26, a202\nQmvva 136,32,120\nv_accvgpr_read_b32 v27, a203\n"
"Qa 8,8,9\nQmvva 140,32,124\nQa 10,10,11\nQa 8,8,10\nQmvva 160,36,112\nQa 16,16,17\nQa 18,18,19\nQmvva 164,36,116\n"
"Qa 16,16,18\nQa 4,4,5\nQmvva 168,36,120\nQa 6,6,7\nQa 4,4,6\nQmvva 172,36,124\nQa 12,12,13\nQa 14,14,15\n"
"Qmvva 192,40,112\nQa 12,12,14\nQa 0,0,1\nQmvva 196,40,116\nQa 2,2,3\nQa 0,0,2\nQmvva 200,40,120\nQa 24,24,25\n"
"Qa 26,26,27\nQmvva 204,40,124\nQa 24,24,26\nQa 20,20,21\nQmvva 224,44,112\nQa 22,22,23\nQa 20,20,22\nQmvva 228,44,116\n"
"v_max3_u32 v9, v8, v16, v4\nv_max3_u32 v9, v9, v12, v0\nQmvva 232,44,120\nv_max3_u32 v9, v9, v24, v20\nv_min3_u32 v11, v8, v16, v4\nQmvva 236,44,124\nv_min3_u32 v11, v11, v12, v0\nv_min3_u32 v11, v11, v24, v20\n"
"Qmava 208,48,112\nv_cmp_lt_u32 vcc, 0x4e800000, v9\ns_mov_b64 s[74:75], vcc\nQmava 212,48,116\nv_cmp_gt_u32 vcc, 0x35800000, v11\ns_or_b64 s[74:75], s[74:75], vcc\nQmava 216,48,120\nv_mov_b32 v30, s73\n"
"v_mov_b32 v31, 0\nQmava 220,48,124\nv_mov_b32 v58, 0xb9800000\nv_mov_b32 v59, 0\nQmava 240,52,112\nv_and_b32 v66, 16, v60\nv_mov_b32 v67, 0xffff\nQmava 244,52,116\n"
"v_lshlrev_b32 v66, v66, v67\ns_mov_b32 vcc_lo, 0x00c0000c\nQmava 248,52,120\ns_mov_b32 vcc_hi, 0xc0000c00\nv_cndmask_b32 v67, 0, v66, vcc\nQmava 252,52,124\ns_lshr_b64 vcc, vcc, 2\nv_cndmask_b32 v66, 0, v66, vcc\n"
"s_lshl_b32 s68, s93, 12\ns_add_u32 s68, s68, %4\nv_lshrrev_b32 v69, 9, v60\nv_lshlrev_b32 v69, 11, v69\nv_bfe_u32 v70, v60, 1, 8\nv_add3_u32 v68, v69, v70, s68\nv_add_u32 v69, 65536, v68\nv_add_u32 v70, 131072, v68\n"
"s_cmp_lg_u64 s[74:75], 0\ns_cbranch_scc1 .Lfb\n.Lfbret1:\nv_rcp_f32 v2, v0\nv_rcp_f32_e64 v6, v4\nv_mfma_f32_16x16x4_4b_bf16 v[80:95], v[30:31], v[58:59], v[80:95]\nv_rcp_f32 v10, v8\nv_rcp_f32 v14, v12\n"
"v_rcp_f32 v18, v16\nv_rcp_f32 v22, v20\nv_rcp_f32 v26, v24\nv_mov_b64 v[0:1], 0\nv_mfma_f32_16x16x4_4b_bf16 v[112:127], v[30:31], v[58:59], v[112:127]\nQk 2,2,2\nv_and_b32 v3, v2, v67\nv_and_b32 v2, v2, v66\n"
"v_mov_b64_e64 v[4:5], 0\nQk 6,6,6\nv_and_b32_e64 v7, v6, v67\nv_mfma_f32_16x16x4_4b_bf16 v[144:159], v[30:31], v[58:59], v[144:159]\nv_and_b32 v6, v6, v66\nv_mov_b64 v[8:9], 0\nQk 10,10,10\nv_and_b32 v11, v10, v67\n"
"v_and_b32 v10, v10, v66\nv_mov_b64_e64 v[12:13], 0\nv_mfma_f32_16x16x4_4b_bf16 v[176:191], v[30:31], v[58:59], v[176:191]\nQk 14,14,14\nv_and_b32 v15, v14, v67\nv_and_b32 v14, v14, v66\nv_mov_b64_e64 v[16:17], 0\nQk 18,18,18\n"
"v_and_b32_e64 v19, v18, v67\nv_mfma_f32_16x16x4_4b_bf16 v[208:223], v[30:31], v[58:59], v[208:223]\nv_and_b32 v18, v18, v66\nv_mov_b64 v[20:21], 0\nQk 22,22,22\nv_and_b32 v23, v22, v67\nv_and_b32 v22, v22, v66\nv_mov_b64_e64 v[24:25], 0\n"
"v_mfma_f32_16x16x4_4b_bf16 v[240:255], v[30:31], v[58:59], v[240:255]\nQk 26,26,26\nv_and_b32 v27, v26, v67\nv_and_b32 v26, v26, v66\nv_mov_b64_e64 v[28:29], 0\nQk 80,80,81\nQk 81,82,83\nQk 82,84,85\n"
"Qk 83,86,87\nQk 112,112,113\nQk 113,114,115\nQzvvv 84,80,0\nQk 114,116,117\nQk 115,118,119\nQzvvv 80,80,2\nQk 144,144,145\n"
"Qk 145,146,147\nQzvvv 116,112,4\nQk 146,148,149\nQk 147,150,151\nQzvvv 112,112,6\nQk 176,176,177\nQk 177,178,179\nv_mfma_f32_16x16x4_4b_bf16 a[224:239], v[30:31], v[58:59], a[224:239]\n"
"Qk 80,80,81\nQk 81,82,83\nQk 82,84,85\nQk 83,86,87\nQk 178,180,181\nQk 179,182,183\nQzvvv 148,144,8\nQk 112,112,113\n"
"Qk 113,114,115\nQzvvv 144,144,10\nQk 114,116,117\nQk 115,118,119\nv_mfma_f32_16x16x4_4b_bf16 v[96:111], v[30:31], v[58:59], v[96:111]\nQk 208,208,209\nQk 209,210,211\nQk 210,212,213\n"
"Qk 211,214,215\nQk 240,240,241\nQk 144,144,145\nQzvvv 180,176,12\nQk 145,146,147\nQk 146,148,149\nQzvvv 176,176,14\nQk 147,150,151\n"
"Qk 241,242,243\nv_mfma_f32_16x16x4_4b_bf16 v[128:143], v[30:31], v[58:59], v[128:143]\nQk 242,244,245\nQk 243,246,247\nv_accvgpr_read_b32 v84, a224\nv_accvgpr_read_b32 v85, a225\nv_accvgpr_read_b32 v86, a226\nQk 176,176,177\n"
"Qzvvv 212,208,16\nQk 177,178,179\nQk 178,180,181\nQzvvv 208,208,18\nQk 179,182,183\nv_accvgpr_read_b32 v87, a227\nv_mfma_f32_16x16x4_4b_bf16 v[160:175], v[30:31], v[58:59], v[160:175]\nv_accvgpr_read_b32 v32, a228\n"
"v_accvgpr_read_b32 v33, a229\nv_accvgpr_read_b32 v34, a230\nv_accvgpr_read_b32 v35, a231\nQk 84,84,85\nQk 85,86,87\nQzvvv 244,240,20\nQk 86,32,33\nQk 87,34,35\n"
"Qzvvv 240,240,22\nQk 208,208,209\nQk 209,210,211\nv_mfma_f32_16x16x4_4b_bf16 v[192:207], v[30:31], v[58:59], v[192:207]\nQk 210,212,213\nQk 211,214,215\nQk 88,88,89\nQk 89,90,91\n"
"Qk 90,92,93\nQk 91,94,95\nQzvvv 32,84,24\nQk 240,240,241\nQk 241,242,243\nQzvvv 84,84,26\nQk 242,244,245\nQk 243,246,247\n"
"v_mfma_f32_16x16x4_4b_bf16 v[224:239], v[30:31], v[58:59], v[224:239]\nQk 120,120,121\nQk 121,122,123\nQk 122,124,125\nQk 123,126,127\nQk 152,152,153\nQk 84,84,85\nQzvvv 92,88,0\n"
"Qk 85,86,87\nQk 86,32,33\nQzvvv 88,88,2\nQk 87,34,35\nQk 153,154,155\nv_mfma_f32_16x16x4_4b_bf16 a[208:223], v[30:31], v[58:59], a[208:223]\nQk 154,156,157\nQk 155,158,159\n"
"s_waitcnt vmcnt(21)\ns_barrier\nQk 184,184,185\nQk 88,88,89\nQzvvv 124,120,4\nQk 89,90,91\nQk 90,92,93\nQzvvv 120,120,6\n"
"Qk 91,94,95\nQk 185,186,187\nv_mfma_f32_16x16x4_4b_bf16 a[240:255], v[30:31], v[58:59], a[240:255]\nQk 186,188,189\nQk 187,190,191\nQta 56,68,0\nQk 216,216,217\nQk 217,218,219\n"
"Qk 120,120,121\nQzvvv 156,152,8\nQk 121,122,123\n.Lpg13:\ns_cbranch_execz .Ltr6\ns_nop 0\nQk 122,124,125\nQzvvv 152,152,10\n"
"Qk 123,126,127\nQk 218,220,221\nQzvvv 188,184,12\nQk 219,222,223\nQta 58,68,1024\nQzvvv 184,184,14\nQk 248,248,249\nQk 249,250,251\n"
"Qk 152,152,153\nQk 153,154,155\nQzvvv 220,216,16\nQk 154,156,157\nQk 155,158,159\nQzvvv 216,216,18\nQk 250,252,253\nQk 251,254,255\n"
"Qk 184,184,185\nQk 185,186,187\nQzvvv 252,248,20\nQk 186,188,189\nQk 187,190,191\nQzvvv 248,248,22\nQta 60,68,8192\nv_accvgpr_read_b32 v116, a232\n"
"v_accvgpr_read_b32 v117, a233\nv_accvgpr_read_b32 v118, a234\nv_accvgpr_read_b32 v119, a235\nv_accvgpr_read_b32 v36, a236\nv_accvgpr_read_b32 v37, a237\nv_accvgpr_read_b32 v38, a238\nv_accvgpr_read_b32 v39, a239\nQk 116,116,117\n"
"Qk 117,118,119\nQk 118,36,37\nQk 119,38,39\nQk 216,216,217\nQk 217,218,219\nQzvvv 36,116,24\nQk 218,220,221\nQk 219,222,223\n"
"Qzvvv 116,116,26\nQta 62,68,9216\ns_mov_b32 s31, 0\ns_nop 0\nQk 96,96,97\nQk 97,98,99\nQk 98,100,101\nQk 99,102,103\n"
"Qk 248,248,249\nQk 249,250,251\nQzvvv 100,96,0\nQk 250,252,253\nQk 251,254,255\nQzvvv 96,96,2\nbuffer_load_dwordx4 a[0:3], v79, s[48:51], s31 offen offset:0 sc1\nQta 64,68,256\n"
"Qk 128,128,129\nQk 129,130,131\nQk 130,132,133\nQk 131,134,135\nQk 116,116,117\nQk 117,118,119\nQzvvv 132,128,4\nQk 118,36,37\n"
"Qk 119,38,39\nQzvvv 128,128,6\nbuffer_load_dwordx4 a[4:7], v79, s[48:51], s31 offen offset:1024 sc1\nQta 66,68,1280\ns_waitcnt lgkmcnt(2)\ns_nop 0\nQzvav 56,56,80\nQk 160,160,161\n"
"Qk 161,162,163\nQmvav 56,60,88\nQk 162,164,165\nQk 163,166,167\nbuffer_load_dwordx4 a[8:11], v79, s[48:51], s31 offen offset:2048 sc1\nQta 68,68,8448\nQzvvv 164,160,8\nQk 192,192,193\n"
"Qk 193,194,195\nQzvvv 160,160,10\nQk 194,196,197\nQk 195,198,199\nbuffer_load_dwordx4 a[12:15], v79, s[48:51], s31 offen offset:3072 sc1\nQta 70,68,9472\nQzvvv 196,192,12\nv_max3_f32 v66, |v56|, |v57|, |v58|\n"
"v_max_f32 v66, v66, |v59|\nQzvvv 192,192,14\nv_mov_b32_e64 v67, v66\ns_mov_b32 s31, 4096\nQk 224,224,225\nQk 225,226,227\nQk 226,228,229\nQk 227,230,231\n"
"buffer_load_dwordx4 a[16:19], v79, s[48:51], s31 offen offset:0 sc1\nv_permlane32_swap_b32_e64 v66, v67\nQzvvv 228,224,16\nv_max_f32 v66, v66, v67\nv_mov_b32 v67, v66\nQzvvv 224,224,18\nv_accvgpr_read_b32 v148, a208\nv_accvgpr_read_b32 v149, a209\n"
"v_accvgpr_read_b32 v150, a210\nv_accvgpr_read_b32 v151, a211\nv_accvgpr_read_b32 v40, a212\nv_accvgpr_read_b32 v41, a213\nv_accvgpr_read_b32 v42, a214\nv_accvgpr_read_b32 v43, a215\nQk 148,148,149\nQk 149,150,151\n"
"Qk 150,40,41\nQk 151,42,43\nbuffer_load_dwordx4 a[20:23], v79, s[48:51], s31 offen offset:1024 sc1\nv_permlane16_swap_b32_e64 v66, v67\nQzvvv 40,148,20\nv_max_f32_e64 v66, v66, v67\nv_accvgpr_read_b32 v180, a240\nQzvvv 148,148,22\n"
"v_accvgpr_read_b32 v181, a241\nv_accvgpr_read_b32 v182, a242\nv_accvgpr_read_b32 v183, a243\nv_accvgpr_read_b32 v44, a244\nv_accvgpr_read_b32 v45, a245\nv_accvgpr_read_b32 v46, a246\nv_accvgpr_read_b32 v47, a247\nQk 180,180,181\n"
"Qk 181,182,183\nQk 182,44,45\nQk 183,46,47\nbuffer_load_dwordx4 a[24:27], v79, s[48:51], s31 offen offset:2048 sc1\nv_max_f32_dpp v66, v66, v66 row_ror:8 row_mask:0xf bank_mask:0xf\nQzvvv 44,180,24\ns_mov_b32 s31, 16384\nQk 104,104,105\n"
"Qzvvv 180,180,26\nQk 105,106,107\nQk 106,108,109\nQk 107,110,111\nbuffer_load_dwordx4 a[28:31], v79, s[48:51], s31 offen offset:0 sc1\nv_max_f32_dpp v66, v66, v66 row_ror:4 row_mask:0xf bank_mask:0xf\nQzvvv 108,104,0\nQk 136,136,137\n"
"Qk 137,138,139\nQzvvv 104,104,2\nQk 138,140,141\nQk 139,142,143\nbuffer_load_dwordx4 a[32:35], v79, s[48:51], s31 offen offset:1024 sc1\nv_max_f32_dpp v66, v66, v66 row_ror:2 row_mask:0xf bank_mask:0xf\nQzvvv 140,136,4\nQk 168,168,169\n"
"Qk 169,170,171\nQzvvv 136,136,6\nQk 170,172,173\nQk 171,174,175\nbuffer_load_dwordx4 a[36:39], v79, s[48:51], s31 offen offset:2048 sc1\nv_max_f32_dpp v66, v66, v66 row_ror:1 row_mask:0xf bank_mask:0xf\nQzvvv 172,168,8\nv_and_b32 v66, 0x7f800000, v66\n"
"v_add_u32 v66, 0x3000000, v66\nQzvvv 168,168,10\nQk 200,200,201\nQk 201,202,203\nQk 202,204,205\nQk 203,206,207\nbuffer_load_dwordx4 a[40:43], v79, s[48:51], s31 offen offset:3072 sc1\nv_min_u32 v66, 0x7e000000, v66\n"
"Qzvvv 204,200,12\nv_or_b32 v66, 0x400000, v66\nds_write_b32 v73, v66 offset:16\nQzvvv 200,200,14\nv_xor_b32 v67, 0x80000000, v66\ns_mov_b32 s31, 20480\nQk 232,232,233\nQk 233,234,235\n"
"Qk 234,236,237\nQk 235,238,239\nbuffer_load_dwordx4 a[44:47], v79, s[48:51], s31 offen offset:0 sc1\nv_accvgpr_write_b32 a112, v67\nQzvvv 236,232,16\nv_accvgpr_write_b32 a113, v67\nv_accvgpr_write_b32 a114, v67\nQzvvv 232,232,18\n"
"v_accvgpr_write_b32 a115, v67\nv_accvgpr_read_b32 v212, a216\nv_accvgpr_read_b32 v213, a217\nv_accvgpr_read_b32 v214, a218\nv_accvgpr_read_b32 v215, a219\nv_accvgpr_read_b32 v48, a220\nv_accvgpr_read_b32 v49, a221\nv_accvgpr_read_b32 v50, a222\n"
"v_accvgpr_read_b32 v51, a223\nQk 212,212,213\nQk 213,214,215\nQk 214,48,49\nQk 215,50,51\nbuffer_load_dwordx4 a[48:51], v79, s[48:51], s31 offen offset:1024 sc1\nv_accvgpr_read_b32 v244, a248\nQzvvv 48,212,20\n"
"v_accvgpr_read_b32 v245, a249\nv_accvgpr_read_b32 v246, a250\nQzvvv 212,212,22\nv_accvgpr_read_b32 v247, a251\nv_accvgpr_read_b32 v52, a252\nv_accvgpr_read_b32 v53, a253\nv_accvgpr_read_b32 v54, a254\nv_accvgpr_read_b32 v55, a255\n"
"Qk 244,244,245\nQk 245,246,247\nQk 246,52,53\nQk 247,54,55\nbuffer_load_dwordx4 a[52:55], v79, s[48:51], s31 offen offset:2048 sc1\ns_nop 0\ns_nop 0\nQzvvv 52,244,24\n"
"Qzvvv 244,244,26\ns_waitcnt lgkmcnt(5)\nQcaav 140,56,84,112\ns_add_u32 m0, s96, 98304\ns_mov_b32 s95, 86016\nQmaav 140,60,116\nbuffer_load_dwordx4 v61, s[52:55], s95 offen lds\nQcaav 136,56,240,112\n"
"s_mov_b32 s56, s20\ns_and_b32 s57, s21, 0xffff\nQmaav 136,60,248\ns_mov_b32 s58, 4194304\nQk 244,244,245\nQcaav 132,56,208,112\nQta 72,68,16384\nQk 245,246,247\n"
"Qmaav 132,60,216\ns_mov_b32 s95, 105472\nQk 246,52,53\nQcaav 128,56,176,112\nbuffer_load_dwordx4 v61, s[52:55], s95 offen offset:1024 lds\nQmaav 128,60,184\nQta 74,68,16896\nQk 247,54,55\n"
"Qcaav 124,56,144,112\nQk 212,212,213\nQk 213,214,215\nQmaav 124,60,152\nQk 214,48,49\nQk 215,50,51\nQcaav 120,56,112,112\nQta 76,68,17408\n"
"Qk 180,180,181\nQmaav 120,60,120\ns_add_u32 s88, s71, 0\nQk 181,182,183\nQcaav 116,56,80,112\nbuffer_store_dwordx4 v[80:83], v79, s[56:59], s88 offen offset:0 sc1\nQmaav 116,60,88\nQta 78,68,17920\n"
"Qk 182,44,45\ns_waitcnt lgkmcnt(5)\nQcaav 144,64,80,112\nQk 183,46,47\nQk 148,148,149\nQmaav 144,68,88\nQk 149,150,151\nQk 150,40,41\n"
"Qcaav 148,64,112,112\nQk 151,42,43\nQk 96,96,97\nQmaav 148,68,120\nQk 97,98,99\nQk 98,100,101\nQcaav 152,64,144,112\nQta 56,68,24576\n"
"Qk 99,102,103\nQmaav 152,68,152\ns_add_u32 m0, s96, 106496\ns_mov_b32 s95, 126976\nQcaav 156,64,176,112\nbuffer_load_dwordx4 v61, s[52:55], s95 offen lds\nQmaav 156,68,184\nQta 58,68,25600\n"
"Qk 168,168,169\nQcaav 160,64,208,112\nQk 169,170,171\nQk 170,172,173\nQmaav 160,68,216\nQk 171,174,175\nQk 136,136,137\nQcaav 164,64,240,112\n"
"Qta 60,68,32768\ns_mov_b32 s95, 146432\nQmaav 164,68,248\nbuffer_load_dwordx4 v61, s[52:55], s95 offen offset:1024 lds\nQcaav 168,64,84,112\nQk 137,138,139\nQk 138,140,141\nQmaav 168,68,116\n"
"Qta 62,68,33792\nQk 139,142,143\ns_waitcnt lgkmcnt(4)\nQcaav 196,72,84,112\nQk 104,104,105\nQk 105,106,107\nQmaav 196,76,116\ns_add_u32 m0, s96, 114688\n"
"s_mov_b32 s95, 86016\nQcaav 192,72,240,112\nbuffer_load_dwordx4 v62, s[52:55], s95 offen lds\nQmaav 192,76,248\nQk 106,108,109\nQk 107,110,111\nQcaav 188,72,208,112\nQta 64,68,24832\n"
"Qk 224,224,225\nQmaav 188,76,216\nbuffer_store_dwordx4 v[112:115], v79, s[56:59], s88 offen offset:1024 sc1\nQcaav 184,72,176,112\nQk 225,226,227\nQk 226,228,229\nQmaav 184,76,184\nQta 66,68,25856\n"
"Qk 227,230,231\nQcaav 180,72,144,112\nQk 192,192,193\nQk 193,194,195\nQmaav 180,76,152\nQk 194,196,197\nQk 195,198,199\nQcaav 176,72,112,112\n"
"Qta 68,68,33024\nv_add_u32 v75, 1, v75\nQmaav 176,76,120\ns_waitcnt vmcnt(35)\nds_write_b32 v73, v75\nQcaav 172,72,80,112\nQk 160,160,161\nQk 161,162,163\n"
"Qmaav 172,76,88\nQta 70,68,34048\ns_mov_b32 s95, 125952\ns_waitcnt lgkmcnt(5)\nQcaav 200,56,80,112\nbuffer_load_dwordx4 v62, s[52:55], s95 offen offset:1024 lds\nQmaav 200,60,88\nQk 162,164,165\n"
"Qk 163,166,167\nQcaav 204,56,112,112\nQk 128,128,129\nQk 129,130,131\nQmaav 204,60,120\nQk 130,132,133\nQk 131,134,135\nQcaav 208,56,144,112\n"
"Qta 72,68,512\nQk 200,200,201\nQmaav 208,60,152\nds_read_b32 v66, v74\ns_add_u32 m0, s96, 122880\ns_mov_b32 s95, 86016\nQcaav 212,56,176,112\nbuffer_load_dwordx4 v63, s[52:55], s95 offen lds\n"
"Qmaav 212,60,184\nQta 74,68,1536\nQk 201,202,203\nQcaav 216,56,208,112\nQk 202,204,205\nQk 203,206,207\nQmaav 216,60,216\ns_waitcnt lgkmcnt(1)\n"
"v_cmp_gt_u32 vcc, v75, v66\nQcaav 220,56,240,112\ns_cbranch_vccnz .Lso40\n.Lro40:\nQk 232,232,233\nQmaav 220,60,248\nQta 76,68,8704\nQk 233,234,235\n"
"Qcaav 224,56,84,112\nbuffer_store_dwordx4 v[144:147], v79, s[56:59], s88 offen offset:2048 sc1\nv_xor_b32 v56, 8, v73\nQmaav 224,60,116\nds_read_b32 v56, v56 offset:16\nQk 234,236,237\ns_waitcnt lgkmcnt(5)\nQcaav 252,64,84,112\n"
"Qta 78,68,9728\nQk 235,238,239\nQmaav 252,68,116\nQcaav 248,64,240,112\nQmaav 248,68,248\nQcaav 244,64,208,112\nQta 56,68,768\nQmaav 244,68,216\n"
"s_mov_b32 s95, 105472\nQcaav 240,64,176,112\nbuffer_load_dwordx4 v63, s[52:55], s95 offen offset:1024 lds\nQmaav 240,68,184\nQta 58,68,1792\ns_waitcnt lgkmcnt(3)\nv_mov_b32 v57, v56\nQcaav 236,64,144,112\n"
"v_mov_b32 v58, v56\nv_mov_b32 v59, v56\nQmaav 236,68,152\nv_accvgpr_write_b32 a80, v56\nv_accvgpr_write_b32 a81, v56\nQcaav 232,64,112,112\nQta 60,68,8960\nv_accvgpr_write_b32 a82, v56\n"
"Qmaav 232,68,120\nv_accvgpr_write_b32 a83, v56\nQcaav 228,64,80,112\nQmaav 228,68,88\nQta 62,68,9984\ns_waitcnt lgkmcnt(4)\nQcvav 0,72,80,56\nQmvav 0,76,88\n"
"Qcvav 4,72,112,56\nbuffer_store_dwordx4 v[176:179], v79, s[56:59], s88 offen offset:3072 sc1\nQmvav 4,76,120\nQcvav 8,72,144,56\nQta 64,68,16640\nQmvav 8,76,152\nQcvav 12,72,176,56\nQmvav 12,76,184\n"
"Qta 66,68,17152\nQcvav 16,72,208,56\nQmvav 16,76,216\nQcvav 20,72,240,56\nQta 68,68,17664\nQmvav 20,76,248\nQcvav 24,72,84,56\nQmvav 24,76,116\n"
"Qta 70,68,18176\ns_waitcnt lgkmcnt(4)\nQcvav 52,56,84,56\nQmvav 52,60,116\nQcvav 48,56,240,56\nQmvav 48,60,248\nQcvav 44,56,208,56\nQta 72,68,25088\n"
"Qmvav 44,60,216\nQcvav 40,56,176,56\nQmvav 40,60,184\nQta 74,68,26112\ns_add_u32 s88, s71, 4096\nQcvav 36,56,144,56\nbuffer_store_dwordx4 v[208:211], v79, s[56:59], s88 offen offset:0 sc1\nQmvav 36,60,152\n"
"Qcvav 32,56,112,56\nQta 76,68,33280\nQmvav 32,60,120\nQcvav 28,56,80,56\nQmvav 28,60,88\nQta 78,68,34304\ns_waitcnt lgkmcnt(4)\nQcvav 92,64,80,56\n"
"Qmvav 92,68,88\nQcvav 124,64,112,56\ns_lshl_b32 s46, s19, 4\nQmvav 124,68,120\ns_cmp_eq_u32 s70, 0\ns_cselect_b32 s46, s46, -1\nQcvav 156,64,144,56\nQta 56,68,25344\n"
"v_mov_b32 v77, 0x7ffff000\nQmvav 156,68,152\nQcvav 188,64,176,56\nQmvav 188,68,184\nQta 58,68,26368\nQcvav 220,64,208,56\nQmvav 220,68,216\nQcvav 252,64,240,56\n"
"Qta 60,68,33536\nQmvav 252,68,248\nQcvav 100,64,84,56\nQmvav 100,68,116\nQta 62,68,34560\ns_waitcnt lgkmcnt(4)\nQcvav 172,72,84,56\nbuffer_store_dwordx4 v[240:243], v79, s[56:59], s88 offen offset:1024 sc1\n"
"Qmvav 172,76,116\nQcvav 140,72,240,56\nQmvav 140,76,248\nQcvav 108,72,208,56\nQta 64,68,40960\nQmvav 108,76,216\nQcvav 228,72,176,56\nQmvav 228,76,184\n"
"Qta 66,68,41984\nQcvav 196,72,144,56\nQmvav 196,76,152\nQcvav 164,72,112,56\nQta 68,68,49152\nQmvav 164,76,120\nQcvav 132,72,80,56\nQmvav 132,76,88\n"
"Qta 70,68,50176\ns_waitcnt lgkmcnt(4)\nQcvav 204,56,80,56\nQmvav 204,60,88\nQcvav 236,56,112,56\nQmvav 236,60,120\nQcaav 96,56,144,80\nQta 72,68,41216\n"
"Qmaav 96,60,152\nQcaav 100,56,176,80\nbuffer_store_dwordx4 v[84:87], v79, s[56:59], s88 offen offset:2048 sc1\nQmaav 100,60,184\nQta 74,68,42240\nQcaav 104,56,208,80\nQmaav 104,60,216\nQcaav 108,56,240,80\n"
"Qta 76,68,49408\nQmaav 108,60,248\nQcaav 112,56,84,80\nQmaav 112,60,116\nQta 78,68,50432\ns_waitcnt vmcnt(15)\ns_waitcnt lgkmcnt(4)\nQmaaa 140,64,24\n"
"Qmaaa 140,68,52\nQmaaa 136,64,20\nQmaaa 136,68,48\nQmaaa 132,64,16\nQta 56,68,41472\nQmaaa 132,68,44\nQmaaa 128,64,12\nQmaaa 128,68,40\n"
".Lpg14:\ns_cbranch_execz .Ltr6\nQta 58,68,42496\nQmaaa 124,64,8\nQmaaa 124,68,36\nQmaaa 120,64,4\nQta 60,68,49664\ns_and_b32 s68, s19, 3\n"
"Qmaaa 120,68,32\ns_add_u32 s88, s71, 16384\nQmaaa 116,64,0\nbuffer_store_dwordx4 v[88:91], v79, s[56:59], s88 offen offset:0 sc1\nQmaaa 116,68,28\nQta 62,68,50688\ns_waitcnt lgkmcnt(4)\nQmaaa 144,72,0\n"
"s_cmp_lt_u32 s68, 1\ns_cselect_b32 s68, 40, 32\nQmaaa 144,76,28\ns_mul_i32 s67, s68, 40960\ns_mul_i32 s74, s68, 32768\nQmaaa 148,72,4\ns_and_b32 s68, s19, 3\nQmaaa 148,76,32\n"
"s_cmp_lt_u32 s68, 2\ns_cselect_b32 s68, 48, 40\nQmaaa 152,72,8\nQta 64,68,41728\ns_mul_i32 s72, s68, 40960\nQmaaa 152,76,36\ns_mul_i32 s75, s68, 32768\ns_and_b32 s68, s19, 3\n"
"Qmaaa 156,72,12\ns_cmp_lt_u32 s68, 3\ns_cselect_b32 s68, 56, 48\nQmaaa 156,76,40\nQta 66,68,42752\ns_mul_i32 s73, s68, 40960\nQmaaa 160,72,16\ns_mul_i32 s77, s68, 32768\n"
"v_add_u32 v58, s91, v60\nQmaaa 160,76,44\ns_mul_i32 s68, s93, 28672\ns_sub_u32 s68, 14336, s68\nQmaaa 164,72,20\nQta 68,68,49920\nv_add_u32 v59, s68, v58\nQmaaa 164,76,48\n"
"Qmaaa 168,72,24\nQmaaa 168,76,52\nQta 70,68,50944\ns_waitcnt lgkmcnt(4)\nQmvaa 24,56,24\nQmvaa 24,60,52\nQmvaa 20,56,20\nQmvaa 20,60,48\n"
"Qmvaa 16,56,16\nQta 72,68,57344\nQmvaa 16,60,44\nbuffer_store_dwordx4 v[120:123], v79, s[56:59], s88 offen offset:1024 sc1\nQmvaa 12,56,12\nQmvaa 12,60,40\nQta 74,68,57856\nQmvaa 8,56,8\n"
"Qmvaa 8,60,36\nQmvaa 4,56,4\nQta 76,68,58368\nv_add_u32 v75, 1, v75\nQmvaa 4,60,32\ns_waitcnt vmcnt(35)\nds_write_b32 v73, v75\nQmvaa 0,56,0\n"
"Qmvaa 0,60,28\nQta 78,68,58880\ns_waitcnt lgkmcnt(5)\nQmvaa 28,64,0\nQmvaa 28,68,28\nQmvaa 32,64,4\nQmvaa 32,68,32\nQmvaa 36,64,8\n"
"Qta 56,68,57600\nQmvaa 36,68,36\nds_read_b32 v66, v74\nQmvaa 40,64,12\nQmvaa 40,68,40\nQta 58,68,58112\nQmvaa 44,64,16\nQmvaa 44,68,44\n"
"s_waitcnt lgkmcnt(1)\nv_cmp_gt_u32 vcc, v75, v66\nQmvaa 48,64,20\ns_cbranch_vccnz .Lso180\n.Lro180:\nQmvaa 48,68,48\nQta 60,68,58624\nQmvaa 52,64,24\n"
"buffer_store_dwordx4 v[152:155], v79, s[56:59], s88 offen offset:2048 sc1\nQmvaa 52,68,52\ns_waitcnt lgkmcnt(4)\nQmaaa 196,72,24\nQta 62,68,59136\nQmaaa 196,76,52\nQmaaa 192,72,20\nQmaaa 192,76,48\n"
"Qmaaa 188,72,16\nQta 64,70,1024\nQmaaa 188,76,44\ns_add_u32 m0, s96, 0\ns_mov_b32 s95, 126976\nQmaaa 184,72,12\nbuffer_load_dwordx4 v63, s[52:55], s95 offen lds\nQmaaa 184,76,40\n"
"Qta 66,70,2048\nQmaaa 180,72,8\nQmaaa 180,76,36\nQmaaa 176,72,4\nQta 68,70,9216\ns_mov_b32 s95, 146432\nQmaaa 176,76,32\nbuffer_load_dwordx4 v63, s[52:55], s95 offen offset:1024 lds\n"
"Qmaaa 172,72,0\nQmaaa 172,76,28\nQta 70,70,10240\ns_waitcnt lgkmcnt(4)\nQmvaa 92,56,0\nQmvaa 92,60,28\nQmvaa 124,56,4\nbuffer_store_dwordx4 v[184:187], v79, s[56:59], s88 offen offset:3072 sc1\n"
"Qmvaa 124,60,32\nQmvaa 156,56,8\nQta 72,70,1280\nQmvaa 156,60,36\nQmvaa 188,56,12\nQmvaa 188,60,40\nQta 74,70,2304\nQmvaa 220,56,16\n"
"Qmvaa 220,60,44\nQmvaa 252,56,20\nQta 76,70,9472\ns_add_u32 m0, s96, 8192\ns_mov_b32 s95, 331776\nQmvaa 252,60,48\nbuffer_load_dwordx4 v61, s[52:55], s95 offen lds\nQmvaa 100,56,24\n"
"Qmvaa 100,60,52\nQta 78,70,10496\ns_waitcnt lgkmcnt(4)\nQmaaa 224,64,24\nQmaaa 224,68,52\ns_mov_b32 s95, 351232\nQmaaa 220,64,20\nbuffer_load_dwordx4 v61, s[52:55], s95 offen offset:1024 lds\n"
"Qmaaa 220,68,48\nQmaaa 216,64,16\nQta 56,70,1536\nQmaaa 216,68,44\nQmaaa 212,64,12\nQmaaa 212,68,40\nQta 58,70,2560\ns_add_u32 s88, s71, 20480\n"
"Qmaaa 208,64,8\nbuffer_store_dwordx4 v[216:219], v79, s[56:59], s88 offen offset:0 sc1\nQmaaa 208,68,36\nQmaaa 204,64,4\nQta 60,70,9728\nQmaaa 204,68,32\nQmaaa 200,64,0\nQmaaa 200,68,28\n"
"Qta 62,70,10752\ns_waitcnt lgkmcnt(4)\nQmaaa 228,72,0\nQmaaa 228,76,28\ns_add_u32 m0, s96, 16384\ns_mov_b32 s95, 372736\nQmaaa 232,72,4\nbuffer_load_dwordx4 v61, s[52:55], s95 offen lds\n"
"Qmaaa 232,76,32\nQmaaa 236,72,8\nQta 64,70,1792\nQmaaa 236,76,36\nQmaaa 240,72,12\nQmaaa 240,76,40\nQta 66,70,2816\ns_mov_b32 s95, 392192\n"
"Qmaaa 244,72,16\nbuffer_load_dwordx4 v61, s[52:55], s95 offen offset:1024 lds\nQmaaa 244,76,44\nQmaaa 248,72,20\nQta 68,70,9984\nQmaaa 248,76,48\nQmaaa 252,72,24\nQmaaa 252,76,52\n"
"Qta 70,70,11008\ns_waitcnt lgkmcnt(4)\nQmvaa 172,56,24\nbuffer_store_dwordx4 v[248:251], v79, s[56:59], s88 offen offset:1024 sc1\nQmvaa 172,60,52\nQmvaa 140,56,20\nQmvaa 140,60,48\nQmvaa 108,56,16\n"
"Qta 72,70,17408\nQmvaa 108,60,44\nQmvaa 228,56,12\nQmvaa 228,60,40\nQta 74,70,18432\ns_add_u32 m0, s96, 24576\ns_mov_b32 s95, 331776\nQmvaa 196,56,8\n"
"buffer_load_dwordx4 v62, s[52:55], s95 offen lds\nQmvaa 196,60,36\nQmvaa 164,56,4\nQta 76,69,0\nQmvaa 164,60,32\nQmvaa 132,56,0\nQmvaa 132,60,28\nQta 78,69,1024\n"
"s_mov_b32 s95, 371712\ns_waitcnt lgkmcnt(4)\nQmvaa 204,64,0\nbuffer_load_dwordx4 v62, s[52:55], s95 offen offset:1024 lds\nQmvaa 204,68,28\nQmvaa 236,64,4\nQmvaa 236,68,32\nQmaaa 96,64,8\n"
"Qta 56,70,17664\nQmaaa 96,68,36\nQmaaa 100,64,12\nbuffer_store_dwordx4 v[116:119], v79, s[56:59], s88 offen offset:2048 sc1\nv_cmp_eq_u32 s[44:45], s46, v60\ns_nop 1\nv_cndmask_b32 v77, v77, v76, s[44:45]\nQmaaa 100,68,40\n"
"Qta 58,70,18688\nQmaaa 104,64,16\nQmaaa 104,68,44\nQmaaa 108,64,20\nQta 60,69,256\nQmaaa 108,68,48\nQmaaa 112,64,24\nQmaaa 112,68,52\n"
"Qta 62,69,1280\ns_mov_b32 s31, 131072\ns_waitcnt lgkmcnt(4)\nQmaav 140,72,180\nbuffer_load_dwordx4 a[0:3], v79, s[48:51], s31 offen offset:0 sc1\nQmaav 140,76,244\nQmaav 136,72,148\nQmaav 136,76,212\n"
"Qmaav 132,72,224\nQta 64,70,17920\nQmaav 132,76,232\nbuffer_load_dwordx4 a[4:7], v79, s[48:51], s31 offen offset:1024 sc1\nQmaav 128,72,192\nQmaav 128,76,200\nQta 66,70,18944\nQmaav 124,72,160\n"
"Qmaav 124,76,168\nbuffer_load_dwordx4 a[8:11], v79, s[48:51], s31 offen offset:2048 sc1\nQmaav 120,72,128\nQta 68,69,512\nQmaav 120,76,136\nQmaav 116,72,96\nQmaav 116,76,104\nQta 70,69,1536\n"
"s_waitcnt lgkmcnt(4)\nQmaav 144,56,96\nbuffer_load_dwordx4 a[12:15], v79, s[48:51], s31 offen offset:3072 sc1\nQmaav 144,60,104\nQmaav 148,56,128\nQmaav 148,60,136\nQmaav 152,56,160\nQta 72,70,18176\n"
"s_mov_b32 s31, 135168\nQmaav 152,60,168\nbuffer_load_dwordx4 a[16:19], v79, s[48:51], s31 offen offset:0 sc1\nQmaav 156,56,192\nQmaav 156,60,200\nQta 74,70,19200\nQmaav 160,56,224\nQmaav 160,60,232\n"
"buffer_load_dwordx4 a[20:23], v79, s[48:51], s31 offen offset:1024 sc1\nQmaav 164,56,148\nQta 76,69,768\nQmaav 164,60,212\nQmaav 168,56,180\nQmaav 168,60,244\nQta 78,69,1792\ns_waitcnt lgkmcnt(4)\n"
"Qmvav 24,64,180\nbuffer_load_dwordx4 a[24:27], v79, s[48:51], s31 offen offset:2048 sc1\nQmvav 24,68,244\nQmvav 20,64,148\nQmvav 20,68,212\nQmvav 16,64,224\nQta 56,69,8192\ns_mov_b32 s31, 147456\n"
"Qmvav 16,68,232\nbuffer_load_dwordx4 a[28:31], v79, s[48:51], s31 offen offset:0 sc1\nQmvav 12,64,192\nQmvav 12,68,200\nQta 58,69,8704\nQmvav 8,64,160\nQmvav 8,68,168\nbuffer_load_dwordx4 a[32:35], v79, s[48:51], s31 offen offset:1024 sc1\n"
"Qmvav 4,64,128\nQta 60,69,9216\nv_add_u32 v75, 1, v75\nQmvav 4,68,136\ns_waitcnt vmcnt(31)\nds_write_b32 v73, v75\nQmvav 0,64,96\nQmvav 0,68,104\n"
"Qta 62,69,9728\ns_waitcnt lgkmcnt(5)\nQmvav 28,72,96\nbuffer_load_dwordx4 a[36:39], v79, s[48:51], s31 offen offset:2048 sc1\nQmvav 28,76,104\nQmvav 32,72,128\nQmvav 32,76,136\nQmvav 36,72,160\n"
"Qta 64,69,8448\nQmvav 36,76,168\nds_read_b32 v66, v74\nQmvav 40,72,192\nbuffer_load_dwordx4 a[40:43], v79, s[48:51], s31 offen offset:3072 sc1\nQmvav 40,76,200\nQta 66,69,8960\nQmvav 44,72,224\n"
"Qmvav 44,76,232\ns_waitcnt lgkmcnt(1)\nv_cmp_gt_u32 vcc, v75, v66\nQmvav 48,72,148\ns_cbranch_vccnz .Lso320\n.Lro320:\nQmvav 48,76,212\nQta 68,69,9472\n"
"s_mov_b32 s31, 151552\nQmvav 52,72,180\nbuffer_load_dwordx4 a[44:47], v79, s[48:51], s31 offen offset:0 sc1\nQmvav 52,76,244\ns_waitcnt lgkmcnt(4)\nQmaav 196,56,180\nQta 70,69,9984\nQmaav 196,60,244\n"
"Qmaav 192,56,148\nbuffer_load_dwordx4 a[48:51], v79, s[48:51], s31 offen offset:1024 sc1\nQmaav 192,60,212\nQmaav 188,56,224\nQta 72,69,16384\nQmaav 188,60,232\nQmaav 184,56,192\nbuffer_load_dwordx4 a[52:55], v79, s[48:51], s31 offen offset:2048 sc1\n"
"Qmaav 184,60,200\nQta 74,69,17408\nQmaav 180,56,160\nQmaav 180,60,168\nQmaav 176,56,128\nQta 76,69,24576\ns_mov_b32 s31, 262144\nQmaav 176,60,136\n"
"buffer_load_dwordx4 v[80:83], v79, s[48:51], s31 offen offset:0 sc1\nQmaav 172,56,96\nQmaav 172,60,104\nQta 78,69,25600\ns_waitcnt lgkmcnt(4)\nQmvav 92,64,96\nQmvav 92,68,104\nbuffer_load_dwordx4 v[112:115], v79, s[48:51], s31 offen offset:1024 sc1\n"
"Qmvav 124,64,128\nQmvav 124,68,136\nQmvav 156,64,160\nQta 56,69,16640\nQmvav 156,68,168\nbuffer_load_dwordx4 v[144:147], v79, s[48:51], s31 offen offset:2048 sc1\nQmvav 188,64,192\nQmvav 188,68,200\n"
"Qta 58,69,17664\nQmvav 220,64,224\nQmvav 220,68,232\nbuffer_load_dwordx4 v[176:179], v79, s[48:51], s31 offen offset:3072 sc1\nQmvav 252,64,148\nQta 60,69,24832\nQmvav 252,68,212\nQmvav 100,64,180\n"
"Qmvav 100,68,244\nQta 62,69,25856\ns_mov_b32 s31, 266240\ns_waitcnt lgkmcnt(4)\nQmaav 224,72,180\nbuffer_load_dwordx4 v[208:211], v79, s[48:51], s31 offen offset:0 sc1\nQmaav 224,76,244\nQmaav 220,72,148\n"
"Qmaav 220,76,212\nQmaav 216,72,224\nQta 64,69,16896\nQmaav 216,76,232\nbuffer_load_dwordx4 v[240:243], v79, s[48:51], s31 offen offset:1024 sc1\nQmaav 212,72,192\nQmaav 212,76,200\nQta 66,69,17920\n"
"Qmaav 208,72,160\nQmaav 208,76,168\nbuffer_load_dwordx4 v[84:87], v79, s[48:51], s31 offen offset:2048 sc1\nQmaav 204,72,128\nQta 68,69,25088\nQmaav 204,76,136\nQmaav 200,72,96\nQmaav 200,76,104\n"
"Qta 70,69,26112\ns_mov_b32 s31, 278528\ns_waitcnt lgkmcnt(4)\nQmaav 228,56,96\nbuffer_load_dwordx4 v[88:91], v79, s[48:51], s31 offen offset:0 sc1\nQmaav 228,60,104\nQmaav 232,56,128\nQmaav 232,60,136\n"
"Qmaav 236,56,160\nQta 72,69,17152\nQmaav 236,60,168\nbuffer_load_dwordx4 v[120:123], v79, s[48:51], s31 offen offset:1024 sc1\nQmaav 240,56,192\nQmaav 240,60,200\nQta 74,69,18176\nQmaav 244,56,224\n"
"Qmaav 244,60,232\nbuffer_load_dwordx4 v[152:155], v79, s[48:51], s31 offen offset:2048 sc1\nQmaav 248,56,148\nQta 76,69,25344\nQmaav 248,60,212\nQmaav 252,56,180\nQmaav 252,60,244\nQta 78,69,26368\n"
"s_waitcnt lgkmcnt(4)\nQmvav 172,64,180\nbuffer_load_dwordx4 v[184:187], v79, s[48:51], s31 offen offset:3072 sc1\nQmvav 172,68,244\nQmvav 140,64,148\nQmvav 140,68,212\nQmvav 108,64,224\nQta 56,69,32768\n"
"s_mov_b32 s31, 282624\nQmvav 108,68,232\nbuffer_load_dwordx4 v[216:219], v79, s[48:51], s31 offen offset:0 sc1\nQmvav 228,64,192\nQmvav 228,68,200\nQta 58,69,33792\nQmvav 196,64,160\nQmvav 196,68,168\n"
"buffer_load_dwordx4 v[248:251], v79, s[48:51], s31 offen offset:1024 sc1\nQmvav 164,64,128\nQta 60,69,40960\nQmvav 164,68,136\nQmvav 132,64,96\nQmvav 132,68,104\nQta 62,69,41984\ns_waitcnt lgkmcnt(4)\n"
"Qmvav 204,72,96\nbuffer_load_dwordx4 v[116:119], v79, s[48:51], s31 offen offset:2048 sc1\nQmvav 204,76,104\nQmvav 236,72,128\nQmvav 236,76,136\nQmaav 96,72,160\nQta 64,69,33024\ns_add_u32 m0, s96, 32768\n"
"s_mov_b32 s95, 331776\nQmaav 96,76,168\nbuffer_load_dwordx4 v63, s[52:55], s95 offen lds\nQmaav 100,72,192\nQmaav 100,76,200\nQta 66,69,34048\nQmaav 104,72,224\nQmaav 104,76,232\n"
"Qmaav 108,72,148\nQta 68,69,41216\ns_mov_b32 s95, 351232\nQmaav 108,76,212\nbuffer_load_dwordx4 v63, s[52:55], s95 offen offset:1024 lds\nQmaav 112,72,180\nQmaav 112,76,244\nQta 70,69,42240\n"
"s_waitcnt vmcnt(16)\ns_waitcnt lgkmcnt(4)\nQmaaa 140,56,24\nQmaaa 140,60,52\ns_add_u32 m0, s96, 40960\ns_mov_b32 s95, 372736\nQmaaa 136,56,20\nbuffer_load_dwordx4 v63, s[52:55], s95 offen lds\n"
"Qmaaa 136,60,48\nQmaaa 132,56,16\nQta 72,69,33280\nQmaaa 132,60,44\nQmaaa 128,56,12\nQmaaa 128,60,40\nQta 74,69,34304\ns_mov_b32 s95, 392192\n"
"Qmaaa 124,56,8\nbuffer_load_dwordx4 v63, s[52:55], s95 offen offset:1024 lds\nQmaaa 124,60,36\nQmaaa 120,56,4\nQta 76,69,41472\ns_add_u32 s88, s71, 131072\nQmaaa 120,60,32\nbuffer_store_dwordx4 v[96:99], v79, s[56:59], s88 offen offset:0 sc1\n"
"Qmaaa 116,56,0\nQmaaa 116,60,28\nQta 78,69,42496\ns_waitcnt lgkmcnt(4)\nQmaaa 144,64,0\nQmaaa 144,68,28\nQmaaa 148,64,4\nQmaaa 148,68,32\n"
"Qmaaa 152,64,8\nQta 56,69,33536\ns_add_u32 m0, s96, 49152\ns_mov_b32 s95, 413696\nQmaaa 152,68,36\nbuffer_load_dwordx4 v61, s[52:55], s95 offen lds\nQmaaa 156,64,12\nQmaaa 156,68,40\n"
"Qta 58,69,34560\nQmaaa 160,64,16\nQmaaa 160,68,44\nQmaaa 164,64,20\nQta 60,69,41728\ns_mov_b32 s95, 433152\nQmaaa 164,68,48\nbuffer_load_dwordx4 v61, s[52:55], s95 offen offset:1024 lds\n"
"Qmaaa 168,64,24\nQmaaa 168,68,52\nQta 62,69,42752\ns_waitcnt lgkmcnt(4)\nQmvaa 24,72,24\nQmvaa 24,76,52\nQmvaa 20,72,20\nbuffer_store_dwordx4 v[128:131], v79, s[56:59], s88 offen offset:1024 sc1\n"
"Qmvaa 20,76,48\nQmvaa 16,72,16\n.Lpg15:\ns_cbranch_execz .Ltr6\nQta 64,69,49152\nQmvaa 16,76,44\nQmvaa 12,72,12\nQmvaa 12,76,40\n"
"Qta 66,69,49664\nQmvaa 8,72,8\nQmvaa 8,76,36\nQmvaa 4,72,4\nQta 68,69,50176\nv_add_u32 v75, 1, v75\nQmvaa 4,76,32\ns_waitcnt vmcnt(36)\n"
"ds_write_b32 v73, v75\ns_add_u32 m0, s96, 57344\ns_mov_b32 s95, 454656\nQmvaa 0,72,0\nbuffer_load_dwordx4 v61, s[52:55], s95 offen lds\nQmvaa 0,76,28\nQta 70,69,50688\ns_waitcnt lgkmcnt(5)\n"
"Qmvaa 28,56,0\nQmvaa 28,60,28\nQmvaa 32,56,4\ns_mov_b32 s95, 474112\nQmvaa 32,60,32\nbuffer_load_dwordx4 v61, s[52:55], s95 offen offset:1024 lds\nQmvaa 36,56,8\nQta 72,69,49408\n"
"Qmvaa 36,60,36\nds_read_b32 v66, v74\nQmvaa 40,56,12\nQmvaa 40,60,40\nQta 74,69,49920\nQmvaa 44,56,16\nbuffer_store_dwordx4 v[160:163], v79, s[56:59], s88 offen offset:2048 sc1\nv_add_u32 v67, 2, v65\n"
"Qmvaa 44,60,44\ns_waitcnt lgkmcnt(1)\nv_cmp_gt_u32 vcc, v75, v66\nQmvaa 48,56,20\ns_cbranch_vccnz .Lso460\n.Lro460:\nQmvaa 48,60,48\nQta 76,69,50432\n"
"Qmvaa 52,56,24\nbuffer_store_dword v67, v77, s[24:27], 0 offen sc1\nQmvaa 52,60,52\ns_waitcnt lgkmcnt(4)\nQmaaa 196,64,24\nQta 78,69,50944\nQmaaa 196,68,52\ns_add_u32 m0, s96, 132096\n"
"s_mov_b32 s95, 413696\nQmaaa 192,64,20\nbuffer_load_dwordx4 v62, s[52:55], s95 offen lds\nQmaaa 192,68,48\nQmaaa 188,64,16\nQta 56,69,57344\nQmaaa 188,68,44\nQmaaa 184,64,12\n"
"Qmaaa 184,68,40\nQta 58,69,58368\ns_mov_b32 s95, 453632\nQmaaa 180,64,8\nbuffer_load_dwordx4 v62, s[52:55], s95 offen offset:1024 lds\nQmaaa 180,68,36\nQmaaa 176,64,4\nQta 60,68,0\n"
"Qmaaa 176,68,32\nQmaaa 172,64,0\nQmaaa 172,68,28\nQta 62,68,1024\ns_waitcnt lgkmcnt(4)\nQmvaa 92,72,0\nbuffer_store_dwordx4 v[192:195], v79, s[56:59], s88 offen offset:3072 sc1\nQmvaa 92,76,28\n"
"Qmvaa 124,72,4\nQmvaa 124,76,32\nQmvaa 156,72,8\nQta 64,69,57600\nQmvaa 156,76,36\nQmvaa 188,72,12\nQmvaa 188,76,40\nQta 66,69,58624\n"
"s_add_u32 m0, s96, 140288\ns_mov_b32 s95, 413696\nQmvaa 220,72,16\nbuffer_load_dwordx4 v63, s[52:55], s95 offen lds\nQmvaa 220,76,44\nQmvaa 252,72,20\nQta 68,68,256\nQmvaa 252,76,48\n"
"Qmvaa 100,72,24\nQmvaa 100,76,52\nQta 70,68,1280\ns_mov_b32 s95, 433152\ns_waitcnt lgkmcnt(4)\nQmaaa 224,56,24\nbuffer_load_dwordx4 v63, s[52:55], s95 offen offset:1024 lds\nQmaaa 224,60,52\n"
"Qmaaa 220,56,20\nQmaaa 220,60,48\nQmaaa 216,56,16\nQta 72,69,57856\nQmaaa 216,60,44\ns_add_u32 s88, s71, 135168\nQmaaa 212,56,12\nbuffer_store_dwordx4 v[224:227], v79, s[56:59], s88 offen offset:0 sc1\n"
"Qmaaa 212,60,40\nQta 74,69,58880\nQmaaa 208,56,8\nQmaaa 208,60,36\nQmaaa 204,56,4\nQta 76,68,512\nQmaaa 204,60,32\nQmaaa 200,56,0\n"
"Qmaaa 200,60,28\nQta 78,68,1536\ns_add_u32 m0, s96, 148480\ns_mov_b32 s95, 454656\ns_waitcnt lgkmcnt(4)\nQmaaa 228,64,0\nbuffer_load_dwordx4 v63, s[52:55], s95 offen lds\nQmaaa 228,68,28\n"
"Qmaaa 232,64,4\nQmaaa 232,68,32\nQmaaa 236,64,8\nQta 56,69,58112\ns_mov_b32 s95, 474112\nQmaaa 236,68,36\nbuffer_load_dwordx4 v63, s[52:55], s95 offen offset:1024 lds\nQmaaa 240,64,12\n"
"Qmaaa 240,68,40\nQta 58,69,59136\nQmaaa 244,64,16\nQmaaa 244,68,44\nQmaaa 248,64,20\nQta 60,68,768\nQmaaa 248,68,48\nQmaaa 252,64,24\n"
"buffer_store_dwordx4 v[148:151], v79, s[56:59], s88 offen offset:1024 sc1\nQmaaa 252,68,52\nQta 62,68,1792\ns_waitcnt lgkmcnt(4)\nQmvaa 172,72,24\nQmvaa 172,76,52\ns_mov_b32 s40, s22\ns_and_b32 s41, s23, 0xffff\n"
"Qmvaa 140,72,20\ns_mov_b32 s42, 571200\ns_lshl_b32 s66, s19, 17\nQmvaa 140,76,48\ns_add_u32 s69, s98, 4194304\ns_add_u32 s98, s66, s69\nQmvaa 108,72,16\nQta 64,68,8192\n"
"s_xor_b32 s66, s66, 524288\nQmvaa 108,76,44\ns_add_u32 m0, s96, 65536\ns_mov_b32 s95, 659456\nQmvaa 228,72,12\nbuffer_load_dwordx4 v61, s[52:55], s95 offen lds\nQmvaa 228,76,40\nQta 66,68,9216\n"
"s_add_u32 s64, s66, s69\nQmvaa 196,72,8\ns_mul_i32 s66, s92, 33792\ns_add_u32 s66, s66, %4\nQmvaa 196,76,36\ns_add_u32 s66, s66, 98304\ns_mul_i32 s68, s93, 96\nQmvaa 164,72,4\n"
"Qta 68,68,16384\ns_mov_b32 s95, 678912\nQmvaa 164,76,32\nbuffer_load_dwordx4 v61, s[52:55], s95 offen offset:1024 lds\nQmvaa 132,72,0\ns_sub_u32 s68, s66, s68\ns_add_u32 s68, s68, 96\nQmvaa 132,76,28\n"
"Qta 70,68,17408\nv_bfe_u32 v148, v60, 4, 4\ns_waitcnt lgkmcnt(4)\nQmvaa 204,56,0\ns_mov_b32 s31, 393216\nv_mul_u32_u24 v148, 208, v148\nQmvaa 204,60,28\nbuffer_load_dwordx4 a[0:3], v79, s[48:51], s31 offen offset:0 sc1\n"
"Qmvaa 236,56,4\nv_lshrrev_b32 v149, 8, v60\nv_lshl_add_u32 v148, v149, 3, v148\nQmvaa 236,60,32\nv_add_u32 v56, s68, v148\ns_lshl_b32 s68, s93, 3\nQmaaa 96,56,8\nQta 72,68,8448\n"
"v_lshrrev_b32 v148, 7, v60\nQmaaa 96,60,36\nbuffer_store_dwordx4 v[180:183], v79, s[56:59], s88 offen offset:2048 sc1\nQmaaa 100,56,12\nv_add_u32 v148, s68, v148\nv_and_b32 v149, 127, v60\nQmaaa 100,60,40\nQta 74,68,9472\n"
"v_and_b32 v150, 64, v60\nQmaaa 104,56,16\nv_lshrrev_b32 v150, 1, v150\nv_mul_u32_u24 v57, 208, v148\nQmaaa 104,60,44\nv_add3_u32 v57, v57, v149, s66\nv_add_u32 v57, v57, v150\nQmaaa 108,56,20\n"
"Qta 76,68,16640\ns_and_b32 s65, s19, 1\nQmaaa 108,60,48\ns_and_b32 s68, s19, 4\ns_lshl_b32 s68, s68, 2\nQmaaa 112,56,24\ns_lshl_b32 s69, s65, 3\ns_add_u32 s68, s68, s69\n"
"Qmaaa 112,60,52\nQta 78,68,17664\ns_lshr_b32 s68, 0xc0004080, s68\ns_waitcnt vmcnt(25)\ns_waitcnt lgkmcnt(4)\nQmaav 140,64,84\nbuffer_load_dwordx4 a[4:7], v79, s[48:51], s31 offen offset:1024 sc1\nQmaav 140,68,116\n"
"s_and_b32 s68, s68, 255\ns_mul_i32 s69, s92, 286720\nQmaav 136,64,240\ns_add_u32 s68, s68, s69\nv_mul_u32_u24 v64, 2560, v148\nQmaav 136,68,248\nv_add3_u32 v64, v64, v149, s68\ns_lshl_b32 s68, s93, 4\n"
"Qmaav 132,64,208\nQta 56,68,8704\nv_lshrrev_b32 v148, 6, v60\nQmaav 132,68,216\nbuffer_load_dwordx4 a[8:11], v79, s[48:51], s31 offen offset:2048 sc1\nQmaav 128,64,176\nv_add_u32 v148, s68, v148\nv_and_b32 v149, 63, v60\n"
"Qmaav 128,68,184\nQta 58,68,9728\nv_and_b32 v150, 32, v60\nQmaav 124,64,144\nv_lshlrev_b32 v150, 1, v150\nv_mul_u32_u24 v71, 208, v148\nQmaav 124,68,152\nbuffer_load_dwordx4 a[12:15], v79, s[48:51], s31 offen offset:3072 sc1\n"
"Qmaav 120,64,112\nQta 60,68,16896\nv_add3_u32 v71, v71, v149, s66\nQmaav 120,68,120\nv_add_u32 v71, v71, v150\ns_xor_b32 s65, s65, 1\nQmaav 116,64,80\ns_lshl_b32 s65, s65, 8\n"
"s_add_u32 s68, s65, s69\nQmaav 116,68,88\nQta 62,68,17920\ns_mov_b32 s31, 397312\ns_waitcnt lgkmcnt(4)\nQmaav 144,72,80\nbuffer_load_dwordx4 a[16:19], v79, s[48:51], s31 offen offset:0 sc1\nQmaav 144,76,88\n"
"v_mul_u32_u24 v72, 2560, v148\nv_add3_u32 v72, v72, v149, s68\nQmaav 148,72,112\nQmaav 148,76,120\nQmaav 152,72,144\nQta 64,68,8960\nQmaav 152,76,152\nbuffer_load_dwordx4 a[20:23], v79, s[48:51], s31 offen offset:1024 sc1\n"
"Qmaav 156,72,176\nQmaav 156,76,184\nQta 66,68,9984\nQmaav 160,72,208\nQmaav 160,76,216\nbuffer_load_dwordx4 a[24:27], v79, s[48:51], s31 offen offset:2048 sc1\nQmaav 164,72,240\nQta 68,68,17152\n"
"Qmaav 164,76,248\nQmaav 168,72,84\nQmaav 168,76,116\nQta 70,68,18176\ns_mov_b32 s31, 409600\ns_waitcnt lgkmcnt(4)\nQmvav 24,56,84\nbuffer_load_dwordx4 a[28:31], v79, s[48:51], s31 offen offset:0 sc1\n"
"Qmvav 24,60,116\nQmvav 20,56,240\nQmvav 20,60,248\nQmvav 16,56,208\nQta 72,68,24576\nQmvav 16,60,216\nbuffer_load_dwordx4 a[32:35], v79, s[48:51], s31 offen offset:1024 sc1\nQmvav 12,56,176\n"
"Qmvav 12,60,184\nQta 74,68,25088\nQmvav 8,56,144\nQmvav 8,60,152\nbuffer_load_dwordx4 a[36:39], v79, s[48:51], s31 offen offset:2048 sc1\nQmvav 4,56,112\nQta 76,68,25600\nv_add_u32 v75, 1, v75\n"
"Qmvav 4,60,120\ns_waitcnt vmcnt(20)\nds_write_b32 v73, v75\nQmvav 0,56,80\nQmvav 0,60,88\nQta 78,68,26112\ns_waitcnt lgkmcnt(5)\nQmvav 28,64,80\n"
"buffer_load_dwordx4 a[40:43], v79, s[48:51], s31 offen offset:3072 sc1\nQmvav 28,68,88\nQmvav 32,64,112\nQmvav 32,68,120\nQmvav 36,64,144\nQta 56,68,24832\nQmvav 36,68,152\nds_read_b32 v66, v74\n"
"s_mov_b32 s31, 413696\nQmvav 40,64,176\nbuffer_load_dwordx4 a[44:47], v79, s[48:51], s31 offen offset:0 sc1\nQmvav 40,68,184\nQta 58,68,25344\nQmvav 44,64,208\nQmvav 44,68,216\ns_waitcnt lgkmcnt(1)\n"
"v_cmp_gt_u32 vcc, v75, v66\nQmvav 48,64,240\ns_cbranch_vccnz .Lso600\n.Lro600:\nQmvav 48,68,248\nQta 60,68,25856\nQmvav 52,64,84\nbuffer_load_dwordx4 a[48:51], v79, s[48:51], s31 offen offset:1024 sc1\n"
"Qmvav 52,68,116\ns_waitcnt lgkmcnt(4)\nQmaav 196,72,84\nQta 62,68,26368\nQmaav 196,76,116\nQmaav 192,72,240\nbuffer_load_dwordx4 a[52:55], v79, s[48:51], s31 offen offset:2048 sc1\nQmaav 192,76,248\n"
"Qmaav 188,72,208\nQta 64,68,32768\nQmaav 188,76,216\nQmaav 184,72,176\nQmaav 184,76,184\nQta 66,68,33792\ns_add_u32 m0, s96, 73728\ns_mov_b32 s95, 700416\n"
"Qmaav 180,72,144\nbuffer_load_dwordx4 v61, s[52:55], s95 offen lds\nQmaav 180,76,152\nQmaav 176,72,112\nQta 68,68,40960\nQmaav 176,76,120\nQmaav 172,72,80\nQmaav 172,76,88\n"
"Qta 70,68,41984\ns_mov_b32 s95, 719872\ns_waitcnt lgkmcnt(4)\nQmvav 92,56,80\nbuffer_load_dwordx4 v61, s[52:55], s95 offen offset:1024 lds\nQmvav 92,60,88\nQmvav 124,56,112\nQmvav 124,60,120\n"
"Qmvav 156,56,144\nQta 72,68,33024\ns_add_u32 m0, s96, 81920\ns_mov_b32 s95, 659456\nQmvav 156,60,152\nbuffer_load_dwordx4 v62, s[52:55], s95 offen lds\nQmvav 188,56,176\nQmvav 188,60,184\n"
"Qta 74,68,34048\nQmvav 220,56,208\nQmvav 220,60,216\nQmvav 252,56,240\nQta 76,68,41216\ns_mov_b32 s95, 699392\nQmvav 252,60,248\nbuffer_load_dwordx4 v62, s[52:55], s95 offen offset:1024 lds\n"
"Qmvav 100,56,84\nQmvav 100,60,116\nQta 78,68,42240\ns_waitcnt lgkmcnt(4)\nQmaav 224,64,84\nQmaav 224,68,116\ns_add_u32 m0, s96, 90112\ns_mov_b32 s95, 659456\n"
"Qmaav 220,64,240\nbuffer_load_dwordx4 v63, s[52:55], s95 offen lds\nQmaav 220,68,248\nQmaav 216,64,208\nQta 56,68,33280\nQmaav 216,68,216\nQmaav 212,64,176\nQmaav 212,68,184\n"
"Qta 58,68,34304\ns_mov_b32 s95, 678912\nQmaav 208,64,144\nbuffer_load_dwordx4 v63, s[52:55], s95 offen offset:1024 lds\nQmaav 208,68,152\nQmaav 204,64,112\nQta 60,68,41472\nQmaav 204,68,120\n"
"Qmaav 200,64,80\nQmaav 200,68,88\nQta 62,68,42496\ns_waitcnt lgkmcnt(4)\nQmaav 228,72,80\nQmaav 228,76,88\ns_add_u32 s88, s71, 147456\nQmaav 232,72,112\n"
"buffer_store_dwordx4 v[104:107], v79, s[56:59], s88 offen offset:0 sc1\nQmaav 232,76,120\nQmaav 236,72,144\nQta 64,68,33536\nQmaav 236,76,152\nQmaav 240,72,176\nQmaav 240,76,184\nQta 66,68,34560\n"
"Qmaav 244,72,208\nQmaav 244,76,216\nQmaav 248,72,240\nQta 68,68,41728\ns_add_u32 m0, s96, 98304\ns_mov_b32 s95, 700416\nQmaav 248,76,248\nbuffer_load_dwordx4 v63, s[52:55], s95 offen lds\n"
"Qmaav 252,72,84\nQmaav 252,76,116\nQta 70,68,42752\ns_waitcnt lgkmcnt(4)\nQmvav 172,56,84\nbuffer_store_dwordx4 v[136:139], v79, s[56:59], s88 offen offset:1024 sc1\nQmvav 172,60,116\nQmvav 140,56,240\n"
"Qmvav 140,60,248\nQmvav 108,56,208\nQta 72,68,49152\nQmvav 108,60,216\nQmvav 228,56,176\nQmvav 228,60,184\nQta 74,68,50176\ns_mov_b32 s95, 719872\n"
"Qmvav 196,56,144\nbuffer_load_dwordx4 v63, s[52:55], s95 offen offset:1024 lds\nQmvav 196,60,152\nQmvav 164,56,112\nQta 76,68,57344\nQmvav 164,60,120\nQmvav 132,56,80\nQmvav 132,60,88\n"
"Qta 78,68,58368\ns_add_u32 m0, s96, 106496\ns_mov_b32 s95, 741376\ns_waitcnt lgkmcnt(4)\nQmvav 204,64,80\nbuffer_load_dwordx4 v61, s[52:55], s95 offen lds\nQmvav 204,68,88\nQmvav 236,64,112\n"
"Qmvav 236,68,120\nQmaav 96,64,144\nQta 56,68,49408\ns_mov_b32 s95, 760832\nQmaav 96,68,152\nbuffer_load_dwordx4 v61, s[52:55], s95 offen offset:1024 lds\nQmaav 100,64,176\nQmaav 100,68,184\n"
"Qta 58,68,50432\nQmaav 104,64,208\ns_mov_b32 s31, 524288\nQmaav 104,68,216\nbuffer_load_dwordx4 v[80:83], v79, s[48:51], s31 offen offset:0 sc1\nQmaav 108,64,240\nQta 60,68,57600\nQmaav 108,68,248\n"
"Qmaav 112,64,84\nQmaav 112,68,116\nQta 62,68,58624\ns_waitcnt vmcnt(13)\ns_waitcnt lgkmcnt(4)\nQmaaa 140,72,24\nbuffer_load_dwordx4 v[112:115], v79, s[48:51], s31 offen offset:1024 sc1\nQmaaa 140,76,52\n"
"Qmaaa 136,72,20\nQmaaa 136,76,48\nQmaaa 132,72,16\nQta 64,68,49664\nQmaaa 132,76,44\nbuffer_load_dwordx4 v[144:147], v79, s[48:51], s31 offen offset:2048 sc1\nQmaaa 128,72,12\nQmaaa 128,76,40\n"
"Qta 66,68,50688\nQmaaa 124,72,8\nQmaaa 124,76,36\nbuffer_load_dwordx4 v[176:179], v79, s[48:51], s31 offen offset:3072 sc1\nQmaaa 120,72,4\nQta 68,68,57856\nQmaaa 120,76,32\nQmaaa 116,72,0\n"
"Qmaaa 116,76,28\nQta 70,68,58880\ns_mov_b32 s31, 528384\ns_waitcnt lgkmcnt(4)\nQmaaa 144,56,0\nbuffer_load_dwordx4 v[208:211], v79, s[48:51], s31 offen offset:0 sc1\nQmaaa 144,60,28\nQmaaa 148,56,4\n"
"Qmaaa 148,60,32\nQmaaa 152,56,8\nQta 72,68,49920\nQmaaa 152,60,36\nbuffer_load_dwordx4 v[240:243], v79, s[48:51], s31 offen offset:1024 sc1\nQmaaa 156,56,12\nQmaaa 156,60,40\nQta 74,68,50944\n"
"Qmaaa 160,56,16\nQmaaa 160,60,44\nbuffer_load_dwordx4 v[84:87], v79, s[48:51], s31 offen offset:2048 sc1\nQmaaa 164,56,20\nQta 76,68,58112\nQmaaa 164,60,48\nQmaaa 168,56,24\nQmaaa 168,60,52\n"
"Qta 78,68,59136\ns_mov_b32 s31, 540672\ns_waitcnt lgkmcnt(4)\nQmvaa 24,64,24\nbuffer_load_dwordx4 v[88:91], v79, s[48:51], s31 offen offset:0 sc1\nQmvaa 24,68,52\nQmvaa 20,64,20\nQmvaa 20,68,48\n"
"Qmvaa 16,64,16\nQta 56,70,1024\nQmvaa 16,68,44\nbuffer_load_dwordx4 v[120:123], v79, s[48:51], s31 offen offset:1024 sc1\nQmvaa 12,64,12\n.Lpg16:\ns_cbranch_execz .Lpg20\nQmvaa 12,68,40\n"
"Qta 58,70,1536\nQmvaa 8,64,8\nQmvaa 8,68,36\nbuffer_load_dwordx4 v[152:155], v79, s[48:51], s31 offen offset:2048 sc1\nQmvaa 4,64,4\nQta 60,70,2048\nv_add_u32 v75, 1, v75\nQmvaa 4,68,32\n"
"s_waitcnt vmcnt(18)\nds_write_b32 v73, v75\nQmvaa 0,64,0\nQmvaa 0,68,28\nQta 62,70,2560\ns_waitcnt lgkmcnt(5)\nQmvaa 28,72,0\nbuffer_load_dwordx4 v[184:187], v79, s[48:51], s31 offen offset:3072 sc1\n"
"Qmvaa 28,76,28\nQmvaa 32,72,4\nQmvaa 32,76,32\nQmvaa 36,72,8\nQta 64,70,1280\nQmvaa 36,76,36\nds_read_b32 v66, v74\ns_mov_b32 s31, 544768\n"
"Qmvaa 40,72,12\nbuffer_load_dwordx4 v[216:219], v79, s[48:51], s31 offen offset:0 sc1\nQmvaa 40,76,40\nQta 66,70,1792\nQmvaa 44,72,16\nQmvaa 44,76,44\ns_waitcnt lgkmcnt(1)\nv_cmp_gt_u32 vcc, v75, v66\n"
"Qmvaa 48,72,20\ns_cbranch_vccnz .Lso740\n.Lro740:\nQmvaa 48,76,48\nQta 68,70,2304\nQmvaa 52,72,24\nbuffer_load_dwordx4 v[248:251], v79, s[48:51], s31 offen offset:1024 sc1\nQmvaa 52,76,52\n"
"s_waitcnt lgkmcnt(4)\nQmaaa 196,56,24\nQta 70,70,2816\nQmaaa 196,60,52\nQmaaa 192,56,20\nbuffer_load_dwordx4 v[116:119], v79, s[48:51], s31 offen offset:2048 sc1\nQmaaa 192,60,48\nQmaaa 188,56,16\n"
"Qta 72,70,9216\nQmaaa 188,60,44\nQmaaa 184,56,12\nQmaaa 184,60,40\nQta 74,70,10240\ns_add_u32 m0, s96, 114688\ns_mov_b32 s95, 782336\nQmaaa 180,56,8\n"
"buffer_load_dwordx4 v61, s[52:55], s95 offen lds\nQmaaa 180,60,36\nQmaaa 176,56,4\nQta 76,70,17408\nQmaaa 176,60,32\nQmaaa 172,56,0\nQmaaa 172,60,28\nQta 78,70,18432\n"
"s_mov_b32 s95, 801792\ns_waitcnt lgkmcnt(4)\nQmvaa 92,64,0\nbuffer_load_dwordx4 v61, s[52:55], s95 offen offset:1024 lds\nQmvaa 92,68,28\nQmvaa 124,64,4\nQmvaa 124,68,32\nQmvaa 156,64,8\n"
"Qta 56,70,9472\ns_add_u32 m0, s96, 122880\ns_mov_b32 s95, 741376\nQmvaa 156,68,36\nbuffer_load_dwordx4 v62, s[52:55], s95 offen lds\nQmvaa 188,64,12\nQmvaa 188,68,40\nQta 58,70,10496\n"
"Qmvaa 220,64,16\nQmvaa 220,68,44\nQmvaa 252,64,20\nQta 60,70,17664\ns_mov_b32 s95, 781312\nQmvaa 252,68,48\nbuffer_load_dwordx4 v62, s[52:55], s95 offen offset:1024 lds\nQmvaa 100,64,24\n"
"Qmvaa 100,68,52\nQta 62,70,18688\ns_waitcnt lgkmcnt(4)\nQmaaa 224,72,24\nQmaaa 224,76,52\ns_add_u32 m0, s96, 0\ns_mov_b32 s95, 741376\nQmaaa 220,72,20\n"
"buffer_load_dwordx4 v63, s[52:55], s95 offen lds\nQmaaa 220,76,48\nQmaaa 216,72,16\nQta 64,70,9728\nQmaaa 216,76,44\nQmaaa 212,72,12\nQmaaa 212,76,40\nQta 66,70,10752\n"
"s_mov_b32 s95, 760832\nQmaaa 208,72,8\nbuffer_load_dwordx4 v63, s[52:55], s95 offen offset:1024 lds\nQmaaa 208,76,36\nQmaaa 204,72,4\nQta 68,70,17920\nQmaaa 204,76,32\nQmaaa 200,72,0\n"
"Qmaaa 200,76,28\nQta 70,70,18944\ns_waitcnt lgkmcnt(4)\nQmaaa 228,56,0\nQmaaa 228,60,28\nQmaaa 232,56,4\nbuffer_store_dwordx4 v[168:171], v79, s[56:59], s88 offen offset:2048 sc1\nQmaaa 232,60,32\n"
"Qmaaa 236,56,8\nQta 72,70,9984\nQmaaa 236,60,36\nQmaaa 240,56,12\nQmaaa 240,60,40\nQta 74,70,11008\nQmaaa 244,56,16\nQmaaa 244,60,44\n"
"Qmaaa 248,56,20\nQta 76,70,18176\ns_add_u32 m0, s96, 8192\ns_mov_b32 s95, 782336\nQmaaa 248,60,48\nbuffer_load_dwordx4 v63, s[52:55], s95 offen lds\nQmaaa 252,56,24\nQmaaa 252,60,52\n"
"Qta 78,70,19200\ns_waitcnt lgkmcnt(4)\nQmvaa 172,64,24\nbuffer_store_dwordx4 v[200:203], v79, s[56:59], s88 offen offset:3072 sc1\nQmvaa 172,68,52\nQmvaa 140,64,20\nQmvaa 140,68,48\nQmvaa 108,64,16\n"
"Qta 56,69,0\nQmvaa 108,68,44\nQmvaa 228,64,12\nQmvaa 228,68,40\nQta 58,69,1024\ns_mov_b32 s95, 801792\nQmvaa 196,64,8\nbuffer_load_dwordx4 v63, s[52:55], s95 offen offset:1024 lds\n"
"Qmvaa 196,68,36\nQmvaa 164,64,4\nQta 60,69,8192\nQmvaa 164,68,32\nQmvaa 132,64,0\nQmvaa 132,68,28\nQta 62,69,9216\ns_add_u32 m0, s96, 16384\n"
"s_mov_b32 s95, 987136\ns_waitcnt lgkmcnt(4)\nQmvaa 204,72,0\nbuffer_load_dwordx4 v61, s[52:55], s95 offen lds\nQmvaa 204,76,28\nQmvaa 236,72,4\nQmvaa 236,76,32\nQmaaa 96,72,8\n"
"Qta 64,69,256\ns_mov_b32 s95, 1006592\nQmaaa 96,76,36\nbuffer_load_dwordx4 v61, s[52:55], s95 offen offset:1024 lds\nQmaaa 100,72,12\nQmaaa 100,76,40\nQta 66,69,1280\nQmaaa 104,72,16\n"
"s_mov_b32 s31, 655360\nQmaaa 104,76,44\nbuffer_load_dwordx4 a[0:3], v79, s[48:51], s31 offen offset:0 sc1\nQmaaa 108,72,20\nQta 68,69,8448\nQmaaa 108,76,48\nQmaaa 112,72,24\nQmaaa 112,76,52\n"
"Qta 70,69,9472\ns_waitcnt vmcnt(13)\ns_waitcnt lgkmcnt(4)\nQmaav 140,56,84\nbuffer_load_dwordx4 a[4:7], v79, s[48:51], s31 offen offset:1024 sc1\nQmaav 140,60,116\nQmaav 136,56,240\nQmaav 136,60,248\n"
"Qmaav 132,56,208\nQta 72,69,512\nQmaav 132,60,216\nbuffer_load_dwordx4 a[8:11], v79, s[48:51], s31 offen offset:2048 sc1\nQmaav 128,56,176\nQmaav 128,60,184\nQta 74,69,1536\nQmaav 124,56,144\n"
"Qmaav 124,60,152\nbuffer_load_dwordx4 a[12:15], v79, s[48:51], s31 offen offset:3072 sc1\nQmaav 120,56,112\nQta 76,69,8704\nQmaav 120,60,120\nQmaav 116,56,80\nQmaav 116,60,88\nQta 78,69,9728\n"
"s_mov_b32 s31, 659456\ns_waitcnt lgkmcnt(4)\nQmaav 144,64,80\nbuffer_load_dwordx4 a[16:19], v79, s[48:51], s31 offen offset:0 sc1\nQmaav 144,68,88\nQmaav 148,64,112\nQmaav 148,68,120\nQmaav 152,64,144\n"
"Qta 56,69,768\nQmaav 152,68,152\nbuffer_load_dwordx4 a[20:23], v79, s[48:51], s31 offen offset:1024 sc1\nQmaav 156,64,176\nQmaav 156,68,184\nQta 58,69,1792\nQmaav 160,64,208\nQmaav 160,68,216\n"
"buffer_load_dwordx4 a[24:27], v79, s[48:51], s31 offen offset:2048 sc1\nQmaav 164,64,240\nQta 60,69,8960\nQmaav 164,68,248\nQmaav 168,64,84\nQmaav 168,68,116\nQta 62,69,9984\ns_mov_b32 s31, 671744\n"
"s_waitcnt lgkmcnt(4)\nQmvav 24,72,84\nbuffer_load_dwordx4 a[28:31], v79, s[48:51], s31 offen offset:0 sc1\nQmvav 24,76,116\nQmvav 20,72,240\nQmvav 20,76,248\nQmvav 16,72,208\nQta 64,69,16384\n"
"Qmvav 16,76,216\nbuffer_load_dwordx4 a[32:35], v79, s[48:51], s31 offen offset:1024 sc1\nQmvav 12,72,176\nQmvav 12,76,184\nQta 66,69,16896\nQmvav 8,72,144\nQmvav 8,76,152\nbuffer_load_dwordx4 a[36:39], v79, s[48:51], s31 offen offset:2048 sc1\n"
"Qmvav 4,72,112\nQta 68,69,17408\nv_add_u32 v75, 1, v75\nQmvav 4,76,120\ns_waitcnt vmcnt(18)\nds_write_b32 v73, v75\nQmvav 0,72,80\nQmvav 0,76,88\n"
"Qta 70,69,17920\ns_waitcnt lgkmcnt(5)\nQmvav 28,56,80\nbuffer_load_dwordx4 a[40:43], v79, s[48:51], s31 offen offset:3072 sc1\nQmvav 28,60,88\nQmvav 32,56,112\nQmvav 32,60,120\nQmvav 36,56,144\n"
"Qta 72,69,16640\nQmvav 36,60,152\nds_read_b32 v66, v74\ns_mov_b32 s31, 675840\nQmvav 40,56,176\nbuffer_load_dwordx4 a[44:47], v79, s[48:51], s31 offen offset:0 sc1\nQmvav 40,60,184\nQta 74,69,17152\n"
"Qmvav 44,56,208\nQmvav 44,60,216\ns_waitcnt lgkmcnt(1)\nv_cmp_gt_u32 vcc, v75, v66\nQmvav 48,56,240\ns_cbranch_vccnz .Lso880\n.Lro880:\nQmvav 48,60,248\n"
"Qta 76,69,17664\nQmvav 52,56,84\nbuffer_load_dwordx4 a[48:51], v79, s[48:51], s31 offen offset:1024 sc1\nQmvav 52,60,116\ns_waitcnt lgkmcnt(4)\nQmaav 196,64,84\nQta 78,69,18176\nQmaav 196,68,116\n"
"Qmaav 192,64,240\nbuffer_load_dwordx4 a[52:55], v79, s[48:51], s31 offen offset:2048 sc1\nQmaav 192,68,248\nQmaav 188,64,208\nQta 56,69,24576\nQmaav 188,68,216\nQmaav 184,64,176\nQmaav 184,68,184\n"
"Qta 58,69,25600\ns_add_u32 m0, s96, 24576\ns_mov_b32 s95, 1028096\nQmaav 180,64,144\nbuffer_load_dwordx4 v61, s[52:55], s95 offen lds\nQmaav 180,68,152\nQmaav 176,64,112\nQta 60,69,32768\n"
"Qmaav 176,68,120\nQmaav 172,64,80\nQmaav 172,68,88\nQta 62,69,33792\ns_mov_b32 s95, 1047552\ns_waitcnt lgkmcnt(4)\nQmvav 92,72,80\nbuffer_load_dwordx4 v61, s[52:55], s95 offen offset:1024 lds\n"
"Qmvav 92,76,88\nQmvav 124,72,112\nQmvav 124,76,120\nQmvav 156,72,144\nQta 64,69,24832\ns_add_u32 m0, s96, 32768\ns_mov_b32 s95, 987136\nQmvav 156,76,152\n"
"buffer_load_dwordx4 v62, s[52:55], s95 offen lds\nQmvav 188,72,176\nQmvav 188,76,184\nQta 66,69,25856\nQmvav 220,72,208\nQmvav 220,76,216\nQmvav 252,72,240\nQta 68,69,33024\n"
"s_mov_b32 s95, 1027072\nQmvav 252,76,248\nbuffer_load_dwordx4 v62, s[52:55], s95 offen offset:1024 lds\nQmvav 100,72,84\nQmvav 100,76,116\nQta 70,69,34048\ns_waitcnt lgkmcnt(4)\nQmaav 224,56,84\n"
"Qmaav 224,60,116\ns_add_u32 m0, s96, 40960\ns_mov_b32 s95, 987136\nQmaav 220,56,240\nbuffer_load_dwordx4 v63, s[52:55], s95 offen lds\nQmaav 220,60,248\nQmaav 216,56,208\nQta 72,69,25088\n"
"Qmaav 216,60,216\nQmaav 212,56,176\nQmaav 212,60,184\nQta 74,69,26112\ns_mov_b32 s95, 1006592\nQmaav 208,56,144\nbuffer_load_dwordx4 v63, s[52:55], s95 offen offset:1024 lds\nQmaav 208,60,152\n"
"Qmaav 204,56,112\nQta 76,69,33280\nQmaav 204,60,120\nQmaav 200,56,80\nQmaav 200,60,88\nQta 78,69,34304\ns_waitcnt lgkmcnt(4)\nQmaav 228,64,80\n"
"Qmaav 228,68,88\ns_add_u32 s88, s71, 151552\nQmaav 232,64,112\nbuffer_store_dwordx4 v[232:235], v79, s[56:59], s88 offen offset:0 sc1\nQmaav 232,68,120\nQmaav 236,64,144\nQta 56,69,25344\nQmaav 236,68,152\n"
"Qmaav 240,64,176\nQmaav 240,68,184\nQta 58,69,26368\nQmaav 244,64,208\nQmaav 244,68,216\nQmaav 248,64,240\nQta 60,69,33536\ns_add_u32 m0, s96, 49152\n"
"s_mov_b32 s95, 1028096\nQmaav 248,68,248\nbuffer_load_dwordx4 v63, s[52:55], s95 offen lds\nQmaav 252,64,84\nQmaav 252,68,116\nQta 62,69,34560\ns_waitcnt lgkmcnt(4)\nQmvav 172,72,84\n"
"buffer_store_dwordx4 v[212:215], v79, s[56:59], s88 offen offset:1024 sc1\nQmvav 172,76,116\nQmvav 140,72,240\nQmvav 140,76,248\nQmvav 108,72,208\nQta 64,69,40960\nQmvav 108,76,216\nQmvav 228,72,176\n"
"Qmvav 228,76,184\nQta 66,69,41984\ns_mov_b32 s95, 1047552\nQmvav 196,72,144\nbuffer_load_dwordx4 v63, s[52:55], s95 offen offset:1024 lds\nQmvav 196,76,152\nQmvav 164,72,112\nQta 68,69,49152\n"
"Qmvav 164,76,120\nQmvav 132,72,80\nQmvav 132,76,88\nQta 70,69,50176\ns_add_u32 m0, s96, 57344\ns_mov_b32 s95, 1069056\ns_waitcnt lgkmcnt(4)\nQmvav 204,56,80\n"
"buffer_load_dwordx4 v61, s[52:55], s95 offen lds\nQmvav 204,60,88\nQmvav 236,56,112\nQmvav 236,60,120\nQmaav 96,56,144\nQta 72,69,41216\ns_mov_b32 s95, 1088512\nQmaav 96,60,152\n"
"buffer_load_dwordx4 v61, s[52:55], s95 offen offset:1024 lds\nQmaav 100,56,176\nQmaav 100,60,184\nQta 74,69,42240\nQmaav 104,56,208\ns_mov_b32 s31, 786432\nQmaav 104,60,216\nbuffer_load_dwordx4 v[80:83], v79, s[48:51], s31 offen offset:0 sc1\n"
"Qmaav 108,56,240\nQta 76,69,49408\nQmaav 108,60,248\nQmaav 112,56,84\nv_accvgpr_read_b32 v96, a96\nv_accvgpr_read_b32 v97, a97\nQmaav 112,60,116\nQta 78,69,50432\n"
"v_accvgpr_read_b32 v98, a98\ns_waitcnt vmcnt(13)\ns_waitcnt lgkmcnt(4)\nQmaaa 140,64,24\nbuffer_load_dwordx4 v[112:115], v79, s[48:51], s31 offen offset:1024 sc1\nQmaaa 140,68,52\nv_accvgpr_read_b32 v99, a99\nv_accvgpr_read_b32 v128, a100\n"
"Qmaaa 136,64,20\nv_accvgpr_read_b32 v129, a101\nv_accvgpr_read_b32 v130, a102\nQmaaa 136,68,48\nv_accvgpr_read_b32 v131, a103\nv_accvgpr_read_b32 v160, a104\nQmaaa 132,64,16\nQta 56,69,41472\n"
"v_accvgpr_read_b32 v161, a105\nQmaaa 132,68,44\nbuffer_load_dwordx4 v[144:147], v79, s[48:51], s31 offen offset:2048 sc1\nQmaaa 128,64,12\nv_accvgpr_read_b32 v162, a106\nv_accvgpr_read_b32 v163, a107\nQmaaa 128,68,40\nQta 58,69,42496\n"
"v_accvgpr_read_b32 v192, a108\nQmaaa 124,64,8\nv_accvgpr_read_b32 v193, a109\nv_accvgpr_read_b32 v194, a110\nQmaaa 124,68,36\nbuffer_load_dwordx4 v[176:179], v79, s[48:51], s31 offen offset:3072 sc1\nQmaaa 120,64,4\nQta 60,69,49664\n"
"v_accvgpr_read_b32 v195, a111\nQmaaa 120,68,32\nv_accvgpr_read_b32 v224, a112\nv_accvgpr_read_b32 v225, a113\nQmaaa 116,64,0\nv_accvgpr_read_b32 v226, a114\nv_accvgpr_read_b32 v227, a115\nQmaaa 116,68,28\n"
"Qta 62,69,50688\ns_mov_b32 s31, 790528\ns_waitcnt lgkmcnt(4)\nQmaaa 144,72,0\nbuffer_load_dwordx4 v[208:211], v79, s[48:51], s31 offen offset:0 sc1\nQmaaa 144,76,28\nQmaaa 148,72,4\nQmaaa 148,76,32\n"
"Qmaaa 152,72,8\nQta 64,69,41728\nQmaaa 152,76,36\nbuffer_load_dwordx4 v[240:243], v79, s[48:51], s31 offen offset:1024 sc1\nQmaaa 156,72,12\nQmaaa 156,76,40\nQta 66,69,42752\nQmaaa 160,72,16\n"
"Qmaaa 160,76,44\nbuffer_load_dwordx4 v[84:87], v79, s[48:51], s31 offen offset:2048 sc1\nQmaaa 164,72,20\nQta 68,69,49920\nQmaaa 164,76,48\nQmaaa 168,72,24\nQmaaa 168,76,52\nQta 70,69,50944\n"
"s_mov_b32 s31, 802816\ns_waitcnt lgkmcnt(4)\nQmvaa 24,56,24\nbuffer_load_dwordx4 v[88:91], v79, s[48:51], s31 offen offset:0 sc1\nQmvaa 24,60,52\nQmvaa 20,56,20\nQmvaa 20,60,48\nQmvaa 16,56,16\n"
"Qta 72,69,57344\nQmvaa 16,60,44\nbuffer_load_dwordx4 v[120:123], v79, s[48:51], s31 offen offset:1024 sc1\nQmvaa 12,56,12\nQmvaa 12,60,40\nQta 74,69,57856\nQmvaa 8,56,8\nQmvaa 8,60,36\n"
"buffer_load_dwordx4 v[152:155], v79, s[48:51], s31 offen offset:2048 sc1\nQmvaa 4,56,4\nQta 76,69,58368\nv_add_u32 v75, 1, v75\nQmvaa 4,60,32\ns_waitcnt vmcnt(18)\nds_write_b32 v73, v75\nQmvaa 0,56,0\n"
"Qmvaa 0,60,28\nQta 78,69,58880\ns_waitcnt lgkmcnt(5)\nQmvaa 28,64,0\nbuffer_load_dwordx4 v[184:187], v79, s[48:51], s31 offen offset:3072 sc1\nQmvaa 28,68,28\nQmvaa 32,64,4\nQmvaa 32,68,32\n"
"Qmvaa 36,64,8\nQta 56,69,57600\nQmvaa 36,68,36\n.Lpg17:\ns_cbranch_execz .Lpg21\nds_read_b32 v66, v74\ns_mov_b32 s31, 806912\nQmvaa 40,64,12\n"
"buffer_load_dwordx4 v[216:219], v79, s[48:51], s31 offen offset:0 sc1\nQmvaa 40,68,40\nQta 58,69,58112\nQmvaa 44,64,16\nQmvaa 44,68,44\ns_waitcnt lgkmcnt(1)\nv_cmp_gt_u32 vcc, v75, v66\nQmvaa 48,64,20\n"
"s_cbranch_vccnz .Lso1020\n.Lro1020:\nQmvaa 48,68,48\nQta 60,69,58624\nQmvaa 52,64,24\nbuffer_load_dwordx4 v[248:251], v79, s[48:51], s31 offen offset:1024 sc1\nQmvaa 52,68,52\ns_waitcnt lgkmcnt(4)\n"
"Qmaaa 196,72,24\nQta 62,69,59136\nQmaaa 196,76,52\nQmaaa 192,72,20\nbuffer_load_dwordx4 v[116:119], v79, s[48:51], s31 offen offset:2048 sc1\nQmaaa 192,76,48\nQmaaa 188,72,16\nQta 64,68,0\n"
"Qmaaa 188,76,44\nQmaaa 184,72,12\nQmaaa 184,76,40\nQta 66,68,1024\ns_add_u32 m0, s96, 132096\ns_mov_b32 s95, 1110016\nQmaaa 180,72,8\nbuffer_load_dwordx4 v61, s[52:55], s95 offen lds\n"
"Qmaaa 180,76,36\nQmaaa 176,72,4\nQta 68,68,8192\nQmaaa 176,76,32\nQmaaa 172,72,0\nQmaaa 172,76,28\nQta 70,68,9216\ns_mov_b32 s95, 1129472\n"
"s_waitcnt lgkmcnt(4)\nQmvaa 92,56,0\nbuffer_load_dwordx4 v61, s[52:55], s95 offen offset:1024 lds\nQmvaa 92,60,28\nQmvaa 124,56,4\nQmvaa 124,60,32\nQmvaa 156,56,8\nQta 72,68,256\n"
"s_add_u32 m0, s96, 140288\ns_mov_b32 s95, 1069056\nQmvaa 156,60,36\nbuffer_load_dwordx4 v62, s[52:55], s95 offen lds\nQmvaa 188,56,12\nQmvaa 188,60,40\nQta 74,68,1280\nQmvaa 220,56,16\n"
"Qmvaa 220,60,44\nQmvaa 252,56,20\nQta 76,68,8448\ns_mov_b32 s95, 1108992\nQmvaa 252,60,48\nbuffer_load_dwordx4 v62, s[52:55], s95 offen offset:1024 lds\nQmvaa 100,56,24\nQmvaa 100,60,52\n"
"Qta 78,68,9472\ns_waitcnt lgkmcnt(4)\nQmaaa 224,64,24\nQmaaa 224,68,52\ns_add_u32 m0, s96, 148480\ns_mov_b32 s95, 1069056\nQmaaa 220,64,20\nbuffer_load_dwordx4 v63, s[52:55], s95 offen lds\n"
"Qmaaa 220,68,48\nQmaaa 216,64,16\nQta 56,68,512\nQmaaa 216,68,44\nQmaaa 212,64,12\nQmaaa 212,68,40\nQta 58,68,1536\ns_mov_b32 s95, 1088512\n"
"Qmaaa 208,64,8\nbuffer_load_dwordx4 v63, s[52:55], s95 offen offset:1024 lds\nQmaaa 208,68,36\nQmaaa 204,64,4\nQta 60,68,8704\nQmaaa 204,68,32\nQmaaa 200,64,0\nQmaaa 200,68,28\n"
"Qta 62,68,9728\ns_waitcnt lgkmcnt(4)\nQmaaa 228,72,0\nQmaaa 228,76,28\nQmaaa 232,72,4\nbuffer_store_dwordx4 v[244:247], v79, s[56:59], s88 offen offset:2048 sc1\nQmaaa 232,76,32\nQmaaa 236,72,8\n"
"Qta 64,68,768\nQmaaa 236,76,36\nQmaaa 240,72,12\nQmaaa 240,76,40\nQta 66,68,1792\nQmaaa 244,72,16\nQmaaa 244,76,44\nQmaaa 248,72,20\n"
"Qta 68,68,8960\ns_add_u32 m0, s96, 65536\ns_mov_b32 s95, 1110016\nQmaaa 248,76,48\nbuffer_load_dwordx4 v63, s[52:55], s95 offen lds\nQmaaa 252,72,24\nQmaaa 252,76,52\nQta 70,68,9984\n"
"s_waitcnt lgkmcnt(4)\nQmvaa 172,56,24\nQmvaa 172,60,52\ns_mov_b32 s95, 1129472\nQmvaa 140,56,20\nbuffer_load_dwordx4 v63, s[52:55], s95 offen offset:1024 lds\nQmvaa 140,60,48\nQmvaa 108,56,16\n"
"Qta 72,68,16384\nQmvaa 108,60,44\nQmvaa 228,56,12\nQmvaa 228,60,40\nQta 74,68,17408\ns_add_u32 m0, s96, 73728\ns_add_u32 s95, s67, 4096\nQmvaa 196,56,8\n"
"buffer_load_dwordx4 v61, s[52:55], s95 offen lds\nQmvaa 196,60,36\nQmvaa 164,56,4\nQta 76,68,24576\nQmvaa 164,60,32\nQmvaa 132,56,0\nQmvaa 132,60,28\nQta 78,68,25600\n"
"s_add_u32 s95, s67, 23552\ns_waitcnt lgkmcnt(4)\nQmvaa 204,64,0\nbuffer_load_dwordx4 v61, s[52:55], s95 offen offset:1024 lds\nQmvaa 204,68,28\nQmvaa 236,64,4\nQmvaa 236,68,32\nQmvaa 96,64,8\n"
"Qta 56,68,16640\ns_mov_b32 s31, 917504\nQmvaa 96,68,36\nbuffer_load_dwordx4 a[0:3], v79, s[48:51], s31 offen offset:0 sc1\nQmvaa 128,64,12\nQmvaa 128,68,40\nQta 58,68,17664\nQmvaa 160,64,16\n"
"Qmvaa 160,68,44\nbuffer_load_dwordx4 a[4:7], v79, s[48:51], s31 offen offset:1024 sc1\nQmvaa 192,64,20\nQta 60,68,24832\nQmvaa 192,68,48\nQmvaa 224,64,24\nQmvaa 224,68,52\nQta 62,68,25856\n"
"s_waitcnt vmcnt(13)\ns_waitcnt lgkmcnt(4)\nQmaav 140,72,84\nbuffer_load_dwordx4 a[8:11], v79, s[48:51], s31 offen offset:2048 sc1\nQmaav 140,76,116\nQmaav 136,72,240\nQmaav 136,76,248\nQmaav 132,72,208\n"
"Qta 64,68,16896\nQmaav 132,76,216\nbuffer_load_dwordx4 a[12:15], v79, s[48:51], s31 offen offset:3072 sc1\nQmaav 128,72,176\nQmaav 128,76,184\nQta 66,68,17920\nQmaav 124,72,144\ns_mov_b32 s31, 921600\n"
"Qmaav 124,76,152\nbuffer_load_dwordx4 a[16:19], v79, s[48:51], s31 offen offset:0 sc1\nQmaav 120,72,112\nQta 68,68,25088\nQmaav 120,76,120\nQmaav 116,72,80\nQmaav 116,76,88\nQta 70,68,26112\n"
"s_waitcnt lgkmcnt(4)\nQmaav 144,56,80\nbuffer_load_dwordx4 a[20:23], v79, s[48:51], s31 offen offset:1024 sc1\nQmaav 144,60,88\nQmaav 148,56,112\nbuffer_load_dword v78, v76, s[24:27], 0 offen sc1\nQmaav 148,60,120\nQmaav 152,56,144\n"
"Qta 72,68,17152\nQmaav 152,60,152\nbuffer_load_dwordx4 a[24:27], v79, s[48:51], s31 offen offset:2048 sc1\nQmaav 156,56,176\nQmaav 156,60,184\nQta 74,68,18176\nQmaav 160,56,208\ns_mov_b32 s31, 933888\n"
"Qmaav 160,60,216\nbuffer_load_dwordx4 a[28:31], v79, s[48:51], s31 offen offset:0 sc1\nQmaav 164,56,240\nQta 76,68,25344\nQmaav 164,60,248\nQmaav 168,56,84\nQmaav 168,60,116\nQta 78,68,26368\n"
"s_waitcnt lgkmcnt(4)\nQmvav 24,64,84\nbuffer_load_dwordx4 a[32:35], v79, s[48:51], s31 offen offset:1024 sc1\nQmvav 24,68,116\nQmvav 20,64,240\nQmvav 20,68,248\nQmvav 16,64,208\nQta 56,68,32768\n"
"Qmvav 16,68,216\nbuffer_load_dwordx4 a[36:39], v79, s[48:51], s31 offen offset:2048 sc1\nQmvav 12,64,176\nQmvav 12,68,184\nQta 58,68,33280\nQmvav 8,64,144\nQmvav 8,68,152\nbuffer_load_dwordx4 a[40:43], v79, s[48:51], s31 offen offset:3072 sc1\n"
"Qmvav 4,64,112\nQta 60,68,33792\nv_add_u32 v75, 1, v75\nQmvav 4,68,120\ns_waitcnt vmcnt(19)\nds_write_b32 v73, v75\nQmvav 0,64,80\nQmvav 0,68,88\n"
"Qta 62,68,34304\ns_mov_b32 s31, 937984\ns_waitcnt lgkmcnt(5)\nQmvav 28,72,80\nbuffer_load_dwordx4 a[44:47], v79, s[48:51], s31 offen offset:0 sc1\nQmvav 28,76,88\nQmvav 32,72,112\nQmvav 32,76,120\n"
"Qmvav 36,72,144\nQta 64,68,33024\nQmvav 36,76,152\nds_read_b32 v66, v74\nQmvav 40,72,176\nbuffer_load_dwordx4 a[48:51], v79, s[48:51], s31 offen offset:1024 sc1\nQmvav 40,76,184\nQta 66,68,33536\n"
"Qmvav 44,72,208\nQmvav 44,76,216\ns_waitcnt lgkmcnt(1)\nv_cmp_gt_u32 vcc, v75, v66\nQmvav 48,72,240\ns_cbranch_vccnz .Lso1160\n.Lro1160:\nQmvav 48,76,248\n"
"Qta 68,68,34048\nQmvav 52,72,84\nbuffer_load_dwordx4 a[52:55], v79, s[48:51], s31 offen offset:2048 sc1\nQmvav 52,76,116\ns_waitcnt lgkmcnt(4)\nQmaav 196,56,84\nQta 70,68,34560\nQmaav 196,60,116\n"
"Qmaav 192,56,240\ns_add_u32 m0, s96, 81920\ns_add_u32 s95, s67, 45056\nQmaav 192,60,248\nbuffer_load_dwordx4 v61, s[52:55], s95 offen lds\nQmaav 188,56,208\nQta 72,68,40960\nQmaav 188,60,216\n"
"Qmaav 184,56,176\nQmaav 184,60,184\nQta 74,68,41984\ns_add_u32 s95, s67, 64512\nQmaav 180,56,144\nbuffer_load_dwordx4 v61, s[52:55], s95 offen offset:1024 lds\nQmaav 180,60,152\nQmaav 176,56,112\n"
"Qta 76,68,49152\nQmaav 176,60,120\nQmaav 172,56,80\nQmaav 172,60,88\nQta 78,68,50176\ns_add_u32 m0, s96, 90112\ns_add_u32 s95, s67, 4096\ns_waitcnt lgkmcnt(4)\n"
"Qmvav 92,64,80\nbuffer_load_dwordx4 v62, s[52:55], s95 offen lds\nQmvav 92,68,88\nQmvav 124,64,112\nQmvav 124,68,120\nQmvav 156,64,144\nQta 56,68,41216\ns_add_u32 s95, s67, 44032\n"
"Qmvav 156,68,152\nbuffer_load_dwordx4 v62, s[52:55], s95 offen offset:1024 lds\nQmvav 188,64,176\ns_waitcnt vmcnt(12)\nv_sub_u32 v66, v78, v65\nv_cmp_gt_u32 vcc, 2, v66\ns_and_b64 vcc, vcc, s[28:29]\ns_cbranch_scc1 .Lf2_w\n"
".Lf2_r:\nQmvav 188,68,184\nQta 58,68,42240\nQmvav 220,64,208\ns_add_u32 s31, s74, 0\nQmvav 220,68,216\nbuffer_load_dwordx4 a[80:83], v79, s[48:51], s31 offen offset:0 sc1\nQmvav 252,64,240\n"
"Qta 60,68,49408\nQmvav 252,68,248\nQmvav 100,64,84\nQmvav 100,68,116\nQta 62,68,50432\ns_waitcnt lgkmcnt(4)\nQmaav 224,72,84\nbuffer_load_dwordx4 a[84:87], v79, s[48:51], s31 offen offset:1024 sc1\n"
"Qmaav 224,76,116\nQmaav 220,72,240\nQmaav 220,76,248\nQmaav 216,72,208\nQta 64,68,41472\nQmaav 216,76,216\nbuffer_load_dwordx4 a[88:91], v79, s[48:51], s31 offen offset:2048 sc1\nQmaav 212,72,176\n"
"Qmaav 212,76,184\nQta 66,68,42496\nQmaav 208,72,144\nQmaav 208,76,152\nbuffer_load_dwordx4 a[92:95], v79, s[48:51], s31 offen offset:3072 sc1\nQmaav 204,72,112\nQta 68,68,49664\nQmaav 204,76,120\n"
"Qmaav 200,72,80\nQmaav 200,76,88\nQta 70,68,50688\ns_add_u32 s31, s74, 4096\ns_waitcnt lgkmcnt(4)\nQmaav 228,56,80\nbuffer_load_dwordx4 a[96:99], v79, s[48:51], s31 offen offset:0 sc1\nQmaav 228,60,88\n"
"Qmaav 232,56,112\nQmaav 232,60,120\nQmaav 236,56,144\nQta 72,68,41728\nQmaav 236,60,152\nbuffer_load_dwordx4 a[100:103], v79, s[48:51], s31 offen offset:1024 sc1\nQmaav 240,56,176\nQmaav 240,60,184\n"
"Qta 74,68,42752\nQmaav 244,56,208\nQmaav 244,60,216\nbuffer_load_dwordx4 a[104:107], v79, s[48:51], s31 offen offset:2048 sc1\nQmaav 248,56,240\nQta 76,68,49920\nQmaav 248,60,248\nQmaav 252,56,84\n"
"Qmaav 252,60,116\nQta 78,68,50944\ns_add_u32 s31, s74, 16384\ns_waitcnt lgkmcnt(4)\nQmvav 172,64,84\nbuffer_load_dwordx4 a[108:111], v79, s[48:51], s31 offen offset:0 sc1\nQmvav 172,68,116\nQmvav 140,64,240\n"
"Qmvav 140,68,248\nQmvav 108,64,208\nQta 56,68,57344\nQmvav 108,68,216\nbuffer_load_dwordx4 a[112:115], v79, s[48:51], s31 offen offset:1024 sc1\nQmvav 228,64,176\nQmvav 228,68,184\nQta 58,68,58368\n"
"Qmvav 196,64,144\nQmvav 196,68,152\nbuffer_load_dwordx4 v[168:171], v79, s[48:51], s31 offen offset:2048 sc1\nQmvav 164,64,112\nQta 60,70,1024\nQmvav 164,68,120\nQmvav 132,64,80\nQmvav 132,68,88\n"
"Qta 62,70,2048\ns_waitcnt lgkmcnt(4)\nQmvav 204,72,80\nbuffer_load_dwordx4 v[200:203], v79, s[48:51], s31 offen offset:3072 sc1\nQmvav 204,76,88\nQmvav 236,72,112\nQmvav 236,76,120\nQmvav 96,72,144\n"
"Qta 64,68,57600\ns_add_u32 s31, s74, 20480\nQmvav 96,76,152\nbuffer_load_dwordx4 v[232:235], v79, s[48:51], s31 offen offset:0 sc1\nQmvav 128,72,176\nQmvav 128,76,184\nQta 66,68,58624\nQmvav 160,72,208\n"
"Qmvav 160,76,216\nbuffer_load_dwordx4 v[212:215], v79, s[48:51], s31 offen offset:1024 sc1\nQmvav 192,72,240\nQta 68,70,1280\nQmvav 192,76,248\nQmvav 224,72,84\nQmvav 224,76,116\nQta 70,70,2304\n"
"s_waitcnt vmcnt(17)\ns_waitcnt lgkmcnt(4)\nQmaaa 140,56,24\nbuffer_load_dwordx4 v[244:247], v79, s[48:51], s31 offen offset:2048 sc1\nQmaaa 140,60,52\nQmaaa 136,56,20\nQmaaa 136,60,48\nQmaaa 132,56,16\n"
"Qta 72,68,57856\ns_add_u32 m0, s96, 98304\ns_add_u32 s95, s67, 4096\nQmaaa 132,60,44\nbuffer_load_dwordx4 v63, s[52:55], s95 offen lds\nQmaaa 128,56,12\nQmaaa 128,60,40\nQta 74,68,58880\n"
"Qmaaa 124,56,8\nQmaaa 124,60,36\nQmaaa 120,56,4\nQta 76,70,1536\ns_add_u32 s95, s67, 23552\nQmaaa 120,60,32\nbuffer_load_dwordx4 v63, s[52:55], s95 offen offset:1024 lds\nQmaaa 116,56,0\n"
"Qmaaa 116,60,28\nQta 78,70,2560\ns_waitcnt lgkmcnt(4)\nQmaaa 144,64,0\nQmaaa 144,68,28\ns_add_u32 m0, s96, 106496\ns_add_u32 s95, s67, 45056\nQmaaa 148,64,4\n"
"buffer_load_dwordx4 v63, s[52:55], s95 offen lds\nQmaaa 148,68,32\nQmaaa 152,64,8\nQta 56,68,58112\nQmaaa 152,68,36\nQmaaa 156,64,12\nQmaaa 156,68,40\nQta 58,68,59136\n"
"s_add_u32 s95, s67, 64512\nQmaaa 160,64,16\nbuffer_load_dwordx4 v63, s[52:55], s95 offen offset:1024 lds\nQmaaa 160,68,44\nQmaaa 164,64,20\nQta 60,70,1792\nQmaaa 164,68,48\nQmaaa 168,64,24\n"
"Qmaaa 168,68,52\nQta 62,70,2816\ns_add_u32 m0, s96, 114688\ns_add_u32 s95, s72, 4096\ns_waitcnt lgkmcnt(4)\nQmvaa 24,72,24\nbuffer_load_dwordx4 v61, s[52:55], s95 offen lds\nQmvaa 24,76,52\n"
"Qmvaa 20,72,20\nQmvaa 20,76,48\nQmvaa 16,72,16\nQta 64,70,9216\ns_add_u32 s95, s72, 23552\nQmvaa 16,76,44\nbuffer_load_dwordx4 v61, s[52:55], s95 offen offset:1024 lds\nQmvaa 12,72,12\n"
"Qmvaa 12,76,40\nQta 66,70,9728\nQmvaa 8,72,8\nQmvaa 8,76,36\nQmvaa 4,72,4\nQta 68,70,10240\nv_add_u32 v75, 1, v75\nQmvaa 4,76,32\n"
"s_waitcnt vmcnt(20)\nds_write_b32 v73, v75\ns_add_u32 m0, s96, 122880\ns_add_u32 s95, s72, 45056\nQmvaa 0,72,0\nbuffer_load_dwordx4 v61, s[52:55], s95 offen lds\nQmvaa 0,76,28\nQta 70,70,10752\n"
"s_waitcnt lgkmcnt(5)\nQmvaa 28,56,0\nQmvaa 28,60,28\nQmvaa 32,56,4\ns_add_u32 s95, s72, 64512\nQmvaa 32,60,32\nbuffer_load_dwordx4 v61, s[52:55], s95 offen offset:1024 lds\nQmvaa 36,56,8\n"
"Qta 72,70,9472\nQmvaa 36,60,36\nds_read_b32 v66, v74\nQmvaa 40,56,12\nQmvaa 40,60,40\nQta 74,70,9984\ns_add_u32 m0, s96, 0\ns_add_u32 s95, s72, 4096\n"
"Qmvaa 44,56,16\nbuffer_load_dwordx4 v62, s[52:55], s95 offen lds\nv_add_u32 v67, 3, v65\nQmvaa 44,60,44\ns_waitcnt lgkmcnt(1)\nv_cmp_gt_u32 vcc, v75, v66\nQmvaa 48,56,20\ns_cbranch_vccnz .Lso1300\n"
".Lro1300:\nQmvaa 48,60,48\nQta 76,70,10496\nQmvaa 52,56,24\nbuffer_store_dword v67, v77, s[24:27], 0 offen sc1\ns_add_u32 s95, s72, 44032\nQmvaa 52,60,52\nbuffer_load_dwordx4 v62, s[52:55], s95 offen offset:1024 lds\n"
"s_waitcnt lgkmcnt(4)\nQmaaa 196,64,24\nQta 78,70,11008\nQmaaa 196,68,52\nQmaaa 192,64,20\nQmaaa 192,68,48\nQmaaa 188,64,16\nQta 56,70,17408\n"
"s_add_u32 m0, s96, 8192\ns_add_u32 s95, s72, 4096\nQmaaa 188,68,44\nbuffer_load_dwordx4 v63, s[52:55], s95 offen lds\nQmaaa 184,64,12\nQmaaa 184,68,40\n.Lpg18:\ns_cbranch_execz .Lpg22\n"
"Qta 58,70,18432\nQmaaa 180,64,8\nQmaaa 180,68,36\nQmaaa 176,64,4\nQta 60,69,0\ns_add_u32 s95, s72, 23552\nQmaaa 176,68,32\nbuffer_load_dwordx4 v63, s[52:55], s95 offen offset:1024 lds\n"
"Qmaaa 172,64,0\nQmaaa 172,68,28\nQta 62,69,1024\ns_waitcnt lgkmcnt(4)\nQmvaa 92,72,0\ns_add_u32 s31, s75, 0\nQmvaa 92,76,28\nbuffer_load_dwordx4 v[80:83], v79, s[48:51], s31 offen offset:0 sc1\n"
"Qmvaa 124,72,4\nQmvaa 124,76,32\nQmvaa 156,72,8\nQta 64,70,17664\nQmvaa 156,76,36\nbuffer_load_dwordx4 v[112:115], v79, s[48:51], s31 offen offset:1024 sc1\nQmvaa 188,72,12\nQmvaa 188,76,40\n"
"Qta 66,70,18688\nQmvaa 220,72,16\nQmvaa 220,76,44\nbuffer_load_dwordx4 v[144:147], v79, s[48:51], s31 offen offset:2048 sc1\nQmvaa 252,72,20\nQta 68,69,256\nQmvaa 252,76,48\nQmvaa 100,72,24\n"
"Qmvaa 100,76,52\nQta 70,69,1280\ns_waitcnt lgkmcnt(4)\nQmaaa 224,56,24\nbuffer_load_dwordx4 v[176:179], v79, s[48:51], s31 offen offset:3072 sc1\nQmaaa 224,60,52\nQmaaa 220,56,20\nQmaaa 220,60,48\n"
"Qmaaa 216,56,16\nQta 72,70,17920\ns_add_u32 s31, s75, 4096\nQmaaa 216,60,44\nbuffer_load_dwordx4 v[208:211], v79, s[48:51], s31 offen offset:0 sc1\nQmaaa 212,56,12\nQmaaa 212,60,40\nQta 74,70,18944\n"
"Qmaaa 208,56,8\nQmaaa 208,60,36\nbuffer_load_dwordx4 v[240:243], v79, s[48:51], s31 offen offset:1024 sc1\nQmaaa 204,56,4\nQta 76,69,512\nQmaaa 204,60,32\nQmaaa 200,56,0\nQmaaa 200,60,28\n"
"Qta 78,69,1536\ns_waitcnt lgkmcnt(4)\nQmaaa 228,64,0\nbuffer_load_dwordx4 v[84:87], v79, s[48:51], s31 offen offset:2048 sc1\nQmaaa 228,68,28\nQmaaa 232,64,4\nQmaaa 232,68,32\nQmaaa 236,64,8\n"
"Qta 56,70,18176\ns_add_u32 s31, s75, 16384\nQmaaa 236,68,36\nbuffer_load_dwordx4 v[88:91], v79, s[48:51], s31 offen offset:0 sc1\nQmaaa 240,64,12\nQmaaa 240,68,40\nQta 58,70,19200\nQmaaa 244,64,16\n"
"Qmaaa 244,68,44\nbuffer_load_dwordx4 v[120:123], v79, s[48:51], s31 offen offset:1024 sc1\nQmaaa 248,64,20\nQta 60,69,768\nQmaaa 248,68,48\nQmaaa 252,64,24\nQmaaa 252,68,52\nQta 62,69,1792\n"
"s_waitcnt lgkmcnt(4)\nQmvaa 172,72,24\nbuffer_load_dwordx4 v[152:155], v79, s[48:51], s31 offen offset:2048 sc1\nQmvaa 172,76,52\nQmvaa 140,72,20\nQmvaa 140,76,48\nQmvaa 108,72,16\nQta 64,69,8192\n"
"Qmvaa 108,76,44\nbuffer_load_dwordx4 v[184:187], v79, s[48:51], s31 offen offset:3072 sc1\nQmvaa 228,72,12\nQmvaa 228,76,40\nQta 66,69,9216\nQmvaa 196,72,8\ns_add_u32 s31, s75, 20480\nQmvaa 196,76,36\n"
"buffer_load_dwordx4 v[216:219], v79, s[48:51], s31 offen offset:0 sc1\nQmvaa 164,72,4\nQta 68,69,16384\nQmvaa 164,76,32\nQmvaa 132,72,0\nQmvaa 132,76,28\nQta 70,69,17408\ns_waitcnt lgkmcnt(4)\n"
"Qmvaa 204,56,0\nbuffer_load_dwordx4 v[248:251], v79, s[48:51], s31 offen offset:1024 sc1\nQmvaa 204,60,28\nQmvaa 236,56,4\nQmvaa 236,60,32\nQmvaa 96,56,8\nQta 72,69,8448\nQmvaa 96,60,36\n"
"buffer_load_dwordx4 v[116:119], v79, s[48:51], s31 offen offset:2048 sc1\nQmvaa 128,56,12\nQmvaa 128,60,40\nQta 74,69,9472\nQmvaa 160,56,16\nQmvaa 160,60,44\nQmvaa 192,56,20\nQta 76,69,16640\n"
"s_add_u32 m0, s96, 16384\ns_add_u32 s95, s72, 45056\nQmvaa 192,60,48\nbuffer_load_dwordx4 v63, s[52:55], s95 offen lds\nQmvaa 224,56,24\nQmvaa 224,60,52\nQta 78,69,17664\ns_waitcnt vmcnt(28)\n"
"s_waitcnt lgkmcnt(4)\nQmaaa 140,64,104\nQmaav 140,68,244\ns_add_u32 s95, s72, 64512\nQmaaa 136,64,100\nbuffer_load_dwordx4 v63, s[52:55], s95 offen offset:1024 lds\nQmaav 136,68,212\nQmaaa 132,64,96\n"
"Qta 56,69,8704\nQmaav 132,68,232\nQmaaa 128,64,92\nQmaav 128,68,200\nQta 58,69,9728\ns_add_u32 m0, s96, 24576\ns_add_u32 s95, s73, 4096\nQmaaa 124,64,88\n"
"buffer_load_dwordx4 v61, s[52:55], s95 offen lds\nQmaav 124,68,168\nQmaaa 120,64,84\nQta 60,69,16896\nQmaaa 120,68,112\nQmaaa 116,64,80\nQmaaa 116,68,108\nQta 62,69,17920\n"
"s_add_u32 s95, s73, 23552\ns_waitcnt lgkmcnt(4)\nQmaaa 144,72,80\nbuffer_load_dwordx4 v61, s[52:55], s95 offen offset:1024 lds\nQmaaa 144,76,108\nQmaaa 148,72,84\nQmaaa 148,76,112\nQmaaa 152,72,88\n"
"Qta 64,69,8960\ns_add_u32 m0, s96, 32768\ns_add_u32 s95, s73, 45056\nQmaav 152,76,168\nbuffer_load_dwordx4 v61, s[52:55], s95 offen lds\nQmaaa 156,72,92\nQmaav 156,76,200\nQta 66,69,9984\n"
"Qmaaa 160,72,96\nQmaav 160,76,232\nQmaaa 164,72,100\nQta 68,69,17152\ns_add_u32 s95, s73, 64512\nQmaav 164,76,212\nbuffer_load_dwordx4 v61, s[52:55], s95 offen offset:1024 lds\nQmaaa 168,72,104\n"
"Qmaav 168,76,244\nQta 70,69,18176\ns_waitcnt lgkmcnt(4)\nQmvaa 24,56,104\nQmvav 24,60,244\ns_add_u32 m0, s96, 40960\ns_add_u32 s95, s73, 4096\nQmvaa 20,56,100\n"
"buffer_load_dwordx4 v62, s[52:55], s95 offen lds\nQmvav 20,60,212\nQmvaa 16,56,96\nQta 72,69,24576\nQmvav 16,60,232\nQmvaa 12,56,92\nQmvav 12,60,200\nQta 74,69,25088\n"
"s_add_u32 s95, s73, 44032\nQmvaa 8,56,88\nbuffer_load_dwordx4 v62, s[52:55], s95 offen offset:1024 lds\nQmvav 8,60,168\nQmvaa 4,56,84\nQta 76,69,25600\nv_add_u32 v75, 1, v75\nQmvaa 4,60,112\n"
"s_waitcnt vmcnt(33)\nds_write_b32 v73, v75\nQmvaa 0,56,80\nQmvaa 0,60,108\nQta 78,69,26112\ns_add_u32 m0, s96, 49152\ns_add_u32 s95, s73, 4096\ns_waitcnt lgkmcnt(5)\n"
"Qmvaa 28,64,80\nbuffer_load_dwordx4 v63, s[52:55], s95 offen lds\nQmvaa 28,68,108\nQmvaa 32,64,84\nQmvaa 32,68,112\nQmvaa 36,64,88\nQta 56,69,24832\nQmvav 36,68,168\n"
"ds_read_b32 v66, v74\ns_add_u32 s95, s73, 23552\nQmvaa 40,64,92\nbuffer_load_dwordx4 v63, s[52:55], s95 offen offset:1024 lds\nQmvav 40,68,200\nQta 58,69,25344\nQmvaa 44,64,96\nQmvav 44,68,232\n"
"s_waitcnt lgkmcnt(1)\nv_cmp_gt_u32 vcc, v75, v66\nQmvaa 48,64,100\ns_cbranch_vccnz .Lso1440\n.Lro1440:\nQmvav 48,68,212\nQta 60,69,25856\nv_add_u32 v75, 1, v75\n"
"Qmvaa 52,64,104\ns_waitcnt vmcnt(29)\nds_write_b32 v73, v75\ns_add_u32 s31, s77, 0\nQmvav 52,68,244\nbuffer_load_dwordx4 a[0:3], v79, s[48:51], s31 offen offset:0 sc1\ns_waitcnt lgkmcnt(5)\nQmaaa 196,72,104\n"
"Qta 62,69,26368\nQmaav 196,76,244\nQmaaa 192,72,100\nQmaav 192,76,212\nbuffer_load_dwordx4 a[4:7], v79, s[48:51], s31 offen offset:1024 sc1\nQmaaa 188,72,96\nQta 64,69,32768\nQmaav 188,76,232\n"
"buffer_load_dword v78, v76, s[24:27], 0 offen sc1\nQmaaa 184,72,92\nds_read_b32 v66, v74\nQmaav 184,76,200\nQta 66,69,33792\nQmaaa 180,72,88\nbuffer_load_dwordx4 a[8:11], v79, s[48:51], s31 offen offset:2048 sc1\nQmaav 180,76,168\n"
"Qmaaa 176,72,84\ns_waitcnt lgkmcnt(1)\nv_cmp_gt_u32 vcc, v75, v66\nQmaaa 176,76,112\ns_cbranch_vccnz .Lso1455\n.Lro1455:\nQmaaa 172,72,80\nQta 68,69,40960\n"
"Qmaaa 172,76,108\nbuffer_load_dwordx4 a[12:15], v79, s[48:51], s31 offen offset:3072 sc1\ns_waitcnt lgkmcnt(4)\nQmvaa 92,56,80\nQmvaa 92,60,108\nQta 70,69,41984\nQmvaa 124,56,84\ns_add_u32 s31, s77, 4096\n"
"Qmvaa 124,60,112\nbuffer_load_dwordx4 a[16:19], v79, s[48:51], s31 offen offset:0 sc1\nQmvaa 156,56,88\nQta 72,69,33024\nQmvav 156,60,168\nQmvaa 188,56,92\nQmvav 188,60,200\nQta 74,69,34048\n"
"Qmvaa 220,56,96\nbuffer_load_dwordx4 a[20:23], v79, s[48:51], s31 offen offset:1024 sc1\nQmvav 220,60,232\nQmvaa 252,56,100\nQta 76,69,41216\nQmvav 252,60,212\nQmvaa 100,56,104\nbuffer_load_dwordx4 a[24:27], v79, s[48:51], s31 offen offset:2048 sc1\n"
"Qmvav 100,60,244\nQta 78,69,42240\ns_waitcnt lgkmcnt(4)\nQmaaa 224,64,104\nQmaav 224,68,244\ns_add_u32 s31, s77, 16384\nQmaaa 220,64,100\nbuffer_load_dwordx4 a[28:31], v79, s[48:51], s31 offen offset:0 sc1\n"
"Qmaav 220,68,212\nQmaaa 216,64,96\nQta 56,69,33280\nQmaav 216,68,232\nQmaaa 212,64,92\nbuffer_load_dwordx4 a[32:35], v79, s[48:51], s31 offen offset:1024 sc1\nQmaav 212,68,200\nQta 58,69,34304\n"
"Qmaaa 208,64,88\nQmaav 208,68,168\nQmaaa 204,64,84\nQta 60,69,41472\nQmaaa 204,68,112\nbuffer_load_dwordx4 a[36:39], v79, s[48:51], s31 offen offset:2048 sc1\nQmaaa 200,64,80\nQmaaa 200,68,108\n"
"Qta 62,69,42496\ns_waitcnt lgkmcnt(4)\nQmaaa 228,72,80\nQmaaa 228,76,108\nbuffer_load_dwordx4 a[40:43], v79, s[48:51], s31 offen offset:3072 sc1\nQmaaa 232,72,84\nQmaaa 232,76,112\nQmaaa 236,72,88\n"
"Qta 64,69,33536\ns_add_u32 s31, s77, 20480\nQmaav 236,76,168\nbuffer_load_dwordx4 a[44:47], v79, s[48:51], s31 offen offset:0 sc1\nQmaaa 240,72,92\nQmaav 240,76,200\nQta 66,69,34560\nQmaaa 244,72,96\n"
"Qmaav 244,76,232\nbuffer_load_dwordx4 a[48:51], v79, s[48:51], s31 offen offset:1024 sc1\nQmaaa 248,72,100\nQta 68,69,41728\nQmaav 248,76,212\nQmaaa 252,72,104\nQmaav 252,76,244\nQta 70,69,42752\n"
"s_waitcnt lgkmcnt(4)\nQmvaa 172,56,104\nbuffer_load_dwordx4 a[52:55], v79, s[48:51], s31 offen offset:2048 sc1\nQmvav 172,60,244\nQmvaa 140,56,100\nQmvav 140,60,212\nQmvaa 108,56,96\nQta 72,69,49152\n"
"s_add_u32 m0, s96, 57344\ns_add_u32 s95, s73, 45056\nQmvav 108,60,232\nbuffer_load_dwordx4 v63, s[52:55], s95 offen lds\nQmvaa 228,56,92\nQmvav 228,60,200\nQta 74,69,50176\nQmvaa 196,56,88\n"
"Qmvav 196,60,168\ns_waitcnt vmcnt(13)\nv_sub_u32 v66, v78, v65\nv_cmp_gt_u32 vcc, 3, v66\ns_and_b64 vcc, vcc, s[28:29]\ns_cbranch_scc1 .Lf3_w\n.Lf3_r:\nQmvaa 164,56,84\n"
"Qta 76,69,57344\ns_add_u32 s95, s73, 64512\nQmvaa 164,60,112\nbuffer_load_dwordx4 v63, s[52:55], s95 offen offset:1024 lds\nv_add_u32 v75, 1, v75\nQmvaa 132,56,80\ns_waitcnt vmcnt(41)\nds_write_b32 v73, v75\n"
"Qmvaa 132,60,108\nQta 78,69,58368\ns_waitcnt lgkmcnt(5)\nQmvaa 204,64,80\ns_add_u32 s31, s74, 131072\nQmvaa 204,68,108\nbuffer_load_dwordx4 a[80:83], v79, s[48:51], s31 offen offset:0 sc1\nQmvaa 236,64,84\n"
"Qmvaa 236,68,112\ns_bitcmp1_b32 s19, 2\ns_cbranch_scc1 .Lrole1\nQmvaa 96,64,88\nQta 56,69,49408\ns_lshl_b32 s46, s19, 4\nQmvav 96,68,168\nbuffer_load_dwordx4 a[84:87], v79, s[48:51], s31 offen offset:1024 sc1\n"
"Qmvaa 128,64,92\nds_read_b32 v66, v74\ns_add_u32 s46, s46, 128\nQmvav 128,68,200\nQta 58,69,50432\nv_mov_b32 v77, 0x7ffff000\nQmvaa 160,64,96\nQmvav 160,68,232\n"
"buffer_load_dwordx4 a[88:91], v79, s[48:51], s31 offen offset:2048 sc1\nQmvaa 192,64,100\ns_waitcnt lgkmcnt(1)\nv_cmp_gt_u32 vcc, v75, v66\nQmvav 192,68,212\ns_cbranch_vccnz .Lso1525r0\n.Lro1525r0:\nQmvaa 224,64,104\n"
"Qta 60,69,57600\nQmvav 224,68,244\nbuffer_load_dwordx4 a[92:95], v79, s[48:51], s31 offen offset:3072 sc1\ns_waitcnt vmcnt(31)\ns_waitcnt lgkmcnt(4)\nQmaav 140,72,84\nQmaav 140,76,116\nQta 62,69,58624\n"
"Qmaav 136,72,240\nQmaav 136,76,248\nQmaav 132,72,208\nQta 64,69,49664\ns_add_u32 m0, s96, 65536\ns_add_u32 s95, s67, 86016\nQmaav 132,76,216\nbuffer_load_dwordx4 v63, s[52:55], s95 offen lds\n"
"Qmaav 128,72,176\nQmaav 128,76,184\nQta 66,69,50688\nQmaav 124,72,144\nQmaav 124,76,152\nQmaav 120,72,112\nQta 68,69,57856\ns_add_u32 s95, s67, 105472\n"
"Qmaav 120,76,120\nbuffer_load_dwordx4 v63, s[52:55], s95 offen offset:1024 lds\nQmaav 116,72,80\nQmaav 116,76,88\nQta 70,69,58880\ns_waitcnt lgkmcnt(4)\nQmaav 144,56,80\nQmaav 144,60,88\n"
"s_add_u32 m0, s96, 73728\ns_add_u32 s95, s67, 126976\nQmaav 148,56,112\nbuffer_load_dwordx4 v63, s[52:55], s95 offen lds\nQmaav 148,60,120\nQmaav 152,56,144\nQta 72,69,49920\nQmaav 152,60,152\n"
"Qmaav 156,56,176\nQmaav 156,60,184\nQta 74,69,50944\ns_add_u32 s95, s67, 146432\nQmaav 160,56,208\nbuffer_load_dwordx4 v63, s[52:55], s95 offen offset:1024 lds\nQmaav 160,60,216\nQmaav 164,56,240\n"
"Qta 76,69,58112\nQmaav 164,60,248\nQmaav 168,56,84\nQmaav 168,60,116\nQta 78,69,59136\ns_add_u32 m0, s96, 81920\ns_add_u32 s95, s72, 86016\ns_waitcnt lgkmcnt(4)\n"
"Qmvav 24,64,84\nbuffer_load_dwordx4 v63, s[52:55], s95 offen lds\nQmvav 24,68,116\nQmvav 20,64,240\nQmvav 20,68,248\nQmvav 16,64,208\nQta 56,68,0\ns_add_u32 s95, s72, 105472\n"
"Qmvav 16,68,216\nbuffer_load_dwordx4 v63, s[52:55], s95 offen offset:1024 lds\nQmvav 12,64,176\nQmvav 12,68,184\nQta 58,68,512\nQmvav 8,64,144\nQmvav 8,68,152\nQmvav 4,64,112\n"
"Qta 60,68,1024\ns_add_u32 m0, s96, 90112\ns_add_u32 s95, s72, 126976\nQmvav 4,68,120\nbuffer_load_dwordx4 v63, s[52:55], s95 offen lds\nQmvav 0,64,80\nQmvav 0,68,88\nQta 62,68,1536\n"
"s_waitcnt lgkmcnt(4)\nQmvav 28,72,80\nQmvav 28,76,88\ns_add_u32 s95, s72, 146432\nQmvav 32,72,112\nbuffer_load_dwordx4 v63, s[52:55], s95 offen offset:1024 lds\nQmvav 32,76,120\nQmvav 36,72,144\n"
"Qta 64,68,256\nQmvav 36,76,152\ns_add_u32 s31, s74, 135168\nQmvav 40,72,176\nbuffer_load_dwordx4 a[96:99], v79, s[48:51], s31 offen offset:0 sc1\nQmvav 40,76,184\nQta 66,68,768\nQmvav 44,72,208\n"
"v_cmp_eq_u32 s[44:45], s46, v60\ns_nop 1\nv_cndmask_b32 v77, v77, v76, s[44:45]\nv_add_u32 v67, 1, v65\ns_xor_b32 s46, s19, 12\ns_lshl_b32 s28, 1, s46\nQmvav 44,76,216\nQmvav 48,72,240\n"
"Qta 68,68,1280\nQmvav 48,76,248\nbuffer_load_dwordx4 a[100:103], v79, s[48:51], s31 offen offset:1024 sc1\nv_add_u32 v75, 1, v75\nQmvav 52,72,84\ns_waitcnt vmcnt(35)\nds_write_b32 v73, v75\nQmvav 52,76,116\n"
"Qta 70,68,1792\ns_waitcnt lgkmcnt(5)\nQmaav 196,56,84\nQmaav 196,60,116\nbuffer_load_dwordx4 a[104:107], v79, s[48:51], s31 offen offset:2048 sc1\nQmaav 192,56,240\nQmaav 192,60,248\nQmaav 188,56,208\n"
"Qta 72,68,8192\ns_add_u32 s31, s74, 147456\nQmaav 188,60,216\nbuffer_load_dwordx4 a[108:111], v79, s[48:51], s31 offen offset:0 sc1\nQmaav 184,56,176\nds_read_b32 v66, v74\nQmaav 184,60,184\nQta 74,68,9216\n"
"Qmaav 180,56,144\nQmaav 180,60,152\nbuffer_load_dwordx4 a[112:115], v79, s[48:51], s31 offen offset:1024 sc1\nQmaav 176,56,112\ns_waitcnt lgkmcnt(1)\nv_cmp_gt_u32 vcc, v75, v66\nQmaav 176,60,120\ns_cbranch_vccnz .Lso1595r0\n"
".Lro1595r0:\nQmaav 172,56,80\nQta 76,68,16384\nQmaav 172,60,88\nbuffer_load_dwordx4 v[168:171], v79, s[48:51], s31 offen offset:2048 sc1\ns_waitcnt lgkmcnt(4)\nQmvav 92,64,80\nQmvav 92,68,88\n"
"Qta 78,68,17408\n.Lpg19:\ns_cbranch_execz .Lpg23\nQmvav 124,64,112\nQmvav 124,68,120\nbuffer_load_dwordx4 v[200:203], v79, s[48:51], s31 offen offset:3072 sc1\nQmvav 156,64,144\nQta 56,68,8448\n"
"Qmvav 156,68,152\nQmvav 188,64,176\nQmvav 188,68,184\nQta 58,68,9472\ns_add_u32 s31, s74, 151552\nQmvav 220,64,208\nbuffer_load_dwordx4 v[232:235], v79, s[48:51], s31 offen offset:0 sc1\nQmvav 220,68,216\n"
"Qmvav 252,64,240\nQta 60,68,16640\nQmvav 252,68,248\nQmvav 100,64,84\nbuffer_load_dwordx4 v[212:215], v79, s[48:51], s31 offen offset:1024 sc1\nQmvav 100,68,116\nQta 62,68,17664\ns_waitcnt lgkmcnt(4)\n"
"Qmaav 224,72,84\nQmaav 224,76,116\nQmaav 220,72,240\nbuffer_load_dwordx4 v[244:247], v79, s[48:51], s31 offen offset:2048 sc1\nQmaav 220,76,248\nQmaav 216,72,208\nQta 64,68,8704\nQmaav 216,76,216\n"
"s_add_u32 m0, s91, 0\ns_add_u32 s31, s77, 131072\nQmaav 212,72,176\nbuffer_load_dwordx4 v79, s[48:51], s31 offen sc1 lds\nQmaav 212,76,184\nQta 66,68,9728\nQmaav 208,72,144\nQmaav 208,76,152\n"
"Qmaav 204,72,112\nQta 68,68,16896\nQmaav 204,76,120\nbuffer_load_dwordx4 v79, s[48:51], s31 offen offset:1024 sc1 lds\nQmaav 200,72,80\nQmaav 200,76,88\nQta 70,68,17920\ns_waitcnt lgkmcnt(4)\n"
"Qmaav 228,56,80\nQmaav 228,60,88\nbuffer_load_dwordx4 v79, s[48:51], s31 offen offset:2048 sc1 lds\nQmaav 232,56,112\nQmaav 232,60,120\nQmaav 236,56,144\nQta 72,68,8960\nQmaav 236,60,152\n"
"buffer_load_dwordx4 v79, s[48:51], s31 offen offset:3072 sc1 lds\nQmaav 240,56,176\nQmaav 240,60,184\nQta 74,68,9984\nQmaav 244,56,208\ns_add_u32 m0, s91, 4096\ns_add_u32 s31, s77, 135168\nQmaav 244,60,216\n"
"buffer_load_dwordx4 v79, s[48:51], s31 offen sc1 lds\nQmaav 248,56,240\nQta 76,68,17152\nQmaav 248,60,248\nQmaav 252,56,84\nQmaav 252,60,116\nQta 78,68,18176\ns_waitcnt lgkmcnt(4)\n"
"Qmvav 172,64,84\nbuffer_load_dwordx4 v79, s[48:51], s31 offen offset:1024 sc1 lds\nQmvav 172,68,116\nQmvav 140,64,240\nQmvav 140,68,248\nQmvav 108,64,208\nQta 56,68,24576\nQmvav 108,68,216\n"
"buffer_load_dwordx4 v79, s[48:51], s31 offen offset:2048 sc1 lds\nQmvav 228,64,176\nQmvav 228,68,184\nQta 58,68,25600\nQmvav 196,64,144\ns_add_u32 m0, s91, 33792\ns_add_u32 s31, s77, 147456\nQmvav 196,68,152\n"
"buffer_load_dwordx4 v79, s[48:51], s31 offen sc1 lds\nQmvav 164,64,112\nQta 60,68,32768\nQmvav 164,68,120\nv_add_u32 v75, 1, v75\nQmvav 132,64,80\ns_waitcnt vmcnt(47)\nds_write_b32 v73, v75\n"
"Qmvav 132,68,88\nQta 62,68,33792\ns_waitcnt lgkmcnt(5)\nQmvav 204,72,80\nbuffer_load_dwordx4 v79, s[48:51], s31 offen offset:1024 sc1 lds\nQmvav 204,76,88\nQmvav 236,72,112\nQmvav 236,76,120\n"
"Qmvav 96,72,144\nQta 64,68,24832\nQmvav 96,76,152\nbuffer_load_dwordx4 v79, s[48:51], s31 offen offset:2048 sc1 lds\nQmvav 128,72,176\nds_read_b32 v66, v74\nQmvav 128,76,184\nQta 66,68,25856\n"
"Qmvav 160,72,208\nQmvav 160,76,216\nbuffer_load_dwordx4 v79, s[48:51], s31 offen offset:3072 sc1 lds\nQmvav 192,72,240\ns_waitcnt lgkmcnt(1)\nv_cmp_gt_u32 vcc, v75, v66\nQmvav 192,76,248\ns_cbranch_vccnz .Lso1665r0\n"
".Lro1665r0:\nQmvav 224,72,84\nQta 68,68,33024\ns_add_u32 m0, s91, 37888\ns_add_u32 s31, s77, 151552\nQmvav 224,76,116\nbuffer_load_dwordx4 v79, s[48:51], s31 offen sc1 lds\ns_waitcnt vmcnt(36)\n"
"s_waitcnt lgkmcnt(4)\nQmaaa 140,56,24\nQmaaa 140,60,52\nQta 70,68,34048\nQmaaa 136,56,20\nQmaaa 136,60,48\nbuffer_load_dwordx4 v79, s[48:51], s31 offen offset:1024 sc1 lds\nQmaaa 132,56,16\n"
"Qta 72,68,25088\nQmaaa 132,60,44\nQmaaa 128,56,12\nQmaaa 128,60,40\nQta 74,68,26112\nQmaaa 124,56,8\nbuffer_load_dwordx4 v79, s[48:51], s31 offen offset:2048 sc1 lds\nQmaaa 124,60,36\n"
"Qmaaa 120,56,4\nQta 76,68,33280\nQmaaa 120,60,32\nQmaaa 116,56,0\nQmaaa 116,60,28\nQta 78,68,34304\ns_add_u32 m0, s96, 0\ns_add_u32 s95, s73, 86016\n"
"s_waitcnt lgkmcnt(4)\nQmaaa 144,64,0\nbuffer_load_dwordx4 v63, s[52:55], s95 offen lds\nQmaaa 144,68,28\nQmaaa 148,64,4\nQmaaa 148,68,32\nQmaaa 152,64,8\nQta 56,68,25344\n"
"s_add_u32 s95, s73, 105472\nQmaaa 152,68,36\nbuffer_load_dwordx4 v63, s[52:55], s95 offen offset:1024 lds\nQmaaa 156,64,12\nQmaaa 156,68,40\nQta 58,68,26368\nQmaaa 160,64,16\nQmaaa 160,68,44\n"
"Qmaaa 164,64,20\nQta 60,68,33536\ns_add_u32 m0, s96, 8192\ns_add_u32 s95, s73, 126976\nQmaaa 164,68,48\nbuffer_load_dwordx4 v63, s[52:55], s95 offen lds\nQmaaa 168,64,24\nQmaaa 168,68,52\n"
"Qta 62,68,34560\ns_waitcnt lgkmcnt(4)\nQmvaa 24,72,24\nQmvaa 24,76,52\ns_add_u32 s95, s73, 146432\nQmvaa 20,72,20\nbuffer_load_dwordx4 v63, s[52:55], s95 offen offset:1024 lds\nQmvaa 20,76,48\n"
"Qmvaa 16,72,16\nQta 64,68,40960\nQmvaa 16,76,44\ns_add_u32 s31, s75, 131072\nQmvaa 12,72,12\nbuffer_load_dwordx4 v[80:83], v79, s[48:51], s31 offen offset:0 sc1\nQmvaa 12,76,40\nQta 66,68,41472\n"
"Qmvaa 8,72,8\nQmvaa 8,76,36\nQmvaa 4,72,4\nQta 68,68,41984\nQmvaa 4,76,32\nbuffer_load_dwordx4 v[112:115], v79, s[48:51], s31 offen offset:1024 sc1\nQmvaa 0,72,0\nQmvaa 0,76,28\n"
"Qta 70,68,42496\ns_waitcnt lgkmcnt(4)\nQmvaa 28,56,0\nQmvaa 28,60,28\nbuffer_load_dwordx4 v[144:147], v79, s[48:51], s31 offen offset:2048 sc1\nQmvaa 32,56,4\nQmvaa 32,60,32\nQmvaa 36,56,8\n"
"Qta 72,68,41216\nQmvaa 36,60,36\nbuffer_load_dwordx4 v[176:179], v79, s[48:51], s31 offen offset:3072 sc1\nQmvaa 40,56,12\nQmvaa 40,60,40\nQta 74,68,41728\nQmvaa 44,56,16\ns_add_u32 s31, s75, 135168\n"
"Qmvaa 44,60,44\nbuffer_load_dwordx4 v[208:211], v79, s[48:51], s31 offen offset:0 sc1\nQmvaa 48,56,20\nQta 76,68,42240\nQmvaa 48,60,48\nv_add_u32 v75, 1, v75\nQmvaa 52,56,24\ns_waitcnt vmcnt(35)\n"
"ds_write_b32 v73, v75\nQmvaa 52,60,52\nQta 78,68,42752\ns_waitcnt lgkmcnt(5)\nQmaaa 196,64,24\nbuffer_load_dwordx4 v[240:243], v79, s[48:51], s31 offen offset:1024 sc1\nQmaaa 196,68,52\nQmaaa 192,64,20\n"
"Qmaaa 192,68,48\nQmaaa 188,64,16\nQta 56,68,49152\nQmaaa 188,68,44\nbuffer_load_dwordx4 v[84:87], v79, s[48:51], s31 offen offset:2048 sc1\nQmaaa 184,64,12\nds_read_b32 v66, v74\nQmaaa 184,68,40\n"
"Qta 58,68,50176\nQmaaa 180,64,8\ns_add_u32 s31, s75, 147456\nQmaaa 180,68,36\nbuffer_load_dwordx4 v[88:91], v79, s[48:51], s31 offen offset:0 sc1\nQmaaa 176,64,4\ns_waitcnt lgkmcnt(1)\nv_cmp_gt_u32 vcc, v75, v66\n"
"Qmaaa 176,68,32\ns_cbranch_vccnz .Lso1735r0\n.Lro1735r0:\nQmaaa 172,64,0\nQta 60,68,57344\nQmaaa 172,68,28\nbuffer_load_dwordx4 v[120:123], v79, s[48:51], s31 offen offset:1024 sc1\ns_waitcnt lgkmcnt(4)\n"
"Qmvaa 92,72,0\nQmvaa 92,76,28\nQta 62,68,58368\nQmvaa 124,72,4\nQmvaa 124,76,32\nbuffer_load_dwordx4 v[152:155], v79, s[48:51], s31 offen offset:2048 sc1\nQmvaa 156,72,8\nQta 64,68,49408\n"
"Qmvaa 156,76,36\nQmvaa 188,72,12\nQmvaa 188,76,40\nQta 66,68,50432\nQmvaa 220,72,16\nbuffer_load_dwordx4 v[184:187], v79, s[48:51], s31 offen offset:3072 sc1\nQmvaa 220,76,44\nQmvaa 252,72,20\n"
"Qta 68,68,57600\nQmvaa 252,76,48\ns_add_u32 s31, s75, 151552\nQmvaa 100,72,24\nbuffer_load_dwordx4 v[216:219], v79, s[48:51], s31 offen offset:0 sc1\nQmvaa 100,76,52\nQta 70,68,58624\ns_waitcnt lgkmcnt(4)\n"
"Qmaaa 224,56,24\nQmaaa 224,60,52\nQmaaa 220,56,20\nbuffer_load_dwordx4 v[248:251], v79, s[48:51], s31 offen offset:1024 sc1\nQmaaa 220,60,48\nQmaaa 216,56,16\nQta 72,68,49664\nQmaaa 216,60,44\n"
"Qmaaa 212,56,12\nbuffer_load_dwordx4 v[116:119], v79, s[48:51], s31 offen offset:2048 sc1\nQmaaa 212,60,40\nQta 74,68,50688\nQmaaa 208,56,8\nQmaaa 208,60,36\nQmaaa 204,56,4\nQta 76,68,57856\n"
"s_add_u32 m0, s96, 16384\ns_add_u32 s95, s67, 86016\nQmaaa 204,60,32\nbuffer_load_dwordx4 v61, s[52:55], s95 offen lds\nQmaaa 200,56,0\nQmaaa 200,60,28\nQta 78,68,58880\ns_waitcnt lgkmcnt(4)\n"
"Qmaaa 228,64,0\nQmaaa 228,68,28\ns_add_u32 s95, s67, 105472\nQmaaa 232,64,4\nbuffer_load_dwordx4 v61, s[52:55], s95 offen offset:1024 lds\nQmaaa 232,68,32\nQmaaa 236,64,8\nQta 56,68,49920\n"
"Qmaaa 236,68,36\nQmaaa 240,64,12\nQmaaa 240,68,40\nQta 58,68,50944\ns_add_u32 m0, s96, 24576\ns_add_u32 s95, s67, 126976\nQmaaa 244,64,16\nbuffer_load_dwordx4 v61, s[52:55], s95 offen lds\n"
"Qmaaa 244,68,44\nQmaaa 248,64,20\nQta 60,68,58112\nQmaaa 248,68,48\nQmaaa 252,64,24\nQmaaa 252,68,52\nQta 62,68,59136\ns_add_u32 s95, s67, 146432\n"
"s_waitcnt lgkmcnt(4)\nQmvaa 172,72,24\nbuffer_load_dwordx4 v61, s[52:55], s95 offen offset:1024 lds\nQmvaa 172,76,52\nQmvaa 140,72,20\nQmvaa 140,76,48\nQmvaa 108,72,16\nQta 64,69,0\n"
"s_add_u32 m0, s96, 32768\ns_add_u32 s95, s72, 86016\nQmvaa 108,76,44\nbuffer_load_dwordx4 v61, s[52:55], s95 offen lds\nQmvaa 228,72,12\nQmvaa 228,76,40\nQta 66,69,1024\nQmvaa 196,72,8\n"
"Qmvaa 196,76,36\nQmvaa 164,72,4\nQta 68,69,8192\ns_add_u32 s95, s72, 105472\nQmvaa 164,76,32\nbuffer_load_dwordx4 v61, s[52:55], s95 offen offset:1024 lds\nv_add_u32 v75, 1, v75\nQmvaa 132,72,0\n"
"s_waitcnt vmcnt(20)\nds_write_b32 v73, v75\nQmvaa 132,76,28\nQta 70,69,9216\ns_waitcnt lgkmcnt(5)\nQmvaa 204,56,0\ns_waitcnt vmcnt(24)\nQda 0,58,0\n"
"Qmvaa 204,60,28\nQmvaa 236,56,4\nQda 28,58,33792\nQmvaa 236,60,32\nQmvaa 96,56,8\nQta 72,69,16384\nQmvaa 96,60,36\nQda 4,58,1024\n"
"Qmvaa 128,56,12\nds_read_b32 v66, v74\nQmvaa 128,60,40\nQta 74,69,17408\nQmvaa 160,56,16\nQda 32,58,34816\nQmvaa 160,60,44\nQmvaa 192,56,20\n"
"s_waitcnt lgkmcnt(2)\nv_cmp_gt_u32 vcc, v75, v66\nQmvaa 192,60,48\ns_cbranch_vccnz .Lso1805r0\n.Lro1805r0:\nQmvaa 224,56,24\nQta 76,69,24576\ns_add_u32 m0, s96, 40960\n"
"s_add_u32 s95, s72, 126976\nQmvaa 224,60,52\nbuffer_load_dwordx4 v61, s[52:55], s95 offen lds\ns_waitcnt vmcnt(39)\ns_waitcnt lgkmcnt(8)\nQmaaa 200,64,80\nQda 8,58,2048\nQmaaa 200,68,108\n"
"Qta 78,69,25600\nQmaaa 204,64,84\nQda 36,58,35840\nQmaaa 204,68,112\nQmaaa 208,64,88\nQta 56,68,0\ns_add_u32 s95, s72, 146432\nQmaav 208,68,168\n"
"buffer_load_dwordx4 v61, s[52:55], s95 offen offset:1024 lds\nQmaaa 212,64,92\nQda 12,58,3072\nQmaav 212,68,200\nQta 58,68,1024\nQmaaa 216,64,96\nQda 40,58,36864\nQmaav 216,68,232\n"
"Qmaaa 220,64,100\nQta 60,68,8192\ns_add_u32 m0, s96, 49152\ns_add_u32 s95, s73, 86016\nQmaav 220,68,212\nbuffer_load_dwordx4 v61, s[52:55], s95 offen lds\nQmaaa 224,64,104\nQda 16,58,4096\n"
"Qmaav 224,68,244\nQta 62,68,9216\ns_waitcnt vmcnt(9)\ns_waitcnt lgkmcnt(8)\nQmaav 200,72,80\nQda 44,58,37888\nQmaav 200,76,88\ns_add_u32 s95, s73, 105472\n"
"Qmaav 204,72,112\nbuffer_load_dwordx4 v61, s[52:55], s95 offen offset:1024 lds\nQmaav 204,76,120\nQda 20,58,5120\nQmaav 208,72,144\nQta 64,69,256\nQmaav 208,76,152\nQda 48,58,38912\n"
"Qmaav 212,72,176\nQmaav 212,76,184\nQta 66,69,1280\ns_add_u32 m0, s96, 57344\ns_add_u32 s95, s73, 126976\nQmaav 216,72,208\nbuffer_load_dwordx4 v61, s[52:55], s95 offen lds\nQmaav 216,76,216\n"
"Qda 24,58,6144\nQmaav 220,72,240\nQta 68,69,8448\nQmaav 220,76,248\nQda 52,58,39936\nQmaav 224,72,84\nQmaav 224,76,116\nQta 70,69,9472\n"
"s_add_u32 s95, s73, 146432\ns_waitcnt lgkmcnt(9)\nQmaaa 200,56,0\nbuffer_load_dwordx4 v61, s[52:55], s95 offen offset:1024 lds\nQmaaa 200,60,28\nQmaaa 204,56,4\nQmaaa 204,60,32\ns_waitcnt lgkmcnt(9)\n"
"Qmaaa 208,56,8\nQta 72,69,16640\nQmaaa 208,60,36\ns_nop 1\nds_write_b128 v58, a[200:203] offset:0\nQmaaa 212,56,12\nQmaaa 212,60,40\nQta 74,69,17664\n"
"s_waitcnt lgkmcnt(4)\nQmaaa 216,56,16\nds_write_b128 v58, a[204:207] offset:1024\nQmaaa 216,60,44\nQmaaa 220,56,20\nQta 76,69,24832\nQmaaa 220,60,48\nds_write_b128 v58, a[208:211] offset:2048\n"
"Qmaaa 224,56,24\nQmaaa 224,60,52\nQta 78,69,25856\ns_waitcnt lgkmcnt(7)\nQmaaa 228,64,80\nds_write_b128 v58, a[212:215] offset:3072\nQmaaa 228,68,108\nQmaaa 232,64,84\n"
"Qmaaa 232,68,112\nds_write_b128 v58, a[216:219] offset:4096\nQmaaa 236,64,88\nQta 56,68,256\nQmaav 236,68,168\nQmaaa 240,64,92\nds_write_b128 v58, a[220:223] offset:5120\nQmaav 240,68,200\n"
"Qta 58,68,1280\nQmaaa 244,64,96\nQmaav 244,68,232\nds_write_b128 v58, a[224:227] offset:6144\nQmaaa 248,64,100\nQta 60,68,8448\nQmaav 248,68,212\nQmaaa 252,64,104\n"
"Qmaav 252,68,244\nQta 62,68,9472\ns_waitcnt lgkmcnt(8)\nQmaav 228,72,80\nv_add_u32 v75, 1, v75\nQmaav 228,76,88\nds_write_b32 v73, v75\nQmaav 232,72,112\n"
"Qmaav 232,76,120\nQmaav 236,72,144\nQta 64,69,512\nQmaav 236,76,152\nQmaav 240,72,176\nQmaav 240,76,184\nQta 66,69,1536\nQmaav 244,72,208\n"
"Qmaav 244,76,216\nds_read_b32 v66, v74\nQmaav 248,72,240\nQta 68,69,8704\nQmaav 248,76,248\nQmaav 252,72,84\nQmaav 252,76,116\ns_waitcnt lgkmcnt(1)\n"
"v_cmp_gt_u32 vcc, v75, v66\ns_waitcnt lgkmcnt(5)\nQmaaa 228,56,0\ns_cbranch_vccnz .Lso1878r0\n.Lro1878r0:\nQmaaa 228,60,28\nQta 70,69,9728\nQmaaa 232,56,4\n"
"Qmaaa 232,60,32\nQdv 148,59,2048\nQmaaa 236,56,8\nQta 72,69,16896\nQmaaa 236,60,36\ns_nop 0\nds_write_b128 v58, a[228:231] offset:33792\nQmaaa 240,56,12\n"
"Qdv 180,59,3072\nQmaaa 240,60,40\nQta 74,69,17920\nQmaaa 244,56,16\nds_write_b128 v58, a[232:235] offset:34816\nQmaaa 244,60,44\nQdv 104,59,5120\n.Lpg20:\n"
"s_cbranch_execz .Lpg24\nQmaaa 248,56,20\nQta 76,69,25088\nQmaaa 248,60,48\nds_write_b128 v58, a[236:239] offset:35840\nQmaaa 252,56,24\nQdv 136,59,0\ns_waitcnt lgkmcnt(9)\n"
"Qa 196,196,148\nQmaaa 252,60,52\nQta 78,69,26112\nQa 197,197,149\ns_waitcnt lgkmcnt(10)\nQmvaa 132,64,80\nds_write_b128 v58, a[240:243] offset:36864\nQmvaa 132,68,108\n"
"Qa 198,198,150\nQa 199,199,151\nQmvaa 164,64,84\ns_waitcnt lgkmcnt(8)\nQa 228,228,180\nQa 229,229,181\nQmvaa 164,68,112\nds_write_b128 v58, a[244:247] offset:37888\n"
"Qmvaa 196,64,88\nQta 56,68,512\nQa 230,230,182\nQa 231,231,183\nQmvav 196,68,168\nQdv 148,59,4096\ns_waitcnt lgkmcnt(8)\nQa 140,140,104\n"
"Qmvaa 228,64,92\nds_write_b128 v58, a[248:251] offset:38912\nQmvav 228,68,200\nQta 58,68,1536\nQa 141,141,105\nQmvaa 108,64,96\nQa 142,142,106\nQa 143,143,107\n"
"Qmvav 108,68,232\nQdv 180,59,6144\ns_waitcnt lgkmcnt(8)\nQa 132,132,136\nQmvaa 140,64,100\nQta 60,68,8704\nQa 133,133,137\nQmvav 140,68,212\n"
"ds_write_b128 v58, a[252:255] offset:39936\nQmvaa 172,64,104\nQa 134,134,138\nQa 135,135,139\nQmvav 172,68,244\nQta 62,68,9728\ns_waitcnt lgkmcnt(6)\nQa 108,108,148\n"
"s_waitcnt lgkmcnt(6)\nQmvav 132,72,80\nQdv 104,59,1024\nv_add_u32 v75, 1, v75\nQmvav 132,76,88\ns_waitcnt vmcnt(10)\nds_write_b32 v73, v75\nQmvav 164,72,112\n"
"Qa 109,109,149\nQa 110,110,150\nQmvav 164,76,120\nQa 111,111,151\ns_waitcnt lgkmcnt(5)\nQa 172,172,180\nQmvav 196,72,144\nQta 64,69,768\n"
"Qa 173,173,181\nQmvav 196,76,152\nQa 174,174,182\nQa 175,175,183\nQmvav 228,72,176\nQmvav 228,76,184\nQta 66,69,1792\ns_waitcnt lgkmcnt(3)\n"
"Qa 164,164,104\nQmvav 108,72,208\nQa 165,165,105\nQa 166,166,106\nQmvav 108,76,216\nds_read_b32 v66, v74\nQa 167,167,107\nQmvav 140,72,240\n"
"Qta 68,69,8960\nQmvav 140,76,248\nQmvav 172,72,84\nQmvav 172,76,116\ns_waitcnt lgkmcnt(1)\nv_cmp_gt_u32 vcc, v75, v66\ns_waitcnt lgkmcnt(6)\nQmvaa 132,56,0\n"
"s_cbranch_vccnz .Lso1920r0\n.Lro1920r0:\ns_add_u32 s68, s64, 0\nQmvaa 132,60,28\nQta 70,69,9984\nQmvaa 164,56,4\nQmvaa 164,60,32\nQdv 136,59,35840\n"
"Qmvaa 196,56,8\nQta 72,69,17152\ns_nop 1\nQk 132,132,133\nQmvaa 196,60,36\nQk 133,134,135\nQmvaa 228,56,12\nQdv 148,59,36864\n"
"Qk 134,164,165\nQmvaa 228,60,40\nQta 74,69,18176\nQk 135,166,167\nQmvaa 108,56,16\nbuffer_store_dwordx4 v[132:135], v79, s[24:27], s68 offen offset:0 sc1\nQk 196,196,197\nQmvaa 108,60,44\n"
"Qdv 180,59,38912\nQk 197,198,199\nQmvaa 140,56,20\nQta 76,69,25344\nQk 198,228,229\nQmvaa 140,60,48\ns_waitcnt lgkmcnt(5)\nQa 96,96,136\n"
"Qa 97,97,137\nQmvaa 172,56,24\nQdv 104,59,33792\nQa 98,98,138\nQmvaa 172,60,52\nQta 78,69,26368\nQa 99,99,139\ns_waitcnt lgkmcnt(11)\n"
"Qmvaa 204,64,80\ns_waitcnt lgkmcnt(5)\nQa 128,128,148\nQa 129,129,149\nQmvaa 204,68,108\nQa 130,130,150\nQa 131,131,151\nQmvaa 236,64,84\n"
"Qk 199,230,231\nQk 108,108,109\nQmvaa 236,68,112\ns_waitcnt lgkmcnt(3)\nQa 192,192,180\nQa 193,193,181\nQmvaa 96,64,88\nQta 56,68,768\n"
"Qa 194,194,182\nQmvav 96,68,168\nQdv 136,59,37888\nQa 195,195,183\nQmvaa 128,64,92\ns_waitcnt lgkmcnt(3)\nQa 204,204,104\nQa 205,205,105\n"
"Qmvav 128,68,200\nQta 58,68,1792\nQa 206,206,106\nQmvaa 160,64,96\nbuffer_store_dwordx4 v[196:199], v79, s[24:27], s68 offen offset:1024 sc1\nQa 207,207,107\nQmvav 160,68,232\nQdv 148,59,39936\n"
"Qk 109,110,111\nQmvaa 192,64,100\nQta 60,68,8960\nQk 110,140,141\nQmvav 192,68,212\nQk 111,142,143\nQk 172,172,173\nQmvaa 224,64,104\n"
"buffer_store_dwordx4 v[108:111], v79, s[24:27], s68 offen offset:2048 sc1\nv_add_u32 v75, 1, v75\nQmvav 224,68,244\ns_waitcnt vmcnt(3)\nds_write_b32 v73, v75\ns_waitcnt lgkmcnt(10)\nQmvav 204,72,80\nQta 62,68,9984\n"
"s_waitcnt lgkmcnt(5)\nQa 160,160,136\nQmvav 204,76,88\nQdv 180,59,34816\nQa 161,161,137\nQmvav 236,72,112\nQa 162,162,138\nQa 163,163,139\n"
"Qmvav 236,76,120\ns_waitcnt lgkmcnt(4)\nQa 224,224,148\nQa 225,225,149\nQmvav 96,72,144\nQta 64,68,16384\nQa 226,226,150\nQmvav 96,76,152\n"
"Qa 227,227,151\nQk 173,174,175\nQmvav 128,72,176\nQmvav 128,76,184\nds_read_b32 v66, v74\ns_waitcnt lgkmcnt(2)\nQa 236,236,180\nQmvav 160,72,208\n"
"Qta 66,68,17408\nQa 237,237,181\nQmvav 160,76,216\nQa 238,238,182\nQa 239,239,183\nQmvav 192,72,240\nQmvav 192,76,248\ns_waitcnt lgkmcnt(1)\n"
"v_cmp_gt_u32 vcc, v75, v66\nQmvav 224,72,84\ns_cbranch_vccnz .Lso1960r0\n.Lro1960r0:\nQmvav 224,76,116\nQta 68,68,24576\ns_add_u32 m0, s96, 65536\ns_add_u32 s95, s67, 86016\n"
"s_waitcnt lgkmcnt(5)\nQmvaa 204,56,0\nbuffer_load_dwordx4 v62, s[52:55], s95 offen lds\nQmvaa 204,60,28\nQmvaa 236,56,4\nQta 70,68,25600\nQmvaa 236,60,32\nQmvaa 96,56,8\n"
"s_nop 3\nQk 174,204,205\nQk 175,206,207\nQmvaa 96,60,36\nQta 72,68,32768\ns_add_u32 s95, s67, 125952\nQmvaa 128,56,12\nbuffer_load_dwordx4 v62, s[52:55], s95 offen offset:1024 lds\n"
"Qmvaa 128,60,40\nbuffer_store_dwordx4 v[172:175], v79, s[24:27], s68 offen offset:3072 sc1\nQk 236,236,237\nQmvaa 160,56,16\nQta 74,68,33792\nQk 237,238,239\nQmvaa 160,60,44\nQk 238,96,97\n"
"Qk 239,98,99\nQmvaa 192,56,20\ns_add_u32 s68, s64, 4096\nQk 128,128,129\nQmvaa 192,60,48\nQta 76,68,40960\ns_add_u32 m0, s96, 73728\ns_add_u32 s95, s72, 86016\n"
"Qmvaa 224,56,24\nbuffer_load_dwordx4 v62, s[52:55], s95 offen lds\nQmvaa 224,60,52\nbuffer_store_dwordx4 v[236:239], v79, s[24:27], s68 offen offset:0 sc1\nQk 129,130,131\ns_waitcnt lgkmcnt(3)\nQmaaa 116,64,80\nQta 78,68,41984\n"
"Qk 130,160,161\nQmaaa 116,68,108\nQk 131,162,163\nQk 192,192,193\nQmaaa 120,64,84\nbuffer_store_dwordx4 v[128:131], v79, s[24:27], s68 offen offset:1024 sc1\ns_add_u32 s95, s72, 125952\nQmaaa 120,68,112\n"
"buffer_load_dwordx4 v62, s[52:55], s95 offen offset:1024 lds\nQmaaa 124,64,88\nQta 56,68,49152\nQk 193,194,195\nQmaav 124,68,168\nQk 194,224,225\nQk 195,226,227\nQmaaa 128,64,92\n"
"buffer_store_dwordx4 v[192:195], v79, s[24:27], s68 offen offset:2048 sc1\nQmaav 128,68,200\nQta 58,68,50176\ns_add_u32 m0, s96, 81920\ns_add_u32 s95, s73, 86016\nQmaaa 132,64,96\nbuffer_load_dwordx4 v62, s[52:55], s95 offen lds\nQmaav 132,68,232\n"
"Qmaaa 136,64,100\nQta 60,68,57344\nQmaav 136,68,212\nQmaaa 140,64,104\nQmaav 140,68,244\nQta 62,68,58368\ns_add_u32 s95, s73, 125952\ns_waitcnt lgkmcnt(4)\n"
"Qmaav 116,72,80\nbuffer_load_dwordx4 v62, s[52:55], s95 offen offset:1024 lds\nQmaav 116,76,88\nQmaav 120,72,112\nv_bfe_u32 v61, v60, 6, 2\nv_lshrrev_b32 v62, 8, v60\nQmaav 120,76,120\nv_xor_b32 v61, v61, v62\n"
"v_bfe_u32 v62, v60, 5, 1\nQmaav 124,72,144\nQta 64,68,16640\nv_lshl_or_b32 v61, v61, 1, v62\nQmaav 124,76,152\nv_and_b32 v62, 16, v60\nv_mov_b32 v63, 0x3f80\nQmaav 128,72,176\n"
"v_lshlrev_b32 v63, v62, v63\nv_min_u32 v62, 1, v61\nQmaav 128,76,184\nQta 66,68,17664\nv_add_u32 v62, -1, v62\nQmaav 132,72,208\nv_and_b32 v62, v62, v63\nv_xor_b32 v61, 1, v61\n"
"Qmaav 132,76,216\nv_min_u32 v61, 1, v61\nv_add_u32 v61, -1, v61\nQmaav 136,72,240\nQta 68,68,24832\nv_and_b32 v61, v61, v63\nQmaav 136,76,248\nv_accvgpr_write_b32 a202, v62\n"
"v_accvgpr_write_b32 a203, v61\nQmaav 140,72,84\nv_accvgpr_write_b32 a200, 0\nv_accvgpr_write_b32 a201, 0\nQmaav 140,76,116\nQta 70,68,25856\nv_accvgpr_write_b32 a204, 0\ns_waitcnt lgkmcnt(4)\n"
"Qmaaa 116,56,0\nv_accvgpr_write_b32 a205, 0\nQmaaa 116,60,28\nQmaaa 120,56,4\nQmaaa 120,60,32\nQmaaa 124,56,8\nQta 72,68,33024\nQmaaa 124,60,36\n"
"s_nop 2\nds_write_b128 v58, a[116:119] offset:0\nQmaaa 128,56,12\nQmaaa 128,60,40\nQta 74,68,34048\nQmaaa 132,56,16\nds_write_b128 v58, a[120:123] offset:1024\nQmaaa 132,60,44\n"
"Qmaaa 136,56,20\nQta 76,68,41216\nQmaaa 136,60,48\nds_write_b128 v58, a[124:127] offset:2048\nQmaaa 140,56,24\nQmaaa 140,60,52\nQta 78,68,42240\ns_waitcnt lgkmcnt(7)\n"
"Qmaaa 144,64,80\nds_write_b128 v58, a[128:131] offset:3072\nQmaaa 144,68,108\nQmaaa 148,64,84\nQmaaa 148,68,112\nds_write_b128 v58, a[132:135] offset:4096\nQmaaa 152,64,88\nQta 56,68,49408\n"
"Qmaav 152,68,168\nQmaaa 156,64,92\nds_write_b128 v58, a[136:139] offset:5120\nQmaav 156,68,200\nQta 58,68,50432\nQmaaa 160,64,96\nQmaav 160,68,232\nds_write_b128 v58, a[140:143] offset:6144\n"
"Qmaaa 164,64,100\nQta 60,68,57600\nQmaav 164,68,212\nQmaaa 168,64,104\nQmaav 168,68,244\nQta 62,68,58624\ns_waitcnt lgkmcnt(8)\nQmaav 144,72,80\n"
"v_add_u32 v75, 1, v75\nQmaav 144,76,88\ns_waitcnt vmcnt(8)\nds_write_b32 v73, v75\nQmaav 148,72,112\nQmaav 148,76,120\nQmaav 152,72,144\nQta 64,68,16896\n"
"Qmaav 152,76,152\nQmaav 156,72,176\nQmaav 156,76,184\nQta 66,68,17920\nQmaav 160,72,208\nQmaav 160,76,216\nds_read_b32 v66, v74\nQmaav 164,72,240\n"
"Qta 68,68,25088\ns_waitcnt vmcnt(2)\nbuffer_store_dword v67, v77, s[24:27], 0 offen sc1\nQmaav 164,76,248\nQmaav 168,72,84\nQmaav 168,76,116\ns_waitcnt lgkmcnt(1)\nv_cmp_gt_u32 vcc, v75, v66\n"
"s_waitcnt lgkmcnt(5)\nQmaaa 144,56,0\ns_cbranch_vccnz .Lso2046r0\n.Lro2046r0:\nQmaaa 144,60,28\nQta 70,68,26112\nQmaaa 148,56,4\nQmaaa 148,60,32\n"
"Qdv 104,59,2048\nQmaaa 152,56,8\nQta 72,68,33280\nQmaaa 152,60,36\ns_nop 0\nds_write_b128 v58, a[144:147] offset:33792\nQmaaa 156,56,12\nQdv 136,59,3072\n"
"Qmaaa 156,60,40\nQta 74,68,34304\nQmaaa 160,56,16\nds_write_b128 v58, a[148:151] offset:34816\nQmaaa 160,60,44\nQdv 148,59,5120\nQmaaa 164,56,20\nQta 76,68,41472\n"
"Qmaaa 164,60,48\nds_write_b128 v58, a[152:155] offset:35840\nQmaaa 168,56,24\nQdv 180,59,0\ns_waitcnt lgkmcnt(9)\nQa 8,8,104\nQmaaa 168,60,52\nQta 78,68,42496\n"
"Qa 9,9,105\ns_waitcnt lgkmcnt(10)\nQmvaa 0,64,80\nds_write_b128 v58, a[156:159] offset:36864\nQmvaa 0,68,108\nQa 10,10,106\nQa 11,11,107\nQmvaa 4,64,84\n"
"s_waitcnt lgkmcnt(8)\nQa 12,12,136\nQa 13,13,137\nQmvaa 4,68,112\nds_write_b128 v58, a[160:163] offset:37888\nQmvaa 8,64,88\nQta 56,68,49664\nQa 14,14,138\n"
"Qa 15,15,139\nQmvav 8,68,168\nQdv 104,59,4096\ns_waitcnt lgkmcnt(8)\nQa 20,20,148\nQmvaa 12,64,92\nds_write_b128 v58, a[164:167] offset:38912\nQmvav 12,68,200\n"
"Qta 58,68,50688\nQa 21,21,149\nQmvaa 16,64,96\nQa 22,22,150\nQa 23,23,151\nQmvav 16,68,232\nQdv 136,59,6144\ns_waitcnt lgkmcnt(8)\n"
"Qa 0,0,180\nQmvaa 20,64,100\nQta 60,68,57856\nQa 1,1,181\nQmvav 20,68,212\nds_write_b128 v58, a[168:171] offset:39936\nQmvaa 24,64,104\nQa 2,2,182\n"
"Qa 3,3,183\nQmvav 24,68,244\nQta 62,68,58880\ns_waitcnt lgkmcnt(6)\nQa 16,16,104\ns_waitcnt lgkmcnt(6)\nQmvav 0,72,80\nQdv 148,59,1024\n"
"v_add_u32 v75, 1, v75\nQmvav 0,76,88\ns_waitcnt vmcnt(1)\nds_write_b32 v73, v75\nQmvav 4,72,112\nQa 17,17,105\nQa 18,18,106\nQmvav 4,76,120\n"
"Qa 19,19,107\ns_waitcnt lgkmcnt(5)\nQa 24,24,136\nQmvav 8,72,144\nQta 64,69,0\nQa 25,25,137\nQmvav 8,76,152\nQa 26,26,138\n"
"Qa 27,27,139\nQmvav 12,72,176\nQmvav 12,76,184\nQta 66,69,512\ns_waitcnt lgkmcnt(3)\nQa 4,4,148\nQmvav 16,72,208\nQa 5,5,149\n"
"Qa 6,6,150\nQmvav 16,76,216\nds_read_b32 v66, v74\nQa 7,7,151\nQmvav 20,72,240\nQta 68,69,1024\nQmvav 20,76,248\nQmvav 24,72,84\n"
"Qmvav 24,76,116\ns_waitcnt lgkmcnt(1)\nv_cmp_gt_u32 vcc, v75, v66\ns_waitcnt lgkmcnt(6)\nQmvaa 0,56,0\ns_cbranch_vccnz .Lso2088r0\n.Lro2088r0:\nQmvaa 0,60,28\n"
"Qta 70,69,1536\nQmvaa 4,56,4\nQmvaa 4,60,32\nQdv 180,59,33792\nQmvaa 8,56,8\nQta 72,69,8192\nQmvaa 8,60,36\nQmvaa 12,56,12\n"
"Qdv 104,59,34816\nQmvaa 12,60,40\nQta 74,69,8704\nQmvaa 16,56,16\nQmvaa 16,60,44\nQdv 136,59,35840\nQmvaa 20,56,20\nQta 76,69,9216\n"
"Qmvaa 20,60,48\ns_waitcnt lgkmcnt(5)\nQa 28,28,180\nQa 29,29,181\nQmvaa 24,56,24\nQdv 148,59,36864\nQa 30,30,182\nQmvaa 24,60,52\n"
"Qta 78,69,9728\nQa 31,31,183\ns_waitcnt lgkmcnt(11)\nQmaaa 172,64,80\ns_waitcnt lgkmcnt(5)\nQa 32,32,104\nQa 33,33,105\nQmaaa 172,68,108\n"
"Qa 34,34,106\nQa 35,35,107\nQmaaa 176,64,84\nQmaaa 176,68,112\ns_waitcnt lgkmcnt(3)\nQa 36,36,136\nQa 37,37,137\nQmaaa 180,64,88\n"
"Qta 56,69,16384\nQa 38,38,138\nQmaav 180,68,168\nQdv 180,59,37888\nQa 39,39,139\nQmaaa 184,64,92\ns_waitcnt lgkmcnt(3)\nQa 40,40,148\n"
"Qa 41,41,149\nQmaav 184,68,200\nQta 58,69,16896\nQa 42,42,150\nQmaaa 188,64,96\nQdv 104,59,38912\nQa 43,43,151\nQmaav 188,68,232\n"
"Qmaaa 192,64,100\nQta 60,69,17408\nQmaav 192,68,212\nQdv 136,59,39936\nQmaaa 196,64,104\ns_waitcnt lgkmcnt(4)\nQa 44,44,180\nQa 45,45,181\n"
"Qmaav 196,68,244\nQta 62,69,17920\nQa 46,46,182\ns_waitcnt lgkmcnt(5)\nQmaav 172,72,80\nQa 47,47,183\nQmaav 172,76,88\nQmaav 176,72,112\n"
"s_waitcnt lgkmcnt(3)\nQa 48,48,104\nQa 49,49,105\nQmaav 176,76,120\nQa 50,50,106\nQa 51,51,107\nQmaav 180,72,144\nQta 64,68,17152\n"
"Qmaav 180,76,152\ns_waitcnt lgkmcnt(2)\nQa 52,52,136\nQa 53,53,137\nQmaav 184,72,176\n.Lpg21:\ns_cbranch_execz .Lpg25\nQa 54,54,138\n"
"Qa 55,55,139\nQmaav 184,76,184\nQta 66,68,18176\nQmaav 188,72,208\nQmaav 188,76,216\nQmaav 192,72,240\nQta 68,68,25344\nQmaav 192,76,248\n"
"Qmaav 196,72,84\nQmaav 196,76,116\nQta 70,68,26368\ns_waitcnt lgkmcnt(4)\nQmaaa 172,56,0\nQmaaa 172,60,28\nQmaaa 176,56,4\nQmaaa 176,60,32\n"
"Qmaaa 180,56,8\nQta 72,68,33536\nQmaaa 180,60,36\ns_nop 2\nds_write_b128 v58, a[172:175] offset:0\nQmaaa 184,56,12\nQmaaa 184,60,40\nQta 74,68,34560\n"
"Qmaaa 188,56,16\nds_write_b128 v58, a[176:179] offset:1024\nQmaaa 188,60,44\nQmaaa 192,56,20\nQta 76,68,41728\nQmaaa 192,60,48\nds_write_b128 v58, a[180:183] offset:2048\nQmaaa 196,56,24\n"
"Qmaaa 196,60,52\nQta 78,68,42752\ns_waitcnt lgkmcnt(7)\nQmvaa 28,64,80\nds_write_b128 v58, a[184:187] offset:3072\nQmvaa 28,68,108\nQmvaa 32,64,84\nQmvaa 32,68,112\n"
"ds_write_b128 v58, a[188:191] offset:4096\nQmvaa 36,64,88\nQta 56,68,49920\nQmvav 36,68,168\nQmvaa 40,64,92\nds_write_b128 v58, a[192:195] offset:5120\nQmvav 40,68,200\nQta 58,68,50944\n"
"Qmvaa 44,64,96\nQmvav 44,68,232\nds_write_b128 v58, a[196:199] offset:6144\nQmvaa 48,64,100\nQta 60,68,58112\nQmvav 48,68,212\nQmvaa 52,64,104\nQmvav 52,68,244\n"
"Qta 62,68,59136\ns_waitcnt lgkmcnt(8)\nQmvav 28,72,80\nv_add_u32 v75, 1, v75\nQmvav 28,76,88\nds_write_b32 v73, v75\nQmvav 32,72,112\nQmvav 32,76,120\n"
"Qmvav 36,72,144\nQta 64,69,256\nQmvav 36,76,152\nQmvav 40,72,176\nQmvav 40,76,184\nQta 66,69,768\nQmvav 44,72,208\nQmvav 44,76,216\n"
"ds_read_b32 v66, v74\nQmvav 48,72,240\nQta 68,69,1280\nQmvav 48,76,248\nQmvav 52,72,84\nQmvav 52,76,116\ns_waitcnt lgkmcnt(1)\nv_cmp_gt_u32 vcc, v75, v66\n"
"s_waitcnt lgkmcnt(5)\nQmvaa 28,56,0\ns_cbranch_vccnz .Lso2172r0\n.Lro2172r0:\nQmvaa 28,60,28\nbuffer_load_dword v78, v76, s[24:27], 0 offen sc1\nQmvaa 32,56,4\nQta 70,69,1792\n"
"Qmvaa 32,60,32\nQdv 148,59,2048\nQmvaa 36,56,8\nQmvaa 36,60,36\nQta 72,69,8448\nQmvaa 40,56,12\nQdv 180,59,3072\nQmvaa 40,60,40\n"
"Qmvaa 44,56,16\nQta 74,69,8960\nQmvaa 44,60,44\nQdv 104,59,5120\nQmvaa 48,56,20\nQmvaa 48,60,48\nQta 76,69,9472\ns_waitcnt lgkmcnt(5)\n"
"Qa 156,156,148\nQmvaa 52,56,24\nQdv 136,59,0\nQa 157,157,149\nQmvaa 52,60,52\nQa 158,158,150\nQa 159,159,151\ns_waitcnt lgkmcnt(10)\n"
"Qmvaa 92,64,80\nQta 78,69,9984\ns_waitcnt lgkmcnt(5)\nQa 188,188,180\nQmvaa 92,68,108\nQa 189,189,181\nQa 190,190,182\nQmvaa 124,64,84\n"
"Qa 191,191,183\nQmvaa 124,68,112\ns_waitcnt lgkmcnt(3)\nQa 252,252,104\nQa 253,253,105\nQmvaa 156,64,88\nQta 56,69,16640\nQa 254,254,106\n"
"Qmvav 156,68,168\nQdv 148,59,4096\nQa 255,255,107\nQmvaa 188,64,92\ns_waitcnt lgkmcnt(3)\nQa 92,92,136\nQa 93,93,137\nQmvav 188,68,200\n"
"Qta 58,69,17152\nQa 94,94,138\nQmvaa 220,64,96\nQa 95,95,139\nQmvav 220,68,232\nQdv 180,59,6144\nQmvaa 252,64,100\nQta 60,69,17664\n"
"Qmvav 252,68,212\nQmvaa 100,64,104\nQmvav 100,68,244\nQta 62,69,18176\ns_waitcnt lgkmcnt(4)\nQa 220,220,148\ns_waitcnt lgkmcnt(4)\nQmvav 92,72,80\n"
"Qdv 104,59,1024\nQa 221,221,149\nQmvav 92,76,88\nQa 222,222,150\nQa 223,223,151\nQmvav 124,72,112\nQmvav 124,76,120\ns_waitcnt vmcnt(0)\n"
"v_sub_u32 v66, v78, v65\nv_cmp_gt_u32 vcc, 1, v66\ns_and_b64 vcc, vcc, s[28:29]\ns_cbranch_scc1 .Lx0_w\n.Lx0_r:\nQmvav 156,72,144\ns_add_u32 s69, s98, 0\nbuffer_load_dwordx4 a[208:211], v79, s[24:27], s69 offen offset:0 sc1\n"
"Qmvav 156,76,152\ns_waitcnt lgkmcnt(3)\nQa 100,100,180\nQa 101,101,181\nQmvav 188,72,176\nbuffer_load_dwordx4 a[212:215], v79, s[24:27], s69 offen offset:1024 sc1\nQmvav 188,76,184\nQa 102,102,182\n"
"Qa 103,103,183\nQmvav 220,72,208\nbuffer_load_dwordx4 a[216:219], v79, s[24:27], s69 offen offset:2048 sc1\nv_add_u32 v75, 1, v75\nQmvav 220,76,216\nds_write_b32 v73, v75\ns_waitcnt lgkmcnt(1)\nQa 124,124,104\n"
"Qmvav 252,72,240\nbuffer_load_dwordx4 a[220:223], v79, s[24:27], s69 offen offset:3072 sc1\nQmvav 252,76,248\nQa 125,125,105\nQa 126,126,106\nQmvav 100,72,84\ns_add_u32 s69, s98, 4096\nbuffer_load_dwordx4 a[236:239], v79, s[24:27], s69 offen offset:3072 sc1\n"
"Qmvav 100,76,116\nQa 127,127,107\ns_waitcnt lgkmcnt(2)\nQmvaa 92,56,0\nbuffer_load_dwordx4 a[224:227], v79, s[24:27], s69 offen offset:0 sc1\nQmvaa 92,60,28\nQmvaa 124,56,4\ns_add_u32 s69, s98, 16384\n"
"buffer_load_dwordx4 a[240:243], v79, s[24:27], s69 offen offset:0 sc1\nQmvaa 124,60,32\nds_read_b32 v66, v74\nQmvaa 156,56,8\ns_add_u32 s69, s98, 4096\nbuffer_load_dwordx4 a[228:231], v79, s[24:27], s69 offen offset:1024 sc1\nQmvaa 156,60,36\nQmvaa 188,56,12\n"
"s_add_u32 s69, s98, 16384\nbuffer_load_dwordx4 a[244:247], v79, s[24:27], s69 offen offset:1024 sc1\nQmvaa 188,60,40\ns_waitcnt lgkmcnt(0)\nv_cmp_gt_u32 vcc, v75, v66\nQmvaa 220,56,16\ns_cbranch_vccnz .Lso2222r0\n.Lro2222r0:\n"
"Qmvaa 220,60,44\ns_add_u32 s69, s98, 4096\nbuffer_load_dwordx4 a[232:235], v79, s[24:27], s69 offen offset:2048 sc1\nQmvaa 252,56,20\nQmvaa 252,60,48\ns_add_u32 s69, s98, 16384\nbuffer_load_dwordx4 a[248:251], v79, s[24:27], s69 offen offset:2048 sc1\nQmvaa 100,56,24\n"
"Qmvaa 100,60,52\ns_waitcnt vmcnt(10)\nQmvaa 92,202,208\nQmvaa 124,200,208\ns_waitcnt vmcnt(9)\nQmvaa 156,202,212\nQmvaa 188,200,212\ns_nop 3\n"
"Qk 92,92,93\nQk 93,94,95\nds_write_b64 v56, v[92:93] offset:64\ns_waitcnt vmcnt(8)\nQmvaa 220,202,216\nQk 124,124,125\nQk 125,126,127\nds_write_b64 v56, v[124:125] offset:3392\n"
"Qmvaa 252,200,216\nQk 156,156,157\nQk 157,158,159\nds_write_b64 v56, v[156:157] offset:6720\ns_waitcnt vmcnt(7)\nQmvaa 100,202,220\nQk 188,188,189\nQk 189,190,191\n"
"ds_write_b64 v56, v[188:189] offset:10048\nQmvaa 0,200,220\nQk 220,220,221\nQk 221,222,223\nds_write_b64 v56, v[220:221] offset:13376\ns_waitcnt vmcnt(6)\nQmvaa 28,202,236\nQk 252,252,253\n"
"Qk 253,254,255\nds_write_b64 v56, v[252:253] offset:16704\ns_waitcnt vmcnt(5)\nQmvaa 4,202,224\nQk 100,100,101\nQk 101,102,103\nds_write_b64 v56, v[100:101] offset:20032\ns_waitcnt lgkmcnt(0)\n"
"s_barrier\nQda 28,71,64\nQda 32,71,6720\nQda 36,71,13376\nQda 40,71,20032\nQmvaa 32,200,236\nQk 0,0,1\nQk 1,2,3\n"
"ds_write_b64 v56, v[0:1] offset:0\ns_mov_b32 s68, 0\ns_waitcnt lgkmcnt(4)\nbuffer_store_dwordx4 a[28:31], v72, s[40:43], s68 offen offset:0 sc1\nQmvaa 8,200,224\nQk 28,28,29\nQk 29,30,31\nds_write_b64 v56, v[28:29] offset:32\n"
"s_mov_b32 s68, 81920\ns_waitcnt lgkmcnt(4)\nbuffer_store_dwordx4 a[32:35], v72, s[40:43], s68 offen offset:0 sc1\ns_waitcnt vmcnt(6)\nQmvaa 36,202,240\nQk 4,4,5\nQk 5,6,7\nds_write_b64 v56, v[4:5] offset:3328\n"
"s_mov_b32 s68, 163840\ns_waitcnt lgkmcnt(4)\nbuffer_store_dwordx4 a[36:39], v72, s[40:43], s68 offen offset:0 sc1\ns_waitcnt vmcnt(6)\nQmvaa 12,202,228\nQk 32,32,33\nQk 33,34,35\nds_write_b64 v56, v[32:33] offset:3360\n"
"s_mov_b32 s68, 245760\ns_cmp_eq_u32 s93, 1\ns_cselect_b32 s68, 0x7ffff000, s68\ns_waitcnt lgkmcnt(4)\nbuffer_store_dwordx4 a[40:43], v72, s[40:43], s68 offen offset:0 sc1\nQmvaa 40,200,240\nQk 8,8,9\nQk 9,10,11\n"
"ds_write_b64 v56, v[8:9] offset:6656\nQmvaa 16,200,228\nQk 36,36,37\nQk 37,38,39\nds_write_b64 v56, v[36:37] offset:6688\ns_waitcnt vmcnt(6)\nQmvaa 44,202,244\nQk 12,12,13\n"
"Qk 13,14,15\nds_write_b64 v56, v[12:13] offset:9984\ns_waitcnt vmcnt(5)\nQmvaa 20,202,232\nQk 40,40,41\nQk 41,42,43\nds_write_b64 v56, v[40:41] offset:10016\ns_waitcnt lgkmcnt(0)\n"
"s_barrier\nQda 0,57,0\nQda 4,57,3328\nQda 8,57,6656\nQda 12,57,9984\nQmvaa 48,200,244\nQk 16,16,17\nQk 17,18,19\n"
"ds_write_b64 v56, v[16:17] offset:13312\ns_mov_b32 s68, 0\ns_waitcnt lgkmcnt(4)\nbuffer_store_dwordx4 a[0:3], v64, s[40:43], s68 offen offset:0 sc1\nQmvaa 24,200,232\nQk 44,44,45\nQk 45,46,47\nds_write_b64 v56, v[44:45] offset:13344\n"
"s_mov_b32 s68, 40960\ns_waitcnt lgkmcnt(4)\nbuffer_store_dwordx4 a[4:7], v64, s[40:43], s68 offen offset:0 sc1\ns_waitcnt vmcnt(6)\nQmvaa 52,202,248\nQk 20,20,21\nQk 21,22,23\nds_write_b64 v56, v[20:21] offset:16640\n"
"s_mov_b32 s68, 81920\ns_waitcnt lgkmcnt(4)\nbuffer_store_dwordx4 a[8:11], v64, s[40:43], s68 offen offset:0 sc1\nQk 48,48,49\nQk 49,50,51\nds_write_b64 v56, v[48:49] offset:16672\ns_mov_b32 s68, 122880\ns_waitcnt lgkmcnt(4)\n"
"buffer_store_dwordx4 a[12:15], v64, s[40:43], s68 offen offset:0 sc1\nQk 24,24,25\nQk 25,26,27\nds_write_b64 v56, v[24:25] offset:19968\nQk 52,52,53\nQk 53,54,55\nds_write_b64 v56, v[52:53] offset:20000\ns_waitcnt lgkmcnt(0)\n"
"s_barrier\nQda 16,57,13312\nQda 20,57,16640\nQda 24,57,19968\ns_mov_b32 s68, 163840\ns_waitcnt lgkmcnt(2)\nbuffer_store_dwordx4 a[16:19], v64, s[40:43], s68 offen offset:0 sc1\ns_mov_b32 s68, 204800\n"
"s_waitcnt lgkmcnt(1)\nbuffer_store_dwordx4 a[20:23], v64, s[40:43], s68 offen offset:0 sc1\ns_mov_b32 s68, 245760\ns_waitcnt lgkmcnt(0)\nbuffer_store_dwordx4 a[24:27], v64, s[40:43], s68 offen offset:0 sc1\ns_branch .Lfin\n.Lrole1:\n.Lpg22:\n"
"s_cbranch_execz .Lpg26\nQmvaa 96,64,88\nQta 56,69,49408\ns_lshl_b32 s46, s19, 4\nQmvav 96,68,168\nbuffer_load_dwordx4 a[84:87], v79, s[48:51], s31 offen offset:1024 sc1\nQmvaa 128,64,92\nds_read_b32 v66, v74\n"
"s_add_u32 s46, s46, 128\nQmvav 128,68,200\nQta 58,69,50432\nv_mov_b32 v77, 0x7ffff000\nQmvaa 160,64,96\nQmvav 160,68,232\nbuffer_load_dwordx4 a[88:91], v79, s[48:51], s31 offen offset:2048 sc1\nQmvaa 192,64,100\n"
"s_waitcnt lgkmcnt(1)\nv_cmp_gt_u32 vcc, v75, v66\nQmvav 192,68,212\ns_cbranch_vccnz .Lso1525r1\n.Lro1525r1:\nQmvaa 224,64,104\nQta 60,69,57600\nQmvav 224,68,244\n"
"buffer_load_dwordx4 a[92:95], v79, s[48:51], s31 offen offset:3072 sc1\ns_waitcnt vmcnt(31)\ns_waitcnt lgkmcnt(4)\nQmaav 140,72,84\nQmaav 140,76,116\nQta 62,69,58624\nQmaav 136,72,240\nQmaav 136,76,248\n"
"Qmaav 132,72,208\nQta 64,69,49664\ns_add_u32 m0, s96, 65536\ns_add_u32 s95, s67, 86016\nQmaav 132,76,216\nbuffer_load_dwordx4 v62, s[52:55], s95 offen lds\nQmaav 128,72,176\nQmaav 128,76,184\n"
"Qta 66,69,50688\nQmaav 124,72,144\nQmaav 124,76,152\nQmaav 120,72,112\nQta 68,69,57856\ns_add_u32 s95, s67, 125952\nQmaav 120,76,120\nbuffer_load_dwordx4 v62, s[52:55], s95 offen offset:1024 lds\n"
"Qmaav 116,72,80\nQmaav 116,76,88\nQta 70,69,58880\ns_waitcnt lgkmcnt(4)\nQmaav 144,56,80\nQmaav 144,60,88\ns_add_u32 m0, s96, 73728\ns_add_u32 s95, s72, 86016\n"
"Qmaav 148,56,112\nbuffer_load_dwordx4 v62, s[52:55], s95 offen lds\nQmaav 148,60,120\nQmaav 152,56,144\nQta 72,69,49920\nQmaav 152,60,152\nQmaav 156,56,176\nQmaav 156,60,184\n"
"Qta 74,69,50944\ns_add_u32 s95, s72, 125952\nQmaav 160,56,208\nbuffer_load_dwordx4 v62, s[52:55], s95 offen offset:1024 lds\nQmaav 160,60,216\nQmaav 164,56,240\nQta 76,69,58112\nQmaav 164,60,248\n"
"Qmaav 168,56,84\nQmaav 168,60,116\nQta 78,69,59136\ns_add_u32 m0, s96, 81920\ns_add_u32 s95, s73, 86016\ns_waitcnt lgkmcnt(4)\nQmvav 24,64,84\nbuffer_load_dwordx4 v62, s[52:55], s95 offen lds\n"
"Qmvav 24,68,116\nQmvav 20,64,240\nQmvav 20,68,248\nQmvav 16,64,208\nQta 56,68,0\ns_add_u32 s95, s73, 125952\nQmvav 16,68,216\nbuffer_load_dwordx4 v62, s[52:55], s95 offen offset:1024 lds\n"
"Qmvav 12,64,176\nQmvav 12,68,184\nQta 58,68,512\nQmvav 8,64,144\nQmvav 8,68,152\nQmvav 4,64,112\nQta 60,68,1024\ns_add_u32 m0, s96, 90112\n"
"s_add_u32 s95, s67, 86016\nQmvav 4,68,120\nbuffer_load_dwordx4 v61, s[52:55], s95 offen lds\nQmvav 0,64,80\nQmvav 0,68,88\nQta 62,68,1536\ns_waitcnt lgkmcnt(4)\nQmvav 28,72,80\n"
"Qmvav 28,76,88\ns_add_u32 s95, s67, 105472\nQmvav 32,72,112\nbuffer_load_dwordx4 v61, s[52:55], s95 offen offset:1024 lds\nQmvav 32,76,120\nQmvav 36,72,144\nQta 64,68,256\nQmvav 36,76,152\n"
"s_add_u32 s31, s74, 135168\nQmvav 40,72,176\nbuffer_load_dwordx4 a[96:99], v79, s[48:51], s31 offen offset:0 sc1\nQmvav 40,76,184\nQta 66,68,768\nQmvav 44,72,208\nv_cmp_eq_u32 s[44:45], s46, v60\ns_nop 1\n"
"v_cndmask_b32 v77, v77, v76, s[44:45]\nv_add_u32 v67, 1, v65\ns_xor_b32 s46, s19, 12\ns_lshl_b32 s28, 1, s46\nQmvav 44,76,216\nQmvav 48,72,240\nQta 68,68,1280\nQmvav 48,76,248\n"
"buffer_load_dwordx4 a[100:103], v79, s[48:51], s31 offen offset:1024 sc1\nv_add_u32 v75, 1, v75\nQmvav 52,72,84\ns_waitcnt vmcnt(35)\nds_write_b32 v73, v75\nQmvav 52,76,116\nQta 70,68,1792\ns_waitcnt lgkmcnt(5)\n"
"Qmaav 196,56,84\nQmaav 196,60,116\nbuffer_load_dwordx4 a[104:107], v79, s[48:51], s31 offen offset:2048 sc1\nQmaav 192,56,240\nQmaav 192,60,248\nQmaav 188,56,208\nQta 72,68,8192\ns_add_u32 s31, s74, 147456\n"
"Qmaav 188,60,216\nbuffer_load_dwordx4 a[108:111], v79, s[48:51], s31 offen offset:0 sc1\nQmaav 184,56,176\nds_read_b32 v66, v74\nQmaav 184,60,184\nQta 74,68,9216\nQmaav 180,56,144\nQmaav 180,60,152\n"
"buffer_load_dwordx4 a[112:115], v79, s[48:51], s31 offen offset:1024 sc1\nQmaav 176,56,112\ns_waitcnt lgkmcnt(1)\nv_cmp_gt_u32 vcc, v75, v66\nQmaav 176,60,120\ns_cbranch_vccnz .Lso1595r1\n.Lro1595r1:\nQmaav 172,56,80\n"
"Qta 76,68,16384\nQmaav 172,60,88\nbuffer_load_dwordx4 v[168:171], v79, s[48:51], s31 offen offset:2048 sc1\ns_waitcnt lgkmcnt(4)\nQmvav 92,64,80\nQmvav 92,68,88\nQta 78,68,17408\nQmvav 124,64,112\n"
"Qmvav 124,68,120\nbuffer_load_dwordx4 v[200:203], v79, s[48:51], s31 offen offset:3072 sc1\nQmvav 156,64,144\nQta 56,68,8448\nQmvav 156,68,152\nQmvav 188,64,176\nQmvav 188,68,184\nQta 58,68,9472\n"
"s_add_u32 s31, s74, 151552\nQmvav 220,64,208\nbuffer_load_dwordx4 v[232:235], v79, s[48:51], s31 offen offset:0 sc1\nQmvav 220,68,216\nQmvav 252,64,240\nQta 60,68,16640\nQmvav 252,68,248\nQmvav 100,64,84\n"
"buffer_load_dwordx4 v[212:215], v79, s[48:51], s31 offen offset:1024 sc1\nQmvav 100,68,116\nQta 62,68,17664\ns_waitcnt lgkmcnt(4)\nQmaav 224,72,84\nQmaav 224,76,116\nQmaav 220,72,240\nbuffer_load_dwordx4 v[244:247], v79, s[48:51], s31 offen offset:2048 sc1\n"
"Qmaav 220,76,248\nQmaav 216,72,208\nQta 64,68,8704\nQmaav 216,76,216\ns_add_u32 m0, s91, 0\ns_add_u32 s31, s77, 131072\nQmaav 212,72,176\n.Lpg23:\n"
"s_cbranch_execz .Ltr8\nbuffer_load_dwordx4 v79, s[48:51], s31 offen sc1 lds\nQmaav 212,76,184\nQta 66,68,9728\nQmaav 208,72,144\nQmaav 208,76,152\nQmaav 204,72,112\nQta 68,68,16896\n"
"Qmaav 204,76,120\nbuffer_load_dwordx4 v79, s[48:51], s31 offen offset:1024 sc1 lds\nQmaav 200,72,80\nQmaav 200,76,88\nQta 70,68,17920\ns_waitcnt lgkmcnt(4)\nQmaav 228,56,80\nQmaav 228,60,88\n"
"buffer_load_dwordx4 v79, s[48:51], s31 offen offset:2048 sc1 lds\nQmaav 232,56,112\nQmaav 232,60,120\nQmaav 236,56,144\nQta 72,68,8960\nQmaav 236,60,152\nbuffer_load_dwordx4 v79, s[48:51], s31 offen offset:3072 sc1 lds\nQmaav 240,56,176\n"
"Qmaav 240,60,184\nQta 74,68,9984\nQmaav 244,56,208\ns_add_u32 m0, s91, 4096\ns_add_u32 s31, s77, 135168\nQmaav 244,60,216\nbuffer_load_dwordx4 v79, s[48:51], s31 offen sc1 lds\nQmaav 248,56,240\n"
"Qta 76,68,17152\nQmaav 248,60,248\nQmaav 252,56,84\nQmaav 252,60,116\nQta 78,68,18176\ns_waitcnt lgkmcnt(4)\nQmvav 172,64,84\nbuffer_load_dwordx4 v79, s[48:51], s31 offen offset:1024 sc1 lds\n"
"Qmvav 172,68,116\nQmvav 140,64,240\nQmvav 140,68,248\nQmvav 108,64,208\nQta 56,68,24576\nQmvav 108,68,216\nbuffer_load_dwordx4 v79, s[48:51], s31 offen offset:2048 sc1 lds\nQmvav 228,64,176\n"
"Qmvav 228,68,184\nQta 58,68,25600\nQmvav 196,64,144\ns_add_u32 m0, s91, 33792\ns_add_u32 s31, s77, 147456\nQmvav 196,68,152\nbuffer_load_dwordx4 v79, s[48:51], s31 offen sc1 lds\nQmvav 164,64,112\n"
"Qta 60,68,32768\nQmvav 164,68,120\nv_add_u32 v75, 1, v75\nQmvav 132,64,80\ns_waitcnt vmcnt(47)\nds_write_b32 v73, v75\nQmvav 132,68,88\nQta 62,68,33792\n"
"s_waitcnt lgkmcnt(5)\nQmvav 204,72,80\nbuffer_load_dwordx4 v79, s[48:51], s31 offen offset:1024 sc1 lds\nQmvav 204,76,88\nQmvav 236,72,112\nQmvav 236,76,120\nQmvav 96,72,144\nQta 64,68,24832\n"
"Qmvav 96,76,152\nbuffer_load_dwordx4 v79, s[48:51], s31 offen offset:2048 sc1 lds\nQmvav 128,72,176\nds_read_b32 v66, v74\nQmvav 128,76,184\nQta 66,68,25856\nQmvav 160,72,208\nQmvav 160,76,216\n"
"buffer_load_dwordx4 v79, s[48:51], s31 offen offset:3072 sc1 lds\nQmvav 192,72,240\ns_waitcnt lgkmcnt(1)\nv_cmp_gt_u32 vcc, v75, v66\nQmvav 192,76,248\ns_cbranch_vccnz .Lso1665r1\n.Lro1665r1:\nQmvav 224,72,84\n"
"Qta 68,68,33024\ns_add_u32 m0, s91, 37888\ns_add_u32 s31, s77, 151552\nQmvav 224,76,116\nbuffer_load_dwordx4 v79, s[48:51], s31 offen sc1 lds\ns_waitcnt vmcnt(36)\ns_waitcnt lgkmcnt(4)\nQmaaa 140,56,24\n"
"Qmaaa 140,60,52\nQta 70,68,34048\nQmaaa 136,56,20\nQmaaa 136,60,48\nbuffer_load_dwordx4 v79, s[48:51], s31 offen offset:1024 sc1 lds\nQmaaa 132,56,16\nQta 72,68,25088\nQmaaa 132,60,44\n"
"Qmaaa 128,56,12\nQmaaa 128,60,40\nQta 74,68,26112\nQmaaa 124,56,8\nbuffer_load_dwordx4 v79, s[48:51], s31 offen offset:2048 sc1 lds\nQmaaa 124,60,36\nQmaaa 120,56,4\nQta 76,68,33280\n"
"Qmaaa 120,60,32\nQmaaa 116,56,0\nQmaaa 116,60,28\nQta 78,68,34304\ns_add_u32 m0, s96, 0\ns_add_u32 s95, s67, 126976\ns_waitcnt lgkmcnt(4)\nQmaaa 144,64,0\n"
"buffer_load_dwordx4 v61, s[52:55], s95 offen lds\nQmaaa 144,68,28\nQmaaa 148,64,4\nQmaaa 148,68,32\nQmaaa 152,64,8\nQta 56,68,25344\ns_add_u32 s95, s67, 146432\nQmaaa 152,68,36\n"
"buffer_load_dwordx4 v61, s[52:55], s95 offen offset:1024 lds\nQmaaa 156,64,12\nQmaaa 156,68,40\nQta 58,68,26368\nQmaaa 160,64,16\ns_add_u32 s31, s75, 131072\nQmaaa 160,68,44\nbuffer_load_dwordx4 v[80:83], v79, s[48:51], s31 offen offset:0 sc1\n"
"Qmaaa 164,64,20\nQta 60,68,33536\nQmaaa 164,68,48\nQmaaa 168,64,24\nQmaaa 168,68,52\nQta 62,68,34560\ns_waitcnt lgkmcnt(4)\nQmvaa 24,72,24\n"
"buffer_load_dwordx4 v[112:115], v79, s[48:51], s31 offen offset:1024 sc1\nQmvaa 24,76,52\nQmvaa 20,72,20\nQmvaa 20,76,48\nQmvaa 16,72,16\nQta 64,68,40960\nQmvaa 16,76,44\nbuffer_load_dwordx4 v[144:147], v79, s[48:51], s31 offen offset:2048 sc1\n"
"Qmvaa 12,72,12\nQmvaa 12,76,40\nQta 66,68,41472\nQmvaa 8,72,8\nQmvaa 8,76,36\nbuffer_load_dwordx4 v[176:179], v79, s[48:51], s31 offen offset:3072 sc1\nQmvaa 4,72,4\nQta 68,68,41984\n"
"Qmvaa 4,76,32\nQmvaa 0,72,0\nQmvaa 0,76,28\nQta 70,68,42496\ns_add_u32 s31, s75, 135168\ns_waitcnt lgkmcnt(4)\nQmvaa 28,56,0\nbuffer_load_dwordx4 v[208:211], v79, s[48:51], s31 offen offset:0 sc1\n"
"Qmvaa 28,60,28\nQmvaa 32,56,4\nQmvaa 32,60,32\nQmvaa 36,56,8\nQta 72,68,41216\nQmvaa 36,60,36\nbuffer_load_dwordx4 v[240:243], v79, s[48:51], s31 offen offset:1024 sc1\nQmvaa 40,56,12\n"
"Qmvaa 40,60,40\nQta 74,68,41728\nQmvaa 44,56,16\nQmvaa 44,60,44\nbuffer_load_dwordx4 v[84:87], v79, s[48:51], s31 offen offset:2048 sc1\nQmvaa 48,56,20\nQta 76,68,42240\nQmvaa 48,60,48\n"
"v_add_u32 v75, 1, v75\nQmvaa 52,56,24\ns_waitcnt vmcnt(37)\nds_write_b32 v73, v75\nQmvaa 52,60,52\nQta 78,68,42752\ns_add_u32 s31, s75, 147456\ns_waitcnt lgkmcnt(5)\n"
"Qmaaa 196,64,24\nbuffer_load_dwordx4 v[88:91], v79, s[48:51], s31 offen offset:0 sc1\nQmaaa 196,68,52\nQmaaa 192,64,20\nQmaaa 192,68,48\nQmaaa 188,64,16\nQta 56,68,49152\nQmaaa 188,68,44\n"
"buffer_load_dwordx4 v[120:123], v79, s[48:51], s31 offen offset:1024 sc1\nQmaaa 184,64,12\nds_read_b32 v66, v74\nQmaaa 184,68,40\nQta 58,68,50176\nQmaaa 180,64,8\nQmaaa 180,68,36\nbuffer_load_dwordx4 v[152:155], v79, s[48:51], s31 offen offset:2048 sc1\n"
"Qmaaa 176,64,4\ns_waitcnt lgkmcnt(1)\nv_cmp_gt_u32 vcc, v75, v66\nQmaaa 176,68,32\ns_cbranch_vccnz .Lso1735r1\n.Lro1735r1:\nQmaaa 172,64,0\nQta 60,68,57344\n"
"Qmaaa 172,68,28\nbuffer_load_dwordx4 v[184:187], v79, s[48:51], s31 offen offset:3072 sc1\ns_waitcnt lgkmcnt(4)\nQmvaa 92,72,0\nQmvaa 92,76,28\nQta 62,68,58368\nQmvaa 124,72,4\ns_add_u32 s31, s75, 151552\n"
"Qmvaa 124,76,32\nbuffer_load_dwordx4 v[216:219], v79, s[48:51], s31 offen offset:0 sc1\nQmvaa 156,72,8\nQta 64,68,49408\nQmvaa 156,76,36\nQmvaa 188,72,12\nQmvaa 188,76,40\nQta 66,68,50432\n"
"Qmvaa 220,72,16\nbuffer_load_dwordx4 v[248:251], v79, s[48:51], s31 offen offset:1024 sc1\nQmvaa 220,76,44\nQmvaa 252,72,20\nQta 68,68,57600\nQmvaa 252,76,48\nQmvaa 100,72,24\nbuffer_load_dwordx4 v[116:119], v79, s[48:51], s31 offen offset:2048 sc1\n"
"Qmvaa 100,76,52\nQta 70,68,58624\ns_waitcnt lgkmcnt(4)\nQmaaa 224,56,24\nQmaaa 224,60,52\nQmaaa 220,56,20\ns_add_u32 m0, s96, 8192\ns_add_u32 s95, s72, 86016\n"
"Qmaaa 220,60,48\nbuffer_load_dwordx4 v61, s[52:55], s95 offen lds\nQmaaa 216,56,16\nQta 72,68,49664\nQmaaa 216,60,44\nQmaaa 212,56,12\nQmaaa 212,60,40\nQta 74,68,50688\n"
"s_add_u32 s95, s72, 105472\nQmaaa 208,56,8\nbuffer_load_dwordx4 v61, s[52:55], s95 offen offset:1024 lds\nQmaaa 208,60,36\nQmaaa 204,56,4\nQta 76,68,57856\nQmaaa 204,60,32\nQmaaa 200,56,0\n"
"Qmaaa 200,60,28\nQta 78,68,58880\ns_add_u32 m0, s96, 16384\ns_add_u32 s95, s72, 126976\ns_waitcnt lgkmcnt(4)\nQmaaa 228,64,0\nbuffer_load_dwordx4 v61, s[52:55], s95 offen lds\nQmaaa 228,68,28\n"
"Qmaaa 232,64,4\nQmaaa 232,68,32\nQmaaa 236,64,8\nQta 56,68,49920\ns_add_u32 s95, s72, 146432\nQmaaa 236,68,36\nbuffer_load_dwordx4 v61, s[52:55], s95 offen offset:1024 lds\nQmaaa 240,64,12\n"
"Qmaaa 240,68,40\nQta 58,68,50944\nQmaaa 244,64,16\nQmaaa 244,68,44\nQmaaa 248,64,20\nQta 60,68,58112\ns_add_u32 m0, s96, 24576\ns_add_u32 s95, s73, 86016\n"
"Qmaaa 248,68,48\nbuffer_load_dwordx4 v61, s[52:55], s95 offen lds\nQmaaa 252,64,24\nQmaaa 252,68,52\nQta 62,68,59136\ns_waitcnt lgkmcnt(4)\nQmvaa 172,72,24\nQmvaa 172,76,52\n"
"s_add_u32 s95, s73, 105472\nQmvaa 140,72,20\nbuffer_load_dwordx4 v61, s[52:55], s95 offen offset:1024 lds\nQmvaa 140,76,48\nQmvaa 108,72,16\nQta 64,69,0\nQmvaa 108,76,44\nQmvaa 228,72,12\n"
"Qmvaa 228,76,40\nQta 66,69,512\ns_add_u32 m0, s96, 32768\ns_add_u32 s95, s73, 126976\nQmvaa 196,72,8\nbuffer_load_dwordx4 v61, s[52:55], s95 offen lds\nQmvaa 196,76,36\nQmvaa 164,72,4\n"
"Qta 68,69,1024\nQmvaa 164,76,32\nv_add_u32 v75, 1, v75\nQmvaa 132,72,0\ns_waitcnt vmcnt(21)\nds_write_b32 v73, v75\nQmvaa 132,76,28\nQta 70,69,1536\n"
"s_add_u32 s95, s73, 146432\ns_waitcnt lgkmcnt(5)\nQmvaa 204,56,0\nbuffer_load_dwordx4 v61, s[52:55], s95 offen offset:1024 lds\nQmvaa 204,60,28\ns_waitcnt vmcnt(24)\nQda 0,58,0\nQmvaa 236,56,4\n"
"Qmvaa 236,60,32\nQda 28,58,33792\nQmvaa 96,56,8\nQta 72,69,8192\nQmvaa 96,60,36\nQda 4,58,1024\nQmvaa 128,56,12\nds_read_b32 v66, v74\n"
"Qmvaa 128,60,40\nQta 74,69,8704\nQmvaa 160,56,16\nQda 32,58,34816\nQmvaa 160,60,44\nQmvaa 192,56,20\ns_waitcnt lgkmcnt(2)\nv_cmp_gt_u32 vcc, v75, v66\n"
"Qmvaa 192,60,48\ns_cbranch_vccnz .Lso1805r1\n.Lro1805r1:\nQmvaa 224,56,24\nQta 76,69,9216\ns_add_u32 m0, s96, 40960\ns_add_u32 s95, s67, 86016\nQmvaa 224,60,52\n"
"buffer_load_dwordx4 v63, s[52:55], s95 offen lds\ns_waitcnt vmcnt(39)\ns_waitcnt lgkmcnt(8)\nQmaaa 172,64,80\nQda 8,58,2048\nQmaaa 172,68,108\nQta 78,69,9728\nQmaaa 176,64,84\n"
"Qda 36,58,35840\nQmaaa 176,68,112\nQmaaa 180,64,88\nQta 56,69,16384\ns_add_u32 s95, s67, 105472\nQmaav 180,68,168\nbuffer_load_dwordx4 v63, s[52:55], s95 offen offset:1024 lds\nQmaaa 184,64,92\n"
"Qda 12,58,3072\nQmaav 184,68,200\nQta 58,69,16896\nQmaaa 188,64,96\nQda 40,58,36864\nQmaav 188,68,232\nQmaaa 192,64,100\nQta 60,69,17408\n"
"s_add_u32 m0, s96, 49152\ns_add_u32 s95, s67, 126976\nQmaav 192,68,212\nbuffer_load_dwordx4 v63, s[52:55], s95 offen lds\nQmaaa 196,64,104\nQda 16,58,4096\nQmaav 196,68,244\nQta 62,69,17920\n"
"s_waitcnt vmcnt(11)\ns_waitcnt lgkmcnt(8)\nQmaav 172,72,80\nQda 44,58,37888\nQmaav 172,76,88\ns_add_u32 s95, s67, 146432\nQmaav 176,72,112\nbuffer_load_dwordx4 v63, s[52:55], s95 offen offset:1024 lds\n"
"Qmaav 176,76,120\nQda 20,58,5120\nQmaav 180,72,144\nQta 64,69,24576\nv_add_u32 v75, 1, v75\nQmaav 180,76,152\ns_waitcnt vmcnt(4)\nds_write_b32 v73, v75\n"
"Qmaav 184,72,176\nQda 48,58,38912\nQmaav 184,76,184\nQta 66,69,25600\ns_add_u32 m0, s96, 57344\ns_add_u32 s95, s72, 86016\nQmaav 188,72,208\nbuffer_load_dwordx4 v63, s[52:55], s95 offen lds\n"
"Qmaav 188,76,216\nQda 24,58,6144\nQmaav 192,72,240\nQta 68,68,0\nQmaav 192,76,248\nQda 52,58,39936\nQmaav 196,72,84\nQmaav 196,76,116\n"
"ds_read_b32 v66, v74\ns_waitcnt lgkmcnt(10)\nQmaaa 172,56,0\nQta 70,68,1024\ns_add_u32 s95, s72, 105472\nQmaaa 172,60,28\nbuffer_load_dwordx4 v63, s[52:55], s95 offen offset:1024 lds\nQmaaa 176,56,4\n"
"Qmaaa 176,60,32\ns_waitcnt lgkmcnt(1)\nv_cmp_gt_u32 vcc, v75, v66\ns_waitcnt lgkmcnt(11)\nQmaaa 180,56,8\ns_cbranch_vccnz .Lso1840r1\n.Lro1840r1:\nQmaaa 180,60,36\n"
"Qta 72,68,8192\nQmaaa 184,56,12\nds_write_b128 v58, a[172:175] offset:0\nQmaaa 184,60,40\ns_waitcnt lgkmcnt(4)\nQmaaa 188,56,16\nQta 74,68,9216\nQmaaa 188,60,44\n"
"ds_write_b128 v58, a[176:179] offset:1024\nQmaaa 192,56,20\nQmaaa 192,60,48\nQta 76,68,16384\nQmaaa 196,56,24\nds_write_b128 v58, a[180:183] offset:2048\nQmaaa 196,60,52\ns_waitcnt lgkmcnt(6)\n"
"Qmaaa 116,64,80\nQta 78,68,17408\nQmaaa 116,68,108\nds_write_b128 v58, a[184:187] offset:3072\nQmaaa 120,64,84\nQmaaa 120,68,112\nQmaaa 124,64,88\nQta 56,68,24576\n"
"Qmaav 124,68,168\nds_write_b128 v58, a[188:191] offset:4096\nQmaaa 128,64,92\nQmaav 128,68,200\nQta 58,68,25600\nQmaaa 132,64,96\nds_write_b128 v58, a[192:195] offset:5120\nQmaav 132,68,232\n"
"Qmaaa 136,64,100\nQta 60,68,32768\nQmaav 136,68,212\nds_write_b128 v58, a[196:199] offset:6144\nQmaaa 140,64,104\nQmaav 140,68,244\nQta 62,68,33792\ns_waitcnt lgkmcnt(8)\n"
"Qmaav 116,72,80\nv_add_u32 v75, 1, v75\nQmaav 116,76,88\nds_write_b32 v73, v75\nQmaav 120,72,112\nQmaav 120,76,120\nQmaav 124,72,144\nQta 64,69,256\n"
"Qmaav 124,76,152\nQmaav 128,72,176\nQmaav 128,76,184\nQta 66,69,768\nQmaav 132,72,208\nQmaav 132,76,216\nds_read_b32 v66, v74\nQmaav 136,72,240\n"
"Qta 68,69,1280\nQmaav 136,76,248\nQmaav 140,72,84\nQmaav 140,76,116\ns_waitcnt lgkmcnt(1)\nv_cmp_gt_u32 vcc, v75, v66\ns_waitcnt lgkmcnt(5)\nQmaaa 116,56,0\n"
"s_cbranch_vccnz .Lso1878r1\n.Lro1878r1:\nQmaaa 116,60,28\nQta 70,69,1792\nQmaaa 120,56,4\nQmaaa 120,60,32\nQdv 148,59,2048\nQmaaa 124,56,8\n"
"Qta 72,69,8448\nQmaaa 124,60,36\ns_nop 0\nds_write_b128 v58, a[116:119] offset:33792\nQmaaa 128,56,12\nQdv 180,59,3072\nQmaaa 128,60,40\nQta 74,69,8960\n"
"Qmaaa 132,56,16\nds_write_b128 v58, a[120:123] offset:34816\nQmaaa 132,60,44\nQdv 104,59,5120\nQmaaa 136,56,20\nQta 76,69,9472\nQmaaa 136,60,48\nds_write_b128 v58, a[124:127] offset:35840\n"
"Qmaaa 140,56,24\nQdv 136,59,0\ns_waitcnt lgkmcnt(9)\nQa 156,156,148\nQmaaa 140,60,52\nQta 78,69,9984\nQa 157,157,149\ns_waitcnt lgkmcnt(10)\n"
"Qmvaa 92,64,80\nds_write_b128 v58, a[128:131] offset:36864\nQmvaa 92,68,108\nQa 158,158,150\nQa 159,159,151\nQmvaa 124,64,84\ns_waitcnt lgkmcnt(8)\nQa 188,188,180\n"
"Qa 189,189,181\nQmvaa 124,68,112\nds_write_b128 v58, a[132:135] offset:37888\nQmvaa 156,64,88\nQta 56,69,16640\nQa 190,190,182\nQa 191,191,183\nQmvav 156,68,168\n"
"Qdv 148,59,4096\ns_waitcnt lgkmcnt(8)\nQa 252,252,104\nQmvaa 188,64,92\nds_write_b128 v58, a[136:139] offset:38912\n.Lpg24:\ns_cbranch_execz .Ltr8\nQmvav 188,68,200\n"
"Qta 58,69,17152\nQa 253,253,105\nQmvaa 220,64,96\nQa 254,254,106\nQa 255,255,107\nQmvav 220,68,232\nQdv 180,59,6144\ns_waitcnt lgkmcnt(8)\n"
"Qa 92,92,136\nQmvaa 252,64,100\nQta 60,69,17664\nQa 93,93,137\nQmvav 252,68,212\nds_write_b128 v58, a[140:143] offset:39936\nQmvaa 100,64,104\nQa 94,94,138\n"
"Qa 95,95,139\nQmvav 100,68,244\nQta 62,69,18176\ns_waitcnt lgkmcnt(6)\nQa 220,220,148\ns_waitcnt lgkmcnt(6)\nQmvav 92,72,80\nQdv 104,59,1024\n"
"v_add_u32 v75, 1, v75\nQmvav 92,76,88\nds_write_b32 v73, v75\nQa 221,221,149\nQmvav 124,72,112\nQa 222,222,150\nQa 223,223,151\nQmvav 124,76,120\n"
"s_waitcnt lgkmcnt(5)\nQa 100,100,180\nQa 101,101,181\nQmvav 156,72,144\nQta 64,69,24832\nQa 102,102,182\nQmvav 156,76,152\nQa 103,103,183\n"
"Qmvav 188,72,176\nQmvav 188,76,184\nQta 66,69,25856\ns_waitcnt lgkmcnt(3)\nQa 124,124,104\nQmvav 220,72,208\nQa 125,125,105\nQa 126,126,106\n"
"Qmvav 220,76,216\nds_read_b32 v66, v74\nQa 127,127,107\nQmvav 252,72,240\nQta 68,68,256\nQmvav 252,76,248\nQmvav 100,72,84\nQmvav 100,76,116\n"
"s_waitcnt lgkmcnt(1)\nv_cmp_gt_u32 vcc, v75, v66\ns_waitcnt lgkmcnt(6)\nQmvaa 92,56,0\ns_cbranch_vccnz .Lso1920r1\n.Lro1920r1:\ns_add_u32 s68, s64, 0\nQmvaa 92,60,28\n"
"Qta 70,68,1280\ns_add_u32 m0, s96, 65536\ns_add_u32 s95, s72, 126976\nQmvaa 124,56,4\nbuffer_load_dwordx4 v63, s[52:55], s95 offen lds\nQmvaa 124,60,32\nQdv 136,59,33792\nQmvaa 156,56,8\n"
"Qta 72,68,8448\nQk 92,92,93\nQmvaa 156,60,36\nQk 93,94,95\nQmvaa 188,56,12\nQdv 148,59,34816\nQk 94,124,125\nQmvaa 188,60,40\n"
"Qta 74,68,9472\ns_add_u32 s95, s72, 146432\nQmvaa 220,56,16\nbuffer_load_dwordx4 v63, s[52:55], s95 offen offset:1024 lds\nQmvaa 220,60,44\nQdv 180,59,35840\nQk 95,126,127\nQmvaa 252,56,20\n"
"Qta 76,68,16640\nbuffer_store_dwordx4 v[92:95], v79, s[24:27], s68 offen offset:0 sc1\nQmvaa 252,60,48\nv_add_u32 v75, 1, v75\ns_waitcnt lgkmcnt(5)\nQa 0,0,136\nQmvaa 100,56,24\nds_write_b32 v73, v75\n"
"Qa 1,1,137\nQmvaa 100,60,52\nQta 78,68,17664\ns_add_u32 m0, s96, 73728\ns_add_u32 s95, s73, 86016\ns_waitcnt lgkmcnt(7)\nQmaaa 144,64,80\nbuffer_load_dwordx4 v63, s[52:55], s95 offen lds\n"
"Qmaaa 144,68,108\nQdv 104,59,36864\nQa 2,2,138\nQmaaa 148,64,84\nQa 3,3,139\ns_waitcnt lgkmcnt(6)\nQa 4,4,148\nQmaaa 148,68,112\n"
"Qa 5,5,149\nQa 6,6,150\nQmaaa 152,64,88\nQta 56,68,24832\ns_add_u32 s95, s73, 105472\nQmaav 152,68,168\nbuffer_load_dwordx4 v63, s[52:55], s95 offen offset:1024 lds\nQmaaa 156,64,92\n"
"ds_read_b32 v66, v74\nQa 7,7,151\nQmaav 156,68,200\nQta 58,68,25856\ns_waitcnt lgkmcnt(7)\nQa 8,8,180\nQmaaa 160,64,96\nQdv 136,59,37888\n"
"Qa 9,9,181\nQmaav 160,68,232\nQa 10,10,182\nQa 11,11,183\nQmaaa 164,64,100\ns_waitcnt lgkmcnt(2)\nv_cmp_gt_u32 vcc, v75, v66\nQmaav 164,68,212\n"
"s_cbranch_vccnz .Lso1945r1\n.Lro1945r1:\ns_waitcnt lgkmcnt(4)\nQa 12,12,104\nQmaaa 168,64,104\nQta 60,68,33024\ns_add_u32 m0, s96, 81920\ns_add_u32 s95, s73, 126976\n"
"Qa 13,13,105\nQa 14,14,106\nQa 15,15,107\nQmaav 168,68,244\nbuffer_load_dwordx4 v63, s[52:55], s95 offen lds\ns_waitcnt lgkmcnt(10)\nQmaav 144,72,80\nQdv 148,59,38912\n"
"s_waitcnt lgkmcnt(2)\nQa 16,16,136\nQmaav 144,76,88\nQta 62,68,34048\nQa 17,17,137\nQmaav 148,72,112\nQa 18,18,138\nQa 19,19,139\n"
"Qmaav 148,76,120\nQdv 180,59,39936\nQk 156,156,157\nQmaav 152,72,144\nQta 64,69,25088\ns_add_u32 s95, s73, 146432\nQmaav 152,76,152\nbuffer_load_dwordx4 v63, s[52:55], s95 offen offset:1024 lds\n"
"Qmaav 156,72,176\ns_waitcnt lgkmcnt(3)\nQa 20,20,148\nQa 21,21,149\nQmaav 156,76,184\nQta 66,69,26112\nQa 22,22,150\nQmaav 160,72,208\n"
"Qa 23,23,151\nv_bfe_u32 v61, v60, 6, 2\nQmaav 160,76,216\nv_lshrrev_b32 v62, 8, v60\nv_xor_b32 v61, v61, v62\nQmaav 164,72,240\nQta 68,68,512\nv_bfe_u32 v62, v60, 5, 1\n"
"Qmaav 164,76,248\ns_waitcnt lgkmcnt(3)\nQa 24,24,180\nQa 25,25,181\nQmaav 168,72,84\nQa 26,26,182\nQa 27,27,183\nQmaav 168,76,116\n"
"Qta 70,68,1536\nv_lshl_or_b32 v61, v61, 1, v62\ns_waitcnt lgkmcnt(5)\nQmaaa 144,56,0\nv_and_b32 v62, 16, v60\nv_mov_b32 v63, 0x3f80\nQmaaa 144,60,28\nv_lshlrev_b32 v63, v62, v63\n"
"v_min_u32 v62, 1, v61\nQmaaa 148,56,4\nv_add_u32 v62, -1, v62\nv_and_b32 v62, v62, v63\nQmaaa 148,60,32\nv_xor_b32 v61, 1, v61\nv_min_u32 v61, 1, v61\nQmaaa 152,56,8\n"
"Qta 72,68,8704\nv_add_u32 v61, -1, v61\nQmaaa 152,60,36\nds_write_b128 v58, a[144:147] offset:0\nQmaaa 156,56,12\nv_and_b32 v61, v61, v63\nv_accvgpr_write_b32 a174, v62\nQmaaa 156,60,40\n"
"Qta 74,68,9728\nv_accvgpr_write_b32 a175, v61\nQmaaa 160,56,16\nds_write_b128 v58, a[148:151] offset:1024\nQmaaa 160,60,44\nv_accvgpr_write_b32 a172, 0\nv_accvgpr_write_b32 a173, 0\nQmaaa 164,56,20\n"
"Qta 76,68,16896\nv_accvgpr_write_b32 a176, 0\nQmaaa 164,60,48\nds_write_b128 v58, a[152:155] offset:2048\nQmaaa 168,56,24\nv_accvgpr_write_b32 a177, 0\nQk 157,158,159\nQmaaa 168,60,52\n"
"Qta 78,68,17920\nQk 158,188,189\ns_waitcnt lgkmcnt(7)\nQmvaa 0,64,80\nds_write_b128 v58, a[156:159] offset:3072\nQmvaa 0,68,108\nQk 159,190,191\nQk 220,220,221\n"
"Qmvaa 4,64,84\nbuffer_store_dwordx4 v[156:159], v79, s[24:27], s68 offen offset:1024 sc1\nQk 221,222,223\nQmvaa 4,68,112\nds_write_b128 v58, a[160:163] offset:4096\nQmvaa 8,64,88\nQta 56,68,25088\nQk 222,252,253\n"
"Qmvav 8,68,168\nQk 223,254,255\nQk 100,100,101\nQmvaa 12,64,92\nds_write_b128 v58, a[164:167] offset:5120\nQmvav 12,68,200\nQta 58,68,26112\nbuffer_store_dwordx4 v[220:223], v79, s[24:27], s68 offen offset:2048 sc1\n"
"Qmvaa 16,64,96\nQk 101,102,103\nQmvav 16,68,232\nds_write_b128 v58, a[168:171] offset:6144\nQmvaa 20,64,100\nQta 60,68,33280\nQmvav 20,68,212\nQmvaa 24,64,104\n"
"Qmvav 24,68,244\nQta 62,68,34304\ns_waitcnt lgkmcnt(8)\nQmvav 0,72,80\nv_add_u32 v75, 1, v75\nQmvav 0,76,88\ns_waitcnt vmcnt(11)\nds_write_b32 v73, v75\n"
"Qmvav 4,72,112\nQmvav 4,76,120\nQmvav 8,72,144\nQta 64,69,25344\nQmvav 8,76,152\nQmvav 12,72,176\nQmvav 12,76,184\nQta 66,69,26368\n"
"Qmvav 16,72,208\nQmvav 16,76,216\nds_read_b32 v66, v74\nQmvav 20,72,240\nQta 68,68,768\nQmvav 20,76,248\nQmvav 24,72,84\nQmvav 24,76,116\n"
"s_waitcnt lgkmcnt(1)\nv_cmp_gt_u32 vcc, v75, v66\ns_waitcnt lgkmcnt(5)\nQmvaa 0,56,0\ns_cbranch_vccnz .Lso2004r1\n.Lro2004r1:\nQmvaa 0,60,28\nQta 70,68,1792\n"
"Qmvaa 4,56,4\nQmvaa 4,60,32\nQdv 104,59,2048\nQmvaa 8,56,8\nQta 72,68,8960\ns_nop 1\nQk 102,0,1\nQmvaa 8,60,36\n"
"Qk 103,2,3\nQmvaa 12,56,12\nQdv 136,59,3072\nbuffer_store_dwordx4 v[100:103], v79, s[24:27], s68 offen offset:3072 sc1\nQmvaa 12,60,40\nQta 74,68,9984\nQk 4,4,5\nQmvaa 16,56,16\n"
"Qk 5,6,7\nQk 6,8,9\nQmvaa 16,60,44\nQdv 148,59,5120\nQk 7,10,11\nQmvaa 20,56,20\nQta 76,68,17152\ns_add_u32 s68, s64, 4096\n"
"Qmvaa 20,60,48\ns_waitcnt lgkmcnt(5)\nQa 36,36,104\nQa 37,37,105\nQmvaa 24,56,24\nQdv 180,59,0\nQa 38,38,106\nQmvaa 24,60,52\n"
"Qta 78,68,18176\nQa 39,39,107\ns_waitcnt lgkmcnt(11)\nQmvaa 28,64,80\ns_waitcnt lgkmcnt(5)\nQa 40,40,136\nQa 41,41,137\nQmvaa 28,68,108\n"
"Qa 42,42,138\nQa 43,43,139\nQmvaa 32,64,84\nbuffer_store_dwordx4 v[4:7], v79, s[24:27], s68 offen offset:0 sc1\nQk 12,12,13\nQmvaa 32,68,112\ns_waitcnt lgkmcnt(3)\nQa 48,48,148\n"
"Qa 49,49,149\nQmvaa 36,64,88\nQta 56,68,25344\nQa 50,50,150\nQmvav 36,68,168\nQdv 104,59,4096\nQa 51,51,151\nQmvaa 40,64,92\n"
"s_waitcnt lgkmcnt(3)\nQa 28,28,180\nQa 29,29,181\nQmvav 40,68,200\nQta 58,68,26368\nQa 30,30,182\nQmvaa 44,64,96\nQa 31,31,183\n"
"Qk 13,14,15\nQmvav 44,68,232\nQdv 136,59,6144\nQk 14,16,17\nQmvaa 48,64,100\nQta 60,68,33536\nQk 15,18,19\nQmvav 48,68,212\n"
"buffer_store_dwordx4 v[12:15], v79, s[24:27], s68 offen offset:1024 sc1\nQk 20,20,21\nQmvaa 52,64,104\nQk 21,22,23\nQk 22,24,25\nQmvav 52,68,244\nQta 62,68,34560\nv_add_u32 v75, 1, v75\n"
"s_waitcnt lgkmcnt(6)\nQmvav 28,72,80\ns_waitcnt vmcnt(5)\nds_write_b32 v73, v75\nQmvav 28,76,88\nQdv 148,59,1024\ns_waitcnt lgkmcnt(6)\nQa 44,44,104\n"
"Qmvav 32,72,112\nQa 45,45,105\nQa 46,46,106\nQmvav 32,76,120\nQa 47,47,107\ns_waitcnt lgkmcnt(4)\nQa 52,52,136\nQmvav 36,72,144\n"
"Qta 64,68,40960\nQa 53,53,137\nQmvav 36,76,152\nQa 54,54,138\nQa 55,55,139\nQmvav 40,72,176\nQk 23,26,27\nQmvav 40,76,184\n"
"Qta 66,68,41984\ns_waitcnt lgkmcnt(2)\nQa 32,32,148\nQmvav 44,72,208\nds_read_b32 v66, v74\nQa 33,33,149\nQmvav 44,76,216\nQa 34,34,150\n"
"Qa 35,35,151\nQmvav 48,72,240\nQta 68,68,49152\nbuffer_store_dwordx4 v[20:23], v79, s[24:27], s68 offen offset:2048 sc1\nQmvav 48,76,248\nQmvav 52,72,84\ns_waitcnt lgkmcnt(1)\nv_cmp_gt_u32 vcc, v75, v66\n"
"Qmvav 52,76,116\ns_cbranch_vccnz .Lso2045r1\n.Lro2045r1:\ns_waitcnt lgkmcnt(6)\nQmvaa 28,56,0\nQta 70,68,50176\nQmvaa 28,60,28\nQmvaa 32,56,4\n"
"Qmvaa 32,60,32\nQmvaa 36,56,8\nQta 72,68,57344\ns_nop 3\nQk 28,28,29\nQmvaa 36,60,36\nQk 29,30,31\nQmvaa 40,56,12\n"
"Qk 30,32,33\nQk 31,34,35\nQmvaa 40,60,40\nQta 74,68,58368\nbuffer_store_dwordx4 v[28:31], v79, s[24:27], s68 offen offset:3072 sc1\nQmvaa 44,56,16\nQk 36,36,37\nQk 37,38,39\n"
"Qmvaa 44,60,44\ns_add_u32 s68, s64, 16384\nQmvaa 48,56,20\nQta 76,69,0\nQk 38,40,41\nQmvaa 48,60,48\nQk 39,42,43\nQmvaa 52,56,24\n"
"buffer_store_dwordx4 v[36:39], v79, s[24:27], s68 offen offset:0 sc1\nQk 44,44,45\nQmvaa 52,60,52\nQta 78,69,1024\nQk 45,46,47\ns_waitcnt lgkmcnt(4)\nQmaaa 200,64,80\nQk 46,48,49\n"
"Qk 47,50,51\nQmaaa 200,68,108\nbuffer_store_dwordx4 v[44:47], v79, s[24:27], s68 offen offset:1024 sc1\nQmaaa 204,64,84\nQk 52,52,53\nQk 53,54,55\nQmaaa 204,68,112\nv_mov_b32 v54, 0\n"
"v_mov_b32 v55, 0\nQmaaa 208,64,88\nQta 56,69,8192\nbuffer_store_dwordx4 v[52:55], v79, s[24:27], s68 offen offset:2048 sc1\nQmaav 208,68,168\nQmaaa 212,64,92\nQmaav 212,68,200\nQta 58,69,9216\n"
"Qmaaa 216,64,96\nQmaav 216,68,232\nQmaaa 220,64,100\nQta 60,69,16384\nQmaav 220,68,212\nQmaaa 224,64,104\nQmaav 224,68,244\nQta 62,69,17408\n"
"s_waitcnt lgkmcnt(4)\nQmaav 200,72,80\nQmaav 200,76,88\nQmaav 204,72,112\nQmaav 204,76,120\nQmaav 208,72,144\nQta 64,68,41216\nQmaav 208,76,152\n"
"Qmaav 212,72,176\nQmaav 212,76,184\nQta 66,68,42240\nQmaav 216,72,208\nQmaav 216,76,216\nQmaav 220,72,240\nQta 68,68,49408\nQmaav 220,76,248\n"
"Qmaav 224,72,84\nQmaav 224,76,116\nQta 70,68,50432\ns_waitcnt lgkmcnt(4)\nQmaaa 200,56,0\nQmaaa 200,60,28\ns_waitcnt vmcnt(0)\nbuffer_store_dword v67, v77, s[24:27], 0 offen sc1\n"
"Qmaaa 204,56,4\nQmaaa 204,60,32\nQmaaa 208,56,8\nQta 72,68,57600\nQmaaa 208,60,36\ns_nop 0\nds_write_b128 v58, a[200:203] offset:33792\nQmaaa 212,56,12\n"
"Qmaaa 212,60,40\nQta 74,68,58624\nQmaaa 216,56,16\nds_write_b128 v58, a[204:207] offset:34816\nQmaaa 216,60,44\nQmaaa 220,56,20\nQta 76,69,256\nQmaaa 220,60,48\n"
"ds_write_b128 v58, a[208:211] offset:35840\nQmaaa 224,56,24\nQmaaa 224,60,52\nQta 78,69,1280\ns_waitcnt lgkmcnt(7)\nQmaaa 228,64,80\nds_write_b128 v58, a[212:215] offset:36864\nQmaaa 228,68,108\n"
"Qmaaa 232,64,84\nQmaaa 232,68,112\nds_write_b128 v58, a[216:219] offset:37888\nQmaaa 236,64,88\nQta 56,69,8448\nQmaav 236,68,168\nQmaaa 240,64,92\nds_write_b128 v58, a[220:223] offset:38912\n"
"Qmaav 240,68,200\nQta 58,69,9472\nQmaaa 244,64,96\nQmaav 244,68,232\nds_write_b128 v58, a[224:227] offset:39936\nQmaaa 248,64,100\nQta 60,69,16640\nQmaav 248,68,212\n"
"Qmaaa 252,64,104\nQmaav 252,68,244\nQta 62,69,17664\ns_waitcnt lgkmcnt(8)\nQmaav 228,72,80\nv_add_u32 v75, 1, v75\nQmaav 228,76,88\nds_write_b32 v73, v75\n"
"Qmaav 232,72,112\nQmaav 232,76,120\nQmaav 236,72,144\nQta 64,68,41472\nQmaav 236,76,152\nQmaav 240,72,176\nQmaav 240,76,184\nQta 66,68,42496\n"
"Qmaav 244,72,208\nQmaav 244,76,216\nds_read_b32 v66, v74\nQmaav 248,72,240\nQta 68,68,49664\nQmaav 248,76,248\nQmaav 252,72,84\nbuffer_load_dword v78, v76, s[24:27], 0 offen sc1\n"
"Qmaav 252,76,116\ns_waitcnt lgkmcnt(1)\nv_cmp_gt_u32 vcc, v75, v66\ns_waitcnt lgkmcnt(5)\nQmaaa 228,56,0\ns_cbranch_vccnz .Lso2130r1\n.Lro2130r1:\nQmaaa 228,60,28\n"
"Qta 70,68,50688\nQmaaa 232,56,4\nQmaaa 232,60,32\nQdv 180,59,35840\nQmaaa 236,56,8\nQta 72,68,57856\nQmaaa 236,60,36\ns_nop 0\n"
"ds_write_b128 v58, a[228:231] offset:0\nQmaaa 240,56,12\nQdv 104,59,36864\nQmaaa 240,60,40\nQta 74,68,58880\nQmaaa 244,56,16\nds_write_b128 v58, a[232:235] offset:1024\n.Lpg25:\n"
"s_cbranch_execz .Ltr8\nQmaaa 244,60,44\nQdv 136,59,38912\nQmaaa 248,56,20\nQta 76,69,512\nQmaaa 248,60,48\nds_write_b128 v58, a[236:239] offset:2048\nQmaaa 252,56,24\n"
"Qdv 148,59,33792\ns_waitcnt lgkmcnt(9)\nQa 196,196,180\nQmaaa 252,60,52\nQta 78,69,1536\nQa 197,197,181\ns_waitcnt lgkmcnt(10)\nQmvaa 132,64,80\n"
"ds_write_b128 v58, a[240:243] offset:3072\nQmvaa 132,68,108\nQa 198,198,182\nQa 199,199,183\nQmvaa 164,64,84\ns_waitcnt lgkmcnt(8)\nQa 228,228,104\nQa 229,229,105\n"
"Qmvaa 164,68,112\nds_write_b128 v58, a[244:247] offset:4096\nQmvaa 196,64,88\nQta 56,69,8704\nQa 230,230,106\nQa 231,231,107\nQmvav 196,68,168\nQdv 180,59,37888\n"
"s_waitcnt lgkmcnt(8)\nQa 140,140,136\nQmvaa 228,64,92\nds_write_b128 v58, a[248:251] offset:5120\nQmvav 228,68,200\nQta 58,69,9728\nQa 141,141,137\nQmvaa 108,64,96\n"
"Qa 142,142,138\nQa 143,143,139\nQmvav 108,68,232\nQdv 104,59,39936\ns_waitcnt lgkmcnt(8)\nQa 132,132,148\nQmvaa 140,64,100\nQta 60,69,16896\n"
"Qa 133,133,149\nQmvav 140,68,212\nds_write_b128 v58, a[252:255] offset:6144\nQmvaa 172,64,104\nQa 134,134,150\nQa 135,135,151\nQmvav 172,68,244\nQta 62,69,17920\n"
"s_waitcnt lgkmcnt(6)\nQa 108,108,180\ns_waitcnt lgkmcnt(6)\nQmvav 132,72,80\nQdv 136,59,34816\nv_add_u32 v75, 1, v75\nQmvav 132,76,88\nds_write_b32 v73, v75\n"
"Qa 109,109,181\nQmvav 164,72,112\nQa 110,110,182\nQa 111,111,183\nQmvav 164,76,120\ns_waitcnt lgkmcnt(5)\nQa 172,172,104\nQa 173,173,105\n"
"Qmvav 196,72,144\nQta 64,68,41728\nQa 174,174,106\nQmvav 196,76,152\nQa 175,175,107\nQmvav 228,72,176\nQmvav 228,76,184\nQta 66,68,42752\n"
"s_waitcnt lgkmcnt(3)\nQa 164,164,136\nQmvav 108,72,208\nQa 165,165,137\nQa 166,166,138\nQmvav 108,76,216\nds_read_b32 v66, v74\nQa 167,167,139\n"
"Qmvav 140,72,240\nQta 68,68,49920\nQmvav 140,76,248\nQmvav 172,72,84\nQmvav 172,76,116\ns_waitcnt lgkmcnt(1)\nv_cmp_gt_u32 vcc, v75, v66\ns_waitcnt lgkmcnt(6)\n"
"Qmvaa 132,56,0\ns_cbranch_vccnz .Lso2172r1\n.Lro2172r1:\nQmvaa 132,60,28\ns_waitcnt vmcnt(0)\nv_sub_u32 v66, v78, v65\nv_cmp_gt_u32 vcc, 1, v66\ns_and_b64 vcc, vcc, s[28:29]\n"
"s_cbranch_scc1 .Lx1_w\n.Lx1_r:\nQmvaa 164,56,4\ns_add_u32 s69, s98, 0\nbuffer_load_dwordx4 a[180:183], v79, s[24:27], s69 offen offset:0 sc1\nQmvaa 164,60,32\nQta 70,68,50944\nQmvaa 196,56,8\n"
"buffer_load_dwordx4 a[192:195], v79, s[24:27], s69 offen offset:3072 sc1\nQmvaa 196,60,36\nQdv 148,59,2048\nQmvaa 228,56,12\ns_add_u32 s69, s98, 4096\nbuffer_load_dwordx4 a[196:199], v79, s[24:27], s69 offen offset:0 sc1\nQmvaa 228,60,40\nQta 72,68,58112\n"
"Qmvaa 108,56,16\ns_add_u32 s69, s98, 0\nbuffer_load_dwordx4 a[184:187], v79, s[24:27], s69 offen offset:1024 sc1\nQmvaa 108,60,44\nQdv 180,59,4096\nQmvaa 140,56,20\ns_add_u32 s69, s98, 4096\nbuffer_load_dwordx4 a[116:119], v79, s[24:27], s69 offen offset:1024 sc1\n"
"Qmvaa 140,60,48\nQta 74,68,59136\ns_waitcnt lgkmcnt(3)\nQa 96,96,148\nQmvaa 172,56,24\ns_add_u32 s69, s98, 0\nbuffer_load_dwordx4 a[188:191], v79, s[24:27], s69 offen offset:2048 sc1\nQmvaa 172,60,52\n"
"Qdv 104,59,0\nQa 97,97,149\ns_waitcnt lgkmcnt(4)\nQmvaa 204,64,80\ns_add_u32 s69, s98, 4096\nbuffer_load_dwordx4 a[120:123], v79, s[24:27], s69 offen offset:2048 sc1\nQmvaa 204,68,108\nQta 76,69,768\n"
"Qa 98,98,150\nQmvaa 236,64,84\nQdv 136,59,1024\nQa 99,99,151\nQmvaa 236,68,112\ns_waitcnt lgkmcnt(4)\nQa 160,160,180\nQa 161,161,181\n"
"Qmvaa 96,64,88\nQta 78,69,1792\nQa 162,162,182\nQmvav 96,68,168\nQdv 148,59,3072\nQa 163,163,183\nQmvaa 128,64,92\ns_waitcnt lgkmcnt(4)\n"
"Qa 204,204,104\nQa 205,205,105\nQmvav 128,68,200\nQta 56,69,8960\nQa 206,206,106\nQmvaa 160,64,96\nQdv 180,59,5120\nQa 207,207,107\n"
"Qmvav 160,68,232\nQmvaa 192,64,100\nQta 58,69,9984\ns_waitcnt lgkmcnt(5)\nQa 236,236,136\nQmvav 192,68,212\nQa 237,237,137\nQa 238,238,138\n"
"Qmvaa 224,64,104\nQa 239,239,139\nQmvav 224,68,244\nQta 60,69,17152\ns_waitcnt lgkmcnt(4)\nQa 128,128,148\ns_waitcnt lgkmcnt(4)\nQmvav 204,72,80\n"
"Qa 129,129,149\nQa 130,130,150\nQmvav 204,76,88\nQa 131,131,151\nQmvav 236,72,112\nQta 62,69,18176\ns_waitcnt lgkmcnt(3)\nQa 192,192,180\n"
"Qmvav 236,76,120\nQa 193,193,181\nQa 194,194,182\nQmvav 96,72,144\nQa 195,195,183\nQmvav 96,76,152\nQmvav 128,72,176\nQmvav 128,76,184\n"
"Qmvav 160,72,208\nQmvav 160,76,216\nQdv 104,59,6144\nQmvav 192,72,240\nQmvav 192,76,248\nv_add_u32 v75, 1, v75\nQmvav 224,72,84\nds_write_b32 v73, v75\n"
"Qmvav 224,76,116\ns_waitcnt lgkmcnt(2)\nQmvaa 204,56,0\nQmvaa 204,60,28\nQmvaa 236,56,4\nQmvaa 236,60,32\ns_waitcnt lgkmcnt(1)\ns_nop 1\n"
"Qa 224,224,104\nQa 225,225,105\nQmvaa 96,56,8\nQa 226,226,106\nQa 227,227,107\nQmvaa 96,60,36\nQmvaa 128,56,12\nds_read_b32 v66, v74\n"
"Qmvaa 128,60,40\nQmvaa 160,56,16\nQmvaa 160,60,44\nQmvaa 192,56,20\ns_waitcnt lgkmcnt(0)\nv_cmp_gt_u32 vcc, v75, v66\nQmvaa 192,60,48\ns_cbranch_vccnz .Lso2225r1\n"
".Lro2225r1:\nQmvaa 224,56,24\nQmvaa 224,60,52\ns_waitcnt vmcnt(6)\nQmvaa 132,174,180\ns_waitcnt vmcnt(5)\nQmvaa 204,172,192\nQmvaa 164,172,180\n"
"s_waitcnt vmcnt(4)\nQmvaa 236,174,196\ns_nop 2\nQk 132,132,133\nQk 133,134,135\nds_write_b64 v56, v[132:133] offset:0\ns_waitcnt vmcnt(3)\nQmvaa 196,174,184\n"
"Qk 204,204,205\nQk 205,206,207\nds_write_b64 v56, v[204:205] offset:32\nQmvaa 96,172,196\nQk 164,164,165\nQk 165,166,167\nds_write_b64 v56, v[164:165] offset:3328\nQmvaa 228,172,184\n"
"Qk 236,236,237\nQk 237,238,239\nds_write_b64 v56, v[236:237] offset:3360\ns_waitcnt lgkmcnt(0)\ns_barrier\nQda 0,57,0\nQda 4,57,3328\ns_waitcnt vmcnt(2)\n"
"Qmvaa 128,174,116\nQk 196,196,197\nQk 197,198,199\nds_write_b64 v56, v[196:197] offset:6656\ns_mov_b32 s68, 0\ns_waitcnt lgkmcnt(2)\nbuffer_store_dwordx4 a[0:3], v64, s[40:43], s68 offen offset:0 sc1\ns_waitcnt vmcnt(2)\n"
"Qmvaa 108,174,188\nQk 96,96,97\nQk 97,98,99\nds_write_b64 v56, v[96:97] offset:6688\ns_mov_b32 s68, 40960\ns_waitcnt lgkmcnt(2)\nbuffer_store_dwordx4 a[4:7], v64, s[40:43], s68 offen offset:0 sc1\nQmvaa 160,172,116\n"
"Qk 228,228,229\nQk 229,230,231\nds_write_b64 v56, v[228:229] offset:9984\nQmvaa 140,172,188\nQk 128,128,129\nQk 129,130,131\nds_write_b64 v56, v[128:129] offset:10016\ns_waitcnt vmcnt(2)\n"
"Qmvaa 192,174,120\nQk 108,108,109\nQk 109,110,111\nds_write_b64 v56, v[108:109] offset:13312\nQmvaa 172,174,192\nQk 160,160,161\nQk 161,162,163\nds_write_b64 v56, v[160:161] offset:13344\n"
"s_waitcnt lgkmcnt(0)\ns_barrier\nQda 8,57,6656\nQda 12,57,9984\nQda 16,57,13312\nQmvaa 224,172,120\nQk 140,140,141\nQk 141,142,143\n"
"ds_write_b64 v56, v[140:141] offset:16640\ns_mov_b32 s68, 81920\ns_waitcnt lgkmcnt(3)\nbuffer_store_dwordx4 a[8:11], v64, s[40:43], s68 offen offset:0 sc1\nQk 192,192,193\nQk 193,194,195\nds_write_b64 v56, v[192:193] offset:16672\ns_mov_b32 s68, 122880\n"
"s_waitcnt lgkmcnt(3)\nbuffer_store_dwordx4 a[12:15], v64, s[40:43], s68 offen offset:0 sc1\nQk 172,172,173\nQk 173,174,175\nds_write_b64 v56, v[172:173] offset:19968\ns_mov_b32 s68, 163840\ns_waitcnt lgkmcnt(3)\nbuffer_store_dwordx4 a[16:19], v64, s[40:43], s68 offen offset:0 sc1\n"
"Qk 224,224,225\nQk 225,226,227\nds_write_b64 v56, v[224:225] offset:20000\ns_waitcnt lgkmcnt(0)\ns_barrier\nQda 20,57,16640\nQda 24,57,19968\ns_mov_b32 s68, 204800\n"
"s_waitcnt lgkmcnt(1)\nbuffer_store_dwordx4 a[20:23], v64, s[40:43], s68 offen offset:0 sc1\ns_mov_b32 s68, 245760\ns_waitcnt lgkmcnt(0)\nbuffer_store_dwordx4 a[24:27], v64, s[40:43], s68 offen offset:0 sc1\n.Lfin:\n.Lpg26:\ns_cbranch_execz .Ltr8\n"
"s_waitcnt vmcnt(0) lgkmcnt(0)\ns_branch .Lexit\n.Lend:\n.Lfb:\ns_waitcnt vmcnt(0) lgkmcnt(0)\nv_lshrrev_b32 v52, 4, v60\nv_and_b32 v53, 15, v52\nv_lshrrev_b32 v52, 4, v52\n"
"v_lshlrev_b32 v66, 8, v53\nv_lshl_add_u32 v66, v52, 4, v66\nv_add_u32 v66, 1024, v66\nv_lshlrev_b32 v67, 10, v52\nv_lshl_add_u32 v67, v53, 1, v67\nv_add_u32 v67, 1024, v67\nv_mov_b32 v52, 0x3f803f80\nv_accvgpr_write_b32 a204, v52\n"
"v_accvgpr_write_b32 a205, v52\nv_accvgpr_write_b32 a206, v52\nv_accvgpr_write_b32 a207, v52\nv_mov_b32 v24, 0xff800000\nv_mov_b32 v28, 0xff800000\nv_mov_b32 v32, 0xff800000\nv_mov_b32 v36, 0xff800000\nv_mov_b32 v40, 0xff800000\n"
"v_mov_b32 v44, 0xff800000\nv_mov_b32 v48, 0xff800000\ns_lshl_b32 s89, s67, 21\ns_lshl_b32 s68, s93, 18\ns_add_u32 s89, s89, s68\ns_mov_b32 s66, 0\n.Lfb1:\ns_add_u32 s68, s89, 4096\n"
"buffer_load_dwordx4 a[112:115], v66, s[40:43], s89 offen offset:0\nbuffer_load_dwordx4 a[116:119], v66, s[40:43], s89 offen offset:64\nbuffer_load_dwordx4 a[120:123], v66, s[40:43], s89 offen offset:128\nbuffer_load_dwordx4 a[124:127], v66, s[40:43], s89 offen offset:192\nbuffer_load_dwordx4 a[128:131], v66, s[40:43], s68 offen offset:0\nbuffer_load_dwordx4 a[132:135], v66, s[40:43], s68 offen offset:64\nbuffer_load_dwordx4 a[136:139], v66, s[40:43], s68 offen offset:128\nbuffer_load_dwordx4 a[140:143], v66, s[40:43], s68 offen offset:192\n"
"s_waitcnt vmcnt(0)\nQzvaa 0,112,0\nQmvaa 0,116,4\nQmvaa 0,120,8\nQmvaa 0,124,12\nQzvaa 4,128,0\nQmvaa 4,132,4\nQmvaa 4,136,8\n"
"Qmvaa 4,140,12\ns_nop 7\nv_max3_f32 v24, v24, v0, v1\nv_max3_f32 v24, v24, v2, v3\nv_max3_f32 v24, v24, v4, v5\nv_max3_f32 v24, v24, v6, v7\nQzvaa 0,112,16\nQmvaa 0,116,20\n"
"Qmvaa 0,120,24\nQmvaa 0,124,28\nQzvaa 4,128,16\nQmvaa 4,132,20\nQmvaa 4,136,24\nQmvaa 4,140,28\ns_nop 7\nv_max3_f32 v28, v28, v0, v1\n"
"v_max3_f32 v28, v28, v2, v3\nv_max3_f32 v28, v28, v4, v5\nv_max3_f32 v28, v28, v6, v7\nQzvaa 0,112,32\nQmvaa 0,116,36\nQmvaa 0,120,40\nQmvaa 0,124,44\nQzvaa 4,128,32\n"
"Qmvaa 4,132,36\nQmvaa 4,136,40\nQmvaa 4,140,44\ns_nop 7\nv_max3_f32 v32, v32, v0, v1\nv_max3_f32 v32, v32, v2, v3\nv_max3_f32 v32, v32, v4, v5\nv_max3_f32 v32, v32, v6, v7\n"
"Qzvaa 0,112,48\nQmvaa 0,116,52\nQmvaa 0,120,56\nQmvaa 0,124,60\nQzvaa 4,128,48\nQmvaa 4,132,52\nQmvaa 4,136,56\nQmvaa 4,140,60\n"
"s_nop 7\nv_max3_f32 v36, v36, v0, v1\nv_max3_f32 v36, v36, v2, v3\nv_max3_f32 v36, v36, v4, v5\nv_max3_f32 v36, v36, v6, v7\nQzvaa 0,112,64\nQmvaa 0,116,68\nQmvaa 0,120,72\n"
"Qmvaa 0,124,76\nQzvaa 4,128,64\nQmvaa 4,132,68\nQmvaa 4,136,72\nQmvaa 4,140,76\ns_nop 7\nv_max3_f32 v40, v40, v0, v1\nv_max3_f32 v40, v40, v2, v3\n"
"v_max3_f32 v40, v40, v4, v5\nv_max3_f32 v40, v40, v6, v7\nQzvaa 0,112,80\nQmvaa 0,116,84\nQmvaa 0,120,88\nQmvaa 0,124,92\nQzvaa 4,128,80\nQmvaa 4,132,84\n"
"Qmvaa 4,136,88\nQmvaa 4,140,92\ns_nop 7\nv_max3_f32 v44, v44, v0, v1\nv_max3_f32 v44, v44, v2, v3\nv_max3_f32 v44, v44, v4, v5\nv_max3_f32 v44, v44, v6, v7\nQzvaa 0,112,96\n"
"Qmvaa 0,116,100\nQmvaa 0,120,104\nQmvaa 0,124,108\nQzvaa 4,128,96\nQmvaa 4,132,100\nQmvaa 4,136,104\nQmvaa 4,140,108\ns_nop 7\n"
"v_max3_f32 v48, v48, v0, v1\nv_max3_f32 v48, v48, v2, v3\nv_max3_f32 v48, v48, v4, v5\nv_max3_f32 v48, v48, v6, v7\ns_add_u32 s89, s89, 8192\ns_add_u32 s66, s66, 1\ns_cmp_lt_u32 s66, 32\ns_cbranch_scc1 .Lfb1\n"
"v_mov_b32 v25, v24\ns_nop 1\nv_permlane32_swap_b32 v24, v25\nv_max_f32 v24, v24, v25\nv_mov_b32 v25, v24\ns_nop 1\nv_permlane16_swap_b32 v24, v25\nv_max_f32 v24, v24, v25\n"
"v_sub_f32 v24, 0, v24\nv_mov_b32 v25, v24\nv_mov_b32 v26, v24\nv_mov_b32 v27, v24\nv_mov_b32 v29, v28\ns_nop 1\nv_permlane32_swap_b32 v28, v29\nv_max_f32 v28, v28, v29\n"
"v_mov_b32 v29, v28\ns_nop 1\nv_permlane16_swap_b32 v28, v29\nv_max_f32 v28, v28, v29\nv_sub_f32 v28, 0, v28\nv_mov_b32 v29, v28\nv_mov_b32 v30, v28\nv_mov_b32 v31, v28\n"
"v_mov_b32 v33, v32\ns_nop 1\nv_permlane32_swap_b32 v32, v33\nv_max_f32 v32, v32, v33\nv_mov_b32 v33, v32\ns_nop 1\nv_permlane16_swap_b32 v32, v33\nv_max_f32 v32, v32, v33\n"
"v_sub_f32 v32, 0, v32\nv_mov_b32 v33, v32\nv_mov_b32 v34, v32\nv_mov_b32 v35, v32\nv_mov_b32 v37, v36\ns_nop 1\nv_permlane32_swap_b32 v36, v37\nv_max_f32 v36, v36, v37\n"
"v_mov_b32 v37, v36\ns_nop 1\nv_permlane16_swap_b32 v36, v37\nv_max_f32 v36, v36, v37\nv_sub_f32 v36, 0, v36\nv_mov_b32 v37, v36\nv_mov_b32 v38, v36\nv_mov_b32 v39, v36\n"
"v_mov_b32 v41, v40\ns_nop 1\nv_permlane32_swap_b32 v40, v41\nv_max_f32 v40, v40, v41\nv_mov_b32 v41, v40\ns_nop 1\nv_permlane16_swap_b32 v40, v41\nv_max_f32 v40, v40, v41\n"
"v_sub_f32 v40, 0, v40\nv_mov_b32 v41, v40\nv_mov_b32 v42, v40\nv_mov_b32 v43, v40\nv_mov_b32 v45, v44\ns_nop 1\nv_permlane32_swap_b32 v44, v45\nv_max_f32 v44, v44, v45\n"
"v_mov_b32 v45, v44\ns_nop 1\nv_permlane16_swap_b32 v44, v45\nv_max_f32 v44, v44, v45\nv_sub_f32 v44, 0, v44\nv_mov_b32 v45, v44\nv_mov_b32 v46, v44\nv_mov_b32 v47, v44\n"
"v_mov_b32 v49, v48\ns_nop 1\nv_permlane32_swap_b32 v48, v49\nv_max_f32 v48, v48, v49\nv_mov_b32 v49, v48\ns_nop 1\nv_permlane16_swap_b32 v48, v49\nv_max_f32 v48, v48, v49\n"
"v_sub_f32 v48, 0, v48\nv_mov_b32 v49, v48\nv_mov_b32 v50, v48\nv_mov_b32 v51, v48\nv_mov_b32 v52, 0\nv_mov_b32 v80, 0\nv_mov_b32 v81, 0\nv_mov_b32 v82, 0\n"
"v_mov_b32 v83, 0\nv_mov_b32 v84, 0\nv_mov_b32 v85, 0\nv_mov_b32 v86, 0\nv_mov_b32 v87, 0\nv_mov_b32 v88, 0\nv_mov_b32 v89, 0\nv_mov_b32 v90, 0\n"
"v_mov_b32 v91, 0\nv_mov_b32 v92, 0\nv_mov_b32 v93, 0\nv_mov_b32 v94, 0\nv_mov_b32 v95, 0\nv_mov_b32 v96, 0\nv_mov_b32 v97, 0\nv_mov_b32 v98, 0\n"
"v_mov_b32 v99, 0\nv_mov_b32 v100, 0\nv_mov_b32 v101, 0\nv_mov_b32 v102, 0\nv_mov_b32 v103, 0\nv_mov_b32 v104, 0\nv_mov_b32 v105, 0\nv_mov_b32 v106, 0\n"
"v_mov_b32 v107, 0\nv_mov_b32 v108, 0\nv_mov_b32 v109, 0\nv_mov_b32 v110, 0\nv_mov_b32 v111, 0\nv_accvgpr_write_b32 a176, v52\nv_accvgpr_write_b32 a177, v52\nv_accvgpr_write_b32 a178, v52\n"
"v_accvgpr_write_b32 a179, v52\nv_mov_b32 v112, 0\nv_mov_b32 v113, 0\nv_mov_b32 v114, 0\nv_mov_b32 v115, 0\nv_mov_b32 v116, 0\nv_mov_b32 v117, 0\nv_mov_b32 v118, 0\n"
"v_mov_b32 v119, 0\nv_mov_b32 v120, 0\nv_mov_b32 v121, 0\nv_mov_b32 v122, 0\nv_mov_b32 v123, 0\nv_mov_b32 v124, 0\nv_mov_b32 v125, 0\nv_mov_b32 v126, 0\n"
"v_mov_b32 v127, 0\nv_mov_b32 v128, 0\nv_mov_b32 v129, 0\nv_mov_b32 v130, 0\nv_mov_b32 v131, 0\nv_mov_b32 v132, 0\nv_mov_b32 v133, 0\nv_mov_b32 v134, 0\n"
"v_mov_b32 v135, 0\nv_mov_b32 v136, 0\nv_mov_b32 v137, 0\nv_mov_b32 v138, 0\nv_mov_b32 v139, 0\nv_mov_b32 v140, 0\nv_mov_b32 v141, 0\nv_mov_b32 v142, 0\n"
"v_mov_b32 v143, 0\nv_accvgpr_write_b32 a180, v52\nv_accvgpr_write_b32 a181, v52\nv_accvgpr_write_b32 a182, v52\nv_accvgpr_write_b32 a183, v52\nv_mov_b32 v144, 0\nv_mov_b32 v145, 0\nv_mov_b32 v146, 0\n"
"v_mov_b32 v147, 0\nv_mov_b32 v148, 0\nv_mov_b32 v149, 0\nv_mov_b32 v150, 0\nv_mov_b32 v151, 0\nv_mov_b32 v152, 0\nv_mov_b32 v153, 0\nv_mov_b32 v154, 0\n"
"v_mov_b32 v155, 0\nv_mov_b32 v156, 0\nv_mov_b32 v157, 0\nv_mov_b32 v158, 0\nv_mov_b32 v159, 0\nv_mov_b32 v160, 0\nv_mov_b32 v161, 0\nv_mov_b32 v162, 0\n"
"v_mov_b32 v163, 0\nv_mov_b32 v164, 0\nv_mov_b32 v165, 0\nv_mov_b32 v166, 0\nv_mov_b32 v167, 0\nv_mov_b32 v168, 0\nv_mov_b32 v169, 0\nv_mov_b32 v170, 0\n"
"v_mov_b32 v171, 0\nv_mov_b32 v172, 0\nv_mov_b32 v173, 0\nv_mov_b32 v174, 0\nv_mov_b32 v175, 0\nv_accvgpr_write_b32 a184, v52\nv_accvgpr_write_b32 a185, v52\nv_accvgpr_write_b32 a186, v52\n"
"v_accvgpr_write_b32 a187, v52\nv_mov_b32 v176, 0\nv_mov_b32 v177, 0\nv_mov_b32 v178, 0\nv_mov_b32 v179, 0\nv_mov_b32 v180, 0\nv_mov_b32 v181, 0\nv_mov_b32 v182, 0\n"
"v_mov_b32 v183, 0\nv_mov_b32 v184, 0\nv_mov_b32 v185, 0\nv_mov_b32 v186, 0\nv_mov_b32 v187, 0\nv_mov_b32 v188, 0\nv_mov_b32 v189, 0\nv_mov_b32 v190, 0\n"
"v_mov_b32 v191, 0\nv_mov_b32 v192, 0\nv_mov_b32 v193, 0\nv_mov_b32 v194, 0\nv_mov_b32 v195, 0\nv_mov_b32 v196, 0\nv_mov_b32 v197, 0\nv_mov_b32 v198, 0\n"
"v_mov_b32 v199, 0\nv_mov_b32 v200, 0\nv_mov_b32 v201, 0\nv_mov_b32 v202, 0\nv_mov_b32 v203, 0\nv_mov_b32 v204, 0\nv_mov_b32 v205, 0\nv_mov_b32 v206, 0\n"
"v_mov_b32 v207, 0\nv_accvgpr_write_b32 a188, v52\nv_accvgpr_write_b32 a189, v52\nv_accvgpr_write_b32 a190, v52\nv_accvgpr_write_b32 a191, v52\nv_mov_b32 v208, 0\nv_mov_b32 v209, 0\nv_mov_b32 v210, 0\n"
"v_mov_b32 v211, 0\nv_mov_b32 v212, 0\nv_mov_b32 v213, 0\nv_mov_b32 v214, 0\nv_mov_b32 v215, 0\nv_mov_b32 v216, 0\nv_mov_b32 v217, 0\nv_mov_b32 v218, 0\n"
"v_mov_b32 v219, 0\nv_mov_b32 v220, 0\nv_mov_b32 v221, 0\nv_mov_b32 v222, 0\nv_mov_b32 v223, 0\nv_mov_b32 v224, 0\nv_mov_b32 v225, 0\nv_mov_b32 v226, 0\n"
"v_mov_b32 v227, 0\nv_mov_b32 v228, 0\nv_mov_b32 v229, 0\nv_mov_b32 v230, 0\nv_mov_b32 v231, 0\nv_mov_b32 v232, 0\nv_mov_b32 v233, 0\nv_mov_b32 v234, 0\n"
"v_mov_b32 v235, 0\nv_mov_b32 v236, 0\nv_mov_b32 v237, 0\nv_mov_b32 v238, 0\nv_mov_b32 v239, 0\nv_accvgpr_write_b32 a192, v52\nv_accvgpr_write_b32 a193, v52\nv_accvgpr_write_b32 a194, v52\n"
"v_accvgpr_write_b32 a195, v52\nv_mov_b32 v240, 0\nv_mov_b32 v241, 0\nv_mov_b32 v242, 0\nv_mov_b32 v243, 0\nv_mov_b32 v244, 0\nv_mov_b32 v245, 0\nv_mov_b32 v246, 0\n"
"v_mov_b32 v247, 0\nv_mov_b32 v248, 0\nv_mov_b32 v249, 0\nv_mov_b32 v250, 0\nv_mov_b32 v251, 0\nv_mov_b32 v252, 0\nv_mov_b32 v253, 0\nv_mov_b32 v254, 0\n"
"v_mov_b32 v255, 0\nv_accvgpr_write_b32 a208, v52\nv_accvgpr_write_b32 a209, v52\nv_accvgpr_write_b32 a210, v52\nv_accvgpr_write_b32 a211, v52\nv_accvgpr_write_b32 a212, v52\nv_accvgpr_write_b32 a213, v52\nv_accvgpr_write_b32 a214, v52\n"
"v_accvgpr_write_b32 a215, v52\nv_accvgpr_write_b32 a216, v52\nv_accvgpr_write_b32 a217, v52\nv_accvgpr_write_b32 a218, v52\nv_accvgpr_write_b32 a219, v52\nv_accvgpr_write_b32 a220, v52\nv_accvgpr_write_b32 a221, v52\nv_accvgpr_write_b32 a222, v52\n"
"v_accvgpr_write_b32 a223, v52\nv_accvgpr_write_b32 a196, v52\nv_accvgpr_write_b32 a197, v52\nv_accvgpr_write_b32 a198, v52\nv_accvgpr_write_b32 a199, v52\nv_accvgpr_write_b32 a224, v52\nv_accvgpr_write_b32 a225, v52\nv_accvgpr_write_b32 a226, v52\n"
"v_accvgpr_write_b32 a227, v52\nv_accvgpr_write_b32 a228, v52\nv_accvgpr_write_b32 a229, v52\nv_accvgpr_write_b32 a230, v52\nv_accvgpr_write_b32 a231, v52\nv_accvgpr_write_b32 a232, v52\nv_accvgpr_write_b32 a233, v52\nv_accvgpr_write_b32 a234, v52\n"
"v_accvgpr_write_b32 a235, v52\nv_accvgpr_write_b32 a236, v52\nv_accvgpr_write_b32 a237, v52\nv_accvgpr_write_b32 a238, v52\nv_accvgpr_write_b32 a239, v52\nv_accvgpr_write_b32 a240, v52\nv_accvgpr_write_b32 a241, v52\nv_accvgpr_write_b32 a242, v52\n"
"v_accvgpr_write_b32 a243, v52\nv_accvgpr_write_b32 a244, v52\nv_accvgpr_write_b32 a245, v52\nv_accvgpr_write_b32 a246, v52\nv_accvgpr_write_b32 a247, v52\nv_accvgpr_write_b32 a248, v52\nv_accvgpr_write_b32 a249, v52\nv_accvgpr_write_b32 a250, v52\n"
"v_accvgpr_write_b32 a251, v52\nv_accvgpr_write_b32 a252, v52\nv_accvgpr_write_b32 a253, v52\nv_accvgpr_write_b32 a254, v52\nv_accvgpr_write_b32 a255, v52\nv_accvgpr_write_b32 a200, v52\nv_accvgpr_write_b32 a201, v52\nv_accvgpr_write_b32 a202, v52\n"
"v_accvgpr_write_b32 a203, v52\ns_lshl_b32 s89, s67, 21\ns_lshl_b32 s68, s93, 18\ns_add_u32 s89, s89, s68\ns_mov_b32 s66, 0\n.Lfb2:\ns_add_u32 s68, s89, 4096\nbuffer_load_dwordx4 a[112:115], v66, s[40:43], s89 offen offset:0\n"
"buffer_load_dwordx4 a[116:119], v66, s[40:43], s89 offen offset:64\nbuffer_load_dwordx4 a[120:123], v66, s[40:43], s89 offen offset:128\nbuffer_load_dwordx4 a[124:127], v66, s[40:43], s89 offen offset:192\nbuffer_load_dwordx4 a[128:131], v66, s[40:43], s68 offen offset:0\nbuffer_load_dwordx4 a[132:135], v66, s[40:43], s68 offen offset:64\nbuffer_load_dwordx4 a[136:139], v66, s[40:43], s68 offen offset:128\nbuffer_load_dwordx4 a[140:143], v66, s[40:43], s68 offen offset:192\nbuffer_load_ushort v8, v67, s[44:47], s89 offen offset:0\n"
"buffer_load_ushort v9, v67, s[44:47], s89 offen offset:256\nbuffer_load_ushort v10, v67, s[44:47], s89 offen offset:512\nbuffer_load_ushort v11, v67, s[44:47], s89 offen offset:768\nbuffer_load_ushort v12, v67, s[44:47], s68 offen offset:0\nbuffer_load_ushort v13, v67, s[44:47], s68 offen offset:256\nbuffer_load_ushort v14, v67, s[44:47], s68 offen offset:512\nbuffer_load_ushort v15, v67, s[44:47], s68 offen offset:768\ns_waitcnt vmcnt(0)\n"
"v_lshl_or_b32 v8, v9, 16, v8\nv_accvgpr_write_b32 a144, v8\nv_lshl_or_b32 v10, v11, 16, v10\nv_accvgpr_write_b32 a145, v10\nv_lshl_or_b32 v12, v13, 16, v12\nv_accvgpr_write_b32 a146, v12\nv_lshl_or_b32 v14, v15, 16, v14\nv_accvgpr_write_b32 a147, v14\n"
"buffer_load_ushort v8, v67, s[44:47], s89 offen offset:32\nbuffer_load_ushort v9, v67, s[44:47], s89 offen offset:288\nbuffer_load_ushort v10, v67, s[44:47], s89 offen offset:544\nbuffer_load_ushort v11, v67, s[44:47], s89 offen offset:800\nbuffer_load_ushort v12, v67, s[44:47], s68 offen offset:32\nbuffer_load_ushort v13, v67, s[44:47], s68 offen offset:288\nbuffer_load_ushort v14, v67, s[44:47], s68 offen offset:544\nbuffer_load_ushort v15, v67, s[44:47], s68 offen offset:800\n"
"s_waitcnt vmcnt(0)\nv_lshl_or_b32 v8, v9, 16, v8\nv_accvgpr_write_b32 a148, v8\nv_lshl_or_b32 v10, v11, 16, v10\nv_accvgpr_write_b32 a149, v10\nv_lshl_or_b32 v12, v13, 16, v12\nv_accvgpr_write_b32 a150, v12\nv_lshl_or_b32 v14, v15, 16, v14\n"
"v_accvgpr_write_b32 a151, v14\nbuffer_load_ushort v8, v67, s[44:47], s89 offen offset:64\nbuffer_load_ushort v9, v67, s[44:47], s89 offen offset:320\nbuffer_load_ushort v10, v67, s[44:47], s89 offen offset:576\nbuffer_load_ushort v11, v67, s[44:47], s89 offen offset:832\nbuffer_load_ushort v12, v67, s[44:47], s68 offen offset:64\nbuffer_load_ushort v13, v67, s[44:47], s68 offen offset:320\nbuffer_load_ushort v14, v67, s[44:47], s68 offen offset:576\n"
"buffer_load_ushort v15, v67, s[44:47], s68 offen offset:832\ns_waitcnt vmcnt(0)\nv_lshl_or_b32 v8, v9, 16, v8\nv_accvgpr_write_b32 a152, v8\nv_lshl_or_b32 v10, v11, 16, v10\nv_accvgpr_write_b32 a153, v10\nv_lshl_or_b32 v12, v13, 16, v12\nv_accvgpr_write_b32 a154, v12\n"
"v_lshl_or_b32 v14, v15, 16, v14\nv_accvgpr_write_b32 a155, v14\nbuffer_load_ushort v8, v67, s[44:47], s89 offen offset:96\nbuffer_load_ushort v9, v67, s[44:47], s89 offen offset:352\nbuffer_load_ushort v10, v67, s[44:47], s89 offen offset:608\nbuffer_load_ushort v11, v67, s[44:47], s89 offen offset:864\nbuffer_load_ushort v12, v67, s[44:47], s68 offen offset:96\nbuffer_load_ushort v13, v67, s[44:47], s68 offen offset:352\n"
"buffer_load_ushort v14, v67, s[44:47], s68 offen offset:608\nbuffer_load_ushort v15, v67, s[44:47], s68 offen offset:864\ns_waitcnt vmcnt(0)\nv_lshl_or_b32 v8, v9, 16, v8\nv_accvgpr_write_b32 a156, v8\nv_lshl_or_b32 v10, v11, 16, v10\nv_accvgpr_write_b32 a157, v10\nv_lshl_or_b32 v12, v13, 16, v12\n"
"v_accvgpr_write_b32 a158, v12\nv_lshl_or_b32 v14, v15, 16, v14\nv_accvgpr_write_b32 a159, v14\nbuffer_load_ushort v8, v67, s[44:47], s89 offen offset:128\nbuffer_load_ushort v9, v67, s[44:47], s89 offen offset:384\nbuffer_load_ushort v10, v67, s[44:47], s89 offen offset:640\nbuffer_load_ushort v11, v67, s[44:47], s89 offen offset:896\nbuffer_load_ushort v12, v67, s[44:47], s68 offen offset:128\n"
"buffer_load_ushort v13, v67, s[44:47], s68 offen offset:384\nbuffer_load_ushort v14, v67, s[44:47], s68 offen offset:640\nbuffer_load_ushort v15, v67, s[44:47], s68 offen offset:896\ns_waitcnt vmcnt(0)\nv_lshl_or_b32 v8, v9, 16, v8\nv_accvgpr_write_b32 a160, v8\nv_lshl_or_b32 v10, v11, 16, v10\nv_accvgpr_write_b32 a161, v10\n"
"v_lshl_or_b32 v12, v13, 16, v12\nv_accvgpr_write_b32 a162, v12\nv_lshl_or_b32 v14, v15, 16, v14\nv_accvgpr_write_b32 a163, v14\nbuffer_load_ushort v8, v67, s[44:47], s89 offen offset:160\nbuffer_load_ushort v9, v67, s[44:47], s89 offen offset:416\nbuffer_load_ushort v10, v67, s[44:47], s89 offen offset:672\nbuffer_load_ushort v11, v67, s[44:47], s89 offen offset:928\n"
"buffer_load_ushort v12, v67, s[44:47], s68 offen offset:160\nbuffer_load_ushort v13, v67, s[44:47], s68 offen offset:416\nbuffer_load_ushort v14, v67, s[44:47], s68 offen offset:672\nbuffer_load_ushort v15, v67, s[44:47], s68 offen offset:928\ns_waitcnt vmcnt(0)\nv_lshl_or_b32 v8, v9, 16, v8\nv_accvgpr_write_b32 a164, v8\nv_lshl_or_b32 v10, v11, 16, v10\n"
"v_accvgpr_write_b32 a165, v10\nv_lshl_or_b32 v12, v13, 16, v12\nv_accvgpr_write_b32 a166, v12\nv_lshl_or_b32 v14, v15, 16, v14\nv_accvgpr_write_b32 a167, v14\nbuffer_load_ushort v8, v67, s[44:47], s89 offen offset:192\nbuffer_load_ushort v9, v67, s[44:47], s89 offen offset:448\nbuffer_load_ushort v10, v67, s[44:47], s89 offen offset:704\n"
"buffer_load_ushort v11, v67, s[44:47], s89 offen offset:960\nbuffer_load_ushort v12, v67, s[44:47], s68 offen offset:192\nbuffer_load_ushort v13, v67, s[44:47], s68 offen offset:448\nbuffer_load_ushort v14, v67, s[44:47], s68 offen offset:704\nbuffer_load_ushort v15, v67, s[44:47], s68 offen offset:960\ns_waitcnt vmcnt(0)\nv_lshl_or_b32 v8, v9, 16, v8\nv_accvgpr_write_b32 a168, v8\n"
"v_lshl_or_b32 v10, v11, 16, v10\nv_accvgpr_write_b32 a169, v10\nv_lshl_or_b32 v12, v13, 16, v12\nv_accvgpr_write_b32 a170, v12\nv_lshl_or_b32 v14, v15, 16, v14\nv_accvgpr_write_b32 a171, v14\nbuffer_load_ushort v8, v67, s[44:47], s89 offen offset:224\nbuffer_load_ushort v9, v67, s[44:47], s89 offen offset:480\n"
"buffer_load_ushort v10, v67, s[44:47], s89 offen offset:736\nbuffer_load_ushort v11, v67, s[44:47], s89 offen offset:992\nbuffer_load_ushort v12, v67, s[44:47], s68 offen offset:224\nbuffer_load_ushort v13, v67, s[44:47], s68 offen offset:480\nbuffer_load_ushort v14, v67, s[44:47], s68 offen offset:736\nbuffer_load_ushort v15, v67, s[44:47], s68 offen offset:992\ns_waitcnt vmcnt(0)\nv_lshl_or_b32 v8, v9, 16, v8\n"
"v_accvgpr_write_b32 a172, v8\nv_lshl_or_b32 v10, v11, 16, v10\nv_accvgpr_write_b32 a173, v10\nv_lshl_or_b32 v12, v13, 16, v12\nv_accvgpr_write_b32 a174, v12\nv_lshl_or_b32 v14, v15, 16, v14\nv_accvgpr_write_b32 a175, v14\nQcvaa 0,112,0,24\n"
"Qmvaa 0,116,4\nQmvaa 0,120,8\nQmvaa 0,124,12\nQcvaa 4,128,0,24\nQmvaa 4,132,4\nQmvaa 4,136,8\nQmvaa 4,140,12\ns_nop 7\n"
"Qe 0\nQe 1\nQe 2\nQe 3\nQe 4\nQe 5\nQe 6\nQe 7\n"
"s_nop 0\nQk 16,0,1\nQk 17,2,3\nQk 18,4,5\nQk 19,6,7\ns_nop 1\nQmvva 80,16,144\nQmvva 84,16,148\n"
"Qmvva 88,16,152\nQmvva 92,16,156\nQmvva 96,16,160\nQmvva 100,16,164\nQmvva 104,16,168\nQmvva 108,16,172\nQmaav 176,204,16\nQcvaa 0,112,16,28\n"
"Qmvaa 0,116,20\nQmvaa 0,120,24\nQmvaa 0,124,28\nQcvaa 4,128,16,28\nQmvaa 4,132,20\nQmvaa 4,136,24\nQmvaa 4,140,28\ns_nop 7\n"
"Qe 0\nQe 1\nQe 2\nQe 3\nQe 4\nQe 5\nQe 6\nQe 7\n"
"s_nop 0\nQk 16,0,1\nQk 17,2,3\nQk 18,4,5\nQk 19,6,7\ns_nop 1\nQmvva 112,16,144\nQmvva 116,16,148\n"
"Qmvva 120,16,152\nQmvva 124,16,156\nQmvva 128,16,160\nQmvva 132,16,164\nQmvva 136,16,168\nQmvva 140,16,172\nQmaav 180,204,16\nQcvaa 0,112,32,32\n"
"Qmvaa 0,116,36\nQmvaa 0,120,40\nQmvaa 0,124,44\nQcvaa 4,128,32,32\nQmvaa 4,132,36\nQmvaa 4,136,40\nQmvaa 4,140,44\ns_nop 7\n"
"Qe 0\nQe 1\nQe 2\nQe 3\nQe 4\nQe 5\nQe 6\nQe 7\n"
"s_nop 0\nQk 16,0,1\nQk 17,2,3\nQk 18,4,5\nQk 19,6,7\ns_nop 1\nQmvva 144,16,144\nQmvva 148,16,148\n"
"Qmvva 152,16,152\nQmvva 156,16,156\nQmvva 160,16,160\nQmvva 164,16,164\nQmvva 168,16,168\nQmvva 172,16,172\nQmaav 184,204,16\nQcvaa 0,112,48,36\n"
"Qmvaa 0,116,52\nQmvaa 0,120,56\nQmvaa 0,124,60\nQcvaa 4,128,48,36\nQmvaa 4,132,52\nQmvaa 4,136,56\nQmvaa 4,140,60\ns_nop 7\n"
"Qe 0\nQe 1\nQe 2\nQe 3\nQe 4\nQe 5\nQe 6\nQe 7\n"
"s_nop 0\nQk 16,0,1\nQk 17,2,3\nQk 18,4,5\nQk 19,6,7\ns_nop 1\nQmvva 176,16,144\nQmvva 180,16,148\n"
"Qmvva 184,16,152\nQmvva 188,16,156\nQmvva 192,16,160\nQmvva 196,16,164\nQmvva 200,16,168\nQmvva 204,16,172\nQmaav 188,204,16\nQcvaa 0,112,64,40\n"
"Qmvaa 0,116,68\nQmvaa 0,120,72\nQmvaa 0,124,76\nQcvaa 4,128,64,40\nQmvaa 4,132,68\nQmvaa 4,136,72\nQmvaa 4,140,76\ns_nop 7\n"
"Qe 0\nQe 1\nQe 2\nQe 3\nQe 4\nQe 5\nQe 6\nQe 7\n"
"s_nop 0\nQk 16,0,1\nQk 17,2,3\nQk 18,4,5\nQk 19,6,7\ns_nop 1\nQmvva 208,16,144\nQmvva 212,16,148\n"
"Qmvva 216,16,152\nQmvva 220,16,156\nQmvva 224,16,160\nQmvva 228,16,164\nQmvva 232,16,168\nQmvva 236,16,172\nQmaav 192,204,16\nQcvaa 0,112,80,44\n"
"Qmvaa 0,116,84\nQmvaa 0,120,88\nQmvaa 0,124,92\nQcvaa 4,128,80,44\nQmvaa 4,132,84\nQmvaa 4,136,88\nQmvaa 4,140,92\ns_nop 7\n"
"Qe 0\nQe 1\nQe 2\nQe 3\nQe 4\nQe 5\nQe 6\nQe 7\n"
"s_nop 0\nQk 16,0,1\nQk 17,2,3\nQk 18,4,5\nQk 19,6,7\ns_nop 1\nQmvva 240,16,144\nQmvva 244,16,148\n"
"Qmvva 248,16,152\nQmvva 252,16,156\nQmava 208,16,160\nQmava 212,16,164\nQmava 216,16,168\nQmava 220,16,172\nQmaav 196,204,16\nQcvaa 0,112,96,48\n"
"Qmvaa 0,116,100\nQmvaa 0,120,104\nQmvaa 0,124,108\nQcvaa 4,128,96,48\nQmvaa 4,132,100\nQmvaa 4,136,104\nQmvaa 4,140,108\ns_nop 7\n"
"Qe 0\nQe 1\nQe 2\nQe 3\nQe 4\nQe 5\nQe 6\nQe 7\n"
"s_nop 0\nQk 16,0,1\nQk 17,2,3\nQk 18,4,5\nQk 19,6,7\ns_nop 1\nQmava 224,16,144\nQmava 228,16,148\n"
"Qmava 232,16,152\nQmava 236,16,156\nQmava 240,16,160\nQmava 244,16,164\nQmava 248,16,168\nQmava 252,16,172\nQmaav 200,204,16\ns_add_u32 s89, s89, 8192\n"
"s_add_u32 s66, s66, 1\ns_cmp_lt_u32 s66, 32\ns_cbranch_scc1 .Lfb2\ns_nop 7\nv_accvgpr_read_b32 v0, a176\nv_accvgpr_read_b32 v4, a180\nv_accvgpr_read_b32 v8, a184\nv_accvgpr_read_b32 v12, a188\n"
"v_accvgpr_read_b32 v16, a192\nv_accvgpr_read_b32 v20, a196\nv_accvgpr_read_b32 v24, a200\ns_mov_b32 s73, 0\nv_mov_b32 v30, s73\nv_mov_b32 v31, 0\nv_mov_b32 v58, 0xb9800000\nv_mov_b32 v59, 0\n"
"v_and_b32 v66, 16, v60\nv_mov_b32 v67, 0xffff\nv_lshlrev_b32 v66, v66, v67\ns_mov_b32 vcc_lo, 0x00c0000c\ns_mov_b32 vcc_hi, 0xc0000c00\nv_cndmask_b32 v67, 0, v66, vcc\ns_lshr_b64 vcc, vcc, 2\nv_cndmask_b32 v66, 0, v66, vcc\n"
"s_cmp_eq_u32 s67, 0\ns_cbranch_scc1 .Lfbret0\ns_branch .Lfbret1\n.Lf2_w:\ns_mov_b32 s97, 0\n.Lf2_l:\ns_sleep 1\nbuffer_load_dword v78, v76, s[24:27], 0 offen sc1\n"
"s_waitcnt vmcnt(0)\ns_add_u32 s97, s97, 1\ns_cmp_gt_u32 s97, 1048576\ns_cbranch_scc1 .Lf2_r\nv_sub_u32 v66, v78, v65\nv_cmp_gt_u32 vcc, 2, v66\ns_and_b64 vcc, vcc, s[28:29]\ns_cbranch_scc1 .Lf2_l\n"
"s_branch .Lf2_r\n.Lf3_w:\ns_mov_b32 s97, 0\n.Lf3_l:\ns_sleep 1\nbuffer_load_dword v78, v76, s[24:27], 0 offen sc1\ns_waitcnt vmcnt(0)\ns_add_u32 s97, s97, 1\n"
"s_cmp_gt_u32 s97, 1048576\ns_cbranch_scc1 .Lf3_r\nv_sub_u32 v66, v78, v65\nv_cmp_gt_u32 vcc, 3, v66\ns_and_b64 vcc, vcc, s[28:29]\ns_cbranch_scc1 .Lf3_l\ns_branch .Lf3_r\n.Lx0_w:\n"
"s_mov_b32 s97, 0\n.Lx0_l:\ns_sleep 1\nbuffer_load_dword v78, v76, s[24:27], 0 offen sc1\ns_waitcnt vmcnt(0)\ns_add_u32 s97, s97, 1\ns_cmp_gt_u32 s97, 1048576\ns_cbranch_scc1 .Lx0_r\n"
"v_sub_u32 v66, v78, v65\nv_cmp_gt_u32 vcc, 1, v66\ns_and_b64 vcc, vcc, s[28:29]\ns_cbranch_scc1 .Lx0_l\ns_branch .Lx0_r\n.Lx1_w:\ns_mov_b32 s97, 0\n.Lx1_l:\n"
"s_sleep 1\nbuffer_load_dword v78, v76, s[24:27], 0 offen sc1\ns_waitcnt vmcnt(0)\ns_add_u32 s97, s97, 1\ns_cmp_gt_u32 s97, 1048576\ns_cbranch_scc1 .Lx1_r\nv_sub_u32 v66, v78, v65\nv_cmp_gt_u32 vcc, 1, v66\n"
"s_and_b64 vcc, vcc, s[28:29]\ns_cbranch_scc1 .Lx1_l\ns_branch .Lx1_r\n.Lexit:\n"
  : "=&s"(t0), "=&s"(t1)
  : "s"(__builtin_amdgcn_kernarg_segment_ptr()), "s"(blockIdx.x), "s"((unsigned)(size_t)lds), "s"(__builtin_amdgcn_readfirstlane(threadIdx.x >> 6))
  : "v0", "v1", "v2", "v3", "v4", "v5", "v6", "v7", "v8", "v9", "v10", "v11", "v12", "v13", "v14", "v15", "v16", "v17", "v18", "v19", "v20", "v21", "v22", "v23", "v24", "v25", "v26", "v27", "v28", "v29", "v30", "v31", "v32", "v33", "v34", "v35", "v36", "v37", "v38", "v39", "v40", "v41", "v42", "v43", "v44", "v45", "v46", "v47", "v48", "v49", "v50", "v51", "v52", "v53", "v54", "v55", "v56", "v57", "v58", "v59", "v60", "v61", "v62", "v63", "v64", "v65", "v66", "v67", "v68", "v69", "v70", "v71", "v72", "v73", "v74", "v75", "v76", "v77", "v78", "v79", "v80", "v81", "v82", "v83", "v84", "v85", "v86", "v87", "v88", "v89", "v90", "v91", "v92", "v93", "v94", "v95", "v96", "v97", "v98", "v99", "v100", "v101", "v102", "v103", "v104", "v105", "v106", "v107", "v108", "v109", "v110", "v111", "v112", "v113", "v114", "v115", "v116", "v117", "v118", "v119", "v120", "v121", "v122", "v123", "v124", "v125", "v126", "v127", "v128", "v129", "v130", "v131", "v132", "v133", "v134", "v135", "v136", "v137", "v138", "v139", "v140", "v141", "v142", "v143", "v144", "v145", "v146", "v147", "v148", "v149", "v150", "v151", "v152", "v153", "v154", "v155", "v156", "v157", "v158", "v159", "v160", "v161", "v162", "v163", "v164", "v165", "v166", "v167", "v168", "v169", "v170", "v171", "v172", "v173", "v174", "v175", "v176", "v177", "v178", "v179", "v180", "v181", "v182", "v183", "v184", "v185", "v186", "v187", "v188", "v189", "v190", "v191", "v192", "v193", "v194", "v195", "v196", "v197", "v198", "v199", "v200", "v201", "v202", "v203", "v204", "v205", "v206", "v207", "v208", "v209", "v210", "v211", "v212", "v213", "v214", "v215", "v216", "v217", "v218", "v219", "v220", "v221", "v222", "v223", "v224", "v225", "v226", "v227", "v228", "v229", "v230", "v231", "v232", "v233", "v234", "v235", "v236", "v237", "v238", "v239", "v240", "v241", "v242", "v243", "v244", "v245", "v246", "v247", "v248", "v249", "v250", "v251", "v252", "v253", "v254", "v255", "a0", "a1", "a2", "a3", "a4", "a5", "a6", "a7", "a8", "a9", "a10", "a11", "a12", "a13", "a14", "a15", "a16", "a17", "a18", "a19", "a20", "a21", "a22", "a23", "a24", "a25", "a26", "a27", "a28", "a29", "a30", "a31", "a32", "a33", "a34", "a35", "a36", "a37", "a38", "a39", "a40", "a41", "a42", "a43", "a44", "a45", "a46", "a47", "a48", "a49", "a50", "a51", "a52", "a53", "a54", "a55", "a56", "a57", "a58", "a59", "a60", "a61", "a62", "a63", "a64", "a65", "a66", "a67", "a68", "a69", "a70", "a71", "a72", "a73", "a74", "a75", "a76", "a77", "a78", "a79", "a80", "a81", "a82", "a83", "a84", "a85", "a86", "a87", "a88", "a89", "a90", "a91", "a92", "a93", "a94", "a95", "a96", "a97", "a98", "a99", "a100", "a101", "a102", "a103", "a104", "a105", "a106", "a107", "a108", "a109", "a110", "a111", "a112", "a113", "a114", "a115", "a116", "a117", "a118", "a119", "a120", "a121", "a122", "a123", "a124", "a125", "a126", "a127", "a128", "a129", "a130", "a131", "a132", "a133", "a134", "a135", "a136", "a137", "a138", "a139", "a140", "a141", "a142", "a143", "a144", "a145", "a146", "a147", "a148", "a149", "a150", "a151", "a152", "a153", "a154", "a155", "a156", "a157", "a158", "a159", "a160", "a161", "a162", "a163", "a164", "a165", "a166", "a167", "a168", "a169", "a170", "a171", "a172", "a173", "a174", "a175", "a176", "a177", "a178", "a179", "a180", "a181", "a182", "a183", "a184", "a185", "a186", "a187", "a188", "a189", "a190", "a191", "a192", "a193", "a194", "a195", "a196", "a197", "a198", "a199", "a200", "a201", "a202", "a203", "a204", "a205", "a206", "a207", "a208", "a209", "a210", "a211", "a212", "a213", "a214", "a215", "a216", "a217", "a218", "a219", "a220", "a221", "a222", "a223", "a224", "a225", "a226", "a227", "a228", "a229", "a230", "a231", "a232", "a233", "a234", "a235", "a236", "a237", "a238", "a239", "a240", "a241", "a242", "a243", "a244", "a245", "a246", "a247", "a248", "a249", "a250", "a251", "a252", "a253", "a254", "a255", "s8", "s9", "s10", "s11", "s12", "s13", "s14", "s15", "s16", "s17", "s18", "s19", "s20", "s21", "s22", "s23", "s24", "s25", "s26", "s27", "s28", "s29", "s30", "s31", "s33", "s34", "s35", "s36", "s37", "s38", "s39", "s40", "s41", "s42", "s43", "s44", "s45", "s46", "s47", "s48", "s49", "s50", "s51", "s52", "s53", "s54", "s55", "s56", "s57", "s58", "s59", "s60", "s61", "s62", "s63", "s64", "s65", "s66", "s67", "s68", "s69", "s70", "s71", "s72", "s73", "s74", "s75", "s76", "s77", "s78", "s79", "s80", "s81", "s82", "s83", "s84", "s85", "s86", "s87", "s88", "s89", "s90", "s91", "s92", "s93", "s94", "s95", "s96", "s97", "s98", "s99", "s100", "s101", "vcc", "scc", "memory");
}

extern "C" void kernel_launch(void* const* d_in, const int* in_sizes, int n_in, void* d_out, int out_size, void* d_ws, size_t ws_size, hipStream_t stream) {
  typedef const unsigned short* P;
  (void)hipFuncSetAttribute((const void*)mha2_asm, hipFuncAttributeMaxDynamicSharedMemorySize, 163840);
  hipLaunchKernelGGL(mha2_asm, dim3(256), dim3(256), 163840, stream, (P)d_in[0], (P)d_in[3], (P)d_in[1], (P)d_in[2], (P)d_in[5], (P)d_in[8], (P)d_in[6], (P)d_in[7],
                     (P)d_in[4], (P)d_in[9], (char*)d_ws, (unsigned short*)d_out);
}
```

```cpp
#include <hip/hip_runtime.h>
#include <stdint.h>

__global__ __launch_bounds__(256, 1) __attribute__((aligned(4096)))
void mha2_asm(const unsigned short* X0, const unsigned short* W0, const unsigned short* K0, const unsigned short* V0,
              const unsigned short* X1, const unsigned short* W1, const unsigned short* K1, const unsigned short* V1,
              const unsigned short* WO0, const unsigned short* WO1, char* ws, unsigned short* out) {
  extern __shared__ __attribute__((aligned(16))) char lds[];
  unsigned t0, t1;
  asm volatile(
".macro Qta d,v,o\nds_read_b64_tr_b16 a[\\d:\\d+1], v\\v offset:\\o\n.endm\n.macro Qda d,v,o\nds_read_b128 a[\\d:\\d+3], v\\v offset:\\o\n.endm\n.macro Qzvaa d,a,b\nv_mfma_f32_16x16x32_bf16 v[\\d:\\d+3], a[\\a:\\a+3], a[\\b:\\b+3], 0\n"
".endm\n.macro Qcvaa d,a,b,c\nv_mfma_f32_16x16x32_bf16 v[\\d:\\d+3], a[\\a:\\a+3], a[\\b:\\b+3], v[\\c:\\c+3]\n.endm\n.macro Qk d,a,b\nv_cvt_pk_bf16_f32 v\\d, v\\a, v\\b\n.endm\n.macro Qmvaa d,a,b\n"
"v_mfma_f32_16x16x32_bf16 v[\\d:\\d+3], a[\\a:\\a+3], a[\\b:\\b+3], v[\\d:\\d+3]\n.endm\n.macro Qtv d,v,o\nds_read_b64_tr_b16 v[\\d:\\d+1], v\\v offset:\\o\n.endm\n.macro Qa d,a,b\nv_add_f32 v\\d, v\\a, v\\b\n.endm\n"
".macro Qe d\nv_exp_f32 v\\d, v\\d\n.endm\n.macro Qmvva d,a,b\nv_mfma_f32_16x16x32_bf16 v[\\d:\\d+3], v[\\a:\\a+3], a[\\b:\\b+3], v[\\d:\\d+3]\n.endm\n.macro Qmava d,a,b\nv_mfma_f32_16x16x32_bf16 a[\\d:\\d+3], v[\\a:\\a+3], a[\\b:\\b+3], a[\\d:\\d+3]\n"
".endm\n.macro Qzvvv d,a,b\nv_mfma_f32_16x16x32_bf16 v[\\d:\\d+3], v[\\a:\\a+3], v[\\b:\\b+3], 0\n.endm\n.macro Qzvav d,a,b\nv_mfma_f32_16x16x32_bf16 v[\\d:\\d+3], a[\\a:\\a+3], v[\\b:\\b+3], 0\n.endm\n.macro Qmvav d,a,b\n"
"v_mfma_f32_16x16x32_bf16 v[\\d:\\d+3], a[\\a:\\a+3], v[\\b:\\b+3], v[\\d:\\d+3]\n.endm\n.macro Qcaav d,a,b,c\nv_mfma_f32_16x16x32_bf16 a[\\d:\\d+3], a[\\a:\\a+3], v[\\b:\\b+3], a[\\c:\\c+3]\n.endm\n.macro Qmaav d,a,b\nv_mfma_f32_16x16x32_bf16 a[\\d:\\d+3], a[\\a:\\a+3], v[\\b:\\b+3], a[\\d:\\d+3]\n.endm\n"
".macro Qcvav d,a,b,c\nv_mfma_f32_16x16x32_bf16 v[\\d:\\d+3], a[\\a:\\a+3], v[\\b:\\b+3], v[\\c:\\c+3]\n.endm\n.macro Qmaaa d,a,b\nv_mfma_f32_16x16x32_bf16 a[\\d:\\d+3], a[\\a:\\a+3], a[\\b:\\b+3], a[\\d:\\d+3]\n.endm\n.macro Qdv d,v,o\nds_read_b128 v[\\d:\\d+3], v\\v offset:\\o\n"
".endm\n.p2align 3\ns_and_b32 s33, %3, 4\ns_lshl_b32 s30, s33, 3\ns_load_dwordx8 s[8:15], %2, s30\ns_lshl_b32 s33, s33, 1\ns_load_dwordx4 s[20:23], %2, 0x50\ns_load_dwordx2 s[16:17], %2, s33 offset:0x40\n"
"s_memtime s[80:81]\ns_memrealtime s[82:83]\ns_bfe_u32 s19, %3, 0x30003\ns_and_b32 s39, %3, 3\ns_lshr_b32 %1, %3, 6\ns_lshl2_add_u32 s30, s39, %1\ns_lshl3_add_u32 %0, s30, s19\ns_and_b32 %1, s19, 4\n"
"s_add_u32 s18, s19, %1\ns_lshr_b32 s39, %5, 1\ns_lshl1_add_u32 s18, s18, s39\ns_lshl_b32 %1, %1, 11\ns_mul_i32 s39, s30, 286720\nv_mbcnt_lo_u32_b32 v0, -1, 0\nv_mbcnt_hi_u32_b32 v0, -1, v0\nv_lshlrev_b32 v60, 4, v0\n"
"s_add_u32 m0, %4, 160768\ns_getpc_b64 s[56:57]\n.Lpc0:\ns_and_b32 s56, s56, 0xffffff80\ns_and_b32 s57, s57, 0xffff\ns_mov_b32 s58, .Lend-.Lpc0\ns_mov_b32 s59, 0x20000\ns_and_b32 s94, %0, 31\n"
"s_lshl_b32 s94, s94, 2\ns_add_u32 s94, s94, %5\ns_lshl_b32 s94, s94, 10\nbuffer_load_dwordx4 v60, s[56:59], s94 offen lds\ns_mov_b32 s54, 5242880\ns_mov_b32 s55, 0x20000\ns_mov_b32 s50, 286720\ns_mov_b32 s51, 0x20000\n"
"s_mov_b32 s70, %5\ns_and_b32 s92, s70, 1\ns_lshr_b32 s93, s70, 1\nv_lshrrev_b32 v2, 4, v0\ns_lshl_b32 s68, s92, 18\nv_and_b32 v4, 3, v0\nv_lshlrev_b32 v4, 4, v4\nv_and_b32 v1, 1, v2\n"
"v_lshl_add_u32 v4, v1, 6, v4\nv_lshrrev_b32 v1, 5, v0\nv_lshl_add_u32 v4, v1, 15, v4\nv_bfe_u32 v1, v0, 2, 2\nv_lshl_add_u32 v4, v1, 13, v4\nv_add_u32 v62, s68, v4\ns_lshl_b32 s87, s18, 8\ns_mul_i32 s62, s70, 4128\n"
"s_add_u32 s62, s62, %4\ns_add_u32 s62, s62, 33024\ns_lshl_b32 s69, s92, 1\nv_lshrrev_b32 v3, 5, v0\nv_and_b32 v4, 3, v0\nv_xor_b32 v4, s69, v4\nv_lshlrev_b32 v4, 4, v4\nv_lshl_add_u32 v4, v3, 6, v4\n"
"v_bfe_u32 v1, v0, 2, 3\nv_mul_u32_u24 v64, 1280, v1\nv_add_u32 v64, v64, v4\ns_mul_i32 s86, s93, 143360\ns_mul_i32 s68, s92, 10240\ns_add_u32 s86, s86, s68\ns_mul_i32 s63, s92, 14336\ns_mul_i32 s68, s93, 7168\n"
"s_add_u32 s63, s63, s68\ns_add_u32 s63, s63, %4\ns_add_u32 s63, s63, 99072\ns_lshl_b32 s34, s33, 1\ns_add_u32 s34, s34, s30\ns_mul_hi_u32 s35, s34, 6291456\ns_mul_i32 s34, s34, 6291456\ns_lshl_b32 s36, s30, 5\n"
"s_and_b32 s37, s18, 30\ns_add_u32 s36, s36, s37\ns_lshl_b32 s36, s36, 18\ns_sub_u32 s36, s36, 1024\ns_subb_u32 s37, 0, 0\ns_mul_i32 s38, s30, 573440\ns_mul_i32 s33, s33, 160\ns_add_u32 s38, s38, s33\n"
"s_and_b32 s33, s19, 3\ns_mul_i32 s33, s33, 320\ns_add_u32 s38, s38, s33\ns_mov_b32 s26, 6291456\ns_mov_b32 s27, 0x20000\nv_and_b32 v5, 7, v0\nv_lshlrev_b32 v5, 7, v5\nv_add_u32 v76, 5242880, v5\n"
"s_lshl_b32 s68, s70, 2\ns_add_u32 s68, s68, 4\nv_bfe_u32 v5, v0, 3, 1\nv_mad_u32_u24 v76, v5, s68, v76\ns_mov_b32 s42, 2622464\ns_mov_b32 s43, 0x20000\ns_mov_b32 s46, 2622464\ns_mov_b32 s47, 0x20000\n"
"s_mov_b32 s58, 0\ns_mov_b32 s59, 0x20000\ns_mov_b32 s65, 0\ns_mov_b32 s60, %1\ns_add_u32 s90, s60, 260096\ns_mov_b32 s67, 0\ns_mov_b32 s72, 0x3e0293ee\ns_lshl_b32 s68, s70, 8\n"
"s_lshl_b32 s69, s93, 5\ns_or_b32 s88, s68, s69\nv_bfe_u32 v1, v0, 2, 2\nv_mul_u32_u24 v4, 0x8100, v1\ns_waitcnt lgkmcnt(0)\ns_mov_b32 s52, s10\ns_and_b32 s53, s11, 0xffff\ns_add_u32 m0, s62, 0\n"
"s_add_u32 s66, s87, 0\nbuffer_load_dwordx4 v62, s[52:55], s66 offen lds\ns_add_u32 s66, s87, 64504\nbuffer_load_dwordx4 v62, s[52:55], s66 offen offset:1032 lds\ns_add_u32 s66, s87, 129008\nbuffer_load_dwordx4 v62, s[52:55], s66 offen offset:2064 lds\ns_add_u32 s66, s87, 193512\nbuffer_load_dwordx4 v62, s[52:55], s66 offen offset:3096 lds\n"
"s_add_u32 s48, s8, s39\ns_addc_u32 s49, s9, 0\ns_and_b32 s49, s49, 0xffff\ns_add_u32 m0, s63, 33024\ns_add_u32 s97, s86, 0\nbuffer_load_dwordx4 v64, s[48:51], s97 offen lds\ns_add_u32 s97, s86, 19456\nbuffer_load_dwordx4 v64, s[48:51], s97 offen offset:1024 lds\n"
"s_add_u32 s97, s86, 38912\nbuffer_load_dwordx4 v64, s[48:51], s97 offen offset:2048 lds\ns_add_u32 s97, s86, 58368\nbuffer_load_dwordx4 v64, s[48:51], s97 offen offset:3072 lds\ns_add_u32 m0, s63, 37120\ns_add_u32 s97, s86, 81920\nbuffer_load_dwordx4 v64, s[48:51], s97 offen lds\ns_add_u32 s97, s86, 101376\n"
"buffer_load_dwordx4 v64, s[48:51], s97 offen offset:1024 lds\ns_add_u32 s97, s86, 120832\nbuffer_load_dwordx4 v64, s[48:51], s97 offen offset:2048 lds\ns_add_u32 m0, s62, 16512\ns_add_u32 s66, s87, 128\nbuffer_load_dwordx4 v62, s[52:55], s66 offen lds\ns_add_u32 s20, s20, s34\ns_addc_u32 s21, s21, s35\n"
"s_mov_b32 s24, s20\ns_and_b32 s25, s21, 0xffff\nbuffer_load_dword v65, v76, s[24:27], 0 offen sc1\ns_add_u32 s40, s12, s36\ns_addc_u32 s41, s13, s37\ns_and_b32 s41, s41, 0xffff\ns_add_u32 s44, s14, s36\ns_addc_u32 s45, s15, s37\n"
"s_add_u32 s66, s87, 64632\nbuffer_load_dwordx4 v62, s[52:55], s66 offen offset:1032 lds\ns_and_b32 s45, s45, 0xffff\ns_mov_b32 s56, s20\ns_and_b32 s57, s21, 0xffff\ns_add_u32 s16, s16, -4096\ns_addc_u32 s17, s17, -1\ns_add_u32 s22, s22, s38\n"
"s_addc_u32 s23, s23, 0\nv_and_b32 v1, 3, v0\nv_lshl_or_b32 v4, v1, 4, v4\nv_and_b32 v1, 1, v2\nv_lshl_or_b32 v4, v1, 7, v4\ns_add_u32 s66, s87, 129136\nbuffer_load_dwordx4 v62, s[52:55], s66 offen offset:2064 lds\nv_lshl_or_b32 v4, v3, 6, v4\n"
"v_xor_b32 v4, s88, v4\nv_add_u32 v69, 1024, v4\nv_bfe_u32 v1, v0, 1, 2\nv_mul_u32_u24 v4, 0x8100, v1\nv_and_b32 v1, 9, v0\nv_lshl_or_b32 v4, v1, 4, v4\nv_lshl_or_b32 v4, v2, 5, v4\nv_xor_b32 v4, s68, v4\n"
"s_add_u32 s66, s87, 193640\nbuffer_load_dwordx4 v62, s[52:55], s66 offen offset:3096 lds\nv_add_u32 v72, 1024, v4\ns_lshl_b32 s68, s70, 12\ns_add_u32 s61, s68, %4\ns_mul_i32 s68, s92, 8320\ns_lshl_b32 s69, s93, 12\ns_add_u32 s68, s68, s69\n"
"s_add_u32 s68, s68, %4\ns_add_u32 s64, s68, 16384\ns_lshl_b32 s69, s93, 11\ns_mov_b64 exec, 0\ns_cmp_eq_u32 s70, 0\ns_cbranch_scc1 .Lpg0\ns_cmp_eq_u32 s70, 1\ns_cbranch_scc1 .Lpg1\n"
"s_cmp_eq_u32 s70, 2\ns_cbranch_scc1 .Lpg2\ns_branch .Lpg3\n.Ltr0:\ns_mov_b64 exec, -1\ns_add_u32 s69, s69, %4\nv_bfe_u32 v3, v0, 3, 1\nv_lshlrev_b32 v4, 1, v3\n"
"v_xor_b32 v4, v4, v2\nv_and_b32 v1, 7, v0\nv_lshl_add_u32 v4, v1, 2, v4\nv_lshlrev_b32 v4, 4, v4\nv_bfe_u32 v1, v0, 2, 2\nv_and_b32 v5, 255, v4\ns_add_u32 m0, s63, 0\ns_add_u32 s97, s86, 128\n"
"buffer_load_dwordx4 v64, s[48:51], s97 offen lds\nv_lshl_or_b32 v1, v1, 12, v5\nv_add_u32 v68, s69, v1\ns_mul_i32 s68, s92, 7168\ns_add_u32 s68, s68, %4\ns_add_u32 s68, s68, 99072\nv_mul_u32_u24 v1, 14336, v3\nv_add3_u32 v63, v1, v4, s68\n"
"v_lshrrev_b32 v3, 5, v0\nv_and_b32 v4, 31, v0\ns_add_u32 s97, s86, 19584\nbuffer_load_dwordx4 v64, s[48:51], s97 offen offset:1024 lds\nv_lshlrev_b32 v4, 3, v4\nv_and_b32 v1, 1, v2\nv_lshl_add_u32 v4, v1, 13, v4\nv_lshl_add_u32 v4, v3, 12, v4\n"
"v_add_u32 v4, 16384, v4\nv_add_u32 v70, s69, v4\nv_add_u32 v71, 66048, v70\ns_mul_i32 s68, s93, 8256\ns_add_u32 s68, s68, %4\ns_add_u32 s97, s86, 39040\nbuffer_load_dwordx4 v64, s[48:51], s97 offen offset:2048 lds\ns_add_u32 s68, s68, 33024\n"
"v_mul_u32_u24 v3, 1032, v2\nv_and_b32 v4, 15, v0\nv_lshlrev_b32 v4, 4, v4\nv_add3_u32 v61, v3, v4, s68\ns_lshl_b32 s68, s70, 2\ns_add_u32 s69, %4, 131968\ns_add_u32 s68, s68, s69\nv_mov_b32 v73, s68\n"
"s_add_u32 s97, s86, 58496\nbuffer_load_dwordx4 v64, s[48:51], s97 offen offset:3072 lds\nv_and_b32 v3, 3, v0\nv_lshl_add_u32 v74, v3, 2, s69\nv_mov_b32 v75, 0\ns_mov_b32 s79, -2031\nds_write_b32 v73, v75\ns_waitcnt lgkmcnt(0)\n"
"s_mul_i32 s91, s70, 7168\ns_add_u32 s91, s91, %4\ns_add_u32 s91, s91, 98304\ns_add_u32 m0, s63, 4096\ns_add_u32 s97, s86, 82048\nbuffer_load_dwordx4 v64, s[48:51], s97 offen lds\ns_lshl_b32 s96, s70, 11\ns_add_u32 s96, s96, %4\n"
"v_and_b32 v3, 1, v0\nv_mul_u32_u24 v3, 0xaaaa, v3\nv_or_b32 v77, 0x4444, v3\ns_and_b32 s68, s19, 4\ns_lshl_b32 s28, 15, s68\ns_mov_b32 s29, 0\ns_and_b32 s68, s19, 4\ns_add_u32 s97, s86, 101504\n"
"buffer_load_dwordx4 v64, s[48:51], s97 offen offset:1024 lds\ns_add_u32 s68, s68, s19\ns_lshl_b32 s71, s68, 18\ns_mul_i32 s69, s70, 0x80100\ns_and_b32 s69, s69, 0x100100\ns_add_u32 s68, s71, s69\ns_lshr_b32 s98, %0, 1\ns_xor_b32 s98, s98, %0\n"
"s_lshl_b32 s98, s98, 11\ns_and_b32 s98, s98, 8192\ns_add_u32 s97, s86, 120960\nbuffer_load_dwordx4 v64, s[48:51], s97 offen offset:2048 lds\ns_add_u32 s68, s68, s98\ns_add_u32 s68, s68, 7168\ns_add_u32 s30, s20, s68\ns_addc_u32 s31, s21, 0\n"
"s_mov_b32 s77, 0\nv_bfe_i32 v8, v0, 3, 1\nv_lshlrev_b32 v4, 10, v0\nv_and_b32 v4, 0x6000, v4\nv_lshrrev_b32 v3, 5, v0\nv_lshl_or_b32 v4, v3, 16, v4\nv_and_b32 v3, 0x70, v60\nv_or_b32 v4, v3, v4\n"
"v_or_b32 v8, 1, v8\nv_mov_b32 v9, v8\nv_mov_b32 v10, v8\nv_mov_b32 v11, v8\nbuffer_store_dwordx4 v[8:11], v4, s[24:27], s68 offen offset:0 sc1\ns_nop 1\ns_lshl_b32 s68, s70, 8\nv_lshl_or_b32 v3, v2, 15, v60\n"
"v_xor_b32 v79, s68, v3\ns_getpc_b64 s[100:101]\n.Lpc1:\ns_min_u32 s94, s94, .Lend-.Lpc1-1024\ns_add_u32 s100, s100, s94\ns_addc_u32 s101, s101, 0\ns_mov_b32 s94, 0x7ffff000\ns_mul_i32 s68, %0, 40960\n"
"s_mul_i32 s69, s93, 20480\ns_add_u32 s68, s68, s69\ns_lshl_b32 s69, s92, 10\ns_add_u32 s68, s68, s69\ns_add_u32 s68, s68, 2048\ns_add_u32 s94, s10, s68\ns_addc_u32 s95, s11, 0\ns_and_b32 s95, s95, 0xffff\n"
"s_mov_b32 s75, 17408\n.Lround:\ns_mov_b64 vcc, 0\ns_waitcnt vmcnt(13)\ns_barrier\nQta 112,61,0\nQta 114,61,512\nQda 208,63,33024\n"
"Qda 212,63,34048\nQda 216,63,35072\nQda 220,63,36096\nQda 224,63,37120\nQda 228,63,38144\nQda 232,63,39168\ns_mov_b64 exec, 0\ns_cmp_eq_u32 s70, 0\n"
"s_cbranch_scc1 .Lpg4\ns_cmp_eq_u32 s70, 1\ns_cbranch_scc1 .Lpg5\ns_cmp_eq_u32 s70, 2\ns_cbranch_scc1 .Lpg6\ns_branch .Lpg7\n.Ltr9:\ns_mov_b64 exec, -1\n"
"Qda 176,63,33536\nQta 116,61,4128\nQta 118,61,4640\ns_add_u32 m0, s62, 33024\ns_add_u32 s66, s87, 524288\nbuffer_load_dwordx4 v62, s[52:55], s66 offen lds\nQta 120,61,8\nQta 122,61,520\n"
"Qta 124,61,4136\ns_add_u32 s66, s87, 588792\nbuffer_load_dwordx4 v62, s[52:55], s66 offen offset:1032 lds\nQta 126,61,4648\nQta 128,61,256\nQta 130,61,768\ns_add_u32 s66, s87, 653296\nbuffer_load_dwordx4 v62, s[52:55], s66 offen offset:2064 lds\n"
"Qta 132,61,4384\nQta 134,61,4896\nQta 136,61,264\ns_add_u32 s66, s87, 717800\nbuffer_load_dwordx4 v62, s[52:55], s66 offen offset:3096 lds\nQta 138,61,776\nQta 140,61,4392\nQta 142,61,4904\n"
"s_add_u32 m0, s62, 49536\ns_add_u32 s66, s87, 524416\nbuffer_load_dwordx4 v62, s[52:55], s66 offen lds\ns_waitcnt lgkmcnt(15)\nQzvaa 0,112,208\nQzvaa 4,112,212\ns_add_u32 s66, s87, 588920\nbuffer_load_dwordx4 v62, s[52:55], s66 offen offset:1032 lds\n"
"Qda 180,63,34560\nQzvaa 8,112,216\nQda 184,63,35584\ns_add_u32 s66, s87, 653424\nbuffer_load_dwordx4 v62, s[52:55], s66 offen offset:2064 lds\nQzvaa 12,112,220\nQda 188,63,36608\nQzvaa 16,112,224\n"
"s_add_u32 s66, s87, 717928\nbuffer_load_dwordx4 v62, s[52:55], s66 offen offset:3096 lds\nQda 192,63,37632\nQzvaa 20,112,228\nQda 196,63,38656\ns_mov_b64 exec, 0\ns_cmp_eq_u32 s70, 0\ns_cbranch_scc1 .Lpg8\n"
"s_cmp_eq_u32 s70, 1\ns_cbranch_scc1 .Lpg9\ns_cmp_eq_u32 s70, 2\ns_cbranch_scc1 .Ltr10\ns_branch .Ltr10\n.Ltr10:\ns_mov_b64 exec, -1\nQzvaa 52,112,232\n"
"Qda 200,63,39680\nv_max3_f32 v0, |v0|, |v1|, |v2|\nv_max3_f32 v4, |v4|, |v5|, |v6|\nv_max3_f32 v8, |v8|, |v9|, |v10|\nv_max3_f32 v12, |v12|, |v13|, |v14|\nv_max3_f32 v16, |v16|, |v17|, |v18|\nv_max3_f32 v20, |v20|, |v21|, |v22|\ns_nop 0\n"
"v_max3_f32 v52, |v52|, |v53|, |v54|\nv_min3_u32 v56, v0, v4, v8\nv_min3_u32 v56, v56, v12, v16\nv_min3_u32 v56, v56, v20, v52\nv_mov_b32 v57, v56\ns_nop 1\nv_permlane32_swap_b32 v56, v57\nv_max_f32 v56, v56, v57\n"
"v_mov_b32 v57, v56\ns_nop 1\nv_permlane16_swap_b32 v56, v57\nv_max_f32 v56, v56, v57\ns_nop 1\nv_min_f32_dpp v56, v56, v56 row_ror:8 row_mask:0xf bank_mask:0xf\ns_nop 1\nv_min_f32_dpp v56, v56, v56 row_ror:4 row_mask:0xf bank_mask:0xf\n"
"s_nop 1\nv_min_f32_dpp v56, v56, v56 row_ror:2 row_mask:0xf bank_mask:0xf\ns_nop 1\nv_min_f32_dpp v56, v56, v56 row_ror:1 row_mask:0xf bank_mask:0xf\nv_and_b32 v56, 0x7f800000, v56\nv_min_u32 v56, 0x78000000, v56\nv_add_u32 v56, 0x5400000, v56\nv_mov_b32 v57, v56\n"
"v_mov_b64 v[58:59], v[56:57]\nv_mul_f32 v52, -v56, s72\n.Lround2:\ns_waitcnt lgkmcnt(6)\nQcvaa 80,112,208,56\nQk 96,96,97\nQk 97,98,99\nQmvaa 80,116,176\n"
"s_add_u32 s68, s71, 0\nQk 98,100,101\nQcvaa 84,120,208,56\ns_cbranch_vccz .Lst27\nbuffer_store_dwordx4 a[0:3], v79, s[56:59], s68 offen offset:0 sc1\n.Lst27:\nQmvaa 84,124,176\nQk 99,102,103\n"
"v_accvgpr_write_b32 a8, v96\nQcvaa 88,128,208,56\ns_add_u32 s68, s71, 16384\nv_accvgpr_write_b32 a9, v97\nQmvaa 88,132,176\ns_cbranch_vccz .Lst26\nbuffer_store_dwordx4 a[4:7], v79, s[56:59], s68 offen offset:0 sc1\n.Lst26:\n"
"Qcvaa 92,136,208,56\nv_accvgpr_write_b32 a10, v98\nv_accvgpr_write_b32 a11, v99\nQmvaa 92,140,176\ns_waitcnt lgkmcnt(0)\nQk 104,104,105\nQcvaa 124,136,212,56\ns_waitcnt vmcnt(16)\n"
"s_barrier\nQmvaa 124,140,180\nQta 144,61,16512\ns_add_u32 m0, s62, 0\ns_add_u32 s69, s87, 1048576\nQcvaa 120,128,212,56\nbuffer_load_dwordx4 v62, s[52:55], s69 offen lds\nQmvaa 120,132,180\n"
"Qta 146,61,17024\nQk 105,106,107\nQcvaa 116,120,212,56\nQk 106,108,109\nQk 107,110,111\nQmvaa 116,124,180\nQta 148,61,20640\ns_add_u32 s69, s87, 1113080\n"
"Qcvaa 112,112,212,56\nbuffer_load_dwordx4 v62, s[52:55], s69 offen offset:1032 lds\nQmvaa 112,116,180\nQta 150,61,21152\nv_accvgpr_write_b32 a12, v104\nQcvaa 144,112,216,56\nv_accvgpr_write_b32 a13, v105\nv_accvgpr_write_b32 a14, v106\n"
"Qmvaa 144,116,184\nQta 152,61,16520\ns_add_u32 s69, s87, 1177584\nQcvaa 148,120,216,56\nbuffer_load_dwordx4 v62, s[52:55], s69 offen offset:2064 lds\nQmvaa 148,124,184\nQta 154,61,17032\nv_accvgpr_write_b32 a15, v107\n"
"Qcvaa 152,128,216,56\nQk 136,136,137\nQk 137,138,139\nQmvaa 152,132,184\nQta 156,61,20648\ns_add_u32 s69, s87, 1242088\nQcvaa 156,136,216,56\nbuffer_load_dwordx4 v62, s[52:55], s69 offen offset:3096 lds\n"
"Qmvaa 156,140,184\nQta 158,61,21160\nQk 138,140,141\nQcvaa 188,136,220,56\nQk 139,142,143\nv_accvgpr_write_b32 a28, v136\nQmvaa 188,140,188\nQta 160,61,16768\n"
"s_add_u32 m0, s63, 33024\ns_add_u32 s88, s86, 256\nQcvaa 184,128,220,56\nbuffer_load_dwordx4 v64, s[48:51], s88 offen lds\nQmvaa 184,132,188\nQta 162,61,17280\nv_accvgpr_write_b32 a29, v137\nQcvaa 180,120,220,56\n"
"v_accvgpr_write_b32 a30, v138\nv_accvgpr_write_b32 a31, v139\nQmvaa 180,124,188\nQta 164,61,20896\ns_add_u32 s88, s86, 19712\nQcvaa 176,112,220,56\nbuffer_load_dwordx4 v64, s[48:51], s88 offen offset:1024 lds\nQmvaa 176,116,188\n"
"Qta 166,61,21408\nQk 128,128,129\nQcvaa 208,112,224,56\nQk 129,130,131\nQk 130,132,133\nQmvaa 208,116,192\nQta 168,61,16776\ns_add_u32 s88, s86, 39168\n"
"Qcvaa 212,120,224,56\nbuffer_load_dwordx4 v64, s[48:51], s88 offen offset:2048 lds\nQmvaa 212,124,192\nQta 170,61,17288\nQk 131,134,135\nQcvaa 216,128,224,56\nv_accvgpr_write_b32 a24, v128\nv_accvgpr_write_b32 a25, v129\n"
"Qmvaa 216,132,192\nQta 172,61,20904\ns_add_u32 s88, s86, 58624\nQcvaa 220,136,224,56\nbuffer_load_dwordx4 v64, s[48:51], s88 offen offset:3072 lds\nQmvaa 220,140,192\nQta 174,61,21416\nv_accvgpr_write_b32 a26, v130\n"
"Qcvaa 252,136,228,56\nv_accvgpr_write_b32 a27, v131\nQk 160,160,161\nQmvaa 252,140,196\ns_add_u32 m0, s63, 37120\ns_add_u32 s88, s86, 82176\nQk 161,162,163\nQcvaa 248,128,228,56\n"
"buffer_load_dwordx4 v64, s[48:51], s88 offen lds\nQmvaa 248,132,196\nQk 162,164,165\nQk 163,166,167\nQcvaa 244,120,228,56\nv_accvgpr_write_b32 a40, v160\nv_accvgpr_write_b32 a41, v161\nQmvaa 244,124,196\n"
"s_add_u32 s88, s86, 101632\nv_accvgpr_write_b32 a42, v162\nQcvaa 240,112,228,56\nbuffer_load_dwordx4 v64, s[48:51], s88 offen offset:1024 lds\nQmvaa 240,116,196\nv_accvgpr_write_b32 a43, v163\nQk 168,168,169\nQcvaa 16,112,232,56\n"
"Qk 169,170,171\nQk 170,172,173\nQmvaa 16,116,200\ns_add_u32 s88, s86, 121088\nQk 171,174,175\nQcvaa 20,120,232,56\nbuffer_load_dwordx4 v64, s[48:51], s88 offen offset:2048 lds\nQmvaa 20,124,200\n"
"v_accvgpr_write_b32 a44, v168\nv_accvgpr_write_b32 a45, v169\nQcvaa 24,128,232,56\nv_accvgpr_write_b32 a46, v170\nv_accvgpr_write_b32 a47, v171\nQmvaa 24,132,200\nQk 200,200,201\nQk 201,202,203\n"
"Qcvaa 28,136,232,56\nQk 202,204,205\nQk 203,206,207\nQmvaa 28,140,200\nv_accvgpr_write_b32 a60, v200\nv_accvgpr_write_b32 a61, v201\ns_waitcnt lgkmcnt(0)\nQcvaa 96,144,208,56\n"
"v_accvgpr_write_b32 a62, v202\nv_accvgpr_write_b32 a63, v203\nQmvaa 96,148,176\ns_add_u32 s68, s71, 0\nQk 192,192,193\nQcvaa 100,152,208,56\ns_cbranch_vccz .Lst25\nbuffer_store_dwordx4 a[16:19], v79, s[56:59], s68 offen offset:1024 sc1\n"
".Lst25:\nQmvaa 100,156,176\nQk 193,194,195\nQk 194,196,197\nQcvaa 104,160,208,56\ns_add_u32 s68, s71, 16384\nQk 195,198,199\nQmvaa 104,164,176\n"
"s_cbranch_vccz .Lst24\nbuffer_store_dwordx4 a[20:23], v79, s[56:59], s68 offen offset:1024 sc1\n.Lst24:\nQcvaa 108,168,208,56\nv_accvgpr_write_b32 a56, v192\nv_accvgpr_write_b32 a57, v193\nQmvaa 108,172,176\ns_waitcnt lgkmcnt(0)\n"
"v_accvgpr_write_b32 a58, v194\nQcvaa 140,168,212,56\n.Lpg0:\ns_cbranch_execz .Ltr0\ns_waitcnt vmcnt(15)\ns_barrier\nQmvaa 140,172,180\nQda 208,63,0\n"
"s_add_u32 m0, s62, 16512\ns_add_u32 s69, s87, 1048704\nQcvaa 136,160,212,56\nbuffer_load_dwordx4 v62, s[52:55], s69 offen lds\nQmvaa 136,164,180\nQda 176,63,512\nv_accvgpr_write_b32 a59, v195\nQcvaa 132,152,212,56\n"
"Qta 112,61,33024\nQk 224,224,225\nQmvaa 132,156,180\ns_add_u32 s69, s87, 1113208\nQk 225,226,227\nQcvaa 128,144,212,56\nbuffer_load_dwordx4 v62, s[52:55], s69 offen offset:1032 lds\nQmvaa 128,148,180\n"
"Qda 212,63,1024\nQk 226,228,229\nQcvaa 160,144,216,56\nQda 180,63,1536\nQk 227,230,231\nQmvaa 160,148,184\nQta 114,61,33536\ns_add_u32 s69, s87, 1177712\n"
"Qcvaa 164,152,216,56\nbuffer_load_dwordx4 v62, s[52:55], s69 offen offset:2064 lds\nQmvaa 164,156,184\nQta 116,61,37152\nv_accvgpr_write_b32 a72, v224\nQcvaa 168,160,216,56\nv_accvgpr_write_b32 a73, v225\nv_accvgpr_write_b32 a74, v226\n"
"Qmvaa 168,164,184\nQta 118,61,37664\ns_add_u32 s69, s87, 1242216\nQcvaa 172,168,216,56\nbuffer_load_dwordx4 v62, s[52:55], s69 offen offset:3096 lds\nQmvaa 172,172,184\nQda 216,63,2048\nv_accvgpr_write_b32 a75, v227\n"
"Qcvaa 204,168,220,56\nQda 184,63,2560\nQk 232,232,233\nQmvaa 204,172,188\nQta 120,61,33032\ns_add_u32 m0, s61, 0\nQcvaa 200,160,220,56\ns_cbranch_vccnz .Ld0q0\n"
"buffer_load_dwordx4 v69, s[40:43], s60 offen nt lds\n.Ld0q0:\nQmvaa 200,164,188\nQta 122,61,33544\nQk 233,234,235\nQcvaa 196,152,220,56\nQk 234,236,237\nQk 235,238,239\n"
"Qmvaa 196,156,188\nQta 124,61,37160\nv_accvgpr_write_b32 a76, v232\nQcvaa 192,144,220,56\nQda 220,63,3072\nv_accvgpr_write_b32 a77, v233\nQmvaa 192,148,188\nQda 188,63,3584\n"
"v_accvgpr_write_b32 a78, v234\nQcvaa 224,144,224,56\nQta 126,61,37672\nv_accvgpr_write_b32 a79, v235\nQmvaa 224,148,192\nQk 32,32,33\nQk 33,34,35\nQcvaa 228,152,224,56\n"
"Qta 128,61,33280\nQk 34,36,37\nQmvaa 228,156,192\nQk 35,38,39\nv_accvgpr_write_b32 a104, v32\nQcvaa 232,160,224,56\nQta 130,61,33792\nv_accvgpr_write_b32 a105, v33\n"
"Qmvaa 232,164,192\nv_accvgpr_write_b32 a106, v34\nv_accvgpr_write_b32 a107, v35\nQcvaa 236,168,224,56\nQda 224,63,4096\nQk 40,40,41\nQmvaa 236,172,192\nQda 192,63,4608\n"
"Qk 41,42,43\nQcvaa 12,168,228,56\nQta 132,61,37408\nQk 42,44,45\nQmvaa 12,172,196\nQk 43,46,47\nv_accvgpr_write_b32 a108, v40\nQcvaa 8,160,228,56\n"
"Qta 134,61,37920\nv_accvgpr_write_b32 a109, v41\nQmvaa 8,164,196\nv_accvgpr_write_b32 a110, v42\nv_accvgpr_write_b32 a111, v43\nQcvaa 4,152,228,56\nQta 136,61,33288\nQmvaa 4,156,196\n"
"Qcvaa 0,144,228,56\nQda 228,63,5120\nQmvaa 0,148,196\nQda 196,63,5632\nQcvaa 32,144,232,56\nQta 138,61,33800\nQmvaa 32,148,200\nQcvaa 36,152,232,56\n"
"Qta 140,61,37416\nQmvaa 36,156,200\nQcvaa 40,160,232,56\nQta 142,61,37928\nQmvaa 40,164,200\nQcvaa 44,168,232,56\nQda 232,63,6144\nQmvaa 44,172,200\n"
"Qda 200,63,6656\ns_waitcnt lgkmcnt(2)\nQmvaa 80,112,208\nQmvaa 80,116,176\ns_add_u32 s68, s71, 0\nQmvaa 84,120,208\ns_cbranch_vccz .Lst23\nbuffer_store_dwordx4 a[32:35], v79, s[56:59], s68 offen offset:2048 sc1\n"
".Lst23:\nQmvaa 84,124,176\nQmvaa 88,128,208\ns_add_u32 s68, s71, 16384\nQmvaa 88,132,176\ns_cbranch_vccz .Lst22\nbuffer_store_dwordx4 a[36:39], v79, s[56:59], s68 offen offset:2048 sc1\n.Lst22:\n"
"Qmvaa 92,136,208\nQmvaa 92,140,176\ns_waitcnt lgkmcnt(0)\nQmvaa 124,136,212\ns_waitcnt vmcnt(16)\ns_barrier\nQmvaa 124,140,180\nQta 144,61,49536\n"
"s_add_u32 m0, s62, 33024\ns_add_u32 s69, s87, 1572864\nQmvaa 120,128,212\nbuffer_load_dwordx4 v62, s[52:55], s69 offen lds\nQmvaa 120,132,180\nQta 146,61,50048\nQmvaa 116,120,212\ns_add_u32 s69, s87, 1637368\n"
"Qmvaa 116,124,180\nbuffer_load_dwordx4 v62, s[52:55], s69 offen offset:1032 lds\nQmvaa 112,112,212\nQta 148,61,53664\nQmvaa 112,116,180\ns_add_u32 s69, s87, 1701872\nQmvaa 144,112,216\nbuffer_load_dwordx4 v62, s[52:55], s69 offen offset:2064 lds\n"
"Qmvaa 144,116,184\nQta 150,61,54176\nQmvaa 148,120,216\ns_add_u32 s69, s87, 1766376\nQmvaa 148,124,184\nbuffer_load_dwordx4 v62, s[52:55], s69 offen offset:3096 lds\nQmvaa 152,128,216\nQta 152,61,49544\n"
"Qmvaa 152,132,184\ns_add_u32 m0, s63, 0\ns_add_u32 s88, s86, 384\nQmvaa 156,136,216\nbuffer_load_dwordx4 v64, s[48:51], s88 offen lds\nQmvaa 156,140,184\nQta 154,61,50056\nQmvaa 188,136,220\n"
"s_add_u32 s88, s86, 19840\nQmvaa 188,140,188\nbuffer_load_dwordx4 v64, s[48:51], s88 offen offset:1024 lds\nQmvaa 184,128,220\nQta 156,61,53672\nQmvaa 184,132,188\ns_add_u32 s88, s86, 39296\nQmvaa 180,120,220\n"
"buffer_load_dwordx4 v64, s[48:51], s88 offen offset:2048 lds\nQmvaa 180,124,188\nQta 158,61,54184\nQmvaa 176,112,220\ns_add_u32 s88, s86, 58752\nQmvaa 176,116,188\nbuffer_load_dwordx4 v64, s[48:51], s88 offen offset:3072 lds\nQmvaa 208,112,224\n"
"Qta 160,61,49792\nQmvaa 208,116,192\ns_add_u32 m0, s63, 4096\ns_add_u32 s88, s86, 82304\nQmvaa 212,120,224\nbuffer_load_dwordx4 v64, s[48:51], s88 offen lds\nQmvaa 212,124,192\nQta 162,61,50304\n"
"Qmvaa 216,128,224\ns_add_u32 s88, s86, 101760\nQmvaa 216,132,192\nbuffer_load_dwordx4 v64, s[48:51], s88 offen offset:1024 lds\nQmvaa 220,136,224\nQta 164,61,53920\nQmvaa 220,140,192\ns_add_u32 s88, s86, 121216\n"
"Qmvaa 252,136,228\nbuffer_load_dwordx4 v64, s[48:51], s88 offen offset:2048 lds\nQmvaa 252,140,196\nQta 166,61,54432\nQmvaa 248,128,228\ns_add_u32 m0, s61, 0\nQmvaa 248,132,196\ns_cbranch_vccnz .Ld0q1\n"
"buffer_load_dwordx4 v69, s[40:43], s60 offen offset:1024 nt lds\n.Ld0q1:\nQmvaa 244,120,228\nQta 168,61,49800\nQmvaa 244,124,196\nQmvaa 240,112,228\nQta 170,61,50312\nQmvaa 240,116,196\n"
"Qmvaa 16,112,232\nQta 172,61,53928\nQmvaa 16,116,200\nQmvaa 20,120,232\nQta 174,61,54440\nQmvaa 20,124,200\nQmvaa 24,128,232\nQmvaa 24,132,200\n"
"Qmvaa 28,136,232\nQmvaa 28,140,200\ns_waitcnt lgkmcnt(0)\nQmvaa 96,144,208\nQmvaa 96,148,176\ns_add_u32 s68, s71, 0\nQmvaa 100,152,208\ns_cbranch_vccz .Lst21\n"
"buffer_store_dwordx4 a[48:51], v79, s[56:59], s68 offen offset:3072 sc1\n.Lst21:\nQmvaa 100,156,176\nQmvaa 104,160,208\ns_add_u32 s68, s71, 16384\nQmvaa 104,164,176\ns_cbranch_vccz .Lst20\nbuffer_store_dwordx4 a[52:55], v79, s[56:59], s68 offen offset:3072 sc1\n"
".Lst20:\nQmvaa 108,168,208\nQmvaa 108,172,176\ns_waitcnt lgkmcnt(0)\nQmvaa 140,168,212\ns_waitcnt vmcnt(17)\ns_barrier\nQmvaa 140,172,180\n"
"Qda 208,63,33024\ns_add_u32 m0, s62, 49536\ns_add_u32 s69, s87, 1572992\nQmvaa 136,160,212\nbuffer_load_dwordx4 v62, s[52:55], s69 offen lds\nQmvaa 136,164,180\nQda 176,63,33536\nQmvaa 132,152,212\n"
"Qta 112,61,0\nQmvaa 132,156,180\ns_add_u32 s69, s87, 1637496\nQmvaa 128,144,212\nbuffer_load_dwordx4 v62, s[52:55], s69 offen offset:1032 lds\nQmvaa 128,148,180\nQda 212,63,34048\nQmvaa 160,144,216\n"
"Qda 180,63,34560\nQmvaa 160,148,184\nQta 114,61,512\ns_add_u32 s69, s87, 1702000\nQmvaa 164,152,216\nbuffer_load_dwordx4 v62, s[52:55], s69 offen offset:2064 lds\nQmvaa 164,156,184\nQta 116,61,4128\n"
"Qmvaa 168,160,216\nQmvaa 168,164,184\nQta 118,61,4640\ns_add_u32 s69, s87, 1766504\nQmvaa 172,168,216\nbuffer_load_dwordx4 v62, s[52:55], s69 offen offset:3096 lds\nQmvaa 172,172,184\nQda 216,63,35072\n"
"Qmvaa 204,168,220\nQda 184,63,35584\nQmvaa 204,172,188\nQta 120,61,8\nQmvaa 200,160,220\nQmvaa 200,164,188\nQta 122,61,520\nQmvaa 196,152,220\n"
"Qmvaa 196,156,188\nQta 124,61,4136\nQmvaa 192,144,220\nQda 220,63,36096\nQmvaa 192,148,188\nQda 188,63,36608\nQmvaa 224,144,224\nQta 126,61,4648\n"
"Qmvaa 224,148,192\nQmvaa 228,152,224\nQta 128,61,256\nQmvaa 228,156,192\nQmvaa 232,160,224\nQta 130,61,768\nQmvaa 232,164,192\nQmvaa 236,168,224\n"
"Qda 224,63,37120\nQmvaa 236,172,192\nQda 192,63,37632\nQmvaa 12,168,228\nQta 132,61,4384\nQmvaa 12,172,196\nQmvaa 8,160,228\nQta 134,61,4896\n"
"Qmvaa 8,164,196\nQmvaa 4,152,228\nQta 136,61,264\nQmvaa 4,156,196\nQmvaa 0,144,228\nQda 228,63,38144\nQmvaa 0,148,196\nQda 196,63,38656\n"
"Qmvaa 32,144,232\nQta 138,61,776\nQmvaa 32,148,200\nQmvaa 36,152,232\nQta 140,61,4392\nQmvaa 36,156,200\nQmvaa 40,160,232\nQta 142,61,4904\n"
"Qmvaa 40,164,200\nQmvaa 44,168,232\nQda 232,63,39168\nQmvaa 44,172,200\nQda 200,63,39680\ns_waitcnt lgkmcnt(2)\nQmvaa 80,112,208\nQmvaa 80,116,176\n"
"s_add_u32 s68, s71, 4096\nQmvaa 84,120,208\ns_cbranch_vccz .Lst19\nbuffer_store_dwordx4 a[64:67], v79, s[56:59], s68 offen offset:0 sc1\n.Lst19:\nQmvaa 84,124,176\nQmvaa 88,128,208\ns_add_u32 s68, s71, 20480\n"
"Qmvaa 88,132,176\ns_cbranch_vccz .Lst18\nbuffer_store_dwordx4 a[68:71], v79, s[56:59], s68 offen offset:0 sc1\n.Lst18:\nQmvaa 92,136,208\nQmvaa 92,140,176\ns_waitcnt lgkmcnt(0)\nQmvaa 124,136,212\n"
"s_waitcnt vmcnt(17)\ns_barrier\nQmvaa 124,140,180\nQta 144,61,16512\ns_add_u32 m0, s62, 0\ns_add_u32 s69, s87, 2097152\nQmvaa 120,128,212\nbuffer_load_dwordx4 v62, s[52:55], s69 offen lds\n"
"Qmvaa 120,132,180\nQta 146,61,17024\nQmvaa 116,120,212\ns_add_u32 s69, s87, 2161656\nQmvaa 116,124,180\nbuffer_load_dwordx4 v62, s[52:55], s69 offen offset:1032 lds\nQmvaa 112,112,212\nQta 148,61,20640\n"
"Qmvaa 112,116,180\ns_add_u32 s69, s87, 2226160\nQmvaa 144,112,216\nbuffer_load_dwordx4 v62, s[52:55], s69 offen offset:2064 lds\nQmvaa 144,116,184\nQta 150,61,21152\nQmvaa 148,120,216\ns_add_u32 s69, s87, 2290664\n"
"Qmvaa 148,124,184\nbuffer_load_dwordx4 v62, s[52:55], s69 offen offset:3096 lds\nQmvaa 152,128,216\nQta 152,61,16520\nQmvaa 152,132,184\ns_add_u32 m0, s63, 33024\ns_add_u32 s88, s86, 512\nQmvaa 156,136,216\n"
"buffer_load_dwordx4 v64, s[48:51], s88 offen lds\nQmvaa 156,140,184\nQta 154,61,17032\nQmvaa 188,136,220\ns_add_u32 s88, s86, 19968\nQmvaa 188,140,188\nbuffer_load_dwordx4 v64, s[48:51], s88 offen offset:1024 lds\nQmvaa 184,128,220\n"
"Qta 156,61,20648\nQmvaa 184,132,188\ns_add_u32 s88, s86, 39424\nQmvaa 180,120,220\nbuffer_load_dwordx4 v64, s[48:51], s88 offen offset:2048 lds\nQmvaa 180,124,188\nQta 158,61,21160\nQmvaa 176,112,220\n"
"s_add_u32 s88, s86, 58880\nQmvaa 176,116,188\nbuffer_load_dwordx4 v64, s[48:51], s88 offen offset:3072 lds\nQmvaa 208,112,224\nQta 160,61,16768\nQmvaa 208,116,192\ns_add_u32 m0, s63, 37120\ns_add_u32 s88, s86, 82432\n"
"Qmvaa 212,120,224\nbuffer_load_dwordx4 v64, s[48:51], s88 offen lds\nQmvaa 212,124,192\nQta 162,61,17280\nQmvaa 216,128,224\ns_add_u32 s88, s86, 101888\nQmvaa 216,132,192\nbuffer_load_dwordx4 v64, s[48:51], s88 offen offset:1024 lds\n"
"Qmvaa 220,136,224\nQta 164,61,20896\nQmvaa 220,140,192\ns_add_u32 s88, s86, 121344\nQmvaa 252,136,228\nbuffer_load_dwordx4 v64, s[48:51], s88 offen offset:2048 lds\nQmvaa 252,140,196\nQta 166,61,21408\n"
"Qmvaa 248,128,228\ns_add_u32 m0, s61, 0\nQmvaa 248,132,196\ns_cbranch_vccnz .Ld0q2\nbuffer_load_dwordx4 v69, s[40:43], s90 offen offset:2048 nt lds\n.Ld0q2:\nQmvaa 244,120,228\nQta 168,61,16776\n"
"Qmvaa 244,124,196\nQmvaa 240,112,228\nQta 170,61,17288\nQmvaa 240,116,196\nQmvaa 16,112,232\nQta 172,61,20904\nQmvaa 16,116,200\nQmvaa 20,120,232\n"
"Qta 174,61,21416\nQmvaa 20,124,200\nQmvaa 24,128,232\nQmvaa 24,132,200\nQmvaa 28,136,232\nQmvaa 28,140,200\ns_waitcnt lgkmcnt(0)\nQmvaa 96,144,208\n"
"Qmvaa 96,148,176\ns_add_u32 s68, s71, 4096\nQmvaa 100,152,208\ns_cbranch_vccz .Lst17\nbuffer_store_dwordx4 a[80:83], v79, s[56:59], s68 offen offset:1024 sc1\n.Lst17:\nQmvaa 100,156,176\nQmvaa 104,160,208\n"
"s_add_u32 s68, s71, 20480\nQmvaa 104,164,176\ns_cbranch_vccz .Lst16\nbuffer_store_dwordx4 a[84:87], v79, s[56:59], s68 offen offset:1024 sc1\n.Lst16:\nQmvaa 108,168,208\nQmvaa 108,172,176\ns_waitcnt lgkmcnt(0)\n"
"Qmvaa 140,168,212\ns_waitcnt vmcnt(17)\ns_barrier\nQmvaa 140,172,180\nQda 208,63,0\ns_add_u32 m0, s62, 16512\ns_add_u32 s69, s87, 2097280\nQmvaa 136,160,212\n"
"buffer_load_dwordx4 v62, s[52:55], s69 offen lds\nQmvaa 136,164,180\nQda 176,63,512\nQmvaa 132,152,212\nQta 112,61,33024\nQmvaa 132,156,180\ns_add_u32 s69, s87, 2161784\nQmvaa 128,144,212\n"
"buffer_load_dwordx4 v62, s[52:55], s69 offen offset:1032 lds\nQmvaa 128,148,180\nQda 212,63,1024\nQmvaa 160,144,216\nQda 180,63,1536\nQmvaa 160,148,184\nQta 114,61,33536\ns_add_u32 s69, s87, 2226288\n"
"Qmvaa 164,152,216\nbuffer_load_dwordx4 v62, s[52:55], s69 offen offset:2064 lds\nQmvaa 164,156,184\nQta 116,61,37152\nQmvaa 168,160,216\nQmvaa 168,164,184\nQta 118,61,37664\ns_add_u32 s69, s87, 2290792\n"
"Qmvaa 172,168,216\nbuffer_load_dwordx4 v62, s[52:55], s69 offen offset:3096 lds\nQmvaa 172,172,184\nQda 216,63,2048\nQmvaa 204,168,220\nQda 184,63,2560\nQmvaa 204,172,188\nQta 120,61,33032\n"
"s_add_u32 m0, s61, 0\nQmvaa 200,160,220\ns_cbranch_vccnz .Ld0q3\nbuffer_load_dwordx4 v69, s[40:43], s90 offen offset:3072 nt lds\n.Ld0q3:\nQmvaa 200,164,188\nQta 122,61,33544\nQmvaa 196,152,220\n"
"Qmvaa 196,156,188\nQta 124,61,37160\nQmvaa 192,144,220\nQda 220,63,3072\nQmvaa 192,148,188\nQda 188,63,3584\nQmvaa 224,144,224\nQta 126,61,37672\n"
"Qmvaa 224,148,192\nQmvaa 228,152,224\nQta 128,61,33280\nQmvaa 228,156,192\nQmvaa 232,160,224\nQta 130,61,33792\nQmvaa 232,164,192\nQmvaa 236,168,224\n"
"Qda 224,63,4096\nQmvaa 236,172,192\nQda 192,63,4608\n.Lpg1:\ns_cbranch_execz .Ltr0\nQmvaa 12,168,228\nQta 132,61,37408\nQmvaa 12,172,196\n"
"Qmvaa 8,160,228\nQta 134,61,37920\nQmvaa 8,164,196\nQmvaa 4,152,228\nQta 136,61,33288\nQmvaa 4,156,196\nQmvaa 0,144,228\nQda 228,63,5120\n"
"Qmvaa 0,148,196\nQda 196,63,5632\nQmvaa 32,144,232\nQta 138,61,33800\nQmvaa 32,148,200\nQmvaa 36,152,232\nQta 140,61,37416\nQmvaa 36,156,200\n"
"Qmvaa 40,160,232\nQta 142,61,37928\nQmvaa 40,164,200\nQmvaa 44,168,232\nQda 232,63,6144\nQmvaa 44,172,200\nQda 200,63,6656\ns_waitcnt lgkmcnt(2)\n"
"Qmvaa 80,112,208\nQmvaa 80,116,176\ns_add_u32 s68, s71, 135168\nQmvaa 84,120,208\ns_cbranch_vccz .Lst15\nbuffer_store_dwordx4 a[88:91], v79, s[56:59], s68 offen offset:1024 sc1\n.Lst15:\nQmvaa 84,124,176\n"
"Qmvaa 88,128,208\ns_add_u32 s68, s71, 151552\nQmvaa 88,132,176\ns_cbranch_vccz .Lst14\nbuffer_store_dwordx4 a[92:95], v79, s[56:59], s68 offen offset:1024 sc1\n.Lst14:\nQmvaa 92,136,208\nQmvaa 92,140,176\n"
"s_waitcnt lgkmcnt(0)\nQmvaa 124,136,212\ns_waitcnt vmcnt(17)\ns_barrier\nQmvaa 124,140,180\nQta 144,61,49536\ns_add_u32 m0, s62, 33024\ns_add_u32 s69, s87, 2621440\n"
"Qmvaa 120,128,212\nbuffer_load_dwordx4 v62, s[52:55], s69 offen lds\nQmvaa 120,132,180\nQta 146,61,50048\ns_mov_b32 s89, 0\nQmvaa 116,120,212\ns_cbranch_vccnz .Lpb0\ns_load_dword s89, s[30:31], 0x0\n"
".Lpb0:\nQmvaa 116,124,180\nQta 148,61,53664\ns_add_u32 s69, s87, 2685944\nQmvaa 112,112,212\nbuffer_load_dwordx4 v62, s[52:55], s69 offen offset:1032 lds\nQmvaa 112,116,180\nQta 150,61,54176\n"
"Qmvaa 144,112,216\ns_add_u32 s77, s77, s89\nQmvaa 144,116,184\nQta 152,61,49544\ns_add_u32 s69, s87, 2750448\nQmvaa 148,120,216\nbuffer_load_dwordx4 v62, s[52:55], s69 offen offset:2064 lds\nQmvaa 148,124,184\n"
"Qta 154,61,50056\nQmvaa 152,128,216\ns_add_u32 s77, s77, s89\nQmvaa 152,132,184\nQta 156,61,53672\ns_add_u32 s69, s87, 2814952\nQmvaa 156,136,216\nbuffer_load_dwordx4 v62, s[52:55], s69 offen offset:3096 lds\n"
"Qmvaa 156,140,184\nQta 158,61,54184\nQmvaa 188,136,220\ns_add_u32 s77, s77, s89\nQmvaa 188,140,188\nQta 160,61,49792\ns_add_u32 m0, s63, 0\ns_add_u32 s88, s86, 640\n"
"Qmvaa 184,128,220\nbuffer_load_dwordx4 v64, s[48:51], s88 offen lds\nQmvaa 184,132,188\nQta 162,61,50304\nQmvaa 180,120,220\ns_add_u32 s77, s77, s89\nQmvaa 180,124,188\nQta 164,61,53920\n"
"s_add_u32 s88, s86, 20096\nQmvaa 176,112,220\nbuffer_load_dwordx4 v64, s[48:51], s88 offen offset:1024 lds\nQmvaa 176,116,188\nQta 166,61,54432\nQmvaa 208,112,224\ns_add_u32 s77, s77, s89\nQmvaa 208,116,192\n"
"Qta 168,61,49800\ns_add_u32 s88, s86, 39552\nQmvaa 212,120,224\nbuffer_load_dwordx4 v64, s[48:51], s88 offen offset:2048 lds\nQmvaa 212,124,192\nQta 170,61,50312\nQmvaa 216,128,224\ns_add_u32 s77, s77, s89\n"
"Qmvaa 216,132,192\nQta 172,61,53928\ns_add_u32 s88, s86, 59008\nQmvaa 220,136,224\nbuffer_load_dwordx4 v64, s[48:51], s88 offen offset:3072 lds\nQmvaa 220,140,192\nQta 174,61,54440\nQmvaa 252,136,228\n"
"s_add_u32 s77, s77, s89\nQmvaa 252,140,196\ns_add_u32 m0, s63, 4096\ns_add_u32 s88, s86, 82560\nQmvaa 248,128,228\nbuffer_load_dwordx4 v64, s[48:51], s88 offen lds\nQmvaa 248,132,196\nQmvaa 244,120,228\n"
"s_add_u32 s77, s77, s89\nQmvaa 244,124,196\ns_add_u32 s88, s86, 102016\nQmvaa 240,112,228\nbuffer_load_dwordx4 v64, s[48:51], s88 offen offset:1024 lds\nQmvaa 240,116,196\nQmvaa 16,112,232\ns_add_u32 s77, s77, s89\n"
"Qmvaa 16,116,200\ns_add_u32 s88, s86, 121472\nQmvaa 20,120,232\nbuffer_load_dwordx4 v64, s[48:51], s88 offen offset:2048 lds\nQmvaa 20,124,200\nQmvaa 24,128,232\ns_add_u32 s77, s77, s89\nQmvaa 24,132,200\n"
"Qmvaa 28,136,232\nQmvaa 28,140,200\ns_waitcnt lgkmcnt(0)\nQmvaa 96,144,208\nQmvaa 96,148,176\ns_add_u32 s68, s71, 4096\nQmvaa 100,152,208\ns_cbranch_vccz .Lst13\n"
"buffer_store_dwordx4 a[96:99], v79, s[56:59], s68 offen offset:2048 sc1\n.Lst13:\nQmvaa 100,156,176\nQmvaa 104,160,208\ns_add_u32 s68, s71, 20480\nQmvaa 104,164,176\ns_cbranch_vccz .Lst12\nbuffer_store_dwordx4 a[100:103], v79, s[56:59], s68 offen offset:2048 sc1\n"
".Lst12:\nQmvaa 108,168,208\nQmvaa 108,172,176\ns_waitcnt lgkmcnt(0)\nQmvaa 140,168,212\ns_waitcnt vmcnt(17)\ns_barrier\nQmvaa 140,172,180\n"
"Qda 208,63,33024\ns_add_u32 m0, s62, 49536\ns_add_u32 s69, s87, 2621568\nQmvaa 136,160,212\nbuffer_load_dwordx4 v62, s[52:55], s69 offen lds\nQmvaa 136,164,180\nQda 176,63,33536\nQmvaa 132,152,212\n"
"Qta 112,61,0\nQmvaa 132,156,180\ns_add_u32 s69, s87, 2686072\nQmvaa 128,144,212\nbuffer_load_dwordx4 v62, s[52:55], s69 offen offset:1032 lds\nQmvaa 128,148,180\nQda 212,63,34048\nQmvaa 160,144,216\n"
"Qda 180,63,34560\nQmvaa 160,148,184\nQta 114,61,512\ns_add_u32 s69, s87, 2750576\nQmvaa 164,152,216\nbuffer_load_dwordx4 v62, s[52:55], s69 offen offset:2064 lds\nQmvaa 164,156,184\nQta 116,61,4128\n"
"Qmvaa 168,160,216\nQmvaa 168,164,184\nQta 118,61,4640\ns_add_u32 s69, s87, 2815080\nQmvaa 172,168,216\nbuffer_load_dwordx4 v62, s[52:55], s69 offen offset:3096 lds\nQmvaa 172,172,184\nQda 216,63,35072\n"
"Qmvaa 204,168,220\nQda 184,63,35584\nQmvaa 204,172,188\nQta 120,61,8\ns_add_u32 m0, s64, 0\nQmvaa 200,160,220\ns_cbranch_vccnz .Ld0q4\nbuffer_load_dwordx4 v72, s[44:47], s60 offen nt lds\n"
".Ld0q4:\nQmvaa 200,164,188\nQta 122,61,520\nQmvaa 196,152,220\nQmvaa 196,156,188\nQta 124,61,4136\nQmvaa 192,144,220\nQda 220,63,36096\n"
"Qmvaa 192,148,188\nQda 188,63,36608\nQmvaa 224,144,224\nQta 126,61,4648\nQmvaa 224,148,192\nQmvaa 228,152,224\nQta 128,61,256\nQmvaa 228,156,192\n"
"Qmvaa 232,160,224\nQta 130,61,768\nQmvaa 232,164,192\nQmvaa 236,168,224\nQda 224,63,37120\nQmvaa 236,172,192\nQda 192,63,37632\nQmvaa 12,168,228\n"
"Qta 132,61,4384\nQmvaa 12,172,196\nQmvaa 8,160,228\nQta 134,61,4896\nQmvaa 8,164,196\nQmvaa 4,152,228\nQta 136,61,264\nQmvaa 4,156,196\n"
"Qmvaa 0,144,228\nQda 228,63,38144\nQmvaa 0,148,196\nQda 196,63,38656\nQmvaa 32,144,232\nQta 138,61,776\nQmvaa 32,148,200\nQmvaa 36,152,232\n"
"Qta 140,61,4392\nQmvaa 36,156,200\nQmvaa 40,160,232\nQta 142,61,4904\nQmvaa 40,164,200\nQmvaa 44,168,232\nQda 232,63,39168\nQmvaa 44,172,200\n"
"Qda 200,63,39680\ns_waitcnt lgkmcnt(2)\nQmvaa 80,112,208\nQmvaa 80,116,176\ns_add_u32 s68, s71, 131072\nQmvaa 84,120,208\ns_cbranch_vccz .Lst11\nbuffer_store_dwordx4 a[8:11], v79, s[56:59], s68 offen offset:0 sc1\n"
".Lst11:\nQmvaa 84,124,176\nQmvaa 88,128,208\ns_add_u32 s68, s71, 147456\nQmvaa 88,132,176\ns_cbranch_vccz .Lst10\nbuffer_store_dwordx4 a[12:15], v79, s[56:59], s68 offen offset:0 sc1\n.Lst10:\n"
"Qmvaa 92,136,208\nQmvaa 92,140,176\ns_waitcnt lgkmcnt(0)\nQmvaa 124,136,212\ns_waitcnt vmcnt(17)\ns_barrier\nQmvaa 124,140,180\nQta 144,61,16512\n"
"s_add_u32 m0, s62, 0\ns_add_u32 s69, s87, 3145728\nQmvaa 120,128,212\nbuffer_load_dwordx4 v62, s[52:55], s69 offen lds\nQmvaa 120,132,180\nQta 146,61,17024\ns_mov_b32 s89, 0\nQmvaa 116,120,212\n"
"s_cbranch_vccnz .Lpb1\ns_load_dword s89, s[30:31], 0x2000\n.Lpb1:\nQmvaa 116,124,180\nQta 148,61,20640\ns_add_u32 s69, s87, 3210232\nQmvaa 112,112,212\nbuffer_load_dwordx4 v62, s[52:55], s69 offen offset:1032 lds\n"
"Qmvaa 112,116,180\nQta 150,61,21152\nQmvaa 144,112,216\ns_add_u32 s77, s77, s89\nQmvaa 144,116,184\nQta 152,61,16520\ns_add_u32 s69, s87, 3274736\nQmvaa 148,120,216\n"
"buffer_load_dwordx4 v62, s[52:55], s69 offen offset:2064 lds\nQmvaa 148,124,184\nQta 154,61,17032\nQmvaa 152,128,216\ns_add_u32 s77, s77, s89\nQmvaa 152,132,184\nQta 156,61,20648\ns_add_u32 s69, s87, 3339240\n"
"Qmvaa 156,136,216\nbuffer_load_dwordx4 v62, s[52:55], s69 offen offset:3096 lds\nQmvaa 156,140,184\nQta 158,61,21160\nQmvaa 188,136,220\ns_add_u32 s77, s77, s89\nQmvaa 188,140,188\nQta 160,61,16768\n"
"s_add_u32 m0, s63, 33024\ns_add_u32 s88, s86, 768\nQmvaa 184,128,220\nbuffer_load_dwordx4 v64, s[48:51], s88 offen lds\nQmvaa 184,132,188\nQta 162,61,17280\nQmvaa 180,120,220\ns_add_u32 s77, s77, s89\n"
"Qmvaa 180,124,188\nQta 164,61,20896\ns_add_u32 s88, s86, 20224\nQmvaa 176,112,220\nbuffer_load_dwordx4 v64, s[48:51], s88 offen offset:1024 lds\nQmvaa 176,116,188\nQta 166,61,21408\nQmvaa 208,112,224\n"
"s_add_u32 s77, s77, s89\nQmvaa 208,116,192\nQta 168,61,16776\ns_add_u32 s88, s86, 39680\nQmvaa 212,120,224\nbuffer_load_dwordx4 v64, s[48:51], s88 offen offset:2048 lds\nQmvaa 212,124,192\nQta 170,61,17288\n"
"Qmvaa 216,128,224\ns_add_u32 s77, s77, s89\nQmvaa 216,132,192\nQta 172,61,20904\ns_add_u32 s88, s86, 59136\nQmvaa 220,136,224\nbuffer_load_dwordx4 v64, s[48:51], s88 offen offset:3072 lds\nQmvaa 220,140,192\n"
"Qta 174,61,21416\nQmvaa 252,136,228\ns_add_u32 s77, s77, s89\nQmvaa 252,140,196\ns_add_u32 m0, s63, 37120\ns_add_u32 s88, s86, 82688\nQmvaa 248,128,228\nbuffer_load_dwordx4 v64, s[48:51], s88 offen lds\n"
"Qmvaa 248,132,196\nQmvaa 244,120,228\ns_add_u32 s77, s77, s89\nQmvaa 244,124,196\ns_add_u32 s88, s86, 102144\nQmvaa 240,112,228\nbuffer_load_dwordx4 v64, s[48:51], s88 offen offset:1024 lds\nQmvaa 240,116,196\n"
"Qmvaa 16,112,232\ns_add_u32 s77, s77, s89\nQmvaa 16,116,200\ns_add_u32 s88, s86, 121600\nQmvaa 20,120,232\nbuffer_load_dwordx4 v64, s[48:51], s88 offen offset:2048 lds\nQmvaa 20,124,200\nQmvaa 24,128,232\n"
"s_add_u32 s77, s77, s89\nQmvaa 24,132,200\nQmvaa 28,136,232\nQmvaa 28,140,200\ns_waitcnt lgkmcnt(0)\nQmvaa 96,144,208\nQmvaa 96,148,176\ns_add_u32 s68, s71, 131072\n"
"Qmvaa 100,152,208\ns_cbranch_vccz .Lst9\nbuffer_store_dwordx4 a[24:27], v79, s[56:59], s68 offen offset:1024 sc1\n.Lst9:\nQmvaa 100,156,176\nQmvaa 104,160,208\ns_add_u32 s68, s71, 147456\nQmvaa 104,164,176\n"
"s_cbranch_vccz .Lst8\nbuffer_store_dwordx4 a[28:31], v79, s[56:59], s68 offen offset:1024 sc1\n.Lst8:\nQmvaa 108,168,208\nQmvaa 108,172,176\ns_waitcnt lgkmcnt(0)\nQmvaa 140,168,212\ns_waitcnt vmcnt(16)\n"
"s_barrier\nQmvaa 140,172,180\nQda 208,63,0\ns_add_u32 m0, s62, 16512\ns_add_u32 s69, s87, 3145856\nQmvaa 136,160,212\nbuffer_load_dwordx4 v62, s[52:55], s69 offen lds\nQmvaa 136,164,180\n"
"Qda 176,63,512\nQmvaa 132,152,212\nQta 112,61,33024\nQmvaa 132,156,180\ns_add_u32 s69, s87, 3210360\nQmvaa 128,144,212\nbuffer_load_dwordx4 v62, s[52:55], s69 offen offset:1032 lds\nQmvaa 128,148,180\n"
"Qda 212,63,1024\nQmvaa 160,144,216\nQda 180,63,1536\nQmvaa 160,148,184\nQta 114,61,33536\ns_add_u32 s69, s87, 3274864\nQmvaa 164,152,216\nbuffer_load_dwordx4 v62, s[52:55], s69 offen offset:2064 lds\n"
"Qmvaa 164,156,184\nQta 116,61,37152\nQmvaa 168,160,216\nQmvaa 168,164,184\nQta 118,61,37664\ns_add_u32 s69, s87, 3339368\nQmvaa 172,168,216\nbuffer_load_dwordx4 v62, s[52:55], s69 offen offset:3096 lds\n"
"Qmvaa 172,172,184\nQda 216,63,2048\nQmvaa 204,168,220\nQda 184,63,2560\nQmvaa 204,172,188\nQta 120,61,33032\ns_add_u32 m0, s64, 0\nQmvaa 200,160,220\n"
"s_cbranch_vccnz .Ld0q5\nbuffer_load_dwordx4 v72, s[44:47], s60 offen offset:1024 nt lds\n.Ld0q5:\nQmvaa 200,164,188\nQta 122,61,33544\nQmvaa 196,152,220\nQmvaa 196,156,188\nQta 124,61,37160\n"
"Qmvaa 192,144,220\nQda 220,63,3072\nQmvaa 192,148,188\nQda 188,63,3584\nQmvaa 224,144,224\nQta 126,61,37672\nQmvaa 224,148,192\nQmvaa 228,152,224\n"
"Qta 128,61,33280\nQmvaa 228,156,192\nQmvaa 232,160,224\nQta 130,61,33792\nQmvaa 232,164,192\nQmvaa 236,168,224\nQda 224,63,4096\nQmvaa 236,172,192\n"
"Qda 192,63,4608\nQmvaa 12,168,228\nQta 132,61,37408\nQmvaa 12,172,196\nQmvaa 8,160,228\nQta 134,61,37920\nQmvaa 8,164,196\nQmvaa 4,152,228\n"
"Qta 136,61,33288\nQmvaa 4,156,196\nQmvaa 0,144,228\nQda 228,63,5120\nQmvaa 0,148,196\nQda 196,63,5632\nQmvaa 32,144,232\nQta 138,61,33800\n"
"Qmvaa 32,148,200\nQmvaa 36,152,232\nQta 140,61,37416\nQmvaa 36,156,200\nQmvaa 40,160,232\nQta 142,61,37928\nQmvaa 40,164,200\nQmvaa 44,168,232\n"
"Qda 232,63,6144\nQmvaa 44,172,200\nQda 200,63,6656\ns_waitcnt lgkmcnt(2)\nQmvaa 80,112,208\nQmvaa 80,116,176\ns_add_u32 s68, s71, 131072\nQmvaa 84,120,208\n"
"s_cbranch_vccz .Lst7\nbuffer_store_dwordx4 a[40:43], v79, s[56:59], s68 offen offset:2048 sc1\n.Lst7:\nQmvaa 84,124,176\nQmvaa 88,128,208\ns_add_u32 s68, s71, 147456\nQmvaa 88,132,176\ns_cbranch_vccz .Lst6\n"
"buffer_store_dwordx4 a[44:47], v79, s[56:59], s68 offen offset:2048 sc1\n.Lst6:\nQmvaa 92,136,208\nQmvaa 92,140,176\ns_waitcnt lgkmcnt(0)\nQmvaa 124,136,212\ns_waitcnt vmcnt(17)\ns_barrier\n"
"Qmvaa 124,140,180\nQta 144,61,49536\ns_add_u32 m0, s62, 33024\ns_add_u32 s69, s87, 3670016\nQmvaa 120,128,212\nbuffer_load_dwordx4 v62, s[52:55], s69 offen lds\nQmvaa 120,132,180\nQta 146,61,50048\n"
"s_mov_b32 s89, 0\nQmvaa 116,120,212\ns_cbranch_vccnz .Lpb2\ns_load_dword s89, s[30:31], 0x6000\n.Lpb2:\nQmvaa 116,124,180\nQta 148,61,53664\ns_add_u32 s69, s87, 3734520\n"
"Qmvaa 112,112,212\nbuffer_load_dwordx4 v62, s[52:55], s69 offen offset:1032 lds\nQmvaa 112,116,180\nQta 150,61,54176\nQmvaa 144,112,216\ns_add_u32 s77, s77, s89\nQmvaa 144,116,184\nQta 152,61,49544\n"
"s_add_u32 s69, s87, 3799024\nQmvaa 148,120,216\nbuffer_load_dwordx4 v62, s[52:55], s69 offen offset:2064 lds\nQmvaa 148,124,184\nQta 154,61,50056\nQmvaa 152,128,216\ns_add_u32 s77, s77, s89\nQmvaa 152,132,184\n"
"Qta 156,61,53672\ns_add_u32 s69, s87, 3863528\nQmvaa 156,136,216\nbuffer_load_dwordx4 v62, s[52:55], s69 offen offset:3096 lds\nQmvaa 156,140,184\nQta 158,61,54184\nQmvaa 188,136,220\ns_add_u32 s77, s77, s89\n"
"Qmvaa 188,140,188\nQta 160,61,49792\ns_add_u32 m0, s63, 0\ns_add_u32 s88, s86, 896\n.Lpg2:\ns_cbranch_execz .Ltr0\nQmvaa 184,128,220\nbuffer_load_dwordx4 v64, s[48:51], s88 offen lds\n"
"Qmvaa 184,132,188\nQta 162,61,50304\nQmvaa 180,120,220\ns_add_u32 s77, s77, s89\nQmvaa 180,124,188\nQta 164,61,53920\ns_add_u32 s88, s86, 20352\nQmvaa 176,112,220\n"
"buffer_load_dwordx4 v64, s[48:51], s88 offen offset:1024 lds\nQmvaa 176,116,188\nQta 166,61,54432\nQmvaa 208,112,224\ns_add_u32 s77, s77, s89\nQmvaa 208,116,192\nQta 168,61,49800\ns_add_u32 s88, s86, 39808\n"
"Qmvaa 212,120,224\nbuffer_load_dwordx4 v64, s[48:51], s88 offen offset:2048 lds\nQmvaa 212,124,192\nQta 170,61,50312\nQmvaa 216,128,224\ns_add_u32 s77, s77, s89\nQmvaa 216,132,192\nQta 172,61,53928\n"
"s_add_u32 s88, s86, 59264\nQmvaa 220,136,224\nbuffer_load_dwordx4 v64, s[48:51], s88 offen offset:3072 lds\nQmvaa 220,140,192\nQta 174,61,54440\nQmvaa 252,136,228\ns_add_u32 s77, s77, s89\nQmvaa 252,140,196\n"
"s_add_u32 m0, s63, 4096\ns_add_u32 s88, s86, 82816\nQmvaa 248,128,228\nbuffer_load_dwordx4 v64, s[48:51], s88 offen lds\nQmvaa 248,132,196\nQmvaa 244,120,228\ns_add_u32 s77, s77, s89\nQmvaa 244,124,196\n"
"s_add_u32 s88, s86, 102272\nQmvaa 240,112,228\nbuffer_load_dwordx4 v64, s[48:51], s88 offen offset:1024 lds\nQmvaa 240,116,196\nQmvaa 16,112,232\ns_add_u32 s77, s77, s89\nQmvaa 16,116,200\ns_add_u32 s88, s86, 121728\n"
"Qmvaa 20,120,232\nbuffer_load_dwordx4 v64, s[48:51], s88 offen offset:2048 lds\nQmvaa 20,124,200\nQmvaa 24,128,232\ns_add_u32 s77, s77, s89\nQmvaa 24,132,200\nQmvaa 28,136,232\nQmvaa 28,140,200\n"
"s_waitcnt lgkmcnt(0)\nQmvaa 96,144,208\nQmvaa 96,148,176\ns_add_u32 s68, s71, 131072\nQmvaa 100,152,208\ns_cbranch_vccz .Lst5\nbuffer_store_dwordx4 a[56:59], v79, s[56:59], s68 offen offset:3072 sc1\n.Lst5:\n"
"Qmvaa 100,156,176\nQmvaa 104,160,208\ns_add_u32 s68, s71, 147456\nQmvaa 104,164,176\ns_cbranch_vccz .Lst4\nbuffer_store_dwordx4 a[60:63], v79, s[56:59], s68 offen offset:3072 sc1\n.Lst4:\nQmvaa 108,168,208\n"
"Qmvaa 108,172,176\ns_waitcnt lgkmcnt(0)\nQmvaa 140,168,212\ns_waitcnt vmcnt(16)\ns_barrier\nQmvaa 140,172,180\nQda 208,63,33024\ns_add_u32 m0, s62, 49536\n"
"s_add_u32 s69, s87, 3670144\nQmvaa 136,160,212\nbuffer_load_dwordx4 v62, s[52:55], s69 offen lds\nQmvaa 136,164,180\nQda 176,63,33536\nQmvaa 132,152,212\nQta 112,61,0\nQmvaa 132,156,180\n"
"s_add_u32 s69, s87, 3734648\nQmvaa 128,144,212\nbuffer_load_dwordx4 v62, s[52:55], s69 offen offset:1032 lds\nQmvaa 128,148,180\nQda 212,63,34048\nQmvaa 160,144,216\nQda 180,63,34560\nQmvaa 160,148,184\n"
"Qta 114,61,512\ns_add_u32 s69, s87, 3799152\nQmvaa 164,152,216\nbuffer_load_dwordx4 v62, s[52:55], s69 offen offset:2064 lds\nQmvaa 164,156,184\nQta 116,61,4128\nQmvaa 168,160,216\nQmvaa 168,164,184\n"
"Qta 118,61,4640\ns_add_u32 s69, s87, 3863656\nQmvaa 172,168,216\nbuffer_load_dwordx4 v62, s[52:55], s69 offen offset:3096 lds\nQmvaa 172,172,184\nQda 216,63,35072\nQmvaa 204,168,220\nQda 184,63,35584\n"
"Qmvaa 204,172,188\nQta 120,61,8\ns_add_u32 m0, s64, 0\nQmvaa 200,160,220\ns_cbranch_vccnz .Ld0q6\nbuffer_load_dwordx4 v72, s[44:47], s90 offen offset:2048 nt lds\n.Ld0q6:\nQmvaa 200,164,188\n"
"Qta 122,61,520\nQmvaa 196,152,220\nQmvaa 196,156,188\nQta 124,61,4136\nQmvaa 192,144,220\nQda 220,63,36096\nQmvaa 192,148,188\nQda 188,63,36608\n"
"Qmvaa 224,144,224\nQta 126,61,4648\nQmvaa 224,148,192\nQmvaa 228,152,224\nQta 128,61,256\nQmvaa 228,156,192\nQmvaa 232,160,224\nQta 130,61,768\n"
"Qmvaa 232,164,192\nQmvaa 236,168,224\nQda 224,63,37120\nQmvaa 236,172,192\nQda 192,63,37632\nQmvaa 12,168,228\nQta 132,61,4384\nQmvaa 12,172,196\n"
"Qmvaa 8,160,228\nQta 134,61,4896\nQmvaa 8,164,196\nQmvaa 4,152,228\nQta 136,61,264\nQmvaa 4,156,196\nQmvaa 0,144,228\nQda 228,63,38144\n"
"Qmvaa 0,148,196\nQda 196,63,38656\nQmvaa 32,144,232\nQta 138,61,776\nQmvaa 32,148,200\nQmvaa 36,152,232\nQta 140,61,4392\nQmvaa 36,156,200\n"
"Qmvaa 40,160,232\nQta 142,61,4904\nQmvaa 40,164,200\nQmvaa 44,168,232\nQda 232,63,39168\nQmvaa 44,172,200\nQda 200,63,39680\ns_waitcnt lgkmcnt(2)\n"
"Qmvaa 80,112,208\nQmvaa 80,116,176\ns_add_u32 s68, s71, 135168\nQmvaa 84,120,208\ns_cbranch_vccz .Lst3\nbuffer_store_dwordx4 a[72:75], v79, s[56:59], s68 offen offset:0 sc1\n.Lst3:\nQmvaa 84,124,176\n"
"Qmvaa 88,128,208\ns_add_u32 s68, s71, 151552\nQmvaa 88,132,176\ns_cbranch_vccz .Lst2\nbuffer_store_dwordx4 a[76:79], v79, s[56:59], s68 offen offset:0 sc1\n.Lst2:\nQmvaa 92,136,208\nQmvaa 92,140,176\n"
"s_waitcnt lgkmcnt(0)\nQmvaa 124,136,212\ns_waitcnt vmcnt(17)\ns_barrier\nQmvaa 124,140,180\nQta 144,61,16512\ns_add_u32 m0, s62, 0\ns_add_u32 s69, s87, 4194304\n"
"Qmvaa 120,128,212\nbuffer_load_dwordx4 v62, s[52:55], s69 offen lds\nQmvaa 120,132,180\nQta 146,61,17024\ns_mov_b32 s89, 0\nQmvaa 116,120,212\ns_cbranch_vccnz .Lpb3\ns_load_dword s89, s[30:31], 0x4000\n"
".Lpb3:\nQmvaa 116,124,180\nQta 148,61,20640\ns_add_u32 s69, s87, 4258808\nQmvaa 112,112,212\nbuffer_load_dwordx4 v62, s[52:55], s69 offen offset:1032 lds\nQmvaa 112,116,180\nQta 150,61,21152\n"
"Qmvaa 144,112,216\ns_add_u32 s77, s77, s89\nQmvaa 144,116,184\nQta 152,61,16520\ns_add_u32 s69, s87, 4323312\nQmvaa 148,120,216\nbuffer_load_dwordx4 v62, s[52:55], s69 offen offset:2064 lds\nQmvaa 148,124,184\n"
"Qta 154,61,17032\nQmvaa 152,128,216\ns_add_u32 s77, s77, s89\nQmvaa 152,132,184\nQta 156,61,20648\ns_add_u32 s69, s87, 4387816\nQmvaa 156,136,216\nbuffer_load_dwordx4 v62, s[52:55], s69 offen offset:3096 lds\n"
"Qmvaa 156,140,184\nQta 158,61,21160\nQmvaa 188,136,220\ns_add_u32 s77, s77, s89\nQmvaa 188,140,188\nQta 160,61,16768\ns_add_u32 m0, s63, 33024\ns_add_u32 s88, s86, 1024\n"
"Qmvaa 184,128,220\nbuffer_load_dwordx4 v64, s[48:51], s88 offen lds\nQmvaa 184,132,188\nQta 162,61,17280\nQmvaa 180,120,220\ns_add_u32 s77, s77, s89\nQmvaa 180,124,188\nQta 164,61,20896\n"
"s_add_u32 s88, s86, 20480\nQmvaa 176,112,220\nbuffer_load_dwordx4 v64, s[48:51], s88 offen offset:1024 lds\nQmvaa 176,116,188\nQta 166,61,21408\nQmvaa 208,112,224\ns_add_u32 s77, s77, s89\nQmvaa 208,116,192\n"
"Qta 168,61,16776\ns_add_u32 s88, s86, 39936\nQmvaa 212,120,224\nbuffer_load_dwordx4 v64, s[48:51], s88 offen offset:2048 lds\nQmvaa 212,124,192\nQta 170,61,17288\nQmvaa 216,128,224\ns_add_u32 s77, s77, s89\n"
"Qmvaa 216,132,192\nQta 172,61,20904\ns_add_u32 s88, s86, 59392\nQmvaa 220,136,224\nbuffer_load_dwordx4 v64, s[48:51], s88 offen offset:3072 lds\nQmvaa 220,140,192\nQta 174,61,21416\nQmvaa 252,136,228\n"
"s_add_u32 s77, s77, s89\nQmvaa 252,140,196\ns_add_u32 m0, s63, 37120\ns_add_u32 s88, s86, 82944\nQmvaa 248,128,228\nbuffer_load_dwordx4 v64, s[48:51], s88 offen lds\nQmvaa 248,132,196\nQmvaa 244,120,228\n"
"s_add_u32 s77, s77, s89\nQmvaa 244,124,196\ns_add_u32 s88, s86, 102400\nQmvaa 240,112,228\nbuffer_load_dwordx4 v64, s[48:51], s88 offen offset:1024 lds\nQmvaa 240,116,196\nQmvaa 16,112,232\ns_add_u32 s77, s77, s89\n"
"Qmvaa 16,116,200\ns_add_u32 s88, s86, 121856\nQmvaa 20,120,232\nbuffer_load_dwordx4 v64, s[48:51], s88 offen offset:2048 lds\nQmvaa 20,124,200\nQmvaa 24,128,232\ns_add_u32 s77, s77, s89\nQmvaa 24,132,200\n"
"Qmvaa 28,136,232\nQmvaa 28,140,200\ns_waitcnt lgkmcnt(0)\nQmvaa 96,144,208\nQmvaa 96,148,176\ns_add_u32 s68, s71, 135168\nQmvaa 100,152,208\ns_cbranch_vccz .Lst1\n"
"buffer_store_dwordx4 a[104:107], v79, s[56:59], s68 offen offset:2048 sc1\n.Lst1:\nQmvaa 100,156,176\nQmvaa 104,160,208\ns_add_u32 s68, s71, 151552\nQmvaa 104,164,176\ns_cbranch_vccz .Lst0\nbuffer_store_dwordx4 a[108:111], v79, s[56:59], s68 offen offset:2048 sc1\n"
".Lst0:\nQmvaa 108,168,208\nQmvaa 108,172,176\ns_waitcnt lgkmcnt(0)\nQmvaa 140,168,212\ns_waitcnt vmcnt(16)\ns_barrier\nQmvaa 140,172,180\n"
"Qda 208,63,0\ns_add_u32 m0, s62, 16512\ns_add_u32 s69, s87, 4194432\nQmvaa 136,160,212\nbuffer_load_dwordx4 v62, s[52:55], s69 offen lds\nQmvaa 136,164,180\nQda 176,63,512\nQmvaa 132,152,212\n"
"Qta 112,61,33024\nQmvaa 132,156,180\ns_add_u32 s69, s87, 4258936\nQmvaa 128,144,212\nbuffer_load_dwordx4 v62, s[52:55], s69 offen offset:1032 lds\nQmvaa 128,148,180\nQda 212,63,1024\nQmvaa 160,144,216\n"
"Qda 180,63,1536\nQmvaa 160,148,184\nQta 114,61,33536\ns_add_u32 s69, s87, 4323440\nQmvaa 164,152,216\nbuffer_load_dwordx4 v62, s[52:55], s69 offen offset:2064 lds\nQmvaa 164,156,184\nQta 116,61,37152\n"
"Qmvaa 168,160,216\nQmvaa 168,164,184\nQta 118,61,37664\ns_add_u32 s69, s87, 4387944\nQmvaa 172,168,216\nbuffer_load_dwordx4 v62, s[52:55], s69 offen offset:3096 lds\nQmvaa 172,172,184\nQda 216,63,2048\n"
"Qmvaa 204,168,220\nQda 184,63,2560\nQmvaa 204,172,188\nQta 120,61,33032\ns_add_u32 m0, s64, 0\nQmvaa 200,160,220\ns_cbranch_vccnz .Ld0q7\nbuffer_load_dwordx4 v72, s[44:47], s90 offen offset:3072 nt lds\n"
"s_add_u32 s65, s65, 2048\ns_xor_b32 s60, s65, %1\ns_add_u32 s90, s60, 260096\n.Ld0q7:\nQmvaa 200,164,188\nQta 122,61,33544\nQmvaa 196,152,220\nQmvaa 196,156,188\n"
"Qta 124,61,37160\nQmvaa 192,144,220\nQda 220,63,3072\nQmvaa 192,148,188\nQda 188,63,3584\nQmvaa 224,144,224\nQta 126,61,37672\nQmvaa 224,148,192\n"
"Qmvaa 228,152,224\nQta 128,61,33280\nQmvaa 228,156,192\nQmvaa 232,160,224\nQta 130,61,33792\nQmvaa 232,164,192\nQmvaa 236,168,224\nQda 224,63,4096\n"
"Qmvaa 236,172,192\nQda 192,63,4608\nQmvaa 12,168,228\nQta 132,61,37408\nQmvaa 12,172,196\nQmvaa 8,160,228\nQta 134,61,37920\nQmvaa 8,164,196\n"
"Qmvaa 4,152,228\nQta 136,61,33288\nQmvaa 4,156,196\nQmvaa 0,144,228\nQda 228,63,5120\nQmvaa 0,148,196\nQda 196,63,5632\nQmvaa 32,144,232\n"
"Qta 138,61,33800\nQmvaa 32,148,200\nQmvaa 36,152,232\nQta 140,61,37416\nQmvaa 36,156,200\nQmvaa 40,160,232\nQta 142,61,37928\nQmvaa 40,164,200\n"
"Qmvaa 44,168,232\nQda 232,63,6144\nQmvaa 44,172,200\nQda 200,63,6656\ns_waitcnt lgkmcnt(2)\nQmvaa 80,112,208\nQmvaa 80,116,176\nQmvaa 84,120,208\n"
"Qmvaa 84,124,176\nQmvaa 88,128,208\nQmvaa 88,132,176\nQmvaa 92,136,208\nQmvaa 92,140,176\ns_waitcnt lgkmcnt(0)\nQmvaa 124,136,212\ns_waitcnt vmcnt(17)\n"
"s_barrier\nQmvaa 124,140,180\nQta 144,61,49536\ns_add_u32 m0, s62, 33024\ns_add_u32 s69, s87, 4718592\nQmvaa 120,128,212\nbuffer_load_dwordx4 v62, s[52:55], s69 offen lds\nQmvaa 120,132,180\n"
"Qta 146,61,50048\nQmvaa 116,120,212\nQmvaa 116,124,180\nQta 148,61,53664\ns_add_u32 s69, s87, 4783096\nQmvaa 112,112,212\nbuffer_load_dwordx4 v62, s[52:55], s69 offen offset:1032 lds\nQmvaa 112,116,180\n"
"Qta 150,61,54176\nQmvaa 144,112,216\nQmvaa 144,116,184\nQta 152,61,49544\ns_add_u32 s69, s87, 4847600\nQmvaa 148,120,216\nbuffer_load_dwordx4 v62, s[52:55], s69 offen offset:2064 lds\nQmvaa 148,124,184\n"
"Qta 154,61,50056\nQmvaa 152,128,216\nQmvaa 152,132,184\nQta 156,61,53672\ns_add_u32 s69, s87, 4912104\nQmvaa 156,136,216\nbuffer_load_dwordx4 v62, s[52:55], s69 offen offset:3096 lds\nQmvaa 156,140,184\n"
"Qta 158,61,54184\nQmvaa 188,136,220\nQmvaa 188,140,188\nQta 160,61,49792\ns_add_u32 m0, s63, 0\ns_add_u32 s88, s86, 1152\nQmvaa 184,128,220\nbuffer_load_dwordx4 v64, s[48:51], s88 offen lds\n"
"Qmvaa 184,132,188\nQta 162,61,50304\nQmvaa 180,120,220\nQmvaa 180,124,188\nQta 164,61,53920\ns_add_u32 s88, s86, 20608\nQmvaa 176,112,220\nbuffer_load_dwordx4 v64, s[48:51], s88 offen offset:1024 lds\n"
"Qmvaa 176,116,188\nQta 166,61,54432\nQmvaa 208,112,224\nQmvaa 208,116,192\nQta 168,61,49800\ns_add_u32 s88, s86, 40064\nQmvaa 212,120,224\nbuffer_load_dwordx4 v64, s[48:51], s88 offen offset:2048 lds\n"
"Qmvaa 212,124,192\nQta 170,61,50312\nQmvaa 216,128,224\nQmvaa 216,132,192\nQta 172,61,53928\ns_add_u32 s88, s86, 59520\nQmvaa 220,136,224\nbuffer_load_dwordx4 v64, s[48:51], s88 offen offset:3072 lds\n"
"Qmvaa 220,140,192\nQta 174,61,54440\nQmvaa 252,136,228\nQmvaa 252,140,196\ns_add_u32 m0, s63, 4096\ns_add_u32 s88, s86, 83072\nQmvaa 248,128,228\nbuffer_load_dwordx4 v64, s[48:51], s88 offen lds\n"
"Qmvaa 248,132,196\nv_mov_b32 v66, s77\nQmvaa 244,120,228\nds_write_b32 v73, v66 offset:16\nQmvaa 244,124,196\ns_add_u32 s88, s86, 102528\nQmvaa 240,112,228\nbuffer_load_dwordx4 v64, s[48:51], s88 offen offset:1024 lds\n"
"Qmvaa 240,116,196\nQmvaa 16,112,232\nQmvaa 16,116,200\ns_add_u32 s88, s86, 121984\nQmvaa 20,120,232\nbuffer_load_dwordx4 v64, s[48:51], s88 offen offset:2048 lds\nQmvaa 20,124,200\nQmvaa 24,128,232\n"
"Qmvaa 24,132,200\nQmvaa 28,136,232\nQmvaa 28,140,200\ns_waitcnt lgkmcnt(1)\nQmvaa 96,144,208\ns_or_b32 s68, s70, s67\ns_cselect_b32 s68, 0, 128\nQmvaa 96,148,176\n"
"v_mov_b32 v66, 0x7ffff000\nv_cmp_gt_u32 vcc, s68, v60\nQmvaa 100,152,208\nv_not_b32 v67, v65\ns_lshl_b32 s68, s19, 2\nQmvaa 100,156,176\nQmvaa 104,160,208\nv_cndmask_b32 v66, v66, v76, vcc\n"
"v_readlane_b32 s30, v67, s19\nQmvaa 104,164,176\nQmvaa 108,168,208\nQmvaa 108,172,176\ns_waitcnt lgkmcnt(0)\nQmvaa 140,168,212\ns_waitcnt vmcnt(16)\ns_barrier\n"
"Qmvaa 140,172,180\nQda 208,63,33024\ns_add_u32 m0, s62, 49536\ns_add_u32 s69, s87, 4718720\nQmvaa 136,160,212\nbuffer_load_dwordx4 v62, s[52:55], s69 offen lds\nQmvaa 136,164,180\nQda 176,63,33536\n"
"Qmvaa 132,152,212\nQta 112,61,0\nQmvaa 132,156,180\ns_add_u32 s69, s87, 4783224\nQmvaa 128,144,212\nbuffer_load_dwordx4 v62, s[52:55], s69 offen offset:1032 lds\nQmvaa 128,148,180\nQda 212,63,34048\n"
"Qmvaa 160,144,216\nQda 180,63,34560\nQmvaa 160,148,184\nQta 114,61,512\n.Lpg3:\ns_cbranch_execz .Ltr0\ns_add_u32 s69, s87, 4847728\nQmvaa 164,152,216\n"
"buffer_load_dwordx4 v62, s[52:55], s69 offen offset:2064 lds\nQmvaa 164,156,184\nQta 116,61,4128\nQmvaa 168,160,216\nQmvaa 168,164,184\nQta 118,61,4640\ns_add_u32 s69, s87, 4912232\nQmvaa 172,168,216\n"
"buffer_load_dwordx4 v62, s[52:55], s69 offen offset:3096 lds\nQmvaa 172,172,184\nQda 216,63,35072\nQmvaa 204,168,220\nQda 184,63,35584\nQmvaa 204,172,188\nQta 120,61,8\nQmvaa 200,160,220\n"
"ds_read_b32 v78, v74 offset:16\nQmvaa 200,164,188\nQta 122,61,520\nQmvaa 196,152,220\nQmvaa 196,156,188\nQta 124,61,4136\nQmvaa 192,144,220\nQda 220,63,36096\n"
"Qmvaa 192,148,188\nQda 188,63,36608\nQmvaa 224,144,224\nQta 126,61,4648\nQmvaa 224,148,192\nQmvaa 228,152,224\nQta 128,61,256\nQmvaa 228,156,192\n"
"Qmvaa 232,160,224\nQta 130,61,768\nQmvaa 232,164,192\nQmvaa 236,168,224\nQda 224,63,37120\nQmvaa 236,172,192\nQda 192,63,37632\nQmvaa 12,168,228\n"
"Qta 132,61,4384\nQmvaa 12,172,196\nQmvaa 8,160,228\nQta 134,61,4896\nQmvaa 8,164,196\nQmvaa 4,152,228\nQta 136,61,264\nQmvaa 4,156,196\n"
"Qmvaa 0,144,228\nQda 228,63,38144\nQmvaa 0,148,196\nQda 196,63,38656\nQmvaa 32,144,232\nQta 138,61,776\nQmvaa 32,148,200\nQmvaa 36,152,232\n"
"Qta 140,61,4392\nQmvaa 36,156,200\nQmvaa 40,160,232\nQta 142,61,4904\nQmvaa 40,164,200\nQmvaa 44,168,232\nQda 232,63,39168\nQmvaa 44,172,200\n"
"Qda 200,63,39680\ns_waitcnt lgkmcnt(2)\nQmvaa 80,112,208\nQmvaa 80,116,176\nQmvaa 84,120,208\nQmvaa 84,124,176\nQmvaa 88,128,208\nQmvaa 88,132,176\n"
"Qmvaa 92,136,208\nQmvaa 92,140,176\ns_waitcnt lgkmcnt(0)\nQmvaa 124,136,212\ns_waitcnt vmcnt(15)\ns_barrier\nQmvaa 124,140,180\nQta 144,61,16512\n"
"s_mov_b32 s56, s94\ns_mov_b32 s57, s95\ns_mov_b32 s58, s75\ns_mov_b32 s94, 0\ns_add_u32 m0, %4, 160768\nQmvaa 120,128,212\nbuffer_load_dwordx4 v60, s[56:59], s94 offen lds\nQmvaa 120,132,180\n"
"Qta 146,61,17024\nQmvaa 116,120,212\nv_add_u32_dpp v78, v78, v78 quad_perm:[1,0,3,2] row_mask:0xf bank_mask:0xf\nQmvaa 116,124,180\nQta 148,61,20640\ns_add_u32 s94, s94, 4096\nQmvaa 112,112,212\nbuffer_load_dwordx4 v60, s[56:59], s94 offen lds\n"
"Qmvaa 112,116,180\nQta 150,61,21152\nQmvaa 144,112,216\nv_add_u32_dpp v78, v78, v78 quad_perm:[2,3,0,1] row_mask:0xf bank_mask:0xf\nQmvaa 144,116,184\nQta 152,61,16520\ns_add_u32 s94, s94, 4096\nQmvaa 148,120,216\n"
"buffer_load_dwordx4 v60, s[56:59], s94 offen lds\nQmvaa 148,124,184\nQta 154,61,17032\nQmvaa 152,128,216\nv_ashrrev_i32 v78, 1, v78\nv_med3_i32 v78, v78, -7, 7\nQmvaa 152,132,184\nQta 156,61,20648\n"
"s_add_u32 s94, s94, 4096\nQmvaa 156,136,216\nbuffer_load_dwordx4 v60, s[56:59], s94 offen lds\nQmvaa 156,140,184\nQta 158,61,21160\nQmvaa 188,136,220\nv_add_lshl_u32 v78, v78, 8, 28\nv_xor_b32 v67, v67, v78\n"
"Qmvaa 188,140,188\nQta 160,61,16768\ns_add_u32 s94, s94, 4096\nQmvaa 184,128,220\nbuffer_load_dwordx4 v60, s[56:59], s94 offen lds\nQmvaa 184,132,188\nQta 162,61,17280\nQmvaa 180,120,220\n"
"buffer_store_dword v67, v66, s[24:27], s68 offen offset:1024 sc1\nQmvaa 180,124,188\nQta 164,61,20896\ns_add_u32 s94, s94, 4096\nQmvaa 176,112,220\nbuffer_load_dwordx4 v60, s[56:59], s94 offen lds\nQmvaa 176,116,188\nQta 166,61,21408\n"
"Qmvaa 208,112,224\ns_lshl_b32 s68, s19, 4\ns_lshl1_add_u32 s33, s70, s67\nQmvaa 208,116,192\nQta 168,61,16776\ns_add_u32 s94, s94, 4096\nQmvaa 212,120,224\nbuffer_load_dwordx4 v60, s[56:59], s94 offen lds\n"
"Qmvaa 212,124,192\nQta 170,61,17288\nQmvaa 216,128,224\ns_cmp_eq_u32 s33, 1\ns_cselect_b32 s68, s68, -1\nQmvaa 216,132,192\nQta 172,61,20904\ns_add_u32 s94, s94, 4096\n"
"Qmvaa 220,136,224\nbuffer_load_dwordx4 v60, s[56:59], s94 offen lds\nQmvaa 220,140,192\nQta 174,61,21416\nQmvaa 252,136,228\nv_mov_b32 v67, 0x7ffff000\nv_cmp_eq_u32 vcc, s68, v60\nQmvaa 252,140,196\n"
"s_add_u32 s94, s94, 4096\nQmvaa 248,128,228\nbuffer_load_dwordx4 v60, s[56:59], s94 offen lds\nQmvaa 248,132,196\nv_cndmask_b32 v67, v67, v76, vcc\nv_add_u32 v66, 1, v65\nQmvaa 244,120,228\nbuffer_store_dword v66, v67, s[24:27], 0 offen sc1\n"
"Qmvaa 244,124,196\ns_add_u32 s94, s94, 4096\nQmvaa 240,112,228\nbuffer_load_dwordx4 v60, s[56:59], s94 offen lds\nQmvaa 240,116,196\nv_mov_b32 v55, 0x3f803f80\ns_mov_b32 vcc_lo, 0x33333333\nQmvaa 16,112,232\n"
"s_mov_b32 vcc_hi, 0xcccccccc\nv_cndmask_b32 v55, 0, v55, vcc\nQmvaa 16,116,200\nv_accvgpr_write_b32 a204, v55\nv_accvgpr_write_b32 a205, v55\nQmvaa 20,120,232\nv_accvgpr_write_b32 a206, v55\nv_accvgpr_write_b32 a207, v55\n"
"Qmvaa 20,124,200\ns_add_u32 s87, s87, 2048\nv_mov_b64 v[54:55], 0\nQmvaa 24,128,232\nglobal_load_dword v78, v60, s[100:101]\nQmvaa 24,132,200\nQmvaa 28,136,232\nQmvaa 28,140,200\n"
"s_waitcnt lgkmcnt(0)\nQmvaa 96,144,208\nQmvaa 96,148,176\nQmvaa 100,152,208\nQmvaa 100,156,176\nQmvaa 104,160,208\nQmvaa 104,164,176\nQmvaa 108,168,208\n"
"Qmvaa 108,172,176\ns_waitcnt lgkmcnt(0)\nQmvaa 140,168,212\ns_waitcnt vmcnt(17)\ns_barrier\nQmvaa 140,172,180\nQda 208,63,0\ns_add_u32 m0, s61, 33024\n"
"Qmvaa 136,160,212\nbuffer_load_dwordx4 v69, s[40:43], s60 offen nt lds\nQmvaa 136,164,180\nQda 176,63,512\nQmvaa 132,152,212\nQta 112,61,33024\nQmvaa 132,156,180\nQtv 48,70,128\n"
"Qmvaa 128,144,212\nbuffer_load_dwordx4 v69, s[40:43], s60 offen offset:1024 nt lds\nQmvaa 128,148,180\nQda 212,63,1024\nQmvaa 160,144,216\nQda 180,63,1536\nQmvaa 160,148,184\nQta 114,61,33536\n"
"Qmvaa 164,152,216\nbuffer_load_dwordx4 v69, s[40:43], s90 offen offset:2048 nt lds\nQmvaa 164,156,184\nQta 116,61,37152\nQmvaa 168,160,216\nQtv 50,70,1152\nQmvaa 168,164,184\nQta 118,61,37664\n"
"Qmvaa 172,168,216\nbuffer_load_dwordx4 v69, s[40:43], s90 offen offset:3072 nt lds\nQmvaa 172,172,184\nQda 216,63,2048\nQmvaa 204,168,220\nQda 184,63,2560\nQmvaa 204,172,188\nQta 120,61,33032\n"
"s_add_u32 m0, s64, 33024\nQmvaa 200,160,220\nbuffer_load_dwordx4 v72, s[44:47], s60 offen nt lds\nQmvaa 200,164,188\nQta 122,61,33544\ns_waitcnt lgkmcnt(5)\nQmvaa 196,152,220\nv_max_f32 v66, |v48|, |v50|\n"
"Qmvaa 196,156,188\nQta 124,61,37160\nQmvaa 192,144,220\nbuffer_load_dwordx4 v72, s[44:47], s60 offen offset:1024 nt lds\nQmvaa 192,148,188\nQda 220,63,3072\nv_mov_b32 v67, v66\nQmvaa 224,144,224\n"
"Qda 188,63,3584\nv_permlane32_swap_b32 v66, v67\nQmvaa 224,148,192\nQta 126,61,37672\nv_max_f32 v66, v66, v67\nQmvaa 228,152,224\nbuffer_load_dwordx4 v72, s[44:47], s90 offen offset:2048 nt lds\nQmvaa 228,156,192\n"
"Qta 128,61,33280\nv_mov_b32 v67, v66\nQmvaa 232,160,224\ns_nop 0\nv_permlane16_swap_b32 v66, v67\nQmvaa 232,164,192\nQta 130,61,33792\nv_max_f32 v66, v66, v67\n"
"Qmvaa 236,168,224\nbuffer_load_dwordx4 v72, s[44:47], s90 offen offset:3072 nt lds\ns_add_u32 s65, s65, 2048\ns_xor_b32 s60, s65, %1\ns_add_u32 s90, s60, 260096\nQmvaa 236,172,192\nQda 224,63,4096\nv_min_f32_dpp v66, v66, v66 row_ror:8 row_mask:0xf bank_mask:0xf\n"
"Qmvaa 12,168,228\nQda 192,63,4608\nv_min_f32_dpp v66, v66, v66 row_ror:4 row_mask:0xf bank_mask:0xf\nQmvaa 12,172,196\nQta 132,61,37408\nQmvaa 8,160,228\nv_min_f32_dpp v66, v66, v66 row_ror:2 row_mask:0xf bank_mask:0xf\nQmvaa 8,164,196\n"
"Qta 134,61,37920\nv_min_f32_dpp v66, v66, v66 row_ror:1 row_mask:0xf bank_mask:0xf\nQmvaa 4,152,228\nQmvaa 4,156,196\nQta 136,61,33288\nv_and_b32 v66, 0x7f000000, v66\nQmvaa 0,144,228\nQda 228,63,5120\n"
"v_or_b32 v66, 0x400000, v66\nQmvaa 0,148,196\nQda 196,63,5632\nQmvaa 32,144,232\nQta 138,61,33800\nv_mov_b32 v67, 0\nQmvaa 32,148,200\nv_mov_b32 v58, 0x39800000\n"
"Qmvaa 36,152,232\nQta 140,61,37416\nQmvaa 36,156,200\nv_mov_b32 v59, 0\nQmvaa 40,160,232\nQta 142,61,37928\nv_readfirstlane_b32 s73, v66\nQmvaa 40,164,200\n"
"Qmvaa 44,168,232\nQda 232,63,6144\nQmvaa 44,172,200\nQda 200,63,6656\ns_waitcnt lgkmcnt(2)\nQmvaa 80,112,208\nQmvaa 80,116,176\nQmvaa 84,120,208\n"
"Qmvaa 84,124,176\nQmvaa 88,128,208\nQmvaa 88,132,176\nQmvaa 92,136,208\nQmvaa 92,140,176\ns_waitcnt lgkmcnt(0)\nQmvaa 124,136,212\ns_waitcnt vmcnt(21)\n"
"s_barrier\nQmvaa 124,140,180\nQta 144,61,49536\nv_fma_f32 v80, v80, s72, v52\nQmvaa 120,128,212\nv_fma_f32 v81, v81, s72, v52\nv_fma_f32 v82, v82, s72, v52\nQmvaa 120,132,180\n"
"Qta 146,61,50048\nv_fma_f32 v83, v83, s72, v52\nQmvaa 116,120,212\nv_fma_f32 v84, v84, s72, v52\nv_fma_f32 v85, v85, s72, v52\nQmvaa 116,124,180\nQta 148,61,53664\nv_fma_f32 v86, v86, s72, v52\n"
"Qmvaa 112,112,212\nv_fma_f32 v87, v87, s72, v52\nQk 80,80,81\nQmvaa 112,116,180\nQta 150,61,54176\nQk 81,82,83\nQmvaa 144,112,216\nQk 82,84,85\n"
"Qk 83,86,87\nQmvaa 144,116,184\nQta 152,61,49544\nv_accvgpr_write_b32 a0, v80\nQmvaa 148,120,216\nv_accvgpr_write_b32 a1, v81\nv_accvgpr_write_b32 a2, v82\nQmvaa 148,124,184\n"
"Qta 154,61,50056\nv_accvgpr_write_b32 a3, v83\nQmvaa 152,128,216\nv_fma_f32 v88, v88, s72, v52\nv_fma_f32 v89, v89, s72, v52\nQmvaa 152,132,184\nQta 156,61,53672\nv_fma_f32 v90, v90, s72, v52\n"
"Qmvaa 156,136,216\nv_fma_f32 v91, v91, s72, v52\nv_fma_f32 v92, v92, s72, v52\nQmvaa 156,140,184\nQta 158,61,54184\nv_fma_f32 v93, v93, s72, v52\nQmvaa 188,136,220\nv_fma_f32 v94, v94, s72, v52\n"
"v_fma_f32 v95, v95, s72, v52\nQmvaa 188,140,188\nQta 160,61,49792\nQk 88,88,89\nQmvaa 184,128,220\nQk 89,90,91\nQk 90,92,93\nQmvaa 184,132,188\n"
"Qta 162,61,50304\nQk 91,94,95\nQmvaa 180,120,220\nv_accvgpr_write_b32 a4, v88\nv_accvgpr_write_b32 a5, v89\nQmvaa 180,124,188\nQta 164,61,53920\nv_accvgpr_write_b32 a6, v90\n"
"Qmvaa 176,112,220\nv_accvgpr_write_b32 a7, v91\nv_fma_f32 v120, v120, s72, v52\nQmvaa 176,116,188\nQta 166,61,54432\nv_fma_f32 v121, v121, s72, v52\nQmvaa 208,112,224\nv_fma_f32 v122, v122, s72, v52\n"
"v_fma_f32 v123, v123, s72, v52\nQmvaa 208,116,192\nQta 168,61,49800\nv_fma_f32 v124, v124, s72, v52\nQmvaa 212,120,224\nv_fma_f32 v125, v125, s72, v52\nv_fma_f32 v126, v126, s72, v52\nQmvaa 212,124,192\n"
"Qta 170,61,50312\nv_fma_f32 v127, v127, s72, v52\nQmvaa 216,128,224\nQk 120,120,121\nQk 121,122,123\nQmvaa 216,132,192\nQta 172,61,53928\nQk 122,124,125\n"
"Qmvaa 220,136,224\nQk 123,126,127\nv_accvgpr_write_b32 a20, v120\nQmvaa 220,140,192\nQta 174,61,54440\nv_accvgpr_write_b32 a21, v121\nQmvaa 252,136,228\nv_accvgpr_write_b32 a22, v122\n"
"v_accvgpr_write_b32 a23, v123\nQmvaa 252,140,196\nv_fma_f32 v112, v112, s72, v52\nv_fma_f32 v113, v113, s72, v52\nQmvaa 248,128,228\nv_fma_f32 v114, v114, s72, v52\nv_fma_f32 v115, v115, s72, v52\nQmvaa 248,132,196\n"
"v_fma_f32 v116, v116, s72, v52\nv_fma_f32 v117, v117, s72, v52\nQmvaa 244,120,228\nv_fma_f32 v118, v118, s72, v52\nv_fma_f32 v119, v119, s72, v52\nQmvaa 244,124,196\nQk 112,112,113\nQk 113,114,115\n"
"Qmvaa 240,112,228\nQk 114,116,117\nQk 115,118,119\nQmvaa 240,116,196\nv_accvgpr_write_b32 a16, v112\nv_accvgpr_write_b32 a17, v113\nQmvaa 16,112,232\nv_accvgpr_write_b32 a18, v114\n"
"v_accvgpr_write_b32 a19, v115\nQmvaa 16,116,200\nv_fma_f32 v144, v144, s72, v52\nv_fma_f32 v145, v145, s72, v52\nQmvaa 20,120,232\nv_fma_f32 v146, v146, s72, v52\nv_fma_f32 v147, v147, s72, v52\nQmvaa 20,124,200\n"
"v_fma_f32 v148, v148, s72, v52\nv_fma_f32 v149, v149, s72, v52\nQmvaa 24,128,232\nv_fma_f32 v150, v150, s72, v52\nv_fma_f32 v151, v151, s72, v52\nQmvaa 24,132,200\nQk 144,144,145\nQk 145,146,147\n"
"Qmvaa 28,136,232\nQk 146,148,149\nQk 147,150,151\nQmvaa 28,140,200\nv_accvgpr_write_b32 a32, v144\nv_accvgpr_write_b32 a33, v145\ns_waitcnt lgkmcnt(0)\nQmvaa 96,144,208\n"
"v_accvgpr_write_b32 a34, v146\nv_accvgpr_write_b32 a35, v147\nQmvaa 96,148,176\nv_fma_f32 v152, v152, s72, v52\nv_fma_f32 v153, v153, s72, v52\nQmvaa 100,152,208\nv_fma_f32 v154, v154, s72, v52\nv_fma_f32 v155, v155, s72, v52\n"
"Qmvaa 100,156,176\nv_fma_f32 v156, v156, s72, v52\nv_fma_f32 v157, v157, s72, v52\nQmvaa 104,160,208\nv_fma_f32 v158, v158, s72, v52\nv_fma_f32 v159, v159, s72, v52\nQmvaa 104,164,176\nQk 152,152,153\n"
"Qk 153,154,155\nQmvaa 108,168,208\nQk 154,156,157\nQk 155,158,159\nQmvaa 108,172,176\ns_waitcnt lgkmcnt(0)\nv_accvgpr_write_b32 a36, v152\nQmvaa 140,168,212\n"
"s_waitcnt vmcnt(36)\ns_barrier\nQmvaa 140,172,180\nQda 112,68,0\ns_add_u32 m0, s61, 66048\nQmvaa 136,160,212\nbuffer_load_dwordx4 v69, s[40:43], s60 offen nt lds\nQmvaa 136,164,180\n"
"Qda 116,68,512\nv_accvgpr_write_b32 a37, v153\nQmvaa 132,152,212\nv_accvgpr_write_b32 a38, v154\nv_accvgpr_write_b32 a39, v155\nQmvaa 132,156,180\nQda 120,68,256\nv_fma_f32 v184, v184, s72, v52\n"
"Qmvaa 128,144,212\nbuffer_load_dwordx4 v69, s[40:43], s60 offen offset:1024 nt lds\nQmvaa 128,148,180\nQda 124,68,768\nv_fma_f32 v185, v185, s72, v52\nQmvaa 160,144,216\nv_fma_f32 v186, v186, s72, v52\nv_fma_f32 v187, v187, s72, v52\n"
"Qmvaa 160,148,184\nQda 128,68,1024\nv_fma_f32 v188, v188, s72, v52\nQmvaa 164,152,216\nbuffer_load_dwordx4 v69, s[40:43], s90 offen offset:2048 nt lds\nQmvaa 164,156,184\nQda 132,68,1536\nv_fma_f32 v189, v189, s72, v52\n"
"Qmvaa 168,160,216\nv_fma_f32 v190, v190, s72, v52\nv_fma_f32 v191, v191, s72, v52\n.Lpg4:\ns_cbranch_execz .Ltr9\nQmvaa 168,164,184\nQda 136,68,1280\nQk 184,184,185\n"
"Qmvaa 172,168,216\nbuffer_load_dwordx4 v69, s[40:43], s90 offen offset:3072 nt lds\nQmvaa 172,172,184\nQda 140,68,1792\nQk 185,186,187\nQmvaa 204,168,220\nQk 186,188,189\nQk 187,190,191\n"
"Qmvaa 204,172,188\ns_add_u32 m0, s64, 66048\nv_accvgpr_write_b32 a52, v184\nQmvaa 200,160,220\nbuffer_load_dwordx4 v72, s[44:47], s60 offen nt lds\nQmvaa 200,164,188\nv_accvgpr_write_b32 a53, v185\nv_accvgpr_write_b32 a54, v186\n"
"Qmvaa 196,152,220\nv_accvgpr_write_b32 a55, v187\nv_fma_f32 v176, v176, s72, v52\nQmvaa 196,156,188\nv_fma_f32 v177, v177, s72, v52\nv_fma_f32 v178, v178, s72, v52\nQmvaa 192,144,220\nbuffer_load_dwordx4 v72, s[44:47], s60 offen offset:1024 nt lds\n"
"Qmvaa 192,148,188\nv_fma_f32 v179, v179, s72, v52\nv_fma_f32 v180, v180, s72, v52\nQmvaa 224,144,224\nv_fma_f32 v181, v181, s72, v52\nv_fma_f32 v182, v182, s72, v52\nQmvaa 224,148,192\nv_fma_f32 v183, v183, s72, v52\n"
"Qk 176,176,177\nQmvaa 228,152,224\nbuffer_load_dwordx4 v72, s[44:47], s90 offen offset:2048 nt lds\nQmvaa 228,156,192\nQk 177,178,179\nQk 178,180,181\nQmvaa 232,160,224\nQk 179,182,183\n"
"v_accvgpr_write_b32 a48, v176\nQmvaa 232,164,192\nv_accvgpr_write_b32 a49, v177\nv_accvgpr_write_b32 a50, v178\nQmvaa 236,168,224\nbuffer_load_dwordx4 v72, s[44:47], s90 offen offset:3072 nt lds\ns_add_u32 s65, s65, 2048\ns_xor_b32 s60, s65, %1\n"
"s_add_u32 s90, s60, 260096\nQmvaa 236,172,192\nv_accvgpr_write_b32 a51, v179\nv_fma_f32 v208, v208, s72, v52\nQmvaa 12,168,228\nv_fma_f32 v209, v209, s72, v52\nv_fma_f32 v210, v210, s72, v52\nQmvaa 12,172,196\n"
"v_fma_f32 v211, v211, s72, v52\nv_fma_f32 v212, v212, s72, v52\nQmvaa 8,160,228\nv_fma_f32 v213, v213, s72, v52\nv_fma_f32 v214, v214, s72, v52\nQmvaa 8,164,196\nv_fma_f32 v215, v215, s72, v52\nQk 208,208,209\n"
"Qmvaa 4,152,228\nQk 209,210,211\nQk 210,212,213\nQmvaa 4,156,196\nQk 211,214,215\nv_accvgpr_write_b32 a64, v208\nQmvaa 0,144,228\nv_accvgpr_write_b32 a65, v209\n"
"v_accvgpr_write_b32 a66, v210\nQmvaa 0,148,196\nv_accvgpr_write_b32 a67, v211\nv_fma_f32 v216, v216, s72, v52\nQmvaa 32,144,232\nv_fma_f32 v217, v217, s72, v52\nv_fma_f32 v218, v218, s72, v52\nQmvaa 32,148,200\n"
"v_fma_f32 v219, v219, s72, v52\nv_fma_f32 v220, v220, s72, v52\nQmvaa 36,152,232\nv_fma_f32 v221, v221, s72, v52\nv_fma_f32 v222, v222, s72, v52\nQmvaa 36,156,200\nv_fma_f32 v223, v223, s72, v52\nQk 216,216,217\n"
"Qmvaa 40,160,232\nQk 217,218,219\nQk 218,220,221\nQmvaa 40,164,200\nQk 219,222,223\nv_accvgpr_write_b32 a68, v216\nQmvaa 44,168,232\nv_accvgpr_write_b32 a69, v217\n"
"v_accvgpr_write_b32 a70, v218\nQmvaa 44,172,200\nv_accvgpr_write_b32 a71, v219\ns_nop 0\nQta 148,70,128\nQta 150,70,1152\nQta 156,70,384\nQta 158,70,1408\n"
"Qta 164,70,640\nQta 166,70,1664\nQta 172,70,896\nQta 174,70,1920\nv_pk_fma_f32 v[96:97], v[96:97], s[72:73], v[52:53] op_sel_hi:[1,0,0]\nv_pk_fma_f32 v[98:99], v[98:99], s[72:73], v[52:53] op_sel_hi:[1,0,0]\nv_pk_fma_f32 v[100:101], v[100:101], s[72:73], v[52:53] op_sel_hi:[1,0,0]\nv_pk_fma_f32 v[102:103], v[102:103], s[72:73], v[52:53] op_sel_hi:[1,0,0]\n"
"v_mfma_f32_16x16x4_4b_bf16 v[80:95], v[66:67], v[58:59], 0\nQk 96,96,97\nQk 97,98,99\nQk 98,100,101\nQk 99,102,103\nv_accvgpr_write_b32 a8, v96\nv_accvgpr_write_b32 a9, v97\nv_accvgpr_write_b32 a10, v98\n"
"v_accvgpr_write_b32 a11, v99\nv_pk_fma_f32 v[104:105], v[104:105], s[72:73], v[52:53] op_sel_hi:[1,0,0]\nv_pk_fma_f32 v[106:107], v[106:107], s[72:73], v[52:53] op_sel_hi:[1,0,0]\nv_pk_fma_f32 v[108:109], v[108:109], s[72:73], v[52:53] op_sel_hi:[1,0,0]\nv_pk_fma_f32 v[110:111], v[110:111], s[72:73], v[52:53] op_sel_hi:[1,0,0]\nv_mfma_f32_16x16x4_4b_bf16 v[112:127], v[66:67], v[58:59], 0\nQk 104,104,105\nQk 105,106,107\n"
"Qk 106,108,109\nQk 107,110,111\nv_accvgpr_write_b32 a12, v104\nv_accvgpr_write_b32 a13, v105\nv_accvgpr_write_b32 a14, v106\nv_accvgpr_write_b32 a15, v107\nv_pk_fma_f32 v[128:129], v[128:129], s[72:73], v[52:53] op_sel_hi:[1,0,0]\nv_pk_fma_f32 v[130:131], v[130:131], s[72:73], v[52:53] op_sel_hi:[1,0,0]\n"
"v_pk_fma_f32 v[132:133], v[132:133], s[72:73], v[52:53] op_sel_hi:[1,0,0]\nv_pk_fma_f32 v[134:135], v[134:135], s[72:73], v[52:53] op_sel_hi:[1,0,0]\nv_mfma_f32_16x16x4_4b_bf16 v[144:159], v[66:67], v[58:59], 0\nQk 128,128,129\nQk 129,130,131\nQk 130,132,133\nQk 131,134,135\nv_accvgpr_write_b32 a24, v128\n"
"v_accvgpr_write_b32 a25, v129\nv_accvgpr_write_b32 a26, v130\nv_accvgpr_write_b32 a27, v131\nv_pk_fma_f32 v[136:137], v[136:137], s[72:73], v[52:53] op_sel_hi:[1,0,0]\nv_pk_fma_f32 v[138:139], v[138:139], s[72:73], v[52:53] op_sel_hi:[1,0,0]\nv_pk_fma_f32 v[140:141], v[140:141], s[72:73], v[52:53] op_sel_hi:[1,0,0]\nv_pk_fma_f32 v[142:143], v[142:143], s[72:73], v[52:53] op_sel_hi:[1,0,0]\nv_mfma_f32_16x16x4_4b_bf16 v[176:191], v[66:67], v[58:59], 0\n"
"Qk 136,136,137\nQk 137,138,139\nQk 138,140,141\nQk 139,142,143\nv_accvgpr_write_b32 a28, v136\nv_accvgpr_write_b32 a29, v137\nv_accvgpr_write_b32 a30, v138\nv_accvgpr_write_b32 a31, v139\n"
"v_pk_fma_f32 v[160:161], v[160:161], s[72:73], v[52:53] op_sel_hi:[1,0,0]\nv_pk_fma_f32 v[162:163], v[162:163], s[72:73], v[52:53] op_sel_hi:[1,0,0]\nv_pk_fma_f32 v[164:165], v[164:165], s[72:73], v[52:53] op_sel_hi:[1,0,0]\nv_pk_fma_f32 v[166:167], v[166:167], s[72:73], v[52:53] op_sel_hi:[1,0,0]\nv_mfma_f32_16x16x4_4b_bf16 v[208:223], v[66:67], v[58:59], 0\nQk 160,160,161\nQk 161,162,163\nQk 162,164,165\n"
"Qk 163,166,167\nv_accvgpr_write_b32 a40, v160\nv_accvgpr_write_b32 a41, v161\nv_accvgpr_write_b32 a42, v162\nv_accvgpr_write_b32 a43, v163\nv_pk_fma_f32 v[168:169], v[168:169], s[72:73], v[52:53] op_sel_hi:[1,0,0]\nv_pk_fma_f32 v[170:171], v[170:171], s[72:73], v[52:53] op_sel_hi:[1,0,0]\nv_pk_fma_f32 v[172:173], v[172:173], s[72:73], v[52:53] op_sel_hi:[1,0,0]\n"
"v_pk_fma_f32 v[174:175], v[174:175], s[72:73], v[52:53] op_sel_hi:[1,0,0]\nv_mfma_f32_16x16x4_4b_bf16 a[208:223], v[66:67], v[58:59], 0\nQk 168,168,169\nQk 169,170,171\nQk 170,172,173\nQk 171,174,175\nv_accvgpr_write_b32 a44, v168\nv_accvgpr_write_b32 a45, v169\n"
"v_accvgpr_write_b32 a46, v170\nv_accvgpr_write_b32 a47, v171\nv_pk_fma_f32 v[192:193], v[192:193], s[72:73], v[52:53] op_sel_hi:[1,0,0]\nv_pk_fma_f32 v[194:195], v[194:195], s[72:73], v[52:53] op_sel_hi:[1,0,0]\nv_pk_fma_f32 v[196:197], v[196:197], s[72:73], v[52:53] op_sel_hi:[1,0,0]\nv_pk_fma_f32 v[198:199], v[198:199], s[72:73], v[52:53] op_sel_hi:[1,0,0]\nv_mfma_f32_16x16x4_4b_bf16 a[224:239], v[66:67], v[58:59], 0\nQk 192,192,193\n"
"Qk 193,194,195\nQk 194,196,197\nQk 195,198,199\nv_accvgpr_write_b32 a56, v192\nv_accvgpr_write_b32 a57, v193\nv_accvgpr_write_b32 a58, v194\nv_accvgpr_write_b32 a59, v195\nv_pk_fma_f32 v[200:201], v[200:201], s[72:73], v[52:53] op_sel_hi:[1,0,0]\n"
"v_pk_fma_f32 v[202:203], v[202:203], s[72:73], v[52:53] op_sel_hi:[1,0,0]\nv_pk_fma_f32 v[204:205], v[204:205], s[72:73], v[52:53] op_sel_hi:[1,0,0]\nv_pk_fma_f32 v[206:207], v[206:207], s[72:73], v[52:53] op_sel_hi:[1,0,0]\nv_mfma_f32_16x16x4_4b_bf16 a[240:255], v[66:67], v[58:59], 0\nQk 200,200,201\nQk 201,202,203\nQk 202,204,205\nQk 203,206,207\n"
"v_accvgpr_write_b32 a60, v200\nv_accvgpr_write_b32 a61, v201\nv_accvgpr_write_b32 a62, v202\nv_accvgpr_write_b32 a63, v203\nv_pk_fma_f32 v[224:225], v[224:225], s[72:73], v[52:53] op_sel_hi:[1,0,0]\nv_pk_fma_f32 v[226:227], v[226:227], s[72:73], v[52:53] op_sel_hi:[1,0,0]\nv_pk_fma_f32 v[228:229], v[228:229], s[72:73], v[52:53] op_sel_hi:[1,0,0]\nv_pk_fma_f32 v[230:231], v[230:231], s[72:73], v[52:53] op_sel_hi:[1,0,0]\n"
"v_mfma_f32_16x16x4_4b_bf16 v[96:111], v[66:67], v[58:59], 0\nQk 224,224,225\nQk 225,226,227\nQk 226,228,229\nQk 227,230,231\nv_accvgpr_write_b32 a72, v224\nv_accvgpr_write_b32 a73, v225\nv_accvgpr_write_b32 a74, v226\n"
"v_accvgpr_write_b32 a75, v227\nv_pk_fma_f32 v[232:233], v[232:233], s[72:73], v[52:53] op_sel_hi:[1,0,0]\nv_pk_fma_f32 v[234:235], v[234:235], s[72:73], v[52:53] op_sel_hi:[1,0,0]\nv_pk_fma_f32 v[236:237], v[236:237], s[72:73], v[52:53] op_sel_hi:[1,0,0]\nv_pk_fma_f32 v[238:239], v[238:239], s[72:73], v[52:53] op_sel_hi:[1,0,0]\nv_mfma_f32_16x16x4_4b_bf16 v[128:143], v[66:67], v[58:59], 0\nQk 232,232,233\nQk 233,234,235\n"
"Qk 234,236,237\nQk 235,238,239\nv_accvgpr_write_b32 a76, v232\nv_accvgpr_write_b32 a77, v233\nv_accvgpr_write_b32 a78, v234\nv_accvgpr_write_b32 a79, v235\nv_pk_fma_f32 v[240:241], v[240:241], s[72:73], v[52:53] op_sel_hi:[1,0,0]\nv_pk_fma_f32 v[242:243], v[242:243], s[72:73], v[52:53] op_sel_hi:[1,0,0]\n"
"v_pk_fma_f32 v[244:245], v[244:245], s[72:73], v[52:53] op_sel_hi:[1,0,0]\nv_pk_fma_f32 v[246:247], v[246:247], s[72:73], v[52:53] op_sel_hi:[1,0,0]\nv_mfma_f32_16x16x4_4b_bf16 v[160:175], v[66:67], v[58:59], 0\nQk 240,240,241\nQk 241,242,243\nQk 242,244,245\nQk 243,246,247\nv_accvgpr_write_b32 a80, v240\n"
"v_accvgpr_write_b32 a81, v241\nv_accvgpr_write_b32 a82, v242\nv_accvgpr_write_b32 a83, v243\nv_pk_fma_f32 v[248:249], v[248:249], s[72:73], v[52:53] op_sel_hi:[1,0,0]\nv_pk_fma_f32 v[250:251], v[250:251], s[72:73], v[52:53] op_sel_hi:[1,0,0]\nv_pk_fma_f32 v[252:253], v[252:253], s[72:73], v[52:53] op_sel_hi:[1,0,0]\nv_pk_fma_f32 v[254:255], v[254:255], s[72:73], v[52:53] op_sel_hi:[1,0,0]\nv_mfma_f32_16x16x4_4b_bf16 v[192:207], v[66:67], v[58:59], 0\n"
"Qk 248,248,249\nQk 249,250,251\nQk 250,252,253\nQk 251,254,255\nv_accvgpr_write_b32 a84, v248\nv_accvgpr_write_b32 a85, v249\nv_accvgpr_write_b32 a86, v250\nv_accvgpr_write_b32 a87, v251\n"
"v_pk_fma_f32 v[0:1], v[0:1], s[72:73], v[52:53] op_sel_hi:[1,0,0]\nv_pk_fma_f32 v[2:3], v[2:3], s[72:73], v[52:53] op_sel_hi:[1,0,0]\nv_pk_fma_f32 v[4:5], v[4:5], s[72:73], v[52:53] op_sel_hi:[1,0,0]\nv_pk_fma_f32 v[6:7], v[6:7], s[72:73], v[52:53] op_sel_hi:[1,0,0]\nv_mfma_f32_16x16x4_4b_bf16 v[224:239], v[66:67], v[58:59], 0\nQk 0,0,1\nQk 1,2,3\nQk 2,4,5\n"
"Qk 3,6,7\nv_accvgpr_write_b32 a88, v0\nv_accvgpr_write_b32 a89, v1\nv_accvgpr_write_b32 a90, v2\nv_accvgpr_write_b32 a91, v3\nv_pk_fma_f32 v[8:9], v[8:9], s[72:73], v[52:53] op_sel_hi:[1,0,0]\nv_pk_fma_f32 v[10:11], v[10:11], s[72:73], v[52:53] op_sel_hi:[1,0,0]\nv_pk_fma_f32 v[12:13], v[12:13], s[72:73], v[52:53] op_sel_hi:[1,0,0]\n"
"v_pk_fma_f32 v[14:15], v[14:15], s[72:73], v[52:53] op_sel_hi:[1,0,0]\nv_mfma_f32_16x16x4_4b_bf16 v[240:255], v[66:67], v[58:59], 0\nQk 8,8,9\nQk 9,10,11\nQk 10,12,13\nQk 11,14,15\nv_accvgpr_write_b32 a92, v8\nv_accvgpr_write_b32 a93, v9\n"
"v_accvgpr_write_b32 a94, v10\nv_accvgpr_write_b32 a95, v11\nv_pk_fma_f32 v[16:17], v[16:17], s[72:73], v[52:53] op_sel_hi:[1,0,0]\nv_pk_fma_f32 v[18:19], v[18:19], s[72:73], v[52:53] op_sel_hi:[1,0,0]\nv_pk_fma_f32 v[20:21], v[20:21], s[72:73], v[52:53] op_sel_hi:[1,0,0]\nv_pk_fma_f32 v[22:23], v[22:23], s[72:73], v[52:53] op_sel_hi:[1,0,0]\nv_mfma_f32_16x16x4_4b_bf16 a[176:191], v[54:55], v[54:55], 0\nQk 16,16,17\n"
"Qk 17,18,19\nQk 18,20,21\nQk 19,22,23\nv_accvgpr_write_b32 a96, v16\nv_accvgpr_write_b32 a97, v17\nv_accvgpr_write_b32 a98, v18\nv_accvgpr_write_b32 a99, v19\nv_pk_fma_f32 v[24:25], v[24:25], s[72:73], v[52:53] op_sel_hi:[1,0,0]\n"
"v_pk_fma_f32 v[26:27], v[26:27], s[72:73], v[52:53] op_sel_hi:[1,0,0]\nv_pk_fma_f32 v[28:29], v[28:29], s[72:73], v[52:53] op_sel_hi:[1,0,0]\nv_pk_fma_f32 v[30:31], v[30:31], s[72:73], v[52:53] op_sel_hi:[1,0,0]\nv_mfma_f32_16x16x4_4b_bf16 a[188:203], v[54:55], v[54:55], 0\nQk 24,24,25\nQk 25,26,27\nQk 26,28,29\nQk 27,30,31\n"
"v_accvgpr_write_b32 a100, v24\nv_accvgpr_write_b32 a101, v25\nv_accvgpr_write_b32 a102, v26\nv_accvgpr_write_b32 a103, v27\nv_pk_fma_f32 v[32:33], v[32:33], s[72:73], v[52:53] op_sel_hi:[1,0,0]\nv_pk_fma_f32 v[34:35], v[34:35], s[72:73], v[52:53] op_sel_hi:[1,0,0]\nv_pk_fma_f32 v[36:37], v[36:37], s[72:73], v[52:53] op_sel_hi:[1,0,0]\nv_pk_fma_f32 v[38:39], v[38:39], s[72:73], v[52:53] op_sel_hi:[1,0,0]\n"
"Qk 32,32,33\nQk 33,34,35\nQk 34,36,37\nQk 35,38,39\nv_accvgpr_write_b32 a104, v32\nv_accvgpr_write_b32 a105, v33\nv_accvgpr_write_b32 a106, v34\nv_accvgpr_write_b32 a107, v35\n"
"v_pk_fma_f32 v[40:41], v[40:41], s[72:73], v[52:53] op_sel_hi:[1,0,0]\nv_pk_fma_f32 v[42:43], v[42:43], s[72:73], v[52:53] op_sel_hi:[1,0,0]\nv_pk_fma_f32 v[44:45], v[44:45], s[72:73], v[52:53] op_sel_hi:[1,0,0]\nv_pk_fma_f32 v[46:47], v[46:47], s[72:73], v[52:53] op_sel_hi:[1,0,0]\nQk 40,40,41\nQk 41,42,43\nQk 42,44,45\nQk 43,46,47\n"
"v_accvgpr_write_b32 a108, v40\nv_accvgpr_write_b32 a109, v41\nv_accvgpr_write_b32 a110, v42\nv_accvgpr_write_b32 a111, v43\ns_waitcnt lgkmcnt(8)\nQzvaa 0,112,0\nQmvaa 0,116,4\ns_memrealtime s[74:75]\n"
"s_memtime s[68:69]\nQmvaa 0,120,8\nQmvaa 0,124,12\nQzvaa 4,140,12\nQmvaa 4,136,8\nQmvaa 4,132,4\nQmvaa 4,128,0\ns_nop 3\n"
"v_max3_f32 v66, v0, v1, v2\ns_nop 2\nv_max3_f32 v66, v66, v3, v4\nv_max3_f32 v66, v66, v5, v6\nv_max_f32 v66, v66, v7\nQzvaa 8,112,16\ns_waitcnt lgkmcnt(0)\nv_mov_b32 v67, v66\n"
"Qmvaa 8,116,20\ns_sub_u32 s68, s68, s80\ns_sub_u32 s69, s74, s82\nQmvaa 8,120,24\ns_mul_i32 s69, s69, 73\ns_lshl_b32 s68, s68, 2\nQmvaa 8,124,28\ns_sub_u32 s75, s67, 1\n"
"s_cmp_gt_u32 s68, s69\ns_cselect_b32 s68, s75, 0\ns_cselect_b32 s31, 6, 0\ns_add_u32 s31, s31, 238\nQzvaa 12,140,28\ns_and_b32 s75, s68, 30\ns_add_u32 s77, s74, 55\nQmvaa 12,136,24\n"
"s_add_u32 s77, s77, s75\nv_permlane32_swap_b32 v66, v67\nQmvaa 12,132,20\nv_max_f32 v66, v66, v67\nv_mov_b32 v67, v66\nQmvaa 12,128,16\ns_nop 0\nv_permlane16_swap_b32 v66, v67\n"
"v_max_f32 v66, v66, v67\ns_nop 1\nv_min_f32_dpp v66, v66, v66 row_ror:8 row_mask:0xf bank_mask:0xf\ns_nop 1\nv_min_f32_dpp v66, v66, v66 row_ror:4 row_mask:0xf bank_mask:0xf\ns_nop 1\nv_min_f32_dpp v66, v66, v66 row_ror:2 row_mask:0xf bank_mask:0xf\ns_nop 1\n"
"v_min_f32_dpp v66, v66, v66 row_ror:1 row_mask:0xf bank_mask:0xf\nv_sub_f32 v56, 0xc1a00000, v66\nv_mov_b32 v57, v56\nv_mov_b32 v58, v56\nv_mov_b32 v59, v56\nQa 0,0,56\nQa 1,1,56\nQa 2,2,56\n"
"Qa 3,3,56\nQe 0\nQe 1\nQe 2\nQe 3\nQk 16,0,1\nQk 17,2,3\nQcvaa 0,112,32,56\n"
"Qa 4,4,56\nQa 5,5,56\nQmvaa 0,116,36\nQa 6,6,56\nQa 7,7,56\nQmvaa 0,120,40\nQe 4\nQe 5\n"
"Qe 6\nQe 7\nQmvaa 0,124,44\nQk 18,4,5\nQk 19,6,7\nQcvaa 4,140,44,56\nQa 8,8,56\nQa 9,9,56\n"
"Qmvaa 4,136,40\nQa 10,10,56\nQa 11,11,56\nQmvaa 4,132,36\nQa 12,12,56\nQa 13,13,56\nQmvaa 4,128,32\ns_waitcnt lgkmcnt(0)\n"
"Qa 14,14,56\nQmvva 96,16,148\nQa 15,15,56\nQmvva 100,16,156\nQe 8\nQmvva 104,16,164\nQe 9\nQe 10\n"
"Qe 11\nQe 12\nQe 13\nQe 14\nQe 15\nQmvva 108,16,172\nQk 20,8,9\nQk 21,10,11\n"
"Qk 22,12,13\nQk 23,14,15\ns_nop 1\nQmvva 140,20,172\nQe 0\nQmvva 136,20,164\nQe 1\nQmvva 132,20,156\n"
"Qe 2\nQmvva 128,20,148\nQe 3\nv_smfmac_f32_16x16x64_bf16 a[176:179], a[204:207], v[16:23], v77\nQcvaa 8,112,48,56\ns_add_u32 m0, s61, 99072\nQk 24,0,1\nQmvaa 8,116,52\n"
"buffer_load_dwordx4 v69, s[40:43], s60 offen nt lds\nQmvaa 8,120,56\nQk 25,2,3\nQmvaa 8,124,60\ns_or_b32 s65, s65, 0x1d8000\ns_add_u32 s65, s65, 2048\nQcvaa 12,140,60,56\ns_andn2_b32 s65, s65, 0x1d8000\n"
"Qmvaa 12,136,56\nQe 4\nQmvaa 12,132,52\nbuffer_load_dwordx4 v69, s[40:43], s60 offen offset:1024 nt lds\nQmvaa 12,128,48\nQe 5\nQcvaa 0,128,64,56\ns_memrealtime s[74:75]\n"
"Qk 26,4,5\nQmvaa 0,132,68\nQe 6\nQmvaa 0,136,72\nQe 7\nQmvaa 0,140,76\nbuffer_load_dwordx4 v69, s[40:43], s90 offen offset:2048 nt lds\nQk 27,6,7\n"
"Qcvaa 4,124,76,56\nQe 8\nQmvaa 4,120,72\nQe 9\nQmvaa 4,116,68\nQe 10\nQmvaa 4,112,64\nQe 11\n"
"buffer_load_dwordx4 v69, s[40:43], s90 offen offset:3072 nt lds\nQmvva 160,24,148\nQe 12\nQmvva 164,24,156\nQe 13\ns_add_u32 m0, s64, 99072\nQmvva 168,24,164\nbuffer_load_dwordx4 v72, s[44:47], s60 offen nt lds\n"
"Qe 14\nQe 15\nQmvva 172,24,172\nQk 28,8,9\nQk 29,10,11\nQk 30,12,13\nQk 31,14,15\ns_nop 1\n"
"Qmvva 204,28,172\nQe 0\nbuffer_load_dwordx4 v72, s[44:47], s60 offen offset:1024 nt lds\nQmvva 200,28,164\nQe 1\nQmvva 196,28,156\nQe 2\nQmvva 192,28,148\n"
"buffer_load_dwordx4 v72, s[44:47], s90 offen offset:2048 nt lds\nv_smfmac_f32_16x16x64_bf16 a[180:183], a[204:207], v[24:31], v77\nQcvaa 8,112,80,56\nQe 3\nQmvaa 8,116,84\nQk 34,0,1\nQk 35,2,3\nQmvaa 8,120,88\n"
"s_waitcnt vmcnt(15)\ns_barrier\nQmvaa 8,124,92\nbuffer_load_dwordx4 v72, s[44:47], s90 offen offset:3072 nt lds\nQcvaa 12,140,92,56\ns_waitcnt lgkmcnt(0)\nQmvaa 12,136,88\ns_cmp_gt_u32 s77, s74\n"
"s_cbranch_scc1 .Lsp0\n.Lrp0:\nQmvaa 12,132,84\ns_and_b32 s75, s68, 35\ns_add_u32 s77, s74, 85\nQmvaa 12,128,80\ns_add_u32 s77, s77, s75\ns_xor_b32 s60, s65, %1\n"
"s_add_u32 s90, s60, 260096\nQcvaa 0,128,96,56\nQda 128,68,34048\nQmvaa 0,132,100\nQda 132,68,34560\nQmvaa 0,136,104\nQda 136,68,34304\nQe 4\n"
"Qe 5\nQe 6\nQe 7\nQmvaa 0,140,108\nQda 140,68,34816\nQk 32,4,5\n.Lpg5:\ns_cbranch_execz .Ltr9\n"
"Qk 33,6,7\nQcvaa 4,124,108,56\nQda 124,68,33792\nQmvaa 4,120,104\nQda 120,68,33280\nQmvaa 4,116,100\nQda 116,68,33536\nQmvaa 4,112,96\n"
"Qda 112,68,33024\nQmvva 224,32,148\nQe 8\nQmvva 228,32,156\nQe 9\nQmvva 232,32,164\nQe 10\nQe 11\n"
"Qmvva 236,32,172\ns_waitcnt lgkmcnt(0)\nQk 36,8,9\nQk 37,10,11\nQcvaa 8,112,0,56\nQe 12\nQmvaa 8,116,4\nv_add_u32 v68, 66048, v68\n"
"Qmvaa 8,120,8\nQe 13\nQe 14\nQe 15\nQmvaa 8,124,12\nQk 38,12,13\nQk 39,14,15\nQcvaa 12,140,12,56\n"
"Qe 0\nQmvaa 12,136,8\nQe 1\nQmvaa 12,132,4\nQe 2\nQmvaa 12,128,0\nQe 3\nv_smfmac_f32_16x16x64_bf16 a[184:187], a[204:207], v[32:39], v77\n"
"Qmava 208,36,148\nQe 4\nQmava 212,36,156\nQe 5\nQmava 216,36,164\nQe 6\nQe 8\nQe 7\n"
"Qmava 220,36,172\nQe 9\nQk 42,0,1\nQk 43,2,3\nQk 40,4,5\nQk 41,6,7\nQta 168,70,768\nQta 170,70,1792\n"
"Qmava 252,40,172\nQe 10\nQta 172,70,33792\nQta 174,70,34816\nQmava 248,40,164\nQe 11\nQta 160,70,512\nQta 162,70,1536\n"
"Qmava 244,40,156\nQk 44,8,9\nQk 45,10,11\nQta 164,70,33536\nQta 166,70,34560\nQmava 240,40,148\nQe 12\nQcvaa 0,112,16,56\n"
"Qta 152,70,256\nQta 154,70,1280\nQmvaa 0,116,20\nQe 13\nQmvaa 0,120,24\nQta 156,70,33280\nQta 158,70,34304\nQmvaa 0,124,28\n"
"Qe 14\nQcvaa 4,140,28,56\nQta 144,70,0\nQta 146,70,1024\nQmvaa 4,136,24\nQe 15\nQmvaa 4,132,20\nQta 148,70,33024\n"
"Qta 150,70,34048\nQmvaa 4,128,16\nQk 46,12,13\nQk 47,14,15\nQcvaa 8,128,32,56\nv_add_u32 v75, 1, v75\nQmvaa 8,132,36\ns_waitcnt vmcnt(8)\n"
"ds_write_b32 v73, v75\nQmvaa 8,136,40\nQe 0\nQmvaa 8,140,44\nQe 1\nQcvaa 12,124,44,56\nQe 2\nQmvaa 12,120,40\n"
"Qe 3\nQmvaa 12,116,36\nQe 4\nQmvaa 12,112,32\ns_waitcnt lgkmcnt(1)\nQk 48,0,1\nv_smfmac_f32_16x16x64_bf16 a[188:191], a[204:207], v[40:47], v77\nQmvva 80,16,144\n"
"Qe 5\nQmvva 80,44,148\nQe 6\nQmvva 84,16,152\nQe 7\nQmvva 84,44,156\nQk 49,2,3\nQk 50,4,5\n"
"Qmvva 88,16,160\nQk 51,6,7\nQmvva 88,44,164\nQe 8\nQmvva 92,16,168\nQe 9\nQmvva 92,44,172\nQe 10\n"
"Qmvva 124,20,168\nQe 11\nQmvva 124,48,172\nds_read_b32 v66, v74\nQk 54,8,9\nQmvva 120,20,160\nQk 55,10,11\nQmvva 120,48,164\n"
"Qe 12\nQmvva 116,20,152\nQe 13\nQmvva 116,48,156\nQe 14\nQmvva 112,20,144\ns_waitcnt lgkmcnt(0)\nv_cmp_gt_u32 vcc, v75, v66\n"
"Qmvva 112,48,148\ns_cbranch_vccnz .Lsa1\n.Lra1:\nQcvaa 0,112,48,56\ns_add_u32 m0, s61, 0\nQk 52,12,13\nQmvaa 0,116,52\nbuffer_load_dwordx4 v69, s[40:43], s60 offen nt lds\n"
"Qmvaa 0,120,56\nQe 15\nQmvaa 0,124,60\ns_add_u32 s65, s65, 2048\nQk 53,14,15\nQcvaa 4,140,60,56\nQmvaa 4,136,56\nQmvaa 4,132,52\n"
"buffer_load_dwordx4 v69, s[40:43], s60 offen offset:1024 nt lds\nQmvaa 4,128,48\nQcvaa 8,128,64,56\ns_memrealtime s[74:75]\nQmvaa 8,132,68\nQe 0\nQmvaa 8,136,72\nQe 1\n"
"Qmvaa 8,140,76\nbuffer_load_dwordx4 v69, s[40:43], s90 offen offset:2048 nt lds\nQcvaa 12,124,76,56\nQe 2\nQmvaa 12,120,72\nQe 3\nQmvaa 12,116,68\nQe 4\n"
"Qmvaa 12,112,64\nQe 5\nv_smfmac_f32_16x16x64_bf16 a[192:195], a[204:207], v[48:55], v77\nQmvva 144,24,144\nbuffer_load_dwordx4 v69, s[40:43], s90 offen offset:3072 nt lds\nQmvva 144,52,148\nQe 6\nQmvva 148,24,152\n"
"Qe 7\nQmvva 148,52,156\ns_add_u32 m0, s64, 0\nQk 20,0,1\nQmvva 152,24,160\nbuffer_load_dwordx4 v72, s[44:47], s60 offen nt lds\nQmvva 152,52,164\nQk 21,2,3\n"
"Qk 22,4,5\nQmvva 156,24,168\nQk 23,6,7\nQmvva 156,52,172\nQe 8\nQmvva 188,28,168\nQe 9\nQmvva 188,20,172\n"
"buffer_load_dwordx4 v72, s[44:47], s60 offen offset:1024 nt lds\nQmvva 184,28,160\nQe 10\nQmvva 184,20,164\nQe 11\nQmvva 180,28,152\nQk 26,8,9\nQk 27,10,11\n"
"Qmvva 180,20,156\nQe 12\nQmvva 176,28,144\nbuffer_load_dwordx4 v72, s[44:47], s90 offen offset:2048 nt lds\nQmvva 176,20,148\nQe 13\nQcvaa 0,112,80,56\nQe 14\n"
"Qmvaa 0,116,84\nQe 15\nQmvaa 0,120,88\nQk 24,12,13\nQk 25,14,15\nQmvaa 0,124,92\nbuffer_load_dwordx4 v72, s[44:47], s90 offen offset:3072 nt lds\nQcvaa 4,140,92,56\n"
"s_waitcnt lgkmcnt(0)\nQmvaa 4,136,88\ns_cmp_gt_u32 s77, s74\ns_cbranch_scc1 .Lsp1\n.Lrp1:\nQmvaa 4,132,84\ns_and_b32 s75, s68, 47\ns_add_u32 s77, s74, 108\n"
"Qmvaa 4,128,80\ns_add_u32 s77, s77, s75\ns_xor_b32 s60, s65, %1\ns_add_u32 s90, s60, 260096\nQcvaa 8,128,96,56\nQda 128,68,1024\ns_add_u32 s79, s79, 2030\nQmvaa 8,132,100\n"
"Qda 132,68,1536\ns_min_u32 s79, s79, s77\nQmvaa 8,136,104\nQda 136,68,1280\nQmvaa 8,140,108\nQda 140,68,1792\nQcvaa 12,124,108,56\nQda 124,68,768\n"
"Qmvaa 12,120,104\nQda 120,68,256\nQmvaa 12,116,100\nQda 116,68,512\nQmvaa 12,112,96\nQda 112,68,0\nv_smfmac_f32_16x16x64_bf16 a[196:199], a[204:207], v[20:27], v77\nQmvva 208,32,144\n"
"Qe 0\nQmvva 208,24,148\nQe 1\nQmvva 212,32,152\nQe 2\nQmvva 212,24,156\nQe 3\nQmvva 216,32,160\n"
"Qk 28,0,1\nQk 29,2,3\nQmvva 216,24,164\nQe 4\nQmvva 220,32,168\nQe 5\nQmvva 220,24,172\ns_waitcnt lgkmcnt(0)\n"
"Qk 30,4,5\nQcvaa 0,112,0,56\nQe 6\nQmvaa 0,116,4\nQe 7\nQmvaa 0,120,8\nQk 31,6,7\nQmvaa 0,124,12\n"
"Qe 8\nQcvaa 4,140,12,56\nQe 9\nQmvaa 4,136,8\nQe 10\nQmvaa 4,132,4\nQe 11\nQmvaa 4,128,0\n"
"Qe 12\nQmvva 240,36,144\nQe 13\nQmvva 240,28,148\nQe 14\nQmvva 244,36,152\nQe 15\nQmvva 244,28,156\n"
"Qk 34,8,9\nQk 35,10,11\nQmvva 248,36,160\nQk 32,12,13\nQk 33,14,15\nQmvva 248,28,164\nQe 0\nQmvva 252,36,168\n"
"Qe 1\nQmvva 252,28,172\nQe 2\nQmava 236,40,168\nQta 168,70,33920\nQta 170,70,34944\nQmava 236,32,172\nQe 3\n"
"Qmava 232,40,160\nQta 172,71,896\nQta 174,71,1920\nQmava 232,32,164\nQk 36,0,1\nQk 37,2,3\nQmava 228,40,152\nQta 160,70,33664\n"
"Qta 162,70,34688\nQmava 228,32,156\nQe 4\nQmava 224,40,144\nQta 164,71,640\nQta 166,71,1664\nQmava 224,32,148\nQe 5\n"
"v_smfmac_f32_16x16x64_bf16 a[200:203], a[204:207], v[28:35], v77\nQta 152,70,33408\nQta 154,70,34432\nQcvaa 8,112,16,56\nQe 6\nQmvaa 8,116,20\nQta 156,71,384\nQta 158,71,1408\n"
"Qmvaa 8,120,24\nQe 7\nQmvaa 8,124,28\nQta 144,70,33152\nQta 146,70,34176\nQcvaa 12,140,28,56\nQk 38,4,5\nQk 39,6,7\n"
"Qmvaa 12,136,24\nQta 148,71,128\nQta 150,71,1152\nQmvaa 12,132,20\ns_lshl_b32 s66, s67, 3\nQmvaa 12,128,16\n.Lhookret:\n.Lloop:\n"
"Qcvaa 0,128,32,56\nv_add_u32 v75, 1, v75\nQmvaa 0,132,36\ns_waitcnt vmcnt(8)\nds_write_b32 v73, v75\nQmvaa 0,136,40\nQe 8\nQmvaa 0,140,44\n"
"Qe 9\nQcvaa 4,124,44,56\nQe 10\nQmvaa 4,120,40\nQe 11\nQmvaa 4,116,36\nQe 12\nQmvaa 4,112,32\n"
"s_waitcnt lgkmcnt(1)\nQk 40,8,9\nQmvva 96,44,144\nQe 13\nQmvva 96,36,148\nQe 14\nQmvva 100,44,152\nQe 15\n"
"Qmvva 100,36,156\nQk 41,10,11\nQk 42,12,13\nQmvva 104,44,160\nQk 43,14,15\nQmvva 104,36,164\nQe 0\nQmvva 108,44,168\n"
"Qe 1\nQmvva 108,36,172\nQe 2\nQmvva 140,48,168\nQe 3\nQmvva 140,40,172\nQk 46,0,1\nQk 47,2,3\n"
"Qmvva 136,48,160\nds_read_b32 v66, v74\nQmvva 136,40,164\nQe 4\nQmvva 132,48,152\nQe 5\nQmvva 132,40,156\nQe 6\n"
"Qmvva 128,48,144\nQe 7\nQmvva 128,40,148\ns_waitcnt lgkmcnt(0)\nv_cmp_gt_u32 vcc, v75, v66\nv_smfmac_f32_16x16x64_bf16 a[176:179], a[204:207], v[36:43], v77\ns_cbranch_vccnz .Lsa2\n.Lra2:\n"
"Qcvaa 8,112,48,56\ns_add_u32 m0, s61, 33024\nQk 44,4,5\nQmvaa 8,116,52\nbuffer_load_dwordx4 v69, s[40:43], s60 offen nt lds\nQmvaa 8,120,56\nQk 45,6,7\nQmvaa 8,124,60\n"
"s_add_u32 s65, s65, 2048\nQcvaa 12,140,60,56\nQmvaa 12,136,56\nQmvaa 12,132,52\nbuffer_load_dwordx4 v69, s[40:43], s60 offen offset:1024 nt lds\nQmvaa 12,128,48\nQcvaa 0,128,64,56\ns_memrealtime s[74:75]\n"
"Qmvaa 0,132,68\nQe 8\nQmvaa 0,136,72\nQe 9\nQmvaa 0,140,76\nbuffer_load_dwordx4 v69, s[40:43], s90 offen offset:2048 nt lds\nQcvaa 4,124,76,56\nQe 10\n"
"Qmvaa 4,120,72\nQe 11\nQmvaa 4,116,68\nQe 12\nQmvaa 4,112,64\nQe 13\nQmvva 160,52,144\nbuffer_load_dwordx4 v69, s[40:43], s90 offen offset:3072 nt lds\n"
"Qmvva 160,44,148\nQe 14\nQmvva 164,52,152\nQe 15\nQmvva 164,44,156\nQk 48,8,9\nQk 49,10,11\nQmvva 168,52,160\n"
"s_add_u32 m0, s64, 33024\nQk 50,12,13\nQmvva 168,44,164\nbuffer_load_dwordx4 v72, s[44:47], s60 offen nt lds\nQmvva 172,52,168\nQk 51,14,15\nQmvva 172,44,172\nQe 0\n"
"Qmvva 204,20,168\nQe 1\nQmvva 204,48,172\nQe 2\nQmvva 200,20,160\nbuffer_load_dwordx4 v72, s[44:47], s60 offen offset:1024 nt lds\nQmvva 200,48,164\nQe 3\n"
"Qmvva 196,20,152\nQk 18,0,1\nQk 19,2,3\nQmvva 196,48,156\nQe 4\nQmvva 192,20,144\nQe 5\nQmvva 192,48,148\n"
"buffer_load_dwordx4 v72, s[44:47], s90 offen offset:2048 nt lds\nv_smfmac_f32_16x16x64_bf16 a[180:183], a[204:207], v[44:51], v77\nQcvaa 8,112,80,56\nQe 6\nQmvaa 8,116,84\nQe 7\nQmvaa 8,120,88\nQk 16,4,5\n"
"Qk 17,6,7\nQmvaa 8,124,92\nbuffer_load_dwordx4 v72, s[44:47], s90 offen offset:3072 nt lds\nQcvaa 12,140,92,56\ns_waitcnt lgkmcnt(0)\ns_cmp_gt_u32 s77, s74\nQmvaa 12,136,88\ns_cbranch_scc1 .Lsp2\n"
".Lrp2:\nQmvaa 12,132,84\ns_add_u32 s77, s74, 220\nQmvaa 12,128,80\ns_xor_b32 s60, s65, %1\ns_add_u32 s90, s60, 260096\nQcvaa 0,128,96,56\nQda 128,68,34048\n"
"s_add_u32 s79, s79, s31\nQmvaa 0,132,100\nQda 132,68,34560\ns_max_u32 s77, s77, s79\nQmvaa 0,136,104\nQda 136,68,34304\nQmvaa 0,140,108\nQda 140,68,34816\n"
"Qcvaa 4,124,108,56\nQda 124,68,33792\nQmvaa 4,120,104\nQda 120,68,33280\nQmvaa 4,116,100\nQda 116,68,33536\nQmvaa 4,112,96\nQda 112,68,33024\n"
"Qmvva 224,24,144\nQe 8\nQmvva 224,16,148\nQe 9\nQmvva 228,24,152\nQe 10\nQmvva 228,16,156\nQe 11\n"
"Qmvva 232,24,160\nQk 20,8,9\nQk 21,10,11\nQmvva 232,16,164\nQe 12\nQmvva 236,24,168\nQe 13\nQmvva 236,16,172\n"
"s_waitcnt lgkmcnt(0)\nQk 22,12,13\nQcvaa 8,112,0,56\nQe 14\nQmvaa 8,116,4\nv_subrev_u32 v68, 66048, v68\nQmvaa 8,120,8\nQe 15\n"
"Qmvaa 8,124,12\nQk 23,14,15\nQcvaa 12,140,12,56\nQe 0\nQmvaa 12,136,8\nQe 1\nQmvaa 12,132,4\nQe 2\n"
"Qmvaa 12,128,0\nQe 3\nv_smfmac_f32_16x16x64_bf16 a[184:187], a[204:207], v[16:23], v77\nQmava 208,28,144\nQe 4\nQmava 208,20,148\nQe 5\nQmava 212,28,152\n"
"Qe 6\nQmava 212,20,156\nQe 7\nQmava 216,28,160\nQk 26,0,1\nQk 27,2,3\nQmava 216,20,164\nQk 24,4,5\n"
"Qk 25,6,7\nQmava 220,28,168\nQe 8\nQmava 220,20,172\nQe 9\nQmava 252,32,168\nQta 168,71,768\nQta 170,71,1792\n"
"Qmava 252,24,172\nQe 10\nQmava 248,32,160\nQta 172,71,33792\nQta 174,71,34816\nQmava 248,24,164\nQe 11\nQmava 244,32,152\n"
"Qta 160,71,512\nQta 162,71,1536\nQmava 244,24,156\nQk 28,8,9\nQk 29,10,11\nQmava 240,32,144\nQta 164,71,33536\nQta 166,71,34560\n"
"Qmava 240,24,148\nQe 12\nQcvaa 0,112,16,56\nQta 152,71,256\nQta 154,71,1280\nQmvaa 0,116,20\nQe 13\nQmvaa 0,120,24\n"
"Qta 156,71,33280\nQta 158,71,34304\nQmvaa 0,124,28\nQe 14\nQcvaa 4,140,28,56\nQta 144,71,0\nQta 146,71,1024\nQmvaa 4,136,24\n"
"Qe 15\nQmvaa 4,132,20\nQta 148,71,33024\nQta 150,71,34048\nQmvaa 4,128,16\nQk 30,12,13\nQk 31,14,15\nQcvaa 8,128,32,56\n"
"v_add_u32 v75, 1, v75\nQmvaa 8,132,36\ns_waitcnt vmcnt(8)\nds_write_b32 v73, v75\nQmvaa 8,136,40\nQe 0\nQmvaa 8,140,44\nQe 1\n"
"Qcvaa 12,124,44,56\nQe 2\nQmvaa 12,120,40\nQe 3\nQmvaa 12,116,36\nQe 4\nQmvaa 12,112,32\ns_waitcnt lgkmcnt(1)\n"
"Qk 32,0,1\nv_smfmac_f32_16x16x64_bf16 a[188:191], a[204:207], v[24:31], v77\nQmvva 80,36,144\nQe 5\nQmvva 80,28,148\nQe 6\nQmvva 84,36,152\nQe 7\n"
"Qmvva 84,28,156\nQk 33,2,3\nQk 34,4,5\nQmvva 88,36,160\nQk 35,6,7\nQmvva 88,28,164\nQe 8\nQmvva 92,36,168\n"
"Qe 9\nQmvva 92,28,172\nQe 10\nQmvva 124,40,168\nQe 11\nQmvva 124,32,172\nds_read_b32 v66, v74\nQk 38,8,9\n"
"Qmvva 120,40,160\nQk 39,10,11\nQmvva 120,32,164\nQe 12\nQmvva 116,40,152\nQe 13\nQmvva 116,32,156\nQe 14\n"
"Qmvva 112,40,144\ns_waitcnt lgkmcnt(0)\nv_cmp_gt_u32 vcc, v75, v66\nQmvva 112,32,148\ns_cbranch_vccnz .Lsa3\n.Lra3:\nQcvaa 0,112,48,56\ns_add_u32 m0, s61, 66048\n"
"Qk 36,12,13\nQmvaa 0,116,52\nbuffer_load_dwordx4 v69, s[40:43], s60 offen nt lds\nQmvaa 0,120,56\nQe 15\nQmvaa 0,124,60\ns_add_u32 s65, s65, 2048\nQk 37,14,15\n"
"Qcvaa 4,140,60,56\nQmvaa 4,136,56\nQmvaa 4,132,52\nbuffer_load_dwordx4 v69, s[40:43], s60 offen offset:1024 nt lds\nQmvaa 4,128,48\nQcvaa 8,128,64,56\ns_nop 0\nQmvaa 8,132,68\n"
"Qe 0\nQmvaa 8,136,72\nQe 1\nQmvaa 8,140,76\nbuffer_load_dwordx4 v69, s[40:43], s90 offen offset:2048 nt lds\nQcvaa 12,124,76,56\nQe 2\nQmvaa 12,120,72\n"
"Qe 3\nQmvaa 12,116,68\nQe 4\nQmvaa 12,112,64\nQe 5\nv_smfmac_f32_16x16x64_bf16 a[192:195], a[204:207], v[32:39], v77\nQmvva 144,44,144\nbuffer_load_dwordx4 v69, s[40:43], s90 offen offset:3072 nt lds\n"
"Qmvva 144,36,148\nQe 6\nQmvva 148,44,152\nQe 7\nQmvva 148,36,156\ns_add_u32 m0, s64, 66048\nQk 40,0,1\nQmvva 152,44,160\n"
"buffer_load_dwordx4 v72, s[44:47], s60 offen nt lds\nQmvva 152,36,164\nQk 41,2,3\nQk 42,4,5\nQmvva 156,44,168\nQk 43,6,7\nQmvva 156,36,172\nQe 8\n"
"Qmvva 188,48,168\nQe 9\nQmvva 188,40,172\nbuffer_load_dwordx4 v72, s[44:47], s60 offen offset:1024 nt lds\nQmvva 184,48,160\nQe 10\nQmvva 184,40,164\nQe 11\n"
"Qmvva 180,48,152\nQk 46,8,9\nQk 47,10,11\nQmvva 180,40,156\nQe 12\nQmvva 176,48,144\nbuffer_load_dwordx4 v72, s[44:47], s90 offen offset:2048 nt lds\nQmvva 176,40,148\n"
"Qe 13\nQcvaa 0,112,80,56\nQe 14\nQmvaa 0,116,84\nQe 15\nQmvaa 0,120,88\nQk 44,12,13\nQk 45,14,15\n"
"Qmvaa 0,124,92\nbuffer_load_dwordx4 v72, s[44:47], s90 offen offset:3072 nt lds\nQcvaa 4,140,92,56\nQmvaa 4,136,88\nQmvaa 4,132,84\nQmvaa 4,128,80\ns_xor_b32 s60, s65, %1\ns_add_u32 s90, s60, 260096\n"
"Qcvaa 8,128,96,56\nQda 128,68,1024\nQmvaa 8,132,100\nQda 132,68,1536\nQmvaa 8,136,104\nQda 136,68,1280\nQmvaa 8,140,108\nQda 140,68,1792\n"
"Qcvaa 12,124,108,56\nQda 124,68,768\nQmvaa 12,120,104\nQda 120,68,256\nQmvaa 12,116,100\nQda 116,68,512\nQmvaa 12,112,96\nQda 112,68,0\n"
"v_smfmac_f32_16x16x64_bf16 a[196:199], a[204:207], v[40:47], v77\nQmvva 208,16,144\nQe 0\nQmvva 208,44,148\nQe 1\nQmvva 212,16,152\nQe 2\nQmvva 212,44,156\n"
"Qe 3\nQmvva 216,16,160\nQk 48,0,1\nQk 49,2,3\nQmvva 216,44,164\nQe 4\nQmvva 220,16,168\nQe 5\n"
"Qmvva 220,44,172\ns_waitcnt lgkmcnt(0)\nQk 50,4,5\nQcvaa 0,112,0,56\nQe 6\nQmvaa 0,116,4\nQe 7\nQmvaa 0,120,8\n"
"Qk 51,6,7\nQmvaa 0,124,12\nQe 8\nQcvaa 4,140,12,56\nQe 9\nQmvaa 4,136,8\nQe 10\nQmvaa 4,132,4\n"
"Qe 11\nQmvaa 4,128,0\nQe 12\nQmvva 240,20,144\nQe 13\nQmvva 240,48,148\nQe 14\nQmvva 244,20,152\n"
"Qe 15\nQmvva 244,48,156\nQk 54,8,9\nQk 55,10,11\nQmvva 248,20,160\nQk 52,12,13\nQk 53,14,15\nQmvva 248,48,164\n"
"Qe 0\nQmvva 252,20,168\nQe 1\nQmvva 252,48,172\nQe 2\nQmava 236,24,168\nQta 168,71,33920\nQta 170,71,34944\n"
"Qmava 236,52,172\nQe 3\nQmava 232,24,160\nQta 172,70,896\nQta 174,70,1920\nQmava 232,52,164\nQk 16,0,1\nQk 17,2,3\n"
"Qmava 228,24,152\nQta 160,71,33664\nQta 162,71,34688\nQmava 228,52,156\nQe 4\nQmava 224,24,144\nQta 164,70,640\nQta 166,70,1664\n"
"Qmava 224,52,148\nQe 5\nv_smfmac_f32_16x16x64_bf16 a[200:203], a[204:207], v[48:55], v77\nQta 152,71,33408\nQta 154,71,34432\nQcvaa 8,112,16,56\nQe 6\nQmvaa 8,116,20\n"
"Qta 156,70,384\nQta 158,70,1408\nQmvaa 8,120,24\nQe 7\nQmvaa 8,124,28\nQta 144,71,33152\nQta 146,71,34176\nQcvaa 12,140,28,56\n"
"Qk 18,4,5\nQk 19,6,7\nQmvaa 12,136,24\nQta 148,70,128\nQta 150,70,1152\nQmvaa 12,132,20\nQmvaa 12,128,16\nQcvaa 0,128,32,56\n"
"v_add_u32 v75, 1, v75\nQmvaa 0,132,36\ns_waitcnt vmcnt(8)\nds_write_b32 v73, v75\nQmvaa 0,136,40\nQe 8\nQmvaa 0,140,44\nQe 9\n"
"Qcvaa 4,124,44,56\nQe 10\nQmvaa 4,120,40\nQe 11\nQmvaa 4,116,36\nQe 12\nQmvaa 4,112,32\ns_waitcnt lgkmcnt(1)\n"
"Qk 20,8,9\nQmvva 96,28,144\nQe 13\nQmvva 96,16,148\nQe 14\nQmvva 100,28,152\nQe 15\nQmvva 100,16,156\n"
"Qk 21,10,11\nQk 22,12,13\nQmvva 104,28,160\nQk 23,14,15\nQmvva 104,16,164\nQe 0\nQmvva 108,28,168\nQe 1\n"
"Qmvva 108,16,172\nQe 2\nQmvva 140,32,168\nQe 3\nQmvva 140,20,172\nQk 26,0,1\nQk 27,2,3\nQmvva 136,32,160\n"
"ds_read_b32 v66, v74\nQmvva 136,20,164\nQe 4\nQmvva 132,32,152\nQe 5\nQmvva 132,20,156\nQe 6\nQmvva 128,32,144\n"
"Qe 7\nQmvva 128,20,148\ns_waitcnt lgkmcnt(0)\nv_cmp_gt_u32 vcc, v75, v66\nv_smfmac_f32_16x16x64_bf16 a[176:179], a[204:207], v[16:23], v77\ns_cbranch_vccnz .Lsa4\n.Lra4:\nQcvaa 8,112,48,56\n"
"s_add_u32 m0, s61, 99072\nQk 24,4,5\nQmvaa 8,116,52\nbuffer_load_dwordx4 v69, s[40:43], s60 offen nt lds\nQmvaa 8,120,56\nQk 25,6,7\nQmvaa 8,124,60\ns_or_b32 s65, s65, 0x1d8000\n"
"s_add_u32 s65, s65, 2048\nQcvaa 12,140,60,56\ns_andn2_b32 s65, s65, 0x1d8000\nQmvaa 12,136,56\nQmvaa 12,132,52\nbuffer_load_dwordx4 v69, s[40:43], s60 offen offset:1024 nt lds\nQmvaa 12,128,48\nQcvaa 0,128,64,56\n"
"s_memrealtime s[74:75]\nQmvaa 0,132,68\nQe 8\nQmvaa 0,136,72\nQe 9\nQmvaa 0,140,76\nbuffer_load_dwordx4 v69, s[40:43], s90 offen offset:2048 nt lds\nQcvaa 4,124,76,56\n"
"Qe 10\nQmvaa 4,120,72\nQe 11\nQmvaa 4,116,68\nQe 12\nQmvaa 4,112,64\nQe 13\nQmvva 160,36,144\n"
"buffer_load_dwordx4 v69, s[40:43], s90 offen offset:3072 nt lds\nQmvva 160,24,148\nQe 14\nQmvva 164,36,152\nQe 15\nQmvva 164,24,156\nQk 28,8,9\nQk 29,10,11\n"
"Qmvva 168,36,160\ns_add_u32 m0, s64, 99072\nQk 30,12,13\nQmvva 168,24,164\nbuffer_load_dwordx4 v72, s[44:47], s60 offen nt lds\nQmvva 172,36,168\nQk 31,14,15\nQmvva 172,24,172\n"
"Qe 0\nQmvva 204,40,168\nQe 1\nQmvva 204,28,172\nQe 2\nQmvva 200,40,160\nbuffer_load_dwordx4 v72, s[44:47], s60 offen offset:1024 nt lds\nQmvva 200,28,164\n"
"Qe 3\nQmvva 196,40,152\nQk 34,0,1\nQk 35,2,3\nQmvva 196,28,156\nQe 4\nQmvva 192,40,144\nQe 5\n"
"Qmvva 192,28,148\nbuffer_load_dwordx4 v72, s[44:47], s90 offen offset:2048 nt lds\nv_smfmac_f32_16x16x64_bf16 a[180:183], a[204:207], v[24:31], v77\nQcvaa 8,112,80,56\nQe 6\nQmvaa 8,116,84\nQe 7\nQmvaa 8,120,88\n"
"Qk 32,4,5\nQk 33,6,7\nQmvaa 8,124,92\nbuffer_load_dwordx4 v72, s[44:47], s90 offen offset:3072 nt lds\nQcvaa 12,140,92,56\ns_waitcnt lgkmcnt(0)\ns_cmp_gt_u32 s77, s74\nQmvaa 12,136,88\n"
"s_cbranch_scc1 .Lsp4\n.Lrp4:\nQmvaa 12,132,84\ns_add_u32 s77, s74, 220\nQmvaa 12,128,80\ns_xor_b32 s60, s65, %1\ns_add_u32 s90, s60, 260096\nQcvaa 0,128,96,56\n"
"Qda 128,68,34048\ns_add_u32 s79, s79, s31\nQmvaa 0,132,100\nQda 132,68,34560\ns_max_u32 s77, s77, s79\nQmvaa 0,136,104\nQda 136,68,34304\nQmvaa 0,140,108\n"
"Qda 140,68,34816\nQcvaa 4,124,108,56\nQda 124,68,33792\nQmvaa 4,120,104\nQda 120,68,33280\nQmvaa 4,116,100\nQda 116,68,33536\nQmvaa 4,112,96\n"
"Qda 112,68,33024\nQmvva 224,44,144\nQe 8\nQmvva 224,32,148\nQe 9\nQmvva 228,44,152\nQe 10\nQmvva 228,32,156\n"
"Qe 11\nQmvva 232,44,160\nQk 36,8,9\nQk 37,10,11\nQmvva 232,32,164\nQe 12\nQmvva 236,44,168\nQe 13\n"
"Qmvva 236,32,172\ns_waitcnt lgkmcnt(0)\nQk 38,12,13\nQcvaa 8,112,0,56\nQe 14\nQmvaa 8,116,4\nv_add_u32 v68, 66048, v68\nQmvaa 8,120,8\n"
"Qe 15\nQmvaa 8,124,12\nQk 39,14,15\nQcvaa 12,140,12,56\nQe 0\nQmvaa 12,136,8\nQe 1\nQmvaa 12,132,4\n"
"Qe 2\nQmvaa 12,128,0\nQe 3\nv_smfmac_f32_16x16x64_bf16 a[184:187], a[204:207], v[32:39], v77\nQmava 208,48,144\nQe 4\nQmava 208,36,148\nQe 5\n"
"Qmava 212,48,152\nQe 6\nQmava 212,36,156\nQe 7\nQmava 216,48,160\nQk 42,0,1\nQk 43,2,3\nQmava 216,36,164\n"
"Qk 40,4,5\nQk 41,6,7\nQmava 220,48,168\nQe 8\nQmava 220,36,172\nQe 9\nQmava 252,52,168\nQta 168,70,768\n"
"Qta 170,70,1792\nQmava 252,40,172\nQe 10\nQmava 248,52,160\nQta 172,70,33792\nQta 174,70,34816\nQmava 248,40,164\nQe 11\n"
"Qmava 244,52,152\nQta 160,70,512\nQta 162,70,1536\nQmava 244,40,156\nQk 44,8,9\nQk 45,10,11\nQmava 240,52,144\nQta 164,70,33536\n"
"Qta 166,70,34560\nQmava 240,40,148\nQe 12\nQcvaa 0,112,16,56\nQta 152,70,256\nQta 154,70,1280\nQmvaa 0,116,20\nQe 13\n"
"Qmvaa 0,120,24\nQta 156,70,33280\nQta 158,70,34304\nQmvaa 0,124,28\nQe 14\nQcvaa 4,140,28,56\nQta 144,70,0\nQta 146,70,1024\n"
"Qmvaa 4,136,24\nQe 15\nQmvaa 4,132,20\nQta 148,70,33024\nQta 150,70,34048\nQmvaa 4,128,16\nQk 46,12,13\nQk 47,14,15\n"
"Qcvaa 8,128,32,56\nv_add_u32 v75, 1, v75\nQmvaa 8,132,36\ns_waitcnt vmcnt(8)\nds_write_b32 v73, v75\nQmvaa 8,136,40\nQe 0\nQmvaa 8,140,44\n"
"Qe 1\nQcvaa 12,124,44,56\nQe 2\nQmvaa 12,120,40\nQe 3\nQmvaa 12,116,36\nQe 4\nQmvaa 12,112,32\n"
"s_waitcnt lgkmcnt(1)\nQk 48,0,1\nv_smfmac_f32_16x16x64_bf16 a[188:191], a[204:207], v[40:47], v77\nQmvva 80,16,144\nQe 5\nQmvva 80,44,148\nQe 6\nQmvva 84,16,152\n"
"Qe 7\nQmvva 84,44,156\nQk 49,2,3\nQk 50,4,5\nQmvva 88,16,160\nQk 51,6,7\nQmvva 88,44,164\nQe 8\n"
"Qmvva 92,16,168\nQe 9\nQmvva 92,44,172\nQe 10\nQmvva 124,20,168\nQe 11\nQmvva 124,48,172\nds_read_b32 v66, v74\n"
"Qk 54,8,9\nQmvva 120,20,160\nQk 55,10,11\nQmvva 120,48,164\nQe 12\nQmvva 116,20,152\nQe 13\nQmvva 116,48,156\n"
"Qe 14\nQmvva 112,20,144\ns_waitcnt lgkmcnt(0)\nv_cmp_gt_u32 vcc, v75, v66\nQmvva 112,48,148\ns_cbranch_vccnz .Lsa5\n.Lra5:\nQcvaa 0,112,48,56\n"
"s_add_u32 m0, s61, 0\nQk 52,12,13\nQmvaa 0,116,52\nbuffer_load_dwordx4 v69, s[40:43], s60 offen nt lds\nQmvaa 0,120,56\nQe 15\nQmvaa 0,124,60\ns_add_u32 s65, s65, 2048\n"
"Qk 53,14,15\nQcvaa 4,140,60,56\nQmvaa 4,136,56\nQmvaa 4,132,52\nbuffer_load_dwordx4 v69, s[40:43], s60 offen offset:1024 nt lds\nQmvaa 4,128,48\nQcvaa 8,128,64,56\ns_nop 0\n"
"Qmvaa 8,132,68\nQe 0\nQmvaa 8,136,72\nQe 1\nQmvaa 8,140,76\nbuffer_load_dwordx4 v69, s[40:43], s90 offen offset:2048 nt lds\nQcvaa 12,124,76,56\nQe 2\n"
"Qmvaa 12,120,72\nQe 3\nQmvaa 12,116,68\nQe 4\nQmvaa 12,112,64\nQe 5\nv_smfmac_f32_16x16x64_bf16 a[192:195], a[204:207], v[48:55], v77\nQmvva 144,24,144\n"
"buffer_load_dwordx4 v69, s[40:43], s90 offen offset:3072 nt lds\nQmvva 144,52,148\nQe 6\nQmvva 148,24,152\nQe 7\nQmvva 148,52,156\ns_add_u32 m0, s64, 0\nQk 20,0,1\n"
"Qmvva 152,24,160\nbuffer_load_dwordx4 v72, s[44:47], s60 offen nt lds\nQmvva 152,52,164\nQk 21,2,3\nQk 22,4,5\nQmvva 156,24,168\nQk 23,6,7\nQmvva 156,52,172\n"
"Qe 8\nQmvva 188,28,168\nQe 9\nQmvva 188,20,172\nbuffer_load_dwordx4 v72, s[44:47], s60 offen offset:1024 nt lds\nQmvva 184,28,160\nQe 10\nQmvva 184,20,164\n"
"Qe 11\nQmvva 180,28,152\nQk 26,8,9\nQk 27,10,11\nQmvva 180,20,156\nQe 12\nQmvva 176,28,144\nbuffer_load_dwordx4 v72, s[44:47], s90 offen offset:2048 nt lds\n"
"Qmvva 176,20,148\nQe 13\nQcvaa 0,112,80,56\nQe 14\nQmvaa 0,116,84\nQe 15\nQmvaa 0,120,88\nQk 24,12,13\n"
"Qk 25,14,15\nQmvaa 0,124,92\nbuffer_load_dwordx4 v72, s[44:47], s90 offen offset:3072 nt lds\nQcvaa 4,140,92,56\nQmvaa 4,136,88\nQmvaa 4,132,84\nQmvaa 4,128,80\ns_xor_b32 s60, s65, %1\n"
"s_add_u32 s90, s60, 260096\nQcvaa 8,128,96,56\nQda 128,68,1024\nQmvaa 8,132,100\nQda 132,68,1536\nQmvaa 8,136,104\nQda 136,68,1280\nQmvaa 8,140,108\n"
"Qda 140,68,1792\nQcvaa 12,124,108,56\nQda 124,68,768\nQmvaa 12,120,104\nQda 120,68,256\nQmvaa 12,116,100\nQda 116,68,512\nQmvaa 12,112,96\n"
"Qda 112,68,0\nv_smfmac_f32_16x16x64_bf16 a[196:199], a[204:207], v[20:27], v77\nQmvva 208,32,144\nQe 0\nQmvva 208,24,148\nQe 1\nQmvva 212,32,152\nQe 2\n"
"Qmvva 212,24,156\nQe 3\nQmvva 216,32,160\nQk 28,0,1\nQk 29,2,3\nQmvva 216,24,164\nQe 4\nQmvva 220,32,168\n"
"Qe 5\nQmvva 220,24,172\ns_waitcnt lgkmcnt(0)\nQk 30,4,5\nQcvaa 0,112,0,56\nQe 6\nQmvaa 0,116,4\nQe 7\n"
"Qmvaa 0,120,8\nQk 31,6,7\nQmvaa 0,124,12\nQe 8\nQcvaa 4,140,12,56\nQe 9\nQmvaa 4,136,8\nQe 10\n"
"Qmvaa 4,132,4\nQe 11\nQmvaa 4,128,0\nQe 12\nQmvva 240,36,144\nQe 13\nQmvva 240,28,148\nQe 14\n"
"Qmvva 244,36,152\nQe 15\nQmvva 244,28,156\nQk 34,8,9\nQk 35,10,11\nQmvva 248,36,160\nQk 32,12,13\nQk 33,14,15\n"
"Qmvva 248,28,164\nQe 0\nQmvva 252,36,168\nQe 1\nQmvva 252,28,172\nQe 2\nQmava 236,40,168\nQta 168,70,33920\n"
"Qta 170,70,34944\nQmava 236,32,172\nQe 3\nQmava 232,40,160\nQta 172,71,896\nQta 174,71,1920\nQmava 232,32,164\nQk 36,0,1\n"
"Qk 37,2,3\nQmava 228,40,152\nQta 160,70,33664\nQta 162,70,34688\nQmava 228,32,156\nQe 4\nQmava 224,40,144\nQta 164,71,640\n"
"Qta 166,71,1664\nQmava 224,32,148\nQe 5\nv_smfmac_f32_16x16x64_bf16 a[200:203], a[204:207], v[28:35], v77\nQta 152,70,33408\nQta 154,70,34432\nQcvaa 8,112,16,56\nQe 6\n"
"Qmvaa 8,116,20\nQta 156,71,384\nQta 158,71,1408\nQmvaa 8,120,24\nQe 7\nQmvaa 8,124,28\nQta 144,70,33152\nQta 146,70,34176\n"
"Qcvaa 12,140,28,56\nQk 38,4,5\nQk 39,6,7\nQmvaa 12,136,24\nQta 148,71,128\nQta 150,71,1152\nQmvaa 12,132,20\ns_add_u32 s66, s66, 1\n"
"s_bitcmp1_b32 32127, s66\nQmvaa 12,128,16\ns_cbranch_scc1 .Lloop\ns_cmp_eq_u32 s66, 9\ns_cbranch_scc1 .Lhook\ns_cmp_lg_u32 s67, 0\ns_cbranch_scc1 .Ltail1\nQcvaa 0,128,32,56\n"
"v_add_u32 v75, 1, v75\nv_bfe_u32 v67, v60, 4, 3\nQmvaa 0,132,36\ns_waitcnt vmcnt(8)\nds_write_b32 v73, v75\nQmvaa 0,136,40\nQe 8\nQmvaa 0,140,44\n"
"Qe 9\nQcvaa 4,124,44,56\nQe 10\nQmvaa 4,120,40\nQe 11\nQmvaa 4,116,36\nQe 12\nQmvaa 4,112,32\n"
"s_waitcnt lgkmcnt(1)\nQk 40,8,9\nQmvva 96,44,144\nQe 13\nQmvva 96,36,148\nQe 14\nQmvva 100,44,152\nQe 15\n"
"Qmvva 100,36,156\nQk 41,10,11\nQk 42,12,13\nQmvva 104,44,160\nQk 43,14,15\ns_lshl_b32 s68, s19, 7\nQmvva 104,36,164\nv_lshlrev_b32 v67, 2, v67\n"
"s_add_u32 s68, s68, 5242880\nQmvva 108,44,168\nQe 0\nQmvva 108,36,172\nQe 1\nQmvva 140,48,168\nQe 2\nQmvva 140,40,172\n"
"Qe 3\nQmvva 136,48,160\nds_read_b32 v66, v74\nQk 46,0,1\nQmvva 136,40,164\nQk 47,2,3\nQmvva 132,48,152\nQe 4\n"
"Qmvva 132,40,156\nQe 5\nQmvva 128,48,144\nQe 6\nQmvva 128,40,148\ns_waitcnt lgkmcnt(0)\nv_cmp_gt_u32 vcc, v75, v66\nv_smfmac_f32_16x16x64_bf16 a[176:179], a[204:207], v[36:43], v77\n"
"s_cbranch_vccnz .Lsa30\n.Lra30:\nQcvaa 8,112,48,56\nQe 7\nQmvaa 8,116,52\ns_add_u32 m0, s62, 0\ns_add_u32 s69, s87, 0\nQk 44,4,5\n"
"Qmvaa 8,120,56\nbuffer_load_dwordx4 v62, s[52:55], s69 offen lds\nQmvaa 8,124,60\nQk 45,6,7\nQcvaa 12,140,60,56\nQmvaa 12,136,56\nbuffer_load_dword v78, v67, s[24:27], s68 offen offset:1024 sc1\nQmvaa 12,132,52\n"
"Qmvaa 12,128,48\ns_add_u32 s69, s87, 64504\nQcvaa 0,128,64,56\nbuffer_load_dwordx4 v62, s[52:55], s69 offen offset:1032 lds\nQmvaa 0,132,68\nQe 8\nQmvaa 0,136,72\nQe 9\n"
"Qmvaa 0,140,76\ns_add_u32 s69, s87, 129008\nQk 48,8,9\nQcvaa 4,124,76,56\nbuffer_load_dwordx4 v62, s[52:55], s69 offen offset:2064 lds\nQmvaa 4,120,72\nQe 10\nQmvaa 4,116,68\n"
"Qe 11\nQmvaa 4,112,64\ns_add_u32 s69, s87, 193512\nQk 49,10,11\nQmvva 160,52,144\nbuffer_load_dwordx4 v62, s[52:55], s69 offen offset:3096 lds\nQmvva 160,44,148\nQe 12\n"
"Qmvva 164,52,152\nQe 13\nQmvva 164,44,156\ns_add_u32 m0, s62, 16512\ns_add_u32 s69, s87, 128\nQk 50,12,13\nQmvva 168,52,160\nbuffer_load_dwordx4 v62, s[52:55], s69 offen lds\n"
"Qmvva 168,44,164\nQe 14\nQmvva 172,52,168\nQe 15\nQmvva 172,44,172\ns_add_u32 s69, s87, 64632\nQk 51,14,15\nQmvva 204,20,168\n"
"buffer_load_dwordx4 v62, s[52:55], s69 offen offset:1032 lds\nQmvva 204,48,172\nQe 0\nQmvva 200,20,160\nQe 1\nQmvva 200,48,164\ns_add_u32 s69, s87, 129136\nQk 18,0,1\n"
"Qmvva 196,20,152\nbuffer_load_dwordx4 v62, s[52:55], s69 offen offset:2064 lds\nQmvva 196,48,156\nQe 2\nQmvva 192,20,144\nQe 3\nQmvva 192,48,148\nQk 19,2,3\n"
"v_smfmac_f32_16x16x64_bf16 a[180:183], a[204:207], v[44:51], v77\ns_add_u32 s69, s87, 193640\nQcvaa 8,112,80,56\nbuffer_load_dwordx4 v62, s[52:55], s69 offen offset:3096 lds\nQmvaa 8,116,84\nQe 4\nQmvaa 8,120,88\ns_add_u32 m0, s63, 33024\n"
"s_add_u32 s88, s86, 0\nQmvaa 8,124,92\nbuffer_load_dwordx4 v64, s[48:51], s88 offen lds\nQcvaa 12,140,92,56\nQe 5\nQmvaa 12,136,88\nQe 6\nQmvaa 12,132,84\n"
"s_add_u32 s88, s86, 19456\nQk 16,4,5\nQmvaa 12,128,80\nbuffer_load_dwordx4 v64, s[48:51], s88 offen offset:1024 lds\nQcvaa 0,128,96,56\nQda 128,68,34048\nQmvaa 0,132,100\nQda 132,68,34560\n"
"Qmvaa 0,136,104\nQda 136,68,34304\nQe 7\nQmvaa 0,140,108\nQda 140,68,34816\nQk 17,6,7\nQcvaa 4,124,108,56\nQda 124,68,33792\n"
"Qmvaa 4,120,104\nQda 120,68,33280\nQmvaa 4,116,100\nQda 116,68,33536\nQmvaa 4,112,96\nQda 112,68,33024\ns_add_u32 s88, s86, 38912\nQmvva 224,24,144\n"
"buffer_load_dwordx4 v64, s[48:51], s88 offen offset:2048 lds\nQmvva 224,16,148\nQe 8\nQmvva 228,24,152\nQe 9\nQmvva 228,16,156\nQe 10\nQmvva 232,24,160\n"
"s_add_u32 s88, s86, 58368\nQk 20,8,9\nQmvva 232,16,164\nbuffer_load_dwordx4 v64, s[48:51], s88 offen offset:3072 lds\nQmvva 236,24,168\nQe 11\nQmvva 236,16,172\ns_waitcnt lgkmcnt(0)\n"
"Qk 21,10,11\nQcvaa 8,112,0,56\nQe 12\nQmvaa 8,116,4\nv_subrev_u32 v68, 66048, v68\ns_add_u32 m0, s63, 37120\ns_add_u32 s88, s86, 81920\nQmvaa 8,120,8\n"
"buffer_load_dwordx4 v64, s[48:51], s88 offen lds\nQe 13\nQe 14\nQe 15\nQmvaa 8,124,12\nQk 22,12,13\nQk 23,14,15\nQcvaa 12,140,12,56\n"
"Qe 0\nQmvaa 12,136,8\nQe 1\nQmvaa 12,132,4\nQe 2\nQmvaa 12,128,0\nQe 3\nv_smfmac_f32_16x16x64_bf16 a[184:187], a[204:207], v[16:23], v77\n"
"Qmava 208,28,144\ns_add_u32 s88, s86, 101376\nQk 26,0,1\nQmava 208,20,148\nbuffer_load_dwordx4 v64, s[48:51], s88 offen offset:1024 lds\nQmava 212,28,152\nQe 4\nQmava 212,20,156\n"
"Qe 5\nQmava 216,28,160\nQe 6\nQmava 216,20,164\nQe 7\nQmava 220,28,168\nQk 27,2,3\nQk 24,4,5\n"
"Qmava 220,20,172\nQk 25,6,7\nQmava 252,32,168\nQta 168,71,768\nQta 170,71,1792\nQmava 252,24,172\nQe 8\nQmava 248,32,160\n"
"Qta 172,71,33792\nQta 174,71,34816\nQmava 248,24,164\nQe 9\nQmava 244,32,152\nQta 160,71,512\nQta 162,71,1536\nQmava 244,24,156\n"
"Qe 10\nQmava 240,32,144\nQta 164,71,33536\nQta 166,71,34560\nQmava 240,24,148\nQe 11\nQcvaa 0,112,16,56\nQta 152,71,256\n"
"Qta 154,71,1280\ns_add_u32 s88, s86, 120832\nQmvaa 0,116,20\nbuffer_load_dwordx4 v64, s[48:51], s88 offen offset:2048 lds\nQmvaa 0,120,24\nQta 156,71,33280\nQta 158,71,34304\nQmvaa 0,124,28\n"
"Qk 28,8,9\nQk 29,10,11\nQcvaa 4,140,28,56\nQta 144,71,0\nQta 146,71,1024\nQmvaa 4,136,24\nQe 12\nQmvaa 4,132,20\n"
"Qta 148,71,33024\nQta 150,71,34048\nQmvaa 4,128,16\nQe 13\nQcvaa 8,128,32,56\nv_add_u32 v75, 1, v75\nQk 30,12,13\nQmvaa 8,132,36\n"
"ds_write_b32 v73, v75\nQmvaa 8,136,40\nQe 14\nQe 15\nQmvaa 8,140,44\nQk 31,14,15\nQcvaa 12,124,44,56\nQe 0\n"
"Qmvaa 12,120,40\nQe 1\nQmvaa 12,116,36\nQe 2\nQmvaa 12,112,32\ns_waitcnt lgkmcnt(1)\nQk 32,0,1\nv_smfmac_f32_16x16x64_bf16 a[188:191], a[204:207], v[24:31], v77\n"
"Qmvva 80,36,144\nQe 3\nQmvva 80,28,148\nQe 4\nQmvva 84,36,152\nQe 5\nQmvva 84,28,156\nQe 6\n"
"Qmvva 88,36,160\nQe 7\nQmvva 88,28,164\nQk 33,2,3\nQk 34,4,5\nQmvva 92,36,168\nQk 35,6,7\nQmvva 92,28,172\n"
"Qe 8\nQmvva 124,40,168\nQe 9\nQmvva 124,32,172\nds_read_b32 v66, v74\nQk 38,8,9\nQmvva 120,40,160\nQe 10\n"
"Qmvva 120,32,164\nQe 11\nQmvva 116,40,152\nQk 39,10,11\nQmvva 116,32,156\nQe 12\nQmvva 112,40,144\ns_waitcnt lgkmcnt(0)\n"
"v_cmp_gt_u32 vcc, v75, v66\nQmvva 112,32,148\ns_cbranch_vccnz .Lsa31\n.Lra31:\nQcvaa 0,112,48,56\nQe 13\nQmvaa 0,116,52\nQe 14\n"
"Qmvaa 0,120,56\ns_add_u32 m0, s62, 33024\ns_add_u32 s69, s87, 524288\nQk 36,12,13\nQmvaa 0,124,60\nbuffer_load_dwordx4 v62, s[52:55], s69 offen lds\nQcvaa 4,140,60,56\nQe 15\n"
"Qmvaa 4,136,56\nQk 37,14,15\nQmvaa 4,132,52\ns_waitcnt vmcnt(15)\nv_xor_b32 v78, s30, v78\nv_and_b32 v66, 0x0fffffff, v78\nv_cmp_ne_u32 vcc, 0, v66\ns_cmp_eq_u64 vcc, 0\n"
"s_cbranch_scc0 .Lack_w\n.Lack_ok:\nQmvaa 4,128,48\ns_add_u32 s69, s87, 588792\nv_lshrrev_b32 v78, 28, v78\nQcvaa 8,128,64,56\nbuffer_load_dwordx4 v62, s[52:55], s69 offen offset:1032 lds\nQmvaa 8,132,68\n"
"Qe 0\nQmvaa 8,136,72\nQe 1\nQmvaa 8,140,76\ns_add_u32 s69, s87, 653296\nQk 40,0,1\nQcvaa 12,124,76,56\nbuffer_load_dwordx4 v62, s[52:55], s69 offen offset:2064 lds\n"
"Qmvaa 12,120,72\nQe 2\nQmvaa 12,116,68\nQe 3\nQmvaa 12,112,64\nQe 4\nv_smfmac_f32_16x16x64_bf16 a[192:195], a[204:207], v[32:39], v77\ns_add_u32 s69, s87, 717800\n"
"Qmvva 144,44,144\nbuffer_load_dwordx4 v62, s[52:55], s69 offen offset:3096 lds\nQmvva 144,36,148\nQe 5\nQmvva 148,44,152\nQe 6\nQmvva 148,36,156\nQe 7\n"
"Qmvva 152,44,160\nQk 41,2,3\nQk 42,4,5\nQmvva 152,36,164\ns_add_u32 m0, s62, 49536\ns_add_u32 s69, s87, 524416\nQk 43,6,7\nQmvva 156,44,168\n"
"buffer_load_dwordx4 v62, s[52:55], s69 offen lds\nQmvva 156,36,172\nQe 8\nQmvva 188,48,168\nQe 9\nQmvva 188,40,172\nQe 10\nQmvva 184,48,160\n"
"s_add_u32 s69, s87, 588920\nQk 46,8,9\nQmvva 184,40,164\nbuffer_load_dwordx4 v62, s[52:55], s69 offen offset:1032 lds\nQmvva 180,48,152\nQe 11\nQmvva 180,40,156\nQk 47,10,11\n"
"v_add_u32_dpp v78, v78, v78 quad_perm:[1,0,3,2] row_mask:0xf bank_mask:0xf\nQmvva 176,48,144\ns_add_u32 s69, s87, 653424\nQmvva 176,40,148\nbuffer_load_dwordx4 v62, s[52:55], s69 offen offset:2064 lds\nQcvaa 0,112,80,56\nQe 12\nQmvaa 0,116,84\n"
"Qe 13\nQmvaa 0,120,88\nQe 14\nQmvaa 0,124,92\nQe 15\nQcvaa 4,140,92,56\nQk 44,12,13\nQk 45,14,15\n"
"Qmvaa 4,136,88\ns_add_u32 s69, s87, 717928\nv_add_u32_dpp v78, v78, v78 quad_perm:[2,3,0,1] row_mask:0xf bank_mask:0xf\nQmvaa 4,132,84\nbuffer_load_dwordx4 v62, s[52:55], s69 offen offset:3096 lds\nQmvaa 4,128,80\nv_readlane_b32 s68, v78, s19\n.Lpg6:\n"
"s_cbranch_execz .Ltr9\nv_readlane_b32 s33, v78, 4\nQcvaa 8,128,96,56\ns_cmp_lt_u32 s68, 32\ns_cselect_b32 s68, 8192, 0\nQmvaa 8,132,100\nQe 0\nQmvaa 8,136,104\n"
"Qe 1\nQmvaa 8,140,108\nQe 2\nQcvaa 12,124,108,56\nQe 3\nQmvaa 12,120,104\nQk 48,0,1\nQk 49,2,3\n"
"Qmvaa 12,116,100\ns_xor_b32 s68, s68, s98\ns_add_u32 s71, s71, s68\nQmvaa 12,112,96\ns_cmp_lt_u32 s33, 32\ns_cselect_b32 s33, 8, 0\nv_smfmac_f32_16x16x64_bf16 a[196:199], a[204:207], v[40:47], v77\nQmvva 208,16,144\n"
"Qta 112,71,33152\ns_xor_b32 s33, s33, %0\nQmvva 208,44,148\nQta 114,71,34176\ns_and_b32 s33, s33, 8\nQmvva 212,16,152\nQta 116,71,33408\ns_lshl_b32 s98, s33, 10\n"
"Qmvva 212,44,156\nQta 118,71,34432\nQmvva 216,16,160\nQta 120,71,33664\nQmvva 216,44,164\nQta 122,71,34688\nQmvva 220,16,168\nQta 124,71,33920\n"
"Qmvva 220,44,172\nQta 126,71,34944\nQe 4\nQe 5\nQe 6\nQe 7\nQk 50,4,5\nQk 51,6,7\n"
"Qmvva 240,20,144\nv_add_u32 v75, 1, v75\nQmvva 240,48,148\nds_write_b32 v73, v75\nQmvva 244,20,152\nQe 8\nQmvva 244,48,156\nQe 9\n"
"Qmvva 248,20,160\nQe 10\nQmvva 248,48,164\nQe 11\nQmvva 252,20,168\nQk 54,8,9\nQk 55,10,11\nQmvva 252,48,172\n"
"Qe 12\nQe 13\nQe 14\nQe 15\nQk 52,12,13\nQk 53,14,15\ns_nop 1\nv_smfmac_f32_16x16x64_bf16 a[200:203], a[204:207], v[48:55], v77\n"
"Qmava 224,24,144\nds_read_b32 v66, v74\nv_accvgpr_read_b32 v0, a176\nQmava 224,52,148\nv_accvgpr_read_b32 v1, a177\nv_accvgpr_read_b32 v6, a178\nQmava 228,24,152\nv_accvgpr_read_b32 v7, a179\n"
"v_accvgpr_read_b32 v8, a180\nQmava 228,52,156\nv_accvgpr_read_b32 v9, a181\nv_accvgpr_read_b32 v14, a182\nQmava 232,24,160\nv_accvgpr_read_b32 v15, a183\nv_accvgpr_read_b32 v16, a184\nQmava 232,52,164\n"
"s_waitcnt lgkmcnt(0)\nv_cmp_gt_u32 vcc, v75, v66\nQmava 236,24,168\ns_cbranch_vccnz .Lsa32\n.Lra32:\nQmava 236,52,172\ns_waitcnt lgkmcnt(0)\nv_accvgpr_read_b32 v17, a185\n"
"Qmvva 96,28,112\ns_add_u32 m0, s63, 0\ns_add_u32 s88, s86, 128\nv_accvgpr_read_b32 v22, a186\nQmvva 100,28,116\nbuffer_load_dwordx4 v64, s[48:51], s88 offen lds\nQmvva 104,28,120\nv_accvgpr_read_b32 v23, a187\n"
"v_accvgpr_read_b32 v24, a188\nQmvva 108,28,124\nv_accvgpr_read_b32 v25, a189\nv_accvgpr_read_b32 v2, a190\nQmvva 128,32,112\ns_add_u32 s88, s86, 19584\nv_accvgpr_read_b32 v3, a191\nQmvva 132,32,116\n"
"buffer_load_dwordx4 v64, s[48:51], s88 offen offset:1024 lds\nQmvva 136,32,120\nv_accvgpr_read_b32 v4, a192\nv_accvgpr_read_b32 v5, a193\nQmvva 140,32,124\nv_accvgpr_read_b32 v10, a194\nv_accvgpr_read_b32 v11, a195\nQmvva 160,36,112\n"
"s_add_u32 s88, s86, 39040\nv_accvgpr_read_b32 v12, a196\nQmvva 164,36,116\nbuffer_load_dwordx4 v64, s[48:51], s88 offen offset:2048 lds\nQmvva 168,36,120\nv_accvgpr_read_b32 v13, a197\nv_accvgpr_read_b32 v18, a198\nQmvva 172,36,124\n"
"v_accvgpr_read_b32 v19, a199\nv_accvgpr_read_b32 v20, a200\nQmvva 192,40,112\ns_add_u32 s88, s86, 58496\nv_accvgpr_read_b32 v21, a201\nQmvva 196,40,116\nbuffer_load_dwordx4 v64, s[48:51], s88 offen offset:3072 lds\nQmvva 200,40,120\n"
"v_accvgpr_read_b32 v26, a202\nv_accvgpr_read_b32 v27, a203\nQmvva 204,40,124\nQa 8,8,9\nQa 10,10,11\nQmvva 224,44,112\ns_add_u32 m0, s63, 4096\ns_add_u32 s88, s86, 82048\n"
"Qa 8,8,10\nQmvva 228,44,116\nbuffer_load_dwordx4 v64, s[48:51], s88 offen lds\nQmvva 232,44,120\nQa 16,16,17\nQa 18,18,19\nQmvva 236,44,124\nQa 16,16,18\n"
"Qa 4,4,5\nQmava 208,48,112\ns_add_u32 s88, s86, 101504\nQa 6,6,7\nQmava 212,48,116\nbuffer_load_dwordx4 v64, s[48:51], s88 offen offset:1024 lds\nQmava 216,48,120\nQa 4,4,6\n"
"Qa 12,12,13\nQmava 220,48,124\nQa 14,14,15\nQa 12,12,14\nQmava 240,52,112\ns_add_u32 s88, s86, 120960\nQa 0,0,1\nQmava 244,52,116\n"
"buffer_load_dwordx4 v64, s[48:51], s88 offen offset:2048 lds\nQmava 248,52,120\nQa 2,2,3\nQa 0,0,2\nQmava 252,52,124\nQa 24,24,25\nQa 26,26,27\nQa 24,24,26\n"
"Qa 20,20,21\nQa 22,22,23\nQa 20,20,22\nv_max3_u32 v9, v8, v16, v4\nv_max3_u32 v9, v9, v12, v0\nv_max3_u32 v9, v9, v24, v20\nv_min3_u32 v11, v8, v16, v4\nv_min3_u32 v11, v11, v12, v0\n"
"v_min3_u32 v11, v11, v24, v20\nv_cmp_lt_u32 vcc, 0x4e800000, v9\ns_mov_b64 s[74:75], vcc\nv_cmp_gt_u32 vcc, 0x35800000, v11\ns_or_b64 s[74:75], s[74:75], vcc\nv_mov_b32 v30, s73\nv_mov_b32 v31, 0\nv_mov_b32 v58, 0xb9800000\n"
"v_mov_b32 v59, 0\nv_and_b32 v66, 16, v60\nv_mov_b32 v67, 0xffff\nv_lshlrev_b32 v66, v66, v67\ns_mov_b32 vcc_lo, 0x00c0000c\ns_mov_b32 vcc_hi, 0xc0000c00\nv_cndmask_b32 v67, 0, v66, vcc\ns_lshr_b64 vcc, vcc, 2\n"
"v_cndmask_b32 v66, 0, v66, vcc\ns_cmp_lg_u64 s[74:75], 0\ns_cbranch_scc1 .Lfb\n.Lfbret0:\nv_rcp_f32 v2, v0\nv_rcp_f32_e64 v6, v4\nv_mfma_f32_16x16x4_4b_bf16 a[208:223], v[30:31], v[58:59], a[208:223]\nv_rcp_f32 v10, v8\n"
"v_rcp_f32 v14, v12\nv_rcp_f32 v18, v16\nv_rcp_f32 v22, v20\nv_rcp_f32 v26, v24\nv_mov_b64 v[0:1], 0\nv_mfma_f32_16x16x4_4b_bf16 a[224:239], v[30:31], v[58:59], a[224:239]\nQk 2,2,2\nv_and_b32 v3, v2, v67\n"
"v_and_b32 v2, v2, v66\nv_mov_b64_e64 v[4:5], 0\nQk 6,6,6\nv_and_b32_e64 v7, v6, v67\nv_mfma_f32_16x16x4_4b_bf16 a[240:255], v[30:31], v[58:59], a[240:255]\nv_and_b32 v6, v6, v66\nv_mov_b64 v[8:9], 0\nQk 10,10,10\n"
"v_and_b32 v11, v10, v67\nv_and_b32 v10, v10, v66\nv_mov_b64_e64 v[12:13], 0\nv_mfma_f32_16x16x4_4b_bf16 v[80:95], v[30:31], v[58:59], v[80:95]\nQk 14,14,14\nv_and_b32 v15, v14, v67\nv_and_b32 v14, v14, v66\nv_mov_b64_e64 v[16:17], 0\n"
"Qk 18,18,18\nv_and_b32_e64 v19, v18, v67\nv_mfma_f32_16x16x4_4b_bf16 v[112:127], v[30:31], v[58:59], v[112:127]\nv_and_b32 v18, v18, v66\nv_mov_b64 v[20:21], 0\nQk 22,22,22\nv_and_b32 v23, v22, v67\nv_and_b32 v22, v22, v66\n"
"v_mov_b64_e64 v[24:25], 0\nv_mfma_f32_16x16x4_4b_bf16 v[144:159], v[30:31], v[58:59], v[144:159]\nQk 26,26,26\nv_and_b32 v27, v26, v67\nv_and_b32 v26, v26, v66\nv_mov_b64_e64 v[28:29], 0\nv_accvgpr_read_b32 v48, a208\nv_accvgpr_read_b32 v49, a209\n"
"v_accvgpr_read_b32 v50, a210\nv_accvgpr_read_b32 v51, a211\nv_accvgpr_read_b32 v52, a212\nv_accvgpr_read_b32 v53, a213\nv_accvgpr_read_b32 v54, a214\nv_accvgpr_read_b32 v55, a215\nQk 48,48,49\nQk 49,50,51\n"
"Qk 50,52,53\nQk 51,54,55\nv_accvgpr_read_b32 v32, a216\nv_accvgpr_read_b32 v33, a217\nQzvvv 52,48,20\nv_accvgpr_read_b32 v34, a218\nv_accvgpr_read_b32 v35, a219\nQzvvv 48,48,22\n"
"v_accvgpr_read_b32 v36, a220\nv_accvgpr_read_b32 v37, a221\nv_accvgpr_read_b32 v38, a222\nv_accvgpr_read_b32 v39, a223\nQk 32,32,33\nQk 33,34,35\nQk 34,36,37\nQk 35,38,39\n"
"v_mfma_f32_16x16x4_4b_bf16 v[240:255], v[30:31], v[58:59], v[240:255]\nv_accvgpr_read_b32 v40, a224\nv_accvgpr_read_b32 v41, a225\nv_accvgpr_read_b32 v42, a226\nv_accvgpr_read_b32 v43, a227\nv_accvgpr_read_b32 v44, a228\nv_accvgpr_read_b32 v45, a229\nQzvvv 36,32,20\n"
"v_accvgpr_read_b32 v46, a230\nv_accvgpr_read_b32 v47, a231\nQzvvv 32,32,22\nQk 40,40,41\nQk 41,42,43\nQk 42,44,45\nQk 43,46,47\nQk 48,48,49\n"
"Qk 49,50,51\nQzvvv 44,40,24\nQk 50,52,53\nQk 51,54,55\nQzvvv 40,40,26\nv_accvgpr_write_b32 a88, v48\nv_accvgpr_write_b32 a89, v49\nv_accvgpr_write_b32 a90, v50\n"
"v_accvgpr_write_b32 a91, v51\nv_mfma_f32_16x16x4_4b_bf16 v[96:111], v[30:31], v[58:59], v[96:111]\nv_accvgpr_read_b32 v48, a232\nv_accvgpr_read_b32 v49, a233\nv_accvgpr_read_b32 v50, a234\nv_accvgpr_read_b32 v51, a235\nv_accvgpr_read_b32 v52, a236\nv_accvgpr_read_b32 v53, a237\n"
"v_accvgpr_read_b32 v54, a238\nv_accvgpr_read_b32 v55, a239\nQk 48,48,49\nQk 49,50,51\nQk 50,52,53\nQk 51,54,55\nQk 32,32,33\nQk 33,34,35\n"
"Qzvvv 52,48,24\nQk 34,36,37\nQk 35,38,39\nQzvvv 48,48,26\nv_accvgpr_write_b32 a92, v32\nv_accvgpr_write_b32 a93, v33\nv_accvgpr_write_b32 a94, v34\nv_accvgpr_write_b32 a95, v35\n"
"v_mfma_f32_16x16x4_4b_bf16 v[176:191], v[30:31], v[58:59], v[176:191]\nv_accvgpr_read_b32 v32, a240\nv_accvgpr_read_b32 v33, a241\nv_accvgpr_read_b32 v34, a242\nv_accvgpr_read_b32 v35, a243\nv_accvgpr_read_b32 v36, a244\nv_accvgpr_read_b32 v37, a245\nv_accvgpr_read_b32 v38, a246\n"
"v_accvgpr_read_b32 v39, a247\nQk 32,32,33\nQk 33,34,35\nQk 34,36,37\nQk 35,38,39\nQk 40,40,41\nQk 41,42,43\nQzvvv 36,32,24\n"
"Qk 42,44,45\nQk 43,46,47\nQzvvv 32,32,26\nv_accvgpr_write_b32 a96, v40\nv_accvgpr_write_b32 a97, v41\nv_accvgpr_write_b32 a98, v42\nv_accvgpr_write_b32 a99, v43\nv_mfma_f32_16x16x4_4b_bf16 v[128:143], v[30:31], v[58:59], v[128:143]\n"
"v_accvgpr_read_b32 v40, a248\nv_accvgpr_read_b32 v41, a249\nv_accvgpr_read_b32 v42, a250\nv_accvgpr_read_b32 v43, a251\nv_accvgpr_read_b32 v44, a252\nv_accvgpr_read_b32 v45, a253\nv_accvgpr_read_b32 v46, a254\nv_accvgpr_read_b32 v47, a255\n"
"Qk 40,40,41\nQk 41,42,43\ns_mov_b64 vcc, -1\ns_nop 0\nQk 42,44,45\nQk 43,46,47\nQk 48,48,49\ns_waitcnt vmcnt(15)\n"
"s_nop 0\nQk 49,50,51\nQzvvv 44,40,24\nQk 50,52,53\ns_barrier\ns_nop 0\nQk 51,54,55\nQzvvv 40,40,26\n"
"v_accvgpr_write_b32 a100, v48\nQta 112,61,0\nQta 114,61,512\nQda 208,63,33024\nQda 212,63,34048\nv_accvgpr_write_b32 a101, v49\nv_accvgpr_write_b32 a102, v50\nv_accvgpr_write_b32 a103, v51\n"
"v_mfma_f32_16x16x4_4b_bf16 v[160:175], v[30:31], v[58:59], v[160:175]\nQk 80,80,81\nQk 81,82,83\nQk 82,84,85\nQk 83,86,87\nQk 88,88,89\nQda 216,63,35072\nQzvvv 84,80,0\n"
"Qda 220,63,36096\nQda 224,63,37120\nQzvvv 80,80,2\nQda 228,63,38144\nQk 89,90,91\nv_mfma_f32_16x16x4_4b_bf16 v[208:223], v[30:31], v[58:59], v[208:223]\nQk 90,92,93\nQk 91,94,95\n"
"Qk 112,112,113\nQk 113,114,115\nQk 114,116,117\nQk 115,118,119\nQzvvv 92,88,0\nQk 80,80,81\nQk 81,82,83\nQzvvv 88,88,2\n"
"Qda 232,63,39168\nQda 176,63,33536\nv_mfma_f32_16x16x4_4b_bf16 v[192:207], v[30:31], v[58:59], v[192:207]\nQta 116,61,4128\nQta 118,61,4640\nQk 82,84,85\nQk 83,86,87\nv_accvgpr_write_b32 a0, v80\n"
"v_accvgpr_write_b32 a1, v81\nQzvvv 116,112,4\nv_accvgpr_write_b32 a2, v82\nv_accvgpr_write_b32 a3, v83\nQzvvv 112,112,6\nQk 120,120,121\nQk 121,122,123\nv_mfma_f32_16x16x4_4b_bf16 v[224:239], v[30:31], v[58:59], v[224:239]\n"
"Qk 122,124,125\nQta 120,61,8\nQta 122,61,520\nQta 124,61,4136\nQta 126,61,4648\nQk 123,126,127\nQk 88,88,89\nQk 89,90,91\n"
"Qzvvv 124,120,4\nQk 90,92,93\nQk 91,94,95\nQzvvv 120,120,6\nv_accvgpr_write_b32 a4, v88\nv_accvgpr_write_b32 a5, v89\nv_accvgpr_write_b32 a6, v90\nv_accvgpr_write_b32 a7, v91\n"
"Qk 144,144,145\nQta 128,61,256\nQta 130,61,768\nQta 132,61,4384\nQta 134,61,4896\nQk 145,146,147\nQk 146,148,149\nQk 147,150,151\n"
"Qk 112,112,113\nQk 113,114,115\nQzvvv 148,144,8\nQk 114,116,117\nQk 115,118,119\nQzvvv 144,144,10\nv_accvgpr_write_b32 a16, v112\nv_accvgpr_write_b32 a17, v113\n"
"v_accvgpr_write_b32 a18, v114\nQta 136,61,264\nQta 138,61,776\nQta 140,61,4392\nQta 142,61,4904\nv_accvgpr_write_b32 a19, v115\nQk 240,240,241\nQk 241,242,243\n"
"Qk 242,244,245\nQk 243,246,247\nQk 120,120,121\nQk 121,122,123\nQzvvv 244,240,20\nQk 122,124,125\nQk 123,126,127\nQzvvv 240,240,22\n"
"v_accvgpr_write_b32 a20, v120\ns_waitcnt lgkmcnt(15)\ns_nop 0\nv_accvgpr_write_b32 a21, v121\nv_accvgpr_write_b32 a22, v122\nv_accvgpr_write_b32 a23, v123\nQzvaa 80,112,208\nQk 248,248,249\n"
"Qk 249,250,251\nQk 250,252,253\nQzvaa 84,112,212\nQda 180,63,34560\nQk 251,254,255\nQk 144,144,145\nQk 145,146,147\nQzvvv 252,248,20\n"
"Qk 146,148,149\nQk 147,150,151\nQzvaa 88,112,216\nQda 184,63,35584\nv_accvgpr_write_b32 a32, v144\nQzvvv 248,248,22\nv_accvgpr_write_b32 a33, v145\nv_accvgpr_write_b32 a34, v146\n"
"v_accvgpr_write_b32 a35, v147\nQk 152,152,153\nQzvaa 92,112,220\nQda 188,63,36608\nQk 153,154,155\nQk 154,156,157\nQk 155,158,159\nQk 240,240,241\n"
"Qk 241,242,243\nQzvvv 156,152,8\nQk 242,244,245\nQk 243,246,247\nQzvaa 112,112,224\nQda 192,63,37632\nv_accvgpr_write_b32 a80, v240\nQzvvv 152,152,10\n"
"v_accvgpr_write_b32 a81, v241\nv_accvgpr_write_b32 a82, v242\nQzvaa 116,112,228\nQda 196,63,38656\nv_accvgpr_write_b32 a83, v243\nQk 96,96,97\nQk 97,98,99\nQk 98,100,101\n"
"Qk 99,102,103\nQk 248,248,249\nQzvaa 120,112,232\nQda 200,63,39680\nQk 249,250,251\nQzvvv 100,96,0\nQk 250,252,253\nQk 251,254,255\n"
"Qzvvv 96,96,2\nv_accvgpr_write_b32 a84, v248\nv_max3_f32 v80, |v80|, |v81|, |v82|\nv_accvgpr_write_b32 a85, v249\nv_accvgpr_write_b32 a86, v250\nv_accvgpr_write_b32 a87, v251\nv_max3_f32 v84, |v84|, |v85|, |v86|\nQk 104,104,105\n"
"Qk 105,106,107\nQk 106,108,109\nv_max3_f32 v88, |v88|, |v89|, |v90|\nQk 107,110,111\nQk 152,152,153\nQk 153,154,155\nv_max3_f32 v92, |v92|, |v93|, |v94|\nQzvvv 108,104,0\n"
"Qk 154,156,157\nQk 155,158,159\nv_max3_f32 v112, |v112|, |v113|, |v114|\nQzvvv 104,104,2\nv_accvgpr_write_b32 a36, v152\nv_accvgpr_write_b32 a37, v153\nv_max3_f32 v116, |v116|, |v117|, |v118|\nv_accvgpr_write_b32 a38, v154\n"
"v_accvgpr_write_b32 a39, v155\nQk 176,176,177\nv_max3_f32 v120, |v120|, |v121|, |v122|\nQk 177,178,179\nQk 178,180,181\nQk 179,182,183\nv_min3_u32 v56, v80, v84, v88\nQk 128,128,129\n"
"Qzvvv 180,176,12\nv_min3_u32 v56, v56, v92, v112\nQk 129,130,131\nQzvvv 176,176,14\nQk 130,132,133\nQk 131,134,135\nv_min3_u32 v56, v56, v116, v120\nQk 136,136,137\n"
"Qzvvv 132,128,4\n.Lpg7:\ns_cbranch_execz .Ltr9\nv_mov_b32 v57, v56\nQk 137,138,139\nQzvvv 128,128,6\nQk 138,140,141\nQk 139,142,143\n"
"v_permlane32_swap_b32_e64 v56, v57\nQk 176,176,177\nQk 177,178,179\nQzvvv 140,136,4\nv_max_f32_e64 v56, v56, v57\nQk 178,180,181\nQk 179,182,183\nQzvvv 136,136,6\n"
"v_mov_b32_e64 v57, v56\nv_accvgpr_write_b32 a48, v176\nv_accvgpr_write_b32 a49, v177\nv_accvgpr_write_b32 a50, v178\nv_permlane16_swap_b32_e64 v56, v57\nv_accvgpr_write_b32 a51, v179\nQk 184,184,185\nQk 185,186,187\n"
"v_max_f32_e64 v56, v56, v57\nQk 186,188,189\nQk 187,190,191\nv_min_f32_dpp v56, v56, v56 row_ror:8 row_mask:0xf bank_mask:0xf\nQk 160,160,161\nQzvvv 188,184,12\nQk 161,162,163\nv_min_f32_dpp v56, v56, v56 row_ror:4 row_mask:0xf bank_mask:0xf\n"
"Qzvvv 184,184,14\nQk 162,164,165\nQk 163,166,167\nv_min_f32_dpp v56, v56, v56 row_ror:2 row_mask:0xf bank_mask:0xf\nQk 168,168,169\nQzvvv 164,160,8\nQk 169,170,171\nv_min_f32_dpp v56, v56, v56 row_ror:1 row_mask:0xf bank_mask:0xf\n"
"Qzvvv 160,160,10\nQk 170,172,173\nQk 171,174,175\nQk 184,184,185\nv_and_b32 v56, 0x7f800000, v56\nQk 185,186,187\nQzvvv 172,168,8\nQk 186,188,189\n"
"v_min_u32 v56, 0x78000000, v56\nQk 187,190,191\nQzvvv 168,168,10\nv_accvgpr_write_b32 a52, v184\nv_add_u32 v56, 0x5400000, v56\nv_accvgpr_write_b32 a53, v185\nv_accvgpr_write_b32 a54, v186\nv_accvgpr_write_b32 a55, v187\n"
"v_mov_b32_e64 v57, v56\nQk 208,208,209\nQk 209,210,211\nQk 210,212,213\nv_mov_b64_e64 v[58:59], v[56:57]\nQk 211,214,215\nv_mul_f32 v52, -v56, s72\nQk 192,192,193\n"
"Qzvvv 212,208,16\nQk 193,194,195\nQk 194,196,197\nQzvvv 208,208,18\nQk 195,198,199\nQk 200,200,201\nQk 201,202,203\nQzvvv 196,192,12\n"
"Qk 202,204,205\nQk 203,206,207\nQzvvv 192,192,14\nQk 216,216,217\nQk 208,208,209\nQk 209,210,211\nQzvvv 204,200,12\nQk 210,212,213\n"
"Qk 211,214,215\nQzvvv 200,200,14\nv_accvgpr_write_b32 a64, v208\nv_accvgpr_write_b32 a65, v209\nv_accvgpr_write_b32 a66, v210\nv_accvgpr_write_b32 a67, v211\nQk 217,218,219\nQk 218,220,221\n"
"Qk 219,222,223\nQk 224,224,225\nQk 225,226,227\nQzvvv 220,216,16\nQk 226,228,229\nQk 227,230,231\nQzvvv 216,216,18\nQk 232,232,233\n"
"Qk 233,234,235\nQzvvv 228,224,16\nQk 234,236,237\nQk 235,238,239\nQzvvv 224,224,18\ns_nop 1\ns_nop 0\nQk 216,216,217\n"
"Qzvvv 236,232,16\nQk 217,218,219\nQk 218,220,221\nQk 219,222,223\nQzvvv 232,232,18\nv_accvgpr_write_b32 a68, v216\nv_accvgpr_write_b32 a69, v217\nv_accvgpr_write_b32 a70, v218\n"
"v_accvgpr_write_b32 a71, v219\ns_mov_b32 s56, s20\ns_and_b32 s57, s21, 0xffff\ns_mov_b32 s94, 0x7ffff000\ns_mul_i32 s68, %0, 40960\ns_lshl_b32 s69, s70, 10\ns_add_u32 s68, s68, s69\ns_add_u32 s68, s68, 4096\n"
"s_add_u32 s94, s16, s68\ns_addc_u32 s95, s17, 0\ns_and_b32 s95, s95, 0xffff\ns_mov_b32 s75, 37888\ns_mov_b32 s58, 4194304\ns_mov_b32 s67, 1\ns_branch .Lround2\n.Lhook:\n"
".Lpg8:\ns_cbranch_execz .Ltr10\ns_mov_b64 exec, 0\ns_cmp_eq_u32 s70, 0\ns_cbranch_scc1 .Lpg10\ns_cmp_eq_u32 s70, 1\ns_cbranch_scc1 .Lpg11\ns_cmp_eq_u32 s70, 2\n"
"s_cbranch_scc1 .Lpg12\ns_branch .Lpg13\n.Ltr6:\ns_mov_b64 exec, -1\ns_mov_b64 exec, 0\ns_cmp_eq_u32 s70, 0\ns_cbranch_scc1 .Lpg14\ns_cmp_eq_u32 s70, 1\n"
"s_cbranch_scc1 .Lpg15\ns_cmp_eq_u32 s70, 2\ns_cbranch_scc1 .Lpg16\ns_branch .Lpg17\n.Ltr8:\ns_mov_b64 exec, -1\ns_branch .Lhookret\n.Lf1_w:\n"
"s_mov_b32 s97, 0\n.Lf1_l:\ns_sleep 1\nbuffer_load_dword v78, v76, s[24:27], 0 offen sc1\ns_waitcnt vmcnt(0)\ns_add_u32 s97, s97, 1\ns_cmp_gt_u32 s97, 1048576\ns_cbranch_scc1 .Lf1_r\n"
"v_sub_u32 v66, v78, v65\nv_cmp_gt_u32 vcc, 1, v66\ns_and_b64 vcc, vcc, s[28:29]\ns_cbranch_scc1 .Lf1_l\ns_branch .Lf1_r\n.Lack_w:\ns_mov_b32 s97, 0\n.Lack_l:\n"
"s_sleep 1\nv_bfe_u32 v66, v60, 4, 3\ns_lshl_b32 s68, s19, 7\nv_lshlrev_b32 v66, 2, v66\ns_add_u32 s68, s68, 5242880\nbuffer_load_dword v78, v66, s[24:27], s68 offen offset:1024 sc1\ns_waitcnt vmcnt(0)\ns_add_u32 s97, s97, 1\n"
"s_cmp_gt_u32 s97, 1048576\ns_cbranch_scc1 .Lack_ok\nv_xor_b32 v78, s30, v78\nv_and_b32 v66, 0x0fffffff, v78\nv_cmp_ne_u32 vcc, 0, v66\ns_cmp_eq_u64 vcc, 0\ns_cbranch_scc0 .Lack_l\ns_branch .Lack_ok\n"
".Lsp0:\ns_sub_u32 s75, s77, s74\ns_cmp_lt_u32 s75, 1024\ns_cbranch_scc0 .Lrp0\ns_memrealtime s[74:75]\ns_waitcnt lgkmcnt(0)\ns_branch .Lsp0\n.Lsa1:\n"
"ds_read_b32 v66, v74\ns_waitcnt lgkmcnt(0)\nv_cmp_gt_u32 vcc, v75, v66\ns_cbranch_vccnz .Lsa1\ns_branch .Lra1\n.Lsp1:\ns_sub_u32 s75, s77, s74\ns_cmp_lt_u32 s75, 1024\n"
"s_cbranch_scc0 .Lrp1\ns_memrealtime s[74:75]\ns_waitcnt lgkmcnt(0)\ns_branch .Lsp1\n.Lsa2:\nds_read_b32 v66, v74\ns_waitcnt lgkmcnt(0)\nv_cmp_gt_u32 vcc, v75, v66\n"
"s_cbranch_vccnz .Lsa2\ns_branch .Lra2\n.Lsp2:\ns_sub_u32 s75, s77, s74\ns_cmp_lt_u32 s75, 1024\ns_cbranch_scc0 .Lrp2\ns_memrealtime s[74:75]\ns_waitcnt lgkmcnt(0)\n"
"s_branch .Lsp2\n.Lsa3:\nds_read_b32 v66, v74\ns_waitcnt lgkmcnt(0)\nv_cmp_gt_u32 vcc, v75, v66\ns_cbranch_vccnz .Lsa3\ns_branch .Lra3\n.Lsa4:\n"
"ds_read_b32 v66, v74\ns_waitcnt lgkmcnt(0)\nv_cmp_gt_u32 vcc, v75, v66\ns_cbranch_vccnz .Lsa4\ns_branch .Lra4\n.Lsp4:\ns_sub_u32 s75, s77, s74\ns_cmp_lt_u32 s75, 1024\n"
"s_cbranch_scc0 .Lrp4\ns_memrealtime s[74:75]\ns_waitcnt lgkmcnt(0)\ns_branch .Lsp4\n.Lsa5:\nds_read_b32 v66, v74\ns_waitcnt lgkmcnt(0)\nv_cmp_gt_u32 vcc, v75, v66\n"
"s_cbranch_vccnz .Lsa5\ns_branch .Lra5\n.Lsa30:\nds_read_b32 v66, v74\ns_waitcnt lgkmcnt(0)\nv_cmp_gt_u32 vcc, v75, v66\ns_cbranch_vccnz .Lsa30\ns_branch .Lra30\n"
".Lsa31:\nds_read_b32 v66, v74\ns_waitcnt lgkmcnt(0)\nv_cmp_gt_u32 vcc, v75, v66\ns_cbranch_vccnz .Lsa31\ns_branch .Lra31\n.Lsa32:\nds_read_b32 v66, v74\n"
"s_waitcnt lgkmcnt(0)\nv_cmp_gt_u32 vcc, v75, v66\ns_cbranch_vccnz .Lsa32\ns_branch .Lra32\n.Lsa30x:\nds_read_b32 v66, v74\ns_waitcnt lgkmcnt(0)\nv_cmp_gt_u32 vcc, v75, v66\n"
"s_cbranch_vccnz .Lsa30x\ns_branch .Lra30x\n.Lsa31x:\nds_read_b32 v66, v74\ns_waitcnt lgkmcnt(0)\nv_cmp_gt_u32 vcc, v75, v66\ns_cbranch_vccnz .Lsa31x\ns_branch .Lra31x\n"
".Lso40:\nds_read_b32 v66, v74\ns_waitcnt lgkmcnt(0)\nv_cmp_gt_u32 vcc, v75, v66\ns_cbranch_vccnz .Lso40\ns_branch .Lro40\n.Lso180:\nds_read_b32 v66, v74\n"
"s_waitcnt lgkmcnt(0)\nv_cmp_gt_u32 vcc, v75, v66\ns_cbranch_vccnz .Lso180\ns_branch .Lro180\n.Lso320:\nds_read_b32 v66, v74\ns_waitcnt lgkmcnt(0)\nv_cmp_gt_u32 vcc, v75, v66\n"
"s_cbranch_vccnz .Lso320\ns_branch .Lro320\n.Lso460:\nds_read_b32 v66, v74\ns_waitcnt lgkmcnt(0)\nv_cmp_gt_u32 vcc, v75, v66\ns_cbranch_vccnz .Lso460\ns_branch .Lro460\n"
".Lso600:\nds_read_b32 v66, v74\ns_waitcnt lgkmcnt(0)\nv_cmp_gt_u32 vcc, v75, v66\ns_cbranch_vccnz .Lso600\ns_branch .Lro600\n.Lso740:\nds_read_b32 v66, v74\n"
"s_waitcnt lgkmcnt(0)\nv_cmp_gt_u32 vcc, v75, v66\ns_cbranch_vccnz .Lso740\ns_branch .Lro740\n.Lso880:\nds_read_b32 v66, v74\ns_waitcnt lgkmcnt(0)\nv_cmp_gt_u32 vcc, v75, v66\n"
"s_cbranch_vccnz .Lso880\ns_branch .Lro880\n.Lso1020:\nds_read_b32 v66, v74\ns_waitcnt lgkmcnt(0)\nv_cmp_gt_u32 vcc, v75, v66\ns_cbranch_vccnz .Lso1020\ns_branch .Lro1020\n"
".Lso1160:\nds_read_b32 v66, v74\ns_waitcnt lgkmcnt(0)\nv_cmp_gt_u32 vcc, v75, v66\ns_cbranch_vccnz .Lso1160\ns_branch .Lro1160\n.Lso1300:\nds_read_b32 v66, v74\n"
"s_waitcnt lgkmcnt(0)\nv_cmp_gt_u32 vcc, v75, v66\ns_cbranch_vccnz .Lso1300\ns_branch .Lro1300\n.Lso1440:\nds_read_b32 v66, v74\ns_waitcnt lgkmcnt(0)\nv_cmp_gt_u32 vcc, v75, v66\n"
"s_cbranch_vccnz .Lso1440\ns_branch .Lro1440\n.Lso1455:\nds_read_b32 v66, v74\ns_waitcnt lgkmcnt(0)\nv_cmp_gt_u32 vcc, v75, v66\ns_cbranch_vccnz .Lso1455\ns_branch .Lro1455\n"
".Lso1525r0:\nds_read_b32 v66, v74\ns_waitcnt lgkmcnt(0)\nv_cmp_gt_u32 vcc, v75, v66\ns_cbranch_vccnz .Lso1525r0\ns_branch .Lro1525r0\n.Lso1595r0:\nds_read_b32 v66, v74\n"
"s_waitcnt lgkmcnt(0)\nv_cmp_gt_u32 vcc, v75, v66\ns_cbranch_vccnz .Lso1595r0\ns_branch .Lro1595r0\n.Lso1665r0:\nds_read_b32 v66, v74\ns_waitcnt lgkmcnt(0)\nv_cmp_gt_u32 vcc, v75, v66\n"
"s_cbranch_vccnz .Lso1665r0\ns_branch .Lro1665r0\n.Lso1735r0:\nds_read_b32 v66, v74\ns_waitcnt lgkmcnt(0)\nv_cmp_gt_u32 vcc, v75, v66\ns_cbranch_vccnz .Lso1735r0\ns_branch .Lro1735r0\n"
".Lso1805r0:\nds_read_b32 v66, v74\ns_waitcnt lgkmcnt(0)\nv_cmp_gt_u32 vcc, v75, v66\ns_cbranch_vccnz .Lso1805r0\ns_branch .Lro1805r0\n.Lso1878r0:\nds_read_b32 v66, v74\n"
"s_waitcnt lgkmcnt(0)\nv_cmp_gt_u32 vcc, v75, v66\ns_cbranch_vccnz .Lso1878r0\ns_branch .Lro1878r0\n.Lso1920r0:\nds_read_b32 v66, v74\ns_waitcnt lgkmcnt(0)\nv_cmp_gt_u32 vcc, v75, v66\n"
"s_cbranch_vccnz .Lso1920r0\ns_branch .Lro1920r0\n.Lso1960r0:\nds_read_b32 v66, v74\ns_waitcnt lgkmcnt(0)\nv_cmp_gt_u32 vcc, v75, v66\ns_cbranch_vccnz .Lso1960r0\ns_branch .Lro1960r0\n"
".Lso2046r0:\nds_read_b32 v66, v74\ns_waitcnt lgkmcnt(0)\nv_cmp_gt_u32 vcc, v75, v66\ns_cbranch_vccnz .Lso2046r0\ns_branch .Lro2046r0\n.Lso2088r0:\nds_read_b32 v66, v74\n"
"s_waitcnt lgkmcnt(0)\nv_cmp_gt_u32 vcc, v75, v66\ns_cbranch_vccnz .Lso2088r0\ns_branch .Lro2088r0\n.Lso2172r0:\nds_read_b32 v66, v74\ns_waitcnt lgkmcnt(0)\nv_cmp_gt_u32 vcc, v75, v66\n"
"s_cbranch_vccnz .Lso2172r0\ns_branch .Lro2172r0\n.Lso2222r0:\nds_read_b32 v66, v74\ns_waitcnt lgkmcnt(0)\nv_cmp_gt_u32 vcc, v75, v66\ns_cbranch_vccnz .Lso2222r0\ns_branch .Lro2222r0\n"
".Lso1525r1:\nds_read_b32 v66, v74\ns_waitcnt lgkmcnt(0)\nv_cmp_gt_u32 vcc, v75, v66\ns_cbranch_vccnz .Lso1525r1\ns_branch .Lro1525r1\n.Lso1595r1:\nds_read_b32 v66, v74\n"
"s_waitcnt lgkmcnt(0)\nv_cmp_gt_u32 vcc, v75, v66\ns_cbranch_vccnz .Lso1595r1\ns_branch .Lro1595r1\n.Lso1665r1:\nds_read_b32 v66, v74\ns_waitcnt lgkmcnt(0)\nv_cmp_gt_u32 vcc, v75, v66\n"
"s_cbranch_vccnz .Lso1665r1\ns_branch .Lro1665r1\n.Lso1735r1:\nds_read_b32 v66, v74\ns_waitcnt lgkmcnt(0)\nv_cmp_gt_u32 vcc, v75, v66\ns_cbranch_vccnz .Lso1735r1\ns_branch .Lro1735r1\n"
".Lso1805r1:\nds_read_b32 v66, v74\ns_waitcnt lgkmcnt(0)\nv_cmp_gt_u32 vcc, v75, v66\ns_cbranch_vccnz .Lso1805r1\ns_branch .Lro1805r1\n.Lso1840r1:\nds_read_b32 v66, v74\n"
"s_waitcnt lgkmcnt(0)\nv_cmp_gt_u32 vcc, v75, v66\ns_cbranch_vccnz .Lso1840r1\ns_branch .Lro1840r1\n.Lso1878r1:\nds_read_b32 v66, v74\ns_waitcnt lgkmcnt(0)\nv_cmp_gt_u32 vcc, v75, v66\n"
"s_cbranch_vccnz .Lso1878r1\ns_branch .Lro1878r1\n.Lso1920r1:\nds_read_b32 v66, v74\ns_waitcnt lgkmcnt(0)\nv_cmp_gt_u32 vcc, v75, v66\ns_cbranch_vccnz .Lso1920r1\ns_branch .Lro1920r1\n"
".Lso1945r1:\nds_read_b32 v66, v74\ns_waitcnt lgkmcnt(0)\nv_cmp_gt_u32 vcc, v75, v66\ns_cbranch_vccnz .Lso1945r1\ns_branch .Lro1945r1\n.Lso2004r1:\nds_read_b32 v66, v74\n"
"s_waitcnt lgkmcnt(0)\nv_cmp_gt_u32 vcc, v75, v66\ns_cbranch_vccnz .Lso2004r1\ns_branch .Lro2004r1\n.Lso2045r1:\nds_read_b32 v66, v74\ns_waitcnt lgkmcnt(0)\nv_cmp_gt_u32 vcc, v75, v66\n"
"s_cbranch_vccnz .Lso2045r1\ns_branch .Lro2045r1\n.Lso2130r1:\nds_read_b32 v66, v74\ns_waitcnt lgkmcnt(0)\nv_cmp_gt_u32 vcc, v75, v66\ns_cbranch_vccnz .Lso2130r1\ns_branch .Lro2130r1\n"
".Lso2172r1:\nds_read_b32 v66, v74\ns_waitcnt lgkmcnt(0)\nv_cmp_gt_u32 vcc, v75, v66\ns_cbranch_vccnz .Lso2172r1\ns_branch .Lro2172r1\n.Lso2225r1:\nds_read_b32 v66, v74\n"
"s_waitcnt lgkmcnt(0)\nv_cmp_gt_u32 vcc, v75, v66\ns_cbranch_vccnz .Lso2225r1\ns_branch .Lro2225r1\n.Ltail1:\n.Lpg9:\ns_cbranch_execz .Ltr10\nQcvaa 0,128,32,56\n"
"v_add_u32 v75, 1, v75\ns_lshr_b32 s88, s19, 2\nQmvaa 0,132,36\ns_waitcnt vmcnt(0)\nds_write_b32 v73, v75\nQmvaa 0,136,40\nQe 8\nQmvaa 0,140,44\n"
"Qe 9\nQcvaa 4,124,44,56\nQe 10\nQmvaa 4,120,40\nQe 11\nQmvaa 4,116,36\nQe 12\nQmvaa 4,112,32\n"
"s_waitcnt lgkmcnt(1)\nQk 40,8,9\nQmvva 96,44,144\nQe 13\nQmvva 96,36,148\nQe 14\nQmvva 100,44,152\nQe 15\n"
"Qmvva 100,36,156\nQk 41,10,11\nQk 42,12,13\nQmvva 104,44,160\nQk 43,14,15\ns_lshl_b32 s88, s88, 4\nQmvva 104,36,164\nQe 0\n"
"Qmvva 108,44,168\nQe 1\nQmvva 108,36,172\nQe 2\nQmvva 140,48,168\nQe 3\nQmvva 140,40,172\ns_add_u32 s88, s88, s93\n"
"s_mul_i32 s68, s88, 163840\nQmvva 136,48,160\nds_read_b32 v66, v74\ns_and_b32 s69, s19, 3\nQmvva 136,40,164\ns_mul_i32 s69, s69, 320\ns_add_u32 s68, s68, s69\nQmvva 132,48,152\n"
"s_add_u32 s52, s16, s68\ns_addc_u32 s53, s17, 0\nQmvva 132,40,156\ns_and_b32 s53, s53, 0xffff\ns_sub_u32 s54, 5246976, s68\nQmvva 128,48,144\ns_and_b32 s87, s19, 3\ns_mul_i32 s87, s87, 327680\n"
"Qmvva 128,40,148\ns_waitcnt lgkmcnt(0)\nv_cmp_gt_u32 vcc, v75, v66\nv_smfmac_f32_16x16x64_bf16 a[176:179], a[204:207], v[36:43], v77\ns_cbranch_vccnz .Lsa30x\n.Lra30x:\nQcvaa 8,112,48,56\ns_add_u32 s87, s87, 1310720\n"
"s_mul_i32 s69, s92, 10240\nQmvaa 8,116,52\nv_bfe_u32 v78, v60, 5, 3\nv_mul_u32_u24 v78, 1280, v78\nQmvaa 8,120,56\nv_and_b32 v67, 16, v60\nv_add3_u32 v67, v67, v78, s69\nQmvaa 8,124,60\n"
"s_and_b32 s69, s19, 1\nv_lshrrev_b32 v78, 8, v60\nQcvaa 12,140,60,56\ns_lshl_b32 s68, s93, 1\nv_xor_b32 v78, s68, v78\nQmvaa 12,136,56\nv_lshl_add_u32 v78, v78, 5, v67\ns_lshr_b32 s68, 128, s69\n"
"Qmvaa 12,132,52\nv_add_u32 v61, s68, v78\nQk 46,0,1\nQmvaa 12,128,48\ns_add_u32 m0, s96, 0\ns_add_u32 s95, s87, 4096\nQk 47,2,3\nQcvaa 0,128,64,56\n"
"buffer_load_dwordx4 v61, s[52:55], s95 offen lds\nQmvaa 0,132,68\nQe 4\nQmvaa 0,136,72\nQe 5\nQe 6\nQe 7\nQmvaa 0,140,76\n"
"s_add_u32 s95, s87, 23552\nQk 44,4,5\nQk 45,6,7\nQcvaa 4,124,76,56\nbuffer_load_dwordx4 v61, s[52:55], s95 offen offset:1024 lds\nQmvaa 4,120,72\nQe 8\nQmvaa 4,116,68\n"
"Qe 9\nQmvaa 4,112,64\ns_add_u32 m0, s96, 8192\ns_add_u32 s95, s87, 45056\ns_mul_i32 s68, s69, 192\nQmvva 160,52,144\nbuffer_load_dwordx4 v61, s[52:55], s95 offen lds\nQmvva 160,44,148\n"
"Qe 10\nQmvva 164,52,152\nQe 11\nQmvva 164,44,156\ns_add_u32 s95, s87, 64512\nv_add_u32 v63, s68, v78\nQmvva 168,52,160\nbuffer_load_dwordx4 v61, s[52:55], s95 offen offset:1024 lds\n"
"Qmvva 168,44,164\nQe 12\nQmvva 172,52,168\nQe 13\nQe 14\nQe 15\nv_bfe_u32 v78, v60, 8, 1\nv_xor_b32 v78, s93, v78\n"
"v_lshl_add_u32 v67, v78, 5, v67\nv_lshrrev_b32 v78, 9, v60\nv_mul_u32_u24 v78, 20480, v78\ns_xor_b32 s69, s69, 1\ns_lshl_b32 s69, s69, 8\nv_add3_u32 v62, v78, v67, s69\nQmvva 172,44,172\ns_add_u32 m0, s96, 16384\n"
"s_add_u32 s95, s87, 4096\nQk 48,8,9\nQk 49,10,11\nQk 50,12,13\nQk 51,14,15\nQmvva 204,20,168\nbuffer_load_dwordx4 v62, s[52:55], s95 offen lds\nQmvva 204,48,172\n"
"Qe 0\nQmvva 200,20,160\nQe 1\nQmvva 200,48,164\ns_add_u32 s95, s87, 44032\nQk 18,0,1\nQmvva 196,20,152\nbuffer_load_dwordx4 v62, s[52:55], s95 offen offset:1024 lds\n"
"Qmvva 196,48,156\nQe 2\nQmvva 192,20,144\nQe 3\nQmvva 192,48,148\nQk 19,2,3\ns_andn2_b32 s68, s71, 786432\nv_smfmac_f32_16x16x64_bf16 a[180:183], a[204:207], v[44:51], v77\n"
"s_add_u32 m0, s96, 24576\ns_add_u32 s95, s87, 4096\nQcvaa 8,112,80,56\nbuffer_load_dwordx4 v63, s[52:55], s95 offen lds\nQmvaa 8,116,84\nQe 4\nQmvaa 8,120,88\ns_add_u32 s95, s87, 23552\n"
"Qmvaa 8,124,92\nbuffer_load_dwordx4 v63, s[52:55], s95 offen offset:1024 lds\nQcvaa 12,140,92,56\nQe 5\nQmvaa 12,136,88\nQe 6\nQmvaa 12,132,84\ns_add_u32 m0, s96, 32768\n"
"s_add_u32 s95, s87, 45056\nQk 16,4,5\nQmvaa 12,128,80\nbuffer_load_dwordx4 v63, s[52:55], s95 offen lds\nQcvaa 0,128,96,56\nQda 128,68,34048\nQmvaa 0,132,100\nQda 132,68,34560\n"
"Qmvaa 0,136,104\nQda 136,68,34304\nQe 7\nQmvaa 0,140,108\nQda 140,68,34816\ns_add_u32 s95, s87, 64512\nQk 17,6,7\nQcvaa 4,124,108,56\n"
"Qda 124,68,33792\nbuffer_load_dwordx4 v63, s[52:55], s95 offen offset:1024 lds\nQmvaa 4,120,104\nQda 120,68,33280\nQmvaa 4,116,100\nQda 116,68,33536\nbuffer_load_dword v78, v76, s[24:27], 0 offen sc1\nQmvaa 4,112,96\n"
"Qda 112,68,33024\ns_add_u32 m0, s96, 40960\ns_mov_b32 s95, 4096\nQmvva 224,24,144\nbuffer_load_dwordx4 v61, s[52:55], s95 offen lds\nQmvva 224,16,148\nQe 8\nQmvva 228,24,152\n"
"Qe 9\nQmvva 228,16,156\nQe 10\nQmvva 232,24,160\ns_mov_b32 s95, 23552\nQk 20,8,9\nQmvva 232,16,164\nbuffer_load_dwordx4 v61, s[52:55], s95 offen offset:1024 lds\n"
"Qmvva 236,24,168\nQe 11\nQmvva 236,16,172\ns_waitcnt lgkmcnt(0)\nQk 21,10,11\nQcvaa 8,112,0,56\nQe 12\nQmvaa 8,116,4\n"
"v_subrev_u32 v68, 66048, v68\ns_add_u32 m0, s96, 49152\ns_mov_b32 s95, 45056\nQmvaa 8,120,8\nbuffer_load_dwordx4 v61, s[52:55], s95 offen lds\nQe 13\nQe 14\nQe 15\n"
"Qmvaa 8,124,12\nQk 22,12,13\nQk 23,14,15\nQcvaa 12,140,12,56\nQe 0\nQmvaa 12,136,8\n.Lpg10:\ns_cbranch_execz .Ltr6\n"
"Qe 1\nQmvaa 12,132,4\nQe 2\nQmvaa 12,128,0\nQe 3\nv_smfmac_f32_16x16x64_bf16 a[184:187], a[204:207], v[16:23], v77\ns_mov_b32 s95, 64512\nQmava 208,28,144\n"
"buffer_load_dwordx4 v61, s[52:55], s95 offen offset:1024 lds\nQmava 208,20,148\nQe 4\nQmava 212,28,152\nQe 5\nQmava 212,20,156\nQe 6\nQmava 216,28,160\n"
"s_add_u32 m0, s96, 57344\ns_mov_b32 s95, 4096\nQk 26,0,1\nQmava 216,20,164\nbuffer_load_dwordx4 v62, s[52:55], s95 offen lds\nQmava 220,28,168\nQe 7\nQmava 220,20,172\n"
"Qk 27,2,3\nQk 24,4,5\nQk 25,6,7\nQmava 252,32,168\nQta 168,71,768\nQta 170,71,1792\nQmava 252,24,172\nQe 8\n"
"Qmava 248,32,160\nQta 172,71,33792\nQta 174,71,34816\ns_mov_b32 s95, 44032\nQmava 248,24,164\nbuffer_load_dwordx4 v62, s[52:55], s95 offen offset:1024 lds\nQmava 244,32,152\nQta 160,71,512\n"
"Qta 162,71,1536\nQmava 244,24,156\nQe 9\nQmava 240,32,144\nQta 164,71,33536\nQta 166,71,34560\ns_add_u32 m0, s96, 132096\ns_mov_b32 s95, 4096\n"
"Qmava 240,24,148\nbuffer_load_dwordx4 v63, s[52:55], s95 offen lds\nQcvaa 0,112,16,56\nQta 152,71,256\nQta 154,71,1280\nQmvaa 0,116,20\nQe 10\nQmvaa 0,120,24\n"
"Qta 156,71,33280\nQta 158,71,34304\nQmvaa 0,124,28\nQe 11\nQcvaa 4,140,28,56\nQta 144,71,0\nQta 146,71,1024\ns_mov_b32 s95, 23552\n"
"Qmvaa 4,136,24\nbuffer_load_dwordx4 v63, s[52:55], s95 offen offset:1024 lds\nQmvaa 4,132,20\nQta 148,71,33024\nQta 150,71,34048\nQmvaa 4,128,16\nQk 28,8,9\nQk 29,10,11\n"
"Qcvaa 8,128,32,56\nv_add_u32 v75, 1, v75\nQmvaa 8,132,36\nds_write_b32 v73, v75\nQmvaa 8,136,40\nQe 12\nQe 13\nQe 14\n"
"Qe 15\nQmvaa 8,140,44\nQk 30,12,13\nQk 31,14,15\nQcvaa 12,124,44,56\nQe 0\nQmvaa 12,120,40\nQe 1\n"
"Qmvaa 12,116,36\nQe 2\nQmvaa 12,112,32\ns_waitcnt lgkmcnt(1)\nQk 32,0,1\nv_smfmac_f32_16x16x64_bf16 a[188:191], a[204:207], v[24:31], v77\nQmvva 80,36,144\nQe 3\n"
"Qmvva 80,28,148\nQe 4\nQmvva 84,36,152\nQe 5\nQmvva 84,28,156\nQe 6\nQmvva 88,36,160\nQe 7\n"
"Qmvva 88,28,164\nQk 33,2,3\nQk 34,4,5\nQmvva 92,36,168\nQk 35,6,7\nQmvva 92,28,172\nQe 8\nQmvva 124,40,168\n"
"Qe 9\nQmvva 124,32,172\nds_read_b32 v66, v74\nQk 38,8,9\nQmvva 120,40,160\nQe 10\nQmvva 120,32,164\nQe 11\n"
"Qmvva 116,40,152\nQk 39,10,11\nQmvva 116,32,156\nQe 12\nQmvva 112,40,144\ns_waitcnt lgkmcnt(0)\nv_cmp_gt_u32 vcc, v75, v66\nQmvva 112,32,148\n"
"s_cbranch_vccnz .Lsa31x\n.Lra31x:\nQcvaa 0,112,48,56\nQe 13\nQmvaa 0,116,52\nQe 14\nQmvaa 0,120,56\nQe 15\n"
"Qmvaa 0,124,60\ns_add_u32 m0, s96, 140288\ns_mov_b32 s95, 45056\nQk 36,12,13\nQcvaa 4,140,60,56\nbuffer_load_dwordx4 v63, s[52:55], s95 offen lds\nQmvaa 4,136,56\nQk 37,14,15\n"
"Qmvaa 4,132,52\ns_add_u32 s48, s20, s68\ns_addc_u32 s49, s21, 0\nQmvaa 4,128,48\ns_mov_b32 s95, 64512\ns_and_b32 s49, s49, 0xffff\nQcvaa 8,128,64,56\nbuffer_load_dwordx4 v63, s[52:55], s95 offen offset:1024 lds\n"
"Qmvaa 8,132,68\nQe 0\nQmvaa 8,136,72\nQe 1\nQmvaa 8,140,76\ns_add_u32 m0, s96, 148480\ns_add_u32 s95, s87, 86016\nQk 40,0,1\n"
"Qcvaa 12,124,76,56\nbuffer_load_dwordx4 v61, s[52:55], s95 offen lds\nQmvaa 12,120,72\nQe 2\nQmvaa 12,116,68\nQe 3\nQmvaa 12,112,64\nQe 4\n"
"v_smfmac_f32_16x16x64_bf16 a[192:195], a[204:207], v[32:39], v77\ns_add_u32 s95, s87, 105472\nQmvva 144,44,144\nbuffer_load_dwordx4 v61, s[52:55], s95 offen offset:1024 lds\nQmvva 144,36,148\nQe 5\nQmvva 148,44,152\ns_add_u32 m0, s96, 65536\n"
"s_add_u32 s95, s87, 126976\nQk 41,2,3\nQmvva 148,36,156\nbuffer_load_dwordx4 v61, s[52:55], s95 offen lds\nQmvva 152,44,160\nQe 6\nQmvva 152,36,164\nQe 7\n"
"Qmvva 156,44,168\nQk 42,4,5\nQk 43,6,7\nQmvva 156,36,172\nQe 8\nQmvva 188,48,168\ns_add_u32 s95, s87, 146432\ns_sub_u32 s50, 4194304, s68\n"
"Qmvva 188,40,172\nbuffer_load_dwordx4 v61, s[52:55], s95 offen offset:1024 lds\nQmvva 184,48,160\nQe 9\nQmvva 184,40,164\nQe 10\nQmvva 180,48,152\nQe 11\n"
"Qmvva 180,40,156\nQk 46,8,9\nQk 47,10,11\nQmvva 176,48,144\ns_add_u32 m0, s96, 73728\ns_add_u32 s95, s87, 86016\ns_add_u32 s71, s71, 1048576\nQmvva 176,40,148\n"
"buffer_load_dwordx4 v62, s[52:55], s95 offen lds\nQcvaa 0,112,80,56\nQe 12\nQmvaa 0,116,84\nQe 13\nQmvaa 0,120,88\ns_add_u32 s95, s87, 125952\nQk 44,12,13\n"
"Qmvaa 0,124,92\nbuffer_load_dwordx4 v62, s[52:55], s95 offen offset:1024 lds\nQcvaa 4,140,92,56\nQe 14\nQmvaa 4,136,88\nQe 15\nQmvaa 4,132,84\ns_waitcnt vmcnt(16)\n"
"v_sub_u32 v66, v78, v65\nv_cmp_gt_u32 vcc, 1, v66\ns_and_b64 vcc, vcc, s[28:29]\ns_cbranch_scc1 .Lf1_w\n.Lf1_r:\nQmvaa 4,128,80\ns_add_u32 m0, s96, 81920\ns_add_u32 s95, s87, 86016\n"
"Qk 45,14,15\nQcvaa 8,128,96,56\nbuffer_load_dwordx4 v63, s[52:55], s95 offen lds\nQmvaa 8,132,100\nQe 0\nQmvaa 8,136,104\nQe 1\nQmvaa 8,140,108\n"
"Qe 2\nQcvaa 12,124,108,56\nQe 3\nQmvaa 12,120,104\nQk 48,0,1\nQk 49,2,3\nQmvaa 12,116,100\nQe 4\n"
"Qmvaa 12,112,96\nQe 5\nv_smfmac_f32_16x16x64_bf16 a[196:199], a[204:207], v[40:47], v77\ns_add_u32 s95, s87, 105472\nQmvva 208,16,144\nQta 112,71,33152\nbuffer_load_dwordx4 v63, s[52:55], s95 offen offset:1024 lds\nQmvva 208,44,148\n"
"Qta 114,71,34176\nQk 50,4,5\nQmvva 212,16,152\nQta 116,71,33408\ns_add_u32 m0, s96, 90112\ns_add_u32 s95, s87, 126976\nQmvva 212,44,156\nQta 118,71,34432\n"
"buffer_load_dwordx4 v63, s[52:55], s95 offen lds\nQmvva 216,16,160\nQta 120,71,33664\nQmvva 216,44,164\nQta 122,71,34688\nQmvva 220,16,168\nQta 124,71,33920\ns_add_u32 s95, s87, 146432\n"
"Qmvva 220,44,172\nQta 126,71,34944\nbuffer_load_dwordx4 v63, s[52:55], s95 offen offset:1024 lds\nQe 6\nQe 7\ns_nop 0\nQk 51,6,7\nQmvva 240,20,144\n"
"Qe 8\nQmvva 240,48,148\nQe 9\nQmvva 244,20,152\nQe 10\nQmvva 244,48,156\nQe 11\nQmvva 248,20,160\n"
"Qk 54,8,9\nQk 55,10,11\nQmvva 248,48,164\nQe 12\nQmvva 252,20,168\nQe 13\nQmvva 252,48,172\nQe 14\n"
"Qk 52,12,13\nQe 15\ns_nop 0\nQk 53,14,15\ns_nop 1\nv_smfmac_f32_16x16x64_bf16 a[200:203], a[204:207], v[48:55], v77\nQmava 224,24,144\nv_accvgpr_read_b32 v0, a176\n"
"v_accvgpr_read_b32 v1, a177\nQmava 224,52,148\nv_accvgpr_read_b32 v6, a178\nv_accvgpr_read_b32 v7, a179\nQmava 228,24,152\nv_accvgpr_read_b32 v8, a180\nv_accvgpr_read_b32 v9, a181\nQmava 228,52,156\n"
"v_accvgpr_read_b32 v14, a182\nv_accvgpr_read_b32 v15, a183\nQmava 232,24,160\nv_accvgpr_read_b32 v16, a184\nv_accvgpr_read_b32 v17, a185\nQmava 232,52,164\nv_accvgpr_read_b32 v22, a186\nv_accvgpr_read_b32 v23, a187\n"
"Qmava 236,24,168\nv_accvgpr_read_b32 v2, a190\nv_accvgpr_read_b32 v3, a191\nQmava 236,52,172\ns_waitcnt lgkmcnt(0)\nv_accvgpr_read_b32 v24, a188\nQmvva 96,28,112\nv_accvgpr_read_b32 v25, a189\n"
"v_accvgpr_read_b32 v4, a192\nQmvva 100,28,116\nv_accvgpr_read_b32 v5, a193\nv_accvgpr_read_b32 v10, a194\nQmvva 104,28,120\nv_accvgpr_read_b32 v11, a195\nv_accvgpr_read_b32 v12, a196\nQmvva 108,28,124\n"
"v_accvgpr_read_b32 v13, a197\nv_accvgpr_read_b32 v18, a198\nQmvva 128,32,112\nv_accvgpr_read_b32 v19, a199\nv_accvgpr_read_b32 v20, a200\nQmvva 132,32,116\nv_accvgpr_read_b32 v21, a201\nv_accvgpr_read_b32 v26, a202\n"
"Qmvva 136,32,120\nv_accvgpr_read_b32 v27, a203\nQa 8,8,9\nQmvva 140,32,124\nQa 10,10,11\nQa 8,8,10\nQmvva 160,36,112\nQa 16,16,17\n"
"Qa 18,18,19\nQmvva 164,36,116\nQa 16,16,18\nQa 4,4,5\nQmvva 168,36,120\nQa 6,6,7\nQa 4,4,6\nQmvva 172,36,124\n"
"Qa 12,12,13\nQa 14,14,15\nQmvva 192,40,112\nQa 12,12,14\nQa 0,0,1\nQmvva 196,40,116\nQa 2,2,3\nQa 0,0,2\n"
"Qmvva 200,40,120\nQa 24,24,25\nQa 26,26,27\nQmvva 204,40,124\nQa 24,24,26\nQa 20,20,21\nQmvva 224,44,112\nQa 22,22,23\n"
"Qa 20,20,22\nQmvva 228,44,116\nv_max3_u32 v9, v8, v16, v4\nv_max3_u32 v9, v9, v12, v0\nQmvva 232,44,120\nv_max3_u32 v9, v9, v24, v20\nv_min3_u32 v11, v8, v16, v4\nQmvva 236,44,124\n"
"v_min3_u32 v11, v11, v12, v0\nv_min3_u32 v11, v11, v24, v20\nQmava 208,48,112\nv_cmp_lt_u32 vcc, 0x4e800000, v9\ns_mov_b64 s[74:75], vcc\nQmava 212,48,116\nv_cmp_gt_u32 vcc, 0x35800000, v11\ns_or_b64 s[74:75], s[74:75], vcc\n"
"Qmava 216,48,120\nv_mov_b32 v30, s73\nv_mov_b32 v31, 0\nQmava 220,48,124\nv_mov_b32 v58, 0xb9800000\nv_mov_b32 v59, 0\nQmava 240,52,112\nv_and_b32 v66, 16, v60\n"
"v_mov_b32 v67, 0xffff\nQmava 244,52,116\nv_lshlrev_b32 v66, v66, v67\ns_mov_b32 vcc_lo, 0x00c0000c\nQmava 248,52,120\ns_mov_b32 vcc_hi, 0xc0000c00\nv_cndmask_b32 v67, 0, v66, vcc\nQmava 252,52,124\n"
"s_lshr_b64 vcc, vcc, 2\nv_cndmask_b32 v66, 0, v66, vcc\ns_lshl_b32 s68, s93, 12\ns_add_u32 s68, s68, %4\nv_lshrrev_b32 v69, 9, v60\nv_lshlrev_b32 v69, 11, v69\nv_bfe_u32 v70, v60, 1, 8\nv_add3_u32 v68, v69, v70, s68\n"
"v_add_u32 v69, 65536, v68\nv_add_u32 v70, 131072, v68\ns_cmp_lg_u64 s[74:75], 0\ns_cbranch_scc1 .Lfb\n.Lfbret1:\nv_rcp_f32 v2, v0\nv_rcp_f32_e64 v6, v4\nv_mfma_f32_16x16x4_4b_bf16 v[80:95], v[30:31], v[58:59], v[80:95]\n"
"v_rcp_f32 v10, v8\nv_rcp_f32 v14, v12\nv_rcp_f32 v18, v16\nv_rcp_f32 v22, v20\nv_rcp_f32 v26, v24\nv_mov_b64 v[0:1], 0\nv_mfma_f32_16x16x4_4b_bf16 v[112:127], v[30:31], v[58:59], v[112:127]\nQk 2,2,2\n"
"v_and_b32 v3, v2, v67\nv_and_b32 v2, v2, v66\nv_mov_b64_e64 v[4:5], 0\nQk 6,6,6\nv_and_b32_e64 v7, v6, v67\nv_mfma_f32_16x16x4_4b_bf16 v[144:159], v[30:31], v[58:59], v[144:159]\nv_and_b32 v6, v6, v66\nv_mov_b64 v[8:9], 0\n"
"Qk 10,10,10\nv_and_b32 v11, v10, v67\nv_and_b32 v10, v10, v66\nv_mov_b64_e64 v[12:13], 0\nv_mfma_f32_16x16x4_4b_bf16 v[176:191], v[30:31], v[58:59], v[176:191]\nQk 14,14,14\nv_and_b32 v15, v14, v67\nv_and_b32 v14, v14, v66\n"
"v_mov_b64_e64 v[16:17], 0\nQk 18,18,18\nv_and_b32_e64 v19, v18, v67\nv_mfma_f32_16x16x4_4b_bf16 v[208:223], v[30:31], v[58:59], v[208:223]\nv_and_b32 v18, v18, v66\nv_mov_b64 v[20:21], 0\nQk 22,22,22\nv_and_b32 v23, v22, v67\n"
"v_and_b32 v22, v22, v66\nv_mov_b64_e64 v[24:25], 0\nv_mfma_f32_16x16x4_4b_bf16 v[240:255], v[30:31], v[58:59], v[240:255]\nQk 26,26,26\nv_and_b32 v27, v26, v67\nv_and_b32 v26, v26, v66\nv_mov_b64_e64 v[28:29], 0\nQk 80,80,81\n"
"Qk 81,82,83\nQk 82,84,85\nQk 83,86,87\nQk 112,112,113\nQk 113,114,115\nQzvvv 84,80,0\nQk 114,116,117\nQk 115,118,119\n"
"Qzvvv 80,80,2\nQk 144,144,145\nQk 145,146,147\nQzvvv 116,112,4\nQk 146,148,149\nQk 147,150,151\nQzvvv 112,112,6\nQk 176,176,177\n"
"Qk 177,178,179\nv_mfma_f32_16x16x4_4b_bf16 a[224:239], v[30:31], v[58:59], a[224:239]\nQk 80,80,81\nQk 81,82,83\nQk 82,84,85\nQk 83,86,87\nQk 178,180,181\nQk 179,182,183\n"
"Qzvvv 148,144,8\nQk 112,112,113\nQk 113,114,115\nQzvvv 144,144,10\nQk 114,116,117\nQk 115,118,119\nv_mfma_f32_16x16x4_4b_bf16 v[96:111], v[30:31], v[58:59], v[96:111]\nQk 208,208,209\n"
"Qk 209,210,211\nQk 210,212,213\nQk 211,214,215\nQk 240,240,241\nQk 144,144,145\nQzvvv 180,176,12\nQk 145,146,147\nQk 146,148,149\n"
"Qzvvv 176,176,14\nQk 147,150,151\nQk 241,242,243\nv_mfma_f32_16x16x4_4b_bf16 v[128:143], v[30:31], v[58:59], v[128:143]\nQk 242,244,245\nQk 243,246,247\nv_accvgpr_read_b32 v84, a224\nv_accvgpr_read_b32 v85, a225\n"
"v_accvgpr_read_b32 v86, a226\nQk 176,176,177\nQzvvv 212,208,16\nQk 177,178,179\nQk 178,180,181\nQzvvv 208,208,18\nQk 179,182,183\nv_accvgpr_read_b32 v87, a227\n"
"v_mfma_f32_16x16x4_4b_bf16 v[160:175], v[30:31], v[58:59], v[160:175]\nv_accvgpr_read_b32 v32, a228\nv_accvgpr_read_b32 v33, a229\nv_accvgpr_read_b32 v34, a230\nv_accvgpr_read_b32 v35, a231\nQk 84,84,85\nQk 85,86,87\nQzvvv 244,240,20\n"
"Qk 86,32,33\nQk 87,34,35\nQzvvv 240,240,22\nQk 208,208,209\nQk 209,210,211\nv_mfma_f32_16x16x4_4b_bf16 v[192:207], v[30:31], v[58:59], v[192:207]\nQk 210,212,213\nQk 211,214,215\n"
"Qk 88,88,89\nQk 89,90,91\nQk 90,92,93\nQk 91,94,95\nQzvvv 32,84,24\nQk 240,240,241\nQk 241,242,243\nQzvvv 84,84,26\n"
"Qk 242,244,245\nQk 243,246,247\nv_mfma_f32_16x16x4_4b_bf16 v[224:239], v[30:31], v[58:59], v[224:239]\nQk 120,120,121\nQk 121,122,123\nQk 122,124,125\nQk 123,126,127\nQk 152,152,153\n"
"Qk 84,84,85\nQzvvv 92,88,0\nQk 85,86,87\nQk 86,32,33\nQzvvv 88,88,2\nQk 87,34,35\nQk 153,154,155\nv_mfma_f32_16x16x4_4b_bf16 a[208:223], v[30:31], v[58:59], a[208:223]\n"
"Qk 154,156,157\nQk 155,158,159\ns_waitcnt vmcnt(21)\ns_barrier\nQk 184,184,185\nQk 88,88,89\nQzvvv 124,120,4\nQk 89,90,91\n"
"Qk 90,92,93\nQzvvv 120,120,6\nQk 91,94,95\nQk 185,186,187\nv_mfma_f32_16x16x4_4b_bf16 a[240:255], v[30:31], v[58:59], a[240:255]\nQk 186,188,189\nQk 187,190,191\nQta 56,68,0\n"
"Qk 216,216,217\nQk 217,218,219\nQk 120,120,121\nQzvvv 156,152,8\nQk 121,122,123\nQk 122,124,125\nQzvvv 152,152,10\nQk 123,126,127\n"
".Lpg11:\ns_cbranch_execz .Ltr6\ns_nop 0\nQk 218,220,221\nQzvvv 188,184,12\nQk 219,222,223\nQta 58,68,1024\nQzvvv 184,184,14\n"
"Qk 248,248,249\nQk 249,250,251\nQk 152,152,153\nQk 153,154,155\nQzvvv 220,216,16\nQk 154,156,157\nQk 155,158,159\nQzvvv 216,216,18\n"
"Qk 250,252,253\nQk 251,254,255\nQk 184,184,185\nQk 185,186,187\nQzvvv 252,248,20\nQk 186,188,189\nQk 187,190,191\nQzvvv 248,248,22\n"
"Qta 60,68,8192\nv_accvgpr_read_b32 v116, a232\nv_accvgpr_read_b32 v117, a233\nv_accvgpr_read_b32 v118, a234\nv_accvgpr_read_b32 v119, a235\nv_accvgpr_read_b32 v36, a236\nv_accvgpr_read_b32 v37, a237\nv_accvgpr_read_b32 v38, a238\n"
"v_accvgpr_read_b32 v39, a239\nQk 116,116,117\nQk 117,118,119\nQk 118,36,37\nQk 119,38,39\nQk 216,216,217\nQk 217,218,219\nQzvvv 36,116,24\n"
"Qk 218,220,221\nQk 219,222,223\nQzvvv 116,116,26\nQta 62,68,9216\ns_mov_b32 s31, 0\ns_nop 0\nQk 96,96,97\nQk 97,98,99\n"
"Qk 98,100,101\nQk 99,102,103\nQk 248,248,249\nQk 249,250,251\nQzvvv 100,96,0\nQk 250,252,253\nQk 251,254,255\nQzvvv 96,96,2\n"
"buffer_load_dwordx4 a[0:3], v79, s[48:51], s31 offen offset:0 sc1\nQta 64,68,256\nQk 128,128,129\nQk 129,130,131\nQk 130,132,133\nQk 131,134,135\nQk 116,116,117\nQk 117,118,119\n"
"Qzvvv 132,128,4\nQk 118,36,37\nQk 119,38,39\nQzvvv 128,128,6\nbuffer_load_dwordx4 a[4:7], v79, s[48:51], s31 offen offset:1024 sc1\nQta 66,68,1280\ns_waitcnt lgkmcnt(2)\ns_nop 0\n"
"Qzvav 56,56,80\nQk 160,160,161\nQk 161,162,163\nQmvav 56,60,88\nQk 162,164,165\nQk 163,166,167\nbuffer_load_dwordx4 a[8:11], v79, s[48:51], s31 offen offset:2048 sc1\nQta 68,68,8448\n"
"Qzvvv 164,160,8\nQk 192,192,193\nQk 193,194,195\nQzvvv 160,160,10\nQk 194,196,197\nQk 195,198,199\nbuffer_load_dwordx4 a[12:15], v79, s[48:51], s31 offen offset:3072 sc1\nQta 70,68,9472\n"
"Qzvvv 196,192,12\nv_max3_f32 v66, |v56|, |v57|, |v58|\nv_max_f32 v66, v66, |v59|\nQzvvv 192,192,14\nv_mov_b32_e64 v67, v66\ns_mov_b32 s31, 4096\nQk 224,224,225\nQk 225,226,227\n"
"Qk 226,228,229\nQk 227,230,231\nbuffer_load_dwordx4 a[16:19], v79, s[48:51], s31 offen offset:0 sc1\nv_permlane32_swap_b32_e64 v66, v67\nQzvvv 228,224,16\nv_max_f32 v66, v66, v67\nv_mov_b32 v67, v66\nQzvvv 224,224,18\n"
"v_accvgpr_read_b32 v148, a208\nv_accvgpr_read_b32 v149, a209\nv_accvgpr_read_b32 v150, a210\nv_accvgpr_read_b32 v151, a211\nv_accvgpr_read_b32 v40, a212\nv_accvgpr_read_b32 v41, a213\nv_accvgpr_read_b32 v42, a214\nv_accvgpr_read_b32 v43, a215\n"
"Qk 148,148,149\nQk 149,150,151\nQk 150,40,41\nQk 151,42,43\nbuffer_load_dwordx4 a[20:23], v79, s[48:51], s31 offen offset:1024 sc1\nv_permlane16_swap_b32_e64 v66, v67\nQzvvv 40,148,20\nv_max_f32_e64 v66, v66, v67\n"
"v_accvgpr_read_b32 v180, a240\nQzvvv 148,148,22\nv_accvgpr_read_b32 v181, a241\nv_accvgpr_read_b32 v182, a242\nv_accvgpr_read_b32 v183, a243\nv_accvgpr_read_b32 v44, a244\nv_accvgpr_read_b32 v45, a245\nv_accvgpr_read_b32 v46, a246\n"
"v_accvgpr_read_b32 v47, a247\nQk 180,180,181\nQk 181,182,183\nQk 182,44,45\nQk 183,46,47\nbuffer_load_dwordx4 a[24:27], v79, s[48:51], s31 offen offset:2048 sc1\nv_max_f32_dpp v66, v66, v66 row_ror:8 row_mask:0xf bank_mask:0xf\nQzvvv 44,180,24\n"
"s_mov_b32 s31, 16384\nQk 104,104,105\nQzvvv 180,180,26\nQk 105,106,107\nQk 106,108,109\nQk 107,110,111\nbuffer_load_dwordx4 a[28:31], v79, s[48:51], s31 offen offset:0 sc1\nv_max_f32_dpp v66, v66, v66 row_ror:4 row_mask:0xf bank_mask:0xf\n"
"Qzvvv 108,104,0\nQk 136,136,137\nQk 137,138,139\nQzvvv 104,104,2\nQk 138,140,141\nQk 139,142,143\nbuffer_load_dwordx4 a[32:35], v79, s[48:51], s31 offen offset:1024 sc1\nv_max_f32_dpp v66, v66, v66 row_ror:2 row_mask:0xf bank_mask:0xf\n"
"Qzvvv 140,136,4\nQk 168,168,169\nQk 169,170,171\nQzvvv 136,136,6\nQk 170,172,173\nQk 171,174,175\nbuffer_load_dwordx4 a[36:39], v79, s[48:51], s31 offen offset:2048 sc1\nv_max_f32_dpp v66, v66, v66 row_ror:1 row_mask:0xf bank_mask:0xf\n"
"Qzvvv 172,168,8\nv_and_b32 v66, 0x7f800000, v66\nv_add_u32 v66, 0x3000000, v66\nQzvvv 168,168,10\nQk 200,200,201\nQk 201,202,203\nQk 202,204,205\nQk 203,206,207\n"
"buffer_load_dwordx4 a[40:43], v79, s[48:51], s31 offen offset:3072 sc1\nv_min_u32 v66, 0x7e000000, v66\nQzvvv 204,200,12\nv_or_b32 v66, 0x400000, v66\nds_write_b32 v73, v66 offset:16\nQzvvv 200,200,14\nv_xor_b32 v67, 0x80000000, v66\ns_mov_b32 s31, 20480\n"
"Qk 232,232,233\nQk 233,234,235\nQk 234,236,237\nQk 235,238,239\nbuffer_load_dwordx4 a[44:47], v79, s[48:51], s31 offen offset:0 sc1\nv_accvgpr_write_b32 a112, v67\nQzvvv 236,232,16\nv_accvgpr_write_b32 a113, v67\n"
"v_accvgpr_write_b32 a114, v67\nQzvvv 232,232,18\nv_accvgpr_write_b32 a115, v67\nv_accvgpr_read_b32 v212, a216\nv_accvgpr_read_b32 v213, a217\nv_accvgpr_read_b32 v214, a218\nv_accvgpr_read_b32 v215, a219\nv_accvgpr_read_b32 v48, a220\n"
"v_accvgpr_read_b32 v49, a221\nv_accvgpr_read_b32 v50, a222\nv_accvgpr_read_b32 v51, a223\nQk 212,212,213\nQk 213,214,215\nQk 214,48,49\nQk 215,50,51\nbuffer_load_dwordx4 a[48:51], v79, s[48:51], s31 offen offset:1024 sc1\n"
"v_accvgpr_read_b32 v244, a248\nQzvvv 48,212,20\nv_accvgpr_read_b32 v245, a249\nv_accvgpr_read_b32 v246, a250\nQzvvv 212,212,22\nv_accvgpr_read_b32 v247, a251\nv_accvgpr_read_b32 v52, a252\nv_accvgpr_read_b32 v53, a253\n"
"v_accvgpr_read_b32 v54, a254\nv_accvgpr_read_b32 v55, a255\nQk 244,244,245\nQk 245,246,247\nQk 246,52,53\nQk 247,54,55\nbuffer_load_dwordx4 a[52:55], v79, s[48:51], s31 offen offset:2048 sc1\ns_nop 0\n"
"s_nop 0\nQzvvv 52,244,24\nQzvvv 244,244,26\ns_waitcnt lgkmcnt(5)\nQcaav 140,56,84,112\ns_add_u32 m0, s96, 98304\ns_mov_b32 s95, 86016\nQmaav 140,60,116\n"
"buffer_load_dwordx4 v61, s[52:55], s95 offen lds\nQcaav 136,56,240,112\ns_mov_b32 s56, s20\ns_and_b32 s57, s21, 0xffff\nQmaav 136,60,248\ns_mov_b32 s58, 4194304\nQk 244,244,245\nQcaav 132,56,208,112\n"
"Qta 72,68,16384\nQk 245,246,247\nQmaav 132,60,216\ns_mov_b32 s95, 105472\nQk 246,52,53\nQcaav 128,56,176,112\nbuffer_load_dwordx4 v61, s[52:55], s95 offen offset:1024 lds\nQmaav 128,60,184\n"
"Qta 74,68,16896\nQk 247,54,55\nQcaav 124,56,144,112\nQk 212,212,213\nQk 213,214,215\nQmaav 124,60,152\nQk 214,48,49\nQk 215,50,51\n"
"Qcaav 120,56,112,112\nQta 76,68,17408\nQk 180,180,181\nQmaav 120,60,120\ns_add_u32 s88, s71, 0\nQk 181,182,183\nQcaav 116,56,80,112\nbuffer_store_dwordx4 v[80:83], v79, s[56:59], s88 offen offset:0 sc1\n"
"Qmaav 116,60,88\nQta 78,68,17920\nQk 182,44,45\ns_waitcnt lgkmcnt(5)\nQcaav 144,64,80,112\nQk 183,46,47\nQk 148,148,149\nQmaav 144,68,88\n"
"Qk 149,150,151\nQk 150,40,41\nQcaav 148,64,112,112\nQk 151,42,43\nQk 96,96,97\nQmaav 148,68,120\nQk 97,98,99\nQk 98,100,101\n"
"Qcaav 152,64,144,112\nQta 56,68,24576\nQk 99,102,103\nQmaav 152,68,152\ns_add_u32 m0, s96, 106496\ns_mov_b32 s95, 126976\nQcaav 156,64,176,112\nbuffer_load_dwordx4 v61, s[52:55], s95 offen lds\n"
"Qmaav 156,68,184\nQta 58,68,25600\nQk 168,168,169\nQcaav 160,64,208,112\nQk 169,170,171\nQk 170,172,173\nQmaav 160,68,216\nQk 171,174,175\n"
"Qk 136,136,137\nQcaav 164,64,240,112\nQta 60,68,32768\ns_mov_b32 s95, 146432\nQmaav 164,68,248\nbuffer_load_dwordx4 v61, s[52:55], s95 offen offset:1024 lds\nQcaav 168,64,84,112\nQk 137,138,139\n"
"Qk 138,140,141\nQmaav 168,68,116\nQta 62,68,33792\nQk 139,142,143\ns_waitcnt lgkmcnt(4)\nQcaav 196,72,84,112\nQk 104,104,105\nQk 105,106,107\n"
"Qmaav 196,76,116\ns_add_u32 m0, s96, 114688\ns_mov_b32 s95, 86016\nQcaav 192,72,240,112\nbuffer_load_dwordx4 v62, s[52:55], s95 offen lds\nQmaav 192,76,248\nQk 106,108,109\nQk 107,110,111\n"
"Qcaav 188,72,208,112\nQta 64,68,24832\nQk 224,224,225\nQmaav 188,76,216\nbuffer_store_dwordx4 v[112:115], v79, s[56:59], s88 offen offset:1024 sc1\nQcaav 184,72,176,112\nQk 225,226,227\nQk 226,228,229\n"
"Qmaav 184,76,184\nQta 66,68,25856\nQk 227,230,231\nQcaav 180,72,144,112\nQk 192,192,193\nQk 193,194,195\nQmaav 180,76,152\nQk 194,196,197\n"
"Qk 195,198,199\nQcaav 176,72,112,112\nQta 68,68,33024\nv_add_u32 v75, 1, v75\nQmaav 176,76,120\ns_waitcnt vmcnt(35)\nds_write_b32 v73, v75\nQcaav 172,72,80,112\n"
"Qk 160,160,161\nQk 161,162,163\nQmaav 172,76,88\nQta 70,68,34048\ns_mov_b32 s95, 125952\ns_waitcnt lgkmcnt(5)\nQcaav 200,56,80,112\nbuffer_load_dwordx4 v62, s[52:55], s95 offen offset:1024 lds\n"
"Qmaav 200,60,88\nQk 162,164,165\nQk 163,166,167\nQcaav 204,56,112,112\nQk 128,128,129\nQk 129,130,131\nQmaav 204,60,120\nQk 130,132,133\n"
"Qk 131,134,135\nQcaav 208,56,144,112\nQta 72,68,512\nQk 200,200,201\nQmaav 208,60,152\nds_read_b32 v66, v74\ns_add_u32 m0, s96, 122880\ns_mov_b32 s95, 86016\n"
"Qcaav 212,56,176,112\nbuffer_load_dwordx4 v63, s[52:55], s95 offen lds\nQmaav 212,60,184\nQta 74,68,1536\nQk 201,202,203\nQcaav 216,56,208,112\nQk 202,204,205\nQk 203,206,207\n"
"Qmaav 216,60,216\ns_waitcnt lgkmcnt(1)\nv_cmp_gt_u32 vcc, v75, v66\nQcaav 220,56,240,112\ns_cbranch_vccnz .Lso40\n.Lro40:\nQmaav 220,60,248\nQta 76,68,8704\n"
"Qk 232,232,233\nQcaav 224,56,84,112\nbuffer_store_dwordx4 v[144:147], v79, s[56:59], s88 offen offset:2048 sc1\nv_xor_b32 v56, 8, v73\nQmaav 224,60,116\nds_read_b32 v56, v56 offset:16\nQk 233,234,235\ns_waitcnt lgkmcnt(5)\n"
"Qcaav 252,64,84,112\nQta 78,68,9728\nQk 234,236,237\nQmaav 252,68,116\nQk 235,238,239\nQcaav 248,64,240,112\nQmaav 248,68,248\nQcaav 244,64,208,112\n"
"Qta 56,68,768\nQmaav 244,68,216\ns_mov_b32 s95, 105472\nQcaav 240,64,176,112\nbuffer_load_dwordx4 v63, s[52:55], s95 offen offset:1024 lds\nQmaav 240,68,184\nQta 58,68,1792\ns_waitcnt lgkmcnt(3)\n"
"v_mov_b32 v57, v56\nQcaav 236,64,144,112\nv_mov_b32 v58, v56\nv_mov_b32 v59, v56\nQmaav 236,68,152\nv_accvgpr_write_b32 a80, v56\nv_accvgpr_write_b32 a81, v56\nQcaav 232,64,112,112\n"
"Qta 60,68,8960\nv_accvgpr_write_b32 a82, v56\nQmaav 232,68,120\nv_accvgpr_write_b32 a83, v56\nQcaav 228,64,80,112\nQmaav 228,68,88\nQta 62,68,9984\ns_waitcnt lgkmcnt(4)\n"
"Qcvav 0,72,80,56\nQmvav 0,76,88\nQcvav 4,72,112,56\nbuffer_store_dwordx4 v[176:179], v79, s[56:59], s88 offen offset:3072 sc1\nQmvav 4,76,120\nQcvav 8,72,144,56\nQta 64,68,16640\nQmvav 8,76,152\n"
"Qcvav 12,72,176,56\nQmvav 12,76,184\nQta 66,68,17152\nQcvav 16,72,208,56\nQmvav 16,76,216\nQcvav 20,72,240,56\nQta 68,68,17664\nQmvav 20,76,248\n"
"Qcvav 24,72,84,56\nQmvav 24,76,116\nQta 70,68,18176\ns_waitcnt lgkmcnt(4)\nQcvav 52,56,84,56\nQmvav 52,60,116\nQcvav 48,56,240,56\nQmvav 48,60,248\n"
"Qcvav 44,56,208,56\nQta 72,68,25088\nQmvav 44,60,216\nQcvav 40,56,176,56\nQmvav 40,60,184\nQta 74,68,26112\ns_add_u32 s88, s71, 4096\nQcvav 36,56,144,56\n"
"buffer_store_dwordx4 v[208:211], v79, s[56:59], s88 offen offset:0 sc1\nQmvav 36,60,152\nQcvav 32,56,112,56\nQta 76,68,33280\nQmvav 32,60,120\nQcvav 28,56,80,56\nQmvav 28,60,88\nQta 78,68,34304\n"
"s_waitcnt lgkmcnt(4)\nQcvav 92,64,80,56\nQmvav 92,68,88\nQcvav 124,64,112,56\ns_lshl_b32 s46, s19, 4\nQmvav 124,68,120\ns_cmp_eq_u32 s70, 0\ns_cselect_b32 s46, s46, -1\n"
"Qcvav 156,64,144,56\nQta 56,68,25344\nv_mov_b32 v77, 0x7ffff000\nQmvav 156,68,152\nQcvav 188,64,176,56\nQmvav 188,68,184\nQta 58,68,26368\nQcvav 220,64,208,56\n"
"Qmvav 220,68,216\nQcvav 252,64,240,56\nQta 60,68,33536\nQmvav 252,68,248\nQcvav 100,64,84,56\nQmvav 100,68,116\nQta 62,68,34560\ns_waitcnt lgkmcnt(4)\n"
"Qcvav 172,72,84,56\nbuffer_store_dwordx4 v[240:243], v79, s[56:59], s88 offen offset:1024 sc1\nQmvav 172,76,116\nQcvav 140,72,240,56\nQmvav 140,76,248\nQcvav 108,72,208,56\nQta 64,68,40960\nQmvav 108,76,216\n"
"Qcvav 228,72,176,56\nQmvav 228,76,184\nQta 66,68,41984\nQcvav 196,72,144,56\nQmvav 196,76,152\nQcvav 164,72,112,56\nQta 68,68,49152\nQmvav 164,76,120\n"
"Qcvav 132,72,80,56\nQmvav 132,76,88\nQta 70,68,50176\ns_waitcnt lgkmcnt(4)\nQcvav 204,56,80,56\nQmvav 204,60,88\nQcvav 236,56,112,56\nQmvav 236,60,120\n"
"Qcaav 96,56,144,80\nQta 72,68,41216\nQmaav 96,60,152\nQcaav 100,56,176,80\nbuffer_store_dwordx4 v[84:87], v79, s[56:59], s88 offen offset:2048 sc1\nQmaav 100,60,184\nQta 74,68,42240\nQcaav 104,56,208,80\n"
"Qmaav 104,60,216\nQcaav 108,56,240,80\nQta 76,68,49408\nQmaav 108,60,248\nQcaav 112,56,84,80\nQmaav 112,60,116\nQta 78,68,50432\ns_waitcnt vmcnt(15)\n"
"s_waitcnt lgkmcnt(4)\nQmaaa 140,64,24\nQmaaa 140,68,52\nQmaaa 136,64,20\nQmaaa 136,68,48\nQmaaa 132,64,16\nQta 56,68,41472\nQmaaa 132,68,44\n"
"Qmaaa 128,64,12\nQmaaa 128,68,40\nQta 58,68,42496\nQmaaa 124,64,8\nQmaaa 124,68,36\n.Lpg12:\ns_cbranch_execz .Ltr6\nQmaaa 120,64,4\n"
"Qta 60,68,49664\ns_and_b32 s68, s19, 3\nQmaaa 120,68,32\ns_add_u32 s88, s71, 16384\nQmaaa 116,64,0\nbuffer_store_dwordx4 v[88:91], v79, s[56:59], s88 offen offset:0 sc1\nQmaaa 116,68,28\nQta 62,68,50688\n"
"s_waitcnt lgkmcnt(4)\nQmaaa 144,72,0\ns_cmp_lt_u32 s68, 1\ns_cselect_b32 s68, 40, 32\nQmaaa 144,76,28\ns_mul_i32 s67, s68, 40960\ns_mul_i32 s74, s68, 32768\nQmaaa 148,72,4\n"
"s_and_b32 s68, s19, 3\nQmaaa 148,76,32\ns_cmp_lt_u32 s68, 2\ns_cselect_b32 s68, 48, 40\nQmaaa 152,72,8\nQta 64,68,41728\ns_mul_i32 s72, s68, 40960\nQmaaa 152,76,36\n"
"s_mul_i32 s75, s68, 32768\ns_and_b32 s68, s19, 3\nQmaaa 156,72,12\ns_cmp_lt_u32 s68, 3\ns_cselect_b32 s68, 56, 48\nQmaaa 156,76,40\nQta 66,68,42752\ns_mul_i32 s73, s68, 40960\n"
"Qmaaa 160,72,16\ns_mul_i32 s77, s68, 32768\nv_add_u32 v58, s91, v60\nQmaaa 160,76,44\ns_mul_i32 s68, s93, 28672\ns_sub_u32 s68, 14336, s68\nQmaaa 164,72,20\nQta 68,68,49920\n"
"v_add_u32 v59, s68, v58\nQmaaa 164,76,48\nQmaaa 168,72,24\nQmaaa 168,76,52\nQta 70,68,50944\ns_waitcnt lgkmcnt(4)\nQmvaa 24,56,24\nQmvaa 24,60,52\n"
"Qmvaa 20,56,20\nQmvaa 20,60,48\nQmvaa 16,56,16\nQta 72,68,57344\nQmvaa 16,60,44\nbuffer_store_dwordx4 v[120:123], v79, s[56:59], s88 offen offset:1024 sc1\nQmvaa 12,56,12\nQmvaa 12,60,40\n"
"Qta 74,68,57856\nQmvaa 8,56,8\nQmvaa 8,60,36\nQmvaa 4,56,4\nQta 76,68,58368\nv_add_u32 v75, 1, v75\nQmvaa 4,60,32\ns_waitcnt vmcnt(35)\n"
"ds_write_b32 v73, v75\nQmvaa 0,56,0\nQmvaa 0,60,28\nQta 78,68,58880\ns_waitcnt lgkmcnt(5)\nQmvaa 28,64,0\nQmvaa 28,68,28\nQmvaa 32,64,4\n"
"Qmvaa 32,68,32\nQmvaa 36,64,8\nQta 56,68,57600\nQmvaa 36,68,36\nds_read_b32 v66, v74\nQmvaa 40,64,12\nQmvaa 40,68,40\nQta 58,68,58112\n"
"Qmvaa 44,64,16\nQmvaa 44,68,44\ns_waitcnt lgkmcnt(1)\nv_cmp_gt_u32 vcc, v75, v66\nQmvaa 48,64,20\ns_cbranch_vccnz .Lso180\n.Lro180:\nQmvaa 48,68,48\n"
"Qta 60,68,58624\nQmvaa 52,64,24\nbuffer_store_dwordx4 v[152:155], v79, s[56:59], s88 offen offset:2048 sc1\nQmvaa 52,68,52\ns_waitcnt lgkmcnt(4)\nQmaaa 196,72,24\nQta 62,68,59136\nQmaaa 196,76,52\n"
"Qmaaa 192,72,20\nQmaaa 192,76,48\nQmaaa 188,72,16\nQta 64,70,1024\nQmaaa 188,76,44\ns_add_u32 m0, s96, 0\ns_mov_b32 s95, 126976\nQmaaa 184,72,12\n"
"buffer_load_dwordx4 v63, s[52:55], s95 offen lds\nQmaaa 184,76,40\nQta 66,70,2048\nQmaaa 180,72,8\nQmaaa 180,76,36\nQmaaa 176,72,4\nQta 68,70,9216\ns_mov_b32 s95, 146432\n"
"Qmaaa 176,76,32\nbuffer_load_dwordx4 v63, s[52:55], s95 offen offset:1024 lds\nQmaaa 172,72,0\nQmaaa 172,76,28\nQta 70,70,10240\ns_waitcnt lgkmcnt(4)\nQmvaa 92,56,0\nQmvaa 92,60,28\n"
"Qmvaa 124,56,4\nbuffer_store_dwordx4 v[184:187], v79, s[56:59], s88 offen offset:3072 sc1\nQmvaa 124,60,32\nQmvaa 156,56,8\nQta 72,70,1280\nQmvaa 156,60,36\nQmvaa 188,56,12\nQmvaa 188,60,40\n"
"Qta 74,70,2304\nQmvaa 220,56,16\nQmvaa 220,60,44\nQmvaa 252,56,20\nQta 76,70,9472\ns_add_u32 m0, s96, 8192\ns_mov_b32 s95, 331776\nQmvaa 252,60,48\n"
"buffer_load_dwordx4 v61, s[52:55], s95 offen lds\nQmvaa 100,56,24\nQmvaa 100,60,52\nQta 78,70,10496\ns_waitcnt lgkmcnt(4)\nQmaaa 224,64,24\nQmaaa 224,68,52\ns_mov_b32 s95, 351232\n"
"Qmaaa 220,64,20\nbuffer_load_dwordx4 v61, s[52:55], s95 offen offset:1024 lds\nQmaaa 220,68,48\nQmaaa 216,64,16\nQta 56,70,1536\nQmaaa 216,68,44\nQmaaa 212,64,12\nQmaaa 212,68,40\n"
"Qta 58,70,2560\ns_add_u32 s88, s71, 20480\nQmaaa 208,64,8\nbuffer_store_dwordx4 v[216:219], v79, s[56:59], s88 offen offset:0 sc1\nQmaaa 208,68,36\nQmaaa 204,64,4\nQta 60,70,9728\nQmaaa 204,68,32\n"
"Qmaaa 200,64,0\nQmaaa 200,68,28\nQta 62,70,10752\ns_waitcnt lgkmcnt(4)\nQmaaa 228,72,0\nQmaaa 228,76,28\ns_add_u32 m0, s96, 16384\ns_mov_b32 s95, 372736\n"
"Qmaaa 232,72,4\nbuffer_load_dwordx4 v61, s[52:55], s95 offen lds\nQmaaa 232,76,32\nQmaaa 236,72,8\nQta 64,70,1792\nQmaaa 236,76,36\nQmaaa 240,72,12\nQmaaa 240,76,40\n"
"Qta 66,70,2816\ns_mov_b32 s95, 392192\nQmaaa 244,72,16\nbuffer_load_dwordx4 v61, s[52:55], s95 offen offset:1024 lds\nQmaaa 244,76,44\nQmaaa 248,72,20\nQta 68,70,9984\nQmaaa 248,76,48\n"
"Qmaaa 252,72,24\nQmaaa 252,76,52\nQta 70,70,11008\ns_waitcnt lgkmcnt(4)\nQmvaa 172,56,24\nbuffer_store_dwordx4 v[248:251], v79, s[56:59], s88 offen offset:1024 sc1\nQmvaa 172,60,52\nQmvaa 140,56,20\n"
"Qmvaa 140,60,48\nQmvaa 108,56,16\nQta 72,70,17408\nQmvaa 108,60,44\nQmvaa 228,56,12\nQmvaa 228,60,40\nQta 74,70,18432\ns_add_u32 m0, s96, 24576\n"
"s_mov_b32 s95, 331776\nQmvaa 196,56,8\nbuffer_load_dwordx4 v62, s[52:55], s95 offen lds\nQmvaa 196,60,36\nQmvaa 164,56,4\nQta 76,69,0\nQmvaa 164,60,32\nQmvaa 132,56,0\n"
"Qmvaa 132,60,28\nQta 78,69,1024\ns_mov_b32 s95, 371712\ns_waitcnt lgkmcnt(4)\nQmvaa 204,64,0\nbuffer_load_dwordx4 v62, s[52:55], s95 offen offset:1024 lds\nQmvaa 204,68,28\nQmvaa 236,64,4\n"
"Qmvaa 236,68,32\nQmaaa 96,64,8\nQta 56,70,17664\nQmaaa 96,68,36\nQmaaa 100,64,12\nbuffer_store_dwordx4 v[116:119], v79, s[56:59], s88 offen offset:2048 sc1\nv_cmp_eq_u32 s[44:45], s46, v60\ns_nop 1\n"
"v_cndmask_b32 v77, v77, v76, s[44:45]\nQmaaa 100,68,40\nQta 58,70,18688\nQmaaa 104,64,16\nQmaaa 104,68,44\nQmaaa 108,64,20\nQta 60,69,256\nQmaaa 108,68,48\n"
"Qmaaa 112,64,24\nQmaaa 112,68,52\nQta 62,69,1280\ns_mov_b32 s31, 131072\ns_waitcnt lgkmcnt(4)\nQmaav 140,72,180\nbuffer_load_dwordx4 a[0:3], v79, s[48:51], s31 offen offset:0 sc1\nQmaav 140,76,244\n"
"Qmaav 136,72,148\nQmaav 136,76,212\nQmaav 132,72,224\nQta 64,70,17920\nQmaav 132,76,232\nbuffer_load_dwordx4 a[4:7], v79, s[48:51], s31 offen offset:1024 sc1\nQmaav 128,72,192\nQmaav 128,76,200\n"
"Qta 66,70,18944\nQmaav 124,72,160\nQmaav 124,76,168\nbuffer_load_dwordx4 a[8:11], v79, s[48:51], s31 offen offset:2048 sc1\nQmaav 120,72,128\nQta 68,69,512\nQmaav 120,76,136\nQmaav 116,72,96\n"
"Qmaav 116,76,104\nQta 70,69,1536\ns_waitcnt lgkmcnt(4)\nQmaav 144,56,96\nbuffer_load_dwordx4 a[12:15], v79, s[48:51], s31 offen offset:3072 sc1\nQmaav 144,60,104\nQmaav 148,56,128\nQmaav 148,60,136\n"
"Qmaav 152,56,160\nQta 72,70,18176\ns_mov_b32 s31, 135168\nQmaav 152,60,168\nbuffer_load_dwordx4 a[16:19], v79, s[48:51], s31 offen offset:0 sc1\nQmaav 156,56,192\nQmaav 156,60,200\nQta 74,70,19200\n"
"Qmaav 160,56,224\nQmaav 160,60,232\nbuffer_load_dwordx4 a[20:23], v79, s[48:51], s31 offen offset:1024 sc1\nQmaav 164,56,148\nQta 76,69,768\nQmaav 164,60,212\nQmaav 168,56,180\nQmaav 168,60,244\n"
"Qta 78,69,1792\ns_waitcnt lgkmcnt(4)\nQmvav 24,64,180\nbuffer_load_dwordx4 a[24:27], v79, s[48:51], s31 offen offset:2048 sc1\nQmvav 24,68,244\nQmvav 20,64,148\nQmvav 20,68,212\nQmvav 16,64,224\n"
"Qta 56,69,8192\ns_mov_b32 s31, 147456\nQmvav 16,68,232\nbuffer_load_dwordx4 a[28:31], v79, s[48:51], s31 offen offset:0 sc1\nQmvav 12,64,192\nQmvav 12,68,200\nQta 58,69,8704\nQmvav 8,64,160\n"
"Qmvav 8,68,168\nbuffer_load_dwordx4 a[32:35], v79, s[48:51], s31 offen offset:1024 sc1\nQmvav 4,64,128\nQta 60,69,9216\nv_add_u32 v75, 1, v75\nQmvav 4,68,136\ns_waitcnt vmcnt(31)\nds_write_b32 v73, v75\n"
"Qmvav 0,64,96\nQmvav 0,68,104\nQta 62,69,9728\ns_waitcnt lgkmcnt(5)\nQmvav 28,72,96\nbuffer_load_dwordx4 a[36:39], v79, s[48:51], s31 offen offset:2048 sc1\nQmvav 28,76,104\nQmvav 32,72,128\n"
"Qmvav 32,76,136\nQmvav 36,72,160\nQta 64,69,8448\nQmvav 36,76,168\nds_read_b32 v66, v74\nQmvav 40,72,192\nbuffer_load_dwordx4 a[40:43], v79, s[48:51], s31 offen offset:3072 sc1\nQmvav 40,76,200\n"
"Qta 66,69,8960\nQmvav 44,72,224\nQmvav 44,76,232\ns_waitcnt lgkmcnt(1)\nv_cmp_gt_u32 vcc, v75, v66\nQmvav 48,72,148\ns_cbranch_vccnz .Lso320\n.Lro320:\n"
"Qmvav 48,76,212\nQta 68,69,9472\ns_mov_b32 s31, 151552\nQmvav 52,72,180\nbuffer_load_dwordx4 a[44:47], v79, s[48:51], s31 offen offset:0 sc1\nQmvav 52,76,244\ns_waitcnt lgkmcnt(4)\nQmaav 196,56,180\n"
"Qta 70,69,9984\nQmaav 196,60,244\nQmaav 192,56,148\nbuffer_load_dwordx4 a[48:51], v79, s[48:51], s31 offen offset:1024 sc1\nQmaav 192,60,212\nQmaav 188,56,224\nQta 72,69,16384\nQmaav 188,60,232\n"
"Qmaav 184,56,192\nbuffer_load_dwordx4 a[52:55], v79, s[48:51], s31 offen offset:2048 sc1\nQmaav 184,60,200\nQta 74,69,17408\nQmaav 180,56,160\nQmaav 180,60,168\nQmaav 176,56,128\nQta 76,69,24576\n"
"s_mov_b32 s31, 262144\nQmaav 176,60,136\nbuffer_load_dwordx4 v[80:83], v79, s[48:51], s31 offen offset:0 sc1\nQmaav 172,56,96\nQmaav 172,60,104\nQta 78,69,25600\ns_waitcnt lgkmcnt(4)\nQmvav 92,64,96\n"
"Qmvav 92,68,104\nbuffer_load_dwordx4 v[112:115], v79, s[48:51], s31 offen offset:1024 sc1\nQmvav 124,64,128\nQmvav 124,68,136\nQmvav 156,64,160\nQta 56,69,16640\nQmvav 156,68,168\nbuffer_load_dwordx4 v[144:147], v79, s[48:51], s31 offen offset:2048 sc1\n"
"Qmvav 188,64,192\nQmvav 188,68,200\nQta 58,69,17664\nQmvav 220,64,224\nQmvav 220,68,232\nbuffer_load_dwordx4 v[176:179], v79, s[48:51], s31 offen offset:3072 sc1\nQmvav 252,64,148\nQta 60,69,24832\n"
"Qmvav 252,68,212\nQmvav 100,64,180\nQmvav 100,68,244\nQta 62,69,25856\ns_mov_b32 s31, 266240\ns_waitcnt lgkmcnt(4)\nQmaav 224,72,180\nbuffer_load_dwordx4 v[208:211], v79, s[48:51], s31 offen offset:0 sc1\n"
"Qmaav 224,76,244\nQmaav 220,72,148\nQmaav 220,76,212\nQmaav 216,72,224\nQta 64,69,16896\nQmaav 216,76,232\nbuffer_load_dwordx4 v[240:243], v79, s[48:51], s31 offen offset:1024 sc1\nQmaav 212,72,192\n"
"Qmaav 212,76,200\nQta 66,69,17920\nQmaav 208,72,160\nQmaav 208,76,168\nbuffer_load_dwordx4 v[84:87], v79, s[48:51], s31 offen offset:2048 sc1\nQmaav 204,72,128\nQta 68,69,25088\nQmaav 204,76,136\n"
"Qmaav 200,72,96\nQmaav 200,76,104\nQta 70,69,26112\ns_mov_b32 s31, 278528\ns_waitcnt lgkmcnt(4)\nQmaav 228,56,96\nbuffer_load_dwordx4 v[88:91], v79, s[48:51], s31 offen offset:0 sc1\nQmaav 228,60,104\n"
"Qmaav 232,56,128\nQmaav 232,60,136\nQmaav 236,56,160\nQta 72,69,17152\nQmaav 236,60,168\nbuffer_load_dwordx4 v[120:123], v79, s[48:51], s31 offen offset:1024 sc1\nQmaav 240,56,192\nQmaav 240,60,200\n"
"Qta 74,69,18176\nQmaav 244,56,224\nQmaav 244,60,232\nbuffer_load_dwordx4 v[152:155], v79, s[48:51], s31 offen offset:2048 sc1\nQmaav 248,56,148\nQta 76,69,25344\nQmaav 248,60,212\nQmaav 252,56,180\n"
"Qmaav 252,60,244\nQta 78,69,26368\ns_waitcnt lgkmcnt(4)\nQmvav 172,64,180\nbuffer_load_dwordx4 v[184:187], v79, s[48:51], s31 offen offset:3072 sc1\nQmvav 172,68,244\nQmvav 140,64,148\nQmvav 140,68,212\n"
"Qmvav 108,64,224\nQta 56,69,32768\ns_mov_b32 s31, 282624\nQmvav 108,68,232\nbuffer_load_dwordx4 v[216:219], v79, s[48:51], s31 offen offset:0 sc1\nQmvav 228,64,192\nQmvav 228,68,200\nQta 58,69,33792\n"
"Qmvav 196,64,160\nQmvav 196,68,168\nbuffer_load_dwordx4 v[248:251], v79, s[48:51], s31 offen offset:1024 sc1\nQmvav 164,64,128\nQta 60,69,40960\nQmvav 164,68,136\nQmvav 132,64,96\nQmvav 132,68,104\n"
"Qta 62,69,41984\ns_waitcnt lgkmcnt(4)\nQmvav 204,72,96\nbuffer_load_dwordx4 v[116:119], v79, s[48:51], s31 offen offset:2048 sc1\nQmvav 204,76,104\nQmvav 236,72,128\nQmvav 236,76,136\nQmaav 96,72,160\n"
"Qta 64,69,33024\ns_add_u32 m0, s96, 32768\ns_mov_b32 s95, 331776\nQmaav 96,76,168\nbuffer_load_dwordx4 v63, s[52:55], s95 offen lds\nQmaav 100,72,192\nQmaav 100,76,200\nQta 66,69,34048\n"
"Qmaav 104,72,224\nQmaav 104,76,232\nQmaav 108,72,148\nQta 68,69,41216\ns_mov_b32 s95, 351232\nQmaav 108,76,212\nbuffer_load_dwordx4 v63, s[52:55], s95 offen offset:1024 lds\nQmaav 112,72,180\n"
"Qmaav 112,76,244\nQta 70,69,42240\ns_waitcnt vmcnt(16)\ns_waitcnt lgkmcnt(4)\nQmaaa 140,56,24\nQmaaa 140,60,52\ns_add_u32 m0, s96, 40960\ns_mov_b32 s95, 372736\n"
"Qmaaa 136,56,20\nbuffer_load_dwordx4 v63, s[52:55], s95 offen lds\nQmaaa 136,60,48\nQmaaa 132,56,16\nQta 72,69,33280\nQmaaa 132,60,44\nQmaaa 128,56,12\nQmaaa 128,60,40\n"
"Qta 74,69,34304\ns_mov_b32 s95, 392192\nQmaaa 124,56,8\nbuffer_load_dwordx4 v63, s[52:55], s95 offen offset:1024 lds\nQmaaa 124,60,36\nQmaaa 120,56,4\nQta 76,69,41472\ns_add_u32 s88, s71, 131072\n"
"Qmaaa 120,60,32\nbuffer_store_dwordx4 v[96:99], v79, s[56:59], s88 offen offset:0 sc1\nQmaaa 116,56,0\nQmaaa 116,60,28\nQta 78,69,42496\ns_waitcnt lgkmcnt(4)\nQmaaa 144,64,0\nQmaaa 144,68,28\n"
"Qmaaa 148,64,4\nQmaaa 148,68,32\nQmaaa 152,64,8\nQta 56,69,33536\ns_add_u32 m0, s96, 49152\ns_mov_b32 s95, 413696\nQmaaa 152,68,36\nbuffer_load_dwordx4 v61, s[52:55], s95 offen lds\n"
"Qmaaa 156,64,12\nQmaaa 156,68,40\nQta 58,69,34560\nQmaaa 160,64,16\nQmaaa 160,68,44\nQmaaa 164,64,20\nQta 60,69,41728\ns_mov_b32 s95, 433152\n"
"Qmaaa 164,68,48\nbuffer_load_dwordx4 v61, s[52:55], s95 offen offset:1024 lds\nQmaaa 168,64,24\nQmaaa 168,68,52\nQta 62,69,42752\ns_waitcnt lgkmcnt(4)\nQmvaa 24,72,24\nQmvaa 24,76,52\n"
"Qmvaa 20,72,20\nbuffer_store_dwordx4 v[128:131], v79, s[56:59], s88 offen offset:1024 sc1\nQmvaa 20,76,48\nQmvaa 16,72,16\nQta 64,69,49152\nQmvaa 16,76,44\nQmvaa 12,72,12\n.Lpg13:\n"
"s_cbranch_execz .Ltr6\nQmvaa 12,76,40\nQta 66,69,49664\nQmvaa 8,72,8\nQmvaa 8,76,36\nQmvaa 4,72,4\nQta 68,69,50176\nv_add_u32 v75, 1, v75\n"
"Qmvaa 4,76,32\ns_waitcnt vmcnt(36)\nds_write_b32 v73, v75\ns_add_u32 m0, s96, 57344\ns_mov_b32 s95, 454656\nQmvaa 0,72,0\nbuffer_load_dwordx4 v61, s[52:55], s95 offen lds\nQmvaa 0,76,28\n"
"Qta 70,69,50688\ns_waitcnt lgkmcnt(5)\nQmvaa 28,56,0\nQmvaa 28,60,28\nQmvaa 32,56,4\ns_mov_b32 s95, 474112\nQmvaa 32,60,32\nbuffer_load_dwordx4 v61, s[52:55], s95 offen offset:1024 lds\n"
"Qmvaa 36,56,8\nQta 72,69,49408\nQmvaa 36,60,36\nds_read_b32 v66, v74\nQmvaa 40,56,12\nQmvaa 40,60,40\nQta 74,69,49920\nQmvaa 44,56,16\n"
"buffer_store_dwordx4 v[160:163], v79, s[56:59], s88 offen offset:2048 sc1\nv_add_u32 v67, 2, v65\nQmvaa 44,60,44\ns_waitcnt lgkmcnt(1)\nv_cmp_gt_u32 vcc, v75, v66\nQmvaa 48,56,20\ns_cbranch_vccnz .Lso460\n.Lro460:\n"
"Qmvaa 48,60,48\nQta 76,69,50432\nQmvaa 52,56,24\nbuffer_store_dword v67, v77, s[24:27], 0 offen sc1\nQmvaa 52,60,52\ns_waitcnt lgkmcnt(4)\nQmaaa 196,64,24\nQta 78,69,50944\n"
"Qmaaa 196,68,52\ns_add_u32 m0, s96, 132096\ns_mov_b32 s95, 413696\nQmaaa 192,64,20\nbuffer_load_dwordx4 v62, s[52:55], s95 offen lds\nQmaaa 192,68,48\nQmaaa 188,64,16\nQta 56,69,57344\n"
"Qmaaa 188,68,44\nQmaaa 184,64,12\nQmaaa 184,68,40\nQta 58,69,58368\ns_mov_b32 s95, 453632\nQmaaa 180,64,8\nbuffer_load_dwordx4 v62, s[52:55], s95 offen offset:1024 lds\nQmaaa 180,68,36\n"
"Qmaaa 176,64,4\nQta 60,68,0\nQmaaa 176,68,32\nQmaaa 172,64,0\nQmaaa 172,68,28\nQta 62,68,1024\ns_waitcnt lgkmcnt(4)\nQmvaa 92,72,0\n"
"buffer_store_dwordx4 v[192:195], v79, s[56:59], s88 offen offset:3072 sc1\nQmvaa 92,76,28\nQmvaa 124,72,4\nQmvaa 124,76,32\nQmvaa 156,72,8\nQta 64,69,57600\nQmvaa 156,76,36\nQmvaa 188,72,12\n"
"Qmvaa 188,76,40\nQta 66,69,58624\ns_add_u32 m0, s96, 140288\ns_mov_b32 s95, 413696\nQmvaa 220,72,16\nbuffer_load_dwordx4 v63, s[52:55], s95 offen lds\nQmvaa 220,76,44\nQmvaa 252,72,20\n"
"Qta 68,68,256\nQmvaa 252,76,48\nQmvaa 100,72,24\nQmvaa 100,76,52\nQta 70,68,1280\ns_mov_b32 s95, 433152\ns_waitcnt lgkmcnt(4)\nQmaaa 224,56,24\n"
"buffer_load_dwordx4 v63, s[52:55], s95 offen offset:1024 lds\nQmaaa 224,60,52\nQmaaa 220,56,20\nQmaaa 220,60,48\nQmaaa 216,56,16\nQta 72,69,57856\nQmaaa 216,60,44\ns_add_u32 s88, s71, 135168\n"
"Qmaaa 212,56,12\nbuffer_store_dwordx4 v[224:227], v79, s[56:59], s88 offen offset:0 sc1\nQmaaa 212,60,40\nQta 74,69,58880\nQmaaa 208,56,8\nQmaaa 208,60,36\nQmaaa 204,56,4\nQta 76,68,512\n"
"Qmaaa 204,60,32\nQmaaa 200,56,0\nQmaaa 200,60,28\nQta 78,68,1536\ns_add_u32 m0, s96, 148480\ns_mov_b32 s95, 454656\ns_waitcnt lgkmcnt(4)\nQmaaa 228,64,0\n"
"buffer_load_dwordx4 v63, s[52:55], s95 offen lds\nQmaaa 228,68,28\nQmaaa 232,64,4\nQmaaa 232,68,32\nQmaaa 236,64,8\nQta 56,69,58112\ns_mov_b32 s95, 474112\nQmaaa 236,68,36\n"
"buffer_load_dwordx4 v63, s[52:55], s95 offen offset:1024 lds\nQmaaa 240,64,12\nQmaaa 240,68,40\nQta 58,69,59136\nQmaaa 244,64,16\nQmaaa 244,68,44\nQmaaa 248,64,20\nQta 60,68,768\n"
"Qmaaa 248,68,48\nQmaaa 252,64,24\nbuffer_store_dwordx4 v[148:151], v79, s[56:59], s88 offen offset:1024 sc1\nQmaaa 252,68,52\nQta 62,68,1792\ns_waitcnt lgkmcnt(4)\nQmvaa 172,72,24\nQmvaa 172,76,52\n"
"s_mov_b32 s40, s22\ns_and_b32 s41, s23, 0xffff\nQmvaa 140,72,20\ns_mov_b32 s42, 571200\ns_lshl_b32 s66, s19, 17\nQmvaa 140,76,48\ns_add_u32 s69, s98, 4194304\ns_add_u32 s98, s66, s69\n"
"Qmvaa 108,72,16\nQta 64,68,8192\ns_xor_b32 s66, s66, 524288\nQmvaa 108,76,44\ns_add_u32 m0, s96, 65536\ns_mov_b32 s95, 659456\nQmvaa 228,72,12\nbuffer_load_dwordx4 v61, s[52:55], s95 offen lds\n"
"Qmvaa 228,76,40\nQta 66,68,9216\ns_add_u32 s64, s66, s69\nQmvaa 196,72,8\ns_mul_i32 s66, s92, 33792\ns_add_u32 s66, s66, %4\nQmvaa 196,76,36\ns_add_u32 s66, s66, 98304\n"
"s_mul_i32 s68, s93, 96\nQmvaa 164,72,4\nQta 68,68,16384\ns_mov_b32 s95, 678912\nQmvaa 164,76,32\nbuffer_load_dwordx4 v61, s[52:55], s95 offen offset:1024 lds\nQmvaa 132,72,0\ns_sub_u32 s68, s66, s68\n"
"s_add_u32 s68, s68, 96\nQmvaa 132,76,28\nQta 70,68,17408\nv_bfe_u32 v148, v60, 4, 4\ns_waitcnt lgkmcnt(4)\nQmvaa 204,56,0\ns_mov_b32 s31, 393216\nv_mul_u32_u24 v148, 208, v148\n"
"Qmvaa 204,60,28\nbuffer_load_dwordx4 a[0:3], v79, s[48:51], s31 offen offset:0 sc1\nQmvaa 236,56,4\nv_lshrrev_b32 v149, 8, v60\nv_lshl_add_u32 v148, v149, 3, v148\nQmvaa 236,60,32\nv_add_u32 v56, s68, v148\ns_lshl_b32 s68, s93, 3\n"
"Qmaaa 96,56,8\nQta 72,68,8448\nv_lshrrev_b32 v148, 7, v60\nQmaaa 96,60,36\nbuffer_store_dwordx4 v[180:183], v79, s[56:59], s88 offen offset:2048 sc1\nQmaaa 100,56,12\nv_add_u32 v148, s68, v148\nv_and_b32 v149, 127, v60\n"
"Qmaaa 100,60,40\nQta 74,68,9472\nv_and_b32 v150, 64, v60\nQmaaa 104,56,16\nv_lshrrev_b32 v150, 1, v150\nv_mul_u32_u24 v57, 208, v148\nQmaaa 104,60,44\nv_add3_u32 v57, v57, v149, s66\n"
"v_add_u32 v57, v57, v150\nQmaaa 108,56,20\nQta 76,68,16640\ns_and_b32 s65, s19, 1\nQmaaa 108,60,48\ns_and_b32 s68, s19, 4\ns_lshl_b32 s68, s68, 2\nQmaaa 112,56,24\n"
"s_lshl_b32 s69, s65, 3\ns_add_u32 s68, s68, s69\nQmaaa 112,60,52\nQta 78,68,17664\ns_lshr_b32 s68, 0xc0004080, s68\ns_waitcnt vmcnt(25)\ns_waitcnt lgkmcnt(4)\nQmaav 140,64,84\n"
"buffer_load_dwordx4 a[4:7], v79, s[48:51], s31 offen offset:1024 sc1\nQmaav 140,68,116\ns_and_b32 s68, s68, 255\ns_mul_i32 s69, s92, 286720\nQmaav 136,64,240\ns_add_u32 s68, s68, s69\nv_mul_u32_u24 v64, 2560, v148\nQmaav 136,68,248\n"
"v_add3_u32 v64, v64, v149, s68\ns_lshl_b32 s68, s93, 4\nQmaav 132,64,208\nQta 56,68,8704\nv_lshrrev_b32 v148, 6, v60\nQmaav 132,68,216\nbuffer_load_dwordx4 a[8:11], v79, s[48:51], s31 offen offset:2048 sc1\nQmaav 128,64,176\n"
"v_add_u32 v148, s68, v148\nv_and_b32 v149, 63, v60\nQmaav 128,68,184\nQta 58,68,9728\nv_and_b32 v150, 32, v60\nQmaav 124,64,144\nv_lshlrev_b32 v150, 1, v150\nv_mul_u32_u24 v71, 208, v148\n"
"Qmaav 124,68,152\nbuffer_load_dwordx4 a[12:15], v79, s[48:51], s31 offen offset:3072 sc1\nQmaav 120,64,112\nQta 60,68,16896\nv_add3_u32 v71, v71, v149, s66\nQmaav 120,68,120\nv_add_u32 v71, v71, v150\ns_xor_b32 s65, s65, 1\n"
"Qmaav 116,64,80\ns_lshl_b32 s65, s65, 8\ns_add_u32 s68, s65, s69\nQmaav 116,68,88\nQta 62,68,17920\ns_mov_b32 s31, 397312\ns_waitcnt lgkmcnt(4)\nQmaav 144,72,80\n"
"buffer_load_dwordx4 a[16:19], v79, s[48:51], s31 offen offset:0 sc1\nQmaav 144,76,88\nv_mul_u32_u24 v72, 2560, v148\nv_add3_u32 v72, v72, v149, s68\nQmaav 148,72,112\nQmaav 148,76,120\nQmaav 152,72,144\nQta 64,68,8960\n"
"Qmaav 152,76,152\nbuffer_load_dwordx4 a[20:23], v79, s[48:51], s31 offen offset:1024 sc1\nQmaav 156,72,176\nQmaav 156,76,184\nQta 66,68,9984\nQmaav 160,72,208\nQmaav 160,76,216\nbuffer_load_dwordx4 a[24:27], v79, s[48:51], s31 offen offset:2048 sc1\n"
"Qmaav 164,72,240\nQta 68,68,17152\nQmaav 164,76,248\nQmaav 168,72,84\nQmaav 168,76,116\nQta 70,68,18176\ns_mov_b32 s31, 409600\ns_waitcnt lgkmcnt(4)\n"
"Qmvav 24,56,84\nbuffer_load_dwordx4 a[28:31], v79, s[48:51], s31 offen offset:0 sc1\nQmvav 24,60,116\nQmvav 20,56,240\nQmvav 20,60,248\nQmvav 16,56,208\nQta 72,68,24576\nQmvav 16,60,216\n"
"buffer_load_dwordx4 a[32:35], v79, s[48:51], s31 offen offset:1024 sc1\nQmvav 12,56,176\nQmvav 12,60,184\nQta 74,68,25088\nQmvav 8,56,144\nQmvav 8,60,152\nbuffer_load_dwordx4 a[36:39], v79, s[48:51], s31 offen offset:2048 sc1\nQmvav 4,56,112\n"
"Qta 76,68,25600\nv_add_u32 v75, 1, v75\nQmvav 4,60,120\ns_waitcnt vmcnt(20)\nds_write_b32 v73, v75\nQmvav 0,56,80\nQmvav 0,60,88\nQta 78,68,26112\n"
"s_waitcnt lgkmcnt(5)\nQmvav 28,64,80\nbuffer_load_dwordx4 a[40:43], v79, s[48:51], s31 offen offset:3072 sc1\nQmvav 28,68,88\nQmvav 32,64,112\nQmvav 32,68,120\nQmvav 36,64,144\nQta 56,68,24832\n"
"Qmvav 36,68,152\nds_read_b32 v66, v74\ns_mov_b32 s31, 413696\nQmvav 40,64,176\nbuffer_load_dwordx4 a[44:47], v79, s[48:51], s31 offen offset:0 sc1\nQmvav 40,68,184\nQta 58,68,25344\nQmvav 44,64,208\n"
"Qmvav 44,68,216\ns_waitcnt lgkmcnt(1)\nv_cmp_gt_u32 vcc, v75, v66\nQmvav 48,64,240\ns_cbranch_vccnz .Lso600\n.Lro600:\nQmvav 48,68,248\nQta 60,68,25856\n"
"Qmvav 52,64,84\nbuffer_load_dwordx4 a[48:51], v79, s[48:51], s31 offen offset:1024 sc1\nQmvav 52,68,116\ns_waitcnt lgkmcnt(4)\nQmaav 196,72,84\nQta 62,68,26368\nQmaav 196,76,116\nQmaav 192,72,240\n"
"buffer_load_dwordx4 a[52:55], v79, s[48:51], s31 offen offset:2048 sc1\nQmaav 192,76,248\nQmaav 188,72,208\nQta 64,68,32768\nQmaav 188,76,216\nQmaav 184,72,176\nQmaav 184,76,184\nQta 66,68,33792\n"
"s_add_u32 m0, s96, 73728\ns_mov_b32 s95, 700416\nQmaav 180,72,144\nbuffer_load_dwordx4 v61, s[52:55], s95 offen lds\nQmaav 180,76,152\nQmaav 176,72,112\nQta 68,68,40960\nQmaav 176,76,120\n"
"Qmaav 172,72,80\nQmaav 172,76,88\nQta 70,68,41984\ns_mov_b32 s95, 719872\ns_waitcnt lgkmcnt(4)\nQmvav 92,56,80\nbuffer_load_dwordx4 v61, s[52:55], s95 offen offset:1024 lds\nQmvav 92,60,88\n"
"Qmvav 124,56,112\nQmvav 124,60,120\nQmvav 156,56,144\nQta 72,68,33024\ns_add_u32 m0, s96, 81920\ns_mov_b32 s95, 659456\nQmvav 156,60,152\nbuffer_load_dwordx4 v62, s[52:55], s95 offen lds\n"
"Qmvav 188,56,176\nQmvav 188,60,184\nQta 74,68,34048\nQmvav 220,56,208\nQmvav 220,60,216\nQmvav 252,56,240\nQta 76,68,41216\ns_mov_b32 s95, 699392\n"
"Qmvav 252,60,248\nbuffer_load_dwordx4 v62, s[52:55], s95 offen offset:1024 lds\nQmvav 100,56,84\nQmvav 100,60,116\nQta 78,68,42240\ns_waitcnt lgkmcnt(4)\nQmaav 224,64,84\nQmaav 224,68,116\n"
"s_add_u32 m0, s96, 90112\ns_mov_b32 s95, 659456\nQmaav 220,64,240\nbuffer_load_dwordx4 v63, s[52:55], s95 offen lds\nQmaav 220,68,248\nQmaav 216,64,208\nQta 56,68,33280\nQmaav 216,68,216\n"
"Qmaav 212,64,176\nQmaav 212,68,184\nQta 58,68,34304\ns_mov_b32 s95, 678912\nQmaav 208,64,144\nbuffer_load_dwordx4 v63, s[52:55], s95 offen offset:1024 lds\nQmaav 208,68,152\nQmaav 204,64,112\n"
"Qta 60,68,41472\nQmaav 204,68,120\nQmaav 200,64,80\nQmaav 200,68,88\nQta 62,68,42496\ns_waitcnt lgkmcnt(4)\nQmaav 228,72,80\nQmaav 228,76,88\n"
"s_add_u32 s88, s71, 147456\nQmaav 232,72,112\nbuffer_store_dwordx4 v[104:107], v79, s[56:59], s88 offen offset:0 sc1\nQmaav 232,76,120\nQmaav 236,72,144\nQta 64,68,33536\nQmaav 236,76,152\nQmaav 240,72,176\n"
"Qmaav 240,76,184\nQta 66,68,34560\nQmaav 244,72,208\nQmaav 244,76,216\nQmaav 248,72,240\nQta 68,68,41728\ns_add_u32 m0, s96, 98304\ns_mov_b32 s95, 700416\n"
"Qmaav 248,76,248\nbuffer_load_dwordx4 v63, s[52:55], s95 offen lds\nQmaav 252,72,84\nQmaav 252,76,116\nQta 70,68,42752\ns_waitcnt lgkmcnt(4)\nQmvav 172,56,84\nbuffer_store_dwordx4 v[136:139], v79, s[56:59], s88 offen offset:1024 sc1\n"
"Qmvav 172,60,116\nQmvav 140,56,240\nQmvav 140,60,248\nQmvav 108,56,208\nQta 72,68,49152\nQmvav 108,60,216\nQmvav 228,56,176\nQmvav 228,60,184\n"
"Qta 74,68,50176\ns_mov_b32 s95, 719872\nQmvav 196,56,144\nbuffer_load_dwordx4 v63, s[52:55], s95 offen offset:1024 lds\nQmvav 196,60,152\nQmvav 164,56,112\nQta 76,68,57344\nQmvav 164,60,120\n"
"Qmvav 132,56,80\nQmvav 132,60,88\nQta 78,68,58368\ns_add_u32 m0, s96, 106496\ns_mov_b32 s95, 741376\ns_waitcnt lgkmcnt(4)\nQmvav 204,64,80\nbuffer_load_dwordx4 v61, s[52:55], s95 offen lds\n"
"Qmvav 204,68,88\nQmvav 236,64,112\nQmvav 236,68,120\nQmaav 96,64,144\nQta 56,68,49408\ns_mov_b32 s95, 760832\nQmaav 96,68,152\nbuffer_load_dwordx4 v61, s[52:55], s95 offen offset:1024 lds\n"
"Qmaav 100,64,176\nQmaav 100,68,184\nQta 58,68,50432\nQmaav 104,64,208\ns_mov_b32 s31, 524288\nQmaav 104,68,216\nbuffer_load_dwordx4 v[80:83], v79, s[48:51], s31 offen offset:0 sc1\nQmaav 108,64,240\n"
"Qta 60,68,57600\nQmaav 108,68,248\nQmaav 112,64,84\nQmaav 112,68,116\nQta 62,68,58624\ns_waitcnt vmcnt(13)\ns_waitcnt lgkmcnt(4)\nQmaaa 140,72,24\n"
"buffer_load_dwordx4 v[112:115], v79, s[48:51], s31 offen offset:1024 sc1\nQmaaa 140,76,52\nQmaaa 136,72,20\nQmaaa 136,76,48\nQmaaa 132,72,16\nQta 64,68,49664\nQmaaa 132,76,44\nbuffer_load_dwordx4 v[144:147], v79, s[48:51], s31 offen offset:2048 sc1\n"
"Qmaaa 128,72,12\nQmaaa 128,76,40\nQta 66,68,50688\nQmaaa 124,72,8\nQmaaa 124,76,36\nbuffer_load_dwordx4 v[176:179], v79, s[48:51], s31 offen offset:3072 sc1\nQmaaa 120,72,4\nQta 68,68,57856\n"
"Qmaaa 120,76,32\nQmaaa 116,72,0\nQmaaa 116,76,28\nQta 70,68,58880\ns_mov_b32 s31, 528384\ns_waitcnt lgkmcnt(4)\nQmaaa 144,56,0\nbuffer_load_dwordx4 v[208:211], v79, s[48:51], s31 offen offset:0 sc1\n"
"Qmaaa 144,60,28\nQmaaa 148,56,4\nQmaaa 148,60,32\nQmaaa 152,56,8\nQta 72,68,49920\nQmaaa 152,60,36\nbuffer_load_dwordx4 v[240:243], v79, s[48:51], s31 offen offset:1024 sc1\nQmaaa 156,56,12\n"
"Qmaaa 156,60,40\nQta 74,68,50944\nQmaaa 160,56,16\nQmaaa 160,60,44\nbuffer_load_dwordx4 v[84:87], v79, s[48:51], s31 offen offset:2048 sc1\nQmaaa 164,56,20\nQta 76,68,58112\nQmaaa 164,60,48\n"
"Qmaaa 168,56,24\nQmaaa 168,60,52\nQta 78,68,59136\ns_mov_b32 s31, 540672\ns_waitcnt lgkmcnt(4)\nQmvaa 24,64,24\nbuffer_load_dwordx4 v[88:91], v79, s[48:51], s31 offen offset:0 sc1\nQmvaa 24,68,52\n"
"Qmvaa 20,64,20\nQmvaa 20,68,48\nQmvaa 16,64,16\nQta 56,70,1024\nQmvaa 16,68,44\nbuffer_load_dwordx4 v[120:123], v79, s[48:51], s31 offen offset:1024 sc1\nQmvaa 12,64,12\nQmvaa 12,68,40\n"
"Qta 58,70,1536\nQmvaa 8,64,8\n.Lpg14:\ns_cbranch_execz .Lpg18\nQmvaa 8,68,36\nbuffer_load_dwordx4 v[152:155], v79, s[48:51], s31 offen offset:2048 sc1\nQmvaa 4,64,4\nQta 60,70,2048\n"
"v_add_u32 v75, 1, v75\nQmvaa 4,68,32\ns_waitcnt vmcnt(18)\nds_write_b32 v73, v75\nQmvaa 0,64,0\nQmvaa 0,68,28\nQta 62,70,2560\ns_waitcnt lgkmcnt(5)\n"
"Qmvaa 28,72,0\nbuffer_load_dwordx4 v[184:187], v79, s[48:51], s31 offen offset:3072 sc1\nQmvaa 28,76,28\nQmvaa 32,72,4\nQmvaa 32,76,32\nQmvaa 36,72,8\nQta 64,70,1280\nQmvaa 36,76,36\n"
"ds_read_b32 v66, v74\ns_mov_b32 s31, 544768\nQmvaa 40,72,12\nbuffer_load_dwordx4 v[216:219], v79, s[48:51], s31 offen offset:0 sc1\nQmvaa 40,76,40\nQta 66,70,1792\nQmvaa 44,72,16\nQmvaa 44,76,44\n"
"s_waitcnt lgkmcnt(1)\nv_cmp_gt_u32 vcc, v75, v66\nQmvaa 48,72,20\ns_cbranch_vccnz .Lso740\n.Lro740:\nQmvaa 48,76,48\nQta 68,70,2304\nQmvaa 52,72,24\n"
"buffer_load_dwordx4 v[248:251], v79, s[48:51], s31 offen offset:1024 sc1\nQmvaa 52,76,52\ns_waitcnt lgkmcnt(4)\nQmaaa 196,56,24\nQta 70,70,2816\nQmaaa 196,60,52\nQmaaa 192,56,20\nbuffer_load_dwordx4 v[116:119], v79, s[48:51], s31 offen offset:2048 sc1\n"
"Qmaaa 192,60,48\nQmaaa 188,56,16\nQta 72,70,9216\nQmaaa 188,60,44\nQmaaa 184,56,12\nQmaaa 184,60,40\nQta 74,70,10240\ns_add_u32 m0, s96, 114688\n"
"s_mov_b32 s95, 782336\nQmaaa 180,56,8\nbuffer_load_dwordx4 v61, s[52:55], s95 offen lds\nQmaaa 180,60,36\nQmaaa 176,56,4\nQta 76,70,17408\nQmaaa 176,60,32\nQmaaa 172,56,0\n"
"Qmaaa 172,60,28\nQta 78,70,18432\ns_mov_b32 s95, 801792\ns_waitcnt lgkmcnt(4)\nQmvaa 92,64,0\nbuffer_load_dwordx4 v61, s[52:55], s95 offen offset:1024 lds\nQmvaa 92,68,28\nQmvaa 124,64,4\n"
"Qmvaa 124,68,32\nQmvaa 156,64,8\nQta 56,70,9472\ns_add_u32 m0, s96, 122880\ns_mov_b32 s95, 741376\nQmvaa 156,68,36\nbuffer_load_dwordx4 v62, s[52:55], s95 offen lds\nQmvaa 188,64,12\n"
"Qmvaa 188,68,40\nQta 58,70,10496\nQmvaa 220,64,16\nQmvaa 220,68,44\nQmvaa 252,64,20\nQta 60,70,17664\ns_mov_b32 s95, 781312\nQmvaa 252,68,48\n"
"buffer_load_dwordx4 v62, s[52:55], s95 offen offset:1024 lds\nQmvaa 100,64,24\nQmvaa 100,68,52\nQta 62,70,18688\ns_waitcnt lgkmcnt(4)\nQmaaa 224,72,24\nQmaaa 224,76,52\ns_add_u32 m0, s96, 0\n"
"s_mov_b32 s95, 741376\nQmaaa 220,72,20\nbuffer_load_dwordx4 v63, s[52:55], s95 offen lds\nQmaaa 220,76,48\nQmaaa 216,72,16\nQta 64,70,9728\nQmaaa 216,76,44\nQmaaa 212,72,12\n"
"Qmaaa 212,76,40\nQta 66,70,10752\ns_mov_b32 s95, 760832\nQmaaa 208,72,8\nbuffer_load_dwordx4 v63, s[52:55], s95 offen offset:1024 lds\nQmaaa 208,76,36\nQmaaa 204,72,4\nQta 68,70,17920\n"
"Qmaaa 204,76,32\nQmaaa 200,72,0\nQmaaa 200,76,28\nQta 70,70,18944\ns_waitcnt lgkmcnt(4)\nQmaaa 228,56,0\nQmaaa 228,60,28\nQmaaa 232,56,4\n"
"buffer_store_dwordx4 v[168:171], v79, s[56:59], s88 offen offset:2048 sc1\nQmaaa 232,60,32\nQmaaa 236,56,8\nQta 72,70,9984\nQmaaa 236,60,36\nQmaaa 240,56,12\nQmaaa 240,60,40\nQta 74,70,11008\n"
"Qmaaa 244,56,16\nQmaaa 244,60,44\nQmaaa 248,56,20\nQta 76,70,18176\ns_add_u32 m0, s96, 8192\ns_mov_b32 s95, 782336\nQmaaa 248,60,48\nbuffer_load_dwordx4 v63, s[52:55], s95 offen lds\n"
"Qmaaa 252,56,24\nQmaaa 252,60,52\nQta 78,70,19200\ns_waitcnt lgkmcnt(4)\nQmvaa 172,64,24\nbuffer_store_dwordx4 v[200:203], v79, s[56:59], s88 offen offset:3072 sc1\nQmvaa 172,68,52\nQmvaa 140,64,20\n"
"Qmvaa 140,68,48\nQmvaa 108,64,16\nQta 56,69,0\nQmvaa 108,68,44\nQmvaa 228,64,12\nQmvaa 228,68,40\nQta 58,69,1024\ns_mov_b32 s95, 801792\n"
"Qmvaa 196,64,8\nbuffer_load_dwordx4 v63, s[52:55], s95 offen offset:1024 lds\nQmvaa 196,68,36\nQmvaa 164,64,4\nQta 60,69,8192\nQmvaa 164,68,32\nQmvaa 132,64,0\nQmvaa 132,68,28\n"
"Qta 62,69,9216\ns_add_u32 m0, s96, 16384\ns_mov_b32 s95, 987136\ns_waitcnt lgkmcnt(4)\nQmvaa 204,72,0\nbuffer_load_dwordx4 v61, s[52:55], s95 offen lds\nQmvaa 204,76,28\nQmvaa 236,72,4\n"
"Qmvaa 236,76,32\nQmaaa 96,72,8\nQta 64,69,256\ns_mov_b32 s95, 1006592\nQmaaa 96,76,36\nbuffer_load_dwordx4 v61, s[52:55], s95 offen offset:1024 lds\nQmaaa 100,72,12\nQmaaa 100,76,40\n"
"Qta 66,69,1280\nQmaaa 104,72,16\ns_mov_b32 s31, 655360\nQmaaa 104,76,44\nbuffer_load_dwordx4 a[0:3], v79, s[48:51], s31 offen offset:0 sc1\nQmaaa 108,72,20\nQta 68,69,8448\nQmaaa 108,76,48\n"
"Qmaaa 112,72,24\nQmaaa 112,76,52\nQta 70,69,9472\ns_waitcnt vmcnt(13)\ns_waitcnt lgkmcnt(4)\nQmaav 140,56,84\nbuffer_load_dwordx4 a[4:7], v79, s[48:51], s31 offen offset:1024 sc1\nQmaav 140,60,116\n"
"Qmaav 136,56,240\nQmaav 136,60,248\nQmaav 132,56,208\nQta 72,69,512\nQmaav 132,60,216\nbuffer_load_dwordx4 a[8:11], v79, s[48:51], s31 offen offset:2048 sc1\nQmaav 128,56,176\nQmaav 128,60,184\n"
"Qta 74,69,1536\nQmaav 124,56,144\nQmaav 124,60,152\nbuffer_load_dwordx4 a[12:15], v79, s[48:51], s31 offen offset:3072 sc1\nQmaav 120,56,112\nQta 76,69,8704\nQmaav 120,60,120\nQmaav 116,56,80\n"
"Qmaav 116,60,88\nQta 78,69,9728\ns_mov_b32 s31, 659456\ns_waitcnt lgkmcnt(4)\nQmaav 144,64,80\nbuffer_load_dwordx4 a[16:19], v79, s[48:51], s31 offen offset:0 sc1\nQmaav 144,68,88\nQmaav 148,64,112\n"
"Qmaav 148,68,120\nQmaav 152,64,144\nQta 56,69,768\nQmaav 152,68,152\nbuffer_load_dwordx4 a[20:23], v79, s[48:51], s31 offen offset:1024 sc1\nQmaav 156,64,176\nQmaav 156,68,184\nQta 58,69,1792\n"
"Qmaav 160,64,208\nQmaav 160,68,216\nbuffer_load_dwordx4 a[24:27], v79, s[48:51], s31 offen offset:2048 sc1\nQmaav 164,64,240\nQta 60,69,8960\nQmaav 164,68,248\nQmaav 168,64,84\nQmaav 168,68,116\n"
"Qta 62,69,9984\ns_mov_b32 s31, 671744\ns_waitcnt lgkmcnt(4)\nQmvav 24,72,84\nbuffer_load_dwordx4 a[28:31], v79, s[48:51], s31 offen offset:0 sc1\nQmvav 24,76,116\nQmvav 20,72,240\nQmvav 20,76,248\n"
"Qmvav 16,72,208\nQta 64,69,16384\nQmvav 16,76,216\nbuffer_load_dwordx4 a[32:35], v79, s[48:51], s31 offen offset:1024 sc1\nQmvav 12,72,176\nQmvav 12,76,184\nQta 66,69,16896\nQmvav 8,72,144\n"
"Qmvav 8,76,152\nbuffer_load_dwordx4 a[36:39], v79, s[48:51], s31 offen offset:2048 sc1\nQmvav 4,72,112\nQta 68,69,17408\nv_add_u32 v75, 1, v75\nQmvav 4,76,120\ns_waitcnt vmcnt(18)\nds_write_b32 v73, v75\n"
"Qmvav 0,72,80\nQmvav 0,76,88\nQta 70,69,17920\ns_waitcnt lgkmcnt(5)\nQmvav 28,56,80\nbuffer_load_dwordx4 a[40:43], v79, s[48:51], s31 offen offset:3072 sc1\nQmvav 28,60,88\nQmvav 32,56,112\n"
"Qmvav 32,60,120\nQmvav 36,56,144\nQta 72,69,16640\nQmvav 36,60,152\nds_read_b32 v66, v74\ns_mov_b32 s31, 675840\nQmvav 40,56,176\nbuffer_load_dwordx4 a[44:47], v79, s[48:51], s31 offen offset:0 sc1\n"
"Qmvav 40,60,184\nQta 74,69,17152\nQmvav 44,56,208\nQmvav 44,60,216\ns_waitcnt lgkmcnt(1)\nv_cmp_gt_u32 vcc, v75, v66\nQmvav 48,56,240\ns_cbranch_vccnz .Lso880\n"
".Lro880:\nQmvav 48,60,248\nQta 76,69,17664\nQmvav 52,56,84\nbuffer_load_dwordx4 a[48:51], v79, s[48:51], s31 offen offset:1024 sc1\nQmvav 52,60,116\ns_waitcnt lgkmcnt(4)\nQmaav 196,64,84\n"
"Qta 78,69,18176\nQmaav 196,68,116\nQmaav 192,64,240\nbuffer_load_dwordx4 a[52:55], v79, s[48:51], s31 offen offset:2048 sc1\nQmaav 192,68,248\nQmaav 188,64,208\nQta 56,69,24576\nQmaav 188,68,216\n"
"Qmaav 184,64,176\nQmaav 184,68,184\nQta 58,69,25600\ns_add_u32 m0, s96, 24576\ns_mov_b32 s95, 1028096\nQmaav 180,64,144\nbuffer_load_dwordx4 v61, s[52:55], s95 offen lds\nQmaav 180,68,152\n"
"Qmaav 176,64,112\nQta 60,69,32768\nQmaav 176,68,120\nQmaav 172,64,80\nQmaav 172,68,88\nQta 62,69,33792\ns_mov_b32 s95, 1047552\ns_waitcnt lgkmcnt(4)\n"
"Qmvav 92,72,80\nbuffer_load_dwordx4 v61, s[52:55], s95 offen offset:1024 lds\nQmvav 92,76,88\nQmvav 124,72,112\nQmvav 124,76,120\nQmvav 156,72,144\nQta 64,69,24832\ns_add_u32 m0, s96, 32768\n"
"s_mov_b32 s95, 987136\nQmvav 156,76,152\nbuffer_load_dwordx4 v62, s[52:55], s95 offen lds\nQmvav 188,72,176\nQmvav 188,76,184\nQta 66,69,25856\nQmvav 220,72,208\nQmvav 220,76,216\n"
"Qmvav 252,72,240\nQta 68,69,33024\ns_mov_b32 s95, 1027072\nQmvav 252,76,248\nbuffer_load_dwordx4 v62, s[52:55], s95 offen offset:1024 lds\nQmvav 100,72,84\nQmvav 100,76,116\nQta 70,69,34048\n"
"s_waitcnt lgkmcnt(4)\nQmaav 224,56,84\nQmaav 224,60,116\ns_add_u32 m0, s96, 40960\ns_mov_b32 s95, 987136\nQmaav 220,56,240\nbuffer_load_dwordx4 v63, s[52:55], s95 offen lds\nQmaav 220,60,248\n"
"Qmaav 216,56,208\nQta 72,69,25088\nQmaav 216,60,216\nQmaav 212,56,176\nQmaav 212,60,184\nQta 74,69,26112\ns_mov_b32 s95, 1006592\nQmaav 208,56,144\n"
"buffer_load_dwordx4 v63, s[52:55], s95 offen offset:1024 lds\nQmaav 208,60,152\nQmaav 204,56,112\nQta 76,69,33280\nQmaav 204,60,120\nQmaav 200,56,80\nQmaav 200,60,88\nQta 78,69,34304\n"
"s_waitcnt lgkmcnt(4)\nQmaav 228,64,80\nQmaav 228,68,88\ns_add_u32 s88, s71, 151552\nQmaav 232,64,112\nbuffer_store_dwordx4 v[232:235], v79, s[56:59], s88 offen offset:0 sc1\nQmaav 232,68,120\nQmaav 236,64,144\n"
"Qta 56,69,25344\nQmaav 236,68,152\nQmaav 240,64,176\nQmaav 240,68,184\nQta 58,69,26368\nQmaav 244,64,208\nQmaav 244,68,216\nQmaav 248,64,240\n"
"Qta 60,69,33536\ns_add_u32 m0, s96, 49152\ns_mov_b32 s95, 1028096\nQmaav 248,68,248\nbuffer_load_dwordx4 v63, s[52:55], s95 offen lds\nQmaav 252,64,84\nQmaav 252,68,116\nQta 62,69,34560\n"
"s_waitcnt lgkmcnt(4)\nQmvav 172,72,84\nbuffer_store_dwordx4 v[212:215], v79, s[56:59], s88 offen offset:1024 sc1\nQmvav 172,76,116\nQmvav 140,72,240\nQmvav 140,76,248\nQmvav 108,72,208\nQta 64,69,40960\n"
"Qmvav 108,76,216\nQmvav 228,72,176\nQmvav 228,76,184\nQta 66,69,41984\ns_mov_b32 s95, 1047552\nQmvav 196,72,144\nbuffer_load_dwordx4 v63, s[52:55], s95 offen offset:1024 lds\nQmvav 196,76,152\n"
"Qmvav 164,72,112\nQta 68,69,49152\nQmvav 164,76,120\nQmvav 132,72,80\nQmvav 132,76,88\nQta 70,69,50176\ns_add_u32 m0, s96, 57344\ns_mov_b32 s95, 1069056\n"
"s_waitcnt lgkmcnt(4)\nQmvav 204,56,80\nbuffer_load_dwordx4 v61, s[52:55], s95 offen lds\nQmvav 204,60,88\nQmvav 236,56,112\nQmvav 236,60,120\nQmaav 96,56,144\nQta 72,69,41216\n"
"s_mov_b32 s95, 1088512\nQmaav 96,60,152\nbuffer_load_dwordx4 v61, s[52:55], s95 offen offset:1024 lds\nQmaav 100,56,176\nQmaav 100,60,184\nQta 74,69,42240\nQmaav 104,56,208\ns_mov_b32 s31, 786432\n"
"Qmaav 104,60,216\nbuffer_load_dwordx4 v[80:83], v79, s[48:51], s31 offen offset:0 sc1\nQmaav 108,56,240\nQta 76,69,49408\nQmaav 108,60,248\nQmaav 112,56,84\nv_accvgpr_read_b32 v96, a96\nv_accvgpr_read_b32 v97, a97\n"
"Qmaav 112,60,116\nQta 78,69,50432\nv_accvgpr_read_b32 v98, a98\ns_waitcnt vmcnt(13)\ns_waitcnt lgkmcnt(4)\nQmaaa 140,64,24\nbuffer_load_dwordx4 v[112:115], v79, s[48:51], s31 offen offset:1024 sc1\nQmaaa 140,68,52\n"
"v_accvgpr_read_b32 v99, a99\nv_accvgpr_read_b32 v128, a100\nQmaaa 136,64,20\nv_accvgpr_read_b32 v129, a101\nv_accvgpr_read_b32 v130, a102\nQmaaa 136,68,48\nv_accvgpr_read_b32 v131, a103\nv_accvgpr_read_b32 v160, a104\n"
"Qmaaa 132,64,16\nQta 56,69,41472\nv_accvgpr_read_b32 v161, a105\nQmaaa 132,68,44\nbuffer_load_dwordx4 v[144:147], v79, s[48:51], s31 offen offset:2048 sc1\nQmaaa 128,64,12\nv_accvgpr_read_b32 v162, a106\nv_accvgpr_read_b32 v163, a107\n"
"Qmaaa 128,68,40\nQta 58,69,42496\nv_accvgpr_read_b32 v192, a108\nQmaaa 124,64,8\nv_accvgpr_read_b32 v193, a109\nv_accvgpr_read_b32 v194, a110\nQmaaa 124,68,36\nbuffer_load_dwordx4 v[176:179], v79, s[48:51], s31 offen offset:3072 sc1\n"
"Qmaaa 120,64,4\nQta 60,69,49664\nv_accvgpr_read_b32 v195, a111\nQmaaa 120,68,32\nv_accvgpr_read_b32 v224, a112\nv_accvgpr_read_b32 v225, a113\nQmaaa 116,64,0\nv_accvgpr_read_b32 v226, a114\n"
"v_accvgpr_read_b32 v227, a115\nQmaaa 116,68,28\nQta 62,69,50688\ns_mov_b32 s31, 790528\ns_waitcnt lgkmcnt(4)\nQmaaa 144,72,0\nbuffer_load_dwordx4 v[208:211], v79, s[48:51], s31 offen offset:0 sc1\nQmaaa 144,76,28\n"
"Qmaaa 148,72,4\nQmaaa 148,76,32\nQmaaa 152,72,8\nQta 64,69,41728\nQmaaa 152,76,36\nbuffer_load_dwordx4 v[240:243], v79, s[48:51], s31 offen offset:1024 sc1\nQmaaa 156,72,12\nQmaaa 156,76,40\n"
"Qta 66,69,42752\nQmaaa 160,72,16\nQmaaa 160,76,44\nbuffer_load_dwordx4 v[84:87], v79, s[48:51], s31 offen offset:2048 sc1\nQmaaa 164,72,20\nQta 68,69,49920\nQmaaa 164,76,48\nQmaaa 168,72,24\n"
"Qmaaa 168,76,52\nQta 70,69,50944\ns_mov_b32 s31, 802816\ns_waitcnt lgkmcnt(4)\nQmvaa 24,56,24\nbuffer_load_dwordx4 v[88:91], v79, s[48:51], s31 offen offset:0 sc1\nQmvaa 24,60,52\nQmvaa 20,56,20\n"
"Qmvaa 20,60,48\nQmvaa 16,56,16\nQta 72,69,57344\nQmvaa 16,60,44\nbuffer_load_dwordx4 v[120:123], v79, s[48:51], s31 offen offset:1024 sc1\nQmvaa 12,56,12\nQmvaa 12,60,40\nQta 74,69,57856\n"
"Qmvaa 8,56,8\nQmvaa 8,60,36\nbuffer_load_dwordx4 v[152:155], v79, s[48:51], s31 offen offset:2048 sc1\nQmvaa 4,56,4\nQta 76,69,58368\nv_add_u32 v75, 1, v75\nQmvaa 4,60,32\ns_waitcnt vmcnt(18)\n"
"ds_write_b32 v73, v75\nQmvaa 0,56,0\nQmvaa 0,60,28\nQta 78,69,58880\ns_waitcnt lgkmcnt(5)\nQmvaa 28,64,0\nbuffer_load_dwordx4 v[184:187], v79, s[48:51], s31 offen offset:3072 sc1\nQmvaa 28,68,28\n"
"Qmvaa 32,64,4\nQmvaa 32,68,32\nQmvaa 36,64,8\nQta 56,69,57600\nQmvaa 36,68,36\nds_read_b32 v66, v74\ns_mov_b32 s31, 806912\nQmvaa 40,64,12\n"
".Lpg15:\ns_cbranch_execz .Lpg19\nbuffer_load_dwordx4 v[216:219], v79, s[48:51], s31 offen offset:0 sc1\nQmvaa 40,68,40\nQta 58,69,58112\nQmvaa 44,64,16\nQmvaa 44,68,44\ns_waitcnt lgkmcnt(1)\n"
"v_cmp_gt_u32 vcc, v75, v66\nQmvaa 48,64,20\ns_cbranch_vccnz .Lso1020\n.Lro1020:\nQmvaa 48,68,48\nQta 60,69,58624\nQmvaa 52,64,24\nbuffer_load_dwordx4 v[248:251], v79, s[48:51], s31 offen offset:1024 sc1\n"
"Qmvaa 52,68,52\ns_waitcnt lgkmcnt(4)\nQmaaa 196,72,24\nQta 62,69,59136\nQmaaa 196,76,52\nQmaaa 192,72,20\nbuffer_load_dwordx4 v[116:119], v79, s[48:51], s31 offen offset:2048 sc1\nQmaaa 192,76,48\n"
"Qmaaa 188,72,16\nQta 64,68,0\nQmaaa 188,76,44\nQmaaa 184,72,12\nQmaaa 184,76,40\nQta 66,68,1024\ns_add_u32 m0, s96, 132096\ns_mov_b32 s95, 1110016\n"
"Qmaaa 180,72,8\nbuffer_load_dwordx4 v61, s[52:55], s95 offen lds\nQmaaa 180,76,36\nQmaaa 176,72,4\nQta 68,68,8192\nQmaaa 176,76,32\nQmaaa 172,72,0\nQmaaa 172,76,28\n"
"Qta 70,68,9216\ns_mov_b32 s95, 1129472\ns_waitcnt lgkmcnt(4)\nQmvaa 92,56,0\nbuffer_load_dwordx4 v61, s[52:55], s95 offen offset:1024 lds\nQmvaa 92,60,28\nQmvaa 124,56,4\nQmvaa 124,60,32\n"
"Qmvaa 156,56,8\nQta 72,68,256\ns_add_u32 m0, s96, 140288\ns_mov_b32 s95, 1069056\nQmvaa 156,60,36\nbuffer_load_dwordx4 v62, s[52:55], s95 offen lds\nQmvaa 188,56,12\nQmvaa 188,60,40\n"
"Qta 74,68,1280\nQmvaa 220,56,16\nQmvaa 220,60,44\nQmvaa 252,56,20\nQta 76,68,8448\ns_mov_b32 s95, 1108992\nQmvaa 252,60,48\nbuffer_load_dwordx4 v62, s[52:55], s95 offen offset:1024 lds\n"
"Qmvaa 100,56,24\nQmvaa 100,60,52\nQta 78,68,9472\ns_waitcnt lgkmcnt(4)\nQmaaa 224,64,24\nQmaaa 224,68,52\ns_add_u32 m0, s96, 148480\ns_mov_b32 s95, 1069056\n"
"Qmaaa 220,64,20\nbuffer_load_dwordx4 v63, s[52:55], s95 offen lds\nQmaaa 220,68,48\nQmaaa 216,64,16\nQta 56,68,512\nQmaaa 216,68,44\nQmaaa 212,64,12\nQmaaa 212,68,40\n"
"Qta 58,68,1536\ns_mov_b32 s95, 1088512\nQmaaa 208,64,8\nbuffer_load_dwordx4 v63, s[52:55], s95 offen offset:1024 lds\nQmaaa 208,68,36\nQmaaa 204,64,4\nQta 60,68,8704\nQmaaa 204,68,32\n"
"Qmaaa 200,64,0\nQmaaa 200,68,28\nQta 62,68,9728\ns_waitcnt lgkmcnt(4)\nQmaaa 228,72,0\nQmaaa 228,76,28\nQmaaa 232,72,4\nbuffer_store_dwordx4 v[244:247], v79, s[56:59], s88 offen offset:2048 sc1\n"
"Qmaaa 232,76,32\nQmaaa 236,72,8\nQta 64,68,768\nQmaaa 236,76,36\nQmaaa 240,72,12\nQmaaa 240,76,40\nQta 66,68,1792\nQmaaa 244,72,16\n"
"Qmaaa 244,76,44\nQmaaa 248,72,20\nQta 68,68,8960\ns_add_u32 m0, s96, 65536\ns_mov_b32 s95, 1110016\nQmaaa 248,76,48\nbuffer_load_dwordx4 v63, s[52:55], s95 offen lds\nQmaaa 252,72,24\n"
"Qmaaa 252,76,52\nQta 70,68,9984\ns_waitcnt lgkmcnt(4)\nQmvaa 172,56,24\nQmvaa 172,60,52\ns_mov_b32 s95, 1129472\nQmvaa 140,56,20\nbuffer_load_dwordx4 v63, s[52:55], s95 offen offset:1024 lds\n"
"Qmvaa 140,60,48\nQmvaa 108,56,16\nQta 72,68,16384\nQmvaa 108,60,44\nQmvaa 228,56,12\nQmvaa 228,60,40\nQta 74,68,17408\ns_add_u32 m0, s96, 73728\n"
"s_add_u32 s95, s67, 4096\nQmvaa 196,56,8\nbuffer_load_dwordx4 v61, s[52:55], s95 offen lds\nQmvaa 196,60,36\nQmvaa 164,56,4\nQta 76,68,24576\nQmvaa 164,60,32\nQmvaa 132,56,0\n"
"Qmvaa 132,60,28\nQta 78,68,25600\ns_add_u32 s95, s67, 23552\ns_waitcnt lgkmcnt(4)\nQmvaa 204,64,0\nbuffer_load_dwordx4 v61, s[52:55], s95 offen offset:1024 lds\nQmvaa 204,68,28\nQmvaa 236,64,4\n"
"Qmvaa 236,68,32\nQmvaa 96,64,8\nQta 56,68,16640\ns_mov_b32 s31, 917504\nQmvaa 96,68,36\nbuffer_load_dwordx4 a[0:3], v79, s[48:51], s31 offen offset:0 sc1\nQmvaa 128,64,12\nQmvaa 128,68,40\n"
"Qta 58,68,17664\nQmvaa 160,64,16\nQmvaa 160,68,44\nbuffer_load_dwordx4 a[4:7], v79, s[48:51], s31 offen offset:1024 sc1\nQmvaa 192,64,20\nQta 60,68,24832\nQmvaa 192,68,48\nQmvaa 224,64,24\n"
"Qmvaa 224,68,52\nQta 62,68,25856\ns_waitcnt vmcnt(13)\ns_waitcnt lgkmcnt(4)\nQmaav 140,72,84\nbuffer_load_dwordx4 a[8:11], v79, s[48:51], s31 offen offset:2048 sc1\nQmaav 140,76,116\nQmaav 136,72,240\n"
"Qmaav 136,76,248\nQmaav 132,72,208\nQta 64,68,16896\nQmaav 132,76,216\nbuffer_load_dwordx4 a[12:15], v79, s[48:51], s31 offen offset:3072 sc1\nQmaav 128,72,176\nQmaav 128,76,184\nQta 66,68,17920\n"
"Qmaav 124,72,144\ns_mov_b32 s31, 921600\nQmaav 124,76,152\nbuffer_load_dwordx4 a[16:19], v79, s[48:51], s31 offen offset:0 sc1\nQmaav 120,72,112\nQta 68,68,25088\nQmaav 120,76,120\nQmaav 116,72,80\n"
"Qmaav 116,76,88\nQta 70,68,26112\ns_waitcnt lgkmcnt(4)\nQmaav 144,56,80\nbuffer_load_dwordx4 a[20:23], v79, s[48:51], s31 offen offset:1024 sc1\nQmaav 144,60,88\nQmaav 148,56,112\nbuffer_load_dword v78, v76, s[24:27], 0 offen sc1\n"
"Qmaav 148,60,120\nQmaav 152,56,144\nQta 72,68,17152\nQmaav 152,60,152\nbuffer_load_dwordx4 a[24:27], v79, s[48:51], s31 offen offset:2048 sc1\nQmaav 156,56,176\nQmaav 156,60,184\nQta 74,68,18176\n"
"Qmaav 160,56,208\ns_mov_b32 s31, 933888\nQmaav 160,60,216\nbuffer_load_dwordx4 a[28:31], v79, s[48:51], s31 offen offset:0 sc1\nQmaav 164,56,240\nQta 76,68,25344\nQmaav 164,60,248\nQmaav 168,56,84\n"
"Qmaav 168,60,116\nQta 78,68,26368\ns_waitcnt lgkmcnt(4)\nQmvav 24,64,84\nbuffer_load_dwordx4 a[32:35], v79, s[48:51], s31 offen offset:1024 sc1\nQmvav 24,68,116\nQmvav 20,64,240\nQmvav 20,68,248\n"
"Qmvav 16,64,208\nQta 56,68,32768\nQmvav 16,68,216\nbuffer_load_dwordx4 a[36:39], v79, s[48:51], s31 offen offset:2048 sc1\nQmvav 12,64,176\nQmvav 12,68,184\nQta 58,68,33280\nQmvav 8,64,144\n"
"Qmvav 8,68,152\nbuffer_load_dwordx4 a[40:43], v79, s[48:51], s31 offen offset:3072 sc1\nQmvav 4,64,112\nQta 60,68,33792\nv_add_u32 v75, 1, v75\nQmvav 4,68,120\ns_waitcnt vmcnt(19)\nds_write_b32 v73, v75\n"
"Qmvav 0,64,80\nQmvav 0,68,88\nQta 62,68,34304\ns_mov_b32 s31, 937984\ns_waitcnt lgkmcnt(5)\nQmvav 28,72,80\nbuffer_load_dwordx4 a[44:47], v79, s[48:51], s31 offen offset:0 sc1\nQmvav 28,76,88\n"
"Qmvav 32,72,112\nQmvav 32,76,120\nQmvav 36,72,144\nQta 64,68,33024\nQmvav 36,76,152\nds_read_b32 v66, v74\nQmvav 40,72,176\nbuffer_load_dwordx4 a[48:51], v79, s[48:51], s31 offen offset:1024 sc1\n"
"Qmvav 40,76,184\nQta 66,68,33536\nQmvav 44,72,208\nQmvav 44,76,216\ns_waitcnt lgkmcnt(1)\nv_cmp_gt_u32 vcc, v75, v66\nQmvav 48,72,240\ns_cbranch_vccnz .Lso1160\n"
".Lro1160:\nQmvav 48,76,248\nQta 68,68,34048\nQmvav 52,72,84\nbuffer_load_dwordx4 a[52:55], v79, s[48:51], s31 offen offset:2048 sc1\nQmvav 52,76,116\ns_waitcnt lgkmcnt(4)\nQmaav 196,56,84\n"
"Qta 70,68,34560\nQmaav 196,60,116\nQmaav 192,56,240\ns_add_u32 m0, s96, 81920\ns_add_u32 s95, s67, 45056\nQmaav 192,60,248\nbuffer_load_dwordx4 v61, s[52:55], s95 offen lds\nQmaav 188,56,208\n"
"Qta 72,68,40960\nQmaav 188,60,216\nQmaav 184,56,176\nQmaav 184,60,184\nQta 74,68,41984\ns_add_u32 s95, s67, 64512\nQmaav 180,56,144\nbuffer_load_dwordx4 v61, s[52:55], s95 offen offset:1024 lds\n"
"Qmaav 180,60,152\nQmaav 176,56,112\nQta 76,68,49152\nQmaav 176,60,120\nQmaav 172,56,80\nQmaav 172,60,88\nQta 78,68,50176\ns_add_u32 m0, s96, 90112\n"
"s_add_u32 s95, s67, 4096\ns_waitcnt lgkmcnt(4)\nQmvav 92,64,80\nbuffer_load_dwordx4 v62, s[52:55], s95 offen lds\nQmvav 92,68,88\nQmvav 124,64,112\nQmvav 124,68,120\nQmvav 156,64,144\n"
"Qta 56,68,41216\ns_add_u32 s95, s67, 44032\nQmvav 156,68,152\nbuffer_load_dwordx4 v62, s[52:55], s95 offen offset:1024 lds\nQmvav 188,64,176\ns_waitcnt vmcnt(12)\nv_sub_u32 v66, v78, v65\nv_cmp_gt_u32 vcc, 2, v66\n"
"s_and_b64 vcc, vcc, s[28:29]\ns_cbranch_scc1 .Lf2_w\n.Lf2_r:\nQmvav 188,68,184\nQta 58,68,42240\nQmvav 220,64,208\ns_add_u32 s31, s74, 0\nQmvav 220,68,216\n"
"buffer_load_dwordx4 a[80:83], v79, s[48:51], s31 offen offset:0 sc1\nQmvav 252,64,240\nQta 60,68,49408\nQmvav 252,68,248\nQmvav 100,64,84\nQmvav 100,68,116\nQta 62,68,50432\ns_waitcnt lgkmcnt(4)\n"
"Qmaav 224,72,84\nbuffer_load_dwordx4 a[84:87], v79, s[48:51], s31 offen offset:1024 sc1\nQmaav 224,76,116\nQmaav 220,72,240\nQmaav 220,76,248\nQmaav 216,72,208\nQta 64,68,41472\nQmaav 216,76,216\n"
"buffer_load_dwordx4 a[88:91], v79, s[48:51], s31 offen offset:2048 sc1\nQmaav 212,72,176\nQmaav 212,76,184\nQta 66,68,42496\nQmaav 208,72,144\nQmaav 208,76,152\nbuffer_load_dwordx4 a[92:95], v79, s[48:51], s31 offen offset:3072 sc1\nQmaav 204,72,112\n"
"Qta 68,68,49664\nQmaav 204,76,120\nQmaav 200,72,80\nQmaav 200,76,88\nQta 70,68,50688\ns_add_u32 s31, s74, 4096\ns_waitcnt lgkmcnt(4)\nQmaav 228,56,80\n"
"buffer_load_dwordx4 a[96:99], v79, s[48:51], s31 offen offset:0 sc1\nQmaav 228,60,88\nQmaav 232,56,112\nQmaav 232,60,120\nQmaav 236,56,144\nQta 72,68,41728\nQmaav 236,60,152\nbuffer_load_dwordx4 a[100:103], v79, s[48:51], s31 offen offset:1024 sc1\n"
"Qmaav 240,56,176\nQmaav 240,60,184\nQta 74,68,42752\nQmaav 244,56,208\nQmaav 244,60,216\nbuffer_load_dwordx4 a[104:107], v79, s[48:51], s31 offen offset:2048 sc1\nQmaav 248,56,240\nQta 76,68,49920\n"
"Qmaav 248,60,248\nQmaav 252,56,84\nQmaav 252,60,116\nQta 78,68,50944\ns_add_u32 s31, s74, 16384\ns_waitcnt lgkmcnt(4)\nQmvav 172,64,84\nbuffer_load_dwordx4 a[108:111], v79, s[48:51], s31 offen offset:0 sc1\n"
"Qmvav 172,68,116\nQmvav 140,64,240\nQmvav 140,68,248\nQmvav 108,64,208\nQta 56,68,57344\nQmvav 108,68,216\nbuffer_load_dwordx4 a[112:115], v79, s[48:51], s31 offen offset:1024 sc1\nQmvav 228,64,176\n"
"Qmvav 228,68,184\nQta 58,68,58368\nQmvav 196,64,144\nQmvav 196,68,152\nbuffer_load_dwordx4 v[168:171], v79, s[48:51], s31 offen offset:2048 sc1\nQmvav 164,64,112\nQta 60,70,1024\nQmvav 164,68,120\n"
"Qmvav 132,64,80\nQmvav 132,68,88\nQta 62,70,2048\ns_waitcnt lgkmcnt(4)\nQmvav 204,72,80\nbuffer_load_dwordx4 v[200:203], v79, s[48:51], s31 offen offset:3072 sc1\nQmvav 204,76,88\nQmvav 236,72,112\n"
"Qmvav 236,76,120\nQmvav 96,72,144\nQta 64,68,57600\ns_add_u32 s31, s74, 20480\nQmvav 96,76,152\nbuffer_load_dwordx4 v[232:235], v79, s[48:51], s31 offen offset:0 sc1\nQmvav 128,72,176\nQmvav 128,76,184\n"
"Qta 66,68,58624\nQmvav 160,72,208\nQmvav 160,76,216\nbuffer_load_dwordx4 v[212:215], v79, s[48:51], s31 offen offset:1024 sc1\nQmvav 192,72,240\nQta 68,70,1280\nQmvav 192,76,248\nQmvav 224,72,84\n"
"Qmvav 224,76,116\nQta 70,70,2304\ns_waitcnt vmcnt(17)\ns_waitcnt lgkmcnt(4)\nQmaaa 140,56,24\nbuffer_load_dwordx4 v[244:247], v79, s[48:51], s31 offen offset:2048 sc1\nQmaaa 140,60,52\nQmaaa 136,56,20\n"
"Qmaaa 136,60,48\nQmaaa 132,56,16\nQta 72,68,57856\ns_add_u32 m0, s96, 98304\ns_add_u32 s95, s67, 4096\nQmaaa 132,60,44\nbuffer_load_dwordx4 v63, s[52:55], s95 offen lds\nQmaaa 128,56,12\n"
"Qmaaa 128,60,40\nQta 74,68,58880\nQmaaa 124,56,8\nQmaaa 124,60,36\nQmaaa 120,56,4\nQta 76,70,1536\ns_add_u32 s95, s67, 23552\nQmaaa 120,60,32\n"
"buffer_load_dwordx4 v63, s[52:55], s95 offen offset:1024 lds\nQmaaa 116,56,0\nQmaaa 116,60,28\nQta 78,70,2560\ns_waitcnt lgkmcnt(4)\nQmaaa 144,64,0\nQmaaa 144,68,28\ns_add_u32 m0, s96, 106496\n"
"s_add_u32 s95, s67, 45056\nQmaaa 148,64,4\nbuffer_load_dwordx4 v63, s[52:55], s95 offen lds\nQmaaa 148,68,32\nQmaaa 152,64,8\nQta 56,68,58112\nQmaaa 152,68,36\nQmaaa 156,64,12\n"
"Qmaaa 156,68,40\nQta 58,68,59136\ns_add_u32 s95, s67, 64512\nQmaaa 160,64,16\nbuffer_load_dwordx4 v63, s[52:55], s95 offen offset:1024 lds\nQmaaa 160,68,44\nQmaaa 164,64,20\nQta 60,70,1792\n"
"Qmaaa 164,68,48\nQmaaa 168,64,24\nQmaaa 168,68,52\nQta 62,70,2816\ns_add_u32 m0, s96, 114688\ns_add_u32 s95, s72, 4096\ns_waitcnt lgkmcnt(4)\nQmvaa 24,72,24\n"
"buffer_load_dwordx4 v61, s[52:55], s95 offen lds\nQmvaa 24,76,52\nQmvaa 20,72,20\nQmvaa 20,76,48\nQmvaa 16,72,16\nQta 64,70,9216\ns_add_u32 s95, s72, 23552\nQmvaa 16,76,44\n"
"buffer_load_dwordx4 v61, s[52:55], s95 offen offset:1024 lds\nQmvaa 12,72,12\nQmvaa 12,76,40\nQta 66,70,9728\nQmvaa 8,72,8\nQmvaa 8,76,36\nQmvaa 4,72,4\nQta 68,70,10240\n"
"v_add_u32 v75, 1, v75\nQmvaa 4,76,32\ns_waitcnt vmcnt(20)\nds_write_b32 v73, v75\ns_add_u32 m0, s96, 122880\ns_add_u32 s95, s72, 45056\nQmvaa 0,72,0\nbuffer_load_dwordx4 v61, s[52:55], s95 offen lds\n"
"Qmvaa 0,76,28\nQta 70,70,10752\ns_waitcnt lgkmcnt(5)\nQmvaa 28,56,0\nQmvaa 28,60,28\nQmvaa 32,56,4\ns_add_u32 s95, s72, 64512\nQmvaa 32,60,32\n"
"buffer_load_dwordx4 v61, s[52:55], s95 offen offset:1024 lds\nQmvaa 36,56,8\nQta 72,70,9472\nQmvaa 36,60,36\nds_read_b32 v66, v74\nQmvaa 40,56,12\nQmvaa 40,60,40\nQta 74,70,9984\n"
"s_add_u32 m0, s96, 0\ns_add_u32 s95, s72, 4096\nQmvaa 44,56,16\nbuffer_load_dwordx4 v62, s[52:55], s95 offen lds\nv_add_u32 v67, 3, v65\nQmvaa 44,60,44\ns_waitcnt lgkmcnt(1)\nv_cmp_gt_u32 vcc, v75, v66\n"
"Qmvaa 48,56,20\ns_cbranch_vccnz .Lso1300\n.Lro1300:\nQmvaa 48,60,48\nQta 76,70,10496\nQmvaa 52,56,24\nbuffer_store_dword v67, v77, s[24:27], 0 offen sc1\ns_add_u32 s95, s72, 44032\n"
"Qmvaa 52,60,52\nbuffer_load_dwordx4 v62, s[52:55], s95 offen offset:1024 lds\ns_waitcnt lgkmcnt(4)\nQmaaa 196,64,24\nQta 78,70,11008\nQmaaa 196,68,52\nQmaaa 192,64,20\nQmaaa 192,68,48\n"
"Qmaaa 188,64,16\nQta 56,70,17408\ns_add_u32 m0, s96, 8192\ns_add_u32 s95, s72, 4096\nQmaaa 188,68,44\nbuffer_load_dwordx4 v63, s[52:55], s95 offen lds\nQmaaa 184,64,12\nQmaaa 184,68,40\n"
"Qta 58,70,18432\nQmaaa 180,64,8\nQmaaa 180,68,36\n.Lpg16:\ns_cbranch_execz .Lpg20\nQmaaa 176,64,4\nQta 60,69,0\ns_add_u32 s95, s72, 23552\n"
"Qmaaa 176,68,32\nbuffer_load_dwordx4 v63, s[52:55], s95 offen offset:1024 lds\nQmaaa 172,64,0\nQmaaa 172,68,28\nQta 62,69,1024\ns_waitcnt lgkmcnt(4)\nQmvaa 92,72,0\ns_add_u32 s31, s75, 0\n"
"Qmvaa 92,76,28\nbuffer_load_dwordx4 v[80:83], v79, s[48:51], s31 offen offset:0 sc1\nQmvaa 124,72,4\nQmvaa 124,76,32\nQmvaa 156,72,8\nQta 64,70,17664\nQmvaa 156,76,36\nbuffer_load_dwordx4 v[112:115], v79, s[48:51], s31 offen offset:1024 sc1\n"
"Qmvaa 188,72,12\nQmvaa 188,76,40\nQta 66,70,18688\nQmvaa 220,72,16\nQmvaa 220,76,44\nbuffer_load_dwordx4 v[144:147], v79, s[48:51], s31 offen offset:2048 sc1\nQmvaa 252,72,20\nQta 68,69,256\n"
"Qmvaa 252,76,48\nQmvaa 100,72,24\nQmvaa 100,76,52\nQta 70,69,1280\ns_waitcnt lgkmcnt(4)\nQmaaa 224,56,24\nbuffer_load_dwordx4 v[176:179], v79, s[48:51], s31 offen offset:3072 sc1\nQmaaa 224,60,52\n"
"Qmaaa 220,56,20\nQmaaa 220,60,48\nQmaaa 216,56,16\nQta 72,70,17920\ns_add_u32 s31, s75, 4096\nQmaaa 216,60,44\nbuffer_load_dwordx4 v[208:211], v79, s[48:51], s31 offen offset:0 sc1\nQmaaa 212,56,12\n"
"Qmaaa 212,60,40\nQta 74,70,18944\nQmaaa 208,56,8\nQmaaa 208,60,36\nbuffer_load_dwordx4 v[240:243], v79, s[48:51], s31 offen offset:1024 sc1\nQmaaa 204,56,4\nQta 76,69,512\nQmaaa 204,60,32\n"
"Qmaaa 200,56,0\nQmaaa 200,60,28\nQta 78,69,1536\ns_waitcnt lgkmcnt(4)\nQmaaa 228,64,0\nbuffer_load_dwordx4 v[84:87], v79, s[48:51], s31 offen offset:2048 sc1\nQmaaa 228,68,28\nQmaaa 232,64,4\n"
"Qmaaa 232,68,32\nQmaaa 236,64,8\nQta 56,70,18176\ns_add_u32 s31, s75, 16384\nQmaaa 236,68,36\nbuffer_load_dwordx4 v[88:91], v79, s[48:51], s31 offen offset:0 sc1\nQmaaa 240,64,12\nQmaaa 240,68,40\n"
"Qta 58,70,19200\nQmaaa 244,64,16\nQmaaa 244,68,44\nbuffer_load_dwordx4 v[120:123], v79, s[48:51], s31 offen offset:1024 sc1\nQmaaa 248,64,20\nQta 60,69,768\nQmaaa 248,68,48\nQmaaa 252,64,24\n"
"Qmaaa 252,68,52\nQta 62,69,1792\ns_waitcnt lgkmcnt(4)\nQmvaa 172,72,24\nbuffer_load_dwordx4 v[152:155], v79, s[48:51], s31 offen offset:2048 sc1\nQmvaa 172,76,52\nQmvaa 140,72,20\nQmvaa 140,76,48\n"
"Qmvaa 108,72,16\nQta 64,69,8192\nQmvaa 108,76,44\nbuffer_load_dwordx4 v[184:187], v79, s[48:51], s31 offen offset:3072 sc1\nQmvaa 228,72,12\nQmvaa 228,76,40\nQta 66,69,9216\nQmvaa 196,72,8\n"
"s_add_u32 s31, s75, 20480\nQmvaa 196,76,36\nbuffer_load_dwordx4 v[216:219], v79, s[48:51], s31 offen offset:0 sc1\nQmvaa 164,72,4\nQta 68,69,16384\nQmvaa 164,76,32\nQmvaa 132,72,0\nQmvaa 132,76,28\n"
"Qta 70,69,17408\ns_waitcnt lgkmcnt(4)\nQmvaa 204,56,0\nbuffer_load_dwordx4 v[248:251], v79, s[48:51], s31 offen offset:1024 sc1\nQmvaa 204,60,28\nQmvaa 236,56,4\nQmvaa 236,60,32\nQmvaa 96,56,8\n"
"Qta 72,69,8448\nQmvaa 96,60,36\nbuffer_load_dwordx4 v[116:119], v79, s[48:51], s31 offen offset:2048 sc1\nQmvaa 128,56,12\nQmvaa 128,60,40\nQta 74,69,9472\nQmvaa 160,56,16\nQmvaa 160,60,44\n"
"Qmvaa 192,56,20\nQta 76,69,16640\ns_add_u32 m0, s96, 16384\ns_add_u32 s95, s72, 45056\nQmvaa 192,60,48\nbuffer_load_dwordx4 v63, s[52:55], s95 offen lds\nQmvaa 224,56,24\nQmvaa 224,60,52\n"
"Qta 78,69,17664\ns_waitcnt vmcnt(28)\ns_waitcnt lgkmcnt(4)\nQmaaa 140,64,104\nQmaav 140,68,244\ns_add_u32 s95, s72, 64512\nQmaaa 136,64,100\nbuffer_load_dwordx4 v63, s[52:55], s95 offen offset:1024 lds\n"
"Qmaav 136,68,212\nQmaaa 132,64,96\nQta 56,69,8704\nQmaav 132,68,232\nQmaaa 128,64,92\nQmaav 128,68,200\nQta 58,69,9728\ns_add_u32 m0, s96, 24576\n"
"s_add_u32 s95, s73, 4096\nQmaaa 124,64,88\nbuffer_load_dwordx4 v61, s[52:55], s95 offen lds\nQmaav 124,68,168\nQmaaa 120,64,84\nQta 60,69,16896\nQmaaa 120,68,112\nQmaaa 116,64,80\n"
"Qmaaa 116,68,108\nQta 62,69,17920\ns_add_u32 s95, s73, 23552\ns_waitcnt lgkmcnt(4)\nQmaaa 144,72,80\nbuffer_load_dwordx4 v61, s[52:55], s95 offen offset:1024 lds\nQmaaa 144,76,108\nQmaaa 148,72,84\n"
"Qmaaa 148,76,112\nQmaaa 152,72,88\nQta 64,69,8960\ns_add_u32 m0, s96, 32768\ns_add_u32 s95, s73, 45056\nQmaav 152,76,168\nbuffer_load_dwordx4 v61, s[52:55], s95 offen lds\nQmaaa 156,72,92\n"
"Qmaav 156,76,200\nQta 66,69,9984\nQmaaa 160,72,96\nQmaav 160,76,232\nQmaaa 164,72,100\nQta 68,69,17152\ns_add_u32 s95, s73, 64512\nQmaav 164,76,212\n"
"buffer_load_dwordx4 v61, s[52:55], s95 offen offset:1024 lds\nQmaaa 168,72,104\nQmaav 168,76,244\nQta 70,69,18176\ns_waitcnt lgkmcnt(4)\nQmvaa 24,56,104\nQmvav 24,60,244\ns_add_u32 m0, s96, 40960\n"
"s_add_u32 s95, s73, 4096\nQmvaa 20,56,100\nbuffer_load_dwordx4 v62, s[52:55], s95 offen lds\nQmvav 20,60,212\nQmvaa 16,56,96\nQta 72,69,24576\nQmvav 16,60,232\nQmvaa 12,56,92\n"
"Qmvav 12,60,200\nQta 74,69,25088\ns_add_u32 s95, s73, 44032\nQmvaa 8,56,88\nbuffer_load_dwordx4 v62, s[52:55], s95 offen offset:1024 lds\nQmvav 8,60,168\nQmvaa 4,56,84\nQta 76,69,25600\n"
"v_add_u32 v75, 1, v75\nQmvaa 4,60,112\ns_waitcnt vmcnt(33)\nds_write_b32 v73, v75\nQmvaa 0,56,80\nQmvaa 0,60,108\nQta 78,69,26112\ns_add_u32 m0, s96, 49152\n"
"s_add_u32 s95, s73, 4096\ns_waitcnt lgkmcnt(5)\nQmvaa 28,64,80\nbuffer_load_dwordx4 v63, s[52:55], s95 offen lds\nQmvaa 28,68,108\nQmvaa 32,64,84\nQmvaa 32,68,112\nQmvaa 36,64,88\n"
"Qta 56,69,24832\nQmvav 36,68,168\nds_read_b32 v66, v74\ns_add_u32 s95, s73, 23552\nQmvaa 40,64,92\nbuffer_load_dwordx4 v63, s[52:55], s95 offen offset:1024 lds\nQmvav 40,68,200\nQta 58,69,25344\n"
"Qmvaa 44,64,96\nQmvav 44,68,232\ns_waitcnt lgkmcnt(1)\nv_cmp_gt_u32 vcc, v75, v66\nQmvaa 48,64,100\ns_cbranch_vccnz .Lso1440\n.Lro1440:\nQmvav 48,68,212\n"
"Qta 60,69,25856\nv_add_u32 v75, 1, v75\nQmvaa 52,64,104\ns_waitcnt vmcnt(29)\nds_write_b32 v73, v75\ns_add_u32 s31, s77, 0\nQmvav 52,68,244\nbuffer_load_dwordx4 a[0:3], v79, s[48:51], s31 offen offset:0 sc1\n"
"s_waitcnt lgkmcnt(5)\nQmaaa 196,72,104\nQta 62,69,26368\nQmaav 196,76,244\nQmaaa 192,72,100\nQmaav 192,76,212\nbuffer_load_dwordx4 a[4:7], v79, s[48:51], s31 offen offset:1024 sc1\nQmaaa 188,72,96\n"
"Qta 64,69,32768\nQmaav 188,76,232\nbuffer_load_dword v78, v76, s[24:27], 0 offen sc1\nQmaaa 184,72,92\nds_read_b32 v66, v74\nQmaav 184,76,200\nQta 66,69,33792\nQmaaa 180,72,88\n"
"buffer_load_dwordx4 a[8:11], v79, s[48:51], s31 offen offset:2048 sc1\nQmaav 180,76,168\nQmaaa 176,72,84\ns_waitcnt lgkmcnt(1)\nv_cmp_gt_u32 vcc, v75, v66\nQmaaa 176,76,112\ns_cbranch_vccnz .Lso1455\n.Lro1455:\n"
"Qmaaa 172,72,80\nQta 68,69,40960\nQmaaa 172,76,108\nbuffer_load_dwordx4 a[12:15], v79, s[48:51], s31 offen offset:3072 sc1\ns_waitcnt lgkmcnt(4)\nQmvaa 92,56,80\nQmvaa 92,60,108\nQta 70,69,41984\n"
"Qmvaa 124,56,84\ns_add_u32 s31, s77, 4096\nQmvaa 124,60,112\nbuffer_load_dwordx4 a[16:19], v79, s[48:51], s31 offen offset:0 sc1\nQmvaa 156,56,88\nQta 72,69,33024\nQmvav 156,60,168\nQmvaa 188,56,92\n"
"Qmvav 188,60,200\nQta 74,69,34048\nQmvaa 220,56,96\nbuffer_load_dwordx4 a[20:23], v79, s[48:51], s31 offen offset:1024 sc1\nQmvav 220,60,232\nQmvaa 252,56,100\nQta 76,69,41216\nQmvav 252,60,212\n"
"Qmvaa 100,56,104\nbuffer_load_dwordx4 a[24:27], v79, s[48:51], s31 offen offset:2048 sc1\nQmvav 100,60,244\nQta 78,69,42240\ns_waitcnt lgkmcnt(4)\nQmaaa 224,64,104\nQmaav 224,68,244\ns_add_u32 s31, s77, 16384\n"
"Qmaaa 220,64,100\nbuffer_load_dwordx4 a[28:31], v79, s[48:51], s31 offen offset:0 sc1\nQmaav 220,68,212\nQmaaa 216,64,96\nQta 56,69,33280\nQmaav 216,68,232\nQmaaa 212,64,92\nbuffer_load_dwordx4 a[32:35], v79, s[48:51], s31 offen offset:1024 sc1\n"
"Qmaav 212,68,200\nQta 58,69,34304\nQmaaa 208,64,88\nQmaav 208,68,168\nQmaaa 204,64,84\nQta 60,69,41472\nQmaaa 204,68,112\nbuffer_load_dwordx4 a[36:39], v79, s[48:51], s31 offen offset:2048 sc1\n"
"Qmaaa 200,64,80\nQmaaa 200,68,108\nQta 62,69,42496\ns_waitcnt lgkmcnt(4)\nQmaaa 228,72,80\nQmaaa 228,76,108\nbuffer_load_dwordx4 a[40:43], v79, s[48:51], s31 offen offset:3072 sc1\nQmaaa 232,72,84\n"
"Qmaaa 232,76,112\nQmaaa 236,72,88\nQta 64,69,33536\ns_add_u32 s31, s77, 20480\nQmaav 236,76,168\nbuffer_load_dwordx4 a[44:47], v79, s[48:51], s31 offen offset:0 sc1\nQmaaa 240,72,92\nQmaav 240,76,200\n"
"Qta 66,69,34560\nQmaaa 244,72,96\nQmaav 244,76,232\nbuffer_load_dwordx4 a[48:51], v79, s[48:51], s31 offen offset:1024 sc1\nQmaaa 248,72,100\nQta 68,69,41728\nQmaav 248,76,212\nQmaaa 252,72,104\n"
"Qmaav 252,76,244\nQta 70,69,42752\ns_waitcnt lgkmcnt(4)\nQmvaa 172,56,104\nbuffer_load_dwordx4 a[52:55], v79, s[48:51], s31 offen offset:2048 sc1\nQmvav 172,60,244\nQmvaa 140,56,100\nQmvav 140,60,212\n"
"Qmvaa 108,56,96\nQta 72,69,49152\ns_add_u32 m0, s96, 57344\ns_add_u32 s95, s73, 45056\nQmvav 108,60,232\nbuffer_load_dwordx4 v63, s[52:55], s95 offen lds\nQmvaa 228,56,92\nQmvav 228,60,200\n"
"Qta 74,69,50176\nQmvaa 196,56,88\nQmvav 196,60,168\ns_waitcnt vmcnt(13)\nv_sub_u32 v66, v78, v65\nv_cmp_gt_u32 vcc, 3, v66\ns_and_b64 vcc, vcc, s[28:29]\ns_cbranch_scc1 .Lf3_w\n"
".Lf3_r:\nQmvaa 164,56,84\nQta 76,69,57344\ns_add_u32 s95, s73, 64512\nQmvaa 164,60,112\nbuffer_load_dwordx4 v63, s[52:55], s95 offen offset:1024 lds\nv_add_u32 v75, 1, v75\nQmvaa 132,56,80\n"
"s_waitcnt vmcnt(41)\nds_write_b32 v73, v75\nQmvaa 132,60,108\nQta 78,69,58368\ns_waitcnt lgkmcnt(5)\nQmvaa 204,64,80\ns_add_u32 s31, s74, 131072\nQmvaa 204,68,108\n"
"buffer_load_dwordx4 a[80:83], v79, s[48:51], s31 offen offset:0 sc1\nQmvaa 236,64,84\nQmvaa 236,68,112\ns_bitcmp1_b32 s19, 2\ns_cbranch_scc1 .Lrole1\nQmvaa 96,64,88\nQta 56,69,49408\ns_lshl_b32 s46, s19, 4\n"
"Qmvav 96,68,168\nbuffer_load_dwordx4 a[84:87], v79, s[48:51], s31 offen offset:1024 sc1\nQmvaa 128,64,92\nds_read_b32 v66, v74\ns_add_u32 s46, s46, 128\nQmvav 128,68,200\nQta 58,69,50432\nv_mov_b32 v77, 0x7ffff000\n"
"Qmvaa 160,64,96\nQmvav 160,68,232\nbuffer_load_dwordx4 a[88:91], v79, s[48:51], s31 offen offset:2048 sc1\nQmvaa 192,64,100\ns_waitcnt lgkmcnt(1)\nv_cmp_gt_u32 vcc, v75, v66\nQmvav 192,68,212\ns_cbranch_vccnz .Lso1525r0\n"
".Lro1525r0:\nQmvaa 224,64,104\nQta 60,69,57600\nQmvav 224,68,244\nbuffer_load_dwordx4 a[92:95], v79, s[48:51], s31 offen offset:3072 sc1\ns_waitcnt vmcnt(31)\ns_waitcnt lgkmcnt(4)\nQmaav 140,72,84\n"
"Qmaav 140,76,116\nQta 62,69,58624\nQmaav 136,72,240\nQmaav 136,76,248\nQmaav 132,72,208\nQta 64,69,49664\ns_add_u32 m0, s96, 65536\ns_add_u32 s95, s67, 86016\n"
"Qmaav 132,76,216\nbuffer_load_dwordx4 v63, s[52:55], s95 offen lds\nQmaav 128,72,176\nQmaav 128,76,184\nQta 66,69,50688\nQmaav 124,72,144\nQmaav 124,76,152\nQmaav 120,72,112\n"
"Qta 68,69,57856\ns_add_u32 s95, s67, 105472\nQmaav 120,76,120\nbuffer_load_dwordx4 v63, s[52:55], s95 offen offset:1024 lds\nQmaav 116,72,80\nQmaav 116,76,88\nQta 70,69,58880\ns_waitcnt lgkmcnt(4)\n"
"Qmaav 144,56,80\nQmaav 144,60,88\ns_add_u32 m0, s96, 73728\ns_add_u32 s95, s67, 126976\nQmaav 148,56,112\nbuffer_load_dwordx4 v63, s[52:55], s95 offen lds\nQmaav 148,60,120\nQmaav 152,56,144\n"
"Qta 72,69,49920\nQmaav 152,60,152\nQmaav 156,56,176\nQmaav 156,60,184\nQta 74,69,50944\ns_add_u32 s95, s67, 146432\nQmaav 160,56,208\nbuffer_load_dwordx4 v63, s[52:55], s95 offen offset:1024 lds\n"
"Qmaav 160,60,216\nQmaav 164,56,240\nQta 76,69,58112\nQmaav 164,60,248\nQmaav 168,56,84\nQmaav 168,60,116\nQta 78,69,59136\ns_add_u32 m0, s96, 81920\n"
"s_add_u32 s95, s72, 86016\ns_waitcnt lgkmcnt(4)\nQmvav 24,64,84\nbuffer_load_dwordx4 v63, s[52:55], s95 offen lds\nQmvav 24,68,116\nQmvav 20,64,240\nQmvav 20,68,248\nQmvav 16,64,208\n"
"Qta 56,68,0\ns_add_u32 s95, s72, 105472\nQmvav 16,68,216\nbuffer_load_dwordx4 v63, s[52:55], s95 offen offset:1024 lds\nQmvav 12,64,176\nQmvav 12,68,184\nQta 58,68,512\nQmvav 8,64,144\n"
"Qmvav 8,68,152\nQmvav 4,64,112\nQta 60,68,1024\ns_add_u32 m0, s96, 90112\ns_add_u32 s95, s72, 126976\nQmvav 4,68,120\nbuffer_load_dwordx4 v63, s[52:55], s95 offen lds\nQmvav 0,64,80\n"
"Qmvav 0,68,88\nQta 62,68,1536\ns_waitcnt lgkmcnt(4)\nQmvav 28,72,80\nQmvav 28,76,88\ns_add_u32 s95, s72, 146432\nQmvav 32,72,112\nbuffer_load_dwordx4 v63, s[52:55], s95 offen offset:1024 lds\n"
"Qmvav 32,76,120\nQmvav 36,72,144\nQta 64,68,256\nQmvav 36,76,152\ns_add_u32 s31, s74, 135168\nQmvav 40,72,176\nbuffer_load_dwordx4 a[96:99], v79, s[48:51], s31 offen offset:0 sc1\nQmvav 40,76,184\n"
"Qta 66,68,768\nQmvav 44,72,208\nv_cmp_eq_u32 s[44:45], s46, v60\ns_nop 1\nv_cndmask_b32 v77, v77, v76, s[44:45]\nv_add_u32 v67, 1, v65\ns_xor_b32 s46, s19, 12\ns_lshl_b32 s28, 1, s46\n"
"Qmvav 44,76,216\nQmvav 48,72,240\nQta 68,68,1280\nQmvav 48,76,248\nbuffer_load_dwordx4 a[100:103], v79, s[48:51], s31 offen offset:1024 sc1\nv_add_u32 v75, 1, v75\nQmvav 52,72,84\ns_waitcnt vmcnt(35)\n"
"ds_write_b32 v73, v75\nQmvav 52,76,116\nQta 70,68,1792\ns_waitcnt lgkmcnt(5)\nQmaav 196,56,84\nQmaav 196,60,116\nbuffer_load_dwordx4 a[104:107], v79, s[48:51], s31 offen offset:2048 sc1\nQmaav 192,56,240\n"
"Qmaav 192,60,248\nQmaav 188,56,208\nQta 72,68,8192\ns_add_u32 s31, s74, 147456\nQmaav 188,60,216\nbuffer_load_dwordx4 a[108:111], v79, s[48:51], s31 offen offset:0 sc1\nQmaav 184,56,176\nds_read_b32 v66, v74\n"
"Qmaav 184,60,184\nQta 74,68,9216\nQmaav 180,56,144\nQmaav 180,60,152\nbuffer_load_dwordx4 a[112:115], v79, s[48:51], s31 offen offset:1024 sc1\nQmaav 176,56,112\ns_waitcnt lgkmcnt(1)\nv_cmp_gt_u32 vcc, v75, v66\n"
"Qmaav 176,60,120\ns_cbranch_vccnz .Lso1595r0\n.Lro1595r0:\nQmaav 172,56,80\nQta 76,68,16384\nQmaav 172,60,88\nbuffer_load_dwordx4 v[168:171], v79, s[48:51], s31 offen offset:2048 sc1\ns_waitcnt lgkmcnt(4)\n"
"Qmvav 92,64,80\nQmvav 92,68,88\nQta 78,68,17408\nQmvav 124,64,112\nQmvav 124,68,120\nbuffer_load_dwordx4 v[200:203], v79, s[48:51], s31 offen offset:3072 sc1\n.Lpg17:\ns_cbranch_execz .Lpg21\n"
"Qmvav 156,64,144\nQta 56,68,8448\nQmvav 156,68,152\nQmvav 188,64,176\nQmvav 188,68,184\nQta 58,68,9472\ns_add_u32 s31, s74, 151552\nQmvav 220,64,208\n"
"buffer_load_dwordx4 v[232:235], v79, s[48:51], s31 offen offset:0 sc1\nQmvav 220,68,216\nQmvav 252,64,240\nQta 60,68,16640\nQmvav 252,68,248\nQmvav 100,64,84\nbuffer_load_dwordx4 v[212:215], v79, s[48:51], s31 offen offset:1024 sc1\nQmvav 100,68,116\n"
"Qta 62,68,17664\ns_waitcnt lgkmcnt(4)\nQmaav 224,72,84\nQmaav 224,76,116\nQmaav 220,72,240\nbuffer_load_dwordx4 v[244:247], v79, s[48:51], s31 offen offset:2048 sc1\nQmaav 220,76,248\nQmaav 216,72,208\n"
"Qta 64,68,8704\nQmaav 216,76,216\ns_add_u32 m0, s91, 0\ns_add_u32 s31, s77, 131072\nQmaav 212,72,176\nbuffer_load_dwordx4 v79, s[48:51], s31 offen sc1 lds\nQmaav 212,76,184\nQta 66,68,9728\n"
"Qmaav 208,72,144\nQmaav 208,76,152\nQmaav 204,72,112\nQta 68,68,16896\nQmaav 204,76,120\nbuffer_load_dwordx4 v79, s[48:51], s31 offen offset:1024 sc1 lds\nQmaav 200,72,80\nQmaav 200,76,88\n"
"Qta 70,68,17920\ns_waitcnt lgkmcnt(4)\nQmaav 228,56,80\nQmaav 228,60,88\nbuffer_load_dwordx4 v79, s[48:51], s31 offen offset:2048 sc1 lds\nQmaav 232,56,112\nQmaav 232,60,120\nQmaav 236,56,144\n"
"Qta 72,68,8960\nQmaav 236,60,152\nbuffer_load_dwordx4 v79, s[48:51], s31 offen offset:3072 sc1 lds\nQmaav 240,56,176\nQmaav 240,60,184\nQta 74,68,9984\nQmaav 244,56,208\ns_add_u32 m0, s91, 4096\n"
"s_add_u32 s31, s77, 135168\nQmaav 244,60,216\nbuffer_load_dwordx4 v79, s[48:51], s31 offen sc1 lds\nQmaav 248,56,240\nQta 76,68,17152\nQmaav 248,60,248\nQmaav 252,56,84\nQmaav 252,60,116\n"
"Qta 78,68,18176\ns_waitcnt lgkmcnt(4)\nQmvav 172,64,84\nbuffer_load_dwordx4 v79, s[48:51], s31 offen offset:1024 sc1 lds\nQmvav 172,68,116\nQmvav 140,64,240\nQmvav 140,68,248\nQmvav 108,64,208\n"
"Qta 56,68,24576\nQmvav 108,68,216\nbuffer_load_dwordx4 v79, s[48:51], s31 offen offset:2048 sc1 lds\nQmvav 228,64,176\nQmvav 228,68,184\nQta 58,68,25600\nQmvav 196,64,144\ns_add_u32 m0, s91, 33792\n"
"s_add_u32 s31, s77, 147456\nQmvav 196,68,152\nbuffer_load_dwordx4 v79, s[48:51], s31 offen sc1 lds\nQmvav 164,64,112\nQta 60,68,32768\nQmvav 164,68,120\nv_add_u32 v75, 1, v75\nQmvav 132,64,80\n"
"s_waitcnt vmcnt(47)\nds_write_b32 v73, v75\nQmvav 132,68,88\nQta 62,68,33792\ns_waitcnt lgkmcnt(5)\nQmvav 204,72,80\nbuffer_load_dwordx4 v79, s[48:51], s31 offen offset:1024 sc1 lds\nQmvav 204,76,88\n"
"Qmvav 236,72,112\nQmvav 236,76,120\nQmvav 96,72,144\nQta 64,68,24832\nQmvav 96,76,152\nbuffer_load_dwordx4 v79, s[48:51], s31 offen offset:2048 sc1 lds\nQmvav 128,72,176\nds_read_b32 v66, v74\n"
"Qmvav 128,76,184\nQta 66,68,25856\nQmvav 160,72,208\nQmvav 160,76,216\nbuffer_load_dwordx4 v79, s[48:51], s31 offen offset:3072 sc1 lds\nQmvav 192,72,240\ns_waitcnt lgkmcnt(1)\nv_cmp_gt_u32 vcc, v75, v66\n"
"Qmvav 192,76,248\ns_cbranch_vccnz .Lso1665r0\n.Lro1665r0:\nQmvav 224,72,84\nQta 68,68,33024\ns_add_u32 m0, s91, 37888\ns_add_u32 s31, s77, 151552\nQmvav 224,76,116\n"
"buffer_load_dwordx4 v79, s[48:51], s31 offen sc1 lds\ns_waitcnt vmcnt(36)\ns_waitcnt lgkmcnt(4)\nQmaaa 140,56,24\nQmaaa 140,60,52\nQta 70,68,34048\nQmaaa 136,56,20\nQmaaa 136,60,48\n"
"buffer_load_dwordx4 v79, s[48:51], s31 offen offset:1024 sc1 lds\nQmaaa 132,56,16\nQta 72,68,25088\nQmaaa 132,60,44\nQmaaa 128,56,12\nQmaaa 128,60,40\nQta 74,68,26112\nQmaaa 124,56,8\n"
"buffer_load_dwordx4 v79, s[48:51], s31 offen offset:2048 sc1 lds\nQmaaa 124,60,36\nQmaaa 120,56,4\nQta 76,68,33280\nQmaaa 120,60,32\nQmaaa 116,56,0\nQmaaa 116,60,28\nQta 78,68,34304\n"
"s_add_u32 m0, s96, 0\ns_add_u32 s95, s73, 86016\ns_waitcnt lgkmcnt(4)\nQmaaa 144,64,0\nbuffer_load_dwordx4 v63, s[52:55], s95 offen lds\nQmaaa 144,68,28\nQmaaa 148,64,4\nQmaaa 148,68,32\n"
"Qmaaa 152,64,8\nQta 56,68,25344\ns_add_u32 s95, s73, 105472\nQmaaa 152,68,36\nbuffer_load_dwordx4 v63, s[52:55], s95 offen offset:1024 lds\nQmaaa 156,64,12\nQmaaa 156,68,40\nQta 58,68,26368\n"
"Qmaaa 160,64,16\nQmaaa 160,68,44\nQmaaa 164,64,20\nQta 60,68,33536\ns_add_u32 m0, s96, 8192\ns_add_u32 s95, s73, 126976\nQmaaa 164,68,48\nbuffer_load_dwordx4 v63, s[52:55], s95 offen lds\n"
"Qmaaa 168,64,24\nQmaaa 168,68,52\nQta 62,68,34560\ns_waitcnt lgkmcnt(4)\nQmvaa 24,72,24\nQmvaa 24,76,52\ns_add_u32 s95, s73, 146432\nQmvaa 20,72,20\n"
"buffer_load_dwordx4 v63, s[52:55], s95 offen offset:1024 lds\nQmvaa 20,76,48\nQmvaa 16,72,16\nQta 64,68,40960\nQmvaa 16,76,44\ns_add_u32 s31, s75, 131072\nQmvaa 12,72,12\nbuffer_load_dwordx4 v[80:83], v79, s[48:51], s31 offen offset:0 sc1\n"
"Qmvaa 12,76,40\nQta 66,68,41472\nQmvaa 8,72,8\nQmvaa 8,76,36\nQmvaa 4,72,4\nQta 68,68,41984\nQmvaa 4,76,32\nbuffer_load_dwordx4 v[112:115], v79, s[48:51], s31 offen offset:1024 sc1\n"
"Qmvaa 0,72,0\nQmvaa 0,76,28\nQta 70,68,42496\ns_waitcnt lgkmcnt(4)\nQmvaa 28,56,0\nQmvaa 28,60,28\nbuffer_load_dwordx4 v[144:147], v79, s[48:51], s31 offen offset:2048 sc1\nQmvaa 32,56,4\n"
"Qmvaa 32,60,32\nQmvaa 36,56,8\nQta 72,68,41216\nQmvaa 36,60,36\nbuffer_load_dwordx4 v[176:179], v79, s[48:51], s31 offen offset:3072 sc1\nQmvaa 40,56,12\nQmvaa 40,60,40\nQta 74,68,41728\n"
"Qmvaa 44,56,16\ns_add_u32 s31, s75, 135168\nQmvaa 44,60,44\nbuffer_load_dwordx4 v[208:211], v79, s[48:51], s31 offen offset:0 sc1\nQmvaa 48,56,20\nQta 76,68,42240\nQmvaa 48,60,48\nv_add_u32 v75, 1, v75\n"
"Qmvaa 52,56,24\ns_waitcnt vmcnt(35)\nds_write_b32 v73, v75\nQmvaa 52,60,52\nQta 78,68,42752\ns_waitcnt lgkmcnt(5)\nQmaaa 196,64,24\nbuffer_load_dwordx4 v[240:243], v79, s[48:51], s31 offen offset:1024 sc1\n"
"Qmaaa 196,68,52\nQmaaa 192,64,20\nQmaaa 192,68,48\nQmaaa 188,64,16\nQta 56,68,49152\nQmaaa 188,68,44\nbuffer_load_dwordx4 v[84:87], v79, s[48:51], s31 offen offset:2048 sc1\nQmaaa 184,64,12\n"
"ds_read_b32 v66, v74\nQmaaa 184,68,40\nQta 58,68,50176\nQmaaa 180,64,8\ns_add_u32 s31, s75, 147456\nQmaaa 180,68,36\nbuffer_load_dwordx4 v[88:91], v79, s[48:51], s31 offen offset:0 sc1\nQmaaa 176,64,4\n"
"s_waitcnt lgkmcnt(1)\nv_cmp_gt_u32 vcc, v75, v66\nQmaaa 176,68,32\ns_cbranch_vccnz .Lso1735r0\n.Lro1735r0:\nQmaaa 172,64,0\nQta 60,68,57344\nQmaaa 172,68,28\n"
"buffer_load_dwordx4 v[120:123], v79, s[48:51], s31 offen offset:1024 sc1\ns_waitcnt lgkmcnt(4)\nQmvaa 92,72,0\nQmvaa 92,76,28\nQta 62,68,58368\nQmvaa 124,72,4\nQmvaa 124,76,32\nbuffer_load_dwordx4 v[152:155], v79, s[48:51], s31 offen offset:2048 sc1\n"
"Qmvaa 156,72,8\nQta 64,68,49408\nQmvaa 156,76,36\nQmvaa 188,72,12\nQmvaa 188,76,40\nQta 66,68,50432\nQmvaa 220,72,16\nbuffer_load_dwordx4 v[184:187], v79, s[48:51], s31 offen offset:3072 sc1\n"
"Qmvaa 220,76,44\nQmvaa 252,72,20\nQta 68,68,57600\nQmvaa 252,76,48\ns_add_u32 s31, s75, 151552\nQmvaa 100,72,24\nbuffer_load_dwordx4 v[216:219], v79, s[48:51], s31 offen offset:0 sc1\nQmvaa 100,76,52\n"
"Qta 70,68,58624\ns_waitcnt lgkmcnt(4)\nQmaaa 224,56,24\nQmaaa 224,60,52\nQmaaa 220,56,20\nbuffer_load_dwordx4 v[248:251], v79, s[48:51], s31 offen offset:1024 sc1\nQmaaa 220,60,48\nQmaaa 216,56,16\n"
"Qta 72,68,49664\nQmaaa 216,60,44\nQmaaa 212,56,12\nbuffer_load_dwordx4 v[116:119], v79, s[48:51], s31 offen offset:2048 sc1\nQmaaa 212,60,40\nQta 74,68,50688\nQmaaa 208,56,8\nQmaaa 208,60,36\n"
"Qmaaa 204,56,4\nQta 76,68,57856\ns_add_u32 m0, s96, 16384\ns_add_u32 s95, s67, 86016\nQmaaa 204,60,32\nbuffer_load_dwordx4 v61, s[52:55], s95 offen lds\nQmaaa 200,56,0\nQmaaa 200,60,28\n"
"Qta 78,68,58880\ns_waitcnt lgkmcnt(4)\nQmaaa 228,64,0\nQmaaa 228,68,28\ns_add_u32 s95, s67, 105472\nQmaaa 232,64,4\nbuffer_load_dwordx4 v61, s[52:55], s95 offen offset:1024 lds\nQmaaa 232,68,32\n"
"Qmaaa 236,64,8\nQta 56,68,49920\nQmaaa 236,68,36\nQmaaa 240,64,12\nQmaaa 240,68,40\nQta 58,68,50944\ns_add_u32 m0, s96, 24576\ns_add_u32 s95, s67, 126976\n"
"Qmaaa 244,64,16\nbuffer_load_dwordx4 v61, s[52:55], s95 offen lds\nQmaaa 244,68,44\nQmaaa 248,64,20\nQta 60,68,58112\nQmaaa 248,68,48\nQmaaa 252,64,24\nQmaaa 252,68,52\n"
"Qta 62,68,59136\ns_add_u32 s95, s67, 146432\ns_waitcnt lgkmcnt(4)\nQmvaa 172,72,24\nbuffer_load_dwordx4 v61, s[52:55], s95 offen offset:1024 lds\nQmvaa 172,76,52\nQmvaa 140,72,20\nQmvaa 140,76,48\n"
"Qmvaa 108,72,16\nQta 64,69,0\ns_add_u32 m0, s96, 32768\ns_add_u32 s95, s72, 86016\nQmvaa 108,76,44\nbuffer_load_dwordx4 v61, s[52:55], s95 offen lds\nQmvaa 228,72,12\nQmvaa 228,76,40\n"
"Qta 66,69,1024\nQmvaa 196,72,8\nQmvaa 196,76,36\nQmvaa 164,72,4\nQta 68,69,8192\ns_add_u32 s95, s72, 105472\nQmvaa 164,76,32\nbuffer_load_dwordx4 v61, s[52:55], s95 offen offset:1024 lds\n"
"v_add_u32 v75, 1, v75\nQmvaa 132,72,0\ns_waitcnt vmcnt(20)\nds_write_b32 v73, v75\nQmvaa 132,76,28\nQta 70,69,9216\ns_waitcnt lgkmcnt(5)\nQmvaa 204,56,0\n"
"s_waitcnt vmcnt(24)\nQda 0,58,0\nQmvaa 204,60,28\nQmvaa 236,56,4\nQda 28,58,33792\nQmvaa 236,60,32\nQmvaa 96,56,8\nQta 72,69,16384\n"
"Qmvaa 96,60,36\nQda 4,58,1024\nQmvaa 128,56,12\nds_read_b32 v66, v74\nQmvaa 128,60,40\nQta 74,69,17408\nQmvaa 160,56,16\nQda 32,58,34816\n"
"Qmvaa 160,60,44\nQmvaa 192,56,20\ns_waitcnt lgkmcnt(2)\nv_cmp_gt_u32 vcc, v75, v66\nQmvaa 192,60,48\ns_cbranch_vccnz .Lso1805r0\n.Lro1805r0:\nQmvaa 224,56,24\n"
"Qta 76,69,24576\ns_add_u32 m0, s96, 40960\ns_add_u32 s95, s72, 126976\nQmvaa 224,60,52\nbuffer_load_dwordx4 v61, s[52:55], s95 offen lds\ns_waitcnt vmcnt(39)\ns_waitcnt lgkmcnt(8)\nQmaaa 200,64,80\n"
"Qda 8,58,2048\nQmaaa 200,68,108\nQta 78,69,25600\nQmaaa 204,64,84\nQda 36,58,35840\nQmaaa 204,68,112\nQmaaa 208,64,88\nQta 56,68,0\n"
"s_add_u32 s95, s72, 146432\nQmaav 208,68,168\nbuffer_load_dwordx4 v61, s[52:55], s95 offen offset:1024 lds\nQmaaa 212,64,92\nQda 12,58,3072\nQmaav 212,68,200\nQta 58,68,1024\nQmaaa 216,64,96\n"
"Qda 40,58,36864\nQmaav 216,68,232\nQmaaa 220,64,100\nQta 60,68,8192\ns_add_u32 m0, s96, 49152\ns_add_u32 s95, s73, 86016\nQmaav 220,68,212\nbuffer_load_dwordx4 v61, s[52:55], s95 offen lds\n"
"Qmaaa 224,64,104\nQda 16,58,4096\nQmaav 224,68,244\nQta 62,68,9216\ns_waitcnt vmcnt(9)\ns_waitcnt lgkmcnt(8)\nQmaav 200,72,80\nQda 44,58,37888\n"
"Qmaav 200,76,88\ns_add_u32 s95, s73, 105472\nQmaav 204,72,112\nbuffer_load_dwordx4 v61, s[52:55], s95 offen offset:1024 lds\nQmaav 204,76,120\nQda 20,58,5120\nQmaav 208,72,144\nQta 64,69,256\n"
"Qmaav 208,76,152\nQda 48,58,38912\nQmaav 212,72,176\nQmaav 212,76,184\nQta 66,69,1280\ns_add_u32 m0, s96, 57344\ns_add_u32 s95, s73, 126976\nQmaav 216,72,208\n"
"buffer_load_dwordx4 v61, s[52:55], s95 offen lds\nQmaav 216,76,216\nQda 24,58,6144\nQmaav 220,72,240\nQta 68,69,8448\nQmaav 220,76,248\nQda 52,58,39936\nQmaav 224,72,84\n"
"Qmaav 224,76,116\nQta 70,69,9472\ns_add_u32 s95, s73, 146432\ns_waitcnt lgkmcnt(9)\nQmaaa 200,56,0\nbuffer_load_dwordx4 v61, s[52:55], s95 offen offset:1024 lds\nQmaaa 200,60,28\nQmaaa 204,56,4\n"
"Qmaaa 204,60,32\ns_waitcnt lgkmcnt(9)\nQmaaa 208,56,8\nQta 72,69,16640\nQmaaa 208,60,36\ns_nop 1\nds_write_b128 v58, a[200:203] offset:0\nQmaaa 212,56,12\n"
"Qmaaa 212,60,40\nQta 74,69,17664\ns_waitcnt lgkmcnt(4)\nQmaaa 216,56,16\nds_write_b128 v58, a[204:207] offset:1024\nQmaaa 216,60,44\nQmaaa 220,56,20\nQta 76,69,24832\n"
"Qmaaa 220,60,48\nds_write_b128 v58, a[208:211] offset:2048\nQmaaa 224,56,24\nQmaaa 224,60,52\nQta 78,69,25856\ns_waitcnt lgkmcnt(7)\nQmaaa 228,64,80\nds_write_b128 v58, a[212:215] offset:3072\n"
"Qmaaa 228,68,108\nQmaaa 232,64,84\nQmaaa 232,68,112\nds_write_b128 v58, a[216:219] offset:4096\nQmaaa 236,64,88\nQta 56,68,256\nQmaav 236,68,168\nQmaaa 240,64,92\n"
"ds_write_b128 v58, a[220:223] offset:5120\nQmaav 240,68,200\nQta 58,68,1280\nQmaaa 244,64,96\nQmaav 244,68,232\nds_write_b128 v58, a[224:227] offset:6144\nQmaaa 248,64,100\nQta 60,68,8448\n"
"Qmaav 248,68,212\nQmaaa 252,64,104\nQmaav 252,68,244\nQta 62,68,9472\ns_waitcnt lgkmcnt(8)\nQmaav 228,72,80\nv_add_u32 v75, 1, v75\nQmaav 228,76,88\n"
"ds_write_b32 v73, v75\nQmaav 232,72,112\nQmaav 232,76,120\nQmaav 236,72,144\nQta 64,69,512\nQmaav 236,76,152\nQmaav 240,72,176\nQmaav 240,76,184\n"
"Qta 66,69,1536\nQmaav 244,72,208\nQmaav 244,76,216\nds_read_b32 v66, v74\nQmaav 248,72,240\nQta 68,69,8704\nQmaav 248,76,248\nQmaav 252,72,84\n"
"Qmaav 252,76,116\ns_waitcnt lgkmcnt(1)\nv_cmp_gt_u32 vcc, v75, v66\ns_waitcnt lgkmcnt(5)\nQmaaa 228,56,0\ns_cbranch_vccnz .Lso1878r0\n.Lro1878r0:\nQmaaa 228,60,28\n"
"Qta 70,69,9728\nQmaaa 232,56,4\nQmaaa 232,60,32\nQdv 148,59,2048\nQmaaa 236,56,8\nQta 72,69,16896\nQmaaa 236,60,36\ns_nop 0\n"
"ds_write_b128 v58, a[228:231] offset:33792\nQmaaa 240,56,12\nQdv 180,59,3072\nQmaaa 240,60,40\nQta 74,69,17920\nQmaaa 244,56,16\nds_write_b128 v58, a[232:235] offset:34816\nQmaaa 244,60,44\n"
"Qdv 104,59,5120\nQmaaa 248,56,20\nQta 76,69,25088\nQmaaa 248,60,48\n.Lpg18:\ns_cbranch_execz .Lpg22\nds_write_b128 v58, a[236:239] offset:35840\nQmaaa 252,56,24\n"
"Qdv 136,59,0\ns_waitcnt lgkmcnt(9)\nQa 196,196,148\nQmaaa 252,60,52\nQta 78,69,26112\nQa 197,197,149\ns_waitcnt lgkmcnt(10)\nQmvaa 132,64,80\n"
"ds_write_b128 v58, a[240:243] offset:36864\nQmvaa 132,68,108\nQa 198,198,150\nQa 199,199,151\nQmvaa 164,64,84\ns_waitcnt lgkmcnt(8)\nQa 228,228,180\nQa 229,229,181\n"
"Qmvaa 164,68,112\nds_write_b128 v58, a[244:247] offset:37888\nQmvaa 196,64,88\nQta 56,68,512\nQa 230,230,182\nQa 231,231,183\nQmvav 196,68,168\nQdv 148,59,4096\n"
"s_waitcnt lgkmcnt(8)\nQa 140,140,104\nQmvaa 228,64,92\nds_write_b128 v58, a[248:251] offset:38912\nQmvav 228,68,200\nQta 58,68,1536\nQa 141,141,105\nQmvaa 108,64,96\n"
"Qa 142,142,106\nQa 143,143,107\nQmvav 108,68,232\nQdv 180,59,6144\ns_waitcnt lgkmcnt(8)\nQa 132,132,136\nQmvaa 140,64,100\nQta 60,68,8704\n"
"Qa 133,133,137\nQmvav 140,68,212\nds_write_b128 v58, a[252:255] offset:39936\nQmvaa 172,64,104\nQa 134,134,138\nQa 135,135,139\nQmvav 172,68,244\nQta 62,68,9728\n"
"s_waitcnt lgkmcnt(6)\nQa 108,108,148\ns_waitcnt lgkmcnt(6)\nQmvav 132,72,80\nQdv 104,59,1024\nv_add_u32 v75, 1, v75\nQmvav 132,76,88\ns_waitcnt vmcnt(10)\n"
"ds_write_b32 v73, v75\nQmvav 164,72,112\nQa 109,109,149\nQa 110,110,150\nQmvav 164,76,120\nQa 111,111,151\ns_waitcnt lgkmcnt(5)\nQa 172,172,180\n"
"Qmvav 196,72,144\nQta 64,69,768\nQa 173,173,181\nQmvav 196,76,152\nQa 174,174,182\nQa 175,175,183\nQmvav 228,72,176\nQmvav 228,76,184\n"
"Qta 66,69,1792\ns_waitcnt lgkmcnt(3)\nQa 164,164,104\nQmvav 108,72,208\nQa 165,165,105\nQa 166,166,106\nQmvav 108,76,216\nds_read_b32 v66, v74\n"
"Qa 167,167,107\nQmvav 140,72,240\nQta 68,69,8960\nQmvav 140,76,248\nQmvav 172,72,84\nQmvav 172,76,116\ns_waitcnt lgkmcnt(1)\nv_cmp_gt_u32 vcc, v75, v66\n"
"s_waitcnt lgkmcnt(6)\nQmvaa 132,56,0\ns_cbranch_vccnz .Lso1920r0\n.Lro1920r0:\nQmvaa 132,60,28\nQta 70,69,9984\ns_add_u32 s68, s64, 0\nQmvaa 164,56,4\n"
"Qmvaa 164,60,32\nQdv 136,59,35840\nQmvaa 196,56,8\nQta 72,69,17152\ns_nop 0\nQk 132,132,133\nQmvaa 196,60,36\nQk 133,134,135\n"
"Qmvaa 228,56,12\nQdv 148,59,36864\nQk 134,164,165\nQmvaa 228,60,40\nQta 74,69,18176\nQk 135,166,167\nQmvaa 108,56,16\nbuffer_store_dwordx4 v[132:135], v79, s[24:27], s68 offen offset:0 sc1\n"
"Qk 196,196,197\nQmvaa 108,60,44\nQdv 180,59,38912\nQk 197,198,199\nQmvaa 140,56,20\nQta 76,69,25344\nQk 198,228,229\nQmvaa 140,60,48\n"
"s_waitcnt lgkmcnt(5)\nQa 96,96,136\nQa 97,97,137\nQmvaa 172,56,24\nQdv 104,59,33792\nQa 98,98,138\nQmvaa 172,60,52\nQta 78,69,26368\n"
"Qa 99,99,139\ns_waitcnt lgkmcnt(11)\nQmvaa 204,64,80\ns_waitcnt lgkmcnt(5)\nQa 128,128,148\nQa 129,129,149\nQmvaa 204,68,108\nQa 130,130,150\n"
"Qa 131,131,151\nQmvaa 236,64,84\nQk 199,230,231\nQk 108,108,109\nQmvaa 236,68,112\ns_waitcnt lgkmcnt(3)\nQa 192,192,180\nQa 193,193,181\n"
"Qmvaa 96,64,88\nQta 56,68,768\nQa 194,194,182\nQmvav 96,68,168\nQdv 136,59,37888\nQa 195,195,183\nQmvaa 128,64,92\ns_waitcnt lgkmcnt(3)\n"
"Qa 204,204,104\nQa 205,205,105\nQmvav 128,68,200\nQta 58,68,1792\nQa 206,206,106\nQmvaa 160,64,96\nbuffer_store_dwordx4 v[196:199], v79, s[24:27], s68 offen offset:1024 sc1\nQa 207,207,107\n"
"Qmvav 160,68,232\nQdv 148,59,39936\nQk 109,110,111\nQmvaa 192,64,100\nQta 60,68,8960\nQk 110,140,141\nQmvav 192,68,212\nQk 111,142,143\n"
"Qk 172,172,173\nQmvaa 224,64,104\nbuffer_store_dwordx4 v[108:111], v79, s[24:27], s68 offen offset:2048 sc1\nv_add_u32 v75, 1, v75\nQmvav 224,68,244\ns_waitcnt vmcnt(3)\nds_write_b32 v73, v75\ns_waitcnt lgkmcnt(10)\n"
"Qmvav 204,72,80\nQta 62,68,9984\ns_waitcnt lgkmcnt(5)\nQa 160,160,136\nQmvav 204,76,88\nQdv 180,59,34816\nQa 161,161,137\nQmvav 236,72,112\n"
"Qa 162,162,138\nQa 163,163,139\nQmvav 236,76,120\ns_waitcnt lgkmcnt(4)\nQa 224,224,148\nQa 225,225,149\nQmvav 96,72,144\nQta 64,68,16384\n"
"Qa 226,226,150\nQmvav 96,76,152\nQa 227,227,151\nQk 173,174,175\nQmvav 128,72,176\nQmvav 128,76,184\nds_read_b32 v66, v74\ns_waitcnt lgkmcnt(2)\n"
"Qa 236,236,180\nQmvav 160,72,208\nQta 66,68,17408\nQa 237,237,181\nQmvav 160,76,216\nQa 238,238,182\nQa 239,239,183\nQmvav 192,72,240\n"
"Qmvav 192,76,248\ns_waitcnt lgkmcnt(1)\nv_cmp_gt_u32 vcc, v75, v66\nQmvav 224,72,84\ns_cbranch_vccnz .Lso1960r0\n.Lro1960r0:\nQmvav 224,76,116\nQta 68,68,24576\n"
"s_add_u32 m0, s96, 65536\ns_add_u32 s95, s67, 86016\ns_waitcnt lgkmcnt(5)\nQmvaa 204,56,0\nbuffer_load_dwordx4 v62, s[52:55], s95 offen lds\nQmvaa 204,60,28\nQmvaa 236,56,4\nQta 70,68,25600\n"
"Qmvaa 236,60,32\nQmvaa 96,56,8\ns_nop 3\nQk 174,204,205\nQk 175,206,207\nQmvaa 96,60,36\nQta 72,68,32768\ns_add_u32 s95, s67, 125952\n"
"Qmvaa 128,56,12\nbuffer_load_dwordx4 v62, s[52:55], s95 offen offset:1024 lds\nQmvaa 128,60,40\nbuffer_store_dwordx4 v[172:175], v79, s[24:27], s68 offen offset:3072 sc1\nQk 236,236,237\nQmvaa 160,56,16\nQta 74,68,33792\nQk 237,238,239\n"
"Qmvaa 160,60,44\nQk 238,96,97\nQk 239,98,99\nQmvaa 192,56,20\ns_add_u32 s68, s64, 4096\nQk 128,128,129\nQmvaa 192,60,48\nQta 76,68,40960\n"
"s_add_u32 m0, s96, 73728\ns_add_u32 s95, s72, 86016\nQmvaa 224,56,24\nbuffer_load_dwordx4 v62, s[52:55], s95 offen lds\nQmvaa 224,60,52\nbuffer_store_dwordx4 v[236:239], v79, s[24:27], s68 offen offset:0 sc1\nQk 129,130,131\ns_waitcnt lgkmcnt(3)\n"
"Qmaaa 116,64,80\nQta 78,68,41984\nQk 130,160,161\nQmaaa 116,68,108\nQk 131,162,163\nQk 192,192,193\nQmaaa 120,64,84\nbuffer_store_dwordx4 v[128:131], v79, s[24:27], s68 offen offset:1024 sc1\n"
"s_add_u32 s95, s72, 125952\nQmaaa 120,68,112\nbuffer_load_dwordx4 v62, s[52:55], s95 offen offset:1024 lds\nQmaaa 124,64,88\nQta 56,68,49152\nQk 193,194,195\nQmaav 124,68,168\nQk 194,224,225\n"
"Qk 195,226,227\nQmaaa 128,64,92\nbuffer_store_dwordx4 v[192:195], v79, s[24:27], s68 offen offset:2048 sc1\nQmaav 128,68,200\nQta 58,68,50176\ns_add_u32 m0, s96, 81920\ns_add_u32 s95, s73, 86016\nQmaaa 132,64,96\n"
"buffer_load_dwordx4 v62, s[52:55], s95 offen lds\nQmaav 132,68,232\nQmaaa 136,64,100\nQta 60,68,57344\nQmaav 136,68,212\nQmaaa 140,64,104\nQmaav 140,68,244\nQta 62,68,58368\n"
"s_add_u32 s95, s73, 125952\ns_waitcnt lgkmcnt(4)\nQmaav 116,72,80\nbuffer_load_dwordx4 v62, s[52:55], s95 offen offset:1024 lds\nQmaav 116,76,88\nQmaav 120,72,112\nv_bfe_u32 v61, v60, 6, 2\nv_lshrrev_b32 v62, 8, v60\n"
"Qmaav 120,76,120\nv_xor_b32 v61, v61, v62\nv_bfe_u32 v62, v60, 5, 1\nQmaav 124,72,144\nQta 64,68,16640\nv_lshl_or_b32 v61, v61, 1, v62\nQmaav 124,76,152\nv_and_b32 v62, 16, v60\n"
"v_mov_b32 v63, 0x3f80\nQmaav 128,72,176\nv_lshlrev_b32 v63, v62, v63\nv_min_u32 v62, 1, v61\nQmaav 128,76,184\nQta 66,68,17664\nv_add_u32 v62, -1, v62\nQmaav 132,72,208\n"
"v_and_b32 v62, v62, v63\nv_xor_b32 v61, 1, v61\nQmaav 132,76,216\nv_min_u32 v61, 1, v61\nv_add_u32 v61, -1, v61\nQmaav 136,72,240\nQta 68,68,24832\nv_and_b32 v61, v61, v63\n"
"Qmaav 136,76,248\nv_accvgpr_write_b32 a202, v62\nv_accvgpr_write_b32 a203, v61\nQmaav 140,72,84\nv_accvgpr_write_b32 a200, 0\nv_accvgpr_write_b32 a201, 0\nQmaav 140,76,116\nQta 70,68,25856\n"
"v_accvgpr_write_b32 a204, 0\ns_waitcnt lgkmcnt(4)\nQmaaa 116,56,0\nv_accvgpr_write_b32 a205, 0\nQmaaa 116,60,28\nQmaaa 120,56,4\nQmaaa 120,60,32\nQmaaa 124,56,8\n"
"Qta 72,68,33024\nQmaaa 124,60,36\ns_nop 2\nds_write_b128 v58, a[116:119] offset:0\nQmaaa 128,56,12\nQmaaa 128,60,40\nQta 74,68,34048\nQmaaa 132,56,16\n"
"ds_write_b128 v58, a[120:123] offset:1024\nQmaaa 132,60,44\nQmaaa 136,56,20\nQta 76,68,41216\nQmaaa 136,60,48\nds_write_b128 v58, a[124:127] offset:2048\nQmaaa 140,56,24\nQmaaa 140,60,52\n"
"Qta 78,68,42240\ns_waitcnt lgkmcnt(7)\nQmaaa 144,64,80\nds_write_b128 v58, a[128:131] offset:3072\nQmaaa 144,68,108\nQmaaa 148,64,84\nQmaaa 148,68,112\nds_write_b128 v58, a[132:135] offset:4096\n"
"Qmaaa 152,64,88\nQta 56,68,49408\nQmaav 152,68,168\nQmaaa 156,64,92\nds_write_b128 v58, a[136:139] offset:5120\nQmaav 156,68,200\nQta 58,68,50432\nQmaaa 160,64,96\n"
"Qmaav 160,68,232\nds_write_b128 v58, a[140:143] offset:6144\nQmaaa 164,64,100\nQta 60,68,57600\nQmaav 164,68,212\nQmaaa 168,64,104\nQmaav 168,68,244\nQta 62,68,58624\n"
"s_waitcnt lgkmcnt(8)\nQmaav 144,72,80\nv_add_u32 v75, 1, v75\nQmaav 144,76,88\ns_waitcnt vmcnt(8)\nds_write_b32 v73, v75\nQmaav 148,72,112\nQmaav 148,76,120\n"
"Qmaav 152,72,144\nQta 64,68,16896\nQmaav 152,76,152\nQmaav 156,72,176\nQmaav 156,76,184\nQta 66,68,17920\nQmaav 160,72,208\nQmaav 160,76,216\n"
"ds_read_b32 v66, v74\nQmaav 164,72,240\nQta 68,68,25088\ns_waitcnt vmcnt(2)\nbuffer_store_dword v67, v77, s[24:27], 0 offen sc1\nQmaav 164,76,248\nQmaav 168,72,84\nQmaav 168,76,116\n"
"s_waitcnt lgkmcnt(1)\nv_cmp_gt_u32 vcc, v75, v66\ns_waitcnt lgkmcnt(5)\nQmaaa 144,56,0\ns_cbranch_vccnz .Lso2046r0\n.Lro2046r0:\nQmaaa 144,60,28\nQta 70,68,26112\n"
"Qmaaa 148,56,4\nQmaaa 148,60,32\nQdv 104,59,2048\nQmaaa 152,56,8\nQta 72,68,33280\nQmaaa 152,60,36\ns_nop 0\nds_write_b128 v58, a[144:147] offset:33792\n"
"Qmaaa 156,56,12\nQdv 136,59,3072\nQmaaa 156,60,40\nQta 74,68,34304\nQmaaa 160,56,16\nds_write_b128 v58, a[148:151] offset:34816\nQmaaa 160,60,44\nQdv 148,59,5120\n"
"Qmaaa 164,56,20\nQta 76,68,41472\nQmaaa 164,60,48\nds_write_b128 v58, a[152:155] offset:35840\nQmaaa 168,56,24\nQdv 180,59,0\ns_waitcnt lgkmcnt(9)\nQa 8,8,104\n"
"Qmaaa 168,60,52\nQta 78,68,42496\nQa 9,9,105\ns_waitcnt lgkmcnt(10)\nQmvaa 0,64,80\nds_write_b128 v58, a[156:159] offset:36864\nQmvaa 0,68,108\nQa 10,10,106\n"
"Qa 11,11,107\nQmvaa 4,64,84\ns_waitcnt lgkmcnt(8)\nQa 12,12,136\nQa 13,13,137\nQmvaa 4,68,112\nds_write_b128 v58, a[160:163] offset:37888\nQmvaa 8,64,88\n"
"Qta 56,68,49664\nQa 14,14,138\nQa 15,15,139\nQmvav 8,68,168\nQdv 104,59,4096\ns_waitcnt lgkmcnt(8)\nQa 20,20,148\nQmvaa 12,64,92\n"
"ds_write_b128 v58, a[164:167] offset:38912\nQmvav 12,68,200\nQta 58,68,50688\nQa 21,21,149\nQmvaa 16,64,96\nQa 22,22,150\nQa 23,23,151\nQmvav 16,68,232\n"
"Qdv 136,59,6144\ns_waitcnt lgkmcnt(8)\nQa 0,0,180\nQmvaa 20,64,100\nQta 60,68,57856\nQa 1,1,181\nQmvav 20,68,212\nds_write_b128 v58, a[168:171] offset:39936\n"
"Qmvaa 24,64,104\nQa 2,2,182\nQa 3,3,183\nQmvav 24,68,244\nQta 62,68,58880\ns_waitcnt lgkmcnt(6)\nQa 16,16,104\ns_waitcnt lgkmcnt(6)\n"
"Qmvav 0,72,80\nQdv 148,59,1024\nv_add_u32 v75, 1, v75\nQmvav 0,76,88\ns_waitcnt vmcnt(1)\nds_write_b32 v73, v75\nQmvav 4,72,112\nQa 17,17,105\n"
"Qa 18,18,106\nQmvav 4,76,120\nQa 19,19,107\ns_waitcnt lgkmcnt(5)\nQa 24,24,136\nQmvav 8,72,144\nQta 64,69,0\nQa 25,25,137\n"
"Qmvav 8,76,152\nQa 26,26,138\nQa 27,27,139\nQmvav 12,72,176\nQmvav 12,76,184\nQta 66,69,512\ns_waitcnt lgkmcnt(3)\nQa 4,4,148\n"
"Qmvav 16,72,208\nQa 5,5,149\nQa 6,6,150\nQmvav 16,76,216\nds_read_b32 v66, v74\nQa 7,7,151\nQmvav 20,72,240\nQta 68,69,1024\n"
"Qmvav 20,76,248\nQmvav 24,72,84\nQmvav 24,76,116\ns_waitcnt lgkmcnt(1)\nv_cmp_gt_u32 vcc, v75, v66\ns_waitcnt lgkmcnt(6)\nQmvaa 0,56,0\ns_cbranch_vccnz .Lso2088r0\n"
".Lro2088r0:\nQmvaa 0,60,28\nQta 70,69,1536\nQmvaa 4,56,4\nQmvaa 4,60,32\nQdv 180,59,33792\nQmvaa 8,56,8\nQta 72,69,8192\n"
"Qmvaa 8,60,36\nQmvaa 12,56,12\nQdv 104,59,34816\nQmvaa 12,60,40\nQta 74,69,8704\nQmvaa 16,56,16\nQmvaa 16,60,44\nQdv 136,59,35840\n"
"Qmvaa 20,56,20\nQta 76,69,9216\nQmvaa 20,60,48\ns_waitcnt lgkmcnt(5)\nQa 28,28,180\nQa 29,29,181\nQmvaa 24,56,24\nQdv 148,59,36864\n"
"Qa 30,30,182\nQmvaa 24,60,52\nQta 78,69,9728\nQa 31,31,183\ns_waitcnt lgkmcnt(11)\nQmaaa 172,64,80\ns_waitcnt lgkmcnt(5)\nQa 32,32,104\n"
"Qa 33,33,105\nQmaaa 172,68,108\nQa 34,34,106\nQa 35,35,107\nQmaaa 176,64,84\nQmaaa 176,68,112\ns_waitcnt lgkmcnt(3)\nQa 36,36,136\n"
"Qa 37,37,137\nQmaaa 180,64,88\nQta 56,69,16384\nQa 38,38,138\nQmaav 180,68,168\nQdv 180,59,37888\nQa 39,39,139\nQmaaa 184,64,92\n"
"s_waitcnt lgkmcnt(3)\nQa 40,40,148\nQa 41,41,149\nQmaav 184,68,200\nQta 58,69,16896\nQa 42,42,150\nQmaaa 188,64,96\nQdv 104,59,38912\n"
"Qa 43,43,151\nQmaav 188,68,232\nQmaaa 192,64,100\nQta 60,69,17408\nQmaav 192,68,212\nQdv 136,59,39936\nQmaaa 196,64,104\ns_waitcnt lgkmcnt(4)\n"
"Qa 44,44,180\nQa 45,45,181\nQmaav 196,68,244\nQta 62,69,17920\nQa 46,46,182\ns_waitcnt lgkmcnt(5)\nQmaav 172,72,80\nQa 47,47,183\n"
"Qmaav 172,76,88\nQmaav 176,72,112\ns_waitcnt lgkmcnt(3)\nQa 48,48,104\nQa 49,49,105\nQmaav 176,76,120\nQa 50,50,106\nQa 51,51,107\n"
"Qmaav 180,72,144\nQta 64,68,17152\nQmaav 180,76,152\ns_waitcnt lgkmcnt(2)\nQa 52,52,136\nQa 53,53,137\nQmaav 184,72,176\nQa 54,54,138\n"
"Qa 55,55,139\nQmaav 184,76,184\nQta 66,68,18176\n.Lpg19:\ns_cbranch_execz .Lpg23\nQmaav 188,72,208\nQmaav 188,76,216\nQmaav 192,72,240\n"
"Qta 68,68,25344\nQmaav 192,76,248\nQmaav 196,72,84\nQmaav 196,76,116\nQta 70,68,26368\ns_waitcnt lgkmcnt(4)\nQmaaa 172,56,0\nQmaaa 172,60,28\n"
"Qmaaa 176,56,4\nQmaaa 176,60,32\nQmaaa 180,56,8\nQta 72,68,33536\nQmaaa 180,60,36\ns_nop 2\nds_write_b128 v58, a[172:175] offset:0\nQmaaa 184,56,12\n"
"Qmaaa 184,60,40\nQta 74,68,34560\nQmaaa 188,56,16\nds_write_b128 v58, a[176:179] offset:1024\nQmaaa 188,60,44\nQmaaa 192,56,20\nQta 76,68,41728\nQmaaa 192,60,48\n"
"ds_write_b128 v58, a[180:183] offset:2048\nQmaaa 196,56,24\nQmaaa 196,60,52\nQta 78,68,42752\ns_waitcnt lgkmcnt(7)\nQmvaa 28,64,80\nds_write_b128 v58, a[184:187] offset:3072\nQmvaa 28,68,108\n"
"Qmvaa 32,64,84\nQmvaa 32,68,112\nds_write_b128 v58, a[188:191] offset:4096\nQmvaa 36,64,88\nQta 56,68,49920\nQmvav 36,68,168\nQmvaa 40,64,92\nds_write_b128 v58, a[192:195] offset:5120\n"
"Qmvav 40,68,200\nQta 58,68,50944\nQmvaa 44,64,96\nQmvav 44,68,232\nds_write_b128 v58, a[196:199] offset:6144\nQmvaa 48,64,100\nQta 60,68,58112\nQmvav 48,68,212\n"
"Qmvaa 52,64,104\nQmvav 52,68,244\nQta 62,68,59136\ns_waitcnt lgkmcnt(8)\nQmvav 28,72,80\nv_add_u32 v75, 1, v75\nQmvav 28,76,88\nds_write_b32 v73, v75\n"
"Qmvav 32,72,112\nQmvav 32,76,120\nQmvav 36,72,144\nQta 64,69,256\nQmvav 36,76,152\nQmvav 40,72,176\nQmvav 40,76,184\nQta 66,69,768\n"
"Qmvav 44,72,208\nQmvav 44,76,216\nds_read_b32 v66, v74\nQmvav 48,72,240\nQta 68,69,1280\nQmvav 48,76,248\nQmvav 52,72,84\nQmvav 52,76,116\n"
"s_waitcnt lgkmcnt(1)\nv_cmp_gt_u32 vcc, v75, v66\ns_waitcnt lgkmcnt(5)\nQmvaa 28,56,0\ns_cbranch_vccnz .Lso2172r0\n.Lro2172r0:\nQmvaa 28,60,28\nbuffer_load_dword v78, v76, s[24:27], 0 offen sc1\n"
"Qmvaa 32,56,4\nQta 70,69,1792\nQmvaa 32,60,32\nQdv 148,59,2048\nQmvaa 36,56,8\nQmvaa 36,60,36\nQta 72,69,8448\nQmvaa 40,56,12\n"
"Qdv 180,59,3072\nQmvaa 40,60,40\nQmvaa 44,56,16\nQta 74,69,8960\nQmvaa 44,60,44\nQdv 104,59,5120\nQmvaa 48,56,20\nQmvaa 48,60,48\n"
"Qta 76,69,9472\ns_waitcnt lgkmcnt(5)\nQa 156,156,148\nQmvaa 52,56,24\nQdv 136,59,0\nQa 157,157,149\nQmvaa 52,60,52\nQa 158,158,150\n"
"Qa 159,159,151\ns_waitcnt lgkmcnt(10)\nQmvaa 92,64,80\nQta 78,69,9984\ns_waitcnt lgkmcnt(5)\nQa 188,188,180\nQmvaa 92,68,108\nQa 189,189,181\n"
"Qa 190,190,182\nQmvaa 124,64,84\nQa 191,191,183\nQmvaa 124,68,112\ns_waitcnt lgkmcnt(3)\nQa 252,252,104\nQa 253,253,105\nQmvaa 156,64,88\n"
"Qta 56,69,16640\nQa 254,254,106\nQmvav 156,68,168\nQdv 148,59,4096\nQa 255,255,107\nQmvaa 188,64,92\ns_waitcnt lgkmcnt(3)\nQa 92,92,136\n"
"Qa 93,93,137\nQmvav 188,68,200\nQta 58,69,17152\nQa 94,94,138\nQmvaa 220,64,96\nQa 95,95,139\nQmvav 220,68,232\nQdv 180,59,6144\n"
"Qmvaa 252,64,100\nQta 60,69,17664\nQmvav 252,68,212\nQmvaa 100,64,104\nQmvav 100,68,244\nQta 62,69,18176\ns_waitcnt lgkmcnt(4)\nQa 220,220,148\n"
"s_waitcnt lgkmcnt(4)\nQmvav 92,72,80\nQdv 104,59,1024\nQa 221,221,149\nQmvav 92,76,88\nQa 222,222,150\nQa 223,223,151\nQmvav 124,72,112\n"
"Qmvav 124,76,120\ns_waitcnt vmcnt(0)\nv_sub_u32 v66, v78, v65\nv_cmp_gt_u32 vcc, 1, v66\ns_and_b64 vcc, vcc, s[28:29]\ns_cbranch_scc1 .Lx0_w\n.Lx0_r:\nQmvav 156,72,144\n"
"s_add_u32 s69, s98, 0\nbuffer_load_dwordx4 a[208:211], v79, s[24:27], s69 offen offset:0 sc1\nQmvav 156,76,152\ns_waitcnt lgkmcnt(3)\nQa 100,100,180\nQa 101,101,181\nQmvav 188,72,176\nbuffer_load_dwordx4 a[212:215], v79, s[24:27], s69 offen offset:1024 sc1\n"
"Qmvav 188,76,184\nQa 102,102,182\nQa 103,103,183\nQmvav 220,72,208\nbuffer_load_dwordx4 a[216:219], v79, s[24:27], s69 offen offset:2048 sc1\nv_add_u32 v75, 1, v75\nQmvav 220,76,216\nds_write_b32 v73, v75\n"
"s_waitcnt lgkmcnt(1)\nQa 124,124,104\nQmvav 252,72,240\nbuffer_load_dwordx4 a[220:223], v79, s[24:27], s69 offen offset:3072 sc1\nQmvav 252,76,248\nQa 125,125,105\nQa 126,126,106\nQmvav 100,72,84\n"
"s_add_u32 s69, s98, 4096\nbuffer_load_dwordx4 a[236:239], v79, s[24:27], s69 offen offset:3072 sc1\nQmvav 100,76,116\nQa 127,127,107\ns_waitcnt lgkmcnt(2)\nQmvaa 92,56,0\nbuffer_load_dwordx4 a[224:227], v79, s[24:27], s69 offen offset:0 sc1\nQmvaa 92,60,28\n"
"Qmvaa 124,56,4\ns_add_u32 s69, s98, 16384\nbuffer_load_dwordx4 a[240:243], v79, s[24:27], s69 offen offset:0 sc1\nQmvaa 124,60,32\nds_read_b32 v66, v74\nQmvaa 156,56,8\ns_add_u32 s69, s98, 4096\nbuffer_load_dwordx4 a[228:231], v79, s[24:27], s69 offen offset:1024 sc1\n"
"Qmvaa 156,60,36\nQmvaa 188,56,12\ns_add_u32 s69, s98, 16384\nbuffer_load_dwordx4 a[244:247], v79, s[24:27], s69 offen offset:1024 sc1\nQmvaa 188,60,40\ns_waitcnt lgkmcnt(0)\nv_cmp_gt_u32 vcc, v75, v66\nQmvaa 220,56,16\n"
"s_cbranch_vccnz .Lso2222r0\n.Lro2222r0:\nQmvaa 220,60,44\ns_add_u32 s69, s98, 4096\nbuffer_load_dwordx4 a[232:235], v79, s[24:27], s69 offen offset:2048 sc1\nQmvaa 252,56,20\nQmvaa 252,60,48\ns_add_u32 s69, s98, 16384\n"
"buffer_load_dwordx4 a[248:251], v79, s[24:27], s69 offen offset:2048 sc1\nQmvaa 100,56,24\nQmvaa 100,60,52\ns_waitcnt vmcnt(10)\nQmvaa 92,202,208\nQmvaa 124,200,208\ns_waitcnt vmcnt(9)\nQmvaa 156,202,212\n"
"Qmvaa 188,200,212\ns_nop 3\nQk 92,92,93\nQk 93,94,95\nds_write_b64 v56, v[92:93] offset:64\ns_waitcnt vmcnt(8)\nQmvaa 220,202,216\nQk 124,124,125\n"
"Qk 125,126,127\nds_write_b64 v56, v[124:125] offset:3392\nQmvaa 252,200,216\nQk 156,156,157\nQk 157,158,159\nds_write_b64 v56, v[156:157] offset:6720\ns_waitcnt vmcnt(7)\nQmvaa 100,202,220\n"
"Qk 188,188,189\nQk 189,190,191\nds_write_b64 v56, v[188:189] offset:10048\nQmvaa 0,200,220\nQk 220,220,221\nQk 221,222,223\nds_write_b64 v56, v[220:221] offset:13376\ns_waitcnt vmcnt(6)\n"
"Qmvaa 28,202,236\nQk 252,252,253\nQk 253,254,255\nds_write_b64 v56, v[252:253] offset:16704\ns_waitcnt vmcnt(5)\nQmvaa 4,202,224\nQk 100,100,101\nQk 101,102,103\n"
"ds_write_b64 v56, v[100:101] offset:20032\ns_waitcnt lgkmcnt(0)\ns_barrier\nQda 28,71,64\nQda 32,71,6720\nQda 36,71,13376\nQda 40,71,20032\nQmvaa 32,200,236\n"
"Qk 0,0,1\nQk 1,2,3\nds_write_b64 v56, v[0:1] offset:0\ns_mov_b32 s68, 0\ns_waitcnt lgkmcnt(4)\nbuffer_store_dwordx4 a[28:31], v72, s[40:43], s68 offen offset:0 sc1\nQmvaa 8,200,224\nQk 28,28,29\n"
"Qk 29,30,31\nds_write_b64 v56, v[28:29] offset:32\ns_mov_b32 s68, 81920\ns_waitcnt lgkmcnt(4)\nbuffer_store_dwordx4 a[32:35], v72, s[40:43], s68 offen offset:0 sc1\ns_waitcnt vmcnt(6)\nQmvaa 36,202,240\nQk 4,4,5\n"
"Qk 5,6,7\nds_write_b64 v56, v[4:5] offset:3328\ns_mov_b32 s68, 163840\ns_waitcnt lgkmcnt(4)\nbuffer_store_dwordx4 a[36:39], v72, s[40:43], s68 offen offset:0 sc1\ns_waitcnt vmcnt(6)\nQmvaa 12,202,228\nQk 32,32,33\n"
"Qk 33,34,35\nds_write_b64 v56, v[32:33] offset:3360\ns_mov_b32 s68, 245760\ns_cmp_eq_u32 s93, 1\ns_cselect_b32 s68, 0x7ffff000, s68\ns_waitcnt lgkmcnt(4)\nbuffer_store_dwordx4 a[40:43], v72, s[40:43], s68 offen offset:0 sc1\nQmvaa 40,200,240\n"
"Qk 8,8,9\nQk 9,10,11\nds_write_b64 v56, v[8:9] offset:6656\nQmvaa 16,200,228\nQk 36,36,37\nQk 37,38,39\nds_write_b64 v56, v[36:37] offset:6688\ns_waitcnt vmcnt(6)\n"
"Qmvaa 44,202,244\nQk 12,12,13\nQk 13,14,15\nds_write_b64 v56, v[12:13] offset:9984\ns_waitcnt vmcnt(5)\nQmvaa 20,202,232\nQk 40,40,41\nQk 41,42,43\n"
"ds_write_b64 v56, v[40:41] offset:10016\ns_waitcnt lgkmcnt(0)\ns_barrier\nQda 0,57,0\nQda 4,57,3328\nQda 8,57,6656\nQda 12,57,9984\nQmvaa 48,200,244\n"
"Qk 16,16,17\nQk 17,18,19\nds_write_b64 v56, v[16:17] offset:13312\ns_mov_b32 s68, 0\ns_waitcnt lgkmcnt(4)\nbuffer_store_dwordx4 a[0:3], v64, s[40:43], s68 offen offset:0 sc1\nQmvaa 24,200,232\nQk 44,44,45\n"
"Qk 45,46,47\nds_write_b64 v56, v[44:45] offset:13344\ns_mov_b32 s68, 40960\ns_waitcnt lgkmcnt(4)\nbuffer_store_dwordx4 a[4:7], v64, s[40:43], s68 offen offset:0 sc1\ns_waitcnt vmcnt(6)\nQmvaa 52,202,248\nQk 20,20,21\n"
"Qk 21,22,23\nds_write_b64 v56, v[20:21] offset:16640\ns_mov_b32 s68, 81920\ns_waitcnt lgkmcnt(4)\nbuffer_store_dwordx4 a[8:11], v64, s[40:43], s68 offen offset:0 sc1\nQk 48,48,49\nQk 49,50,51\nds_write_b64 v56, v[48:49] offset:16672\n"
"s_mov_b32 s68, 122880\ns_waitcnt lgkmcnt(4)\nbuffer_store_dwordx4 a[12:15], v64, s[40:43], s68 offen offset:0 sc1\nQk 24,24,25\nQk 25,26,27\nds_write_b64 v56, v[24:25] offset:19968\nQk 52,52,53\nQk 53,54,55\n"
"ds_write_b64 v56, v[52:53] offset:20000\ns_waitcnt lgkmcnt(0)\ns_barrier\nQda 16,57,13312\nQda 20,57,16640\nQda 24,57,19968\ns_mov_b32 s68, 163840\ns_waitcnt lgkmcnt(2)\n"
"buffer_store_dwordx4 a[16:19], v64, s[40:43], s68 offen offset:0 sc1\ns_mov_b32 s68, 204800\ns_waitcnt lgkmcnt(1)\nbuffer_store_dwordx4 a[20:23], v64, s[40:43], s68 offen offset:0 sc1\ns_mov_b32 s68, 245760\ns_waitcnt lgkmcnt(0)\nbuffer_store_dwordx4 a[24:27], v64, s[40:43], s68 offen offset:0 sc1\ns_branch .Lfin\n"
".Lrole1:\n.Lpg20:\ns_cbranch_execz .Lpg24\nQmvaa 96,64,88\nQta 56,69,49408\ns_lshl_b32 s46, s19, 4\nQmvav 96,68,168\nbuffer_load_dwordx4 a[84:87], v79, s[48:51], s31 offen offset:1024 sc1\n"
"Qmvaa 128,64,92\nds_read_b32 v66, v74\ns_add_u32 s46, s46, 128\nQmvav 128,68,200\nQta 58,69,50432\nv_mov_b32 v77, 0x7ffff000\nQmvaa 160,64,96\nQmvav 160,68,232\n"
"buffer_load_dwordx4 a[88:91], v79, s[48:51], s31 offen offset:2048 sc1\nQmvaa 192,64,100\ns_waitcnt lgkmcnt(1)\nv_cmp_gt_u32 vcc, v75, v66\nQmvav 192,68,212\ns_cbranch_vccnz .Lso1525r1\n.Lro1525r1:\nQmvaa 224,64,104\n"
"Qta 60,69,57600\nQmvav 224,68,244\nbuffer_load_dwordx4 a[92:95], v79, s[48:51], s31 offen offset:3072 sc1\ns_waitcnt vmcnt(31)\ns_waitcnt lgkmcnt(4)\nQmaav 140,72,84\nQmaav 140,76,116\nQta 62,69,58624\n"
"Qmaav 136,72,240\nQmaav 136,76,248\nQmaav 132,72,208\nQta 64,69,49664\ns_add_u32 m0, s96, 65536\ns_add_u32 s95, s67, 86016\nQmaav 132,76,216\nbuffer_load_dwordx4 v62, s[52:55], s95 offen lds\n"
"Qmaav 128,72,176\nQmaav 128,76,184\nQta 66,69,50688\nQmaav 124,72,144\nQmaav 124,76,152\nQmaav 120,72,112\nQta 68,69,57856\ns_add_u32 s95, s67, 125952\n"
"Qmaav 120,76,120\nbuffer_load_dwordx4 v62, s[52:55], s95 offen offset:1024 lds\nQmaav 116,72,80\nQmaav 116,76,88\nQta 70,69,58880\ns_waitcnt lgkmcnt(4)\nQmaav 144,56,80\nQmaav 144,60,88\n"
"s_add_u32 m0, s96, 73728\ns_add_u32 s95, s72, 86016\nQmaav 148,56,112\nbuffer_load_dwordx4 v62, s[52:55], s95 offen lds\nQmaav 148,60,120\nQmaav 152,56,144\nQta 72,69,49920\nQmaav 152,60,152\n"
"Qmaav 156,56,176\nQmaav 156,60,184\nQta 74,69,50944\ns_add_u32 s95, s72, 125952\nQmaav 160,56,208\nbuffer_load_dwordx4 v62, s[52:55], s95 offen offset:1024 lds\nQmaav 160,60,216\nQmaav 164,56,240\n"
"Qta 76,69,58112\nQmaav 164,60,248\nQmaav 168,56,84\nQmaav 168,60,116\nQta 78,69,59136\ns_add_u32 m0, s96, 81920\ns_add_u32 s95, s73, 86016\ns_waitcnt lgkmcnt(4)\n"
"Qmvav 24,64,84\nbuffer_load_dwordx4 v62, s[52:55], s95 offen lds\nQmvav 24,68,116\nQmvav 20,64,240\nQmvav 20,68,248\nQmvav 16,64,208\nQta 56,68,0\ns_add_u32 s95, s73, 125952\n"
"Qmvav 16,68,216\nbuffer_load_dwordx4 v62, s[52:55], s95 offen offset:1024 lds\nQmvav 12,64,176\nQmvav 12,68,184\nQta 58,68,512\nQmvav 8,64,144\nQmvav 8,68,152\nQmvav 4,64,112\n"
"Qta 60,68,1024\ns_add_u32 m0, s96, 90112\ns_add_u32 s95, s67, 86016\nQmvav 4,68,120\nbuffer_load_dwordx4 v61, s[52:55], s95 offen lds\nQmvav 0,64,80\nQmvav 0,68,88\nQta 62,68,1536\n"
"s_waitcnt lgkmcnt(4)\nQmvav 28,72,80\nQmvav 28,76,88\ns_add_u32 s95, s67, 105472\nQmvav 32,72,112\nbuffer_load_dwordx4 v61, s[52:55], s95 offen offset:1024 lds\nQmvav 32,76,120\nQmvav 36,72,144\n"
"Qta 64,68,256\nQmvav 36,76,152\ns_add_u32 s31, s74, 135168\nQmvav 40,72,176\nbuffer_load_dwordx4 a[96:99], v79, s[48:51], s31 offen offset:0 sc1\nQmvav 40,76,184\nQta 66,68,768\nQmvav 44,72,208\n"
"v_cmp_eq_u32 s[44:45], s46, v60\ns_nop 1\nv_cndmask_b32 v77, v77, v76, s[44:45]\nv_add_u32 v67, 1, v65\ns_xor_b32 s46, s19, 12\ns_lshl_b32 s28, 1, s46\nQmvav 44,76,216\nQmvav 48,72,240\n"
"Qta 68,68,1280\nQmvav 48,76,248\nbuffer_load_dwordx4 a[100:103], v79, s[48:51], s31 offen offset:1024 sc1\nv_add_u32 v75, 1, v75\nQmvav 52,72,84\ns_waitcnt vmcnt(35)\nds_write_b32 v73, v75\nQmvav 52,76,116\n"
"Qta 70,68,1792\ns_waitcnt lgkmcnt(5)\nQmaav 196,56,84\nQmaav 196,60,116\nbuffer_load_dwordx4 a[104:107], v79, s[48:51], s31 offen offset:2048 sc1\nQmaav 192,56,240\nQmaav 192,60,248\nQmaav 188,56,208\n"
"Qta 72,68,8192\ns_add_u32 s31, s74, 147456\nQmaav 188,60,216\nbuffer_load_dwordx4 a[108:111], v79, s[48:51], s31 offen offset:0 sc1\nQmaav 184,56,176\nds_read_b32 v66, v74\nQmaav 184,60,184\nQta 74,68,9216\n"
"Qmaav 180,56,144\nQmaav 180,60,152\nbuffer_load_dwordx4 a[112:115], v79, s[48:51], s31 offen offset:1024 sc1\nQmaav 176,56,112\ns_waitcnt lgkmcnt(1)\nv_cmp_gt_u32 vcc, v75, v66\nQmaav 176,60,120\ns_cbranch_vccnz .Lso1595r1\n"
".Lro1595r1:\nQmaav 172,56,80\nQta 76,68,16384\nQmaav 172,60,88\nbuffer_load_dwordx4 v[168:171], v79, s[48:51], s31 offen offset:2048 sc1\ns_waitcnt lgkmcnt(4)\nQmvav 92,64,80\nQmvav 92,68,88\n"
"Qta 78,68,17408\nQmvav 124,64,112\nQmvav 124,68,120\nbuffer_load_dwordx4 v[200:203], v79, s[48:51], s31 offen offset:3072 sc1\nQmvav 156,64,144\nQta 56,68,8448\nQmvav 156,68,152\nQmvav 188,64,176\n"
"Qmvav 188,68,184\nQta 58,68,9472\ns_add_u32 s31, s74, 151552\nQmvav 220,64,208\nbuffer_load_dwordx4 v[232:235], v79, s[48:51], s31 offen offset:0 sc1\nQmvav 220,68,216\nQmvav 252,64,240\nQta 60,68,16640\n"
"Qmvav 252,68,248\nQmvav 100,64,84\nbuffer_load_dwordx4 v[212:215], v79, s[48:51], s31 offen offset:1024 sc1\nQmvav 100,68,116\nQta 62,68,17664\ns_waitcnt lgkmcnt(4)\nQmaav 224,72,84\nQmaav 224,76,116\n"
"Qmaav 220,72,240\nbuffer_load_dwordx4 v[244:247], v79, s[48:51], s31 offen offset:2048 sc1\nQmaav 220,76,248\nQmaav 216,72,208\nQta 64,68,8704\nQmaav 216,76,216\ns_add_u32 m0, s91, 0\ns_add_u32 s31, s77, 131072\n"
"Qmaav 212,72,176\nbuffer_load_dwordx4 v79, s[48:51], s31 offen sc1 lds\nQmaav 212,76,184\nQta 66,68,9728\n.Lpg21:\ns_cbranch_execz .Ltr8\nQmaav 208,72,144\nQmaav 208,76,152\n"
"Qmaav 204,72,112\nQta 68,68,16896\nQmaav 204,76,120\nbuffer_load_dwordx4 v79, s[48:51], s31 offen offset:1024 sc1 lds\nQmaav 200,72,80\nQmaav 200,76,88\nQta 70,68,17920\ns_waitcnt lgkmcnt(4)\n"
"Qmaav 228,56,80\nQmaav 228,60,88\nbuffer_load_dwordx4 v79, s[48:51], s31 offen offset:2048 sc1 lds\nQmaav 232,56,112\nQmaav 232,60,120\nQmaav 236,56,144\nQta 72,68,8960\nQmaav 236,60,152\n"
"buffer_load_dwordx4 v79, s[48:51], s31 offen offset:3072 sc1 lds\nQmaav 240,56,176\nQmaav 240,60,184\nQta 74,68,9984\nQmaav 244,56,208\ns_add_u32 m0, s91, 4096\ns_add_u32 s31, s77, 135168\nQmaav 244,60,216\n"
"buffer_load_dwordx4 v79, s[48:51], s31 offen sc1 lds\nQmaav 248,56,240\nQta 76,68,17152\nQmaav 248,60,248\nQmaav 252,56,84\nQmaav 252,60,116\nQta 78,68,18176\ns_waitcnt lgkmcnt(4)\n"
"Qmvav 172,64,84\nbuffer_load_dwordx4 v79, s[48:51], s31 offen offset:1024 sc1 lds\nQmvav 172,68,116\nQmvav 140,64,240\nQmvav 140,68,248\nQmvav 108,64,208\nQta 56,68,24576\nQmvav 108,68,216\n"
"buffer_load_dwordx4 v79, s[48:51], s31 offen offset:2048 sc1 lds\nQmvav 228,64,176\nQmvav 228,68,184\nQta 58,68,25600\nQmvav 196,64,144\ns_add_u32 m0, s91, 33792\ns_add_u32 s31, s77, 147456\nQmvav 196,68,152\n"
"buffer_load_dwordx4 v79, s[48:51], s31 offen sc1 lds\nQmvav 164,64,112\nQta 60,68,32768\nQmvav 164,68,120\nv_add_u32 v75, 1, v75\nQmvav 132,64,80\ns_waitcnt vmcnt(47)\nds_write_b32 v73, v75\n"
"Qmvav 132,68,88\nQta 62,68,33792\ns_waitcnt lgkmcnt(5)\nQmvav 204,72,80\nbuffer_load_dwordx4 v79, s[48:51], s31 offen offset:1024 sc1 lds\nQmvav 204,76,88\nQmvav 236,72,112\nQmvav 236,76,120\n"
"Qmvav 96,72,144\nQta 64,68,24832\nQmvav 96,76,152\nbuffer_load_dwordx4 v79, s[48:51], s31 offen offset:2048 sc1 lds\nQmvav 128,72,176\nds_read_b32 v66, v74\nQmvav 128,76,184\nQta 66,68,25856\n"
"Qmvav 160,72,208\nQmvav 160,76,216\nbuffer_load_dwordx4 v79, s[48:51], s31 offen offset:3072 sc1 lds\nQmvav 192,72,240\ns_waitcnt lgkmcnt(1)\nv_cmp_gt_u32 vcc, v75, v66\nQmvav 192,76,248\ns_cbranch_vccnz .Lso1665r1\n"
".Lro1665r1:\nQmvav 224,72,84\nQta 68,68,33024\ns_add_u32 m0, s91, 37888\ns_add_u32 s31, s77, 151552\nQmvav 224,76,116\nbuffer_load_dwordx4 v79, s[48:51], s31 offen sc1 lds\ns_waitcnt vmcnt(36)\n"
"s_waitcnt lgkmcnt(4)\nQmaaa 140,56,24\nQmaaa 140,60,52\nQta 70,68,34048\nQmaaa 136,56,20\nQmaaa 136,60,48\nbuffer_load_dwordx4 v79, s[48:51], s31 offen offset:1024 sc1 lds\nQmaaa 132,56,16\n"
"Qta 72,68,25088\nQmaaa 132,60,44\nQmaaa 128,56,12\nQmaaa 128,60,40\nQta 74,68,26112\nQmaaa 124,56,8\nbuffer_load_dwordx4 v79, s[48:51], s31 offen offset:2048 sc1 lds\nQmaaa 124,60,36\n"
"Qmaaa 120,56,4\nQta 76,68,33280\nQmaaa 120,60,32\nQmaaa 116,56,0\nQmaaa 116,60,28\nQta 78,68,34304\ns_add_u32 m0, s96, 0\ns_add_u32 s95, s67, 126976\n"
"s_waitcnt lgkmcnt(4)\nQmaaa 144,64,0\nbuffer_load_dwordx4 v61, s[52:55], s95 offen lds\nQmaaa 144,68,28\nQmaaa 148,64,4\nQmaaa 148,68,32\nQmaaa 152,64,8\nQta 56,68,25344\n"
"s_add_u32 s95, s67, 146432\nQmaaa 152,68,36\nbuffer_load_dwordx4 v61, s[52:55], s95 offen offset:1024 lds\nQmaaa 156,64,12\nQmaaa 156,68,40\nQta 58,68,26368\nQmaaa 160,64,16\ns_add_u32 s31, s75, 131072\n"
"Qmaaa 160,68,44\nbuffer_load_dwordx4 v[80:83], v79, s[48:51], s31 offen offset:0 sc1\nQmaaa 164,64,20\nQta 60,68,33536\nQmaaa 164,68,48\nQmaaa 168,64,24\nQmaaa 168,68,52\nQta 62,68,34560\n"
"s_waitcnt lgkmcnt(4)\nQmvaa 24,72,24\nbuffer_load_dwordx4 v[112:115], v79, s[48:51], s31 offen offset:1024 sc1\nQmvaa 24,76,52\nQmvaa 20,72,20\nQmvaa 20,76,48\nQmvaa 16,72,16\nQta 64,68,40960\n"
"Qmvaa 16,76,44\nbuffer_load_dwordx4 v[144:147], v79, s[48:51], s31 offen offset:2048 sc1\nQmvaa 12,72,12\nQmvaa 12,76,40\nQta 66,68,41472\nQmvaa 8,72,8\nQmvaa 8,76,36\nbuffer_load_dwordx4 v[176:179], v79, s[48:51], s31 offen offset:3072 sc1\n"
"Qmvaa 4,72,4\nQta 68,68,41984\nQmvaa 4,76,32\nQmvaa 0,72,0\nQmvaa 0,76,28\nQta 70,68,42496\ns_add_u32 s31, s75, 135168\ns_waitcnt lgkmcnt(4)\n"
"Qmvaa 28,56,0\nbuffer_load_dwordx4 v[208:211], v79, s[48:51], s31 offen offset:0 sc1\nQmvaa 28,60,28\nQmvaa 32,56,4\nQmvaa 32,60,32\nQmvaa 36,56,8\nQta 72,68,41216\nQmvaa 36,60,36\n"
"buffer_load_dwordx4 v[240:243], v79, s[48:51], s31 offen offset:1024 sc1\nQmvaa 40,56,12\nQmvaa 40,60,40\nQta 74,68,41728\nQmvaa 44,56,16\nQmvaa 44,60,44\nbuffer_load_dwordx4 v[84:87], v79, s[48:51], s31 offen offset:2048 sc1\nQmvaa 48,56,20\n"
"Qta 76,68,42240\nQmvaa 48,60,48\nv_add_u32 v75, 1, v75\nQmvaa 52,56,24\ns_waitcnt vmcnt(37)\nds_write_b32 v73, v75\nQmvaa 52,60,52\nQta 78,68,42752\n"
"s_add_u32 s31, s75, 147456\ns_waitcnt lgkmcnt(5)\nQmaaa 196,64,24\nbuffer_load_dwordx4 v[88:91], v79, s[48:51], s31 offen offset:0 sc1\nQmaaa 196,68,52\nQmaaa 192,64,20\nQmaaa 192,68,48\nQmaaa 188,64,16\n"
"Qta 56,68,49152\nQmaaa 188,68,44\nbuffer_load_dwordx4 v[120:123], v79, s[48:51], s31 offen offset:1024 sc1\nQmaaa 184,64,12\nds_read_b32 v66, v74\nQmaaa 184,68,40\nQta 58,68,50176\nQmaaa 180,64,8\n"
"Qmaaa 180,68,36\nbuffer_load_dwordx4 v[152:155], v79, s[48:51], s31 offen offset:2048 sc1\nQmaaa 176,64,4\ns_waitcnt lgkmcnt(1)\nv_cmp_gt_u32 vcc, v75, v66\nQmaaa 176,68,32\ns_cbranch_vccnz .Lso1735r1\n.Lro1735r1:\n"
"Qmaaa 172,64,0\nQta 60,68,57344\nQmaaa 172,68,28\nbuffer_load_dwordx4 v[184:187], v79, s[48:51], s31 offen offset:3072 sc1\ns_waitcnt lgkmcnt(4)\nQmvaa 92,72,0\nQmvaa 92,76,28\nQta 62,68,58368\n"
"Qmvaa 124,72,4\ns_add_u32 s31, s75, 151552\nQmvaa 124,76,32\nbuffer_load_dwordx4 v[216:219], v79, s[48:51], s31 offen offset:0 sc1\nQmvaa 156,72,8\nQta 64,68,49408\nQmvaa 156,76,36\nQmvaa 188,72,12\n"
"Qmvaa 188,76,40\nQta 66,68,50432\nQmvaa 220,72,16\nbuffer_load_dwordx4 v[248:251], v79, s[48:51], s31 offen offset:1024 sc1\nQmvaa 220,76,44\nQmvaa 252,72,20\nQta 68,68,57600\nQmvaa 252,76,48\n"
"Qmvaa 100,72,24\nbuffer_load_dwordx4 v[116:119], v79, s[48:51], s31 offen offset:2048 sc1\nQmvaa 100,76,52\nQta 70,68,58624\ns_waitcnt lgkmcnt(4)\nQmaaa 224,56,24\nQmaaa 224,60,52\nQmaaa 220,56,20\n"
"s_add_u32 m0, s96, 8192\ns_add_u32 s95, s72, 86016\nQmaaa 220,60,48\nbuffer_load_dwordx4 v61, s[52:55], s95 offen lds\nQmaaa 216,56,16\nQta 72,68,49664\nQmaaa 216,60,44\nQmaaa 212,56,12\n"
"Qmaaa 212,60,40\nQta 74,68,50688\ns_add_u32 s95, s72, 105472\nQmaaa 208,56,8\nbuffer_load_dwordx4 v61, s[52:55], s95 offen offset:1024 lds\nQmaaa 208,60,36\nQmaaa 204,56,4\nQta 76,68,57856\n"
"Qmaaa 204,60,32\nQmaaa 200,56,0\nQmaaa 200,60,28\nQta 78,68,58880\ns_add_u32 m0, s96, 16384\ns_add_u32 s95, s72, 126976\ns_waitcnt lgkmcnt(4)\nQmaaa 228,64,0\n"
"buffer_load_dwordx4 v61, s[52:55], s95 offen lds\nQmaaa 228,68,28\nQmaaa 232,64,4\nQmaaa 232,68,32\nQmaaa 236,64,8\nQta 56,68,49920\ns_add_u32 s95, s72, 146432\nQmaaa 236,68,36\n"
"buffer_load_dwordx4 v61, s[52:55], s95 offen offset:1024 lds\nQmaaa 240,64,12\nQmaaa 240,68,40\nQta 58,68,50944\nQmaaa 244,64,16\nQmaaa 244,68,44\nQmaaa 248,64,20\nQta 60,68,58112\n"
"s_add_u32 m0, s96, 24576\ns_add_u32 s95, s73, 86016\nQmaaa 248,68,48\nbuffer_load_dwordx4 v61, s[52:55], s95 offen lds\nQmaaa 252,64,24\nQmaaa 252,68,52\nQta 62,68,59136\ns_waitcnt lgkmcnt(4)\n"
"Qmvaa 172,72,24\nQmvaa 172,76,52\ns_add_u32 s95, s73, 105472\nQmvaa 140,72,20\nbuffer_load_dwordx4 v61, s[52:55], s95 offen offset:1024 lds\nQmvaa 140,76,48\nQmvaa 108,72,16\nQta 64,69,0\n"
"Qmvaa 108,76,44\nQmvaa 228,72,12\nQmvaa 228,76,40\nQta 66,69,512\ns_add_u32 m0, s96, 32768\ns_add_u32 s95, s73, 126976\nQmvaa 196,72,8\nbuffer_load_dwordx4 v61, s[52:55], s95 offen lds\n"
"Qmvaa 196,76,36\nQmvaa 164,72,4\nQta 68,69,1024\nQmvaa 164,76,32\nv_add_u32 v75, 1, v75\nQmvaa 132,72,0\ns_waitcnt vmcnt(21)\nds_write_b32 v73, v75\n"
"Qmvaa 132,76,28\nQta 70,69,1536\ns_add_u32 s95, s73, 146432\ns_waitcnt lgkmcnt(5)\nQmvaa 204,56,0\nbuffer_load_dwordx4 v61, s[52:55], s95 offen offset:1024 lds\nQmvaa 204,60,28\ns_waitcnt vmcnt(24)\n"
"Qda 0,58,0\nQmvaa 236,56,4\nQmvaa 236,60,32\nQda 28,58,33792\nQmvaa 96,56,8\nQta 72,69,8192\nQmvaa 96,60,36\nQda 4,58,1024\n"
"Qmvaa 128,56,12\nds_read_b32 v66, v74\nQmvaa 128,60,40\nQta 74,69,8704\nQmvaa 160,56,16\nQda 32,58,34816\nQmvaa 160,60,44\nQmvaa 192,56,20\n"
"s_waitcnt lgkmcnt(2)\nv_cmp_gt_u32 vcc, v75, v66\nQmvaa 192,60,48\ns_cbranch_vccnz .Lso1805r1\n.Lro1805r1:\nQmvaa 224,56,24\nQta 76,69,9216\ns_add_u32 m0, s96, 40960\n"
"s_add_u32 s95, s67, 86016\nQmvaa 224,60,52\nbuffer_load_dwordx4 v63, s[52:55], s95 offen lds\ns_waitcnt vmcnt(39)\ns_waitcnt lgkmcnt(8)\nQmaaa 172,64,80\nQda 8,58,2048\nQmaaa 172,68,108\n"
"Qta 78,69,9728\nQmaaa 176,64,84\nQda 36,58,35840\nQmaaa 176,68,112\nQmaaa 180,64,88\nQta 56,69,16384\ns_add_u32 s95, s67, 105472\nQmaav 180,68,168\n"
"buffer_load_dwordx4 v63, s[52:55], s95 offen offset:1024 lds\nQmaaa 184,64,92\nQda 12,58,3072\nQmaav 184,68,200\nQta 58,69,16896\nQmaaa 188,64,96\nQda 40,58,36864\nQmaav 188,68,232\n"
"Qmaaa 192,64,100\nQta 60,69,17408\ns_add_u32 m0, s96, 49152\ns_add_u32 s95, s67, 126976\nQmaav 192,68,212\nbuffer_load_dwordx4 v63, s[52:55], s95 offen lds\nQmaaa 196,64,104\nQda 16,58,4096\n"
"Qmaav 196,68,244\nQta 62,69,17920\ns_waitcnt vmcnt(11)\ns_waitcnt lgkmcnt(8)\nQmaav 172,72,80\nQda 44,58,37888\nQmaav 172,76,88\ns_add_u32 s95, s67, 146432\n"
"Qmaav 176,72,112\nbuffer_load_dwordx4 v63, s[52:55], s95 offen offset:1024 lds\nQmaav 176,76,120\nQda 20,58,5120\nQmaav 180,72,144\nQta 64,69,24576\nv_add_u32 v75, 1, v75\nQmaav 180,76,152\n"
"s_waitcnt vmcnt(4)\nds_write_b32 v73, v75\nQmaav 184,72,176\nQda 48,58,38912\nQmaav 184,76,184\nQta 66,69,25600\ns_add_u32 m0, s96, 57344\ns_add_u32 s95, s72, 86016\n"
"Qmaav 188,72,208\nbuffer_load_dwordx4 v63, s[52:55], s95 offen lds\nQmaav 188,76,216\nQda 24,58,6144\nQmaav 192,72,240\nQta 68,68,0\nQmaav 192,76,248\nQda 52,58,39936\n"
"Qmaav 196,72,84\nQmaav 196,76,116\nds_read_b32 v66, v74\ns_waitcnt lgkmcnt(10)\nQmaaa 172,56,0\nQta 70,68,1024\ns_add_u32 s95, s72, 105472\nQmaaa 172,60,28\n"
"buffer_load_dwordx4 v63, s[52:55], s95 offen offset:1024 lds\nQmaaa 176,56,4\nQmaaa 176,60,32\ns_waitcnt lgkmcnt(1)\nv_cmp_gt_u32 vcc, v75, v66\ns_waitcnt lgkmcnt(11)\nQmaaa 180,56,8\ns_cbranch_vccnz .Lso1840r1\n"
".Lro1840r1:\nQmaaa 180,60,36\nQta 72,68,8192\nQmaaa 184,56,12\nds_write_b128 v58, a[172:175] offset:0\nQmaaa 184,60,40\ns_waitcnt lgkmcnt(4)\nQmaaa 188,56,16\n"
"Qta 74,68,9216\nQmaaa 188,60,44\nds_write_b128 v58, a[176:179] offset:1024\nQmaaa 192,56,20\nQmaaa 192,60,48\nQta 76,68,16384\nQmaaa 196,56,24\nds_write_b128 v58, a[180:183] offset:2048\n"
"Qmaaa 196,60,52\ns_waitcnt lgkmcnt(6)\nQmaaa 116,64,80\nQta 78,68,17408\nQmaaa 116,68,108\nds_write_b128 v58, a[184:187] offset:3072\nQmaaa 120,64,84\nQmaaa 120,68,112\n"
"Qmaaa 124,64,88\nQta 56,68,24576\nQmaav 124,68,168\nds_write_b128 v58, a[188:191] offset:4096\nQmaaa 128,64,92\nQmaav 128,68,200\nQta 58,68,25600\nQmaaa 132,64,96\n"
"ds_write_b128 v58, a[192:195] offset:5120\nQmaav 132,68,232\nQmaaa 136,64,100\nQta 60,68,32768\nQmaav 136,68,212\nds_write_b128 v58, a[196:199] offset:6144\nQmaaa 140,64,104\nQmaav 140,68,244\n"
"Qta 62,68,33792\ns_waitcnt lgkmcnt(8)\nQmaav 116,72,80\nv_add_u32 v75, 1, v75\nQmaav 116,76,88\nds_write_b32 v73, v75\nQmaav 120,72,112\nQmaav 120,76,120\n"
"Qmaav 124,72,144\nQta 64,69,256\nQmaav 124,76,152\nQmaav 128,72,176\nQmaav 128,76,184\nQta 66,69,768\nQmaav 132,72,208\nQmaav 132,76,216\n"
"ds_read_b32 v66, v74\nQmaav 136,72,240\nQta 68,69,1280\nQmaav 136,76,248\nQmaav 140,72,84\nQmaav 140,76,116\ns_waitcnt lgkmcnt(1)\nv_cmp_gt_u32 vcc, v75, v66\n"
"s_waitcnt lgkmcnt(5)\nQmaaa 116,56,0\ns_cbranch_vccnz .Lso1878r1\n.Lro1878r1:\nQmaaa 116,60,28\nQta 70,69,1792\nQmaaa 120,56,4\nQmaaa 120,60,32\n"
"Qdv 148,59,2048\nQmaaa 124,56,8\nQta 72,69,8448\nQmaaa 124,60,36\ns_nop 0\nds_write_b128 v58, a[116:119] offset:33792\nQmaaa 128,56,12\nQdv 180,59,3072\n"
"Qmaaa 128,60,40\nQta 74,69,8960\nQmaaa 132,56,16\nds_write_b128 v58, a[120:123] offset:34816\nQmaaa 132,60,44\nQdv 104,59,5120\nQmaaa 136,56,20\nQta 76,69,9472\n"
"Qmaaa 136,60,48\nds_write_b128 v58, a[124:127] offset:35840\nQmaaa 140,56,24\nQdv 136,59,0\ns_waitcnt lgkmcnt(9)\nQa 156,156,148\nQmaaa 140,60,52\nQta 78,69,9984\n"
"Qa 157,157,149\ns_waitcnt lgkmcnt(10)\nQmvaa 92,64,80\nds_write_b128 v58, a[128:131] offset:36864\nQmvaa 92,68,108\nQa 158,158,150\nQa 159,159,151\nQmvaa 124,64,84\n"
"s_waitcnt lgkmcnt(8)\nQa 188,188,180\nQa 189,189,181\nQmvaa 124,68,112\nds_write_b128 v58, a[132:135] offset:37888\nQmvaa 156,64,88\nQta 56,69,16640\nQa 190,190,182\n"
"Qa 191,191,183\nQmvav 156,68,168\nQdv 148,59,4096\ns_waitcnt lgkmcnt(8)\nQa 252,252,104\nQmvaa 188,64,92\nds_write_b128 v58, a[136:139] offset:38912\nQmvav 188,68,200\n"
"Qta 58,69,17152\nQa 253,253,105\nQmvaa 220,64,96\n.Lpg22:\ns_cbranch_execz .Ltr8\nQa 254,254,106\nQa 255,255,107\nQmvav 220,68,232\n"
"Qdv 180,59,6144\ns_waitcnt lgkmcnt(8)\nQa 92,92,136\nQmvaa 252,64,100\nQta 60,69,17664\nQa 93,93,137\nQmvav 252,68,212\nds_write_b128 v58, a[140:143] offset:39936\n"
"Qmvaa 100,64,104\nQa 94,94,138\nQa 95,95,139\nQmvav 100,68,244\nQta 62,69,18176\ns_waitcnt lgkmcnt(6)\nQa 220,220,148\ns_waitcnt lgkmcnt(6)\n"
"Qmvav 92,72,80\nQdv 104,59,1024\nv_add_u32 v75, 1, v75\nQmvav 92,76,88\nds_write_b32 v73, v75\nQa 221,221,149\nQmvav 124,72,112\nQa 222,222,150\n"
"Qa 223,223,151\nQmvav 124,76,120\ns_waitcnt lgkmcnt(5)\nQa 100,100,180\nQa 101,101,181\nQmvav 156,72,144\nQta 64,69,24832\nQa 102,102,182\n"
"Qmvav 156,76,152\nQa 103,103,183\nQmvav 188,72,176\nQmvav 188,76,184\nQta 66,69,25856\ns_waitcnt lgkmcnt(3)\nQa 124,124,104\nQmvav 220,72,208\n"
"Qa 125,125,105\nQa 126,126,106\nQmvav 220,76,216\nds_read_b32 v66, v74\nQa 127,127,107\nQmvav 252,72,240\nQta 68,68,256\nQmvav 252,76,248\n"
"Qmvav 100,72,84\nQmvav 100,76,116\ns_waitcnt lgkmcnt(1)\nv_cmp_gt_u32 vcc, v75, v66\ns_waitcnt lgkmcnt(6)\nQmvaa 92,56,0\ns_cbranch_vccnz .Lso1920r1\n.Lro1920r1:\n"
"Qmvaa 92,60,28\nQta 70,68,1280\ns_add_u32 m0, s96, 65536\ns_add_u32 s95, s72, 126976\nQmvaa 124,56,4\nbuffer_load_dwordx4 v63, s[52:55], s95 offen lds\nQmvaa 124,60,32\nQdv 136,59,33792\n"
"s_add_u32 s68, s64, 0\nQmvaa 156,56,8\nQta 72,68,8448\nQk 92,92,93\nQmvaa 156,60,36\nQk 93,94,95\nQmvaa 188,56,12\nQdv 148,59,34816\n"
"Qk 94,124,125\nQmvaa 188,60,40\nQta 74,68,9472\ns_add_u32 s95, s72, 146432\nQmvaa 220,56,16\nbuffer_load_dwordx4 v63, s[52:55], s95 offen offset:1024 lds\nQmvaa 220,60,44\nQdv 180,59,35840\n"
"Qk 95,126,127\nQmvaa 252,56,20\nQta 76,68,16640\nbuffer_store_dwordx4 v[92:95], v79, s[24:27], s68 offen offset:0 sc1\nQmvaa 252,60,48\nv_add_u32 v75, 1, v75\ns_waitcnt lgkmcnt(5)\nQa 0,0,136\n"
"Qmvaa 100,56,24\nds_write_b32 v73, v75\nQa 1,1,137\nQmvaa 100,60,52\nQta 78,68,17664\ns_add_u32 m0, s96, 73728\ns_add_u32 s95, s73, 86016\ns_waitcnt lgkmcnt(7)\n"
"Qmaaa 144,64,80\nbuffer_load_dwordx4 v63, s[52:55], s95 offen lds\nQmaaa 144,68,108\nQdv 104,59,36864\nQa 2,2,138\nQmaaa 148,64,84\nQa 3,3,139\ns_waitcnt lgkmcnt(6)\n"
"Qa 4,4,148\nQmaaa 148,68,112\nQa 5,5,149\nQa 6,6,150\nQmaaa 152,64,88\nQta 56,68,24832\ns_add_u32 s95, s73, 105472\nQmaav 152,68,168\n"
"buffer_load_dwordx4 v63, s[52:55], s95 offen offset:1024 lds\nQmaaa 156,64,92\nds_read_b32 v66, v74\nQa 7,7,151\nQmaav 156,68,200\nQta 58,68,25856\ns_waitcnt lgkmcnt(7)\nQa 8,8,180\n"
"Qmaaa 160,64,96\nQdv 136,59,37888\nQa 9,9,181\nQmaav 160,68,232\nQa 10,10,182\nQa 11,11,183\nQmaaa 164,64,100\ns_waitcnt lgkmcnt(2)\n"
"v_cmp_gt_u32 vcc, v75, v66\nQmaav 164,68,212\ns_cbranch_vccnz .Lso1945r1\n.Lro1945r1:\nQmaaa 168,64,104\nQta 60,68,33024\ns_add_u32 m0, s96, 81920\ns_add_u32 s95, s73, 126976\n"
"s_waitcnt lgkmcnt(5)\nQa 12,12,104\nQa 13,13,105\nQa 14,14,106\nQa 15,15,107\nQmaav 168,68,244\nbuffer_load_dwordx4 v63, s[52:55], s95 offen lds\ns_waitcnt lgkmcnt(10)\n"
"Qmaav 144,72,80\nQdv 148,59,38912\ns_waitcnt lgkmcnt(2)\nQa 16,16,136\nQmaav 144,76,88\nQta 62,68,34048\nQa 17,17,137\nQmaav 148,72,112\n"
"Qa 18,18,138\nQa 19,19,139\nQmaav 148,76,120\nQdv 180,59,39936\nQk 156,156,157\nQmaav 152,72,144\nQta 64,69,25088\ns_add_u32 s95, s73, 146432\n"
"Qmaav 152,76,152\nbuffer_load_dwordx4 v63, s[52:55], s95 offen offset:1024 lds\nQmaav 156,72,176\ns_waitcnt lgkmcnt(3)\nQa 20,20,148\nQa 21,21,149\nQmaav 156,76,184\nQta 66,69,26112\n"
"Qa 22,22,150\nQmaav 160,72,208\nQa 23,23,151\nv_bfe_u32 v61, v60, 6, 2\nQmaav 160,76,216\nv_lshrrev_b32 v62, 8, v60\nv_xor_b32 v61, v61, v62\nQmaav 164,72,240\n"
"Qta 68,68,512\nv_bfe_u32 v62, v60, 5, 1\nQmaav 164,76,248\ns_waitcnt lgkmcnt(3)\nQa 24,24,180\nQa 25,25,181\nQmaav 168,72,84\nQa 26,26,182\n"
"Qa 27,27,183\nQmaav 168,76,116\nQta 70,68,1536\nv_lshl_or_b32 v61, v61, 1, v62\ns_waitcnt lgkmcnt(5)\nQmaaa 144,56,0\nv_and_b32 v62, 16, v60\nv_mov_b32 v63, 0x3f80\n"
"Qmaaa 144,60,28\nv_lshlrev_b32 v63, v62, v63\nv_min_u32 v62, 1, v61\nQmaaa 148,56,4\nv_add_u32 v62, -1, v62\nv_and_b32 v62, v62, v63\nQmaaa 148,60,32\nv_xor_b32 v61, 1, v61\n"
"v_min_u32 v61, 1, v61\nQmaaa 152,56,8\nQta 72,68,8704\nv_add_u32 v61, -1, v61\nQmaaa 152,60,36\nds_write_b128 v58, a[144:147] offset:0\nQmaaa 156,56,12\nv_and_b32 v61, v61, v63\n"
"v_accvgpr_write_b32 a174, v62\nQmaaa 156,60,40\nQta 74,68,9728\nv_accvgpr_write_b32 a175, v61\nQmaaa 160,56,16\nds_write_b128 v58, a[148:151] offset:1024\nQmaaa 160,60,44\nv_accvgpr_write_b32 a172, 0\n"
"v_accvgpr_write_b32 a173, 0\nQmaaa 164,56,20\nQta 76,68,16896\nv_accvgpr_write_b32 a176, 0\nQmaaa 164,60,48\nds_write_b128 v58, a[152:155] offset:2048\nQmaaa 168,56,24\nv_accvgpr_write_b32 a177, 0\n"
"Qk 157,158,159\nQmaaa 168,60,52\nQta 78,68,17920\nQk 158,188,189\ns_waitcnt lgkmcnt(7)\nQmvaa 0,64,80\nds_write_b128 v58, a[156:159] offset:3072\nQmvaa 0,68,108\n"
"Qk 159,190,191\nQk 220,220,221\nQmvaa 4,64,84\nbuffer_store_dwordx4 v[156:159], v79, s[24:27], s68 offen offset:1024 sc1\nQk 221,222,223\nQmvaa 4,68,112\nds_write_b128 v58, a[160:163] offset:4096\nQmvaa 8,64,88\n"
"Qta 56,68,25088\nQk 222,252,253\nQmvav 8,68,168\nQk 223,254,255\nQk 100,100,101\nQmvaa 12,64,92\nds_write_b128 v58, a[164:167] offset:5120\nQmvav 12,68,200\n"
"Qta 58,68,26112\nbuffer_store_dwordx4 v[220:223], v79, s[24:27], s68 offen offset:2048 sc1\nQmvaa 16,64,96\nQk 101,102,103\nQmvav 16,68,232\nds_write_b128 v58, a[168:171] offset:6144\nQmvaa 20,64,100\nQta 60,68,33280\n"
"Qmvav 20,68,212\nQmvaa 24,64,104\nQmvav 24,68,244\nQta 62,68,34304\ns_waitcnt lgkmcnt(8)\nQmvav 0,72,80\nv_add_u32 v75, 1, v75\nQmvav 0,76,88\n"
"s_waitcnt vmcnt(11)\nds_write_b32 v73, v75\nQmvav 4,72,112\nQmvav 4,76,120\nQmvav 8,72,144\nQta 64,69,25344\nQmvav 8,76,152\nQmvav 12,72,176\n"
"Qmvav 12,76,184\nQta 66,69,26368\nQmvav 16,72,208\nQmvav 16,76,216\nds_read_b32 v66, v74\nQmvav 20,72,240\nQta 68,68,768\nQmvav 20,76,248\n"
"Qmvav 24,72,84\nQmvav 24,76,116\ns_waitcnt lgkmcnt(1)\nv_cmp_gt_u32 vcc, v75, v66\ns_waitcnt lgkmcnt(5)\nQmvaa 0,56,0\ns_cbranch_vccnz .Lso2004r1\n.Lro2004r1:\n"
"Qmvaa 0,60,28\nQta 70,68,1792\nQmvaa 4,56,4\nQmvaa 4,60,32\nQdv 104,59,2048\nQmvaa 8,56,8\nQta 72,68,8960\ns_nop 1\n"
"Qk 102,0,1\nQmvaa 8,60,36\nQk 103,2,3\nQmvaa 12,56,12\nQdv 136,59,3072\nbuffer_store_dwordx4 v[100:103], v79, s[24:27], s68 offen offset:3072 sc1\nQmvaa 12,60,40\nQta 74,68,9984\n"
"Qk 4,4,5\nQmvaa 16,56,16\nQk 5,6,7\nQk 6,8,9\nQmvaa 16,60,44\nQdv 148,59,5120\nQk 7,10,11\nQmvaa 20,56,20\n"
"Qta 76,68,17152\ns_add_u32 s68, s64, 4096\nQmvaa 20,60,48\ns_waitcnt lgkmcnt(5)\nQa 36,36,104\nQa 37,37,105\nQmvaa 24,56,24\nQdv 180,59,0\n"
"Qa 38,38,106\nQmvaa 24,60,52\nQta 78,68,18176\nQa 39,39,107\ns_waitcnt lgkmcnt(11)\nQmvaa 28,64,80\ns_waitcnt lgkmcnt(5)\nQa 40,40,136\n"
"Qa 41,41,137\nQmvaa 28,68,108\nQa 42,42,138\nQa 43,43,139\nQmvaa 32,64,84\nbuffer_store_dwordx4 v[4:7], v79, s[24:27], s68 offen offset:0 sc1\nQk 12,12,13\nQmvaa 32,68,112\n"
"s_waitcnt lgkmcnt(3)\nQa 48,48,148\nQa 49,49,149\nQmvaa 36,64,88\nQta 56,68,25344\nQa 50,50,150\nQmvav 36,68,168\nQdv 104,59,4096\n"
"Qa 51,51,151\nQmvaa 40,64,92\ns_waitcnt lgkmcnt(3)\nQa 28,28,180\nQa 29,29,181\nQmvav 40,68,200\nQta 58,68,26368\nQa 30,30,182\n"
"Qmvaa 44,64,96\nQa 31,31,183\nQk 13,14,15\nQmvav 44,68,232\nQdv 136,59,6144\nQk 14,16,17\nQmvaa 48,64,100\nQta 60,68,33536\n"
"Qk 15,18,19\nQmvav 48,68,212\nbuffer_store_dwordx4 v[12:15], v79, s[24:27], s68 offen offset:1024 sc1\nQk 20,20,21\nQmvaa 52,64,104\nQk 21,22,23\nQk 22,24,25\nQmvav 52,68,244\n"
"Qta 62,68,34560\nv_add_u32 v75, 1, v75\ns_waitcnt lgkmcnt(6)\nQmvav 28,72,80\ns_waitcnt vmcnt(5)\nds_write_b32 v73, v75\nQmvav 28,76,88\nQdv 148,59,1024\n"
"s_waitcnt lgkmcnt(6)\nQa 44,44,104\nQmvav 32,72,112\nQa 45,45,105\nQa 46,46,106\nQmvav 32,76,120\nQa 47,47,107\ns_waitcnt lgkmcnt(4)\n"
"Qa 52,52,136\nQmvav 36,72,144\nQta 64,68,40960\nQa 53,53,137\nQmvav 36,76,152\nQa 54,54,138\nQa 55,55,139\nQmvav 40,72,176\n"
"Qk 23,26,27\nQmvav 40,76,184\nQta 66,68,41984\ns_waitcnt lgkmcnt(2)\nQa 32,32,148\nQmvav 44,72,208\nds_read_b32 v66, v74\nQa 33,33,149\n"
"Qmvav 44,76,216\nQa 34,34,150\nQa 35,35,151\nQmvav 48,72,240\nQta 68,68,49152\nbuffer_store_dwordx4 v[20:23], v79, s[24:27], s68 offen offset:2048 sc1\nQmvav 48,76,248\nQmvav 52,72,84\n"
"s_waitcnt lgkmcnt(1)\nv_cmp_gt_u32 vcc, v75, v66\nQmvav 52,76,116\ns_cbranch_vccnz .Lso2045r1\n.Lro2045r1:\ns_waitcnt lgkmcnt(6)\nQmvaa 28,56,0\nQta 70,68,50176\n"
"Qmvaa 28,60,28\nQmvaa 32,56,4\nQmvaa 32,60,32\nQmvaa 36,56,8\nQta 72,68,57344\ns_nop 3\nQk 28,28,29\nQmvaa 36,60,36\n"
"Qk 29,30,31\nQmvaa 40,56,12\nQk 30,32,33\nQk 31,34,35\nQmvaa 40,60,40\nQta 74,68,58368\nbuffer_store_dwordx4 v[28:31], v79, s[24:27], s68 offen offset:3072 sc1\nQmvaa 44,56,16\n"
"Qk 36,36,37\nQk 37,38,39\nQmvaa 44,60,44\ns_add_u32 s68, s64, 16384\nQmvaa 48,56,20\nQta 76,69,0\nQk 38,40,41\nQmvaa 48,60,48\n"
"Qk 39,42,43\nQmvaa 52,56,24\nbuffer_store_dwordx4 v[36:39], v79, s[24:27], s68 offen offset:0 sc1\nQk 44,44,45\nQmvaa 52,60,52\nQta 78,69,1024\nQk 45,46,47\ns_waitcnt lgkmcnt(4)\n"
"Qmaaa 200,64,80\nQk 46,48,49\nQk 47,50,51\nQmaaa 200,68,108\nbuffer_store_dwordx4 v[44:47], v79, s[24:27], s68 offen offset:1024 sc1\nQmaaa 204,64,84\nQk 52,52,53\nQk 53,54,55\n"
"Qmaaa 204,68,112\nv_mov_b32 v54, 0\nv_mov_b32 v55, 0\nQmaaa 208,64,88\nQta 56,69,8192\nbuffer_store_dwordx4 v[52:55], v79, s[24:27], s68 offen offset:2048 sc1\nQmaav 208,68,168\nQmaaa 212,64,92\n"
"Qmaav 212,68,200\nQta 58,69,9216\nQmaaa 216,64,96\nQmaav 216,68,232\nQmaaa 220,64,100\nQta 60,69,16384\nQmaav 220,68,212\nQmaaa 224,64,104\n"
"Qmaav 224,68,244\nQta 62,69,17408\ns_waitcnt lgkmcnt(4)\nQmaav 200,72,80\nQmaav 200,76,88\nQmaav 204,72,112\nQmaav 204,76,120\nQmaav 208,72,144\n"
"Qta 64,68,41216\nQmaav 208,76,152\nQmaav 212,72,176\nQmaav 212,76,184\nQta 66,68,42240\nQmaav 216,72,208\nQmaav 216,76,216\nQmaav 220,72,240\n"
"Qta 68,68,49408\nQmaav 220,76,248\nQmaav 224,72,84\nQmaav 224,76,116\nQta 70,68,50432\ns_waitcnt lgkmcnt(4)\nQmaaa 200,56,0\nQmaaa 200,60,28\n"
"s_waitcnt vmcnt(0)\nbuffer_store_dword v67, v77, s[24:27], 0 offen sc1\nQmaaa 204,56,4\nQmaaa 204,60,32\nQmaaa 208,56,8\nQta 72,68,57600\nQmaaa 208,60,36\ns_nop 0\n"
"ds_write_b128 v58, a[200:203] offset:33792\nQmaaa 212,56,12\nQmaaa 212,60,40\nQta 74,68,58624\nQmaaa 216,56,16\nds_write_b128 v58, a[204:207] offset:34816\nQmaaa 216,60,44\nQmaaa 220,56,20\n"
"Qta 76,69,256\nQmaaa 220,60,48\nds_write_b128 v58, a[208:211] offset:35840\nQmaaa 224,56,24\nQmaaa 224,60,52\nQta 78,69,1280\ns_waitcnt lgkmcnt(7)\nQmaaa 228,64,80\n"
"ds_write_b128 v58, a[212:215] offset:36864\nQmaaa 228,68,108\nQmaaa 232,64,84\nQmaaa 232,68,112\nds_write_b128 v58, a[216:219] offset:37888\nQmaaa 236,64,88\nQta 56,69,8448\nQmaav 236,68,168\n"
"Qmaaa 240,64,92\nds_write_b128 v58, a[220:223] offset:38912\nQmaav 240,68,200\nQta 58,69,9472\nQmaaa 244,64,96\nQmaav 244,68,232\nds_write_b128 v58, a[224:227] offset:39936\nQmaaa 248,64,100\n"
"Qta 60,69,16640\nQmaav 248,68,212\nQmaaa 252,64,104\nQmaav 252,68,244\nQta 62,69,17664\ns_waitcnt lgkmcnt(8)\nQmaav 228,72,80\nv_add_u32 v75, 1, v75\n"
"Qmaav 228,76,88\nds_write_b32 v73, v75\nQmaav 232,72,112\nQmaav 232,76,120\nQmaav 236,72,144\nQta 64,68,41472\nQmaav 236,76,152\nQmaav 240,72,176\n"
"Qmaav 240,76,184\nQta 66,68,42496\nQmaav 244,72,208\nQmaav 244,76,216\nds_read_b32 v66, v74\nQmaav 248,72,240\nQta 68,68,49664\nQmaav 248,76,248\n"
"Qmaav 252,72,84\nbuffer_load_dword v78, v76, s[24:27], 0 offen sc1\nQmaav 252,76,116\ns_waitcnt lgkmcnt(1)\nv_cmp_gt_u32 vcc, v75, v66\ns_waitcnt lgkmcnt(5)\nQmaaa 228,56,0\ns_cbranch_vccnz .Lso2130r1\n"
".Lro2130r1:\nQmaaa 228,60,28\nQta 70,68,50688\nQmaaa 232,56,4\nQmaaa 232,60,32\nQdv 180,59,35840\nQmaaa 236,56,8\nQta 72,68,57856\n"
"Qmaaa 236,60,36\ns_nop 0\nds_write_b128 v58, a[228:231] offset:0\nQmaaa 240,56,12\nQdv 104,59,36864\nQmaaa 240,60,40\nQta 74,68,58880\nQmaaa 244,56,16\n"
"ds_write_b128 v58, a[232:235] offset:1024\nQmaaa 244,60,44\nQdv 136,59,38912\nQmaaa 248,56,20\n.Lpg23:\ns_cbranch_execz .Ltr8\nQta 76,69,512\nQmaaa 248,60,48\n"
"ds_write_b128 v58, a[236:239] offset:2048\nQmaaa 252,56,24\nQdv 148,59,33792\ns_waitcnt lgkmcnt(9)\nQa 196,196,180\nQmaaa 252,60,52\nQta 78,69,1536\nQa 197,197,181\n"
"s_waitcnt lgkmcnt(10)\nQmvaa 132,64,80\nds_write_b128 v58, a[240:243] offset:3072\nQmvaa 132,68,108\nQa 198,198,182\nQa 199,199,183\nQmvaa 164,64,84\ns_waitcnt lgkmcnt(8)\n"
"Qa 228,228,104\nQa 229,229,105\nQmvaa 164,68,112\nds_write_b128 v58, a[244:247] offset:4096\nQmvaa 196,64,88\nQta 56,69,8704\nQa 230,230,106\nQa 231,231,107\n"
"Qmvav 196,68,168\nQdv 180,59,37888\ns_waitcnt lgkmcnt(8)\nQa 140,140,136\nQmvaa 228,64,92\nds_write_b128 v58, a[248:251] offset:5120\nQmvav 228,68,200\nQta 58,69,9728\n"
"Qa 141,141,137\nQmvaa 108,64,96\nQa 142,142,138\nQa 143,143,139\nQmvav 108,68,232\nQdv 104,59,39936\ns_waitcnt lgkmcnt(8)\nQa 132,132,148\n"
"Qmvaa 140,64,100\nQta 60,69,16896\nQa 133,133,149\nQmvav 140,68,212\nds_write_b128 v58, a[252:255] offset:6144\nQmvaa 172,64,104\nQa 134,134,150\nQa 135,135,151\n"
"Qmvav 172,68,244\nQta 62,69,17920\ns_waitcnt lgkmcnt(6)\nQa 108,108,180\ns_waitcnt lgkmcnt(6)\nQmvav 132,72,80\nQdv 136,59,34816\nv_add_u32 v75, 1, v75\n"
"Qmvav 132,76,88\nds_write_b32 v73, v75\nQa 109,109,181\nQmvav 164,72,112\nQa 110,110,182\nQa 111,111,183\nQmvav 164,76,120\ns_waitcnt lgkmcnt(5)\n"
"Qa 172,172,104\nQa 173,173,105\nQmvav 196,72,144\nQta 64,68,41728\nQa 174,174,106\nQmvav 196,76,152\nQa 175,175,107\nQmvav 228,72,176\n"
"Qmvav 228,76,184\nQta 66,68,42752\ns_waitcnt lgkmcnt(3)\nQa 164,164,136\nQmvav 108,72,208\nQa 165,165,137\nQa 166,166,138\nQmvav 108,76,216\n"
"ds_read_b32 v66, v74\nQa 167,167,139\nQmvav 140,72,240\nQta 68,68,49920\nQmvav 140,76,248\nQmvav 172,72,84\nQmvav 172,76,116\ns_waitcnt lgkmcnt(1)\n"
"v_cmp_gt_u32 vcc, v75, v66\ns_waitcnt lgkmcnt(6)\nQmvaa 132,56,0\ns_cbranch_vccnz .Lso2172r1\n.Lro2172r1:\nQmvaa 132,60,28\ns_waitcnt vmcnt(0)\nv_sub_u32 v66, v78, v65\n"
"v_cmp_gt_u32 vcc, 1, v66\ns_and_b64 vcc, vcc, s[28:29]\ns_cbranch_scc1 .Lx1_w\n.Lx1_r:\nQmvaa 164,56,4\ns_add_u32 s69, s98, 0\nbuffer_load_dwordx4 a[180:183], v79, s[24:27], s69 offen offset:0 sc1\nQmvaa 164,60,32\n"
"Qta 70,68,50944\nQmvaa 196,56,8\nbuffer_load_dwordx4 a[192:195], v79, s[24:27], s69 offen offset:3072 sc1\nQmvaa 196,60,36\nQdv 148,59,2048\nQmvaa 228,56,12\ns_add_u32 s69, s98, 4096\nbuffer_load_dwordx4 a[196:199], v79, s[24:27], s69 offen offset:0 sc1\n"
"Qmvaa 228,60,40\nQta 72,68,58112\nQmvaa 108,56,16\ns_add_u32 s69, s98, 0\nbuffer_load_dwordx4 a[184:187], v79, s[24:27], s69 offen offset:1024 sc1\nQmvaa 108,60,44\nQdv 180,59,4096\nQmvaa 140,56,20\n"
"s_add_u32 s69, s98, 4096\nbuffer_load_dwordx4 a[116:119], v79, s[24:27], s69 offen offset:1024 sc1\nQmvaa 140,60,48\nQta 74,68,59136\ns_waitcnt lgkmcnt(3)\nQa 96,96,148\nQmvaa 172,56,24\ns_add_u32 s69, s98, 0\n"
"buffer_load_dwordx4 a[188:191], v79, s[24:27], s69 offen offset:2048 sc1\nQmvaa 172,60,52\nQdv 104,59,0\nQa 97,97,149\ns_waitcnt lgkmcnt(4)\nQmvaa 204,64,80\ns_add_u32 s69, s98, 4096\nbuffer_load_dwordx4 a[120:123], v79, s[24:27], s69 offen offset:2048 sc1\n"
"Qmvaa 204,68,108\nQta 76,69,768\nQa 98,98,150\nQmvaa 236,64,84\nQdv 136,59,1024\nQa 99,99,151\nQmvaa 236,68,112\ns_waitcnt lgkmcnt(4)\n"
"Qa 160,160,180\nQa 161,161,181\nQmvaa 96,64,88\nQta 78,69,1792\nQa 162,162,182\nQmvav 96,68,168\nQdv 148,59,3072\nQa 163,163,183\n"
"Qmvaa 128,64,92\ns_waitcnt lgkmcnt(4)\nQa 204,204,104\nQa 205,205,105\nQmvav 128,68,200\nQta 56,69,8960\nQa 206,206,106\nQmvaa 160,64,96\n"
"Qdv 180,59,5120\nQa 207,207,107\nQmvav 160,68,232\nQmvaa 192,64,100\nQta 58,69,9984\ns_waitcnt lgkmcnt(5)\nQa 236,236,136\nQmvav 192,68,212\n"
"Qa 237,237,137\nQa 238,238,138\nQmvaa 224,64,104\nQa 239,239,139\nQmvav 224,68,244\nQta 60,69,17152\ns_waitcnt lgkmcnt(4)\nQa 128,128,148\n"
"s_waitcnt lgkmcnt(4)\nQmvav 204,72,80\nQa 129,129,149\nQa 130,130,150\nQmvav 204,76,88\nQa 131,131,151\nQmvav 236,72,112\nQta 62,69,18176\n"
"s_waitcnt lgkmcnt(3)\nQa 192,192,180\nQmvav 236,76,120\nQa 193,193,181\nQa 194,194,182\nQmvav 96,72,144\nQa 195,195,183\nQmvav 96,76,152\n"
"Qmvav 128,72,176\nQmvav 128,76,184\nQmvav 160,72,208\nQmvav 160,76,216\nQdv 104,59,6144\nQmvav 192,72,240\nQmvav 192,76,248\nv_add_u32 v75, 1, v75\n"
"Qmvav 224,72,84\nds_write_b32 v73, v75\nQmvav 224,76,116\ns_waitcnt lgkmcnt(2)\nQmvaa 204,56,0\nQmvaa 204,60,28\nQmvaa 236,56,4\nQmvaa 236,60,32\n"
"s_waitcnt lgkmcnt(1)\ns_nop 1\nQa 224,224,104\nQa 225,225,105\nQmvaa 96,56,8\nQa 226,226,106\nQa 227,227,107\nQmvaa 96,60,36\n"
"Qmvaa 128,56,12\nds_read_b32 v66, v74\nQmvaa 128,60,40\nQmvaa 160,56,16\nQmvaa 160,60,44\nQmvaa 192,56,20\ns_waitcnt lgkmcnt(0)\nv_cmp_gt_u32 vcc, v75, v66\n"
"Qmvaa 192,60,48\ns_cbranch_vccnz .Lso2225r1\n.Lro2225r1:\nQmvaa 224,56,24\nQmvaa 224,60,52\ns_waitcnt vmcnt(6)\nQmvaa 132,174,180\ns_waitcnt vmcnt(5)\n"
"Qmvaa 204,172,192\nQmvaa 164,172,180\ns_waitcnt vmcnt(4)\nQmvaa 236,174,196\ns_nop 2\nQk 132,132,133\nQk 133,134,135\nds_write_b64 v56, v[132:133] offset:0\n"
"s_waitcnt vmcnt(3)\nQmvaa 196,174,184\nQk 204,204,205\nQk 205,206,207\nds_write_b64 v56, v[204:205] offset:32\nQmvaa 96,172,196\nQk 164,164,165\nQk 165,166,167\n"
"ds_write_b64 v56, v[164:165] offset:3328\nQmvaa 228,172,184\nQk 236,236,237\nQk 237,238,239\nds_write_b64 v56, v[236:237] offset:3360\ns_waitcnt lgkmcnt(0)\ns_barrier\nQda 0,57,0\n"
"Qda 4,57,3328\ns_waitcnt vmcnt(2)\nQmvaa 128,174,116\nQk 196,196,197\nQk 197,198,199\nds_write_b64 v56, v[196:197] offset:6656\ns_mov_b32 s68, 0\ns_waitcnt lgkmcnt(2)\n"
"buffer_store_dwordx4 a[0:3], v64, s[40:43], s68 offen offset:0 sc1\ns_waitcnt vmcnt(2)\nQmvaa 108,174,188\nQk 96,96,97\nQk 97,98,99\nds_write_b64 v56, v[96:97] offset:6688\ns_mov_b32 s68, 40960\ns_waitcnt lgkmcnt(2)\n"
"buffer_store_dwordx4 a[4:7], v64, s[40:43], s68 offen offset:0 sc1\nQmvaa 160,172,116\nQk 228,228,229\nQk 229,230,231\nds_write_b64 v56, v[228:229] offset:9984\nQmvaa 140,172,188\nQk 128,128,129\nQk 129,130,131\n"
"ds_write_b64 v56, v[128:129] offset:10016\ns_waitcnt vmcnt(2)\nQmvaa 192,174,120\nQk 108,108,109\nQk 109,110,111\nds_write_b64 v56, v[108:109] offset:13312\nQmvaa 172,174,192\nQk 160,160,161\n"
"Qk 161,162,163\nds_write_b64 v56, v[160:161] offset:13344\ns_waitcnt lgkmcnt(0)\ns_barrier\nQda 8,57,6656\nQda 12,57,9984\nQda 16,57,13312\nQmvaa 224,172,120\n"
"Qk 140,140,141\nQk 141,142,143\nds_write_b64 v56, v[140:141] offset:16640\ns_mov_b32 s68, 81920\ns_waitcnt lgkmcnt(3)\nbuffer_store_dwordx4 a[8:11], v64, s[40:43], s68 offen offset:0 sc1\nQk 192,192,193\nQk 193,194,195\n"
"ds_write_b64 v56, v[192:193] offset:16672\ns_mov_b32 s68, 122880\ns_waitcnt lgkmcnt(3)\nbuffer_store_dwordx4 a[12:15], v64, s[40:43], s68 offen offset:0 sc1\nQk 172,172,173\nQk 173,174,175\nds_write_b64 v56, v[172:173] offset:19968\ns_mov_b32 s68, 163840\n"
"s_waitcnt lgkmcnt(3)\nbuffer_store_dwordx4 a[16:19], v64, s[40:43], s68 offen offset:0 sc1\nQk 224,224,225\nQk 225,226,227\nds_write_b64 v56, v[224:225] offset:20000\ns_waitcnt lgkmcnt(0)\ns_barrier\nQda 20,57,16640\n"
"Qda 24,57,19968\ns_mov_b32 s68, 204800\ns_waitcnt lgkmcnt(1)\nbuffer_store_dwordx4 a[20:23], v64, s[40:43], s68 offen offset:0 sc1\ns_mov_b32 s68, 245760\ns_waitcnt lgkmcnt(0)\nbuffer_store_dwordx4 a[24:27], v64, s[40:43], s68 offen offset:0 sc1\n.Lfin:\n"
".Lpg24:\ns_cbranch_execz .Ltr8\ns_waitcnt vmcnt(0) lgkmcnt(0)\ns_branch .Lexit\n.Lend:\n.Lfb:\ns_waitcnt vmcnt(0) lgkmcnt(0)\nv_lshrrev_b32 v52, 4, v60\n"
"v_and_b32 v53, 15, v52\nv_lshrrev_b32 v52, 4, v52\nv_lshlrev_b32 v66, 8, v53\nv_lshl_add_u32 v66, v52, 4, v66\nv_add_u32 v66, 1024, v66\nv_lshlrev_b32 v67, 10, v52\nv_lshl_add_u32 v67, v53, 1, v67\nv_add_u32 v67, 1024, v67\n"
"v_mov_b32 v52, 0x3f803f80\nv_accvgpr_write_b32 a204, v52\nv_accvgpr_write_b32 a205, v52\nv_accvgpr_write_b32 a206, v52\nv_accvgpr_write_b32 a207, v52\nv_mov_b32 v24, 0xff800000\nv_mov_b32 v28, 0xff800000\nv_mov_b32 v32, 0xff800000\n"
"v_mov_b32 v36, 0xff800000\nv_mov_b32 v40, 0xff800000\nv_mov_b32 v44, 0xff800000\nv_mov_b32 v48, 0xff800000\ns_lshl_b32 s89, s67, 21\ns_lshl_b32 s68, s93, 18\ns_add_u32 s89, s89, s68\ns_mov_b32 s66, 0\n"
".Lfb1:\ns_add_u32 s68, s89, 4096\nbuffer_load_dwordx4 a[112:115], v66, s[40:43], s89 offen offset:0\nbuffer_load_dwordx4 a[116:119], v66, s[40:43], s89 offen offset:64\nbuffer_load_dwordx4 a[120:123], v66, s[40:43], s89 offen offset:128\nbuffer_load_dwordx4 a[124:127], v66, s[40:43], s89 offen offset:192\nbuffer_load_dwordx4 a[128:131], v66, s[40:43], s68 offen offset:0\nbuffer_load_dwordx4 a[132:135], v66, s[40:43], s68 offen offset:64\n"
"buffer_load_dwordx4 a[136:139], v66, s[40:43], s68 offen offset:128\nbuffer_load_dwordx4 a[140:143], v66, s[40:43], s68 offen offset:192\ns_waitcnt vmcnt(0)\nQzvaa 0,112,0\nQmvaa 0,116,4\nQmvaa 0,120,8\nQmvaa 0,124,12\nQzvaa 4,128,0\n"
"Qmvaa 4,132,4\nQmvaa 4,136,8\nQmvaa 4,140,12\ns_nop 7\nv_max3_f32 v24, v24, v0, v1\nv_max3_f32 v24, v24, v2, v3\nv_max3_f32 v24, v24, v4, v5\nv_max3_f32 v24, v24, v6, v7\n"
"Qzvaa 0,112,16\nQmvaa 0,116,20\nQmvaa 0,120,24\nQmvaa 0,124,28\nQzvaa 4,128,16\nQmvaa 4,132,20\nQmvaa 4,136,24\nQmvaa 4,140,28\n"
"s_nop 7\nv_max3_f32 v28, v28, v0, v1\nv_max3_f32 v28, v28, v2, v3\nv_max3_f32 v28, v28, v4, v5\nv_max3_f32 v28, v28, v6, v7\nQzvaa 0,112,32\nQmvaa 0,116,36\nQmvaa 0,120,40\n"
"Qmvaa 0,124,44\nQzvaa 4,128,32\nQmvaa 4,132,36\nQmvaa 4,136,40\nQmvaa 4,140,44\ns_nop 7\nv_max3_f32 v32, v32, v0, v1\nv_max3_f32 v32, v32, v2, v3\n"
"v_max3_f32 v32, v32, v4, v5\nv_max3_f32 v32, v32, v6, v7\nQzvaa 0,112,48\nQmvaa 0,116,52\nQmvaa 0,120,56\nQmvaa 0,124,60\nQzvaa 4,128,48\nQmvaa 4,132,52\n"
"Qmvaa 4,136,56\nQmvaa 4,140,60\ns_nop 7\nv_max3_f32 v36, v36, v0, v1\nv_max3_f32 v36, v36, v2, v3\nv_max3_f32 v36, v36, v4, v5\nv_max3_f32 v36, v36, v6, v7\nQzvaa 0,112,64\n"
"Qmvaa 0,116,68\nQmvaa 0,120,72\nQmvaa 0,124,76\nQzvaa 4,128,64\nQmvaa 4,132,68\nQmvaa 4,136,72\nQmvaa 4,140,76\ns_nop 7\n"
"v_max3_f32 v40, v40, v0, v1\nv_max3_f32 v40, v40, v2, v3\nv_max3_f32 v40, v40, v4, v5\nv_max3_f32 v40, v40, v6, v7\nQzvaa 0,112,80\nQmvaa 0,116,84\nQmvaa 0,120,88\nQmvaa 0,124,92\n"
"Qzvaa 4,128,80\nQmvaa 4,132,84\nQmvaa 4,136,88\nQmvaa 4,140,92\ns_nop 7\nv_max3_f32 v44, v44, v0, v1\nv_max3_f32 v44, v44, v2, v3\nv_max3_f32 v44, v44, v4, v5\n"
"v_max3_f32 v44, v44, v6, v7\nQzvaa 0,112,96\nQmvaa 0,116,100\nQmvaa 0,120,104\nQmvaa 0,124,108\nQzvaa 4,128,96\nQmvaa 4,132,100\nQmvaa 4,136,104\n"
"Qmvaa 4,140,108\ns_nop 7\nv_max3_f32 v48, v48, v0, v1\nv_max3_f32 v48, v48, v2, v3\nv_max3_f32 v48, v48, v4, v5\nv_max3_f32 v48, v48, v6, v7\ns_add_u32 s89, s89, 8192\ns_add_u32 s66, s66, 1\n"
"s_cmp_lt_u32 s66, 32\ns_cbranch_scc1 .Lfb1\nv_mov_b32 v25, v24\ns_nop 1\nv_permlane32_swap_b32 v24, v25\nv_max_f32 v24, v24, v25\nv_mov_b32 v25, v24\ns_nop 1\n"
"v_permlane16_swap_b32 v24, v25\nv_max_f32 v24, v24, v25\nv_sub_f32 v24, 0, v24\nv_mov_b32 v25, v24\nv_mov_b32 v26, v24\nv_mov_b32 v27, v24\nv_mov_b32 v29, v28\ns_nop 1\n"
"v_permlane32_swap_b32 v28, v29\nv_max_f32 v28, v28, v29\nv_mov_b32 v29, v28\ns_nop 1\nv_permlane16_swap_b32 v28, v29\nv_max_f32 v28, v28, v29\nv_sub_f32 v28, 0, v28\nv_mov_b32 v29, v28\n"
"v_mov_b32 v30, v28\nv_mov_b32 v31, v28\nv_mov_b32 v33, v32\ns_nop 1\nv_permlane32_swap_b32 v32, v33\nv_max_f32 v32, v32, v33\nv_mov_b32 v33, v32\ns_nop 1\n"
"v_permlane16_swap_b32 v32, v33\nv_max_f32 v32, v32, v33\nv_sub_f32 v32, 0, v32\nv_mov_b32 v33, v32\nv_mov_b32 v34, v32\nv_mov_b32 v35, v32\nv_mov_b32 v37, v36\ns_nop 1\n"
"v_permlane32_swap_b32 v36, v37\nv_max_f32 v36, v36, v37\nv_mov_b32 v37, v36\ns_nop 1\nv_permlane16_swap_b32 v36, v37\nv_max_f32 v36, v36, v37\nv_sub_f32 v36, 0, v36\nv_mov_b32 v37, v36\n"
"v_mov_b32 v38, v36\nv_mov_b32 v39, v36\nv_mov_b32 v41, v40\ns_nop 1\nv_permlane32_swap_b32 v40, v41\nv_max_f32 v40, v40, v41\nv_mov_b32 v41, v40\ns_nop 1\n"
"v_permlane16_swap_b32 v40, v41\nv_max_f32 v40, v40, v41\nv_sub_f32 v40, 0, v40\nv_mov_b32 v41, v40\nv_mov_b32 v42, v40\nv_mov_b32 v43, v40\nv_mov_b32 v45, v44\ns_nop 1\n"
"v_permlane32_swap_b32 v44, v45\nv_max_f32 v44, v44, v45\nv_mov_b32 v45, v44\ns_nop 1\nv_permlane16_swap_b32 v44, v45\nv_max_f32 v44, v44, v45\nv_sub_f32 v44, 0, v44\nv_mov_b32 v45, v44\n"
"v_mov_b32 v46, v44\nv_mov_b32 v47, v44\nv_mov_b32 v49, v48\ns_nop 1\nv_permlane32_swap_b32 v48, v49\nv_max_f32 v48, v48, v49\nv_mov_b32 v49, v48\ns_nop 1\n"
"v_permlane16_swap_b32 v48, v49\nv_max_f32 v48, v48, v49\nv_sub_f32 v48, 0, v48\nv_mov_b32 v49, v48\nv_mov_b32 v50, v48\nv_mov_b32 v51, v48\nv_mov_b32 v52, 0\nv_mov_b32 v80, 0\n"
"v_mov_b32 v81, 0\nv_mov_b32 v82, 0\nv_mov_b32 v83, 0\nv_mov_b32 v84, 0\nv_mov_b32 v85, 0\nv_mov_b32 v86, 0\nv_mov_b32 v87, 0\nv_mov_b32 v88, 0\n"
"v_mov_b32 v89, 0\nv_mov_b32 v90, 0\nv_mov_b32 v91, 0\nv_mov_b32 v92, 0\nv_mov_b32 v93, 0\nv_mov_b32 v94, 0\nv_mov_b32 v95, 0\nv_mov_b32 v96, 0\n"
"v_mov_b32 v97, 0\nv_mov_b32 v98, 0\nv_mov_b32 v99, 0\nv_mov_b32 v100, 0\nv_mov_b32 v101, 0\nv_mov_b32 v102, 0\nv_mov_b32 v103, 0\nv_mov_b32 v104, 0\n"
"v_mov_b32 v105, 0\nv_mov_b32 v106, 0\nv_mov_b32 v107, 0\nv_mov_b32 v108, 0\nv_mov_b32 v109, 0\nv_mov_b32 v110, 0\nv_mov_b32 v111, 0\nv_accvgpr_write_b32 a176, v52\n"
"v_accvgpr_write_b32 a177, v52\nv_accvgpr_write_b32 a178, v52\nv_accvgpr_write_b32 a179, v52\nv_mov_b32 v112, 0\nv_mov_b32 v113, 0\nv_mov_b32 v114, 0\nv_mov_b32 v115, 0\nv_mov_b32 v116, 0\n"
"v_mov_b32 v117, 0\nv_mov_b32 v118, 0\nv_mov_b32 v119, 0\nv_mov_b32 v120, 0\nv_mov_b32 v121, 0\nv_mov_b32 v122, 0\nv_mov_b32 v123, 0\nv_mov_b32 v124, 0\n"
"v_mov_b32 v125, 0\nv_mov_b32 v126, 0\nv_mov_b32 v127, 0\nv_mov_b32 v128, 0\nv_mov_b32 v129, 0\nv_mov_b32 v130, 0\nv_mov_b32 v131, 0\nv_mov_b32 v132, 0\n"
"v_mov_b32 v133, 0\nv_mov_b32 v134, 0\nv_mov_b32 v135, 0\nv_mov_b32 v136, 0\nv_mov_b32 v137, 0\nv_mov_b32 v138, 0\nv_mov_b32 v139, 0\nv_mov_b32 v140, 0\n"
"v_mov_b32 v141, 0\nv_mov_b32 v142, 0\nv_mov_b32 v143, 0\nv_accvgpr_write_b32 a180, v52\nv_accvgpr_write_b32 a181, v52\nv_accvgpr_write_b32 a182, v52\nv_accvgpr_write_b32 a183, v52\nv_mov_b32 v144, 0\n"
"v_mov_b32 v145, 0\nv_mov_b32 v146, 0\nv_mov_b32 v147, 0\nv_mov_b32 v148, 0\nv_mov_b32 v149, 0\nv_mov_b32 v150, 0\nv_mov_b32 v151, 0\nv_mov_b32 v152, 0\n"
"v_mov_b32 v153, 0\nv_mov_b32 v154, 0\nv_mov_b32 v155, 0\nv_mov_b32 v156, 0\nv_mov_b32 v157, 0\nv_mov_b32 v158, 0\nv_mov_b32 v159, 0\nv_mov_b32 v160, 0\n"
"v_mov_b32 v161, 0\nv_mov_b32 v162, 0\nv_mov_b32 v163, 0\nv_mov_b32 v164, 0\nv_mov_b32 v165, 0\nv_mov_b32 v166, 0\nv_mov_b32 v167, 0\nv_mov_b32 v168, 0\n"
"v_mov_b32 v169, 0\nv_mov_b32 v170, 0\nv_mov_b32 v171, 0\nv_mov_b32 v172, 0\nv_mov_b32 v173, 0\nv_mov_b32 v174, 0\nv_mov_b32 v175, 0\nv_accvgpr_write_b32 a184, v52\n"
"v_accvgpr_write_b32 a185, v52\nv_accvgpr_write_b32 a186, v52\nv_accvgpr_write_b32 a187, v52\nv_mov_b32 v176, 0\nv_mov_b32 v177, 0\nv_mov_b32 v178, 0\nv_mov_b32 v179, 0\nv_mov_b32 v180, 0\n"
"v_mov_b32 v181, 0\nv_mov_b32 v182, 0\nv_mov_b32 v183, 0\nv_mov_b32 v184, 0\nv_mov_b32 v185, 0\nv_mov_b32 v186, 0\nv_mov_b32 v187, 0\nv_mov_b32 v188, 0\n"
"v_mov_b32 v189, 0\nv_mov_b32 v190, 0\nv_mov_b32 v191, 0\nv_mov_b32 v192, 0\nv_mov_b32 v193, 0\nv_mov_b32 v194, 0\nv_mov_b32 v195, 0\nv_mov_b32 v196, 0\n"
"v_mov_b32 v197, 0\nv_mov_b32 v198, 0\nv_mov_b32 v199, 0\nv_mov_b32 v200, 0\nv_mov_b32 v201, 0\nv_mov_b32 v202, 0\nv_mov_b32 v203, 0\nv_mov_b32 v204, 0\n"
"v_mov_b32 v205, 0\nv_mov_b32 v206, 0\nv_mov_b32 v207, 0\nv_accvgpr_write_b32 a188, v52\nv_accvgpr_write_b32 a189, v52\nv_accvgpr_write_b32 a190, v52\nv_accvgpr_write_b32 a191, v52\nv_mov_b32 v208, 0\n"
"v_mov_b32 v209, 0\nv_mov_b32 v210, 0\nv_mov_b32 v211, 0\nv_mov_b32 v212, 0\nv_mov_b32 v213, 0\nv_mov_b32 v214, 0\nv_mov_b32 v215, 0\nv_mov_b32 v216, 0\n"
"v_mov_b32 v217, 0\nv_mov_b32 v218, 0\nv_mov_b32 v219, 0\nv_mov_b32 v220, 0\nv_mov_b32 v221, 0\nv_mov_b32 v222, 0\nv_mov_b32 v223, 0\nv_mov_b32 v224, 0\n"
"v_mov_b32 v225, 0\nv_mov_b32 v226, 0\nv_mov_b32 v227, 0\nv_mov_b32 v228, 0\nv_mov_b32 v229, 0\nv_mov_b32 v230, 0\nv_mov_b32 v231, 0\nv_mov_b32 v232, 0\n"
"v_mov_b32 v233, 0\nv_mov_b32 v234, 0\nv_mov_b32 v235, 0\nv_mov_b32 v236, 0\nv_mov_b32 v237, 0\nv_mov_b32 v238, 0\nv_mov_b32 v239, 0\nv_accvgpr_write_b32 a192, v52\n"
"v_accvgpr_write_b32 a193, v52\nv_accvgpr_write_b32 a194, v52\nv_accvgpr_write_b32 a195, v52\nv_mov_b32 v240, 0\nv_mov_b32 v241, 0\nv_mov_b32 v242, 0\nv_mov_b32 v243, 0\nv_mov_b32 v244, 0\n"
"v_mov_b32 v245, 0\nv_mov_b32 v246, 0\nv_mov_b32 v247, 0\nv_mov_b32 v248, 0\nv_mov_b32 v249, 0\nv_mov_b32 v250, 0\nv_mov_b32 v251, 0\nv_mov_b32 v252, 0\n"
"v_mov_b32 v253, 0\nv_mov_b32 v254, 0\nv_mov_b32 v255, 0\nv_accvgpr_write_b32 a208, v52\nv_accvgpr_write_b32 a209, v52\nv_accvgpr_write_b32 a210, v52\nv_accvgpr_write_b32 a211, v52\nv_accvgpr_write_b32 a212, v52\n"
"v_accvgpr_write_b32 a213, v52\nv_accvgpr_write_b32 a214, v52\nv_accvgpr_write_b32 a215, v52\nv_accvgpr_write_b32 a216, v52\nv_accvgpr_write_b32 a217, v52\nv_accvgpr_write_b32 a218, v52\nv_accvgpr_write_b32 a219, v52\nv_accvgpr_write_b32 a220, v52\n"
"v_accvgpr_write_b32 a221, v52\nv_accvgpr_write_b32 a222, v52\nv_accvgpr_write_b32 a223, v52\nv_accvgpr_write_b32 a196, v52\nv_accvgpr_write_b32 a197, v52\nv_accvgpr_write_b32 a198, v52\nv_accvgpr_write_b32 a199, v52\nv_accvgpr_write_b32 a224, v52\n"
"v_accvgpr_write_b32 a225, v52\nv_accvgpr_write_b32 a226, v52\nv_accvgpr_write_b32 a227, v52\nv_accvgpr_write_b32 a228, v52\nv_accvgpr_write_b32 a229, v52\nv_accvgpr_write_b32 a230, v52\nv_accvgpr_write_b32 a231, v52\nv_accvgpr_write_b32 a232, v52\n"
"v_accvgpr_write_b32 a233, v52\nv_accvgpr_write_b32 a234, v52\nv_accvgpr_write_b32 a235, v52\nv_accvgpr_write_b32 a236, v52\nv_accvgpr_write_b32 a237, v52\nv_accvgpr_write_b32 a238, v52\nv_accvgpr_write_b32 a239, v52\nv_accvgpr_write_b32 a240, v52\n"
"v_accvgpr_write_b32 a241, v52\nv_accvgpr_write_b32 a242, v52\nv_accvgpr_write_b32 a243, v52\nv_accvgpr_write_b32 a244, v52\nv_accvgpr_write_b32 a245, v52\nv_accvgpr_write_b32 a246, v52\nv_accvgpr_write_b32 a247, v52\nv_accvgpr_write_b32 a248, v52\n"
"v_accvgpr_write_b32 a249, v52\nv_accvgpr_write_b32 a250, v52\nv_accvgpr_write_b32 a251, v52\nv_accvgpr_write_b32 a252, v52\nv_accvgpr_write_b32 a253, v52\nv_accvgpr_write_b32 a254, v52\nv_accvgpr_write_b32 a255, v52\nv_accvgpr_write_b32 a200, v52\n"
"v_accvgpr_write_b32 a201, v52\nv_accvgpr_write_b32 a202, v52\nv_accvgpr_write_b32 a203, v52\ns_lshl_b32 s89, s67, 21\ns_lshl_b32 s68, s93, 18\ns_add_u32 s89, s89, s68\ns_mov_b32 s66, 0\n.Lfb2:\n"
"s_add_u32 s68, s89, 4096\nbuffer_load_dwordx4 a[112:115], v66, s[40:43], s89 offen offset:0\nbuffer_load_dwordx4 a[116:119], v66, s[40:43], s89 offen offset:64\nbuffer_load_dwordx4 a[120:123], v66, s[40:43], s89 offen offset:128\nbuffer_load_dwordx4 a[124:127], v66, s[40:43], s89 offen offset:192\nbuffer_load_dwordx4 a[128:131], v66, s[40:43], s68 offen offset:0\nbuffer_load_dwordx4 a[132:135], v66, s[40:43], s68 offen offset:64\nbuffer_load_dwordx4 a[136:139], v66, s[40:43], s68 offen offset:128\n"
"buffer_load_dwordx4 a[140:143], v66, s[40:43], s68 offen offset:192\nbuffer_load_ushort v8, v67, s[44:47], s89 offen offset:0\nbuffer_load_ushort v9, v67, s[44:47], s89 offen offset:256\nbuffer_load_ushort v10, v67, s[44:47], s89 offen offset:512\nbuffer_load_ushort v11, v67, s[44:47], s89 offen offset:768\nbuffer_load_ushort v12, v67, s[44:47], s68 offen offset:0\nbuffer_load_ushort v13, v67, s[44:47], s68 offen offset:256\nbuffer_load_ushort v14, v67, s[44:47], s68 offen offset:512\n"
"buffer_load_ushort v15, v67, s[44:47], s68 offen offset:768\ns_waitcnt vmcnt(0)\nv_lshl_or_b32 v8, v9, 16, v8\nv_accvgpr_write_b32 a144, v8\nv_lshl_or_b32 v10, v11, 16, v10\nv_accvgpr_write_b32 a145, v10\nv_lshl_or_b32 v12, v13, 16, v12\nv_accvgpr_write_b32 a146, v12\n"
"v_lshl_or_b32 v14, v15, 16, v14\nv_accvgpr_write_b32 a147, v14\nbuffer_load_ushort v8, v67, s[44:47], s89 offen offset:32\nbuffer_load_ushort v9, v67, s[44:47], s89 offen offset:288\nbuffer_load_ushort v10, v67, s[44:47], s89 offen offset:544\nbuffer_load_ushort v11, v67, s[44:47], s89 offen offset:800\nbuffer_load_ushort v12, v67, s[44:47], s68 offen offset:32\nbuffer_load_ushort v13, v67, s[44:47], s68 offen offset:288\n"
"buffer_load_ushort v14, v67, s[44:47], s68 offen offset:544\nbuffer_load_ushort v15, v67, s[44:47], s68 offen offset:800\ns_waitcnt vmcnt(0)\nv_lshl_or_b32 v8, v9, 16, v8\nv_accvgpr_write_b32 a148, v8\nv_lshl_or_b32 v10, v11, 16, v10\nv_accvgpr_write_b32 a149, v10\nv_lshl_or_b32 v12, v13, 16, v12\n"
"v_accvgpr_write_b32 a150, v12\nv_lshl_or_b32 v14, v15, 16, v14\nv_accvgpr_write_b32 a151, v14\nbuffer_load_ushort v8, v67, s[44:47], s89 offen offset:64\nbuffer_load_ushort v9, v67, s[44:47], s89 offen offset:320\nbuffer_load_ushort v10, v67, s[44:47], s89 offen offset:576\nbuffer_load_ushort v11, v67, s[44:47], s89 offen offset:832\nbuffer_load_ushort v12, v67, s[44:47], s68 offen offset:64\n"
"buffer_load_ushort v13, v67, s[44:47], s68 offen offset:320\nbuffer_load_ushort v14, v67, s[44:47], s68 offen offset:576\nbuffer_load_ushort v15, v67, s[44:47], s68 offen offset:832\ns_waitcnt vmcnt(0)\nv_lshl_or_b32 v8, v9, 16, v8\nv_accvgpr_write_b32 a152, v8\nv_lshl_or_b32 v10, v11, 16, v10\nv_accvgpr_write_b32 a153, v10\n"
"v_lshl_or_b32 v12, v13, 16, v12\nv_accvgpr_write_b32 a154, v12\nv_lshl_or_b32 v14, v15, 16, v14\nv_accvgpr_write_b32 a155, v14\nbuffer_load_ushort v8, v67, s[44:47], s89 offen offset:96\nbuffer_load_ushort v9, v67, s[44:47], s89 offen offset:352\nbuffer_load_ushort v10, v67, s[44:47], s89 offen offset:608\nbuffer_load_ushort v11, v67, s[44:47], s89 offen offset:864\n"
"buffer_load_ushort v12, v67, s[44:47], s68 offen offset:96\nbuffer_load_ushort v13, v67, s[44:47], s68 offen offset:352\nbuffer_load_ushort v14, v67, s[44:47], s68 offen offset:608\nbuffer_load_ushort v15, v67, s[44:47], s68 offen offset:864\ns_waitcnt vmcnt(0)\nv_lshl_or_b32 v8, v9, 16, v8\nv_accvgpr_write_b32 a156, v8\nv_lshl_or_b32 v10, v11, 16, v10\n"
"v_accvgpr_write_b32 a157, v10\nv_lshl_or_b32 v12, v13, 16, v12\nv_accvgpr_write_b32 a158, v12\nv_lshl_or_b32 v14, v15, 16, v14\nv_accvgpr_write_b32 a159, v14\nbuffer_load_ushort v8, v67, s[44:47], s89 offen offset:128\nbuffer_load_ushort v9, v67, s[44:47], s89 offen offset:384\nbuffer_load_ushort v10, v67, s[44:47], s89 offen offset:640\n"
"buffer_load_ushort v11, v67, s[44:47], s89 offen offset:896\nbuffer_load_ushort v12, v67, s[44:47], s68 offen offset:128\nbuffer_load_ushort v13, v67, s[44:47], s68 offen offset:384\nbuffer_load_ushort v14, v67, s[44:47], s68 offen offset:640\nbuffer_load_ushort v15, v67, s[44:47], s68 offen offset:896\ns_waitcnt vmcnt(0)\nv_lshl_or_b32 v8, v9, 16, v8\nv_accvgpr_write_b32 a160, v8\n"
"v_lshl_or_b32 v10, v11, 16, v10\nv_accvgpr_write_b32 a161, v10\nv_lshl_or_b32 v12, v13, 16, v12\nv_accvgpr_write_b32 a162, v12\nv_lshl_or_b32 v14, v15, 16, v14\nv_accvgpr_write_b32 a163, v14\nbuffer_load_ushort v8, v67, s[44:47], s89 offen offset:160\nbuffer_load_ushort v9, v67, s[44:47], s89 offen offset:416\n"
"buffer_load_ushort v10, v67, s[44:47], s89 offen offset:672\nbuffer_load_ushort v11, v67, s[44:47], s89 offen offset:928\nbuffer_load_ushort v12, v67, s[44:47], s68 offen offset:160\nbuffer_load_ushort v13, v67, s[44:47], s68 offen offset:416\nbuffer_load_ushort v14, v67, s[44:47], s68 offen offset:672\nbuffer_load_ushort v15, v67, s[44:47], s68 offen offset:928\ns_waitcnt vmcnt(0)\nv_lshl_or_b32 v8, v9, 16, v8\n"
"v_accvgpr_write_b32 a164, v8\nv_lshl_or_b32 v10, v11, 16, v10\nv_accvgpr_write_b32 a165, v10\nv_lshl_or_b32 v12, v13, 16, v12\nv_accvgpr_write_b32 a166, v12\nv_lshl_or_b32 v14, v15, 16, v14\nv_accvgpr_write_b32 a167, v14\nbuffer_load_ushort v8, v67, s[44:47], s89 offen offset:192\n"
"buffer_load_ushort v9, v67, s[44:47], s89 offen offset:448\nbuffer_load_ushort v10, v67, s[44:47], s89 offen offset:704\nbuffer_load_ushort v11, v67, s[44:47], s89 offen offset:960\nbuffer_load_ushort v12, v67, s[44:47], s68 offen offset:192\nbuffer_load_ushort v13, v67, s[44:47], s68 offen offset:448\nbuffer_load_ushort v14, v67, s[44:47], s68 offen offset:704\nbuffer_load_ushort v15, v67, s[44:47], s68 offen offset:960\ns_waitcnt vmcnt(0)\n"
"v_lshl_or_b32 v8, v9, 16, v8\nv_accvgpr_write_b32 a168, v8\nv_lshl_or_b32 v10, v11, 16, v10\nv_accvgpr_write_b32 a169, v10\nv_lshl_or_b32 v12, v13, 16, v12\nv_accvgpr_write_b32 a170, v12\nv_lshl_or_b32 v14, v15, 16, v14\nv_accvgpr_write_b32 a171, v14\n"
"buffer_load_ushort v8, v67, s[44:47], s89 offen offset:224\nbuffer_load_ushort v9, v67, s[44:47], s89 offen offset:480\nbuffer_load_ushort v10, v67, s[44:47], s89 offen offset:736\nbuffer_load_ushort v11, v67, s[44:47], s89 offen offset:992\nbuffer_load_ushort v12, v67, s[44:47], s68 offen offset:224\nbuffer_load_ushort v13, v67, s[44:47], s68 offen offset:480\nbuffer_load_ushort v14, v67, s[44:47], s68 offen offset:736\nbuffer_load_ushort v15, v67, s[44:47], s68 offen offset:992\n"
"s_waitcnt vmcnt(0)\nv_lshl_or_b32 v8, v9, 16, v8\nv_accvgpr_write_b32 a172, v8\nv_lshl_or_b32 v10, v11, 16, v10\nv_accvgpr_write_b32 a173, v10\nv_lshl_or_b32 v12, v13, 16, v12\nv_accvgpr_write_b32 a174, v12\nv_lshl_or_b32 v14, v15, 16, v14\n"
"v_accvgpr_write_b32 a175, v14\nQcvaa 0,112,0,24\nQmvaa 0,116,4\nQmvaa 0,120,8\nQmvaa 0,124,12\nQcvaa 4,128,0,24\nQmvaa 4,132,4\nQmvaa 4,136,8\n"
"Qmvaa 4,140,12\ns_nop 7\nQe 0\nQe 1\nQe 2\nQe 3\nQe 4\nQe 5\n"
"Qe 6\nQe 7\ns_nop 0\nQk 16,0,1\nQk 17,2,3\nQk 18,4,5\nQk 19,6,7\ns_nop 1\n"
"Qmvva 80,16,144\nQmvva 84,16,148\nQmvva 88,16,152\nQmvva 92,16,156\nQmvva 96,16,160\nQmvva 100,16,164\nQmvva 104,16,168\nQmvva 108,16,172\n"
"Qmaav 176,204,16\nQcvaa 0,112,16,28\nQmvaa 0,116,20\nQmvaa 0,120,24\nQmvaa 0,124,28\nQcvaa 4,128,16,28\nQmvaa 4,132,20\nQmvaa 4,136,24\n"
"Qmvaa 4,140,28\ns_nop 7\nQe 0\nQe 1\nQe 2\nQe 3\nQe 4\nQe 5\n"
"Qe 6\nQe 7\ns_nop 0\nQk 16,0,1\nQk 17,2,3\nQk 18,4,5\nQk 19,6,7\ns_nop 1\n"
"Qmvva 112,16,144\nQmvva 116,16,148\nQmvva 120,16,152\nQmvva 124,16,156\nQmvva 128,16,160\nQmvva 132,16,164\nQmvva 136,16,168\nQmvva 140,16,172\n"
"Qmaav 180,204,16\nQcvaa 0,112,32,32\nQmvaa 0,116,36\nQmvaa 0,120,40\nQmvaa 0,124,44\nQcvaa 4,128,32,32\nQmvaa 4,132,36\nQmvaa 4,136,40\n"
"Qmvaa 4,140,44\ns_nop 7\nQe 0\nQe 1\nQe 2\nQe 3\nQe 4\nQe 5\n"
"Qe 6\nQe 7\ns_nop 0\nQk 16,0,1\nQk 17,2,3\nQk 18,4,5\nQk 19,6,7\ns_nop 1\n"
"Qmvva 144,16,144\nQmvva 148,16,148\nQmvva 152,16,152\nQmvva 156,16,156\nQmvva 160,16,160\nQmvva 164,16,164\nQmvva 168,16,168\nQmvva 172,16,172\n"
"Qmaav 184,204,16\nQcvaa 0,112,48,36\nQmvaa 0,116,52\nQmvaa 0,120,56\nQmvaa 0,124,60\nQcvaa 4,128,48,36\nQmvaa 4,132,52\nQmvaa 4,136,56\n"
"Qmvaa 4,140,60\ns_nop 7\nQe 0\nQe 1\nQe 2\nQe 3\nQe 4\nQe 5\n"
"Qe 6\nQe 7\ns_nop 0\nQk 16,0,1\nQk 17,2,3\nQk 18,4,5\nQk 19,6,7\ns_nop 1\n"
"Qmvva 176,16,144\nQmvva 180,16,148\nQmvva 184,16,152\nQmvva 188,16,156\nQmvva 192,16,160\nQmvva 196,16,164\nQmvva 200,16,168\nQmvva 204,16,172\n"
"Qmaav 188,204,16\nQcvaa 0,112,64,40\nQmvaa 0,116,68\nQmvaa 0,120,72\nQmvaa 0,124,76\nQcvaa 4,128,64,40\nQmvaa 4,132,68\nQmvaa 4,136,72\n"
"Qmvaa 4,140,76\ns_nop 7\nQe 0\nQe 1\nQe 2\nQe 3\nQe 4\nQe 5\n"
"Qe 6\nQe 7\ns_nop 0\nQk 16,0,1\nQk 17,2,3\nQk 18,4,5\nQk 19,6,7\ns_nop 1\n"
"Qmvva 208,16,144\nQmvva 212,16,148\nQmvva 216,16,152\nQmvva 220,16,156\nQmvva 224,16,160\nQmvva 228,16,164\nQmvva 232,16,168\nQmvva 236,16,172\n"
"Qmaav 192,204,16\nQcvaa 0,112,80,44\nQmvaa 0,116,84\nQmvaa 0,120,88\nQmvaa 0,124,92\nQcvaa 4,128,80,44\nQmvaa 4,132,84\nQmvaa 4,136,88\n"
"Qmvaa 4,140,92\ns_nop 7\nQe 0\nQe 1\nQe 2\nQe 3\nQe 4\nQe 5\n"
"Qe 6\nQe 7\ns_nop 0\nQk 16,0,1\nQk 17,2,3\nQk 18,4,5\nQk 19,6,7\ns_nop 1\n"
"Qmvva 240,16,144\nQmvva 244,16,148\nQmvva 248,16,152\nQmvva 252,16,156\nQmava 208,16,160\nQmava 212,16,164\nQmava 216,16,168\nQmava 220,16,172\n"
"Qmaav 196,204,16\nQcvaa 0,112,96,48\nQmvaa 0,116,100\nQmvaa 0,120,104\nQmvaa 0,124,108\nQcvaa 4,128,96,48\nQmvaa 4,132,100\nQmvaa 4,136,104\n"
"Qmvaa 4,140,108\ns_nop 7\nQe 0\nQe 1\nQe 2\nQe 3\nQe 4\nQe 5\n"
"Qe 6\nQe 7\ns_nop 0\nQk 16,0,1\nQk 17,2,3\nQk 18,4,5\nQk 19,6,7\ns_nop 1\n"
"Qmava 224,16,144\nQmava 228,16,148\nQmava 232,16,152\nQmava 236,16,156\nQmava 240,16,160\nQmava 244,16,164\nQmava 248,16,168\nQmava 252,16,172\n"
"Qmaav 200,204,16\ns_add_u32 s89, s89, 8192\ns_add_u32 s66, s66, 1\ns_cmp_lt_u32 s66, 32\ns_cbranch_scc1 .Lfb2\ns_nop 7\nv_accvgpr_read_b32 v0, a176\nv_accvgpr_read_b32 v4, a180\n"
"v_accvgpr_read_b32 v8, a184\nv_accvgpr_read_b32 v12, a188\nv_accvgpr_read_b32 v16, a192\nv_accvgpr_read_b32 v20, a196\nv_accvgpr_read_b32 v24, a200\ns_mov_b32 s73, 0\nv_mov_b32 v30, s73\nv_mov_b32 v31, 0\n"
"v_mov_b32 v58, 0xb9800000\nv_mov_b32 v59, 0\nv_and_b32 v66, 16, v60\nv_mov_b32 v67, 0xffff\nv_lshlrev_b32 v66, v66, v67\ns_mov_b32 vcc_lo, 0x00c0000c\ns_mov_b32 vcc_hi, 0xc0000c00\nv_cndmask_b32 v67, 0, v66, vcc\n"
"s_lshr_b64 vcc, vcc, 2\nv_cndmask_b32 v66, 0, v66, vcc\ns_cmp_eq_u32 s67, 0\ns_cbranch_scc1 .Lfbret0\ns_branch .Lfbret1\n.Lf2_w:\ns_mov_b32 s97, 0\n.Lf2_l:\n"
"s_sleep 1\nbuffer_load_dword v78, v76, s[24:27], 0 offen sc1\ns_waitcnt vmcnt(0)\ns_add_u32 s97, s97, 1\ns_cmp_gt_u32 s97, 1048576\ns_cbranch_scc1 .Lf2_r\nv_sub_u32 v66, v78, v65\nv_cmp_gt_u32 vcc, 2, v66\n"
"s_and_b64 vcc, vcc, s[28:29]\ns_cbranch_scc1 .Lf2_l\ns_branch .Lf2_r\n.Lf3_w:\ns_mov_b32 s97, 0\n.Lf3_l:\ns_sleep 1\nbuffer_load_dword v78, v76, s[24:27], 0 offen sc1\n"
"s_waitcnt vmcnt(0)\ns_add_u32 s97, s97, 1\ns_cmp_gt_u32 s97, 1048576\ns_cbranch_scc1 .Lf3_r\nv_sub_u32 v66, v78, v65\nv_cmp_gt_u32 vcc, 3, v66\ns_and_b64 vcc, vcc, s[28:29]\ns_cbranch_scc1 .Lf3_l\n"
"s_branch .Lf3_r\n.Lx0_w:\ns_mov_b32 s97, 0\n.Lx0_l:\ns_sleep 1\nbuffer_load_dword v78, v76, s[24:27], 0 offen sc1\ns_waitcnt vmcnt(0)\ns_add_u32 s97, s97, 1\n"
"s_cmp_gt_u32 s97, 1048576\ns_cbranch_scc1 .Lx0_r\nv_sub_u32 v66, v78, v65\nv_cmp_gt_u32 vcc, 1, v66\ns_and_b64 vcc, vcc, s[28:29]\ns_cbranch_scc1 .Lx0_l\ns_branch .Lx0_r\n.Lx1_w:\n"
"s_mov_b32 s97, 0\n.Lx1_l:\ns_sleep 1\nbuffer_load_dword v78, v76, s[24:27], 0 offen sc1\ns_waitcnt vmcnt(0)\ns_add_u32 s97, s97, 1\ns_cmp_gt_u32 s97, 1048576\ns_cbranch_scc1 .Lx1_r\n"
"v_sub_u32 v66, v78, v65\nv_cmp_gt_u32 vcc, 1, v66\ns_and_b64 vcc, vcc, s[28:29]\ns_cbranch_scc1 .Lx1_l\ns_branch .Lx1_r\n.Lexit:\n"
  : "=&s"(t0), "=&s"(t1)
  : "s"(__builtin_amdgcn_kernarg_segment_ptr()), "s"(blockIdx.x), "s"((unsigned)(size_t)lds), "s"(__builtin_amdgcn_readfirstlane(threadIdx.x >> 6))
  : "v0", "v1", "v2", "v3", "v4", "v5", "v6", "v7", "v8", "v9", "v10", "v11", "v12", "v13", "v14", "v15", "v16", "v17", "v18", "v19", "v20", "v21", "v22", "v23", "v24", "v25", "v26", "v27", "v28", "v29", "v30", "v31", "v32", "v33", "v34", "v35", "v36", "v37", "v38", "v39", "v40", "v41", "v42", "v43", "v44", "v45", "v46", "v47", "v48", "v49", "v50", "v51", "v52", "v53", "v54", "v55", "v56", "v57", "v58", "v59", "v60", "v61", "v62", "v63", "v64", "v65", "v66", "v67", "v68", "v69", "v70", "v71", "v72", "v73", "v74", "v75", "v76", "v77", "v78", "v79", "v80", "v81", "v82", "v83", "v84", "v85", "v86", "v87", "v88", "v89", "v90", "v91", "v92", "v93", "v94", "v95", "v96", "v97", "v98", "v99", "v100", "v101", "v102", "v103", "v104", "v105", "v106", "v107", "v108", "v109", "v110", "v111", "v112", "v113", "v114", "v115", "v116", "v117", "v118", "v119", "v120", "v121", "v122", "v123", "v124", "v125", "v126", "v127", "v128", "v129", "v130", "v131", "v132", "v133", "v134", "v135", "v136", "v137", "v138", "v139", "v140", "v141", "v142", "v143", "v144", "v145", "v146", "v147", "v148", "v149", "v150", "v151", "v152", "v153", "v154", "v155", "v156", "v157", "v158", "v159", "v160", "v161", "v162", "v163", "v164", "v165", "v166", "v167", "v168", "v169", "v170", "v171", "v172", "v173", "v174", "v175", "v176", "v177", "v178", "v179", "v180", "v181", "v182", "v183", "v184", "v185", "v186", "v187", "v188", "v189", "v190", "v191", "v192", "v193", "v194", "v195", "v196", "v197", "v198", "v199", "v200", "v201", "v202", "v203", "v204", "v205", "v206", "v207", "v208", "v209", "v210", "v211", "v212", "v213", "v214", "v215", "v216", "v217", "v218", "v219", "v220", "v221", "v222", "v223", "v224", "v225", "v226", "v227", "v228", "v229", "v230", "v231", "v232", "v233", "v234", "v235", "v236", "v237", "v238", "v239", "v240", "v241", "v242", "v243", "v244", "v245", "v246", "v247", "v248", "v249", "v250", "v251", "v252", "v253", "v254", "v255", "a0", "a1", "a2", "a3", "a4", "a5", "a6", "a7", "a8", "a9", "a10", "a11", "a12", "a13", "a14", "a15", "a16", "a17", "a18", "a19", "a20", "a21", "a22", "a23", "a24", "a25", "a26", "a27", "a28", "a29", "a30", "a31", "a32", "a33", "a34", "a35", "a36", "a37", "a38", "a39", "a40", "a41", "a42", "a43", "a44", "a45", "a46", "a47", "a48", "a49", "a50", "a51", "a52", "a53", "a54", "a55", "a56", "a57", "a58", "a59", "a60", "a61", "a62", "a63", "a64", "a65", "a66", "a67", "a68", "a69", "a70", "a71", "a72", "a73", "a74", "a75", "a76", "a77", "a78", "a79", "a80", "a81", "a82", "a83", "a84", "a85", "a86", "a87", "a88", "a89", "a90", "a91", "a92", "a93", "a94", "a95", "a96", "a97", "a98", "a99", "a100", "a101", "a102", "a103", "a104", "a105", "a106", "a107", "a108", "a109", "a110", "a111", "a112", "a113", "a114", "a115", "a116", "a117", "a118", "a119", "a120", "a121", "a122", "a123", "a124", "a125", "a126", "a127", "a128", "a129", "a130", "a131", "a132", "a133", "a134", "a135", "a136", "a137", "a138", "a139", "a140", "a141", "a142", "a143", "a144", "a145", "a146", "a147", "a148", "a149", "a150", "a151", "a152", "a153", "a154", "a155", "a156", "a157", "a158", "a159", "a160", "a161", "a162", "a163", "a164", "a165", "a166", "a167", "a168", "a169", "a170", "a171", "a172", "a173", "a174", "a175", "a176", "a177", "a178", "a179", "a180", "a181", "a182", "a183", "a184", "a185", "a186", "a187", "a188", "a189", "a190", "a191", "a192", "a193", "a194", "a195", "a196", "a197", "a198", "a199", "a200", "a201", "a202", "a203", "a204", "a205", "a206", "a207", "a208", "a209", "a210", "a211", "a212", "a213", "a214", "a215", "a216", "a217", "a218", "a219", "a220", "a221", "a222", "a223", "a224", "a225", "a226", "a227", "a228", "a229", "a230", "a231", "a232", "a233", "a234", "a235", "a236", "a237", "a238", "a239", "a240", "a241", "a242", "a243", "a244", "a245", "a246", "a247", "a248", "a249", "a250", "a251", "a252", "a253", "a254", "a255", "s8", "s9", "s10", "s11", "s12", "s13", "s14", "s15", "s16", "s17", "s18", "s19", "s20", "s21", "s22", "s23", "s24", "s25", "s26", "s27", "s28", "s29", "s30", "s31", "s33", "s34", "s35", "s36", "s37", "s38", "s39", "s40", "s41", "s42", "s43", "s44", "s45", "s46", "s47", "s48", "s49", "s50", "s51", "s52", "s53", "s54", "s55", "s56", "s57", "s58", "s59", "s60", "s61", "s62", "s63", "s64", "s65", "s66", "s67", "s68", "s69", "s70", "s71", "s72", "s73", "s74", "s75", "s76", "s77", "s78", "s79", "s80", "s81", "s82", "s83", "s84", "s85", "s86", "s87", "s88", "s89", "s90", "s91", "s92", "s93", "s94", "s95", "s96", "s97", "s98", "s99", "s100", "s101", "vcc", "scc", "memory");
}

extern "C" void kernel_launch(void* const* d_in, const int* in_sizes, int n_in, void* d_out, int out_size, void* d_ws, size_t ws_size, hipStream_t stream) {
  typedef const unsigned short* P;
  (void)hipFuncSetAttribute((const void*)mha2_asm, hipFuncAttributeMaxDynamicSharedMemorySize, 163840);
  hipLaunchKernelGGL(mha2_asm, dim3(256), dim3(256), 163840, stream, (P)d_in[0], (P)d_in[3], (P)d_in[1], (P)d_in[2], (P)d_in[5], (P)d_in[8], (P)d_in[6], (P)d_in[7],
                     (P)d_in[4], (P)d_in[9], (char*)d_ws, (unsigned short*)d_out);
}
```

```cpp
#include <hip/hip_runtime.h>
#include <stdint.h>

__global__ __launch_bounds__(256, 1) __attribute__((aligned(4096)))
void mha2_asm(const unsigned short* X0, const unsigned short* W0, const unsigned short* K0, const unsigned short* V0,
              const unsigned short* X1, const unsigned short* W1, const unsigned short* K1, const unsigned short* V1,
              const unsigned short* WO0, const unsigned short* WO1, char* ws, unsigned short* out) {
  extern __shared__ __attribute__((aligned(16))) char lds[];
  unsigned t0, t1;
  asm volatile(
".macro Qta d,v,o\nds_read_b64_tr_b16 a[\\d:\\d+1], v\\v offset:\\o\n.endm\n.macro Qda d,v,o\nds_read_b128 a[\\d:\\d+3], v\\v offset:\\o\n.endm\n.macro Qzvaa d,a,b\nv_mfma_f32_16x16x32_bf16 v[\\d:\\d+3], a[\\a:\\a+3], a[\\b:\\b+3], 0\n"
".endm\n.macro Qcvaa d,a,b,c\nv_mfma_f32_16x16x32_bf16 v[\\d:\\d+3], a[\\a:\\a+3], a[\\b:\\b+3], v[\\c:\\c+3]\n.endm\n.macro Qk d,a,b\nv_cvt_pk_bf16_f32 v\\d, v\\a, v\\b\n.endm\n.macro Qmvaa d,a,b\n"
"v_mfma_f32_16x16x32_bf16 v[\\d:\\d+3], a[\\a:\\a+3], a[\\b:\\b+3], v[\\d:\\d+3]\n.endm\n.macro Qtv d,v,o\nds_read_b64_tr_b16 v[\\d:\\d+1], v\\v offset:\\o\n.endm\n.macro Qa d,a,b\nv_add_f32 v\\d, v\\a, v\\b\n.endm\n"
".macro Qe d\nv_exp_f32 v\\d, v\\d\n.endm\n.macro Qmvva d,a,b\nv_mfma_f32_16x16x32_bf16 v[\\d:\\d+3], v[\\a:\\a+3], a[\\b:\\b+3], v[\\d:\\d+3]\n.endm\n.macro Qmava d,a,b\nv_mfma_f32_16x16x32_bf16 a[\\d:\\d+3], v[\\a:\\a+3], a[\\b:\\b+3], a[\\d:\\d+3]\n"
".endm\n.macro Qzvvv d,a,b\nv_mfma_f32_16x16x32_bf16 v[\\d:\\d+3], v[\\a:\\a+3], v[\\b:\\b+3], 0\n.endm\n.macro Qzvav d,a,b\nv_mfma_f32_16x16x32_bf16 v[\\d:\\d+3], a[\\a:\\a+3], v[\\b:\\b+3], 0\n.endm\n.macro Qmvav d,a,b\n"
"v_mfma_f32_16x16x32_bf16 v[\\d:\\d+3], a[\\a:\\a+3], v[\\b:\\b+3], v[\\d:\\d+3]\n.endm\n.macro Qcaav d,a,b,c\nv_mfma_f32_16x16x32_bf16 a[\\d:\\d+3], a[\\a:\\a+3], v[\\b:\\b+3], a[\\c:\\c+3]\n.endm\n.macro Qmaav d,a,b\nv_mfma_f32_16x16x32_bf16 a[\\d:\\d+3], a[\\a:\\a+3], v[\\b:\\b+3], a[\\d:\\d+3]\n.endm\n"
".macro Qcvav d,a,b,c\nv_mfma_f32_16x16x32_bf16 v[\\d:\\d+3], a[\\a:\\a+3], v[\\b:\\b+3], v[\\c:\\c+3]\n.endm\n.macro Qmaaa d,a,b\nv_mfma_f32_16x16x32_bf16 a[\\d:\\d+3], a[\\a:\\a+3], a[\\b:\\b+3], a[\\d:\\d+3]\n.endm\n.macro Qdv d,v,o\nds_read_b128 v[\\d:\\d+3], v\\v offset:\\o\n"
".endm\n.p2align 3\ns_and_b32 s33, %3, 4\ns_lshl_b32 s30, s33, 3\ns_load_dwordx8 s[8:15], %2, s30\ns_lshl_b32 s33, s33, 1\ns_load_dwordx4 s[20:23], %2, 0x50\ns_load_dwordx2 s[16:17], %2, s33 offset:0x40\n"
"s_memtime s[80:81]\ns_memrealtime s[82:83]\ns_bfe_u32 s19, %3, 0x30003\ns_and_b32 s39, %3, 3\ns_lshr_b32 %1, %3, 6\ns_lshl2_add_u32 s30, s39, %1\ns_lshl3_add_u32 %0, s30, s19\ns_and_b32 %1, s19, 4\n"
"s_add_u32 s18, s19, %1\ns_lshr_b32 s39, %5, 1\ns_lshl1_add_u32 s18, s18, s39\ns_lshl_b32 %1, %1, 11\ns_mul_i32 s39, s30, 286720\nv_mbcnt_lo_u32_b32 v0, -1, 0\nv_mbcnt_hi_u32_b32 v0, -1, v0\nv_lshlrev_b32 v60, 4, v0\n"
"s_add_u32 m0, %4, 160768\ns_getpc_b64 s[56:57]\n.Lpc0:\ns_and_b32 s56, s56, 0xffffff80\ns_and_b32 s57, s57, 0xffff\ns_mov_b32 s58, .Lend-.Lpc0\ns_mov_b32 s59, 0x20000\ns_and_b32 s94, %0, 31\n"
"s_lshl_b32 s94, s94, 2\ns_add_u32 s94, s94, %5\ns_lshl_b32 s94, s94, 10\nbuffer_load_dwordx4 v60, s[56:59], s94 offen lds\ns_mov_b32 s54, 5242880\ns_mov_b32 s55, 0x20000\ns_mov_b32 s50, 286720\ns_mov_b32 s51, 0x20000\n"
"s_mov_b32 s70, %5\ns_and_b32 s92, s70, 1\ns_lshr_b32 s93, s70, 1\nv_lshrrev_b32 v2, 4, v0\ns_lshl_b32 s68, s92, 18\nv_and_b32 v4, 3, v0\nv_lshlrev_b32 v4, 4, v4\nv_and_b32 v1, 1, v2\n"
"v_lshl_add_u32 v4, v1, 6, v4\nv_lshrrev_b32 v1, 5, v0\nv_lshl_add_u32 v4, v1, 15, v4\nv_bfe_u32 v1, v0, 2, 2\nv_lshl_add_u32 v4, v1, 13, v4\nv_add_u32 v62, s68, v4\ns_lshl_b32 s87, s18, 8\ns_mul_i32 s62, s70, 4128\n"
"s_add_u32 s62, s62, %4\ns_add_u32 s62, s62, 33024\ns_lshl_b32 s69, s92, 1\nv_lshrrev_b32 v3, 5, v0\nv_and_b32 v4, 3, v0\nv_xor_b32 v4, s69, v4\nv_lshlrev_b32 v4, 4, v4\nv_lshl_add_u32 v4, v3, 6, v4\n"
"v_bfe_u32 v1, v0, 2, 3\nv_mul_u32_u24 v64, 1280, v1\nv_add_u32 v64, v64, v4\ns_mul_i32 s86, s93, 143360\ns_mul_i32 s68, s92, 10240\ns_add_u32 s86, s86, s68\ns_mul_i32 s63, s92, 14336\ns_mul_i32 s68, s93, 7168\n"
"s_add_u32 s63, s63, s68\ns_add_u32 s63, s63, %4\ns_add_u32 s63, s63, 99072\ns_lshl_b32 s34, s33, 1\ns_add_u32 s34, s34, s30\ns_mul_hi_u32 s35, s34, 6291456\ns_mul_i32 s34, s34, 6291456\ns_lshl_b32 s36, s30, 5\n"
"s_and_b32 s37, s18, 30\ns_add_u32 s36, s36, s37\ns_lshl_b32 s36, s36, 18\ns_sub_u32 s36, s36, 1024\ns_subb_u32 s37, 0, 0\ns_mul_i32 s38, s30, 573440\ns_mul_i32 s33, s33, 160\ns_add_u32 s38, s38, s33\n"
"s_and_b32 s33, s19, 3\ns_mul_i32 s33, s33, 320\ns_add_u32 s38, s38, s33\ns_mov_b32 s26, 6291456\ns_mov_b32 s27, 0x20000\nv_and_b32 v5, 7, v0\nv_lshlrev_b32 v5, 7, v5\nv_add_u32 v76, 5242880, v5\n"
"s_lshl_b32 s68, s70, 2\ns_add_u32 s68, s68, 4\nv_bfe_u32 v5, v0, 3, 1\nv_mad_u32_u24 v76, v5, s68, v76\ns_mov_b32 s42, 2622464\ns_mov_b32 s43, 0x20000\ns_mov_b32 s46, 2622464\ns_mov_b32 s47, 0x20000\n"
"s_mov_b32 s58, 0\ns_mov_b32 s59, 0x20000\ns_mov_b32 s65, 0\ns_mov_b32 s60, %1\ns_add_u32 s90, s60, 260096\ns_mov_b32 s67, 0\ns_mov_b32 s72, 0x3e0293ee\ns_lshl_b32 s68, s70, 8\n"
"s_lshl_b32 s69, s93, 5\ns_or_b32 s88, s68, s69\nv_bfe_u32 v1, v0, 2, 2\nv_mul_u32_u24 v4, 0x8100, v1\ns_waitcnt lgkmcnt(0)\ns_mov_b32 s52, s10\ns_and_b32 s53, s11, 0xffff\ns_add_u32 m0, s62, 0\n"
"s_add_u32 s66, s87, 0\nbuffer_load_dwordx4 v62, s[52:55], s66 offen lds\ns_add_u32 s66, s87, 64504\nbuffer_load_dwordx4 v62, s[52:55], s66 offen offset:1032 lds\ns_add_u32 s66, s87, 129008\nbuffer_load_dwordx4 v62, s[52:55], s66 offen offset:2064 lds\ns_add_u32 s66, s87, 193512\nbuffer_load_dwordx4 v62, s[52:55], s66 offen offset:3096 lds\n"
"s_add_u32 s48, s8, s39\ns_addc_u32 s49, s9, 0\ns_and_b32 s49, s49, 0xffff\ns_add_u32 m0, s63, 33024\ns_add_u32 s97, s86, 0\nbuffer_load_dwordx4 v64, s[48:51], s97 offen lds\ns_add_u32 s97, s86, 19456\nbuffer_load_dwordx4 v64, s[48:51], s97 offen offset:1024 lds\n"
"s_add_u32 s97, s86, 38912\nbuffer_load_dwordx4 v64, s[48:51], s97 offen offset:2048 lds\ns_add_u32 s97, s86, 58368\nbuffer_load_dwordx4 v64, s[48:51], s97 offen offset:3072 lds\ns_add_u32 m0, s63, 37120\ns_add_u32 s97, s86, 81920\nbuffer_load_dwordx4 v64, s[48:51], s97 offen lds\ns_add_u32 s97, s86, 101376\n"
"buffer_load_dwordx4 v64, s[48:51], s97 offen offset:1024 lds\ns_add_u32 s97, s86, 120832\nbuffer_load_dwordx4 v64, s[48:51], s97 offen offset:2048 lds\ns_add_u32 m0, s62, 16512\ns_add_u32 s66, s87, 128\nbuffer_load_dwordx4 v62, s[52:55], s66 offen lds\ns_add_u32 s20, s20, s34\ns_addc_u32 s21, s21, s35\n"
"s_mov_b32 s24, s20\ns_and_b32 s25, s21, 0xffff\nbuffer_load_dword v65, v76, s[24:27], 0 offen sc1\ns_add_u32 s40, s12, s36\ns_addc_u32 s41, s13, s37\ns_and_b32 s41, s41, 0xffff\ns_add_u32 s44, s14, s36\ns_addc_u32 s45, s15, s37\n"
"s_add_u32 s66, s87, 64632\nbuffer_load_dwordx4 v62, s[52:55], s66 offen offset:1032 lds\ns_and_b32 s45, s45, 0xffff\ns_mov_b32 s56, s20\ns_and_b32 s57, s21, 0xffff\ns_add_u32 s16, s16, -4096\ns_addc_u32 s17, s17, -1\ns_add_u32 s22, s22, s38\n"
"s_addc_u32 s23, s23, 0\nv_and_b32 v1, 3, v0\nv_lshl_or_b32 v4, v1, 4, v4\nv_and_b32 v1, 1, v2\nv_lshl_or_b32 v4, v1, 7, v4\ns_add_u32 s66, s87, 129136\nbuffer_load_dwordx4 v62, s[52:55], s66 offen offset:2064 lds\nv_lshl_or_b32 v4, v3, 6, v4\n"
"v_xor_b32 v4, s88, v4\nv_add_u32 v69, 1024, v4\nv_bfe_u32 v1, v0, 1, 2\nv_mul_u32_u24 v4, 0x8100, v1\nv_and_b32 v1, 9, v0\nv_lshl_or_b32 v4, v1, 4, v4\nv_lshl_or_b32 v4, v2, 5, v4\nv_xor_b32 v4, s68, v4\n"
"s_add_u32 s66, s87, 193640\nbuffer_load_dwordx4 v62, s[52:55], s66 offen offset:3096 lds\nv_add_u32 v72, 1024, v4\ns_lshl_b32 s68, s70, 12\ns_add_u32 s61, s68, %4\ns_mul_i32 s68, s92, 8320\ns_lshl_b32 s69, s93, 12\ns_add_u32 s68, s68, s69\n"
"s_add_u32 s68, s68, %4\ns_add_u32 s64, s68, 16384\ns_lshl_b32 s69, s93, 11\ns_mov_b64 exec, 0\ns_cmp_eq_u32 s70, 0\ns_cbranch_scc1 .Lpg0\ns_cmp_eq_u32 s70, 1\ns_cbranch_scc1 .Lpg1\n"
"s_cmp_eq_u32 s70, 2\ns_cbranch_scc1 .Lpg2\ns_branch .Lpg3\n.Ltr0:\ns_mov_b64 exec, -1\ns_add_u32 s69, s69, %4\nv_bfe_u32 v3, v0, 3, 1\nv_lshlrev_b32 v4, 1, v3\n"
"v_xor_b32 v4, v4, v2\nv_and_b32 v1, 7, v0\nv_lshl_add_u32 v4, v1, 2, v4\nv_lshlrev_b32 v4, 4, v4\nv_bfe_u32 v1, v0, 2, 2\nv_and_b32 v5, 255, v4\ns_add_u32 m0, s63, 0\ns_add_u32 s97, s86, 128\n"
"buffer_load_dwordx4 v64, s[48:51], s97 offen lds\nv_lshl_or_b32 v1, v1, 12, v5\nv_add_u32 v68, s69, v1\ns_mul_i32 s68, s92, 7168\ns_add_u32 s68, s68, %4\ns_add_u32 s68, s68, 99072\nv_mul_u32_u24 v1, 14336, v3\nv_add3_u32 v63, v1, v4, s68\n"
"v_lshrrev_b32 v3, 5, v0\nv_and_b32 v4, 31, v0\ns_add_u32 s97, s86, 19584\nbuffer_load_dwordx4 v64, s[48:51], s97 offen offset:1024 lds\nv_lshlrev_b32 v4, 3, v4\nv_and_b32 v1, 1, v2\nv_lshl_add_u32 v4, v1, 13, v4\nv_lshl_add_u32 v4, v3, 12, v4\n"
"v_add_u32 v4, 16384, v4\nv_add_u32 v70, s69, v4\nv_add_u32 v71, 66048, v70\ns_mul_i32 s68, s93, 8256\ns_add_u32 s68, s68, %4\ns_add_u32 s97, s86, 39040\nbuffer_load_dwordx4 v64, s[48:51], s97 offen offset:2048 lds\ns_add_u32 s68, s68, 33024\n"
"v_mul_u32_u24 v3, 1032, v2\nv_and_b32 v4, 15, v0\nv_lshlrev_b32 v4, 4, v4\nv_add3_u32 v61, v3, v4, s68\ns_lshl_b32 s68, s70, 2\ns_add_u32 s69, %4, 131968\ns_add_u32 s68, s68, s69\nv_mov_b32 v73, s68\n"
"s_add_u32 s97, s86, 58496\nbuffer_load_dwordx4 v64, s[48:51], s97 offen offset:3072 lds\nv_and_b32 v3, 3, v0\nv_lshl_add_u32 v74, v3, 2, s69\nv_mov_b32 v75, 0\ns_mov_b32 s79, -2031\nds_write_b32 v73, v75\ns_waitcnt lgkmcnt(0)\n"
"s_mul_i32 s91, s70, 7168\ns_add_u32 s91, s91, %4\ns_add_u32 s91, s91, 98304\ns_add_u32 m0, s63, 4096\ns_add_u32 s97, s86, 82048\nbuffer_load_dwordx4 v64, s[48:51], s97 offen lds\ns_lshl_b32 s96, s70, 11\ns_add_u32 s96, s96, %4\n"
"v_and_b32 v3, 1, v0\nv_mul_u32_u24 v3, 0xaaaa, v3\nv_or_b32 v77, 0x4444, v3\ns_and_b32 s68, s19, 4\ns_lshl_b32 s28, 15, s68\ns_mov_b32 s29, 0\ns_and_b32 s68, s19, 4\ns_add_u32 s97, s86, 101504\n"
"buffer_load_dwordx4 v64, s[48:51], s97 offen offset:1024 lds\ns_add_u32 s68, s68, s19\ns_lshl_b32 s71, s68, 18\ns_mul_i32 s69, s70, 0x80100\ns_and_b32 s69, s69, 0x100100\ns_add_u32 s68, s71, s69\ns_lshr_b32 s98, %0, 1\ns_xor_b32 s98, s98, %0\n"
"s_lshl_b32 s98, s98, 11\ns_and_b32 s98, s98, 8192\ns_add_u32 s97, s86, 120960\nbuffer_load_dwordx4 v64, s[48:51], s97 offen offset:2048 lds\ns_add_u32 s68, s68, s98\ns_add_u32 s68, s68, 7168\ns_add_u32 s30, s20, s68\ns_addc_u32 s31, s21, 0\n"
"s_mov_b32 s77, 0\nv_bfe_i32 v8, v0, 3, 1\nv_lshlrev_b32 v4, 10, v0\nv_and_b32 v4, 0x6000, v4\nv_lshrrev_b32 v3, 5, v0\nv_lshl_or_b32 v4, v3, 16, v4\nv_and_b32 v3, 0x70, v60\nv_or_b32 v4, v3, v4\n"
"v_or_b32 v8, 1, v8\nv_mov_b32 v9, v8\nv_mov_b32 v10, v8\nv_mov_b32 v11, v8\nbuffer_store_dwordx4 v[8:11], v4, s[24:27], s68 offen offset:0 sc1\ns_nop 1\ns_lshl_b32 s68, s70, 8\nv_lshl_or_b32 v3, v2, 15, v60\n"
"v_xor_b32 v79, s68, v3\ns_getpc_b64 s[100:101]\n.Lpc1:\ns_min_u32 s94, s94, .Lend-.Lpc1-1024\ns_add_u32 s100, s100, s94\ns_addc_u32 s101, s101, 0\ns_mov_b32 s94, 0x7ffff000\ns_mul_i32 s68, %0, 40960\n"
"s_mul_i32 s69, s93, 20480\ns_add_u32 s68, s68, s69\ns_lshl_b32 s69, s92, 10\ns_add_u32 s68, s68, s69\ns_add_u32 s68, s68, 2048\ns_add_u32 s94, s10, s68\ns_addc_u32 s95, s11, 0\ns_and_b32 s95, s95, 0xffff\n"
"s_mov_b32 s75, 17408\n.Lround:\ns_mov_b64 vcc, 0\ns_waitcnt vmcnt(13)\ns_barrier\nQta 112,61,0\nQta 114,61,512\nQda 208,63,33024\n"
"Qda 212,63,34048\nQda 216,63,35072\nQda 220,63,36096\nQda 224,63,37120\nQda 228,63,38144\nQda 232,63,39168\ns_mov_b64 exec, 0\ns_cmp_eq_u32 s70, 0\n"
"s_cbranch_scc1 .Lpg4\ns_cmp_eq_u32 s70, 1\ns_cbranch_scc1 .Lpg5\ns_cmp_eq_u32 s70, 2\ns_cbranch_scc1 .Lpg6\ns_branch .Lpg7\n.Ltr9:\ns_mov_b64 exec, -1\n"
"Qda 176,63,33536\nQta 116,61,4128\nQta 118,61,4640\ns_add_u32 m0, s62, 33024\ns_add_u32 s66, s87, 524288\nbuffer_load_dwordx4 v62, s[52:55], s66 offen lds\nQta 120,61,8\nQta 122,61,520\n"
"Qta 124,61,4136\ns_add_u32 s66, s87, 588792\nbuffer_load_dwordx4 v62, s[52:55], s66 offen offset:1032 lds\nQta 126,61,4648\nQta 128,61,256\nQta 130,61,768\ns_add_u32 s66, s87, 653296\nbuffer_load_dwordx4 v62, s[52:55], s66 offen offset:2064 lds\n"
"Qta 132,61,4384\nQta 134,61,4896\nQta 136,61,264\ns_add_u32 s66, s87, 717800\nbuffer_load_dwordx4 v62, s[52:55], s66 offen offset:3096 lds\nQta 138,61,776\nQta 140,61,4392\nQta 142,61,4904\n"
"s_add_u32 m0, s62, 49536\ns_add_u32 s66, s87, 524416\nbuffer_load_dwordx4 v62, s[52:55], s66 offen lds\ns_waitcnt lgkmcnt(15)\nQzvaa 0,112,208\nQzvaa 4,112,212\ns_add_u32 s66, s87, 588920\nbuffer_load_dwordx4 v62, s[52:55], s66 offen offset:1032 lds\n"
"Qda 180,63,34560\nQzvaa 8,112,216\nQda 184,63,35584\ns_add_u32 s66, s87, 653424\nbuffer_load_dwordx4 v62, s[52:55], s66 offen offset:2064 lds\nQzvaa 12,112,220\nQda 188,63,36608\nQzvaa 16,112,224\n"
"s_add_u32 s66, s87, 717928\nbuffer_load_dwordx4 v62, s[52:55], s66 offen offset:3096 lds\nQda 192,63,37632\nQzvaa 20,112,228\nQda 196,63,38656\nQzvaa 52,112,232\nQda 200,63,39680\nv_max3_f32 v0, |v0|, |v1|, |v2|\n"
"v_max3_f32 v4, |v4|, |v5|, |v6|\nv_max3_f32 v8, |v8|, |v9|, |v10|\nv_max3_f32 v12, |v12|, |v13|, |v14|\nv_max3_f32 v16, |v16|, |v17|, |v18|\nv_max3_f32 v20, |v20|, |v21|, |v22|\ns_nop 0\nv_max3_f32 v52, |v52|, |v53|, |v54|\nv_min3_u32 v56, v0, v4, v8\n"
"v_min3_u32 v56, v56, v12, v16\nv_min3_u32 v56, v56, v20, v52\nv_mov_b32 v57, v56\ns_nop 1\nv_permlane32_swap_b32 v56, v57\nv_max_f32 v56, v56, v57\nv_mov_b32 v57, v56\ns_nop 1\n"
"v_permlane16_swap_b32 v56, v57\nv_max_f32 v56, v56, v57\ns_nop 1\nv_min_f32_dpp v56, v56, v56 row_ror:8 row_mask:0xf bank_mask:0xf\ns_nop 1\nv_min_f32_dpp v56, v56, v56 row_ror:4 row_mask:0xf bank_mask:0xf\ns_nop 1\nv_min_f32_dpp v56, v56, v56 row_ror:2 row_mask:0xf bank_mask:0xf\n"
"s_nop 1\nv_min_f32_dpp v56, v56, v56 row_ror:1 row_mask:0xf bank_mask:0xf\nv_and_b32 v56, 0x7f800000, v56\nv_min_u32 v56, 0x78000000, v56\nv_add_u32 v56, 0x5400000, v56\nv_mov_b32 v57, v56\nv_mov_b64 v[58:59], v[56:57]\nv_mul_f32 v52, -v56, s72\n"
".Lround2:\ns_waitcnt lgkmcnt(6)\nQcvaa 80,112,208,56\nQk 96,96,97\nQk 97,98,99\nQmvaa 80,116,176\ns_add_u32 s68, s71, 0\nQk 98,100,101\n"
"Qcvaa 84,120,208,56\ns_cbranch_vccz .Lst27\nbuffer_store_dwordx4 a[0:3], v79, s[56:59], s68 offen offset:0 sc1\n.Lst27:\nQmvaa 84,124,176\nQk 99,102,103\nv_accvgpr_write_b32 a8, v96\nQcvaa 88,128,208,56\n"
"s_add_u32 s68, s71, 16384\nv_accvgpr_write_b32 a9, v97\nQmvaa 88,132,176\ns_cbranch_vccz .Lst26\nbuffer_store_dwordx4 a[4:7], v79, s[56:59], s68 offen offset:0 sc1\n.Lst26:\nQcvaa 92,136,208,56\nv_accvgpr_write_b32 a10, v98\n"
"v_accvgpr_write_b32 a11, v99\nQmvaa 92,140,176\ns_waitcnt lgkmcnt(0)\nQk 104,104,105\nQcvaa 124,136,212,56\ns_waitcnt vmcnt(16)\ns_barrier\nQmvaa 124,140,180\n"
"Qta 144,61,16512\ns_add_u32 m0, s62, 0\ns_add_u32 s69, s87, 1048576\nQcvaa 120,128,212,56\nbuffer_load_dwordx4 v62, s[52:55], s69 offen lds\nQmvaa 120,132,180\nQta 146,61,17024\nQk 105,106,107\n"
"Qcvaa 116,120,212,56\nQk 106,108,109\nQk 107,110,111\nQmvaa 116,124,180\nQta 148,61,20640\ns_add_u32 s69, s87, 1113080\nQcvaa 112,112,212,56\nbuffer_load_dwordx4 v62, s[52:55], s69 offen offset:1032 lds\n"
"Qmvaa 112,116,180\nQta 150,61,21152\nv_accvgpr_write_b32 a12, v104\nQcvaa 144,112,216,56\nv_accvgpr_write_b32 a13, v105\nv_accvgpr_write_b32 a14, v106\nQmvaa 144,116,184\nQta 152,61,16520\n"
"s_add_u32 s69, s87, 1177584\nQcvaa 148,120,216,56\nbuffer_load_dwordx4 v62, s[52:55], s69 offen offset:2064 lds\nQmvaa 148,124,184\nQta 154,61,17032\nv_accvgpr_write_b32 a15, v107\nQcvaa 152,128,216,56\nQk 136,136,137\n"
"Qk 137,138,139\nQmvaa 152,132,184\nQta 156,61,20648\ns_add_u32 s69, s87, 1242088\nQcvaa 156,136,216,56\nbuffer_load_dwordx4 v62, s[52:55], s69 offen offset:3096 lds\nQmvaa 156,140,184\nQta 158,61,21160\n"
"Qk 138,140,141\nQcvaa 188,136,220,56\nQk 139,142,143\nv_accvgpr_write_b32 a28, v136\nQmvaa 188,140,188\nQta 160,61,16768\ns_add_u32 m0, s63, 33024\ns_add_u32 s88, s86, 256\n"
"Qcvaa 184,128,220,56\nbuffer_load_dwordx4 v64, s[48:51], s88 offen lds\nQmvaa 184,132,188\nQta 162,61,17280\nv_accvgpr_write_b32 a29, v137\nQcvaa 180,120,220,56\nv_accvgpr_write_b32 a30, v138\nv_accvgpr_write_b32 a31, v139\n"
"Qmvaa 180,124,188\nQta 164,61,20896\ns_add_u32 s88, s86, 19712\nQcvaa 176,112,220,56\nbuffer_load_dwordx4 v64, s[48:51], s88 offen offset:1024 lds\nQmvaa 176,116,188\nQta 166,61,21408\nQk 128,128,129\n"
"Qcvaa 208,112,224,56\nQk 129,130,131\nQk 130,132,133\nQmvaa 208,116,192\nQta 168,61,16776\ns_add_u32 s88, s86, 39168\nQcvaa 212,120,224,56\nbuffer_load_dwordx4 v64, s[48:51], s88 offen offset:2048 lds\n"
"Qmvaa 212,124,192\nQta 170,61,17288\nQk 131,134,135\nQcvaa 216,128,224,56\nv_accvgpr_write_b32 a24, v128\nv_accvgpr_write_b32 a25, v129\nQmvaa 216,132,192\nQta 172,61,20904\n"
"s_add_u32 s88, s86, 58624\nQcvaa 220,136,224,56\nbuffer_load_dwordx4 v64, s[48:51], s88 offen offset:3072 lds\nQmvaa 220,140,192\nQta 174,61,21416\nv_accvgpr_write_b32 a26, v130\nQcvaa 252,136,228,56\nv_accvgpr_write_b32 a27, v131\n"
"Qk 160,160,161\nQmvaa 252,140,196\ns_add_u32 m0, s63, 37120\ns_add_u32 s88, s86, 82176\nQk 161,162,163\nQcvaa 248,128,228,56\nbuffer_load_dwordx4 v64, s[48:51], s88 offen lds\nQmvaa 248,132,196\n"
"Qk 162,164,165\nQk 163,166,167\nQcvaa 244,120,228,56\nv_accvgpr_write_b32 a40, v160\nv_accvgpr_write_b32 a41, v161\nQmvaa 244,124,196\ns_add_u32 s88, s86, 101632\nv_accvgpr_write_b32 a42, v162\n"
"Qcvaa 240,112,228,56\nbuffer_load_dwordx4 v64, s[48:51], s88 offen offset:1024 lds\nQmvaa 240,116,196\nv_accvgpr_write_b32 a43, v163\nQk 168,168,169\nQcvaa 16,112,232,56\nQk 169,170,171\nQk 170,172,173\n"
"Qmvaa 16,116,200\ns_add_u32 s88, s86, 121088\nQk 171,174,175\nQcvaa 20,120,232,56\nbuffer_load_dwordx4 v64, s[48:51], s88 offen offset:2048 lds\nQmvaa 20,124,200\nv_accvgpr_write_b32 a44, v168\nv_accvgpr_write_b32 a45, v169\n"
"Qcvaa 24,128,232,56\nv_accvgpr_write_b32 a46, v170\nv_accvgpr_write_b32 a47, v171\nQmvaa 24,132,200\nQk 200,200,201\nQk 201,202,203\nQcvaa 28,136,232,56\nQk 202,204,205\n"
"Qk 203,206,207\nQmvaa 28,140,200\nv_accvgpr_write_b32 a60, v200\nv_accvgpr_write_b32 a61, v201\ns_waitcnt lgkmcnt(0)\nQcvaa 96,144,208,56\nv_accvgpr_write_b32 a62, v202\nv_accvgpr_write_b32 a63, v203\n"
"Qmvaa 96,148,176\ns_add_u32 s68, s71, 0\nQk 192,192,193\nQcvaa 100,152,208,56\ns_cbranch_vccz .Lst25\nbuffer_store_dwordx4 a[16:19], v79, s[56:59], s68 offen offset:1024 sc1\n.Lst25:\nQmvaa 100,156,176\n"
"Qk 193,194,195\nQk 194,196,197\nQcvaa 104,160,208,56\ns_add_u32 s68, s71, 16384\nQk 195,198,199\nQmvaa 104,164,176\ns_cbranch_vccz .Lst24\nbuffer_store_dwordx4 a[20:23], v79, s[56:59], s68 offen offset:1024 sc1\n"
".Lst24:\nQcvaa 108,168,208,56\nv_accvgpr_write_b32 a56, v192\nv_accvgpr_write_b32 a57, v193\nQmvaa 108,172,176\ns_waitcnt lgkmcnt(0)\nv_accvgpr_write_b32 a58, v194\nQcvaa 140,168,212,56\n"
"s_waitcnt vmcnt(15)\ns_barrier\nQmvaa 140,172,180\nQda 208,63,0\ns_add_u32 m0, s62, 16512\ns_add_u32 s69, s87, 1048704\n.Lpg0:\ns_cbranch_execz .Ltr0\n"
"Qcvaa 136,160,212,56\nbuffer_load_dwordx4 v62, s[52:55], s69 offen lds\nQmvaa 136,164,180\nQda 176,63,512\nv_accvgpr_write_b32 a59, v195\nQcvaa 132,152,212,56\nQta 112,61,33024\nQk 224,224,225\n"
"Qmvaa 132,156,180\ns_add_u32 s69, s87, 1113208\nQk 225,226,227\nQcvaa 128,144,212,56\nbuffer_load_dwordx4 v62, s[52:55], s69 offen offset:1032 lds\nQmvaa 128,148,180\nQda 212,63,1024\nQk 226,228,229\n"
"Qcvaa 160,144,216,56\nQda 180,63,1536\nQk 227,230,231\nQmvaa 160,148,184\nQta 114,61,33536\ns_add_u32 s69, s87, 1177712\nQcvaa 164,152,216,56\nbuffer_load_dwordx4 v62, s[52:55], s69 offen offset:2064 lds\n"
"Qmvaa 164,156,184\nQta 116,61,37152\nv_accvgpr_write_b32 a72, v224\nQcvaa 168,160,216,56\nv_accvgpr_write_b32 a73, v225\nv_accvgpr_write_b32 a74, v226\nQmvaa 168,164,184\nQta 118,61,37664\n"
"s_add_u32 s69, s87, 1242216\nQcvaa 172,168,216,56\nbuffer_load_dwordx4 v62, s[52:55], s69 offen offset:3096 lds\nQmvaa 172,172,184\nQda 216,63,2048\nv_accvgpr_write_b32 a75, v227\nQcvaa 204,168,220,56\nQda 184,63,2560\n"
"Qk 232,232,233\nQmvaa 204,172,188\nQta 120,61,33032\ns_add_u32 m0, s61, 0\nQcvaa 200,160,220,56\ns_cbranch_vccnz .Ld0q0\nbuffer_load_dwordx4 v69, s[40:43], s60 offen nt lds\n.Ld0q0:\n"
"Qmvaa 200,164,188\nQta 122,61,33544\nQk 233,234,235\nQcvaa 196,152,220,56\nQk 234,236,237\nQk 235,238,239\nQmvaa 196,156,188\nQta 124,61,37160\n"
"v_accvgpr_write_b32 a76, v232\nQcvaa 192,144,220,56\nQda 220,63,3072\nv_accvgpr_write_b32 a77, v233\nQmvaa 192,148,188\nQda 188,63,3584\nv_accvgpr_write_b32 a78, v234\nQcvaa 224,144,224,56\n"
"Qta 126,61,37672\nv_accvgpr_write_b32 a79, v235\nQmvaa 224,148,192\nQk 32,32,33\nQk 33,34,35\nQcvaa 228,152,224,56\nQta 128,61,33280\nQk 34,36,37\n"
"Qmvaa 228,156,192\nQk 35,38,39\nv_accvgpr_write_b32 a104, v32\nQcvaa 232,160,224,56\nQta 130,61,33792\nv_accvgpr_write_b32 a105, v33\nQmvaa 232,164,192\nv_accvgpr_write_b32 a106, v34\n"
"v_accvgpr_write_b32 a107, v35\nQcvaa 236,168,224,56\nQda 224,63,4096\nQk 40,40,41\nQmvaa 236,172,192\nQda 192,63,4608\nQk 41,42,43\nQcvaa 12,168,228,56\n"
"Qta 132,61,37408\nQk 42,44,45\nQmvaa 12,172,196\nQk 43,46,47\nv_accvgpr_write_b32 a108, v40\nQcvaa 8,160,228,56\nQta 134,61,37920\nv_accvgpr_write_b32 a109, v41\n"
"Qmvaa 8,164,196\nv_accvgpr_write_b32 a110, v42\nv_accvgpr_write_b32 a111, v43\nQcvaa 4,152,228,56\nQta 136,61,33288\nQmvaa 4,156,196\nQcvaa 0,144,228,56\nQda 228,63,5120\n"
"Qmvaa 0,148,196\nQda 196,63,5632\nQcvaa 32,144,232,56\nQta 138,61,33800\nQmvaa 32,148,200\nQcvaa 36,152,232,56\nQta 140,61,37416\nQmvaa 36,156,200\n"
"Qcvaa 40,160,232,56\nQta 142,61,37928\nQmvaa 40,164,200\nQcvaa 44,168,232,56\nQda 232,63,6144\nQmvaa 44,172,200\nQda 200,63,6656\ns_waitcnt lgkmcnt(2)\n"
"Qmvaa 80,112,208\nQmvaa 80,116,176\ns_add_u32 s68, s71, 0\nQmvaa 84,120,208\ns_cbranch_vccz .Lst23\nbuffer_store_dwordx4 a[32:35], v79, s[56:59], s68 offen offset:2048 sc1\n.Lst23:\nQmvaa 84,124,176\n"
"Qmvaa 88,128,208\ns_add_u32 s68, s71, 16384\nQmvaa 88,132,176\ns_cbranch_vccz .Lst22\nbuffer_store_dwordx4 a[36:39], v79, s[56:59], s68 offen offset:2048 sc1\n.Lst22:\nQmvaa 92,136,208\nQmvaa 92,140,176\n"
"s_waitcnt lgkmcnt(0)\nQmvaa 124,136,212\ns_waitcnt vmcnt(16)\ns_barrier\nQmvaa 124,140,180\nQta 144,61,49536\ns_add_u32 m0, s62, 33024\ns_add_u32 s69, s87, 1572864\n"
"Qmvaa 120,128,212\nbuffer_load_dwordx4 v62, s[52:55], s69 offen lds\nQmvaa 120,132,180\nQta 146,61,50048\nQmvaa 116,120,212\ns_add_u32 s69, s87, 1637368\nQmvaa 116,124,180\nbuffer_load_dwordx4 v62, s[52:55], s69 offen offset:1032 lds\n"
"Qmvaa 112,112,212\nQta 148,61,53664\nQmvaa 112,116,180\ns_add_u32 s69, s87, 1701872\nQmvaa 144,112,216\nbuffer_load_dwordx4 v62, s[52:55], s69 offen offset:2064 lds\nQmvaa 144,116,184\nQta 150,61,54176\n"
"Qmvaa 148,120,216\ns_add_u32 s69, s87, 1766376\nQmvaa 148,124,184\nbuffer_load_dwordx4 v62, s[52:55], s69 offen offset:3096 lds\nQmvaa 152,128,216\nQta 152,61,49544\nQmvaa 152,132,184\ns_add_u32 m0, s63, 0\n"
"s_add_u32 s88, s86, 384\nQmvaa 156,136,216\nbuffer_load_dwordx4 v64, s[48:51], s88 offen lds\nQmvaa 156,140,184\nQta 154,61,50056\nQmvaa 188,136,220\ns_add_u32 s88, s86, 19840\nQmvaa 188,140,188\n"
"buffer_load_dwordx4 v64, s[48:51], s88 offen offset:1024 lds\nQmvaa 184,128,220\nQta 156,61,53672\nQmvaa 184,132,188\ns_add_u32 s88, s86, 39296\nQmvaa 180,120,220\nbuffer_load_dwordx4 v64, s[48:51], s88 offen offset:2048 lds\nQmvaa 180,124,188\n"
"Qta 158,61,54184\nQmvaa 176,112,220\ns_add_u32 s88, s86, 58752\nQmvaa 176,116,188\nbuffer_load_dwordx4 v64, s[48:51], s88 offen offset:3072 lds\nQmvaa 208,112,224\nQta 160,61,49792\nQmvaa 208,116,192\n"
"s_add_u32 m0, s63, 4096\ns_add_u32 s88, s86, 82304\nQmvaa 212,120,224\nbuffer_load_dwordx4 v64, s[48:51], s88 offen lds\nQmvaa 212,124,192\nQta 162,61,50304\nQmvaa 216,128,224\ns_add_u32 s88, s86, 101760\n"
"Qmvaa 216,132,192\nbuffer_load_dwordx4 v64, s[48:51], s88 offen offset:1024 lds\nQmvaa 220,136,224\nQta 164,61,53920\nQmvaa 220,140,192\ns_add_u32 s88, s86, 121216\nQmvaa 252,136,228\nbuffer_load_dwordx4 v64, s[48:51], s88 offen offset:2048 lds\n"
"Qmvaa 252,140,196\nQta 166,61,54432\nQmvaa 248,128,228\ns_add_u32 m0, s61, 0\nQmvaa 248,132,196\ns_cbranch_vccnz .Ld0q1\nbuffer_load_dwordx4 v69, s[40:43], s60 offen offset:1024 nt lds\n.Ld0q1:\n"
"Qmvaa 244,120,228\nQta 168,61,49800\nQmvaa 244,124,196\nQmvaa 240,112,228\nQta 170,61,50312\nQmvaa 240,116,196\nQmvaa 16,112,232\nQta 172,61,53928\n"
"Qmvaa 16,116,200\nQmvaa 20,120,232\nQta 174,61,54440\nQmvaa 20,124,200\nQmvaa 24,128,232\nQmvaa 24,132,200\nQmvaa 28,136,232\nQmvaa 28,140,200\n"
"s_waitcnt lgkmcnt(0)\nQmvaa 96,144,208\nQmvaa 96,148,176\ns_add_u32 s68, s71, 0\nQmvaa 100,152,208\ns_cbranch_vccz .Lst21\nbuffer_store_dwordx4 a[48:51], v79, s[56:59], s68 offen offset:3072 sc1\n.Lst21:\n"
"Qmvaa 100,156,176\nQmvaa 104,160,208\ns_add_u32 s68, s71, 16384\nQmvaa 104,164,176\ns_cbranch_vccz .Lst20\nbuffer_store_dwordx4 a[52:55], v79, s[56:59], s68 offen offset:3072 sc1\n.Lst20:\nQmvaa 108,168,208\n"
"Qmvaa 108,172,176\ns_waitcnt lgkmcnt(0)\nQmvaa 140,168,212\ns_waitcnt vmcnt(17)\ns_barrier\nQmvaa 140,172,180\nQda 208,63,33024\ns_add_u32 m0, s62, 49536\n"
"s_add_u32 s69, s87, 1572992\nQmvaa 136,160,212\nbuffer_load_dwordx4 v62, s[52:55], s69 offen lds\nQmvaa 136,164,180\nQda 176,63,33536\nQmvaa 132,152,212\nQta 112,61,0\nQmvaa 132,156,180\n"
"s_add_u32 s69, s87, 1637496\nQmvaa 128,144,212\nbuffer_load_dwordx4 v62, s[52:55], s69 offen offset:1032 lds\nQmvaa 128,148,180\nQda 212,63,34048\nQmvaa 160,144,216\nQda 180,63,34560\nQmvaa 160,148,184\n"
"Qta 114,61,512\ns_add_u32 s69, s87, 1702000\nQmvaa 164,152,216\nbuffer_load_dwordx4 v62, s[52:55], s69 offen offset:2064 lds\nQmvaa 164,156,184\nQta 116,61,4128\nQmvaa 168,160,216\nQmvaa 168,164,184\n"
"Qta 118,61,4640\ns_add_u32 s69, s87, 1766504\nQmvaa 172,168,216\nbuffer_load_dwordx4 v62, s[52:55], s69 offen offset:3096 lds\nQmvaa 172,172,184\nQda 216,63,35072\nQmvaa 204,168,220\nQda 184,63,35584\n"
"Qmvaa 204,172,188\nQta 120,61,8\nQmvaa 200,160,220\nQmvaa 200,164,188\nQta 122,61,520\nQmvaa 196,152,220\nQmvaa 196,156,188\nQta 124,61,4136\n"
"Qmvaa 192,144,220\nQda 220,63,36096\nQmvaa 192,148,188\nQda 188,63,36608\nQmvaa 224,144,224\nQta 126,61,4648\nQmvaa 224,148,192\nQmvaa 228,152,224\n"
"Qta 128,61,256\nQmvaa 228,156,192\nQmvaa 232,160,224\nQta 130,61,768\nQmvaa 232,164,192\nQmvaa 236,168,224\nQda 224,63,37120\nQmvaa 236,172,192\n"
"Qda 192,63,37632\nQmvaa 12,168,228\nQta 132,61,4384\nQmvaa 12,172,196\nQmvaa 8,160,228\nQta 134,61,4896\nQmvaa 8,164,196\nQmvaa 4,152,228\n"
"Qta 136,61,264\nQmvaa 4,156,196\nQmvaa 0,144,228\nQda 228,63,38144\nQmvaa 0,148,196\nQda 196,63,38656\nQmvaa 32,144,232\nQta 138,61,776\n"
"Qmvaa 32,148,200\nQmvaa 36,152,232\nQta 140,61,4392\nQmvaa 36,156,200\nQmvaa 40,160,232\nQta 142,61,4904\nQmvaa 40,164,200\nQmvaa 44,168,232\n"
"Qda 232,63,39168\nQmvaa 44,172,200\nQda 200,63,39680\ns_waitcnt lgkmcnt(2)\nQmvaa 80,112,208\nQmvaa 80,116,176\ns_add_u32 s68, s71, 4096\nQmvaa 84,120,208\n"
"s_cbranch_vccz .Lst19\nbuffer_store_dwordx4 a[64:67], v79, s[56:59], s68 offen offset:0 sc1\n.Lst19:\nQmvaa 84,124,176\nQmvaa 88,128,208\ns_add_u32 s68, s71, 20480\nQmvaa 88,132,176\ns_cbranch_vccz .Lst18\n"
"buffer_store_dwordx4 a[68:71], v79, s[56:59], s68 offen offset:0 sc1\n.Lst18:\nQmvaa 92,136,208\nQmvaa 92,140,176\ns_waitcnt lgkmcnt(0)\nQmvaa 124,136,212\ns_waitcnt vmcnt(17)\ns_barrier\n"
"Qmvaa 124,140,180\nQta 144,61,16512\ns_add_u32 m0, s62, 0\ns_add_u32 s69, s87, 2097152\nQmvaa 120,128,212\nbuffer_load_dwordx4 v62, s[52:55], s69 offen lds\nQmvaa 120,132,180\nQta 146,61,17024\n"
"Qmvaa 116,120,212\ns_add_u32 s69, s87, 2161656\nQmvaa 116,124,180\nbuffer_load_dwordx4 v62, s[52:55], s69 offen offset:1032 lds\nQmvaa 112,112,212\nQta 148,61,20640\nQmvaa 112,116,180\ns_add_u32 s69, s87, 2226160\n"
"Qmvaa 144,112,216\nbuffer_load_dwordx4 v62, s[52:55], s69 offen offset:2064 lds\nQmvaa 144,116,184\nQta 150,61,21152\nQmvaa 148,120,216\ns_add_u32 s69, s87, 2290664\nQmvaa 148,124,184\nbuffer_load_dwordx4 v62, s[52:55], s69 offen offset:3096 lds\n"
"Qmvaa 152,128,216\nQta 152,61,16520\nQmvaa 152,132,184\ns_add_u32 m0, s63, 33024\ns_add_u32 s88, s86, 512\nQmvaa 156,136,216\nbuffer_load_dwordx4 v64, s[48:51], s88 offen lds\nQmvaa 156,140,184\n"
"Qta 154,61,17032\nQmvaa 188,136,220\ns_add_u32 s88, s86, 19968\nQmvaa 188,140,188\nbuffer_load_dwordx4 v64, s[48:51], s88 offen offset:1024 lds\nQmvaa 184,128,220\nQta 156,61,20648\nQmvaa 184,132,188\n"
"s_add_u32 s88, s86, 39424\nQmvaa 180,120,220\nbuffer_load_dwordx4 v64, s[48:51], s88 offen offset:2048 lds\nQmvaa 180,124,188\nQta 158,61,21160\nQmvaa 176,112,220\ns_add_u32 s88, s86, 58880\nQmvaa 176,116,188\n"
"buffer_load_dwordx4 v64, s[48:51], s88 offen offset:3072 lds\nQmvaa 208,112,224\nQta 160,61,16768\nQmvaa 208,116,192\ns_add_u32 m0, s63, 37120\ns_add_u32 s88, s86, 82432\nQmvaa 212,120,224\nbuffer_load_dwordx4 v64, s[48:51], s88 offen lds\n"
"Qmvaa 212,124,192\nQta 162,61,17280\nQmvaa 216,128,224\ns_add_u32 s88, s86, 101888\nQmvaa 216,132,192\nbuffer_load_dwordx4 v64, s[48:51], s88 offen offset:1024 lds\nQmvaa 220,136,224\nQta 164,61,20896\n"
"Qmvaa 220,140,192\ns_add_u32 s88, s86, 121344\nQmvaa 252,136,228\nbuffer_load_dwordx4 v64, s[48:51], s88 offen offset:2048 lds\nQmvaa 252,140,196\nQta 166,61,21408\nQmvaa 248,128,228\ns_add_u32 m0, s61, 0\n"
"Qmvaa 248,132,196\ns_cbranch_vccnz .Ld0q2\nbuffer_load_dwordx4 v69, s[40:43], s90 offen offset:2048 nt lds\n.Ld0q2:\nQmvaa 244,120,228\nQta 168,61,16776\nQmvaa 244,124,196\nQmvaa 240,112,228\n"
"Qta 170,61,17288\nQmvaa 240,116,196\nQmvaa 16,112,232\nQta 172,61,20904\nQmvaa 16,116,200\nQmvaa 20,120,232\nQta 174,61,21416\nQmvaa 20,124,200\n"
"Qmvaa 24,128,232\nQmvaa 24,132,200\nQmvaa 28,136,232\nQmvaa 28,140,200\ns_waitcnt lgkmcnt(0)\nQmvaa 96,144,208\nQmvaa 96,148,176\ns_add_u32 s68, s71, 4096\n"
"Qmvaa 100,152,208\ns_cbranch_vccz .Lst17\nbuffer_store_dwordx4 a[80:83], v79, s[56:59], s68 offen offset:1024 sc1\n.Lst17:\nQmvaa 100,156,176\nQmvaa 104,160,208\ns_add_u32 s68, s71, 20480\nQmvaa 104,164,176\n"
"s_cbranch_vccz .Lst16\nbuffer_store_dwordx4 a[84:87], v79, s[56:59], s68 offen offset:1024 sc1\n.Lst16:\nQmvaa 108,168,208\nQmvaa 108,172,176\ns_waitcnt lgkmcnt(0)\nQmvaa 140,168,212\ns_waitcnt vmcnt(17)\n"
"s_barrier\nQmvaa 140,172,180\nQda 208,63,0\ns_add_u32 m0, s62, 16512\ns_add_u32 s69, s87, 2097280\nQmvaa 136,160,212\nbuffer_load_dwordx4 v62, s[52:55], s69 offen lds\nQmvaa 136,164,180\n"
"Qda 176,63,512\nQmvaa 132,152,212\nQta 112,61,33024\nQmvaa 132,156,180\ns_add_u32 s69, s87, 2161784\nQmvaa 128,144,212\nbuffer_load_dwordx4 v62, s[52:55], s69 offen offset:1032 lds\nQmvaa 128,148,180\n"
"Qda 212,63,1024\nQmvaa 160,144,216\nQda 180,63,1536\nQmvaa 160,148,184\nQta 114,61,33536\ns_add_u32 s69, s87, 2226288\nQmvaa 164,152,216\nbuffer_load_dwordx4 v62, s[52:55], s69 offen offset:2064 lds\n"
"Qmvaa 164,156,184\nQta 116,61,37152\nQmvaa 168,160,216\nQmvaa 168,164,184\nQta 118,61,37664\ns_add_u32 s69, s87, 2290792\nQmvaa 172,168,216\nbuffer_load_dwordx4 v62, s[52:55], s69 offen offset:3096 lds\n"
"Qmvaa 172,172,184\nQda 216,63,2048\nQmvaa 204,168,220\nQda 184,63,2560\nQmvaa 204,172,188\nQta 120,61,33032\ns_add_u32 m0, s61, 0\nQmvaa 200,160,220\n"
"s_cbranch_vccnz .Ld0q3\nbuffer_load_dwordx4 v69, s[40:43], s90 offen offset:3072 nt lds\n.Ld0q3:\nQmvaa 200,164,188\nQta 122,61,33544\nQmvaa 196,152,220\nQmvaa 196,156,188\nQta 124,61,37160\n"
"Qmvaa 192,144,220\nQda 220,63,3072\nQmvaa 192,148,188\nQda 188,63,3584\nQmvaa 224,144,224\nQta 126,61,37672\nQmvaa 224,148,192\nQmvaa 228,152,224\n"
"Qta 128,61,33280\nQmvaa 228,156,192\nQmvaa 232,160,224\nQta 130,61,33792\nQmvaa 232,164,192\nQmvaa 236,168,224\nQda 224,63,4096\nQmvaa 236,172,192\n"
"Qda 192,63,4608\nQmvaa 12,168,228\nQta 132,61,37408\nQmvaa 12,172,196\nQmvaa 8,160,228\n.Lpg1:\ns_cbranch_execz .Ltr0\nQta 134,61,37920\n"
"Qmvaa 8,164,196\nQmvaa 4,152,228\nQta 136,61,33288\nQmvaa 4,156,196\nQmvaa 0,144,228\nQda 228,63,5120\nQmvaa 0,148,196\nQda 196,63,5632\n"
"Qmvaa 32,144,232\nQta 138,61,33800\nQmvaa 32,148,200\nQmvaa 36,152,232\nQta 140,61,37416\nQmvaa 36,156,200\nQmvaa 40,160,232\nQta 142,61,37928\n"
"Qmvaa 40,164,200\nQmvaa 44,168,232\nQda 232,63,6144\nQmvaa 44,172,200\nQda 200,63,6656\ns_waitcnt lgkmcnt(2)\nQmvaa 80,112,208\nQmvaa 80,116,176\n"
"s_add_u32 s68, s71, 135168\nQmvaa 84,120,208\ns_cbranch_vccz .Lst15\nbuffer_store_dwordx4 a[88:91], v79, s[56:59], s68 offen offset:1024 sc1\n.Lst15:\nQmvaa 84,124,176\nQmvaa 88,128,208\ns_add_u32 s68, s71, 151552\n"
"Qmvaa 88,132,176\ns_cbranch_vccz .Lst14\nbuffer_store_dwordx4 a[92:95], v79, s[56:59], s68 offen offset:1024 sc1\n.Lst14:\nQmvaa 92,136,208\nQmvaa 92,140,176\ns_waitcnt lgkmcnt(0)\nQmvaa 124,136,212\n"
"s_waitcnt vmcnt(17)\ns_barrier\nQmvaa 124,140,180\nQta 144,61,49536\ns_add_u32 m0, s62, 33024\ns_add_u32 s69, s87, 2621440\nQmvaa 120,128,212\nbuffer_load_dwordx4 v62, s[52:55], s69 offen lds\n"
"Qmvaa 120,132,180\nQta 146,61,50048\ns_mov_b32 s89, 0\nQmvaa 116,120,212\ns_cbranch_vccnz .Lpb0\ns_load_dword s89, s[30:31], 0x0\n.Lpb0:\nQmvaa 116,124,180\n"
"Qta 148,61,53664\ns_add_u32 s69, s87, 2685944\nQmvaa 112,112,212\nbuffer_load_dwordx4 v62, s[52:55], s69 offen offset:1032 lds\nQmvaa 112,116,180\nQta 150,61,54176\nQmvaa 144,112,216\ns_add_u32 s77, s77, s89\n"
"Qmvaa 144,116,184\nQta 152,61,49544\ns_add_u32 s69, s87, 2750448\nQmvaa 148,120,216\nbuffer_load_dwordx4 v62, s[52:55], s69 offen offset:2064 lds\nQmvaa 148,124,184\nQta 154,61,50056\nQmvaa 152,128,216\n"
"s_add_u32 s77, s77, s89\nQmvaa 152,132,184\nQta 156,61,53672\ns_add_u32 s69, s87, 2814952\nQmvaa 156,136,216\nbuffer_load_dwordx4 v62, s[52:55], s69 offen offset:3096 lds\nQmvaa 156,140,184\nQta 158,61,54184\n"
"Qmvaa 188,136,220\ns_add_u32 s77, s77, s89\nQmvaa 188,140,188\nQta 160,61,49792\ns_add_u32 m0, s63, 0\ns_add_u32 s88, s86, 640\nQmvaa 184,128,220\nbuffer_load_dwordx4 v64, s[48:51], s88 offen lds\n"
"Qmvaa 184,132,188\nQta 162,61,50304\nQmvaa 180,120,220\ns_add_u32 s77, s77, s89\nQmvaa 180,124,188\nQta 164,61,53920\ns_add_u32 s88, s86, 20096\nQmvaa 176,112,220\n"
"buffer_load_dwordx4 v64, s[48:51], s88 offen offset:1024 lds\nQmvaa 176,116,188\nQta 166,61,54432\nQmvaa 208,112,224\ns_add_u32 s77, s77, s89\nQmvaa 208,116,192\nQta 168,61,49800\ns_add_u32 s88, s86, 39552\n"
"Qmvaa 212,120,224\nbuffer_load_dwordx4 v64, s[48:51], s88 offen offset:2048 lds\nQmvaa 212,124,192\nQta 170,61,50312\nQmvaa 216,128,224\ns_add_u32 s77, s77, s89\nQmvaa 216,132,192\nQta 172,61,53928\n"
"s_add_u32 s88, s86, 59008\nQmvaa 220,136,224\nbuffer_load_dwordx4 v64, s[48:51], s88 offen offset:3072 lds\nQmvaa 220,140,192\nQta 174,61,54440\nQmvaa 252,136,228\ns_add_u32 s77, s77, s89\nQmvaa 252,140,196\n"
"s_add_u32 m0, s63, 4096\ns_add_u32 s88, s86, 82560\nQmvaa 248,128,228\nbuffer_load_dwordx4 v64, s[48:51], s88 offen lds\nQmvaa 248,132,196\nQmvaa 244,120,228\ns_add_u32 s77, s77, s89\nQmvaa 244,124,196\n"
"s_add_u32 s88, s86, 102016\nQmvaa 240,112,228\nbuffer_load_dwordx4 v64, s[48:51], s88 offen offset:1024 lds\nQmvaa 240,116,196\nQmvaa 16,112,232\ns_add_u32 s77, s77, s89\nQmvaa 16,116,200\ns_add_u32 s88, s86, 121472\n"
"Qmvaa 20,120,232\nbuffer_load_dwordx4 v64, s[48:51], s88 offen offset:2048 lds\nQmvaa 20,124,200\nQmvaa 24,128,232\ns_add_u32 s77, s77, s89\nQmvaa 24,132,200\nQmvaa 28,136,232\nQmvaa 28,140,200\n"
"s_waitcnt lgkmcnt(0)\nQmvaa 96,144,208\nQmvaa 96,148,176\ns_add_u32 s68, s71, 4096\nQmvaa 100,152,208\ns_cbranch_vccz .Lst13\nbuffer_store_dwordx4 a[96:99], v79, s[56:59], s68 offen offset:2048 sc1\n.Lst13:\n"
"Qmvaa 100,156,176\nQmvaa 104,160,208\ns_add_u32 s68, s71, 20480\nQmvaa 104,164,176\ns_cbranch_vccz .Lst12\nbuffer_store_dwordx4 a[100:103], v79, s[56:59], s68 offen offset:2048 sc1\n.Lst12:\nQmvaa 108,168,208\n"
"Qmvaa 108,172,176\ns_waitcnt lgkmcnt(0)\nQmvaa 140,168,212\ns_waitcnt vmcnt(17)\ns_barrier\nQmvaa 140,172,180\nQda 208,63,33024\ns_add_u32 m0, s62, 49536\n"
"s_add_u32 s69, s87, 2621568\nQmvaa 136,160,212\nbuffer_load_dwordx4 v62, s[52:55], s69 offen lds\nQmvaa 136,164,180\nQda 176,63,33536\nQmvaa 132,152,212\nQta 112,61,0\nQmvaa 132,156,180\n"
"s_add_u32 s69, s87, 2686072\nQmvaa 128,144,212\nbuffer_load_dwordx4 v62, s[52:55], s69 offen offset:1032 lds\nQmvaa 128,148,180\nQda 212,63,34048\nQmvaa 160,144,216\nQda 180,63,34560\nQmvaa 160,148,184\n"
"Qta 114,61,512\ns_add_u32 s69, s87, 2750576\nQmvaa 164,152,216\nbuffer_load_dwordx4 v62, s[52:55], s69 offen offset:2064 lds\nQmvaa 164,156,184\nQta 116,61,4128\nQmvaa 168,160,216\nQmvaa 168,164,184\n"
"Qta 118,61,4640\ns_add_u32 s69, s87, 2815080\nQmvaa 172,168,216\nbuffer_load_dwordx4 v62, s[52:55], s69 offen offset:3096 lds\nQmvaa 172,172,184\nQda 216,63,35072\nQmvaa 204,168,220\nQda 184,63,35584\n"
"Qmvaa 204,172,188\nQta 120,61,8\ns_add_u32 m0, s64, 0\nQmvaa 200,160,220\ns_cbranch_vccnz .Ld0q4\nbuffer_load_dwordx4 v72, s[44:47], s60 offen nt lds\n.Ld0q4:\nQmvaa 200,164,188\n"
"Qta 122,61,520\nQmvaa 196,152,220\nQmvaa 196,156,188\nQta 124,61,4136\nQmvaa 192,144,220\nQda 220,63,36096\nQmvaa 192,148,188\nQda 188,63,36608\n"
"Qmvaa 224,144,224\nQta 126,61,4648\nQmvaa 224,148,192\nQmvaa 228,152,224\nQta 128,61,256\nQmvaa 228,156,192\nQmvaa 232,160,224\nQta 130,61,768\n"
"Qmvaa 232,164,192\nQmvaa 236,168,224\nQda 224,63,37120\nQmvaa 236,172,192\nQda 192,63,37632\nQmvaa 12,168,228\nQta 132,61,4384\nQmvaa 12,172,196\n"
"Qmvaa 8,160,228\nQta 134,61,4896\nQmvaa 8,164,196\nQmvaa 4,152,228\nQta 136,61,264\nQmvaa 4,156,196\nQmvaa 0,144,228\nQda 228,63,38144\n"
"Qmvaa 0,148,196\nQda 196,63,38656\nQmvaa 32,144,232\nQta 138,61,776\nQmvaa 32,148,200\nQmvaa 36,152,232\nQta 140,61,4392\nQmvaa 36,156,200\n"
"Qmvaa 40,160,232\nQta 142,61,4904\nQmvaa 40,164,200\nQmvaa 44,168,232\nQda 232,63,39168\nQmvaa 44,172,200\nQda 200,63,39680\ns_waitcnt lgkmcnt(2)\n"
"Qmvaa 80,112,208\nQmvaa 80,116,176\ns_add_u32 s68, s71, 131072\nQmvaa 84,120,208\ns_cbranch_vccz .Lst11\nbuffer_store_dwordx4 a[8:11], v79, s[56:59], s68 offen offset:0 sc1\n.Lst11:\nQmvaa 84,124,176\n"
"Qmvaa 88,128,208\ns_add_u32 s68, s71, 147456\nQmvaa 88,132,176\ns_cbranch_vccz .Lst10\nbuffer_store_dwordx4 a[12:15], v79, s[56:59], s68 offen offset:0 sc1\n.Lst10:\nQmvaa 92,136,208\nQmvaa 92,140,176\n"
"s_waitcnt lgkmcnt(0)\nQmvaa 124,136,212\ns_waitcnt vmcnt(17)\ns_barrier\nQmvaa 124,140,180\nQta 144,61,16512\ns_add_u32 m0, s62, 0\ns_add_u32 s69, s87, 3145728\n"
"Qmvaa 120,128,212\nbuffer_load_dwordx4 v62, s[52:55], s69 offen lds\nQmvaa 120,132,180\nQta 146,61,17024\ns_mov_b32 s89, 0\nQmvaa 116,120,212\ns_cbranch_vccnz .Lpb1\ns_load_dword s89, s[30:31], 0x2000\n"
".Lpb1:\nQmvaa 116,124,180\nQta 148,61,20640\ns_add_u32 s69, s87, 3210232\nQmvaa 112,112,212\nbuffer_load_dwordx4 v62, s[52:55], s69 offen offset:1032 lds\nQmvaa 112,116,180\nQta 150,61,21152\n"
"Qmvaa 144,112,216\ns_add_u32 s77, s77, s89\nQmvaa 144,116,184\nQta 152,61,16520\ns_add_u32 s69, s87, 3274736\nQmvaa 148,120,216\nbuffer_load_dwordx4 v62, s[52:55], s69 offen offset:2064 lds\nQmvaa 148,124,184\n"
"Qta 154,61,17032\nQmvaa 152,128,216\ns_add_u32 s77, s77, s89\nQmvaa 152,132,184\nQta 156,61,20648\ns_add_u32 s69, s87, 3339240\nQmvaa 156,136,216\nbuffer_load_dwordx4 v62, s[52:55], s69 offen offset:3096 lds\n"
"Qmvaa 156,140,184\nQta 158,61,21160\nQmvaa 188,136,220\ns_add_u32 s77, s77, s89\nQmvaa 188,140,188\nQta 160,61,16768\ns_add_u32 m0, s63, 33024\ns_add_u32 s88, s86, 768\n"
"Qmvaa 184,128,220\nbuffer_load_dwordx4 v64, s[48:51], s88 offen lds\nQmvaa 184,132,188\nQta 162,61,17280\nQmvaa 180,120,220\ns_add_u32 s77, s77, s89\nQmvaa 180,124,188\nQta 164,61,20896\n"
"s_add_u32 s88, s86, 20224\nQmvaa 176,112,220\nbuffer_load_dwordx4 v64, s[48:51], s88 offen offset:1024 lds\nQmvaa 176,116,188\nQta 166,61,21408\nQmvaa 208,112,224\ns_add_u32 s77, s77, s89\nQmvaa 208,116,192\n"
"Qta 168,61,16776\ns_add_u32 s88, s86, 39680\nQmvaa 212,120,224\nbuffer_load_dwordx4 v64, s[48:51], s88 offen offset:2048 lds\nQmvaa 212,124,192\nQta 170,61,17288\nQmvaa 216,128,224\ns_add_u32 s77, s77, s89\n"
"Qmvaa 216,132,192\nQta 172,61,20904\ns_add_u32 s88, s86, 59136\nQmvaa 220,136,224\nbuffer_load_dwordx4 v64, s[48:51], s88 offen offset:3072 lds\nQmvaa 220,140,192\nQta 174,61,21416\nQmvaa 252,136,228\n"
"s_add_u32 s77, s77, s89\nQmvaa 252,140,196\ns_add_u32 m0, s63, 37120\ns_add_u32 s88, s86, 82688\nQmvaa 248,128,228\nbuffer_load_dwordx4 v64, s[48:51], s88 offen lds\nQmvaa 248,132,196\nQmvaa 244,120,228\n"
"s_add_u32 s77, s77, s89\nQmvaa 244,124,196\ns_add_u32 s88, s86, 102144\nQmvaa 240,112,228\nbuffer_load_dwordx4 v64, s[48:51], s88 offen offset:1024 lds\nQmvaa 240,116,196\nQmvaa 16,112,232\ns_add_u32 s77, s77, s89\n"
"Qmvaa 16,116,200\ns_add_u32 s88, s86, 121600\nQmvaa 20,120,232\nbuffer_load_dwordx4 v64, s[48:51], s88 offen offset:2048 lds\nQmvaa 20,124,200\nQmvaa 24,128,232\ns_add_u32 s77, s77, s89\nQmvaa 24,132,200\n"
"Qmvaa 28,136,232\nQmvaa 28,140,200\ns_waitcnt lgkmcnt(0)\nQmvaa 96,144,208\nQmvaa 96,148,176\ns_add_u32 s68, s71, 131072\nQmvaa 100,152,208\ns_cbranch_vccz .Lst9\n"
"buffer_store_dwordx4 a[24:27], v79, s[56:59], s68 offen offset:1024 sc1\n.Lst9:\nQmvaa 100,156,176\nQmvaa 104,160,208\ns_add_u32 s68, s71, 147456\nQmvaa 104,164,176\ns_cbranch_vccz .Lst8\nbuffer_store_dwordx4 a[28:31], v79, s[56:59], s68 offen offset:1024 sc1\n"
".Lst8:\nQmvaa 108,168,208\nQmvaa 108,172,176\ns_waitcnt lgkmcnt(0)\nQmvaa 140,168,212\ns_waitcnt vmcnt(16)\ns_barrier\nQmvaa 140,172,180\n"
"Qda 208,63,0\ns_add_u32 m0, s62, 16512\ns_add_u32 s69, s87, 3145856\nQmvaa 136,160,212\nbuffer_load_dwordx4 v62, s[52:55], s69 offen lds\nQmvaa 136,164,180\nQda 176,63,512\nQmvaa 132,152,212\n"
"Qta 112,61,33024\nQmvaa 132,156,180\ns_add_u32 s69, s87, 3210360\nQmvaa 128,144,212\nbuffer_load_dwordx4 v62, s[52:55], s69 offen offset:1032 lds\nQmvaa 128,148,180\nQda 212,63,1024\nQmvaa 160,144,216\n"
"Qda 180,63,1536\nQmvaa 160,148,184\nQta 114,61,33536\ns_add_u32 s69, s87, 3274864\nQmvaa 164,152,216\nbuffer_load_dwordx4 v62, s[52:55], s69 offen offset:2064 lds\nQmvaa 164,156,184\nQta 116,61,37152\n"
"Qmvaa 168,160,216\nQmvaa 168,164,184\nQta 118,61,37664\ns_add_u32 s69, s87, 3339368\nQmvaa 172,168,216\nbuffer_load_dwordx4 v62, s[52:55], s69 offen offset:3096 lds\nQmvaa 172,172,184\nQda 216,63,2048\n"
"Qmvaa 204,168,220\nQda 184,63,2560\nQmvaa 204,172,188\nQta 120,61,33032\ns_add_u32 m0, s64, 0\nQmvaa 200,160,220\ns_cbranch_vccnz .Ld0q5\nbuffer_load_dwordx4 v72, s[44:47], s60 offen offset:1024 nt lds\n"
".Ld0q5:\nQmvaa 200,164,188\nQta 122,61,33544\nQmvaa 196,152,220\nQmvaa 196,156,188\nQta 124,61,37160\nQmvaa 192,144,220\nQda 220,63,3072\n"
"Qmvaa 192,148,188\nQda 188,63,3584\nQmvaa 224,144,224\nQta 126,61,37672\nQmvaa 224,148,192\nQmvaa 228,152,224\nQta 128,61,33280\nQmvaa 228,156,192\n"
"Qmvaa 232,160,224\nQta 130,61,33792\nQmvaa 232,164,192\nQmvaa 236,168,224\nQda 224,63,4096\nQmvaa 236,172,192\nQda 192,63,4608\nQmvaa 12,168,228\n"
"Qta 132,61,37408\nQmvaa 12,172,196\nQmvaa 8,160,228\nQta 134,61,37920\nQmvaa 8,164,196\nQmvaa 4,152,228\nQta 136,61,33288\nQmvaa 4,156,196\n"
"Qmvaa 0,144,228\nQda 228,63,5120\nQmvaa 0,148,196\nQda 196,63,5632\nQmvaa 32,144,232\nQta 138,61,33800\nQmvaa 32,148,200\nQmvaa 36,152,232\n"
"Qta 140,61,37416\nQmvaa 36,156,200\nQmvaa 40,160,232\nQta 142,61,37928\nQmvaa 40,164,200\nQmvaa 44,168,232\nQda 232,63,6144\nQmvaa 44,172,200\n"
"Qda 200,63,6656\ns_waitcnt lgkmcnt(2)\nQmvaa 80,112,208\nQmvaa 80,116,176\ns_add_u32 s68, s71, 131072\nQmvaa 84,120,208\ns_cbranch_vccz .Lst7\nbuffer_store_dwordx4 a[40:43], v79, s[56:59], s68 offen offset:2048 sc1\n"
".Lst7:\nQmvaa 84,124,176\nQmvaa 88,128,208\ns_add_u32 s68, s71, 147456\nQmvaa 88,132,176\ns_cbranch_vccz .Lst6\nbuffer_store_dwordx4 a[44:47], v79, s[56:59], s68 offen offset:2048 sc1\n.Lst6:\n"
"Qmvaa 92,136,208\nQmvaa 92,140,176\ns_waitcnt lgkmcnt(0)\nQmvaa 124,136,212\ns_waitcnt vmcnt(17)\ns_barrier\nQmvaa 124,140,180\nQta 144,61,49536\n"
"s_add_u32 m0, s62, 33024\ns_add_u32 s69, s87, 3670016\nQmvaa 120,128,212\nbuffer_load_dwordx4 v62, s[52:55], s69 offen lds\nQmvaa 120,132,180\nQta 146,61,50048\ns_mov_b32 s89, 0\nQmvaa 116,120,212\n"
"s_cbranch_vccnz .Lpb2\ns_load_dword s89, s[30:31], 0x6000\n.Lpb2:\nQmvaa 116,124,180\nQta 148,61,53664\ns_add_u32 s69, s87, 3734520\nQmvaa 112,112,212\nbuffer_load_dwordx4 v62, s[52:55], s69 offen offset:1032 lds\n"
"Qmvaa 112,116,180\nQta 150,61,54176\nQmvaa 144,112,216\ns_add_u32 s77, s77, s89\nQmvaa 144,116,184\nQta 152,61,49544\ns_add_u32 s69, s87, 3799024\nQmvaa 148,120,216\n"
"buffer_load_dwordx4 v62, s[52:55], s69 offen offset:2064 lds\nQmvaa 148,124,184\nQta 154,61,50056\nQmvaa 152,128,216\ns_add_u32 s77, s77, s89\nQmvaa 152,132,184\nQta 156,61,53672\ns_add_u32 s69, s87, 3863528\n"
"Qmvaa 156,136,216\nbuffer_load_dwordx4 v62, s[52:55], s69 offen offset:3096 lds\nQmvaa 156,140,184\nQta 158,61,54184\nQmvaa 188,136,220\ns_add_u32 s77, s77, s89\nQmvaa 188,140,188\nQta 160,61,49792\n"
"s_add_u32 m0, s63, 0\ns_add_u32 s88, s86, 896\nQmvaa 184,128,220\nbuffer_load_dwordx4 v64, s[48:51], s88 offen lds\nQmvaa 184,132,188\nQta 162,61,50304\nQmvaa 180,120,220\n.Lpg2:\n"
"s_cbranch_execz .Ltr0\ns_add_u32 s77, s77, s89\nQmvaa 180,124,188\nQta 164,61,53920\ns_add_u32 s88, s86, 20352\nQmvaa 176,112,220\nbuffer_load_dwordx4 v64, s[48:51], s88 offen offset:1024 lds\nQmvaa 176,116,188\n"
"Qta 166,61,54432\nQmvaa 208,112,224\ns_add_u32 s77, s77, s89\nQmvaa 208,116,192\nQta 168,61,49800\ns_add_u32 s88, s86, 39808\nQmvaa 212,120,224\nbuffer_load_dwordx4 v64, s[48:51], s88 offen offset:2048 lds\n"
"Qmvaa 212,124,192\nQta 170,61,50312\nQmvaa 216,128,224\ns_add_u32 s77, s77, s89\nQmvaa 216,132,192\nQta 172,61,53928\ns_add_u32 s88, s86, 59264\nQmvaa 220,136,224\n"
"buffer_load_dwordx4 v64, s[48:51], s88 offen offset:3072 lds\nQmvaa 220,140,192\nQta 174,61,54440\nQmvaa 252,136,228\ns_add_u32 s77, s77, s89\nQmvaa 252,140,196\ns_add_u32 m0, s63, 4096\ns_add_u32 s88, s86, 82816\n"
"Qmvaa 248,128,228\nbuffer_load_dwordx4 v64, s[48:51], s88 offen lds\nQmvaa 248,132,196\nQmvaa 244,120,228\ns_add_u32 s77, s77, s89\nQmvaa 244,124,196\ns_add_u32 s88, s86, 102272\nQmvaa 240,112,228\n"
"buffer_load_dwordx4 v64, s[48:51], s88 offen offset:1024 lds\nQmvaa 240,116,196\nQmvaa 16,112,232\ns_add_u32 s77, s77, s89\nQmvaa 16,116,200\ns_add_u32 s88, s86, 121728\nQmvaa 20,120,232\nbuffer_load_dwordx4 v64, s[48:51], s88 offen offset:2048 lds\n"
"Qmvaa 20,124,200\nQmvaa 24,128,232\ns_add_u32 s77, s77, s89\nQmvaa 24,132,200\nQmvaa 28,136,232\nQmvaa 28,140,200\ns_waitcnt lgkmcnt(0)\nQmvaa 96,144,208\n"
"Qmvaa 96,148,176\ns_add_u32 s68, s71, 131072\nQmvaa 100,152,208\ns_cbranch_vccz .Lst5\nbuffer_store_dwordx4 a[56:59], v79, s[56:59], s68 offen offset:3072 sc1\n.Lst5:\nQmvaa 100,156,176\nQmvaa 104,160,208\n"
"s_add_u32 s68, s71, 147456\nQmvaa 104,164,176\ns_cbranch_vccz .Lst4\nbuffer_store_dwordx4 a[60:63], v79, s[56:59], s68 offen offset:3072 sc1\n.Lst4:\nQmvaa 108,168,208\nQmvaa 108,172,176\ns_waitcnt lgkmcnt(0)\n"
"Qmvaa 140,168,212\ns_waitcnt vmcnt(16)\ns_barrier\nQmvaa 140,172,180\nQda 208,63,33024\ns_add_u32 m0, s62, 49536\ns_add_u32 s69, s87, 3670144\nQmvaa 136,160,212\n"
"buffer_load_dwordx4 v62, s[52:55], s69 offen lds\nQmvaa 136,164,180\nQda 176,63,33536\nQmvaa 132,152,212\nQta 112,61,0\nQmvaa 132,156,180\ns_add_u32 s69, s87, 3734648\nQmvaa 128,144,212\n"
"buffer_load_dwordx4 v62, s[52:55], s69 offen offset:1032 lds\nQmvaa 128,148,180\nQda 212,63,34048\nQmvaa 160,144,216\nQda 180,63,34560\nQmvaa 160,148,184\nQta 114,61,512\ns_add_u32 s69, s87, 3799152\n"
"Qmvaa 164,152,216\nbuffer_load_dwordx4 v62, s[52:55], s69 offen offset:2064 lds\nQmvaa 164,156,184\nQta 116,61,4128\nQmvaa 168,160,216\nQmvaa 168,164,184\nQta 118,61,4640\ns_add_u32 s69, s87, 3863656\n"
"Qmvaa 172,168,216\nbuffer_load_dwordx4 v62, s[52:55], s69 offen offset:3096 lds\nQmvaa 172,172,184\nQda 216,63,35072\nQmvaa 204,168,220\nQda 184,63,35584\nQmvaa 204,172,188\nQta 120,61,8\n"
"s_add_u32 m0, s64, 0\nQmvaa 200,160,220\ns_cbranch_vccnz .Ld0q6\nbuffer_load_dwordx4 v72, s[44:47], s90 offen offset:2048 nt lds\n.Ld0q6:\nQmvaa 200,164,188\nQta 122,61,520\nQmvaa 196,152,220\n"
"Qmvaa 196,156,188\nQta 124,61,4136\nQmvaa 192,144,220\nQda 220,63,36096\nQmvaa 192,148,188\nQda 188,63,36608\nQmvaa 224,144,224\nQta 126,61,4648\n"
"Qmvaa 224,148,192\nQmvaa 228,152,224\nQta 128,61,256\nQmvaa 228,156,192\nQmvaa 232,160,224\nQta 130,61,768\nQmvaa 232,164,192\nQmvaa 236,168,224\n"
"Qda 224,63,37120\nQmvaa 236,172,192\nQda 192,63,37632\nQmvaa 12,168,228\nQta 132,61,4384\nQmvaa 12,172,196\nQmvaa 8,160,228\nQta 134,61,4896\n"
"Qmvaa 8,164,196\nQmvaa 4,152,228\nQta 136,61,264\nQmvaa 4,156,196\nQmvaa 0,144,228\nQda 228,63,38144\nQmvaa 0,148,196\nQda 196,63,38656\n"
"Qmvaa 32,144,232\nQta 138,61,776\nQmvaa 32,148,200\nQmvaa 36,152,232\nQta 140,61,4392\nQmvaa 36,156,200\nQmvaa 40,160,232\nQta 142,61,4904\n"
"Qmvaa 40,164,200\nQmvaa 44,168,232\nQda 232,63,39168\nQmvaa 44,172,200\nQda 200,63,39680\ns_waitcnt lgkmcnt(2)\nQmvaa 80,112,208\nQmvaa 80,116,176\n"
"s_add_u32 s68, s71, 135168\nQmvaa 84,120,208\ns_cbranch_vccz .Lst3\nbuffer_store_dwordx4 a[72:75], v79, s[56:59], s68 offen offset:0 sc1\n.Lst3:\nQmvaa 84,124,176\nQmvaa 88,128,208\ns_add_u32 s68, s71, 151552\n"
"Qmvaa 88,132,176\ns_cbranch_vccz .Lst2\nbuffer_store_dwordx4 a[76:79], v79, s[56:59], s68 offen offset:0 sc1\n.Lst2:\nQmvaa 92,136,208\nQmvaa 92,140,176\ns_waitcnt lgkmcnt(0)\nQmvaa 124,136,212\n"
"s_waitcnt vmcnt(17)\ns_barrier\nQmvaa 124,140,180\nQta 144,61,16512\ns_add_u32 m0, s62, 0\ns_add_u32 s69, s87, 4194304\nQmvaa 120,128,212\nbuffer_load_dwordx4 v62, s[52:55], s69 offen lds\n"
"Qmvaa 120,132,180\nQta 146,61,17024\ns_mov_b32 s89, 0\nQmvaa 116,120,212\ns_cbranch_vccnz .Lpb3\ns_load_dword s89, s[30:31], 0x4000\n.Lpb3:\nQmvaa 116,124,180\n"
"Qta 148,61,20640\ns_add_u32 s69, s87, 4258808\nQmvaa 112,112,212\nbuffer_load_dwordx4 v62, s[52:55], s69 offen offset:1032 lds\nQmvaa 112,116,180\nQta 150,61,21152\nQmvaa 144,112,216\ns_add_u32 s77, s77, s89\n"
"Qmvaa 144,116,184\nQta 152,61,16520\ns_add_u32 s69, s87, 4323312\nQmvaa 148,120,216\nbuffer_load_dwordx4 v62, s[52:55], s69 offen offset:2064 lds\nQmvaa 148,124,184\nQta 154,61,17032\nQmvaa 152,128,216\n"
"s_add_u32 s77, s77, s89\nQmvaa 152,132,184\nQta 156,61,20648\ns_add_u32 s69, s87, 4387816\nQmvaa 156,136,216\nbuffer_load_dwordx4 v62, s[52:55], s69 offen offset:3096 lds\nQmvaa 156,140,184\nQta 158,61,21160\n"
"Qmvaa 188,136,220\ns_add_u32 s77, s77, s89\nQmvaa 188,140,188\nQta 160,61,16768\ns_add_u32 m0, s63, 33024\ns_add_u32 s88, s86, 1024\nQmvaa 184,128,220\nbuffer_load_dwordx4 v64, s[48:51], s88 offen lds\n"
"Qmvaa 184,132,188\nQta 162,61,17280\nQmvaa 180,120,220\ns_add_u32 s77, s77, s89\nQmvaa 180,124,188\nQta 164,61,20896\ns_add_u32 s88, s86, 20480\nQmvaa 176,112,220\n"
"buffer_load_dwordx4 v64, s[48:51], s88 offen offset:1024 lds\nQmvaa 176,116,188\nQta 166,61,21408\nQmvaa 208,112,224\ns_add_u32 s77, s77, s89\nQmvaa 208,116,192\nQta 168,61,16776\ns_add_u32 s88, s86, 39936\n"
"Qmvaa 212,120,224\nbuffer_load_dwordx4 v64, s[48:51], s88 offen offset:2048 lds\nQmvaa 212,124,192\nQta 170,61,17288\nQmvaa 216,128,224\ns_add_u32 s77, s77, s89\nQmvaa 216,132,192\nQta 172,61,20904\n"
"s_add_u32 s88, s86, 59392\nQmvaa 220,136,224\nbuffer_load_dwordx4 v64, s[48:51], s88 offen offset:3072 lds\nQmvaa 220,140,192\nQta 174,61,21416\nQmvaa 252,136,228\ns_add_u32 s77, s77, s89\nQmvaa 252,140,196\n"
"s_add_u32 m0, s63, 37120\ns_add_u32 s88, s86, 82944\nQmvaa 248,128,228\nbuffer_load_dwordx4 v64, s[48:51], s88 offen lds\nQmvaa 248,132,196\nQmvaa 244,120,228\ns_add_u32 s77, s77, s89\nQmvaa 244,124,196\n"
"s_add_u32 s88, s86, 102400\nQmvaa 240,112,228\nbuffer_load_dwordx4 v64, s[48:51], s88 offen offset:1024 lds\nQmvaa 240,116,196\nQmvaa 16,112,232\ns_add_u32 s77, s77, s89\nQmvaa 16,116,200\ns_add_u32 s88, s86, 121856\n"
"Qmvaa 20,120,232\nbuffer_load_dwordx4 v64, s[48:51], s88 offen offset:2048 lds\nQmvaa 20,124,200\nQmvaa 24,128,232\ns_add_u32 s77, s77, s89\nQmvaa 24,132,200\nQmvaa 28,136,232\nQmvaa 28,140,200\n"
"s_waitcnt lgkmcnt(0)\nQmvaa 96,144,208\nQmvaa 96,148,176\ns_add_u32 s68, s71, 135168\nQmvaa 100,152,208\ns_cbranch_vccz .Lst1\nbuffer_store_dwordx4 a[104:107], v79, s[56:59], s68 offen offset:2048 sc1\n.Lst1:\n"
"Qmvaa 100,156,176\nQmvaa 104,160,208\ns_add_u32 s68, s71, 151552\nQmvaa 104,164,176\ns_cbranch_vccz .Lst0\nbuffer_store_dwordx4 a[108:111], v79, s[56:59], s68 offen offset:2048 sc1\n.Lst0:\nQmvaa 108,168,208\n"
"Qmvaa 108,172,176\ns_waitcnt lgkmcnt(0)\nQmvaa 140,168,212\ns_waitcnt vmcnt(16)\ns_barrier\nQmvaa 140,172,180\nQda 208,63,0\ns_add_u32 m0, s62, 16512\n"
"s_add_u32 s69, s87, 4194432\nQmvaa 136,160,212\nbuffer_load_dwordx4 v62, s[52:55], s69 offen lds\nQmvaa 136,164,180\nQda 176,63,512\nQmvaa 132,152,212\nQta 112,61,33024\nQmvaa 132,156,180\n"
"s_add_u32 s69, s87, 4258936\nQmvaa 128,144,212\nbuffer_load_dwordx4 v62, s[52:55], s69 offen offset:1032 lds\nQmvaa 128,148,180\nQda 212,63,1024\nQmvaa 160,144,216\nQda 180,63,1536\nQmvaa 160,148,184\n"
"Qta 114,61,33536\ns_add_u32 s69, s87, 4323440\nQmvaa 164,152,216\nbuffer_load_dwordx4 v62, s[52:55], s69 offen offset:2064 lds\nQmvaa 164,156,184\nQta 116,61,37152\nQmvaa 168,160,216\nQmvaa 168,164,184\n"
"Qta 118,61,37664\ns_add_u32 s69, s87, 4387944\nQmvaa 172,168,216\nbuffer_load_dwordx4 v62, s[52:55], s69 offen offset:3096 lds\nQmvaa 172,172,184\nQda 216,63,2048\nQmvaa 204,168,220\nQda 184,63,2560\n"
"Qmvaa 204,172,188\nQta 120,61,33032\ns_add_u32 m0, s64, 0\nQmvaa 200,160,220\ns_cbranch_vccnz .Ld0q7\nbuffer_load_dwordx4 v72, s[44:47], s90 offen offset:3072 nt lds\ns_add_u32 s65, s65, 2048\ns_xor_b32 s60, s65, %1\n"
"s_add_u32 s90, s60, 260096\n.Ld0q7:\nQmvaa 200,164,188\nQta 122,61,33544\nQmvaa 196,152,220\nQmvaa 196,156,188\nQta 124,61,37160\nQmvaa 192,144,220\n"
"Qda 220,63,3072\nQmvaa 192,148,188\nQda 188,63,3584\nQmvaa 224,144,224\nQta 126,61,37672\nQmvaa 224,148,192\nQmvaa 228,152,224\nQta 128,61,33280\n"
"Qmvaa 228,156,192\nQmvaa 232,160,224\nQta 130,61,33792\nQmvaa 232,164,192\nQmvaa 236,168,224\nQda 224,63,4096\nQmvaa 236,172,192\nQda 192,63,4608\n"
"Qmvaa 12,168,228\nQta 132,61,37408\nQmvaa 12,172,196\nQmvaa 8,160,228\nQta 134,61,37920\nQmvaa 8,164,196\nQmvaa 4,152,228\nQta 136,61,33288\n"
"Qmvaa 4,156,196\nQmvaa 0,144,228\nQda 228,63,5120\nQmvaa 0,148,196\nQda 196,63,5632\nQmvaa 32,144,232\nQta 138,61,33800\nQmvaa 32,148,200\n"
"Qmvaa 36,152,232\nQta 140,61,37416\nQmvaa 36,156,200\nQmvaa 40,160,232\nQta 142,61,37928\nQmvaa 40,164,200\nQmvaa 44,168,232\nQda 232,63,6144\n"
"Qmvaa 44,172,200\nQda 200,63,6656\ns_waitcnt lgkmcnt(2)\nQmvaa 80,112,208\nQmvaa 80,116,176\nQmvaa 84,120,208\nQmvaa 84,124,176\nQmvaa 88,128,208\n"
"Qmvaa 88,132,176\nQmvaa 92,136,208\nQmvaa 92,140,176\ns_waitcnt lgkmcnt(0)\nQmvaa 124,136,212\ns_waitcnt vmcnt(17)\ns_barrier\nQmvaa 124,140,180\n"
"Qta 144,61,49536\ns_add_u32 m0, s62, 33024\ns_add_u32 s69, s87, 4718592\nQmvaa 120,128,212\nbuffer_load_dwordx4 v62, s[52:55], s69 offen lds\nQmvaa 120,132,180\nQta 146,61,50048\nQmvaa 116,120,212\n"
"Qmvaa 116,124,180\nQta 148,61,53664\ns_add_u32 s69, s87, 4783096\nQmvaa 112,112,212\nbuffer_load_dwordx4 v62, s[52:55], s69 offen offset:1032 lds\nQmvaa 112,116,180\nQta 150,61,54176\nQmvaa 144,112,216\n"
"Qmvaa 144,116,184\nQta 152,61,49544\ns_add_u32 s69, s87, 4847600\nQmvaa 148,120,216\nbuffer_load_dwordx4 v62, s[52:55], s69 offen offset:2064 lds\nQmvaa 148,124,184\nQta 154,61,50056\nQmvaa 152,128,216\n"
"Qmvaa 152,132,184\nQta 156,61,53672\ns_add_u32 s69, s87, 4912104\nQmvaa 156,136,216\nbuffer_load_dwordx4 v62, s[52:55], s69 offen offset:3096 lds\nQmvaa 156,140,184\nQta 158,61,54184\nQmvaa 188,136,220\n"
"Qmvaa 188,140,188\nQta 160,61,49792\ns_add_u32 m0, s63, 0\ns_add_u32 s88, s86, 1152\nQmvaa 184,128,220\nbuffer_load_dwordx4 v64, s[48:51], s88 offen lds\nQmvaa 184,132,188\nQta 162,61,50304\n"
"Qmvaa 180,120,220\nQmvaa 180,124,188\nQta 164,61,53920\ns_add_u32 s88, s86, 20608\nQmvaa 176,112,220\nbuffer_load_dwordx4 v64, s[48:51], s88 offen offset:1024 lds\nQmvaa 176,116,188\nQta 166,61,54432\n"
"Qmvaa 208,112,224\nQmvaa 208,116,192\nQta 168,61,49800\ns_add_u32 s88, s86, 40064\nQmvaa 212,120,224\nbuffer_load_dwordx4 v64, s[48:51], s88 offen offset:2048 lds\nQmvaa 212,124,192\nQta 170,61,50312\n"
"Qmvaa 216,128,224\nQmvaa 216,132,192\nQta 172,61,53928\ns_add_u32 s88, s86, 59520\nQmvaa 220,136,224\nbuffer_load_dwordx4 v64, s[48:51], s88 offen offset:3072 lds\nQmvaa 220,140,192\nQta 174,61,54440\n"
"Qmvaa 252,136,228\nQmvaa 252,140,196\ns_add_u32 m0, s63, 4096\ns_add_u32 s88, s86, 83072\nQmvaa 248,128,228\nbuffer_load_dwordx4 v64, s[48:51], s88 offen lds\nQmvaa 248,132,196\nv_mov_b32 v66, s77\n"
"Qmvaa 244,120,228\nds_write_b32 v73, v66 offset:16\nQmvaa 244,124,196\ns_add_u32 s88, s86, 102528\nQmvaa 240,112,228\nbuffer_load_dwordx4 v64, s[48:51], s88 offen offset:1024 lds\nQmvaa 240,116,196\nQmvaa 16,112,232\n"
"Qmvaa 16,116,200\ns_add_u32 s88, s86, 121984\nQmvaa 20,120,232\nbuffer_load_dwordx4 v64, s[48:51], s88 offen offset:2048 lds\nQmvaa 20,124,200\nQmvaa 24,128,232\nQmvaa 24,132,200\nQmvaa 28,136,232\n"
"Qmvaa 28,140,200\ns_waitcnt lgkmcnt(1)\nQmvaa 96,144,208\ns_or_b32 s68, s70, s67\ns_cselect_b32 s68, 0, 128\nQmvaa 96,148,176\nv_mov_b32 v66, 0x7ffff000\nv_cmp_gt_u32 vcc, s68, v60\n"
"Qmvaa 100,152,208\nv_not_b32 v67, v65\ns_lshl_b32 s68, s19, 2\nQmvaa 100,156,176\nQmvaa 104,160,208\nv_cndmask_b32 v66, v66, v76, vcc\nv_readlane_b32 s30, v67, s19\nQmvaa 104,164,176\n"
"Qmvaa 108,168,208\nQmvaa 108,172,176\ns_waitcnt lgkmcnt(0)\nQmvaa 140,168,212\ns_waitcnt vmcnt(16)\ns_barrier\nQmvaa 140,172,180\nQda 208,63,33024\n"
"s_add_u32 m0, s62, 49536\ns_add_u32 s69, s87, 4718720\nQmvaa 136,160,212\nbuffer_load_dwordx4 v62, s[52:55], s69 offen lds\nQmvaa 136,164,180\nQda 176,63,33536\nQmvaa 132,152,212\nQta 112,61,0\n"
"Qmvaa 132,156,180\ns_add_u32 s69, s87, 4783224\nQmvaa 128,144,212\nbuffer_load_dwordx4 v62, s[52:55], s69 offen offset:1032 lds\nQmvaa 128,148,180\nQda 212,63,34048\nQmvaa 160,144,216\nQda 180,63,34560\n"
"Qmvaa 160,148,184\nQta 114,61,512\ns_add_u32 s69, s87, 4847728\nQmvaa 164,152,216\nbuffer_load_dwordx4 v62, s[52:55], s69 offen offset:2064 lds\nQmvaa 164,156,184\n.Lpg3:\ns_cbranch_execz .Ltr0\n"
"Qta 116,61,4128\nQmvaa 168,160,216\nQmvaa 168,164,184\nQta 118,61,4640\ns_add_u32 s69, s87, 4912232\nQmvaa 172,168,216\nbuffer_load_dwordx4 v62, s[52:55], s69 offen offset:3096 lds\nQmvaa 172,172,184\n"
"Qda 216,63,35072\nQmvaa 204,168,220\nQda 184,63,35584\nQmvaa 204,172,188\nQta 120,61,8\nQmvaa 200,160,220\nds_read_b32 v78, v74 offset:16\nQmvaa 200,164,188\n"
"Qta 122,61,520\nQmvaa 196,152,220\nQmvaa 196,156,188\nQta 124,61,4136\nQmvaa 192,144,220\nQda 220,63,36096\nQmvaa 192,148,188\nQda 188,63,36608\n"
"Qmvaa 224,144,224\nQta 126,61,4648\nQmvaa 224,148,192\nQmvaa 228,152,224\nQta 128,61,256\nQmvaa 228,156,192\nQmvaa 232,160,224\nQta 130,61,768\n"
"Qmvaa 232,164,192\nQmvaa 236,168,224\nQda 224,63,37120\nQmvaa 236,172,192\nQda 192,63,37632\nQmvaa 12,168,228\nQta 132,61,4384\nQmvaa 12,172,196\n"
"Qmvaa 8,160,228\nQta 134,61,4896\nQmvaa 8,164,196\nQmvaa 4,152,228\nQta 136,61,264\nQmvaa 4,156,196\nQmvaa 0,144,228\nQda 228,63,38144\n"
"Qmvaa 0,148,196\nQda 196,63,38656\nQmvaa 32,144,232\nQta 138,61,776\nQmvaa 32,148,200\nQmvaa 36,152,232\nQta 140,61,4392\nQmvaa 36,156,200\n"
"Qmvaa 40,160,232\nQta 142,61,4904\nQmvaa 40,164,200\nQmvaa 44,168,232\nQda 232,63,39168\nQmvaa 44,172,200\nQda 200,63,39680\ns_waitcnt lgkmcnt(2)\n"
"Qmvaa 80,112,208\nQmvaa 80,116,176\nQmvaa 84,120,208\nQmvaa 84,124,176\nQmvaa 88,128,208\nQmvaa 88,132,176\nQmvaa 92,136,208\nQmvaa 92,140,176\n"
"s_waitcnt lgkmcnt(0)\nQmvaa 124,136,212\ns_waitcnt vmcnt(15)\ns_barrier\nQmvaa 124,140,180\nQta 144,61,16512\ns_mov_b32 s56, s94\ns_mov_b32 s57, s95\n"
"s_mov_b32 s58, s75\ns_mov_b32 s94, 0\ns_add_u32 m0, %4, 160768\nQmvaa 120,128,212\nbuffer_load_dwordx4 v60, s[56:59], s94 offen lds\nQmvaa 120,132,180\nQta 146,61,17024\nQmvaa 116,120,212\n"
"v_add_u32_dpp v78, v78, v78 quad_perm:[1,0,3,2] row_mask:0xf bank_mask:0xf\nQmvaa 116,124,180\nQta 148,61,20640\ns_add_u32 s94, s94, 4096\nQmvaa 112,112,212\nbuffer_load_dwordx4 v60, s[56:59], s94 offen lds\nQmvaa 112,116,180\nQta 150,61,21152\n"
"Qmvaa 144,112,216\nv_add_u32_dpp v78, v78, v78 quad_perm:[2,3,0,1] row_mask:0xf bank_mask:0xf\nQmvaa 144,116,184\nQta 152,61,16520\ns_add_u32 s94, s94, 4096\nQmvaa 148,120,216\nbuffer_load_dwordx4 v60, s[56:59], s94 offen lds\nQmvaa 148,124,184\n"
"Qta 154,61,17032\nQmvaa 152,128,216\nv_ashrrev_i32 v78, 1, v78\nv_med3_i32 v78, v78, -7, 7\nQmvaa 152,132,184\nQta 156,61,20648\ns_add_u32 s94, s94, 4096\nQmvaa 156,136,216\n"
"buffer_load_dwordx4 v60, s[56:59], s94 offen lds\nQmvaa 156,140,184\nQta 158,61,21160\nQmvaa 188,136,220\nv_add_lshl_u32 v78, v78, 8, 28\nv_xor_b32 v67, v67, v78\nQmvaa 188,140,188\nQta 160,61,16768\n"
"s_add_u32 s94, s94, 4096\nQmvaa 184,128,220\nbuffer_load_dwordx4 v60, s[56:59], s94 offen lds\nQmvaa 184,132,188\nQta 162,61,17280\nQmvaa 180,120,220\nbuffer_store_dword v67, v66, s[24:27], s68 offen offset:1024 sc1\nQmvaa 180,124,188\n"
"Qta 164,61,20896\ns_add_u32 s94, s94, 4096\nQmvaa 176,112,220\nbuffer_load_dwordx4 v60, s[56:59], s94 offen lds\nQmvaa 176,116,188\nQta 166,61,21408\nQmvaa 208,112,224\ns_lshl_b32 s68, s19, 4\n"
"s_lshl1_add_u32 s33, s70, s67\nQmvaa 208,116,192\nQta 168,61,16776\ns_add_u32 s94, s94, 4096\nQmvaa 212,120,224\nbuffer_load_dwordx4 v60, s[56:59], s94 offen lds\nQmvaa 212,124,192\nQta 170,61,17288\n"
"Qmvaa 216,128,224\ns_cmp_eq_u32 s33, 1\ns_cselect_b32 s68, s68, -1\nQmvaa 216,132,192\nQta 172,61,20904\ns_add_u32 s94, s94, 4096\nQmvaa 220,136,224\nbuffer_load_dwordx4 v60, s[56:59], s94 offen lds\n"
"Qmvaa 220,140,192\nQta 174,61,21416\nQmvaa 252,136,228\nv_mov_b32 v67, 0x7ffff000\nv_cmp_eq_u32 vcc, s68, v60\nQmvaa 252,140,196\ns_add_u32 s94, s94, 4096\nQmvaa 248,128,228\n"
"buffer_load_dwordx4 v60, s[56:59], s94 offen lds\nQmvaa 248,132,196\nv_cndmask_b32 v67, v67, v76, vcc\nv_add_u32 v66, 1, v65\nQmvaa 244,120,228\nbuffer_store_dword v66, v67, s[24:27], 0 offen sc1\nQmvaa 244,124,196\ns_add_u32 s94, s94, 4096\n"
"Qmvaa 240,112,228\nbuffer_load_dwordx4 v60, s[56:59], s94 offen lds\nQmvaa 240,116,196\nv_mov_b32 v55, 0x3f803f80\ns_mov_b32 vcc_lo, 0x33333333\nQmvaa 16,112,232\ns_mov_b32 vcc_hi, 0xcccccccc\nv_cndmask_b32 v55, 0, v55, vcc\n"
"Qmvaa 16,116,200\nv_accvgpr_write_b32 a204, v55\nv_accvgpr_write_b32 a205, v55\nQmvaa 20,120,232\nv_accvgpr_write_b32 a206, v55\nv_accvgpr_write_b32 a207, v55\nQmvaa 20,124,200\ns_add_u32 s87, s87, 2048\n"
"v_mov_b64 v[54:55], 0\nQmvaa 24,128,232\nglobal_load_dword v78, v60, s[100:101]\nQmvaa 24,132,200\nQmvaa 28,136,232\nQmvaa 28,140,200\ns_waitcnt lgkmcnt(0)\nQmvaa 96,144,208\n"
"Qmvaa 96,148,176\nQmvaa 100,152,208\nQmvaa 100,156,176\nQmvaa 104,160,208\nQmvaa 104,164,176\nQmvaa 108,168,208\nQmvaa 108,172,176\ns_waitcnt lgkmcnt(0)\n"
"Qmvaa 140,168,212\ns_waitcnt vmcnt(17)\ns_barrier\nQmvaa 140,172,180\nQda 208,63,0\ns_add_u32 m0, s61, 33024\nQmvaa 136,160,212\nbuffer_load_dwordx4 v69, s[40:43], s60 offen nt lds\n"
"Qmvaa 136,164,180\nQda 176,63,512\nQmvaa 132,152,212\nQta 112,61,33024\nQmvaa 132,156,180\nQtv 48,70,128\nQmvaa 128,144,212\nbuffer_load_dwordx4 v69, s[40:43], s60 offen offset:1024 nt lds\n"
"Qmvaa 128,148,180\nQda 212,63,1024\nQmvaa 160,144,216\nQda 180,63,1536\nQmvaa 160,148,184\nQta 114,61,33536\nQmvaa 164,152,216\nbuffer_load_dwordx4 v69, s[40:43], s90 offen offset:2048 nt lds\n"
"Qmvaa 164,156,184\nQta 116,61,37152\nQmvaa 168,160,216\nQtv 50,70,1152\nQmvaa 168,164,184\nQta 118,61,37664\nQmvaa 172,168,216\nbuffer_load_dwordx4 v69, s[40:43], s90 offen offset:3072 nt lds\n"
"Qmvaa 172,172,184\nQda 216,63,2048\nQmvaa 204,168,220\nQda 184,63,2560\nQmvaa 204,172,188\nQta 120,61,33032\ns_add_u32 m0, s64, 33024\nQmvaa 200,160,220\n"
"buffer_load_dwordx4 v72, s[44:47], s60 offen nt lds\nQmvaa 200,164,188\nQta 122,61,33544\ns_waitcnt lgkmcnt(5)\nQmvaa 196,152,220\nv_max_f32 v66, |v48|, |v50|\nQmvaa 196,156,188\nQta 124,61,37160\n"
"Qmvaa 192,144,220\nbuffer_load_dwordx4 v72, s[44:47], s60 offen offset:1024 nt lds\nQmvaa 192,148,188\nQda 220,63,3072\nv_mov_b32 v67, v66\nQmvaa 224,144,224\nQda 188,63,3584\nv_permlane32_swap_b32 v66, v67\n"
"Qmvaa 224,148,192\nQta 126,61,37672\nv_max_f32 v66, v66, v67\nQmvaa 228,152,224\nbuffer_load_dwordx4 v72, s[44:47], s90 offen offset:2048 nt lds\nQmvaa 228,156,192\nQta 128,61,33280\nv_mov_b32 v67, v66\n"
"Qmvaa 232,160,224\ns_nop 0\nv_permlane16_swap_b32 v66, v67\nQmvaa 232,164,192\nQta 130,61,33792\nv_max_f32 v66, v66, v67\nQmvaa 236,168,224\nbuffer_load_dwordx4 v72, s[44:47], s90 offen offset:3072 nt lds\n"
"s_add_u32 s65, s65, 2048\ns_xor_b32 s60, s65, %1\ns_add_u32 s90, s60, 260096\nQmvaa 236,172,192\nQda 224,63,4096\nv_min_f32_dpp v66, v66, v66 row_ror:8 row_mask:0xf bank_mask:0xf\nQmvaa 12,168,228\nQda 192,63,4608\n"
"v_min_f32_dpp v66, v66, v66 row_ror:4 row_mask:0xf bank_mask:0xf\nQmvaa 12,172,196\nQta 132,61,37408\nQmvaa 8,160,228\nv_min_f32_dpp v66, v66, v66 row_ror:2 row_mask:0xf bank_mask:0xf\nQmvaa 8,164,196\nQta 134,61,37920\nv_min_f32_dpp v66, v66, v66 row_ror:1 row_mask:0xf bank_mask:0xf\n"
"Qmvaa 4,152,228\nQmvaa 4,156,196\nQta 136,61,33288\nv_and_b32 v66, 0x7f000000, v66\nQmvaa 0,144,228\nQda 228,63,5120\nv_or_b32 v66, 0x400000, v66\nQmvaa 0,148,196\n"
"Qda 196,63,5632\nQmvaa 32,144,232\nQta 138,61,33800\nv_mov_b32 v67, 0\nQmvaa 32,148,200\nv_mov_b32 v58, 0x39800000\nQmvaa 36,152,232\nQta 140,61,37416\n"
"Qmvaa 36,156,200\nv_mov_b32 v59, 0\nQmvaa 40,160,232\nQta 142,61,37928\nv_readfirstlane_b32 s73, v66\nQmvaa 40,164,200\nQmvaa 44,168,232\nQda 232,63,6144\n"
"Qmvaa 44,172,200\nQda 200,63,6656\ns_waitcnt lgkmcnt(2)\nQmvaa 80,112,208\nQmvaa 80,116,176\nQmvaa 84,120,208\nQmvaa 84,124,176\nQmvaa 88,128,208\n"
"Qmvaa 88,132,176\nQmvaa 92,136,208\nQmvaa 92,140,176\ns_waitcnt lgkmcnt(0)\nQmvaa 124,136,212\ns_waitcnt vmcnt(21)\ns_barrier\nQmvaa 124,140,180\n"
"Qta 144,61,49536\nv_fma_f32 v80, v80, s72, v52\nQmvaa 120,128,212\nv_fma_f32 v81, v81, s72, v52\nv_fma_f32 v82, v82, s72, v52\nQmvaa 120,132,180\nQta 146,61,50048\nv_fma_f32 v83, v83, s72, v52\n"
"Qmvaa 116,120,212\nv_fma_f32 v84, v84, s72, v52\nv_fma_f32 v85, v85, s72, v52\nQmvaa 116,124,180\nQta 148,61,53664\nv_fma_f32 v86, v86, s72, v52\nQmvaa 112,112,212\nv_fma_f32 v87, v87, s72, v52\n"
"Qk 80,80,81\nQmvaa 112,116,180\nQta 150,61,54176\nQk 81,82,83\nQmvaa 144,112,216\nQk 82,84,85\nQk 83,86,87\nQmvaa 144,116,184\n"
"Qta 152,61,49544\nv_accvgpr_write_b32 a0, v80\nQmvaa 148,120,216\nv_accvgpr_write_b32 a1, v81\nv_accvgpr_write_b32 a2, v82\nQmvaa 148,124,184\nQta 154,61,50056\nv_accvgpr_write_b32 a3, v83\n"
"Qmvaa 152,128,216\nv_fma_f32 v88, v88, s72, v52\nv_fma_f32 v89, v89, s72, v52\nQmvaa 152,132,184\nQta 156,61,53672\nv_fma_f32 v90, v90, s72, v52\nQmvaa 156,136,216\nv_fma_f32 v91, v91, s72, v52\n"
"v_fma_f32 v92, v92, s72, v52\nQmvaa 156,140,184\nQta 158,61,54184\nv_fma_f32 v93, v93, s72, v52\nQmvaa 188,136,220\nv_fma_f32 v94, v94, s72, v52\nv_fma_f32 v95, v95, s72, v52\nQmvaa 188,140,188\n"
"Qta 160,61,49792\nQk 88,88,89\nQmvaa 184,128,220\nQk 89,90,91\nQk 90,92,93\nQmvaa 184,132,188\nQta 162,61,50304\nQk 91,94,95\n"
"Qmvaa 180,120,220\nv_accvgpr_write_b32 a4, v88\nv_accvgpr_write_b32 a5, v89\nQmvaa 180,124,188\nQta 164,61,53920\nv_accvgpr_write_b32 a6, v90\nQmvaa 176,112,220\nv_accvgpr_write_b32 a7, v91\n"
"v_fma_f32 v120, v120, s72, v52\nQmvaa 176,116,188\nQta 166,61,54432\nv_fma_f32 v121, v121, s72, v52\nQmvaa 208,112,224\nv_fma_f32 v122, v122, s72, v52\nv_fma_f32 v123, v123, s72, v52\nQmvaa 208,116,192\n"
"Qta 168,61,49800\nv_fma_f32 v124, v124, s72, v52\nQmvaa 212,120,224\nv_fma_f32 v125, v125, s72, v52\nv_fma_f32 v126, v126, s72, v52\nQmvaa 212,124,192\nQta 170,61,50312\nv_fma_f32 v127, v127, s72, v52\n"
"Qmvaa 216,128,224\nQk 120,120,121\nQk 121,122,123\nQmvaa 216,132,192\nQta 172,61,53928\nQk 122,124,125\nQmvaa 220,136,224\nQk 123,126,127\n"
"v_accvgpr_write_b32 a20, v120\nQmvaa 220,140,192\nQta 174,61,54440\nv_accvgpr_write_b32 a21, v121\nQmvaa 252,136,228\nv_accvgpr_write_b32 a22, v122\nv_accvgpr_write_b32 a23, v123\nQmvaa 252,140,196\n"
"v_fma_f32 v112, v112, s72, v52\nv_fma_f32 v113, v113, s72, v52\nQmvaa 248,128,228\nv_fma_f32 v114, v114, s72, v52\nv_fma_f32 v115, v115, s72, v52\nQmvaa 248,132,196\nv_fma_f32 v116, v116, s72, v52\nv_fma_f32 v117, v117, s72, v52\n"
"Qmvaa 244,120,228\nv_fma_f32 v118, v118, s72, v52\nv_fma_f32 v119, v119, s72, v52\nQmvaa 244,124,196\nQk 112,112,113\nQk 113,114,115\nQmvaa 240,112,228\nQk 114,116,117\n"
"Qk 115,118,119\nQmvaa 240,116,196\nv_accvgpr_write_b32 a16, v112\nv_accvgpr_write_b32 a17, v113\nQmvaa 16,112,232\nv_accvgpr_write_b32 a18, v114\nv_accvgpr_write_b32 a19, v115\nQmvaa 16,116,200\n"
"v_fma_f32 v144, v144, s72, v52\nv_fma_f32 v145, v145, s72, v52\nQmvaa 20,120,232\nv_fma_f32 v146, v146, s72, v52\nv_fma_f32 v147, v147, s72, v52\nQmvaa 20,124,200\nv_fma_f32 v148, v148, s72, v52\nv_fma_f32 v149, v149, s72, v52\n"
"Qmvaa 24,128,232\nv_fma_f32 v150, v150, s72, v52\nv_fma_f32 v151, v151, s72, v52\nQmvaa 24,132,200\nQk 144,144,145\nQk 145,146,147\nQmvaa 28,136,232\nQk 146,148,149\n"
"Qk 147,150,151\nQmvaa 28,140,200\nv_accvgpr_write_b32 a32, v144\nv_accvgpr_write_b32 a33, v145\ns_waitcnt lgkmcnt(0)\nQmvaa 96,144,208\nv_accvgpr_write_b32 a34, v146\nv_accvgpr_write_b32 a35, v147\n"
"Qmvaa 96,148,176\nv_fma_f32 v152, v152, s72, v52\nv_fma_f32 v153, v153, s72, v52\nQmvaa 100,152,208\nv_fma_f32 v154, v154, s72, v52\nv_fma_f32 v155, v155, s72, v52\nQmvaa 100,156,176\nv_fma_f32 v156, v156, s72, v52\n"
"v_fma_f32 v157, v157, s72, v52\nQmvaa 104,160,208\nv_fma_f32 v158, v158, s72, v52\nv_fma_f32 v159, v159, s72, v52\nQmvaa 104,164,176\nQk 152,152,153\nQk 153,154,155\nQmvaa 108,168,208\n"
"Qk 154,156,157\nQk 155,158,159\nQmvaa 108,172,176\ns_waitcnt lgkmcnt(0)\nv_accvgpr_write_b32 a36, v152\nQmvaa 140,168,212\ns_waitcnt vmcnt(36)\ns_barrier\n"
"Qmvaa 140,172,180\nQda 112,68,0\ns_add_u32 m0, s61, 66048\nQmvaa 136,160,212\nbuffer_load_dwordx4 v69, s[40:43], s60 offen nt lds\nQmvaa 136,164,180\nQda 116,68,512\nv_accvgpr_write_b32 a37, v153\n"
"Qmvaa 132,152,212\nv_accvgpr_write_b32 a38, v154\nv_accvgpr_write_b32 a39, v155\nQmvaa 132,156,180\nQda 120,68,256\nv_fma_f32 v184, v184, s72, v52\nQmvaa 128,144,212\nbuffer_load_dwordx4 v69, s[40:43], s60 offen offset:1024 nt lds\n"
"Qmvaa 128,148,180\nQda 124,68,768\nv_fma_f32 v185, v185, s72, v52\nQmvaa 160,144,216\nv_fma_f32 v186, v186, s72, v52\nv_fma_f32 v187, v187, s72, v52\nQmvaa 160,148,184\nQda 128,68,1024\n"
"v_fma_f32 v188, v188, s72, v52\nQmvaa 164,152,216\nbuffer_load_dwordx4 v69, s[40:43], s90 offen offset:2048 nt lds\nQmvaa 164,156,184\nQda 132,68,1536\nv_fma_f32 v189, v189, s72, v52\nQmvaa 168,160,216\nv_fma_f32 v190, v190, s72, v52\n"
"v_fma_f32 v191, v191, s72, v52\nQmvaa 168,164,184\nQda 136,68,1280\nQk 184,184,185\nQmvaa 172,168,216\n.Lpg4:\ns_cbranch_execz .Ltr9\nbuffer_load_dwordx4 v69, s[40:43], s90 offen offset:3072 nt lds\n"
"Qmvaa 172,172,184\nQda 140,68,1792\nQk 185,186,187\nQmvaa 204,168,220\nQk 186,188,189\nQk 187,190,191\nQmvaa 204,172,188\ns_add_u32 m0, s64, 66048\n"
"v_accvgpr_write_b32 a52, v184\nQmvaa 200,160,220\nbuffer_load_dwordx4 v72, s[44:47], s60 offen nt lds\nQmvaa 200,164,188\nv_accvgpr_write_b32 a53, v185\nv_accvgpr_write_b32 a54, v186\nQmvaa 196,152,220\nv_accvgpr_write_b32 a55, v187\n"
"v_fma_f32 v176, v176, s72, v52\nQmvaa 196,156,188\nv_fma_f32 v177, v177, s72, v52\nv_fma_f32 v178, v178, s72, v52\nQmvaa 192,144,220\nbuffer_load_dwordx4 v72, s[44:47], s60 offen offset:1024 nt lds\nQmvaa 192,148,188\nv_fma_f32 v179, v179, s72, v52\n"
"v_fma_f32 v180, v180, s72, v52\nQmvaa 224,144,224\nv_fma_f32 v181, v181, s72, v52\nv_fma_f32 v182, v182, s72, v52\nQmvaa 224,148,192\nv_fma_f32 v183, v183, s72, v52\nQk 176,176,177\nQmvaa 228,152,224\n"
"buffer_load_dwordx4 v72, s[44:47], s90 offen offset:2048 nt lds\nQmvaa 228,156,192\nQk 177,178,179\nQk 178,180,181\nQmvaa 232,160,224\nQk 179,182,183\nv_accvgpr_write_b32 a48, v176\nQmvaa 232,164,192\n"
"v_accvgpr_write_b32 a49, v177\nv_accvgpr_write_b32 a50, v178\nQmvaa 236,168,224\nbuffer_load_dwordx4 v72, s[44:47], s90 offen offset:3072 nt lds\ns_add_u32 s65, s65, 2048\ns_xor_b32 s60, s65, %1\ns_add_u32 s90, s60, 260096\nQmvaa 236,172,192\n"
"v_accvgpr_write_b32 a51, v179\nv_fma_f32 v208, v208, s72, v52\nQmvaa 12,168,228\nv_fma_f32 v209, v209, s72, v52\nv_fma_f32 v210, v210, s72, v52\nQmvaa 12,172,196\nv_fma_f32 v211, v211, s72, v52\nv_fma_f32 v212, v212, s72, v52\n"
"Qmvaa 8,160,228\nv_fma_f32 v213, v213, s72, v52\nv_fma_f32 v214, v214, s72, v52\nQmvaa 8,164,196\nv_fma_f32 v215, v215, s72, v52\nQk 208,208,209\nQmvaa 4,152,228\nQk 209,210,211\n"
"Qk 210,212,213\nQmvaa 4,156,196\nQk 211,214,215\nv_accvgpr_write_b32 a64, v208\nQmvaa 0,144,228\nv_accvgpr_write_b32 a65, v209\nv_accvgpr_write_b32 a66, v210\nQmvaa 0,148,196\n"
"v_accvgpr_write_b32 a67, v211\nv_fma_f32 v216, v216, s72, v52\nQmvaa 32,144,232\nv_fma_f32 v217, v217, s72, v52\nv_fma_f32 v218, v218, s72, v52\nQmvaa 32,148,200\nv_fma_f32 v219, v219, s72, v52\nv_fma_f32 v220, v220, s72, v52\n"
"Qmvaa 36,152,232\nv_fma_f32 v221, v221, s72, v52\nv_fma_f32 v222, v222, s72, v52\nQmvaa 36,156,200\nv_fma_f32 v223, v223, s72, v52\nQk 216,216,217\nQmvaa 40,160,232\nQk 217,218,219\n"
"Qk 218,220,221\nQmvaa 40,164,200\nQk 219,222,223\nv_accvgpr_write_b32 a68, v216\nQmvaa 44,168,232\nv_accvgpr_write_b32 a69, v217\nv_accvgpr_write_b32 a70, v218\nQmvaa 44,172,200\n"
"v_accvgpr_write_b32 a71, v219\nQta 148,70,128\nQta 150,70,1152\nQta 156,70,384\nQta 158,70,1408\nQta 164,70,640\nQta 166,70,1664\nQta 172,70,896\n"
"Qta 174,70,1920\nv_pk_fma_f32 v[96:97], v[96:97], s[72:73], v[52:53] op_sel_hi:[1,0,0]\nv_pk_fma_f32 v[98:99], v[98:99], s[72:73], v[52:53] op_sel_hi:[1,0,0]\nv_pk_fma_f32 v[100:101], v[100:101], s[72:73], v[52:53] op_sel_hi:[1,0,0]\nv_pk_fma_f32 v[102:103], v[102:103], s[72:73], v[52:53] op_sel_hi:[1,0,0]\nv_mfma_f32_16x16x4_4b_bf16 v[80:95], v[66:67], v[58:59], 0\nQk 96,96,97\nQk 97,98,99\n"
"Qk 98,100,101\nQk 99,102,103\nv_accvgpr_write_b32 a8, v96\nv_accvgpr_write_b32 a9, v97\nv_accvgpr_write_b32 a10, v98\nv_accvgpr_write_b32 a11, v99\nv_pk_fma_f32 v[104:105], v[104:105], s[72:73], v[52:53] op_sel_hi:[1,0,0]\nv_pk_fma_f32 v[106:107], v[106:107], s[72:73], v[52:53] op_sel_hi:[1,0,0]\n"
"v_pk_fma_f32 v[108:109], v[108:109], s[72:73], v[52:53] op_sel_hi:[1,0,0]\nv_pk_fma_f32 v[110:111], v[110:111], s[72:73], v[52:53] op_sel_hi:[1,0,0]\nv_mfma_f32_16x16x4_4b_bf16 v[112:127], v[66:67], v[58:59], 0\nQk 104,104,105\nQk 105,106,107\nQk 106,108,109\nQk 107,110,111\nv_accvgpr_write_b32 a12, v104\n"
"v_accvgpr_write_b32 a13, v105\nv_accvgpr_write_b32 a14, v106\nv_accvgpr_write_b32 a15, v107\nv_pk_fma_f32 v[128:129], v[128:129], s[72:73], v[52:53] op_sel_hi:[1,0,0]\nv_pk_fma_f32 v[130:131], v[130:131], s[72:73], v[52:53] op_sel_hi:[1,0,0]\nv_pk_fma_f32 v[132:133], v[132:133], s[72:73], v[52:53] op_sel_hi:[1,0,0]\nv_pk_fma_f32 v[134:135], v[134:135], s[72:73], v[52:53] op_sel_hi:[1,0,0]\nv_mfma_f32_16x16x4_4b_bf16 v[144:159], v[66:67], v[58:59], 0\n"
"Qk 128,128,129\nQk 129,130,131\nQk 130,132,133\nQk 131,134,135\nv_accvgpr_write_b32 a24, v128\nv_accvgpr_write_b32 a25, v129\nv_accvgpr_write_b32 a26, v130\nv_accvgpr_write_b32 a27, v131\n"
"v_pk_fma_f32 v[136:137], v[136:137], s[72:73], v[52:53] op_sel_hi:[1,0,0]\nv_pk_fma_f32 v[138:139], v[138:139], s[72:73], v[52:53] op_sel_hi:[1,0,0]\nv_pk_fma_f32 v[140:141], v[140:141], s[72:73], v[52:53] op_sel_hi:[1,0,0]\nv_pk_fma_f32 v[142:143], v[142:143], s[72:73], v[52:53] op_sel_hi:[1,0,0]\nv_mfma_f32_16x16x4_4b_bf16 v[176:191], v[66:67], v[58:59], 0\nQk 136,136,137\nQk 137,138,139\nQk 138,140,141\n"
"Qk 139,142,143\nv_accvgpr_write_b32 a28, v136\nv_accvgpr_write_b32 a29, v137\nv_accvgpr_write_b32 a30, v138\nv_accvgpr_write_b32 a31, v139\nv_pk_fma_f32 v[160:161], v[160:161], s[72:73], v[52:53] op_sel_hi:[1,0,0]\nv_pk_fma_f32 v[162:163], v[162:163], s[72:73], v[52:53] op_sel_hi:[1,0,0]\nv_pk_fma_f32 v[164:165], v[164:165], s[72:73], v[52:53] op_sel_hi:[1,0,0]\n"
"v_pk_fma_f32 v[166:167], v[166:167], s[72:73], v[52:53] op_sel_hi:[1,0,0]\nv_mfma_f32_16x16x4_4b_bf16 v[208:223], v[66:67], v[58:59], 0\nQk 160,160,161\nQk 161,162,163\nQk 162,164,165\nQk 163,166,167\nv_accvgpr_write_b32 a40, v160\nv_accvgpr_write_b32 a41, v161\n"
"v_accvgpr_write_b32 a42, v162\nv_accvgpr_write_b32 a43, v163\nv_pk_fma_f32 v[168:169], v[168:169], s[72:73], v[52:53] op_sel_hi:[1,0,0]\nv_pk_fma_f32 v[170:171], v[170:171], s[72:73], v[52:53] op_sel_hi:[1,0,0]\nv_pk_fma_f32 v[172:173], v[172:173], s[72:73], v[52:53] op_sel_hi:[1,0,0]\nv_pk_fma_f32 v[174:175], v[174:175], s[72:73], v[52:53] op_sel_hi:[1,0,0]\nv_mfma_f32_16x16x4_4b_bf16 a[208:223], v[66:67], v[58:59], 0\nQk 168,168,169\n"
"Qk 169,170,171\nQk 170,172,173\nQk 171,174,175\nv_accvgpr_write_b32 a44, v168\nv_accvgpr_write_b32 a45, v169\nv_accvgpr_write_b32 a46, v170\nv_accvgpr_write_b32 a47, v171\nv_pk_fma_f32 v[192:193], v[192:193], s[72:73], v[52:53] op_sel_hi:[1,0,0]\n"
"v_pk_fma_f32 v[194:195], v[194:195], s[72:73], v[52:53] op_sel_hi:[1,0,0]\nv_pk_fma_f32 v[196:197], v[196:197], s[72:73], v[52:53] op_sel_hi:[1,0,0]\nv_pk_fma_f32 v[198:199], v[198:199], s[72:73], v[52:53] op_sel_hi:[1,0,0]\nv_mfma_f32_16x16x4_4b_bf16 a[224:239], v[66:67], v[58:59], 0\nQk 192,192,193\nQk 193,194,195\nQk 194,196,197\nQk 195,198,199\n"
"v_accvgpr_write_b32 a56, v192\nv_accvgpr_write_b32 a57, v193\nv_accvgpr_write_b32 a58, v194\nv_accvgpr_write_b32 a59, v195\nv_pk_fma_f32 v[200:201], v[200:201], s[72:73], v[52:53] op_sel_hi:[1,0,0]\nv_pk_fma_f32 v[202:203], v[202:203], s[72:73], v[52:53] op_sel_hi:[1,0,0]\nv_pk_fma_f32 v[204:205], v[204:205], s[72:73], v[52:53] op_sel_hi:[1,0,0]\nv_pk_fma_f32 v[206:207], v[206:207], s[72:73], v[52:53] op_sel_hi:[1,0,0]\n"
"v_mfma_f32_16x16x4_4b_bf16 a[240:255], v[66:67], v[58:59], 0\nQk 200,200,201\nQk 201,202,203\nQk 202,204,205\nQk 203,206,207\nv_accvgpr_write_b32 a60, v200\nv_accvgpr_write_b32 a61, v201\nv_accvgpr_write_b32 a62, v202\n"
"v_accvgpr_write_b32 a63, v203\nv_pk_fma_f32 v[224:225], v[224:225], s[72:73], v[52:53] op_sel_hi:[1,0,0]\nv_pk_fma_f32 v[226:227], v[226:227], s[72:73], v[52:53] op_sel_hi:[1,0,0]\nv_pk_fma_f32 v[228:229], v[228:229], s[72:73], v[52:53] op_sel_hi:[1,0,0]\nv_pk_fma_f32 v[230:231], v[230:231], s[72:73], v[52:53] op_sel_hi:[1,0,0]\nv_mfma_f32_16x16x4_4b_bf16 v[96:111], v[66:67], v[58:59], 0\nQk 224,224,225\nQk 225,226,227\n"
"Qk 226,228,229\nQk 227,230,231\nv_accvgpr_write_b32 a72, v224\nv_accvgpr_write_b32 a73, v225\nv_accvgpr_write_b32 a74, v226\nv_accvgpr_write_b32 a75, v227\nv_pk_fma_f32 v[232:233], v[232:233], s[72:73], v[52:53] op_sel_hi:[1,0,0]\nv_pk_fma_f32 v[234:235], v[234:235], s[72:73], v[52:53] op_sel_hi:[1,0,0]\n"
"v_pk_fma_f32 v[236:237], v[236:237], s[72:73], v[52:53] op_sel_hi:[1,0,0]\nv_pk_fma_f32 v[238:239], v[238:239], s[72:73], v[52:53] op_sel_hi:[1,0,0]\nv_mfma_f32_16x16x4_4b_bf16 v[128:143], v[66:67], v[58:59], 0\nQk 232,232,233\nQk 233,234,235\nQk 234,236,237\nQk 235,238,239\nv_accvgpr_write_b32 a76, v232\n"
"v_accvgpr_write_b32 a77, v233\nv_accvgpr_write_b32 a78, v234\nv_accvgpr_write_b32 a79, v235\nv_pk_fma_f32 v[240:241], v[240:241], s[72:73], v[52:53] op_sel_hi:[1,0,0]\nv_pk_fma_f32 v[242:243], v[242:243], s[72:73], v[52:53] op_sel_hi:[1,0,0]\nv_pk_fma_f32 v[244:245], v[244:245], s[72:73], v[52:53] op_sel_hi:[1,0,0]\nv_pk_fma_f32 v[246:247], v[246:247], s[72:73], v[52:53] op_sel_hi:[1,0,0]\nv_mfma_f32_16x16x4_4b_bf16 v[160:175], v[66:67], v[58:59], 0\n"
"Qk 240,240,241\nQk 241,242,243\nQk 242,244,245\nQk 243,246,247\nv_accvgpr_write_b32 a80, v240\nv_accvgpr_write_b32 a81, v241\nv_accvgpr_write_b32 a82, v242\nv_accvgpr_write_b32 a83, v243\n"
"v_pk_fma_f32 v[248:249], v[248:249], s[72:73], v[52:53] op_sel_hi:[1,0,0]\nv_pk_fma_f32 v[250:251], v[250:251], s[72:73], v[52:53] op_sel_hi:[1,0,0]\nv_pk_fma_f32 v[252:253], v[252:253], s[72:73], v[52:53] op_sel_hi:[1,0,0]\nv_pk_fma_f32 v[254:255], v[254:255], s[72:73], v[52:53] op_sel_hi:[1,0,0]\nv_mfma_f32_16x16x4_4b_bf16 v[192:207], v[66:67], v[58:59], 0\nQk 248,248,249\nQk 249,250,251\nQk 250,252,253\n"
"Qk 251,254,255\nv_accvgpr_write_b32 a84, v248\nv_accvgpr_write_b32 a85, v249\nv_accvgpr_write_b32 a86, v250\nv_accvgpr_write_b32 a87, v251\nv_pk_fma_f32 v[0:1], v[0:1], s[72:73], v[52:53] op_sel_hi:[1,0,0]\nv_pk_fma_f32 v[2:3], v[2:3], s[72:73], v[52:53] op_sel_hi:[1,0,0]\nv_pk_fma_f32 v[4:5], v[4:5], s[72:73], v[52:53] op_sel_hi:[1,0,0]\n"
"v_pk_fma_f32 v[6:7], v[6:7], s[72:73], v[52:53] op_sel_hi:[1,0,0]\nv_mfma_f32_16x16x4_4b_bf16 v[224:239], v[66:67], v[58:59], 0\nQk 0,0,1\nQk 1,2,3\nQk 2,4,5\nQk 3,6,7\nv_accvgpr_write_b32 a88, v0\nv_accvgpr_write_b32 a89, v1\n"
"v_accvgpr_write_b32 a90, v2\nv_accvgpr_write_b32 a91, v3\nv_pk_fma_f32 v[8:9], v[8:9], s[72:73], v[52:53] op_sel_hi:[1,0,0]\nv_pk_fma_f32 v[10:11], v[10:11], s[72:73], v[52:53] op_sel_hi:[1,0,0]\nv_pk_fma_f32 v[12:13], v[12:13], s[72:73], v[52:53] op_sel_hi:[1,0,0]\nv_pk_fma_f32 v[14:15], v[14:15], s[72:73], v[52:53] op_sel_hi:[1,0,0]\nv_mfma_f32_16x16x4_4b_bf16 v[240:255], v[66:67], v[58:59], 0\nQk 8,8,9\n"
"Qk 9,10,11\nQk 10,12,13\nQk 11,14,15\nv_accvgpr_write_b32 a92, v8\nv_accvgpr_write_b32 a93, v9\nv_accvgpr_write_b32 a94, v10\nv_accvgpr_write_b32 a95, v11\nv_pk_fma_f32 v[16:17], v[16:17], s[72:73], v[52:53] op_sel_hi:[1,0,0]\n"
"v_pk_fma_f32 v[18:19], v[18:19], s[72:73], v[52:53] op_sel_hi:[1,0,0]\nv_pk_fma_f32 v[20:21], v[20:21], s[72:73], v[52:53] op_sel_hi:[1,0,0]\nv_pk_fma_f32 v[22:23], v[22:23], s[72:73], v[52:53] op_sel_hi:[1,0,0]\nv_mfma_f32_16x16x4_4b_bf16 a[176:191], v[54:55], v[54:55], 0\nQk 16,16,17\nQk 17,18,19\nQk 18,20,21\nQk 19,22,23\n"
"v_accvgpr_write_b32 a96, v16\nv_accvgpr_write_b32 a97, v17\nv_accvgpr_write_b32 a98, v18\nv_accvgpr_write_b32 a99, v19\nv_pk_fma_f32 v[24:25], v[24:25], s[72:73], v[52:53] op_sel_hi:[1,0,0]\nv_pk_fma_f32 v[26:27], v[26:27], s[72:73], v[52:53] op_sel_hi:[1,0,0]\nv_pk_fma_f32 v[28:29], v[28:29], s[72:73], v[52:53] op_sel_hi:[1,0,0]\nv_pk_fma_f32 v[30:31], v[30:31], s[72:73], v[52:53] op_sel_hi:[1,0,0]\n"
"v_mfma_f32_16x16x4_4b_bf16 a[188:203], v[54:55], v[54:55], 0\nQk 24,24,25\nQk 25,26,27\nQk 26,28,29\nQk 27,30,31\nv_accvgpr_write_b32 a100, v24\nv_accvgpr_write_b32 a101, v25\nv_accvgpr_write_b32 a102, v26\n"
"v_accvgpr_write_b32 a103, v27\nv_pk_fma_f32 v[32:33], v[32:33], s[72:73], v[52:53] op_sel_hi:[1,0,0]\nv_pk_fma_f32 v[34:35], v[34:35], s[72:73], v[52:53] op_sel_hi:[1,0,0]\nv_pk_fma_f32 v[36:37], v[36:37], s[72:73], v[52:53] op_sel_hi:[1,0,0]\nv_pk_fma_f32 v[38:39], v[38:39], s[72:73], v[52:53] op_sel_hi:[1,0,0]\nQk 32,32,33\nQk 33,34,35\nQk 34,36,37\n"
"Qk 35,38,39\nv_accvgpr_write_b32 a104, v32\nv_accvgpr_write_b32 a105, v33\nv_accvgpr_write_b32 a106, v34\nv_accvgpr_write_b32 a107, v35\nv_pk_fma_f32 v[40:41], v[40:41], s[72:73], v[52:53] op_sel_hi:[1,0,0]\nv_pk_fma_f32 v[42:43], v[42:43], s[72:73], v[52:53] op_sel_hi:[1,0,0]\nv_pk_fma_f32 v[44:45], v[44:45], s[72:73], v[52:53] op_sel_hi:[1,0,0]\n"
"v_pk_fma_f32 v[46:47], v[46:47], s[72:73], v[52:53] op_sel_hi:[1,0,0]\nQk 40,40,41\nQk 41,42,43\nQk 42,44,45\nQk 43,46,47\nv_accvgpr_write_b32 a108, v40\nv_accvgpr_write_b32 a109, v41\nv_accvgpr_write_b32 a110, v42\n"
"v_accvgpr_write_b32 a111, v43\ns_waitcnt lgkmcnt(8)\nQzvaa 0,112,0\nQmvaa 0,116,4\ns_memrealtime s[74:75]\ns_memtime s[68:69]\nQmvaa 0,120,8\nQmvaa 0,124,12\n"
"Qzvaa 4,140,12\nQmvaa 4,136,8\nQmvaa 4,132,4\nQmvaa 4,128,0\ns_nop 3\nv_max3_f32 v66, v0, v1, v2\ns_nop 2\nv_max3_f32 v66, v66, v3, v4\n"
"v_max3_f32 v66, v66, v5, v6\nv_max_f32 v66, v66, v7\nQzvaa 8,112,16\ns_waitcnt lgkmcnt(0)\nv_mov_b32 v67, v66\nQmvaa 8,116,20\ns_sub_u32 s68, s68, s80\ns_sub_u32 s69, s74, s82\n"
"Qmvaa 8,120,24\ns_mul_i32 s69, s69, 73\ns_lshl_b32 s68, s68, 2\nQmvaa 8,124,28\ns_sub_u32 s75, s67, 1\ns_cmp_gt_u32 s68, s69\ns_cselect_b32 s68, s75, 0\ns_cselect_b32 s31, 6, 0\n"
"s_add_u32 s31, s31, 238\nQzvaa 12,140,28\ns_and_b32 s75, s68, 30\ns_add_u32 s77, s74, 55\nQmvaa 12,136,24\ns_add_u32 s77, s77, s75\nv_permlane32_swap_b32 v66, v67\nQmvaa 12,132,20\n"
"v_max_f32 v66, v66, v67\nv_mov_b32 v67, v66\nQmvaa 12,128,16\ns_nop 0\nv_permlane16_swap_b32 v66, v67\nv_max_f32 v66, v66, v67\ns_nop 1\nv_min_f32_dpp v66, v66, v66 row_ror:8 row_mask:0xf bank_mask:0xf\n"
"s_nop 1\nv_min_f32_dpp v66, v66, v66 row_ror:4 row_mask:0xf bank_mask:0xf\ns_nop 1\nv_min_f32_dpp v66, v66, v66 row_ror:2 row_mask:0xf bank_mask:0xf\ns_nop 1\nv_min_f32_dpp v66, v66, v66 row_ror:1 row_mask:0xf bank_mask:0xf\nv_sub_f32 v56, 0xc1a00000, v66\nv_mov_b32 v57, v56\n"
"v_mov_b32 v58, v56\nv_mov_b32 v59, v56\nQa 0,0,56\nQa 1,1,56\nQa 2,2,56\nQa 3,3,56\nQe 0\nQe 1\n"
"Qe 2\nQe 3\nQk 16,0,1\nQk 17,2,3\nQcvaa 0,112,32,56\nQa 4,4,56\nQa 5,5,56\nQmvaa 0,116,36\n"
"Qa 6,6,56\nQa 7,7,56\nQmvaa 0,120,40\nQe 4\nQe 5\nQe 6\nQe 7\nQmvaa 0,124,44\n"
"Qk 18,4,5\nQk 19,6,7\nQcvaa 4,140,44,56\nQa 8,8,56\nQa 9,9,56\nQmvaa 4,136,40\nQa 10,10,56\nQa 11,11,56\n"
"Qmvaa 4,132,36\nQa 12,12,56\nQa 13,13,56\nQmvaa 4,128,32\ns_waitcnt lgkmcnt(0)\nQa 14,14,56\nQmvva 96,16,148\nQa 15,15,56\n"
"Qmvva 100,16,156\nQe 8\nQmvva 104,16,164\nQe 9\nQe 10\nQe 11\nQe 12\nQe 13\n"
"Qe 14\nQe 15\nQmvva 108,16,172\nQk 20,8,9\nQk 21,10,11\nQk 22,12,13\nQk 23,14,15\ns_nop 1\n"
"Qmvva 140,20,172\nQe 0\nQmvva 136,20,164\nQe 1\nQmvva 132,20,156\nQe 2\nQmvva 128,20,148\nQe 3\n"
"v_smfmac_f32_16x16x64_bf16 a[176:179], a[204:207], v[16:23], v77\nQcvaa 8,112,48,56\ns_add_u32 m0, s61, 99072\nQk 24,0,1\nQmvaa 8,116,52\nbuffer_load_dwordx4 v69, s[40:43], s60 offen nt lds\nQmvaa 8,120,56\nQk 25,2,3\n"
"Qmvaa 8,124,60\ns_or_b32 s65, s65, 0x1d8000\ns_add_u32 s65, s65, 2048\nQcvaa 12,140,60,56\ns_andn2_b32 s65, s65, 0x1d8000\nQmvaa 12,136,56\nQe 4\nQmvaa 12,132,52\n"
"buffer_load_dwordx4 v69, s[40:43], s60 offen offset:1024 nt lds\nQmvaa 12,128,48\nQe 5\nQcvaa 0,128,64,56\ns_memrealtime s[74:75]\nQk 26,4,5\nQmvaa 0,132,68\nQe 6\n"
"Qmvaa 0,136,72\nQe 7\nQmvaa 0,140,76\nbuffer_load_dwordx4 v69, s[40:43], s90 offen offset:2048 nt lds\nQk 27,6,7\nQcvaa 4,124,76,56\nQe 8\nQmvaa 4,120,72\n"
"Qe 9\nQmvaa 4,116,68\nQe 10\nQmvaa 4,112,64\nQe 11\nbuffer_load_dwordx4 v69, s[40:43], s90 offen offset:3072 nt lds\nQmvva 160,24,148\nQe 12\n"
"Qmvva 164,24,156\nQe 13\ns_add_u32 m0, s64, 99072\nQmvva 168,24,164\nbuffer_load_dwordx4 v72, s[44:47], s60 offen nt lds\nQe 14\nQe 15\nQmvva 172,24,172\n"
"Qk 28,8,9\nQk 29,10,11\nQk 30,12,13\nQk 31,14,15\ns_nop 1\nQmvva 204,28,172\nQe 0\nbuffer_load_dwordx4 v72, s[44:47], s60 offen offset:1024 nt lds\n"
"Qmvva 200,28,164\nQe 1\nQmvva 196,28,156\nQe 2\nQmvva 192,28,148\nbuffer_load_dwordx4 v72, s[44:47], s90 offen offset:2048 nt lds\nv_smfmac_f32_16x16x64_bf16 a[180:183], a[204:207], v[24:31], v77\nQcvaa 8,112,80,56\n"
"Qe 3\nQmvaa 8,116,84\nQk 34,0,1\nQk 35,2,3\nQmvaa 8,120,88\ns_waitcnt vmcnt(15)\ns_barrier\nQmvaa 8,124,92\n"
"buffer_load_dwordx4 v72, s[44:47], s90 offen offset:3072 nt lds\nQcvaa 12,140,92,56\ns_waitcnt lgkmcnt(0)\nQmvaa 12,136,88\ns_cmp_gt_u32 s77, s74\ns_cbranch_scc1 .Lsp0\n.Lrp0:\nQmvaa 12,132,84\n"
"s_and_b32 s75, s68, 35\ns_add_u32 s77, s74, 85\nQmvaa 12,128,80\ns_add_u32 s77, s77, s75\ns_xor_b32 s60, s65, %1\ns_add_u32 s90, s60, 260096\nQcvaa 0,128,96,56\nQda 128,68,34048\n"
"Qmvaa 0,132,100\nQda 132,68,34560\nQmvaa 0,136,104\nQda 136,68,34304\nQe 4\nQe 5\nQe 6\nQe 7\n"
"Qmvaa 0,140,108\nQda 140,68,34816\nQk 32,4,5\nQk 33,6,7\nQcvaa 4,124,108,56\nQda 124,68,33792\nQmvaa 4,120,104\nQda 120,68,33280\n"
".Lpg5:\ns_cbranch_execz .Ltr9\nQmvaa 4,116,100\nQda 116,68,33536\nQmvaa 4,112,96\nQda 112,68,33024\nQmvva 224,32,148\nQe 8\n"
"Qmvva 228,32,156\nQe 9\nQmvva 232,32,164\nQe 10\nQe 11\nQmvva 236,32,172\ns_waitcnt lgkmcnt(0)\nQk 36,8,9\n"
"Qk 37,10,11\nQcvaa 8,112,0,56\nQe 12\nQmvaa 8,116,4\nv_add_u32 v68, 66048, v68\nQmvaa 8,120,8\nQe 13\nQe 14\n"
"Qe 15\nQmvaa 8,124,12\nQk 38,12,13\nQk 39,14,15\nQcvaa 12,140,12,56\nQe 0\nQmvaa 12,136,8\nQe 1\n"
"Qmvaa 12,132,4\nQe 2\nQmvaa 12,128,0\nQe 3\nv_smfmac_f32_16x16x64_bf16 a[184:187], a[204:207], v[32:39], v77\nQmava 208,36,148\nQe 4\nQmava 212,36,156\n"
"Qe 5\nQmava 216,36,164\nQe 6\nQe 8\nQe 7\nQmava 220,36,172\nQe 9\nQk 42,0,1\n"
"Qk 43,2,3\nQk 40,4,5\nQk 41,6,7\nQta 168,70,768\nQta 170,70,1792\nQmava 252,40,172\nQe 10\nQta 172,70,33792\n"
"Qta 174,70,34816\nQmava 248,40,164\nQe 11\nQta 160,70,512\nQta 162,70,1536\nQmava 244,40,156\nQk 44,8,9\nQk 45,10,11\n"
"Qta 164,70,33536\nQta 166,70,34560\nQmava 240,40,148\nQe 12\nQcvaa 0,112,16,56\nQta 152,70,256\nQta 154,70,1280\nQmvaa 0,116,20\n"
"Qe 13\nQmvaa 0,120,24\nQta 156,70,33280\nQta 158,70,34304\nQmvaa 0,124,28\nQe 14\nQcvaa 4,140,28,56\nQta 144,70,0\n"
"Qta 146,70,1024\nQmvaa 4,136,24\nQe 15\nQmvaa 4,132,20\nQta 148,70,33024\nQta 150,70,34048\nQmvaa 4,128,16\nQk 46,12,13\n"
"Qk 47,14,15\nQcvaa 8,128,32,56\nv_add_u32 v75, 1, v75\nQmvaa 8,132,36\ns_waitcnt vmcnt(8)\nds_write_b32 v73, v75\nQmvaa 8,136,40\nQe 0\n"
"Qmvaa 8,140,44\nQe 1\nQcvaa 12,124,44,56\nQe 2\nQmvaa 12,120,40\nQe 3\nQmvaa 12,116,36\nQe 4\n"
"Qmvaa 12,112,32\ns_waitcnt lgkmcnt(1)\nQk 48,0,1\nv_smfmac_f32_16x16x64_bf16 a[188:191], a[204:207], v[40:47], v77\nQmvva 80,16,144\nQe 5\nQmvva 80,44,148\nQe 6\n"
"Qmvva 84,16,152\nQe 7\nQmvva 84,44,156\nQk 49,2,3\nQk 50,4,5\nQmvva 88,16,160\nQk 51,6,7\nQmvva 88,44,164\n"
"Qe 8\nQmvva 92,16,168\nQe 9\nQmvva 92,44,172\nQe 10\nQmvva 124,20,168\nQe 11\nQmvva 124,48,172\n"
"ds_read_b32 v66, v74\nQk 54,8,9\nQmvva 120,20,160\nQk 55,10,11\nQmvva 120,48,164\nQe 12\nQmvva 116,20,152\nQe 13\n"
"Qmvva 116,48,156\nQe 14\nQmvva 112,20,144\ns_waitcnt lgkmcnt(0)\nv_cmp_gt_u32 vcc, v75, v66\nQmvva 112,48,148\ns_cbranch_vccnz .Lsa1\n.Lra1:\n"
"Qcvaa 0,112,48,56\ns_add_u32 m0, s61, 0\nQk 52,12,13\nQmvaa 0,116,52\nbuffer_load_dwordx4 v69, s[40:43], s60 offen nt lds\nQmvaa 0,120,56\nQe 15\nQmvaa 0,124,60\n"
"s_add_u32 s65, s65, 2048\nQk 53,14,15\nQcvaa 4,140,60,56\nQmvaa 4,136,56\nQmvaa 4,132,52\nbuffer_load_dwordx4 v69, s[40:43], s60 offen offset:1024 nt lds\nQmvaa 4,128,48\nQcvaa 8,128,64,56\n"
"s_memrealtime s[74:75]\nQmvaa 8,132,68\nQe 0\nQmvaa 8,136,72\nQe 1\nQmvaa 8,140,76\nbuffer_load_dwordx4 v69, s[40:43], s90 offen offset:2048 nt lds\nQcvaa 12,124,76,56\n"
"Qe 2\nQmvaa 12,120,72\nQe 3\nQmvaa 12,116,68\nQe 4\nQmvaa 12,112,64\nQe 5\nv_smfmac_f32_16x16x64_bf16 a[192:195], a[204:207], v[48:55], v77\n"
"Qmvva 144,24,144\nbuffer_load_dwordx4 v69, s[40:43], s90 offen offset:3072 nt lds\nQmvva 144,52,148\nQe 6\nQmvva 148,24,152\nQe 7\nQmvva 148,52,156\ns_add_u32 m0, s64, 0\n"
"Qk 20,0,1\nQmvva 152,24,160\nbuffer_load_dwordx4 v72, s[44:47], s60 offen nt lds\nQmvva 152,52,164\nQk 21,2,3\nQk 22,4,5\nQmvva 156,24,168\nQk 23,6,7\n"
"Qmvva 156,52,172\nQe 8\nQmvva 188,28,168\nQe 9\nQmvva 188,20,172\nbuffer_load_dwordx4 v72, s[44:47], s60 offen offset:1024 nt lds\nQmvva 184,28,160\nQe 10\n"
"Qmvva 184,20,164\nQe 11\nQmvva 180,28,152\nQk 26,8,9\nQk 27,10,11\nQmvva 180,20,156\nQe 12\nQmvva 176,28,144\n"
"buffer_load_dwordx4 v72, s[44:47], s90 offen offset:2048 nt lds\nQmvva 176,20,148\nQe 13\nQcvaa 0,112,80,56\nQe 14\nQmvaa 0,116,84\nQe 15\nQmvaa 0,120,88\n"
"Qk 24,12,13\nQk 25,14,15\nQmvaa 0,124,92\nbuffer_load_dwordx4 v72, s[44:47], s90 offen offset:3072 nt lds\nQcvaa 4,140,92,56\ns_waitcnt lgkmcnt(0)\nQmvaa 4,136,88\ns_cmp_gt_u32 s77, s74\n"
"s_cbranch_scc1 .Lsp1\n.Lrp1:\nQmvaa 4,132,84\ns_and_b32 s75, s68, 47\ns_add_u32 s77, s74, 108\nQmvaa 4,128,80\ns_add_u32 s77, s77, s75\ns_xor_b32 s60, s65, %1\n"
"s_add_u32 s90, s60, 260096\nQcvaa 8,128,96,56\nQda 128,68,1024\ns_add_u32 s79, s79, 2030\nQmvaa 8,132,100\nQda 132,68,1536\ns_min_u32 s79, s79, s77\nQmvaa 8,136,104\n"
"Qda 136,68,1280\nQmvaa 8,140,108\nQda 140,68,1792\nQcvaa 12,124,108,56\nQda 124,68,768\nQmvaa 12,120,104\nQda 120,68,256\nQmvaa 12,116,100\n"
"Qda 116,68,512\nQmvaa 12,112,96\nQda 112,68,0\nv_smfmac_f32_16x16x64_bf16 a[196:199], a[204:207], v[20:27], v77\nQmvva 208,32,144\nQe 0\nQmvva 208,24,148\nQe 1\n"
"Qmvva 212,32,152\nQe 2\nQmvva 212,24,156\nQe 3\nQmvva 216,32,160\nQk 28,0,1\nQk 29,2,3\nQmvva 216,24,164\n"
"Qe 4\nQmvva 220,32,168\nQe 5\nQmvva 220,24,172\ns_waitcnt lgkmcnt(0)\nQk 30,4,5\nQcvaa 0,112,0,56\nQe 6\n"
"Qmvaa 0,116,4\nQe 7\nQmvaa 0,120,8\nQk 31,6,7\nQmvaa 0,124,12\nQe 8\nQcvaa 4,140,12,56\nQe 9\n"
"Qmvaa 4,136,8\nQe 10\nQmvaa 4,132,4\nQe 11\nQmvaa 4,128,0\nQe 12\nQmvva 240,36,144\nQe 13\n"
"Qmvva 240,28,148\nQe 14\nQmvva 244,36,152\nQe 15\nQmvva 244,28,156\nQk 34,8,9\nQk 35,10,11\nQmvva 248,36,160\n"
"Qk 32,12,13\nQk 33,14,15\nQmvva 248,28,164\nQe 0\nQmvva 252,36,168\nQe 1\nQmvva 252,28,172\nQe 2\n"
"Qmava 236,40,168\nQta 168,70,33920\nQta 170,70,34944\nQmava 236,32,172\nQe 3\nQmava 232,40,160\nQta 172,71,896\nQta 174,71,1920\n"
"Qmava 232,32,164\nQk 36,0,1\nQk 37,2,3\nQmava 228,40,152\nQta 160,70,33664\nQta 162,70,34688\nQmava 228,32,156\nQe 4\n"
"Qmava 224,40,144\nQta 164,71,640\nQta 166,71,1664\nQmava 224,32,148\nQe 5\nv_smfmac_f32_16x16x64_bf16 a[200:203], a[204:207], v[28:35], v77\nQta 152,70,33408\nQta 154,70,34432\n"
"Qcvaa 8,112,16,56\nQe 6\nQmvaa 8,116,20\nQta 156,71,384\nQta 158,71,1408\nQmvaa 8,120,24\nQe 7\nQmvaa 8,124,28\n"
"Qta 144,70,33152\nQta 146,70,34176\nQcvaa 12,140,28,56\nQk 38,4,5\nQk 39,6,7\nQmvaa 12,136,24\nQta 148,71,128\nQta 150,71,1152\n"
"Qmvaa 12,132,20\ns_lshl_b32 s66, s67, 3\nQmvaa 12,128,16\n.Lhookret:\n.Lloop:\nQcvaa 0,128,32,56\nv_add_u32 v75, 1, v75\nQmvaa 0,132,36\n"
"s_waitcnt vmcnt(8)\nds_write_b32 v73, v75\nQmvaa 0,136,40\nQe 8\nQmvaa 0,140,44\nQe 9\nQcvaa 4,124,44,56\nQe 10\n"
"Qmvaa 4,120,40\nQe 11\nQmvaa 4,116,36\nQe 12\nQmvaa 4,112,32\ns_waitcnt lgkmcnt(1)\nQk 40,8,9\nQmvva 96,44,144\n"
"Qe 13\nQmvva 96,36,148\nQe 14\nQmvva 100,44,152\nQe 15\nQmvva 100,36,156\nQk 41,10,11\nQk 42,12,13\n"
"Qmvva 104,44,160\nQk 43,14,15\nQmvva 104,36,164\nQe 0\nQmvva 108,44,168\nQe 1\nQmvva 108,36,172\nQe 2\n"
"Qmvva 140,48,168\nQe 3\nQmvva 140,40,172\nQk 46,0,1\nQk 47,2,3\nQmvva 136,48,160\nds_read_b32 v66, v74\nQmvva 136,40,164\n"
"Qe 4\nQmvva 132,48,152\nQe 5\nQmvva 132,40,156\nQe 6\nQmvva 128,48,144\nQe 7\nQmvva 128,40,148\n"
"s_waitcnt lgkmcnt(0)\nv_cmp_gt_u32 vcc, v75, v66\nv_smfmac_f32_16x16x64_bf16 a[176:179], a[204:207], v[36:43], v77\ns_cbranch_vccnz .Lsa2\n.Lra2:\nQcvaa 8,112,48,56\ns_add_u32 m0, s61, 33024\nQk 44,4,5\n"
"Qmvaa 8,116,52\nbuffer_load_dwordx4 v69, s[40:43], s60 offen nt lds\nQmvaa 8,120,56\nQk 45,6,7\nQmvaa 8,124,60\ns_add_u32 s65, s65, 2048\nQcvaa 12,140,60,56\nQmvaa 12,136,56\n"
"Qmvaa 12,132,52\nbuffer_load_dwordx4 v69, s[40:43], s60 offen offset:1024 nt lds\nQmvaa 12,128,48\nQcvaa 0,128,64,56\ns_memrealtime s[74:75]\nQmvaa 0,132,68\nQe 8\nQmvaa 0,136,72\n"
"Qe 9\nQmvaa 0,140,76\nbuffer_load_dwordx4 v69, s[40:43], s90 offen offset:2048 nt lds\nQcvaa 4,124,76,56\nQe 10\nQmvaa 4,120,72\nQe 11\nQmvaa 4,116,68\n"
"Qe 12\nQmvaa 4,112,64\nQe 13\nQmvva 160,52,144\nbuffer_load_dwordx4 v69, s[40:43], s90 offen offset:3072 nt lds\nQmvva 160,44,148\nQe 14\nQmvva 164,52,152\n"
"Qe 15\nQmvva 164,44,156\nQk 48,8,9\nQk 49,10,11\nQmvva 168,52,160\ns_add_u32 m0, s64, 33024\nQk 50,12,13\nQmvva 168,44,164\n"
"buffer_load_dwordx4 v72, s[44:47], s60 offen nt lds\nQmvva 172,52,168\nQk 51,14,15\nQmvva 172,44,172\nQe 0\nQmvva 204,20,168\nQe 1\nQmvva 204,48,172\n"
"Qe 2\nQmvva 200,20,160\nbuffer_load_dwordx4 v72, s[44:47], s60 offen offset:1024 nt lds\nQmvva 200,48,164\nQe 3\nQmvva 196,20,152\nQk 18,0,1\nQk 19,2,3\n"
"Qmvva 196,48,156\nQe 4\nQmvva 192,20,144\nQe 5\nQmvva 192,48,148\nbuffer_load_dwordx4 v72, s[44:47], s90 offen offset:2048 nt lds\nv_smfmac_f32_16x16x64_bf16 a[180:183], a[204:207], v[44:51], v77\nQcvaa 8,112,80,56\n"
"Qe 6\nQmvaa 8,116,84\nQe 7\nQmvaa 8,120,88\nQk 16,4,5\nQk 17,6,7\nQmvaa 8,124,92\nbuffer_load_dwordx4 v72, s[44:47], s90 offen offset:3072 nt lds\n"
"Qcvaa 12,140,92,56\ns_waitcnt lgkmcnt(0)\ns_cmp_gt_u32 s77, s74\nQmvaa 12,136,88\ns_cbranch_scc1 .Lsp2\n.Lrp2:\nQmvaa 12,132,84\ns_add_u32 s77, s74, 220\n"
"Qmvaa 12,128,80\ns_xor_b32 s60, s65, %1\ns_add_u32 s90, s60, 260096\nQcvaa 0,128,96,56\nQda 128,68,34048\ns_add_u32 s79, s79, s31\nQmvaa 0,132,100\nQda 132,68,34560\n"
"s_max_u32 s77, s77, s79\nQmvaa 0,136,104\nQda 136,68,34304\nQmvaa 0,140,108\nQda 140,68,34816\nQcvaa 4,124,108,56\nQda 124,68,33792\nQmvaa 4,120,104\n"
"Qda 120,68,33280\nQmvaa 4,116,100\nQda 116,68,33536\nQmvaa 4,112,96\nQda 112,68,33024\nQmvva 224,24,144\nQe 8\nQmvva 224,16,148\n"
"Qe 9\nQmvva 228,24,152\nQe 10\nQmvva 228,16,156\nQe 11\nQmvva 232,24,160\nQk 20,8,9\nQk 21,10,11\n"
"Qmvva 232,16,164\nQe 12\nQmvva 236,24,168\nQe 13\nQmvva 236,16,172\ns_waitcnt lgkmcnt(0)\nQk 22,12,13\nQcvaa 8,112,0,56\n"
"Qe 14\nQmvaa 8,116,4\nv_subrev_u32 v68, 66048, v68\nQmvaa 8,120,8\nQe 15\nQmvaa 8,124,12\nQk 23,14,15\nQcvaa 12,140,12,56\n"
"Qe 0\nQmvaa 12,136,8\nQe 1\nQmvaa 12,132,4\nQe 2\nQmvaa 12,128,0\nQe 3\nv_smfmac_f32_16x16x64_bf16 a[184:187], a[204:207], v[16:23], v77\n"
"Qmava 208,28,144\nQe 4\nQmava 208,20,148\nQe 5\nQmava 212,28,152\nQe 6\nQmava 212,20,156\nQe 7\n"
"Qmava 216,28,160\nQk 26,0,1\nQk 27,2,3\nQmava 216,20,164\nQk 24,4,5\nQk 25,6,7\nQmava 220,28,168\nQe 8\n"
"Qmava 220,20,172\nQe 9\nQmava 252,32,168\nQta 168,71,768\nQta 170,71,1792\nQmava 252,24,172\nQe 10\nQmava 248,32,160\n"
"Qta 172,71,33792\nQta 174,71,34816\nQmava 248,24,164\nQe 11\nQmava 244,32,152\nQta 160,71,512\nQta 162,71,1536\nQmava 244,24,156\n"
"Qk 28,8,9\nQk 29,10,11\nQmava 240,32,144\nQta 164,71,33536\nQta 166,71,34560\nQmava 240,24,148\nQe 12\nQcvaa 0,112,16,56\n"
"Qta 152,71,256\nQta 154,71,1280\nQmvaa 0,116,20\nQe 13\nQmvaa 0,120,24\nQta 156,71,33280\nQta 158,71,34304\nQmvaa 0,124,28\n"
"Qe 14\nQcvaa 4,140,28,56\nQta 144,71,0\nQta 146,71,1024\nQmvaa 4,136,24\nQe 15\nQmvaa 4,132,20\nQta 148,71,33024\n"
"Qta 150,71,34048\nQmvaa 4,128,16\nQk 30,12,13\nQk 31,14,15\nQcvaa 8,128,32,56\nv_add_u32 v75, 1, v75\nQmvaa 8,132,36\ns_waitcnt vmcnt(8)\n"
"ds_write_b32 v73, v75\nQmvaa 8,136,40\nQe 0\nQmvaa 8,140,44\nQe 1\nQcvaa 12,124,44,56\nQe 2\nQmvaa 12,120,40\n"
"Qe 3\nQmvaa 12,116,36\nQe 4\nQmvaa 12,112,32\ns_waitcnt lgkmcnt(1)\nQk 32,0,1\nv_smfmac_f32_16x16x64_bf16 a[188:191], a[204:207], v[24:31], v77\nQmvva 80,36,144\n"
"Qe 5\nQmvva 80,28,148\nQe 6\nQmvva 84,36,152\nQe 7\nQmvva 84,28,156\nQk 33,2,3\nQk 34,4,5\n"
"Qmvva 88,36,160\nQk 35,6,7\nQmvva 88,28,164\nQe 8\nQmvva 92,36,168\nQe 9\nQmvva 92,28,172\nQe 10\n"
"Qmvva 124,40,168\nQe 11\nQmvva 124,32,172\nds_read_b32 v66, v74\nQk 38,8,9\nQmvva 120,40,160\nQk 39,10,11\nQmvva 120,32,164\n"
"Qe 12\nQmvva 116,40,152\nQe 13\nQmvva 116,32,156\nQe 14\nQmvva 112,40,144\ns_waitcnt lgkmcnt(0)\nv_cmp_gt_u32 vcc, v75, v66\n"
"Qmvva 112,32,148\ns_cbranch_vccnz .Lsa3\n.Lra3:\nQcvaa 0,112,48,56\ns_add_u32 m0, s61, 66048\nQk 36,12,13\nQmvaa 0,116,52\nbuffer_load_dwordx4 v69, s[40:43], s60 offen nt lds\n"
"Qmvaa 0,120,56\nQe 15\nQmvaa 0,124,60\ns_add_u32 s65, s65, 2048\nQk 37,14,15\nQcvaa 4,140,60,56\nQmvaa 4,136,56\nQmvaa 4,132,52\n"
"buffer_load_dwordx4 v69, s[40:43], s60 offen offset:1024 nt lds\nQmvaa 4,128,48\nQcvaa 8,128,64,56\ns_nop 0\nQmvaa 8,132,68\nQe 0\nQmvaa 8,136,72\nQe 1\n"
"Qmvaa 8,140,76\nbuffer_load_dwordx4 v69, s[40:43], s90 offen offset:2048 nt lds\nQcvaa 12,124,76,56\nQe 2\nQmvaa 12,120,72\nQe 3\nQmvaa 12,116,68\nQe 4\n"
"Qmvaa 12,112,64\nQe 5\nv_smfmac_f32_16x16x64_bf16 a[192:195], a[204:207], v[32:39], v77\nQmvva 144,44,144\nbuffer_load_dwordx4 v69, s[40:43], s90 offen offset:3072 nt lds\nQmvva 144,36,148\nQe 6\nQmvva 148,44,152\n"
"Qe 7\nQmvva 148,36,156\ns_add_u32 m0, s64, 66048\nQk 40,0,1\nQmvva 152,44,160\nbuffer_load_dwordx4 v72, s[44:47], s60 offen nt lds\nQmvva 152,36,164\nQk 41,2,3\n"
"Qk 42,4,5\nQmvva 156,44,168\nQk 43,6,7\nQmvva 156,36,172\nQe 8\nQmvva 188,48,168\nQe 9\nQmvva 188,40,172\n"
"buffer_load_dwordx4 v72, s[44:47], s60 offen offset:1024 nt lds\nQmvva 184,48,160\nQe 10\nQmvva 184,40,164\nQe 11\nQmvva 180,48,152\nQk 46,8,9\nQk 47,10,11\n"
"Qmvva 180,40,156\nQe 12\nQmvva 176,48,144\nbuffer_load_dwordx4 v72, s[44:47], s90 offen offset:2048 nt lds\nQmvva 176,40,148\nQe 13\nQcvaa 0,112,80,56\nQe 14\n"
"Qmvaa 0,116,84\nQe 15\nQmvaa 0,120,88\nQk 44,12,13\nQk 45,14,15\nQmvaa 0,124,92\nbuffer_load_dwordx4 v72, s[44:47], s90 offen offset:3072 nt lds\nQcvaa 4,140,92,56\n"
"Qmvaa 4,136,88\nQmvaa 4,132,84\nQmvaa 4,128,80\ns_xor_b32 s60, s65, %1\ns_add_u32 s90, s60, 260096\nQcvaa 8,128,96,56\nQda 128,68,1024\nQmvaa 8,132,100\n"
"Qda 132,68,1536\nQmvaa 8,136,104\nQda 136,68,1280\nQmvaa 8,140,108\nQda 140,68,1792\nQcvaa 12,124,108,56\nQda 124,68,768\nQmvaa 12,120,104\n"
"Qda 120,68,256\nQmvaa 12,116,100\nQda 116,68,512\nQmvaa 12,112,96\nQda 112,68,0\nv_smfmac_f32_16x16x64_bf16 a[196:199], a[204:207], v[40:47], v77\nQmvva 208,16,144\nQe 0\n"
"Qmvva 208,44,148\nQe 1\nQmvva 212,16,152\nQe 2\nQmvva 212,44,156\nQe 3\nQmvva 216,16,160\nQk 48,0,1\n"
"Qk 49,2,3\nQmvva 216,44,164\nQe 4\nQmvva 220,16,168\nQe 5\nQmvva 220,44,172\ns_waitcnt lgkmcnt(0)\nQk 50,4,5\n"
"Qcvaa 0,112,0,56\nQe 6\nQmvaa 0,116,4\nQe 7\nQmvaa 0,120,8\nQk 51,6,7\nQmvaa 0,124,12\nQe 8\n"
"Qcvaa 4,140,12,56\nQe 9\nQmvaa 4,136,8\nQe 10\nQmvaa 4,132,4\nQe 11\nQmvaa 4,128,0\nQe 12\n"
"Qmvva 240,20,144\nQe 13\nQmvva 240,48,148\nQe 14\nQmvva 244,20,152\nQe 15\nQmvva 244,48,156\nQk 54,8,9\n"
"Qk 55,10,11\nQmvva 248,20,160\nQk 52,12,13\nQk 53,14,15\nQmvva 248,48,164\nQe 0\nQmvva 252,20,168\nQe 1\n"
"Qmvva 252,48,172\nQe 2\nQmava 236,24,168\nQta 168,71,33920\nQta 170,71,34944\nQmava 236,52,172\nQe 3\nQmava 232,24,160\n"
"Qta 172,70,896\nQta 174,70,1920\nQmava 232,52,164\nQk 16,0,1\nQk 17,2,3\nQmava 228,24,152\nQta 160,71,33664\nQta 162,71,34688\n"
"Qmava 228,52,156\nQe 4\nQmava 224,24,144\nQta 164,70,640\nQta 166,70,1664\nQmava 224,52,148\nQe 5\nv_smfmac_f32_16x16x64_bf16 a[200:203], a[204:207], v[48:55], v77\n"
"Qta 152,71,33408\nQta 154,71,34432\nQcvaa 8,112,16,56\nQe 6\nQmvaa 8,116,20\nQta 156,70,384\nQta 158,70,1408\nQmvaa 8,120,24\n"
"Qe 7\nQmvaa 8,124,28\nQta 144,71,33152\nQta 146,71,34176\nQcvaa 12,140,28,56\nQk 18,4,5\nQk 19,6,7\nQmvaa 12,136,24\n"
"Qta 148,70,128\nQta 150,70,1152\nQmvaa 12,132,20\nQmvaa 12,128,16\nQcvaa 0,128,32,56\nv_add_u32 v75, 1, v75\nQmvaa 0,132,36\ns_waitcnt vmcnt(8)\n"
"ds_write_b32 v73, v75\nQmvaa 0,136,40\nQe 8\nQmvaa 0,140,44\nQe 9\nQcvaa 4,124,44,56\nQe 10\nQmvaa 4,120,40\n"
"Qe 11\nQmvaa 4,116,36\nQe 12\nQmvaa 4,112,32\ns_waitcnt lgkmcnt(1)\nQk 20,8,9\nQmvva 96,28,144\nQe 13\n"
"Qmvva 96,16,148\nQe 14\nQmvva 100,28,152\nQe 15\nQmvva 100,16,156\nQk 21,10,11\nQk 22,12,13\nQmvva 104,28,160\n"
"Qk 23,14,15\nQmvva 104,16,164\nQe 0\nQmvva 108,28,168\nQe 1\nQmvva 108,16,172\nQe 2\nQmvva 140,32,168\n"
"Qe 3\nQmvva 140,20,172\nQk 26,0,1\nQk 27,2,3\nQmvva 136,32,160\nds_read_b32 v66, v74\nQmvva 136,20,164\nQe 4\n"
"Qmvva 132,32,152\nQe 5\nQmvva 132,20,156\nQe 6\nQmvva 128,32,144\nQe 7\nQmvva 128,20,148\ns_waitcnt lgkmcnt(0)\n"
"v_cmp_gt_u32 vcc, v75, v66\nv_smfmac_f32_16x16x64_bf16 a[176:179], a[204:207], v[16:23], v77\ns_cbranch_vccnz .Lsa4\n.Lra4:\nQcvaa 8,112,48,56\ns_add_u32 m0, s61, 99072\nQk 24,4,5\nQmvaa 8,116,52\n"
"buffer_load_dwordx4 v69, s[40:43], s60 offen nt lds\nQmvaa 8,120,56\nQk 25,6,7\nQmvaa 8,124,60\ns_or_b32 s65, s65, 0x1d8000\ns_add_u32 s65, s65, 2048\nQcvaa 12,140,60,56\ns_andn2_b32 s65, s65, 0x1d8000\n"
"Qmvaa 12,136,56\nQmvaa 12,132,52\nbuffer_load_dwordx4 v69, s[40:43], s60 offen offset:1024 nt lds\nQmvaa 12,128,48\nQcvaa 0,128,64,56\ns_memrealtime s[74:75]\nQmvaa 0,132,68\nQe 8\n"
"Qmvaa 0,136,72\nQe 9\nQmvaa 0,140,76\nbuffer_load_dwordx4 v69, s[40:43], s90 offen offset:2048 nt lds\nQcvaa 4,124,76,56\nQe 10\nQmvaa 4,120,72\nQe 11\n"
"Qmvaa 4,116,68\nQe 12\nQmvaa 4,112,64\nQe 13\nQmvva 160,36,144\nbuffer_load_dwordx4 v69, s[40:43], s90 offen offset:3072 nt lds\nQmvva 160,24,148\nQe 14\n"
"Qmvva 164,36,152\nQe 15\nQmvva 164,24,156\nQk 28,8,9\nQk 29,10,11\nQmvva 168,36,160\ns_add_u32 m0, s64, 99072\nQk 30,12,13\n"
"Qmvva 168,24,164\nbuffer_load_dwordx4 v72, s[44:47], s60 offen nt lds\nQmvva 172,36,168\nQk 31,14,15\nQmvva 172,24,172\nQe 0\nQmvva 204,40,168\nQe 1\n"
"Qmvva 204,28,172\nQe 2\nQmvva 200,40,160\nbuffer_load_dwordx4 v72, s[44:47], s60 offen offset:1024 nt lds\nQmvva 200,28,164\nQe 3\nQmvva 196,40,152\nQk 34,0,1\n"
"Qk 35,2,3\nQmvva 196,28,156\nQe 4\nQmvva 192,40,144\nQe 5\nQmvva 192,28,148\nbuffer_load_dwordx4 v72, s[44:47], s90 offen offset:2048 nt lds\nv_smfmac_f32_16x16x64_bf16 a[180:183], a[204:207], v[24:31], v77\n"
"Qcvaa 8,112,80,56\nQe 6\nQmvaa 8,116,84\nQe 7\nQmvaa 8,120,88\nQk 32,4,5\nQk 33,6,7\nQmvaa 8,124,92\n"
"buffer_load_dwordx4 v72, s[44:47], s90 offen offset:3072 nt lds\nQcvaa 12,140,92,56\ns_waitcnt lgkmcnt(0)\ns_cmp_gt_u32 s77, s74\nQmvaa 12,136,88\ns_cbranch_scc1 .Lsp4\n.Lrp4:\nQmvaa 12,132,84\n"
"s_add_u32 s77, s74, 220\nQmvaa 12,128,80\ns_xor_b32 s60, s65, %1\ns_add_u32 s90, s60, 260096\nQcvaa 0,128,96,56\nQda 128,68,34048\ns_add_u32 s79, s79, s31\nQmvaa 0,132,100\n"
"Qda 132,68,34560\ns_max_u32 s77, s77, s79\nQmvaa 0,136,104\nQda 136,68,34304\nQmvaa 0,140,108\nQda 140,68,34816\nQcvaa 4,124,108,56\nQda 124,68,33792\n"
"Qmvaa 4,120,104\nQda 120,68,33280\nQmvaa 4,116,100\nQda 116,68,33536\nQmvaa 4,112,96\nQda 112,68,33024\nQmvva 224,44,144\nQe 8\n"
"Qmvva 224,32,148\nQe 9\nQmvva 228,44,152\nQe 10\nQmvva 228,32,156\nQe 11\nQmvva 232,44,160\nQk 36,8,9\n"
"Qk 37,10,11\nQmvva 232,32,164\nQe 12\nQmvva 236,44,168\nQe 13\nQmvva 236,32,172\ns_waitcnt lgkmcnt(0)\nQk 38,12,13\n"
"Qcvaa 8,112,0,56\nQe 14\nQmvaa 8,116,4\nv_add_u32 v68, 66048, v68\nQmvaa 8,120,8\nQe 15\nQmvaa 8,124,12\nQk 39,14,15\n"
"Qcvaa 12,140,12,56\nQe 0\nQmvaa 12,136,8\nQe 1\nQmvaa 12,132,4\nQe 2\nQmvaa 12,128,0\nQe 3\n"
"v_smfmac_f32_16x16x64_bf16 a[184:187], a[204:207], v[32:39], v77\nQmava 208,48,144\nQe 4\nQmava 208,36,148\nQe 5\nQmava 212,48,152\nQe 6\nQmava 212,36,156\n"
"Qe 7\nQmava 216,48,160\nQk 42,0,1\nQk 43,2,3\nQmava 216,36,164\nQk 40,4,5\nQk 41,6,7\nQmava 220,48,168\n"
"Qe 8\nQmava 220,36,172\nQe 9\nQmava 252,52,168\nQta 168,70,768\nQta 170,70,1792\nQmava 252,40,172\nQe 10\n"
"Qmava 248,52,160\nQta 172,70,33792\nQta 174,70,34816\nQmava 248,40,164\nQe 11\nQmava 244,52,152\nQta 160,70,512\nQta 162,70,1536\n"
"Qmava 244,40,156\nQk 44,8,9\nQk 45,10,11\nQmava 240,52,144\nQta 164,70,33536\nQta 166,70,34560\nQmava 240,40,148\nQe 12\n"
"Qcvaa 0,112,16,56\nQta 152,70,256\nQta 154,70,1280\nQmvaa 0,116,20\nQe 13\nQmvaa 0,120,24\nQta 156,70,33280\nQta 158,70,34304\n"
"Qmvaa 0,124,28\nQe 14\nQcvaa 4,140,28,56\nQta 144,70,0\nQta 146,70,1024\nQmvaa 4,136,24\nQe 15\nQmvaa 4,132,20\n"
"Qta 148,70,33024\nQta 150,70,34048\nQmvaa 4,128,16\nQk 46,12,13\nQk 47,14,15\nQcvaa 8,128,32,56\nv_add_u32 v75, 1, v75\nQmvaa 8,132,36\n"
"s_waitcnt vmcnt(8)\nds_write_b32 v73, v75\nQmvaa 8,136,40\nQe 0\nQmvaa 8,140,44\nQe 1\nQcvaa 12,124,44,56\nQe 2\n"
"Qmvaa 12,120,40\nQe 3\nQmvaa 12,116,36\nQe 4\nQmvaa 12,112,32\ns_waitcnt lgkmcnt(1)\nQk 48,0,1\nv_smfmac_f32_16x16x64_bf16 a[188:191], a[204:207], v[40:47], v77\n"
"Qmvva 80,16,144\nQe 5\nQmvva 80,44,148\nQe 6\nQmvva 84,16,152\nQe 7\nQmvva 84,44,156\nQk 49,2,3\n"
"Qk 50,4,5\nQmvva 88,16,160\nQk 51,6,7\nQmvva 88,44,164\nQe 8\nQmvva 92,16,168\nQe 9\nQmvva 92,44,172\n"
"Qe 10\nQmvva 124,20,168\nQe 11\nQmvva 124,48,172\nds_read_b32 v66, v74\nQk 54,8,9\nQmvva 120,20,160\nQk 55,10,11\n"
"Qmvva 120,48,164\nQe 12\nQmvva 116,20,152\nQe 13\nQmvva 116,48,156\nQe 14\nQmvva 112,20,144\ns_waitcnt lgkmcnt(0)\n"
"v_cmp_gt_u32 vcc, v75, v66\nQmvva 112,48,148\ns_cbranch_vccnz .Lsa5\n.Lra5:\nQcvaa 0,112,48,56\ns_add_u32 m0, s61, 0\nQk 52,12,13\nQmvaa 0,116,52\n"
"buffer_load_dwordx4 v69, s[40:43], s60 offen nt lds\nQmvaa 0,120,56\nQe 15\nQmvaa 0,124,60\ns_add_u32 s65, s65, 2048\nQk 53,14,15\nQcvaa 4,140,60,56\nQmvaa 4,136,56\n"
"Qmvaa 4,132,52\nbuffer_load_dwordx4 v69, s[40:43], s60 offen offset:1024 nt lds\nQmvaa 4,128,48\nQcvaa 8,128,64,56\ns_nop 0\nQmvaa 8,132,68\nQe 0\nQmvaa 8,136,72\n"
"Qe 1\nQmvaa 8,140,76\nbuffer_load_dwordx4 v69, s[40:43], s90 offen offset:2048 nt lds\nQcvaa 12,124,76,56\nQe 2\nQmvaa 12,120,72\nQe 3\nQmvaa 12,116,68\n"
"Qe 4\nQmvaa 12,112,64\nQe 5\nv_smfmac_f32_16x16x64_bf16 a[192:195], a[204:207], v[48:55], v77\nQmvva 144,24,144\nbuffer_load_dwordx4 v69, s[40:43], s90 offen offset:3072 nt lds\nQmvva 144,52,148\nQe 6\n"
"Qmvva 148,24,152\nQe 7\nQmvva 148,52,156\ns_add_u32 m0, s64, 0\nQk 20,0,1\nQmvva 152,24,160\nbuffer_load_dwordx4 v72, s[44:47], s60 offen nt lds\nQmvva 152,52,164\n"
"Qk 21,2,3\nQk 22,4,5\nQmvva 156,24,168\nQk 23,6,7\nQmvva 156,52,172\nQe 8\nQmvva 188,28,168\nQe 9\n"
"Qmvva 188,20,172\nbuffer_load_dwordx4 v72, s[44:47], s60 offen offset:1024 nt lds\nQmvva 184,28,160\nQe 10\nQmvva 184,20,164\nQe 11\nQmvva 180,28,152\nQk 26,8,9\n"
"Qk 27,10,11\nQmvva 180,20,156\nQe 12\nQmvva 176,28,144\nbuffer_load_dwordx4 v72, s[44:47], s90 offen offset:2048 nt lds\nQmvva 176,20,148\nQe 13\nQcvaa 0,112,80,56\n"
"Qe 14\nQmvaa 0,116,84\nQe 15\nQmvaa 0,120,88\nQk 24,12,13\nQk 25,14,15\nQmvaa 0,124,92\nbuffer_load_dwordx4 v72, s[44:47], s90 offen offset:3072 nt lds\n"
"Qcvaa 4,140,92,56\nQmvaa 4,136,88\nQmvaa 4,132,84\nQmvaa 4,128,80\ns_xor_b32 s60, s65, %1\ns_add_u32 s90, s60, 260096\nQcvaa 8,128,96,56\nQda 128,68,1024\n"
"Qmvaa 8,132,100\nQda 132,68,1536\nQmvaa 8,136,104\nQda 136,68,1280\nQmvaa 8,140,108\nQda 140,68,1792\nQcvaa 12,124,108,56\nQda 124,68,768\n"
"Qmvaa 12,120,104\nQda 120,68,256\nQmvaa 12,116,100\nQda 116,68,512\nQmvaa 12,112,96\nQda 112,68,0\nv_smfmac_f32_16x16x64_bf16 a[196:199], a[204:207], v[20:27], v77\nQmvva 208,32,144\n"
"Qe 0\nQmvva 208,24,148\nQe 1\nQmvva 212,32,152\nQe 2\nQmvva 212,24,156\nQe 3\nQmvva 216,32,160\n"
"Qk 28,0,1\nQk 29,2,3\nQmvva 216,24,164\nQe 4\nQmvva 220,32,168\nQe 5\nQmvva 220,24,172\ns_waitcnt lgkmcnt(0)\n"
"Qk 30,4,5\nQcvaa 0,112,0,56\nQe 6\nQmvaa 0,116,4\nQe 7\nQmvaa 0,120,8\nQk 31,6,7\nQmvaa 0,124,12\n"
"Qe 8\nQcvaa 4,140,12,56\nQe 9\nQmvaa 4,136,8\nQe 10\nQmvaa 4,132,4\nQe 11\nQmvaa 4,128,0\n"
"Qe 12\nQmvva 240,36,144\nQe 13\nQmvva 240,28,148\nQe 14\nQmvva 244,36,152\nQe 15\nQmvva 244,28,156\n"
"Qk 34,8,9\nQk 35,10,11\nQmvva 248,36,160\nQk 32,12,13\nQk 33,14,15\nQmvva 248,28,164\nQe 0\nQmvva 252,36,168\n"
"Qe 1\nQmvva 252,28,172\nQe 2\nQmava 236,40,168\nQta 168,70,33920\nQta 170,70,34944\nQmava 236,32,172\nQe 3\n"
"Qmava 232,40,160\nQta 172,71,896\nQta 174,71,1920\nQmava 232,32,164\nQk 36,0,1\nQk 37,2,3\nQmava 228,40,152\nQta 160,70,33664\n"
"Qta 162,70,34688\nQmava 228,32,156\nQe 4\nQmava 224,40,144\nQta 164,71,640\nQta 166,71,1664\nQmava 224,32,148\nQe 5\n"
"v_smfmac_f32_16x16x64_bf16 a[200:203], a[204:207], v[28:35], v77\nQta 152,70,33408\nQta 154,70,34432\nQcvaa 8,112,16,56\nQe 6\nQmvaa 8,116,20\nQta 156,71,384\nQta 158,71,1408\n"
"Qmvaa 8,120,24\nQe 7\nQmvaa 8,124,28\nQta 144,70,33152\nQta 146,70,34176\nQcvaa 12,140,28,56\nQk 38,4,5\nQk 39,6,7\n"
"Qmvaa 12,136,24\nQta 148,71,128\nQta 150,71,1152\nQmvaa 12,132,20\ns_add_u32 s66, s66, 1\ns_bitcmp1_b32 32127, s66\nQmvaa 12,128,16\ns_cbranch_scc1 .Lloop\n"
"s_cmp_eq_u32 s66, 9\ns_cbranch_scc1 .Lhook\ns_cmp_lg_u32 s67, 0\ns_cbranch_scc1 .Ltail1\nQcvaa 0,128,32,56\nv_add_u32 v75, 1, v75\nv_bfe_u32 v67, v60, 4, 3\nQmvaa 0,132,36\n"
"s_waitcnt vmcnt(8)\nds_write_b32 v73, v75\nQmvaa 0,136,40\nQe 8\nQmvaa 0,140,44\nQe 9\nQcvaa 4,124,44,56\nQe 10\n"
"Qmvaa 4,120,40\nQe 11\nQmvaa 4,116,36\nQe 12\nQmvaa 4,112,32\ns_waitcnt lgkmcnt(1)\nQk 40,8,9\nQmvva 96,44,144\n"
"Qe 13\nQmvva 96,36,148\nQe 14\nQmvva 100,44,152\nQe 15\nQmvva 100,36,156\nQk 41,10,11\nQk 42,12,13\n"
"Qmvva 104,44,160\nQk 43,14,15\ns_lshl_b32 s68, s19, 7\nQmvva 104,36,164\nv_lshlrev_b32 v67, 2, v67\ns_add_u32 s68, s68, 5242880\nQmvva 108,44,168\nQe 0\n"
"Qmvva 108,36,172\nQe 1\nQmvva 140,48,168\nQe 2\nQmvva 140,40,172\nQe 3\nQmvva 136,48,160\nds_read_b32 v66, v74\n"
"Qk 46,0,1\nQmvva 136,40,164\nQk 47,2,3\nQmvva 132,48,152\nQe 4\nQmvva 132,40,156\nQe 5\nQmvva 128,48,144\n"
"Qe 6\nQmvva 128,40,148\ns_waitcnt lgkmcnt(0)\nv_cmp_gt_u32 vcc, v75, v66\nv_smfmac_f32_16x16x64_bf16 a[176:179], a[204:207], v[36:43], v77\ns_cbranch_vccnz .Lsa30\n.Lra30:\nQcvaa 8,112,48,56\n"
"Qe 7\nQmvaa 8,116,52\ns_add_u32 m0, s62, 0\ns_add_u32 s69, s87, 0\nQk 44,4,5\nQmvaa 8,120,56\nbuffer_load_dwordx4 v62, s[52:55], s69 offen lds\nQmvaa 8,124,60\n"
"Qk 45,6,7\nQcvaa 12,140,60,56\nQmvaa 12,136,56\nbuffer_load_dword v78, v67, s[24:27], s68 offen offset:1024 sc1\nQmvaa 12,132,52\nQmvaa 12,128,48\ns_add_u32 s69, s87, 64504\nQcvaa 0,128,64,56\n"
"buffer_load_dwordx4 v62, s[52:55], s69 offen offset:1032 lds\nQmvaa 0,132,68\nQe 8\nQmvaa 0,136,72\nQe 9\nQmvaa 0,140,76\ns_add_u32 s69, s87, 129008\nQk 48,8,9\n"
"Qcvaa 4,124,76,56\nbuffer_load_dwordx4 v62, s[52:55], s69 offen offset:2064 lds\nQmvaa 4,120,72\nQe 10\nQmvaa 4,116,68\nQe 11\nQmvaa 4,112,64\ns_add_u32 s69, s87, 193512\n"
"Qk 49,10,11\nQmvva 160,52,144\nbuffer_load_dwordx4 v62, s[52:55], s69 offen offset:3096 lds\nQmvva 160,44,148\nQe 12\nQmvva 164,52,152\nQe 13\nQmvva 164,44,156\n"
"s_add_u32 m0, s62, 16512\ns_add_u32 s69, s87, 128\nQk 50,12,13\nQmvva 168,52,160\nbuffer_load_dwordx4 v62, s[52:55], s69 offen lds\nQmvva 168,44,164\nQe 14\nQmvva 172,52,168\n"
"Qe 15\nQmvva 172,44,172\ns_add_u32 s69, s87, 64632\nQk 51,14,15\nQmvva 204,20,168\nbuffer_load_dwordx4 v62, s[52:55], s69 offen offset:1032 lds\nQmvva 204,48,172\nQe 0\n"
"Qmvva 200,20,160\nQe 1\nQmvva 200,48,164\ns_add_u32 s69, s87, 129136\nQk 18,0,1\nQmvva 196,20,152\nbuffer_load_dwordx4 v62, s[52:55], s69 offen offset:2064 lds\nQmvva 196,48,156\n"
"Qe 2\nQmvva 192,20,144\nQe 3\nQmvva 192,48,148\nQk 19,2,3\nv_smfmac_f32_16x16x64_bf16 a[180:183], a[204:207], v[44:51], v77\ns_add_u32 s69, s87, 193640\nQcvaa 8,112,80,56\n"
"buffer_load_dwordx4 v62, s[52:55], s69 offen offset:3096 lds\nQmvaa 8,116,84\nQe 4\nQmvaa 8,120,88\ns_add_u32 m0, s63, 33024\ns_add_u32 s88, s86, 0\nQmvaa 8,124,92\nbuffer_load_dwordx4 v64, s[48:51], s88 offen lds\n"
"Qcvaa 12,140,92,56\nQe 5\nQmvaa 12,136,88\nQe 6\nQmvaa 12,132,84\ns_add_u32 s88, s86, 19456\nQk 16,4,5\nQmvaa 12,128,80\n"
"buffer_load_dwordx4 v64, s[48:51], s88 offen offset:1024 lds\nQcvaa 0,128,96,56\nQda 128,68,34048\nQmvaa 0,132,100\nQda 132,68,34560\nQmvaa 0,136,104\nQda 136,68,34304\nQe 7\n"
"Qmvaa 0,140,108\nQda 140,68,34816\nQk 17,6,7\nQcvaa 4,124,108,56\nQda 124,68,33792\nQmvaa 4,120,104\nQda 120,68,33280\nQmvaa 4,116,100\n"
"Qda 116,68,33536\nQmvaa 4,112,96\nQda 112,68,33024\ns_add_u32 s88, s86, 38912\nQmvva 224,24,144\nbuffer_load_dwordx4 v64, s[48:51], s88 offen offset:2048 lds\nQmvva 224,16,148\nQe 8\n"
"Qmvva 228,24,152\nQe 9\nQmvva 228,16,156\nQe 10\nQmvva 232,24,160\ns_add_u32 s88, s86, 58368\nQk 20,8,9\nQmvva 232,16,164\n"
"buffer_load_dwordx4 v64, s[48:51], s88 offen offset:3072 lds\nQmvva 236,24,168\nQe 11\nQmvva 236,16,172\ns_waitcnt lgkmcnt(0)\nQk 21,10,11\nQcvaa 8,112,0,56\nQe 12\n"
"Qmvaa 8,116,4\nv_subrev_u32 v68, 66048, v68\ns_add_u32 m0, s63, 37120\ns_add_u32 s88, s86, 81920\nQmvaa 8,120,8\nbuffer_load_dwordx4 v64, s[48:51], s88 offen lds\nQe 13\nQe 14\n"
"Qe 15\nQmvaa 8,124,12\nQk 22,12,13\nQk 23,14,15\nQcvaa 12,140,12,56\nQe 0\nQmvaa 12,136,8\nQe 1\n"
"Qmvaa 12,132,4\nQe 2\nQmvaa 12,128,0\nQe 3\nv_smfmac_f32_16x16x64_bf16 a[184:187], a[204:207], v[16:23], v77\nQmava 208,28,144\ns_add_u32 s88, s86, 101376\nQk 26,0,1\n"
"Qmava 208,20,148\nbuffer_load_dwordx4 v64, s[48:51], s88 offen offset:1024 lds\nQmava 212,28,152\nQe 4\nQmava 212,20,156\nQe 5\nQmava 216,28,160\nQe 6\n"
"Qmava 216,20,164\nQe 7\nQmava 220,28,168\nQk 27,2,3\nQk 24,4,5\nQmava 220,20,172\nQk 25,6,7\nQmava 252,32,168\n"
"Qta 168,71,768\nQta 170,71,1792\nQmava 252,24,172\nQe 8\nQmava 248,32,160\nQta 172,71,33792\nQta 174,71,34816\nQmava 248,24,164\n"
"Qe 9\nQmava 244,32,152\nQta 160,71,512\nQta 162,71,1536\nQmava 244,24,156\nQe 10\nQmava 240,32,144\nQta 164,71,33536\n"
"Qta 166,71,34560\nQmava 240,24,148\nQe 11\nQcvaa 0,112,16,56\nQta 152,71,256\nQta 154,71,1280\ns_add_u32 s88, s86, 120832\nQmvaa 0,116,20\n"
"buffer_load_dwordx4 v64, s[48:51], s88 offen offset:2048 lds\nQmvaa 0,120,24\nQta 156,71,33280\nQta 158,71,34304\nQmvaa 0,124,28\nQk 28,8,9\nQk 29,10,11\nQcvaa 4,140,28,56\n"
"Qta 144,71,0\nQta 146,71,1024\nQmvaa 4,136,24\nQe 12\nQmvaa 4,132,20\nQta 148,71,33024\nQta 150,71,34048\nQmvaa 4,128,16\n"
"Qe 13\nQcvaa 8,128,32,56\nv_add_u32 v75, 1, v75\nQk 30,12,13\nQmvaa 8,132,36\nds_write_b32 v73, v75\nQmvaa 8,136,40\nQe 14\n"
"Qe 15\nQmvaa 8,140,44\nQk 31,14,15\nQcvaa 12,124,44,56\nQe 0\nQmvaa 12,120,40\nQe 1\nQmvaa 12,116,36\n"
"Qe 2\nQmvaa 12,112,32\ns_waitcnt lgkmcnt(1)\nQk 32,0,1\nv_smfmac_f32_16x16x64_bf16 a[188:191], a[204:207], v[24:31], v77\nQmvva 80,36,144\nQe 3\nQmvva 80,28,148\n"
"Qe 4\nQmvva 84,36,152\nQe 5\nQmvva 84,28,156\nQe 6\nQmvva 88,36,160\nQe 7\nQmvva 88,28,164\n"
"Qk 33,2,3\nQk 34,4,5\nQmvva 92,36,168\nQk 35,6,7\nQmvva 92,28,172\nQe 8\nQmvva 124,40,168\nQe 9\n"
"Qmvva 124,32,172\nds_read_b32 v66, v74\nQk 38,8,9\nQmvva 120,40,160\nQe 10\nQmvva 120,32,164\nQe 11\nQmvva 116,40,152\n"
"Qk 39,10,11\nQmvva 116,32,156\nQe 12\nQmvva 112,40,144\ns_waitcnt lgkmcnt(0)\nv_cmp_gt_u32 vcc, v75, v66\nQmvva 112,32,148\ns_cbranch_vccnz .Lsa31\n"
".Lra31:\nQcvaa 0,112,48,56\nQe 13\nQmvaa 0,116,52\nQe 14\nQmvaa 0,120,56\ns_add_u32 m0, s62, 33024\ns_add_u32 s69, s87, 524288\n"
"Qk 36,12,13\nQmvaa 0,124,60\nbuffer_load_dwordx4 v62, s[52:55], s69 offen lds\nQcvaa 4,140,60,56\nQe 15\nQmvaa 4,136,56\nQk 37,14,15\nQmvaa 4,132,52\n"
"s_waitcnt vmcnt(15)\nv_xor_b32 v78, s30, v78\nv_and_b32 v66, 0x0fffffff, v78\nv_cmp_ne_u32 vcc, 0, v66\ns_cmp_eq_u64 vcc, 0\ns_cbranch_scc0 .Lack_w\n.Lack_ok:\nQmvaa 4,128,48\n"
"s_add_u32 s69, s87, 588792\nv_lshrrev_b32 v78, 28, v78\nQcvaa 8,128,64,56\nbuffer_load_dwordx4 v62, s[52:55], s69 offen offset:1032 lds\nQmvaa 8,132,68\nQe 0\nQmvaa 8,136,72\nQe 1\n"
"Qmvaa 8,140,76\ns_add_u32 s69, s87, 653296\nQk 40,0,1\nQcvaa 12,124,76,56\nbuffer_load_dwordx4 v62, s[52:55], s69 offen offset:2064 lds\nQmvaa 12,120,72\nQe 2\nQmvaa 12,116,68\n"
"Qe 3\nQmvaa 12,112,64\nQe 4\nv_smfmac_f32_16x16x64_bf16 a[192:195], a[204:207], v[32:39], v77\ns_add_u32 s69, s87, 717800\nQmvva 144,44,144\nbuffer_load_dwordx4 v62, s[52:55], s69 offen offset:3096 lds\nQmvva 144,36,148\n"
"Qe 5\nQmvva 148,44,152\nQe 6\nQmvva 148,36,156\nQe 7\nQmvva 152,44,160\nQk 41,2,3\nQk 42,4,5\n"
"Qmvva 152,36,164\ns_add_u32 m0, s62, 49536\ns_add_u32 s69, s87, 524416\nQk 43,6,7\nQmvva 156,44,168\nbuffer_load_dwordx4 v62, s[52:55], s69 offen lds\nQmvva 156,36,172\nQe 8\n"
"Qmvva 188,48,168\nQe 9\nQmvva 188,40,172\nQe 10\nQmvva 184,48,160\ns_add_u32 s69, s87, 588920\nQk 46,8,9\nQmvva 184,40,164\n"
"buffer_load_dwordx4 v62, s[52:55], s69 offen offset:1032 lds\nQmvva 180,48,152\nQe 11\nQmvva 180,40,156\nQk 47,10,11\nv_add_u32_dpp v78, v78, v78 quad_perm:[1,0,3,2] row_mask:0xf bank_mask:0xf\nQmvva 176,48,144\ns_add_u32 s69, s87, 653424\n"
"Qmvva 176,40,148\nbuffer_load_dwordx4 v62, s[52:55], s69 offen offset:2064 lds\nQcvaa 0,112,80,56\nQe 12\nQmvaa 0,116,84\nQe 13\nQmvaa 0,120,88\nQe 14\n"
"Qmvaa 0,124,92\nQe 15\nQcvaa 4,140,92,56\nQk 44,12,13\nQk 45,14,15\nQmvaa 4,136,88\ns_add_u32 s69, s87, 717928\nv_add_u32_dpp v78, v78, v78 quad_perm:[2,3,0,1] row_mask:0xf bank_mask:0xf\n"
"Qmvaa 4,132,84\nbuffer_load_dwordx4 v62, s[52:55], s69 offen offset:3096 lds\nQmvaa 4,128,80\nv_readlane_b32 s68, v78, s19\nv_readlane_b32 s33, v78, 4\nQcvaa 8,128,96,56\ns_cmp_lt_u32 s68, 32\ns_cselect_b32 s68, 8192, 0\n"
"Qmvaa 8,132,100\nQe 0\n.Lpg6:\ns_cbranch_execz .Ltr9\nQmvaa 8,136,104\nQe 1\nQmvaa 8,140,108\nQe 2\n"
"Qcvaa 12,124,108,56\nQe 3\nQmvaa 12,120,104\nQk 48,0,1\nQk 49,2,3\nQmvaa 12,116,100\ns_xor_b32 s68, s68, s98\ns_add_u32 s71, s71, s68\n"
"Qmvaa 12,112,96\ns_cmp_lt_u32 s33, 32\ns_cselect_b32 s33, 8, 0\nv_smfmac_f32_16x16x64_bf16 a[196:199], a[204:207], v[40:47], v77\nQmvva 208,16,144\nQta 112,71,33152\ns_xor_b32 s33, s33, %0\nQmvva 208,44,148\n"
"Qta 114,71,34176\ns_and_b32 s33, s33, 8\nQmvva 212,16,152\nQta 116,71,33408\ns_lshl_b32 s98, s33, 10\nQmvva 212,44,156\nQta 118,71,34432\nQmvva 216,16,160\n"
"Qta 120,71,33664\nQmvva 216,44,164\nQta 122,71,34688\nQmvva 220,16,168\nQta 124,71,33920\nQmvva 220,44,172\nQta 126,71,34944\nQe 4\n"
"Qe 5\nQe 6\nQe 7\nQk 50,4,5\nQk 51,6,7\nQmvva 240,20,144\nv_add_u32 v75, 1, v75\nQmvva 240,48,148\n"
"ds_write_b32 v73, v75\nQmvva 244,20,152\nQe 8\nQmvva 244,48,156\nQe 9\nQmvva 248,20,160\nQe 10\nQmvva 248,48,164\n"
"Qe 11\nQmvva 252,20,168\nQk 54,8,9\nQk 55,10,11\nQmvva 252,48,172\nQe 12\nQe 13\nQe 14\n"
"Qe 15\nQk 52,12,13\nQk 53,14,15\ns_nop 1\nv_smfmac_f32_16x16x64_bf16 a[200:203], a[204:207], v[48:55], v77\nQmava 224,24,144\nds_read_b32 v66, v74\nv_accvgpr_read_b32 v0, a176\n"
"Qmava 224,52,148\nv_accvgpr_read_b32 v1, a177\nv_accvgpr_read_b32 v6, a178\nQmava 228,24,152\nv_accvgpr_read_b32 v7, a179\nv_accvgpr_read_b32 v8, a180\nQmava 228,52,156\nv_accvgpr_read_b32 v9, a181\n"
"v_accvgpr_read_b32 v14, a182\nQmava 232,24,160\nv_accvgpr_read_b32 v15, a183\nv_accvgpr_read_b32 v16, a184\nQmava 232,52,164\ns_waitcnt lgkmcnt(0)\nv_cmp_gt_u32 vcc, v75, v66\nQmava 236,24,168\n"
"s_cbranch_vccnz .Lsa32\n.Lra32:\nQmava 236,52,172\ns_waitcnt lgkmcnt(0)\nv_accvgpr_read_b32 v17, a185\nQmvva 96,28,112\ns_add_u32 m0, s63, 0\ns_add_u32 s88, s86, 128\n"
"v_accvgpr_read_b32 v22, a186\nQmvva 100,28,116\nbuffer_load_dwordx4 v64, s[48:51], s88 offen lds\nQmvva 104,28,120\nv_accvgpr_read_b32 v23, a187\nv_accvgpr_read_b32 v24, a188\nQmvva 108,28,124\nv_accvgpr_read_b32 v25, a189\n"
"v_accvgpr_read_b32 v2, a190\nQmvva 128,32,112\ns_add_u32 s88, s86, 19584\nv_accvgpr_read_b32 v3, a191\nQmvva 132,32,116\nbuffer_load_dwordx4 v64, s[48:51], s88 offen offset:1024 lds\nQmvva 136,32,120\nv_accvgpr_read_b32 v4, a192\n"
"v_accvgpr_read_b32 v5, a193\nQmvva 140,32,124\nv_accvgpr_read_b32 v10, a194\nv_accvgpr_read_b32 v11, a195\nQmvva 160,36,112\ns_add_u32 s88, s86, 39040\nv_accvgpr_read_b32 v12, a196\nQmvva 164,36,116\n"
"buffer_load_dwordx4 v64, s[48:51], s88 offen offset:2048 lds\nQmvva 168,36,120\nv_accvgpr_read_b32 v13, a197\nv_accvgpr_read_b32 v18, a198\nQmvva 172,36,124\nv_accvgpr_read_b32 v19, a199\nv_accvgpr_read_b32 v20, a200\nQmvva 192,40,112\n"
"s_add_u32 s88, s86, 58496\nv_accvgpr_read_b32 v21, a201\nQmvva 196,40,116\nbuffer_load_dwordx4 v64, s[48:51], s88 offen offset:3072 lds\nQmvva 200,40,120\nv_accvgpr_read_b32 v26, a202\nv_accvgpr_read_b32 v27, a203\nQmvva 204,40,124\n"
"Qa 8,8,9\nQa 10,10,11\nQmvva 224,44,112\ns_add_u32 m0, s63, 4096\ns_add_u32 s88, s86, 82048\nQa 8,8,10\nQmvva 228,44,116\nbuffer_load_dwordx4 v64, s[48:51], s88 offen lds\n"
"Qmvva 232,44,120\nQa 16,16,17\nQa 18,18,19\nQmvva 236,44,124\nQa 16,16,18\nQa 4,4,5\nQmava 208,48,112\ns_add_u32 s88, s86, 101504\n"
"Qa 6,6,7\nQmava 212,48,116\nbuffer_load_dwordx4 v64, s[48:51], s88 offen offset:1024 lds\nQmava 216,48,120\nQa 4,4,6\nQa 12,12,13\nQmava 220,48,124\nQa 14,14,15\n"
"Qa 12,12,14\nQmava 240,52,112\ns_add_u32 s88, s86, 120960\nQa 0,0,1\nQmava 244,52,116\nbuffer_load_dwordx4 v64, s[48:51], s88 offen offset:2048 lds\nQmava 248,52,120\nQa 2,2,3\n"
"Qa 0,0,2\nQmava 252,52,124\nQa 24,24,25\nQa 26,26,27\nQa 24,24,26\nQa 20,20,21\nQa 22,22,23\nQa 20,20,22\n"
"v_max3_u32 v9, v8, v16, v4\nv_max3_u32 v9, v9, v12, v0\nv_max3_u32 v9, v9, v24, v20\nv_min3_u32 v11, v8, v16, v4\nv_min3_u32 v11, v11, v12, v0\nv_min3_u32 v11, v11, v24, v20\nv_cmp_lt_u32 vcc, 0x4e800000, v9\ns_mov_b64 s[74:75], vcc\n"
"v_cmp_gt_u32 vcc, 0x35800000, v11\ns_or_b64 s[74:75], s[74:75], vcc\nv_mov_b32 v30, s73\nv_mov_b32 v31, 0\nv_mov_b32 v58, 0xb9800000\nv_mov_b32 v59, 0\nv_and_b32 v66, 16, v60\nv_mov_b32 v67, 0xffff\n"
"v_lshlrev_b32 v66, v66, v67\ns_mov_b32 vcc_lo, 0x00c0000c\ns_mov_b32 vcc_hi, 0xc0000c00\nv_cndmask_b32 v67, 0, v66, vcc\ns_lshr_b64 vcc, vcc, 2\nv_cndmask_b32 v66, 0, v66, vcc\ns_cmp_lg_u64 s[74:75], 0\ns_cbranch_scc1 .Lfb\n"
".Lfbret0:\nv_rcp_f32 v2, v0\nv_rcp_f32_e64 v6, v4\nv_mfma_f32_16x16x4_4b_bf16 a[208:223], v[30:31], v[58:59], a[208:223]\nv_rcp_f32 v10, v8\nv_rcp_f32 v14, v12\nv_rcp_f32 v18, v16\nv_rcp_f32 v22, v20\n"
"v_rcp_f32 v26, v24\nv_mov_b64 v[0:1], 0\nv_mfma_f32_16x16x4_4b_bf16 a[224:239], v[30:31], v[58:59], a[224:239]\nQk 2,2,2\nv_and_b32 v3, v2, v67\nv_and_b32 v2, v2, v66\nv_mov_b64_e64 v[4:5], 0\nQk 6,6,6\n"
"v_and_b32_e64 v7, v6, v67\nv_mfma_f32_16x16x4_4b_bf16 a[240:255], v[30:31], v[58:59], a[240:255]\nv_and_b32 v6, v6, v66\nv_mov_b64 v[8:9], 0\nQk 10,10,10\nv_and_b32 v11, v10, v67\nv_and_b32 v10, v10, v66\nv_mov_b64_e64 v[12:13], 0\n"
"v_mfma_f32_16x16x4_4b_bf16 v[80:95], v[30:31], v[58:59], v[80:95]\nQk 14,14,14\nv_and_b32 v15, v14, v67\nv_and_b32 v14, v14, v66\nv_mov_b64_e64 v[16:17], 0\nQk 18,18,18\nv_and_b32_e64 v19, v18, v67\nv_mfma_f32_16x16x4_4b_bf16 v[112:127], v[30:31], v[58:59], v[112:127]\n"
"v_and_b32 v18, v18, v66\nv_mov_b64 v[20:21], 0\nQk 22,22,22\nv_and_b32 v23, v22, v67\nv_and_b32 v22, v22, v66\nv_mov_b64_e64 v[24:25], 0\nv_mfma_f32_16x16x4_4b_bf16 v[144:159], v[30:31], v[58:59], v[144:159]\nQk 26,26,26\n"
"v_and_b32 v27, v26, v67\nv_and_b32 v26, v26, v66\nv_mov_b64_e64 v[28:29], 0\nv_accvgpr_read_b32 v48, a208\nv_accvgpr_read_b32 v49, a209\nv_accvgpr_read_b32 v50, a210\nv_accvgpr_read_b32 v51, a211\nv_accvgpr_read_b32 v52, a212\n"
"v_accvgpr_read_b32 v53, a213\nv_accvgpr_read_b32 v54, a214\nv_accvgpr_read_b32 v55, a215\nQk 48,48,49\nQk 49,50,51\nQk 50,52,53\nQk 51,54,55\nv_accvgpr_read_b32 v32, a216\n"
"v_accvgpr_read_b32 v33, a217\nQzvvv 52,48,20\nv_accvgpr_read_b32 v34, a218\nv_accvgpr_read_b32 v35, a219\nQzvvv 48,48,22\nv_accvgpr_read_b32 v36, a220\nv_accvgpr_read_b32 v37, a221\nv_accvgpr_read_b32 v38, a222\n"
"v_accvgpr_read_b32 v39, a223\nQk 32,32,33\nQk 33,34,35\nQk 34,36,37\nQk 35,38,39\nv_mfma_f32_16x16x4_4b_bf16 v[240:255], v[30:31], v[58:59], v[240:255]\nv_accvgpr_read_b32 v40, a224\nv_accvgpr_read_b32 v41, a225\n"
"v_accvgpr_read_b32 v42, a226\nv_accvgpr_read_b32 v43, a227\nv_accvgpr_read_b32 v44, a228\nv_accvgpr_read_b32 v45, a229\nQzvvv 36,32,20\nv_accvgpr_read_b32 v46, a230\nv_accvgpr_read_b32 v47, a231\nQzvvv 32,32,22\n"
"Qk 40,40,41\nQk 41,42,43\nQk 42,44,45\nQk 43,46,47\nQk 48,48,49\nQk 49,50,51\nQzvvv 44,40,24\nQk 50,52,53\n"
"Qk 51,54,55\nQzvvv 40,40,26\nv_accvgpr_write_b32 a88, v48\nv_accvgpr_write_b32 a89, v49\nv_accvgpr_write_b32 a90, v50\nv_accvgpr_write_b32 a91, v51\nv_mfma_f32_16x16x4_4b_bf16 v[96:111], v[30:31], v[58:59], v[96:111]\nv_accvgpr_read_b32 v48, a232\n"
"v_accvgpr_read_b32 v49, a233\nv_accvgpr_read_b32 v50, a234\nv_accvgpr_read_b32 v51, a235\nv_accvgpr_read_b32 v52, a236\nv_accvgpr_read_b32 v53, a237\nv_accvgpr_read_b32 v54, a238\nv_accvgpr_read_b32 v55, a239\nQk 48,48,49\n"
"Qk 49,50,51\nQk 50,52,53\nQk 51,54,55\nQk 32,32,33\nQk 33,34,35\nQzvvv 52,48,24\nQk 34,36,37\nQk 35,38,39\n"
"Qzvvv 48,48,26\nv_accvgpr_write_b32 a92, v32\nv_accvgpr_write_b32 a93, v33\nv_accvgpr_write_b32 a94, v34\nv_accvgpr_write_b32 a95, v35\nv_mfma_f32_16x16x4_4b_bf16 v[176:191], v[30:31], v[58:59], v[176:191]\nv_accvgpr_read_b32 v32, a240\nv_accvgpr_read_b32 v33, a241\n"
"v_accvgpr_read_b32 v34, a242\nv_accvgpr_read_b32 v35, a243\nv_accvgpr_read_b32 v36, a244\nv_accvgpr_read_b32 v37, a245\nv_accvgpr_read_b32 v38, a246\nv_accvgpr_read_b32 v39, a247\nQk 32,32,33\nQk 33,34,35\n"
"Qk 34,36,37\nQk 35,38,39\nQk 40,40,41\nQk 41,42,43\nQzvvv 36,32,24\nQk 42,44,45\nQk 43,46,47\nQzvvv 32,32,26\n"
"v_accvgpr_write_b32 a96, v40\nv_accvgpr_write_b32 a97, v41\nv_accvgpr_write_b32 a98, v42\nv_accvgpr_write_b32 a99, v43\nv_mfma_f32_16x16x4_4b_bf16 v[128:143], v[30:31], v[58:59], v[128:143]\nv_accvgpr_read_b32 v40, a248\nv_accvgpr_read_b32 v41, a249\nv_accvgpr_read_b32 v42, a250\n"
"v_accvgpr_read_b32 v43, a251\nv_accvgpr_read_b32 v44, a252\nv_accvgpr_read_b32 v45, a253\nv_accvgpr_read_b32 v46, a254\nv_accvgpr_read_b32 v47, a255\nQk 40,40,41\nQk 41,42,43\ns_mov_b64 vcc, -1\n"
"s_nop 0\nQk 42,44,45\nQk 43,46,47\nQk 48,48,49\ns_waitcnt vmcnt(15)\ns_nop 0\nQk 49,50,51\nQzvvv 44,40,24\n"
"Qk 50,52,53\ns_barrier\ns_nop 0\nQk 51,54,55\nQzvvv 40,40,26\nv_accvgpr_write_b32 a100, v48\nQta 112,61,0\nQta 114,61,512\n"
"Qda 208,63,33024\nQda 212,63,34048\nv_accvgpr_write_b32 a101, v49\nv_accvgpr_write_b32 a102, v50\nv_accvgpr_write_b32 a103, v51\nv_mfma_f32_16x16x4_4b_bf16 v[160:175], v[30:31], v[58:59], v[160:175]\nQk 80,80,81\nQk 81,82,83\n"
"Qk 82,84,85\nQk 83,86,87\nQk 88,88,89\nQda 216,63,35072\nQzvvv 84,80,0\nQda 220,63,36096\nQda 224,63,37120\nQzvvv 80,80,2\n"
"Qda 228,63,38144\nQk 89,90,91\nv_mfma_f32_16x16x4_4b_bf16 v[208:223], v[30:31], v[58:59], v[208:223]\nQk 90,92,93\nQk 91,94,95\nQk 112,112,113\nQk 113,114,115\nQk 114,116,117\n"
"Qk 115,118,119\nQzvvv 92,88,0\nQk 80,80,81\nQk 81,82,83\nQzvvv 88,88,2\nQda 232,63,39168\nQda 176,63,33536\nv_mfma_f32_16x16x4_4b_bf16 v[192:207], v[30:31], v[58:59], v[192:207]\n"
"Qta 116,61,4128\nQta 118,61,4640\nQk 82,84,85\nQk 83,86,87\nv_accvgpr_write_b32 a0, v80\nv_accvgpr_write_b32 a1, v81\nQzvvv 116,112,4\nv_accvgpr_write_b32 a2, v82\n"
"v_accvgpr_write_b32 a3, v83\nQzvvv 112,112,6\nQk 120,120,121\nQk 121,122,123\nv_mfma_f32_16x16x4_4b_bf16 v[224:239], v[30:31], v[58:59], v[224:239]\nQk 122,124,125\nQta 120,61,8\nQta 122,61,520\n"
"Qta 124,61,4136\nQta 126,61,4648\nQk 123,126,127\nQk 88,88,89\nQk 89,90,91\nQzvvv 124,120,4\nQk 90,92,93\nQk 91,94,95\n"
"Qzvvv 120,120,6\nv_accvgpr_write_b32 a4, v88\nv_accvgpr_write_b32 a5, v89\nv_accvgpr_write_b32 a6, v90\nv_accvgpr_write_b32 a7, v91\nQk 144,144,145\nQta 128,61,256\nQta 130,61,768\n"
"Qta 132,61,4384\nQta 134,61,4896\nQk 145,146,147\nQk 146,148,149\nQk 147,150,151\nQk 112,112,113\nQk 113,114,115\nQzvvv 148,144,8\n"
"Qk 114,116,117\nQk 115,118,119\nQzvvv 144,144,10\nv_accvgpr_write_b32 a16, v112\nv_accvgpr_write_b32 a17, v113\nv_accvgpr_write_b32 a18, v114\nQta 136,61,264\nQta 138,61,776\n"
"Qta 140,61,4392\nQta 142,61,4904\nv_accvgpr_write_b32 a19, v115\nQk 240,240,241\nQk 241,242,243\nQk 242,244,245\nQk 243,246,247\nQk 120,120,121\n"
"Qk 121,122,123\nQzvvv 244,240,20\nQk 122,124,125\nQk 123,126,127\nQzvvv 240,240,22\nv_accvgpr_write_b32 a20, v120\ns_waitcnt lgkmcnt(15)\ns_nop 0\n"
"v_accvgpr_write_b32 a21, v121\nv_accvgpr_write_b32 a22, v122\nv_accvgpr_write_b32 a23, v123\nQzvaa 80,112,208\nQk 248,248,249\nQk 249,250,251\nQk 250,252,253\nQzvaa 84,112,212\n"
"Qda 180,63,34560\nQk 251,254,255\nQk 144,144,145\nQk 145,146,147\nQzvvv 252,248,20\nQk 146,148,149\nQk 147,150,151\nQzvaa 88,112,216\n"
"Qda 184,63,35584\nv_accvgpr_write_b32 a32, v144\nQzvvv 248,248,22\nv_accvgpr_write_b32 a33, v145\nv_accvgpr_write_b32 a34, v146\nv_accvgpr_write_b32 a35, v147\nQk 152,152,153\nQzvaa 92,112,220\n"
"Qda 188,63,36608\nQk 153,154,155\nQk 154,156,157\nQk 155,158,159\nQk 240,240,241\nQk 241,242,243\nQzvvv 156,152,8\nQk 242,244,245\n"
"Qk 243,246,247\nQzvaa 112,112,224\nQda 192,63,37632\nv_accvgpr_write_b32 a80, v240\nQzvvv 152,152,10\nv_accvgpr_write_b32 a81, v241\nv_accvgpr_write_b32 a82, v242\nQzvaa 116,112,228\n"
"Qda 196,63,38656\nv_accvgpr_write_b32 a83, v243\nQk 96,96,97\nQk 97,98,99\nQk 98,100,101\nQk 99,102,103\nQk 248,248,249\nQzvaa 120,112,232\n"
"Qda 200,63,39680\nQk 249,250,251\nQzvvv 100,96,0\nQk 250,252,253\nQk 251,254,255\nQzvvv 96,96,2\nv_accvgpr_write_b32 a84, v248\nv_max3_f32 v80, |v80|, |v81|, |v82|\n"
"v_accvgpr_write_b32 a85, v249\nv_accvgpr_write_b32 a86, v250\nv_accvgpr_write_b32 a87, v251\nv_max3_f32 v84, |v84|, |v85|, |v86|\nQk 104,104,105\nQk 105,106,107\nQk 106,108,109\nv_max3_f32 v88, |v88|, |v89|, |v90|\n"
"Qk 107,110,111\nQk 152,152,153\nQk 153,154,155\nv_max3_f32 v92, |v92|, |v93|, |v94|\nQzvvv 108,104,0\nQk 154,156,157\nQk 155,158,159\nv_max3_f32 v112, |v112|, |v113|, |v114|\n"
"Qzvvv 104,104,2\nv_accvgpr_write_b32 a36, v152\nv_accvgpr_write_b32 a37, v153\nv_max3_f32 v116, |v116|, |v117|, |v118|\nv_accvgpr_write_b32 a38, v154\nv_accvgpr_write_b32 a39, v155\nQk 176,176,177\nv_max3_f32 v120, |v120|, |v121|, |v122|\n"
"Qk 177,178,179\nQk 178,180,181\nQk 179,182,183\nv_min3_u32 v56, v80, v84, v88\nQk 128,128,129\nQzvvv 180,176,12\nv_min3_u32 v56, v56, v92, v112\nQk 129,130,131\n"
"Qzvvv 176,176,14\nQk 130,132,133\nQk 131,134,135\nv_min3_u32 v56, v56, v116, v120\nQk 136,136,137\nQzvvv 132,128,4\nv_mov_b32_e64 v57, v56\nQk 137,138,139\n"
"Qzvvv 128,128,6\nQk 138,140,141\nQk 139,142,143\n.Lpg7:\ns_cbranch_execz .Ltr9\nv_permlane32_swap_b32 v56, v57\nQk 176,176,177\nQk 177,178,179\n"
"Qzvvv 140,136,4\nv_max_f32_e64 v56, v56, v57\nQk 178,180,181\nQk 179,182,183\nQzvvv 136,136,6\nv_mov_b32_e64 v57, v56\nv_accvgpr_write_b32 a48, v176\nv_accvgpr_write_b32 a49, v177\n"
"v_accvgpr_write_b32 a50, v178\nv_permlane16_swap_b32_e64 v56, v57\nv_accvgpr_write_b32 a51, v179\nQk 184,184,185\nQk 185,186,187\nv_max_f32_e64 v56, v56, v57\nQk 186,188,189\nQk 187,190,191\n"
"v_min_f32_dpp v56, v56, v56 row_ror:8 row_mask:0xf bank_mask:0xf\nQk 160,160,161\nQzvvv 188,184,12\nQk 161,162,163\nv_min_f32_dpp v56, v56, v56 row_ror:4 row_mask:0xf bank_mask:0xf\nQzvvv 184,184,14\nQk 162,164,165\nQk 163,166,167\n"
"v_min_f32_dpp v56, v56, v56 row_ror:2 row_mask:0xf bank_mask:0xf\nQk 168,168,169\nQzvvv 164,160,8\nQk 169,170,171\nv_min_f32_dpp v56, v56, v56 row_ror:1 row_mask:0xf bank_mask:0xf\nQzvvv 160,160,10\nQk 170,172,173\nQk 171,174,175\n"
"Qk 184,184,185\nv_and_b32 v56, 0x7f800000, v56\nQk 185,186,187\nQzvvv 172,168,8\nQk 186,188,189\nv_min_u32 v56, 0x78000000, v56\nQk 187,190,191\nQzvvv 168,168,10\n"
"v_accvgpr_write_b32 a52, v184\nv_add_u32 v56, 0x5400000, v56\nv_accvgpr_write_b32 a53, v185\nv_accvgpr_write_b32 a54, v186\nv_accvgpr_write_b32 a55, v187\nv_mov_b32_e64 v57, v56\nQk 208,208,209\nQk 209,210,211\n"
"Qk 210,212,213\nv_mov_b64_e64 v[58:59], v[56:57]\nQk 211,214,215\nv_mul_f32 v52, -v56, s72\nQk 192,192,193\nQzvvv 212,208,16\nQk 193,194,195\nQk 194,196,197\n"
"Qzvvv 208,208,18\nQk 195,198,199\nQk 200,200,201\nQk 201,202,203\nQzvvv 196,192,12\nQk 202,204,205\nQk 203,206,207\nQzvvv 192,192,14\n"
"Qk 216,216,217\nQk 208,208,209\nQk 209,210,211\nQzvvv 204,200,12\nQk 210,212,213\nQk 211,214,215\nQzvvv 200,200,14\nv_accvgpr_write_b32 a64, v208\n"
"v_accvgpr_write_b32 a65, v209\nv_accvgpr_write_b32 a66, v210\nv_accvgpr_write_b32 a67, v211\nQk 217,218,219\nQk 218,220,221\nQk 219,222,223\nQk 224,224,225\nQk 225,226,227\n"
"Qzvvv 220,216,16\nQk 226,228,229\nQk 227,230,231\nQzvvv 216,216,18\nQk 232,232,233\nQk 233,234,235\nQzvvv 228,224,16\nQk 234,236,237\n"
"Qk 235,238,239\nQzvvv 224,224,18\ns_nop 1\ns_nop 0\nQk 216,216,217\nQzvvv 236,232,16\nQk 217,218,219\nQk 218,220,221\n"
"Qk 219,222,223\nQzvvv 232,232,18\nv_accvgpr_write_b32 a68, v216\nv_accvgpr_write_b32 a69, v217\nv_accvgpr_write_b32 a70, v218\nv_accvgpr_write_b32 a71, v219\ns_mov_b32 s56, s20\ns_and_b32 s57, s21, 0xffff\n"
"s_mov_b32 s94, 0x7ffff000\ns_mul_i32 s68, %0, 40960\ns_lshl_b32 s69, s70, 10\ns_add_u32 s68, s68, s69\ns_add_u32 s68, s68, 4096\ns_add_u32 s94, s16, s68\ns_addc_u32 s95, s17, 0\ns_and_b32 s95, s95, 0xffff\n"
"s_mov_b32 s75, 37888\ns_mov_b32 s58, 4194304\ns_mov_b32 s67, 1\ns_branch .Lround2\n.Lhook:\ns_mov_b64 exec, 0\ns_cmp_eq_u32 s70, 0\ns_cbranch_scc1 .Lpg8\n"
"s_cmp_eq_u32 s70, 1\ns_cbranch_scc1 .Lpg9\ns_cmp_eq_u32 s70, 2\ns_cbranch_scc1 .Lpg10\ns_branch .Lpg11\n.Ltr8:\ns_mov_b64 exec, -1\ns_branch .Lhookret\n"
".Lf1_w:\ns_mov_b32 s97, 0\n.Lf1_l:\ns_sleep 1\nbuffer_load_dword v78, v76, s[24:27], 0 offen sc1\ns_waitcnt vmcnt(0)\ns_add_u32 s97, s97, 1\ns_cmp_gt_u32 s97, 1048576\n"
"s_cbranch_scc1 .Lf1_r\nv_sub_u32 v66, v78, v65\nv_cmp_gt_u32 vcc, 1, v66\ns_and_b64 vcc, vcc, s[28:29]\ns_cbranch_scc1 .Lf1_l\ns_branch .Lf1_r\n.Lack_w:\ns_mov_b32 s97, 0\n"
".Lack_l:\ns_sleep 1\nv_bfe_u32 v66, v60, 4, 3\ns_lshl_b32 s68, s19, 7\nv_lshlrev_b32 v66, 2, v66\ns_add_u32 s68, s68, 5242880\nbuffer_load_dword v78, v66, s[24:27], s68 offen offset:1024 sc1\ns_waitcnt vmcnt(0)\n"
"s_add_u32 s97, s97, 1\ns_cmp_gt_u32 s97, 1048576\ns_cbranch_scc1 .Lack_ok\nv_xor_b32 v78, s30, v78\nv_and_b32 v66, 0x0fffffff, v78\nv_cmp_ne_u32 vcc, 0, v66\ns_cmp_eq_u64 vcc, 0\ns_cbranch_scc0 .Lack_l\n"
"s_branch .Lack_ok\n.Lsp0:\ns_sub_u32 s75, s77, s74\ns_cmp_lt_u32 s75, 1024\ns_cbranch_scc0 .Lrp0\ns_memrealtime s[74:75]\ns_waitcnt lgkmcnt(0)\ns_branch .Lsp0\n"
".Lsa1:\nds_read_b32 v66, v74\ns_waitcnt lgkmcnt(0)\nv_cmp_gt_u32 vcc, v75, v66\ns_cbranch_vccnz .Lsa1\ns_branch .Lra1\n.Lsp1:\ns_sub_u32 s75, s77, s74\n"
"s_cmp_lt_u32 s75, 1024\ns_cbranch_scc0 .Lrp1\ns_memrealtime s[74:75]\ns_waitcnt lgkmcnt(0)\ns_branch .Lsp1\n.Lsa2:\nds_read_b32 v66, v74\ns_waitcnt lgkmcnt(0)\n"
"v_cmp_gt_u32 vcc, v75, v66\ns_cbranch_vccnz .Lsa2\ns_branch .Lra2\n.Lsp2:\ns_sub_u32 s75, s77, s74\ns_cmp_lt_u32 s75, 1024\ns_cbranch_scc0 .Lrp2\ns_memrealtime s[74:75]\n"
"s_waitcnt lgkmcnt(0)\ns_branch .Lsp2\n.Lsa3:\nds_read_b32 v66, v74\ns_waitcnt lgkmcnt(0)\nv_cmp_gt_u32 vcc, v75, v66\ns_cbranch_vccnz .Lsa3\ns_branch .Lra3\n"
".Lsa4:\nds_read_b32 v66, v74\ns_waitcnt lgkmcnt(0)\nv_cmp_gt_u32 vcc, v75, v66\ns_cbranch_vccnz .Lsa4\ns_branch .Lra4\n.Lsp4:\ns_sub_u32 s75, s77, s74\n"
"s_cmp_lt_u32 s75, 1024\ns_cbranch_scc0 .Lrp4\ns_memrealtime s[74:75]\ns_waitcnt lgkmcnt(0)\ns_branch .Lsp4\n.Lsa5:\nds_read_b32 v66, v74\ns_waitcnt lgkmcnt(0)\n"
"v_cmp_gt_u32 vcc, v75, v66\ns_cbranch_vccnz .Lsa5\ns_branch .Lra5\n.Lsa30:\nds_read_b32 v66, v74\ns_waitcnt lgkmcnt(0)\nv_cmp_gt_u32 vcc, v75, v66\ns_cbranch_vccnz .Lsa30\n"
"s_branch .Lra30\n.Lsa31:\nds_read_b32 v66, v74\ns_waitcnt lgkmcnt(0)\nv_cmp_gt_u32 vcc, v75, v66\ns_cbranch_vccnz .Lsa31\ns_branch .Lra31\n.Lsa32:\n"
"ds_read_b32 v66, v74\ns_waitcnt lgkmcnt(0)\nv_cmp_gt_u32 vcc, v75, v66\ns_cbranch_vccnz .Lsa32\ns_branch .Lra32\n.Lsa30x:\nds_read_b32 v66, v74\ns_waitcnt lgkmcnt(0)\n"
"v_cmp_gt_u32 vcc, v75, v66\ns_cbranch_vccnz .Lsa30x\ns_branch .Lra30x\n.Lsa31x:\nds_read_b32 v66, v74\ns_waitcnt lgkmcnt(0)\nv_cmp_gt_u32 vcc, v75, v66\ns_cbranch_vccnz .Lsa31x\n"
"s_branch .Lra31x\n.Lso40:\nds_read_b32 v66, v74\ns_waitcnt lgkmcnt(0)\nv_cmp_gt_u32 vcc, v75, v66\ns_cbranch_vccnz .Lso40\ns_branch .Lro40\n.Lso180:\n"
"ds_read_b32 v66, v74\ns_waitcnt lgkmcnt(0)\nv_cmp_gt_u32 vcc, v75, v66\ns_cbranch_vccnz .Lso180\ns_branch .Lro180\n.Lso320:\nds_read_b32 v66, v74\ns_waitcnt lgkmcnt(0)\n"
"v_cmp_gt_u32 vcc, v75, v66\ns_cbranch_vccnz .Lso320\ns_branch .Lro320\n.Lso460:\nds_read_b32 v66, v74\ns_waitcnt lgkmcnt(0)\nv_cmp_gt_u32 vcc, v75, v66\ns_cbranch_vccnz .Lso460\n"
"s_branch .Lro460\n.Lso600:\nds_read_b32 v66, v74\ns_waitcnt lgkmcnt(0)\nv_cmp_gt_u32 vcc, v75, v66\ns_cbranch_vccnz .Lso600\ns_branch .Lro600\n.Lso740:\n"
"ds_read_b32 v66, v74\ns_waitcnt lgkmcnt(0)\nv_cmp_gt_u32 vcc, v75, v66\ns_cbranch_vccnz .Lso740\ns_branch .Lro740\n.Lso880:\nds_read_b32 v66, v74\ns_waitcnt lgkmcnt(0)\n"
"v_cmp_gt_u32 vcc, v75, v66\ns_cbranch_vccnz .Lso880\ns_branch .Lro880\n.Lso1020:\nds_read_b32 v66, v74\ns_waitcnt lgkmcnt(0)\nv_cmp_gt_u32 vcc, v75, v66\ns_cbranch_vccnz .Lso1020\n"
"s_branch .Lro1020\n.Lso1160:\nds_read_b32 v66, v74\ns_waitcnt lgkmcnt(0)\nv_cmp_gt_u32 vcc, v75, v66\ns_cbranch_vccnz .Lso1160\ns_branch .Lro1160\n.Lso1300:\n"
"ds_read_b32 v66, v74\ns_waitcnt lgkmcnt(0)\nv_cmp_gt_u32 vcc, v75, v66\ns_cbranch_vccnz .Lso1300\ns_branch .Lro1300\n.Lso1440:\nds_read_b32 v66, v74\ns_waitcnt lgkmcnt(0)\n"
"v_cmp_gt_u32 vcc, v75, v66\ns_cbranch_vccnz .Lso1440\ns_branch .Lro1440\n.Lso1455:\nds_read_b32 v66, v74\ns_waitcnt lgkmcnt(0)\nv_cmp_gt_u32 vcc, v75, v66\ns_cbranch_vccnz .Lso1455\n"
"s_branch .Lro1455\n.Lso1525r0:\nds_read_b32 v66, v74\ns_waitcnt lgkmcnt(0)\nv_cmp_gt_u32 vcc, v75, v66\ns_cbranch_vccnz .Lso1525r0\ns_branch .Lro1525r0\n.Lso1595r0:\n"
"ds_read_b32 v66, v74\ns_waitcnt lgkmcnt(0)\nv_cmp_gt_u32 vcc, v75, v66\ns_cbranch_vccnz .Lso1595r0\ns_branch .Lro1595r0\n.Lso1665r0:\nds_read_b32 v66, v74\ns_waitcnt lgkmcnt(0)\n"
"v_cmp_gt_u32 vcc, v75, v66\ns_cbranch_vccnz .Lso1665r0\ns_branch .Lro1665r0\n.Lso1735r0:\nds_read_b32 v66, v74\ns_waitcnt lgkmcnt(0)\nv_cmp_gt_u32 vcc, v75, v66\ns_cbranch_vccnz .Lso1735r0\n"
"s_branch .Lro1735r0\n.Lso1805r0:\nds_read_b32 v66, v74\ns_waitcnt lgkmcnt(0)\nv_cmp_gt_u32 vcc, v75, v66\ns_cbranch_vccnz .Lso1805r0\ns_branch .Lro1805r0\n.Lso1878r0:\n"
"ds_read_b32 v66, v74\ns_waitcnt lgkmcnt(0)\nv_cmp_gt_u32 vcc, v75, v66\ns_cbranch_vccnz .Lso1878r0\ns_branch .Lro1878r0\n.Lso1920r0:\nds_read_b32 v66, v74\ns_waitcnt lgkmcnt(0)\n"
"v_cmp_gt_u32 vcc, v75, v66\ns_cbranch_vccnz .Lso1920r0\ns_branch .Lro1920r0\n.Lso1960r0:\nds_read_b32 v66, v74\ns_waitcnt lgkmcnt(0)\nv_cmp_gt_u32 vcc, v75, v66\ns_cbranch_vccnz .Lso1960r0\n"
"s_branch .Lro1960r0\n.Lso2046r0:\nds_read_b32 v66, v74\ns_waitcnt lgkmcnt(0)\nv_cmp_gt_u32 vcc, v75, v66\ns_cbranch_vccnz .Lso2046r0\ns_branch .Lro2046r0\n.Lso2088r0:\n"
"ds_read_b32 v66, v74\ns_waitcnt lgkmcnt(0)\nv_cmp_gt_u32 vcc, v75, v66\ns_cbranch_vccnz .Lso2088r0\ns_branch .Lro2088r0\n.Lso2172r0:\nds_read_b32 v66, v74\ns_waitcnt lgkmcnt(0)\n"
"v_cmp_gt_u32 vcc, v75, v66\ns_cbranch_vccnz .Lso2172r0\ns_branch .Lro2172r0\n.Lso2222r0:\nds_read_b32 v66, v74\ns_waitcnt lgkmcnt(0)\nv_cmp_gt_u32 vcc, v75, v66\ns_cbranch_vccnz .Lso2222r0\n"
"s_branch .Lro2222r0\n.Lso1525r1:\nds_read_b32 v66, v74\ns_waitcnt lgkmcnt(0)\nv_cmp_gt_u32 vcc, v75, v66\ns_cbranch_vccnz .Lso1525r1\ns_branch .Lro1525r1\n.Lso1595r1:\n"
"ds_read_b32 v66, v74\ns_waitcnt lgkmcnt(0)\nv_cmp_gt_u32 vcc, v75, v66\ns_cbranch_vccnz .Lso1595r1\ns_branch .Lro1595r1\n.Lso1665r1:\nds_read_b32 v66, v74\ns_waitcnt lgkmcnt(0)\n"
"v_cmp_gt_u32 vcc, v75, v66\ns_cbranch_vccnz .Lso1665r1\ns_branch .Lro1665r1\n.Lso1735r1:\nds_read_b32 v66, v74\ns_waitcnt lgkmcnt(0)\nv_cmp_gt_u32 vcc, v75, v66\ns_cbranch_vccnz .Lso1735r1\n"
"s_branch .Lro1735r1\n.Lso1805r1:\nds_read_b32 v66, v74\ns_waitcnt lgkmcnt(0)\nv_cmp_gt_u32 vcc, v75, v66\ns_cbranch_vccnz .Lso1805r1\ns_branch .Lro1805r1\n.Lso1840r1:\n"
"ds_read_b32 v66, v74\ns_waitcnt lgkmcnt(0)\nv_cmp_gt_u32 vcc, v75, v66\ns_cbranch_vccnz .Lso1840r1\ns_branch .Lro1840r1\n.Lso1878r1:\nds_read_b32 v66, v74\ns_waitcnt lgkmcnt(0)\n"
"v_cmp_gt_u32 vcc, v75, v66\ns_cbranch_vccnz .Lso1878r1\ns_branch .Lro1878r1\n.Lso1920r1:\nds_read_b32 v66, v74\ns_waitcnt lgkmcnt(0)\nv_cmp_gt_u32 vcc, v75, v66\ns_cbranch_vccnz .Lso1920r1\n"
"s_branch .Lro1920r1\n.Lso1945r1:\nds_read_b32 v66, v74\ns_waitcnt lgkmcnt(0)\nv_cmp_gt_u32 vcc, v75, v66\ns_cbranch_vccnz .Lso1945r1\ns_branch .Lro1945r1\n.Lso2004r1:\n"
"ds_read_b32 v66, v74\ns_waitcnt lgkmcnt(0)\nv_cmp_gt_u32 vcc, v75, v66\ns_cbranch_vccnz .Lso2004r1\ns_branch .Lro2004r1\n.Lso2045r1:\nds_read_b32 v66, v74\ns_waitcnt lgkmcnt(0)\n"
"v_cmp_gt_u32 vcc, v75, v66\ns_cbranch_vccnz .Lso2045r1\ns_branch .Lro2045r1\n.Lso2130r1:\nds_read_b32 v66, v74\ns_waitcnt lgkmcnt(0)\nv_cmp_gt_u32 vcc, v75, v66\ns_cbranch_vccnz .Lso2130r1\n"
"s_branch .Lro2130r1\n.Lso2172r1:\nds_read_b32 v66, v74\ns_waitcnt lgkmcnt(0)\nv_cmp_gt_u32 vcc, v75, v66\ns_cbranch_vccnz .Lso2172r1\ns_branch .Lro2172r1\n.Lso2225r1:\n"
"ds_read_b32 v66, v74\ns_waitcnt lgkmcnt(0)\nv_cmp_gt_u32 vcc, v75, v66\ns_cbranch_vccnz .Lso2225r1\ns_branch .Lro2225r1\n.Ltail1:\n.Lpg8:\ns_cbranch_execz .Lpg12\n"
"Qcvaa 0,128,32,56\nv_add_u32 v75, 1, v75\ns_lshr_b32 s88, s19, 2\nQmvaa 0,132,36\ns_waitcnt vmcnt(0)\nds_write_b32 v73, v75\nQmvaa 0,136,40\nQe 8\n"
"Qmvaa 0,140,44\nQe 9\nQcvaa 4,124,44,56\nQe 10\nQmvaa 4,120,40\nQe 11\nQmvaa 4,116,36\nQe 12\n"
"Qmvaa 4,112,32\ns_waitcnt lgkmcnt(1)\nQk 40,8,9\nQmvva 96,44,144\nQe 13\nQmvva 96,36,148\nQe 14\nQmvva 100,44,152\n"
"Qe 15\nQmvva 100,36,156\nQk 41,10,11\nQk 42,12,13\nQmvva 104,44,160\nQk 43,14,15\ns_lshl_b32 s88, s88, 4\nQmvva 104,36,164\n"
"Qe 0\nQmvva 108,44,168\nQe 1\nQmvva 108,36,172\nQe 2\nQmvva 140,48,168\nQe 3\nQmvva 140,40,172\n"
"s_add_u32 s88, s88, s93\ns_mul_i32 s68, s88, 163840\nQmvva 136,48,160\nds_read_b32 v66, v74\ns_and_b32 s69, s19, 3\nQmvva 136,40,164\ns_mul_i32 s69, s69, 320\ns_add_u32 s68, s68, s69\n"
"Qmvva 132,48,152\ns_add_u32 s52, s16, s68\ns_addc_u32 s53, s17, 0\nQmvva 132,40,156\ns_and_b32 s53, s53, 0xffff\ns_sub_u32 s54, 5246976, s68\nQmvva 128,48,144\ns_and_b32 s87, s19, 3\n"
"s_mul_i32 s87, s87, 327680\nQmvva 128,40,148\ns_waitcnt lgkmcnt(0)\nv_cmp_gt_u32 vcc, v75, v66\nv_smfmac_f32_16x16x64_bf16 a[176:179], a[204:207], v[36:43], v77\ns_cbranch_vccnz .Lsa30x\n.Lra30x:\nQcvaa 8,112,48,56\n"
"s_add_u32 s87, s87, 1310720\ns_mul_i32 s69, s92, 10240\nQmvaa 8,116,52\nv_bfe_u32 v78, v60, 5, 3\nv_mul_u32_u24 v78, 1280, v78\nQmvaa 8,120,56\nv_and_b32 v67, 16, v60\nv_add3_u32 v67, v67, v78, s69\n"
"Qmvaa 8,124,60\ns_and_b32 s69, s19, 1\nv_lshrrev_b32 v78, 8, v60\nQcvaa 12,140,60,56\ns_lshl_b32 s68, s93, 1\nv_xor_b32 v78, s68, v78\nQmvaa 12,136,56\nv_lshl_add_u32 v78, v78, 5, v67\n"
"s_lshr_b32 s68, 128, s69\nQmvaa 12,132,52\nv_add_u32 v61, s68, v78\nQk 46,0,1\nQmvaa 12,128,48\ns_add_u32 m0, s96, 0\ns_add_u32 s95, s87, 4096\nQk 47,2,3\n"
"Qcvaa 0,128,64,56\nbuffer_load_dwordx4 v61, s[52:55], s95 offen lds\nQmvaa 0,132,68\nQe 4\nQmvaa 0,136,72\nQe 5\nQe 6\nQe 7\n"
"Qmvaa 0,140,76\ns_add_u32 s95, s87, 23552\nQk 44,4,5\nQk 45,6,7\nQcvaa 4,124,76,56\nbuffer_load_dwordx4 v61, s[52:55], s95 offen offset:1024 lds\nQmvaa 4,120,72\nQe 8\n"
"Qmvaa 4,116,68\nQe 9\nQmvaa 4,112,64\ns_add_u32 m0, s96, 8192\ns_add_u32 s95, s87, 45056\ns_mul_i32 s68, s69, 192\nQmvva 160,52,144\nbuffer_load_dwordx4 v61, s[52:55], s95 offen lds\n"
"Qmvva 160,44,148\nQe 10\nQmvva 164,52,152\nQe 11\nQmvva 164,44,156\ns_add_u32 s95, s87, 64512\nv_add_u32 v63, s68, v78\nQmvva 168,52,160\n"
"buffer_load_dwordx4 v61, s[52:55], s95 offen offset:1024 lds\nQmvva 168,44,164\nQe 12\nQmvva 172,52,168\nQe 13\nQe 14\nQe 15\nv_bfe_u32 v78, v60, 8, 1\n"
"v_xor_b32 v78, s93, v78\nv_lshl_add_u32 v67, v78, 5, v67\nv_lshrrev_b32 v78, 9, v60\nv_mul_u32_u24 v78, 20480, v78\ns_xor_b32 s69, s69, 1\ns_lshl_b32 s69, s69, 8\nv_add3_u32 v62, v78, v67, s69\nQmvva 172,44,172\n"
"s_add_u32 m0, s96, 16384\ns_add_u32 s95, s87, 4096\nQk 48,8,9\nQk 49,10,11\nQk 50,12,13\nQk 51,14,15\nQmvva 204,20,168\nbuffer_load_dwordx4 v62, s[52:55], s95 offen lds\n"
"Qmvva 204,48,172\nQe 0\nQmvva 200,20,160\nQe 1\nQmvva 200,48,164\ns_add_u32 s95, s87, 44032\nQk 18,0,1\nQmvva 196,20,152\n"
"buffer_load_dwordx4 v62, s[52:55], s95 offen offset:1024 lds\nQmvva 196,48,156\nQe 2\nQmvva 192,20,144\nQe 3\nQmvva 192,48,148\nQk 19,2,3\ns_andn2_b32 s68, s71, 786432\n"
"v_smfmac_f32_16x16x64_bf16 a[180:183], a[204:207], v[44:51], v77\ns_add_u32 m0, s96, 24576\ns_add_u32 s95, s87, 4096\nQcvaa 8,112,80,56\nbuffer_load_dwordx4 v63, s[52:55], s95 offen lds\nQmvaa 8,116,84\nQe 4\nQmvaa 8,120,88\n"
"s_add_u32 s95, s87, 23552\nQmvaa 8,124,92\nbuffer_load_dwordx4 v63, s[52:55], s95 offen offset:1024 lds\nQcvaa 12,140,92,56\nQe 5\nQmvaa 12,136,88\nQe 6\nQmvaa 12,132,84\n"
"s_add_u32 m0, s96, 32768\ns_add_u32 s95, s87, 45056\nQk 16,4,5\nQmvaa 12,128,80\nbuffer_load_dwordx4 v63, s[52:55], s95 offen lds\nQcvaa 0,128,96,56\nQda 128,68,34048\nQmvaa 0,132,100\n"
"Qda 132,68,34560\nQmvaa 0,136,104\nQda 136,68,34304\nQe 7\nQmvaa 0,140,108\nQda 140,68,34816\ns_add_u32 s95, s87, 64512\nQk 17,6,7\n"
"Qcvaa 4,124,108,56\nQda 124,68,33792\nbuffer_load_dwordx4 v63, s[52:55], s95 offen offset:1024 lds\nQmvaa 4,120,104\nQda 120,68,33280\nQmvaa 4,116,100\nQda 116,68,33536\nbuffer_load_dword v78, v76, s[24:27], 0 offen sc1\n"
"Qmvaa 4,112,96\nQda 112,68,33024\ns_add_u32 m0, s96, 40960\ns_mov_b32 s95, 4096\nQmvva 224,24,144\nbuffer_load_dwordx4 v61, s[52:55], s95 offen lds\nQmvva 224,16,148\nQe 8\n"
"Qmvva 228,24,152\nQe 9\nQmvva 228,16,156\nQe 10\nQmvva 232,24,160\ns_mov_b32 s95, 23552\nQk 20,8,9\nQmvva 232,16,164\n"
"buffer_load_dwordx4 v61, s[52:55], s95 offen offset:1024 lds\nQmvva 236,24,168\nQe 11\nQmvva 236,16,172\ns_waitcnt lgkmcnt(0)\nQk 21,10,11\nQcvaa 8,112,0,56\nQe 12\n"
"Qmvaa 8,116,4\nv_subrev_u32 v68, 66048, v68\ns_add_u32 m0, s96, 49152\ns_mov_b32 s95, 45056\nQmvaa 8,120,8\nbuffer_load_dwordx4 v61, s[52:55], s95 offen lds\nQe 13\nQe 14\n"
"Qe 15\nQmvaa 8,124,12\nQk 22,12,13\nQk 23,14,15\nQcvaa 12,140,12,56\nQe 0\nQmvaa 12,136,8\nQe 1\n"
"Qmvaa 12,132,4\nQe 2\nQmvaa 12,128,0\nQe 3\nv_smfmac_f32_16x16x64_bf16 a[184:187], a[204:207], v[16:23], v77\ns_mov_b32 s95, 64512\nQmava 208,28,144\nbuffer_load_dwordx4 v61, s[52:55], s95 offen offset:1024 lds\n"
"Qmava 208,20,148\nQe 4\nQmava 212,28,152\n.Lpg9:\ns_cbranch_execz .Lpg13\nQe 5\nQmava 212,20,156\nQe 6\n"
"Qmava 216,28,160\ns_add_u32 m0, s96, 57344\ns_mov_b32 s95, 4096\nQk 26,0,1\nQmava 216,20,164\nbuffer_load_dwordx4 v62, s[52:55], s95 offen lds\nQmava 220,28,168\nQe 7\n"
"Qmava 220,20,172\nQk 27,2,3\nQk 24,4,5\nQk 25,6,7\nQmava 252,32,168\nQta 168,71,768\nQta 170,71,1792\nQmava 252,24,172\n"
"Qe 8\nQmava 248,32,160\nQta 172,71,33792\nQta 174,71,34816\ns_mov_b32 s95, 44032\nQmava 248,24,164\nbuffer_load_dwordx4 v62, s[52:55], s95 offen offset:1024 lds\nQmava 244,32,152\n"
"Qta 160,71,512\nQta 162,71,1536\nQmava 244,24,156\nQe 9\nQmava 240,32,144\nQta 164,71,33536\nQta 166,71,34560\ns_add_u32 m0, s96, 132096\n"
"s_mov_b32 s95, 4096\nQmava 240,24,148\nbuffer_load_dwordx4 v63, s[52:55], s95 offen lds\nQcvaa 0,112,16,56\nQta 152,71,256\nQta 154,71,1280\nQmvaa 0,116,20\nQe 10\n"
"Qmvaa 0,120,24\nQta 156,71,33280\nQta 158,71,34304\nQmvaa 0,124,28\nQe 11\nQcvaa 4,140,28,56\nQta 144,71,0\nQta 146,71,1024\n"
"s_mov_b32 s95, 23552\nQmvaa 4,136,24\nbuffer_load_dwordx4 v63, s[52:55], s95 offen offset:1024 lds\nQmvaa 4,132,20\nQta 148,71,33024\nQta 150,71,34048\nQmvaa 4,128,16\nQk 28,8,9\n"
"Qk 29,10,11\nQcvaa 8,128,32,56\nv_add_u32 v75, 1, v75\nQmvaa 8,132,36\nds_write_b32 v73, v75\nQmvaa 8,136,40\nQe 12\nQe 13\n"
"Qe 14\nQe 15\nQmvaa 8,140,44\nQk 30,12,13\nQk 31,14,15\nQcvaa 12,124,44,56\nQe 0\nQmvaa 12,120,40\n"
"Qe 1\nQmvaa 12,116,36\nQe 2\nQmvaa 12,112,32\ns_waitcnt lgkmcnt(1)\nQk 32,0,1\nv_smfmac_f32_16x16x64_bf16 a[188:191], a[204:207], v[24:31], v77\nQmvva 80,36,144\n"
"Qe 3\nQmvva 80,28,148\nQe 4\nQmvva 84,36,152\nQe 5\nQmvva 84,28,156\nQe 6\nQmvva 88,36,160\n"
"Qe 7\nQmvva 88,28,164\nQk 33,2,3\nQk 34,4,5\nQmvva 92,36,168\nQk 35,6,7\nQmvva 92,28,172\nQe 8\n"
"Qmvva 124,40,168\nQe 9\nQmvva 124,32,172\nds_read_b32 v66, v74\nQk 38,8,9\nQmvva 120,40,160\nQe 10\nQmvva 120,32,164\n"
"Qe 11\nQmvva 116,40,152\nQk 39,10,11\nQmvva 116,32,156\nQe 12\nQmvva 112,40,144\ns_waitcnt lgkmcnt(0)\nv_cmp_gt_u32 vcc, v75, v66\n"
"Qmvva 112,32,148\ns_cbranch_vccnz .Lsa31x\n.Lra31x:\nQcvaa 0,112,48,56\nQe 13\nQmvaa 0,116,52\nQe 14\nQmvaa 0,120,56\n"
"Qe 15\nQmvaa 0,124,60\ns_add_u32 m0, s96, 140288\ns_mov_b32 s95, 45056\nQk 36,12,13\nQcvaa 4,140,60,56\nbuffer_load_dwordx4 v63, s[52:55], s95 offen lds\nQmvaa 4,136,56\n"
"Qk 37,14,15\nQmvaa 4,132,52\ns_add_u32 s48, s20, s68\ns_addc_u32 s49, s21, 0\nQmvaa 4,128,48\ns_mov_b32 s95, 64512\ns_and_b32 s49, s49, 0xffff\nQcvaa 8,128,64,56\n"
"buffer_load_dwordx4 v63, s[52:55], s95 offen offset:1024 lds\nQmvaa 8,132,68\nQe 0\nQmvaa 8,136,72\nQe 1\nQmvaa 8,140,76\ns_add_u32 m0, s96, 148480\ns_add_u32 s95, s87, 86016\n"
"Qk 40,0,1\nQcvaa 12,124,76,56\nbuffer_load_dwordx4 v61, s[52:55], s95 offen lds\nQmvaa 12,120,72\nQe 2\nQmvaa 12,116,68\nQe 3\nQmvaa 12,112,64\n"
"Qe 4\nv_smfmac_f32_16x16x64_bf16 a[192:195], a[204:207], v[32:39], v77\ns_add_u32 s95, s87, 105472\nQmvva 144,44,144\nbuffer_load_dwordx4 v61, s[52:55], s95 offen offset:1024 lds\nQmvva 144,36,148\nQe 5\nQmvva 148,44,152\n"
"s_add_u32 m0, s96, 65536\ns_add_u32 s95, s87, 126976\nQk 41,2,3\nQmvva 148,36,156\nbuffer_load_dwordx4 v61, s[52:55], s95 offen lds\nQmvva 152,44,160\nQe 6\nQmvva 152,36,164\n"
"Qe 7\nQmvva 156,44,168\nQk 42,4,5\nQk 43,6,7\nQmvva 156,36,172\nQe 8\nQmvva 188,48,168\ns_add_u32 s95, s87, 146432\n"
"s_sub_u32 s50, 4194304, s68\nQmvva 188,40,172\nbuffer_load_dwordx4 v61, s[52:55], s95 offen offset:1024 lds\nQmvva 184,48,160\nQe 9\nQmvva 184,40,164\nQe 10\nQmvva 180,48,152\n"
"Qe 11\nQmvva 180,40,156\nQk 46,8,9\nQk 47,10,11\nQmvva 176,48,144\ns_add_u32 m0, s96, 73728\ns_add_u32 s95, s87, 86016\ns_add_u32 s71, s71, 1048576\n"
"Qmvva 176,40,148\nbuffer_load_dwordx4 v62, s[52:55], s95 offen lds\nQcvaa 0,112,80,56\nQe 12\nQmvaa 0,116,84\nQe 13\nQmvaa 0,120,88\ns_add_u32 s95, s87, 125952\n"
"Qk 44,12,13\nQmvaa 0,124,92\nbuffer_load_dwordx4 v62, s[52:55], s95 offen offset:1024 lds\nQcvaa 4,140,92,56\nQe 14\nQmvaa 4,136,88\nQe 15\nQmvaa 4,132,84\n"
"s_waitcnt vmcnt(16)\nv_sub_u32 v66, v78, v65\nv_cmp_gt_u32 vcc, 1, v66\ns_and_b64 vcc, vcc, s[28:29]\ns_cbranch_scc1 .Lf1_w\n.Lf1_r:\nQmvaa 4,128,80\ns_add_u32 m0, s96, 81920\n"
"s_add_u32 s95, s87, 86016\nQk 45,14,15\nQcvaa 8,128,96,56\nbuffer_load_dwordx4 v63, s[52:55], s95 offen lds\nQmvaa 8,132,100\nQe 0\nQmvaa 8,136,104\nQe 1\n"
"Qmvaa 8,140,108\nQe 2\nQcvaa 12,124,108,56\nQe 3\nQmvaa 12,120,104\nQk 48,0,1\nQk 49,2,3\nQmvaa 12,116,100\n"
"Qe 4\nQmvaa 12,112,96\nQe 5\nv_smfmac_f32_16x16x64_bf16 a[196:199], a[204:207], v[40:47], v77\ns_add_u32 s95, s87, 105472\nQmvva 208,16,144\nQta 112,71,33152\nbuffer_load_dwordx4 v63, s[52:55], s95 offen offset:1024 lds\n"
"Qmvva 208,44,148\nQta 114,71,34176\nQk 50,4,5\nQmvva 212,16,152\nQta 116,71,33408\ns_add_u32 m0, s96, 90112\ns_add_u32 s95, s87, 126976\nQmvva 212,44,156\n"
"Qta 118,71,34432\nbuffer_load_dwordx4 v63, s[52:55], s95 offen lds\nQmvva 216,16,160\nQta 120,71,33664\nQmvva 216,44,164\nQta 122,71,34688\nQmvva 220,16,168\nQta 124,71,33920\n"
"s_add_u32 s95, s87, 146432\nQmvva 220,44,172\nQta 126,71,34944\nbuffer_load_dwordx4 v63, s[52:55], s95 offen offset:1024 lds\nQe 6\nQe 7\ns_nop 0\nQk 51,6,7\n"
"Qmvva 240,20,144\nQe 8\nQmvva 240,48,148\nQe 9\nQmvva 244,20,152\nQe 10\nQmvva 244,48,156\nQe 11\n"
"Qmvva 248,20,160\nQk 54,8,9\nQk 55,10,11\nQmvva 248,48,164\nQe 12\nQmvva 252,20,168\nQe 13\nQmvva 252,48,172\n"
"Qe 14\nQk 52,12,13\nQe 15\ns_nop 0\nQk 53,14,15\ns_nop 1\nv_smfmac_f32_16x16x64_bf16 a[200:203], a[204:207], v[48:55], v77\nQmava 224,24,144\n"
"v_accvgpr_read_b32 v0, a176\nv_accvgpr_read_b32 v1, a177\nQmava 224,52,148\nv_accvgpr_read_b32 v6, a178\nv_accvgpr_read_b32 v7, a179\nQmava 228,24,152\nv_accvgpr_read_b32 v8, a180\nv_accvgpr_read_b32 v9, a181\n"
"Qmava 228,52,156\nv_accvgpr_read_b32 v14, a182\nv_accvgpr_read_b32 v15, a183\nQmava 232,24,160\nv_accvgpr_read_b32 v16, a184\nv_accvgpr_read_b32 v17, a185\nQmava 232,52,164\nv_accvgpr_read_b32 v22, a186\n"
"v_accvgpr_read_b32 v23, a187\nQmava 236,24,168\nv_accvgpr_read_b32 v2, a190\nv_accvgpr_read_b32 v3, a191\nQmava 236,52,172\ns_waitcnt lgkmcnt(0)\nv_accvgpr_read_b32 v24, a188\nQmvva 96,28,112\n"
"v_accvgpr_read_b32 v25, a189\nv_accvgpr_read_b32 v4, a192\nQmvva 100,28,116\nv_accvgpr_read_b32 v5, a193\nv_accvgpr_read_b32 v10, a194\nQmvva 104,28,120\nv_accvgpr_read_b32 v11, a195\nv_accvgpr_read_b32 v12, a196\n"
"Qmvva 108,28,124\nv_accvgpr_read_b32 v13, a197\nv_accvgpr_read_b32 v18, a198\nQmvva 128,32,112\nv_accvgpr_read_b32 v19, a199\nv_accvgpr_read_b32 v20, a200\nQmvva 132,32,116\nv_accvgpr_read_b32 v21, a201\n"
"v_accvgpr_read_b32 v26, a202\nQmvva 136,32,120\nv_accvgpr_read_b32 v27, a203\nQa 8,8,9\nQmvva 140,32,124\nQa 10,10,11\nQa 8,8,10\nQmvva 160,36,112\n"
"Qa 16,16,17\nQa 18,18,19\nQmvva 164,36,116\nQa 16,16,18\nQa 4,4,5\nQmvva 168,36,120\nQa 6,6,7\nQa 4,4,6\n"
"Qmvva 172,36,124\nQa 12,12,13\nQa 14,14,15\nQmvva 192,40,112\nQa 12,12,14\nQa 0,0,1\nQmvva 196,40,116\nQa 2,2,3\n"
"Qa 0,0,2\nQmvva 200,40,120\nQa 24,24,25\nQa 26,26,27\nQmvva 204,40,124\nQa 24,24,26\nQa 20,20,21\nQmvva 224,44,112\n"
"Qa 22,22,23\nQa 20,20,22\nQmvva 228,44,116\nv_max3_u32 v9, v8, v16, v4\nv_max3_u32 v9, v9, v12, v0\nQmvva 232,44,120\nv_max3_u32 v9, v9, v24, v20\nv_min3_u32 v11, v8, v16, v4\n"
"Qmvva 236,44,124\nv_min3_u32 v11, v11, v12, v0\nv_min3_u32 v11, v11, v24, v20\nQmava 208,48,112\nv_cmp_lt_u32 vcc, 0x4e800000, v9\ns_mov_b64 s[74:75], vcc\nQmava 212,48,116\nv_cmp_gt_u32 vcc, 0x35800000, v11\n"
"s_or_b64 s[74:75], s[74:75], vcc\nQmava 216,48,120\nv_mov_b32 v30, s73\nv_mov_b32 v31, 0\nQmava 220,48,124\nv_mov_b32 v58, 0xb9800000\nv_mov_b32 v59, 0\nQmava 240,52,112\n"
"v_and_b32 v66, 16, v60\nv_mov_b32 v67, 0xffff\nQmava 244,52,116\nv_lshlrev_b32 v66, v66, v67\ns_mov_b32 vcc_lo, 0x00c0000c\nQmava 248,52,120\ns_mov_b32 vcc_hi, 0xc0000c00\nv_cndmask_b32 v67, 0, v66, vcc\n"
"Qmava 252,52,124\ns_lshr_b64 vcc, vcc, 2\nv_cndmask_b32 v66, 0, v66, vcc\ns_lshl_b32 s68, s93, 12\ns_add_u32 s68, s68, %4\nv_lshrrev_b32 v69, 9, v60\nv_lshlrev_b32 v69, 11, v69\nv_bfe_u32 v70, v60, 1, 8\n"
"v_add3_u32 v68, v69, v70, s68\nv_add_u32 v69, 65536, v68\nv_add_u32 v70, 131072, v68\ns_cmp_lg_u64 s[74:75], 0\ns_cbranch_scc1 .Lfb\n.Lfbret1:\nv_rcp_f32 v2, v0\nv_rcp_f32_e64 v6, v4\n"
"v_mfma_f32_16x16x4_4b_bf16 v[80:95], v[30:31], v[58:59], v[80:95]\nv_rcp_f32 v10, v8\nv_rcp_f32 v14, v12\nv_rcp_f32 v18, v16\nv_rcp_f32 v22, v20\nv_rcp_f32 v26, v24\nv_mov_b64 v[0:1], 0\nv_mfma_f32_16x16x4_4b_bf16 v[112:127], v[30:31], v[58:59], v[112:127]\n"
"Qk 2,2,2\nv_and_b32 v3, v2, v67\nv_and_b32 v2, v2, v66\nv_mov_b64_e64 v[4:5], 0\nQk 6,6,6\nv_and_b32_e64 v7, v6, v67\nv_mfma_f32_16x16x4_4b_bf16 v[144:159], v[30:31], v[58:59], v[144:159]\nv_and_b32 v6, v6, v66\n"
"v_mov_b64 v[8:9], 0\nQk 10,10,10\nv_and_b32 v11, v10, v67\nv_and_b32 v10, v10, v66\nv_mov_b64_e64 v[12:13], 0\nv_mfma_f32_16x16x4_4b_bf16 v[176:191], v[30:31], v[58:59], v[176:191]\nQk 14,14,14\nv_and_b32 v15, v14, v67\n"
"v_and_b32 v14, v14, v66\nv_mov_b64_e64 v[16:17], 0\nQk 18,18,18\nv_and_b32_e64 v19, v18, v67\nv_mfma_f32_16x16x4_4b_bf16 v[208:223], v[30:31], v[58:59], v[208:223]\nv_and_b32 v18, v18, v66\nv_mov_b64 v[20:21], 0\nQk 22,22,22\n"
"v_and_b32 v23, v22, v67\nv_and_b32 v22, v22, v66\nv_mov_b64_e64 v[24:25], 0\nv_mfma_f32_16x16x4_4b_bf16 v[240:255], v[30:31], v[58:59], v[240:255]\nQk 26,26,26\nv_and_b32 v27, v26, v67\nv_and_b32 v26, v26, v66\nv_mov_b64_e64 v[28:29], 0\n"
"Qk 80,80,81\nQk 81,82,83\nQk 82,84,85\nQk 83,86,87\nQk 112,112,113\nQk 113,114,115\nQzvvv 84,80,0\nQk 114,116,117\n"
"Qk 115,118,119\nQzvvv 80,80,2\nQk 144,144,145\nQk 145,146,147\nQzvvv 116,112,4\nQk 146,148,149\nQk 147,150,151\nQzvvv 112,112,6\n"
"Qk 176,176,177\nQk 177,178,179\nv_mfma_f32_16x16x4_4b_bf16 a[224:239], v[30:31], v[58:59], a[224:239]\nQk 80,80,81\nQk 81,82,83\nQk 82,84,85\nQk 83,86,87\nQk 178,180,181\n"
"Qk 179,182,183\nQzvvv 148,144,8\nQk 112,112,113\nQk 113,114,115\nQzvvv 144,144,10\nQk 114,116,117\nQk 115,118,119\nv_mfma_f32_16x16x4_4b_bf16 v[96:111], v[30:31], v[58:59], v[96:111]\n"
"Qk 208,208,209\nQk 209,210,211\nQk 210,212,213\nQk 211,214,215\nQk 240,240,241\nQk 144,144,145\nQzvvv 180,176,12\nQk 145,146,147\n"
"Qk 146,148,149\nQzvvv 176,176,14\nQk 147,150,151\nQk 241,242,243\nv_mfma_f32_16x16x4_4b_bf16 v[128:143], v[30:31], v[58:59], v[128:143]\nQk 242,244,245\nQk 243,246,247\nv_accvgpr_read_b32 v84, a224\n"
"v_accvgpr_read_b32 v85, a225\nv_accvgpr_read_b32 v86, a226\nQk 176,176,177\nQzvvv 212,208,16\nQk 177,178,179\nQk 178,180,181\nQzvvv 208,208,18\nQk 179,182,183\n"
"v_accvgpr_read_b32 v87, a227\nv_mfma_f32_16x16x4_4b_bf16 v[160:175], v[30:31], v[58:59], v[160:175]\nv_accvgpr_read_b32 v32, a228\nv_accvgpr_read_b32 v33, a229\nv_accvgpr_read_b32 v34, a230\nv_accvgpr_read_b32 v35, a231\nQk 84,84,85\nQk 85,86,87\n"
"Qzvvv 244,240,20\nQk 86,32,33\nQk 87,34,35\nQzvvv 240,240,22\nQk 208,208,209\nQk 209,210,211\nv_mfma_f32_16x16x4_4b_bf16 v[192:207], v[30:31], v[58:59], v[192:207]\nQk 210,212,213\n"
"Qk 211,214,215\nQk 88,88,89\nQk 89,90,91\nQk 90,92,93\nQk 91,94,95\nQzvvv 32,84,24\nQk 240,240,241\nQk 241,242,243\n"
"Qzvvv 84,84,26\nQk 242,244,245\nQk 243,246,247\nv_mfma_f32_16x16x4_4b_bf16 v[224:239], v[30:31], v[58:59], v[224:239]\nQk 120,120,121\nQk 121,122,123\nQk 122,124,125\nQk 123,126,127\n"
"Qk 152,152,153\nQk 84,84,85\nQzvvv 92,88,0\nQk 85,86,87\nQk 86,32,33\nQzvvv 88,88,2\nQk 87,34,35\nQk 153,154,155\n"
"v_mfma_f32_16x16x4_4b_bf16 a[208:223], v[30:31], v[58:59], a[208:223]\nQk 154,156,157\nQk 155,158,159\ns_waitcnt vmcnt(21)\ns_barrier\nQk 184,184,185\nQk 88,88,89\nQzvvv 124,120,4\n"
"Qk 89,90,91\nQk 90,92,93\nQzvvv 120,120,6\nQk 91,94,95\nQk 185,186,187\nv_mfma_f32_16x16x4_4b_bf16 a[240:255], v[30:31], v[58:59], a[240:255]\nQk 186,188,189\nQk 187,190,191\n"
"Qta 56,68,0\nQk 216,216,217\nQk 217,218,219\nQk 120,120,121\nQzvvv 156,152,8\nQk 121,122,123\nQk 122,124,125\nQzvvv 152,152,10\n"
"Qk 123,126,127\nQk 218,220,221\nQzvvv 188,184,12\nQk 219,222,223\nQta 58,68,1024\nQzvvv 184,184,14\nQk 248,248,249\nQk 249,250,251\n"
"Qk 152,152,153\nQk 153,154,155\nQzvvv 220,216,16\n.Lpg10:\ns_cbranch_execz .Lpg14\ns_nop 0\nQk 154,156,157\nQk 155,158,159\n"
"Qzvvv 216,216,18\nQk 250,252,253\nQk 251,254,255\nQk 184,184,185\nQk 185,186,187\nQzvvv 252,248,20\nQk 186,188,189\nQk 187,190,191\n"
"Qzvvv 248,248,22\nQta 60,68,8192\nv_accvgpr_read_b32 v116, a232\nv_accvgpr_read_b32 v117, a233\nv_accvgpr_read_b32 v118, a234\nv_accvgpr_read_b32 v119, a235\nv_accvgpr_read_b32 v36, a236\nv_accvgpr_read_b32 v37, a237\n"
"v_accvgpr_read_b32 v38, a238\nv_accvgpr_read_b32 v39, a239\nQk 116,116,117\nQk 117,118,119\nQk 118,36,37\nQk 119,38,39\nQk 216,216,217\nQk 217,218,219\n"
"Qzvvv 36,116,24\nQk 218,220,221\nQk 219,222,223\nQzvvv 116,116,26\nQta 62,68,9216\ns_mov_b32 s31, 0\ns_nop 0\nQk 96,96,97\n"
"Qk 97,98,99\nQk 98,100,101\nQk 99,102,103\nQk 248,248,249\nQk 249,250,251\nQzvvv 100,96,0\nQk 250,252,253\nQk 251,254,255\n"
"Qzvvv 96,96,2\nbuffer_load_dwordx4 a[0:3], v79, s[48:51], s31 offen offset:0 sc1\nQta 64,68,256\nQk 128,128,129\nQk 129,130,131\nQk 130,132,133\nQk 131,134,135\nQk 116,116,117\n"
"Qk 117,118,119\nQzvvv 132,128,4\nQk 118,36,37\nQk 119,38,39\nQzvvv 128,128,6\nbuffer_load_dwordx4 a[4:7], v79, s[48:51], s31 offen offset:1024 sc1\nQta 66,68,1280\ns_waitcnt lgkmcnt(2)\n"
"s_nop 0\nQzvav 56,56,80\nQk 160,160,161\nQk 161,162,163\nQmvav 56,60,88\nQk 162,164,165\nQk 163,166,167\nbuffer_load_dwordx4 a[8:11], v79, s[48:51], s31 offen offset:2048 sc1\n"
"Qta 68,68,8448\nQzvvv 164,160,8\nQk 192,192,193\nQk 193,194,195\nQzvvv 160,160,10\nQk 194,196,197\nQk 195,198,199\nbuffer_load_dwordx4 a[12:15], v79, s[48:51], s31 offen offset:3072 sc1\n"
"Qta 70,68,9472\nQzvvv 196,192,12\nv_max3_f32 v66, |v56|, |v57|, |v58|\nv_max_f32 v66, v66, |v59|\nQzvvv 192,192,14\nv_mov_b32_e64 v67, v66\ns_mov_b32 s31, 4096\nQk 224,224,225\n"
"Qk 225,226,227\nQk 226,228,229\nQk 227,230,231\nbuffer_load_dwordx4 a[16:19], v79, s[48:51], s31 offen offset:0 sc1\nv_permlane32_swap_b32_e64 v66, v67\nQzvvv 228,224,16\nv_max_f32 v66, v66, v67\nv_mov_b32 v67, v66\n"
"Qzvvv 224,224,18\nv_accvgpr_read_b32 v148, a208\nv_accvgpr_read_b32 v149, a209\nv_accvgpr_read_b32 v150, a210\nv_accvgpr_read_b32 v151, a211\nv_accvgpr_read_b32 v40, a212\nv_accvgpr_read_b32 v41, a213\nv_accvgpr_read_b32 v42, a214\n"
"v_accvgpr_read_b32 v43, a215\nQk 148,148,149\nQk 149,150,151\nQk 150,40,41\nQk 151,42,43\nbuffer_load_dwordx4 a[20:23], v79, s[48:51], s31 offen offset:1024 sc1\nv_permlane16_swap_b32_e64 v66, v67\nQzvvv 40,148,20\n"
"v_max_f32_e64 v66, v66, v67\nv_accvgpr_read_b32 v180, a240\nQzvvv 148,148,22\nv_accvgpr_read_b32 v181, a241\nv_accvgpr_read_b32 v182, a242\nv_accvgpr_read_b32 v183, a243\nv_accvgpr_read_b32 v44, a244\nv_accvgpr_read_b32 v45, a245\n"
"v_accvgpr_read_b32 v46, a246\nv_accvgpr_read_b32 v47, a247\nQk 180,180,181\nQk 181,182,183\nQk 182,44,45\nQk 183,46,47\nbuffer_load_dwordx4 a[24:27], v79, s[48:51], s31 offen offset:2048 sc1\nv_max_f32_dpp v66, v66, v66 row_ror:8 row_mask:0xf bank_mask:0xf\n"
"Qzvvv 44,180,24\ns_mov_b32 s31, 16384\nQk 104,104,105\nQzvvv 180,180,26\nQk 105,106,107\nQk 106,108,109\nQk 107,110,111\nbuffer_load_dwordx4 a[28:31], v79, s[48:51], s31 offen offset:0 sc1\n"
"v_max_f32_dpp v66, v66, v66 row_ror:4 row_mask:0xf bank_mask:0xf\nQzvvv 108,104,0\nQk 136,136,137\nQk 137,138,139\nQzvvv 104,104,2\nQk 138,140,141\nQk 139,142,143\nbuffer_load_dwordx4 a[32:35], v79, s[48:51], s31 offen offset:1024 sc1\n"
"v_max_f32_dpp v66, v66, v66 row_ror:2 row_mask:0xf bank_mask:0xf\nQzvvv 140,136,4\nQk 168,168,169\nQk 169,170,171\nQzvvv 136,136,6\nQk 170,172,173\nQk 171,174,175\nbuffer_load_dwordx4 a[36:39], v79, s[48:51], s31 offen offset:2048 sc1\n"
"v_max_f32_dpp v66, v66, v66 row_ror:1 row_mask:0xf bank_mask:0xf\nQzvvv 172,168,8\nv_and_b32 v66, 0x7f800000, v66\nv_add_u32 v66, 0x3000000, v66\nQzvvv 168,168,10\nQk 200,200,201\nQk 201,202,203\nQk 202,204,205\n"
"Qk 203,206,207\nbuffer_load_dwordx4 a[40:43], v79, s[48:51], s31 offen offset:3072 sc1\nv_min_u32 v66, 0x7e000000, v66\nQzvvv 204,200,12\nv_or_b32 v66, 0x400000, v66\nds_write_b32 v73, v66 offset:16\nQzvvv 200,200,14\nv_xor_b32 v67, 0x80000000, v66\n"
"s_mov_b32 s31, 20480\nQk 232,232,233\nQk 233,234,235\nQk 234,236,237\nQk 235,238,239\nbuffer_load_dwordx4 a[44:47], v79, s[48:51], s31 offen offset:0 sc1\nv_accvgpr_write_b32 a112, v67\nQzvvv 236,232,16\n"
"v_accvgpr_write_b32 a113, v67\nv_accvgpr_write_b32 a114, v67\nQzvvv 232,232,18\nv_accvgpr_write_b32 a115, v67\nv_accvgpr_read_b32 v212, a216\nv_accvgpr_read_b32 v213, a217\nv_accvgpr_read_b32 v214, a218\nv_accvgpr_read_b32 v215, a219\n"
"v_accvgpr_read_b32 v48, a220\nv_accvgpr_read_b32 v49, a221\nv_accvgpr_read_b32 v50, a222\nv_accvgpr_read_b32 v51, a223\nQk 212,212,213\nQk 213,214,215\nQk 214,48,49\nQk 215,50,51\n"
"buffer_load_dwordx4 a[48:51], v79, s[48:51], s31 offen offset:1024 sc1\nv_accvgpr_read_b32 v244, a248\nQzvvv 48,212,20\nv_accvgpr_read_b32 v245, a249\nv_accvgpr_read_b32 v246, a250\nQzvvv 212,212,22\nv_accvgpr_read_b32 v247, a251\nv_accvgpr_read_b32 v52, a252\n"
"v_accvgpr_read_b32 v53, a253\nv_accvgpr_read_b32 v54, a254\nv_accvgpr_read_b32 v55, a255\nQk 244,244,245\nQk 245,246,247\nQk 246,52,53\nQk 247,54,55\nbuffer_load_dwordx4 a[52:55], v79, s[48:51], s31 offen offset:2048 sc1\n"
"s_nop 0\ns_nop 0\nQzvvv 52,244,24\nQzvvv 244,244,26\ns_waitcnt lgkmcnt(5)\nQcaav 140,56,84,112\ns_add_u32 m0, s96, 98304\ns_mov_b32 s95, 86016\n"
"Qmaav 140,60,116\nbuffer_load_dwordx4 v61, s[52:55], s95 offen lds\nQcaav 136,56,240,112\ns_mov_b32 s56, s20\ns_and_b32 s57, s21, 0xffff\nQmaav 136,60,248\ns_mov_b32 s58, 4194304\nQk 244,244,245\n"
"Qcaav 132,56,208,112\nQta 72,68,16384\nQk 245,246,247\nQmaav 132,60,216\ns_mov_b32 s95, 105472\nQk 246,52,53\nQcaav 128,56,176,112\nbuffer_load_dwordx4 v61, s[52:55], s95 offen offset:1024 lds\n"
"Qmaav 128,60,184\nQta 74,68,16896\nQk 247,54,55\nQcaav 124,56,144,112\nQk 212,212,213\nQk 213,214,215\nQmaav 124,60,152\nQk 214,48,49\n"
"Qk 215,50,51\nQcaav 120,56,112,112\nQta 76,68,17408\nQk 180,180,181\nQmaav 120,60,120\ns_add_u32 s88, s71, 0\nQk 181,182,183\nQcaav 116,56,80,112\n"
"buffer_store_dwordx4 v[80:83], v79, s[56:59], s88 offen offset:0 sc1\nQmaav 116,60,88\nQta 78,68,17920\nQk 182,44,45\ns_waitcnt lgkmcnt(5)\nQcaav 144,64,80,112\nQk 183,46,47\nQk 148,148,149\n"
"Qmaav 144,68,88\nQk 149,150,151\nQk 150,40,41\nQcaav 148,64,112,112\nQk 151,42,43\nQk 96,96,97\nQmaav 148,68,120\nQk 97,98,99\n"
"Qk 98,100,101\nQcaav 152,64,144,112\nQta 56,68,24576\nQk 99,102,103\nQmaav 152,68,152\ns_add_u32 m0, s96, 106496\ns_mov_b32 s95, 126976\nQcaav 156,64,176,112\n"
"buffer_load_dwordx4 v61, s[52:55], s95 offen lds\nQmaav 156,68,184\nQta 58,68,25600\nQk 168,168,169\nQcaav 160,64,208,112\nQk 169,170,171\nQk 170,172,173\nQmaav 160,68,216\n"
"Qk 171,174,175\nQk 136,136,137\nQcaav 164,64,240,112\nQta 60,68,32768\ns_mov_b32 s95, 146432\nQmaav 164,68,248\nbuffer_load_dwordx4 v61, s[52:55], s95 offen offset:1024 lds\nQcaav 168,64,84,112\n"
"Qk 137,138,139\nQk 138,140,141\nQmaav 168,68,116\nQta 62,68,33792\nQk 139,142,143\ns_waitcnt lgkmcnt(4)\nQcaav 196,72,84,112\nQk 104,104,105\n"
"Qk 105,106,107\nQmaav 196,76,116\ns_add_u32 m0, s96, 114688\ns_mov_b32 s95, 86016\nQcaav 192,72,240,112\nbuffer_load_dwordx4 v62, s[52:55], s95 offen lds\nQmaav 192,76,248\nQk 106,108,109\n"
"Qk 107,110,111\nQcaav 188,72,208,112\nQta 64,68,24832\nQk 224,224,225\nQmaav 188,76,216\nbuffer_store_dwordx4 v[112:115], v79, s[56:59], s88 offen offset:1024 sc1\nQcaav 184,72,176,112\nQk 225,226,227\n"
"Qk 226,228,229\nQmaav 184,76,184\nQta 66,68,25856\nQk 227,230,231\nQcaav 180,72,144,112\nQk 192,192,193\nQk 193,194,195\nQmaav 180,76,152\n"
"Qk 194,196,197\nQk 195,198,199\nQcaav 176,72,112,112\nQta 68,68,33024\nv_add_u32 v75, 1, v75\nQmaav 176,76,120\ns_waitcnt vmcnt(35)\nds_write_b32 v73, v75\n"
"Qcaav 172,72,80,112\nQk 160,160,161\nQk 161,162,163\nQmaav 172,76,88\nQta 70,68,34048\ns_mov_b32 s95, 125952\ns_waitcnt lgkmcnt(5)\nQcaav 200,56,80,112\n"
"buffer_load_dwordx4 v62, s[52:55], s95 offen offset:1024 lds\nQmaav 200,60,88\nQk 162,164,165\nQk 163,166,167\nQcaav 204,56,112,112\nQk 128,128,129\nQk 129,130,131\nQmaav 204,60,120\n"
"Qk 130,132,133\nQk 131,134,135\nQcaav 208,56,144,112\nQta 72,68,512\nQk 200,200,201\nQmaav 208,60,152\nds_read_b32 v66, v74\ns_add_u32 m0, s96, 122880\n"
"s_mov_b32 s95, 86016\nQcaav 212,56,176,112\nbuffer_load_dwordx4 v63, s[52:55], s95 offen lds\nQmaav 212,60,184\nQta 74,68,1536\nQk 201,202,203\nQcaav 216,56,208,112\nQk 202,204,205\n"
"Qk 203,206,207\nQmaav 216,60,216\ns_waitcnt lgkmcnt(1)\nv_cmp_gt_u32 vcc, v75, v66\nQcaav 220,56,240,112\ns_cbranch_vccnz .Lso40\n.Lro40:\nQmaav 220,60,248\n"
"Qta 76,68,8704\nQk 232,232,233\nQcaav 224,56,84,112\nbuffer_store_dwordx4 v[144:147], v79, s[56:59], s88 offen offset:2048 sc1\nv_xor_b32 v56, 8, v73\nQmaav 224,60,116\nds_read_b32 v56, v56 offset:16\nQk 233,234,235\n"
"s_waitcnt lgkmcnt(5)\nQcaav 252,64,84,112\nQta 78,68,9728\nQk 234,236,237\nQmaav 252,68,116\nQk 235,238,239\nQcaav 248,64,240,112\nQmaav 248,68,248\n"
"Qcaav 244,64,208,112\nQta 56,68,768\nQmaav 244,68,216\ns_mov_b32 s95, 105472\nQcaav 240,64,176,112\nbuffer_load_dwordx4 v63, s[52:55], s95 offen offset:1024 lds\nQmaav 240,68,184\nQta 58,68,1792\n"
"s_waitcnt lgkmcnt(3)\nv_mov_b32 v57, v56\nQcaav 236,64,144,112\nv_mov_b32 v58, v56\nv_mov_b32 v59, v56\nQmaav 236,68,152\nv_accvgpr_write_b32 a80, v56\nv_accvgpr_write_b32 a81, v56\n"
"Qcaav 232,64,112,112\nQta 60,68,8960\nv_accvgpr_write_b32 a82, v56\nQmaav 232,68,120\nv_accvgpr_write_b32 a83, v56\nQcaav 228,64,80,112\nQmaav 228,68,88\nQta 62,68,9984\n"
"s_waitcnt lgkmcnt(4)\nQcvav 0,72,80,56\nQmvav 0,76,88\nQcvav 4,72,112,56\nbuffer_store_dwordx4 v[176:179], v79, s[56:59], s88 offen offset:3072 sc1\nQmvav 4,76,120\nQcvav 8,72,144,56\nQta 64,68,16640\n"
"Qmvav 8,76,152\nQcvav 12,72,176,56\nQmvav 12,76,184\nQta 66,68,17152\nQcvav 16,72,208,56\nQmvav 16,76,216\nQcvav 20,72,240,56\nQta 68,68,17664\n"
"Qmvav 20,76,248\nQcvav 24,72,84,56\nQmvav 24,76,116\nQta 70,68,18176\ns_waitcnt lgkmcnt(4)\nQcvav 52,56,84,56\nQmvav 52,60,116\nQcvav 48,56,240,56\n"
"Qmvav 48,60,248\nQcvav 44,56,208,56\nQta 72,68,25088\nQmvav 44,60,216\nQcvav 40,56,176,56\nQmvav 40,60,184\nQta 74,68,26112\ns_add_u32 s88, s71, 4096\n"
"Qcvav 36,56,144,56\nbuffer_store_dwordx4 v[208:211], v79, s[56:59], s88 offen offset:0 sc1\nQmvav 36,60,152\nQcvav 32,56,112,56\nQta 76,68,33280\nQmvav 32,60,120\nQcvav 28,56,80,56\nQmvav 28,60,88\n"
"Qta 78,68,34304\ns_waitcnt lgkmcnt(4)\nQcvav 92,64,80,56\nQmvav 92,68,88\nQcvav 124,64,112,56\ns_lshl_b32 s46, s19, 4\nQmvav 124,68,120\ns_cmp_eq_u32 s70, 0\n"
"s_cselect_b32 s46, s46, -1\nQcvav 156,64,144,56\nQta 56,68,25344\nv_mov_b32 v77, 0x7ffff000\nQmvav 156,68,152\nQcvav 188,64,176,56\nQmvav 188,68,184\nQta 58,68,26368\n"
"Qcvav 220,64,208,56\nQmvav 220,68,216\nQcvav 252,64,240,56\nQta 60,68,33536\nQmvav 252,68,248\nQcvav 100,64,84,56\nQmvav 100,68,116\nQta 62,68,34560\n"
"s_waitcnt lgkmcnt(4)\nQcvav 172,72,84,56\nbuffer_store_dwordx4 v[240:243], v79, s[56:59], s88 offen offset:1024 sc1\nQmvav 172,76,116\nQcvav 140,72,240,56\nQmvav 140,76,248\nQcvav 108,72,208,56\nQta 64,68,40960\n"
"Qmvav 108,76,216\nQcvav 228,72,176,56\nQmvav 228,76,184\nQta 66,68,41984\nQcvav 196,72,144,56\nQmvav 196,76,152\nQcvav 164,72,112,56\nQta 68,68,49152\n"
"Qmvav 164,76,120\nQcvav 132,72,80,56\nQmvav 132,76,88\nQta 70,68,50176\ns_waitcnt lgkmcnt(4)\nQcvav 204,56,80,56\nQmvav 204,60,88\nQcvav 236,56,112,56\n"
"Qmvav 236,60,120\nQcaav 96,56,144,80\nQta 72,68,41216\nQmaav 96,60,152\nQcaav 100,56,176,80\nbuffer_store_dwordx4 v[84:87], v79, s[56:59], s88 offen offset:2048 sc1\nQmaav 100,60,184\nQta 74,68,42240\n"
"Qcaav 104,56,208,80\nQmaav 104,60,216\nQcaav 108,56,240,80\nQta 76,68,49408\nQmaav 108,60,248\nQcaav 112,56,84,80\nQmaav 112,60,116\nQta 78,68,50432\n"
"s_waitcnt vmcnt(15)\ns_waitcnt lgkmcnt(4)\nQmaaa 140,64,24\nQmaaa 140,68,52\nQmaaa 136,64,20\nQmaaa 136,68,48\nQmaaa 132,64,16\nQta 56,68,41472\n"
"Qmaaa 132,68,44\nQmaaa 128,64,12\nQmaaa 128,68,40\nQta 58,68,42496\nQmaaa 124,64,8\nQmaaa 124,68,36\nQmaaa 120,64,4\nQta 60,68,49664\n"
"s_and_b32 s68, s19, 3\nQmaaa 120,68,32\ns_add_u32 s88, s71, 16384\nQmaaa 116,64,0\nbuffer_store_dwordx4 v[88:91], v79, s[56:59], s88 offen offset:0 sc1\nQmaaa 116,68,28\nQta 62,68,50688\ns_waitcnt lgkmcnt(4)\n"
"Qmaaa 144,72,0\n.Lpg11:\ns_cbranch_execz .Lpg15\ns_cmp_lt_u32 s68, 1\ns_cselect_b32 s68, 40, 32\nQmaaa 144,76,28\ns_mul_i32 s67, s68, 40960\ns_mul_i32 s74, s68, 32768\n"
"Qmaaa 148,72,4\ns_and_b32 s68, s19, 3\nQmaaa 148,76,32\ns_cmp_lt_u32 s68, 2\ns_cselect_b32 s68, 48, 40\nQmaaa 152,72,8\nQta 64,68,41728\ns_mul_i32 s72, s68, 40960\n"
"Qmaaa 152,76,36\ns_mul_i32 s75, s68, 32768\ns_and_b32 s68, s19, 3\nQmaaa 156,72,12\ns_cmp_lt_u32 s68, 3\ns_cselect_b32 s68, 56, 48\nQmaaa 156,76,40\nQta 66,68,42752\n"
"s_mul_i32 s73, s68, 40960\nQmaaa 160,72,16\ns_mul_i32 s77, s68, 32768\nv_add_u32 v58, s91, v60\nQmaaa 160,76,44\ns_mul_i32 s68, s93, 28672\ns_sub_u32 s68, 14336, s68\nQmaaa 164,72,20\n"
"Qta 68,68,49920\nv_add_u32 v59, s68, v58\nQmaaa 164,76,48\nQmaaa 168,72,24\nQmaaa 168,76,52\nQta 70,68,50944\ns_waitcnt lgkmcnt(4)\nQmvaa 24,56,24\n"
"Qmvaa 24,60,52\nQmvaa 20,56,20\nQmvaa 20,60,48\nQmvaa 16,56,16\nQta 72,68,57344\nQmvaa 16,60,44\nbuffer_store_dwordx4 v[120:123], v79, s[56:59], s88 offen offset:1024 sc1\nQmvaa 12,56,12\n"
"Qmvaa 12,60,40\nQta 74,68,57856\nQmvaa 8,56,8\nQmvaa 8,60,36\nQmvaa 4,56,4\nQta 76,68,58368\nv_add_u32 v75, 1, v75\nQmvaa 4,60,32\n"
"s_waitcnt vmcnt(35)\nds_write_b32 v73, v75\nQmvaa 0,56,0\nQmvaa 0,60,28\nQta 78,68,58880\ns_waitcnt lgkmcnt(5)\nQmvaa 28,64,0\nQmvaa 28,68,28\n"
"Qmvaa 32,64,4\nQmvaa 32,68,32\nQmvaa 36,64,8\nQta 56,68,57600\nQmvaa 36,68,36\nds_read_b32 v66, v74\nQmvaa 40,64,12\nQmvaa 40,68,40\n"
"Qta 58,68,58112\nQmvaa 44,64,16\nQmvaa 44,68,44\ns_waitcnt lgkmcnt(1)\nv_cmp_gt_u32 vcc, v75, v66\nQmvaa 48,64,20\ns_cbranch_vccnz .Lso180\n.Lro180:\n"
"Qmvaa 48,68,48\nQta 60,68,58624\nQmvaa 52,64,24\nbuffer_store_dwordx4 v[152:155], v79, s[56:59], s88 offen offset:2048 sc1\nQmvaa 52,68,52\ns_waitcnt lgkmcnt(4)\nQmaaa 196,72,24\nQta 62,68,59136\n"
"Qmaaa 196,76,52\nQmaaa 192,72,20\nQmaaa 192,76,48\nQmaaa 188,72,16\nQta 64,70,1024\nQmaaa 188,76,44\ns_add_u32 m0, s96, 0\ns_mov_b32 s95, 126976\n"
"Qmaaa 184,72,12\nbuffer_load_dwordx4 v63, s[52:55], s95 offen lds\nQmaaa 184,76,40\nQta 66,70,2048\nQmaaa 180,72,8\nQmaaa 180,76,36\nQmaaa 176,72,4\nQta 68,70,9216\n"
"s_mov_b32 s95, 146432\nQmaaa 176,76,32\nbuffer_load_dwordx4 v63, s[52:55], s95 offen offset:1024 lds\nQmaaa 172,72,0\nQmaaa 172,76,28\nQta 70,70,10240\ns_waitcnt lgkmcnt(4)\nQmvaa 92,56,0\n"
"Qmvaa 92,60,28\nQmvaa 124,56,4\nbuffer_store_dwordx4 v[184:187], v79, s[56:59], s88 offen offset:3072 sc1\nQmvaa 124,60,32\nQmvaa 156,56,8\nQta 72,70,1280\nQmvaa 156,60,36\nQmvaa 188,56,12\n"
"Qmvaa 188,60,40\nQta 74,70,2304\nQmvaa 220,56,16\nQmvaa 220,60,44\nQmvaa 252,56,20\nQta 76,70,9472\ns_add_u32 m0, s96, 8192\ns_mov_b32 s95, 331776\n"
"Qmvaa 252,60,48\nbuffer_load_dwordx4 v61, s[52:55], s95 offen lds\nQmvaa 100,56,24\nQmvaa 100,60,52\nQta 78,70,10496\ns_waitcnt lgkmcnt(4)\nQmaaa 224,64,24\nQmaaa 224,68,52\n"
"s_mov_b32 s95, 351232\nQmaaa 220,64,20\nbuffer_load_dwordx4 v61, s[52:55], s95 offen offset:1024 lds\nQmaaa 220,68,48\nQmaaa 216,64,16\nQta 56,70,1536\nQmaaa 216,68,44\nQmaaa 212,64,12\n"
"Qmaaa 212,68,40\nQta 58,70,2560\ns_add_u32 s88, s71, 20480\nQmaaa 208,64,8\nbuffer_store_dwordx4 v[216:219], v79, s[56:59], s88 offen offset:0 sc1\nQmaaa 208,68,36\nQmaaa 204,64,4\nQta 60,70,9728\n"
"Qmaaa 204,68,32\nQmaaa 200,64,0\nQmaaa 200,68,28\nQta 62,70,10752\ns_waitcnt lgkmcnt(4)\nQmaaa 228,72,0\nQmaaa 228,76,28\ns_add_u32 m0, s96, 16384\n"
"s_mov_b32 s95, 372736\nQmaaa 232,72,4\nbuffer_load_dwordx4 v61, s[52:55], s95 offen lds\nQmaaa 232,76,32\nQmaaa 236,72,8\nQta 64,70,1792\nQmaaa 236,76,36\nQmaaa 240,72,12\n"
"Qmaaa 240,76,40\nQta 66,70,2816\ns_mov_b32 s95, 392192\nQmaaa 244,72,16\nbuffer_load_dwordx4 v61, s[52:55], s95 offen offset:1024 lds\nQmaaa 244,76,44\nQmaaa 248,72,20\nQta 68,70,9984\n"
"Qmaaa 248,76,48\nQmaaa 252,72,24\nQmaaa 252,76,52\nQta 70,70,11008\ns_waitcnt lgkmcnt(4)\nQmvaa 172,56,24\nbuffer_store_dwordx4 v[248:251], v79, s[56:59], s88 offen offset:1024 sc1\nQmvaa 172,60,52\n"
"Qmvaa 140,56,20\nQmvaa 140,60,48\nQmvaa 108,56,16\nQta 72,70,17408\nQmvaa 108,60,44\nQmvaa 228,56,12\nQmvaa 228,60,40\nQta 74,70,18432\n"
"s_add_u32 m0, s96, 24576\ns_mov_b32 s95, 331776\nQmvaa 196,56,8\nbuffer_load_dwordx4 v62, s[52:55], s95 offen lds\nQmvaa 196,60,36\nQmvaa 164,56,4\nQta 76,69,0\nQmvaa 164,60,32\n"
"Qmvaa 132,56,0\nQmvaa 132,60,28\nQta 78,69,1024\ns_mov_b32 s95, 371712\ns_waitcnt lgkmcnt(4)\nQmvaa 204,64,0\nbuffer_load_dwordx4 v62, s[52:55], s95 offen offset:1024 lds\nQmvaa 204,68,28\n"
"Qmvaa 236,64,4\nQmvaa 236,68,32\nQmaaa 96,64,8\nQta 56,70,17664\nQmaaa 96,68,36\nQmaaa 100,64,12\nbuffer_store_dwordx4 v[116:119], v79, s[56:59], s88 offen offset:2048 sc1\nv_cmp_eq_u32 s[44:45], s46, v60\n"
"s_nop 1\nv_cndmask_b32 v77, v77, v76, s[44:45]\nQmaaa 100,68,40\nQta 58,70,18688\nQmaaa 104,64,16\nQmaaa 104,68,44\nQmaaa 108,64,20\nQta 60,69,256\n"
"Qmaaa 108,68,48\nQmaaa 112,64,24\nQmaaa 112,68,52\nQta 62,69,1280\ns_mov_b32 s31, 131072\ns_waitcnt lgkmcnt(4)\nQmaav 140,72,180\nbuffer_load_dwordx4 a[0:3], v79, s[48:51], s31 offen offset:0 sc1\n"
"Qmaav 140,76,244\nQmaav 136,72,148\nQmaav 136,76,212\nQmaav 132,72,224\nQta 64,70,17920\nQmaav 132,76,232\nbuffer_load_dwordx4 a[4:7], v79, s[48:51], s31 offen offset:1024 sc1\nQmaav 128,72,192\n"
"Qmaav 128,76,200\nQta 66,70,18944\nQmaav 124,72,160\nQmaav 124,76,168\nbuffer_load_dwordx4 a[8:11], v79, s[48:51], s31 offen offset:2048 sc1\nQmaav 120,72,128\nQta 68,69,512\nQmaav 120,76,136\n"
"Qmaav 116,72,96\nQmaav 116,76,104\nQta 70,69,1536\ns_waitcnt lgkmcnt(4)\nQmaav 144,56,96\nbuffer_load_dwordx4 a[12:15], v79, s[48:51], s31 offen offset:3072 sc1\nQmaav 144,60,104\nQmaav 148,56,128\n"
"Qmaav 148,60,136\nQmaav 152,56,160\nQta 72,70,18176\ns_mov_b32 s31, 135168\nQmaav 152,60,168\nbuffer_load_dwordx4 a[16:19], v79, s[48:51], s31 offen offset:0 sc1\nQmaav 156,56,192\nQmaav 156,60,200\n"
"Qta 74,70,19200\nQmaav 160,56,224\nQmaav 160,60,232\nbuffer_load_dwordx4 a[20:23], v79, s[48:51], s31 offen offset:1024 sc1\nQmaav 164,56,148\nQta 76,69,768\nQmaav 164,60,212\nQmaav 168,56,180\n"
"Qmaav 168,60,244\nQta 78,69,1792\ns_waitcnt lgkmcnt(4)\nQmvav 24,64,180\nbuffer_load_dwordx4 a[24:27], v79, s[48:51], s31 offen offset:2048 sc1\nQmvav 24,68,244\nQmvav 20,64,148\nQmvav 20,68,212\n"
"Qmvav 16,64,224\nQta 56,69,8192\ns_mov_b32 s31, 147456\nQmvav 16,68,232\nbuffer_load_dwordx4 a[28:31], v79, s[48:51], s31 offen offset:0 sc1\nQmvav 12,64,192\nQmvav 12,68,200\nQta 58,69,8704\n"
"Qmvav 8,64,160\nQmvav 8,68,168\nbuffer_load_dwordx4 a[32:35], v79, s[48:51], s31 offen offset:1024 sc1\nQmvav 4,64,128\nQta 60,69,9216\nv_add_u32 v75, 1, v75\nQmvav 4,68,136\ns_waitcnt vmcnt(31)\n"
"ds_write_b32 v73, v75\nQmvav 0,64,96\nQmvav 0,68,104\nQta 62,69,9728\ns_waitcnt lgkmcnt(5)\nQmvav 28,72,96\nbuffer_load_dwordx4 a[36:39], v79, s[48:51], s31 offen offset:2048 sc1\nQmvav 28,76,104\n"
"Qmvav 32,72,128\nQmvav 32,76,136\nQmvav 36,72,160\nQta 64,69,8448\nQmvav 36,76,168\nds_read_b32 v66, v74\nQmvav 40,72,192\nbuffer_load_dwordx4 a[40:43], v79, s[48:51], s31 offen offset:3072 sc1\n"
"Qmvav 40,76,200\nQta 66,69,8960\nQmvav 44,72,224\nQmvav 44,76,232\ns_waitcnt lgkmcnt(1)\nv_cmp_gt_u32 vcc, v75, v66\nQmvav 48,72,148\ns_cbranch_vccnz .Lso320\n"
".Lro320:\nQmvav 48,76,212\nQta 68,69,9472\ns_mov_b32 s31, 151552\nQmvav 52,72,180\nbuffer_load_dwordx4 a[44:47], v79, s[48:51], s31 offen offset:0 sc1\nQmvav 52,76,244\ns_waitcnt lgkmcnt(4)\n"
"Qmaav 196,56,180\nQta 70,69,9984\nQmaav 196,60,244\nQmaav 192,56,148\nbuffer_load_dwordx4 a[48:51], v79, s[48:51], s31 offen offset:1024 sc1\nQmaav 192,60,212\nQmaav 188,56,224\nQta 72,69,16384\n"
"Qmaav 188,60,232\nQmaav 184,56,192\nbuffer_load_dwordx4 a[52:55], v79, s[48:51], s31 offen offset:2048 sc1\nQmaav 184,60,200\nQta 74,69,17408\nQmaav 180,56,160\nQmaav 180,60,168\nQmaav 176,56,128\n"
"Qta 76,69,24576\ns_mov_b32 s31, 262144\nQmaav 176,60,136\nbuffer_load_dwordx4 v[80:83], v79, s[48:51], s31 offen offset:0 sc1\nQmaav 172,56,96\nQmaav 172,60,104\nQta 78,69,25600\ns_waitcnt lgkmcnt(4)\n"
"Qmvav 92,64,96\nQmvav 92,68,104\nbuffer_load_dwordx4 v[112:115], v79, s[48:51], s31 offen offset:1024 sc1\nQmvav 124,64,128\nQmvav 124,68,136\nQmvav 156,64,160\nQta 56,69,16640\nQmvav 156,68,168\n"
"buffer_load_dwordx4 v[144:147], v79, s[48:51], s31 offen offset:2048 sc1\nQmvav 188,64,192\nQmvav 188,68,200\nQta 58,69,17664\nQmvav 220,64,224\nQmvav 220,68,232\nbuffer_load_dwordx4 v[176:179], v79, s[48:51], s31 offen offset:3072 sc1\nQmvav 252,64,148\n"
"Qta 60,69,24832\nQmvav 252,68,212\nQmvav 100,64,180\nQmvav 100,68,244\nQta 62,69,25856\ns_mov_b32 s31, 266240\ns_waitcnt lgkmcnt(4)\nQmaav 224,72,180\n"
"buffer_load_dwordx4 v[208:211], v79, s[48:51], s31 offen offset:0 sc1\nQmaav 224,76,244\nQmaav 220,72,148\nQmaav 220,76,212\nQmaav 216,72,224\nQta 64,69,16896\nQmaav 216,76,232\nbuffer_load_dwordx4 v[240:243], v79, s[48:51], s31 offen offset:1024 sc1\n"
"Qmaav 212,72,192\nQmaav 212,76,200\nQta 66,69,17920\nQmaav 208,72,160\nQmaav 208,76,168\nbuffer_load_dwordx4 v[84:87], v79, s[48:51], s31 offen offset:2048 sc1\nQmaav 204,72,128\nQta 68,69,25088\n"
"Qmaav 204,76,136\nQmaav 200,72,96\nQmaav 200,76,104\nQta 70,69,26112\ns_mov_b32 s31, 278528\ns_waitcnt lgkmcnt(4)\nQmaav 228,56,96\nbuffer_load_dwordx4 v[88:91], v79, s[48:51], s31 offen offset:0 sc1\n"
"Qmaav 228,60,104\nQmaav 232,56,128\nQmaav 232,60,136\nQmaav 236,56,160\nQta 72,69,17152\nQmaav 236,60,168\nbuffer_load_dwordx4 v[120:123], v79, s[48:51], s31 offen offset:1024 sc1\nQmaav 240,56,192\n"
"Qmaav 240,60,200\nQta 74,69,18176\nQmaav 244,56,224\nQmaav 244,60,232\nbuffer_load_dwordx4 v[152:155], v79, s[48:51], s31 offen offset:2048 sc1\nQmaav 248,56,148\nQta 76,69,25344\nQmaav 248,60,212\n"
"Qmaav 252,56,180\nQmaav 252,60,244\nQta 78,69,26368\ns_waitcnt lgkmcnt(4)\nQmvav 172,64,180\nbuffer_load_dwordx4 v[184:187], v79, s[48:51], s31 offen offset:3072 sc1\nQmvav 172,68,244\nQmvav 140,64,148\n"
"Qmvav 140,68,212\nQmvav 108,64,224\nQta 56,69,32768\ns_mov_b32 s31, 282624\nQmvav 108,68,232\nbuffer_load_dwordx4 v[216:219], v79, s[48:51], s31 offen offset:0 sc1\nQmvav 228,64,192\nQmvav 228,68,200\n"
"Qta 58,69,33792\nQmvav 196,64,160\nQmvav 196,68,168\nbuffer_load_dwordx4 v[248:251], v79, s[48:51], s31 offen offset:1024 sc1\nQmvav 164,64,128\nQta 60,69,40960\nQmvav 164,68,136\nQmvav 132,64,96\n"
"Qmvav 132,68,104\nQta 62,69,41984\ns_waitcnt lgkmcnt(4)\nQmvav 204,72,96\nbuffer_load_dwordx4 v[116:119], v79, s[48:51], s31 offen offset:2048 sc1\nQmvav 204,76,104\nQmvav 236,72,128\nQmvav 236,76,136\n"
"Qmaav 96,72,160\nQta 64,69,33024\ns_add_u32 m0, s96, 32768\ns_mov_b32 s95, 331776\nQmaav 96,76,168\nbuffer_load_dwordx4 v63, s[52:55], s95 offen lds\nQmaav 100,72,192\nQmaav 100,76,200\n"
"Qta 66,69,34048\nQmaav 104,72,224\nQmaav 104,76,232\nQmaav 108,72,148\nQta 68,69,41216\ns_mov_b32 s95, 351232\nQmaav 108,76,212\nbuffer_load_dwordx4 v63, s[52:55], s95 offen offset:1024 lds\n"
"Qmaav 112,72,180\nQmaav 112,76,244\nQta 70,69,42240\ns_waitcnt vmcnt(16)\ns_waitcnt lgkmcnt(4)\nQmaaa 140,56,24\nQmaaa 140,60,52\ns_add_u32 m0, s96, 40960\n"
"s_mov_b32 s95, 372736\nQmaaa 136,56,20\nbuffer_load_dwordx4 v63, s[52:55], s95 offen lds\nQmaaa 136,60,48\nQmaaa 132,56,16\nQta 72,69,33280\nQmaaa 132,60,44\nQmaaa 128,56,12\n"
"Qmaaa 128,60,40\nQta 74,69,34304\ns_mov_b32 s95, 392192\nQmaaa 124,56,8\nbuffer_load_dwordx4 v63, s[52:55], s95 offen offset:1024 lds\nQmaaa 124,60,36\nQmaaa 120,56,4\nQta 76,69,41472\n"
"s_add_u32 s88, s71, 131072\nQmaaa 120,60,32\nbuffer_store_dwordx4 v[96:99], v79, s[56:59], s88 offen offset:0 sc1\nQmaaa 116,56,0\nQmaaa 116,60,28\nQta 78,69,42496\ns_waitcnt lgkmcnt(4)\nQmaaa 144,64,0\n"
"Qmaaa 144,68,28\nQmaaa 148,64,4\nQmaaa 148,68,32\nQmaaa 152,64,8\nQta 56,69,33536\ns_add_u32 m0, s96, 49152\ns_mov_b32 s95, 413696\nQmaaa 152,68,36\n"
"buffer_load_dwordx4 v61, s[52:55], s95 offen lds\nQmaaa 156,64,12\nQmaaa 156,68,40\nQta 58,69,34560\nQmaaa 160,64,16\nQmaaa 160,68,44\nQmaaa 164,64,20\nQta 60,69,41728\n"
"s_mov_b32 s95, 433152\nQmaaa 164,68,48\nbuffer_load_dwordx4 v61, s[52:55], s95 offen offset:1024 lds\nQmaaa 168,64,24\nQmaaa 168,68,52\nQta 62,69,42752\ns_waitcnt lgkmcnt(4)\nQmvaa 24,72,24\n"
"Qmvaa 24,76,52\nQmvaa 20,72,20\nbuffer_store_dwordx4 v[128:131], v79, s[56:59], s88 offen offset:1024 sc1\nQmvaa 20,76,48\nQmvaa 16,72,16\nQta 64,69,49152\nQmvaa 16,76,44\nQmvaa 12,72,12\n"
"Qmvaa 12,76,40\nQta 66,69,49664\nQmvaa 8,72,8\nQmvaa 8,76,36\nQmvaa 4,72,4\nQta 68,69,50176\nv_add_u32 v75, 1, v75\nQmvaa 4,76,32\n"
"s_waitcnt vmcnt(36)\nds_write_b32 v73, v75\ns_add_u32 m0, s96, 57344\n.Lpg12:\ns_cbranch_execz .Lpg16\ns_mov_b32 s95, 454656\nQmvaa 0,72,0\nbuffer_load_dwordx4 v61, s[52:55], s95 offen lds\n"
"Qmvaa 0,76,28\nQta 70,69,50688\ns_waitcnt lgkmcnt(5)\nQmvaa 28,56,0\nQmvaa 28,60,28\nQmvaa 32,56,4\ns_mov_b32 s95, 474112\nQmvaa 32,60,32\n"
"buffer_load_dwordx4 v61, s[52:55], s95 offen offset:1024 lds\nQmvaa 36,56,8\nQta 72,69,49408\nQmvaa 36,60,36\nds_read_b32 v66, v74\nQmvaa 40,56,12\nQmvaa 40,60,40\nQta 74,69,49920\n"
"Qmvaa 44,56,16\nbuffer_store_dwordx4 v[160:163], v79, s[56:59], s88 offen offset:2048 sc1\nv_add_u32 v67, 2, v65\nQmvaa 44,60,44\ns_waitcnt lgkmcnt(1)\nv_cmp_gt_u32 vcc, v75, v66\nQmvaa 48,56,20\ns_cbranch_vccnz .Lso460\n"
".Lro460:\nQmvaa 48,60,48\nQta 76,69,50432\nQmvaa 52,56,24\nbuffer_store_dword v67, v77, s[24:27], 0 offen sc1\nQmvaa 52,60,52\ns_waitcnt lgkmcnt(4)\nQmaaa 196,64,24\n"
"Qta 78,69,50944\nQmaaa 196,68,52\ns_add_u32 m0, s96, 132096\ns_mov_b32 s95, 413696\nQmaaa 192,64,20\nbuffer_load_dwordx4 v62, s[52:55], s95 offen lds\nQmaaa 192,68,48\nQmaaa 188,64,16\n"
"Qta 56,69,57344\nQmaaa 188,68,44\nQmaaa 184,64,12\nQmaaa 184,68,40\nQta 58,69,58368\ns_mov_b32 s95, 453632\nQmaaa 180,64,8\nbuffer_load_dwordx4 v62, s[52:55], s95 offen offset:1024 lds\n"
"Qmaaa 180,68,36\nQmaaa 176,64,4\nQta 60,68,0\nQmaaa 176,68,32\nQmaaa 172,64,0\nQmaaa 172,68,28\nQta 62,68,1024\ns_waitcnt lgkmcnt(4)\n"
"Qmvaa 92,72,0\nbuffer_store_dwordx4 v[192:195], v79, s[56:59], s88 offen offset:3072 sc1\nQmvaa 92,76,28\nQmvaa 124,72,4\nQmvaa 124,76,32\nQmvaa 156,72,8\nQta 64,69,57600\nQmvaa 156,76,36\n"
"Qmvaa 188,72,12\nQmvaa 188,76,40\nQta 66,69,58624\ns_add_u32 m0, s96, 140288\ns_mov_b32 s95, 413696\nQmvaa 220,72,16\nbuffer_load_dwordx4 v63, s[52:55], s95 offen lds\nQmvaa 220,76,44\n"
"Qmvaa 252,72,20\nQta 68,68,256\nQmvaa 252,76,48\nQmvaa 100,72,24\nQmvaa 100,76,52\nQta 70,68,1280\ns_mov_b32 s95, 433152\ns_waitcnt lgkmcnt(4)\n"
"Qmaaa 224,56,24\nbuffer_load_dwordx4 v63, s[52:55], s95 offen offset:1024 lds\nQmaaa 224,60,52\nQmaaa 220,56,20\nQmaaa 220,60,48\nQmaaa 216,56,16\nQta 72,69,57856\nQmaaa 216,60,44\n"
"s_add_u32 s88, s71, 135168\nQmaaa 212,56,12\nbuffer_store_dwordx4 v[224:227], v79, s[56:59], s88 offen offset:0 sc1\nQmaaa 212,60,40\nQta 74,69,58880\nQmaaa 208,56,8\nQmaaa 208,60,36\nQmaaa 204,56,4\n"
"Qta 76,68,512\nQmaaa 204,60,32\nQmaaa 200,56,0\nQmaaa 200,60,28\nQta 78,68,1536\ns_add_u32 m0, s96, 148480\ns_mov_b32 s95, 454656\ns_waitcnt lgkmcnt(4)\n"
"Qmaaa 228,64,0\nbuffer_load_dwordx4 v63, s[52:55], s95 offen lds\nQmaaa 228,68,28\nQmaaa 232,64,4\nQmaaa 232,68,32\nQmaaa 236,64,8\nQta 56,69,58112\ns_mov_b32 s95, 474112\n"
"Qmaaa 236,68,36\nbuffer_load_dwordx4 v63, s[52:55], s95 offen offset:1024 lds\nQmaaa 240,64,12\nQmaaa 240,68,40\nQta 58,69,59136\nQmaaa 244,64,16\nQmaaa 244,68,44\nQmaaa 248,64,20\n"
"Qta 60,68,768\nQmaaa 248,68,48\nQmaaa 252,64,24\nbuffer_store_dwordx4 v[148:151], v79, s[56:59], s88 offen offset:1024 sc1\nQmaaa 252,68,52\nQta 62,68,1792\ns_waitcnt lgkmcnt(4)\nQmvaa 172,72,24\n"
"Qmvaa 172,76,52\ns_mov_b32 s40, s22\ns_and_b32 s41, s23, 0xffff\nQmvaa 140,72,20\ns_mov_b32 s42, 571200\ns_lshl_b32 s66, s19, 17\nQmvaa 140,76,48\ns_add_u32 s69, s98, 4194304\n"
"s_add_u32 s98, s66, s69\nQmvaa 108,72,16\nQta 64,68,8192\ns_xor_b32 s66, s66, 524288\nQmvaa 108,76,44\ns_add_u32 m0, s96, 65536\ns_mov_b32 s95, 659456\nQmvaa 228,72,12\n"
"buffer_load_dwordx4 v61, s[52:55], s95 offen lds\nQmvaa 228,76,40\nQta 66,68,9216\ns_add_u32 s64, s66, s69\nQmvaa 196,72,8\ns_mul_i32 s66, s92, 33792\ns_add_u32 s66, s66, %4\nQmvaa 196,76,36\n"
"s_add_u32 s66, s66, 98304\ns_mul_i32 s68, s93, 96\nQmvaa 164,72,4\nQta 68,68,16384\ns_mov_b32 s95, 678912\nQmvaa 164,76,32\nbuffer_load_dwordx4 v61, s[52:55], s95 offen offset:1024 lds\nQmvaa 132,72,0\n"
"s_sub_u32 s68, s66, s68\ns_add_u32 s68, s68, 96\nQmvaa 132,76,28\nQta 70,68,17408\nv_bfe_u32 v148, v60, 4, 4\ns_waitcnt lgkmcnt(4)\nQmvaa 204,56,0\ns_mov_b32 s31, 393216\n"
"v_mul_u32_u24 v148, 208, v148\nQmvaa 204,60,28\nbuffer_load_dwordx4 a[0:3], v79, s[48:51], s31 offen offset:0 sc1\nQmvaa 236,56,4\nv_lshrrev_b32 v149, 8, v60\nv_lshl_add_u32 v148, v149, 3, v148\nQmvaa 236,60,32\nv_add_u32 v56, s68, v148\n"
"s_lshl_b32 s68, s93, 3\nQmaaa 96,56,8\nQta 72,68,8448\nv_lshrrev_b32 v148, 7, v60\nQmaaa 96,60,36\nbuffer_store_dwordx4 v[180:183], v79, s[56:59], s88 offen offset:2048 sc1\nQmaaa 100,56,12\nv_add_u32 v148, s68, v148\n"
"v_and_b32 v149, 127, v60\nQmaaa 100,60,40\nQta 74,68,9472\nv_and_b32 v150, 64, v60\nQmaaa 104,56,16\nv_lshrrev_b32 v150, 1, v150\nv_mul_u32_u24 v57, 208, v148\nQmaaa 104,60,44\n"
"v_add3_u32 v57, v57, v149, s66\nv_add_u32 v57, v57, v150\nQmaaa 108,56,20\nQta 76,68,16640\ns_and_b32 s65, s19, 1\nQmaaa 108,60,48\ns_and_b32 s68, s19, 4\ns_lshl_b32 s68, s68, 2\n"
"Qmaaa 112,56,24\ns_lshl_b32 s69, s65, 3\ns_add_u32 s68, s68, s69\nQmaaa 112,60,52\nQta 78,68,17664\ns_lshr_b32 s68, 0xc0004080, s68\ns_waitcnt vmcnt(25)\ns_waitcnt lgkmcnt(4)\n"
"Qmaav 140,64,84\nbuffer_load_dwordx4 a[4:7], v79, s[48:51], s31 offen offset:1024 sc1\nQmaav 140,68,116\ns_and_b32 s68, s68, 255\ns_mul_i32 s69, s92, 286720\nQmaav 136,64,240\ns_add_u32 s68, s68, s69\nv_mul_u32_u24 v64, 2560, v148\n"
"Qmaav 136,68,248\nv_add3_u32 v64, v64, v149, s68\ns_lshl_b32 s68, s93, 4\nQmaav 132,64,208\nQta 56,68,8704\nv_lshrrev_b32 v148, 6, v60\nQmaav 132,68,216\nbuffer_load_dwordx4 a[8:11], v79, s[48:51], s31 offen offset:2048 sc1\n"
"Qmaav 128,64,176\nv_add_u32 v148, s68, v148\nv_and_b32 v149, 63, v60\nQmaav 128,68,184\nQta 58,68,9728\nv_and_b32 v150, 32, v60\nQmaav 124,64,144\nv_lshlrev_b32 v150, 1, v150\n"
"v_mul_u32_u24 v71, 208, v148\nQmaav 124,68,152\nbuffer_load_dwordx4 a[12:15], v79, s[48:51], s31 offen offset:3072 sc1\nQmaav 120,64,112\nQta 60,68,16896\nv_add3_u32 v71, v71, v149, s66\nQmaav 120,68,120\nv_add_u32 v71, v71, v150\n"
"s_xor_b32 s65, s65, 1\nQmaav 116,64,80\ns_lshl_b32 s65, s65, 8\ns_add_u32 s68, s65, s69\nQmaav 116,68,88\nQta 62,68,17920\ns_mov_b32 s31, 397312\ns_waitcnt lgkmcnt(4)\n"
"Qmaav 144,72,80\nbuffer_load_dwordx4 a[16:19], v79, s[48:51], s31 offen offset:0 sc1\nQmaav 144,76,88\nv_mul_u32_u24 v72, 2560, v148\nv_add3_u32 v72, v72, v149, s68\nQmaav 148,72,112\nQmaav 148,76,120\nQmaav 152,72,144\n"
"Qta 64,68,8960\nQmaav 152,76,152\nbuffer_load_dwordx4 a[20:23], v79, s[48:51], s31 offen offset:1024 sc1\nQmaav 156,72,176\nQmaav 156,76,184\nQta 66,68,9984\nQmaav 160,72,208\nQmaav 160,76,216\n"
"buffer_load_dwordx4 a[24:27], v79, s[48:51], s31 offen offset:2048 sc1\nQmaav 164,72,240\nQta 68,68,17152\nQmaav 164,76,248\nQmaav 168,72,84\nQmaav 168,76,116\nQta 70,68,18176\ns_mov_b32 s31, 409600\n"
"s_waitcnt lgkmcnt(4)\nQmvav 24,56,84\nbuffer_load_dwordx4 a[28:31], v79, s[48:51], s31 offen offset:0 sc1\nQmvav 24,60,116\nQmvav 20,56,240\nQmvav 20,60,248\nQmvav 16,56,208\nQta 72,68,24576\n"
"Qmvav 16,60,216\nbuffer_load_dwordx4 a[32:35], v79, s[48:51], s31 offen offset:1024 sc1\nQmvav 12,56,176\nQmvav 12,60,184\nQta 74,68,25088\nQmvav 8,56,144\nQmvav 8,60,152\nbuffer_load_dwordx4 a[36:39], v79, s[48:51], s31 offen offset:2048 sc1\n"
"Qmvav 4,56,112\nQta 76,68,25600\nv_add_u32 v75, 1, v75\nQmvav 4,60,120\ns_waitcnt vmcnt(20)\nds_write_b32 v73, v75\nQmvav 0,56,80\nQmvav 0,60,88\n"
"Qta 78,68,26112\ns_waitcnt lgkmcnt(5)\nQmvav 28,64,80\nbuffer_load_dwordx4 a[40:43], v79, s[48:51], s31 offen offset:3072 sc1\nQmvav 28,68,88\nQmvav 32,64,112\nQmvav 32,68,120\nQmvav 36,64,144\n"
"Qta 56,68,24832\nQmvav 36,68,152\nds_read_b32 v66, v74\ns_mov_b32 s31, 413696\nQmvav 40,64,176\nbuffer_load_dwordx4 a[44:47], v79, s[48:51], s31 offen offset:0 sc1\nQmvav 40,68,184\nQta 58,68,25344\n"
"Qmvav 44,64,208\nQmvav 44,68,216\ns_waitcnt lgkmcnt(1)\nv_cmp_gt_u32 vcc, v75, v66\nQmvav 48,64,240\ns_cbranch_vccnz .Lso600\n.Lro600:\nQmvav 48,68,248\n"
"Qta 60,68,25856\nQmvav 52,64,84\nbuffer_load_dwordx4 a[48:51], v79, s[48:51], s31 offen offset:1024 sc1\nQmvav 52,68,116\ns_waitcnt lgkmcnt(4)\nQmaav 196,72,84\nQta 62,68,26368\nQmaav 196,76,116\n"
"Qmaav 192,72,240\nbuffer_load_dwordx4 a[52:55], v79, s[48:51], s31 offen offset:2048 sc1\nQmaav 192,76,248\nQmaav 188,72,208\nQta 64,68,32768\nQmaav 188,76,216\nQmaav 184,72,176\nQmaav 184,76,184\n"
"Qta 66,68,33792\ns_add_u32 m0, s96, 73728\ns_mov_b32 s95, 700416\nQmaav 180,72,144\nbuffer_load_dwordx4 v61, s[52:55], s95 offen lds\nQmaav 180,76,152\nQmaav 176,72,112\nQta 68,68,40960\n"
"Qmaav 176,76,120\nQmaav 172,72,80\nQmaav 172,76,88\nQta 70,68,41984\ns_mov_b32 s95, 719872\ns_waitcnt lgkmcnt(4)\nQmvav 92,56,80\nbuffer_load_dwordx4 v61, s[52:55], s95 offen offset:1024 lds\n"
"Qmvav 92,60,88\nQmvav 124,56,112\nQmvav 124,60,120\nQmvav 156,56,144\nQta 72,68,33024\ns_add_u32 m0, s96, 81920\ns_mov_b32 s95, 659456\nQmvav 156,60,152\n"
"buffer_load_dwordx4 v62, s[52:55], s95 offen lds\nQmvav 188,56,176\nQmvav 188,60,184\nQta 74,68,34048\nQmvav 220,56,208\nQmvav 220,60,216\nQmvav 252,56,240\nQta 76,68,41216\n"
"s_mov_b32 s95, 699392\nQmvav 252,60,248\nbuffer_load_dwordx4 v62, s[52:55], s95 offen offset:1024 lds\nQmvav 100,56,84\nQmvav 100,60,116\nQta 78,68,42240\ns_waitcnt lgkmcnt(4)\nQmaav 224,64,84\n"
"Qmaav 224,68,116\ns_add_u32 m0, s96, 90112\ns_mov_b32 s95, 659456\nQmaav 220,64,240\nbuffer_load_dwordx4 v63, s[52:55], s95 offen lds\nQmaav 220,68,248\nQmaav 216,64,208\nQta 56,68,33280\n"
"Qmaav 216,68,216\nQmaav 212,64,176\nQmaav 212,68,184\nQta 58,68,34304\ns_mov_b32 s95, 678912\nQmaav 208,64,144\nbuffer_load_dwordx4 v63, s[52:55], s95 offen offset:1024 lds\nQmaav 208,68,152\n"
"Qmaav 204,64,112\nQta 60,68,41472\nQmaav 204,68,120\nQmaav 200,64,80\nQmaav 200,68,88\nQta 62,68,42496\ns_waitcnt lgkmcnt(4)\nQmaav 228,72,80\n"
"Qmaav 228,76,88\ns_add_u32 s88, s71, 147456\nQmaav 232,72,112\nbuffer_store_dwordx4 v[104:107], v79, s[56:59], s88 offen offset:0 sc1\nQmaav 232,76,120\nQmaav 236,72,144\nQta 64,68,33536\nQmaav 236,76,152\n"
"Qmaav 240,72,176\nQmaav 240,76,184\nQta 66,68,34560\nQmaav 244,72,208\nQmaav 244,76,216\nQmaav 248,72,240\nQta 68,68,41728\ns_add_u32 m0, s96, 98304\n"
"s_mov_b32 s95, 700416\nQmaav 248,76,248\nbuffer_load_dwordx4 v63, s[52:55], s95 offen lds\nQmaav 252,72,84\nQmaav 252,76,116\nQta 70,68,42752\ns_waitcnt lgkmcnt(4)\nQmvav 172,56,84\n"
"buffer_store_dwordx4 v[136:139], v79, s[56:59], s88 offen offset:1024 sc1\nQmvav 172,60,116\nQmvav 140,56,240\nQmvav 140,60,248\nQmvav 108,56,208\nQta 72,68,49152\nQmvav 108,60,216\nQmvav 228,56,176\n"
"Qmvav 228,60,184\nQta 74,68,50176\ns_mov_b32 s95, 719872\nQmvav 196,56,144\nbuffer_load_dwordx4 v63, s[52:55], s95 offen offset:1024 lds\nQmvav 196,60,152\nQmvav 164,56,112\nQta 76,68,57344\n"
"Qmvav 164,60,120\nQmvav 132,56,80\nQmvav 132,60,88\nQta 78,68,58368\ns_add_u32 m0, s96, 106496\ns_mov_b32 s95, 741376\ns_waitcnt lgkmcnt(4)\nQmvav 204,64,80\n"
"buffer_load_dwordx4 v61, s[52:55], s95 offen lds\nQmvav 204,68,88\nQmvav 236,64,112\nQmvav 236,68,120\nQmaav 96,64,144\nQta 56,68,49408\ns_mov_b32 s95, 760832\nQmaav 96,68,152\n"
"buffer_load_dwordx4 v61, s[52:55], s95 offen offset:1024 lds\nQmaav 100,64,176\nQmaav 100,68,184\nQta 58,68,50432\nQmaav 104,64,208\ns_mov_b32 s31, 524288\nQmaav 104,68,216\nbuffer_load_dwordx4 v[80:83], v79, s[48:51], s31 offen offset:0 sc1\n"
"Qmaav 108,64,240\nQta 60,68,57600\nQmaav 108,68,248\nQmaav 112,64,84\nQmaav 112,68,116\nQta 62,68,58624\ns_waitcnt vmcnt(13)\ns_waitcnt lgkmcnt(4)\n"
"Qmaaa 140,72,24\nbuffer_load_dwordx4 v[112:115], v79, s[48:51], s31 offen offset:1024 sc1\nQmaaa 140,76,52\nQmaaa 136,72,20\nQmaaa 136,76,48\nQmaaa 132,72,16\nQta 64,68,49664\nQmaaa 132,76,44\n"
"buffer_load_dwordx4 v[144:147], v79, s[48:51], s31 offen offset:2048 sc1\nQmaaa 128,72,12\nQmaaa 128,76,40\nQta 66,68,50688\nQmaaa 124,72,8\nQmaaa 124,76,36\nbuffer_load_dwordx4 v[176:179], v79, s[48:51], s31 offen offset:3072 sc1\nQmaaa 120,72,4\n"
"Qta 68,68,57856\nQmaaa 120,76,32\nQmaaa 116,72,0\nQmaaa 116,76,28\nQta 70,68,58880\ns_mov_b32 s31, 528384\ns_waitcnt lgkmcnt(4)\nQmaaa 144,56,0\n"
"buffer_load_dwordx4 v[208:211], v79, s[48:51], s31 offen offset:0 sc1\nQmaaa 144,60,28\nQmaaa 148,56,4\nQmaaa 148,60,32\nQmaaa 152,56,8\nQta 72,68,49920\nQmaaa 152,60,36\nbuffer_load_dwordx4 v[240:243], v79, s[48:51], s31 offen offset:1024 sc1\n"
"Qmaaa 156,56,12\nQmaaa 156,60,40\nQta 74,68,50944\nQmaaa 160,56,16\nQmaaa 160,60,44\nbuffer_load_dwordx4 v[84:87], v79, s[48:51], s31 offen offset:2048 sc1\nQmaaa 164,56,20\nQta 76,68,58112\n"
"Qmaaa 164,60,48\nQmaaa 168,56,24\nQmaaa 168,60,52\nQta 78,68,59136\ns_mov_b32 s31, 540672\ns_waitcnt lgkmcnt(4)\nQmvaa 24,64,24\nbuffer_load_dwordx4 v[88:91], v79, s[48:51], s31 offen offset:0 sc1\n"
"Qmvaa 24,68,52\nQmvaa 20,64,20\nQmvaa 20,68,48\nQmvaa 16,64,16\nQta 56,70,1024\nQmvaa 16,68,44\nbuffer_load_dwordx4 v[120:123], v79, s[48:51], s31 offen offset:1024 sc1\nQmvaa 12,64,12\n"
"Qmvaa 12,68,40\nQta 58,70,1536\nQmvaa 8,64,8\nQmvaa 8,68,36\nbuffer_load_dwordx4 v[152:155], v79, s[48:51], s31 offen offset:2048 sc1\nQmvaa 4,64,4\nQta 60,70,2048\nv_add_u32 v75, 1, v75\n"
"Qmvaa 4,68,32\ns_waitcnt vmcnt(18)\nds_write_b32 v73, v75\nQmvaa 0,64,0\nQmvaa 0,68,28\nQta 62,70,2560\n.Lpg13:\ns_cbranch_execz .Lpg17\n"
"s_waitcnt lgkmcnt(5)\nQmvaa 28,72,0\nbuffer_load_dwordx4 v[184:187], v79, s[48:51], s31 offen offset:3072 sc1\nQmvaa 28,76,28\nQmvaa 32,72,4\nQmvaa 32,76,32\nQmvaa 36,72,8\nQta 64,70,1280\n"
"Qmvaa 36,76,36\nds_read_b32 v66, v74\ns_mov_b32 s31, 544768\nQmvaa 40,72,12\nbuffer_load_dwordx4 v[216:219], v79, s[48:51], s31 offen offset:0 sc1\nQmvaa 40,76,40\nQta 66,70,1792\nQmvaa 44,72,16\n"
"Qmvaa 44,76,44\ns_waitcnt lgkmcnt(1)\nv_cmp_gt_u32 vcc, v75, v66\nQmvaa 48,72,20\ns_cbranch_vccnz .Lso740\n.Lro740:\nQmvaa 48,76,48\nQta 68,70,2304\n"
"Qmvaa 52,72,24\nbuffer_load_dwordx4 v[248:251], v79, s[48:51], s31 offen offset:1024 sc1\nQmvaa 52,76,52\ns_waitcnt lgkmcnt(4)\nQmaaa 196,56,24\nQta 70,70,2816\nQmaaa 196,60,52\nQmaaa 192,56,20\n"
"buffer_load_dwordx4 v[116:119], v79, s[48:51], s31 offen offset:2048 sc1\nQmaaa 192,60,48\nQmaaa 188,56,16\nQta 72,70,9216\nQmaaa 188,60,44\nQmaaa 184,56,12\nQmaaa 184,60,40\nQta 74,70,10240\n"
"s_add_u32 m0, s96, 114688\ns_mov_b32 s95, 782336\nQmaaa 180,56,8\nbuffer_load_dwordx4 v61, s[52:55], s95 offen lds\nQmaaa 180,60,36\nQmaaa 176,56,4\nQta 76,70,17408\nQmaaa 176,60,32\n"
"Qmaaa 172,56,0\nQmaaa 172,60,28\nQta 78,70,18432\ns_mov_b32 s95, 801792\ns_waitcnt lgkmcnt(4)\nQmvaa 92,64,0\nbuffer_load_dwordx4 v61, s[52:55], s95 offen offset:1024 lds\nQmvaa 92,68,28\n"
"Qmvaa 124,64,4\nQmvaa 124,68,32\nQmvaa 156,64,8\nQta 56,70,9472\ns_add_u32 m0, s96, 122880\ns_mov_b32 s95, 741376\nQmvaa 156,68,36\nbuffer_load_dwordx4 v62, s[52:55], s95 offen lds\n"
"Qmvaa 188,64,12\nQmvaa 188,68,40\nQta 58,70,10496\nQmvaa 220,64,16\nQmvaa 220,68,44\nQmvaa 252,64,20\nQta 60,70,17664\ns_mov_b32 s95, 781312\n"
"Qmvaa 252,68,48\nbuffer_load_dwordx4 v62, s[52:55], s95 offen offset:1024 lds\nQmvaa 100,64,24\nQmvaa 100,68,52\nQta 62,70,18688\ns_waitcnt lgkmcnt(4)\nQmaaa 224,72,24\nQmaaa 224,76,52\n"
"s_add_u32 m0, s96, 0\ns_mov_b32 s95, 741376\nQmaaa 220,72,20\nbuffer_load_dwordx4 v63, s[52:55], s95 offen lds\nQmaaa 220,76,48\nQmaaa 216,72,16\nQta 64,70,9728\nQmaaa 216,76,44\n"
"Qmaaa 212,72,12\nQmaaa 212,76,40\nQta 66,70,10752\ns_mov_b32 s95, 760832\nQmaaa 208,72,8\nbuffer_load_dwordx4 v63, s[52:55], s95 offen offset:1024 lds\nQmaaa 208,76,36\nQmaaa 204,72,4\n"
"Qta 68,70,17920\nQmaaa 204,76,32\nQmaaa 200,72,0\nQmaaa 200,76,28\nQta 70,70,18944\ns_waitcnt lgkmcnt(4)\nQmaaa 228,56,0\nQmaaa 228,60,28\n"
"Qmaaa 232,56,4\nbuffer_store_dwordx4 v[168:171], v79, s[56:59], s88 offen offset:2048 sc1\nQmaaa 232,60,32\nQmaaa 236,56,8\nQta 72,70,9984\nQmaaa 236,60,36\nQmaaa 240,56,12\nQmaaa 240,60,40\n"
"Qta 74,70,11008\nQmaaa 244,56,16\nQmaaa 244,60,44\nQmaaa 248,56,20\nQta 76,70,18176\ns_add_u32 m0, s96, 8192\ns_mov_b32 s95, 782336\nQmaaa 248,60,48\n"
"buffer_load_dwordx4 v63, s[52:55], s95 offen lds\nQmaaa 252,56,24\nQmaaa 252,60,52\nQta 78,70,19200\ns_waitcnt lgkmcnt(4)\nQmvaa 172,64,24\nbuffer_store_dwordx4 v[200:203], v79, s[56:59], s88 offen offset:3072 sc1\nQmvaa 172,68,52\n"
"Qmvaa 140,64,20\nQmvaa 140,68,48\nQmvaa 108,64,16\nQta 56,69,0\nQmvaa 108,68,44\nQmvaa 228,64,12\nQmvaa 228,68,40\nQta 58,69,1024\n"
"s_mov_b32 s95, 801792\nQmvaa 196,64,8\nbuffer_load_dwordx4 v63, s[52:55], s95 offen offset:1024 lds\nQmvaa 196,68,36\nQmvaa 164,64,4\nQta 60,69,8192\nQmvaa 164,68,32\nQmvaa 132,64,0\n"
"Qmvaa 132,68,28\nQta 62,69,9216\ns_add_u32 m0, s96, 16384\ns_mov_b32 s95, 987136\ns_waitcnt lgkmcnt(4)\nQmvaa 204,72,0\nbuffer_load_dwordx4 v61, s[52:55], s95 offen lds\nQmvaa 204,76,28\n"
"Qmvaa 236,72,4\nQmvaa 236,76,32\nQmaaa 96,72,8\nQta 64,69,256\ns_mov_b32 s95, 1006592\nQmaaa 96,76,36\nbuffer_load_dwordx4 v61, s[52:55], s95 offen offset:1024 lds\nQmaaa 100,72,12\n"
"Qmaaa 100,76,40\nQta 66,69,1280\nQmaaa 104,72,16\ns_mov_b32 s31, 655360\nQmaaa 104,76,44\nbuffer_load_dwordx4 a[0:3], v79, s[48:51], s31 offen offset:0 sc1\nQmaaa 108,72,20\nQta 68,69,8448\n"
"Qmaaa 108,76,48\nQmaaa 112,72,24\nQmaaa 112,76,52\nQta 70,69,9472\ns_waitcnt vmcnt(13)\ns_waitcnt lgkmcnt(4)\nQmaav 140,56,84\nbuffer_load_dwordx4 a[4:7], v79, s[48:51], s31 offen offset:1024 sc1\n"
"Qmaav 140,60,116\nQmaav 136,56,240\nQmaav 136,60,248\nQmaav 132,56,208\nQta 72,69,512\nQmaav 132,60,216\nbuffer_load_dwordx4 a[8:11], v79, s[48:51], s31 offen offset:2048 sc1\nQmaav 128,56,176\n"
"Qmaav 128,60,184\nQta 74,69,1536\nQmaav 124,56,144\nQmaav 124,60,152\nbuffer_load_dwordx4 a[12:15], v79, s[48:51], s31 offen offset:3072 sc1\nQmaav 120,56,112\nQta 76,69,8704\nQmaav 120,60,120\n"
"Qmaav 116,56,80\nQmaav 116,60,88\nQta 78,69,9728\ns_mov_b32 s31, 659456\ns_waitcnt lgkmcnt(4)\nQmaav 144,64,80\nbuffer_load_dwordx4 a[16:19], v79, s[48:51], s31 offen offset:0 sc1\nQmaav 144,68,88\n"
"Qmaav 148,64,112\nQmaav 148,68,120\nQmaav 152,64,144\nQta 56,69,768\nQmaav 152,68,152\nbuffer_load_dwordx4 a[20:23], v79, s[48:51], s31 offen offset:1024 sc1\nQmaav 156,64,176\nQmaav 156,68,184\n"
"Qta 58,69,1792\nQmaav 160,64,208\nQmaav 160,68,216\nbuffer_load_dwordx4 a[24:27], v79, s[48:51], s31 offen offset:2048 sc1\nQmaav 164,64,240\nQta 60,69,8960\nQmaav 164,68,248\nQmaav 168,64,84\n"
"Qmaav 168,68,116\nQta 62,69,9984\ns_mov_b32 s31, 671744\ns_waitcnt lgkmcnt(4)\nQmvav 24,72,84\nbuffer_load_dwordx4 a[28:31], v79, s[48:51], s31 offen offset:0 sc1\nQmvav 24,76,116\nQmvav 20,72,240\n"
"Qmvav 20,76,248\nQmvav 16,72,208\nQta 64,69,16384\nQmvav 16,76,216\nbuffer_load_dwordx4 a[32:35], v79, s[48:51], s31 offen offset:1024 sc1\nQmvav 12,72,176\nQmvav 12,76,184\nQta 66,69,16896\n"
"Qmvav 8,72,144\nQmvav 8,76,152\nbuffer_load_dwordx4 a[36:39], v79, s[48:51], s31 offen offset:2048 sc1\nQmvav 4,72,112\nQta 68,69,17408\nv_add_u32 v75, 1, v75\nQmvav 4,76,120\ns_waitcnt vmcnt(18)\n"
"ds_write_b32 v73, v75\nQmvav 0,72,80\nQmvav 0,76,88\nQta 70,69,17920\ns_waitcnt lgkmcnt(5)\nQmvav 28,56,80\nbuffer_load_dwordx4 a[40:43], v79, s[48:51], s31 offen offset:3072 sc1\nQmvav 28,60,88\n"
"Qmvav 32,56,112\nQmvav 32,60,120\nQmvav 36,56,144\nQta 72,69,16640\nQmvav 36,60,152\nds_read_b32 v66, v74\ns_mov_b32 s31, 675840\nQmvav 40,56,176\n"
"buffer_load_dwordx4 a[44:47], v79, s[48:51], s31 offen offset:0 sc1\nQmvav 40,60,184\nQta 74,69,17152\nQmvav 44,56,208\nQmvav 44,60,216\ns_waitcnt lgkmcnt(1)\nv_cmp_gt_u32 vcc, v75, v66\nQmvav 48,56,240\n"
"s_cbranch_vccnz .Lso880\n.Lro880:\nQmvav 48,60,248\nQta 76,69,17664\nQmvav 52,56,84\nbuffer_load_dwordx4 a[48:51], v79, s[48:51], s31 offen offset:1024 sc1\nQmvav 52,60,116\ns_waitcnt lgkmcnt(4)\n"
"Qmaav 196,64,84\nQta 78,69,18176\nQmaav 196,68,116\nQmaav 192,64,240\nbuffer_load_dwordx4 a[52:55], v79, s[48:51], s31 offen offset:2048 sc1\nQmaav 192,68,248\nQmaav 188,64,208\nQta 56,69,24576\n"
"Qmaav 188,68,216\nQmaav 184,64,176\nQmaav 184,68,184\nQta 58,69,25600\ns_add_u32 m0, s96, 24576\ns_mov_b32 s95, 1028096\nQmaav 180,64,144\nbuffer_load_dwordx4 v61, s[52:55], s95 offen lds\n"
"Qmaav 180,68,152\nQmaav 176,64,112\nQta 60,69,32768\nQmaav 176,68,120\nQmaav 172,64,80\nQmaav 172,68,88\nQta 62,69,33792\ns_mov_b32 s95, 1047552\n"
"s_waitcnt lgkmcnt(4)\nQmvav 92,72,80\nbuffer_load_dwordx4 v61, s[52:55], s95 offen offset:1024 lds\nQmvav 92,76,88\nQmvav 124,72,112\nQmvav 124,76,120\nQmvav 156,72,144\nQta 64,69,24832\n"
"s_add_u32 m0, s96, 32768\ns_mov_b32 s95, 987136\nQmvav 156,76,152\nbuffer_load_dwordx4 v62, s[52:55], s95 offen lds\nQmvav 188,72,176\nQmvav 188,76,184\nQta 66,69,25856\nQmvav 220,72,208\n"
"Qmvav 220,76,216\nQmvav 252,72,240\nQta 68,69,33024\ns_mov_b32 s95, 1027072\nQmvav 252,76,248\nbuffer_load_dwordx4 v62, s[52:55], s95 offen offset:1024 lds\nQmvav 100,72,84\nQmvav 100,76,116\n"
"Qta 70,69,34048\ns_waitcnt lgkmcnt(4)\nQmaav 224,56,84\nQmaav 224,60,116\ns_add_u32 m0, s96, 40960\ns_mov_b32 s95, 987136\nQmaav 220,56,240\nbuffer_load_dwordx4 v63, s[52:55], s95 offen lds\n"
"Qmaav 220,60,248\nQmaav 216,56,208\nQta 72,69,25088\nQmaav 216,60,216\nQmaav 212,56,176\nQmaav 212,60,184\nQta 74,69,26112\ns_mov_b32 s95, 1006592\n"
"Qmaav 208,56,144\nbuffer_load_dwordx4 v63, s[52:55], s95 offen offset:1024 lds\nQmaav 208,60,152\nQmaav 204,56,112\nQta 76,69,33280\nQmaav 204,60,120\nQmaav 200,56,80\nQmaav 200,60,88\n"
"Qta 78,69,34304\ns_waitcnt lgkmcnt(4)\nQmaav 228,64,80\nQmaav 228,68,88\ns_add_u32 s88, s71, 151552\nQmaav 232,64,112\nbuffer_store_dwordx4 v[232:235], v79, s[56:59], s88 offen offset:0 sc1\nQmaav 232,68,120\n"
"Qmaav 236,64,144\nQta 56,69,25344\nQmaav 236,68,152\nQmaav 240,64,176\nQmaav 240,68,184\nQta 58,69,26368\nQmaav 244,64,208\nQmaav 244,68,216\n"
"Qmaav 248,64,240\nQta 60,69,33536\ns_add_u32 m0, s96, 49152\ns_mov_b32 s95, 1028096\nQmaav 248,68,248\nbuffer_load_dwordx4 v63, s[52:55], s95 offen lds\nQmaav 252,64,84\nQmaav 252,68,116\n"
"Qta 62,69,34560\ns_waitcnt lgkmcnt(4)\nQmvav 172,72,84\nbuffer_store_dwordx4 v[212:215], v79, s[56:59], s88 offen offset:1024 sc1\nQmvav 172,76,116\nQmvav 140,72,240\nQmvav 140,76,248\nQmvav 108,72,208\n"
"Qta 64,69,40960\nQmvav 108,76,216\nQmvav 228,72,176\nQmvav 228,76,184\nQta 66,69,41984\ns_mov_b32 s95, 1047552\nQmvav 196,72,144\nbuffer_load_dwordx4 v63, s[52:55], s95 offen offset:1024 lds\n"
"Qmvav 196,76,152\nQmvav 164,72,112\nQta 68,69,49152\nQmvav 164,76,120\nQmvav 132,72,80\nQmvav 132,76,88\nQta 70,69,50176\ns_add_u32 m0, s96, 57344\n"
"s_mov_b32 s95, 1069056\ns_waitcnt lgkmcnt(4)\nQmvav 204,56,80\nbuffer_load_dwordx4 v61, s[52:55], s95 offen lds\nQmvav 204,60,88\nQmvav 236,56,112\nQmvav 236,60,120\nQmaav 96,56,144\n"
"Qta 72,69,41216\ns_mov_b32 s95, 1088512\nQmaav 96,60,152\nbuffer_load_dwordx4 v61, s[52:55], s95 offen offset:1024 lds\nQmaav 100,56,176\nQmaav 100,60,184\nQta 74,69,42240\nQmaav 104,56,208\n"
"s_mov_b32 s31, 786432\nQmaav 104,60,216\nbuffer_load_dwordx4 v[80:83], v79, s[48:51], s31 offen offset:0 sc1\nQmaav 108,56,240\nQta 76,69,49408\nQmaav 108,60,248\nQmaav 112,56,84\nv_accvgpr_read_b32 v96, a96\n"
"v_accvgpr_read_b32 v97, a97\nQmaav 112,60,116\nQta 78,69,50432\nv_accvgpr_read_b32 v98, a98\ns_waitcnt vmcnt(13)\ns_waitcnt lgkmcnt(4)\nQmaaa 140,64,24\nbuffer_load_dwordx4 v[112:115], v79, s[48:51], s31 offen offset:1024 sc1\n"
"Qmaaa 140,68,52\nv_accvgpr_read_b32 v99, a99\nv_accvgpr_read_b32 v128, a100\nQmaaa 136,64,20\nv_accvgpr_read_b32 v129, a101\nv_accvgpr_read_b32 v130, a102\nQmaaa 136,68,48\nv_accvgpr_read_b32 v131, a103\n"
"v_accvgpr_read_b32 v160, a104\nQmaaa 132,64,16\nQta 56,69,41472\nv_accvgpr_read_b32 v161, a105\nQmaaa 132,68,44\nbuffer_load_dwordx4 v[144:147], v79, s[48:51], s31 offen offset:2048 sc1\nQmaaa 128,64,12\nv_accvgpr_read_b32 v162, a106\n"
"v_accvgpr_read_b32 v163, a107\nQmaaa 128,68,40\nQta 58,69,42496\nv_accvgpr_read_b32 v192, a108\nQmaaa 124,64,8\nv_accvgpr_read_b32 v193, a109\nv_accvgpr_read_b32 v194, a110\nQmaaa 124,68,36\n"
"buffer_load_dwordx4 v[176:179], v79, s[48:51], s31 offen offset:3072 sc1\nQmaaa 120,64,4\nQta 60,69,49664\nv_accvgpr_read_b32 v195, a111\nQmaaa 120,68,32\nv_accvgpr_read_b32 v224, a112\nv_accvgpr_read_b32 v225, a113\nQmaaa 116,64,0\n"
"v_accvgpr_read_b32 v226, a114\nv_accvgpr_read_b32 v227, a115\nQmaaa 116,68,28\nQta 62,69,50688\ns_mov_b32 s31, 790528\ns_waitcnt lgkmcnt(4)\nQmaaa 144,72,0\nbuffer_load_dwordx4 v[208:211], v79, s[48:51], s31 offen offset:0 sc1\n"
"Qmaaa 144,76,28\nQmaaa 148,72,4\nQmaaa 148,76,32\nQmaaa 152,72,8\nQta 64,69,41728\nQmaaa 152,76,36\nbuffer_load_dwordx4 v[240:243], v79, s[48:51], s31 offen offset:1024 sc1\nQmaaa 156,72,12\n"
"Qmaaa 156,76,40\nQta 66,69,42752\nQmaaa 160,72,16\nQmaaa 160,76,44\nbuffer_load_dwordx4 v[84:87], v79, s[48:51], s31 offen offset:2048 sc1\nQmaaa 164,72,20\nQta 68,69,49920\nQmaaa 164,76,48\n"
"Qmaaa 168,72,24\nQmaaa 168,76,52\nQta 70,69,50944\ns_mov_b32 s31, 802816\ns_waitcnt lgkmcnt(4)\nQmvaa 24,56,24\nbuffer_load_dwordx4 v[88:91], v79, s[48:51], s31 offen offset:0 sc1\nQmvaa 24,60,52\n"
"Qmvaa 20,56,20\nQmvaa 20,60,48\nQmvaa 16,56,16\nQta 72,69,57344\nQmvaa 16,60,44\nbuffer_load_dwordx4 v[120:123], v79, s[48:51], s31 offen offset:1024 sc1\nQmvaa 12,56,12\nQmvaa 12,60,40\n"
"Qta 74,69,57856\nQmvaa 8,56,8\nQmvaa 8,60,36\nbuffer_load_dwordx4 v[152:155], v79, s[48:51], s31 offen offset:2048 sc1\nQmvaa 4,56,4\nQta 76,69,58368\nv_add_u32 v75, 1, v75\nQmvaa 4,60,32\n"
"s_waitcnt vmcnt(18)\nds_write_b32 v73, v75\nQmvaa 0,56,0\nQmvaa 0,60,28\nQta 78,69,58880\ns_waitcnt lgkmcnt(5)\nQmvaa 28,64,0\nbuffer_load_dwordx4 v[184:187], v79, s[48:51], s31 offen offset:3072 sc1\n"
"Qmvaa 28,68,28\nQmvaa 32,64,4\nQmvaa 32,68,32\nQmvaa 36,64,8\nQta 56,69,57600\nQmvaa 36,68,36\nds_read_b32 v66, v74\ns_mov_b32 s31, 806912\n"
"Qmvaa 40,64,12\nbuffer_load_dwordx4 v[216:219], v79, s[48:51], s31 offen offset:0 sc1\nQmvaa 40,68,40\nQta 58,69,58112\nQmvaa 44,64,16\nQmvaa 44,68,44\ns_waitcnt lgkmcnt(1)\nv_cmp_gt_u32 vcc, v75, v66\n"
"Qmvaa 48,64,20\ns_cbranch_vccnz .Lso1020\n.Lro1020:\nQmvaa 48,68,48\nQta 60,69,58624\n.Lpg14:\ns_cbranch_execz .Lpg18\nQmvaa 52,64,24\n"
"buffer_load_dwordx4 v[248:251], v79, s[48:51], s31 offen offset:1024 sc1\nQmvaa 52,68,52\ns_waitcnt lgkmcnt(4)\nQmaaa 196,72,24\nQta 62,69,59136\nQmaaa 196,76,52\nQmaaa 192,72,20\nbuffer_load_dwordx4 v[116:119], v79, s[48:51], s31 offen offset:2048 sc1\n"
"Qmaaa 192,76,48\nQmaaa 188,72,16\nQta 64,68,0\nQmaaa 188,76,44\nQmaaa 184,72,12\nQmaaa 184,76,40\nQta 66,68,1024\ns_add_u32 m0, s96, 132096\n"
"s_mov_b32 s95, 1110016\nQmaaa 180,72,8\nbuffer_load_dwordx4 v61, s[52:55], s95 offen lds\nQmaaa 180,76,36\nQmaaa 176,72,4\nQta 68,68,8192\nQmaaa 176,76,32\nQmaaa 172,72,0\n"
"Qmaaa 172,76,28\nQta 70,68,9216\ns_mov_b32 s95, 1129472\ns_waitcnt lgkmcnt(4)\nQmvaa 92,56,0\nbuffer_load_dwordx4 v61, s[52:55], s95 offen offset:1024 lds\nQmvaa 92,60,28\nQmvaa 124,56,4\n"
"Qmvaa 124,60,32\nQmvaa 156,56,8\nQta 72,68,256\ns_add_u32 m0, s96, 140288\ns_mov_b32 s95, 1069056\nQmvaa 156,60,36\nbuffer_load_dwordx4 v62, s[52:55], s95 offen lds\nQmvaa 188,56,12\n"
"Qmvaa 188,60,40\nQta 74,68,1280\nQmvaa 220,56,16\nQmvaa 220,60,44\nQmvaa 252,56,20\nQta 76,68,8448\ns_mov_b32 s95, 1108992\nQmvaa 252,60,48\n"
"buffer_load_dwordx4 v62, s[52:55], s95 offen offset:1024 lds\nQmvaa 100,56,24\nQmvaa 100,60,52\nQta 78,68,9472\ns_waitcnt lgkmcnt(4)\nQmaaa 224,64,24\nQmaaa 224,68,52\ns_add_u32 m0, s96, 148480\n"
"s_mov_b32 s95, 1069056\nQmaaa 220,64,20\nbuffer_load_dwordx4 v63, s[52:55], s95 offen lds\nQmaaa 220,68,48\nQmaaa 216,64,16\nQta 56,68,512\nQmaaa 216,68,44\nQmaaa 212,64,12\n"
"Qmaaa 212,68,40\nQta 58,68,1536\ns_mov_b32 s95, 1088512\nQmaaa 208,64,8\nbuffer_load_dwordx4 v63, s[52:55], s95 offen offset:1024 lds\nQmaaa 208,68,36\nQmaaa 204,64,4\nQta 60,68,8704\n"
"Qmaaa 204,68,32\nQmaaa 200,64,0\nQmaaa 200,68,28\nQta 62,68,9728\ns_waitcnt lgkmcnt(4)\nQmaaa 228,72,0\nQmaaa 228,76,28\nQmaaa 232,72,4\n"
"buffer_store_dwordx4 v[244:247], v79, s[56:59], s88 offen offset:2048 sc1\nQmaaa 232,76,32\nQmaaa 236,72,8\nQta 64,68,768\nQmaaa 236,76,36\nQmaaa 240,72,12\nQmaaa 240,76,40\nQta 66,68,1792\n"
"Qmaaa 244,72,16\nQmaaa 244,76,44\nQmaaa 248,72,20\nQta 68,68,8960\ns_add_u32 m0, s96, 65536\ns_mov_b32 s95, 1110016\nQmaaa 248,76,48\nbuffer_load_dwordx4 v63, s[52:55], s95 offen lds\n"
"Qmaaa 252,72,24\nQmaaa 252,76,52\nQta 70,68,9984\ns_waitcnt lgkmcnt(4)\nQmvaa 172,56,24\nQmvaa 172,60,52\ns_mov_b32 s95, 1129472\nQmvaa 140,56,20\n"
"buffer_load_dwordx4 v63, s[52:55], s95 offen offset:1024 lds\nQmvaa 140,60,48\nQmvaa 108,56,16\nQta 72,68,16384\nQmvaa 108,60,44\nQmvaa 228,56,12\nQmvaa 228,60,40\nQta 74,68,17408\n"
"s_add_u32 m0, s96, 73728\ns_add_u32 s95, s67, 4096\nQmvaa 196,56,8\nbuffer_load_dwordx4 v61, s[52:55], s95 offen lds\nQmvaa 196,60,36\nQmvaa 164,56,4\nQta 76,68,24576\nQmvaa 164,60,32\n"
"Qmvaa 132,56,0\nQmvaa 132,60,28\nQta 78,68,25600\ns_add_u32 s95, s67, 23552\ns_waitcnt lgkmcnt(4)\nQmvaa 204,64,0\nbuffer_load_dwordx4 v61, s[52:55], s95 offen offset:1024 lds\nQmvaa 204,68,28\n"
"Qmvaa 236,64,4\nQmvaa 236,68,32\nQmvaa 96,64,8\nQta 56,68,16640\ns_mov_b32 s31, 917504\nQmvaa 96,68,36\nbuffer_load_dwordx4 a[0:3], v79, s[48:51], s31 offen offset:0 sc1\nQmvaa 128,64,12\n"
"Qmvaa 128,68,40\nQta 58,68,17664\nQmvaa 160,64,16\nQmvaa 160,68,44\nbuffer_load_dwordx4 a[4:7], v79, s[48:51], s31 offen offset:1024 sc1\nQmvaa 192,64,20\nQta 60,68,24832\nQmvaa 192,68,48\n"
"Qmvaa 224,64,24\nQmvaa 224,68,52\nQta 62,68,25856\ns_waitcnt vmcnt(13)\ns_waitcnt lgkmcnt(4)\nQmaav 140,72,84\nbuffer_load_dwordx4 a[8:11], v79, s[48:51], s31 offen offset:2048 sc1\nQmaav 140,76,116\n"
"Qmaav 136,72,240\nQmaav 136,76,248\nQmaav 132,72,208\nQta 64,68,16896\nQmaav 132,76,216\nbuffer_load_dwordx4 a[12:15], v79, s[48:51], s31 offen offset:3072 sc1\nQmaav 128,72,176\nQmaav 128,76,184\n"
"Qta 66,68,17920\nQmaav 124,72,144\ns_mov_b32 s31, 921600\nQmaav 124,76,152\nbuffer_load_dwordx4 a[16:19], v79, s[48:51], s31 offen offset:0 sc1\nQmaav 120,72,112\nQta 68,68,25088\nQmaav 120,76,120\n"
"Qmaav 116,72,80\nQmaav 116,76,88\nQta 70,68,26112\ns_waitcnt lgkmcnt(4)\nQmaav 144,56,80\nbuffer_load_dwordx4 a[20:23], v79, s[48:51], s31 offen offset:1024 sc1\nQmaav 144,60,88\nQmaav 148,56,112\n"
"buffer_load_dword v78, v76, s[24:27], 0 offen sc1\nQmaav 148,60,120\nQmaav 152,56,144\nQta 72,68,17152\nQmaav 152,60,152\nbuffer_load_dwordx4 a[24:27], v79, s[48:51], s31 offen offset:2048 sc1\nQmaav 156,56,176\nQmaav 156,60,184\n"
"Qta 74,68,18176\nQmaav 160,56,208\ns_mov_b32 s31, 933888\nQmaav 160,60,216\nbuffer_load_dwordx4 a[28:31], v79, s[48:51], s31 offen offset:0 sc1\nQmaav 164,56,240\nQta 76,68,25344\nQmaav 164,60,248\n"
"Qmaav 168,56,84\nQmaav 168,60,116\nQta 78,68,26368\ns_waitcnt lgkmcnt(4)\nQmvav 24,64,84\nbuffer_load_dwordx4 a[32:35], v79, s[48:51], s31 offen offset:1024 sc1\nQmvav 24,68,116\nQmvav 20,64,240\n"
"Qmvav 20,68,248\nQmvav 16,64,208\nQta 56,68,32768\nQmvav 16,68,216\nbuffer_load_dwordx4 a[36:39], v79, s[48:51], s31 offen offset:2048 sc1\nQmvav 12,64,176\nQmvav 12,68,184\nQta 58,68,33280\n"
"Qmvav 8,64,144\nQmvav 8,68,152\nbuffer_load_dwordx4 a[40:43], v79, s[48:51], s31 offen offset:3072 sc1\nQmvav 4,64,112\nQta 60,68,33792\nv_add_u32 v75, 1, v75\nQmvav 4,68,120\ns_waitcnt vmcnt(19)\n"
"ds_write_b32 v73, v75\nQmvav 0,64,80\nQmvav 0,68,88\nQta 62,68,34304\ns_mov_b32 s31, 937984\ns_waitcnt lgkmcnt(5)\nQmvav 28,72,80\nbuffer_load_dwordx4 a[44:47], v79, s[48:51], s31 offen offset:0 sc1\n"
"Qmvav 28,76,88\nQmvav 32,72,112\nQmvav 32,76,120\nQmvav 36,72,144\nQta 64,68,33024\nQmvav 36,76,152\nds_read_b32 v66, v74\nQmvav 40,72,176\n"
"buffer_load_dwordx4 a[48:51], v79, s[48:51], s31 offen offset:1024 sc1\nQmvav 40,76,184\nQta 66,68,33536\nQmvav 44,72,208\nQmvav 44,76,216\ns_waitcnt lgkmcnt(1)\nv_cmp_gt_u32 vcc, v75, v66\nQmvav 48,72,240\n"
"s_cbranch_vccnz .Lso1160\n.Lro1160:\nQmvav 48,76,248\nQta 68,68,34048\nQmvav 52,72,84\nbuffer_load_dwordx4 a[52:55], v79, s[48:51], s31 offen offset:2048 sc1\nQmvav 52,76,116\ns_waitcnt lgkmcnt(4)\n"
"Qmaav 196,56,84\nQta 70,68,34560\nQmaav 196,60,116\nQmaav 192,56,240\ns_add_u32 m0, s96, 81920\ns_add_u32 s95, s67, 45056\nQmaav 192,60,248\nbuffer_load_dwordx4 v61, s[52:55], s95 offen lds\n"
"Qmaav 188,56,208\nQta 72,68,40960\nQmaav 188,60,216\nQmaav 184,56,176\nQmaav 184,60,184\nQta 74,68,41984\ns_add_u32 s95, s67, 64512\nQmaav 180,56,144\n"
"buffer_load_dwordx4 v61, s[52:55], s95 offen offset:1024 lds\nQmaav 180,60,152\nQmaav 176,56,112\nQta 76,68,49152\nQmaav 176,60,120\nQmaav 172,56,80\nQmaav 172,60,88\nQta 78,68,50176\n"
"s_add_u32 m0, s96, 90112\ns_add_u32 s95, s67, 4096\ns_waitcnt lgkmcnt(4)\nQmvav 92,64,80\nbuffer_load_dwordx4 v62, s[52:55], s95 offen lds\nQmvav 92,68,88\nQmvav 124,64,112\nQmvav 124,68,120\n"
"Qmvav 156,64,144\nQta 56,68,41216\ns_add_u32 s95, s67, 44032\nQmvav 156,68,152\nbuffer_load_dwordx4 v62, s[52:55], s95 offen offset:1024 lds\nQmvav 188,64,176\ns_waitcnt vmcnt(12)\nv_sub_u32 v66, v78, v65\n"
"v_cmp_gt_u32 vcc, 2, v66\ns_and_b64 vcc, vcc, s[28:29]\ns_cbranch_scc1 .Lf2_w\n.Lf2_r:\nQmvav 188,68,184\nQta 58,68,42240\nQmvav 220,64,208\ns_add_u32 s31, s74, 0\n"
"Qmvav 220,68,216\nbuffer_load_dwordx4 a[80:83], v79, s[48:51], s31 offen offset:0 sc1\nQmvav 252,64,240\nQta 60,68,49408\nQmvav 252,68,248\nQmvav 100,64,84\nQmvav 100,68,116\nQta 62,68,50432\n"
"s_waitcnt lgkmcnt(4)\nQmaav 224,72,84\nbuffer_load_dwordx4 a[84:87], v79, s[48:51], s31 offen offset:1024 sc1\nQmaav 224,76,116\nQmaav 220,72,240\nQmaav 220,76,248\nQmaav 216,72,208\nQta 64,68,41472\n"
"Qmaav 216,76,216\nbuffer_load_dwordx4 a[88:91], v79, s[48:51], s31 offen offset:2048 sc1\nQmaav 212,72,176\nQmaav 212,76,184\nQta 66,68,42496\nQmaav 208,72,144\nQmaav 208,76,152\nbuffer_load_dwordx4 a[92:95], v79, s[48:51], s31 offen offset:3072 sc1\n"
"Qmaav 204,72,112\nQta 68,68,49664\nQmaav 204,76,120\nQmaav 200,72,80\nQmaav 200,76,88\nQta 70,68,50688\ns_add_u32 s31, s74, 4096\ns_waitcnt lgkmcnt(4)\n"
"Qmaav 228,56,80\nbuffer_load_dwordx4 a[96:99], v79, s[48:51], s31 offen offset:0 sc1\nQmaav 228,60,88\nQmaav 232,56,112\nQmaav 232,60,120\nQmaav 236,56,144\nQta 72,68,41728\nQmaav 236,60,152\n"
"buffer_load_dwordx4 a[100:103], v79, s[48:51], s31 offen offset:1024 sc1\nQmaav 240,56,176\nQmaav 240,60,184\nQta 74,68,42752\nQmaav 244,56,208\nQmaav 244,60,216\nbuffer_load_dwordx4 a[104:107], v79, s[48:51], s31 offen offset:2048 sc1\nQmaav 248,56,240\n"
"Qta 76,68,49920\nQmaav 248,60,248\nQmaav 252,56,84\nQmaav 252,60,116\nQta 78,68,50944\ns_add_u32 s31, s74, 16384\ns_waitcnt lgkmcnt(4)\nQmvav 172,64,84\n"
"buffer_load_dwordx4 a[108:111], v79, s[48:51], s31 offen offset:0 sc1\nQmvav 172,68,116\nQmvav 140,64,240\nQmvav 140,68,248\nQmvav 108,64,208\nQta 56,68,57344\nQmvav 108,68,216\nbuffer_load_dwordx4 a[112:115], v79, s[48:51], s31 offen offset:1024 sc1\n"
"Qmvav 228,64,176\nQmvav 228,68,184\nQta 58,68,58368\nQmvav 196,64,144\nQmvav 196,68,152\nbuffer_load_dwordx4 v[168:171], v79, s[48:51], s31 offen offset:2048 sc1\nQmvav 164,64,112\nQta 60,70,1024\n"
"Qmvav 164,68,120\nQmvav 132,64,80\nQmvav 132,68,88\nQta 62,70,2048\ns_waitcnt lgkmcnt(4)\nQmvav 204,72,80\nbuffer_load_dwordx4 v[200:203], v79, s[48:51], s31 offen offset:3072 sc1\nQmvav 204,76,88\n"
"Qmvav 236,72,112\nQmvav 236,76,120\nQmvav 96,72,144\nQta 64,68,57600\ns_add_u32 s31, s74, 20480\nQmvav 96,76,152\nbuffer_load_dwordx4 v[232:235], v79, s[48:51], s31 offen offset:0 sc1\nQmvav 128,72,176\n"
"Qmvav 128,76,184\nQta 66,68,58624\nQmvav 160,72,208\nQmvav 160,76,216\nbuffer_load_dwordx4 v[212:215], v79, s[48:51], s31 offen offset:1024 sc1\nQmvav 192,72,240\nQta 68,70,1280\nQmvav 192,76,248\n"
"Qmvav 224,72,84\nQmvav 224,76,116\nQta 70,70,2304\ns_waitcnt vmcnt(17)\ns_waitcnt lgkmcnt(4)\nQmaaa 140,56,24\nbuffer_load_dwordx4 v[244:247], v79, s[48:51], s31 offen offset:2048 sc1\nQmaaa 140,60,52\n"
"Qmaaa 136,56,20\nQmaaa 136,60,48\nQmaaa 132,56,16\nQta 72,68,57856\ns_add_u32 m0, s96, 98304\ns_add_u32 s95, s67, 4096\nQmaaa 132,60,44\nbuffer_load_dwordx4 v63, s[52:55], s95 offen lds\n"
"Qmaaa 128,56,12\nQmaaa 128,60,40\nQta 74,68,58880\nQmaaa 124,56,8\nQmaaa 124,60,36\nQmaaa 120,56,4\nQta 76,70,1536\ns_add_u32 s95, s67, 23552\n"
"Qmaaa 120,60,32\nbuffer_load_dwordx4 v63, s[52:55], s95 offen offset:1024 lds\nQmaaa 116,56,0\nQmaaa 116,60,28\nQta 78,70,2560\ns_waitcnt lgkmcnt(4)\nQmaaa 144,64,0\nQmaaa 144,68,28\n"
"s_add_u32 m0, s96, 106496\ns_add_u32 s95, s67, 45056\nQmaaa 148,64,4\nbuffer_load_dwordx4 v63, s[52:55], s95 offen lds\nQmaaa 148,68,32\nQmaaa 152,64,8\nQta 56,68,58112\nQmaaa 152,68,36\n"
"Qmaaa 156,64,12\nQmaaa 156,68,40\nQta 58,68,59136\ns_add_u32 s95, s67, 64512\nQmaaa 160,64,16\nbuffer_load_dwordx4 v63, s[52:55], s95 offen offset:1024 lds\nQmaaa 160,68,44\nQmaaa 164,64,20\n"
"Qta 60,70,1792\nQmaaa 164,68,48\nQmaaa 168,64,24\nQmaaa 168,68,52\nQta 62,70,2816\ns_add_u32 m0, s96, 114688\ns_add_u32 s95, s72, 4096\ns_waitcnt lgkmcnt(4)\n"
"Qmvaa 24,72,24\nbuffer_load_dwordx4 v61, s[52:55], s95 offen lds\nQmvaa 24,76,52\nQmvaa 20,72,20\nQmvaa 20,76,48\nQmvaa 16,72,16\nQta 64,70,9216\ns_add_u32 s95, s72, 23552\n"
"Qmvaa 16,76,44\nbuffer_load_dwordx4 v61, s[52:55], s95 offen offset:1024 lds\nQmvaa 12,72,12\nQmvaa 12,76,40\nQta 66,70,9728\nQmvaa 8,72,8\nQmvaa 8,76,36\nQmvaa 4,72,4\n"
"Qta 68,70,10240\nv_add_u32 v75, 1, v75\nQmvaa 4,76,32\ns_waitcnt vmcnt(20)\nds_write_b32 v73, v75\ns_add_u32 m0, s96, 122880\ns_add_u32 s95, s72, 45056\nQmvaa 0,72,0\n"
"buffer_load_dwordx4 v61, s[52:55], s95 offen lds\nQmvaa 0,76,28\nQta 70,70,10752\ns_waitcnt lgkmcnt(5)\nQmvaa 28,56,0\nQmvaa 28,60,28\nQmvaa 32,56,4\ns_add_u32 s95, s72, 64512\n"
"Qmvaa 32,60,32\nbuffer_load_dwordx4 v61, s[52:55], s95 offen offset:1024 lds\nQmvaa 36,56,8\nQta 72,70,9472\nQmvaa 36,60,36\nds_read_b32 v66, v74\nQmvaa 40,56,12\nQmvaa 40,60,40\n"
"Qta 74,70,9984\ns_add_u32 m0, s96, 0\ns_add_u32 s95, s72, 4096\nQmvaa 44,56,16\nbuffer_load_dwordx4 v62, s[52:55], s95 offen lds\nv_add_u32 v67, 3, v65\nQmvaa 44,60,44\ns_waitcnt lgkmcnt(1)\n"
"v_cmp_gt_u32 vcc, v75, v66\nQmvaa 48,56,20\ns_cbranch_vccnz .Lso1300\n.Lro1300:\nQmvaa 48,60,48\nQta 76,70,10496\nQmvaa 52,56,24\nbuffer_store_dword v67, v77, s[24:27], 0 offen sc1\n"
"s_add_u32 s95, s72, 44032\nQmvaa 52,60,52\nbuffer_load_dwordx4 v62, s[52:55], s95 offen offset:1024 lds\ns_waitcnt lgkmcnt(4)\nQmaaa 196,64,24\nQta 78,70,11008\nQmaaa 196,68,52\nQmaaa 192,64,20\n"
"Qmaaa 192,68,48\nQmaaa 188,64,16\nQta 56,70,17408\ns_add_u32 m0, s96, 8192\ns_add_u32 s95, s72, 4096\nQmaaa 188,68,44\nbuffer_load_dwordx4 v63, s[52:55], s95 offen lds\nQmaaa 184,64,12\n"
"Qmaaa 184,68,40\nQta 58,70,18432\nQmaaa 180,64,8\nQmaaa 180,68,36\nQmaaa 176,64,4\nQta 60,69,0\ns_add_u32 s95, s72, 23552\nQmaaa 176,68,32\n"
"buffer_load_dwordx4 v63, s[52:55], s95 offen offset:1024 lds\nQmaaa 172,64,0\nQmaaa 172,68,28\nQta 62,69,1024\ns_waitcnt lgkmcnt(4)\nQmvaa 92,72,0\ns_add_u32 s31, s75, 0\n.Lpg15:\n"
"s_cbranch_execz .Lpg19\nQmvaa 92,76,28\nbuffer_load_dwordx4 v[80:83], v79, s[48:51], s31 offen offset:0 sc1\nQmvaa 124,72,4\nQmvaa 124,76,32\nQmvaa 156,72,8\nQta 64,70,17664\nQmvaa 156,76,36\n"
"buffer_load_dwordx4 v[112:115], v79, s[48:51], s31 offen offset:1024 sc1\nQmvaa 188,72,12\nQmvaa 188,76,40\nQta 66,70,18688\nQmvaa 220,72,16\nQmvaa 220,76,44\nbuffer_load_dwordx4 v[144:147], v79, s[48:51], s31 offen offset:2048 sc1\nQmvaa 252,72,20\n"
"Qta 68,69,256\nQmvaa 252,76,48\nQmvaa 100,72,24\nQmvaa 100,76,52\nQta 70,69,1280\ns_waitcnt lgkmcnt(4)\nQmaaa 224,56,24\nbuffer_load_dwordx4 v[176:179], v79, s[48:51], s31 offen offset:3072 sc1\n"
"Qmaaa 224,60,52\nQmaaa 220,56,20\nQmaaa 220,60,48\nQmaaa 216,56,16\nQta 72,70,17920\ns_add_u32 s31, s75, 4096\nQmaaa 216,60,44\nbuffer_load_dwordx4 v[208:211], v79, s[48:51], s31 offen offset:0 sc1\n"
"Qmaaa 212,56,12\nQmaaa 212,60,40\nQta 74,70,18944\nQmaaa 208,56,8\nQmaaa 208,60,36\nbuffer_load_dwordx4 v[240:243], v79, s[48:51], s31 offen offset:1024 sc1\nQmaaa 204,56,4\nQta 76,69,512\n"
"Qmaaa 204,60,32\nQmaaa 200,56,0\nQmaaa 200,60,28\nQta 78,69,1536\ns_waitcnt lgkmcnt(4)\nQmaaa 228,64,0\nbuffer_load_dwordx4 v[84:87], v79, s[48:51], s31 offen offset:2048 sc1\nQmaaa 228,68,28\n"
"Qmaaa 232,64,4\nQmaaa 232,68,32\nQmaaa 236,64,8\nQta 56,70,18176\ns_add_u32 s31, s75, 16384\nQmaaa 236,68,36\nbuffer_load_dwordx4 v[88:91], v79, s[48:51], s31 offen offset:0 sc1\nQmaaa 240,64,12\n"
"Qmaaa 240,68,40\nQta 58,70,19200\nQmaaa 244,64,16\nQmaaa 244,68,44\nbuffer_load_dwordx4 v[120:123], v79, s[48:51], s31 offen offset:1024 sc1\nQmaaa 248,64,20\nQta 60,69,768\nQmaaa 248,68,48\n"
"Qmaaa 252,64,24\nQmaaa 252,68,52\nQta 62,69,1792\ns_waitcnt lgkmcnt(4)\nQmvaa 172,72,24\nbuffer_load_dwordx4 v[152:155], v79, s[48:51], s31 offen offset:2048 sc1\nQmvaa 172,76,52\nQmvaa 140,72,20\n"
"Qmvaa 140,76,48\nQmvaa 108,72,16\nQta 64,69,8192\nQmvaa 108,76,44\nbuffer_load_dwordx4 v[184:187], v79, s[48:51], s31 offen offset:3072 sc1\nQmvaa 228,72,12\nQmvaa 228,76,40\nQta 66,69,9216\n"
"Qmvaa 196,72,8\ns_add_u32 s31, s75, 20480\nQmvaa 196,76,36\nbuffer_load_dwordx4 v[216:219], v79, s[48:51], s31 offen offset:0 sc1\nQmvaa 164,72,4\nQta 68,69,16384\nQmvaa 164,76,32\nQmvaa 132,72,0\n"
"Qmvaa 132,76,28\nQta 70,69,17408\ns_waitcnt lgkmcnt(4)\nQmvaa 204,56,0\nbuffer_load_dwordx4 v[248:251], v79, s[48:51], s31 offen offset:1024 sc1\nQmvaa 204,60,28\nQmvaa 236,56,4\nQmvaa 236,60,32\n"
"Qmvaa 96,56,8\nQta 72,69,8448\nQmvaa 96,60,36\nbuffer_load_dwordx4 v[116:119], v79, s[48:51], s31 offen offset:2048 sc1\nQmvaa 128,56,12\nQmvaa 128,60,40\nQta 74,69,9472\nQmvaa 160,56,16\n"
"Qmvaa 160,60,44\nQmvaa 192,56,20\nQta 76,69,16640\ns_add_u32 m0, s96, 16384\ns_add_u32 s95, s72, 45056\nQmvaa 192,60,48\nbuffer_load_dwordx4 v63, s[52:55], s95 offen lds\nQmvaa 224,56,24\n"
"Qmvaa 224,60,52\nQta 78,69,17664\ns_waitcnt vmcnt(28)\ns_waitcnt lgkmcnt(4)\nQmaaa 140,64,104\nQmaav 140,68,244\ns_add_u32 s95, s72, 64512\nQmaaa 136,64,100\n"
"buffer_load_dwordx4 v63, s[52:55], s95 offen offset:1024 lds\nQmaav 136,68,212\nQmaaa 132,64,96\nQta 56,69,8704\nQmaav 132,68,232\nQmaaa 128,64,92\nQmaav 128,68,200\nQta 58,69,9728\n"
"s_add_u32 m0, s96, 24576\ns_add_u32 s95, s73, 4096\nQmaaa 124,64,88\nbuffer_load_dwordx4 v61, s[52:55], s95 offen lds\nQmaav 124,68,168\nQmaaa 120,64,84\nQta 60,69,16896\nQmaaa 120,68,112\n"
"Qmaaa 116,64,80\nQmaaa 116,68,108\nQta 62,69,17920\ns_add_u32 s95, s73, 23552\ns_waitcnt lgkmcnt(4)\nQmaaa 144,72,80\nbuffer_load_dwordx4 v61, s[52:55], s95 offen offset:1024 lds\nQmaaa 144,76,108\n"
"Qmaaa 148,72,84\nQmaaa 148,76,112\nQmaaa 152,72,88\nQta 64,69,8960\ns_add_u32 m0, s96, 32768\ns_add_u32 s95, s73, 45056\nQmaav 152,76,168\nbuffer_load_dwordx4 v61, s[52:55], s95 offen lds\n"
"Qmaaa 156,72,92\nQmaav 156,76,200\nQta 66,69,9984\nQmaaa 160,72,96\nQmaav 160,76,232\nQmaaa 164,72,100\nQta 68,69,17152\ns_add_u32 s95, s73, 64512\n"
"Qmaav 164,76,212\nbuffer_load_dwordx4 v61, s[52:55], s95 offen offset:1024 lds\nQmaaa 168,72,104\nQmaav 168,76,244\nQta 70,69,18176\ns_waitcnt lgkmcnt(4)\nQmvaa 24,56,104\nQmvav 24,60,244\n"
"s_add_u32 m0, s96, 40960\ns_add_u32 s95, s73, 4096\nQmvaa 20,56,100\nbuffer_load_dwordx4 v62, s[52:55], s95 offen lds\nQmvav 20,60,212\nQmvaa 16,56,96\nQta 72,69,24576\nQmvav 16,60,232\n"
"Qmvaa 12,56,92\nQmvav 12,60,200\nQta 74,69,25088\ns_add_u32 s95, s73, 44032\nQmvaa 8,56,88\nbuffer_load_dwordx4 v62, s[52:55], s95 offen offset:1024 lds\nQmvav 8,60,168\nQmvaa 4,56,84\n"
"Qta 76,69,25600\nv_add_u32 v75, 1, v75\nQmvaa 4,60,112\ns_waitcnt vmcnt(33)\nds_write_b32 v73, v75\nQmvaa 0,56,80\nQmvaa 0,60,108\nQta 78,69,26112\n"
"s_add_u32 m0, s96, 49152\ns_add_u32 s95, s73, 4096\ns_waitcnt lgkmcnt(5)\nQmvaa 28,64,80\nbuffer_load_dwordx4 v63, s[52:55], s95 offen lds\nQmvaa 28,68,108\nQmvaa 32,64,84\nQmvaa 32,68,112\n"
"Qmvaa 36,64,88\nQta 56,69,24832\nQmvav 36,68,168\nds_read_b32 v66, v74\ns_add_u32 s95, s73, 23552\nQmvaa 40,64,92\nbuffer_load_dwordx4 v63, s[52:55], s95 offen offset:1024 lds\nQmvav 40,68,200\n"
"Qta 58,69,25344\nQmvaa 44,64,96\nQmvav 44,68,232\ns_waitcnt lgkmcnt(1)\nv_cmp_gt_u32 vcc, v75, v66\nQmvaa 48,64,100\ns_cbranch_vccnz .Lso1440\n.Lro1440:\n"
"Qmvav 48,68,212\nQta 60,69,25856\nv_add_u32 v75, 1, v75\nQmvaa 52,64,104\ns_waitcnt vmcnt(29)\nds_write_b32 v73, v75\ns_add_u32 s31, s77, 0\nQmvav 52,68,244\n"
"buffer_load_dwordx4 a[0:3], v79, s[48:51], s31 offen offset:0 sc1\ns_waitcnt lgkmcnt(5)\nQmaaa 196,72,104\nQta 62,69,26368\nQmaav 196,76,244\nQmaaa 192,72,100\nQmaav 192,76,212\nbuffer_load_dwordx4 a[4:7], v79, s[48:51], s31 offen offset:1024 sc1\n"
"Qmaaa 188,72,96\nQta 64,69,32768\nQmaav 188,76,232\nbuffer_load_dword v78, v76, s[24:27], 0 offen sc1\nQmaaa 184,72,92\nds_read_b32 v66, v74\nQmaav 184,76,200\nQta 66,69,33792\n"
"Qmaaa 180,72,88\nbuffer_load_dwordx4 a[8:11], v79, s[48:51], s31 offen offset:2048 sc1\nQmaav 180,76,168\nQmaaa 176,72,84\ns_waitcnt lgkmcnt(1)\nv_cmp_gt_u32 vcc, v75, v66\nQmaaa 176,76,112\ns_cbranch_vccnz .Lso1455\n"
".Lro1455:\nQmaaa 172,72,80\nQta 68,69,40960\nQmaaa 172,76,108\nbuffer_load_dwordx4 a[12:15], v79, s[48:51], s31 offen offset:3072 sc1\ns_waitcnt lgkmcnt(4)\nQmvaa 92,56,80\nQmvaa 92,60,108\n"
"Qta 70,69,41984\nQmvaa 124,56,84\ns_add_u32 s31, s77, 4096\nQmvaa 124,60,112\nbuffer_load_dwordx4 a[16:19], v79, s[48:51], s31 offen offset:0 sc1\nQmvaa 156,56,88\nQta 72,69,33024\nQmvav 156,60,168\n"
"Qmvaa 188,56,92\nQmvav 188,60,200\nQta 74,69,34048\nQmvaa 220,56,96\nbuffer_load_dwordx4 a[20:23], v79, s[48:51], s31 offen offset:1024 sc1\nQmvav 220,60,232\nQmvaa 252,56,100\nQta 76,69,41216\n"
"Qmvav 252,60,212\nQmvaa 100,56,104\nbuffer_load_dwordx4 a[24:27], v79, s[48:51], s31 offen offset:2048 sc1\nQmvav 100,60,244\nQta 78,69,42240\ns_waitcnt lgkmcnt(4)\nQmaaa 224,64,104\nQmaav 224,68,244\n"
"s_add_u32 s31, s77, 16384\nQmaaa 220,64,100\nbuffer_load_dwordx4 a[28:31], v79, s[48:51], s31 offen offset:0 sc1\nQmaav 220,68,212\nQmaaa 216,64,96\nQta 56,69,33280\nQmaav 216,68,232\nQmaaa 212,64,92\n"
"buffer_load_dwordx4 a[32:35], v79, s[48:51], s31 offen offset:1024 sc1\nQmaav 212,68,200\nQta 58,69,34304\nQmaaa 208,64,88\nQmaav 208,68,168\nQmaaa 204,64,84\nQta 60,69,41472\nQmaaa 204,68,112\n"
"buffer_load_dwordx4 a[36:39], v79, s[48:51], s31 offen offset:2048 sc1\nQmaaa 200,64,80\nQmaaa 200,68,108\nQta 62,69,42496\ns_waitcnt lgkmcnt(4)\nQmaaa 228,72,80\nQmaaa 228,76,108\nbuffer_load_dwordx4 a[40:43], v79, s[48:51], s31 offen offset:3072 sc1\n"
"Qmaaa 232,72,84\nQmaaa 232,76,112\nQmaaa 236,72,88\nQta 64,69,33536\ns_add_u32 s31, s77, 20480\nQmaav 236,76,168\nbuffer_load_dwordx4 a[44:47], v79, s[48:51], s31 offen offset:0 sc1\nQmaaa 240,72,92\n"
"Qmaav 240,76,200\nQta 66,69,34560\nQmaaa 244,72,96\nQmaav 244,76,232\nbuffer_load_dwordx4 a[48:51], v79, s[48:51], s31 offen offset:1024 sc1\nQmaaa 248,72,100\nQta 68,69,41728\nQmaav 248,76,212\n"
"Qmaaa 252,72,104\nQmaav 252,76,244\nQta 70,69,42752\ns_waitcnt lgkmcnt(4)\nQmvaa 172,56,104\nbuffer_load_dwordx4 a[52:55], v79, s[48:51], s31 offen offset:2048 sc1\nQmvav 172,60,244\nQmvaa 140,56,100\n"
"Qmvav 140,60,212\nQmvaa 108,56,96\nQta 72,69,49152\ns_add_u32 m0, s96, 57344\ns_add_u32 s95, s73, 45056\nQmvav 108,60,232\nbuffer_load_dwordx4 v63, s[52:55], s95 offen lds\nQmvaa 228,56,92\n"
"Qmvav 228,60,200\nQta 74,69,50176\nQmvaa 196,56,88\nQmvav 196,60,168\ns_waitcnt vmcnt(13)\nv_sub_u32 v66, v78, v65\nv_cmp_gt_u32 vcc, 3, v66\ns_and_b64 vcc, vcc, s[28:29]\n"
"s_cbranch_scc1 .Lf3_w\n.Lf3_r:\nQmvaa 164,56,84\nQta 76,69,57344\ns_add_u32 s95, s73, 64512\nQmvaa 164,60,112\nbuffer_load_dwordx4 v63, s[52:55], s95 offen offset:1024 lds\nv_add_u32 v75, 1, v75\n"
"Qmvaa 132,56,80\ns_waitcnt vmcnt(41)\nds_write_b32 v73, v75\nQmvaa 132,60,108\nQta 78,69,58368\ns_waitcnt lgkmcnt(5)\nQmvaa 204,64,80\ns_add_u32 s31, s74, 131072\n"
"Qmvaa 204,68,108\nbuffer_load_dwordx4 a[80:83], v79, s[48:51], s31 offen offset:0 sc1\nQmvaa 236,64,84\nQmvaa 236,68,112\ns_bitcmp1_b32 s19, 2\ns_cbranch_scc1 .Lrole1\nQmvaa 96,64,88\nQta 56,69,49408\n"
"s_lshl_b32 s46, s19, 4\nQmvav 96,68,168\nbuffer_load_dwordx4 a[84:87], v79, s[48:51], s31 offen offset:1024 sc1\nQmvaa 128,64,92\nds_read_b32 v66, v74\ns_add_u32 s46, s46, 128\nQmvav 128,68,200\nQta 58,69,50432\n"
"v_mov_b32 v77, 0x7ffff000\nQmvaa 160,64,96\nQmvav 160,68,232\nbuffer_load_dwordx4 a[88:91], v79, s[48:51], s31 offen offset:2048 sc1\nQmvaa 192,64,100\ns_waitcnt lgkmcnt(1)\nv_cmp_gt_u32 vcc, v75, v66\nQmvav 192,68,212\n"
"s_cbranch_vccnz .Lso1525r0\n.Lro1525r0:\nQmvaa 224,64,104\nQta 60,69,57600\nQmvav 224,68,244\nbuffer_load_dwordx4 a[92:95], v79, s[48:51], s31 offen offset:3072 sc1\ns_waitcnt vmcnt(31)\ns_waitcnt lgkmcnt(4)\n"
"Qmaav 140,72,84\nQmaav 140,76,116\nQta 62,69,58624\nQmaav 136,72,240\nQmaav 136,76,248\nQmaav 132,72,208\nQta 64,69,49664\ns_add_u32 m0, s96, 65536\n"
"s_add_u32 s95, s67, 86016\nQmaav 132,76,216\nbuffer_load_dwordx4 v63, s[52:55], s95 offen lds\nQmaav 128,72,176\nQmaav 128,76,184\nQta 66,69,50688\nQmaav 124,72,144\nQmaav 124,76,152\n"
"Qmaav 120,72,112\nQta 68,69,57856\ns_add_u32 s95, s67, 105472\nQmaav 120,76,120\nbuffer_load_dwordx4 v63, s[52:55], s95 offen offset:1024 lds\nQmaav 116,72,80\nQmaav 116,76,88\nQta 70,69,58880\n"
"s_waitcnt lgkmcnt(4)\nQmaav 144,56,80\nQmaav 144,60,88\ns_add_u32 m0, s96, 73728\ns_add_u32 s95, s67, 126976\nQmaav 148,56,112\nbuffer_load_dwordx4 v63, s[52:55], s95 offen lds\nQmaav 148,60,120\n"
"Qmaav 152,56,144\nQta 72,69,49920\nQmaav 152,60,152\nQmaav 156,56,176\nQmaav 156,60,184\nQta 74,69,50944\ns_add_u32 s95, s67, 146432\nQmaav 160,56,208\n"
"buffer_load_dwordx4 v63, s[52:55], s95 offen offset:1024 lds\nQmaav 160,60,216\nQmaav 164,56,240\nQta 76,69,58112\nQmaav 164,60,248\nQmaav 168,56,84\nQmaav 168,60,116\nQta 78,69,59136\n"
"s_add_u32 m0, s96, 81920\ns_add_u32 s95, s72, 86016\ns_waitcnt lgkmcnt(4)\nQmvav 24,64,84\nbuffer_load_dwordx4 v63, s[52:55], s95 offen lds\nQmvav 24,68,116\nQmvav 20,64,240\nQmvav 20,68,248\n"
"Qmvav 16,64,208\nQta 56,68,0\ns_add_u32 s95, s72, 105472\nQmvav 16,68,216\nbuffer_load_dwordx4 v63, s[52:55], s95 offen offset:1024 lds\nQmvav 12,64,176\nQmvav 12,68,184\nQta 58,68,512\n"
"Qmvav 8,64,144\nQmvav 8,68,152\nQmvav 4,64,112\nQta 60,68,1024\ns_add_u32 m0, s96, 90112\ns_add_u32 s95, s72, 126976\nQmvav 4,68,120\nbuffer_load_dwordx4 v63, s[52:55], s95 offen lds\n"
"Qmvav 0,64,80\nQmvav 0,68,88\nQta 62,68,1536\ns_waitcnt lgkmcnt(4)\nQmvav 28,72,80\nQmvav 28,76,88\ns_add_u32 s95, s72, 146432\nQmvav 32,72,112\n"
"buffer_load_dwordx4 v63, s[52:55], s95 offen offset:1024 lds\nQmvav 32,76,120\nQmvav 36,72,144\nQta 64,68,256\nQmvav 36,76,152\ns_add_u32 s31, s74, 135168\nQmvav 40,72,176\nbuffer_load_dwordx4 a[96:99], v79, s[48:51], s31 offen offset:0 sc1\n"
"Qmvav 40,76,184\nQta 66,68,768\nQmvav 44,72,208\nv_cmp_eq_u32 s[44:45], s46, v60\ns_nop 1\nv_cndmask_b32 v77, v77, v76, s[44:45]\nv_add_u32 v67, 1, v65\ns_xor_b32 s46, s19, 12\n"
"s_lshl_b32 s28, 1, s46\nQmvav 44,76,216\nQmvav 48,72,240\nQta 68,68,1280\nQmvav 48,76,248\nbuffer_load_dwordx4 a[100:103], v79, s[48:51], s31 offen offset:1024 sc1\nv_add_u32 v75, 1, v75\nQmvav 52,72,84\n"
"s_waitcnt vmcnt(35)\nds_write_b32 v73, v75\nQmvav 52,76,116\nQta 70,68,1792\ns_waitcnt lgkmcnt(5)\nQmaav 196,56,84\nQmaav 196,60,116\nbuffer_load_dwordx4 a[104:107], v79, s[48:51], s31 offen offset:2048 sc1\n"
"Qmaav 192,56,240\nQmaav 192,60,248\nQmaav 188,56,208\nQta 72,68,8192\ns_add_u32 s31, s74, 147456\nQmaav 188,60,216\nbuffer_load_dwordx4 a[108:111], v79, s[48:51], s31 offen offset:0 sc1\nQmaav 184,56,176\n"
"ds_read_b32 v66, v74\nQmaav 184,60,184\nQta 74,68,9216\nQmaav 180,56,144\nQmaav 180,60,152\nbuffer_load_dwordx4 a[112:115], v79, s[48:51], s31 offen offset:1024 sc1\nQmaav 176,56,112\ns_waitcnt lgkmcnt(1)\n"
"v_cmp_gt_u32 vcc, v75, v66\nQmaav 176,60,120\ns_cbranch_vccnz .Lso1595r0\n.Lro1595r0:\nQmaav 172,56,80\nQta 76,68,16384\nQmaav 172,60,88\nbuffer_load_dwordx4 v[168:171], v79, s[48:51], s31 offen offset:2048 sc1\n"
"s_waitcnt lgkmcnt(4)\nQmvav 92,64,80\nQmvav 92,68,88\nQta 78,68,17408\nQmvav 124,64,112\nQmvav 124,68,120\nbuffer_load_dwordx4 v[200:203], v79, s[48:51], s31 offen offset:3072 sc1\nQmvav 156,64,144\n"
"Qta 56,68,8448\nQmvav 156,68,152\nQmvav 188,64,176\nQmvav 188,68,184\nQta 58,68,9472\ns_add_u32 s31, s74, 151552\nQmvav 220,64,208\nbuffer_load_dwordx4 v[232:235], v79, s[48:51], s31 offen offset:0 sc1\n"
"Qmvav 220,68,216\n.Lpg16:\ns_cbranch_execz .Lpg20\nQmvav 252,64,240\nQta 60,68,16640\nQmvav 252,68,248\nQmvav 100,64,84\nbuffer_load_dwordx4 v[212:215], v79, s[48:51], s31 offen offset:1024 sc1\n"
"Qmvav 100,68,116\nQta 62,68,17664\ns_waitcnt lgkmcnt(4)\nQmaav 224,72,84\nQmaav 224,76,116\nQmaav 220,72,240\nbuffer_load_dwordx4 v[244:247], v79, s[48:51], s31 offen offset:2048 sc1\nQmaav 220,76,248\n"
"Qmaav 216,72,208\nQta 64,68,8704\nQmaav 216,76,216\ns_add_u32 m0, s91, 0\ns_add_u32 s31, s77, 131072\nQmaav 212,72,176\nbuffer_load_dwordx4 v79, s[48:51], s31 offen sc1 lds\nQmaav 212,76,184\n"
"Qta 66,68,9728\nQmaav 208,72,144\nQmaav 208,76,152\nQmaav 204,72,112\nQta 68,68,16896\nQmaav 204,76,120\nbuffer_load_dwordx4 v79, s[48:51], s31 offen offset:1024 sc1 lds\nQmaav 200,72,80\n"
"Qmaav 200,76,88\nQta 70,68,17920\ns_waitcnt lgkmcnt(4)\nQmaav 228,56,80\nQmaav 228,60,88\nbuffer_load_dwordx4 v79, s[48:51], s31 offen offset:2048 sc1 lds\nQmaav 232,56,112\nQmaav 232,60,120\n"
"Qmaav 236,56,144\nQta 72,68,8960\nQmaav 236,60,152\nbuffer_load_dwordx4 v79, s[48:51], s31 offen offset:3072 sc1 lds\nQmaav 240,56,176\nQmaav 240,60,184\nQta 74,68,9984\nQmaav 244,56,208\n"
"s_add_u32 m0, s91, 4096\ns_add_u32 s31, s77, 135168\nQmaav 244,60,216\nbuffer_load_dwordx4 v79, s[48:51], s31 offen sc1 lds\nQmaav 248,56,240\nQta 76,68,17152\nQmaav 248,60,248\nQmaav 252,56,84\n"
"Qmaav 252,60,116\nQta 78,68,18176\ns_waitcnt lgkmcnt(4)\nQmvav 172,64,84\nbuffer_load_dwordx4 v79, s[48:51], s31 offen offset:1024 sc1 lds\nQmvav 172,68,116\nQmvav 140,64,240\nQmvav 140,68,248\n"
"Qmvav 108,64,208\nQta 56,68,24576\nQmvav 108,68,216\nbuffer_load_dwordx4 v79, s[48:51], s31 offen offset:2048 sc1 lds\nQmvav 228,64,176\nQmvav 228,68,184\nQta 58,68,25600\nQmvav 196,64,144\n"
"s_add_u32 m0, s91, 33792\ns_add_u32 s31, s77, 147456\nQmvav 196,68,152\nbuffer_load_dwordx4 v79, s[48:51], s31 offen sc1 lds\nQmvav 164,64,112\nQta 60,68,32768\nQmvav 164,68,120\nv_add_u32 v75, 1, v75\n"
"Qmvav 132,64,80\ns_waitcnt vmcnt(47)\nds_write_b32 v73, v75\nQmvav 132,68,88\nQta 62,68,33792\ns_waitcnt lgkmcnt(5)\nQmvav 204,72,80\nbuffer_load_dwordx4 v79, s[48:51], s31 offen offset:1024 sc1 lds\n"
"Qmvav 204,76,88\nQmvav 236,72,112\nQmvav 236,76,120\nQmvav 96,72,144\nQta 64,68,24832\nQmvav 96,76,152\nbuffer_load_dwordx4 v79, s[48:51], s31 offen offset:2048 sc1 lds\nQmvav 128,72,176\n"
"ds_read_b32 v66, v74\nQmvav 128,76,184\nQta 66,68,25856\nQmvav 160,72,208\nQmvav 160,76,216\nbuffer_load_dwordx4 v79, s[48:51], s31 offen offset:3072 sc1 lds\nQmvav 192,72,240\ns_waitcnt lgkmcnt(1)\n"
"v_cmp_gt_u32 vcc, v75, v66\nQmvav 192,76,248\ns_cbranch_vccnz .Lso1665r0\n.Lro1665r0:\nQmvav 224,72,84\nQta 68,68,33024\ns_add_u32 m0, s91, 37888\ns_add_u32 s31, s77, 151552\n"
"Qmvav 224,76,116\nbuffer_load_dwordx4 v79, s[48:51], s31 offen sc1 lds\ns_waitcnt vmcnt(36)\ns_waitcnt lgkmcnt(4)\nQmaaa 140,56,24\nQmaaa 140,60,52\nQta 70,68,34048\nQmaaa 136,56,20\n"
"Qmaaa 136,60,48\nbuffer_load_dwordx4 v79, s[48:51], s31 offen offset:1024 sc1 lds\nQmaaa 132,56,16\nQta 72,68,25088\nQmaaa 132,60,44\nQmaaa 128,56,12\nQmaaa 128,60,40\nQta 74,68,26112\n"
"Qmaaa 124,56,8\nbuffer_load_dwordx4 v79, s[48:51], s31 offen offset:2048 sc1 lds\nQmaaa 124,60,36\nQmaaa 120,56,4\nQta 76,68,33280\nQmaaa 120,60,32\nQmaaa 116,56,0\nQmaaa 116,60,28\n"
"Qta 78,68,34304\ns_add_u32 m0, s96, 0\ns_add_u32 s95, s73, 86016\ns_waitcnt lgkmcnt(4)\nQmaaa 144,64,0\nbuffer_load_dwordx4 v63, s[52:55], s95 offen lds\nQmaaa 144,68,28\nQmaaa 148,64,4\n"
"Qmaaa 148,68,32\nQmaaa 152,64,8\nQta 56,68,25344\ns_add_u32 s95, s73, 105472\nQmaaa 152,68,36\nbuffer_load_dwordx4 v63, s[52:55], s95 offen offset:1024 lds\nQmaaa 156,64,12\nQmaaa 156,68,40\n"
"Qta 58,68,26368\nQmaaa 160,64,16\nQmaaa 160,68,44\nQmaaa 164,64,20\nQta 60,68,33536\ns_add_u32 m0, s96, 8192\ns_add_u32 s95, s73, 126976\nQmaaa 164,68,48\n"
"buffer_load_dwordx4 v63, s[52:55], s95 offen lds\nQmaaa 168,64,24\nQmaaa 168,68,52\nQta 62,68,34560\ns_waitcnt lgkmcnt(4)\nQmvaa 24,72,24\nQmvaa 24,76,52\ns_add_u32 s95, s73, 146432\n"
"Qmvaa 20,72,20\nbuffer_load_dwordx4 v63, s[52:55], s95 offen offset:1024 lds\nQmvaa 20,76,48\nQmvaa 16,72,16\nQta 64,68,40960\nQmvaa 16,76,44\ns_add_u32 s31, s75, 131072\nQmvaa 12,72,12\n"
"buffer_load_dwordx4 v[80:83], v79, s[48:51], s31 offen offset:0 sc1\nQmvaa 12,76,40\nQta 66,68,41472\nQmvaa 8,72,8\nQmvaa 8,76,36\nQmvaa 4,72,4\nQta 68,68,41984\nQmvaa 4,76,32\n"
"buffer_load_dwordx4 v[112:115], v79, s[48:51], s31 offen offset:1024 sc1\nQmvaa 0,72,0\nQmvaa 0,76,28\nQta 70,68,42496\ns_waitcnt lgkmcnt(4)\nQmvaa 28,56,0\nQmvaa 28,60,28\nbuffer_load_dwordx4 v[144:147], v79, s[48:51], s31 offen offset:2048 sc1\n"
"Qmvaa 32,56,4\nQmvaa 32,60,32\nQmvaa 36,56,8\nQta 72,68,41216\nQmvaa 36,60,36\nbuffer_load_dwordx4 v[176:179], v79, s[48:51], s31 offen offset:3072 sc1\nQmvaa 40,56,12\nQmvaa 40,60,40\n"
"Qta 74,68,41728\nQmvaa 44,56,16\ns_add_u32 s31, s75, 135168\nQmvaa 44,60,44\nbuffer_load_dwordx4 v[208:211], v79, s[48:51], s31 offen offset:0 sc1\nQmvaa 48,56,20\nQta 76,68,42240\nQmvaa 48,60,48\n"
"v_add_u32 v75, 1, v75\nQmvaa 52,56,24\ns_waitcnt vmcnt(35)\nds_write_b32 v73, v75\nQmvaa 52,60,52\nQta 78,68,42752\ns_waitcnt lgkmcnt(5)\nQmaaa 196,64,24\n"
"buffer_load_dwordx4 v[240:243], v79, s[48:51], s31 offen offset:1024 sc1\nQmaaa 196,68,52\nQmaaa 192,64,20\nQmaaa 192,68,48\nQmaaa 188,64,16\nQta 56,68,49152\nQmaaa 188,68,44\nbuffer_load_dwordx4 v[84:87], v79, s[48:51], s31 offen offset:2048 sc1\n"
"Qmaaa 184,64,12\nds_read_b32 v66, v74\nQmaaa 184,68,40\nQta 58,68,50176\nQmaaa 180,64,8\ns_add_u32 s31, s75, 147456\nQmaaa 180,68,36\nbuffer_load_dwordx4 v[88:91], v79, s[48:51], s31 offen offset:0 sc1\n"
"Qmaaa 176,64,4\ns_waitcnt lgkmcnt(1)\nv_cmp_gt_u32 vcc, v75, v66\nQmaaa 176,68,32\ns_cbranch_vccnz .Lso1735r0\n.Lro1735r0:\nQmaaa 172,64,0\nQta 60,68,57344\n"
"Qmaaa 172,68,28\nbuffer_load_dwordx4 v[120:123], v79, s[48:51], s31 offen offset:1024 sc1\ns_waitcnt lgkmcnt(4)\nQmvaa 92,72,0\nQmvaa 92,76,28\nQta 62,68,58368\nQmvaa 124,72,4\nQmvaa 124,76,32\n"
"buffer_load_dwordx4 v[152:155], v79, s[48:51], s31 offen offset:2048 sc1\nQmvaa 156,72,8\nQta 64,68,49408\nQmvaa 156,76,36\nQmvaa 188,72,12\nQmvaa 188,76,40\nQta 66,68,50432\nQmvaa 220,72,16\n"
"buffer_load_dwordx4 v[184:187], v79, s[48:51], s31 offen offset:3072 sc1\nQmvaa 220,76,44\nQmvaa 252,72,20\nQta 68,68,57600\nQmvaa 252,76,48\ns_add_u32 s31, s75, 151552\nQmvaa 100,72,24\nbuffer_load_dwordx4 v[216:219], v79, s[48:51], s31 offen offset:0 sc1\n"
"Qmvaa 100,76,52\nQta 70,68,58624\ns_waitcnt lgkmcnt(4)\nQmaaa 224,56,24\nQmaaa 224,60,52\nQmaaa 220,56,20\nbuffer_load_dwordx4 v[248:251], v79, s[48:51], s31 offen offset:1024 sc1\nQmaaa 220,60,48\n"
"Qmaaa 216,56,16\nQta 72,68,49664\nQmaaa 216,60,44\nQmaaa 212,56,12\nbuffer_load_dwordx4 v[116:119], v79, s[48:51], s31 offen offset:2048 sc1\nQmaaa 212,60,40\nQta 74,68,50688\nQmaaa 208,56,8\n"
"Qmaaa 208,60,36\nQmaaa 204,56,4\nQta 76,68,57856\ns_add_u32 m0, s96, 16384\ns_add_u32 s95, s67, 86016\nQmaaa 204,60,32\nbuffer_load_dwordx4 v61, s[52:55], s95 offen lds\nQmaaa 200,56,0\n"
"Qmaaa 200,60,28\nQta 78,68,58880\ns_waitcnt lgkmcnt(4)\nQmaaa 228,64,0\nQmaaa 228,68,28\ns_add_u32 s95, s67, 105472\nQmaaa 232,64,4\nbuffer_load_dwordx4 v61, s[52:55], s95 offen offset:1024 lds\n"
"Qmaaa 232,68,32\nQmaaa 236,64,8\nQta 56,68,49920\nQmaaa 236,68,36\nQmaaa 240,64,12\nQmaaa 240,68,40\nQta 58,68,50944\ns_add_u32 m0, s96, 24576\n"
"s_add_u32 s95, s67, 126976\nQmaaa 244,64,16\nbuffer_load_dwordx4 v61, s[52:55], s95 offen lds\nQmaaa 244,68,44\nQmaaa 248,64,20\nQta 60,68,58112\nQmaaa 248,68,48\nQmaaa 252,64,24\n"
"Qmaaa 252,68,52\nQta 62,68,59136\ns_add_u32 s95, s67, 146432\ns_waitcnt lgkmcnt(4)\nQmvaa 172,72,24\nbuffer_load_dwordx4 v61, s[52:55], s95 offen offset:1024 lds\nQmvaa 172,76,52\nQmvaa 140,72,20\n"
"Qmvaa 140,76,48\nQmvaa 108,72,16\nQta 64,69,0\ns_add_u32 m0, s96, 32768\ns_add_u32 s95, s72, 86016\nQmvaa 108,76,44\nbuffer_load_dwordx4 v61, s[52:55], s95 offen lds\nQmvaa 228,72,12\n"
"Qmvaa 228,76,40\nQta 66,69,1024\nQmvaa 196,72,8\nQmvaa 196,76,36\nQmvaa 164,72,4\nQta 68,69,8192\ns_add_u32 s95, s72, 105472\nQmvaa 164,76,32\n"
"buffer_load_dwordx4 v61, s[52:55], s95 offen offset:1024 lds\nv_add_u32 v75, 1, v75\nQmvaa 132,72,0\ns_waitcnt vmcnt(20)\nds_write_b32 v73, v75\nQmvaa 132,76,28\nQta 70,69,9216\ns_waitcnt lgkmcnt(5)\n"
"Qmvaa 204,56,0\ns_waitcnt vmcnt(24)\nQda 0,58,0\nQmvaa 204,60,28\nQmvaa 236,56,4\nQda 28,58,33792\nQmvaa 236,60,32\nQmvaa 96,56,8\n"
"Qta 72,69,16384\nQmvaa 96,60,36\nQda 4,58,1024\nQmvaa 128,56,12\nds_read_b32 v66, v74\nQmvaa 128,60,40\nQta 74,69,17408\nQmvaa 160,56,16\n"
"Qda 32,58,34816\nQmvaa 160,60,44\nQmvaa 192,56,20\ns_waitcnt lgkmcnt(2)\nv_cmp_gt_u32 vcc, v75, v66\nQmvaa 192,60,48\ns_cbranch_vccnz .Lso1805r0\n.Lro1805r0:\n"
"Qmvaa 224,56,24\nQta 76,69,24576\ns_add_u32 m0, s96, 40960\ns_add_u32 s95, s72, 126976\nQmvaa 224,60,52\nbuffer_load_dwordx4 v61, s[52:55], s95 offen lds\ns_waitcnt vmcnt(39)\ns_waitcnt lgkmcnt(8)\n"
"Qmaaa 200,64,80\nQda 8,58,2048\nQmaaa 200,68,108\nQta 78,69,25600\nQmaaa 204,64,84\nQda 36,58,35840\nQmaaa 204,68,112\nQmaaa 208,64,88\n"
"Qta 56,68,0\ns_add_u32 s95, s72, 146432\nQmaav 208,68,168\nbuffer_load_dwordx4 v61, s[52:55], s95 offen offset:1024 lds\nQmaaa 212,64,92\nQda 12,58,3072\nQmaav 212,68,200\nQta 58,68,1024\n"
"Qmaaa 216,64,96\nQda 40,58,36864\nQmaav 216,68,232\nQmaaa 220,64,100\nQta 60,68,8192\ns_add_u32 m0, s96, 49152\ns_add_u32 s95, s73, 86016\nQmaav 220,68,212\n"
"buffer_load_dwordx4 v61, s[52:55], s95 offen lds\nQmaaa 224,64,104\nQda 16,58,4096\nQmaav 224,68,244\nQta 62,68,9216\ns_waitcnt vmcnt(9)\ns_waitcnt lgkmcnt(8)\nQmaav 200,72,80\n"
"Qda 44,58,37888\nQmaav 200,76,88\ns_add_u32 s95, s73, 105472\nQmaav 204,72,112\nbuffer_load_dwordx4 v61, s[52:55], s95 offen offset:1024 lds\nQmaav 204,76,120\nQda 20,58,5120\nQmaav 208,72,144\n"
"Qta 64,69,256\nQmaav 208,76,152\nQda 48,58,38912\nQmaav 212,72,176\nQmaav 212,76,184\nQta 66,69,1280\ns_add_u32 m0, s96, 57344\ns_add_u32 s95, s73, 126976\n"
"Qmaav 216,72,208\nbuffer_load_dwordx4 v61, s[52:55], s95 offen lds\nQmaav 216,76,216\nQda 24,58,6144\nQmaav 220,72,240\nQta 68,69,8448\nQmaav 220,76,248\nQda 52,58,39936\n"
"Qmaav 224,72,84\nQmaav 224,76,116\nQta 70,69,9472\ns_add_u32 s95, s73, 146432\ns_waitcnt lgkmcnt(9)\nQmaaa 200,56,0\nbuffer_load_dwordx4 v61, s[52:55], s95 offen offset:1024 lds\nQmaaa 200,60,28\n"
"Qmaaa 204,56,4\nQmaaa 204,60,32\ns_waitcnt lgkmcnt(9)\nQmaaa 208,56,8\nQta 72,69,16640\nQmaaa 208,60,36\ns_nop 1\nds_write_b128 v58, a[200:203] offset:0\n"
"Qmaaa 212,56,12\nQmaaa 212,60,40\nQta 74,69,17664\ns_waitcnt lgkmcnt(4)\nQmaaa 216,56,16\nds_write_b128 v58, a[204:207] offset:1024\nQmaaa 216,60,44\nQmaaa 220,56,20\n"
"Qta 76,69,24832\nQmaaa 220,60,48\nds_write_b128 v58, a[208:211] offset:2048\nQmaaa 224,56,24\nQmaaa 224,60,52\nQta 78,69,25856\ns_waitcnt lgkmcnt(7)\nQmaaa 228,64,80\n"
"ds_write_b128 v58, a[212:215] offset:3072\nQmaaa 228,68,108\nQmaaa 232,64,84\nQmaaa 232,68,112\nds_write_b128 v58, a[216:219] offset:4096\nQmaaa 236,64,88\nQta 56,68,256\nQmaav 236,68,168\n"
"Qmaaa 240,64,92\nds_write_b128 v58, a[220:223] offset:5120\nQmaav 240,68,200\nQta 58,68,1280\nQmaaa 244,64,96\nQmaav 244,68,232\nds_write_b128 v58, a[224:227] offset:6144\nQmaaa 248,64,100\n"
"Qta 60,68,8448\nQmaav 248,68,212\nQmaaa 252,64,104\nQmaav 252,68,244\nQta 62,68,9472\ns_waitcnt lgkmcnt(8)\nQmaav 228,72,80\nv_add_u32 v75, 1, v75\n"
"Qmaav 228,76,88\nds_write_b32 v73, v75\nQmaav 232,72,112\nQmaav 232,76,120\nQmaav 236,72,144\nQta 64,69,512\nQmaav 236,76,152\nQmaav 240,72,176\n"
"Qmaav 240,76,184\nQta 66,69,1536\nQmaav 244,72,208\nQmaav 244,76,216\nds_read_b32 v66, v74\nQmaav 248,72,240\nQta 68,69,8704\nQmaav 248,76,248\n"
"Qmaav 252,72,84\nQmaav 252,76,116\ns_waitcnt lgkmcnt(1)\nv_cmp_gt_u32 vcc, v75, v66\ns_waitcnt lgkmcnt(5)\nQmaaa 228,56,0\ns_cbranch_vccnz .Lso1878r0\n.Lro1878r0:\n"
"Qmaaa 228,60,28\nQta 70,69,9728\nQmaaa 232,56,4\nQmaaa 232,60,32\nQdv 148,59,2048\nQmaaa 236,56,8\nQta 72,69,16896\nQmaaa 236,60,36\n"
"s_nop 0\nds_write_b128 v58, a[228:231] offset:33792\nQmaaa 240,56,12\nQdv 180,59,3072\nQmaaa 240,60,40\nQta 74,69,17920\nQmaaa 244,56,16\nds_write_b128 v58, a[232:235] offset:34816\n"
"Qmaaa 244,60,44\nQdv 104,59,5120\nQmaaa 248,56,20\nQta 76,69,25088\nQmaaa 248,60,48\nds_write_b128 v58, a[236:239] offset:35840\nQmaaa 252,56,24\nQdv 136,59,0\n"
"s_waitcnt lgkmcnt(9)\nQa 196,196,148\nQmaaa 252,60,52\nQta 78,69,26112\nQa 197,197,149\ns_waitcnt lgkmcnt(10)\nQmvaa 132,64,80\nds_write_b128 v58, a[240:243] offset:36864\n"
"Qmvaa 132,68,108\n.Lpg17:\ns_cbranch_execz .Lpg21\nQa 198,198,150\nQa 199,199,151\nQmvaa 164,64,84\ns_waitcnt lgkmcnt(8)\nQa 228,228,180\n"
"Qa 229,229,181\nQmvaa 164,68,112\nds_write_b128 v58, a[244:247] offset:37888\nQmvaa 196,64,88\nQta 56,68,512\nQa 230,230,182\nQa 231,231,183\nQmvav 196,68,168\n"
"Qdv 148,59,4096\ns_waitcnt lgkmcnt(8)\nQa 140,140,104\nQmvaa 228,64,92\nds_write_b128 v58, a[248:251] offset:38912\nQmvav 228,68,200\nQta 58,68,1536\nQa 141,141,105\n"
"Qmvaa 108,64,96\nQa 142,142,106\nQa 143,143,107\nQmvav 108,68,232\nQdv 180,59,6144\ns_waitcnt lgkmcnt(8)\nQa 132,132,136\nQmvaa 140,64,100\n"
"Qta 60,68,8704\nQa 133,133,137\nQmvav 140,68,212\nds_write_b128 v58, a[252:255] offset:39936\nQmvaa 172,64,104\nQa 134,134,138\nQa 135,135,139\nQmvav 172,68,244\n"
"Qta 62,68,9728\ns_waitcnt lgkmcnt(6)\nQa 108,108,148\ns_waitcnt lgkmcnt(6)\nQmvav 132,72,80\nQdv 104,59,1024\nv_add_u32 v75, 1, v75\nQmvav 132,76,88\n"
"s_waitcnt vmcnt(10)\nds_write_b32 v73, v75\nQmvav 164,72,112\nQa 109,109,149\nQa 110,110,150\nQmvav 164,76,120\nQa 111,111,151\ns_waitcnt lgkmcnt(5)\n"
"Qa 172,172,180\nQmvav 196,72,144\nQta 64,69,768\nQa 173,173,181\nQmvav 196,76,152\nQa 174,174,182\nQa 175,175,183\nQmvav 228,72,176\n"
"Qmvav 228,76,184\nQta 66,69,1792\ns_waitcnt lgkmcnt(3)\nQa 164,164,104\nQmvav 108,72,208\nQa 165,165,105\nQa 166,166,106\nQmvav 108,76,216\n"
"ds_read_b32 v66, v74\nQa 167,167,107\nQmvav 140,72,240\nQta 68,69,8960\nQmvav 140,76,248\nQmvav 172,72,84\nQmvav 172,76,116\ns_waitcnt lgkmcnt(1)\n"
"v_cmp_gt_u32 vcc, v75, v66\ns_waitcnt lgkmcnt(6)\nQmvaa 132,56,0\ns_cbranch_vccnz .Lso1920r0\n.Lro1920r0:\nQmvaa 132,60,28\nQta 70,69,9984\ns_add_u32 s68, s64, 0\n"
"Qmvaa 164,56,4\nQmvaa 164,60,32\nQdv 136,59,35840\nQmvaa 196,56,8\nQta 72,69,17152\ns_nop 0\nQk 132,132,133\nQmvaa 196,60,36\n"
"Qk 133,134,135\nQmvaa 228,56,12\nQdv 148,59,36864\nQk 134,164,165\nQmvaa 228,60,40\nQta 74,69,18176\nQk 135,166,167\nQmvaa 108,56,16\n"
"buffer_store_dwordx4 v[132:135], v79, s[24:27], s68 offen offset:0 sc1\nQk 196,196,197\nQmvaa 108,60,44\nQdv 180,59,38912\nQk 197,198,199\nQmvaa 140,56,20\nQta 76,69,25344\nQk 198,228,229\n"
"Qmvaa 140,60,48\ns_waitcnt lgkmcnt(5)\nQa 96,96,136\nQa 97,97,137\nQmvaa 172,56,24\nQdv 104,59,33792\nQa 98,98,138\nQmvaa 172,60,52\n"
"Qta 78,69,26368\nQa 99,99,139\ns_waitcnt lgkmcnt(11)\nQmvaa 204,64,80\ns_waitcnt lgkmcnt(5)\nQa 128,128,148\nQa 129,129,149\nQmvaa 204,68,108\n"
"Qa 130,130,150\nQa 131,131,151\nQmvaa 236,64,84\nQk 199,230,231\nQk 108,108,109\nQmvaa 236,68,112\ns_waitcnt lgkmcnt(3)\nQa 192,192,180\n"
"Qa 193,193,181\nQmvaa 96,64,88\nQta 56,68,768\nQa 194,194,182\nQmvav 96,68,168\nQdv 136,59,37888\nQa 195,195,183\nQmvaa 128,64,92\n"
"s_waitcnt lgkmcnt(3)\nQa 204,204,104\nQa 205,205,105\nQmvav 128,68,200\nQta 58,68,1792\nQa 206,206,106\nQmvaa 160,64,96\nbuffer_store_dwordx4 v[196:199], v79, s[24:27], s68 offen offset:1024 sc1\n"
"Qa 207,207,107\nQmvav 160,68,232\nQdv 148,59,39936\nQk 109,110,111\nQmvaa 192,64,100\nQta 60,68,8960\nQk 110,140,141\nQmvav 192,68,212\n"
"Qk 111,142,143\nQk 172,172,173\nQmvaa 224,64,104\nbuffer_store_dwordx4 v[108:111], v79, s[24:27], s68 offen offset:2048 sc1\nv_add_u32 v75, 1, v75\nQmvav 224,68,244\ns_waitcnt vmcnt(3)\nds_write_b32 v73, v75\n"
"s_waitcnt lgkmcnt(10)\nQmvav 204,72,80\nQta 62,68,9984\ns_waitcnt lgkmcnt(5)\nQa 160,160,136\nQmvav 204,76,88\nQdv 180,59,34816\nQa 161,161,137\n"
"Qmvav 236,72,112\nQa 162,162,138\nQa 163,163,139\nQmvav 236,76,120\ns_waitcnt lgkmcnt(4)\nQa 224,224,148\nQa 225,225,149\nQmvav 96,72,144\n"
"Qta 64,68,16384\nQa 226,226,150\nQmvav 96,76,152\nQa 227,227,151\nQk 173,174,175\nQmvav 128,72,176\nQmvav 128,76,184\nds_read_b32 v66, v74\n"
"s_waitcnt lgkmcnt(2)\nQa 236,236,180\nQmvav 160,72,208\nQta 66,68,17408\nQa 237,237,181\nQmvav 160,76,216\nQa 238,238,182\nQa 239,239,183\n"
"Qmvav 192,72,240\nQmvav 192,76,248\ns_waitcnt lgkmcnt(1)\nv_cmp_gt_u32 vcc, v75, v66\nQmvav 224,72,84\ns_cbranch_vccnz .Lso1960r0\n.Lro1960r0:\nQmvav 224,76,116\n"
"Qta 68,68,24576\ns_add_u32 m0, s96, 65536\ns_add_u32 s95, s67, 86016\ns_waitcnt lgkmcnt(5)\nQmvaa 204,56,0\nbuffer_load_dwordx4 v62, s[52:55], s95 offen lds\nQmvaa 204,60,28\nQmvaa 236,56,4\n"
"Qta 70,68,25600\nQmvaa 236,60,32\nQmvaa 96,56,8\ns_nop 3\nQk 174,204,205\nQk 175,206,207\nQmvaa 96,60,36\nQta 72,68,32768\n"
"s_add_u32 s95, s67, 125952\nQmvaa 128,56,12\nbuffer_load_dwordx4 v62, s[52:55], s95 offen offset:1024 lds\nQmvaa 128,60,40\nbuffer_store_dwordx4 v[172:175], v79, s[24:27], s68 offen offset:3072 sc1\nQk 236,236,237\nQmvaa 160,56,16\nQta 74,68,33792\n"
"Qk 237,238,239\nQmvaa 160,60,44\nQk 238,96,97\nQk 239,98,99\nQmvaa 192,56,20\ns_add_u32 s68, s64, 4096\nQk 128,128,129\nQmvaa 192,60,48\n"
"Qta 76,68,40960\ns_add_u32 m0, s96, 73728\ns_add_u32 s95, s72, 86016\nQmvaa 224,56,24\nbuffer_load_dwordx4 v62, s[52:55], s95 offen lds\nQmvaa 224,60,52\nbuffer_store_dwordx4 v[236:239], v79, s[24:27], s68 offen offset:0 sc1\nQk 129,130,131\n"
"s_waitcnt lgkmcnt(3)\nQmaaa 116,64,80\nQta 78,68,41984\nQk 130,160,161\nQmaaa 116,68,108\nQk 131,162,163\nQk 192,192,193\nQmaaa 120,64,84\n"
"buffer_store_dwordx4 v[128:131], v79, s[24:27], s68 offen offset:1024 sc1\ns_add_u32 s95, s72, 125952\nQmaaa 120,68,112\nbuffer_load_dwordx4 v62, s[52:55], s95 offen offset:1024 lds\nQmaaa 124,64,88\nQta 56,68,49152\nQk 193,194,195\nQmaav 124,68,168\n"
"Qk 194,224,225\nQk 195,226,227\nQmaaa 128,64,92\nbuffer_store_dwordx4 v[192:195], v79, s[24:27], s68 offen offset:2048 sc1\nQmaav 128,68,200\nQta 58,68,50176\ns_add_u32 m0, s96, 81920\ns_add_u32 s95, s73, 86016\n"
"Qmaaa 132,64,96\nbuffer_load_dwordx4 v62, s[52:55], s95 offen lds\nQmaav 132,68,232\nQmaaa 136,64,100\nQta 60,68,57344\nQmaav 136,68,212\nQmaaa 140,64,104\nQmaav 140,68,244\n"
"Qta 62,68,58368\ns_add_u32 s95, s73, 125952\ns_waitcnt lgkmcnt(4)\nQmaav 116,72,80\nbuffer_load_dwordx4 v62, s[52:55], s95 offen offset:1024 lds\nQmaav 116,76,88\nQmaav 120,72,112\nv_bfe_u32 v61, v60, 6, 2\n"
"v_lshrrev_b32 v62, 8, v60\nQmaav 120,76,120\nv_xor_b32 v61, v61, v62\nv_bfe_u32 v62, v60, 5, 1\nQmaav 124,72,144\nQta 64,68,16640\nv_lshl_or_b32 v61, v61, 1, v62\nQmaav 124,76,152\n"
"v_and_b32 v62, 16, v60\nv_mov_b32 v63, 0x3f80\nQmaav 128,72,176\nv_lshlrev_b32 v63, v62, v63\nv_min_u32 v62, 1, v61\nQmaav 128,76,184\nQta 66,68,17664\nv_add_u32 v62, -1, v62\n"
"Qmaav 132,72,208\nv_and_b32 v62, v62, v63\nv_xor_b32 v61, 1, v61\nQmaav 132,76,216\nv_min_u32 v61, 1, v61\nv_add_u32 v61, -1, v61\nQmaav 136,72,240\nQta 68,68,24832\n"
"v_and_b32 v61, v61, v63\nQmaav 136,76,248\nv_accvgpr_write_b32 a202, v62\nv_accvgpr_write_b32 a203, v61\nQmaav 140,72,84\nv_accvgpr_write_b32 a200, 0\nv_accvgpr_write_b32 a201, 0\nQmaav 140,76,116\n"
"Qta 70,68,25856\nv_accvgpr_write_b32 a204, 0\ns_waitcnt lgkmcnt(4)\nQmaaa 116,56,0\nv_accvgpr_write_b32 a205, 0\nQmaaa 116,60,28\nQmaaa 120,56,4\nQmaaa 120,60,32\n"
"Qmaaa 124,56,8\nQta 72,68,33024\nQmaaa 124,60,36\ns_nop 2\nds_write_b128 v58, a[116:119] offset:0\nQmaaa 128,56,12\nQmaaa 128,60,40\nQta 74,68,34048\n"
"Qmaaa 132,56,16\nds_write_b128 v58, a[120:123] offset:1024\nQmaaa 132,60,44\nQmaaa 136,56,20\nQta 76,68,41216\nQmaaa 136,60,48\nds_write_b128 v58, a[124:127] offset:2048\nQmaaa 140,56,24\n"
"Qmaaa 140,60,52\nQta 78,68,42240\ns_waitcnt lgkmcnt(7)\nQmaaa 144,64,80\nds_write_b128 v58, a[128:131] offset:3072\nQmaaa 144,68,108\nQmaaa 148,64,84\nQmaaa 148,68,112\n"
"ds_write_b128 v58, a[132:135] offset:4096\nQmaaa 152,64,88\nQta 56,68,49408\nQmaav 152,68,168\nQmaaa 156,64,92\nds_write_b128 v58, a[136:139] offset:5120\nQmaav 156,68,200\nQta 58,68,50432\n"
"Qmaaa 160,64,96\nQmaav 160,68,232\nds_write_b128 v58, a[140:143] offset:6144\nQmaaa 164,64,100\nQta 60,68,57600\nQmaav 164,68,212\nQmaaa 168,64,104\nQmaav 168,68,244\n"
"Qta 62,68,58624\ns_waitcnt lgkmcnt(8)\nQmaav 144,72,80\nv_add_u32 v75, 1, v75\nQmaav 144,76,88\ns_waitcnt vmcnt(8)\nds_write_b32 v73, v75\nQmaav 148,72,112\n"
"Qmaav 148,76,120\nQmaav 152,72,144\nQta 64,68,16896\nQmaav 152,76,152\nQmaav 156,72,176\nQmaav 156,76,184\nQta 66,68,17920\nQmaav 160,72,208\n"
"Qmaav 160,76,216\nds_read_b32 v66, v74\nQmaav 164,72,240\nQta 68,68,25088\ns_waitcnt vmcnt(2)\nbuffer_store_dword v67, v77, s[24:27], 0 offen sc1\nQmaav 164,76,248\nQmaav 168,72,84\n"
"Qmaav 168,76,116\ns_waitcnt lgkmcnt(1)\nv_cmp_gt_u32 vcc, v75, v66\ns_waitcnt lgkmcnt(5)\nQmaaa 144,56,0\ns_cbranch_vccnz .Lso2046r0\n.Lro2046r0:\nQmaaa 144,60,28\n"
"Qta 70,68,26112\nQmaaa 148,56,4\nQmaaa 148,60,32\nQdv 104,59,2048\nQmaaa 152,56,8\nQta 72,68,33280\nQmaaa 152,60,36\ns_nop 0\n"
"ds_write_b128 v58, a[144:147] offset:33792\nQmaaa 156,56,12\nQdv 136,59,3072\nQmaaa 156,60,40\nQta 74,68,34304\nQmaaa 160,56,16\nds_write_b128 v58, a[148:151] offset:34816\nQmaaa 160,60,44\n"
"Qdv 148,59,5120\nQmaaa 164,56,20\nQta 76,68,41472\nQmaaa 164,60,48\nds_write_b128 v58, a[152:155] offset:35840\nQmaaa 168,56,24\nQdv 180,59,0\ns_waitcnt lgkmcnt(9)\n"
"Qa 8,8,104\nQmaaa 168,60,52\nQta 78,68,42496\nQa 9,9,105\ns_waitcnt lgkmcnt(10)\nQmvaa 0,64,80\nds_write_b128 v58, a[156:159] offset:36864\nQmvaa 0,68,108\n"
"Qa 10,10,106\nQa 11,11,107\nQmvaa 4,64,84\ns_waitcnt lgkmcnt(8)\nQa 12,12,136\nQa 13,13,137\nQmvaa 4,68,112\nds_write_b128 v58, a[160:163] offset:37888\n"
"Qmvaa 8,64,88\nQta 56,68,49664\nQa 14,14,138\nQa 15,15,139\nQmvav 8,68,168\nQdv 104,59,4096\ns_waitcnt lgkmcnt(8)\nQa 20,20,148\n"
"Qmvaa 12,64,92\nds_write_b128 v58, a[164:167] offset:38912\nQmvav 12,68,200\nQta 58,68,50688\nQa 21,21,149\nQmvaa 16,64,96\nQa 22,22,150\nQa 23,23,151\n"
"Qmvav 16,68,232\nQdv 136,59,6144\ns_waitcnt lgkmcnt(8)\nQa 0,0,180\nQmvaa 20,64,100\nQta 60,68,57856\nQa 1,1,181\nQmvav 20,68,212\n"
"ds_write_b128 v58, a[168:171] offset:39936\nQmvaa 24,64,104\nQa 2,2,182\nQa 3,3,183\nQmvav 24,68,244\nQta 62,68,58880\ns_waitcnt lgkmcnt(6)\nQa 16,16,104\n"
"s_waitcnt lgkmcnt(6)\nQmvav 0,72,80\nQdv 148,59,1024\nv_add_u32 v75, 1, v75\nQmvav 0,76,88\ns_waitcnt vmcnt(1)\nds_write_b32 v73, v75\nQmvav 4,72,112\n"
"Qa 17,17,105\nQa 18,18,106\nQmvav 4,76,120\nQa 19,19,107\ns_waitcnt lgkmcnt(5)\nQa 24,24,136\nQmvav 8,72,144\nQta 64,69,0\n"
"Qa 25,25,137\nQmvav 8,76,152\nQa 26,26,138\nQa 27,27,139\nQmvav 12,72,176\nQmvav 12,76,184\nQta 66,69,512\ns_waitcnt lgkmcnt(3)\n"
"Qa 4,4,148\nQmvav 16,72,208\nQa 5,5,149\nQa 6,6,150\nQmvav 16,76,216\nds_read_b32 v66, v74\nQa 7,7,151\nQmvav 20,72,240\n"
"Qta 68,69,1024\nQmvav 20,76,248\nQmvav 24,72,84\nQmvav 24,76,116\ns_waitcnt lgkmcnt(1)\nv_cmp_gt_u32 vcc, v75, v66\ns_waitcnt lgkmcnt(6)\nQmvaa 0,56,0\n"
"s_cbranch_vccnz .Lso2088r0\n.Lro2088r0:\nQmvaa 0,60,28\nQta 70,69,1536\nQmvaa 4,56,4\nQmvaa 4,60,32\nQdv 180,59,33792\nQmvaa 8,56,8\n"
"Qta 72,69,8192\nQmvaa 8,60,36\nQmvaa 12,56,12\nQdv 104,59,34816\nQmvaa 12,60,40\nQta 74,69,8704\nQmvaa 16,56,16\nQmvaa 16,60,44\n"
"Qdv 136,59,35840\nQmvaa 20,56,20\nQta 76,69,9216\nQmvaa 20,60,48\ns_waitcnt lgkmcnt(5)\nQa 28,28,180\nQa 29,29,181\nQmvaa 24,56,24\n"
"Qdv 148,59,36864\nQa 30,30,182\nQmvaa 24,60,52\nQta 78,69,9728\nQa 31,31,183\ns_waitcnt lgkmcnt(11)\nQmaaa 172,64,80\ns_waitcnt lgkmcnt(5)\n"
"Qa 32,32,104\nQa 33,33,105\nQmaaa 172,68,108\nQa 34,34,106\nQa 35,35,107\nQmaaa 176,64,84\nQmaaa 176,68,112\ns_waitcnt lgkmcnt(3)\n"
"Qa 36,36,136\nQa 37,37,137\nQmaaa 180,64,88\nQta 56,69,16384\nQa 38,38,138\nQmaav 180,68,168\nQdv 180,59,37888\nQa 39,39,139\n"
"Qmaaa 184,64,92\ns_waitcnt lgkmcnt(3)\nQa 40,40,148\nQa 41,41,149\nQmaav 184,68,200\nQta 58,69,16896\nQa 42,42,150\nQmaaa 188,64,96\n"
"Qdv 104,59,38912\nQa 43,43,151\nQmaav 188,68,232\nQmaaa 192,64,100\nQta 60,69,17408\nQmaav 192,68,212\nQdv 136,59,39936\nQmaaa 196,64,104\n"
"s_waitcnt lgkmcnt(4)\nQa 44,44,180\nQa 45,45,181\nQmaav 196,68,244\nQta 62,69,17920\nQa 46,46,182\ns_waitcnt lgkmcnt(5)\nQmaav 172,72,80\n"
"Qa 47,47,183\nQmaav 172,76,88\nQmaav 176,72,112\ns_waitcnt lgkmcnt(3)\nQa 48,48,104\nQa 49,49,105\nQmaav 176,76,120\nQa 50,50,106\n"
"Qa 51,51,107\nQmaav 180,72,144\nQta 64,68,17152\nQmaav 180,76,152\ns_waitcnt lgkmcnt(2)\nQa 52,52,136\nQa 53,53,137\nQmaav 184,72,176\n"
"Qa 54,54,138\nQa 55,55,139\nQmaav 184,76,184\nQta 66,68,18176\nQmaav 188,72,208\nQmaav 188,76,216\nQmaav 192,72,240\nQta 68,68,25344\n"
"Qmaav 192,76,248\nQmaav 196,72,84\nQmaav 196,76,116\nQta 70,68,26368\ns_waitcnt lgkmcnt(4)\nQmaaa 172,56,0\n.Lpg18:\ns_cbranch_execz .Lpg22\n"
"Qmaaa 172,60,28\nQmaaa 176,56,4\nQmaaa 176,60,32\nQmaaa 180,56,8\nQta 72,68,33536\nQmaaa 180,60,36\ns_nop 2\nds_write_b128 v58, a[172:175] offset:0\n"
"Qmaaa 184,56,12\nQmaaa 184,60,40\nQta 74,68,34560\nQmaaa 188,56,16\nds_write_b128 v58, a[176:179] offset:1024\nQmaaa 188,60,44\nQmaaa 192,56,20\nQta 76,68,41728\n"
"Qmaaa 192,60,48\nds_write_b128 v58, a[180:183] offset:2048\nQmaaa 196,56,24\nQmaaa 196,60,52\nQta 78,68,42752\ns_waitcnt lgkmcnt(7)\nQmvaa 28,64,80\nds_write_b128 v58, a[184:187] offset:3072\n"
"Qmvaa 28,68,108\nQmvaa 32,64,84\nQmvaa 32,68,112\nds_write_b128 v58, a[188:191] offset:4096\nQmvaa 36,64,88\nQta 56,68,49920\nQmvav 36,68,168\nQmvaa 40,64,92\n"
"ds_write_b128 v58, a[192:195] offset:5120\nQmvav 40,68,200\nQta 58,68,50944\nQmvaa 44,64,96\nQmvav 44,68,232\nds_write_b128 v58, a[196:199] offset:6144\nQmvaa 48,64,100\nQta 60,68,58112\n"
"Qmvav 48,68,212\nQmvaa 52,64,104\nQmvav 52,68,244\nQta 62,68,59136\ns_waitcnt lgkmcnt(8)\nQmvav 28,72,80\nv_add_u32 v75, 1, v75\nQmvav 28,76,88\n"
"ds_write_b32 v73, v75\nQmvav 32,72,112\nQmvav 32,76,120\nQmvav 36,72,144\nQta 64,69,256\nQmvav 36,76,152\nQmvav 40,72,176\nQmvav 40,76,184\n"
"Qta 66,69,768\nQmvav 44,72,208\nQmvav 44,76,216\nds_read_b32 v66, v74\nQmvav 48,72,240\nQta 68,69,1280\nQmvav 48,76,248\nQmvav 52,72,84\n"
"Qmvav 52,76,116\ns_waitcnt lgkmcnt(1)\nv_cmp_gt_u32 vcc, v75, v66\ns_waitcnt lgkmcnt(5)\nQmvaa 28,56,0\ns_cbranch_vccnz .Lso2172r0\n.Lro2172r0:\nQmvaa 28,60,28\n"
"buffer_load_dword v78, v76, s[24:27], 0 offen sc1\nQmvaa 32,56,4\nQta 70,69,1792\nQmvaa 32,60,32\nQdv 148,59,2048\nQmvaa 36,56,8\nQmvaa 36,60,36\nQta 72,69,8448\n"
"Qmvaa 40,56,12\nQdv 180,59,3072\nQmvaa 40,60,40\nQmvaa 44,56,16\nQta 74,69,8960\nQmvaa 44,60,44\nQdv 104,59,5120\nQmvaa 48,56,20\n"
"Qmvaa 48,60,48\nQta 76,69,9472\ns_waitcnt lgkmcnt(5)\nQa 156,156,148\nQmvaa 52,56,24\nQdv 136,59,0\nQa 157,157,149\nQmvaa 52,60,52\n"
"Qa 158,158,150\nQa 159,159,151\ns_waitcnt lgkmcnt(10)\nQmvaa 92,64,80\nQta 78,69,9984\ns_waitcnt lgkmcnt(5)\nQa 188,188,180\nQmvaa 92,68,108\n"
"Qa 189,189,181\nQa 190,190,182\nQmvaa 124,64,84\nQa 191,191,183\nQmvaa 124,68,112\ns_waitcnt lgkmcnt(3)\nQa 252,252,104\nQa 253,253,105\n"
"Qmvaa 156,64,88\nQta 56,69,16640\nQa 254,254,106\nQmvav 156,68,168\nQdv 148,59,4096\nQa 255,255,107\nQmvaa 188,64,92\ns_waitcnt lgkmcnt(3)\n"
"Qa 92,92,136\nQa 93,93,137\nQmvav 188,68,200\nQta 58,69,17152\nQa 94,94,138\nQmvaa 220,64,96\nQa 95,95,139\nQmvav 220,68,232\n"
"Qdv 180,59,6144\nQmvaa 252,64,100\nQta 60,69,17664\nQmvav 252,68,212\nQmvaa 100,64,104\nQmvav 100,68,244\nQta 62,69,18176\ns_waitcnt lgkmcnt(4)\n"
"Qa 220,220,148\ns_waitcnt lgkmcnt(4)\nQmvav 92,72,80\nQdv 104,59,1024\nQa 221,221,149\nQmvav 92,76,88\nQa 222,222,150\nQa 223,223,151\n"
"Qmvav 124,72,112\nQmvav 124,76,120\ns_waitcnt vmcnt(0)\nv_sub_u32 v66, v78, v65\nv_cmp_gt_u32 vcc, 1, v66\ns_and_b64 vcc, vcc, s[28:29]\ns_cbranch_scc1 .Lx0_w\n.Lx0_r:\n"
"Qmvav 156,72,144\ns_add_u32 s69, s98, 0\nbuffer_load_dwordx4 a[208:211], v79, s[24:27], s69 offen offset:0 sc1\nQmvav 156,76,152\ns_waitcnt lgkmcnt(3)\nQa 100,100,180\nQa 101,101,181\nQmvav 188,72,176\n"
"buffer_load_dwordx4 a[212:215], v79, s[24:27], s69 offen offset:1024 sc1\nQmvav 188,76,184\nQa 102,102,182\nQa 103,103,183\nQmvav 220,72,208\nbuffer_load_dwordx4 a[216:219], v79, s[24:27], s69 offen offset:2048 sc1\nv_add_u32 v75, 1, v75\nQmvav 220,76,216\n"
"ds_write_b32 v73, v75\ns_waitcnt lgkmcnt(1)\nQa 124,124,104\nQmvav 252,72,240\nbuffer_load_dwordx4 a[220:223], v79, s[24:27], s69 offen offset:3072 sc1\nQmvav 252,76,248\nQa 125,125,105\nQa 126,126,106\n"
"Qmvav 100,72,84\ns_add_u32 s69, s98, 4096\nbuffer_load_dwordx4 a[236:239], v79, s[24:27], s69 offen offset:3072 sc1\nQmvav 100,76,116\nQa 127,127,107\ns_waitcnt lgkmcnt(2)\nQmvaa 92,56,0\nbuffer_load_dwordx4 a[224:227], v79, s[24:27], s69 offen offset:0 sc1\n"
"Qmvaa 92,60,28\nQmvaa 124,56,4\ns_add_u32 s69, s98, 16384\nbuffer_load_dwordx4 a[240:243], v79, s[24:27], s69 offen offset:0 sc1\nQmvaa 124,60,32\nds_read_b32 v66, v74\nQmvaa 156,56,8\ns_add_u32 s69, s98, 4096\n"
"buffer_load_dwordx4 a[228:231], v79, s[24:27], s69 offen offset:1024 sc1\nQmvaa 156,60,36\nQmvaa 188,56,12\ns_add_u32 s69, s98, 16384\nbuffer_load_dwordx4 a[244:247], v79, s[24:27], s69 offen offset:1024 sc1\nQmvaa 188,60,40\ns_waitcnt lgkmcnt(0)\nv_cmp_gt_u32 vcc, v75, v66\n"
"Qmvaa 220,56,16\ns_cbranch_vccnz .Lso2222r0\n.Lro2222r0:\nQmvaa 220,60,44\ns_add_u32 s69, s98, 4096\nbuffer_load_dwordx4 a[232:235], v79, s[24:27], s69 offen offset:2048 sc1\nQmvaa 252,56,20\nQmvaa 252,60,48\n"
"s_add_u32 s69, s98, 16384\nbuffer_load_dwordx4 a[248:251], v79, s[24:27], s69 offen offset:2048 sc1\nQmvaa 100,56,24\nQmvaa 100,60,52\ns_waitcnt vmcnt(10)\nQmvaa 92,202,208\nQmvaa 124,200,208\ns_waitcnt vmcnt(9)\n"
"Qmvaa 156,202,212\nQmvaa 188,200,212\ns_nop 3\nQk 92,92,93\nQk 93,94,95\nds_write_b64 v56, v[92:93] offset:64\ns_waitcnt vmcnt(8)\nQmvaa 220,202,216\n"
"Qk 124,124,125\nQk 125,126,127\nds_write_b64 v56, v[124:125] offset:3392\nQmvaa 252,200,216\nQk 156,156,157\nQk 157,158,159\nds_write_b64 v56, v[156:157] offset:6720\ns_waitcnt vmcnt(7)\n"
"Qmvaa 100,202,220\nQk 188,188,189\nQk 189,190,191\nds_write_b64 v56, v[188:189] offset:10048\nQmvaa 0,200,220\nQk 220,220,221\nQk 221,222,223\nds_write_b64 v56, v[220:221] offset:13376\n"
"s_waitcnt vmcnt(6)\nQmvaa 28,202,236\nQk 252,252,253\nQk 253,254,255\nds_write_b64 v56, v[252:253] offset:16704\ns_waitcnt vmcnt(5)\nQmvaa 4,202,224\nQk 100,100,101\n"
"Qk 101,102,103\nds_write_b64 v56, v[100:101] offset:20032\ns_waitcnt lgkmcnt(0)\ns_barrier\nQda 28,71,64\nQda 32,71,6720\nQda 36,71,13376\nQda 40,71,20032\n"
"Qmvaa 32,200,236\nQk 0,0,1\nQk 1,2,3\nds_write_b64 v56, v[0:1] offset:0\ns_mov_b32 s68, 0\ns_waitcnt lgkmcnt(4)\nbuffer_store_dwordx4 a[28:31], v72, s[40:43], s68 offen offset:0 sc1\nQmvaa 8,200,224\n"
"Qk 28,28,29\nQk 29,30,31\nds_write_b64 v56, v[28:29] offset:32\ns_mov_b32 s68, 81920\ns_waitcnt lgkmcnt(4)\nbuffer_store_dwordx4 a[32:35], v72, s[40:43], s68 offen offset:0 sc1\ns_waitcnt vmcnt(6)\nQmvaa 36,202,240\n"
"Qk 4,4,5\nQk 5,6,7\nds_write_b64 v56, v[4:5] offset:3328\ns_mov_b32 s68, 163840\ns_waitcnt lgkmcnt(4)\nbuffer_store_dwordx4 a[36:39], v72, s[40:43], s68 offen offset:0 sc1\ns_waitcnt vmcnt(6)\nQmvaa 12,202,228\n"
"Qk 32,32,33\nQk 33,34,35\nds_write_b64 v56, v[32:33] offset:3360\ns_mov_b32 s68, 245760\ns_cmp_eq_u32 s93, 1\ns_cselect_b32 s68, 0x7ffff000, s68\ns_waitcnt lgkmcnt(4)\nbuffer_store_dwordx4 a[40:43], v72, s[40:43], s68 offen offset:0 sc1\n"
"Qmvaa 40,200,240\nQk 8,8,9\nQk 9,10,11\nds_write_b64 v56, v[8:9] offset:6656\nQmvaa 16,200,228\nQk 36,36,37\nQk 37,38,39\nds_write_b64 v56, v[36:37] offset:6688\n"
"s_waitcnt vmcnt(6)\nQmvaa 44,202,244\nQk 12,12,13\nQk 13,14,15\nds_write_b64 v56, v[12:13] offset:9984\ns_waitcnt vmcnt(5)\nQmvaa 20,202,232\nQk 40,40,41\n"
"Qk 41,42,43\nds_write_b64 v56, v[40:41] offset:10016\ns_waitcnt lgkmcnt(0)\ns_barrier\nQda 0,57,0\nQda 4,57,3328\nQda 8,57,6656\nQda 12,57,9984\n"
"Qmvaa 48,200,244\nQk 16,16,17\nQk 17,18,19\nds_write_b64 v56, v[16:17] offset:13312\ns_mov_b32 s68, 0\ns_waitcnt lgkmcnt(4)\nbuffer_store_dwordx4 a[0:3], v64, s[40:43], s68 offen offset:0 sc1\nQmvaa 24,200,232\n"
"Qk 44,44,45\nQk 45,46,47\nds_write_b64 v56, v[44:45] offset:13344\ns_mov_b32 s68, 40960\ns_waitcnt lgkmcnt(4)\nbuffer_store_dwordx4 a[4:7], v64, s[40:43], s68 offen offset:0 sc1\ns_waitcnt vmcnt(6)\nQmvaa 52,202,248\n"
"Qk 20,20,21\nQk 21,22,23\nds_write_b64 v56, v[20:21] offset:16640\ns_mov_b32 s68, 81920\ns_waitcnt lgkmcnt(4)\nbuffer_store_dwordx4 a[8:11], v64, s[40:43], s68 offen offset:0 sc1\nQk 48,48,49\nQk 49,50,51\n"
"ds_write_b64 v56, v[48:49] offset:16672\ns_mov_b32 s68, 122880\ns_waitcnt lgkmcnt(4)\nbuffer_store_dwordx4 a[12:15], v64, s[40:43], s68 offen offset:0 sc1\nQk 24,24,25\nQk 25,26,27\nds_write_b64 v56, v[24:25] offset:19968\nQk 52,52,53\n"
"Qk 53,54,55\nds_write_b64 v56, v[52:53] offset:20000\ns_waitcnt lgkmcnt(0)\ns_barrier\nQda 16,57,13312\nQda 20,57,16640\nQda 24,57,19968\ns_mov_b32 s68, 163840\n"
"s_waitcnt lgkmcnt(2)\nbuffer_store_dwordx4 a[16:19], v64, s[40:43], s68 offen offset:0 sc1\ns_mov_b32 s68, 204800\ns_waitcnt lgkmcnt(1)\nbuffer_store_dwordx4 a[20:23], v64, s[40:43], s68 offen offset:0 sc1\ns_mov_b32 s68, 245760\ns_waitcnt lgkmcnt(0)\nbuffer_store_dwordx4 a[24:27], v64, s[40:43], s68 offen offset:0 sc1\n"
"s_branch .Lfin\n.Lrole1:\n.Lpg19:\ns_cbranch_execz .Lpg23\nQmvaa 96,64,88\nQta 56,69,49408\ns_lshl_b32 s46, s19, 4\nQmvav 96,68,168\n"
"buffer_load_dwordx4 a[84:87], v79, s[48:51], s31 offen offset:1024 sc1\nQmvaa 128,64,92\nds_read_b32 v66, v74\ns_add_u32 s46, s46, 128\nQmvav 128,68,200\nQta 58,69,50432\nv_mov_b32 v77, 0x7ffff000\nQmvaa 160,64,96\n"
"Qmvav 160,68,232\nbuffer_load_dwordx4 a[88:91], v79, s[48:51], s31 offen offset:2048 sc1\nQmvaa 192,64,100\ns_waitcnt lgkmcnt(1)\nv_cmp_gt_u32 vcc, v75, v66\nQmvav 192,68,212\ns_cbranch_vccnz .Lso1525r1\n.Lro1525r1:\n"
"Qmvaa 224,64,104\nQta 60,69,57600\nQmvav 224,68,244\nbuffer_load_dwordx4 a[92:95], v79, s[48:51], s31 offen offset:3072 sc1\ns_waitcnt vmcnt(31)\ns_waitcnt lgkmcnt(4)\nQmaav 140,72,84\nQmaav 140,76,116\n"
"Qta 62,69,58624\nQmaav 136,72,240\nQmaav 136,76,248\nQmaav 132,72,208\nQta 64,69,49664\ns_add_u32 m0, s96, 65536\ns_add_u32 s95, s67, 86016\nQmaav 132,76,216\n"
"buffer_load_dwordx4 v62, s[52:55], s95 offen lds\nQmaav 128,72,176\nQmaav 128,76,184\nQta 66,69,50688\nQmaav 124,72,144\nQmaav 124,76,152\nQmaav 120,72,112\nQta 68,69,57856\n"
"s_add_u32 s95, s67, 125952\nQmaav 120,76,120\nbuffer_load_dwordx4 v62, s[52:55], s95 offen offset:1024 lds\nQmaav 116,72,80\nQmaav 116,76,88\nQta 70,69,58880\ns_waitcnt lgkmcnt(4)\nQmaav 144,56,80\n"
"Qmaav 144,60,88\ns_add_u32 m0, s96, 73728\ns_add_u32 s95, s72, 86016\nQmaav 148,56,112\nbuffer_load_dwordx4 v62, s[52:55], s95 offen lds\nQmaav 148,60,120\nQmaav 152,56,144\nQta 72,69,49920\n"
"Qmaav 152,60,152\nQmaav 156,56,176\nQmaav 156,60,184\nQta 74,69,50944\ns_add_u32 s95, s72, 125952\nQmaav 160,56,208\nbuffer_load_dwordx4 v62, s[52:55], s95 offen offset:1024 lds\nQmaav 160,60,216\n"
"Qmaav 164,56,240\nQta 76,69,58112\nQmaav 164,60,248\nQmaav 168,56,84\nQmaav 168,60,116\nQta 78,69,59136\ns_add_u32 m0, s96, 81920\ns_add_u32 s95, s73, 86016\n"
"s_waitcnt lgkmcnt(4)\nQmvav 24,64,84\nbuffer_load_dwordx4 v62, s[52:55], s95 offen lds\nQmvav 24,68,116\nQmvav 20,64,240\nQmvav 20,68,248\nQmvav 16,64,208\nQta 56,68,0\n"
"s_add_u32 s95, s73, 125952\nQmvav 16,68,216\nbuffer_load_dwordx4 v62, s[52:55], s95 offen offset:1024 lds\nQmvav 12,64,176\nQmvav 12,68,184\nQta 58,68,512\nQmvav 8,64,144\nQmvav 8,68,152\n"
"Qmvav 4,64,112\nQta 60,68,1024\ns_add_u32 m0, s96, 90112\ns_add_u32 s95, s67, 86016\nQmvav 4,68,120\nbuffer_load_dwordx4 v61, s[52:55], s95 offen lds\nQmvav 0,64,80\nQmvav 0,68,88\n"
"Qta 62,68,1536\ns_waitcnt lgkmcnt(4)\nQmvav 28,72,80\nQmvav 28,76,88\ns_add_u32 s95, s67, 105472\nQmvav 32,72,112\nbuffer_load_dwordx4 v61, s[52:55], s95 offen offset:1024 lds\nQmvav 32,76,120\n"
"Qmvav 36,72,144\nQta 64,68,256\nQmvav 36,76,152\ns_add_u32 s31, s74, 135168\nQmvav 40,72,176\nbuffer_load_dwordx4 a[96:99], v79, s[48:51], s31 offen offset:0 sc1\nQmvav 40,76,184\nQta 66,68,768\n"
"Qmvav 44,72,208\nv_cmp_eq_u32 s[44:45], s46, v60\ns_nop 1\nv_cndmask_b32 v77, v77, v76, s[44:45]\nv_add_u32 v67, 1, v65\ns_xor_b32 s46, s19, 12\ns_lshl_b32 s28, 1, s46\nQmvav 44,76,216\n"
"Qmvav 48,72,240\nQta 68,68,1280\nQmvav 48,76,248\nbuffer_load_dwordx4 a[100:103], v79, s[48:51], s31 offen offset:1024 sc1\nv_add_u32 v75, 1, v75\nQmvav 52,72,84\ns_waitcnt vmcnt(35)\nds_write_b32 v73, v75\n"
"Qmvav 52,76,116\nQta 70,68,1792\ns_waitcnt lgkmcnt(5)\nQmaav 196,56,84\nQmaav 196,60,116\nbuffer_load_dwordx4 a[104:107], v79, s[48:51], s31 offen offset:2048 sc1\nQmaav 192,56,240\nQmaav 192,60,248\n"
"Qmaav 188,56,208\nQta 72,68,8192\ns_add_u32 s31, s74, 147456\nQmaav 188,60,216\nbuffer_load_dwordx4 a[108:111], v79, s[48:51], s31 offen offset:0 sc1\nQmaav 184,56,176\nds_read_b32 v66, v74\nQmaav 184,60,184\n"
"Qta 74,68,9216\nQmaav 180,56,144\nQmaav 180,60,152\nbuffer_load_dwordx4 a[112:115], v79, s[48:51], s31 offen offset:1024 sc1\nQmaav 176,56,112\ns_waitcnt lgkmcnt(1)\nv_cmp_gt_u32 vcc, v75, v66\nQmaav 176,60,120\n"
"s_cbranch_vccnz .Lso1595r1\n.Lro1595r1:\nQmaav 172,56,80\nQta 76,68,16384\nQmaav 172,60,88\nbuffer_load_dwordx4 v[168:171], v79, s[48:51], s31 offen offset:2048 sc1\ns_waitcnt lgkmcnt(4)\nQmvav 92,64,80\n"
"Qmvav 92,68,88\nQta 78,68,17408\nQmvav 124,64,112\nQmvav 124,68,120\nbuffer_load_dwordx4 v[200:203], v79, s[48:51], s31 offen offset:3072 sc1\nQmvav 156,64,144\nQta 56,68,8448\nQmvav 156,68,152\n"
"Qmvav 188,64,176\nQmvav 188,68,184\nQta 58,68,9472\ns_add_u32 s31, s74, 151552\nQmvav 220,64,208\nbuffer_load_dwordx4 v[232:235], v79, s[48:51], s31 offen offset:0 sc1\nQmvav 220,68,216\nQmvav 252,64,240\n"
"Qta 60,68,16640\nQmvav 252,68,248\nQmvav 100,64,84\nbuffer_load_dwordx4 v[212:215], v79, s[48:51], s31 offen offset:1024 sc1\nQmvav 100,68,116\nQta 62,68,17664\ns_waitcnt lgkmcnt(4)\nQmaav 224,72,84\n"
"Qmaav 224,76,116\nQmaav 220,72,240\nbuffer_load_dwordx4 v[244:247], v79, s[48:51], s31 offen offset:2048 sc1\nQmaav 220,76,248\nQmaav 216,72,208\nQta 64,68,8704\nQmaav 216,76,216\ns_add_u32 m0, s91, 0\n"
"s_add_u32 s31, s77, 131072\nQmaav 212,72,176\nbuffer_load_dwordx4 v79, s[48:51], s31 offen sc1 lds\nQmaav 212,76,184\nQta 66,68,9728\nQmaav 208,72,144\nQmaav 208,76,152\nQmaav 204,72,112\n"
"Qta 68,68,16896\nQmaav 204,76,120\nbuffer_load_dwordx4 v79, s[48:51], s31 offen offset:1024 sc1 lds\nQmaav 200,72,80\nQmaav 200,76,88\nQta 70,68,17920\ns_waitcnt lgkmcnt(4)\n.Lpg20:\n"
"s_cbranch_execz .Ltr8\nQmaav 228,56,80\nQmaav 228,60,88\nbuffer_load_dwordx4 v79, s[48:51], s31 offen offset:2048 sc1 lds\nQmaav 232,56,112\nQmaav 232,60,120\nQmaav 236,56,144\nQta 72,68,8960\n"
"Qmaav 236,60,152\nbuffer_load_dwordx4 v79, s[48:51], s31 offen offset:3072 sc1 lds\nQmaav 240,56,176\nQmaav 240,60,184\nQta 74,68,9984\nQmaav 244,56,208\ns_add_u32 m0, s91, 4096\ns_add_u32 s31, s77, 135168\n"
"Qmaav 244,60,216\nbuffer_load_dwordx4 v79, s[48:51], s31 offen sc1 lds\nQmaav 248,56,240\nQta 76,68,17152\nQmaav 248,60,248\nQmaav 252,56,84\nQmaav 252,60,116\nQta 78,68,18176\n"
"s_waitcnt lgkmcnt(4)\nQmvav 172,64,84\nbuffer_load_dwordx4 v79, s[48:51], s31 offen offset:1024 sc1 lds\nQmvav 172,68,116\nQmvav 140,64,240\nQmvav 140,68,248\nQmvav 108,64,208\nQta 56,68,24576\n"
"Qmvav 108,68,216\nbuffer_load_dwordx4 v79, s[48:51], s31 offen offset:2048 sc1 lds\nQmvav 228,64,176\nQmvav 228,68,184\nQta 58,68,25600\nQmvav 196,64,144\ns_add_u32 m0, s91, 33792\ns_add_u32 s31, s77, 147456\n"
"Qmvav 196,68,152\nbuffer_load_dwordx4 v79, s[48:51], s31 offen sc1 lds\nQmvav 164,64,112\nQta 60,68,32768\nQmvav 164,68,120\nv_add_u32 v75, 1, v75\nQmvav 132,64,80\ns_waitcnt vmcnt(47)\n"
"ds_write_b32 v73, v75\nQmvav 132,68,88\nQta 62,68,33792\ns_waitcnt lgkmcnt(5)\nQmvav 204,72,80\nbuffer_load_dwordx4 v79, s[48:51], s31 offen offset:1024 sc1 lds\nQmvav 204,76,88\nQmvav 236,72,112\n"
"Qmvav 236,76,120\nQmvav 96,72,144\nQta 64,68,24832\nQmvav 96,76,152\nbuffer_load_dwordx4 v79, s[48:51], s31 offen offset:2048 sc1 lds\nQmvav 128,72,176\nds_read_b32 v66, v74\nQmvav 128,76,184\n"
"Qta 66,68,25856\nQmvav 160,72,208\nQmvav 160,76,216\nbuffer_load_dwordx4 v79, s[48:51], s31 offen offset:3072 sc1 lds\nQmvav 192,72,240\ns_waitcnt lgkmcnt(1)\nv_cmp_gt_u32 vcc, v75, v66\nQmvav 192,76,248\n"
"s_cbranch_vccnz .Lso1665r1\n.Lro1665r1:\nQmvav 224,72,84\nQta 68,68,33024\ns_add_u32 m0, s91, 37888\ns_add_u32 s31, s77, 151552\nQmvav 224,76,116\nbuffer_load_dwordx4 v79, s[48:51], s31 offen sc1 lds\n"
"s_waitcnt vmcnt(36)\ns_waitcnt lgkmcnt(4)\nQmaaa 140,56,24\nQmaaa 140,60,52\nQta 70,68,34048\nQmaaa 136,56,20\nQmaaa 136,60,48\nbuffer_load_dwordx4 v79, s[48:51], s31 offen offset:1024 sc1 lds\n"
"Qmaaa 132,56,16\nQta 72,68,25088\nQmaaa 132,60,44\nQmaaa 128,56,12\nQmaaa 128,60,40\nQta 74,68,26112\nQmaaa 124,56,8\nbuffer_load_dwordx4 v79, s[48:51], s31 offen offset:2048 sc1 lds\n"
"Qmaaa 124,60,36\nQmaaa 120,56,4\nQta 76,68,33280\nQmaaa 120,60,32\nQmaaa 116,56,0\nQmaaa 116,60,28\nQta 78,68,34304\ns_add_u32 m0, s96, 0\n"
"s_add_u32 s95, s67, 126976\ns_waitcnt lgkmcnt(4)\nQmaaa 144,64,0\nbuffer_load_dwordx4 v61, s[52:55], s95 offen lds\nQmaaa 144,68,28\nQmaaa 148,64,4\nQmaaa 148,68,32\nQmaaa 152,64,8\n"
"Qta 56,68,25344\ns_add_u32 s95, s67, 146432\nQmaaa 152,68,36\nbuffer_load_dwordx4 v61, s[52:55], s95 offen offset:1024 lds\nQmaaa 156,64,12\nQmaaa 156,68,40\nQta 58,68,26368\nQmaaa 160,64,16\n"
"s_add_u32 s31, s75, 131072\nQmaaa 160,68,44\nbuffer_load_dwordx4 v[80:83], v79, s[48:51], s31 offen offset:0 sc1\nQmaaa 164,64,20\nQta 60,68,33536\nQmaaa 164,68,48\nQmaaa 168,64,24\nQmaaa 168,68,52\n"
"Qta 62,68,34560\ns_waitcnt lgkmcnt(4)\nQmvaa 24,72,24\nbuffer_load_dwordx4 v[112:115], v79, s[48:51], s31 offen offset:1024 sc1\nQmvaa 24,76,52\nQmvaa 20,72,20\nQmvaa 20,76,48\nQmvaa 16,72,16\n"
"Qta 64,68,40960\nQmvaa 16,76,44\nbuffer_load_dwordx4 v[144:147], v79, s[48:51], s31 offen offset:2048 sc1\nQmvaa 12,72,12\nQmvaa 12,76,40\nQta 66,68,41472\nQmvaa 8,72,8\nQmvaa 8,76,36\n"
"buffer_load_dwordx4 v[176:179], v79, s[48:51], s31 offen offset:3072 sc1\nQmvaa 4,72,4\nQta 68,68,41984\nQmvaa 4,76,32\nQmvaa 0,72,0\nQmvaa 0,76,28\nQta 70,68,42496\ns_add_u32 s31, s75, 135168\n"
"s_waitcnt lgkmcnt(4)\nQmvaa 28,56,0\nbuffer_load_dwordx4 v[208:211], v79, s[48:51], s31 offen offset:0 sc1\nQmvaa 28,60,28\nQmvaa 32,56,4\nQmvaa 32,60,32\nQmvaa 36,56,8\nQta 72,68,41216\n"
"Qmvaa 36,60,36\nbuffer_load_dwordx4 v[240:243], v79, s[48:51], s31 offen offset:1024 sc1\nQmvaa 40,56,12\nQmvaa 40,60,40\nQta 74,68,41728\nQmvaa 44,56,16\nQmvaa 44,60,44\nbuffer_load_dwordx4 v[84:87], v79, s[48:51], s31 offen offset:2048 sc1\n"
"Qmvaa 48,56,20\nQta 76,68,42240\nQmvaa 48,60,48\nv_add_u32 v75, 1, v75\nQmvaa 52,56,24\ns_waitcnt vmcnt(37)\nds_write_b32 v73, v75\nQmvaa 52,60,52\n"
"Qta 78,68,42752\ns_add_u32 s31, s75, 147456\ns_waitcnt lgkmcnt(5)\nQmaaa 196,64,24\nbuffer_load_dwordx4 v[88:91], v79, s[48:51], s31 offen offset:0 sc1\nQmaaa 196,68,52\nQmaaa 192,64,20\nQmaaa 192,68,48\n"
"Qmaaa 188,64,16\nQta 56,68,49152\nQmaaa 188,68,44\nbuffer_load_dwordx4 v[120:123], v79, s[48:51], s31 offen offset:1024 sc1\nQmaaa 184,64,12\nds_read_b32 v66, v74\nQmaaa 184,68,40\nQta 58,68,50176\n"
"Qmaaa 180,64,8\nQmaaa 180,68,36\nbuffer_load_dwordx4 v[152:155], v79, s[48:51], s31 offen offset:2048 sc1\nQmaaa 176,64,4\ns_waitcnt lgkmcnt(1)\nv_cmp_gt_u32 vcc, v75, v66\nQmaaa 176,68,32\ns_cbranch_vccnz .Lso1735r1\n"
".Lro1735r1:\nQmaaa 172,64,0\nQta 60,68,57344\nQmaaa 172,68,28\nbuffer_load_dwordx4 v[184:187], v79, s[48:51], s31 offen offset:3072 sc1\ns_waitcnt lgkmcnt(4)\nQmvaa 92,72,0\nQmvaa 92,76,28\n"
"Qta 62,68,58368\nQmvaa 124,72,4\ns_add_u32 s31, s75, 151552\nQmvaa 124,76,32\nbuffer_load_dwordx4 v[216:219], v79, s[48:51], s31 offen offset:0 sc1\nQmvaa 156,72,8\nQta 64,68,49408\nQmvaa 156,76,36\n"
"Qmvaa 188,72,12\nQmvaa 188,76,40\nQta 66,68,50432\nQmvaa 220,72,16\nbuffer_load_dwordx4 v[248:251], v79, s[48:51], s31 offen offset:1024 sc1\nQmvaa 220,76,44\nQmvaa 252,72,20\nQta 68,68,57600\n"
"Qmvaa 252,76,48\nQmvaa 100,72,24\nbuffer_load_dwordx4 v[116:119], v79, s[48:51], s31 offen offset:2048 sc1\nQmvaa 100,76,52\nQta 70,68,58624\ns_waitcnt lgkmcnt(4)\nQmaaa 224,56,24\nQmaaa 224,60,52\n"
"Qmaaa 220,56,20\ns_add_u32 m0, s96, 8192\ns_add_u32 s95, s72, 86016\nQmaaa 220,60,48\nbuffer_load_dwordx4 v61, s[52:55], s95 offen lds\nQmaaa 216,56,16\nQta 72,68,49664\nQmaaa 216,60,44\n"
"Qmaaa 212,56,12\nQmaaa 212,60,40\nQta 74,68,50688\ns_add_u32 s95, s72, 105472\nQmaaa 208,56,8\nbuffer_load_dwordx4 v61, s[52:55], s95 offen offset:1024 lds\nQmaaa 208,60,36\nQmaaa 204,56,4\n"
"Qta 76,68,57856\nQmaaa 204,60,32\nQmaaa 200,56,0\nQmaaa 200,60,28\nQta 78,68,58880\ns_add_u32 m0, s96, 16384\ns_add_u32 s95, s72, 126976\ns_waitcnt lgkmcnt(4)\n"
"Qmaaa 228,64,0\nbuffer_load_dwordx4 v61, s[52:55], s95 offen lds\nQmaaa 228,68,28\nQmaaa 232,64,4\nQmaaa 232,68,32\nQmaaa 236,64,8\nQta 56,68,49920\ns_add_u32 s95, s72, 146432\n"
"Qmaaa 236,68,36\nbuffer_load_dwordx4 v61, s[52:55], s95 offen offset:1024 lds\nQmaaa 240,64,12\nQmaaa 240,68,40\nQta 58,68,50944\nQmaaa 244,64,16\nQmaaa 244,68,44\nQmaaa 248,64,20\n"
"Qta 60,68,58112\ns_add_u32 m0, s96, 24576\ns_add_u32 s95, s73, 86016\nQmaaa 248,68,48\nbuffer_load_dwordx4 v61, s[52:55], s95 offen lds\nQmaaa 252,64,24\nQmaaa 252,68,52\nQta 62,68,59136\n"
"s_waitcnt lgkmcnt(4)\nQmvaa 172,72,24\nQmvaa 172,76,52\ns_add_u32 s95, s73, 105472\nQmvaa 140,72,20\nbuffer_load_dwordx4 v61, s[52:55], s95 offen offset:1024 lds\nQmvaa 140,76,48\nQmvaa 108,72,16\n"
"Qta 64,69,0\nQmvaa 108,76,44\nQmvaa 228,72,12\nQmvaa 228,76,40\nQta 66,69,512\ns_add_u32 m0, s96, 32768\ns_add_u32 s95, s73, 126976\nQmvaa 196,72,8\n"
"buffer_load_dwordx4 v61, s[52:55], s95 offen lds\nQmvaa 196,76,36\nQmvaa 164,72,4\nQta 68,69,1024\nQmvaa 164,76,32\nv_add_u32 v75, 1, v75\nQmvaa 132,72,0\ns_waitcnt vmcnt(21)\n"
"ds_write_b32 v73, v75\nQmvaa 132,76,28\nQta 70,69,1536\ns_add_u32 s95, s73, 146432\ns_waitcnt lgkmcnt(5)\nQmvaa 204,56,0\nbuffer_load_dwordx4 v61, s[52:55], s95 offen offset:1024 lds\nQmvaa 204,60,28\n"
"s_waitcnt vmcnt(24)\nQda 0,58,0\nQmvaa 236,56,4\nQmvaa 236,60,32\nQda 28,58,33792\nQmvaa 96,56,8\nQta 72,69,8192\nQmvaa 96,60,36\n"
"Qda 4,58,1024\nQmvaa 128,56,12\nds_read_b32 v66, v74\nQmvaa 128,60,40\nQta 74,69,8704\nQmvaa 160,56,16\nQda 32,58,34816\nQmvaa 160,60,44\n"
"Qmvaa 192,56,20\ns_waitcnt lgkmcnt(2)\nv_cmp_gt_u32 vcc, v75, v66\nQmvaa 192,60,48\ns_cbranch_vccnz .Lso1805r1\n.Lro1805r1:\nQmvaa 224,56,24\nQta 76,69,9216\n"
"s_add_u32 m0, s96, 40960\ns_add_u32 s95, s67, 86016\nQmvaa 224,60,52\nbuffer_load_dwordx4 v63, s[52:55], s95 offen lds\ns_waitcnt vmcnt(39)\ns_waitcnt lgkmcnt(8)\nQmaaa 172,64,80\nQda 8,58,2048\n"
"Qmaaa 172,68,108\nQta 78,69,9728\nQmaaa 176,64,84\nQda 36,58,35840\nQmaaa 176,68,112\nQmaaa 180,64,88\nQta 56,69,16384\ns_add_u32 s95, s67, 105472\n"
"Qmaav 180,68,168\nbuffer_load_dwordx4 v63, s[52:55], s95 offen offset:1024 lds\nQmaaa 184,64,92\nQda 12,58,3072\nQmaav 184,68,200\nQta 58,69,16896\nQmaaa 188,64,96\nQda 40,58,36864\n"
"Qmaav 188,68,232\nQmaaa 192,64,100\nQta 60,69,17408\ns_add_u32 m0, s96, 49152\ns_add_u32 s95, s67, 126976\nQmaav 192,68,212\nbuffer_load_dwordx4 v63, s[52:55], s95 offen lds\nQmaaa 196,64,104\n"
"Qda 16,58,4096\nQmaav 196,68,244\nQta 62,69,17920\ns_waitcnt vmcnt(11)\ns_waitcnt lgkmcnt(8)\nQmaav 172,72,80\nQda 44,58,37888\nQmaav 172,76,88\n"
"s_add_u32 s95, s67, 146432\nQmaav 176,72,112\nbuffer_load_dwordx4 v63, s[52:55], s95 offen offset:1024 lds\nQmaav 176,76,120\nQda 20,58,5120\nQmaav 180,72,144\nQta 64,69,24576\nv_add_u32 v75, 1, v75\n"
"Qmaav 180,76,152\ns_waitcnt vmcnt(4)\nds_write_b32 v73, v75\nQmaav 184,72,176\nQda 48,58,38912\nQmaav 184,76,184\nQta 66,69,25600\ns_add_u32 m0, s96, 57344\n"
"s_add_u32 s95, s72, 86016\nQmaav 188,72,208\nbuffer_load_dwordx4 v63, s[52:55], s95 offen lds\nQmaav 188,76,216\nQda 24,58,6144\nQmaav 192,72,240\nQta 68,68,0\nQmaav 192,76,248\n"
"Qda 52,58,39936\nQmaav 196,72,84\nQmaav 196,76,116\nds_read_b32 v66, v74\ns_waitcnt lgkmcnt(10)\nQmaaa 172,56,0\nQta 70,68,1024\ns_add_u32 s95, s72, 105472\n"
"Qmaaa 172,60,28\nbuffer_load_dwordx4 v63, s[52:55], s95 offen offset:1024 lds\nQmaaa 176,56,4\nQmaaa 176,60,32\ns_waitcnt lgkmcnt(1)\nv_cmp_gt_u32 vcc, v75, v66\ns_waitcnt lgkmcnt(11)\nQmaaa 180,56,8\n"
"s_cbranch_vccnz .Lso1840r1\n.Lro1840r1:\nQmaaa 180,60,36\nQta 72,68,8192\nQmaaa 184,56,12\nds_write_b128 v58, a[172:175] offset:0\nQmaaa 184,60,40\ns_waitcnt lgkmcnt(4)\n"
"Qmaaa 188,56,16\nQta 74,68,9216\nQmaaa 188,60,44\nds_write_b128 v58, a[176:179] offset:1024\nQmaaa 192,56,20\nQmaaa 192,60,48\nQta 76,68,16384\nQmaaa 196,56,24\n"
"ds_write_b128 v58, a[180:183] offset:2048\nQmaaa 196,60,52\ns_waitcnt lgkmcnt(6)\nQmaaa 116,64,80\nQta 78,68,17408\nQmaaa 116,68,108\nds_write_b128 v58, a[184:187] offset:3072\nQmaaa 120,64,84\n"
"Qmaaa 120,68,112\nQmaaa 124,64,88\nQta 56,68,24576\nQmaav 124,68,168\nds_write_b128 v58, a[188:191] offset:4096\nQmaaa 128,64,92\nQmaav 128,68,200\nQta 58,68,25600\n"
"Qmaaa 132,64,96\nds_write_b128 v58, a[192:195] offset:5120\nQmaav 132,68,232\nQmaaa 136,64,100\nQta 60,68,32768\nQmaav 136,68,212\nds_write_b128 v58, a[196:199] offset:6144\nQmaaa 140,64,104\n"
"Qmaav 140,68,244\nQta 62,68,33792\ns_waitcnt lgkmcnt(8)\nQmaav 116,72,80\nv_add_u32 v75, 1, v75\nQmaav 116,76,88\nds_write_b32 v73, v75\nQmaav 120,72,112\n"
"Qmaav 120,76,120\nQmaav 124,72,144\nQta 64,69,256\nQmaav 124,76,152\nQmaav 128,72,176\nQmaav 128,76,184\nQta 66,69,768\nQmaav 132,72,208\n"
"Qmaav 132,76,216\nds_read_b32 v66, v74\nQmaav 136,72,240\nQta 68,69,1280\nQmaav 136,76,248\nQmaav 140,72,84\nQmaav 140,76,116\ns_waitcnt lgkmcnt(1)\n"
"v_cmp_gt_u32 vcc, v75, v66\ns_waitcnt lgkmcnt(5)\nQmaaa 116,56,0\ns_cbranch_vccnz .Lso1878r1\n.Lro1878r1:\nQmaaa 116,60,28\nQta 70,69,1792\nQmaaa 120,56,4\n"
"Qmaaa 120,60,32\nQdv 148,59,2048\nQmaaa 124,56,8\nQta 72,69,8448\nQmaaa 124,60,36\ns_nop 0\nds_write_b128 v58, a[116:119] offset:33792\nQmaaa 128,56,12\n"
"Qdv 180,59,3072\nQmaaa 128,60,40\nQta 74,69,8960\nQmaaa 132,56,16\nds_write_b128 v58, a[120:123] offset:34816\nQmaaa 132,60,44\nQdv 104,59,5120\nQmaaa 136,56,20\n"
"Qta 76,69,9472\nQmaaa 136,60,48\nds_write_b128 v58, a[124:127] offset:35840\nQmaaa 140,56,24\nQdv 136,59,0\ns_waitcnt lgkmcnt(9)\nQa 156,156,148\nQmaaa 140,60,52\n"
"Qta 78,69,9984\nQa 157,157,149\ns_waitcnt lgkmcnt(10)\nQmvaa 92,64,80\nds_write_b128 v58, a[128:131] offset:36864\nQmvaa 92,68,108\nQa 158,158,150\nQa 159,159,151\n"
"Qmvaa 124,64,84\ns_waitcnt lgkmcnt(8)\nQa 188,188,180\nQa 189,189,181\nQmvaa 124,68,112\nds_write_b128 v58, a[132:135] offset:37888\nQmvaa 156,64,88\nQta 56,69,16640\n"
"Qa 190,190,182\nQa 191,191,183\nQmvav 156,68,168\nQdv 148,59,4096\ns_waitcnt lgkmcnt(8)\nQa 252,252,104\nQmvaa 188,64,92\nds_write_b128 v58, a[136:139] offset:38912\n"
"Qmvav 188,68,200\nQta 58,69,17152\nQa 253,253,105\nQmvaa 220,64,96\nQa 254,254,106\nQa 255,255,107\nQmvav 220,68,232\nQdv 180,59,6144\n"
"s_waitcnt lgkmcnt(8)\nQa 92,92,136\nQmvaa 252,64,100\nQta 60,69,17664\nQa 93,93,137\nQmvav 252,68,212\nds_write_b128 v58, a[140:143] offset:39936\nQmvaa 100,64,104\n"
".Lpg21:\ns_cbranch_execz .Ltr8\nQa 94,94,138\nQa 95,95,139\nQmvav 100,68,244\nQta 62,69,18176\ns_waitcnt lgkmcnt(6)\nQa 220,220,148\n"
"s_waitcnt lgkmcnt(6)\nQmvav 92,72,80\nQdv 104,59,1024\nv_add_u32 v75, 1, v75\nQmvav 92,76,88\nds_write_b32 v73, v75\nQa 221,221,149\nQmvav 124,72,112\n"
"Qa 222,222,150\nQa 223,223,151\nQmvav 124,76,120\ns_waitcnt lgkmcnt(5)\nQa 100,100,180\nQa 101,101,181\nQmvav 156,72,144\nQta 64,69,24832\n"
"Qa 102,102,182\nQmvav 156,76,152\nQa 103,103,183\nQmvav 188,72,176\nQmvav 188,76,184\nQta 66,69,25856\ns_waitcnt lgkmcnt(3)\nQa 124,124,104\n"
"Qmvav 220,72,208\nQa 125,125,105\nQa 126,126,106\nQmvav 220,76,216\nds_read_b32 v66, v74\nQa 127,127,107\nQmvav 252,72,240\nQta 68,68,256\n"
"Qmvav 252,76,248\nQmvav 100,72,84\nQmvav 100,76,116\ns_waitcnt lgkmcnt(1)\nv_cmp_gt_u32 vcc, v75, v66\ns_waitcnt lgkmcnt(6)\nQmvaa 92,56,0\ns_cbranch_vccnz .Lso1920r1\n"
".Lro1920r1:\nQmvaa 92,60,28\nQta 70,68,1280\ns_add_u32 m0, s96, 65536\ns_add_u32 s95, s72, 126976\nQmvaa 124,56,4\nbuffer_load_dwordx4 v63, s[52:55], s95 offen lds\nQmvaa 124,60,32\n"
"Qdv 136,59,33792\ns_add_u32 s68, s64, 0\nQmvaa 156,56,8\nQta 72,68,8448\nQk 92,92,93\nQmvaa 156,60,36\nQk 93,94,95\nQmvaa 188,56,12\n"
"Qdv 148,59,34816\nQk 94,124,125\nQmvaa 188,60,40\nQta 74,68,9472\ns_add_u32 s95, s72, 146432\nQmvaa 220,56,16\nbuffer_load_dwordx4 v63, s[52:55], s95 offen offset:1024 lds\nQmvaa 220,60,44\n"
"Qdv 180,59,35840\nQk 95,126,127\nQmvaa 252,56,20\nQta 76,68,16640\nbuffer_store_dwordx4 v[92:95], v79, s[24:27], s68 offen offset:0 sc1\nQmvaa 252,60,48\nv_add_u32 v75, 1, v75\ns_waitcnt lgkmcnt(5)\n"
"Qa 0,0,136\nQmvaa 100,56,24\nds_write_b32 v73, v75\nQa 1,1,137\nQmvaa 100,60,52\nQta 78,68,17664\ns_add_u32 m0, s96, 73728\ns_add_u32 s95, s73, 86016\n"
"s_waitcnt lgkmcnt(7)\nQmaaa 144,64,80\nbuffer_load_dwordx4 v63, s[52:55], s95 offen lds\nQmaaa 144,68,108\nQdv 104,59,36864\nQa 2,2,138\nQmaaa 148,64,84\nQa 3,3,139\n"
"s_waitcnt lgkmcnt(6)\nQa 4,4,148\nQmaaa 148,68,112\nQa 5,5,149\nQa 6,6,150\nQmaaa 152,64,88\nQta 56,68,24832\ns_add_u32 s95, s73, 105472\n"
"Qmaav 152,68,168\nbuffer_load_dwordx4 v63, s[52:55], s95 offen offset:1024 lds\nQmaaa 156,64,92\nds_read_b32 v66, v74\nQa 7,7,151\nQmaav 156,68,200\nQta 58,68,25856\ns_waitcnt lgkmcnt(7)\n"
"Qa 8,8,180\nQmaaa 160,64,96\nQdv 136,59,37888\nQa 9,9,181\nQmaav 160,68,232\nQa 10,10,182\nQa 11,11,183\nQmaaa 164,64,100\n"
"s_waitcnt lgkmcnt(2)\nv_cmp_gt_u32 vcc, v75, v66\nQmaav 164,68,212\ns_cbranch_vccnz .Lso1945r1\n.Lro1945r1:\nQmaaa 168,64,104\nQta 60,68,33024\ns_add_u32 m0, s96, 81920\n"
"s_add_u32 s95, s73, 126976\ns_waitcnt lgkmcnt(5)\nQa 12,12,104\nQa 13,13,105\nQa 14,14,106\nQa 15,15,107\nQmaav 168,68,244\nbuffer_load_dwordx4 v63, s[52:55], s95 offen lds\n"
"s_waitcnt lgkmcnt(10)\nQmaav 144,72,80\nQdv 148,59,38912\ns_waitcnt lgkmcnt(2)\nQa 16,16,136\nQmaav 144,76,88\nQta 62,68,34048\nQa 17,17,137\n"
"Qmaav 148,72,112\nQa 18,18,138\nQa 19,19,139\nQmaav 148,76,120\nQdv 180,59,39936\nQk 156,156,157\nQmaav 152,72,144\nQta 64,69,25088\n"
"s_add_u32 s95, s73, 146432\nQmaav 152,76,152\nbuffer_load_dwordx4 v63, s[52:55], s95 offen offset:1024 lds\nQmaav 156,72,176\ns_waitcnt lgkmcnt(3)\nQa 20,20,148\nQa 21,21,149\nQmaav 156,76,184\n"
"Qta 66,69,26112\nQa 22,22,150\nQmaav 160,72,208\nQa 23,23,151\nv_bfe_u32 v61, v60, 6, 2\nQmaav 160,76,216\nv_lshrrev_b32 v62, 8, v60\nv_xor_b32 v61, v61, v62\n"
"Qmaav 164,72,240\nQta 68,68,512\nv_bfe_u32 v62, v60, 5, 1\nQmaav 164,76,248\ns_waitcnt lgkmcnt(3)\nQa 24,24,180\nQa 25,25,181\nQmaav 168,72,84\n"
"Qa 26,26,182\nQa 27,27,183\nQmaav 168,76,116\nQta 70,68,1536\nv_lshl_or_b32 v61, v61, 1, v62\ns_waitcnt lgkmcnt(5)\nQmaaa 144,56,0\nv_and_b32 v62, 16, v60\n"
"v_mov_b32 v63, 0x3f80\nQmaaa 144,60,28\nv_lshlrev_b32 v63, v62, v63\nv_min_u32 v62, 1, v61\nQmaaa 148,56,4\nv_add_u32 v62, -1, v62\nv_and_b32 v62, v62, v63\nQmaaa 148,60,32\n"
"v_xor_b32 v61, 1, v61\nv_min_u32 v61, 1, v61\nQmaaa 152,56,8\nQta 72,68,8704\nv_add_u32 v61, -1, v61\nQmaaa 152,60,36\nds_write_b128 v58, a[144:147] offset:0\nQmaaa 156,56,12\n"
"v_and_b32 v61, v61, v63\nv_accvgpr_write_b32 a174, v62\nQmaaa 156,60,40\nQta 74,68,9728\nv_accvgpr_write_b32 a175, v61\nQmaaa 160,56,16\nds_write_b128 v58, a[148:151] offset:1024\nQmaaa 160,60,44\n"
"v_accvgpr_write_b32 a172, 0\nv_accvgpr_write_b32 a173, 0\nQmaaa 164,56,20\nQta 76,68,16896\nv_accvgpr_write_b32 a176, 0\nQmaaa 164,60,48\nds_write_b128 v58, a[152:155] offset:2048\nQmaaa 168,56,24\n"
"v_accvgpr_write_b32 a177, 0\nQk 157,158,159\nQmaaa 168,60,52\nQta 78,68,17920\nQk 158,188,189\ns_waitcnt lgkmcnt(7)\nQmvaa 0,64,80\nds_write_b128 v58, a[156:159] offset:3072\n"
"Qmvaa 0,68,108\nQk 159,190,191\nQk 220,220,221\nQmvaa 4,64,84\nbuffer_store_dwordx4 v[156:159], v79, s[24:27], s68 offen offset:1024 sc1\nQk 221,222,223\nQmvaa 4,68,112\nds_write_b128 v58, a[160:163] offset:4096\n"
"Qmvaa 8,64,88\nQta 56,68,25088\nQk 222,252,253\nQmvav 8,68,168\nQk 223,254,255\nQk 100,100,101\nQmvaa 12,64,92\nds_write_b128 v58, a[164:167] offset:5120\n"
"Qmvav 12,68,200\nQta 58,68,26112\nbuffer_store_dwordx4 v[220:223], v79, s[24:27], s68 offen offset:2048 sc1\nQmvaa 16,64,96\nQk 101,102,103\nQmvav 16,68,232\nds_write_b128 v58, a[168:171] offset:6144\nQmvaa 20,64,100\n"
"Qta 60,68,33280\nQmvav 20,68,212\nQmvaa 24,64,104\nQmvav 24,68,244\nQta 62,68,34304\ns_waitcnt lgkmcnt(8)\nQmvav 0,72,80\nv_add_u32 v75, 1, v75\n"
"Qmvav 0,76,88\ns_waitcnt vmcnt(11)\nds_write_b32 v73, v75\nQmvav 4,72,112\nQmvav 4,76,120\nQmvav 8,72,144\nQta 64,69,25344\nQmvav 8,76,152\n"
"Qmvav 12,72,176\nQmvav 12,76,184\nQta 66,69,26368\nQmvav 16,72,208\nQmvav 16,76,216\nds_read_b32 v66, v74\nQmvav 20,72,240\nQta 68,68,768\n"
"Qmvav 20,76,248\nQmvav 24,72,84\nQmvav 24,76,116\ns_waitcnt lgkmcnt(1)\nv_cmp_gt_u32 vcc, v75, v66\ns_waitcnt lgkmcnt(5)\nQmvaa 0,56,0\ns_cbranch_vccnz .Lso2004r1\n"
".Lro2004r1:\nQmvaa 0,60,28\nQta 70,68,1792\nQmvaa 4,56,4\nQmvaa 4,60,32\nQdv 104,59,2048\nQmvaa 8,56,8\nQta 72,68,8960\n"
"s_nop 1\nQk 102,0,1\nQmvaa 8,60,36\nQk 103,2,3\nQmvaa 12,56,12\nQdv 136,59,3072\nbuffer_store_dwordx4 v[100:103], v79, s[24:27], s68 offen offset:3072 sc1\nQmvaa 12,60,40\n"
"Qta 74,68,9984\nQk 4,4,5\nQmvaa 16,56,16\nQk 5,6,7\nQk 6,8,9\nQmvaa 16,60,44\nQdv 148,59,5120\nQk 7,10,11\n"
"Qmvaa 20,56,20\nQta 76,68,17152\ns_add_u32 s68, s64, 4096\nQmvaa 20,60,48\ns_waitcnt lgkmcnt(5)\nQa 36,36,104\nQa 37,37,105\nQmvaa 24,56,24\n"
"Qdv 180,59,0\nQa 38,38,106\nQmvaa 24,60,52\nQta 78,68,18176\nQa 39,39,107\ns_waitcnt lgkmcnt(11)\nQmvaa 28,64,80\ns_waitcnt lgkmcnt(5)\n"
"Qa 40,40,136\nQa 41,41,137\nQmvaa 28,68,108\nQa 42,42,138\nQa 43,43,139\nQmvaa 32,64,84\nbuffer_store_dwordx4 v[4:7], v79, s[24:27], s68 offen offset:0 sc1\nQk 12,12,13\n"
"Qmvaa 32,68,112\ns_waitcnt lgkmcnt(3)\nQa 48,48,148\nQa 49,49,149\nQmvaa 36,64,88\nQta 56,68,25344\nQa 50,50,150\nQmvav 36,68,168\n"
"Qdv 104,59,4096\nQa 51,51,151\nQmvaa 40,64,92\ns_waitcnt lgkmcnt(3)\nQa 28,28,180\nQa 29,29,181\nQmvav 40,68,200\nQta 58,68,26368\n"
"Qa 30,30,182\nQmvaa 44,64,96\nQa 31,31,183\nQk 13,14,15\nQmvav 44,68,232\nQdv 136,59,6144\nQk 14,16,17\nQmvaa 48,64,100\n"
"Qta 60,68,33536\nQk 15,18,19\nQmvav 48,68,212\nbuffer_store_dwordx4 v[12:15], v79, s[24:27], s68 offen offset:1024 sc1\nQk 20,20,21\nQmvaa 52,64,104\nQk 21,22,23\nQk 22,24,25\n"
"Qmvav 52,68,244\nQta 62,68,34560\nv_add_u32 v75, 1, v75\ns_waitcnt lgkmcnt(6)\nQmvav 28,72,80\ns_waitcnt vmcnt(5)\nds_write_b32 v73, v75\nQmvav 28,76,88\n"
"Qdv 148,59,1024\ns_waitcnt lgkmcnt(6)\nQa 44,44,104\nQmvav 32,72,112\nQa 45,45,105\nQa 46,46,106\nQmvav 32,76,120\nQa 47,47,107\n"
"s_waitcnt lgkmcnt(4)\nQa 52,52,136\nQmvav 36,72,144\nQta 64,68,40960\nQa 53,53,137\nQmvav 36,76,152\nQa 54,54,138\nQa 55,55,139\n"
"Qmvav 40,72,176\nQk 23,26,27\nQmvav 40,76,184\nQta 66,68,41984\ns_waitcnt lgkmcnt(2)\nQa 32,32,148\nQmvav 44,72,208\nds_read_b32 v66, v74\n"
"Qa 33,33,149\nQmvav 44,76,216\nQa 34,34,150\nQa 35,35,151\nQmvav 48,72,240\nQta 68,68,49152\nbuffer_store_dwordx4 v[20:23], v79, s[24:27], s68 offen offset:2048 sc1\nQmvav 48,76,248\n"
"Qmvav 52,72,84\ns_waitcnt lgkmcnt(1)\nv_cmp_gt_u32 vcc, v75, v66\nQmvav 52,76,116\ns_cbranch_vccnz .Lso2045r1\n.Lro2045r1:\ns_waitcnt lgkmcnt(6)\nQmvaa 28,56,0\n"
"Qta 70,68,50176\nQmvaa 28,60,28\nQmvaa 32,56,4\nQmvaa 32,60,32\nQmvaa 36,56,8\nQta 72,68,57344\ns_nop 3\nQk 28,28,29\n"
"Qmvaa 36,60,36\nQk 29,30,31\nQmvaa 40,56,12\nQk 30,32,33\nQk 31,34,35\nQmvaa 40,60,40\nQta 74,68,58368\nbuffer_store_dwordx4 v[28:31], v79, s[24:27], s68 offen offset:3072 sc1\n"
"Qmvaa 44,56,16\nQk 36,36,37\nQk 37,38,39\nQmvaa 44,60,44\ns_add_u32 s68, s64, 16384\nQmvaa 48,56,20\nQta 76,69,0\nQk 38,40,41\n"
"Qmvaa 48,60,48\nQk 39,42,43\nQmvaa 52,56,24\nbuffer_store_dwordx4 v[36:39], v79, s[24:27], s68 offen offset:0 sc1\nQk 44,44,45\nQmvaa 52,60,52\nQta 78,69,1024\nQk 45,46,47\n"
"s_waitcnt lgkmcnt(4)\nQmaaa 200,64,80\nQk 46,48,49\nQk 47,50,51\nQmaaa 200,68,108\nbuffer_store_dwordx4 v[44:47], v79, s[24:27], s68 offen offset:1024 sc1\nQmaaa 204,64,84\nQk 52,52,53\n"
"Qk 53,54,55\nQmaaa 204,68,112\nv_mov_b32 v54, 0\nv_mov_b32 v55, 0\nQmaaa 208,64,88\nQta 56,69,8192\nbuffer_store_dwordx4 v[52:55], v79, s[24:27], s68 offen offset:2048 sc1\nQmaav 208,68,168\n"
"Qmaaa 212,64,92\nQmaav 212,68,200\nQta 58,69,9216\nQmaaa 216,64,96\nQmaav 216,68,232\nQmaaa 220,64,100\nQta 60,69,16384\nQmaav 220,68,212\n"
"Qmaaa 224,64,104\nQmaav 224,68,244\nQta 62,69,17408\ns_waitcnt lgkmcnt(4)\nQmaav 200,72,80\nQmaav 200,76,88\nQmaav 204,72,112\nQmaav 204,76,120\n"
"Qmaav 208,72,144\nQta 64,68,41216\nQmaav 208,76,152\nQmaav 212,72,176\nQmaav 212,76,184\nQta 66,68,42240\nQmaav 216,72,208\nQmaav 216,76,216\n"
"Qmaav 220,72,240\nQta 68,68,49408\nQmaav 220,76,248\nQmaav 224,72,84\nQmaav 224,76,116\nQta 70,68,50432\ns_waitcnt lgkmcnt(4)\nQmaaa 200,56,0\n"
"Qmaaa 200,60,28\ns_waitcnt vmcnt(0)\nbuffer_store_dword v67, v77, s[24:27], 0 offen sc1\nQmaaa 204,56,4\nQmaaa 204,60,32\nQmaaa 208,56,8\nQta 72,68,57600\nQmaaa 208,60,36\n"
"s_nop 0\nds_write_b128 v58, a[200:203] offset:33792\nQmaaa 212,56,12\nQmaaa 212,60,40\nQta 74,68,58624\nQmaaa 216,56,16\nds_write_b128 v58, a[204:207] offset:34816\nQmaaa 216,60,44\n"
"Qmaaa 220,56,20\nQta 76,69,256\nQmaaa 220,60,48\nds_write_b128 v58, a[208:211] offset:35840\nQmaaa 224,56,24\nQmaaa 224,60,52\nQta 78,69,1280\ns_waitcnt lgkmcnt(7)\n"
"Qmaaa 228,64,80\nds_write_b128 v58, a[212:215] offset:36864\nQmaaa 228,68,108\nQmaaa 232,64,84\nQmaaa 232,68,112\nds_write_b128 v58, a[216:219] offset:37888\nQmaaa 236,64,88\nQta 56,69,8448\n"
"Qmaav 236,68,168\nQmaaa 240,64,92\nds_write_b128 v58, a[220:223] offset:38912\nQmaav 240,68,200\nQta 58,69,9472\nQmaaa 244,64,96\nQmaav 244,68,232\nds_write_b128 v58, a[224:227] offset:39936\n"
"Qmaaa 248,64,100\nQta 60,69,16640\nQmaav 248,68,212\nQmaaa 252,64,104\nQmaav 252,68,244\nQta 62,69,17664\ns_waitcnt lgkmcnt(8)\nQmaav 228,72,80\n"
"v_add_u32 v75, 1, v75\nQmaav 228,76,88\nds_write_b32 v73, v75\nQmaav 232,72,112\nQmaav 232,76,120\nQmaav 236,72,144\nQta 64,68,41472\nQmaav 236,76,152\n"
"Qmaav 240,72,176\nQmaav 240,76,184\nQta 66,68,42496\nQmaav 244,72,208\nQmaav 244,76,216\nds_read_b32 v66, v74\nQmaav 248,72,240\nQta 68,68,49664\n"
"Qmaav 248,76,248\nQmaav 252,72,84\nbuffer_load_dword v78, v76, s[24:27], 0 offen sc1\nQmaav 252,76,116\ns_waitcnt lgkmcnt(1)\nv_cmp_gt_u32 vcc, v75, v66\ns_waitcnt lgkmcnt(5)\nQmaaa 228,56,0\n"
"s_cbranch_vccnz .Lso2130r1\n.Lro2130r1:\nQmaaa 228,60,28\nQta 70,68,50688\nQmaaa 232,56,4\nQmaaa 232,60,32\nQdv 180,59,35840\nQmaaa 236,56,8\n"
"Qta 72,68,57856\nQmaaa 236,60,36\ns_nop 0\nds_write_b128 v58, a[228:231] offset:0\nQmaaa 240,56,12\nQdv 104,59,36864\nQmaaa 240,60,40\nQta 74,68,58880\n"
"Qmaaa 244,56,16\nds_write_b128 v58, a[232:235] offset:1024\nQmaaa 244,60,44\nQdv 136,59,38912\nQmaaa 248,56,20\nQta 76,69,512\nQmaaa 248,60,48\nds_write_b128 v58, a[236:239] offset:2048\n"
"Qmaaa 252,56,24\nQdv 148,59,33792\ns_waitcnt lgkmcnt(9)\nQa 196,196,180\nQmaaa 252,60,52\nQta 78,69,1536\nQa 197,197,181\ns_waitcnt lgkmcnt(10)\n"
"Qmvaa 132,64,80\n.Lpg22:\ns_cbranch_execz .Ltr8\nds_write_b128 v58, a[240:243] offset:3072\nQmvaa 132,68,108\nQa 198,198,182\nQa 199,199,183\nQmvaa 164,64,84\n"
"s_waitcnt lgkmcnt(8)\nQa 228,228,104\nQa 229,229,105\nQmvaa 164,68,112\nds_write_b128 v58, a[244:247] offset:4096\nQmvaa 196,64,88\nQta 56,69,8704\nQa 230,230,106\n"
"Qa 231,231,107\nQmvav 196,68,168\nQdv 180,59,37888\ns_waitcnt lgkmcnt(8)\nQa 140,140,136\nQmvaa 228,64,92\nds_write_b128 v58, a[248:251] offset:5120\nQmvav 228,68,200\n"
"Qta 58,69,9728\nQa 141,141,137\nQmvaa 108,64,96\nQa 142,142,138\nQa 143,143,139\nQmvav 108,68,232\nQdv 104,59,39936\ns_waitcnt lgkmcnt(8)\n"
"Qa 132,132,148\nQmvaa 140,64,100\nQta 60,69,16896\nQa 133,133,149\nQmvav 140,68,212\nds_write_b128 v58, a[252:255] offset:6144\nQmvaa 172,64,104\nQa 134,134,150\n"
"Qa 135,135,151\nQmvav 172,68,244\nQta 62,69,17920\ns_waitcnt lgkmcnt(6)\nQa 108,108,180\ns_waitcnt lgkmcnt(6)\nQmvav 132,72,80\nQdv 136,59,34816\n"
"v_add_u32 v75, 1, v75\nQmvav 132,76,88\nds_write_b32 v73, v75\nQa 109,109,181\nQmvav 164,72,112\nQa 110,110,182\nQa 111,111,183\nQmvav 164,76,120\n"
"s_waitcnt lgkmcnt(5)\nQa 172,172,104\nQa 173,173,105\nQmvav 196,72,144\nQta 64,68,41728\nQa 174,174,106\nQmvav 196,76,152\nQa 175,175,107\n"
"Qmvav 228,72,176\nQmvav 228,76,184\nQta 66,68,42752\ns_waitcnt lgkmcnt(3)\nQa 164,164,136\nQmvav 108,72,208\nQa 165,165,137\nQa 166,166,138\n"
"Qmvav 108,76,216\nds_read_b32 v66, v74\nQa 167,167,139\nQmvav 140,72,240\nQta 68,68,49920\nQmvav 140,76,248\nQmvav 172,72,84\nQmvav 172,76,116\n"
"s_waitcnt lgkmcnt(1)\nv_cmp_gt_u32 vcc, v75, v66\ns_waitcnt lgkmcnt(6)\nQmvaa 132,56,0\ns_cbranch_vccnz .Lso2172r1\n.Lro2172r1:\nQmvaa 132,60,28\ns_waitcnt vmcnt(0)\n"
"v_sub_u32 v66, v78, v65\nv_cmp_gt_u32 vcc, 1, v66\ns_and_b64 vcc, vcc, s[28:29]\ns_cbranch_scc1 .Lx1_w\n.Lx1_r:\nQmvaa 164,56,4\ns_add_u32 s69, s98, 0\nbuffer_load_dwordx4 a[180:183], v79, s[24:27], s69 offen offset:0 sc1\n"
"Qmvaa 164,60,32\nQta 70,68,50944\nQmvaa 196,56,8\nbuffer_load_dwordx4 a[192:195], v79, s[24:27], s69 offen offset:3072 sc1\nQmvaa 196,60,36\nQdv 148,59,2048\nQmvaa 228,56,12\ns_add_u32 s69, s98, 4096\n"
"buffer_load_dwordx4 a[196:199], v79, s[24:27], s69 offen offset:0 sc1\nQmvaa 228,60,40\nQta 72,68,58112\nQmvaa 108,56,16\ns_add_u32 s69, s98, 0\nbuffer_load_dwordx4 a[184:187], v79, s[24:27], s69 offen offset:1024 sc1\nQmvaa 108,60,44\nQdv 180,59,4096\n"
"Qmvaa 140,56,20\ns_add_u32 s69, s98, 4096\nbuffer_load_dwordx4 a[116:119], v79, s[24:27], s69 offen offset:1024 sc1\nQmvaa 140,60,48\nQta 74,68,59136\ns_waitcnt lgkmcnt(3)\nQa 96,96,148\nQmvaa 172,56,24\n"
"s_add_u32 s69, s98, 0\nbuffer_load_dwordx4 a[188:191], v79, s[24:27], s69 offen offset:2048 sc1\nQmvaa 172,60,52\nQdv 104,59,0\nQa 97,97,149\ns_waitcnt lgkmcnt(4)\nQmvaa 204,64,80\ns_add_u32 s69, s98, 4096\n"
"buffer_load_dwordx4 a[120:123], v79, s[24:27], s69 offen offset:2048 sc1\nQmvaa 204,68,108\nQta 76,69,768\nQa 98,98,150\nQmvaa 236,64,84\nQdv 136,59,1024\nQa 99,99,151\nQmvaa 236,68,112\n"
"s_waitcnt lgkmcnt(4)\nQa 160,160,180\nQa 161,161,181\nQmvaa 96,64,88\nQta 78,69,1792\nQa 162,162,182\nQmvav 96,68,168\nQdv 148,59,3072\n"
"Qa 163,163,183\nQmvaa 128,64,92\ns_waitcnt lgkmcnt(4)\nQa 204,204,104\nQa 205,205,105\nQmvav 128,68,200\nQta 56,69,8960\nQa 206,206,106\n"
"Qmvaa 160,64,96\nQdv 180,59,5120\nQa 207,207,107\nQmvav 160,68,232\nQmvaa 192,64,100\nQta 58,69,9984\ns_waitcnt lgkmcnt(5)\nQa 236,236,136\n"
"Qmvav 192,68,212\nQa 237,237,137\nQa 238,238,138\nQmvaa 224,64,104\nQa 239,239,139\nQmvav 224,68,244\nQta 60,69,17152\ns_waitcnt lgkmcnt(4)\n"
"Qa 128,128,148\ns_waitcnt lgkmcnt(4)\nQmvav 204,72,80\nQa 129,129,149\nQa 130,130,150\nQmvav 204,76,88\nQa 131,131,151\nQmvav 236,72,112\n"
"Qta 62,69,18176\ns_waitcnt lgkmcnt(3)\nQa 192,192,180\nQmvav 236,76,120\nQa 193,193,181\nQa 194,194,182\nQmvav 96,72,144\nQa 195,195,183\n"
"Qmvav 96,76,152\nQmvav 128,72,176\nQmvav 128,76,184\nQmvav 160,72,208\nQmvav 160,76,216\nQdv 104,59,6144\nQmvav 192,72,240\nQmvav 192,76,248\n"
"v_add_u32 v75, 1, v75\nQmvav 224,72,84\nds_write_b32 v73, v75\nQmvav 224,76,116\ns_waitcnt lgkmcnt(2)\nQmvaa 204,56,0\nQmvaa 204,60,28\nQmvaa 236,56,4\n"
"Qmvaa 236,60,32\ns_waitcnt lgkmcnt(1)\ns_nop 1\nQa 224,224,104\nQa 225,225,105\nQmvaa 96,56,8\nQa 226,226,106\nQa 227,227,107\n"
"Qmvaa 96,60,36\nQmvaa 128,56,12\nds_read_b32 v66, v74\nQmvaa 128,60,40\nQmvaa 160,56,16\nQmvaa 160,60,44\nQmvaa 192,56,20\ns_waitcnt lgkmcnt(0)\n"
"v_cmp_gt_u32 vcc, v75, v66\nQmvaa 192,60,48\ns_cbranch_vccnz .Lso2225r1\n.Lro2225r1:\nQmvaa 224,56,24\nQmvaa 224,60,52\ns_waitcnt vmcnt(6)\nQmvaa 132,174,180\n"
"s_waitcnt vmcnt(5)\nQmvaa 204,172,192\nQmvaa 164,172,180\ns_waitcnt vmcnt(4)\nQmvaa 236,174,196\ns_nop 2\nQk 132,132,133\nQk 133,134,135\n"
"ds_write_b64 v56, v[132:133] offset:0\ns_waitcnt vmcnt(3)\nQmvaa 196,174,184\nQk 204,204,205\nQk 205,206,207\nds_write_b64 v56, v[204:205] offset:32\nQmvaa 96,172,196\nQk 164,164,165\n"
"Qk 165,166,167\nds_write_b64 v56, v[164:165] offset:3328\nQmvaa 228,172,184\nQk 236,236,237\nQk 237,238,239\nds_write_b64 v56, v[236:237] offset:3360\ns_waitcnt lgkmcnt(0)\ns_barrier\n"
"Qda 0,57,0\nQda 4,57,3328\ns_waitcnt vmcnt(2)\nQmvaa 128,174,116\nQk 196,196,197\nQk 197,198,199\nds_write_b64 v56, v[196:197] offset:6656\ns_mov_b32 s68, 0\n"
"s_waitcnt lgkmcnt(2)\nbuffer_store_dwordx4 a[0:3], v64, s[40:43], s68 offen offset:0 sc1\ns_waitcnt vmcnt(2)\nQmvaa 108,174,188\nQk 96,96,97\nQk 97,98,99\nds_write_b64 v56, v[96:97] offset:6688\ns_mov_b32 s68, 40960\n"
"s_waitcnt lgkmcnt(2)\nbuffer_store_dwordx4 a[4:7], v64, s[40:43], s68 offen offset:0 sc1\nQmvaa 160,172,116\nQk 228,228,229\nQk 229,230,231\nds_write_b64 v56, v[228:229] offset:9984\nQmvaa 140,172,188\nQk 128,128,129\n"
"Qk 129,130,131\nds_write_b64 v56, v[128:129] offset:10016\ns_waitcnt vmcnt(2)\nQmvaa 192,174,120\nQk 108,108,109\nQk 109,110,111\nds_write_b64 v56, v[108:109] offset:13312\nQmvaa 172,174,192\n"
"Qk 160,160,161\nQk 161,162,163\nds_write_b64 v56, v[160:161] offset:13344\ns_waitcnt lgkmcnt(0)\ns_barrier\nQda 8,57,6656\nQda 12,57,9984\nQda 16,57,13312\n"
"Qmvaa 224,172,120\nQk 140,140,141\nQk 141,142,143\nds_write_b64 v56, v[140:141] offset:16640\ns_mov_b32 s68, 81920\ns_waitcnt lgkmcnt(3)\nbuffer_store_dwordx4 a[8:11], v64, s[40:43], s68 offen offset:0 sc1\nQk 192,192,193\n"
"Qk 193,194,195\nds_write_b64 v56, v[192:193] offset:16672\ns_mov_b32 s68, 122880\ns_waitcnt lgkmcnt(3)\nbuffer_store_dwordx4 a[12:15], v64, s[40:43], s68 offen offset:0 sc1\nQk 172,172,173\nQk 173,174,175\nds_write_b64 v56, v[172:173] offset:19968\n"
"s_mov_b32 s68, 163840\ns_waitcnt lgkmcnt(3)\nbuffer_store_dwordx4 a[16:19], v64, s[40:43], s68 offen offset:0 sc1\nQk 224,224,225\nQk 225,226,227\nds_write_b64 v56, v[224:225] offset:20000\ns_waitcnt lgkmcnt(0)\ns_barrier\n"
"Qda 20,57,16640\nQda 24,57,19968\ns_mov_b32 s68, 204800\ns_waitcnt lgkmcnt(1)\nbuffer_store_dwordx4 a[20:23], v64, s[40:43], s68 offen offset:0 sc1\ns_mov_b32 s68, 245760\ns_waitcnt lgkmcnt(0)\nbuffer_store_dwordx4 a[24:27], v64, s[40:43], s68 offen offset:0 sc1\n"
".Lfin:\n.Lpg23:\ns_cbranch_execz .Ltr8\ns_waitcnt vmcnt(0) lgkmcnt(0)\ns_branch .Lexit\n.Lend:\n.Lfb:\ns_waitcnt vmcnt(0) lgkmcnt(0)\n"
"v_lshrrev_b32 v52, 4, v60\nv_and_b32 v53, 15, v52\nv_lshrrev_b32 v52, 4, v52\nv_lshlrev_b32 v66, 8, v53\nv_lshl_add_u32 v66, v52, 4, v66\nv_add_u32 v66, 1024, v66\nv_lshlrev_b32 v67, 10, v52\nv_lshl_add_u32 v67, v53, 1, v67\n"
"v_add_u32 v67, 1024, v67\nv_mov_b32 v52, 0x3f803f80\nv_accvgpr_write_b32 a204, v52\nv_accvgpr_write_b32 a205, v52\nv_accvgpr_write_b32 a206, v52\nv_accvgpr_write_b32 a207, v52\nv_mov_b32 v24, 0xff800000\nv_mov_b32 v28, 0xff800000\n"
"v_mov_b32 v32, 0xff800000\nv_mov_b32 v36, 0xff800000\nv_mov_b32 v40, 0xff800000\nv_mov_b32 v44, 0xff800000\nv_mov_b32 v48, 0xff800000\ns_lshl_b32 s89, s67, 21\ns_lshl_b32 s68, s93, 18\ns_add_u32 s89, s89, s68\n"
"s_mov_b32 s66, 0\n.Lfb1:\ns_add_u32 s68, s89, 4096\nbuffer_load_dwordx4 a[112:115], v66, s[40:43], s89 offen offset:0\nbuffer_load_dwordx4 a[116:119], v66, s[40:43], s89 offen offset:64\nbuffer_load_dwordx4 a[120:123], v66, s[40:43], s89 offen offset:128\nbuffer_load_dwordx4 a[124:127], v66, s[40:43], s89 offen offset:192\nbuffer_load_dwordx4 a[128:131], v66, s[40:43], s68 offen offset:0\n"
"buffer_load_dwordx4 a[132:135], v66, s[40:43], s68 offen offset:64\nbuffer_load_dwordx4 a[136:139], v66, s[40:43], s68 offen offset:128\nbuffer_load_dwordx4 a[140:143], v66, s[40:43], s68 offen offset:192\ns_waitcnt vmcnt(0)\nQzvaa 0,112,0\nQmvaa 0,116,4\nQmvaa 0,120,8\nQmvaa 0,124,12\n"
"Qzvaa 4,128,0\nQmvaa 4,132,4\nQmvaa 4,136,8\nQmvaa 4,140,12\ns_nop 7\nv_max3_f32 v24, v24, v0, v1\nv_max3_f32 v24, v24, v2, v3\nv_max3_f32 v24, v24, v4, v5\n"
"v_max3_f32 v24, v24, v6, v7\nQzvaa 0,112,16\nQmvaa 0,116,20\nQmvaa 0,120,24\nQmvaa 0,124,28\nQzvaa 4,128,16\nQmvaa 4,132,20\nQmvaa 4,136,24\n"
"Qmvaa 4,140,28\ns_nop 7\nv_max3_f32 v28, v28, v0, v1\nv_max3_f32 v28, v28, v2, v3\nv_max3_f32 v28, v28, v4, v5\nv_max3_f32 v28, v28, v6, v7\nQzvaa 0,112,32\nQmvaa 0,116,36\n"
"Qmvaa 0,120,40\nQmvaa 0,124,44\nQzvaa 4,128,32\nQmvaa 4,132,36\nQmvaa 4,136,40\nQmvaa 4,140,44\ns_nop 7\nv_max3_f32 v32, v32, v0, v1\n"
"v_max3_f32 v32, v32, v2, v3\nv_max3_f32 v32, v32, v4, v5\nv_max3_f32 v32, v32, v6, v7\nQzvaa 0,112,48\nQmvaa 0,116,52\nQmvaa 0,120,56\nQmvaa 0,124,60\nQzvaa 4,128,48\n"
"Qmvaa 4,132,52\nQmvaa 4,136,56\nQmvaa 4,140,60\ns_nop 7\nv_max3_f32 v36, v36, v0, v1\nv_max3_f32 v36, v36, v2, v3\nv_max3_f32 v36, v36, v4, v5\nv_max3_f32 v36, v36, v6, v7\n"
"Qzvaa 0,112,64\nQmvaa 0,116,68\nQmvaa 0,120,72\nQmvaa 0,124,76\nQzvaa 4,128,64\nQmvaa 4,132,68\nQmvaa 4,136,72\nQmvaa 4,140,76\n"
"s_nop 7\nv_max3_f32 v40, v40, v0, v1\nv_max3_f32 v40, v40, v2, v3\nv_max3_f32 v40, v40, v4, v5\nv_max3_f32 v40, v40, v6, v7\nQzvaa 0,112,80\nQmvaa 0,116,84\nQmvaa 0,120,88\n"
"Qmvaa 0,124,92\nQzvaa 4,128,80\nQmvaa 4,132,84\nQmvaa 4,136,88\nQmvaa 4,140,92\ns_nop 7\nv_max3_f32 v44, v44, v0, v1\nv_max3_f32 v44, v44, v2, v3\n"
"v_max3_f32 v44, v44, v4, v5\nv_max3_f32 v44, v44, v6, v7\nQzvaa 0,112,96\nQmvaa 0,116,100\nQmvaa 0,120,104\nQmvaa 0,124,108\nQzvaa 4,128,96\nQmvaa 4,132,100\n"
"Qmvaa 4,136,104\nQmvaa 4,140,108\ns_nop 7\nv_max3_f32 v48, v48, v0, v1\nv_max3_f32 v48, v48, v2, v3\nv_max3_f32 v48, v48, v4, v5\nv_max3_f32 v48, v48, v6, v7\ns_add_u32 s89, s89, 8192\n"
"s_add_u32 s66, s66, 1\ns_cmp_lt_u32 s66, 32\ns_cbranch_scc1 .Lfb1\nv_mov_b32 v25, v24\ns_nop 1\nv_permlane32_swap_b32 v24, v25\nv_max_f32 v24, v24, v25\nv_mov_b32 v25, v24\n"
"s_nop 1\nv_permlane16_swap_b32 v24, v25\nv_max_f32 v24, v24, v25\nv_sub_f32 v24, 0, v24\nv_mov_b32 v25, v24\nv_mov_b32 v26, v24\nv_mov_b32 v27, v24\nv_mov_b32 v29, v28\n"
"s_nop 1\nv_permlane32_swap_b32 v28, v29\nv_max_f32 v28, v28, v29\nv_mov_b32 v29, v28\ns_nop 1\nv_permlane16_swap_b32 v28, v29\nv_max_f32 v28, v28, v29\nv_sub_f32 v28, 0, v28\n"
"v_mov_b32 v29, v28\nv_mov_b32 v30, v28\nv_mov_b32 v31, v28\nv_mov_b32 v33, v32\ns_nop 1\nv_permlane32_swap_b32 v32, v33\nv_max_f32 v32, v32, v33\nv_mov_b32 v33, v32\n"
"s_nop 1\nv_permlane16_swap_b32 v32, v33\nv_max_f32 v32, v32, v33\nv_sub_f32 v32, 0, v32\nv_mov_b32 v33, v32\nv_mov_b32 v34, v32\nv_mov_b32 v35, v32\nv_mov_b32 v37, v36\n"
"s_nop 1\nv_permlane32_swap_b32 v36, v37\nv_max_f32 v36, v36, v37\nv_mov_b32 v37, v36\ns_nop 1\nv_permlane16_swap_b32 v36, v37\nv_max_f32 v36, v36, v37\nv_sub_f32 v36, 0, v36\n"
"v_mov_b32 v37, v36\nv_mov_b32 v38, v36\nv_mov_b32 v39, v36\nv_mov_b32 v41, v40\ns_nop 1\nv_permlane32_swap_b32 v40, v41\nv_max_f32 v40, v40, v41\nv_mov_b32 v41, v40\n"
"s_nop 1\nv_permlane16_swap_b32 v40, v41\nv_max_f32 v40, v40, v41\nv_sub_f32 v40, 0, v40\nv_mov_b32 v41, v40\nv_mov_b32 v42, v40\nv_mov_b32 v43, v40\nv_mov_b32 v45, v44\n"
"s_nop 1\nv_permlane32_swap_b32 v44, v45\nv_max_f32 v44, v44, v45\nv_mov_b32 v45, v44\ns_nop 1\nv_permlane16_swap_b32 v44, v45\nv_max_f32 v44, v44, v45\nv_sub_f32 v44, 0, v44\n"
"v_mov_b32 v45, v44\nv_mov_b32 v46, v44\nv_mov_b32 v47, v44\nv_mov_b32 v49, v48\ns_nop 1\nv_permlane32_swap_b32 v48, v49\nv_max_f32 v48, v48, v49\nv_mov_b32 v49, v48\n"
"s_nop 1\nv_permlane16_swap_b32 v48, v49\nv_max_f32 v48, v48, v49\nv_sub_f32 v48, 0, v48\nv_mov_b32 v49, v48\nv_mov_b32 v50, v48\nv_mov_b32 v51, v48\nv_mov_b32 v52, 0\n"
"v_mov_b32 v80, 0\nv_mov_b32 v81, 0\nv_mov_b32 v82, 0\nv_mov_b32 v83, 0\nv_mov_b32 v84, 0\nv_mov_b32 v85, 0\nv_mov_b32 v86, 0\nv_mov_b32 v87, 0\n"
"v_mov_b32 v88, 0\nv_mov_b32 v89, 0\nv_mov_b32 v90, 0\nv_mov_b32 v91, 0\nv_mov_b32 v92, 0\nv_mov_b32 v93, 0\nv_mov_b32 v94, 0\nv_mov_b32 v95, 0\n"
"v_mov_b32 v96, 0\nv_mov_b32 v97, 0\nv_mov_b32 v98, 0\nv_mov_b32 v99, 0\nv_mov_b32 v100, 0\nv_mov_b32 v101, 0\nv_mov_b32 v102, 0\nv_mov_b32 v103, 0\n"
"v_mov_b32 v104, 0\nv_mov_b32 v105, 0\nv_mov_b32 v106, 0\nv_mov_b32 v107, 0\nv_mov_b32 v108, 0\nv_mov_b32 v109, 0\nv_mov_b32 v110, 0\nv_mov_b32 v111, 0\n"
"v_accvgpr_write_b32 a176, v52\nv_accvgpr_write_b32 a177, v52\nv_accvgpr_write_b32 a178, v52\nv_accvgpr_write_b32 a179, v52\nv_mov_b32 v112, 0\nv_mov_b32 v113, 0\nv_mov_b32 v114, 0\nv_mov_b32 v115, 0\n"
"v_mov_b32 v116, 0\nv_mov_b32 v117, 0\nv_mov_b32 v118, 0\nv_mov_b32 v119, 0\nv_mov_b32 v120, 0\nv_mov_b32 v121, 0\nv_mov_b32 v122, 0\nv_mov_b32 v123, 0\n"
"v_mov_b32 v124, 0\nv_mov_b32 v125, 0\nv_mov_b32 v126, 0\nv_mov_b32 v127, 0\nv_mov_b32 v128, 0\nv_mov_b32 v129, 0\nv_mov_b32 v130, 0\nv_mov_b32 v131, 0\n"
"v_mov_b32 v132, 0\nv_mov_b32 v133, 0\nv_mov_b32 v134, 0\nv_mov_b32 v135, 0\nv_mov_b32 v136, 0\nv_mov_b32 v137, 0\nv_mov_b32 v138, 0\nv_mov_b32 v139, 0\n"
"v_mov_b32 v140, 0\nv_mov_b32 v141, 0\nv_mov_b32 v142, 0\nv_mov_b32 v143, 0\nv_accvgpr_write_b32 a180, v52\nv_accvgpr_write_b32 a181, v52\nv_accvgpr_write_b32 a182, v52\nv_accvgpr_write_b32 a183, v52\n"
"v_mov_b32 v144, 0\nv_mov_b32 v145, 0\nv_mov_b32 v146, 0\nv_mov_b32 v147, 0\nv_mov_b32 v148, 0\nv_mov_b32 v149, 0\nv_mov_b32 v150, 0\nv_mov_b32 v151, 0\n"
"v_mov_b32 v152, 0\nv_mov_b32 v153, 0\nv_mov_b32 v154, 0\nv_mov_b32 v155, 0\nv_mov_b32 v156, 0\nv_mov_b32 v157, 0\nv_mov_b32 v158, 0\nv_mov_b32 v159, 0\n"
"v_mov_b32 v160, 0\nv_mov_b32 v161, 0\nv_mov_b32 v162, 0\nv_mov_b32 v163, 0\nv_mov_b32 v164, 0\nv_mov_b32 v165, 0\nv_mov_b32 v166, 0\nv_mov_b32 v167, 0\n"
"v_mov_b32 v168, 0\nv_mov_b32 v169, 0\nv_mov_b32 v170, 0\nv_mov_b32 v171, 0\nv_mov_b32 v172, 0\nv_mov_b32 v173, 0\nv_mov_b32 v174, 0\nv_mov_b32 v175, 0\n"
"v_accvgpr_write_b32 a184, v52\nv_accvgpr_write_b32 a185, v52\nv_accvgpr_write_b32 a186, v52\nv_accvgpr_write_b32 a187, v52\nv_mov_b32 v176, 0\nv_mov_b32 v177, 0\nv_mov_b32 v178, 0\nv_mov_b32 v179, 0\n"
"v_mov_b32 v180, 0\nv_mov_b32 v181, 0\nv_mov_b32 v182, 0\nv_mov_b32 v183, 0\nv_mov_b32 v184, 0\nv_mov_b32 v185, 0\nv_mov_b32 v186, 0\nv_mov_b32 v187, 0\n"
"v_mov_b32 v188, 0\nv_mov_b32 v189, 0\nv_mov_b32 v190, 0\nv_mov_b32 v191, 0\nv_mov_b32 v192, 0\nv_mov_b32 v193, 0\nv_mov_b32 v194, 0\nv_mov_b32 v195, 0\n"
"v_mov_b32 v196, 0\nv_mov_b32 v197, 0\nv_mov_b32 v198, 0\nv_mov_b32 v199, 0\nv_mov_b32 v200, 0\nv_mov_b32 v201, 0\nv_mov_b32 v202, 0\nv_mov_b32 v203, 0\n"
"v_mov_b32 v204, 0\nv_mov_b32 v205, 0\nv_mov_b32 v206, 0\nv_mov_b32 v207, 0\nv_accvgpr_write_b32 a188, v52\nv_accvgpr_write_b32 a189, v52\nv_accvgpr_write_b32 a190, v52\nv_accvgpr_write_b32 a191, v52\n"
"v_mov_b32 v208, 0\nv_mov_b32 v209, 0\nv_mov_b32 v210, 0\nv_mov_b32 v211, 0\nv_mov_b32 v212, 0\nv_mov_b32 v213, 0\nv_mov_b32 v214, 0\nv_mov_b32 v215, 0\n"
"v_mov_b32 v216, 0\nv_mov_b32 v217, 0\nv_mov_b32 v218, 0\nv_mov_b32 v219, 0\nv_mov_b32 v220, 0\nv_mov_b32 v221, 0\nv_mov_b32 v222, 0\nv_mov_b32 v223, 0\n"
"v_mov_b32 v224, 0\nv_mov_b32 v225, 0\nv_mov_b32 v226, 0\nv_mov_b32 v227, 0\nv_mov_b32 v228, 0\nv_mov_b32 v229, 0\nv_mov_b32 v230, 0\nv_mov_b32 v231, 0\n"
"v_mov_b32 v232, 0\nv_mov_b32 v233, 0\nv_mov_b32 v234, 0\nv_mov_b32 v235, 0\nv_mov_b32 v236, 0\nv_mov_b32 v237, 0\nv_mov_b32 v238, 0\nv_mov_b32 v239, 0\n"
"v_accvgpr_write_b32 a192, v52\nv_accvgpr_write_b32 a193, v52\nv_accvgpr_write_b32 a194, v52\nv_accvgpr_write_b32 a195, v52\nv_mov_b32 v240, 0\nv_mov_b32 v241, 0\nv_mov_b32 v242, 0\nv_mov_b32 v243, 0\n"
"v_mov_b32 v244, 0\nv_mov_b32 v245, 0\nv_mov_b32 v246, 0\nv_mov_b32 v247, 0\nv_mov_b32 v248, 0\nv_mov_b32 v249, 0\nv_mov_b32 v250, 0\nv_mov_b32 v251, 0\n"
"v_mov_b32 v252, 0\nv_mov_b32 v253, 0\nv_mov_b32 v254, 0\nv_mov_b32 v255, 0\nv_accvgpr_write_b32 a208, v52\nv_accvgpr_write_b32 a209, v52\nv_accvgpr_write_b32 a210, v52\nv_accvgpr_write_b32 a211, v52\n"
"v_accvgpr_write_b32 a212, v52\nv_accvgpr_write_b32 a213, v52\nv_accvgpr_write_b32 a214, v52\nv_accvgpr_write_b32 a215, v52\nv_accvgpr_write_b32 a216, v52\nv_accvgpr_write_b32 a217, v52\nv_accvgpr_write_b32 a218, v52\nv_accvgpr_write_b32 a219, v52\n"
"v_accvgpr_write_b32 a220, v52\nv_accvgpr_write_b32 a221, v52\nv_accvgpr_write_b32 a222, v52\nv_accvgpr_write_b32 a223, v52\nv_accvgpr_write_b32 a196, v52\nv_accvgpr_write_b32 a197, v52\nv_accvgpr_write_b32 a198, v52\nv_accvgpr_write_b32 a199, v52\n"
"v_accvgpr_write_b32 a224, v52\nv_accvgpr_write_b32 a225, v52\nv_accvgpr_write_b32 a226, v52\nv_accvgpr_write_b32 a227, v52\nv_accvgpr_write_b32 a228, v52\nv_accvgpr_write_b32 a229, v52\nv_accvgpr_write_b32 a230, v52\nv_accvgpr_write_b32 a231, v52\n"
"v_accvgpr_write_b32 a232, v52\nv_accvgpr_write_b32 a233, v52\nv_accvgpr_write_b32 a234, v52\nv_accvgpr_write_b32 a235, v52\nv_accvgpr_write_b32 a236, v52\nv_accvgpr_write_b32 a237, v52\nv_accvgpr_write_b32 a238, v52\nv_accvgpr_write_b32 a239, v52\n"
"v_accvgpr_write_b32 a240, v52\nv_accvgpr_write_b32 a241, v52\nv_accvgpr_write_b32 a242, v52\nv_accvgpr_write_b32 a243, v52\nv_accvgpr_write_b32 a244, v52\nv_accvgpr_write_b32 a245, v52\nv_accvgpr_write_b32 a246, v52\nv_accvgpr_write_b32 a247, v52\n"
"v_accvgpr_write_b32 a248, v52\nv_accvgpr_write_b32 a249, v52\nv_accvgpr_write_b32 a250, v52\nv_accvgpr_write_b32 a251, v52\nv_accvgpr_write_b32 a252, v52\nv_accvgpr_write_b32 a253, v52\nv_accvgpr_write_b32 a254, v52\nv_accvgpr_write_b32 a255, v52\n"
"v_accvgpr_write_b32 a200, v52\nv_accvgpr_write_b32 a201, v52\nv_accvgpr_write_b32 a202, v52\nv_accvgpr_write_b32 a203, v52\ns_lshl_b32 s89, s67, 21\ns_lshl_b32 s68, s93, 18\ns_add_u32 s89, s89, s68\ns_mov_b32 s66, 0\n"
".Lfb2:\ns_add_u32 s68, s89, 4096\nbuffer_load_dwordx4 a[112:115], v66, s[40:43], s89 offen offset:0\nbuffer_load_dwordx4 a[116:119], v66, s[40:43], s89 offen offset:64\nbuffer_load_dwordx4 a[120:123], v66, s[40:43], s89 offen offset:128\nbuffer_load_dwordx4 a[124:127], v66, s[40:43], s89 offen offset:192\nbuffer_load_dwordx4 a[128:131], v66, s[40:43], s68 offen offset:0\nbuffer_load_dwordx4 a[132:135], v66, s[40:43], s68 offen offset:64\n"
"buffer_load_dwordx4 a[136:139], v66, s[40:43], s68 offen offset:128\nbuffer_load_dwordx4 a[140:143], v66, s[40:43], s68 offen offset:192\nbuffer_load_ushort v8, v67, s[44:47], s89 offen offset:0\nbuffer_load_ushort v9, v67, s[44:47], s89 offen offset:256\nbuffer_load_ushort v10, v67, s[44:47], s89 offen offset:512\nbuffer_load_ushort v11, v67, s[44:47], s89 offen offset:768\nbuffer_load_ushort v12, v67, s[44:47], s68 offen offset:0\nbuffer_load_ushort v13, v67, s[44:47], s68 offen offset:256\n"
"buffer_load_ushort v14, v67, s[44:47], s68 offen offset:512\nbuffer_load_ushort v15, v67, s[44:47], s68 offen offset:768\ns_waitcnt vmcnt(0)\nv_lshl_or_b32 v8, v9, 16, v8\nv_accvgpr_write_b32 a144, v8\nv_lshl_or_b32 v10, v11, 16, v10\nv_accvgpr_write_b32 a145, v10\nv_lshl_or_b32 v12, v13, 16, v12\n"
"v_accvgpr_write_b32 a146, v12\nv_lshl_or_b32 v14, v15, 16, v14\nv_accvgpr_write_b32 a147, v14\nbuffer_load_ushort v8, v67, s[44:47], s89 offen offset:32\nbuffer_load_ushort v9, v67, s[44:47], s89 offen offset:288\nbuffer_load_ushort v10, v67, s[44:47], s89 offen offset:544\nbuffer_load_ushort v11, v67, s[44:47], s89 offen offset:800\nbuffer_load_ushort v12, v67, s[44:47], s68 offen offset:32\n"
"buffer_load_ushort v13, v67, s[44:47], s68 offen offset:288\nbuffer_load_ushort v14, v67, s[44:47], s68 offen offset:544\nbuffer_load_ushort v15, v67, s[44:47], s68 offen offset:800\ns_waitcnt vmcnt(0)\nv_lshl_or_b32 v8, v9, 16, v8\nv_accvgpr_write_b32 a148, v8\nv_lshl_or_b32 v10, v11, 16, v10\nv_accvgpr_write_b32 a149, v10\n"
"v_lshl_or_b32 v12, v13, 16, v12\nv_accvgpr_write_b32 a150, v12\nv_lshl_or_b32 v14, v15, 16, v14\nv_accvgpr_write_b32 a151, v14\nbuffer_load_ushort v8, v67, s[44:47], s89 offen offset:64\nbuffer_load_ushort v9, v67, s[44:47], s89 offen offset:320\nbuffer_load_ushort v10, v67, s[44:47], s89 offen offset:576\nbuffer_load_ushort v11, v67, s[44:47], s89 offen offset:832\n"
"buffer_load_ushort v12, v67, s[44:47], s68 offen offset:64\nbuffer_load_ushort v13, v67, s[44:47], s68 offen offset:320\nbuffer_load_ushort v14, v67, s[44:47], s68 offen offset:576\nbuffer_load_ushort v15, v67, s[44:47], s68 offen offset:832\ns_waitcnt vmcnt(0)\nv_lshl_or_b32 v8, v9, 16, v8\nv_accvgpr_write_b32 a152, v8\nv_lshl_or_b32 v10, v11, 16, v10\n"
"v_accvgpr_write_b32 a153, v10\nv_lshl_or_b32 v12, v13, 16, v12\nv_accvgpr_write_b32 a154, v12\nv_lshl_or_b32 v14, v15, 16, v14\nv_accvgpr_write_b32 a155, v14\nbuffer_load_ushort v8, v67, s[44:47], s89 offen offset:96\nbuffer_load_ushort v9, v67, s[44:47], s89 offen offset:352\nbuffer_load_ushort v10, v67, s[44:47], s89 offen offset:608\n"
"buffer_load_ushort v11, v67, s[44:47], s89 offen offset:864\nbuffer_load_ushort v12, v67, s[44:47], s68 offen offset:96\nbuffer_load_ushort v13, v67, s[44:47], s68 offen offset:352\nbuffer_load_ushort v14, v67, s[44:47], s68 offen offset:608\nbuffer_load_ushort v15, v67, s[44:47], s68 offen offset:864\ns_waitcnt vmcnt(0)\nv_lshl_or_b32 v8, v9, 16, v8\nv_accvgpr_write_b32 a156, v8\n"
"v_lshl_or_b32 v10, v11, 16, v10\nv_accvgpr_write_b32 a157, v10\nv_lshl_or_b32 v12, v13, 16, v12\nv_accvgpr_write_b32 a158, v12\nv_lshl_or_b32 v14, v15, 16, v14\nv_accvgpr_write_b32 a159, v14\nbuffer_load_ushort v8, v67, s[44:47], s89 offen offset:128\nbuffer_load_ushort v9, v67, s[44:47], s89 offen offset:384\n"
"buffer_load_ushort v10, v67, s[44:47], s89 offen offset:640\nbuffer_load_ushort v11, v67, s[44:47], s89 offen offset:896\nbuffer_load_ushort v12, v67, s[44:47], s68 offen offset:128\nbuffer_load_ushort v13, v67, s[44:47], s68 offen offset:384\nbuffer_load_ushort v14, v67, s[44:47], s68 offen offset:640\nbuffer_load_ushort v15, v67, s[44:47], s68 offen offset:896\ns_waitcnt vmcnt(0)\nv_lshl_or_b32 v8, v9, 16, v8\n"
"v_accvgpr_write_b32 a160, v8\nv_lshl_or_b32 v10, v11, 16, v10\nv_accvgpr_write_b32 a161, v10\nv_lshl_or_b32 v12, v13, 16, v12\nv_accvgpr_write_b32 a162, v12\nv_lshl_or_b32 v14, v15, 16, v14\nv_accvgpr_write_b32 a163, v14\nbuffer_load_ushort v8, v67, s[44:47], s89 offen offset:160\n"
"buffer_load_ushort v9, v67, s[44:47], s89 offen offset:416\nbuffer_load_ushort v10, v67, s[44:47], s89 offen offset:672\nbuffer_load_ushort v11, v67, s[44:47], s89 offen offset:928\nbuffer_load_ushort v12, v67, s[44:47], s68 offen offset:160\nbuffer_load_ushort v13, v67, s[44:47], s68 offen offset:416\nbuffer_load_ushort v14, v67, s[44:47], s68 offen offset:672\nbuffer_load_ushort v15, v67, s[44:47], s68 offen offset:928\ns_waitcnt vmcnt(0)\n"
"v_lshl_or_b32 v8, v9, 16, v8\nv_accvgpr_write_b32 a164, v8\nv_lshl_or_b32 v10, v11, 16, v10\nv_accvgpr_write_b32 a165, v10\nv_lshl_or_b32 v12, v13, 16, v12\nv_accvgpr_write_b32 a166, v12\nv_lshl_or_b32 v14, v15, 16, v14\nv_accvgpr_write_b32 a167, v14\n"
"buffer_load_ushort v8, v67, s[44:47], s89 offen offset:192\nbuffer_load_ushort v9, v67, s[44:47], s89 offen offset:448\nbuffer_load_ushort v10, v67, s[44:47], s89 offen offset:704\nbuffer_load_ushort v11, v67, s[44:47], s89 offen offset:960\nbuffer_load_ushort v12, v67, s[44:47], s68 offen offset:192\nbuffer_load_ushort v13, v67, s[44:47], s68 offen offset:448\nbuffer_load_ushort v14, v67, s[44:47], s68 offen offset:704\nbuffer_load_ushort v15, v67, s[44:47], s68 offen offset:960\n"
"s_waitcnt vmcnt(0)\nv_lshl_or_b32 v8, v9, 16, v8\nv_accvgpr_write_b32 a168, v8\nv_lshl_or_b32 v10, v11, 16, v10\nv_accvgpr_write_b32 a169, v10\nv_lshl_or_b32 v12, v13, 16, v12\nv_accvgpr_write_b32 a170, v12\nv_lshl_or_b32 v14, v15, 16, v14\n"
"v_accvgpr_write_b32 a171, v14\nbuffer_load_ushort v8, v67, s[44:47], s89 offen offset:224\nbuffer_load_ushort v9, v67, s[44:47], s89 offen offset:480\nbuffer_load_ushort v10, v67, s[44:47], s89 offen offset:736\nbuffer_load_ushort v11, v67, s[44:47], s89 offen offset:992\nbuffer_load_ushort v12, v67, s[44:47], s68 offen offset:224\nbuffer_load_ushort v13, v67, s[44:47], s68 offen offset:480\nbuffer_load_ushort v14, v67, s[44:47], s68 offen offset:736\n"
"buffer_load_ushort v15, v67, s[44:47], s68 offen offset:992\ns_waitcnt vmcnt(0)\nv_lshl_or_b32 v8, v9, 16, v8\nv_accvgpr_write_b32 a172, v8\nv_lshl_or_b32 v10, v11, 16, v10\nv_accvgpr_write_b32 a173, v10\nv_lshl_or_b32 v12, v13, 16, v12\nv_accvgpr_write_b32 a174, v12\n"
"v_lshl_or_b32 v14, v15, 16, v14\nv_accvgpr_write_b32 a175, v14\nQcvaa 0,112,0,24\nQmvaa 0,116,4\nQmvaa 0,120,8\nQmvaa 0,124,12\nQcvaa 4,128,0,24\nQmvaa 4,132,4\n"
"Qmvaa 4,136,8\nQmvaa 4,140,12\ns_nop 7\nQe 0\nQe 1\nQe 2\nQe 3\nQe 4\n"
"Qe 5\nQe 6\nQe 7\ns_nop 0\nQk 16,0,1\nQk 17,2,3\nQk 18,4,5\nQk 19,6,7\n"
"s_nop 1\nQmvva 80,16,144\nQmvva 84,16,148\nQmvva 88,16,152\nQmvva 92,16,156\nQmvva 96,16,160\nQmvva 100,16,164\nQmvva 104,16,168\n"
"Qmvva 108,16,172\nQmaav 176,204,16\nQcvaa 0,112,16,28\nQmvaa 0,116,20\nQmvaa 0,120,24\nQmvaa 0,124,28\nQcvaa 4,128,16,28\nQmvaa 4,132,20\n"
"Qmvaa 4,136,24\nQmvaa 4,140,28\ns_nop 7\nQe 0\nQe 1\nQe 2\nQe 3\nQe 4\n"
"Qe 5\nQe 6\nQe 7\ns_nop 0\nQk 16,0,1\nQk 17,2,3\nQk 18,4,5\nQk 19,6,7\n"
"s_nop 1\nQmvva 112,16,144\nQmvva 116,16,148\nQmvva 120,16,152\nQmvva 124,16,156\nQmvva 128,16,160\nQmvva 132,16,164\nQmvva 136,16,168\n"
"Qmvva 140,16,172\nQmaav 180,204,16\nQcvaa 0,112,32,32\nQmvaa 0,116,36\nQmvaa 0,120,40\nQmvaa 0,124,44\nQcvaa 4,128,32,32\nQmvaa 4,132,36\n"
"Qmvaa 4,136,40\nQmvaa 4,140,44\ns_nop 7\nQe 0\nQe 1\nQe 2\nQe 3\nQe 4\n"
"Qe 5\nQe 6\nQe 7\ns_nop 0\nQk 16,0,1\nQk 17,2,3\nQk 18,4,5\nQk 19,6,7\n"
"s_nop 1\nQmvva 144,16,144\nQmvva 148,16,148\nQmvva 152,16,152\nQmvva 156,16,156\nQmvva 160,16,160\nQmvva 164,16,164\nQmvva 168,16,168\n"
"Qmvva 172,16,172\nQmaav 184,204,16\nQcvaa 0,112,48,36\nQmvaa 0,116,52\nQmvaa 0,120,56\nQmvaa 0,124,60\nQcvaa 4,128,48,36\nQmvaa 4,132,52\n"
"Qmvaa 4,136,56\nQmvaa 4,140,60\ns_nop 7\nQe 0\nQe 1\nQe 2\nQe 3\nQe 4\n"
"Qe 5\nQe 6\nQe 7\ns_nop 0\nQk 16,0,1\nQk 17,2,3\nQk 18,4,5\nQk 19,6,7\n"
"s_nop 1\nQmvva 176,16,144\nQmvva 180,16,148\nQmvva 184,16,152\nQmvva 188,16,156\nQmvva 192,16,160\nQmvva 196,16,164\nQmvva 200,16,168\n"
"Qmvva 204,16,172\nQmaav 188,204,16\nQcvaa 0,112,64,40\nQmvaa 0,116,68\nQmvaa 0,120,72\nQmvaa 0,124,76\nQcvaa 4,128,64,40\nQmvaa 4,132,68\n"
"Qmvaa 4,136,72\nQmvaa 4,140,76\ns_nop 7\nQe 0\nQe 1\nQe 2\nQe 3\nQe 4\n"
"Qe 5\nQe 6\nQe 7\ns_nop 0\nQk 16,0,1\nQk 17,2,3\nQk 18,4,5\nQk 19,6,7\n"
"s_nop 1\nQmvva 208,16,144\nQmvva 212,16,148\nQmvva 216,16,152\nQmvva 220,16,156\nQmvva 224,16,160\nQmvva 228,16,164\nQmvva 232,16,168\n"
"Qmvva 236,16,172\nQmaav 192,204,16\nQcvaa 0,112,80,44\nQmvaa 0,116,84\nQmvaa 0,120,88\nQmvaa 0,124,92\nQcvaa 4,128,80,44\nQmvaa 4,132,84\n"
"Qmvaa 4,136,88\nQmvaa 4,140,92\ns_nop 7\nQe 0\nQe 1\nQe 2\nQe 3\nQe 4\n"
"Qe 5\nQe 6\nQe 7\ns_nop 0\nQk 16,0,1\nQk 17,2,3\nQk 18,4,5\nQk 19,6,7\n"
"s_nop 1\nQmvva 240,16,144\nQmvva 244,16,148\nQmvva 248,16,152\nQmvva 252,16,156\nQmava 208,16,160\nQmava 212,16,164\nQmava 216,16,168\n"
"Qmava 220,16,172\nQmaav 196,204,16\nQcvaa 0,112,96,48\nQmvaa 0,116,100\nQmvaa 0,120,104\nQmvaa 0,124,108\nQcvaa 4,128,96,48\nQmvaa 4,132,100\n"
"Qmvaa 4,136,104\nQmvaa 4,140,108\ns_nop 7\nQe 0\nQe 1\nQe 2\nQe 3\nQe 4\n"
"Qe 5\nQe 6\nQe 7\ns_nop 0\nQk 16,0,1\nQk 17,2,3\nQk 18,4,5\nQk 19,6,7\n"
"s_nop 1\nQmava 224,16,144\nQmava 228,16,148\nQmava 232,16,152\nQmava 236,16,156\nQmava 240,16,160\nQmava 244,16,164\nQmava 248,16,168\n"
"Qmava 252,16,172\nQmaav 200,204,16\ns_add_u32 s89, s89, 8192\ns_add_u32 s66, s66, 1\ns_cmp_lt_u32 s66, 32\ns_cbranch_scc1 .Lfb2\ns_nop 7\nv_accvgpr_read_b32 v0, a176\n"
"v_accvgpr_read_b32 v4, a180\nv_accvgpr_read_b32 v8, a184\nv_accvgpr_read_b32 v12, a188\nv_accvgpr_read_b32 v16, a192\nv_accvgpr_read_b32 v20, a196\nv_accvgpr_read_b32 v24, a200\ns_mov_b32 s73, 0\nv_mov_b32 v30, s73\n"
"v_mov_b32 v31, 0\nv_mov_b32 v58, 0xb9800000\nv_mov_b32 v59, 0\nv_and_b32 v66, 16, v60\nv_mov_b32 v67, 0xffff\nv_lshlrev_b32 v66, v66, v67\ns_mov_b32 vcc_lo, 0x00c0000c\ns_mov_b32 vcc_hi, 0xc0000c00\n"
"v_cndmask_b32 v67, 0, v66, vcc\ns_lshr_b64 vcc, vcc, 2\nv_cndmask_b32 v66, 0, v66, vcc\ns_cmp_eq_u32 s67, 0\ns_cbranch_scc1 .Lfbret0\ns_branch .Lfbret1\n.Lf2_w:\ns_mov_b32 s97, 0\n"
".Lf2_l:\ns_sleep 1\nbuffer_load_dword v78, v76, s[24:27], 0 offen sc1\ns_waitcnt vmcnt(0)\ns_add_u32 s97, s97, 1\ns_cmp_gt_u32 s97, 1048576\ns_cbranch_scc1 .Lf2_r\nv_sub_u32 v66, v78, v65\n"
"v_cmp_gt_u32 vcc, 2, v66\ns_and_b64 vcc, vcc, s[28:29]\ns_cbranch_scc1 .Lf2_l\ns_branch .Lf2_r\n.Lf3_w:\ns_mov_b32 s97, 0\n.Lf3_l:\ns_sleep 1\n"
"buffer_load_dword v78, v76, s[24:27], 0 offen sc1\ns_waitcnt vmcnt(0)\ns_add_u32 s97, s97, 1\ns_cmp_gt_u32 s97, 1048576\ns_cbranch_scc1 .Lf3_r\nv_sub_u32 v66, v78, v65\nv_cmp_gt_u32 vcc, 3, v66\ns_and_b64 vcc, vcc, s[28:29]\n"
"s_cbranch_scc1 .Lf3_l\ns_branch .Lf3_r\n.Lx0_w:\ns_mov_b32 s97, 0\n.Lx0_l:\ns_sleep 1\nbuffer_load_dword v78, v76, s[24:27], 0 offen sc1\ns_waitcnt vmcnt(0)\n"
"s_add_u32 s97, s97, 1\ns_cmp_gt_u32 s97, 1048576\ns_cbranch_scc1 .Lx0_r\nv_sub_u32 v66, v78, v65\nv_cmp_gt_u32 vcc, 1, v66\ns_and_b64 vcc, vcc, s[28:29]\ns_cbranch_scc1 .Lx0_l\ns_branch .Lx0_r\n"
".Lx1_w:\ns_mov_b32 s97, 0\n.Lx1_l:\ns_sleep 1\nbuffer_load_dword v78, v76, s[24:27], 0 offen sc1\ns_waitcnt vmcnt(0)\ns_add_u32 s97, s97, 1\ns_cmp_gt_u32 s97, 1048576\n"
"s_cbranch_scc1 .Lx1_r\nv_sub_u32 v66, v78, v65\nv_cmp_gt_u32 vcc, 1, v66\ns_and_b64 vcc, vcc, s[28:29]\ns_cbranch_scc1 .Lx1_l\ns_branch .Lx1_r\n.Lexit:\n"
  : "=&s"(t0), "=&s"(t1)
  : "s"(__builtin_amdgcn_kernarg_segment_ptr()), "s"(blockIdx.x), "s"((unsigned)(size_t)lds), "s"(__builtin_amdgcn_readfirstlane(threadIdx.x >> 6))
  : "v0", "v1", "v2", "v3", "v4", "v5", "v6", "v7", "v8", "v9", "v10", "v11", "v12", "v13", "v14", "v15", "v16", "v17", "v18", "v19", "v20", "v21", "v22", "v23", "v24", "v25", "v26", "v27", "v28", "v29", "v30", "v31", "v32", "v33", "v34", "v35", "v36", "v37", "v38", "v39", "v40", "v41", "v42", "v43", "v44", "v45", "v46", "v47", "v48", "v49", "v50", "v51", "v52", "v53", "v54", "v55", "v56", "v57", "v58", "v59", "v60", "v61", "v62", "v63", "v64", "v65", "v66", "v67", "v68", "v69", "v70", "v71", "v72", "v73", "v74", "v75", "v76", "v77", "v78", "v79", "v80", "v81", "v82", "v83", "v84", "v85", "v86", "v87", "v88", "v89", "v90", "v91", "v92", "v93", "v94", "v95", "v96", "v97", "v98", "v99", "v100", "v101", "v102", "v103", "v104", "v105", "v106", "v107", "v108", "v109", "v110", "v111", "v112", "v113", "v114", "v115", "v116", "v117", "v118", "v119", "v120", "v121", "v122", "v123", "v124", "v125", "v126", "v127", "v128", "v129", "v130", "v131", "v132", "v133", "v134", "v135", "v136", "v137", "v138", "v139", "v140", "v141", "v142", "v143", "v144", "v145", "v146", "v147", "v148", "v149", "v150", "v151", "v152", "v153", "v154", "v155", "v156", "v157", "v158", "v159", "v160", "v161", "v162", "v163", "v164", "v165", "v166", "v167", "v168", "v169", "v170", "v171", "v172", "v173", "v174", "v175", "v176", "v177", "v178", "v179", "v180", "v181", "v182", "v183", "v184", "v185", "v186", "v187", "v188", "v189", "v190", "v191", "v192", "v193", "v194", "v195", "v196", "v197", "v198", "v199", "v200", "v201", "v202", "v203", "v204", "v205", "v206", "v207", "v208", "v209", "v210", "v211", "v212", "v213", "v214", "v215", "v216", "v217", "v218", "v219", "v220", "v221", "v222", "v223", "v224", "v225", "v226", "v227", "v228", "v229", "v230", "v231", "v232", "v233", "v234", "v235", "v236", "v237", "v238", "v239", "v240", "v241", "v242", "v243", "v244", "v245", "v246", "v247", "v248", "v249", "v250", "v251", "v252", "v253", "v254", "v255", "a0", "a1", "a2", "a3", "a4", "a5", "a6", "a7", "a8", "a9", "a10", "a11", "a12", "a13", "a14", "a15", "a16", "a17", "a18", "a19", "a20", "a21", "a22", "a23", "a24", "a25", "a26", "a27", "a28", "a29", "a30", "a31", "a32", "a33", "a34", "a35", "a36", "a37", "a38", "a39", "a40", "a41", "a42", "a43", "a44", "a45", "a46", "a47", "a48", "a49", "a50", "a51", "a52", "a53", "a54", "a55", "a56", "a57", "a58", "a59", "a60", "a61", "a62", "a63", "a64", "a65", "a66", "a67", "a68", "a69", "a70", "a71", "a72", "a73", "a74", "a75", "a76", "a77", "a78", "a79", "a80", "a81", "a82", "a83", "a84", "a85", "a86", "a87", "a88", "a89", "a90", "a91", "a92", "a93", "a94", "a95", "a96", "a97", "a98", "a99", "a100", "a101", "a102", "a103", "a104", "a105", "a106", "a107", "a108", "a109", "a110", "a111", "a112", "a113", "a114", "a115", "a116", "a117", "a118", "a119", "a120", "a121", "a122", "a123", "a124", "a125", "a126", "a127", "a128", "a129", "a130", "a131", "a132", "a133", "a134", "a135", "a136", "a137", "a138", "a139", "a140", "a141", "a142", "a143", "a144", "a145", "a146", "a147", "a148", "a149", "a150", "a151", "a152", "a153", "a154", "a155", "a156", "a157", "a158", "a159", "a160", "a161", "a162", "a163", "a164", "a165", "a166", "a167", "a168", "a169", "a170", "a171", "a172", "a173", "a174", "a175", "a176", "a177", "a178", "a179", "a180", "a181", "a182", "a183", "a184", "a185", "a186", "a187", "a188", "a189", "a190", "a191", "a192", "a193", "a194", "a195", "a196", "a197", "a198", "a199", "a200", "a201", "a202", "a203", "a204", "a205", "a206", "a207", "a208", "a209", "a210", "a211", "a212", "a213", "a214", "a215", "a216", "a217", "a218", "a219", "a220", "a221", "a222", "a223", "a224", "a225", "a226", "a227", "a228", "a229", "a230", "a231", "a232", "a233", "a234", "a235", "a236", "a237", "a238", "a239", "a240", "a241", "a242", "a243", "a244", "a245", "a246", "a247", "a248", "a249", "a250", "a251", "a252", "a253", "a254", "a255", "s8", "s9", "s10", "s11", "s12", "s13", "s14", "s15", "s16", "s17", "s18", "s19", "s20", "s21", "s22", "s23", "s24", "s25", "s26", "s27", "s28", "s29", "s30", "s31", "s33", "s34", "s35", "s36", "s37", "s38", "s39", "s40", "s41", "s42", "s43", "s44", "s45", "s46", "s47", "s48", "s49", "s50", "s51", "s52", "s53", "s54", "s55", "s56", "s57", "s58", "s59", "s60", "s61", "s62", "s63", "s64", "s65", "s66", "s67", "s68", "s69", "s70", "s71", "s72", "s73", "s74", "s75", "s76", "s77", "s78", "s79", "s80", "s81", "s82", "s83", "s84", "s85", "s86", "s87", "s88", "s89", "s90", "s91", "s92", "s93", "s94", "s95", "s96", "s97", "s98", "s99", "s100", "s101", "vcc", "scc", "memory");
}

extern "C" void kernel_launch(void* const* d_in, const int* in_sizes, int n_in, void* d_out, int out_size, void* d_ws, size_t ws_size, hipStream_t stream) {
  typedef const unsigned short* P;
  (void)hipFuncSetAttribute((const void*)mha2_asm, hipFuncAttributeMaxDynamicSharedMemorySize, 163840);
  hipLaunchKernelGGL(mha2_asm, dim3(256), dim3(256), 163840, stream, (P)d_in[0], (P)d_in[3], (P)d_in[1], (P)d_in[2], (P)d_in[5], (P)d_in[8], (P)d_in[6], (P)d_in[7],
                     (P)d_in[4], (P)d_in[9], (char*)d_ws, (unsigned short*)d_out);
}
```
